```python
import torch

def _softmax(x, axis=-1):
    return torch.softmax(x, dim=axis)

def _gelu(x):
    return torch.nn.functional.gelu(x, approximate='tanh')

def _silu(x):
    return torch.nn.functional.silu(x)

def _sigmoid(x):
    return torch.sigmoid(x)

def _layernorm(x, axis=-1, eps=1e-05):
    m = x.mean(dim=axis, keepdim=True)
    v = x.var(dim=axis, keepdim=True, unbiased=False)
    return (x - m) / torch.sqrt(v + eps)

def _scatter_add(tgt, idx, src, axis=0):
    return tgt.clone().index_add_(axis, idx, src)

def _segment_sum(src, seg, n):
    out = torch.zeros((n,) + src.shape[1:], dtype=src.dtype)
    return out.index_add_(0, seg, src)

def _pad(x, pw):
    flat = tuple((v for pair in reversed(pw) for v in pair))
    return torch.nn.functional.pad(x, flat)
kernel_name = 'gated_mix__ssm_conv_h3__memory_aug_recurrent_mem_tr__prompt'

def setup_inputs():

    _w = lambda a: (lambda t: t.to(torch.bfloat16) if t.is_floating_point() else t)(torch.as_tensor(a))

    def _A_setup():
        B, S, D = (1, 2048, 1024)
        ks = 4
        return {'q': _w((lambda *s: torch.randn(*s, dtype=torch.bfloat16) * 0.05)(B, S, D)), 'k': _w((lambda *s: torch.randn(*s, dtype=torch.bfloat16) * 0.4)(B, S, D)), 'v': _w((lambda *s: torch.randn(*s, dtype=torch.bfloat16) * 0.4)(B, S, D)), 'shift_k': _w((lambda *s: torch.randn(*s, dtype=torch.bfloat16) * 0.4)(D, ks) * 0.5), 'ssm_k': _w((lambda *s: torch.randn(*s, dtype=torch.bfloat16) * 0.4)(D, S) * (1.0 / S ** 0.5)), 'ssm_D': _w((lambda *s: torch.randn(*s, dtype=torch.bfloat16) * 0.4)(D))}
    _A_in = _A_setup()

    def _B_setup():
        B, S, Dm = (1, 2048, 1024)
        H, Dh = (32, 128)
        M, seg = (8, 32)
        seg = min(seg, S)
        S = S // seg * seg
        return {'x': _w((lambda *s: torch.randn(*s, dtype=torch.bfloat16) * 0.05)(B, S, Dm)), 'mem0': _w((lambda *s: torch.randn(*s, dtype=torch.bfloat16) * 0.4)(B, M, Dm)), 'Wq': _w((lambda *s: torch.randn(*s, dtype=torch.bfloat16) * 6.4)(Dm, H, Dh) * Dm ** (-0.5)), 'Wk': _w((lambda *s: torch.randn(*s, dtype=torch.bfloat16) * 6.4)(Dm, H, Dh) * Dm ** (-0.5)), 'Wv': _w((lambda *s: torch.randn(*s, dtype=torch.bfloat16) * 0.4)(Dm, H, Dh) * Dm ** (-0.5)), 'Wo': _w((lambda *s: torch.randn(*s, dtype=torch.bfloat16) * 0.4)(H, Dh, Dm) * (H * Dh) ** (-0.5)), 'seg_len': seg}
    _B_in = _B_setup()
    return {'A_q': _A_in['q'], 'A_k': _A_in['k'], 'A_v': _A_in['v'], 'A_shift_k': _A_in['shift_k'], 'A_ssm_k': _A_in['ssm_k'], 'A_ssm_D': _A_in['ssm_D'], 'B_x': _B_in['x'], 'B_mem0': _B_in['mem0'], 'B_Wq': _B_in['Wq'], 'B_Wk': _B_in['Wk'], 'B_Wv': _B_in['Wv'], 'B_Wo': _B_in['Wo'], 'B_seg_len': _B_in['seg_len'], 'gate_w': _w((lambda *s: torch.randn(*s, dtype=torch.bfloat16) * 0.05)(1024) * 16.0)}

def reference(A_q, A_k, A_v, A_shift_k, A_ssm_k, A_ssm_D, B_x, B_mem0, B_Wq, B_Wk, B_Wv, B_Wo, B_seg_len, gate_w):

    def _A_ref(q, k, v, shift_k, ssm_k, ssm_D):
        B, S, D = q.shape
        ks = shift_k.shape[-1]
        k_pad = _pad(k, ((0, 0), (ks - 1, 0), (0, 0)))
        k_shift = torch.zeros_like(k)
        for t in range(ks):
            k_shift = k_shift + k_pad[:, t:t + S, :] * shift_k[:, ks - 1 - t][None, None, :]
        kv = k_shift * v
        L = 2 * S
        kv_pad = _pad(kv, ((0, 0), (0, S), (0, 0)))
        kf_pad = _pad(ssm_k, ((0, 0), (0, S)))
        kv_f = (lambda x, axes=(-2, -1): torch.fft.rfft2(x.float(), dim=axes))(kv_pad[:, None, :, :], axes=(1, 2))
        kf_f = (lambda x, axes=(-2, -1): torch.fft.rfft2(x.float(), dim=axes))(kf_pad[:, None, :], axes=(1, 2))
        kf_f = (lambda x, *d: x.permute(*d))(kf_f, 1, 2, 0)
        y_f = kv_f * kf_f[None, :, :, :]
        y = (lambda x, s, axes=(-2, -1): torch.fft.irfft2(x, s=s, dim=axes).to(torch.bfloat16))(y_f, (1, L), axes=(1, 2))[:, 0, :S, :]
        y = y + kv * ssm_D[None, None, :]
        return q * y

    def _B_ref(x, mem0, Wq, Wk, Wv, Wo, seg_len):
        B, S, Dm = x.shape
        H, Dh = (Wq.shape[1], Wq.shape[2])
        M = mem0.shape[1]
        scale = Dh ** (-0.5)
        n_seg = S // seg_len
        L = M + seg_len
        idx = torch.arange(L)
        mask = (idx[:, None] < M) | (idx[:, None] >= idx[None, :])
        mem = mem0
        outs = []
        for s in range(n_seg):
            xs = x[:, s * seg_len:(s + 1) * seg_len, :].clone()
            h = torch.concatenate([mem, xs], axis=1)
            q = torch.einsum('btd,dhe->bhte', h, Wq)
            k = torch.einsum('btd,dhe->bhte', h, Wk)
            v = torch.einsum('btd,dhe->bhte', h, Wv)
            scores = torch.einsum('bhqe,bhke->bhqk', q, k) * scale
            scores = torch.where(mask[None, None], scores, -1000000000.0)
            p = _softmax(scores, axis=-1)
            o = torch.einsum('bhqk,bhke->bhqe', p, v)
            o = torch.einsum('bhte,hed->btd', o, Wo)
            mem = o[:, :M, :].clone()
            outs.append(o[:, M:, :])
        return torch.concatenate(outs, axis=1)
    x = A_q
    g = _sigmoid((x * gate_w).sum(-1, keepdims=True))
    a = _A_ref(x, A_k, A_v, A_shift_k, A_ssm_k, A_ssm_D)
    b = _B_ref(x, B_mem0, B_Wq, B_Wk, B_Wv, B_Wo, B_seg_len)
    return g * a + (1 - g) * b
```

```cpp
#include <hip/hip_runtime.h>
#include <stdint.h>
#include <type_traits>
typedef unsigned short bf16_t;
using bf16x8 = __attribute__((ext_vector_type(8))) short;
using f32x4 = __attribute__((ext_vector_type(4))) float;
typedef unsigned v4u __attribute__((ext_vector_type(4)));
typedef unsigned v2u __attribute__((ext_vector_type(2)));
typedef __amdgpu_buffer_rsrc_t rsrc_t;

__device__ __forceinline__ float bf2f(bf16_t b) { return __uint_as_float(((unsigned)b) << 16); }
typedef __bf16 hbf2 __attribute__((ext_vector_type(2)));
typedef float f32x2 __attribute__((ext_vector_type(2)));
__device__ __forceinline__ unsigned pack2(float a, float b) { f32x2 v = {a, b}; return __builtin_bit_cast(unsigned, __builtin_convertvector(v, hbf2)); }
__device__ __forceinline__ float rbfh(float f) { return __uint_as_float(pack2(f, 0.f) << 16); }
__device__ __forceinline__ unsigned nosent(unsigned x) {
    typedef unsigned short us2 __attribute__((ext_vector_type(2)));
    const us2 a = __builtin_bit_cast(us2, x), c = {(unsigned short)0xFFFE, (unsigned short)0xFFFE}; return __builtin_bit_cast(unsigned, __builtin_elementwise_min(a, c)); }

constexpr int S = 2048, D = 1024, H = 32, DH = 128, M = 8, SEG = 32, NSEG = 64;

constexpr unsigned W_FLG = 0;
constexpr unsigned F_KVQ = W_FLG;
constexpr unsigned F_CONV = W_FLG + 32768;
constexpr unsigned F_ERR = W_FLG + 32768 + 256;
constexpr unsigned F_PREP = W_FLG + 32768 + 512;
constexpr unsigned F_CAL = W_FLG + 33792;
constexpr unsigned F_TKT = W_FLG + 34816;
constexpr unsigned F_TQ = W_FLG + 36864;
constexpr unsigned F_PROG = W_FLG + 49152;
constexpr unsigned F_DUM = W_FLG + 53248;
constexpr unsigned F_OP = W_FLG + 65536;
constexpr unsigned F_ATT = W_FLG + 131072;
constexpr unsigned F_RDY = W_FLG + 147456;
constexpr unsigned FLG_BYTES = 180224;
constexpr unsigned HO_KVM = W_FLG + FLG_BYTES;
constexpr unsigned W_A = HO_KVM + 64 * 32 * 4096;
constexpr unsigned W_GATE = W_A + S * D * 2;
constexpr unsigned W_QX = W_GATE + 8192;
constexpr unsigned W_KX = W_QX + S * 4096 * 2;
constexpr unsigned W_VF = W_KX + S * 4096 * 2;
constexpr unsigned W_OX = W_VF + 64 * 32 * 8192;
constexpr unsigned W_PART = W_OX + S * 4096 * 2;
constexpr unsigned W_DBG = W_PART + 32 * 4 * 64 * 1024 * 4;
constexpr unsigned W_U = W_DBG + 256 * 64 * 16 * 8;
constexpr unsigned W_TOTAL = W_U + S * D * 2;

__device__ __forceinline__ v4u ld16(rsrc_t r, unsigned off) { return __builtin_amdgcn_raw_buffer_load_b128(r, off, 0, 16); }
__device__ __forceinline__ v2u ld8(rsrc_t r, unsigned off) { return __builtin_amdgcn_raw_buffer_load_b64(r, off, 0, 16); }
__device__ __forceinline__ unsigned ld4(rsrc_t r, unsigned off) { return __builtin_amdgcn_raw_buffer_load_b32(r, off, 0, 16); }
__device__ __forceinline__ void st16(rsrc_t r, unsigned off, v4u v) { __builtin_amdgcn_raw_buffer_store_b128(v, r, off, 0, 16); }
__device__ __forceinline__ void st8(rsrc_t r, unsigned off, v2u v) { __builtin_amdgcn_raw_buffer_store_b64(v, r, off, 0, 16); }
__device__ __forceinline__ void st4(rsrc_t r, unsigned off, unsigned v) { __builtin_amdgcn_raw_buffer_store_b32(v, r, off, 0, 16); }
__device__ __forceinline__ bool okdw(unsigned x) { return ((x & 0xFFFFu) != 0xFFFFu) & ((x >> 16) != 0xFFFFu); }
typedef unsigned short us2_t __attribute__((ext_vector_type(2)));
__device__ __forceinline__ unsigned pkmax(unsigned a, unsigned b) { return __builtin_bit_cast(unsigned, __builtin_elementwise_max(__builtin_bit_cast(us2_t, a), __builtin_bit_cast(us2_t, b))); }
__device__ __forceinline__ unsigned pkmax4(v4u v) { return pkmax(pkmax(v.x, v.y), pkmax(v.z, v.w)); }
__device__ __forceinline__ bool ok16(v4u v) { return okdw(pkmax4(v)); }
__device__ __forceinline__ bool ok8(v2u v) { return okdw(v.x) & okdw(v.y); }
__device__ __forceinline__ bf16x8 as_frag(v4u v) { return __builtin_bit_cast(bf16x8, v); }
#define MFMA(a, b, c) __builtin_amdgcn_mfma_f32_16x16x32_bf16((a), (b), (c), 0, 0, 0)
#define D1 144
#define D2 100
#define D3 76
#define LT_A 4
#define LB0 12
#define LC0 20
#define LA0 24
#define TR_UP 4
#define TR_DN 2
#define L_DEC 3
#define WARM1 0
#define KROT 0
#define STAG 1
#define UP_SH 2
#define EPI_SLP 0
#define SITA 1
#define PAIRX 4
#define DUE_P 1
#define LATE 1
#define EARLY_FROM 56
#define SKEW 0
#define OUT_WARM 1
#define ADAPT 1
#define RPL 3
#define CV_POL 2
#define POLL_SLP 4
#define XWAIT 0
#define PEN 60
#define MEMBAR() asm volatile("" ::: "memory")
#define FT(i) do { } while (0)
#define ENDPGM() asm volatile("s_endpgm" ::: "memory")
#define DRAIN() asm volatile("s_waitcnt vmcnt(0)" ::: "memory")

__device__ __forceinline__ float xor16_max(float x) { auto r = __builtin_amdgcn_permlane16_swap(__float_as_uint(x), __float_as_uint(x), false, false); return fmaxf(__uint_as_float(r[0]), __uint_as_float(r[1])); }
__device__ __forceinline__ float xor32_max(float x) { auto r = __builtin_amdgcn_permlane32_swap(__float_as_uint(x), __float_as_uint(x), false, false); return fmaxf(__uint_as_float(r[0]), __uint_as_float(r[1])); }
__device__ __forceinline__ float xor16_add(float x) { auto r = __builtin_amdgcn_permlane16_swap(__float_as_uint(x), __float_as_uint(x), false, false); return __uint_as_float(r[0]) + __uint_as_float(r[1]); }
__device__ __forceinline__ float xor32_add(float x) { auto r = __builtin_amdgcn_permlane32_swap(__float_as_uint(x), __float_as_uint(x), false, false); return __uint_as_float(r[0]) + __uint_as_float(r[1]); }
__device__ __forceinline__ v2u tr_read(unsigned lds_addr) { v2u r; asm volatile("ds_read_b64_tr_b16 %0, %1\n\ts_waitcnt lgkmcnt(0)" : "=&v"(r) : "v"(lds_addr) : "memory"); return r; }

typedef short v4i16_t __attribute__((ext_vector_type(4)));
__device__ __forceinline__ v2u trb(unsigned lds_addr) { return __builtin_bit_cast(v2u, __builtin_amdgcn_ds_read_tr16_b64_v4i16((__attribute__((address_space(3))) v4i16_t*)(size_t)lds_addr)); }
__device__ __forceinline__ int otid_(int wv) { int t; asm volatile("v_mbcnt_lo_u32_b32 %0, -1, 0\n\tv_mbcnt_hi_u32_b32 %0, -1, %0" : "=v"(t)); return wv * 64 + t; }
#define otid() otid_(wv)
__device__ __forceinline__ bool wait_word(rsrc_t r, unsigned off, unsigned want, unsigned code) {
    for (unsigned spin = 0;; ++spin) { MEMBAR();
        const unsigned v = __builtin_amdgcn_readfirstlane(ld4(r, off));
        if (v >= want) return true;
        if (spin > (1u << 20)) { st4(r, F_ERR, code); return false; }
        __builtin_amdgcn_s_sleep(8); }
}

__device__ __forceinline__ unsigned rtnow() { return (unsigned)__builtin_amdgcn_s_memrealtime(); }
__device__ __forceinline__ void wait_until(unsigned target) { for (int i = 0; i < 4096; ++i) { if ((int)(rtnow() - target) >= 0) break; } }
__device__ __forceinline__ void wait_long(unsigned target) { for (int i = 0; i < 4096; ++i) { if ((int)(rtnow() - target) >= 0) break; __builtin_amdgcn_s_sleep(8); } }
constexpr int RED_G = 320;
constexpr int RED_T = 4 * RED_G;
constexpr int L_RED = 0;
constexpr int L_KX = 65536;
constexpr int KXP = 272;
constexpr int L_VF = L_KX + 2 * 32 * KXP;
constexpr int L_QKV = L_VF + 16384;
constexpr int L_OFF = L_QKV + 6144;
constexpr int L_STMP = L_OFF + 256;
constexpr int L_TOTAL = 163840;
constexpr unsigned U_A = 0, U_S = 32, U_B = 36, U_INIT = 113, U_C = 128, U_END = 512;
#define PLACE 1
#define CAL_N 5
#define STEP_MIN 200u

struct Params {
    const bf16_t *Aq, *Ak, *Av, *shift_k, *ssm_k, *ssm_D, *mem0, *Wq, *Wk, *Wv, *Wo, *gate_w;
    bf16_t* out; unsigned char* ws;
};
__host__ __device__ __forceinline__ unsigned hx_base(const void* ws) { const size_t a = (size_t)ws; return (unsigned)((((a + W_TOTAL + 0x7FFFFFu) >> 23) << 23) - a); }

#define STAMP(i) do { } while (0)
#define BSTAMP(i) do { } while (0)
#define EV(code) do { } while (0)
#define SPINS(i) do { } while (0)
typedef short v4s __attribute__((ext_vector_type(4)));
typedef unsigned v32u __attribute__((ext_vector_type(32)));
typedef unsigned v16u __attribute__((ext_vector_type(16)));
static_assert(L_VF - L_KX == 16384 + 1024, "fragment read offsets");
#define MFMA16(a, b, c) __builtin_amdgcn_mfma_f32_16x16x16bf16_1k(__builtin_bit_cast(v4s, (a)), __builtin_bit_cast(v4s, (b)), (c), 0, 0, 0)
__device__ __forceinline__ unsigned ror8(unsigned x) { return (unsigned)__builtin_amdgcn_update_dpp(0, (int)x, 0x128, 0xF, 0xF, false); }
__device__ __forceinline__ unsigned qx1(unsigned x) { return (unsigned)__builtin_amdgcn_update_dpp(0, (int)x, 0xB1, 0xF, 0xF, false); }
__device__ __forceinline__ unsigned wave_pkmax(unsigned d) {
    d = pkmax(d, (unsigned)__builtin_amdgcn_update_dpp((int)d, (int)d, 0xB1, 0xF, 0xF, false)); d = pkmax(d, (unsigned)__builtin_amdgcn_update_dpp((int)d, (int)d, 0x4E, 0xF, 0xF, false));
    d = pkmax(d, (unsigned)__builtin_amdgcn_update_dpp((int)d, (int)d, 0x124, 0xF, 0xF, false)); d = pkmax(d, (unsigned)__builtin_amdgcn_update_dpp((int)d, (int)d, 0x128, 0xF, 0xF, false));
    { auto q = __builtin_amdgcn_permlane16_swap(d, d, false, false); d = pkmax(q[0], q[1]); }
    { auto q = __builtin_amdgcn_permlane32_swap(d, d, false, false); d = pkmax(q[0], q[1]); }
    return d; }
__device__ __forceinline__ int wave_max(int d) {
    d = max(d, __builtin_amdgcn_update_dpp(d, d, 0xB1, 0xF, 0xF, false)); d = max(d, __builtin_amdgcn_update_dpp(d, d, 0x4E, 0xF, 0xF, false));
    d = max(d, __builtin_amdgcn_update_dpp(d, d, 0x124, 0xF, 0xF, false)); d = max(d, __builtin_amdgcn_update_dpp(d, d, 0x128, 0xF, 0xF, false));
    { auto q = __builtin_amdgcn_permlane16_swap((unsigned)d, (unsigned)d, false, false); d = max((int)q[0], (int)q[1]); }
    { auto q = __builtin_amdgcn_permlane32_swap((unsigned)d, (unsigned)d, false, false); d = max((int)q[0], (int)q[1]); }
    return d; }

__device__ __forceinline__ v4u mk_srd(const void* p, unsigned bytes) { const size_t a = (size_t)p; v4u d;
    d.x = __builtin_amdgcn_readfirstlane((unsigned)a); d.y = __builtin_amdgcn_readfirstlane((unsigned)(a >> 32) & 0xFFFFu); d.z = __builtin_amdgcn_readfirstlane(bytes); d.w = 0x00020000u; return d; }
__device__ __forceinline__ bf16x8 ldsa(unsigned addr) { return *(const __attribute__((address_space(3))) bf16x8*)(size_t)addr; }
constexpr unsigned HX_BYTES = 2097152, OOB = 0xF0000000u;
constexpr unsigned F_RDY0 = W_FLG + 34304;
constexpr int L_KV = 90112, L_MS = 139264, L_IMG = 155648, IMG_PS = 1056, IMG_MS = 2112, LB_R = 8192, LB_STG = 16384, LB_STGP = 8704;
static_assert(F_KVQ == 0 && F_ERR == 33024 && F_CAL == 33792 && F_RDY0 == 34304 && F_PROG == 49152 && W_QX == 12771328 && W_KX == 29548544 && W_OX == 63102976 && W_VF == 46325760 && W_DBG == 113434624 && L_KV == 90112 && L_MS == 139264 && L_IMG == 155648 && IMG_PS == 1056 && IMG_MS == 2112 && LB_R == 8192 && LB_STG == 16384 && LB_STGP == 8704, "constants of the generated code");
#define REC_S_IN "{s[8:11]}"(sh), "{s[12:15]}"(sw), "{s[16:19]}"(s1), "{s[20:23]}"(s2), "{s29}"(D1), "{s30}"(D2), "{s43}"(w), "{s44}"(id), "{s49}"(rb), "{s50}"(PEN), "{s51}"(LT_A)
__device__ __forceinline__ void rec_a(const Params& p, unsigned lb, const int wv, const int id, const int cp) {
    const int w = wv, l = otid() & 63, n = l & 15, g = l >> 4, h = id, rb = id;
    const v4u sh = mk_srd(p.ws + hx_base(p.ws), HX_BYTES - 8192), sw = mk_srd(p.ws, W_TOTAL), s1 = mk_srd(w < 2 ? p.Wq : p.Wk, 1024 * 4096 * 2), s2 = s1;
    const int r8 = l >> 3, pc = l & 7, pp = n >> 3, bb = (n >> 2) & 1, f = (g & 1) | (pp << 1);
    const unsigned stg = lb + 131072 + w * 8192;
    const unsigned g_w = r8 * 8192 + h * 256 + (w & 1) * 128 + pc * 16;
    const unsigned wb0 = stg + r8 * 128 + (((pc >> 1) ^ (r8 >> 2)) << 5) + (pc & 1) * 16, wb1 = wb0 ^ 64u;
    const unsigned rb0 = stg + (16 * (g >> 1) + 8 * pp + 4 * (g & 1) + bb) * 128 + (f << 5) + (n & 3) * 8;
    const unsigned a_mem = cp * 32768 + (w >> 1) * 16384 + (w & 1) * 4096 + (g >> 1) * 512 + ((n & 7) >> 2) * 256 + ((g & 1) * 2 + pp) * 64 + (n & 3) * 16;
    const unsigned l_ms = lb + L_MS + l * 16;
    const unsigned l_iw = lb + L_IMG + (w >> 1) * IMG_MS + (g & 1) * IMG_PS + (64 * (w & 1) + 16 * (g >> 1) + n) * 8;
    const unsigned l_iq = lb + L_IMG + (n & 1) * IMG_PS + (8 * g + (n >> 2)) * 8;
    const unsigned a_vm = (16 + (h >> 2)) * 16384 + (h & 3) * 2048 + w * 512 + n * 32 + (g & 1) * 16;
    const unsigned a_o = n < 8 ? (8 + (n >> 2) * 4 + (h >> 3)) * 16384 + (h & 7) * 1024 + w * 256 + g * 64 + (n & 3) * 16 : OOB;
    const unsigned a_ox = (((w & 1) * 32 + h) * 4 + w) * 1024 + l * 16;
    asm volatile(
        "s_lshl_b32 s34, s43, 2\n\t"
        "s_add_u32 s34, s34, 0\n\t"
        "s_and_b32 s34, s34, 15\n\t"
        "s_lshl_b32 s34, s34, 19\n\t"
        "buffer_load_dwordx4 v[224:227], v96, s[16:19], s34 offen nt\n\t"
        "s_add_u32 s34, s34, 0x10000\n\t"
        "buffer_load_dwordx4 v[228:231], v96, s[16:19], s34 offen nt\n\t"
        "s_add_u32 s34, s34, 0x10000\n\t"
        "buffer_load_dwordx4 v[232:235], v96, s[16:19], s34 offen nt\n\t"
        "s_add_u32 s34, s34, 0x10000\n\t"
        "buffer_load_dwordx4 v[236:239], v96, s[16:19], s34 offen nt\n\t"
        "s_add_u32 s34, s34, 0x10000\n\t"
        "buffer_load_dwordx4 v[240:243], v96, s[16:19], s34 offen nt\n\t"
        "s_add_u32 s34, s34, 0x10000\n\t"
        "buffer_load_dwordx4 v[244:247], v96, s[16:19], s34 offen nt\n\t"
        "s_add_u32 s34, s34, 0x10000\n\t"
        "buffer_load_dwordx4 v[248:251], v96, s[16:19], s34 offen nt\n\t"
        "s_add_u32 s34, s34, 0x10000\n\t"
        "buffer_load_dwordx4 v[252:255], v96, s[16:19], s34 offen nt\n\t"
        "s_lshl_b32 s34, s43, 2\n\t"
        "s_add_u32 s34, s34, 1\n\t"
        "s_and_b32 s34, s34, 15\n\t"
        "s_lshl_b32 s34, s34, 19\n\t"
        "buffer_load_dwordx4 v[0:3], v96, s[16:19], s34 offen nt\n\t"
        "s_add_u32 s34, s34, 0x10000\n\t"
        "buffer_load_dwordx4 v[4:7], v96, s[16:19], s34 offen nt\n\t"
        "s_add_u32 s34, s34, 0x10000\n\t"
        "buffer_load_dwordx4 v[8:11], v96, s[16:19], s34 offen nt\n\t"
        "s_add_u32 s34, s34, 0x10000\n\t"
        "buffer_load_dwordx4 v[12:15], v96, s[16:19], s34 offen nt\n\t"
        "s_add_u32 s34, s34, 0x10000\n\t"
        "buffer_load_dwordx4 v[16:19], v96, s[16:19], s34 offen nt\n\t"
        "s_add_u32 s34, s34, 0x10000\n\t"
        "buffer_load_dwordx4 v[20:23], v96, s[16:19], s34 offen nt\n\t"
        "s_add_u32 s34, s34, 0x10000\n\t"
        "buffer_load_dwordx4 v[24:27], v96, s[16:19], s34 offen nt\n\t"
        "s_add_u32 s34, s34, 0x10000\n\t"
        "buffer_load_dwordx4 v[28:31], v96, s[16:19], s34 offen nt\n\t"
        "s_lshl_b32 s34, s43, 2\n\t"
        "s_add_u32 s34, s34, 2\n\t"
        "s_and_b32 s34, s34, 15\n\t"
        "s_lshl_b32 s34, s34, 19\n\t"
        "buffer_load_dwordx4 v[32:35], v96, s[16:19], s34 offen nt\n\t"
        "s_add_u32 s34, s34, 0x10000\n\t"
        "buffer_load_dwordx4 v[36:39], v96, s[16:19], s34 offen nt\n\t"
        "s_add_u32 s34, s34, 0x10000\n\t"
        "buffer_load_dwordx4 v[40:43], v96, s[16:19], s34 offen nt\n\t"
        "s_add_u32 s34, s34, 0x10000\n\t"
        "buffer_load_dwordx4 v[44:47], v96, s[16:19], s34 offen nt\n\t"
        "s_add_u32 s34, s34, 0x10000\n\t"
        "buffer_load_dwordx4 v[48:51], v96, s[16:19], s34 offen nt\n\t"
        "s_add_u32 s34, s34, 0x10000\n\t"
        "buffer_load_dwordx4 v[52:55], v96, s[16:19], s34 offen nt\n\t"
        "s_add_u32 s34, s34, 0x10000\n\t"
        "buffer_load_dwordx4 v[56:59], v96, s[16:19], s34 offen nt\n\t"
        "s_add_u32 s34, s34, 0x10000\n\t"
        "buffer_load_dwordx4 v[60:63], v96, s[16:19], s34 offen nt\n\t"
        "s_lshl_b32 s34, s43, 2\n\t"
        "s_add_u32 s34, s34, 3\n\t"
        "s_and_b32 s34, s34, 15\n\t"
        "s_lshl_b32 s34, s34, 19\n\t"
        "buffer_load_dwordx4 v[64:67], v96, s[16:19], s34 offen nt\n\t"
        "s_add_u32 s34, s34, 0x10000\n\t"
        "buffer_load_dwordx4 v[68:71], v96, s[16:19], s34 offen nt\n\t"
        "s_add_u32 s34, s34, 0x10000\n\t"
        "buffer_load_dwordx4 v[72:75], v96, s[16:19], s34 offen nt\n\t"
        "s_add_u32 s34, s34, 0x10000\n\t"
        "buffer_load_dwordx4 v[76:79], v96, s[16:19], s34 offen nt\n\t"
        "s_add_u32 s34, s34, 0x10000\n\t"
        "buffer_load_dwordx4 v[80:83], v96, s[16:19], s34 offen nt\n\t"
        "s_add_u32 s34, s34, 0x10000\n\t"
        "buffer_load_dwordx4 v[84:87], v96, s[16:19], s34 offen nt\n\t"
        "s_add_u32 s34, s34, 0x10000\n\t"
        "buffer_load_dwordx4 v[88:91], v96, s[16:19], s34 offen nt\n\t"
        "s_add_u32 s34, s34, 0x10000\n\t"
        "buffer_load_dwordx4 v[92:95], v96, s[16:19], s34 offen nt\n\t"
        "s_lshl_b32 s34, s43, 2\n\t"
        "s_add_u32 s34, s34, 4\n\t"
        "s_and_b32 s34, s34, 15\n\t"
        "s_lshl_b32 s34, s34, 19\n\t"
        "buffer_load_dwordx4 a[96:99], v96, s[16:19], s34 offen nt\n\t"
        "s_add_u32 s34, s34, 0x10000\n\t"
        "buffer_load_dwordx4 a[100:103], v96, s[16:19], s34 offen nt\n\t"
        "s_add_u32 s34, s34, 0x10000\n\t"
        "buffer_load_dwordx4 a[104:107], v96, s[16:19], s34 offen nt\n\t"
        "s_add_u32 s34, s34, 0x10000\n\t"
        "buffer_load_dwordx4 a[108:111], v96, s[16:19], s34 offen nt\n\t"
        "s_add_u32 s34, s34, 0x10000\n\t"
        "buffer_load_dwordx4 a[112:115], v96, s[16:19], s34 offen nt\n\t"
        "s_add_u32 s34, s34, 0x10000\n\t"
        "buffer_load_dwordx4 a[116:119], v96, s[16:19], s34 offen nt\n\t"
        "s_add_u32 s34, s34, 0x10000\n\t"
        "buffer_load_dwordx4 a[120:123], v96, s[16:19], s34 offen nt\n\t"
        "s_add_u32 s34, s34, 0x10000\n\t"
        "buffer_load_dwordx4 a[124:127], v96, s[16:19], s34 offen nt\n\t"
        "s_lshl_b32 s34, s43, 2\n\t"
        "s_add_u32 s34, s34, 5\n\t"
        "s_and_b32 s34, s34, 15\n\t"
        "s_lshl_b32 s34, s34, 19\n\t"
        "buffer_load_dwordx4 a[128:131], v96, s[16:19], s34 offen nt\n\t"
        "s_add_u32 s34, s34, 0x10000\n\t"
        "buffer_load_dwordx4 a[132:135], v96, s[16:19], s34 offen nt\n\t"
        "s_add_u32 s34, s34, 0x10000\n\t"
        "buffer_load_dwordx4 a[136:139], v96, s[16:19], s34 offen nt\n\t"
        "s_add_u32 s34, s34, 0x10000\n\t"
        "buffer_load_dwordx4 a[140:143], v96, s[16:19], s34 offen nt\n\t"
        "s_add_u32 s34, s34, 0x10000\n\t"
        "buffer_load_dwordx4 a[144:147], v96, s[16:19], s34 offen nt\n\t"
        "s_add_u32 s34, s34, 0x10000\n\t"
        "buffer_load_dwordx4 a[148:151], v96, s[16:19], s34 offen nt\n\t"
        "s_add_u32 s34, s34, 0x10000\n\t"
        "buffer_load_dwordx4 a[152:155], v96, s[16:19], s34 offen nt\n\t"
        "s_add_u32 s34, s34, 0x10000\n\t"
        "buffer_load_dwordx4 a[156:159], v96, s[16:19], s34 offen nt\n\t"
        "s_lshl_b32 s34, s43, 2\n\t"
        "s_add_u32 s34, s34, 6\n\t"
        "s_and_b32 s34, s34, 15\n\t"
        "s_lshl_b32 s34, s34, 19\n\t"
        "buffer_load_dwordx4 a[160:163], v96, s[16:19], s34 offen nt\n\t"
        "s_add_u32 s34, s34, 0x10000\n\t"
        "buffer_load_dwordx4 a[164:167], v96, s[16:19], s34 offen nt\n\t"
        "s_add_u32 s34, s34, 0x10000\n\t"
        "buffer_load_dwordx4 a[168:171], v96, s[16:19], s34 offen nt\n\t"
        "s_add_u32 s34, s34, 0x10000\n\t"
        "buffer_load_dwordx4 a[172:175], v96, s[16:19], s34 offen nt\n\t"
        "s_add_u32 s34, s34, 0x10000\n\t"
        "buffer_load_dwordx4 a[176:179], v96, s[16:19], s34 offen nt\n\t"
        "s_add_u32 s34, s34, 0x10000\n\t"
        "buffer_load_dwordx4 a[180:183], v96, s[16:19], s34 offen nt\n\t"
        "s_add_u32 s34, s34, 0x10000\n\t"
        "buffer_load_dwordx4 a[184:187], v96, s[16:19], s34 offen nt\n\t"
        "s_add_u32 s34, s34, 0x10000\n\t"
        "buffer_load_dwordx4 a[188:191], v96, s[16:19], s34 offen nt\n\t"
        "v_cmp_le_i32_e64 s[60:61], 0, v126\n\t"
        "v_cmp_le_i32_e64 s[62:63], 1, v126\n\t"
        "v_cmp_le_i32_e64 s[64:65], 2, v126\n\t"
        "v_cmp_le_i32_e64 s[66:67], 3, v126\n\t"
        "v_cmp_le_i32_e64 s[68:69], 16, v126\n\t"
        "v_cmp_le_i32_e64 s[70:71], 17, v126\n\t"
        "v_cmp_le_i32_e64 s[72:73], 18, v126\n\t"
        "v_cmp_le_i32_e64 s[74:75], 19, v126\n\t"
        "s_mul_i32 s35, s43, 10240\n\t"
        "v_add_u32_e32 v126, s35, v124\n\t"
        "s_waitcnt vmcnt(48)\n\t"
        "ds_write_b128 v97, v[224:227] offset:0\n\t"
        "ds_write_b128 v98, v[228:231] offset:1024\n\t"
        "ds_write_b128 v97, v[232:235] offset:2048\n\t"
        "ds_write_b128 v98, v[236:239] offset:3072\n\t"
        "ds_write_b128 v97, v[240:243] offset:4096\n\t"
        "ds_write_b128 v98, v[244:247] offset:5120\n\t"
        "ds_write_b128 v97, v[248:251] offset:6144\n\t"
        "ds_write_b128 v98, v[252:255] offset:7168\n\t"
        "ds_read_b64_tr_b16 a[0:1], v99 offset:0\n\t"
        "ds_read_b64_tr_b16 a[2:3], v99 offset:256\n\t"
        "ds_read_b64_tr_b16 a[4:5], v99 offset:4096\n\t"
        "ds_read_b64_tr_b16 a[6:7], v99 offset:4352\n\t"
        "s_waitcnt lgkmcnt(8)\n\t"
        "ds_read_b64_tr_b16 a[8:9], v100 offset:0\n\t"
        "ds_read_b64_tr_b16 a[10:11], v100 offset:256\n\t"
        "ds_read_b64_tr_b16 a[12:13], v100 offset:4096\n\t"
        "ds_read_b64_tr_b16 a[14:15], v100 offset:4352\n\t"
        "s_waitcnt lgkmcnt(8)\n\t"
        "ds_read_b64_tr_b16 a[16:17], v101 offset:0\n\t"
        "ds_read_b64_tr_b16 a[18:19], v101 offset:256\n\t"
        "ds_read_b64_tr_b16 a[20:21], v101 offset:4096\n\t"
        "ds_read_b64_tr_b16 a[22:23], v101 offset:4352\n\t"
        "s_waitcnt lgkmcnt(8)\n\t"
        "ds_read_b64_tr_b16 v[104:105], v102 offset:0\n\t"
        "ds_read_b64_tr_b16 v[106:107], v102 offset:256\n\t"
        "ds_read_b64_tr_b16 v[108:109], v102 offset:4096\n\t"
        "ds_read_b64_tr_b16 v[110:111], v102 offset:4352\n\t"
        "s_waitcnt lgkmcnt(0)\n\t"
        "ds_write_b128 v117, v[104:107] offset:0\n\t"
        "ds_write_b128 v117, v[108:111] offset:1024\n\t"
        "s_lshl_b32 s34, s43, 2\n\t"
        "s_add_u32 s34, s34, 7\n\t"
        "s_and_b32 s34, s34, 15\n\t"
        "s_lshl_b32 s34, s34, 19\n\t"
        "buffer_load_dwordx4 a[192:195], v96, s[16:19], s34 offen nt\n\t"
        "s_add_u32 s34, s34, 0x10000\n\t"
        "buffer_load_dwordx4 a[196:199], v96, s[16:19], s34 offen nt\n\t"
        "s_add_u32 s34, s34, 0x10000\n\t"
        "buffer_load_dwordx4 a[200:203], v96, s[16:19], s34 offen nt\n\t"
        "s_add_u32 s34, s34, 0x10000\n\t"
        "buffer_load_dwordx4 a[204:207], v96, s[16:19], s34 offen nt\n\t"
        "s_add_u32 s34, s34, 0x10000\n\t"
        "buffer_load_dwordx4 a[208:211], v96, s[16:19], s34 offen nt\n\t"
        "s_add_u32 s34, s34, 0x10000\n\t"
        "buffer_load_dwordx4 a[212:215], v96, s[16:19], s34 offen nt\n\t"
        "s_add_u32 s34, s34, 0x10000\n\t"
        "buffer_load_dwordx4 a[216:219], v96, s[16:19], s34 offen nt\n\t"
        "s_add_u32 s34, s34, 0x10000\n\t"
        "buffer_load_dwordx4 a[220:223], v96, s[16:19], s34 offen nt\n\t"
        "s_waitcnt vmcnt(48)\n\t"
        "ds_write_b128 v97, v[0:3] offset:0\n\t"
        "ds_write_b128 v98, v[4:7] offset:1024\n\t"
        "ds_write_b128 v97, v[8:11] offset:2048\n\t"
        "ds_write_b128 v98, v[12:15] offset:3072\n\t"
        "ds_write_b128 v97, v[16:19] offset:4096\n\t"
        "ds_write_b128 v98, v[20:23] offset:5120\n\t"
        "ds_write_b128 v97, v[24:27] offset:6144\n\t"
        "ds_write_b128 v98, v[28:31] offset:7168\n\t"
        "ds_read_b64_tr_b16 a[24:25], v99 offset:0\n\t"
        "ds_read_b64_tr_b16 a[26:27], v99 offset:256\n\t"
        "ds_read_b64_tr_b16 a[28:29], v99 offset:4096\n\t"
        "ds_read_b64_tr_b16 a[30:31], v99 offset:4352\n\t"
        "s_waitcnt lgkmcnt(8)\n\t"
        "ds_read_b64_tr_b16 a[32:33], v100 offset:0\n\t"
        "ds_read_b64_tr_b16 a[34:35], v100 offset:256\n\t"
        "ds_read_b64_tr_b16 a[36:37], v100 offset:4096\n\t"
        "ds_read_b64_tr_b16 a[38:39], v100 offset:4352\n\t"
        "s_waitcnt lgkmcnt(8)\n\t"
        "ds_read_b64_tr_b16 a[40:41], v101 offset:0\n\t"
        "ds_read_b64_tr_b16 a[42:43], v101 offset:256\n\t"
        "ds_read_b64_tr_b16 a[44:45], v101 offset:4096\n\t"
        "ds_read_b64_tr_b16 a[46:47], v101 offset:4352\n\t"
        "s_waitcnt lgkmcnt(8)\n\t"
        "ds_read_b64_tr_b16 v[104:105], v102 offset:0\n\t"
        "ds_read_b64_tr_b16 v[106:107], v102 offset:256\n\t"
        "ds_read_b64_tr_b16 v[108:109], v102 offset:4096\n\t"
        "ds_read_b64_tr_b16 v[110:111], v102 offset:4352\n\t"
        "s_waitcnt lgkmcnt(0)\n\t"
        "ds_write_b128 v117, v[104:107] offset:2048\n\t"
        "ds_write_b128 v117, v[108:111] offset:3072\n\t"
        "s_lshl_b32 s34, s43, 2\n\t"
        "s_add_u32 s34, s34, 8\n\t"
        "s_and_b32 s34, s34, 15\n\t"
        "s_lshl_b32 s34, s34, 19\n\t"
        "buffer_load_dwordx4 a[224:227], v96, s[16:19], s34 offen nt\n\t"
        "s_add_u32 s34, s34, 0x10000\n\t"
        "buffer_load_dwordx4 a[228:231], v96, s[16:19], s34 offen nt\n\t"
        "s_add_u32 s34, s34, 0x10000\n\t"
        "buffer_load_dwordx4 a[232:235], v96, s[16:19], s34 offen nt\n\t"
        "s_add_u32 s34, s34, 0x10000\n\t"
        "buffer_load_dwordx4 a[236:239], v96, s[16:19], s34 offen nt\n\t"
        "s_add_u32 s34, s34, 0x10000\n\t"
        "buffer_load_dwordx4 a[240:243], v96, s[16:19], s34 offen nt\n\t"
        "s_add_u32 s34, s34, 0x10000\n\t"
        "buffer_load_dwordx4 a[244:247], v96, s[16:19], s34 offen nt\n\t"
        "s_add_u32 s34, s34, 0x10000\n\t"
        "buffer_load_dwordx4 a[248:251], v96, s[16:19], s34 offen nt\n\t"
        "s_add_u32 s34, s34, 0x10000\n\t"
        "buffer_load_dwordx4 a[252:255], v96, s[16:19], s34 offen nt\n\t"
        "s_waitcnt vmcnt(48)\n\t"
        "ds_write_b128 v97, v[32:35] offset:0\n\t"
        "ds_write_b128 v98, v[36:39] offset:1024\n\t"
        "ds_write_b128 v97, v[40:43] offset:2048\n\t"
        "ds_write_b128 v98, v[44:47] offset:3072\n\t"
        "ds_write_b128 v97, v[48:51] offset:4096\n\t"
        "ds_write_b128 v98, v[52:55] offset:5120\n\t"
        "ds_write_b128 v97, v[56:59] offset:6144\n\t"
        "ds_write_b128 v98, v[60:63] offset:7168\n\t"
        "ds_read_b64_tr_b16 a[48:49], v99 offset:0\n\t"
        "ds_read_b64_tr_b16 a[50:51], v99 offset:256\n\t"
        "ds_read_b64_tr_b16 a[52:53], v99 offset:4096\n\t"
        "ds_read_b64_tr_b16 a[54:55], v99 offset:4352\n\t"
        "s_waitcnt lgkmcnt(8)\n\t"
        "ds_read_b64_tr_b16 a[56:57], v100 offset:0\n\t"
        "ds_read_b64_tr_b16 a[58:59], v100 offset:256\n\t"
        "ds_read_b64_tr_b16 a[60:61], v100 offset:4096\n\t"
        "ds_read_b64_tr_b16 a[62:63], v100 offset:4352\n\t"
        "s_waitcnt lgkmcnt(8)\n\t"
        "ds_read_b64_tr_b16 a[64:65], v101 offset:0\n\t"
        "ds_read_b64_tr_b16 a[66:67], v101 offset:256\n\t"
        "ds_read_b64_tr_b16 a[68:69], v101 offset:4096\n\t"
        "ds_read_b64_tr_b16 a[70:71], v101 offset:4352\n\t"
        "s_waitcnt lgkmcnt(8)\n\t"
        "ds_read_b64_tr_b16 v[104:105], v102 offset:0\n\t"
        "ds_read_b64_tr_b16 v[106:107], v102 offset:256\n\t"
        "ds_read_b64_tr_b16 v[108:109], v102 offset:4096\n\t"
        "ds_read_b64_tr_b16 v[110:111], v102 offset:4352\n\t"
        "s_waitcnt lgkmcnt(0)\n\t"
        "ds_write_b128 v117, v[104:107] offset:4096\n\t"
        "ds_write_b128 v117, v[108:111] offset:5120\n\t"
        "s_lshl_b32 s34, s43, 2\n\t"
        "s_add_u32 s34, s34, 9\n\t"
        "s_and_b32 s34, s34, 15\n\t"
        "s_lshl_b32 s34, s34, 19\n\t"
        "buffer_load_dwordx4 v[128:131], v96, s[16:19], s34 offen nt\n\t"
        "s_add_u32 s34, s34, 0x10000\n\t"
        "buffer_load_dwordx4 v[132:135], v96, s[16:19], s34 offen nt\n\t"
        "s_add_u32 s34, s34, 0x10000\n\t"
        "buffer_load_dwordx4 v[136:139], v96, s[16:19], s34 offen nt\n\t"
        "s_add_u32 s34, s34, 0x10000\n\t"
        "buffer_load_dwordx4 v[140:143], v96, s[16:19], s34 offen nt\n\t"
        "s_add_u32 s34, s34, 0x10000\n\t"
        "buffer_load_dwordx4 v[144:147], v96, s[16:19], s34 offen nt\n\t"
        "s_add_u32 s34, s34, 0x10000\n\t"
        "buffer_load_dwordx4 v[148:151], v96, s[16:19], s34 offen nt\n\t"
        "s_add_u32 s34, s34, 0x10000\n\t"
        "buffer_load_dwordx4 v[152:155], v96, s[16:19], s34 offen nt\n\t"
        "s_add_u32 s34, s34, 0x10000\n\t"
        "buffer_load_dwordx4 v[156:159], v96, s[16:19], s34 offen nt\n\t"
        "s_waitcnt vmcnt(48)\n\t"
        "ds_write_b128 v97, v[64:67] offset:0\n\t"
        "ds_write_b128 v98, v[68:71] offset:1024\n\t"
        "ds_write_b128 v97, v[72:75] offset:2048\n\t"
        "ds_write_b128 v98, v[76:79] offset:3072\n\t"
        "ds_write_b128 v97, v[80:83] offset:4096\n\t"
        "ds_write_b128 v98, v[84:87] offset:5120\n\t"
        "ds_write_b128 v97, v[88:91] offset:6144\n\t"
        "ds_write_b128 v98, v[92:95] offset:7168\n\t"
        "ds_read_b64_tr_b16 a[72:73], v99 offset:0\n\t"
        "ds_read_b64_tr_b16 a[74:75], v99 offset:256\n\t"
        "ds_read_b64_tr_b16 a[76:77], v99 offset:4096\n\t"
        "ds_read_b64_tr_b16 a[78:79], v99 offset:4352\n\t"
        "s_waitcnt lgkmcnt(8)\n\t"
        "ds_read_b64_tr_b16 a[80:81], v100 offset:0\n\t"
        "ds_read_b64_tr_b16 a[82:83], v100 offset:256\n\t"
        "ds_read_b64_tr_b16 a[84:85], v100 offset:4096\n\t"
        "ds_read_b64_tr_b16 a[86:87], v100 offset:4352\n\t"
        "s_waitcnt lgkmcnt(8)\n\t"
        "ds_read_b64_tr_b16 a[88:89], v101 offset:0\n\t"
        "ds_read_b64_tr_b16 a[90:91], v101 offset:256\n\t"
        "ds_read_b64_tr_b16 a[92:93], v101 offset:4096\n\t"
        "ds_read_b64_tr_b16 a[94:95], v101 offset:4352\n\t"
        "s_waitcnt lgkmcnt(8)\n\t"
        "ds_read_b64_tr_b16 v[104:105], v102 offset:0\n\t"
        "ds_read_b64_tr_b16 v[106:107], v102 offset:256\n\t"
        "ds_read_b64_tr_b16 v[108:109], v102 offset:4096\n\t"
        "ds_read_b64_tr_b16 v[110:111], v102 offset:4352\n\t"
        "s_waitcnt lgkmcnt(0)\n\t"
        "ds_write_b128 v117, v[104:107] offset:6144\n\t"
        "ds_write_b128 v117, v[108:111] offset:7168\n\t"
        "s_lshl_b32 s34, s43, 2\n\t"
        "s_add_u32 s34, s34, 10\n\t"
        "s_and_b32 s34, s34, 15\n\t"
        "s_lshl_b32 s34, s34, 19\n\t"
        "buffer_load_dwordx4 v[160:163], v96, s[16:19], s34 offen nt\n\t"
        "s_add_u32 s34, s34, 0x10000\n\t"
        "buffer_load_dwordx4 v[164:167], v96, s[16:19], s34 offen nt\n\t"
        "s_add_u32 s34, s34, 0x10000\n\t"
        "buffer_load_dwordx4 v[168:171], v96, s[16:19], s34 offen nt\n\t"
        "s_add_u32 s34, s34, 0x10000\n\t"
        "buffer_load_dwordx4 v[172:175], v96, s[16:19], s34 offen nt\n\t"
        "s_add_u32 s34, s34, 0x10000\n\t"
        "buffer_load_dwordx4 v[176:179], v96, s[16:19], s34 offen nt\n\t"
        "s_add_u32 s34, s34, 0x10000\n\t"
        "buffer_load_dwordx4 v[180:183], v96, s[16:19], s34 offen nt\n\t"
        "s_add_u32 s34, s34, 0x10000\n\t"
        "buffer_load_dwordx4 v[184:187], v96, s[16:19], s34 offen nt\n\t"
        "s_add_u32 s34, s34, 0x10000\n\t"
        "buffer_load_dwordx4 v[188:191], v96, s[16:19], s34 offen nt\n\t"
        "s_waitcnt vmcnt(48)\n\t"
        "ds_write_b128 v97, a[96:99] offset:0\n\t"
        "ds_write_b128 v98, a[100:103] offset:1024\n\t"
        "ds_write_b128 v97, a[104:107] offset:2048\n\t"
        "ds_write_b128 v98, a[108:111] offset:3072\n\t"
        "ds_write_b128 v97, a[112:115] offset:4096\n\t"
        "ds_write_b128 v98, a[116:119] offset:5120\n\t"
        "ds_write_b128 v97, a[120:123] offset:6144\n\t"
        "ds_write_b128 v98, a[124:127] offset:7168\n\t"
        "ds_read_b64_tr_b16 a[96:97], v99 offset:0\n\t"
        "ds_read_b64_tr_b16 a[98:99], v99 offset:256\n\t"
        "ds_read_b64_tr_b16 a[100:101], v99 offset:4096\n\t"
        "ds_read_b64_tr_b16 a[102:103], v99 offset:4352\n\t"
        "s_waitcnt lgkmcnt(8)\n\t"
        "ds_read_b64_tr_b16 a[104:105], v100 offset:0\n\t"
        "ds_read_b64_tr_b16 a[106:107], v100 offset:256\n\t"
        "ds_read_b64_tr_b16 a[108:109], v100 offset:4096\n\t"
        "ds_read_b64_tr_b16 a[110:111], v100 offset:4352\n\t"
        "s_waitcnt lgkmcnt(8)\n\t"
        "ds_read_b64_tr_b16 a[112:113], v101 offset:0\n\t"
        "ds_read_b64_tr_b16 a[114:115], v101 offset:256\n\t"
        "ds_read_b64_tr_b16 a[116:117], v101 offset:4096\n\t"
        "ds_read_b64_tr_b16 a[118:119], v101 offset:4352\n\t"
        "s_waitcnt lgkmcnt(8)\n\t"
        "ds_read_b64_tr_b16 v[104:105], v102 offset:0\n\t"
        "ds_read_b64_tr_b16 v[106:107], v102 offset:256\n\t"
        "ds_read_b64_tr_b16 v[108:109], v102 offset:4096\n\t"
        "ds_read_b64_tr_b16 v[110:111], v102 offset:4352\n\t"
        "s_waitcnt lgkmcnt(0)\n\t"
        "ds_write_b128 v117, v[104:107] offset:8192\n\t"
        "ds_write_b128 v117, v[108:111] offset:9216\n\t"
        "s_lshl_b32 s34, s43, 2\n\t"
        "s_add_u32 s34, s34, 11\n\t"
        "s_and_b32 s34, s34, 15\n\t"
        "s_lshl_b32 s34, s34, 19\n\t"
        "buffer_load_dwordx4 v[192:195], v96, s[16:19], s34 offen nt\n\t"
        "s_add_u32 s34, s34, 0x10000\n\t"
        "buffer_load_dwordx4 v[196:199], v96, s[16:19], s34 offen nt\n\t"
        "s_add_u32 s34, s34, 0x10000\n\t"
        "buffer_load_dwordx4 v[200:203], v96, s[16:19], s34 offen nt\n\t"
        "s_add_u32 s34, s34, 0x10000\n\t"
        "buffer_load_dwordx4 v[204:207], v96, s[16:19], s34 offen nt\n\t"
        "s_add_u32 s34, s34, 0x10000\n\t"
        "buffer_load_dwordx4 v[208:211], v96, s[16:19], s34 offen nt\n\t"
        "s_add_u32 s34, s34, 0x10000\n\t"
        "buffer_load_dwordx4 v[212:215], v96, s[16:19], s34 offen nt\n\t"
        "s_add_u32 s34, s34, 0x10000\n\t"
        "buffer_load_dwordx4 v[216:219], v96, s[16:19], s34 offen nt\n\t"
        "s_add_u32 s34, s34, 0x10000\n\t"
        "buffer_load_dwordx4 v[220:223], v96, s[16:19], s34 offen nt\n\t"
        "s_waitcnt vmcnt(48)\n\t"
        "ds_write_b128 v97, a[128:131] offset:0\n\t"
        "ds_write_b128 v98, a[132:135] offset:1024\n\t"
        "ds_write_b128 v97, a[136:139] offset:2048\n\t"
        "ds_write_b128 v98, a[140:143] offset:3072\n\t"
        "ds_write_b128 v97, a[144:147] offset:4096\n\t"
        "ds_write_b128 v98, a[148:151] offset:5120\n\t"
        "ds_write_b128 v97, a[152:155] offset:6144\n\t"
        "ds_write_b128 v98, a[156:159] offset:7168\n\t"
        "ds_read_b64_tr_b16 a[120:121], v99 offset:0\n\t"
        "ds_read_b64_tr_b16 a[122:123], v99 offset:256\n\t"
        "ds_read_b64_tr_b16 a[124:125], v99 offset:4096\n\t"
        "ds_read_b64_tr_b16 a[126:127], v99 offset:4352\n\t"
        "s_waitcnt lgkmcnt(8)\n\t"
        "ds_read_b64_tr_b16 a[128:129], v100 offset:0\n\t"
        "ds_read_b64_tr_b16 a[130:131], v100 offset:256\n\t"
        "ds_read_b64_tr_b16 a[132:133], v100 offset:4096\n\t"
        "ds_read_b64_tr_b16 a[134:135], v100 offset:4352\n\t"
        "s_waitcnt lgkmcnt(8)\n\t"
        "ds_read_b64_tr_b16 a[136:137], v101 offset:0\n\t"
        "ds_read_b64_tr_b16 a[138:139], v101 offset:256\n\t"
        "ds_read_b64_tr_b16 a[140:141], v101 offset:4096\n\t"
        "ds_read_b64_tr_b16 a[142:143], v101 offset:4352\n\t"
        "s_waitcnt lgkmcnt(8)\n\t"
        "ds_read_b64_tr_b16 v[104:105], v102 offset:0\n\t"
        "ds_read_b64_tr_b16 v[106:107], v102 offset:256\n\t"
        "ds_read_b64_tr_b16 v[108:109], v102 offset:4096\n\t"
        "ds_read_b64_tr_b16 v[110:111], v102 offset:4352\n\t"
        "s_waitcnt lgkmcnt(0)\n\t"
        "ds_write_b128 v117, v[104:107] offset:10240\n\t"
        "ds_write_b128 v117, v[108:111] offset:11264\n\t"
        "s_lshl_b32 s34, s43, 2\n\t"
        "s_add_u32 s34, s34, 12\n\t"
        "s_and_b32 s34, s34, 15\n\t"
        "s_lshl_b32 s34, s34, 19\n\t"
        "buffer_load_dwordx4 v[224:227], v96, s[16:19], s34 offen nt\n\t"
        "s_add_u32 s34, s34, 0x10000\n\t"
        "buffer_load_dwordx4 v[228:231], v96, s[16:19], s34 offen nt\n\t"
        "s_add_u32 s34, s34, 0x10000\n\t"
        "buffer_load_dwordx4 v[232:235], v96, s[16:19], s34 offen nt\n\t"
        "s_add_u32 s34, s34, 0x10000\n\t"
        "buffer_load_dwordx4 v[236:239], v96, s[16:19], s34 offen nt\n\t"
        "s_add_u32 s34, s34, 0x10000\n\t"
        "buffer_load_dwordx4 v[240:243], v96, s[16:19], s34 offen nt\n\t"
        "s_add_u32 s34, s34, 0x10000\n\t"
        "buffer_load_dwordx4 v[244:247], v96, s[16:19], s34 offen nt\n\t"
        "s_add_u32 s34, s34, 0x10000\n\t"
        "buffer_load_dwordx4 v[248:251], v96, s[16:19], s34 offen nt\n\t"
        "s_add_u32 s34, s34, 0x10000\n\t"
        "buffer_load_dwordx4 v[252:255], v96, s[16:19], s34 offen nt\n\t"
        "s_waitcnt vmcnt(48)\n\t"
        "ds_write_b128 v97, a[160:163] offset:0\n\t"
        "ds_write_b128 v98, a[164:167] offset:1024\n\t"
        "ds_write_b128 v97, a[168:171] offset:2048\n\t"
        "ds_write_b128 v98, a[172:175] offset:3072\n\t"
        "ds_write_b128 v97, a[176:179] offset:4096\n\t"
        "ds_write_b128 v98, a[180:183] offset:5120\n\t"
        "ds_write_b128 v97, a[184:187] offset:6144\n\t"
        "ds_write_b128 v98, a[188:191] offset:7168\n\t"
        "ds_read_b64_tr_b16 a[144:145], v99 offset:0\n\t"
        "ds_read_b64_tr_b16 a[146:147], v99 offset:256\n\t"
        "ds_read_b64_tr_b16 a[148:149], v99 offset:4096\n\t"
        "ds_read_b64_tr_b16 a[150:151], v99 offset:4352\n\t"
        "s_waitcnt lgkmcnt(8)\n\t"
        "ds_read_b64_tr_b16 a[152:153], v100 offset:0\n\t"
        "ds_read_b64_tr_b16 a[154:155], v100 offset:256\n\t"
        "ds_read_b64_tr_b16 a[156:157], v100 offset:4096\n\t"
        "ds_read_b64_tr_b16 a[158:159], v100 offset:4352\n\t"
        "s_waitcnt lgkmcnt(8)\n\t"
        "ds_read_b64_tr_b16 a[160:161], v101 offset:0\n\t"
        "ds_read_b64_tr_b16 a[162:163], v101 offset:256\n\t"
        "ds_read_b64_tr_b16 a[164:165], v101 offset:4096\n\t"
        "ds_read_b64_tr_b16 a[166:167], v101 offset:4352\n\t"
        "s_waitcnt lgkmcnt(8)\n\t"
        "ds_read_b64_tr_b16 v[104:105], v102 offset:0\n\t"
        "ds_read_b64_tr_b16 v[106:107], v102 offset:256\n\t"
        "ds_read_b64_tr_b16 v[108:109], v102 offset:4096\n\t"
        "ds_read_b64_tr_b16 v[110:111], v102 offset:4352\n\t"
        "s_waitcnt lgkmcnt(0)\n\t"
        "ds_write_b128 v117, v[104:107] offset:12288\n\t"
        "ds_write_b128 v117, v[108:111] offset:13312\n\t"
        "s_lshl_b32 s34, s43, 2\n\t"
        "s_add_u32 s34, s34, 13\n\t"
        "s_and_b32 s34, s34, 15\n\t"
        "s_lshl_b32 s34, s34, 19\n\t"
        "buffer_load_dwordx4 v[0:3], v96, s[16:19], s34 offen nt\n\t"
        "s_add_u32 s34, s34, 0x10000\n\t"
        "buffer_load_dwordx4 v[4:7], v96, s[16:19], s34 offen nt\n\t"
        "s_add_u32 s34, s34, 0x10000\n\t"
        "buffer_load_dwordx4 v[8:11], v96, s[16:19], s34 offen nt\n\t"
        "s_add_u32 s34, s34, 0x10000\n\t"
        "buffer_load_dwordx4 v[12:15], v96, s[16:19], s34 offen nt\n\t"
        "s_add_u32 s34, s34, 0x10000\n\t"
        "buffer_load_dwordx4 v[16:19], v96, s[16:19], s34 offen nt\n\t"
        "s_add_u32 s34, s34, 0x10000\n\t"
        "buffer_load_dwordx4 v[20:23], v96, s[16:19], s34 offen nt\n\t"
        "s_add_u32 s34, s34, 0x10000\n\t"
        "buffer_load_dwordx4 v[24:27], v96, s[16:19], s34 offen nt\n\t"
        "s_add_u32 s34, s34, 0x10000\n\t"
        "buffer_load_dwordx4 v[28:31], v96, s[16:19], s34 offen nt\n\t"
        "s_waitcnt vmcnt(48)\n\t"
        "ds_write_b128 v97, a[192:195] offset:0\n\t"
        "ds_write_b128 v98, a[196:199] offset:1024\n\t"
        "ds_write_b128 v97, a[200:203] offset:2048\n\t"
        "ds_write_b128 v98, a[204:207] offset:3072\n\t"
        "ds_write_b128 v97, a[208:211] offset:4096\n\t"
        "ds_write_b128 v98, a[212:215] offset:5120\n\t"
        "ds_write_b128 v97, a[216:219] offset:6144\n\t"
        "ds_write_b128 v98, a[220:223] offset:7168\n\t"
        "ds_read_b64_tr_b16 a[168:169], v99 offset:0\n\t"
        "ds_read_b64_tr_b16 a[170:171], v99 offset:256\n\t"
        "ds_read_b64_tr_b16 a[172:173], v99 offset:4096\n\t"
        "ds_read_b64_tr_b16 a[174:175], v99 offset:4352\n\t"
        "s_waitcnt lgkmcnt(8)\n\t"
        "ds_read_b64_tr_b16 a[176:177], v100 offset:0\n\t"
        "ds_read_b64_tr_b16 a[178:179], v100 offset:256\n\t"
        "ds_read_b64_tr_b16 a[180:181], v100 offset:4096\n\t"
        "ds_read_b64_tr_b16 a[182:183], v100 offset:4352\n\t"
        "s_waitcnt lgkmcnt(8)\n\t"
        "ds_read_b64_tr_b16 a[184:185], v101 offset:0\n\t"
        "ds_read_b64_tr_b16 a[186:187], v101 offset:256\n\t"
        "ds_read_b64_tr_b16 a[188:189], v101 offset:4096\n\t"
        "ds_read_b64_tr_b16 a[190:191], v101 offset:4352\n\t"
        "s_waitcnt lgkmcnt(8)\n\t"
        "ds_read_b64_tr_b16 v[104:105], v102 offset:0\n\t"
        "ds_read_b64_tr_b16 v[106:107], v102 offset:256\n\t"
        "ds_read_b64_tr_b16 v[108:109], v102 offset:4096\n\t"
        "ds_read_b64_tr_b16 v[110:111], v102 offset:4352\n\t"
        "s_waitcnt lgkmcnt(0)\n\t"
        "ds_write_b128 v117, v[104:107] offset:14336\n\t"
        "ds_write_b128 v117, v[108:111] offset:15360\n\t"
        "s_lshl_b32 s34, s43, 2\n\t"
        "s_add_u32 s34, s34, 14\n\t"
        "s_and_b32 s34, s34, 15\n\t"
        "s_lshl_b32 s34, s34, 19\n\t"
        "buffer_load_dwordx4 v[32:35], v96, s[16:19], s34 offen nt\n\t"
        "s_add_u32 s34, s34, 0x10000\n\t"
        "buffer_load_dwordx4 v[36:39], v96, s[16:19], s34 offen nt\n\t"
        "s_add_u32 s34, s34, 0x10000\n\t"
        "buffer_load_dwordx4 v[40:43], v96, s[16:19], s34 offen nt\n\t"
        "s_add_u32 s34, s34, 0x10000\n\t"
        "buffer_load_dwordx4 v[44:47], v96, s[16:19], s34 offen nt\n\t"
        "s_add_u32 s34, s34, 0x10000\n\t"
        "buffer_load_dwordx4 v[48:51], v96, s[16:19], s34 offen nt\n\t"
        "s_add_u32 s34, s34, 0x10000\n\t"
        "buffer_load_dwordx4 v[52:55], v96, s[16:19], s34 offen nt\n\t"
        "s_add_u32 s34, s34, 0x10000\n\t"
        "buffer_load_dwordx4 v[56:59], v96, s[16:19], s34 offen nt\n\t"
        "s_add_u32 s34, s34, 0x10000\n\t"
        "buffer_load_dwordx4 v[60:63], v96, s[16:19], s34 offen nt\n\t"
        "s_waitcnt vmcnt(48)\n\t"
        "ds_write_b128 v97, a[224:227] offset:0\n\t"
        "ds_write_b128 v98, a[228:231] offset:1024\n\t"
        "ds_write_b128 v97, a[232:235] offset:2048\n\t"
        "ds_write_b128 v98, a[236:239] offset:3072\n\t"
        "ds_write_b128 v97, a[240:243] offset:4096\n\t"
        "ds_write_b128 v98, a[244:247] offset:5120\n\t"
        "ds_write_b128 v97, a[248:251] offset:6144\n\t"
        "ds_write_b128 v98, a[252:255] offset:7168\n\t"
        "ds_read_b64_tr_b16 a[192:193], v99 offset:0\n\t"
        "ds_read_b64_tr_b16 a[194:195], v99 offset:256\n\t"
        "ds_read_b64_tr_b16 a[196:197], v99 offset:4096\n\t"
        "ds_read_b64_tr_b16 a[198:199], v99 offset:4352\n\t"
        "s_waitcnt lgkmcnt(8)\n\t"
        "ds_read_b64_tr_b16 a[200:201], v100 offset:0\n\t"
        "ds_read_b64_tr_b16 a[202:203], v100 offset:256\n\t"
        "ds_read_b64_tr_b16 a[204:205], v100 offset:4096\n\t"
        "ds_read_b64_tr_b16 a[206:207], v100 offset:4352\n\t"
        "s_waitcnt lgkmcnt(8)\n\t"
        "ds_read_b64_tr_b16 a[208:209], v101 offset:0\n\t"
        "ds_read_b64_tr_b16 a[210:211], v101 offset:256\n\t"
        "ds_read_b64_tr_b16 a[212:213], v101 offset:4096\n\t"
        "ds_read_b64_tr_b16 a[214:215], v101 offset:4352\n\t"
        "s_waitcnt lgkmcnt(8)\n\t"
        "ds_read_b64_tr_b16 v[104:105], v102 offset:0\n\t"
        "ds_read_b64_tr_b16 v[106:107], v102 offset:256\n\t"
        "ds_read_b64_tr_b16 v[108:109], v102 offset:4096\n\t"
        "ds_read_b64_tr_b16 v[110:111], v102 offset:4352\n\t"
        "s_waitcnt lgkmcnt(0)\n\t"
        "ds_write_b128 v117, v[104:107] offset:16384\n\t"
        "ds_write_b128 v117, v[108:111] offset:17408\n\t"
        "s_lshl_b32 s34, s43, 2\n\t"
        "s_add_u32 s34, s34, 15\n\t"
        "s_and_b32 s34, s34, 15\n\t"
        "s_lshl_b32 s34, s34, 19\n\t"
        "buffer_load_dwordx4 v[64:67], v96, s[16:19], s34 offen nt\n\t"
        "s_add_u32 s34, s34, 0x10000\n\t"
        "buffer_load_dwordx4 v[68:71], v96, s[16:19], s34 offen nt\n\t"
        "s_add_u32 s34, s34, 0x10000\n\t"
        "buffer_load_dwordx4 v[72:75], v96, s[16:19], s34 offen nt\n\t"
        "s_add_u32 s34, s34, 0x10000\n\t"
        "buffer_load_dwordx4 v[76:79], v96, s[16:19], s34 offen nt\n\t"
        "s_add_u32 s34, s34, 0x10000\n\t"
        "buffer_load_dwordx4 v[80:83], v96, s[16:19], s34 offen nt\n\t"
        "s_add_u32 s34, s34, 0x10000\n\t"
        "buffer_load_dwordx4 v[84:87], v96, s[16:19], s34 offen nt\n\t"
        "s_add_u32 s34, s34, 0x10000\n\t"
        "buffer_load_dwordx4 v[88:91], v96, s[16:19], s34 offen nt\n\t"
        "s_add_u32 s34, s34, 0x10000\n\t"
        "buffer_load_dwordx4 v[92:95], v96, s[16:19], s34 offen nt\n\t"
        "s_waitcnt vmcnt(48)\n\t"
        "ds_write_b128 v97, v[128:131] offset:0\n\t"
        "ds_write_b128 v98, v[132:135] offset:1024\n\t"
        "ds_write_b128 v97, v[136:139] offset:2048\n\t"
        "ds_write_b128 v98, v[140:143] offset:3072\n\t"
        "ds_write_b128 v97, v[144:147] offset:4096\n\t"
        "ds_write_b128 v98, v[148:151] offset:5120\n\t"
        "ds_write_b128 v97, v[152:155] offset:6144\n\t"
        "ds_write_b128 v98, v[156:159] offset:7168\n\t"
        "ds_read_b64_tr_b16 a[216:217], v99 offset:0\n\t"
        "ds_read_b64_tr_b16 a[218:219], v99 offset:256\n\t"
        "ds_read_b64_tr_b16 a[220:221], v99 offset:4096\n\t"
        "ds_read_b64_tr_b16 a[222:223], v99 offset:4352\n\t"
        "s_waitcnt lgkmcnt(8)\n\t"
        "ds_read_b64_tr_b16 a[224:225], v100 offset:0\n\t"
        "ds_read_b64_tr_b16 a[226:227], v100 offset:256\n\t"
        "ds_read_b64_tr_b16 a[228:229], v100 offset:4096\n\t"
        "ds_read_b64_tr_b16 a[230:231], v100 offset:4352\n\t"
        "s_waitcnt lgkmcnt(8)\n\t"
        "ds_read_b64_tr_b16 a[232:233], v101 offset:0\n\t"
        "ds_read_b64_tr_b16 a[234:235], v101 offset:256\n\t"
        "ds_read_b64_tr_b16 a[236:237], v101 offset:4096\n\t"
        "ds_read_b64_tr_b16 a[238:239], v101 offset:4352\n\t"
        "s_waitcnt lgkmcnt(8)\n\t"
        "ds_read_b64_tr_b16 v[104:105], v102 offset:0\n\t"
        "ds_read_b64_tr_b16 v[106:107], v102 offset:256\n\t"
        "ds_read_b64_tr_b16 v[108:109], v102 offset:4096\n\t"
        "ds_read_b64_tr_b16 v[110:111], v102 offset:4352\n\t"
        "s_waitcnt lgkmcnt(0)\n\t"
        "ds_write_b128 v117, v[104:107] offset:18432\n\t"
        "ds_write_b128 v117, v[108:111] offset:19456\n\t"
        "s_waitcnt vmcnt(40)\n\t"
        "ds_write_b128 v97, v[160:163] offset:0\n\t"
        "ds_write_b128 v98, v[164:167] offset:1024\n\t"
        "ds_write_b128 v97, v[168:171] offset:2048\n\t"
        "ds_write_b128 v98, v[172:175] offset:3072\n\t"
        "ds_write_b128 v97, v[176:179] offset:4096\n\t"
        "ds_write_b128 v98, v[180:183] offset:5120\n\t"
        "ds_write_b128 v97, v[184:187] offset:6144\n\t"
        "ds_write_b128 v98, v[188:191] offset:7168\n\t"
        "ds_read_b64_tr_b16 a[240:241], v99 offset:0\n\t"
        "ds_read_b64_tr_b16 a[242:243], v99 offset:256\n\t"
        "ds_read_b64_tr_b16 a[244:245], v99 offset:4096\n\t"
        "ds_read_b64_tr_b16 a[246:247], v99 offset:4352\n\t"
        "s_waitcnt lgkmcnt(8)\n\t"
        "ds_read_b64_tr_b16 a[248:249], v100 offset:0\n\t"
        "ds_read_b64_tr_b16 a[250:251], v100 offset:256\n\t"
        "ds_read_b64_tr_b16 a[252:253], v100 offset:4096\n\t"
        "ds_read_b64_tr_b16 a[254:255], v100 offset:4352\n\t"
        "s_waitcnt lgkmcnt(8)\n\t"
        "ds_read_b64_tr_b16 v[128:129], v101 offset:0\n\t"
        "ds_read_b64_tr_b16 v[130:131], v101 offset:256\n\t"
        "ds_read_b64_tr_b16 v[132:133], v101 offset:4096\n\t"
        "ds_read_b64_tr_b16 v[134:135], v101 offset:4352\n\t"
        "s_waitcnt lgkmcnt(8)\n\t"
        "ds_read_b64_tr_b16 v[104:105], v102 offset:0\n\t"
        "ds_read_b64_tr_b16 v[106:107], v102 offset:256\n\t"
        "ds_read_b64_tr_b16 v[108:109], v102 offset:4096\n\t"
        "ds_read_b64_tr_b16 v[110:111], v102 offset:4352\n\t"
        "s_waitcnt lgkmcnt(0)\n\t"
        "ds_write_b128 v117, v[104:107] offset:20480\n\t"
        "ds_write_b128 v117, v[108:111] offset:21504\n\t"
        "s_waitcnt vmcnt(32)\n\t"
        "ds_write_b128 v97, v[192:195] offset:0\n\t"
        "ds_write_b128 v98, v[196:199] offset:1024\n\t"
        "ds_write_b128 v97, v[200:203] offset:2048\n\t"
        "ds_write_b128 v98, v[204:207] offset:3072\n\t"
        "ds_write_b128 v97, v[208:211] offset:4096\n\t"
        "ds_write_b128 v98, v[212:215] offset:5120\n\t"
        "ds_write_b128 v97, v[216:219] offset:6144\n\t"
        "ds_write_b128 v98, v[220:223] offset:7168\n\t"
        "ds_read_b64_tr_b16 v[136:137], v99 offset:0\n\t"
        "ds_read_b64_tr_b16 v[138:139], v99 offset:256\n\t"
        "ds_read_b64_tr_b16 v[140:141], v99 offset:4096\n\t"
        "ds_read_b64_tr_b16 v[142:143], v99 offset:4352\n\t"
        "s_waitcnt lgkmcnt(8)\n\t"
        "ds_read_b64_tr_b16 v[144:145], v100 offset:0\n\t"
        "ds_read_b64_tr_b16 v[146:147], v100 offset:256\n\t"
        "ds_read_b64_tr_b16 v[148:149], v100 offset:4096\n\t"
        "ds_read_b64_tr_b16 v[150:151], v100 offset:4352\n\t"
        "s_waitcnt lgkmcnt(8)\n\t"
        "ds_read_b64_tr_b16 v[152:153], v101 offset:0\n\t"
        "ds_read_b64_tr_b16 v[154:155], v101 offset:256\n\t"
        "ds_read_b64_tr_b16 v[156:157], v101 offset:4096\n\t"
        "ds_read_b64_tr_b16 v[158:159], v101 offset:4352\n\t"
        "s_waitcnt lgkmcnt(8)\n\t"
        "ds_read_b64_tr_b16 v[104:105], v102 offset:0\n\t"
        "ds_read_b64_tr_b16 v[106:107], v102 offset:256\n\t"
        "ds_read_b64_tr_b16 v[108:109], v102 offset:4096\n\t"
        "ds_read_b64_tr_b16 v[110:111], v102 offset:4352\n\t"
        "s_waitcnt lgkmcnt(0)\n\t"
        "ds_write_b128 v126, v[104:107] offset:0\n\t"
        "ds_write_b128 v126, v[108:111] offset:1024\n\t"
        "s_waitcnt vmcnt(24)\n\t"
        "ds_write_b128 v97, v[224:227] offset:0\n\t"
        "ds_write_b128 v98, v[228:231] offset:1024\n\t"
        "ds_write_b128 v97, v[232:235] offset:2048\n\t"
        "ds_write_b128 v98, v[236:239] offset:3072\n\t"
        "ds_write_b128 v97, v[240:243] offset:4096\n\t"
        "ds_write_b128 v98, v[244:247] offset:5120\n\t"
        "ds_write_b128 v97, v[248:251] offset:6144\n\t"
        "ds_write_b128 v98, v[252:255] offset:7168\n\t"
        "ds_read_b64_tr_b16 v[160:161], v99 offset:0\n\t"
        "ds_read_b64_tr_b16 v[162:163], v99 offset:256\n\t"
        "ds_read_b64_tr_b16 v[164:165], v99 offset:4096\n\t"
        "ds_read_b64_tr_b16 v[166:167], v99 offset:4352\n\t"
        "s_waitcnt lgkmcnt(8)\n\t"
        "ds_read_b64_tr_b16 v[168:169], v100 offset:0\n\t"
        "ds_read_b64_tr_b16 v[170:171], v100 offset:256\n\t"
        "ds_read_b64_tr_b16 v[172:173], v100 offset:4096\n\t"
        "ds_read_b64_tr_b16 v[174:175], v100 offset:4352\n\t"
        "s_waitcnt lgkmcnt(8)\n\t"
        "ds_read_b64_tr_b16 v[176:177], v101 offset:0\n\t"
        "ds_read_b64_tr_b16 v[178:179], v101 offset:256\n\t"
        "ds_read_b64_tr_b16 v[180:181], v101 offset:4096\n\t"
        "ds_read_b64_tr_b16 v[182:183], v101 offset:4352\n\t"
        "s_waitcnt lgkmcnt(8)\n\t"
        "ds_read_b64_tr_b16 v[104:105], v102 offset:0\n\t"
        "ds_read_b64_tr_b16 v[106:107], v102 offset:256\n\t"
        "ds_read_b64_tr_b16 v[108:109], v102 offset:4096\n\t"
        "ds_read_b64_tr_b16 v[110:111], v102 offset:4352\n\t"
        "s_waitcnt lgkmcnt(0)\n\t"
        "ds_write_b128 v126, v[104:107] offset:2048\n\t"
        "ds_write_b128 v126, v[108:111] offset:3072\n\t"
        "s_waitcnt vmcnt(16)\n\t"
        "ds_write_b128 v97, v[0:3] offset:0\n\t"
        "ds_write_b128 v98, v[4:7] offset:1024\n\t"
        "ds_write_b128 v97, v[8:11] offset:2048\n\t"
        "ds_write_b128 v98, v[12:15] offset:3072\n\t"
        "ds_write_b128 v97, v[16:19] offset:4096\n\t"
        "ds_write_b128 v98, v[20:23] offset:5120\n\t"
        "ds_write_b128 v97, v[24:27] offset:6144\n\t"
        "ds_write_b128 v98, v[28:31] offset:7168\n\t"
        "ds_read_b64_tr_b16 v[184:185], v99 offset:0\n\t"
        "ds_read_b64_tr_b16 v[186:187], v99 offset:256\n\t"
        "ds_read_b64_tr_b16 v[188:189], v99 offset:4096\n\t"
        "ds_read_b64_tr_b16 v[190:191], v99 offset:4352\n\t"
        "s_waitcnt lgkmcnt(8)\n\t"
        "ds_read_b64_tr_b16 v[192:193], v100 offset:0\n\t"
        "ds_read_b64_tr_b16 v[194:195], v100 offset:256\n\t"
        "ds_read_b64_tr_b16 v[196:197], v100 offset:4096\n\t"
        "ds_read_b64_tr_b16 v[198:199], v100 offset:4352\n\t"
        "s_waitcnt lgkmcnt(8)\n\t"
        "ds_read_b64_tr_b16 v[200:201], v101 offset:0\n\t"
        "ds_read_b64_tr_b16 v[202:203], v101 offset:256\n\t"
        "ds_read_b64_tr_b16 v[204:205], v101 offset:4096\n\t"
        "ds_read_b64_tr_b16 v[206:207], v101 offset:4352\n\t"
        "s_waitcnt lgkmcnt(8)\n\t"
        "ds_read_b64_tr_b16 v[104:105], v102 offset:0\n\t"
        "ds_read_b64_tr_b16 v[106:107], v102 offset:256\n\t"
        "ds_read_b64_tr_b16 v[108:109], v102 offset:4096\n\t"
        "ds_read_b64_tr_b16 v[110:111], v102 offset:4352\n\t"
        "s_waitcnt lgkmcnt(0)\n\t"
        "ds_write_b128 v126, v[104:107] offset:4096\n\t"
        "ds_write_b128 v126, v[108:111] offset:5120\n\t"
        "s_waitcnt vmcnt(8)\n\t"
        "ds_write_b128 v97, v[32:35] offset:0\n\t"
        "ds_write_b128 v98, v[36:39] offset:1024\n\t"
        "ds_write_b128 v97, v[40:43] offset:2048\n\t"
        "ds_write_b128 v98, v[44:47] offset:3072\n\t"
        "ds_write_b128 v97, v[48:51] offset:4096\n\t"
        "ds_write_b128 v98, v[52:55] offset:5120\n\t"
        "ds_write_b128 v97, v[56:59] offset:6144\n\t"
        "ds_write_b128 v98, v[60:63] offset:7168\n\t"
        "ds_read_b64_tr_b16 v[208:209], v99 offset:0\n\t"
        "ds_read_b64_tr_b16 v[210:211], v99 offset:256\n\t"
        "ds_read_b64_tr_b16 v[212:213], v99 offset:4096\n\t"
        "ds_read_b64_tr_b16 v[214:215], v99 offset:4352\n\t"
        "s_waitcnt lgkmcnt(8)\n\t"
        "ds_read_b64_tr_b16 v[216:217], v100 offset:0\n\t"
        "ds_read_b64_tr_b16 v[218:219], v100 offset:256\n\t"
        "ds_read_b64_tr_b16 v[220:221], v100 offset:4096\n\t"
        "ds_read_b64_tr_b16 v[222:223], v100 offset:4352\n\t"
        "s_waitcnt lgkmcnt(8)\n\t"
        "ds_read_b64_tr_b16 v[224:225], v101 offset:0\n\t"
        "ds_read_b64_tr_b16 v[226:227], v101 offset:256\n\t"
        "ds_read_b64_tr_b16 v[228:229], v101 offset:4096\n\t"
        "ds_read_b64_tr_b16 v[230:231], v101 offset:4352\n\t"
        "s_waitcnt lgkmcnt(8)\n\t"
        "ds_read_b64_tr_b16 v[104:105], v102 offset:0\n\t"
        "ds_read_b64_tr_b16 v[106:107], v102 offset:256\n\t"
        "ds_read_b64_tr_b16 v[108:109], v102 offset:4096\n\t"
        "ds_read_b64_tr_b16 v[110:111], v102 offset:4352\n\t"
        "s_waitcnt lgkmcnt(0)\n\t"
        "ds_write_b128 v126, v[104:107] offset:6144\n\t"
        "ds_write_b128 v126, v[108:111] offset:7168\n\t"
        "s_waitcnt vmcnt(0)\n\t"
        "ds_write_b128 v97, v[64:67] offset:0\n\t"
        "ds_write_b128 v98, v[68:71] offset:1024\n\t"
        "ds_write_b128 v97, v[72:75] offset:2048\n\t"
        "ds_write_b128 v98, v[76:79] offset:3072\n\t"
        "ds_write_b128 v97, v[80:83] offset:4096\n\t"
        "ds_write_b128 v98, v[84:87] offset:5120\n\t"
        "ds_write_b128 v97, v[88:91] offset:6144\n\t"
        "ds_write_b128 v98, v[92:95] offset:7168\n\t"
        "ds_read_b64_tr_b16 v[232:233], v99 offset:0\n\t"
        "ds_read_b64_tr_b16 v[234:235], v99 offset:256\n\t"
        "ds_read_b64_tr_b16 v[236:237], v99 offset:4096\n\t"
        "ds_read_b64_tr_b16 v[238:239], v99 offset:4352\n\t"
        "s_waitcnt lgkmcnt(8)\n\t"
        "ds_read_b64_tr_b16 v[240:241], v100 offset:0\n\t"
        "ds_read_b64_tr_b16 v[242:243], v100 offset:256\n\t"
        "ds_read_b64_tr_b16 v[244:245], v100 offset:4096\n\t"
        "ds_read_b64_tr_b16 v[246:247], v100 offset:4352\n\t"
        "s_waitcnt lgkmcnt(8)\n\t"
        "ds_read_b64_tr_b16 v[248:249], v101 offset:0\n\t"
        "ds_read_b64_tr_b16 v[250:251], v101 offset:256\n\t"
        "ds_read_b64_tr_b16 v[252:253], v101 offset:4096\n\t"
        "ds_read_b64_tr_b16 v[254:255], v101 offset:4352\n\t"
        "s_waitcnt lgkmcnt(8)\n\t"
        "ds_read_b64_tr_b16 v[104:105], v102 offset:0\n\t"
        "ds_read_b64_tr_b16 v[106:107], v102 offset:256\n\t"
        "ds_read_b64_tr_b16 v[108:109], v102 offset:4096\n\t"
        "ds_read_b64_tr_b16 v[110:111], v102 offset:4352\n\t"
        "s_waitcnt lgkmcnt(0)\n\t"
        "ds_write_b128 v126, v[104:107] offset:8192\n\t"
        "ds_write_b128 v126, v[108:111] offset:9216\n\t"
        "s_waitcnt lgkmcnt(0)\n\t"
        "v_mov_b32_e32 v110, v103\n\t"
        "ds_read_b128 v[64:67], v126 offset:0\n\t"
        "ds_read_b128 v[68:71], v126 offset:1024\n\t"
        "ds_read_b128 v[72:75], v126 offset:2048\n\t"
        "ds_read_b128 v[76:79], v126 offset:3072\n\t"
        "ds_read_b128 v[80:83], v126 offset:4096\n\t"
        "ds_read_b128 v[84:87], v126 offset:5120\n\t"
        "ds_read_b128 v[88:91], v126 offset:6144\n\t"
        "ds_read_b128 v[92:95], v126 offset:7168\n\t"
        "ds_read_b128 v[96:99], v126 offset:8192\n\t"
        "ds_read_b128 v[100:103], v126 offset:9216\n\t"
        "s_waitcnt lgkmcnt(0)\n\t"
        "v_mov_b32_e32 v126, 0\n\t"
        "s_barrier\n\t"
        "s_mov_b32 s48, 0\n\t"
        "s_mov_b32 s54, 0\n\t"
        "s_movk_i32 s56, 24576\n\t"
        "s_lshl_b32 s35, s44, 7\n\t"
        "v_lshrrev_b32_e32 v111, 5, v125\n\t"
        "v_and_b32_e32 v111, 60, v111\n\t"
        "buffer_load_dword v111, v111, s[12:15], s35 offen offset:0 sc1\n\t"
        "s_lshr_b32 s36, s48, 1\n\t"
        "s_cmp_gt_u32 s54, s36\n\t"
        "s_cbranch_scc1 LA_kv0ok%=\n\t"
        "s_mov_b32 s42, 0\n\t"
        "LA_kv0%=:\n\t"
        "s_waitcnt vmcnt(0)\n\t"
        "v_min_u32_dpp v111, v111, v111 quad_perm:[1,0,3,2] row_mask:0xf bank_mask:0xf\n\t"
        "s_nop 1\n\t"
        "v_min_u32_dpp v111, v111, v111 quad_perm:[2,3,0,1] row_mask:0xf bank_mask:0xf\n\t"
        "s_nop 1\n\t"
        "v_min_u32_dpp v111, v111, v111 row_ror:4 row_mask:0xf bank_mask:0xf\n\t"
        "s_nop 1\n\t"
        "v_min_u32_dpp v111, v111, v111 row_ror:8 row_mask:0xf bank_mask:0xf\n\t"
        "s_nop 0\n\t"
        "v_readfirstlane_b32 s54, v111\n\t"
        "s_cmp_gt_u32 s54, s36\n\t"
        "s_cbranch_scc1 LA_kv0ok%=\n\t"
        "s_sleep 4\n\t"
        "s_lshl_b32 s35, s44, 7\n\t"
        "v_lshrrev_b32_e32 v111, 5, v125\n\t"
        "v_and_b32_e32 v111, 60, v111\n\t"
        "buffer_load_dword v111, v111, s[12:15], s35 offen offset:0 sc1\n\t"
        "s_add_u32 s42, s42, 1\n\t"
        "s_cmp_lt_u32 s42, 0x100000\n\t"
        "s_cbranch_scc1 LA_kv0%=\n\t"
        "s_movk_i32 s34, 16\n\t"
        "s_branch LA_die%=\n\t"
        "LA_kv0ok%=:\n\t"
        "s_and_b32 s36, s48, 1\n\t"
        "s_mul_i32 s36, s36, 24576\n\t"
        "s_add_u32 s36, s36, s55\n\t"
        "s_lshl_b32 s37, s48, 18\n\t"
        "s_lshl_b32 s34, s43, 11\n\t"
        "s_add_u32 s37, s37, s34\n\t"
        "s_add_u32 m0, s36, 0\n\t"
        "s_add_u32 s34, s37, 29548544\n\t"
        "buffer_load_dwordx4 v122, s[12:15], s34 offen sc1 lds\n\t"
        "s_add_u32 m0, s36, 1024\n\t"
        "s_add_u32 s34, s37, 29549568\n\t"
        "buffer_load_dwordx4 v122, s[12:15], s34 offen sc1 lds\n\t"
        "s_add_u32 m0, s36, 8192\n\t"
        "s_add_u32 s34, s37, 12771328\n\t"
        "buffer_load_dwordx4 v122, s[12:15], s34 offen sc1 lds\n\t"
        "s_add_u32 m0, s36, 9216\n\t"
        "s_add_u32 s34, s37, 12772352\n\t"
        "buffer_load_dwordx4 v122, s[12:15], s34 offen sc1 lds\n\t"
        "s_add_u32 m0, s36, 16384\n\t"
        "s_add_u32 s34, s37, 46325760\n\t"
        "buffer_load_dwordx4 v122, s[12:15], s34 offen sc1 lds\n\t"
        "s_add_u32 m0, s36, 17408\n\t"
        "s_add_u32 s34, s37, 46326784\n\t"
        "buffer_load_dwordx4 v122, s[12:15], s34 offen sc1 lds\n\t"
        "s_mov_b32 s42, 0\n\t"
        "v_mbcnt_lo_u32_b32 v2, -1, 0\n\t"
        "v_mbcnt_hi_u32_b32 v2, -1, v2\n\t"
        "v_and_b32_e32 v3, 31, v2\n\t"
        "v_lshlrev_b32_e32 v3, 2, v3\n\t"
        "s_mov_b32 s35, 33792\n\t"
        "LA_cal%=:\n\t"
        "buffer_load_dword v0, v3, s[12:15], s35 offen sc1\n\t"
        "s_waitcnt vmcnt(0)\n\t"
        "v_cmp_eq_u32_e32 vcc, 0, v0\n\t"
        "s_cbranch_vccz LA_calok%=\n\t"
        "s_sleep 4\n\t"
        "s_add_u32 s42, s42, 1\n\t"
        "s_cmp_lt_u32 s42, 0x100000\n\t"
        "s_cbranch_scc1 LA_cal%=\n\t"
        "s_movk_i32 s34, 8\n\t"
        "s_branch LA_die%=\n\t"
        "LA_calok%=:\n\t"
        "v_cmp_eq_u32_e32 vcc, 3, v0\n\t"
        "s_nop 0\n\t"
        "s_bcnt1_i32_b64 s34, vcc\n\t"
        "s_cmp_ge_u32 s34, 32\n\t"
        "s_cselect_b32 s34, 8192, 0\n\t"
        "s_add_u32 s8, s8, s34\n\t"
        "s_addc_u32 s9, s9, 0\n\t"
        "s_waitcnt vmcnt(0) lgkmcnt(0)\n\t"
        "s_barrier\n\t"
        "s_memrealtime s[32:33]\n\t"
        "s_lshl_b32 s35, s49, 2\n\t"
        "s_add_u32 s35, s35, 34304\n\t"
        "s_cmp_eq_u32 s43, 0\n\t"
        "s_cselect_b32 s35, s35, 0xf0000000\n\t"
        "s_waitcnt lgkmcnt(0)\n\t"
        "s_or_b32 s25, s32, 1\n\t"
        "v_mov_b32_e32 v0, s25\n\t"
        "s_mov_b64 exec, 1\n\t"
        "buffer_store_dword v0, off, s[12:15], s35 sc1\n\t"
        "s_mov_b64 exec, -1\n\t"
        "v_min_u32_e32 v3, 47, v2\n\t"
        "v_lshlrev_b32_e32 v3, 3, v3\n\t"
        "s_mov_b32 s35, 34304\n\t"
        "s_mov_b32 s42, 0\n\t"
        "LA_rdy%=:\n\t"
        "buffer_load_dwordx2 v[0:1], v3, s[12:15], s35 offen sc1\n\t"
        "s_waitcnt vmcnt(0)\n\t"
        "v_min_u32_e32 v2, v0, v1\n\t"
        "v_cmp_eq_u32_e32 vcc, 0, v2\n\t"
        "s_cbranch_vccz LA_rdyok%=\n\t"
        "s_sleep 6\n\t"
        "s_add_u32 s42, s42, 1\n\t"
        "s_cmp_lt_u32 s42, 0x100000\n\t"
        "s_cbranch_scc1 LA_rdy%=\n\t"
        "s_movk_i32 s34, 9\n\t"
        "s_branch LA_die%=\n\t"
        "LA_rdyok%=:\n\t"
        "v_subrev_u32_e32 v0, s25, v0\n\t"
        "v_subrev_u32_e32 v1, s25, v1\n\t"
        "v_max_i32_e32 v0, v0, v1\n\t"
        "s_nop 1\n\t"
        "v_max_i32_dpp v0, v0, v0 quad_perm:[1,0,3,2] row_mask:0xf bank_mask:0xf\n\t"
        "s_nop 1\n\t"
        "v_max_i32_dpp v0, v0, v0 quad_perm:[2,3,0,1] row_mask:0xf bank_mask:0xf\n\t"
        "s_nop 1\n\t"
        "v_max_i32_dpp v0, v0, v0 row_ror:4 row_mask:0xf bank_mask:0xf\n\t"
        "s_nop 1\n\t"
        "v_max_i32_dpp v0, v0, v0 row_ror:8 row_mask:0xf bank_mask:0xf\n\t"
        "v_mov_b32_e32 v1, v0\n\t"
        "s_nop 1\n\t"
        "v_permlane16_swap_b32_e32 v0, v1\n\t"
        "v_max_i32_e32 v0, v0, v1\n\t"
        "v_mov_b32_e32 v1, v0\n\t"
        "s_nop 1\n\t"
        "v_permlane32_swap_b32_e32 v0, v1\n\t"
        "v_max_i32_e32 v0, v0, v1\n\t"
        "s_nop 0\n\t"
        "v_readfirstlane_b32 s34, v0\n\t"
        "s_nop 0\n\t"
        "s_add_u32 s25, s25, s34\n\t"
        "s_add_u32 s25, s25, 120\n\t"
        "s_and_b32 s25, s25, -4\n\t"
        "s_mov_b32 s24, 0\n\t"
        "s_mov_b32 s26, 0\n\t"
        "s_mov_b32 s27, 0x80000\n\t"
        "s_mov_b32 s28, 0x180000\n\t"
        "s_mov_b32 s31, 0\n\t"
        "s_mov_b32 s47, 0\n\t"
        "s_mov_b32 s38, 0xff00ff00\n\t"
        "s_mov_b32 s39, 0xff00ff00\n\t"
        "s_mov_b32 s40, 0\n\t"
        "s_mov_b32 s41, -1\n\t"
        "ds_read_b128 v[32:35], v117 offset:0\n\t"
        "ds_read_b128 v[36:39], v117 offset:1024\n\t"
        "ds_read_b128 v[40:43], v117 offset:2048\n\t"
        "ds_read_b128 v[44:47], v117 offset:3072\n\t"
        "ds_read_b128 v[48:51], v117 offset:4096\n\t"
        "ds_read_b128 v[52:55], v117 offset:5120\n\t"
        "ds_read_b128 v[56:59], v117 offset:6144\n\t"
        "ds_read_b128 v[60:63], v117 offset:7168\n\t"
        "v_mov_b64_e32 v[0:1], 0\n\t"
        "v_mov_b64_e32 v[2:3], 0\n\t"
        "v_mov_b64_e32 v[4:5], 0\n\t"
        "v_mov_b64_e32 v[6:7], 0\n\t"
        "v_mov_b64_e32 v[8:9], 0\n\t"
        "v_mov_b64_e32 v[10:11], 0\n\t"
        "v_mov_b64_e32 v[12:13], 0\n\t"
        "v_mov_b64_e32 v[14:15], 0\n\t"
        "LA_loop%=:\n\t"
        "s_mov_b32 s42, 0\n\t"
        "s_sub_u32 s35, s25, 1\n\t"
        "LA_wa%=:\n\t"
        "s_memrealtime s[32:33]\n\t"
        "s_waitcnt lgkmcnt(0)\n\t"
        "s_sub_u32 s34, s35, s32\n\t"
        "s_cmpk_lt_u32 s34, 0x2000\n\t"
        "s_cbranch_scc1 LA_wa%=\n\t"
        "LA_rd%=:\n\t"
        "buffer_load_dwordx4 v[16:19], v112, s[8:11], s26 offen offset:0 sc1\n\t"
        "s_nop 9\n\t"
        "buffer_load_dwordx4 v[20:23], v112, s[8:11], s26 offen offset:1024 sc1\n\t"
        "s_nop 9\n\t"
        "buffer_load_dwordx4 v[24:27], v112, s[8:11], s26 offen offset:2048 sc1\n\t"
        "s_nop 9\n\t"
        "buffer_load_dwordx4 v[28:31], v112, s[8:11], s26 offen offset:3072 sc1\n\t"
        "s_waitcnt vmcnt(3)\n\t"
        "v_smfmac_f32_16x16x64_bf16 v[12:15], v[16:19], v[32:39], v110\n\t"
        "ds_write_b128 v113, v[16:19] offset:0\n\t"
        "v_smfmac_f32_16x16x64_bf16 v[0:3], v[16:19], a[0:7], v110\n\t"
        "ds_read_b128 v[32:35], v117 offset:8192\n\t"
        "v_max3_u32 v111, v16, v17, v18\n\t"
        "v_smfmac_f32_16x16x64_bf16 v[4:7], v[16:19], a[8:15], v110\n\t"
        "ds_read_b128 v[36:39], v117 offset:9216\n\t"
        "v_max_u32_e32 v111, v19, v111\n\t"
        "v_smfmac_f32_16x16x64_bf16 v[8:11], v[16:19], a[16:23], v110\n\t"
        "s_waitcnt vmcnt(2)\n\t"
        "v_smfmac_f32_16x16x64_bf16 v[12:15], v[20:23], v[40:47], v110\n\t"
        "ds_write_b128 v113, v[20:23] offset:1024\n\t"
        "v_smfmac_f32_16x16x64_bf16 v[0:3], v[20:23], a[24:31], v110\n\t"
        "ds_read_b128 v[40:43], v117 offset:10240\n\t"
        "v_max3_u32 v111, v20, v21, v111\n\t"
        "v_smfmac_f32_16x16x64_bf16 v[4:7], v[20:23], a[32:39], v110\n\t"
        "ds_read_b128 v[44:47], v117 offset:11264\n\t"
        "v_max3_u32 v111, v22, v23, v111\n\t"
        "v_smfmac_f32_16x16x64_bf16 v[8:11], v[20:23], a[40:47], v110\n\t"
        "s_waitcnt vmcnt(1)\n\t"
        "v_smfmac_f32_16x16x64_bf16 v[12:15], v[24:27], v[48:55], v110\n\t"
        "ds_write_b128 v113, v[24:27] offset:2048\n\t"
        "v_smfmac_f32_16x16x64_bf16 v[0:3], v[24:27], a[48:55], v110\n\t"
        "ds_read_b128 v[48:51], v117 offset:12288\n\t"
        "v_max3_u32 v111, v24, v25, v111\n\t"
        "v_smfmac_f32_16x16x64_bf16 v[4:7], v[24:27], a[56:63], v110\n\t"
        "ds_read_b128 v[52:55], v117 offset:13312\n\t"
        "v_max3_u32 v111, v26, v27, v111\n\t"
        "v_smfmac_f32_16x16x64_bf16 v[8:11], v[24:27], a[64:71], v110\n\t"
        "s_waitcnt vmcnt(0)\n\t"
        "ds_write_b128 v113, v[28:31] offset:3072\n\t"
        "v_max3_u32 v111, v28, v29, v111\n\t"
        "v_max3_u32 v111, v30, v31, v111\n\t"
        "v_cmp_eq_u32_e32 vcc, -1, v111\n\t"
        "s_waitcnt lgkmcnt(0)\n\t"
        "s_cbranch_vccnz LA_rdfail%=\n\t"
        "s_barrier\n\t"
        "ds_read_b128 v[16:19], v114 offset:0\n\t"
        "ds_read_b128 v[20:23], v114 offset:1024\n\t"
        "ds_read_b128 v[24:27], v114 offset:2048\n\t"
        "v_smfmac_f32_16x16x64_bf16 v[12:15], v[28:31], v[56:63], v110\n\t"
        "ds_read_b128 v[56:59], v117 offset:14336\n\t"
        "v_smfmac_f32_16x16x64_bf16 v[0:3], v[28:31], a[72:79], v110\n\t"
        "ds_read_b128 v[60:63], v117 offset:15360\n\t"
        "v_smfmac_f32_16x16x64_bf16 v[4:7], v[28:31], a[80:87], v110\n\t"
        "v_smfmac_f32_16x16x64_bf16 v[8:11], v[28:31], a[88:95], v110\n\t"
        "ds_read_b128 v[28:31], v114 offset:3072\n\t"
        "s_waitcnt lgkmcnt(5)\n\t"
        "v_smfmac_f32_16x16x64_bf16 v[12:15], v[16:19], v[32:39], v110\n\t"
        "ds_read_b128 v[32:35], v117 offset:16384\n\t"
        "v_smfmac_f32_16x16x64_bf16 v[0:3], v[16:19], a[96:103], v110\n\t"
        "ds_read_b128 v[36:39], v117 offset:17408\n\t"
        "v_smfmac_f32_16x16x64_bf16 v[4:7], v[16:19], a[104:111], v110\n\t"
        "v_smfmac_f32_16x16x64_bf16 v[8:11], v[16:19], a[112:119], v110\n\t"
        "ds_read_b128 v[16:19], v115 offset:0\n\t"
        "s_waitcnt lgkmcnt(7)\n\t"
        "v_smfmac_f32_16x16x64_bf16 v[12:15], v[20:23], v[40:47], v110\n\t"
        "ds_read_b128 v[40:43], v117 offset:18432\n\t"
        "v_smfmac_f32_16x16x64_bf16 v[0:3], v[20:23], a[120:127], v110\n\t"
        "ds_read_b128 v[44:47], v117 offset:19456\n\t"
        "v_smfmac_f32_16x16x64_bf16 v[4:7], v[20:23], a[128:135], v110\n\t"
        "v_smfmac_f32_16x16x64_bf16 v[8:11], v[20:23], a[136:143], v110\n\t"
        "ds_read_b128 v[20:23], v115 offset:1024\n\t"
        "s_waitcnt lgkmcnt(9)\n\t"
        "v_smfmac_f32_16x16x64_bf16 v[12:15], v[24:27], v[48:55], v110\n\t"
        "ds_read_b128 v[48:51], v117 offset:20480\n\t"
        "v_smfmac_f32_16x16x64_bf16 v[0:3], v[24:27], a[144:151], v110\n\t"
        "ds_read_b128 v[52:55], v117 offset:21504\n\t"
        "v_smfmac_f32_16x16x64_bf16 v[4:7], v[24:27], a[152:159], v110\n\t"
        "v_smfmac_f32_16x16x64_bf16 v[8:11], v[24:27], a[160:167], v110\n\t"
        "ds_read_b128 v[24:27], v115 offset:2048\n\t"
        "s_waitcnt lgkmcnt(9)\n\t"
        "v_smfmac_f32_16x16x64_bf16 v[12:15], v[28:31], v[56:63], v110\n\t"
        "ds_read_b128 v[56:59], v124 offset:3072\n\t"
        "v_smfmac_f32_16x16x64_bf16 v[0:3], v[28:31], a[168:175], v110\n\t"
        "ds_read_b128 v[60:63], v124 offset:7168\n\t"
        "v_smfmac_f32_16x16x64_bf16 v[4:7], v[28:31], a[176:183], v110\n\t"
        "v_smfmac_f32_16x16x64_bf16 v[8:11], v[28:31], a[184:191], v110\n\t"
        "ds_read_b128 v[28:31], v115 offset:3072\n\t"
        "s_waitcnt lgkmcnt(9)\n\t"
        "v_smfmac_f32_16x16x64_bf16 v[12:15], v[16:19], v[32:39], v110\n\t"
        "ds_read_b128 v[32:35], v124 offset:0\n\t"
        "v_smfmac_f32_16x16x64_bf16 v[0:3], v[16:19], a[192:199], v110\n\t"
        "ds_read_b128 v[36:39], v124 offset:4096\n\t"
        "v_smfmac_f32_16x16x64_bf16 v[4:7], v[16:19], a[200:207], v110\n\t"
        "v_smfmac_f32_16x16x64_bf16 v[8:11], v[16:19], a[208:215], v110\n\t"
        "ds_read_b128 v[16:19], v116 offset:0\n\t"
        "s_waitcnt lgkmcnt(9)\n\t"
        "v_smfmac_f32_16x16x64_bf16 v[12:15], v[20:23], v[40:47], v110\n\t"
        "ds_read_b128 v[40:43], v124 offset:1024\n\t"
        "v_smfmac_f32_16x16x64_bf16 v[0:3], v[20:23], a[216:223], v110\n\t"
        "ds_read_b128 v[44:47], v124 offset:5120\n\t"
        "v_smfmac_f32_16x16x64_bf16 v[4:7], v[20:23], a[224:231], v110\n\t"
        "v_smfmac_f32_16x16x64_bf16 v[8:11], v[20:23], a[232:239], v110\n\t"
        "ds_read_b128 v[20:23], v116 offset:1024\n\t"
        "s_waitcnt lgkmcnt(9)\n\t"
        "v_smfmac_f32_16x16x64_bf16 v[12:15], v[24:27], v[48:55], v110\n\t"
        "ds_read_b128 v[48:51], v124 offset:2048\n\t"
        "v_smfmac_f32_16x16x64_bf16 v[0:3], v[24:27], a[240:247], v110\n\t"
        "ds_read_b128 v[52:55], v124 offset:6144\n\t"
        "v_smfmac_f32_16x16x64_bf16 v[4:7], v[24:27], a[248:255], v110\n\t"
        "v_smfmac_f32_16x16x64_bf16 v[8:11], v[24:27], v[128:135], v110\n\t"
        "ds_read_b128 v[24:27], v116 offset:2048\n\t"
        "s_waitcnt lgkmcnt(9)\n\t"
        "v_smfmac_f32_16x16x64_bf16 v[12:15], v[28:31], v[64:71], v110\n\t"
        "v_smfmac_f32_16x16x64_bf16 v[0:3], v[28:31], v[136:143], v110\n\t"
        "v_smfmac_f32_16x16x64_bf16 v[4:7], v[28:31], v[144:151], v110\n\t"
        "v_smfmac_f32_16x16x64_bf16 v[8:11], v[28:31], v[152:159], v110\n\t"
        "ds_read_b128 v[28:31], v116 offset:3072\n\t"
        "s_waitcnt lgkmcnt(7)\n\t"
        "v_smfmac_f32_16x16x64_bf16 v[12:15], v[16:19], v[72:79], v110\n\t"
        "v_smfmac_f32_16x16x64_bf16 v[0:3], v[16:19], v[160:167], v110\n\t"
        "v_smfmac_f32_16x16x64_bf16 v[4:7], v[16:19], v[168:175], v110\n\t"
        "v_smfmac_f32_16x16x64_bf16 v[8:11], v[16:19], v[176:183], v110\n\t"
        "s_waitcnt lgkmcnt(4)\n\t"
        "v_smfmac_f32_16x16x64_bf16 v[0:3], v[20:23], v[184:191], v110\n\t"
        "v_smfmac_f32_16x16x64_bf16 v[4:7], v[20:23], v[192:199], v110\n\t"
        "buffer_load_dwordx4 v[104:107], v120, s[8:11], s26 offen sc1\n\t"
        "s_waitcnt lgkmcnt(1)\n\t"
        "v_smfmac_f32_16x16x64_bf16 v[0:3], v[24:27], v[208:215], v110\n\t"
        "v_smfmac_f32_16x16x64_bf16 v[4:7], v[24:27], v[216:223], v110\n\t"
        "buffer_store_dword v126, v127, s[12:15], 0 offen sc1\n\t"
        "s_waitcnt lgkmcnt(0)\n\t"
        "v_smfmac_f32_16x16x64_bf16 v[0:3], v[28:31], v[232:239], v110\n\t"
        "v_smfmac_f32_16x16x64_bf16 v[4:7], v[28:31], v[240:247], v110\n\t"
        "v_smfmac_f32_16x16x64_bf16 v[12:15], v[20:23], v[80:87], v110\n\t"
        "s_nop 1\n\t"
        "v_smfmac_f32_16x16x64_bf16 v[8:11], v[20:23], v[200:207], v110\n\t"
        "s_nop 1\n\t"
        "v_smfmac_f32_16x16x64_bf16 v[12:15], v[24:27], v[88:95], v110\n\t"
        "s_nop 0\n\t"
        "v_permlane32_swap_b32_e32 v0, v4\n\t"
        "v_permlane32_swap_b32_e32 v1, v5\n\t"
        "v_permlane32_swap_b32_e32 v2, v6\n\t"
        "v_smfmac_f32_16x16x64_bf16 v[8:11], v[24:27], v[224:231], v110\n\t"
        "v_permlane32_swap_b32_e32 v3, v7\n\t"
        "v_add_f32_e32 v0, v0, v4\n\t"
        "v_add_f32_e32 v1, v1, v5\n\t"
        "v_smfmac_f32_16x16x64_bf16 v[12:15], v[28:31], v[96:103], v110\n\t"
        "v_add_f32_e32 v2, v2, v6\n\t"
        "v_add_f32_e32 v3, v3, v7\n\t"
        "v_cvt_pk_bf16_f32 v4, v0, v1\n\t"
        "v_smfmac_f32_16x16x64_bf16 v[8:11], v[28:31], v[248:255], v110\n\t"
        "v_cvt_pk_bf16_f32 v5, v2, v3\n\t"
        "ds_write_b64 v118, v[4:5]\n\t"
        "s_lshl_b32 s34, s43, 11\n\t"
        "v_add_u32_e32 v108, s34, v124\n\t"
        "s_nop 3\n\t"
        "v_permlane32_swap_b32_e32 v8, v12\n\t"
        "v_permlane32_swap_b32_e32 v9, v13\n\t"
        "v_permlane32_swap_b32_e32 v10, v14\n\t"
        "v_permlane32_swap_b32_e32 v11, v15\n\t"
        "v_add_f32_e32 v8, v8, v12\n\t"
        "v_add_f32_e32 v9, v9, v13\n\t"
        "v_add_f32_e32 v10, v10, v14\n\t"
        "v_add_f32_e32 v11, v11, v15\n\t"
        "v_cvt_pk_bf16_f32 v12, v8, v9\n\t"
        "v_cvt_pk_bf16_f32 v13, v10, v11\n\t"
        "ds_write_b64 v118, v[12:13] offset:256\n\t"
        "s_waitcnt lgkmcnt(0)\n\t"
        "s_barrier\n\t"
        "ds_read_b64_tr_b16 v[16:17], v119 offset:0\n\t"
        "ds_read_b64_tr_b16 v[18:19], v119 offset:32\n\t"
        "ds_read_b64_tr_b16 v[20:21], v119 offset:256\n\t"
        "ds_read_b64_tr_b16 v[22:23], v119 offset:288\n\t"
        "ds_read_b64_tr_b16 v[24:25], v119 offset:512\n\t"
        "ds_read_b64_tr_b16 v[26:27], v119 offset:544\n\t"
        "ds_read_b64_tr_b16 v[28:29], v119 offset:768\n\t"
        "ds_read_b64_tr_b16 v[30:31], v119 offset:800\n\t"
        "s_waitcnt lgkmcnt(6)\n\t"
        "v_mfma_f32_16x16x32_bf16 v[0:3], v[32:35], v[16:19], 0\n\t"
        "ds_read_b64_tr_b16 v[32:33], v119 offset:2112\n\t"
        "ds_read_b64_tr_b16 v[34:35], v119 offset:2144\n\t"
        "v_mfma_f32_16x16x32_bf16 v[4:7], v[36:39], v[16:19], 0\n\t"
        "ds_read_b64_tr_b16 v[36:37], v119 offset:2368\n\t"
        "ds_read_b64_tr_b16 v[38:39], v119 offset:2400\n\t"
        "s_waitcnt lgkmcnt(8)\n\t"
        "v_mfma_f32_16x16x32_bf16 v[0:3], v[40:43], v[20:23], v[0:3]\n\t"
        "ds_read_b64_tr_b16 v[40:41], v119 offset:2624\n\t"
        "ds_read_b64_tr_b16 v[42:43], v119 offset:2656\n\t"
        "v_mfma_f32_16x16x32_bf16 v[4:7], v[44:47], v[20:23], v[4:7]\n\t"
        "ds_read_b64_tr_b16 v[44:45], v119 offset:2880\n\t"
        "ds_read_b64_tr_b16 v[46:47], v119 offset:2912\n\t"
        "s_waitcnt lgkmcnt(10)\n\t"
        "v_mfma_f32_16x16x32_bf16 v[0:3], v[48:51], v[24:27], v[0:3]\n\t"
        "ds_read_b128 v[48:51], v108 offset:16384\n\t"
        "v_mfma_f32_16x16x32_bf16 v[4:7], v[52:55], v[24:27], v[4:7]\n\t"
        "ds_read_b128 v[52:55], v108 offset:17408\n\t"
        "s_waitcnt lgkmcnt(10)\n\t"
        "v_mfma_f32_16x16x32_bf16 v[0:3], v[56:59], v[28:31], v[0:3]\n\t"
        "v_mfma_f32_16x16x32_bf16 v[4:7], v[60:63], v[28:31], v[4:7]\n\t"
        "s_waitcnt lgkmcnt(2)\n\t"
        "v_mfma_f32_16x16x32_bf16 v[8:11], v[32:35], v[16:19], 0\n\t"
        "s_nop 1\n\t"
        "v_mfma_f32_16x16x32_bf16 v[8:11], v[36:39], v[20:23], v[8:11]\n\t"
        "s_nop 1\n\t"
        "v_mfma_f32_16x16x32_bf16 v[8:11], v[40:43], v[24:27], v[8:11]\n\t"
        "v_max3_f32 v59, v0, v1, v2\n\t"
        "v_max3_f32 v59, v59, v3, v4\n\t"
        "v_mfma_f32_16x16x32_bf16 v[8:11], v[44:47], v[28:31], v[8:11]\n\t"
        "v_max3_f32 v59, v59, v5, v6\n\t"
        "v_max_f32_e32 v60, v59, v7\n\t"
        "v_max_f32_e32 v59, v59, v7\n\t"
        "v_cndmask_b32_e64 v0, v0, v4, s[38:39]\n\t"
        "v_cndmask_b32_e64 v1, v1, v5, s[38:39]\n\t"
        "v_permlane32_swap_b32_e32 v59, v60\n\t"
        "v_cndmask_b32_e64 v2, v2, v6, s[38:39]\n\t"
        "v_cndmask_b32_e64 v3, v3, v7, s[38:39]\n\t"
        "v_max3_f32 v59, v59, v60, v8\n\t"
        "v_max3_f32 v59, v59, v9, v10\n\t"
        "v_max_f32_e32 v60, v59, v11\n\t"
        "v_max_f32_e32 v59, v59, v11\n\t"
        "v_cndmask_b32_e64 v8, v8, v9, s[38:39]\n\t"
        "v_cndmask_b32_e64 v10, v10, v11, s[38:39]\n\t"
        "v_permlane16_swap_b32_e32 v59, v60\n\t"
        "v_max_f32_e32 v59, v59, v60\n\t"
        "v_mul_f32_e32 v59, 0xbe02940c, v59\n\t"
        "v_cndmask_b32_e64 v58, v8, v10, s[40:41]\n\t"
        "v_fmamk_f32 v0, v0, 0x3e02940c, v59\n\t"
        "v_exp_f32_e32 v0, v0\n\t"
        "v_fmamk_f32 v1, v1, 0x3e02940c, v59\n\t"
        "v_exp_f32_e32 v1, v1\n\t"
        "v_fmamk_f32 v2, v2, 0x3e02940c, v59\n\t"
        "v_exp_f32_e32 v2, v2\n\t"
        "v_fmamk_f32 v3, v3, 0x3e02940c, v59\n\t"
        "v_exp_f32_e32 v3, v3\n\t"
        "v_fmamk_f32 v58, v58, 0x3e02940c, v59\n\t"
        "v_cvt_pk_bf16_f32 v12, v0, v1\n\t"
        "v_cvt_pk_bf16_f32 v13, v2, v3\n\t"
        "v_exp_f32_e32 v58, v58\n\t"
        "v_add_f32_e32 v60, v0, v1\n\t"
        "v_mov_b32_dpp v14, v12 row_ror:8 row_mask:0xf bank_mask:0xf\n\t"
        "v_mov_b32_dpp v15, v13 row_ror:8 row_mask:0xf bank_mask:0xf\n\t"
        "v_mov_b32_dpp v59, v58 row_ror:8 row_mask:0xf bank_mask:0xf\n\t"
        "v_add_f32_e32 v61, v2, v3\n\t"
        "s_waitcnt lgkmcnt(0)\n\t"
        "v_mfma_f32_16x16x32_bf16 v[4:7], v[48:51], v[12:15], 0\n\t"
        "v_cvt_pk_bf16_f32 v59, v58, v59\n\t"
        "v_add_f32_e32 v60, v60, v58\n\t"
        "v_mfma_f32_16x16x32_bf16 v[8:11], v[52:55], v[12:15], 0\n\t"
        "v_cndmask_b32_e64 v56, v59, 0, s[40:41]\n\t"
        "v_cndmask_b32_e64 v57, 0, v59, s[40:41]\n\t"
        "v_add_f32_e32 v59, v60, v61\n\t"
        "s_waitcnt vmcnt(1)\n\t"
        "v_max3_u32 v111, v104, v105, v106\n\t"
        "v_mfma_f32_16x16x16_bf16 v[4:7], v[104:105], v[56:57], v[4:7]\n\t"
        "v_add_f32_dpp v60, v59, v59 row_ror:8 row_mask:0xf bank_mask:0xf\n\t"
        "v_add_f32_dpp v61, v59, v59 row_ror:8 row_mask:0xf bank_mask:0xf\n\t"
        "v_mfma_f32_16x16x16_bf16 v[8:11], v[106:107], v[56:57], v[8:11]\n\t"
        "v_max_u32_e32 v111, v111, v107\n\t"
        "v_permlane16_swap_b32_e32 v60, v61\n\t"
        "v_add_f32_e32 v59, v60, v61\n\t"
        "v_add_f32_e32 v60, v60, v61\n\t"
        "v_cmp_eq_u32_e32 vcc, -1, v111\n\t"
        "s_nop 0\n\t"
        "v_permlane32_swap_b32_e32 v59, v60\n\t"
        "v_add_f32_e32 v59, v59, v60\n\t"
        "v_rcp_f32_e32 v59, v59\n\t"
        "s_cbranch_vccnz LA_vmfail%=\n\t"
        "LA_vmok%=:\n\t"
        "v_mul_f32_e32 v4, v4, v59\n\t"
        "v_mul_f32_e32 v5, v5, v59\n\t"
        "v_cvt_pk_bf16_f32 v0, v4, v5\n\t"
        "v_min_u32_e32 v0, -2, v0\n\t"
        "v_mul_f32_e32 v6, v6, v59\n\t"
        "v_mul_f32_e32 v7, v7, v59\n\t"
        "v_cvt_pk_bf16_f32 v1, v6, v7\n\t"
        "v_min_u32_e32 v1, -2, v1\n\t"
        "v_mul_f32_e32 v8, v8, v59\n\t"
        "v_mul_f32_e32 v9, v9, v59\n\t"
        "v_cvt_pk_bf16_f32 v2, v8, v9\n\t"
        "v_min_u32_e32 v2, -2, v2\n\t"
        "v_mul_f32_e32 v10, v10, v59\n\t"
        "v_mul_f32_e32 v11, v11, v59\n\t"
        "v_cvt_pk_bf16_f32 v3, v10, v11\n\t"
        "v_min_u32_e32 v3, -2, v3\n\t"
        "buffer_store_dwordx4 v[0:3], v121, s[8:11], s26 offen sc1\n\t"
        "s_memrealtime s[52:53]\n\t"
        "s_and_b32 s35, s43, 1\n\t"
        "s_lshl_b32 s35, s35, 12\n\t"
        "v_add_u32_e32 v109, s35, v124\n\t"
        "ds_read_b128 v[16:19], v109 offset:8192\n\t"
        "ds_read_b128 v[20:23], v109 offset:9216\n\t"
        "ds_read_b128 v[24:27], v109 offset:10240\n\t"
        "ds_read_b128 v[28:31], v109 offset:11264\n\t"
        "ds_read_b128 v[56:59], v124 offset:0\n\t"
        "ds_read_b128 v[60:63], v124 offset:4096\n\t"
        "ds_read_b128 v[12:15], v124 offset:1024\n\t"
        "v_xor_b32_e32 v7, 0x800, v108\n\t"
        "s_add_u32 s48, s24, 1\n\t"
        "s_min_u32 s48, s48, 63\n\t"
        "buffer_load_dwordx2 v[108:109], v125, s[8:11], s26 offen sc1\n\t"
        "s_lshl_b32 s35, s44, 7\n\t"
        "v_lshrrev_b32_e32 v111, 5, v125\n\t"
        "v_and_b32_e32 v111, 60, v111\n\t"
        "buffer_load_dword v111, v111, s[12:15], s35 offen offset:0 sc1\n\t"
        "s_lshr_b32 s36, s48, 1\n\t"
        "s_cmp_gt_u32 s54, s36\n\t"
        "s_cbranch_scc1 LA_kvok%=\n\t"
        "s_mov_b32 s42, 0\n\t"
        "LA_kv%=:\n\t"
        "s_waitcnt vmcnt(0)\n\t"
        "v_min_u32_dpp v111, v111, v111 quad_perm:[1,0,3,2] row_mask:0xf bank_mask:0xf\n\t"
        "s_nop 1\n\t"
        "v_min_u32_dpp v111, v111, v111 quad_perm:[2,3,0,1] row_mask:0xf bank_mask:0xf\n\t"
        "s_nop 1\n\t"
        "v_min_u32_dpp v111, v111, v111 row_ror:4 row_mask:0xf bank_mask:0xf\n\t"
        "s_nop 1\n\t"
        "v_min_u32_dpp v111, v111, v111 row_ror:8 row_mask:0xf bank_mask:0xf\n\t"
        "s_nop 0\n\t"
        "v_readfirstlane_b32 s54, v111\n\t"
        "s_cmp_gt_u32 s54, s36\n\t"
        "s_cbranch_scc1 LA_kvok%=\n\t"
        "s_sleep 4\n\t"
        "s_lshl_b32 s35, s44, 7\n\t"
        "v_lshrrev_b32_e32 v111, 5, v125\n\t"
        "v_and_b32_e32 v111, 60, v111\n\t"
        "buffer_load_dword v111, v111, s[12:15], s35 offen offset:0 sc1\n\t"
        "s_add_u32 s42, s42, 1\n\t"
        "s_cmp_lt_u32 s42, 0x100000\n\t"
        "s_cbranch_scc1 LA_kv%=\n\t"
        "s_movk_i32 s34, 16\n\t"
        "s_branch LA_die%=\n\t"
        "LA_kvok%=:\n\t"
        "s_add_u32 s35, s24, 1\n\t"
        "s_and_b32 s36, s35, 1\n\t"
        "s_mul_i32 s36, s36, 24576\n\t"
        "s_add_u32 s36, s36, s55\n\t"
        "s_lshl_b32 s37, s48, 18\n\t"
        "s_lshl_b32 s34, s43, 11\n\t"
        "s_add_u32 s37, s37, s34\n\t"
        "s_waitcnt lgkmcnt(3)\n\t"
        "v_mfma_f32_16x16x32_bf16 v[8:11], v[32:35], v[16:19], 0\n\t"
        "v_mfma_f32_16x16x32_bf16 v[8:11], v[36:39], v[20:23], v[8:11]\n\t"
        "v_mfma_f32_16x16x32_bf16 v[8:11], v[40:43], v[24:27], v[8:11]\n\t"
        "v_mfma_f32_16x16x32_bf16 v[8:11], v[44:47], v[28:31], v[8:11]\n\t"
        "v_mov_b32_e32 v43, v7\n\t"
        "s_waitcnt lgkmcnt(2)\n\t"
        "v_mfma_f32_16x16x32_bf16 v[0:3], v[56:59], v[16:19], 0\n\t"
        "ds_read_b128 v[56:59], v124 offset:5120\n\t"
        "s_waitcnt lgkmcnt(2)\n\t"
        "v_mfma_f32_16x16x32_bf16 v[4:7], v[60:63], v[16:19], 0\n\t"
        "ds_read_b128 v[60:63], v124 offset:2048\n\t"
        "s_waitcnt lgkmcnt(2)\n\t"
        "v_mfma_f32_16x16x32_bf16 v[0:3], v[12:15], v[20:23], v[0:3]\n\t"
        "ds_read_b128 v[12:15], v124 offset:6144\n\t"
        "s_waitcnt lgkmcnt(2)\n\t"
        "v_mfma_f32_16x16x32_bf16 v[4:7], v[56:59], v[20:23], v[4:7]\n\t"
        "ds_read_b128 v[56:59], v124 offset:3072\n\t"
        "s_waitcnt lgkmcnt(2)\n\t"
        "v_mfma_f32_16x16x32_bf16 v[0:3], v[60:63], v[24:27], v[0:3]\n\t"
        "ds_read_b128 v[60:63], v124 offset:7168\n\t"
        "s_waitcnt lgkmcnt(2)\n\t"
        "v_mfma_f32_16x16x32_bf16 v[4:7], v[12:15], v[24:27], v[4:7]\n\t"
        "v_xor_b32_e32 v42, 0x200, v120\n\t"
        "buffer_load_dwordx4 v[12:15], v42, s[8:11], s26 offen sc1\n\t"
        "s_waitcnt lgkmcnt(1)\n\t"
        "v_mfma_f32_16x16x32_bf16 v[0:3], v[56:59], v[28:31], v[0:3]\n\t"
        "s_waitcnt lgkmcnt(0)\n\t"
        "v_mfma_f32_16x16x32_bf16 v[4:7], v[60:63], v[28:31], v[4:7]\n\t"
        "ds_read_b128 v[56:59], v43 offset:16384\n\t"
        "ds_read_b128 v[60:63], v43 offset:17408\n\t"
        "v_mov_b64_e32 v[44:45], -1\n\t"
        "v_mov_b64_e32 v[46:47], -1\n\t"
        "v_mov_b32_e32 v40, 0xce6e6b28\n\t"
        "s_add_u32 m0, s36, 0\n\t"
        "s_add_u32 s34, s37, 29548544\n\t"
        "buffer_load_dwordx4 v122, s[12:15], s34 offen sc1 lds\n\t"
        "v_cvt_pk_bf16_f32 v8, 0, v8\n\t"
        "v_cvt_pk_bf16_f32 v9, 0, v9\n\t"
        "v_cvt_pk_bf16_f32 v10, 0, v10\n\t"
        "v_cvt_pk_bf16_f32 v11, 0, v11\n\t"
        "v_cvt_pk_bf16_f32 v0, 0, v0\n\t"
        "v_cvt_pk_bf16_f32 v1, 0, v1\n\t"
        "s_add_u32 m0, s36, 1024\n\t"
        "s_add_u32 s34, s37, 29549568\n\t"
        "buffer_load_dwordx4 v122, s[12:15], s34 offen sc1 lds\n\t"
        "v_cvt_pk_bf16_f32 v2, 0, v2\n\t"
        "v_cvt_pk_bf16_f32 v3, 0, v3\n\t"
        "v_cvt_pk_bf16_f32 v4, 0, v4\n\t"
        "v_cvt_pk_bf16_f32 v5, 0, v5\n\t"
        "v_cvt_pk_bf16_f32 v6, 0, v6\n\t"
        "v_cvt_pk_bf16_f32 v7, 0, v7\n\t"
        "s_add_u32 m0, s36, 8192\n\t"
        "s_add_u32 s34, s37, 12771328\n\t"
        "buffer_load_dwordx4 v122, s[12:15], s34 offen sc1 lds\n\t"
        "v_mul_f32_e32 v8, 0x3db504f3, v8\n\t"
        "v_mul_f32_e32 v9, 0x3db504f3, v9\n\t"
        "v_mul_f32_e32 v10, 0x3db504f3, v10\n\t"
        "v_mul_f32_e32 v11, 0x3db504f3, v11\n\t"
        "v_mul_f32_e32 v0, 0x3db504f3, v0\n\t"
        "v_mul_f32_e32 v1, 0x3db504f3, v1\n\t"
        "s_add_u32 m0, s36, 9216\n\t"
        "s_add_u32 s34, s37, 12772352\n\t"
        "buffer_load_dwordx4 v122, s[12:15], s34 offen sc1 lds\n\t"
        "v_mul_f32_e32 v2, 0x3db504f3, v2\n\t"
        "v_mul_f32_e32 v3, 0x3db504f3, v3\n\t"
        "v_mul_f32_e32 v4, 0x3db504f3, v4\n\t"
        "v_mul_f32_e32 v5, 0x3db504f3, v5\n\t"
        "v_mul_f32_e32 v6, 0x3db504f3, v6\n\t"
        "v_mul_f32_e32 v7, 0x3db504f3, v7\n\t"
        "s_add_u32 m0, s36, 16384\n\t"
        "s_add_u32 s34, s37, 46325760\n\t"
        "buffer_load_dwordx4 v122, s[12:15], s34 offen sc1 lds\n\t"
        "v_cvt_pk_bf16_f32 v8, 0, v8\n\t"
        "v_cvt_pk_bf16_f32 v9, 0, v9\n\t"
        "v_cvt_pk_bf16_f32 v10, 0, v10\n\t"
        "v_cvt_pk_bf16_f32 v11, 0, v11\n\t"
        "v_cvt_pk_bf16_f32 v0, 0, v0\n\t"
        "v_cvt_pk_bf16_f32 v1, 0, v1\n\t"
        "s_add_u32 m0, s36, 17408\n\t"
        "s_add_u32 s34, s37, 46326784\n\t"
        "buffer_load_dwordx4 v122, s[12:15], s34 offen sc1 lds\n\t"
        "v_cvt_pk_bf16_f32 v2, 0, v2\n\t"
        "v_cvt_pk_bf16_f32 v3, 0, v3\n\t"
        "v_cvt_pk_bf16_f32 v4, 0, v4\n\t"
        "v_cvt_pk_bf16_f32 v5, 0, v5\n\t"
        "v_cvt_pk_bf16_f32 v6, 0, v6\n\t"
        "v_cvt_pk_bf16_f32 v7, 0, v7\n\t"
        "buffer_store_dwordx4 v[44:47], v121, s[8:11], s28 offen sc1\n\t"
        "v_cndmask_b32_e64 v0, v40, v0, s[60:61]\n\t"
        "v_cndmask_b32_e64 v1, v40, v1, s[62:63]\n\t"
        "v_cndmask_b32_e64 v2, v40, v2, s[64:65]\n\t"
        "v_cndmask_b32_e64 v3, v40, v3, s[66:67]\n\t"
        "v_cndmask_b32_e64 v4, v40, v4, s[68:69]\n\t"
        "v_cndmask_b32_e64 v5, v40, v5, s[70:71]\n\t"
        "v_cndmask_b32_e64 v6, v40, v6, s[72:73]\n\t"
        "v_cndmask_b32_e64 v7, v40, v7, s[74:75]\n\t"
        "v_max3_f32 v41, v0, v1, v2\n\t"
        "v_max3_f32 v41, v41, v3, v4\n\t"
        "v_max3_f32 v41, v41, v5, v6\n\t"
        "v_max3_f32 v41, v41, v7, v8\n\t"
        "v_max3_f32 v41, v41, v9, v10\n\t"
        "v_max_f32_e32 v42, v41, v11\n\t"
        "v_max_f32_e32 v41, v41, v11\n\t"
        "s_nop 1\n\t"
        "v_permlane16_swap_b32_e32 v41, v42\n\t"
        "v_max_f32_e32 v43, v41, v42\n\t"
        "v_max_f32_e32 v41, v41, v42\n\t"
        "s_nop 1\n\t"
        "v_permlane32_swap_b32_e32 v41, v43\n\t"
        "v_max_f32_e32 v41, v41, v43\n\t"
        "v_mul_f32_e32 v41, 0xbfb8aa3b, v41\n\t"
        "v_fmamk_f32 v0, v0, 0x3fb8aa3b, v41\n\t"
        "v_exp_f32_e32 v0, v0\n\t"
        "v_fmamk_f32 v1, v1, 0x3fb8aa3b, v41\n\t"
        "v_exp_f32_e32 v1, v1\n\t"
        "v_fmamk_f32 v2, v2, 0x3fb8aa3b, v41\n\t"
        "v_exp_f32_e32 v2, v2\n\t"
        "v_fmamk_f32 v3, v3, 0x3fb8aa3b, v41\n\t"
        "v_exp_f32_e32 v3, v3\n\t"
        "v_fmamk_f32 v4, v4, 0x3fb8aa3b, v41\n\t"
        "v_exp_f32_e32 v4, v4\n\t"
        "v_fmamk_f32 v5, v5, 0x3fb8aa3b, v41\n\t"
        "v_exp_f32_e32 v5, v5\n\t"
        "v_fmamk_f32 v6, v6, 0x3fb8aa3b, v41\n\t"
        "v_exp_f32_e32 v6, v6\n\t"
        "v_fmamk_f32 v7, v7, 0x3fb8aa3b, v41\n\t"
        "v_exp_f32_e32 v7, v7\n\t"
        "v_fmamk_f32 v8, v8, 0x3fb8aa3b, v41\n\t"
        "v_exp_f32_e32 v8, v8\n\t"
        "v_fmamk_f32 v9, v9, 0x3fb8aa3b, v41\n\t"
        "v_exp_f32_e32 v9, v9\n\t"
        "v_fmamk_f32 v10, v10, 0x3fb8aa3b, v41\n\t"
        "v_exp_f32_e32 v10, v10\n\t"
        "v_fmamk_f32 v11, v11, 0x3fb8aa3b, v41\n\t"
        "v_exp_f32_e32 v11, v11\n\t"
        "v_cndmask_b32_e64 v8, v8, 0, s[40:41]\n\t"
        "v_cndmask_b32_e64 v9, v9, 0, s[40:41]\n\t"
        "v_cndmask_b32_e64 v10, v10, 0, s[40:41]\n\t"
        "v_cndmask_b32_e64 v11, v11, 0, s[40:41]\n\t"
        "v_add_f32_e32 v41, v0, v1\n\t"
        "v_add_f32_e32 v41, v41, v2\n\t"
        "v_add_f32_e32 v41, v41, v3\n\t"
        "v_add_f32_e32 v41, v41, v4\n\t"
        "v_add_f32_e32 v41, v41, v5\n\t"
        "v_add_f32_e32 v41, v41, v6\n\t"
        "v_add_f32_e32 v41, v41, v7\n\t"
        "v_add_f32_e32 v41, v41, v8\n\t"
        "v_add_f32_e32 v41, v41, v9\n\t"
        "v_add_f32_e32 v41, v41, v10\n\t"
        "v_add_f32_e32 v42, v41, v11\n\t"
        "v_add_f32_e32 v41, v41, v11\n\t"
        "s_nop 1\n\t"
        "v_permlane16_swap_b32_e32 v41, v42\n\t"
        "v_add_f32_e32 v43, v41, v42\n\t"
        "v_add_f32_e32 v41, v41, v42\n\t"
        "s_nop 1\n\t"
        "v_permlane32_swap_b32_e32 v41, v43\n\t"
        "v_add_f32_e32 v41, v41, v43\n\t"
        "v_rcp_f32_e32 v41, v41\n\t"
        "s_nop 0\n\t"
        "v_mul_f32_e32 v0, v0, v41\n\t"
        "v_mul_f32_e32 v1, v1, v41\n\t"
        "v_cvt_pk_bf16_f32 v32, v0, v1\n\t"
        "v_mul_f32_e32 v2, v2, v41\n\t"
        "v_mul_f32_e32 v3, v3, v41\n\t"
        "v_cvt_pk_bf16_f32 v33, v2, v3\n\t"
        "v_mul_f32_e32 v4, v4, v41\n\t"
        "v_mul_f32_e32 v5, v5, v41\n\t"
        "v_cvt_pk_bf16_f32 v34, v4, v5\n\t"
        "v_mul_f32_e32 v6, v6, v41\n\t"
        "v_mul_f32_e32 v7, v7, v41\n\t"
        "v_cvt_pk_bf16_f32 v35, v6, v7\n\t"
        "v_mul_f32_e32 v8, v8, v41\n\t"
        "v_mul_f32_e32 v9, v9, v41\n\t"
        "v_cvt_pk_bf16_f32 v36, v8, v9\n\t"
        "v_mul_f32_e32 v10, v10, v41\n\t"
        "v_mul_f32_e32 v11, v11, v41\n\t"
        "v_cvt_pk_bf16_f32 v37, v10, v11\n\t"
        "s_waitcnt lgkmcnt(0)\n\t"
        "v_mfma_f32_16x16x32_bf16 v[16:19], v[48:51], v[32:35], 0\n\t"
        "v_mfma_f32_16x16x32_bf16 v[20:23], v[52:55], v[32:35], 0\n\t"
        "v_mfma_f32_16x16x32_bf16 v[24:27], v[56:59], v[32:35], 0\n\t"
        "v_mfma_f32_16x16x32_bf16 v[28:31], v[60:63], v[32:35], 0\n\t"
        "s_waitcnt vmcnt(7)\n\t"
        "v_max3_u32 v43, v12, v13, v14\n\t"
        "v_max_u32_e32 v43, v43, v15\n\t"
        "v_cmp_eq_u32_e32 vcc, -1, v43\n\t"
        "s_nop 0\n\t"
        "s_cbranch_vccnz LA_v2fail%=\n\t"
        "LA_v2ok%=:\n\t"
        "v_mfma_f32_16x16x16_bf16 v[16:19], v[104:105], v[36:37], v[16:19]\n\t"
        "v_mfma_f32_16x16x16_bf16 v[20:23], v[106:107], v[36:37], v[20:23]\n\t"
        "v_mfma_f32_16x16x16_bf16 v[24:27], v[12:13], v[36:37], v[24:27]\n\t"
        "v_mfma_f32_16x16x16_bf16 v[28:31], v[14:15], v[36:37], v[28:31]\n\t"
        "s_lshl_b32 s34, s24, 18\n\t"
        "s_add_u32 s34, s34, 63102976\n\t"
        "v_xor_b32_e32 v43, 0x400, v123\n\t"
        "s_nop 1\n\t"
        "v_cvt_pk_bf16_f32 v0, v16, v17\n\t"
        "v_cvt_pk_bf16_f32 v1, v18, v19\n\t"
        "v_cvt_pk_bf16_f32 v2, v20, v21\n\t"
        "v_cvt_pk_bf16_f32 v3, v22, v23\n\t"
        "buffer_store_dwordx4 v[0:3], v123, s[12:15], s34 offen sc1\n\t"
        "v_cvt_pk_bf16_f32 v4, v24, v25\n\t"
        "v_cvt_pk_bf16_f32 v5, v26, v27\n\t"
        "v_cvt_pk_bf16_f32 v6, v28, v29\n\t"
        "v_cvt_pk_bf16_f32 v7, v30, v31\n\t"
        "buffer_store_dwordx4 v[4:7], v43, s[12:15], s34 offen sc1\n\t"
        "s_add_u32 s34, s24, 1\n\t"
        "v_mov_b32_e32 v126, s34\n\t"
        "s_cmp_eq_u32 s24, 63\n\t"
        "s_cbranch_scc1 LA_last%=\n\t"
        "v_add_u32_e32 v124, s56, v124\n\t"
        "s_sub_u32 s56, 0, s56\n\t"
        "ds_read_b128 v[32:35], v117 offset:0\n\t"
        "ds_read_b128 v[36:39], v117 offset:1024\n\t"
        "ds_read_b128 v[40:43], v117 offset:2048\n\t"
        "ds_read_b128 v[44:47], v117 offset:3072\n\t"
        "ds_read_b128 v[48:51], v117 offset:4096\n\t"
        "ds_read_b128 v[52:55], v117 offset:5120\n\t"
        "ds_read_b128 v[56:59], v117 offset:6144\n\t"
        "ds_read_b128 v[60:63], v117 offset:7168\n\t"
        "v_mov_b64_e32 v[0:1], 0\n\t"
        "v_mov_b64_e32 v[2:3], 0\n\t"
        "v_mov_b64_e32 v[4:5], 0\n\t"
        "v_mov_b64_e32 v[6:7], 0\n\t"
        "v_mov_b64_e32 v[8:9], 0\n\t"
        "v_mov_b64_e32 v[10:11], 0\n\t"
        "v_mov_b64_e32 v[12:13], 0\n\t"
        "v_mov_b64_e32 v[14:15], 0\n\t"
        "v_min_u32_dpp v111, v111, v111 quad_perm:[1,0,3,2] row_mask:0xf bank_mask:0xf\n\t"
        "s_nop 1\n\t"
        "v_min_u32_dpp v111, v111, v111 quad_perm:[2,3,0,1] row_mask:0xf bank_mask:0xf\n\t"
        "s_nop 1\n\t"
        "v_min_u32_dpp v111, v111, v111 row_ror:4 row_mask:0xf bank_mask:0xf\n\t"
        "s_nop 1\n\t"
        "v_min_u32_dpp v111, v111, v111 row_ror:8 row_mask:0xf bank_mask:0xf\n\t"
        "s_nop 0\n\t"
        "v_readfirstlane_b32 s54, v111\n\t"
        "v_cmp_ne_u32_e32 vcc, s24, v109\n\t"
        "s_cbranch_vccz LA_stok%=\n\t"
        "s_mov_b32 s42, 0\n\t"
        "LA_stre%=:\n\t"
        "buffer_load_dwordx2 v[108:109], v125, s[8:11], s26 offen sc1\n\t"
        "s_waitcnt vmcnt(0)\n\t"
        "v_cmp_ne_u32_e32 vcc, s24, v109\n\t"
        "s_cbranch_vccz LA_stok%=\n\t"
        "s_add_u32 s42, s42, 1\n\t"
        "s_cmp_lt_u32 s42, 0x100000\n\t"
        "s_cbranch_scc1 LA_stre%=\n\t"
        "s_movk_i32 s34, 5\n\t"
        "s_branch LA_die%=\n\t"
        "LA_stok%=:\n\t"
        "v_subrev_u32_e32 v108, s25, v108\n\t"
        "s_nop 1\n\t"
        "v_max_i32_dpp v108, v108, v108 quad_perm:[1,0,3,2] row_mask:0xf bank_mask:0xf\n\t"
        "s_nop 1\n\t"
        "v_max_i32_dpp v108, v108, v108 quad_perm:[2,3,0,1] row_mask:0xf bank_mask:0xf\n\t"
        "s_nop 1\n\t"
        "v_max_i32_dpp v108, v108, v108 row_ror:4 row_mask:0xf bank_mask:0xf\n\t"
        "s_nop 1\n\t"
        "v_max_i32_dpp v108, v108, v108 row_ror:8 row_mask:0xf bank_mask:0xf\n\t"
        "v_mov_b32_e32 v109, v108\n\t"
        "s_nop 1\n\t"
        "v_permlane16_swap_b32_e32 v108, v109\n\t"
        "v_max_i32_e32 v108, v108, v109\n\t"
        "v_mov_b32_e32 v109, v108\n\t"
        "s_nop 1\n\t"
        "v_permlane32_swap_b32_e32 v108, v109\n\t"
        "v_max_i32_e32 v108, v108, v109\n\t"
        "s_nop 0\n\t"
        "v_readfirstlane_b32 s34, v108\n\t"
        "s_nop 0\n\t"
        "s_add_i32 s34, s34, s51\n\t"
        "s_add_u32 s35, s31, s50\n\t"
        "s_mul_i32 s36, s50, 3\n\t"
        "s_min_u32 s35, s35, s36\n\t"
        "s_cmp_gt_i32 s34, 0\n\t"
        "s_cselect_b32 s31, s35, 0\n\t"
        "s_cselect_b32 s34, s34, 0\n\t"
        "s_add_u32 s34, s34, s31\n\t"
        "s_add_u32 s34, s34, 3\n\t"
        "s_and_b32 s34, s34, -4\n\t"
        "s_add_u32 s37, s25, s34\n\t"
        "s_add_u32 s45, s37, s29\n\t"
        "s_add_u32 s25, s45, s30\n\t"
        "s_add_u32 s24, s24, 1\n\t"
        "s_mov_b32 s28, s26\n\t"
        "s_mov_b32 s26, s27\n\t"
        "s_add_u32 s27, s27, 0x80000\n\t"
        "s_and_b32 s27, s27, 0x1fffff\n\t"
        "s_branch LA_loop%=\n\t"
        "LA_last%=:\n\t"
        "s_waitcnt vmcnt(0)\n\t"
        "s_barrier\n\t"
        "buffer_store_dword v126, v127, s[12:15], 0 offen sc1\n\t"
        "s_endpgm\n\t"
        "LA_v2fail%=:\n\t"
        "s_mov_b32 s42, 0\n\t"
        "LA_v2re%=:\n\t"
        "v_xor_b32_e32 v43, 0x200, v120\n\t"
        "buffer_load_dwordx4 v[12:15], v43, s[8:11], s26 offen sc1\n\t"
        "s_waitcnt vmcnt(0)\n\t"
        "v_max3_u32 v43, v12, v13, v14\n\t"
        "v_max_u32_e32 v43, v43, v15\n\t"
        "v_cmp_eq_u32_e32 vcc, -1, v43\n\t"
        "s_nop 0\n\t"
        "s_cbranch_vccz LA_v2ok%=\n\t"
        "s_add_u32 s42, s42, 1\n\t"
        "s_cmp_lt_u32 s42, 0x100000\n\t"
        "s_cbranch_scc1 LA_v2re%=\n\t"
        "s_movk_i32 s34, 6\n\t"
        "s_branch LA_die%=\n\t"
        "LA_rdfail%=:\n\t"
        "s_or_b32 s47, s47, 1\n\t"
        "s_nop 7\n\t"
        "ds_read_b128 v[32:35], v117 offset:0\n\t"
        "ds_read_b128 v[36:39], v117 offset:1024\n\t"
        "ds_read_b128 v[40:43], v117 offset:2048\n\t"
        "ds_read_b128 v[44:47], v117 offset:3072\n\t"
        "ds_read_b128 v[48:51], v117 offset:4096\n\t"
        "ds_read_b128 v[52:55], v117 offset:5120\n\t"
        "ds_read_b128 v[56:59], v117 offset:6144\n\t"
        "ds_read_b128 v[60:63], v117 offset:7168\n\t"
        "v_mov_b64_e32 v[0:1], 0\n\t"
        "v_mov_b64_e32 v[2:3], 0\n\t"
        "v_mov_b64_e32 v[4:5], 0\n\t"
        "v_mov_b64_e32 v[6:7], 0\n\t"
        "v_mov_b64_e32 v[8:9], 0\n\t"
        "v_mov_b64_e32 v[10:11], 0\n\t"
        "v_mov_b64_e32 v[12:13], 0\n\t"
        "v_mov_b64_e32 v[14:15], 0\n\t"
        "s_waitcnt lgkmcnt(0)\n\t"
        "s_add_u32 s42, s42, 1\n\t"
        "s_cmp_lt_u32 s42, 0x100000\n\t"
        "s_cbranch_scc1 LA_rd%=\n\t"
        "s_movk_i32 s34, 1\n\t"
        "s_branch LA_die%=\n\t"
        "LA_vmfail%=:\n\t"
        "s_or_b32 s47, s47, 2\n\t"
        "s_mov_b32 s42, 0\n\t"
        "LA_vmre%=:\n\t"
        "buffer_load_dwordx4 v[104:107], v120, s[8:11], s26 offen sc1\n\t"
        "v_mfma_f32_16x16x32_bf16 v[4:7], v[48:51], v[12:15], 0\n\t"
        "v_mfma_f32_16x16x32_bf16 v[8:11], v[52:55], v[12:15], 0\n\t"
        "s_nop 7\n\t"
        "s_waitcnt vmcnt(0)\n\t"
        "v_mfma_f32_16x16x16_bf16 v[4:7], v[104:105], v[56:57], v[4:7]\n\t"
        "v_mfma_f32_16x16x16_bf16 v[8:11], v[106:107], v[56:57], v[8:11]\n\t"
        "v_max3_u32 v111, v104, v105, v106\n\t"
        "v_max_u32_e32 v111, v111, v107\n\t"
        "v_cmp_eq_u32_e32 vcc, -1, v111\n\t"
        "s_nop 7\n\t"
        "s_cbranch_vccz LA_vmok%=\n\t"
        "s_add_u32 s42, s42, 1\n\t"
        "s_cmp_lt_u32 s42, 0x100000\n\t"
        "s_cbranch_scc1 LA_vmre%=\n\t"
        "s_movk_i32 s34, 2\n\t"
        "s_branch LA_die%=\n\t"
        "LA_die%=:\n\t"
        "v_mov_b32_e32 v30, s34\n\t"
        "s_mov_b32 s35, 33024\n\t"
        "s_mov_b64 exec, 1\n\t"
        "buffer_store_dword v30, off, s[12:15], s35 sc1\n\t"
        "s_endpgm\n\t"
        :: REC_S_IN, "{v96}"(g_w), "{v97}"(wb0), "{v98}"(wb1), "{v99}"(rb0), "{v100}"(rb0 ^ 32u), "{v101}"(rb0 ^ 64u), "{v102}"(rb0 ^ 96u),
           "{v112}"(a_mem), "{v113}"(l_ms + w * 4096), "{v114}"(l_ms + ((w + 1) & 3) * 4096), "{v115}"(l_ms + ((w + 2) & 3) * 4096), "{v116}"(l_ms + ((w + 3) & 3) * 4096),
           "{v117}"(lb + w * 22528 + l * 16), "{v118}"(l_iw), "{v119}"(l_iq), "{v120}"(a_vm), "{v121}"(a_o), "{v122}"(h * 8192 + l * 16), "{v123}"(a_ox), "{v124}"(lb + L_KV + l * 16), "{s55}"(lb + L_KV + w * 2048),
           "{v125}"((24 + cp) * 16384 + l * 128), "{v126}"(16 * (w & 1) + n - 4 * g), "{v103}"(pp ? 0xEEEEEEEEu : 0x44444444u),
           "{v127}"((l == 0 && w == 0) ? F_PROG + h * 128 : OOB)
        : "memory", "vcc", "scc", "v0", "v255", "a0", "a255", "s101");
    __builtin_unreachable();
}
__device__ __forceinline__ void rec_b(const Params& p, unsigned lb, const int wv, const int id, const int rg, const int cp) {
    const int w = wv, l = otid() & 63, n = l & 15, g = l >> 4, cs = id, rb = 32 + id * 2 + rg;
    const v4u sh = mk_srd(p.ws + hx_base(p.ws), HX_BYTES - 8192), sw = mk_srd(p.ws, W_TOTAL), s1 = mk_srd(p.Wv, 1024 * 4096 * 2), s2 = mk_srd(p.Wo, 1024 * 4096 * 2);
    const int r8 = l >> 3, pc = l & 7, pp = n >> 3, bb = (n >> 2) & 1, f = (g & 1) | (pp << 1);
    const unsigned stg = lb + LB_STG + w * LB_STGP;
    const unsigned g_v = r8 * 8192 + cs * 256 + rg * 128 + pc * 16, g_o = (l >> 2) * 2048 + cs * 64 + (l & 3) * 16;
    const unsigned wb0 = stg + r8 * 128 + (((pc >> 1) ^ (r8 >> 2)) << 5) + (pc & 1) * 16, wb1 = wb0 ^ 64u;
    const unsigned rb0 = stg + (16 * (g >> 1) + 8 * pp + 4 * (g & 1) + bb) * 128 + (f << 5) + (n & 3) * 8;
    const unsigned o_r = stg + pp * (2048 + 128) + (g & 1) * (1024 + 32) + (bb + 4 * (g >> 1)) * 64 + (n & 3) * 8;
    const unsigned a_mem = cp * 32768 + (w >> 1) * 16384 + (w & 1) * 4096 + (g >> 1) * 512 + ((n & 7) >> 2) * 256 + ((g & 1) * 2 + pp) * 64 + (n & 3) * 16;
    const unsigned a_vmw = w < 2 ? (16 + (cs >> 2)) * 16384 + (cs & 3) * 2048 + (2 * rg + w) * 512 + n * 32 + (g & 1) * 16 + (g >> 1) * 8 : OOB;
    const unsigned a_or = (8 + rg * 4 + w) * 16384 + (n >> 2) * 256 + g * 64 + (n & 3) * 16;
    const unsigned a_mw = (RPL != 0 && w == 3) ? OOB : w * 32768 + (cs >> 4) * 16384 + ((cs >> 1) & 7) * 1024 + (cs & 1) * 512 + rg * 256 + l * 4;
    const unsigned a_sw = (w == 0 && l < 4) ? (24 + l) * 16384 + (id * 2 + rg) * 128 : OOB;
    const unsigned m0v = min(*(const unsigned*)(p.mem0 + (4 * rg + ((l >> 2) & 3)) * 1024 + cs * 32 + g * 8 + (l & 3) * 2), 0xFFFFFFFEu);
    asm volatile(
        "s_lshl_b32 s34, s43, 21\n\t"
        "s_add_u32 s34, s34, 0\n\t"
        "buffer_load_dwordx4 v[128:131], v96, s[16:19], s34 offen nt\n\t"
        "s_add_u32 s34, s34, 0x10000\n\t"
        "buffer_load_dwordx4 v[132:135], v96, s[16:19], s34 offen nt\n\t"
        "s_add_u32 s34, s34, 0x10000\n\t"
        "buffer_load_dwordx4 v[136:139], v96, s[16:19], s34 offen nt\n\t"
        "s_add_u32 s34, s34, 0x10000\n\t"
        "buffer_load_dwordx4 v[140:143], v96, s[16:19], s34 offen nt\n\t"
        "s_add_u32 s34, s34, 0x10000\n\t"
        "buffer_load_dwordx4 v[144:147], v96, s[16:19], s34 offen nt\n\t"
        "s_add_u32 s34, s34, 0x10000\n\t"
        "buffer_load_dwordx4 v[148:151], v96, s[16:19], s34 offen nt\n\t"
        "s_add_u32 s34, s34, 0x10000\n\t"
        "buffer_load_dwordx4 v[152:155], v96, s[16:19], s34 offen nt\n\t"
        "s_add_u32 s34, s34, 0x10000\n\t"
        "buffer_load_dwordx4 v[156:159], v96, s[16:19], s34 offen nt\n\t"
        "s_lshl_b32 s34, s43, 21\n\t"
        "s_add_u32 s34, s34, 524288\n\t"
        "buffer_load_dwordx4 v[160:163], v96, s[16:19], s34 offen nt\n\t"
        "s_add_u32 s34, s34, 0x10000\n\t"
        "buffer_load_dwordx4 v[164:167], v96, s[16:19], s34 offen nt\n\t"
        "s_add_u32 s34, s34, 0x10000\n\t"
        "buffer_load_dwordx4 v[168:171], v96, s[16:19], s34 offen nt\n\t"
        "s_add_u32 s34, s34, 0x10000\n\t"
        "buffer_load_dwordx4 v[172:175], v96, s[16:19], s34 offen nt\n\t"
        "s_add_u32 s34, s34, 0x10000\n\t"
        "buffer_load_dwordx4 v[176:179], v96, s[16:19], s34 offen nt\n\t"
        "s_add_u32 s34, s34, 0x10000\n\t"
        "buffer_load_dwordx4 v[180:183], v96, s[16:19], s34 offen nt\n\t"
        "s_add_u32 s34, s34, 0x10000\n\t"
        "buffer_load_dwordx4 v[184:187], v96, s[16:19], s34 offen nt\n\t"
        "s_add_u32 s34, s34, 0x10000\n\t"
        "buffer_load_dwordx4 v[188:191], v96, s[16:19], s34 offen nt\n\t"
        "s_lshl_b32 s34, s43, 21\n\t"
        "s_add_u32 s34, s34, 1048576\n\t"
        "buffer_load_dwordx4 v[192:195], v96, s[16:19], s34 offen nt\n\t"
        "s_add_u32 s34, s34, 0x10000\n\t"
        "buffer_load_dwordx4 v[196:199], v96, s[16:19], s34 offen nt\n\t"
        "s_add_u32 s34, s34, 0x10000\n\t"
        "buffer_load_dwordx4 v[200:203], v96, s[16:19], s34 offen nt\n\t"
        "s_add_u32 s34, s34, 0x10000\n\t"
        "buffer_load_dwordx4 v[204:207], v96, s[16:19], s34 offen nt\n\t"
        "s_add_u32 s34, s34, 0x10000\n\t"
        "buffer_load_dwordx4 v[208:211], v96, s[16:19], s34 offen nt\n\t"
        "s_add_u32 s34, s34, 0x10000\n\t"
        "buffer_load_dwordx4 v[212:215], v96, s[16:19], s34 offen nt\n\t"
        "s_add_u32 s34, s34, 0x10000\n\t"
        "buffer_load_dwordx4 v[216:219], v96, s[16:19], s34 offen nt\n\t"
        "s_add_u32 s34, s34, 0x10000\n\t"
        "buffer_load_dwordx4 v[220:223], v96, s[16:19], s34 offen nt\n\t"
        "s_lshl_b32 s34, s43, 21\n\t"
        "s_add_u32 s34, s34, 1572864\n\t"
        "buffer_load_dwordx4 v[224:227], v96, s[16:19], s34 offen nt\n\t"
        "s_add_u32 s34, s34, 0x10000\n\t"
        "buffer_load_dwordx4 v[228:231], v96, s[16:19], s34 offen nt\n\t"
        "s_add_u32 s34, s34, 0x10000\n\t"
        "buffer_load_dwordx4 v[232:235], v96, s[16:19], s34 offen nt\n\t"
        "s_add_u32 s34, s34, 0x10000\n\t"
        "buffer_load_dwordx4 v[236:239], v96, s[16:19], s34 offen nt\n\t"
        "s_add_u32 s34, s34, 0x10000\n\t"
        "buffer_load_dwordx4 v[240:243], v96, s[16:19], s34 offen nt\n\t"
        "s_add_u32 s34, s34, 0x10000\n\t"
        "buffer_load_dwordx4 v[244:247], v96, s[16:19], s34 offen nt\n\t"
        "s_add_u32 s34, s34, 0x10000\n\t"
        "buffer_load_dwordx4 v[248:251], v96, s[16:19], s34 offen nt\n\t"
        "s_add_u32 s34, s34, 0x10000\n\t"
        "buffer_load_dwordx4 v[252:255], v96, s[16:19], s34 offen nt\n\t"
        "s_lshl_b32 s34, s43, 21\n\t"
        "s_add_u32 s34, s34, 0\n\t"
        "buffer_load_dwordx4 a[0:3], v97, s[20:23], s34 offen nt\n\t"
        "s_add_u32 s34, s34, 0x8000\n\t"
        "buffer_load_dwordx4 a[4:7], v97, s[20:23], s34 offen nt\n\t"
        "s_add_u32 s34, s34, 0x8000\n\t"
        "buffer_load_dwordx4 a[8:11], v97, s[20:23], s34 offen nt\n\t"
        "s_add_u32 s34, s34, 0x8000\n\t"
        "buffer_load_dwordx4 a[12:15], v97, s[20:23], s34 offen nt\n\t"
        "s_add_u32 s34, s34, 0x8000\n\t"
        "buffer_load_dwordx4 a[16:19], v97, s[20:23], s34 offen nt\n\t"
        "s_add_u32 s34, s34, 0x8000\n\t"
        "buffer_load_dwordx4 a[20:23], v97, s[20:23], s34 offen nt\n\t"
        "s_add_u32 s34, s34, 0x8000\n\t"
        "buffer_load_dwordx4 a[24:27], v97, s[20:23], s34 offen nt\n\t"
        "s_add_u32 s34, s34, 0x8000\n\t"
        "buffer_load_dwordx4 a[28:31], v97, s[20:23], s34 offen nt\n\t"
        "s_lshl_b32 s34, s43, 21\n\t"
        "s_add_u32 s34, s34, 262144\n\t"
        "buffer_load_dwordx4 a[32:35], v97, s[20:23], s34 offen nt\n\t"
        "s_add_u32 s34, s34, 0x8000\n\t"
        "buffer_load_dwordx4 a[36:39], v97, s[20:23], s34 offen nt\n\t"
        "s_add_u32 s34, s34, 0x8000\n\t"
        "buffer_load_dwordx4 a[40:43], v97, s[20:23], s34 offen nt\n\t"
        "s_add_u32 s34, s34, 0x8000\n\t"
        "buffer_load_dwordx4 a[44:47], v97, s[20:23], s34 offen nt\n\t"
        "s_add_u32 s34, s34, 0x8000\n\t"
        "buffer_load_dwordx4 a[48:51], v97, s[20:23], s34 offen nt\n\t"
        "s_add_u32 s34, s34, 0x8000\n\t"
        "buffer_load_dwordx4 a[52:55], v97, s[20:23], s34 offen nt\n\t"
        "s_add_u32 s34, s34, 0x8000\n\t"
        "buffer_load_dwordx4 a[56:59], v97, s[20:23], s34 offen nt\n\t"
        "s_add_u32 s34, s34, 0x8000\n\t"
        "buffer_load_dwordx4 a[60:63], v97, s[20:23], s34 offen nt\n\t"
        "s_lshl_b32 s34, s43, 21\n\t"
        "s_add_u32 s34, s34, 524288\n\t"
        "buffer_load_dwordx4 a[64:67], v97, s[20:23], s34 offen nt\n\t"
        "s_add_u32 s34, s34, 0x8000\n\t"
        "buffer_load_dwordx4 a[68:71], v97, s[20:23], s34 offen nt\n\t"
        "s_add_u32 s34, s34, 0x8000\n\t"
        "buffer_load_dwordx4 a[72:75], v97, s[20:23], s34 offen nt\n\t"
        "s_add_u32 s34, s34, 0x8000\n\t"
        "buffer_load_dwordx4 a[76:79], v97, s[20:23], s34 offen nt\n\t"
        "s_add_u32 s34, s34, 0x8000\n\t"
        "buffer_load_dwordx4 a[80:83], v97, s[20:23], s34 offen nt\n\t"
        "s_add_u32 s34, s34, 0x8000\n\t"
        "buffer_load_dwordx4 a[84:87], v97, s[20:23], s34 offen nt\n\t"
        "s_add_u32 s34, s34, 0x8000\n\t"
        "buffer_load_dwordx4 a[88:91], v97, s[20:23], s34 offen nt\n\t"
        "s_add_u32 s34, s34, 0x8000\n\t"
        "buffer_load_dwordx4 a[92:95], v97, s[20:23], s34 offen nt\n\t"
        "s_waitcnt vmcnt(48)\n\t"
        "s_lshl_b32 s34, s43, 21\n\t"
        "s_add_u32 s34, s34, 786432\n\t"
        "buffer_load_dwordx4 a[96:99], v97, s[20:23], s34 offen nt\n\t"
        "s_add_u32 s34, s34, 0x8000\n\t"
        "buffer_load_dwordx4 a[100:103], v97, s[20:23], s34 offen nt\n\t"
        "s_add_u32 s34, s34, 0x8000\n\t"
        "buffer_load_dwordx4 a[104:107], v97, s[20:23], s34 offen nt\n\t"
        "s_add_u32 s34, s34, 0x8000\n\t"
        "buffer_load_dwordx4 a[108:111], v97, s[20:23], s34 offen nt\n\t"
        "s_add_u32 s34, s34, 0x8000\n\t"
        "buffer_load_dwordx4 a[112:115], v97, s[20:23], s34 offen nt\n\t"
        "s_add_u32 s34, s34, 0x8000\n\t"
        "buffer_load_dwordx4 a[116:119], v97, s[20:23], s34 offen nt\n\t"
        "s_add_u32 s34, s34, 0x8000\n\t"
        "buffer_load_dwordx4 a[120:123], v97, s[20:23], s34 offen nt\n\t"
        "s_add_u32 s34, s34, 0x8000\n\t"
        "buffer_load_dwordx4 a[124:127], v97, s[20:23], s34 offen nt\n\t"
        "ds_write_b128 v98, v[128:131] offset:0\n\t"
        "ds_write_b128 v99, v[132:135] offset:1024\n\t"
        "ds_write_b128 v98, v[136:139] offset:2048\n\t"
        "ds_write_b128 v99, v[140:143] offset:3072\n\t"
        "ds_write_b128 v98, v[144:147] offset:4096\n\t"
        "ds_write_b128 v99, v[148:151] offset:5120\n\t"
        "ds_write_b128 v98, v[152:155] offset:6144\n\t"
        "ds_write_b128 v99, v[156:159] offset:7168\n\t"
        "ds_read_b64_tr_b16 v[128:129], v100 offset:0\n\t"
        "ds_read_b64_tr_b16 v[130:131], v100 offset:256\n\t"
        "ds_read_b64_tr_b16 v[132:133], v100 offset:4096\n\t"
        "ds_read_b64_tr_b16 v[134:135], v100 offset:4352\n\t"
        "s_waitcnt lgkmcnt(8)\n\t"
        "ds_read_b64_tr_b16 v[136:137], v101 offset:0\n\t"
        "ds_read_b64_tr_b16 v[138:139], v101 offset:256\n\t"
        "ds_read_b64_tr_b16 v[140:141], v101 offset:4096\n\t"
        "ds_read_b64_tr_b16 v[142:143], v101 offset:4352\n\t"
        "s_waitcnt lgkmcnt(8)\n\t"
        "ds_read_b64_tr_b16 v[144:145], v102 offset:0\n\t"
        "ds_read_b64_tr_b16 v[146:147], v102 offset:256\n\t"
        "ds_read_b64_tr_b16 v[148:149], v102 offset:4096\n\t"
        "ds_read_b64_tr_b16 v[150:151], v102 offset:4352\n\t"
        "s_waitcnt lgkmcnt(8)\n\t"
        "ds_read_b64_tr_b16 v[152:153], v103 offset:0\n\t"
        "ds_read_b64_tr_b16 v[154:155], v103 offset:256\n\t"
        "ds_read_b64_tr_b16 v[156:157], v103 offset:4096\n\t"
        "ds_read_b64_tr_b16 v[158:159], v103 offset:4352\n\t"
        "s_waitcnt lgkmcnt(0)\n\t"
        "s_waitcnt vmcnt(48)\n\t"
        "s_lshl_b32 s34, s43, 21\n\t"
        "s_add_u32 s34, s34, 1048576\n\t"
        "buffer_load_dwordx4 a[128:131], v97, s[20:23], s34 offen nt\n\t"
        "s_add_u32 s34, s34, 0x8000\n\t"
        "buffer_load_dwordx4 a[132:135], v97, s[20:23], s34 offen nt\n\t"
        "s_add_u32 s34, s34, 0x8000\n\t"
        "buffer_load_dwordx4 a[136:139], v97, s[20:23], s34 offen nt\n\t"
        "s_add_u32 s34, s34, 0x8000\n\t"
        "buffer_load_dwordx4 a[140:143], v97, s[20:23], s34 offen nt\n\t"
        "s_add_u32 s34, s34, 0x8000\n\t"
        "buffer_load_dwordx4 a[144:147], v97, s[20:23], s34 offen nt\n\t"
        "s_add_u32 s34, s34, 0x8000\n\t"
        "buffer_load_dwordx4 a[148:151], v97, s[20:23], s34 offen nt\n\t"
        "s_add_u32 s34, s34, 0x8000\n\t"
        "buffer_load_dwordx4 a[152:155], v97, s[20:23], s34 offen nt\n\t"
        "s_add_u32 s34, s34, 0x8000\n\t"
        "buffer_load_dwordx4 a[156:159], v97, s[20:23], s34 offen nt\n\t"
        "ds_write_b128 v98, v[160:163] offset:0\n\t"
        "ds_write_b128 v99, v[164:167] offset:1024\n\t"
        "ds_write_b128 v98, v[168:171] offset:2048\n\t"
        "ds_write_b128 v99, v[172:175] offset:3072\n\t"
        "ds_write_b128 v98, v[176:179] offset:4096\n\t"
        "ds_write_b128 v99, v[180:183] offset:5120\n\t"
        "ds_write_b128 v98, v[184:187] offset:6144\n\t"
        "ds_write_b128 v99, v[188:191] offset:7168\n\t"
        "ds_read_b64_tr_b16 v[160:161], v100 offset:0\n\t"
        "ds_read_b64_tr_b16 v[162:163], v100 offset:256\n\t"
        "ds_read_b64_tr_b16 v[164:165], v100 offset:4096\n\t"
        "ds_read_b64_tr_b16 v[166:167], v100 offset:4352\n\t"
        "s_waitcnt lgkmcnt(8)\n\t"
        "ds_read_b64_tr_b16 v[168:169], v101 offset:0\n\t"
        "ds_read_b64_tr_b16 v[170:171], v101 offset:256\n\t"
        "ds_read_b64_tr_b16 v[172:173], v101 offset:4096\n\t"
        "ds_read_b64_tr_b16 v[174:175], v101 offset:4352\n\t"
        "s_waitcnt lgkmcnt(8)\n\t"
        "ds_read_b64_tr_b16 v[176:177], v102 offset:0\n\t"
        "ds_read_b64_tr_b16 v[178:179], v102 offset:256\n\t"
        "ds_read_b64_tr_b16 v[180:181], v102 offset:4096\n\t"
        "ds_read_b64_tr_b16 v[182:183], v102 offset:4352\n\t"
        "s_waitcnt lgkmcnt(8)\n\t"
        "ds_read_b64_tr_b16 v[184:185], v103 offset:0\n\t"
        "ds_read_b64_tr_b16 v[186:187], v103 offset:256\n\t"
        "ds_read_b64_tr_b16 v[188:189], v103 offset:4096\n\t"
        "ds_read_b64_tr_b16 v[190:191], v103 offset:4352\n\t"
        "s_waitcnt lgkmcnt(0)\n\t"
        "s_waitcnt vmcnt(48)\n\t"
        "s_lshl_b32 s34, s43, 21\n\t"
        "s_add_u32 s34, s34, 1310720\n\t"
        "buffer_load_dwordx4 a[160:163], v97, s[20:23], s34 offen nt\n\t"
        "s_add_u32 s34, s34, 0x8000\n\t"
        "buffer_load_dwordx4 a[164:167], v97, s[20:23], s34 offen nt\n\t"
        "s_add_u32 s34, s34, 0x8000\n\t"
        "buffer_load_dwordx4 a[168:171], v97, s[20:23], s34 offen nt\n\t"
        "s_add_u32 s34, s34, 0x8000\n\t"
        "buffer_load_dwordx4 a[172:175], v97, s[20:23], s34 offen nt\n\t"
        "s_add_u32 s34, s34, 0x8000\n\t"
        "buffer_load_dwordx4 a[176:179], v97, s[20:23], s34 offen nt\n\t"
        "s_add_u32 s34, s34, 0x8000\n\t"
        "buffer_load_dwordx4 a[180:183], v97, s[20:23], s34 offen nt\n\t"
        "s_add_u32 s34, s34, 0x8000\n\t"
        "buffer_load_dwordx4 a[184:187], v97, s[20:23], s34 offen nt\n\t"
        "s_add_u32 s34, s34, 0x8000\n\t"
        "buffer_load_dwordx4 a[188:191], v97, s[20:23], s34 offen nt\n\t"
        "ds_write_b128 v98, v[192:195] offset:0\n\t"
        "ds_write_b128 v99, v[196:199] offset:1024\n\t"
        "ds_write_b128 v98, v[200:203] offset:2048\n\t"
        "ds_write_b128 v99, v[204:207] offset:3072\n\t"
        "ds_write_b128 v98, v[208:211] offset:4096\n\t"
        "ds_write_b128 v99, v[212:215] offset:5120\n\t"
        "ds_write_b128 v98, v[216:219] offset:6144\n\t"
        "ds_write_b128 v99, v[220:223] offset:7168\n\t"
        "ds_read_b64_tr_b16 v[192:193], v100 offset:0\n\t"
        "ds_read_b64_tr_b16 v[194:195], v100 offset:256\n\t"
        "ds_read_b64_tr_b16 v[196:197], v100 offset:4096\n\t"
        "ds_read_b64_tr_b16 v[198:199], v100 offset:4352\n\t"
        "s_waitcnt lgkmcnt(8)\n\t"
        "ds_read_b64_tr_b16 v[200:201], v101 offset:0\n\t"
        "ds_read_b64_tr_b16 v[202:203], v101 offset:256\n\t"
        "ds_read_b64_tr_b16 v[204:205], v101 offset:4096\n\t"
        "ds_read_b64_tr_b16 v[206:207], v101 offset:4352\n\t"
        "s_waitcnt lgkmcnt(8)\n\t"
        "ds_read_b64_tr_b16 v[208:209], v102 offset:0\n\t"
        "ds_read_b64_tr_b16 v[210:211], v102 offset:256\n\t"
        "ds_read_b64_tr_b16 v[212:213], v102 offset:4096\n\t"
        "ds_read_b64_tr_b16 v[214:215], v102 offset:4352\n\t"
        "s_waitcnt lgkmcnt(8)\n\t"
        "ds_read_b64_tr_b16 v[216:217], v103 offset:0\n\t"
        "ds_read_b64_tr_b16 v[218:219], v103 offset:256\n\t"
        "ds_read_b64_tr_b16 v[220:221], v103 offset:4096\n\t"
        "ds_read_b64_tr_b16 v[222:223], v103 offset:4352\n\t"
        "s_waitcnt lgkmcnt(0)\n\t"
        "s_waitcnt vmcnt(48)\n\t"
        "s_lshl_b32 s34, s43, 21\n\t"
        "s_add_u32 s34, s34, 1572864\n\t"
        "buffer_load_dwordx4 a[192:195], v97, s[20:23], s34 offen nt\n\t"
        "s_add_u32 s34, s34, 0x8000\n\t"
        "buffer_load_dwordx4 a[196:199], v97, s[20:23], s34 offen nt\n\t"
        "s_add_u32 s34, s34, 0x8000\n\t"
        "buffer_load_dwordx4 a[200:203], v97, s[20:23], s34 offen nt\n\t"
        "s_add_u32 s34, s34, 0x8000\n\t"
        "buffer_load_dwordx4 a[204:207], v97, s[20:23], s34 offen nt\n\t"
        "s_add_u32 s34, s34, 0x8000\n\t"
        "buffer_load_dwordx4 a[208:211], v97, s[20:23], s34 offen nt\n\t"
        "s_add_u32 s34, s34, 0x8000\n\t"
        "buffer_load_dwordx4 a[212:215], v97, s[20:23], s34 offen nt\n\t"
        "s_add_u32 s34, s34, 0x8000\n\t"
        "buffer_load_dwordx4 a[216:219], v97, s[20:23], s34 offen nt\n\t"
        "s_add_u32 s34, s34, 0x8000\n\t"
        "buffer_load_dwordx4 a[220:223], v97, s[20:23], s34 offen nt\n\t"
        "ds_write_b128 v98, v[224:227] offset:0\n\t"
        "ds_write_b128 v99, v[228:231] offset:1024\n\t"
        "ds_write_b128 v98, v[232:235] offset:2048\n\t"
        "ds_write_b128 v99, v[236:239] offset:3072\n\t"
        "ds_write_b128 v98, v[240:243] offset:4096\n\t"
        "ds_write_b128 v99, v[244:247] offset:5120\n\t"
        "ds_write_b128 v98, v[248:251] offset:6144\n\t"
        "ds_write_b128 v99, v[252:255] offset:7168\n\t"
        "ds_read_b64_tr_b16 v[224:225], v100 offset:0\n\t"
        "ds_read_b64_tr_b16 v[226:227], v100 offset:256\n\t"
        "ds_read_b64_tr_b16 v[228:229], v100 offset:4096\n\t"
        "ds_read_b64_tr_b16 v[230:231], v100 offset:4352\n\t"
        "s_waitcnt lgkmcnt(8)\n\t"
        "ds_read_b64_tr_b16 v[232:233], v101 offset:0\n\t"
        "ds_read_b64_tr_b16 v[234:235], v101 offset:256\n\t"
        "ds_read_b64_tr_b16 v[236:237], v101 offset:4096\n\t"
        "ds_read_b64_tr_b16 v[238:239], v101 offset:4352\n\t"
        "s_waitcnt lgkmcnt(8)\n\t"
        "ds_read_b64_tr_b16 v[240:241], v102 offset:0\n\t"
        "ds_read_b64_tr_b16 v[242:243], v102 offset:256\n\t"
        "ds_read_b64_tr_b16 v[244:245], v102 offset:4096\n\t"
        "ds_read_b64_tr_b16 v[246:247], v102 offset:4352\n\t"
        "s_waitcnt lgkmcnt(8)\n\t"
        "ds_read_b64_tr_b16 v[248:249], v103 offset:0\n\t"
        "ds_read_b64_tr_b16 v[250:251], v103 offset:256\n\t"
        "ds_read_b64_tr_b16 v[252:253], v103 offset:4096\n\t"
        "ds_read_b64_tr_b16 v[254:255], v103 offset:4352\n\t"
        "s_waitcnt lgkmcnt(0)\n\t"
        "s_waitcnt vmcnt(48)\n\t"
        "s_lshl_b32 s34, s43, 21\n\t"
        "s_add_u32 s34, s34, 1835008\n\t"
        "buffer_load_dwordx4 a[224:227], v97, s[20:23], s34 offen nt\n\t"
        "s_add_u32 s34, s34, 0x8000\n\t"
        "buffer_load_dwordx4 a[228:231], v97, s[20:23], s34 offen nt\n\t"
        "s_add_u32 s34, s34, 0x8000\n\t"
        "buffer_load_dwordx4 a[232:235], v97, s[20:23], s34 offen nt\n\t"
        "s_add_u32 s34, s34, 0x8000\n\t"
        "buffer_load_dwordx4 a[236:239], v97, s[20:23], s34 offen nt\n\t"
        "s_add_u32 s34, s34, 0x8000\n\t"
        "buffer_load_dwordx4 a[240:243], v97, s[20:23], s34 offen nt\n\t"
        "s_add_u32 s34, s34, 0x8000\n\t"
        "buffer_load_dwordx4 a[244:247], v97, s[20:23], s34 offen nt\n\t"
        "s_add_u32 s34, s34, 0x8000\n\t"
        "buffer_load_dwordx4 a[248:251], v97, s[20:23], s34 offen nt\n\t"
        "s_add_u32 s34, s34, 0x8000\n\t"
        "buffer_load_dwordx4 a[252:255], v97, s[20:23], s34 offen nt\n\t"
        "ds_write_b128 v122, a[0:3] offset:0\n\t"
        "ds_write_b128 v122, a[4:7] offset:1056\n\t"
        "ds_write_b128 v122, a[8:11] offset:2176\n\t"
        "ds_write_b128 v122, a[12:15] offset:3232\n\t"
        "ds_write_b128 v122, a[16:19] offset:4352\n\t"
        "ds_write_b128 v122, a[20:23] offset:5408\n\t"
        "ds_write_b128 v122, a[24:27] offset:6528\n\t"
        "ds_write_b128 v122, a[28:31] offset:7584\n\t"
        "ds_read_b64_tr_b16 a[0:1], v123 offset:0\n\t"
        "ds_read_b64_tr_b16 a[2:3], v123 offset:128\n\t"
        "ds_read_b64_tr_b16 a[4:5], v123 offset:512\n\t"
        "ds_read_b64_tr_b16 a[6:7], v123 offset:640\n\t"
        "s_waitcnt lgkmcnt(8)\n\t"
        "ds_read_b64_tr_b16 a[8:9], v123 offset:32\n\t"
        "ds_read_b64_tr_b16 a[10:11], v123 offset:160\n\t"
        "ds_read_b64_tr_b16 a[12:13], v123 offset:544\n\t"
        "ds_read_b64_tr_b16 a[14:15], v123 offset:672\n\t"
        "s_waitcnt lgkmcnt(8)\n\t"
        "ds_read_b64_tr_b16 a[16:17], v123 offset:4352\n\t"
        "ds_read_b64_tr_b16 a[18:19], v123 offset:4480\n\t"
        "ds_read_b64_tr_b16 a[20:21], v123 offset:4864\n\t"
        "ds_read_b64_tr_b16 a[22:23], v123 offset:4992\n\t"
        "s_waitcnt lgkmcnt(8)\n\t"
        "ds_read_b64_tr_b16 a[24:25], v123 offset:4384\n\t"
        "ds_read_b64_tr_b16 a[26:27], v123 offset:4512\n\t"
        "ds_read_b64_tr_b16 a[28:29], v123 offset:4896\n\t"
        "ds_read_b64_tr_b16 a[30:31], v123 offset:5024\n\t"
        "s_waitcnt lgkmcnt(8)\n\t"
        "s_waitcnt lgkmcnt(0)\n\t"
        "s_waitcnt vmcnt(48)\n\t"
        "ds_write_b128 v122, a[32:35] offset:0\n\t"
        "ds_write_b128 v122, a[36:39] offset:1056\n\t"
        "ds_write_b128 v122, a[40:43] offset:2176\n\t"
        "ds_write_b128 v122, a[44:47] offset:3232\n\t"
        "ds_write_b128 v122, a[48:51] offset:4352\n\t"
        "ds_write_b128 v122, a[52:55] offset:5408\n\t"
        "ds_write_b128 v122, a[56:59] offset:6528\n\t"
        "ds_write_b128 v122, a[60:63] offset:7584\n\t"
        "ds_read_b64_tr_b16 a[32:33], v123 offset:0\n\t"
        "ds_read_b64_tr_b16 a[34:35], v123 offset:128\n\t"
        "ds_read_b64_tr_b16 a[36:37], v123 offset:512\n\t"
        "ds_read_b64_tr_b16 a[38:39], v123 offset:640\n\t"
        "s_waitcnt lgkmcnt(8)\n\t"
        "ds_read_b64_tr_b16 a[40:41], v123 offset:32\n\t"
        "ds_read_b64_tr_b16 a[42:43], v123 offset:160\n\t"
        "ds_read_b64_tr_b16 a[44:45], v123 offset:544\n\t"
        "ds_read_b64_tr_b16 a[46:47], v123 offset:672\n\t"
        "s_waitcnt lgkmcnt(8)\n\t"
        "ds_read_b64_tr_b16 a[48:49], v123 offset:4352\n\t"
        "ds_read_b64_tr_b16 a[50:51], v123 offset:4480\n\t"
        "ds_read_b64_tr_b16 a[52:53], v123 offset:4864\n\t"
        "ds_read_b64_tr_b16 a[54:55], v123 offset:4992\n\t"
        "s_waitcnt lgkmcnt(8)\n\t"
        "ds_read_b64_tr_b16 a[56:57], v123 offset:4384\n\t"
        "ds_read_b64_tr_b16 a[58:59], v123 offset:4512\n\t"
        "ds_read_b64_tr_b16 a[60:61], v123 offset:4896\n\t"
        "ds_read_b64_tr_b16 a[62:63], v123 offset:5024\n\t"
        "s_waitcnt lgkmcnt(8)\n\t"
        "s_waitcnt lgkmcnt(0)\n\t"
        "s_waitcnt vmcnt(40)\n\t"
        "ds_write_b128 v122, a[64:67] offset:0\n\t"
        "ds_write_b128 v122, a[68:71] offset:1056\n\t"
        "ds_write_b128 v122, a[72:75] offset:2176\n\t"
        "ds_write_b128 v122, a[76:79] offset:3232\n\t"
        "ds_write_b128 v122, a[80:83] offset:4352\n\t"
        "ds_write_b128 v122, a[84:87] offset:5408\n\t"
        "ds_write_b128 v122, a[88:91] offset:6528\n\t"
        "ds_write_b128 v122, a[92:95] offset:7584\n\t"
        "ds_read_b64_tr_b16 a[64:65], v123 offset:0\n\t"
        "ds_read_b64_tr_b16 a[66:67], v123 offset:128\n\t"
        "ds_read_b64_tr_b16 a[68:69], v123 offset:512\n\t"
        "ds_read_b64_tr_b16 a[70:71], v123 offset:640\n\t"
        "s_waitcnt lgkmcnt(8)\n\t"
        "ds_read_b64_tr_b16 a[72:73], v123 offset:32\n\t"
        "ds_read_b64_tr_b16 a[74:75], v123 offset:160\n\t"
        "ds_read_b64_tr_b16 a[76:77], v123 offset:544\n\t"
        "ds_read_b64_tr_b16 a[78:79], v123 offset:672\n\t"
        "s_waitcnt lgkmcnt(8)\n\t"
        "ds_read_b64_tr_b16 a[80:81], v123 offset:4352\n\t"
        "ds_read_b64_tr_b16 a[82:83], v123 offset:4480\n\t"
        "ds_read_b64_tr_b16 a[84:85], v123 offset:4864\n\t"
        "ds_read_b64_tr_b16 a[86:87], v123 offset:4992\n\t"
        "s_waitcnt lgkmcnt(8)\n\t"
        "ds_read_b64_tr_b16 a[88:89], v123 offset:4384\n\t"
        "ds_read_b64_tr_b16 a[90:91], v123 offset:4512\n\t"
        "ds_read_b64_tr_b16 a[92:93], v123 offset:4896\n\t"
        "ds_read_b64_tr_b16 a[94:95], v123 offset:5024\n\t"
        "s_waitcnt lgkmcnt(8)\n\t"
        "s_waitcnt lgkmcnt(0)\n\t"
        "s_waitcnt vmcnt(32)\n\t"
        "ds_write_b128 v122, a[96:99] offset:0\n\t"
        "ds_write_b128 v122, a[100:103] offset:1056\n\t"
        "ds_write_b128 v122, a[104:107] offset:2176\n\t"
        "ds_write_b128 v122, a[108:111] offset:3232\n\t"
        "ds_write_b128 v122, a[112:115] offset:4352\n\t"
        "ds_write_b128 v122, a[116:119] offset:5408\n\t"
        "ds_write_b128 v122, a[120:123] offset:6528\n\t"
        "ds_write_b128 v122, a[124:127] offset:7584\n\t"
        "ds_read_b64_tr_b16 a[96:97], v123 offset:0\n\t"
        "ds_read_b64_tr_b16 a[98:99], v123 offset:128\n\t"
        "ds_read_b64_tr_b16 a[100:101], v123 offset:512\n\t"
        "ds_read_b64_tr_b16 a[102:103], v123 offset:640\n\t"
        "s_waitcnt lgkmcnt(8)\n\t"
        "ds_read_b64_tr_b16 a[104:105], v123 offset:32\n\t"
        "ds_read_b64_tr_b16 a[106:107], v123 offset:160\n\t"
        "ds_read_b64_tr_b16 a[108:109], v123 offset:544\n\t"
        "ds_read_b64_tr_b16 a[110:111], v123 offset:672\n\t"
        "s_waitcnt lgkmcnt(8)\n\t"
        "ds_read_b64_tr_b16 a[112:113], v123 offset:4352\n\t"
        "ds_read_b64_tr_b16 a[114:115], v123 offset:4480\n\t"
        "ds_read_b64_tr_b16 a[116:117], v123 offset:4864\n\t"
        "ds_read_b64_tr_b16 a[118:119], v123 offset:4992\n\t"
        "s_waitcnt lgkmcnt(8)\n\t"
        "ds_read_b64_tr_b16 a[120:121], v123 offset:4384\n\t"
        "ds_read_b64_tr_b16 a[122:123], v123 offset:4512\n\t"
        "ds_read_b64_tr_b16 a[124:125], v123 offset:4896\n\t"
        "ds_read_b64_tr_b16 a[126:127], v123 offset:5024\n\t"
        "s_waitcnt lgkmcnt(8)\n\t"
        "s_waitcnt lgkmcnt(0)\n\t"
        "s_waitcnt vmcnt(24)\n\t"
        "ds_write_b128 v122, a[128:131] offset:0\n\t"
        "ds_write_b128 v122, a[132:135] offset:1056\n\t"
        "ds_write_b128 v122, a[136:139] offset:2176\n\t"
        "ds_write_b128 v122, a[140:143] offset:3232\n\t"
        "ds_write_b128 v122, a[144:147] offset:4352\n\t"
        "ds_write_b128 v122, a[148:151] offset:5408\n\t"
        "ds_write_b128 v122, a[152:155] offset:6528\n\t"
        "ds_write_b128 v122, a[156:159] offset:7584\n\t"
        "ds_read_b64_tr_b16 a[128:129], v123 offset:0\n\t"
        "ds_read_b64_tr_b16 a[130:131], v123 offset:128\n\t"
        "ds_read_b64_tr_b16 a[132:133], v123 offset:512\n\t"
        "ds_read_b64_tr_b16 a[134:135], v123 offset:640\n\t"
        "s_waitcnt lgkmcnt(8)\n\t"
        "ds_read_b64_tr_b16 a[136:137], v123 offset:32\n\t"
        "ds_read_b64_tr_b16 a[138:139], v123 offset:160\n\t"
        "ds_read_b64_tr_b16 a[140:141], v123 offset:544\n\t"
        "ds_read_b64_tr_b16 a[142:143], v123 offset:672\n\t"
        "s_waitcnt lgkmcnt(8)\n\t"
        "ds_read_b64_tr_b16 a[144:145], v123 offset:4352\n\t"
        "ds_read_b64_tr_b16 a[146:147], v123 offset:4480\n\t"
        "ds_read_b64_tr_b16 a[148:149], v123 offset:4864\n\t"
        "ds_read_b64_tr_b16 a[150:151], v123 offset:4992\n\t"
        "s_waitcnt lgkmcnt(8)\n\t"
        "ds_read_b64_tr_b16 a[152:153], v123 offset:4384\n\t"
        "ds_read_b64_tr_b16 a[154:155], v123 offset:4512\n\t"
        "ds_read_b64_tr_b16 a[156:157], v123 offset:4896\n\t"
        "ds_read_b64_tr_b16 a[158:159], v123 offset:5024\n\t"
        "s_waitcnt lgkmcnt(8)\n\t"
        "s_waitcnt lgkmcnt(0)\n\t"
        "s_waitcnt vmcnt(16)\n\t"
        "ds_write_b128 v122, a[160:163] offset:0\n\t"
        "ds_write_b128 v122, a[164:167] offset:1056\n\t"
        "ds_write_b128 v122, a[168:171] offset:2176\n\t"
        "ds_write_b128 v122, a[172:175] offset:3232\n\t"
        "ds_write_b128 v122, a[176:179] offset:4352\n\t"
        "ds_write_b128 v122, a[180:183] offset:5408\n\t"
        "ds_write_b128 v122, a[184:187] offset:6528\n\t"
        "ds_write_b128 v122, a[188:191] offset:7584\n\t"
        "ds_read_b64_tr_b16 a[160:161], v123 offset:0\n\t"
        "ds_read_b64_tr_b16 a[162:163], v123 offset:128\n\t"
        "ds_read_b64_tr_b16 a[164:165], v123 offset:512\n\t"
        "ds_read_b64_tr_b16 a[166:167], v123 offset:640\n\t"
        "s_waitcnt lgkmcnt(8)\n\t"
        "ds_read_b64_tr_b16 a[168:169], v123 offset:32\n\t"
        "ds_read_b64_tr_b16 a[170:171], v123 offset:160\n\t"
        "ds_read_b64_tr_b16 a[172:173], v123 offset:544\n\t"
        "ds_read_b64_tr_b16 a[174:175], v123 offset:672\n\t"
        "s_waitcnt lgkmcnt(8)\n\t"
        "ds_read_b64_tr_b16 a[176:177], v123 offset:4352\n\t"
        "ds_read_b64_tr_b16 a[178:179], v123 offset:4480\n\t"
        "ds_read_b64_tr_b16 a[180:181], v123 offset:4864\n\t"
        "ds_read_b64_tr_b16 a[182:183], v123 offset:4992\n\t"
        "s_waitcnt lgkmcnt(8)\n\t"
        "ds_read_b64_tr_b16 a[184:185], v123 offset:4384\n\t"
        "ds_read_b64_tr_b16 a[186:187], v123 offset:4512\n\t"
        "ds_read_b64_tr_b16 a[188:189], v123 offset:4896\n\t"
        "ds_read_b64_tr_b16 a[190:191], v123 offset:5024\n\t"
        "s_waitcnt lgkmcnt(8)\n\t"
        "s_waitcnt lgkmcnt(0)\n\t"
        "s_waitcnt vmcnt(8)\n\t"
        "ds_write_b128 v122, a[192:195] offset:0\n\t"
        "ds_write_b128 v122, a[196:199] offset:1056\n\t"
        "ds_write_b128 v122, a[200:203] offset:2176\n\t"
        "ds_write_b128 v122, a[204:207] offset:3232\n\t"
        "ds_write_b128 v122, a[208:211] offset:4352\n\t"
        "ds_write_b128 v122, a[212:215] offset:5408\n\t"
        "ds_write_b128 v122, a[216:219] offset:6528\n\t"
        "ds_write_b128 v122, a[220:223] offset:7584\n\t"
        "ds_read_b64_tr_b16 a[192:193], v123 offset:0\n\t"
        "ds_read_b64_tr_b16 a[194:195], v123 offset:128\n\t"
        "ds_read_b64_tr_b16 a[196:197], v123 offset:512\n\t"
        "ds_read_b64_tr_b16 a[198:199], v123 offset:640\n\t"
        "s_waitcnt lgkmcnt(8)\n\t"
        "ds_read_b64_tr_b16 a[200:201], v123 offset:32\n\t"
        "ds_read_b64_tr_b16 a[202:203], v123 offset:160\n\t"
        "ds_read_b64_tr_b16 a[204:205], v123 offset:544\n\t"
        "ds_read_b64_tr_b16 a[206:207], v123 offset:672\n\t"
        "s_waitcnt lgkmcnt(8)\n\t"
        "ds_read_b64_tr_b16 a[208:209], v123 offset:4352\n\t"
        "ds_read_b64_tr_b16 a[210:211], v123 offset:4480\n\t"
        "ds_read_b64_tr_b16 a[212:213], v123 offset:4864\n\t"
        "ds_read_b64_tr_b16 a[214:215], v123 offset:4992\n\t"
        "s_waitcnt lgkmcnt(8)\n\t"
        "ds_read_b64_tr_b16 a[216:217], v123 offset:4384\n\t"
        "ds_read_b64_tr_b16 a[218:219], v123 offset:4512\n\t"
        "ds_read_b64_tr_b16 a[220:221], v123 offset:4896\n\t"
        "ds_read_b64_tr_b16 a[222:223], v123 offset:5024\n\t"
        "s_waitcnt lgkmcnt(8)\n\t"
        "s_waitcnt lgkmcnt(0)\n\t"
        "s_waitcnt vmcnt(0)\n\t"
        "ds_write_b128 v122, a[224:227] offset:0\n\t"
        "ds_write_b128 v122, a[228:231] offset:1056\n\t"
        "ds_write_b128 v122, a[232:235] offset:2176\n\t"
        "ds_write_b128 v122, a[236:239] offset:3232\n\t"
        "ds_write_b128 v122, a[240:243] offset:4352\n\t"
        "ds_write_b128 v122, a[244:247] offset:5408\n\t"
        "ds_write_b128 v122, a[248:251] offset:6528\n\t"
        "ds_write_b128 v122, a[252:255] offset:7584\n\t"
        "ds_read_b64_tr_b16 a[224:225], v123 offset:0\n\t"
        "ds_read_b64_tr_b16 a[226:227], v123 offset:128\n\t"
        "ds_read_b64_tr_b16 a[228:229], v123 offset:512\n\t"
        "ds_read_b64_tr_b16 a[230:231], v123 offset:640\n\t"
        "s_waitcnt lgkmcnt(8)\n\t"
        "ds_read_b64_tr_b16 a[232:233], v123 offset:32\n\t"
        "ds_read_b64_tr_b16 a[234:235], v123 offset:160\n\t"
        "ds_read_b64_tr_b16 a[236:237], v123 offset:544\n\t"
        "ds_read_b64_tr_b16 a[238:239], v123 offset:672\n\t"
        "s_waitcnt lgkmcnt(8)\n\t"
        "ds_read_b64_tr_b16 a[240:241], v123 offset:4352\n\t"
        "ds_read_b64_tr_b16 a[242:243], v123 offset:4480\n\t"
        "ds_read_b64_tr_b16 a[244:245], v123 offset:4864\n\t"
        "ds_read_b64_tr_b16 a[246:247], v123 offset:4992\n\t"
        "s_waitcnt lgkmcnt(8)\n\t"
        "ds_read_b64_tr_b16 a[248:249], v123 offset:4384\n\t"
        "ds_read_b64_tr_b16 a[250:251], v123 offset:4512\n\t"
        "ds_read_b64_tr_b16 a[252:253], v123 offset:4896\n\t"
        "ds_read_b64_tr_b16 a[254:255], v123 offset:5024\n\t"
        "s_waitcnt lgkmcnt(8)\n\t"
        "s_waitcnt lgkmcnt(0)\n\t"
        "s_mov_b32 s42, 0\n\t"
        "v_mbcnt_lo_u32_b32 v2, -1, 0\n\t"
        "v_mbcnt_hi_u32_b32 v2, -1, v2\n\t"
        "v_and_b32_e32 v3, 31, v2\n\t"
        "v_lshlrev_b32_e32 v3, 2, v3\n\t"
        "s_mov_b32 s35, 33792\n\t"
        "LB_cal%=:\n\t"
        "buffer_load_dword v0, v3, s[12:15], s35 offen sc1\n\t"
        "s_waitcnt vmcnt(0)\n\t"
        "v_cmp_eq_u32_e32 vcc, 0, v0\n\t"
        "s_cbranch_vccz LB_calok%=\n\t"
        "s_sleep 4\n\t"
        "s_add_u32 s42, s42, 1\n\t"
        "s_cmp_lt_u32 s42, 0x100000\n\t"
        "s_cbranch_scc1 LB_cal%=\n\t"
        "s_movk_i32 s34, 8\n\t"
        "s_branch LB_die%=\n\t"
        "LB_calok%=:\n\t"
        "v_cmp_eq_u32_e32 vcc, 3, v0\n\t"
        "s_nop 0\n\t"
        "s_bcnt1_i32_b64 s34, vcc\n\t"
        "s_cmp_ge_u32 s34, 32\n\t"
        "s_cselect_b32 s34, 8192, 0\n\t"
        "s_add_u32 s8, s8, s34\n\t"
        "s_addc_u32 s9, s9, 0\n\t"
        "buffer_store_dword v109, v116, s[8:11], 0 offen sc1\n\t"
        "s_memrealtime s[32:33]\n\t"
        "s_waitcnt lgkmcnt(0)\n\t"
        "v_mov_b32_e32 v110, s32\n\t"
        "v_mov_b32_e32 v111, 0\n\t"
        "s_waitcnt vmcnt(0) lgkmcnt(0)\n\t"
        "s_barrier\n\t"
        "s_memrealtime s[32:33]\n\t"
        "s_lshl_b32 s35, s49, 2\n\t"
        "s_add_u32 s35, s35, 34304\n\t"
        "s_cmp_eq_u32 s43, 0\n\t"
        "s_cselect_b32 s35, s35, 0xf0000000\n\t"
        "s_waitcnt lgkmcnt(0)\n\t"
        "s_or_b32 s25, s32, 1\n\t"
        "v_mov_b32_e32 v0, s25\n\t"
        "s_mov_b64 exec, 1\n\t"
        "buffer_store_dword v0, off, s[12:15], s35 sc1\n\t"
        "s_mov_b64 exec, -1\n\t"
        "v_min_u32_e32 v3, 47, v2\n\t"
        "v_lshlrev_b32_e32 v3, 3, v3\n\t"
        "s_mov_b32 s35, 34304\n\t"
        "s_mov_b32 s42, 0\n\t"
        "LB_rdy%=:\n\t"
        "buffer_load_dwordx2 v[0:1], v3, s[12:15], s35 offen sc1\n\t"
        "s_waitcnt vmcnt(0)\n\t"
        "v_min_u32_e32 v2, v0, v1\n\t"
        "v_cmp_eq_u32_e32 vcc, 0, v2\n\t"
        "s_cbranch_vccz LB_rdyok%=\n\t"
        "s_sleep 6\n\t"
        "s_add_u32 s42, s42, 1\n\t"
        "s_cmp_lt_u32 s42, 0x100000\n\t"
        "s_cbranch_scc1 LB_rdy%=\n\t"
        "s_movk_i32 s34, 9\n\t"
        "s_branch LB_die%=\n\t"
        "LB_rdyok%=:\n\t"
        "v_subrev_u32_e32 v0, s25, v0\n\t"
        "v_subrev_u32_e32 v1, s25, v1\n\t"
        "v_max_i32_e32 v0, v0, v1\n\t"
        "s_nop 1\n\t"
        "v_max_i32_dpp v0, v0, v0 quad_perm:[1,0,3,2] row_mask:0xf bank_mask:0xf\n\t"
        "s_nop 1\n\t"
        "v_max_i32_dpp v0, v0, v0 quad_perm:[2,3,0,1] row_mask:0xf bank_mask:0xf\n\t"
        "s_nop 1\n\t"
        "v_max_i32_dpp v0, v0, v0 row_ror:4 row_mask:0xf bank_mask:0xf\n\t"
        "s_nop 1\n\t"
        "v_max_i32_dpp v0, v0, v0 row_ror:8 row_mask:0xf bank_mask:0xf\n\t"
        "v_mov_b32_e32 v1, v0\n\t"
        "s_nop 1\n\t"
        "v_permlane16_swap_b32_e32 v0, v1\n\t"
        "v_max_i32_e32 v0, v0, v1\n\t"
        "v_mov_b32_e32 v1, v0\n\t"
        "s_nop 1\n\t"
        "v_permlane32_swap_b32_e32 v0, v1\n\t"
        "v_max_i32_e32 v0, v0, v1\n\t"
        "s_nop 0\n\t"
        "v_readfirstlane_b32 s34, v0\n\t"
        "s_nop 0\n\t"
        "s_add_u32 s25, s25, s34\n\t"
        "s_add_u32 s25, s25, 120\n\t"
        "s_and_b32 s25, s25, -4\n\t"
        "s_mov_b32 s24, 0\n\t"
        "s_mov_b32 s26, 0\n\t"
        "s_mov_b32 s27, 0x80000\n\t"
        "s_mov_b32 s28, 0x180000\n\t"
        "s_mov_b32 s31, 0\n\t"
        "s_mov_b32 s47, 0\n\t"
        "s_mov_b32 s38, 0xff00ff00\n\t"
        "s_mov_b32 s39, 0xff00ff00\n\t"
        "s_mov_b32 s40, 0\n\t"
        "s_mov_b32 s41, -1\n\t"
        "v_mov_b64_e32 v[0:1], 0\n\t"
        "v_mov_b64_e32 v[2:3], 0\n\t"
        "v_mov_b64_e32 v[4:5], 0\n\t"
        "v_mov_b64_e32 v[6:7], 0\n\t"
        "v_mov_b64_e32 v[8:9], 0\n\t"
        "v_mov_b64_e32 v[10:11], 0\n\t"
        "v_mov_b64_e32 v[12:13], 0\n\t"
        "v_mov_b64_e32 v[14:15], 0\n\t"
        "LB_loop%=:\n\t"
        "s_mov_b32 s42, 0\n\t"
        "s_sub_u32 s35, s25, 1\n\t"
        "LB_wa%=:\n\t"
        "s_memrealtime s[32:33]\n\t"
        "s_waitcnt lgkmcnt(0)\n\t"
        "s_sub_u32 s34, s35, s32\n\t"
        "s_cmpk_lt_u32 s34, 0x2000\n\t"
        "s_cbranch_scc1 LB_wa%=\n\t"
        "LB_rd%=:\n\t"
        "buffer_load_dwordx4 v[16:19], v112, s[8:11], s26 offen offset:0 sc1\n\t"
        "buffer_load_dwordx4 v[20:23], v112, s[8:11], s26 offen offset:1024 sc1\n\t"
        "buffer_load_dwordx4 v[24:27], v112, s[8:11], s26 offen offset:2048 sc1\n\t"
        "buffer_load_dwordx4 v[28:31], v112, s[8:11], s26 offen offset:3072 sc1\n\t"
        "s_waitcnt vmcnt(3)\n\t"
        "v_smfmac_f32_16x16x64_bf16 v[0:3], v[16:19], v[128:135], v106\n\t"
        "v_max3_u32 v108, v16, v17, v18\n\t"
        "v_max_u32_e32 v108, v19, v108\n\t"
        "v_smfmac_f32_16x16x64_bf16 v[4:7], v[16:19], v[136:143], v106\n\t"
        "v_smfmac_f32_16x16x64_bf16 v[8:11], v[16:19], v[144:151], v106\n\t"
        "v_smfmac_f32_16x16x64_bf16 v[12:15], v[16:19], v[152:159], v106\n\t"
        "s_waitcnt vmcnt(2)\n\t"
        "v_smfmac_f32_16x16x64_bf16 v[0:3], v[20:23], v[160:167], v106\n\t"
        "v_max3_u32 v108, v20, v21, v108\n\t"
        "v_max3_u32 v108, v22, v23, v108\n\t"
        "v_smfmac_f32_16x16x64_bf16 v[4:7], v[20:23], v[168:175], v106\n\t"
        "v_smfmac_f32_16x16x64_bf16 v[8:11], v[20:23], v[176:183], v106\n\t"
        "v_smfmac_f32_16x16x64_bf16 v[12:15], v[20:23], v[184:191], v106\n\t"
        "s_waitcnt vmcnt(1)\n\t"
        "v_smfmac_f32_16x16x64_bf16 v[0:3], v[24:27], v[192:199], v106\n\t"
        "v_max3_u32 v108, v24, v25, v108\n\t"
        "v_max3_u32 v108, v26, v27, v108\n\t"
        "v_smfmac_f32_16x16x64_bf16 v[4:7], v[24:27], v[200:207], v106\n\t"
        "v_smfmac_f32_16x16x64_bf16 v[8:11], v[24:27], v[208:215], v106\n\t"
        "v_smfmac_f32_16x16x64_bf16 v[12:15], v[24:27], v[216:223], v106\n\t"
        "s_waitcnt vmcnt(0)\n\t"
        "v_smfmac_f32_16x16x64_bf16 v[0:3], v[28:31], v[224:231], v106\n\t"
        "v_max3_u32 v108, v28, v29, v108\n\t"
        "v_max3_u32 v108, v30, v31, v108\n\t"
        "v_smfmac_f32_16x16x64_bf16 v[4:7], v[28:31], v[232:239], v106\n\t"
        "v_cmp_eq_u32_e32 vcc, -1, v108\n\t"
        "v_smfmac_f32_16x16x64_bf16 v[8:11], v[28:31], v[240:247], v106\n\t"
        "v_smfmac_f32_16x16x64_bf16 v[12:15], v[28:31], v[248:255], v106\n\t"
        "s_cbranch_vccnz LB_rdfail%=\n\t"
        "buffer_store_dwordx2 v[110:111], v117, s[8:11], s26 offen sc1\n\t"
        "s_nop 2\n\t"
        "v_permlane32_swap_b32_e32 v0, v4\n\t"
        "v_permlane32_swap_b32_e32 v1, v5\n\t"
        "v_permlane32_swap_b32_e32 v2, v6\n\t"
        "v_permlane32_swap_b32_e32 v3, v7\n\t"
        "v_add_f32_e32 v0, v0, v4\n\t"
        "v_add_f32_e32 v1, v1, v5\n\t"
        "v_add_f32_e32 v2, v2, v6\n\t"
        "v_add_f32_e32 v3, v3, v7\n\t"
        "ds_write_b128 v118, v[0:3] offset:0\n\t"
        "v_permlane32_swap_b32_e32 v8, v12\n\t"
        "v_permlane32_swap_b32_e32 v9, v13\n\t"
        "v_permlane32_swap_b32_e32 v10, v14\n\t"
        "v_permlane32_swap_b32_e32 v11, v15\n\t"
        "v_add_f32_e32 v8, v8, v12\n\t"
        "v_add_f32_e32 v9, v9, v13\n\t"
        "v_add_f32_e32 v10, v10, v14\n\t"
        "v_add_f32_e32 v11, v11, v15\n\t"
        "ds_write_b128 v118, v[8:11] offset:1024\n\t"
        "s_waitcnt lgkmcnt(0)\n\t"
        "s_barrier\n\t"
        "ds_read_b128 v[16:19], v119 offset:0\n\t"
        "ds_read_b128 v[20:23], v119 offset:2048\n\t"
        "ds_read_b128 v[24:27], v119 offset:4096\n\t"
        "ds_read_b128 v[28:31], v119 offset:6144\n\t"
        "s_waitcnt lgkmcnt(2)\n\t"
        "v_add_f32_e32 v16, v16, v20\n\t"
        "v_add_f32_e32 v17, v17, v21\n\t"
        "v_add_f32_e32 v18, v18, v22\n\t"
        "v_add_f32_e32 v19, v19, v23\n\t"
        "s_waitcnt lgkmcnt(0)\n\t"
        "v_add_f32_e32 v24, v24, v28\n\t"
        "v_add_f32_e32 v25, v25, v29\n\t"
        "v_add_f32_e32 v26, v26, v30\n\t"
        "v_add_f32_e32 v27, v27, v31\n\t"
        "v_add_f32_e32 v16, v16, v24\n\t"
        "v_add_f32_e32 v17, v17, v25\n\t"
        "v_add_f32_e32 v18, v18, v26\n\t"
        "v_add_f32_e32 v19, v19, v27\n\t"
        "v_cvt_pk_bf16_f32 v20, v16, v17\n\t"
        "v_min_u32_e32 v20, -2, v20\n\t"
        "v_cvt_pk_bf16_f32 v21, v18, v19\n\t"
        "v_min_u32_e32 v21, -2, v21\n\t"
        "buffer_store_dwordx2 v[20:21], v114, s[8:11], s26 offen sc1\n\t"
        "s_cmp_eq_u32 s24, 63\n\t"
        "s_cbranch_scc1 LB_end%=\n\t"
        "s_memrealtime s[52:53]\n\t"
        "v_mov_b64_e32 v[24:25], -1\n\t"
        "buffer_store_dword v24, v116, s[8:11], s28 offen sc1\n\t"
        "s_xor_b32 s34, s26, 0x100000\n\t"
        "buffer_store_dwordx2 v[24:25], v114, s[8:11], s34 offen sc1\n\t"
        "v_mov_b64_e32 v[32:33], 0\n\t"
        "v_mov_b64_e32 v[34:35], 0\n\t"
        "v_mov_b64_e32 v[36:37], 0\n\t"
        "v_mov_b64_e32 v[38:39], 0\n\t"
        "v_mov_b64_e32 v[40:41], 0\n\t"
        "v_mov_b64_e32 v[42:43], 0\n\t"
        "v_mov_b64_e32 v[44:45], 0\n\t"
        "v_mov_b64_e32 v[46:47], 0\n\t"
        "v_mov_b64_e32 v[0:1], 0\n\t"
        "v_mov_b64_e32 v[2:3], 0\n\t"
        "v_mov_b64_e32 v[4:5], 0\n\t"
        "v_mov_b64_e32 v[6:7], 0\n\t"
        "v_mov_b64_e32 v[8:9], 0\n\t"
        "v_mov_b64_e32 v[10:11], 0\n\t"
        "v_mov_b64_e32 v[12:13], 0\n\t"
        "v_mov_b64_e32 v[14:15], 0\n\t"
        "s_add_u32 s46, s26, 4096\n\t"
        "s_sleep 3\n\t"
        "buffer_load_dwordx2 v[104:105], v113, s[8:11], s26 offen sc1\n\t"
        "s_waitcnt vmcnt(0) lgkmcnt(0)\n\t"
        "v_cmp_ne_u32_e32 vcc, s24, v105\n\t"
        "s_cbranch_vccz LB_stok%=\n\t"
        "s_mov_b32 s42, 0\n\t"
        "LB_stre%=:\n\t"
        "buffer_load_dwordx2 v[104:105], v113, s[8:11], s26 offen sc1\n\t"
        "s_waitcnt vmcnt(0)\n\t"
        "v_cmp_ne_u32_e32 vcc, s24, v105\n\t"
        "s_cbranch_vccz LB_stok%=\n\t"
        "s_add_u32 s42, s42, 1\n\t"
        "s_cmp_lt_u32 s42, 0x100000\n\t"
        "s_cbranch_scc1 LB_stre%=\n\t"
        "s_movk_i32 s34, 5\n\t"
        "s_branch LB_die%=\n\t"
        "LB_stok%=:\n\t"
        "v_subrev_u32_e32 v104, s25, v104\n\t"
        "s_nop 1\n\t"
        "v_max_i32_dpp v104, v104, v104 quad_perm:[1,0,3,2] row_mask:0xf bank_mask:0xf\n\t"
        "s_nop 1\n\t"
        "v_max_i32_dpp v104, v104, v104 quad_perm:[2,3,0,1] row_mask:0xf bank_mask:0xf\n\t"
        "s_nop 1\n\t"
        "v_max_i32_dpp v104, v104, v104 row_ror:4 row_mask:0xf bank_mask:0xf\n\t"
        "s_nop 1\n\t"
        "v_max_i32_dpp v104, v104, v104 row_ror:8 row_mask:0xf bank_mask:0xf\n\t"
        "v_mov_b32_e32 v105, v104\n\t"
        "s_nop 1\n\t"
        "v_permlane16_swap_b32_e32 v104, v105\n\t"
        "v_max_i32_e32 v104, v104, v105\n\t"
        "v_mov_b32_e32 v105, v104\n\t"
        "s_nop 1\n\t"
        "v_permlane32_swap_b32_e32 v104, v105\n\t"
        "v_max_i32_e32 v104, v104, v105\n\t"
        "s_nop 0\n\t"
        "v_readfirstlane_b32 s34, v104\n\t"
        "s_nop 0\n\t"
        "s_add_i32 s34, s34, s51\n\t"
        "s_add_u32 s35, s31, s50\n\t"
        "s_mul_i32 s36, s50, 3\n\t"
        "s_min_u32 s35, s35, s36\n\t"
        "s_cmp_gt_i32 s34, 0\n\t"
        "s_cselect_b32 s31, s35, 0\n\t"
        "s_cselect_b32 s34, s34, 0\n\t"
        "s_add_u32 s34, s34, s31\n\t"
        "s_add_u32 s34, s34, 3\n\t"
        "s_and_b32 s34, s34, -4\n\t"
        "s_add_u32 s37, s25, s34\n\t"
        "s_add_u32 s45, s37, s29\n\t"
        "s_add_u32 s25, s45, s30\n\t"
        "s_add_u32 s34, s24, 1\n\t"
        "v_mov_b32_e32 v111, s34\n\t"
        "s_mov_b32 s42, 0\n\t"
        "s_sub_u32 s35, s45, 1\n\t"
        "LB_wo%=:\n\t"
        "s_memrealtime s[32:33]\n\t"
        "s_waitcnt lgkmcnt(0)\n\t"
        "s_sub_u32 s34, s35, s32\n\t"
        "s_cmpk_lt_u32 s34, 0x2000\n\t"
        "s_cbranch_scc1 LB_wo%=\n\t"
        "LB_ro%=:\n\t"
        "buffer_load_dwordx4 v[64:67], v115, s[8:11], s26 offen offset:0 sc1\n\t"
        "s_nop 10\n\t"
        "buffer_load_dwordx4 v[68:71], v115, s[8:11], s26 offen offset:1024 sc1\n\t"
        "s_nop 10\n\t"
        "buffer_load_dwordx4 v[72:75], v115, s[8:11], s26 offen offset:2048 sc1\n\t"
        "s_nop 10\n\t"
        "buffer_load_dwordx4 v[76:79], v115, s[8:11], s26 offen offset:3072 sc1\n\t"
        "s_nop 10\n\t"
        "buffer_load_dwordx4 v[80:83], v115, s[8:11], s46 offen offset:0 sc1\n\t"
        "s_nop 10\n\t"
        "buffer_load_dwordx4 v[84:87], v115, s[8:11], s46 offen offset:1024 sc1\n\t"
        "s_nop 10\n\t"
        "buffer_load_dwordx4 v[88:91], v115, s[8:11], s46 offen offset:2048 sc1\n\t"
        "s_nop 10\n\t"
        "buffer_load_dwordx4 v[92:95], v115, s[8:11], s46 offen offset:3072 sc1\n\t"
        "s_waitcnt vmcnt(7)\n\t"
        "v_smfmac_f32_16x16x64_bf16 v[40:43], v[64:67], a[16:23], v107\n\t"
        "v_max3_u32 v108, v64, v65, v66\n\t"
        "v_max_u32_e32 v108, v67, v108\n\t"
        "v_smfmac_f32_16x16x64_bf16 v[36:39], v[64:67], a[8:15], v107\n\t"
        "v_smfmac_f32_16x16x64_bf16 v[32:35], v[64:67], a[0:7], v107\n\t"
        "v_smfmac_f32_16x16x64_bf16 v[44:47], v[64:67], a[24:31], v107\n\t"
        "s_waitcnt vmcnt(6)\n\t"
        "v_smfmac_f32_16x16x64_bf16 v[40:43], v[68:71], a[48:55], v107\n\t"
        "v_max3_u32 v108, v68, v69, v108\n\t"
        "v_max3_u32 v108, v70, v71, v108\n\t"
        "v_smfmac_f32_16x16x64_bf16 v[36:39], v[68:71], a[40:47], v107\n\t"
        "v_smfmac_f32_16x16x64_bf16 v[32:35], v[68:71], a[32:39], v107\n\t"
        "v_smfmac_f32_16x16x64_bf16 v[44:47], v[68:71], a[56:63], v107\n\t"
        "s_waitcnt vmcnt(5)\n\t"
        "v_smfmac_f32_16x16x64_bf16 v[40:43], v[72:75], a[80:87], v107\n\t"
        "v_max3_u32 v108, v72, v73, v108\n\t"
        "v_max3_u32 v108, v74, v75, v108\n\t"
        "v_smfmac_f32_16x16x64_bf16 v[36:39], v[72:75], a[72:79], v107\n\t"
        "v_smfmac_f32_16x16x64_bf16 v[32:35], v[72:75], a[64:71], v107\n\t"
        "v_smfmac_f32_16x16x64_bf16 v[44:47], v[72:75], a[88:95], v107\n\t"
        "s_waitcnt vmcnt(4)\n\t"
        "v_smfmac_f32_16x16x64_bf16 v[40:43], v[76:79], a[112:119], v107\n\t"
        "v_max3_u32 v108, v76, v77, v108\n\t"
        "v_max3_u32 v108, v78, v79, v108\n\t"
        "v_smfmac_f32_16x16x64_bf16 v[36:39], v[76:79], a[104:111], v107\n\t"
        "v_smfmac_f32_16x16x64_bf16 v[32:35], v[76:79], a[96:103], v107\n\t"
        "v_smfmac_f32_16x16x64_bf16 v[44:47], v[76:79], a[120:127], v107\n\t"
        "s_waitcnt vmcnt(3)\n\t"
        "v_smfmac_f32_16x16x64_bf16 v[40:43], v[80:83], a[144:151], v107\n\t"
        "v_max3_u32 v108, v80, v81, v108\n\t"
        "v_max3_u32 v108, v82, v83, v108\n\t"
        "v_smfmac_f32_16x16x64_bf16 v[36:39], v[80:83], a[136:143], v107\n\t"
        "v_smfmac_f32_16x16x64_bf16 v[32:35], v[80:83], a[128:135], v107\n\t"
        "v_smfmac_f32_16x16x64_bf16 v[44:47], v[80:83], a[152:159], v107\n\t"
        "s_waitcnt vmcnt(2)\n\t"
        "v_smfmac_f32_16x16x64_bf16 v[40:43], v[84:87], a[176:183], v107\n\t"
        "v_max3_u32 v108, v84, v85, v108\n\t"
        "v_max3_u32 v108, v86, v87, v108\n\t"
        "v_smfmac_f32_16x16x64_bf16 v[36:39], v[84:87], a[168:175], v107\n\t"
        "v_smfmac_f32_16x16x64_bf16 v[32:35], v[84:87], a[160:167], v107\n\t"
        "v_smfmac_f32_16x16x64_bf16 v[44:47], v[84:87], a[184:191], v107\n\t"
        "s_waitcnt vmcnt(1)\n\t"
        "v_smfmac_f32_16x16x64_bf16 v[40:43], v[88:91], a[208:215], v107\n\t"
        "v_max3_u32 v108, v88, v89, v108\n\t"
        "v_max3_u32 v108, v90, v91, v108\n\t"
        "v_smfmac_f32_16x16x64_bf16 v[36:39], v[88:91], a[200:207], v107\n\t"
        "v_smfmac_f32_16x16x64_bf16 v[32:35], v[88:91], a[192:199], v107\n\t"
        "v_smfmac_f32_16x16x64_bf16 v[44:47], v[88:91], a[216:223], v107\n\t"
        "s_waitcnt vmcnt(0)\n\t"
        "v_smfmac_f32_16x16x64_bf16 v[40:43], v[92:95], a[240:247], v107\n\t"
        "v_max3_u32 v108, v92, v93, v108\n\t"
        "v_max3_u32 v108, v94, v95, v108\n\t"
        "v_smfmac_f32_16x16x64_bf16 v[36:39], v[92:95], a[232:239], v107\n\t"
        "v_cmp_eq_u32_e32 vcc, -1, v108\n\t"
        "v_smfmac_f32_16x16x64_bf16 v[32:35], v[92:95], a[224:231], v107\n\t"
        "v_smfmac_f32_16x16x64_bf16 v[44:47], v[92:95], a[248:255], v107\n\t"
        "s_cbranch_vccnz LB_rofail%=\n\t"
        "s_nop 3\n\t"
        "v_permlane32_swap_b32_e32 v40, v36\n\t"
        "v_permlane32_swap_b32_e32 v41, v37\n\t"
        "v_permlane32_swap_b32_e32 v42, v38\n\t"
        "v_permlane32_swap_b32_e32 v43, v39\n\t"
        "v_add_f32_e32 v32, v32, v36\n\t"
        "v_add_f32_e32 v33, v33, v37\n\t"
        "v_add_f32_e32 v34, v34, v38\n\t"
        "v_add_f32_e32 v35, v35, v39\n\t"
        "v_add_f32_dpp v32, v40, v44 quad_perm:[0,1,2,3] row_mask:0xc bank_mask:0xf\n\t"
        "v_add_f32_dpp v33, v41, v45 quad_perm:[0,1,2,3] row_mask:0xc bank_mask:0xf\n\t"
        "v_add_f32_dpp v34, v42, v46 quad_perm:[0,1,2,3] row_mask:0xc bank_mask:0xf\n\t"
        "v_add_f32_dpp v35, v43, v47 quad_perm:[0,1,2,3] row_mask:0xc bank_mask:0xf\n\t"
        "v_permlane16_swap_b32_e32 v32, v33\n\t"
        "s_nop 0\n\t"
        "v_permlane16_swap_b32_e32 v34, v35\n\t"
        "v_add_f32_e32 v32, v32, v33\n\t"
        "v_add_f32_e32 v34, v34, v35\n\t"
        "ds_write2_b32 v120, v32, v34 offset0:0 offset1:64\n\t"
        "s_waitcnt lgkmcnt(0)\n\t"
        "s_barrier\n\t"
        "ds_read2st64_b64 v[64:67], v121 offset0:0 offset1:1\n\t"
        "ds_read2st64_b64 v[68:71], v121 offset0:2 offset1:3\n\t"
        "s_waitcnt lgkmcnt(1)\n\t"
        "v_add_f32_e32 v64, v64, v66\n\t"
        "v_add_f32_e32 v65, v65, v67\n\t"
        "s_waitcnt lgkmcnt(0)\n\t"
        "v_add_f32_e32 v68, v68, v70\n\t"
        "v_add_f32_e32 v69, v69, v71\n\t"
        "v_add_f32_e32 v64, v64, v68\n\t"
        "v_add_f32_e32 v65, v65, v69\n\t"
        "v_cvt_pk_bf16_f32 v64, v64, v65\n\t"
        "v_min_u32_e32 v64, -2, v64\n\t"
        "buffer_store_dword v64, v116, s[8:11], s27 offen sc1\n\t"
        "s_memrealtime s[32:33]\n\t"
        "s_waitcnt lgkmcnt(0)\n\t"
        "v_mov_b32_e32 v110, s32\n\t"
        "s_add_u32 s24, s24, 1\n\t"
        "s_mov_b32 s28, s26\n\t"
        "s_mov_b32 s26, s27\n\t"
        "s_add_u32 s27, s27, 0x80000\n\t"
        "s_and_b32 s27, s27, 0x1fffff\n\t"
        "s_branch LB_loop%=\n\t"
        "LB_end%=:\n\t"
        "s_endpgm\n\t"
        "LB_rdfail%=:\n\t"
        "s_or_b32 s47, s47, 1\n\t"
        "s_nop 7\n\t"
        "v_mov_b64_e32 v[0:1], 0\n\t"
        "v_mov_b64_e32 v[2:3], 0\n\t"
        "v_mov_b64_e32 v[4:5], 0\n\t"
        "v_mov_b64_e32 v[6:7], 0\n\t"
        "v_mov_b64_e32 v[8:9], 0\n\t"
        "v_mov_b64_e32 v[10:11], 0\n\t"
        "v_mov_b64_e32 v[12:13], 0\n\t"
        "v_mov_b64_e32 v[14:15], 0\n\t"
        "s_add_u32 s42, s42, 1\n\t"
        "s_cmp_lt_u32 s42, 0x100000\n\t"
        "s_cbranch_scc1 LB_rd%=\n\t"
        "s_movk_i32 s34, 3\n\t"
        "s_branch LB_die%=\n\t"
        "LB_rofail%=:\n\t"
        "s_or_b32 s47, s47, 4\n\t"
        "s_nop 7\n\t"
        "v_mov_b64_e32 v[32:33], 0\n\t"
        "v_mov_b64_e32 v[34:35], 0\n\t"
        "v_mov_b64_e32 v[36:37], 0\n\t"
        "v_mov_b64_e32 v[38:39], 0\n\t"
        "v_mov_b64_e32 v[40:41], 0\n\t"
        "v_mov_b64_e32 v[42:43], 0\n\t"
        "v_mov_b64_e32 v[44:45], 0\n\t"
        "v_mov_b64_e32 v[46:47], 0\n\t"
        "s_waitcnt lgkmcnt(0)\n\t"
        "s_add_u32 s42, s42, 1\n\t"
        "s_cmp_lt_u32 s42, 0x100000\n\t"
        "s_cbranch_scc1 LB_ro%=\n\t"
        "s_movk_i32 s34, 4\n\t"
        "s_branch LB_die%=\n\t"
        "LB_die%=:\n\t"
        "v_mov_b32_e32 v30, s34\n\t"
        "s_mov_b32 s35, 33024\n\t"
        "s_mov_b64 exec, 1\n\t"
        "buffer_store_dword v30, off, s[12:15], s35 sc1\n\t"
        "s_endpgm\n\t"
        :: REC_S_IN, "{v96}"(g_v), "{v97}"(g_o), "{v98}"(wb0), "{v99}"(wb1), "{v100}"(rb0), "{v101}"(rb0 ^ 32u), "{v102}"(rb0 ^ 64u), "{v103}"(rb0 ^ 96u),
           "{v106}"(pp ? 0xEEEEEEEEu : 0x44444444u), "{v107}"(bb ? 0xEEEEEEEEu : 0x44444444u), "{v109}"(m0v),
           "{v112}"(a_mem), "{v113}"((24 + cp) * 16384 + l * 128), "{v114}"(a_vmw), "{v115}"(a_or), "{v116}"(a_mw), "{v117}"(a_sw),
           "{v118}"(lb + w * 2048 + l * 16), "{v119}"(lb + (w & 1) * 1024 + l * 16), "{v120}"(lb + LB_R + w * 512 + (g & 1) * 128 + ((g >> 1) * 16 + n) * 4),
           "{v121}"(lb + LB_R + ((l >> 2) & 3) * 128 + g * 32 + (l & 3) * 8), "{v122}"(stg + l * 16), "{v123}"(o_r)
        : "memory", "vcc", "scc", "v0", "v255", "a0", "a255", "s101");
    __builtin_unreachable();
}
__device__ __forceinline__ void rec_helper(const Params& p, rsrc_t r, unsigned char* lds, const int wv, const int hi) {
    const int tid = otid(), l = tid & 63;
    const rsrc_t rh = __builtin_amdgcn_make_buffer_rsrc(p.ws + hx_base(p.ws), 0, HX_BYTES, 0x00020000);
    const v4u ones = {0xFFFFFFFFu, 0xFFFFFFFFu, 0xFFFFFFFFu, 0xFFFFFFFFu};
    const unsigned ca = l >= 8 ? OOB : (31 + wv * 32) * 16384 + hi * 256 + l * 16;
    unsigned best0 = ~0u, best1 = ~0u;
    for (int i = 0; i < CAL_N; ++i) {
        const unsigned t0 = (unsigned)__builtin_amdgcn_s_memtime(); __builtin_amdgcn_s_waitcnt(0);
        st16(rh, ca, ones); DRAIN();
        const unsigned t1 = (unsigned)__builtin_amdgcn_s_memtime(); __builtin_amdgcn_s_waitcnt(0);
        st16(rh, ca + 8192, ones); DRAIN();
        const unsigned t2 = (unsigned)__builtin_amdgcn_s_memtime(); __builtin_amdgcn_s_waitcnt(0);
        best0 = min(best0, t1 - t0); best1 = min(best1, t2 - t1); }
    if (l == 0) *(v2u*)(lds + wv * 8) = (v2u){best0, best1};
#pragma unroll
    for (int i = 0; i < 16; ++i) st16(rh, hi * 65536 + i * 4096 + tid * 16, ones);
    DRAIN();
    __syncthreads();
    if (tid == 0) { const v4u a = *(const v4u*)lds, b = *(const v4u*)(lds + 16);
        best0 = min(min(a.x, a.z), min(b.x, b.z)); best1 = min(min(a.y, a.w), min(b.y, b.w));
        st4(r, F_CAL + hi * 4, best1 < best0 ? 3u : 2u); }
    __syncthreads();
}
using f32x16 = __attribute__((ext_vector_type(16))) float;
constexpr int CV_RP = 80;
constexpr int CV_U = 96 * CV_RP;
constexpr int CV_KP = 4384;
constexpr int CV_K0 = 8 * CV_U;
__device__ __forceinline__ float blo(unsigned x) { return __uint_as_float(x << 16); }
__device__ __forceinline__ float bhi(unsigned x) { return __uint_as_float(x & 0xFFFF0000u); }
__device__ __forceinline__ void gate_block(const Params& p, rsrc_t r, const int hi, const int wv) {
    const int l = otid() & 63, t0 = hi * 64 + wv * 16;
    v4u xg[16][2], gw[2];
#pragma unroll
    for (int q = 0; q < 2; ++q) gw[q] = *(const v4u*)(p.gate_w + q * 512 + l * 8);
#pragma unroll
    for (int i = 0; i < 16; ++i)
#pragma unroll
        for (int q = 0; q < 2; ++q) xg[i][q] = *(const v4u*)(p.Aq + (size_t)(t0 + i) * D + q * 512 + l * 8);
    float mine = 0.f;
#pragma unroll
    for (int i = 0; i < 16; ++i) { float acc = 0.f;
#pragma unroll
        for (int q = 0; q < 2; ++q)
#pragma unroll
            for (int e = 0; e < 4; ++e) { const unsigned pr = pack2(blo(xg[i][q][e]) * blo(gw[q][e]), bhi(xg[i][q][e]) * bhi(gw[q][e])); acc += blo(pr); acc += bhi(pr); }
        for (int o = 32; o > 0; o >>= 1) acc += __shfl_xor(acc, o);
        if (l == i) mine = acc; }
    if (l < 16) { const float z = rbfh(mine); st4(r, W_GATE + (t0 + l) * 4, __float_as_uint(rbfh(1.f / (1.f + expf(-z))))); }
}
__device__ __forceinline__ void conv_block(const Params& p, rsrc_t r, unsigned char* lds, const int wv, const int hi, const int j) {
    (void)j;
    const int tid = otid(), w = wv, l = tid & 63, Q = tid >> 2, cg = tid & 3, ch0 = hi * 32 + cg * 8;
    for (int i = tid; i < 8 * 640; i += 256) *(unsigned*)(lds + (i / 640) * CV_U + (i % 640) * 4) = 0u;
    unsigned char* kc = lds + CV_K0 + w * 4 * CV_KP;
    if (l < 32) {
#pragma unroll
        for (int q = 0; q < 4; ++q) { *(unsigned*)(kc + q * CV_KP + l * 4) = 0u; *(unsigned*)(kc + q * CV_KP + 4224 + l * 4) = 0u; } }
    v4u u[32];
    {
        v4u sk[4]; float sv[8][4];
#pragma unroll
        for (int q = 0; q < 4; ++q) sk[q] = *(const v4u*)(p.shift_k + ch0 * 4 + q * 8);
        const rsrc_t rk = __builtin_amdgcn_make_buffer_rsrc((void*)p.Ak, 0, S * D * 2, 0x00020000), rv = __builtin_amdgcn_make_buffer_rsrc((void*)p.Av, 0, S * D * 2, 0x00020000);
        const unsigned ro = (unsigned)(Q * 32 * D + ch0) * 2u;
        v4u kr[19], kn[16];
#pragma unroll
        for (int i = 0; i < 19; ++i) kr[i] = __builtin_amdgcn_raw_buffer_load_b128(rk, (i < 3 && Q == 0) ? OOB : ro + (i - 3) * 2048, 0, CV_POL);
#pragma unroll
        for (int i = 0; i < 16; ++i) u[i] = __builtin_amdgcn_raw_buffer_load_b128(rv, ro + i * 2048, 0, CV_POL);
#pragma unroll
        for (int i = 0; i < 16; ++i) kn[i] = __builtin_amdgcn_raw_buffer_load_b128(rk, ro + (16 + i) * 2048, 0, CV_POL);
#pragma unroll
        for (int c8 = 0; c8 < 8; ++c8)
#pragma unroll
            for (int tp = 0; tp < 4; ++tp) { const unsigned sw = sk[c8 >> 1][(c8 & 1) * 2 + (tp >> 1)]; sv[c8][tp] = (tp & 1) ? bhi(sw) : blo(sw); }
        auto row = [&](int i, const v4u& k0, const v4u& k1, const v4u& k2, const v4u& k3) {
#pragma unroll
            for (int q = 0; q < 4; ++q) {
                const float a = fmaf(blo(k3[q]), sv[2 * q][0], fmaf(blo(k2[q]), sv[2 * q][1], fmaf(blo(k1[q]), sv[2 * q][2], blo(k0[q]) * sv[2 * q][3])));
                const float b = fmaf(bhi(k3[q]), sv[2 * q + 1][0], fmaf(bhi(k2[q]), sv[2 * q + 1][1], fmaf(bhi(k1[q]), sv[2 * q + 1][2], bhi(k0[q]) * sv[2 * q + 1][3])));
                const unsigned ks = pack2(a, b);
                u[i][q] = pack2(blo(ks) * blo(u[i][q]), bhi(ks) * bhi(u[i][q])); } };
        __builtin_amdgcn_sched_barrier(0);
#pragma unroll
        for (int i = 0; i < 16; ++i) { row(i, kr[i], kr[i + 1], kr[i + 2], kr[i + 3]); u[16 + i] = __builtin_amdgcn_raw_buffer_load_b128(rv, ro + (16 + i) * 2048, 0, CV_POL); __builtin_amdgcn_sched_barrier(0); }
#pragma unroll
        for (int i = 0; i < 16; ++i) row(16 + i, i < 3 ? kr[16 + i] : kn[i - 3], i < 2 ? kr[17 + i] : kn[i - 2], i < 1 ? kr[18] : kn[i - 1], kn[i]);
    }
    BSTAMP(8);
    const rsrc_t rs = __builtin_amdgcn_make_buffer_rsrc((void*)p.ssm_k, 0, S * D * 2, 0x00020000);
    const int b = l & 31, kg = l >> 5, cc = (b + 1) & 3;
    v4u kraw[4]; v2u kext[4];
    auto kload = [&](int n) { const unsigned o = (unsigned)((hi * 32 + w * 8 + n) * S + l * 8) * 2u;
#pragma unroll
        for (int i = 0; i < 4; ++i) { kraw[i] = __builtin_amdgcn_raw_buffer_load_b128(rs, o + i * 1024, 0, CV_POL);
            kext[i] = __builtin_amdgcn_raw_buffer_load_b64(rs, (i == 3 && l == 63) ? OOB : o + i * 1024 + 16, 0, 0); } };
    kload(0);
    const v4u dv = *(const v4u*)(p.ssm_D + ch0);
    unsigned char* const urow = lds + 2 * cg * CV_U + (32 + Q) * CV_RP;
#pragma unroll
    for (int rd = 0; rd < 4; ++rd) {
#pragma unroll
        for (int G = 0; G < 4; ++G) { v4u lo, hh;
#pragma unroll
            for (int q = 0; q < 4; ++q) { const unsigned x = u[8 * G + 7 - 2 * q][rd], y = u[8 * G + 6 - 2 * q][rd];
                lo[q] = __builtin_amdgcn_perm(y, x, 0x05040100u); hh[q] = __builtin_amdgcn_perm(y, x, 0x07060302u); }
            *(v4u*)(urow + G * 16) = lo; *(v4u*)(urow + CV_U + G * 16) = hh; }
        __syncthreads();
#pragma unroll 1
        for (int ci = 0; ci < 2; ++ci) {
            const int c8 = 2 * w + ci;
#pragma unroll
            for (int i = 0; i < 4; ++i) { const v4u e = kraw[i]; const v2u f = kext[i]; unsigned char* q = kc + 128 + (i * 64 + l) * 16;
                const unsigned s0 = __builtin_amdgcn_alignbit(e.y, e.x, 16), s1 = __builtin_amdgcn_alignbit(e.z, e.y, 16), s2 = __builtin_amdgcn_alignbit(e.w, e.z, 16),
                               s3 = __builtin_amdgcn_alignbit(f.x, e.w, 16), s4 = __builtin_amdgcn_alignbit(f.y, f.x, 16);
                *(v4u*)q = e; *(v4u*)(q + CV_KP) = (v4u){s0, s1, s2, s3}; *(v4u*)(q + 2 * CV_KP) = (v4u){e.y, e.z, e.w, f.x}; *(v4u*)(q + 3 * CV_KP) = (v4u){s1, s2, s3, s4};
                if (i == 0 && l == 0) {
                    *(v2u*)(q + CV_KP - 8) = (v2u){0u, e.x << 16}; *(v2u*)(q + 2 * CV_KP - 8) = (v2u){0u, e.x}; *(v2u*)(q + 3 * CV_KP - 8) = (v2u){e.x << 16, s0}; } }
            if (2 * rd + ci < 7) kload(2 * rd + ci + 1);
            __builtin_amdgcn_wave_barrier();
            f32x16 acc0, acc1;
#pragma unroll
            for (int i = 0; i < 16; ++i) { acc0[i] = 0.f; acc1[i] = 0.f; }
            const unsigned char* kb = kc + cc * CV_KP + (b - 8 * kg - 7 + 64 - cc) * 2;
            const unsigned char* ub = lds + c8 * CV_U + (32 + b) * CV_RP + kg * 16;
            v2u ka[4][2], kz[4][2]; bf16x8 fa[4], fz[4];
            auto ld_k = [&](int set, int un, int hi2) { const int dl = un >> 1, kh = un & 1; const unsigned char* q = kb + (32 * dl - 16 * kh) * 2 + hi2 * 2048;
                if (hi2) { kz[set][0] = *(const v2u*)q; kz[set][1] = *(const v2u*)(q + 8); } else { ka[set][0] = *(const v2u*)q; ka[set][1] = *(const v2u*)(q + 8); } };
            auto ld_f = [&](int set, int un) { const int dl = un >> 1, kh = un & 1;
                fa[set] = *(const bf16x8*)(ub + (32 - dl) * CV_RP + kh * 32); fz[set] = *(const bf16x8*)(ub - dl * CV_RP + kh * 32); };
#pragma unroll
            for (int q = 0; q < 2; ++q) { ld_k(q, q, 0); ld_f(q, q); ld_k(q, q, 1); }
#pragma unroll 1
            for (int u0 = 0; u0 < 64; u0 += 4) {
#pragma unroll
                for (int q = 0; q < 4; ++q) {
                    const int un = min(u0 + q + 2, 63), ns = (q + 2) & 3;
                    const v4u kav = {ka[q][0].x, ka[q][0].y, ka[q][1].x, ka[q][1].y}, kzv = {kz[q][0].x, kz[q][0].y, kz[q][1].x, kz[q][1].y};
                    __builtin_amdgcn_sched_barrier(0);
                    asm volatile("s_nop 1\n\tv_mfma_f32_32x32x16_bf16 %0, %2, %1, %0" : "+v"(acc1) : "v"(fa[q]), "v"(kav));
                    ld_k(ns, un, 0);
                    __builtin_amdgcn_sched_barrier(0);
                    asm volatile("v_mfma_f32_32x32x16_bf16 %0, %2, %1, %0" : "+v"(acc0) : "v"(fz[q]), "v"(kav));
                    ld_f(ns, un);
                    __builtin_amdgcn_sched_barrier(0);
                    asm volatile("v_mfma_f32_32x32x16_bf16 %0, %2, %1, %0" : "+v"(acc1) : "v"(fz[q]), "v"(kzv));
                    ld_k(ns, un, 1);
                }
            }
            asm volatile("s_waitcnt lgkmcnt(0)\n\ts_nop 7\n\ts_nop 7" : "+v"(acc0), "+v"(acc1) :: "memory");
            unsigned char* uc = lds + c8 * CV_U + (32 + b) * CV_RP + kg * 8;
#pragma unroll
            for (int q = 0; q < 4; ++q) {
                *(v2u*)(uc + q * 16) = (v2u){pack2(acc0[4 * q], acc0[4 * q + 1]), pack2(acc0[4 * q + 2], acc0[4 * q + 3])};
                *(v2u*)(uc + 32 * CV_RP + q * 16) = (v2u){pack2(acc1[4 * q], acc1[4 * q + 1]), pack2(acc1[4 * q + 2], acc1[4 * q + 3])}; }
            __builtin_amdgcn_wave_barrier();
        }
        __syncthreads();
        const float d0 = blo(dv[rd]), d1 = bhi(dv[rd]);
#pragma unroll
        for (int G = 0; G < 4; ++G) { const v4u lo = *(const v4u*)(urow + G * 16), hh = *(const v4u*)(urow + CV_U + G * 16);
#pragma unroll
            for (int q = 0; q < 8; ++q) { const unsigned x = u[8 * G + q][rd], ud = pack2(blo(x) * d0, bhi(x) * d1);
                u[8 * G + q][rd] = pack2(((q & 1) ? bhi(lo[q >> 1]) : blo(lo[q >> 1])) + blo(ud), ((q & 1) ? bhi(hh[q >> 1]) : blo(hh[q >> 1])) + bhi(ud)); } }
        if (rd == 0) BSTAMP(9);
    }
    BSTAMP(11);
#pragma unroll
    for (int hf = 0; hf < 2; ++hf) { v4u qv[16];
#pragma unroll
        for (int i = 0; i < 16; ++i) qv[i] = *(const v4u*)(p.Aq + (size_t)(Q * 32 + hf * 16 + i) * D + ch0);
#pragma unroll
        for (int i = 0; i < 16; ++i) { v4u o;
#pragma unroll
            for (int q = 0; q < 4; ++q) o[q] = pack2(blo(qv[i][q]) * blo(u[hf * 16 + i][q]), bhi(qv[i][q]) * bhi(u[hf * 16 + i][q]));
            st16(r, W_A + hi * 131072 + (hf * 16 + i) * 4096 + tid * 16, o); } }
    DRAIN(); __syncthreads();
    if (tid == 0) __hip_atomic_fetch_add((unsigned*)(p.ws + F_CONV), 1u, __ATOMIC_RELAXED, __HIP_MEMORY_SCOPE_AGENT);
}

__device__ __forceinline__ void proj_block(const Params& p, unsigned char* lds, const int wv, const int h, const int c, const int j) {
    const int w = wv, l = otid() & 63, n = l & 15, g = l >> 4, tm = w >> 1, tn = w & 1;
    const unsigned lb = __builtin_amdgcn_readfirstlane((unsigned)(size_t)lds);
    const v4u sx = mk_srd(p.Aq, S * D * 2), sq = mk_srd(p.Wq, 1024 * 4096 * 2), sk = mk_srd(p.Wk, 1024 * 4096 * 2), sv = mk_srd(p.Wv, 1024 * 4096 * 2), sw = mk_srd(p.ws, W_TOTAL);
    const unsigned lx = lb + (32 * tm + n) * 128 + ((g ^ (n & 7)) << 4), lr = lb + 131072 + (8 * g + (n >> 2)) * 64;
    const unsigned aq = (tn ? W_KX : W_QX) + (tm * 32 + h) * 8192 + c * 1024 + l * 16;
    unsigned t0, xo, so, cn; unsigned long long ts;
    asm volatile(
        "s_mov_b32 %[xo], 0\n\t"
        "s_mov_b32 %[so], 0xf0000000\n\t"
        "s_mov_b32 %[cn], 0\n\t"
        "s_add_u32 m0, %[ld], 0\n\t"
        "s_add_u32 %[t0], %[xo], 0\n\t"
        "buffer_load_dwordx4 v100, %[sx], %[t0] offen lds\n\t"
        "s_add_u32 m0, %[ld], 1024\n\t"
        "s_add_u32 %[t0], %[xo], 16384\n\t"
        "buffer_load_dwordx4 v100, %[sx], %[t0] offen lds\n\t"
        "s_add_u32 m0, %[ld], 8192\n\t"
        "s_add_u32 %[t0], %[xo], 128\n\t"
        "buffer_load_dwordx4 v100, %[sx], %[t0] offen lds\n\t"
        "s_add_u32 m0, %[ld], 9216\n\t"
        "s_add_u32 %[t0], %[xo], 16512\n\t"
        "buffer_load_dwordx4 v100, %[sx], %[t0] offen lds\n\t"
        "s_add_u32 m0, %[ld], 16384\n\t"
        "s_add_u32 %[t0], %[xo], 256\n\t"
        "buffer_load_dwordx4 v100, %[sx], %[t0] offen lds\n\t"
        "s_add_u32 m0, %[ld], 17408\n\t"
        "s_add_u32 %[t0], %[xo], 16640\n\t"
        "buffer_load_dwordx4 v100, %[sx], %[t0] offen lds\n\t"
        "s_add_u32 m0, %[ld], 24576\n\t"
        "s_add_u32 %[t0], %[xo], 384\n\t"
        "buffer_load_dwordx4 v100, %[sx], %[t0] offen lds\n\t"
        "s_add_u32 m0, %[ld], 25600\n\t"
        "s_add_u32 %[t0], %[xo], 16768\n\t"
        "buffer_load_dwordx4 v100, %[sx], %[t0] offen lds\n\t"
        "s_mov_b32 %[t0], 0\n\t"
        "buffer_load_dwordx4 a[192:195], v101, %[sq], %[t0] offen nt\n\t"
        "buffer_load_dwordx4 a[196:199], v101, %[sk], %[t0] offen nt\n\t"
        "buffer_load_dwordx4 a[200:203], v101, %[sv], %[t0] offen nt\n\t"
        "s_mov_b32 %[t0], 524288\n\t"
        "buffer_load_dwordx4 a[204:207], v101, %[sq], %[t0] offen nt\n\t"
        "buffer_load_dwordx4 a[208:211], v101, %[sk], %[t0] offen nt\n\t"
        "buffer_load_dwordx4 a[212:215], v101, %[sv], %[t0] offen nt\n\t"
        "s_mov_b32 %[t0], 1048576\n\t"
        "buffer_load_dwordx4 a[216:219], v101, %[sq], %[t0] offen nt\n\t"
        "buffer_load_dwordx4 a[220:223], v101, %[sk], %[t0] offen nt\n\t"
        "buffer_load_dwordx4 a[224:227], v101, %[sv], %[t0] offen nt\n\t"
        "s_mov_b32 %[t0], 1572864\n\t"
        "buffer_load_dwordx4 a[228:231], v101, %[sq], %[t0] offen nt\n\t"
        "buffer_load_dwordx4 a[232:235], v101, %[sk], %[t0] offen nt\n\t"
        "buffer_load_dwordx4 a[236:239], v101, %[sv], %[t0] offen nt\n\t"
        "s_mov_b32 %[t0], 2097152\n\t"
        "buffer_load_dwordx4 a[240:243], v101, %[sq], %[t0] offen nt\n\t"
        "buffer_load_dwordx4 a[244:247], v101, %[sk], %[t0] offen nt\n\t"
        "buffer_load_dwordx4 a[248:251], v101, %[sv], %[t0] offen nt\n\t"
        "s_mov_b32 %[t0], 2621440\n\t"
        "buffer_load_dwordx4 a[252:255], v101, %[sq], %[t0] offen nt\n\t"
        "buffer_load_dwordx4 v[128:131], v101, %[sk], %[t0] offen nt\n\t"
        "buffer_load_dwordx4 v[132:135], v101, %[sv], %[t0] offen nt\n\t"
        "s_mov_b32 %[t0], 3145728\n\t"
        "buffer_load_dwordx4 v[136:139], v101, %[sq], %[t0] offen nt\n\t"
        "buffer_load_dwordx4 v[140:143], v101, %[sk], %[t0] offen nt\n\t"
        "buffer_load_dwordx4 v[144:147], v101, %[sv], %[t0] offen nt\n\t"
        "s_mov_b32 %[t0], 3670016\n\t"
        "buffer_load_dwordx4 v[148:151], v101, %[sq], %[t0] offen nt\n\t"
        "buffer_load_dwordx4 v[152:155], v101, %[sk], %[t0] offen nt\n\t"
        "buffer_load_dwordx4 v[156:159], v101, %[sv], %[t0] offen nt\n\t"
        "s_mov_b32 %[t0], 4194304\n\t"
        "buffer_load_dwordx4 v[160:163], v101, %[sq], %[t0] offen nt\n\t"
        "buffer_load_dwordx4 v[164:167], v101, %[sk], %[t0] offen nt\n\t"
        "buffer_load_dwordx4 v[168:171], v101, %[sv], %[t0] offen nt\n\t"
        "s_mov_b32 %[t0], 4718592\n\t"
        "buffer_load_dwordx4 v[172:175], v101, %[sq], %[t0] offen nt\n\t"
        "buffer_load_dwordx4 v[176:179], v101, %[sk], %[t0] offen nt\n\t"
        "buffer_load_dwordx4 v[180:183], v101, %[sv], %[t0] offen nt\n\t"
        "s_mov_b32 %[t0], 5242880\n\t"
        "buffer_load_dwordx4 v[184:187], v101, %[sq], %[t0] offen nt\n\t"
        "buffer_load_dwordx4 v[188:191], v101, %[sk], %[t0] offen nt\n\t"
        "buffer_load_dwordx4 v[192:195], v101, %[sv], %[t0] offen nt\n\t"
        "s_mov_b32 %[t0], 5767168\n\t"
        "buffer_load_dwordx4 v[196:199], v101, %[sq], %[t0] offen nt\n\t"
        "buffer_load_dwordx4 v[200:203], v101, %[sk], %[t0] offen nt\n\t"
        "buffer_load_dwordx4 v[204:207], v101, %[sv], %[t0] offen nt\n\t"
        "s_mov_b32 %[t0], 6291456\n\t"
        "buffer_load_dwordx4 v[208:211], v101, %[sq], %[t0] offen nt\n\t"
        "buffer_load_dwordx4 v[212:215], v101, %[sk], %[t0] offen nt\n\t"
        "buffer_load_dwordx4 v[216:219], v101, %[sv], %[t0] offen nt\n\t"
        "s_mov_b32 %[t0], 6815744\n\t"
        "buffer_load_dwordx4 v[220:223], v101, %[sq], %[t0] offen nt\n\t"
        "buffer_load_dwordx4 v[224:227], v101, %[sk], %[t0] offen nt\n\t"
        "buffer_load_dwordx4 v[228:231], v101, %[sv], %[t0] offen nt\n\t"
        "s_mov_b32 %[t0], 7340032\n\t"
        "buffer_load_dwordx4 v[232:235], v101, %[sq], %[t0] offen nt\n\t"
        "buffer_load_dwordx4 v[236:239], v101, %[sk], %[t0] offen nt\n\t"
        "buffer_load_dwordx4 v[240:243], v101, %[sv], %[t0] offen nt\n\t"
        "s_mov_b32 %[t0], 7864320\n\t"
        "buffer_load_dwordx4 v[244:247], v101, %[sq], %[t0] offen nt\n\t"
        "buffer_load_dwordx4 v[248:251], v101, %[sk], %[t0] offen nt\n\t"
        "buffer_load_dwordx4 v[252:255], v101, %[sv], %[t0] offen nt\n\t"
        "s_waitcnt vmcnt(45)\n\t"
        "ds_write_b128 v102, a[192:195] offset:0\n\t"
        "ds_write_b128 v102, a[196:199] offset:4096\n\t"
        "ds_write_b128 v102, a[200:203] offset:8192\n\t"
        "s_waitcnt lgkmcnt(0)\n\t"
        "s_barrier\n\t"
        "ds_read_b64_tr_b16 a[0:1], v103 offset:0\n\t"
        "ds_read_b64_tr_b16 a[2:3], v103 offset:256\n\t"
        "ds_read_b64_tr_b16 a[4:5], v103 offset:8\n\t"
        "ds_read_b64_tr_b16 a[6:7], v103 offset:264\n\t"
        "ds_read_b64_tr_b16 a[8:9], v104 offset:0\n\t"
        "ds_read_b64_tr_b16 a[10:11], v104 offset:256\n\t"
        "ds_read_b64_tr_b16 a[12:13], v103 offset:2048\n\t"
        "ds_read_b64_tr_b16 a[14:15], v103 offset:2304\n\t"
        "ds_read_b64_tr_b16 a[16:17], v103 offset:2056\n\t"
        "ds_read_b64_tr_b16 a[18:19], v103 offset:2312\n\t"
        "ds_read_b64_tr_b16 a[20:21], v104 offset:2048\n\t"
        "ds_read_b64_tr_b16 a[22:23], v104 offset:2304\n\t"
        "s_waitcnt vmcnt(42)\n\t"
        "ds_write_b128 v102, a[204:207] offset:16384\n\t"
        "ds_write_b128 v102, a[208:211] offset:20480\n\t"
        "ds_write_b128 v102, a[212:215] offset:24576\n\t"
        "s_waitcnt lgkmcnt(0)\n\t"
        "s_barrier\n\t"
        "ds_read_b64_tr_b16 a[24:25], v103 offset:16384\n\t"
        "ds_read_b64_tr_b16 a[26:27], v103 offset:16640\n\t"
        "ds_read_b64_tr_b16 a[28:29], v103 offset:16392\n\t"
        "ds_read_b64_tr_b16 a[30:31], v103 offset:16648\n\t"
        "ds_read_b64_tr_b16 a[32:33], v104 offset:16384\n\t"
        "ds_read_b64_tr_b16 a[34:35], v104 offset:16640\n\t"
        "ds_read_b64_tr_b16 a[36:37], v103 offset:18432\n\t"
        "ds_read_b64_tr_b16 a[38:39], v103 offset:18688\n\t"
        "ds_read_b64_tr_b16 a[40:41], v103 offset:18440\n\t"
        "ds_read_b64_tr_b16 a[42:43], v103 offset:18696\n\t"
        "ds_read_b64_tr_b16 a[44:45], v104 offset:18432\n\t"
        "ds_read_b64_tr_b16 a[46:47], v104 offset:18688\n\t"
        "s_waitcnt vmcnt(39)\n\t"
        "ds_write_b128 v102, a[216:219] offset:0\n\t"
        "ds_write_b128 v102, a[220:223] offset:4096\n\t"
        "ds_write_b128 v102, a[224:227] offset:8192\n\t"
        "s_add_u32 m0, %[ld], 32768\n\t"
        "s_add_u32 %[t0], %[xo], 512\n\t"
        "buffer_load_dwordx4 v100, %[sx], %[t0] offen lds\n\t"
        "s_add_u32 m0, %[ld], 33792\n\t"
        "s_add_u32 %[t0], %[xo], 16896\n\t"
        "buffer_load_dwordx4 v100, %[sx], %[t0] offen lds\n\t"
        "s_add_u32 m0, %[ld], 40960\n\t"
        "s_add_u32 %[t0], %[xo], 640\n\t"
        "buffer_load_dwordx4 v100, %[sx], %[t0] offen lds\n\t"
        "s_add_u32 m0, %[ld], 41984\n\t"
        "s_add_u32 %[t0], %[xo], 17024\n\t"
        "buffer_load_dwordx4 v100, %[sx], %[t0] offen lds\n\t"
        "s_add_u32 m0, %[ld], 49152\n\t"
        "s_add_u32 %[t0], %[xo], 768\n\t"
        "buffer_load_dwordx4 v100, %[sx], %[t0] offen lds\n\t"
        "s_add_u32 m0, %[ld], 50176\n\t"
        "s_add_u32 %[t0], %[xo], 17152\n\t"
        "buffer_load_dwordx4 v100, %[sx], %[t0] offen lds\n\t"
        "s_add_u32 m0, %[ld], 57344\n\t"
        "s_add_u32 %[t0], %[xo], 896\n\t"
        "buffer_load_dwordx4 v100, %[sx], %[t0] offen lds\n\t"
        "s_add_u32 m0, %[ld], 58368\n\t"
        "s_add_u32 %[t0], %[xo], 17280\n\t"
        "buffer_load_dwordx4 v100, %[sx], %[t0] offen lds\n\t"
        "s_waitcnt lgkmcnt(0)\n\t"
        "s_barrier\n\t"
        "ds_read_b64_tr_b16 a[48:49], v103 offset:0\n\t"
        "ds_read_b64_tr_b16 a[50:51], v103 offset:256\n\t"
        "ds_read_b64_tr_b16 a[52:53], v103 offset:8\n\t"
        "ds_read_b64_tr_b16 a[54:55], v103 offset:264\n\t"
        "ds_read_b64_tr_b16 a[56:57], v104 offset:0\n\t"
        "ds_read_b64_tr_b16 a[58:59], v104 offset:256\n\t"
        "ds_read_b64_tr_b16 a[60:61], v103 offset:2048\n\t"
        "ds_read_b64_tr_b16 a[62:63], v103 offset:2304\n\t"
        "ds_read_b64_tr_b16 a[64:65], v103 offset:2056\n\t"
        "ds_read_b64_tr_b16 a[66:67], v103 offset:2312\n\t"
        "ds_read_b64_tr_b16 a[68:69], v104 offset:2048\n\t"
        "ds_read_b64_tr_b16 a[70:71], v104 offset:2304\n\t"
        "s_waitcnt vmcnt(44)\n\t"
        "ds_write_b128 v102, a[228:231] offset:16384\n\t"
        "ds_write_b128 v102, a[232:235] offset:20480\n\t"
        "ds_write_b128 v102, a[236:239] offset:24576\n\t"
        "s_waitcnt lgkmcnt(0)\n\t"
        "s_barrier\n\t"
        "ds_read_b64_tr_b16 a[72:73], v103 offset:16384\n\t"
        "ds_read_b64_tr_b16 a[74:75], v103 offset:16640\n\t"
        "ds_read_b64_tr_b16 a[76:77], v103 offset:16392\n\t"
        "ds_read_b64_tr_b16 a[78:79], v103 offset:16648\n\t"
        "ds_read_b64_tr_b16 a[80:81], v104 offset:16384\n\t"
        "ds_read_b64_tr_b16 a[82:83], v104 offset:16640\n\t"
        "ds_read_b64_tr_b16 a[84:85], v103 offset:18432\n\t"
        "ds_read_b64_tr_b16 a[86:87], v103 offset:18688\n\t"
        "ds_read_b64_tr_b16 a[88:89], v103 offset:18440\n\t"
        "ds_read_b64_tr_b16 a[90:91], v103 offset:18696\n\t"
        "ds_read_b64_tr_b16 a[92:93], v104 offset:18432\n\t"
        "ds_read_b64_tr_b16 a[94:95], v104 offset:18688\n\t"
        "s_waitcnt vmcnt(41)\n\t"
        "ds_write_b128 v102, a[240:243] offset:0\n\t"
        "ds_write_b128 v102, a[244:247] offset:4096\n\t"
        "ds_write_b128 v102, a[248:251] offset:8192\n\t"
        "s_waitcnt lgkmcnt(0)\n\t"
        "s_barrier\n\t"
        "ds_read_b64_tr_b16 a[96:97], v103 offset:0\n\t"
        "ds_read_b64_tr_b16 a[98:99], v103 offset:256\n\t"
        "ds_read_b64_tr_b16 a[100:101], v103 offset:8\n\t"
        "ds_read_b64_tr_b16 a[102:103], v103 offset:264\n\t"
        "ds_read_b64_tr_b16 a[104:105], v104 offset:0\n\t"
        "ds_read_b64_tr_b16 a[106:107], v104 offset:256\n\t"
        "ds_read_b64_tr_b16 a[108:109], v103 offset:2048\n\t"
        "ds_read_b64_tr_b16 a[110:111], v103 offset:2304\n\t"
        "ds_read_b64_tr_b16 a[112:113], v103 offset:2056\n\t"
        "ds_read_b64_tr_b16 a[114:115], v103 offset:2312\n\t"
        "ds_read_b64_tr_b16 a[116:117], v104 offset:2048\n\t"
        "ds_read_b64_tr_b16 a[118:119], v104 offset:2304\n\t"
        "s_waitcnt vmcnt(38)\n\t"
        "ds_write_b128 v102, a[252:255] offset:16384\n\t"
        "ds_write_b128 v102, v[128:131] offset:20480\n\t"
        "ds_write_b128 v102, v[132:135] offset:24576\n\t"
        "s_add_u32 m0, %[ld], 65536\n\t"
        "s_add_u32 %[t0], %[xo], 1024\n\t"
        "buffer_load_dwordx4 v100, %[sx], %[t0] offen lds\n\t"
        "s_add_u32 m0, %[ld], 66560\n\t"
        "s_add_u32 %[t0], %[xo], 17408\n\t"
        "buffer_load_dwordx4 v100, %[sx], %[t0] offen lds\n\t"
        "s_add_u32 m0, %[ld], 73728\n\t"
        "s_add_u32 %[t0], %[xo], 1152\n\t"
        "buffer_load_dwordx4 v100, %[sx], %[t0] offen lds\n\t"
        "s_add_u32 m0, %[ld], 74752\n\t"
        "s_add_u32 %[t0], %[xo], 17536\n\t"
        "buffer_load_dwordx4 v100, %[sx], %[t0] offen lds\n\t"
        "s_add_u32 m0, %[ld], 81920\n\t"
        "s_add_u32 %[t0], %[xo], 1280\n\t"
        "buffer_load_dwordx4 v100, %[sx], %[t0] offen lds\n\t"
        "s_add_u32 m0, %[ld], 82944\n\t"
        "s_add_u32 %[t0], %[xo], 17664\n\t"
        "buffer_load_dwordx4 v100, %[sx], %[t0] offen lds\n\t"
        "s_add_u32 m0, %[ld], 90112\n\t"
        "s_add_u32 %[t0], %[xo], 1408\n\t"
        "buffer_load_dwordx4 v100, %[sx], %[t0] offen lds\n\t"
        "s_add_u32 m0, %[ld], 91136\n\t"
        "s_add_u32 %[t0], %[xo], 17792\n\t"
        "buffer_load_dwordx4 v100, %[sx], %[t0] offen lds\n\t"
        "s_waitcnt lgkmcnt(0)\n\t"
        "s_barrier\n\t"
        "ds_read_b64_tr_b16 a[120:121], v103 offset:16384\n\t"
        "ds_read_b64_tr_b16 a[122:123], v103 offset:16640\n\t"
        "ds_read_b64_tr_b16 a[124:125], v103 offset:16392\n\t"
        "ds_read_b64_tr_b16 a[126:127], v103 offset:16648\n\t"
        "ds_read_b64_tr_b16 a[128:129], v104 offset:16384\n\t"
        "ds_read_b64_tr_b16 a[130:131], v104 offset:16640\n\t"
        "ds_read_b64_tr_b16 a[132:133], v103 offset:18432\n\t"
        "ds_read_b64_tr_b16 a[134:135], v103 offset:18688\n\t"
        "ds_read_b64_tr_b16 a[136:137], v103 offset:18440\n\t"
        "ds_read_b64_tr_b16 a[138:139], v103 offset:18696\n\t"
        "ds_read_b64_tr_b16 a[140:141], v104 offset:18432\n\t"
        "ds_read_b64_tr_b16 a[142:143], v104 offset:18688\n\t"
        "s_waitcnt vmcnt(43)\n\t"
        "ds_write_b128 v102, v[136:139] offset:0\n\t"
        "ds_write_b128 v102, v[140:143] offset:4096\n\t"
        "ds_write_b128 v102, v[144:147] offset:8192\n\t"
        "s_waitcnt lgkmcnt(0)\n\t"
        "s_barrier\n\t"
        "ds_read_b64_tr_b16 a[144:145], v103 offset:0\n\t"
        "ds_read_b64_tr_b16 a[146:147], v103 offset:256\n\t"
        "ds_read_b64_tr_b16 a[148:149], v103 offset:8\n\t"
        "ds_read_b64_tr_b16 a[150:151], v103 offset:264\n\t"
        "ds_read_b64_tr_b16 a[152:153], v104 offset:0\n\t"
        "ds_read_b64_tr_b16 a[154:155], v104 offset:256\n\t"
        "ds_read_b64_tr_b16 a[156:157], v103 offset:2048\n\t"
        "ds_read_b64_tr_b16 a[158:159], v103 offset:2304\n\t"
        "ds_read_b64_tr_b16 a[160:161], v103 offset:2056\n\t"
        "ds_read_b64_tr_b16 a[162:163], v103 offset:2312\n\t"
        "ds_read_b64_tr_b16 a[164:165], v104 offset:2048\n\t"
        "ds_read_b64_tr_b16 a[166:167], v104 offset:2304\n\t"
        "s_waitcnt vmcnt(40)\n\t"
        "ds_write_b128 v102, v[148:151] offset:16384\n\t"
        "ds_write_b128 v102, v[152:155] offset:20480\n\t"
        "ds_write_b128 v102, v[156:159] offset:24576\n\t"
        "s_waitcnt lgkmcnt(0)\n\t"
        "s_barrier\n\t"
        "ds_read_b64_tr_b16 a[168:169], v103 offset:16384\n\t"
        "ds_read_b64_tr_b16 a[170:171], v103 offset:16640\n\t"
        "ds_read_b64_tr_b16 a[172:173], v103 offset:16392\n\t"
        "ds_read_b64_tr_b16 a[174:175], v103 offset:16648\n\t"
        "ds_read_b64_tr_b16 a[176:177], v104 offset:16384\n\t"
        "ds_read_b64_tr_b16 a[178:179], v104 offset:16640\n\t"
        "ds_read_b64_tr_b16 a[180:181], v103 offset:18432\n\t"
        "ds_read_b64_tr_b16 a[182:183], v103 offset:18688\n\t"
        "ds_read_b64_tr_b16 a[184:185], v103 offset:18440\n\t"
        "ds_read_b64_tr_b16 a[186:187], v103 offset:18696\n\t"
        "ds_read_b64_tr_b16 a[188:189], v104 offset:18432\n\t"
        "ds_read_b64_tr_b16 a[190:191], v104 offset:18688\n\t"
        "s_waitcnt vmcnt(37)\n\t"
        "ds_write_b128 v102, v[160:163] offset:0\n\t"
        "ds_write_b128 v102, v[164:167] offset:4096\n\t"
        "ds_write_b128 v102, v[168:171] offset:8192\n\t"
        "s_add_u32 m0, %[ld], 98304\n\t"
        "s_add_u32 %[t0], %[xo], 1536\n\t"
        "buffer_load_dwordx4 v100, %[sx], %[t0] offen lds\n\t"
        "s_add_u32 m0, %[ld], 99328\n\t"
        "s_add_u32 %[t0], %[xo], 17920\n\t"
        "buffer_load_dwordx4 v100, %[sx], %[t0] offen lds\n\t"
        "s_add_u32 m0, %[ld], 106496\n\t"
        "s_add_u32 %[t0], %[xo], 1664\n\t"
        "buffer_load_dwordx4 v100, %[sx], %[t0] offen lds\n\t"
        "s_add_u32 m0, %[ld], 107520\n\t"
        "s_add_u32 %[t0], %[xo], 18048\n\t"
        "buffer_load_dwordx4 v100, %[sx], %[t0] offen lds\n\t"
        "s_add_u32 m0, %[ld], 114688\n\t"
        "s_add_u32 %[t0], %[xo], 1792\n\t"
        "buffer_load_dwordx4 v100, %[sx], %[t0] offen lds\n\t"
        "s_add_u32 m0, %[ld], 115712\n\t"
        "s_add_u32 %[t0], %[xo], 18176\n\t"
        "buffer_load_dwordx4 v100, %[sx], %[t0] offen lds\n\t"
        "s_add_u32 m0, %[ld], 122880\n\t"
        "s_add_u32 %[t0], %[xo], 1920\n\t"
        "buffer_load_dwordx4 v100, %[sx], %[t0] offen lds\n\t"
        "s_add_u32 m0, %[ld], 123904\n\t"
        "s_add_u32 %[t0], %[xo], 18304\n\t"
        "buffer_load_dwordx4 v100, %[sx], %[t0] offen lds\n\t"
        "s_waitcnt lgkmcnt(0)\n\t"
        "s_barrier\n\t"
        "ds_read_b64_tr_b16 a[192:193], v103 offset:0\n\t"
        "ds_read_b64_tr_b16 a[194:195], v103 offset:256\n\t"
        "ds_read_b64_tr_b16 a[196:197], v103 offset:8\n\t"
        "ds_read_b64_tr_b16 a[198:199], v103 offset:264\n\t"
        "ds_read_b64_tr_b16 a[200:201], v104 offset:0\n\t"
        "ds_read_b64_tr_b16 a[202:203], v104 offset:256\n\t"
        "ds_read_b64_tr_b16 a[204:205], v103 offset:2048\n\t"
        "ds_read_b64_tr_b16 a[206:207], v103 offset:2304\n\t"
        "ds_read_b64_tr_b16 a[208:209], v103 offset:2056\n\t"
        "ds_read_b64_tr_b16 a[210:211], v103 offset:2312\n\t"
        "ds_read_b64_tr_b16 a[212:213], v104 offset:2048\n\t"
        "ds_read_b64_tr_b16 a[214:215], v104 offset:2304\n\t"
        "s_waitcnt vmcnt(42)\n\t"
        "ds_write_b128 v102, v[172:175] offset:16384\n\t"
        "ds_write_b128 v102, v[176:179] offset:20480\n\t"
        "ds_write_b128 v102, v[180:183] offset:24576\n\t"
        "s_waitcnt lgkmcnt(0)\n\t"
        "s_barrier\n\t"
        "ds_read_b64_tr_b16 a[216:217], v103 offset:16384\n\t"
        "ds_read_b64_tr_b16 a[218:219], v103 offset:16640\n\t"
        "ds_read_b64_tr_b16 a[220:221], v103 offset:16392\n\t"
        "ds_read_b64_tr_b16 a[222:223], v103 offset:16648\n\t"
        "ds_read_b64_tr_b16 a[224:225], v104 offset:16384\n\t"
        "ds_read_b64_tr_b16 a[226:227], v104 offset:16640\n\t"
        "ds_read_b64_tr_b16 a[228:229], v103 offset:18432\n\t"
        "ds_read_b64_tr_b16 a[230:231], v103 offset:18688\n\t"
        "ds_read_b64_tr_b16 a[232:233], v103 offset:18440\n\t"
        "ds_read_b64_tr_b16 a[234:235], v103 offset:18696\n\t"
        "ds_read_b64_tr_b16 a[236:237], v104 offset:18432\n\t"
        "ds_read_b64_tr_b16 a[238:239], v104 offset:18688\n\t"
        "s_waitcnt vmcnt(39)\n\t"
        "ds_write_b128 v102, v[184:187] offset:0\n\t"
        "ds_write_b128 v102, v[188:191] offset:4096\n\t"
        "ds_write_b128 v102, v[192:195] offset:8192\n\t"
        "s_waitcnt lgkmcnt(0)\n\t"
        "s_barrier\n\t"
        "ds_read_b64_tr_b16 a[240:241], v103 offset:0\n\t"
        "ds_read_b64_tr_b16 a[242:243], v103 offset:256\n\t"
        "ds_read_b64_tr_b16 a[244:245], v103 offset:8\n\t"
        "ds_read_b64_tr_b16 a[246:247], v103 offset:264\n\t"
        "ds_read_b64_tr_b16 a[248:249], v104 offset:0\n\t"
        "ds_read_b64_tr_b16 a[250:251], v104 offset:256\n\t"
        "ds_read_b64_tr_b16 a[252:253], v103 offset:2048\n\t"
        "ds_read_b64_tr_b16 a[254:255], v103 offset:2304\n\t"
        "ds_read_b64_tr_b16 v[128:129], v103 offset:2056\n\t"
        "ds_read_b64_tr_b16 v[130:131], v103 offset:2312\n\t"
        "ds_read_b64_tr_b16 v[132:133], v104 offset:2048\n\t"
        "ds_read_b64_tr_b16 v[134:135], v104 offset:2304\n\t"
        "s_waitcnt vmcnt(36)\n\t"
        "ds_write_b128 v102, v[196:199] offset:16384\n\t"
        "ds_write_b128 v102, v[200:203] offset:20480\n\t"
        "ds_write_b128 v102, v[204:207] offset:24576\n\t"
        "s_waitcnt lgkmcnt(0)\n\t"
        "s_barrier\n\t"
        "ds_read_b64_tr_b16 v[136:137], v103 offset:16384\n\t"
        "ds_read_b64_tr_b16 v[138:139], v103 offset:16640\n\t"
        "ds_read_b64_tr_b16 v[140:141], v103 offset:16392\n\t"
        "ds_read_b64_tr_b16 v[142:143], v103 offset:16648\n\t"
        "ds_read_b64_tr_b16 v[144:145], v104 offset:16384\n\t"
        "ds_read_b64_tr_b16 v[146:147], v104 offset:16640\n\t"
        "ds_read_b64_tr_b16 v[148:149], v103 offset:18432\n\t"
        "ds_read_b64_tr_b16 v[150:151], v103 offset:18688\n\t"
        "ds_read_b64_tr_b16 v[152:153], v103 offset:18440\n\t"
        "ds_read_b64_tr_b16 v[154:155], v103 offset:18696\n\t"
        "ds_read_b64_tr_b16 v[156:157], v104 offset:18432\n\t"
        "ds_read_b64_tr_b16 v[158:159], v104 offset:18688\n\t"
        "s_waitcnt vmcnt(33)\n\t"
        "ds_write_b128 v102, v[208:211] offset:0\n\t"
        "ds_write_b128 v102, v[212:215] offset:4096\n\t"
        "ds_write_b128 v102, v[216:219] offset:8192\n\t"
        "s_waitcnt lgkmcnt(0)\n\t"
        "s_barrier\n\t"
        "ds_read_b64_tr_b16 v[160:161], v103 offset:0\n\t"
        "ds_read_b64_tr_b16 v[162:163], v103 offset:256\n\t"
        "ds_read_b64_tr_b16 v[164:165], v103 offset:8\n\t"
        "ds_read_b64_tr_b16 v[166:167], v103 offset:264\n\t"
        "ds_read_b64_tr_b16 v[168:169], v104 offset:0\n\t"
        "ds_read_b64_tr_b16 v[170:171], v104 offset:256\n\t"
        "ds_read_b64_tr_b16 v[172:173], v103 offset:2048\n\t"
        "ds_read_b64_tr_b16 v[174:175], v103 offset:2304\n\t"
        "ds_read_b64_tr_b16 v[176:177], v103 offset:2056\n\t"
        "ds_read_b64_tr_b16 v[178:179], v103 offset:2312\n\t"
        "ds_read_b64_tr_b16 v[180:181], v104 offset:2048\n\t"
        "ds_read_b64_tr_b16 v[182:183], v104 offset:2304\n\t"
        "s_waitcnt vmcnt(30)\n\t"
        "ds_write_b128 v102, v[220:223] offset:16384\n\t"
        "ds_write_b128 v102, v[224:227] offset:20480\n\t"
        "ds_write_b128 v102, v[228:231] offset:24576\n\t"
        "s_waitcnt lgkmcnt(0)\n\t"
        "s_barrier\n\t"
        "ds_read_b64_tr_b16 v[184:185], v103 offset:16384\n\t"
        "ds_read_b64_tr_b16 v[186:187], v103 offset:16640\n\t"
        "ds_read_b64_tr_b16 v[188:189], v103 offset:16392\n\t"
        "ds_read_b64_tr_b16 v[190:191], v103 offset:16648\n\t"
        "ds_read_b64_tr_b16 v[192:193], v104 offset:16384\n\t"
        "ds_read_b64_tr_b16 v[194:195], v104 offset:16640\n\t"
        "ds_read_b64_tr_b16 v[196:197], v103 offset:18432\n\t"
        "ds_read_b64_tr_b16 v[198:199], v103 offset:18688\n\t"
        "ds_read_b64_tr_b16 v[200:201], v103 offset:18440\n\t"
        "ds_read_b64_tr_b16 v[202:203], v103 offset:18696\n\t"
        "ds_read_b64_tr_b16 v[204:205], v104 offset:18432\n\t"
        "ds_read_b64_tr_b16 v[206:207], v104 offset:18688\n\t"
        "s_waitcnt vmcnt(27)\n\t"
        "ds_write_b128 v102, v[232:235] offset:0\n\t"
        "ds_write_b128 v102, v[236:239] offset:4096\n\t"
        "ds_write_b128 v102, v[240:243] offset:8192\n\t"
        "s_waitcnt lgkmcnt(0)\n\t"
        "s_barrier\n\t"
        "ds_read_b64_tr_b16 v[208:209], v103 offset:0\n\t"
        "ds_read_b64_tr_b16 v[210:211], v103 offset:256\n\t"
        "ds_read_b64_tr_b16 v[212:213], v103 offset:8\n\t"
        "ds_read_b64_tr_b16 v[214:215], v103 offset:264\n\t"
        "ds_read_b64_tr_b16 v[216:217], v104 offset:0\n\t"
        "ds_read_b64_tr_b16 v[218:219], v104 offset:256\n\t"
        "ds_read_b64_tr_b16 v[220:221], v103 offset:2048\n\t"
        "ds_read_b64_tr_b16 v[222:223], v103 offset:2304\n\t"
        "ds_read_b64_tr_b16 v[224:225], v103 offset:2056\n\t"
        "ds_read_b64_tr_b16 v[226:227], v103 offset:2312\n\t"
        "ds_read_b64_tr_b16 v[228:229], v104 offset:2048\n\t"
        "ds_read_b64_tr_b16 v[230:231], v104 offset:2304\n\t"
        "s_waitcnt vmcnt(24)\n\t"
        "ds_write_b128 v102, v[244:247] offset:16384\n\t"
        "ds_write_b128 v102, v[248:251] offset:20480\n\t"
        "ds_write_b128 v102, v[252:255] offset:24576\n\t"
        "s_waitcnt lgkmcnt(0)\n\t"
        "s_barrier\n\t"
        "ds_read_b64_tr_b16 v[232:233], v103 offset:16384\n\t"
        "ds_read_b64_tr_b16 v[234:235], v103 offset:16640\n\t"
        "ds_read_b64_tr_b16 v[236:237], v103 offset:16392\n\t"
        "ds_read_b64_tr_b16 v[238:239], v103 offset:16648\n\t"
        "ds_read_b64_tr_b16 v[240:241], v104 offset:16384\n\t"
        "ds_read_b64_tr_b16 v[242:243], v104 offset:16640\n\t"
        "ds_read_b64_tr_b16 v[244:245], v103 offset:18432\n\t"
        "ds_read_b64_tr_b16 v[246:247], v103 offset:18688\n\t"
        "ds_read_b64_tr_b16 v[248:249], v103 offset:18440\n\t"
        "ds_read_b64_tr_b16 v[250:251], v103 offset:18696\n\t"
        "ds_read_b64_tr_b16 v[252:253], v104 offset:18432\n\t"
        "ds_read_b64_tr_b16 v[254:255], v104 offset:18688\n\t"
        "s_waitcnt vmcnt(0) lgkmcnt(0)\n\t"
        "s_barrier\n\t"
        "ds_read_b128 v[56:59], v105 offset:0\n\t"
        "ds_read_b128 v[60:63], v105 offset:2048\n\t"
        "ds_read_b128 v[64:67], v106 offset:0\n\t"
        "ds_read_b128 v[68:71], v106 offset:2048\n\t"
        "ds_read_b128 v[72:75], v105 offset:8192\n\t"
        "ds_read_b128 v[76:79], v105 offset:10240\n\t"
        "ds_read_b128 v[80:83], v106 offset:8192\n\t"
        "LG_loop%=:\n\t"
        "s_waitcnt lgkmcnt(6)\n\t"
        "v_mfma_f32_16x16x32_bf16 v[8:11], a[0:3], v[56:59], 0\n\t"
        "v_mfma_f32_16x16x32_bf16 v[12:15], a[4:7], v[56:59], 0\n\t"
        "ds_read_b128 v[84:87], v106 offset:10240\n\t"
        "v_mfma_f32_16x16x32_bf16 v[16:19], v[56:59], a[8:11], 0\n\t"
        "s_add_u32 m0, %[ld], 107520\n\t"
        "s_add_u32 %[t0], %[xo], 18048\n\t"
        "s_waitcnt lgkmcnt(6)\n\t"
        "v_mfma_f32_16x16x32_bf16 v[20:23], a[0:3], v[60:63], 0\n\t"
        "buffer_load_dwordx4 v100, %[sx], %[t0] offen lds\n\t"
        "v_mfma_f32_16x16x32_bf16 v[24:27], a[4:7], v[60:63], 0\n\t"
        "ds_read_b128 v[56:59], v105 offset:16384\n\t"
        "v_mfma_f32_16x16x32_bf16 v[28:31], v[60:63], a[8:11], 0\n\t"
        "s_waitcnt lgkmcnt(6)\n\t"
        "v_mfma_f32_16x16x32_bf16 v[8:11], a[12:15], v[64:67], v[8:11]\n\t"
        "v_cvt_pk_bf16_f32 v88, v32, v33\n\t"
        "v_cvt_pk_bf16_f32 v89, v34, v35\n\t"
        "v_mfma_f32_16x16x32_bf16 v[12:15], a[16:19], v[64:67], v[12:15]\n\t"
        "ds_read_b128 v[60:63], v105 offset:18432\n\t"
        "v_cvt_pk_bf16_f32 v90, v36, v37\n\t"
        "v_mfma_f32_16x16x32_bf16 v[16:19], v[64:67], a[20:23], v[16:19]\n\t"
        "s_add_u32 m0, %[ld], 114688\n\t"
        "s_add_u32 %[t0], %[xo], 1792\n\t"
        "s_waitcnt lgkmcnt(6)\n\t"
        "v_mfma_f32_16x16x32_bf16 v[20:23], a[12:15], v[68:71], v[20:23]\n\t"
        "buffer_load_dwordx4 v100, %[sx], %[t0] offen lds\n\t"
        "v_mfma_f32_16x16x32_bf16 v[24:27], a[16:19], v[68:71], v[24:27]\n\t"
        "ds_read_b128 v[64:67], v106 offset:16384\n\t"
        "v_cvt_pk_bf16_f32 v91, v38, v39\n\t"
        "v_mfma_f32_16x16x32_bf16 v[28:31], v[68:71], a[20:23], v[28:31]\n\t"
        "buffer_store_dwordx4 v[88:91], v109, %[sw], %[so] offen sc1\n\t"
        "s_waitcnt lgkmcnt(6)\n\t"
        "v_mfma_f32_16x16x32_bf16 v[8:11], a[24:27], v[72:75], v[8:11]\n\t"
        "v_cvt_pk_bf16_f32 v92, v44, v45\n\t"
        "v_cvt_pk_bf16_f32 v93, v46, v47\n\t"
        "v_mfma_f32_16x16x32_bf16 v[12:15], a[28:31], v[72:75], v[12:15]\n\t"
        "ds_read_b128 v[68:71], v106 offset:18432\n\t"
        "v_cvt_pk_bf16_f32 v94, v48, v49\n\t"
        "v_mfma_f32_16x16x32_bf16 v[16:19], v[72:75], a[32:35], v[16:19]\n\t"
        "s_add_u32 m0, %[ld], 115712\n\t"
        "s_add_u32 %[t0], %[xo], 18176\n\t"
        "s_waitcnt lgkmcnt(6)\n\t"
        "v_mfma_f32_16x16x32_bf16 v[20:23], a[24:27], v[76:79], v[20:23]\n\t"
        "buffer_load_dwordx4 v100, %[sx], %[t0] offen lds\n\t"
        "v_mfma_f32_16x16x32_bf16 v[24:27], a[28:31], v[76:79], v[24:27]\n\t"
        "ds_read_b128 v[72:75], v105 offset:24576\n\t"
        "v_cvt_pk_bf16_f32 v95, v50, v51\n\t"
        "v_mfma_f32_16x16x32_bf16 v[28:31], v[76:79], a[32:35], v[28:31]\n\t"
        "buffer_store_dwordx4 v[92:95], v110, %[sw], %[so] offen sc1\n\t"
        "s_waitcnt lgkmcnt(6)\n\t"
        "v_mfma_f32_16x16x32_bf16 v[8:11], a[36:39], v[80:83], v[8:11]\n\t"
        "v_cvt_pk_bf16_f32 v96, v40, v41\n\t"
        "v_cvt_pk_bf16_f32 v97, v42, v43\n\t"
        "v_mfma_f32_16x16x32_bf16 v[12:15], a[40:43], v[80:83], v[12:15]\n\t"
        "ds_read_b128 v[76:79], v105 offset:26624\n\t"
        "v_cvt_pk_bf16_f32 v98, v52, v53\n\t"
        "v_mfma_f32_16x16x32_bf16 v[16:19], v[80:83], a[44:47], v[16:19]\n\t"
        "s_add_u32 m0, %[ld], 122880\n\t"
        "s_add_u32 %[t0], %[xo], 1920\n\t"
        "s_waitcnt lgkmcnt(6)\n\t"
        "v_mfma_f32_16x16x32_bf16 v[20:23], a[36:39], v[84:87], v[20:23]\n\t"
        "buffer_load_dwordx4 v100, %[sx], %[t0] offen lds\n\t"
        "v_mfma_f32_16x16x32_bf16 v[24:27], a[40:43], v[84:87], v[24:27]\n\t"
        "ds_read_b128 v[80:83], v106 offset:24576\n\t"
        "v_cvt_pk_bf16_f32 v99, v54, v55\n\t"
        "v_mfma_f32_16x16x32_bf16 v[28:31], v[84:87], a[44:47], v[28:31]\n\t"
        "buffer_store_dwordx4 v[96:99], v111, %[sw], %[so] offen sc1\n\t"
        "s_waitcnt lgkmcnt(6)\n\t"
        "v_mfma_f32_16x16x32_bf16 v[8:11], a[48:51], v[56:59], v[8:11]\n\t"
        "s_lshl_b32 %[so], %[cn], 19\n\t"
        "v_mfma_f32_16x16x32_bf16 v[12:15], a[52:55], v[56:59], v[12:15]\n\t"
        "ds_read_b128 v[84:87], v106 offset:26624\n\t"
        "v_mfma_f32_16x16x32_bf16 v[16:19], v[56:59], a[56:59], v[16:19]\n\t"
        "s_add_u32 m0, %[ld], 123904\n\t"
        "s_add_u32 %[t0], %[xo], 18304\n\t"
        "s_waitcnt lgkmcnt(6)\n\t"
        "v_mfma_f32_16x16x32_bf16 v[20:23], a[48:51], v[60:63], v[20:23]\n\t"
        "buffer_load_dwordx4 v100, %[sx], %[t0] offen lds\n\t"
        "v_mfma_f32_16x16x32_bf16 v[24:27], a[52:55], v[60:63], v[24:27]\n\t"
        "v_mfma_f32_16x16x32_bf16 v[28:31], v[60:63], a[56:59], v[28:31]\n\t"
        "s_waitcnt lgkmcnt(5)\n\t"
        "v_mfma_f32_16x16x32_bf16 v[8:11], a[60:63], v[64:67], v[8:11]\n\t"
        "s_waitcnt vmcnt(19) lgkmcnt(0)\n\t"
        "s_barrier\n\t"
        "s_add_u32 %[xo], %[xo], 0x20000\n\t"
        "v_mfma_f32_16x16x32_bf16 v[12:15], a[64:67], v[64:67], v[12:15]\n\t"
        "ds_read_b128 v[56:59], v105 offset:32768\n\t"
        "v_mfma_f32_16x16x32_bf16 v[16:19], v[64:67], a[68:71], v[16:19]\n\t"
        "ds_read_b128 v[60:63], v105 offset:34816\n\t"
        "s_add_u32 m0, %[ld], 0\n\t"
        "s_add_u32 %[t0], %[xo], 0\n\t"
        "v_mfma_f32_16x16x32_bf16 v[20:23], a[60:63], v[68:71], v[20:23]\n\t"
        "buffer_load_dwordx4 v100, %[sx], %[t0] offen lds\n\t"
        "v_mfma_f32_16x16x32_bf16 v[24:27], a[64:67], v[68:71], v[24:27]\n\t"
        "ds_read_b128 v[64:67], v106 offset:32768\n\t"
        "v_mfma_f32_16x16x32_bf16 v[28:31], v[68:71], a[68:71], v[28:31]\n\t"
        "v_mfma_f32_16x16x32_bf16 v[8:11], a[72:75], v[72:75], v[8:11]\n\t"
        "v_mfma_f32_16x16x32_bf16 v[12:15], a[76:79], v[72:75], v[12:15]\n\t"
        "ds_read_b128 v[68:71], v106 offset:34816\n\t"
        "v_mfma_f32_16x16x32_bf16 v[16:19], v[72:75], a[80:83], v[16:19]\n\t"
        "s_add_u32 m0, %[ld], 1024\n\t"
        "s_add_u32 %[t0], %[xo], 16384\n\t"
        "v_mfma_f32_16x16x32_bf16 v[20:23], a[72:75], v[76:79], v[20:23]\n\t"
        "buffer_load_dwordx4 v100, %[sx], %[t0] offen lds\n\t"
        "v_mfma_f32_16x16x32_bf16 v[24:27], a[76:79], v[76:79], v[24:27]\n\t"
        "ds_read_b128 v[72:75], v105 offset:40960\n\t"
        "v_mfma_f32_16x16x32_bf16 v[28:31], v[76:79], a[80:83], v[28:31]\n\t"
        "v_mfma_f32_16x16x32_bf16 v[8:11], a[84:87], v[80:83], v[8:11]\n\t"
        "v_mfma_f32_16x16x32_bf16 v[12:15], a[88:91], v[80:83], v[12:15]\n\t"
        "ds_read_b128 v[76:79], v105 offset:43008\n\t"
        "v_mfma_f32_16x16x32_bf16 v[16:19], v[80:83], a[92:95], v[16:19]\n\t"
        "s_add_u32 m0, %[ld], 8192\n\t"
        "s_add_u32 %[t0], %[xo], 128\n\t"
        "v_mfma_f32_16x16x32_bf16 v[20:23], a[84:87], v[84:87], v[20:23]\n\t"
        "buffer_load_dwordx4 v100, %[sx], %[t0] offen lds\n\t"
        "v_mfma_f32_16x16x32_bf16 v[24:27], a[88:91], v[84:87], v[24:27]\n\t"
        "ds_read_b128 v[80:83], v106 offset:40960\n\t"
        "v_mfma_f32_16x16x32_bf16 v[28:31], v[84:87], a[92:95], v[28:31]\n\t"
        "s_waitcnt lgkmcnt(6)\n\t"
        "v_mfma_f32_16x16x32_bf16 v[8:11], a[96:99], v[56:59], v[8:11]\n\t"
        "v_mfma_f32_16x16x32_bf16 v[12:15], a[100:103], v[56:59], v[12:15]\n\t"
        "ds_read_b128 v[84:87], v106 offset:43008\n\t"
        "v_mfma_f32_16x16x32_bf16 v[16:19], v[56:59], a[104:107], v[16:19]\n\t"
        "s_add_u32 m0, %[ld], 9216\n\t"
        "s_add_u32 %[t0], %[xo], 16512\n\t"
        "s_waitcnt lgkmcnt(6)\n\t"
        "v_mfma_f32_16x16x32_bf16 v[20:23], a[96:99], v[60:63], v[20:23]\n\t"
        "buffer_load_dwordx4 v100, %[sx], %[t0] offen lds\n\t"
        "v_mfma_f32_16x16x32_bf16 v[24:27], a[100:103], v[60:63], v[24:27]\n\t"
        "ds_read_b128 v[56:59], v105 offset:49152\n\t"
        "v_mfma_f32_16x16x32_bf16 v[28:31], v[60:63], a[104:107], v[28:31]\n\t"
        "s_waitcnt lgkmcnt(6)\n\t"
        "v_mfma_f32_16x16x32_bf16 v[8:11], a[108:111], v[64:67], v[8:11]\n\t"
        "v_mfma_f32_16x16x32_bf16 v[12:15], a[112:115], v[64:67], v[12:15]\n\t"
        "ds_read_b128 v[60:63], v105 offset:51200\n\t"
        "v_mfma_f32_16x16x32_bf16 v[16:19], v[64:67], a[116:119], v[16:19]\n\t"
        "s_add_u32 m0, %[ld], 16384\n\t"
        "s_add_u32 %[t0], %[xo], 256\n\t"
        "s_waitcnt lgkmcnt(6)\n\t"
        "v_mfma_f32_16x16x32_bf16 v[20:23], a[108:111], v[68:71], v[20:23]\n\t"
        "buffer_load_dwordx4 v100, %[sx], %[t0] offen lds\n\t"
        "v_mfma_f32_16x16x32_bf16 v[24:27], a[112:115], v[68:71], v[24:27]\n\t"
        "ds_read_b128 v[64:67], v106 offset:49152\n\t"
        "v_mfma_f32_16x16x32_bf16 v[28:31], v[68:71], a[116:119], v[28:31]\n\t"
        "s_waitcnt lgkmcnt(6)\n\t"
        "v_mfma_f32_16x16x32_bf16 v[8:11], a[120:123], v[72:75], v[8:11]\n\t"
        "v_mfma_f32_16x16x32_bf16 v[12:15], a[124:127], v[72:75], v[12:15]\n\t"
        "ds_read_b128 v[68:71], v106 offset:51200\n\t"
        "v_mfma_f32_16x16x32_bf16 v[16:19], v[72:75], a[128:131], v[16:19]\n\t"
        "s_add_u32 m0, %[ld], 17408\n\t"
        "s_add_u32 %[t0], %[xo], 16640\n\t"
        "s_waitcnt lgkmcnt(6)\n\t"
        "v_mfma_f32_16x16x32_bf16 v[20:23], a[120:123], v[76:79], v[20:23]\n\t"
        "buffer_load_dwordx4 v100, %[sx], %[t0] offen lds\n\t"
        "v_mfma_f32_16x16x32_bf16 v[24:27], a[124:127], v[76:79], v[24:27]\n\t"
        "ds_read_b128 v[72:75], v105 offset:57344\n\t"
        "v_mfma_f32_16x16x32_bf16 v[28:31], v[76:79], a[128:131], v[28:31]\n\t"
        "s_waitcnt lgkmcnt(6)\n\t"
        "v_mfma_f32_16x16x32_bf16 v[8:11], a[132:135], v[80:83], v[8:11]\n\t"
        "v_mfma_f32_16x16x32_bf16 v[12:15], a[136:139], v[80:83], v[12:15]\n\t"
        "ds_read_b128 v[76:79], v105 offset:59392\n\t"
        "v_mfma_f32_16x16x32_bf16 v[16:19], v[80:83], a[140:143], v[16:19]\n\t"
        "s_add_u32 m0, %[ld], 24576\n\t"
        "s_add_u32 %[t0], %[xo], 384\n\t"
        "s_waitcnt lgkmcnt(6)\n\t"
        "v_mfma_f32_16x16x32_bf16 v[20:23], a[132:135], v[84:87], v[20:23]\n\t"
        "buffer_load_dwordx4 v100, %[sx], %[t0] offen lds\n\t"
        "v_mfma_f32_16x16x32_bf16 v[24:27], a[136:139], v[84:87], v[24:27]\n\t"
        "ds_read_b128 v[80:83], v106 offset:57344\n\t"
        "v_mfma_f32_16x16x32_bf16 v[28:31], v[84:87], a[140:143], v[28:31]\n\t"
        "s_waitcnt lgkmcnt(6)\n\t"
        "v_mfma_f32_16x16x32_bf16 v[8:11], a[144:147], v[56:59], v[8:11]\n\t"
        "v_mfma_f32_16x16x32_bf16 v[12:15], a[148:151], v[56:59], v[12:15]\n\t"
        "ds_read_b128 v[84:87], v106 offset:59392\n\t"
        "v_mfma_f32_16x16x32_bf16 v[16:19], v[56:59], a[152:155], v[16:19]\n\t"
        "s_add_u32 m0, %[ld], 25600\n\t"
        "s_add_u32 %[t0], %[xo], 16768\n\t"
        "s_waitcnt lgkmcnt(6)\n\t"
        "v_mfma_f32_16x16x32_bf16 v[20:23], a[144:147], v[60:63], v[20:23]\n\t"
        "buffer_load_dwordx4 v100, %[sx], %[t0] offen lds\n\t"
        "v_mfma_f32_16x16x32_bf16 v[24:27], a[148:151], v[60:63], v[24:27]\n\t"
        "v_mfma_f32_16x16x32_bf16 v[28:31], v[60:63], a[152:155], v[28:31]\n\t"
        "s_waitcnt lgkmcnt(5)\n\t"
        "v_mfma_f32_16x16x32_bf16 v[8:11], a[156:159], v[64:67], v[8:11]\n\t"
        "s_waitcnt vmcnt(9) lgkmcnt(0)\n\t"
        "s_barrier\n\t"
        "v_mfma_f32_16x16x32_bf16 v[12:15], a[160:163], v[64:67], v[12:15]\n\t"
        "ds_read_b128 v[56:59], v107 offset:0\n\t"
        "v_mfma_f32_16x16x32_bf16 v[16:19], v[64:67], a[164:167], v[16:19]\n\t"
        "ds_read_b128 v[60:63], v107 offset:2048\n\t"
        "s_add_u32 m0, %[ld], 32768\n\t"
        "s_add_u32 %[t0], %[xo], 512\n\t"
        "v_mfma_f32_16x16x32_bf16 v[20:23], a[156:159], v[68:71], v[20:23]\n\t"
        "buffer_load_dwordx4 v100, %[sx], %[t0] offen lds\n\t"
        "v_mfma_f32_16x16x32_bf16 v[24:27], a[160:163], v[68:71], v[24:27]\n\t"
        "ds_read_b128 v[64:67], v108 offset:0\n\t"
        "v_mfma_f32_16x16x32_bf16 v[28:31], v[68:71], a[164:167], v[28:31]\n\t"
        "v_mfma_f32_16x16x32_bf16 v[8:11], a[168:171], v[72:75], v[8:11]\n\t"
        "v_mfma_f32_16x16x32_bf16 v[12:15], a[172:175], v[72:75], v[12:15]\n\t"
        "ds_read_b128 v[68:71], v108 offset:2048\n\t"
        "v_mov_b32_e32 v114, %[cn]\n\t"
        "buffer_store_dword v114, v113, %[sw], 0 offen sc1\n\t"
        "s_add_u32 %[cn], %[cn], 1\n\t"
        "v_mfma_f32_16x16x32_bf16 v[16:19], v[72:75], a[176:179], v[16:19]\n\t"
        "s_add_u32 m0, %[ld], 33792\n\t"
        "s_add_u32 %[t0], %[xo], 16896\n\t"
        "v_mfma_f32_16x16x32_bf16 v[20:23], a[168:171], v[76:79], v[20:23]\n\t"
        "buffer_load_dwordx4 v100, %[sx], %[t0] offen lds\n\t"
        "v_mfma_f32_16x16x32_bf16 v[24:27], a[172:175], v[76:79], v[24:27]\n\t"
        "ds_read_b128 v[72:75], v107 offset:8192\n\t"
        "v_mfma_f32_16x16x32_bf16 v[28:31], v[76:79], a[176:179], v[28:31]\n\t"
        "v_mfma_f32_16x16x32_bf16 v[8:11], a[180:183], v[80:83], v[8:11]\n\t"
        "v_mfma_f32_16x16x32_bf16 v[12:15], a[184:187], v[80:83], v[12:15]\n\t"
        "ds_read_b128 v[76:79], v107 offset:10240\n\t"
        "v_mfma_f32_16x16x32_bf16 v[16:19], v[80:83], a[188:191], v[16:19]\n\t"
        "s_add_u32 m0, %[ld], 40960\n\t"
        "s_add_u32 %[t0], %[xo], 640\n\t"
        "v_mfma_f32_16x16x32_bf16 v[20:23], a[180:183], v[84:87], v[20:23]\n\t"
        "buffer_load_dwordx4 v100, %[sx], %[t0] offen lds\n\t"
        "v_mfma_f32_16x16x32_bf16 v[24:27], a[184:187], v[84:87], v[24:27]\n\t"
        "ds_read_b128 v[80:83], v108 offset:8192\n\t"
        "v_mfma_f32_16x16x32_bf16 v[28:31], v[84:87], a[188:191], v[28:31]\n\t"
        "s_waitcnt lgkmcnt(6)\n\t"
        "v_mfma_f32_16x16x32_bf16 v[8:11], a[192:195], v[56:59], v[8:11]\n\t"
        "v_mfma_f32_16x16x32_bf16 v[12:15], a[196:199], v[56:59], v[12:15]\n\t"
        "ds_read_b128 v[84:87], v108 offset:10240\n\t"
        "v_mfma_f32_16x16x32_bf16 v[16:19], v[56:59], a[200:203], v[16:19]\n\t"
        "s_add_u32 m0, %[ld], 41984\n\t"
        "s_add_u32 %[t0], %[xo], 17024\n\t"
        "s_waitcnt lgkmcnt(6)\n\t"
        "v_mfma_f32_16x16x32_bf16 v[20:23], a[192:195], v[60:63], v[20:23]\n\t"
        "buffer_load_dwordx4 v100, %[sx], %[t0] offen lds\n\t"
        "v_mfma_f32_16x16x32_bf16 v[24:27], a[196:199], v[60:63], v[24:27]\n\t"
        "ds_read_b128 v[56:59], v107 offset:16384\n\t"
        "v_mfma_f32_16x16x32_bf16 v[28:31], v[60:63], a[200:203], v[28:31]\n\t"
        "s_waitcnt lgkmcnt(6)\n\t"
        "v_mfma_f32_16x16x32_bf16 v[8:11], a[204:207], v[64:67], v[8:11]\n\t"
        "v_mfma_f32_16x16x32_bf16 v[12:15], a[208:211], v[64:67], v[12:15]\n\t"
        "ds_read_b128 v[60:63], v107 offset:18432\n\t"
        "v_mfma_f32_16x16x32_bf16 v[16:19], v[64:67], a[212:215], v[16:19]\n\t"
        "s_add_u32 m0, %[ld], 49152\n\t"
        "s_add_u32 %[t0], %[xo], 768\n\t"
        "s_waitcnt lgkmcnt(6)\n\t"
        "v_mfma_f32_16x16x32_bf16 v[20:23], a[204:207], v[68:71], v[20:23]\n\t"
        "buffer_load_dwordx4 v100, %[sx], %[t0] offen lds\n\t"
        "v_mfma_f32_16x16x32_bf16 v[24:27], a[208:211], v[68:71], v[24:27]\n\t"
        "ds_read_b128 v[64:67], v108 offset:16384\n\t"
        "v_mfma_f32_16x16x32_bf16 v[28:31], v[68:71], a[212:215], v[28:31]\n\t"
        "s_waitcnt lgkmcnt(6)\n\t"
        "v_mfma_f32_16x16x32_bf16 v[8:11], a[216:219], v[72:75], v[8:11]\n\t"
        "v_mfma_f32_16x16x32_bf16 v[12:15], a[220:223], v[72:75], v[12:15]\n\t"
        "ds_read_b128 v[68:71], v108 offset:18432\n\t"
        "v_mfma_f32_16x16x32_bf16 v[16:19], v[72:75], a[224:227], v[16:19]\n\t"
        "s_add_u32 m0, %[ld], 50176\n\t"
        "s_add_u32 %[t0], %[xo], 17152\n\t"
        "s_waitcnt lgkmcnt(6)\n\t"
        "v_mfma_f32_16x16x32_bf16 v[20:23], a[216:219], v[76:79], v[20:23]\n\t"
        "buffer_load_dwordx4 v100, %[sx], %[t0] offen lds\n\t"
        "v_mfma_f32_16x16x32_bf16 v[24:27], a[220:223], v[76:79], v[24:27]\n\t"
        "ds_read_b128 v[72:75], v107 offset:24576\n\t"
        "v_mfma_f32_16x16x32_bf16 v[28:31], v[76:79], a[224:227], v[28:31]\n\t"
        "s_waitcnt lgkmcnt(6)\n\t"
        "v_mfma_f32_16x16x32_bf16 v[8:11], a[228:231], v[80:83], v[8:11]\n\t"
        "v_mfma_f32_16x16x32_bf16 v[12:15], a[232:235], v[80:83], v[12:15]\n\t"
        "ds_read_b128 v[76:79], v107 offset:26624\n\t"
        "v_mfma_f32_16x16x32_bf16 v[16:19], v[80:83], a[236:239], v[16:19]\n\t"
        "s_add_u32 m0, %[ld], 57344\n\t"
        "s_add_u32 %[t0], %[xo], 896\n\t"
        "s_waitcnt lgkmcnt(6)\n\t"
        "v_mfma_f32_16x16x32_bf16 v[20:23], a[228:231], v[84:87], v[20:23]\n\t"
        "buffer_load_dwordx4 v100, %[sx], %[t0] offen lds\n\t"
        "v_mfma_f32_16x16x32_bf16 v[24:27], a[232:235], v[84:87], v[24:27]\n\t"
        "ds_read_b128 v[80:83], v108 offset:24576\n\t"
        "v_mfma_f32_16x16x32_bf16 v[28:31], v[84:87], a[236:239], v[28:31]\n\t"
        "s_waitcnt lgkmcnt(6)\n\t"
        "v_mfma_f32_16x16x32_bf16 v[8:11], a[240:243], v[56:59], v[8:11]\n\t"
        "v_mfma_f32_16x16x32_bf16 v[12:15], a[244:247], v[56:59], v[12:15]\n\t"
        "ds_read_b128 v[84:87], v108 offset:26624\n\t"
        "v_mfma_f32_16x16x32_bf16 v[16:19], v[56:59], a[248:251], v[16:19]\n\t"
        "s_add_u32 m0, %[ld], 58368\n\t"
        "s_add_u32 %[t0], %[xo], 17280\n\t"
        "s_waitcnt lgkmcnt(6)\n\t"
        "v_mfma_f32_16x16x32_bf16 v[20:23], a[240:243], v[60:63], v[20:23]\n\t"
        "buffer_load_dwordx4 v100, %[sx], %[t0] offen lds\n\t"
        "v_mfma_f32_16x16x32_bf16 v[24:27], a[244:247], v[60:63], v[24:27]\n\t"
        "v_mfma_f32_16x16x32_bf16 v[28:31], v[60:63], a[248:251], v[28:31]\n\t"
        "s_waitcnt lgkmcnt(5)\n\t"
        "v_mfma_f32_16x16x32_bf16 v[8:11], a[252:255], v[64:67], v[8:11]\n\t"
        "s_waitcnt vmcnt(17) lgkmcnt(0)\n\t"
        "s_barrier\n\t"
        "v_mfma_f32_16x16x32_bf16 v[12:15], v[128:131], v[64:67], v[12:15]\n\t"
        "ds_read_b128 v[56:59], v107 offset:32768\n\t"
        "v_mfma_f32_16x16x32_bf16 v[16:19], v[64:67], v[132:135], v[16:19]\n\t"
        "ds_read_b128 v[60:63], v107 offset:34816\n\t"
        "s_add_u32 m0, %[ld], 65536\n\t"
        "s_add_u32 %[t0], %[xo], 1024\n\t"
        "v_mfma_f32_16x16x32_bf16 v[20:23], a[252:255], v[68:71], v[20:23]\n\t"
        "buffer_load_dwordx4 v100, %[sx], %[t0] offen lds\n\t"
        "v_mfma_f32_16x16x32_bf16 v[24:27], v[128:131], v[68:71], v[24:27]\n\t"
        "ds_read_b128 v[64:67], v108 offset:32768\n\t"
        "v_mfma_f32_16x16x32_bf16 v[28:31], v[68:71], v[132:135], v[28:31]\n\t"
        "v_mfma_f32_16x16x32_bf16 v[8:11], v[136:139], v[72:75], v[8:11]\n\t"
        "v_mfma_f32_16x16x32_bf16 v[12:15], v[140:143], v[72:75], v[12:15]\n\t"
        "ds_read_b128 v[68:71], v108 offset:34816\n\t"
        "v_mfma_f32_16x16x32_bf16 v[16:19], v[72:75], v[144:147], v[16:19]\n\t"
        "s_add_u32 m0, %[ld], 66560\n\t"
        "s_add_u32 %[t0], %[xo], 17408\n\t"
        "v_mfma_f32_16x16x32_bf16 v[20:23], v[136:139], v[76:79], v[20:23]\n\t"
        "buffer_load_dwordx4 v100, %[sx], %[t0] offen lds\n\t"
        "v_mfma_f32_16x16x32_bf16 v[24:27], v[140:143], v[76:79], v[24:27]\n\t"
        "ds_read_b128 v[72:75], v107 offset:40960\n\t"
        "v_mfma_f32_16x16x32_bf16 v[28:31], v[76:79], v[144:147], v[28:31]\n\t"
        "v_mfma_f32_16x16x32_bf16 v[8:11], v[148:151], v[80:83], v[8:11]\n\t"
        "v_mfma_f32_16x16x32_bf16 v[12:15], v[152:155], v[80:83], v[12:15]\n\t"
        "ds_read_b128 v[76:79], v107 offset:43008\n\t"
        "v_mfma_f32_16x16x32_bf16 v[16:19], v[80:83], v[156:159], v[16:19]\n\t"
        "s_add_u32 m0, %[ld], 73728\n\t"
        "s_add_u32 %[t0], %[xo], 1152\n\t"
        "v_mfma_f32_16x16x32_bf16 v[20:23], v[148:151], v[84:87], v[20:23]\n\t"
        "buffer_load_dwordx4 v100, %[sx], %[t0] offen lds\n\t"
        "v_mfma_f32_16x16x32_bf16 v[24:27], v[152:155], v[84:87], v[24:27]\n\t"
        "ds_read_b128 v[80:83], v108 offset:40960\n\t"
        "v_mfma_f32_16x16x32_bf16 v[28:31], v[84:87], v[156:159], v[28:31]\n\t"
        "s_waitcnt lgkmcnt(6)\n\t"
        "v_mfma_f32_16x16x32_bf16 v[8:11], v[160:163], v[56:59], v[8:11]\n\t"
        "v_mfma_f32_16x16x32_bf16 v[12:15], v[164:167], v[56:59], v[12:15]\n\t"
        "ds_read_b128 v[84:87], v108 offset:43008\n\t"
        "v_mfma_f32_16x16x32_bf16 v[16:19], v[56:59], v[168:171], v[16:19]\n\t"
        "s_add_u32 m0, %[ld], 74752\n\t"
        "s_add_u32 %[t0], %[xo], 17536\n\t"
        "s_waitcnt lgkmcnt(6)\n\t"
        "v_mfma_f32_16x16x32_bf16 v[20:23], v[160:163], v[60:63], v[20:23]\n\t"
        "buffer_load_dwordx4 v100, %[sx], %[t0] offen lds\n\t"
        "v_mfma_f32_16x16x32_bf16 v[24:27], v[164:167], v[60:63], v[24:27]\n\t"
        "ds_read_b128 v[56:59], v107 offset:49152\n\t"
        "v_mfma_f32_16x16x32_bf16 v[28:31], v[60:63], v[168:171], v[28:31]\n\t"
        "s_waitcnt lgkmcnt(6)\n\t"
        "v_mfma_f32_16x16x32_bf16 v[8:11], v[172:175], v[64:67], v[8:11]\n\t"
        "v_mfma_f32_16x16x32_bf16 v[12:15], v[176:179], v[64:67], v[12:15]\n\t"
        "ds_read_b128 v[60:63], v107 offset:51200\n\t"
        "v_mfma_f32_16x16x32_bf16 v[16:19], v[64:67], v[180:183], v[16:19]\n\t"
        "s_add_u32 m0, %[ld], 81920\n\t"
        "s_add_u32 %[t0], %[xo], 1280\n\t"
        "s_waitcnt lgkmcnt(6)\n\t"
        "v_mfma_f32_16x16x32_bf16 v[20:23], v[172:175], v[68:71], v[20:23]\n\t"
        "buffer_load_dwordx4 v100, %[sx], %[t0] offen lds\n\t"
        "v_mfma_f32_16x16x32_bf16 v[24:27], v[176:179], v[68:71], v[24:27]\n\t"
        "ds_read_b128 v[64:67], v108 offset:49152\n\t"
        "v_mfma_f32_16x16x32_bf16 v[28:31], v[68:71], v[180:183], v[28:31]\n\t"
        "s_waitcnt lgkmcnt(6)\n\t"
        "v_mfma_f32_16x16x32_bf16 v[8:11], v[184:187], v[72:75], v[8:11]\n\t"
        "v_mfma_f32_16x16x32_bf16 v[12:15], v[188:191], v[72:75], v[12:15]\n\t"
        "ds_read_b128 v[68:71], v108 offset:51200\n\t"
        "v_mfma_f32_16x16x32_bf16 v[16:19], v[72:75], v[192:195], v[16:19]\n\t"
        "s_add_u32 m0, %[ld], 82944\n\t"
        "s_add_u32 %[t0], %[xo], 17664\n\t"
        "s_waitcnt lgkmcnt(6)\n\t"
        "v_mfma_f32_16x16x32_bf16 v[20:23], v[184:187], v[76:79], v[20:23]\n\t"
        "buffer_load_dwordx4 v100, %[sx], %[t0] offen lds\n\t"
        "v_mfma_f32_16x16x32_bf16 v[24:27], v[188:191], v[76:79], v[24:27]\n\t"
        "ds_read_b128 v[72:75], v107 offset:57344\n\t"
        "v_mfma_f32_16x16x32_bf16 v[28:31], v[76:79], v[192:195], v[28:31]\n\t"
        "s_waitcnt lgkmcnt(6)\n\t"
        "v_mfma_f32_16x16x32_bf16 v[8:11], v[196:199], v[80:83], v[8:11]\n\t"
        "v_mfma_f32_16x16x32_bf16 v[12:15], v[200:203], v[80:83], v[12:15]\n\t"
        "ds_read_b128 v[76:79], v107 offset:59392\n\t"
        "v_mfma_f32_16x16x32_bf16 v[16:19], v[80:83], v[204:207], v[16:19]\n\t"
        "s_add_u32 m0, %[ld], 90112\n\t"
        "s_add_u32 %[t0], %[xo], 1408\n\t"
        "s_waitcnt lgkmcnt(6)\n\t"
        "v_mfma_f32_16x16x32_bf16 v[20:23], v[196:199], v[84:87], v[20:23]\n\t"
        "buffer_load_dwordx4 v100, %[sx], %[t0] offen lds\n\t"
        "v_mfma_f32_16x16x32_bf16 v[24:27], v[200:203], v[84:87], v[24:27]\n\t"
        "ds_read_b128 v[80:83], v108 offset:57344\n\t"
        "v_mfma_f32_16x16x32_bf16 v[28:31], v[84:87], v[204:207], v[28:31]\n\t"
        "s_waitcnt lgkmcnt(6)\n\t"
        "v_mfma_f32_16x16x32_bf16 v[8:11], v[208:211], v[56:59], v[8:11]\n\t"
        "v_mfma_f32_16x16x32_bf16 v[12:15], v[212:215], v[56:59], v[12:15]\n\t"
        "ds_read_b128 v[84:87], v108 offset:59392\n\t"
        "v_mfma_f32_16x16x32_bf16 v[16:19], v[56:59], v[216:219], v[16:19]\n\t"
        "s_add_u32 m0, %[ld], 91136\n\t"
        "s_add_u32 %[t0], %[xo], 17792\n\t"
        "s_waitcnt lgkmcnt(6)\n\t"
        "v_mfma_f32_16x16x32_bf16 v[20:23], v[208:211], v[60:63], v[20:23]\n\t"
        "buffer_load_dwordx4 v100, %[sx], %[t0] offen lds\n\t"
        "v_mfma_f32_16x16x32_bf16 v[24:27], v[212:215], v[60:63], v[24:27]\n\t"
        "v_mfma_f32_16x16x32_bf16 v[28:31], v[60:63], v[216:219], v[28:31]\n\t"
        "s_waitcnt lgkmcnt(5)\n\t"
        "v_mfma_f32_16x16x32_bf16 v[8:11], v[220:223], v[64:67], v[8:11]\n\t"
        "s_waitcnt vmcnt(17) lgkmcnt(0)\n\t"
        "s_barrier\n\t"
        "v_mfma_f32_16x16x32_bf16 v[12:15], v[224:227], v[64:67], v[12:15]\n\t"
        "ds_read_b128 v[56:59], v105 offset:0\n\t"
        "v_mfma_f32_16x16x32_bf16 v[16:19], v[64:67], v[228:231], v[16:19]\n\t"
        "ds_read_b128 v[60:63], v105 offset:2048\n\t"
        "s_add_u32 m0, %[ld], 98304\n\t"
        "s_add_u32 %[t0], %[xo], 1536\n\t"
        "v_mfma_f32_16x16x32_bf16 v[20:23], v[220:223], v[68:71], v[20:23]\n\t"
        "buffer_load_dwordx4 v100, %[sx], %[t0] offen lds\n\t"
        "v_mfma_f32_16x16x32_bf16 v[24:27], v[224:227], v[68:71], v[24:27]\n\t"
        "ds_read_b128 v[64:67], v106 offset:0\n\t"
        "v_mfma_f32_16x16x32_bf16 v[28:31], v[68:71], v[228:231], v[28:31]\n\t"
        "v_mfma_f32_16x16x32_bf16 v[8:11], v[232:235], v[72:75], v[8:11]\n\t"
        "v_mfma_f32_16x16x32_bf16 v[12:15], v[236:239], v[72:75], v[12:15]\n\t"
        "ds_read_b128 v[68:71], v106 offset:2048\n\t"
        "v_mfma_f32_16x16x32_bf16 v[16:19], v[72:75], v[240:243], v[16:19]\n\t"
        "s_add_u32 m0, %[ld], 99328\n\t"
        "s_add_u32 %[t0], %[xo], 17920\n\t"
        "v_mfma_f32_16x16x32_bf16 v[20:23], v[232:235], v[76:79], v[20:23]\n\t"
        "buffer_load_dwordx4 v100, %[sx], %[t0] offen lds\n\t"
        "v_mfma_f32_16x16x32_bf16 v[24:27], v[236:239], v[76:79], v[24:27]\n\t"
        "ds_read_b128 v[72:75], v105 offset:8192\n\t"
        "v_mfma_f32_16x16x32_bf16 v[28:31], v[76:79], v[240:243], v[28:31]\n\t"
        "v_mfma_f32_16x16x32_bf16 v[8:11], v[244:247], v[80:83], v[8:11]\n\t"
        "v_mfma_f32_16x16x32_bf16 v[12:15], v[248:251], v[80:83], v[12:15]\n\t"
        "ds_read_b128 v[76:79], v105 offset:10240\n\t"
        "v_mfma_f32_16x16x32_bf16 v[16:19], v[80:83], v[252:255], v[16:19]\n\t"
        "s_add_u32 m0, %[ld], 106496\n\t"
        "s_add_u32 %[t0], %[xo], 1664\n\t"
        "v_mfma_f32_16x16x32_bf16 v[20:23], v[244:247], v[84:87], v[20:23]\n\t"
        "buffer_load_dwordx4 v100, %[sx], %[t0] offen lds\n\t"
        "v_mfma_f32_16x16x32_bf16 v[24:27], v[248:251], v[84:87], v[24:27]\n\t"
        "ds_read_b128 v[80:83], v106 offset:8192\n\t"
        "v_mfma_f32_16x16x32_bf16 v[28:31], v[84:87], v[252:255], v[28:31]\n\t"
        "s_waitcnt lgkmcnt(6)\n\t"
        "v_mfma_f32_16x16x32_bf16 v[32:35], a[0:3], v[56:59], 0\n\t"
        "v_mfma_f32_16x16x32_bf16 v[36:39], a[4:7], v[56:59], 0\n\t"
        "ds_read_b128 v[84:87], v106 offset:10240\n\t"
        "v_mfma_f32_16x16x32_bf16 v[40:43], v[56:59], a[8:11], 0\n\t"
        "s_add_u32 m0, %[ld], 107520\n\t"
        "s_add_u32 %[t0], %[xo], 18048\n\t"
        "s_waitcnt lgkmcnt(6)\n\t"
        "v_mfma_f32_16x16x32_bf16 v[44:47], a[0:3], v[60:63], 0\n\t"
        "buffer_load_dwordx4 v100, %[sx], %[t0] offen lds\n\t"
        "v_mfma_f32_16x16x32_bf16 v[48:51], a[4:7], v[60:63], 0\n\t"
        "ds_read_b128 v[56:59], v105 offset:16384\n\t"
        "v_mfma_f32_16x16x32_bf16 v[52:55], v[60:63], a[8:11], 0\n\t"
        "s_waitcnt lgkmcnt(6)\n\t"
        "v_mfma_f32_16x16x32_bf16 v[32:35], a[12:15], v[64:67], v[32:35]\n\t"
        "v_cvt_pk_bf16_f32 v88, v8, v9\n\t"
        "v_cvt_pk_bf16_f32 v89, v10, v11\n\t"
        "v_mfma_f32_16x16x32_bf16 v[36:39], a[16:19], v[64:67], v[36:39]\n\t"
        "ds_read_b128 v[60:63], v105 offset:18432\n\t"
        "v_cvt_pk_bf16_f32 v90, v12, v13\n\t"
        "v_mfma_f32_16x16x32_bf16 v[40:43], v[64:67], a[20:23], v[40:43]\n\t"
        "s_add_u32 m0, %[ld], 114688\n\t"
        "s_add_u32 %[t0], %[xo], 1792\n\t"
        "s_waitcnt lgkmcnt(6)\n\t"
        "v_mfma_f32_16x16x32_bf16 v[44:47], a[12:15], v[68:71], v[44:47]\n\t"
        "buffer_load_dwordx4 v100, %[sx], %[t0] offen lds\n\t"
        "v_mfma_f32_16x16x32_bf16 v[48:51], a[16:19], v[68:71], v[48:51]\n\t"
        "ds_read_b128 v[64:67], v106 offset:16384\n\t"
        "v_cvt_pk_bf16_f32 v91, v14, v15\n\t"
        "v_mfma_f32_16x16x32_bf16 v[52:55], v[68:71], a[20:23], v[52:55]\n\t"
        "buffer_store_dwordx4 v[88:91], v109, %[sw], %[so] offen sc1\n\t"
        "s_waitcnt lgkmcnt(6)\n\t"
        "v_mfma_f32_16x16x32_bf16 v[32:35], a[24:27], v[72:75], v[32:35]\n\t"
        "v_cvt_pk_bf16_f32 v92, v20, v21\n\t"
        "v_cvt_pk_bf16_f32 v93, v22, v23\n\t"
        "v_mfma_f32_16x16x32_bf16 v[36:39], a[28:31], v[72:75], v[36:39]\n\t"
        "ds_read_b128 v[68:71], v106 offset:18432\n\t"
        "v_cvt_pk_bf16_f32 v94, v24, v25\n\t"
        "v_mfma_f32_16x16x32_bf16 v[40:43], v[72:75], a[32:35], v[40:43]\n\t"
        "s_add_u32 m0, %[ld], 115712\n\t"
        "s_add_u32 %[t0], %[xo], 18176\n\t"
        "s_waitcnt lgkmcnt(6)\n\t"
        "v_mfma_f32_16x16x32_bf16 v[44:47], a[24:27], v[76:79], v[44:47]\n\t"
        "buffer_load_dwordx4 v100, %[sx], %[t0] offen lds\n\t"
        "v_mfma_f32_16x16x32_bf16 v[48:51], a[28:31], v[76:79], v[48:51]\n\t"
        "ds_read_b128 v[72:75], v105 offset:24576\n\t"
        "v_cvt_pk_bf16_f32 v95, v26, v27\n\t"
        "v_mfma_f32_16x16x32_bf16 v[52:55], v[76:79], a[32:35], v[52:55]\n\t"
        "buffer_store_dwordx4 v[92:95], v110, %[sw], %[so] offen sc1\n\t"
        "s_waitcnt lgkmcnt(6)\n\t"
        "v_mfma_f32_16x16x32_bf16 v[32:35], a[36:39], v[80:83], v[32:35]\n\t"
        "v_cvt_pk_bf16_f32 v96, v16, v17\n\t"
        "v_cvt_pk_bf16_f32 v97, v18, v19\n\t"
        "v_mfma_f32_16x16x32_bf16 v[36:39], a[40:43], v[80:83], v[36:39]\n\t"
        "ds_read_b128 v[76:79], v105 offset:26624\n\t"
        "v_cvt_pk_bf16_f32 v98, v28, v29\n\t"
        "v_mfma_f32_16x16x32_bf16 v[40:43], v[80:83], a[44:47], v[40:43]\n\t"
        "s_add_u32 m0, %[ld], 122880\n\t"
        "s_add_u32 %[t0], %[xo], 1920\n\t"
        "s_waitcnt lgkmcnt(6)\n\t"
        "v_mfma_f32_16x16x32_bf16 v[44:47], a[36:39], v[84:87], v[44:47]\n\t"
        "buffer_load_dwordx4 v100, %[sx], %[t0] offen lds\n\t"
        "v_mfma_f32_16x16x32_bf16 v[48:51], a[40:43], v[84:87], v[48:51]\n\t"
        "ds_read_b128 v[80:83], v106 offset:24576\n\t"
        "v_cvt_pk_bf16_f32 v99, v30, v31\n\t"
        "v_mfma_f32_16x16x32_bf16 v[52:55], v[84:87], a[44:47], v[52:55]\n\t"
        "buffer_store_dwordx4 v[96:99], v111, %[sw], %[so] offen sc1\n\t"
        "s_waitcnt lgkmcnt(6)\n\t"
        "v_mfma_f32_16x16x32_bf16 v[32:35], a[48:51], v[56:59], v[32:35]\n\t"
        "s_lshl_b32 %[so], %[cn], 19\n\t"
        "v_mfma_f32_16x16x32_bf16 v[36:39], a[52:55], v[56:59], v[36:39]\n\t"
        "ds_read_b128 v[84:87], v106 offset:26624\n\t"
        "v_mfma_f32_16x16x32_bf16 v[40:43], v[56:59], a[56:59], v[40:43]\n\t"
        "s_add_u32 m0, %[ld], 123904\n\t"
        "s_add_u32 %[t0], %[xo], 18304\n\t"
        "s_waitcnt lgkmcnt(6)\n\t"
        "v_mfma_f32_16x16x32_bf16 v[44:47], a[48:51], v[60:63], v[44:47]\n\t"
        "buffer_load_dwordx4 v100, %[sx], %[t0] offen lds\n\t"
        "v_mfma_f32_16x16x32_bf16 v[48:51], a[52:55], v[60:63], v[48:51]\n\t"
        "v_mfma_f32_16x16x32_bf16 v[52:55], v[60:63], a[56:59], v[52:55]\n\t"
        "s_waitcnt lgkmcnt(5)\n\t"
        "v_mfma_f32_16x16x32_bf16 v[32:35], a[60:63], v[64:67], v[32:35]\n\t"
        "s_waitcnt vmcnt(19) lgkmcnt(0)\n\t"
        "s_barrier\n\t"
        "s_add_u32 %[xo], %[xo], 0x20000\n\t"
        "v_mfma_f32_16x16x32_bf16 v[36:39], a[64:67], v[64:67], v[36:39]\n\t"
        "ds_read_b128 v[56:59], v105 offset:32768\n\t"
        "v_mfma_f32_16x16x32_bf16 v[40:43], v[64:67], a[68:71], v[40:43]\n\t"
        "ds_read_b128 v[60:63], v105 offset:34816\n\t"
        "s_add_u32 m0, %[ld], 0\n\t"
        "s_add_u32 %[t0], %[xo], 0\n\t"
        "v_mfma_f32_16x16x32_bf16 v[44:47], a[60:63], v[68:71], v[44:47]\n\t"
        "buffer_load_dwordx4 v100, %[sx], %[t0] offen lds\n\t"
        "v_mfma_f32_16x16x32_bf16 v[48:51], a[64:67], v[68:71], v[48:51]\n\t"
        "ds_read_b128 v[64:67], v106 offset:32768\n\t"
        "v_mfma_f32_16x16x32_bf16 v[52:55], v[68:71], a[68:71], v[52:55]\n\t"
        "v_mfma_f32_16x16x32_bf16 v[32:35], a[72:75], v[72:75], v[32:35]\n\t"
        "v_mfma_f32_16x16x32_bf16 v[36:39], a[76:79], v[72:75], v[36:39]\n\t"
        "ds_read_b128 v[68:71], v106 offset:34816\n\t"
        "v_mfma_f32_16x16x32_bf16 v[40:43], v[72:75], a[80:83], v[40:43]\n\t"
        "s_add_u32 m0, %[ld], 1024\n\t"
        "s_add_u32 %[t0], %[xo], 16384\n\t"
        "v_mfma_f32_16x16x32_bf16 v[44:47], a[72:75], v[76:79], v[44:47]\n\t"
        "buffer_load_dwordx4 v100, %[sx], %[t0] offen lds\n\t"
        "v_mfma_f32_16x16x32_bf16 v[48:51], a[76:79], v[76:79], v[48:51]\n\t"
        "ds_read_b128 v[72:75], v105 offset:40960\n\t"
        "v_mfma_f32_16x16x32_bf16 v[52:55], v[76:79], a[80:83], v[52:55]\n\t"
        "v_mfma_f32_16x16x32_bf16 v[32:35], a[84:87], v[80:83], v[32:35]\n\t"
        "v_mfma_f32_16x16x32_bf16 v[36:39], a[88:91], v[80:83], v[36:39]\n\t"
        "ds_read_b128 v[76:79], v105 offset:43008\n\t"
        "v_mfma_f32_16x16x32_bf16 v[40:43], v[80:83], a[92:95], v[40:43]\n\t"
        "s_add_u32 m0, %[ld], 8192\n\t"
        "s_add_u32 %[t0], %[xo], 128\n\t"
        "v_mfma_f32_16x16x32_bf16 v[44:47], a[84:87], v[84:87], v[44:47]\n\t"
        "buffer_load_dwordx4 v100, %[sx], %[t0] offen lds\n\t"
        "v_mfma_f32_16x16x32_bf16 v[48:51], a[88:91], v[84:87], v[48:51]\n\t"
        "ds_read_b128 v[80:83], v106 offset:40960\n\t"
        "v_mfma_f32_16x16x32_bf16 v[52:55], v[84:87], a[92:95], v[52:55]\n\t"
        "s_waitcnt lgkmcnt(6)\n\t"
        "v_mfma_f32_16x16x32_bf16 v[32:35], a[96:99], v[56:59], v[32:35]\n\t"
        "v_mfma_f32_16x16x32_bf16 v[36:39], a[100:103], v[56:59], v[36:39]\n\t"
        "ds_read_b128 v[84:87], v106 offset:43008\n\t"
        "v_mfma_f32_16x16x32_bf16 v[40:43], v[56:59], a[104:107], v[40:43]\n\t"
        "s_add_u32 m0, %[ld], 9216\n\t"
        "s_add_u32 %[t0], %[xo], 16512\n\t"
        "s_waitcnt lgkmcnt(6)\n\t"
        "v_mfma_f32_16x16x32_bf16 v[44:47], a[96:99], v[60:63], v[44:47]\n\t"
        "buffer_load_dwordx4 v100, %[sx], %[t0] offen lds\n\t"
        "v_mfma_f32_16x16x32_bf16 v[48:51], a[100:103], v[60:63], v[48:51]\n\t"
        "ds_read_b128 v[56:59], v105 offset:49152\n\t"
        "v_mfma_f32_16x16x32_bf16 v[52:55], v[60:63], a[104:107], v[52:55]\n\t"
        "s_waitcnt lgkmcnt(6)\n\t"
        "v_mfma_f32_16x16x32_bf16 v[32:35], a[108:111], v[64:67], v[32:35]\n\t"
        "v_mfma_f32_16x16x32_bf16 v[36:39], a[112:115], v[64:67], v[36:39]\n\t"
        "ds_read_b128 v[60:63], v105 offset:51200\n\t"
        "v_mfma_f32_16x16x32_bf16 v[40:43], v[64:67], a[116:119], v[40:43]\n\t"
        "s_add_u32 m0, %[ld], 16384\n\t"
        "s_add_u32 %[t0], %[xo], 256\n\t"
        "s_waitcnt lgkmcnt(6)\n\t"
        "v_mfma_f32_16x16x32_bf16 v[44:47], a[108:111], v[68:71], v[44:47]\n\t"
        "buffer_load_dwordx4 v100, %[sx], %[t0] offen lds\n\t"
        "v_mfma_f32_16x16x32_bf16 v[48:51], a[112:115], v[68:71], v[48:51]\n\t"
        "ds_read_b128 v[64:67], v106 offset:49152\n\t"
        "v_mfma_f32_16x16x32_bf16 v[52:55], v[68:71], a[116:119], v[52:55]\n\t"
        "s_waitcnt lgkmcnt(6)\n\t"
        "v_mfma_f32_16x16x32_bf16 v[32:35], a[120:123], v[72:75], v[32:35]\n\t"
        "v_mfma_f32_16x16x32_bf16 v[36:39], a[124:127], v[72:75], v[36:39]\n\t"
        "ds_read_b128 v[68:71], v106 offset:51200\n\t"
        "v_mfma_f32_16x16x32_bf16 v[40:43], v[72:75], a[128:131], v[40:43]\n\t"
        "s_add_u32 m0, %[ld], 17408\n\t"
        "s_add_u32 %[t0], %[xo], 16640\n\t"
        "s_waitcnt lgkmcnt(6)\n\t"
        "v_mfma_f32_16x16x32_bf16 v[44:47], a[120:123], v[76:79], v[44:47]\n\t"
        "buffer_load_dwordx4 v100, %[sx], %[t0] offen lds\n\t"
        "v_mfma_f32_16x16x32_bf16 v[48:51], a[124:127], v[76:79], v[48:51]\n\t"
        "ds_read_b128 v[72:75], v105 offset:57344\n\t"
        "v_mfma_f32_16x16x32_bf16 v[52:55], v[76:79], a[128:131], v[52:55]\n\t"
        "s_waitcnt lgkmcnt(6)\n\t"
        "v_mfma_f32_16x16x32_bf16 v[32:35], a[132:135], v[80:83], v[32:35]\n\t"
        "v_mfma_f32_16x16x32_bf16 v[36:39], a[136:139], v[80:83], v[36:39]\n\t"
        "ds_read_b128 v[76:79], v105 offset:59392\n\t"
        "v_mfma_f32_16x16x32_bf16 v[40:43], v[80:83], a[140:143], v[40:43]\n\t"
        "s_add_u32 m0, %[ld], 24576\n\t"
        "s_add_u32 %[t0], %[xo], 384\n\t"
        "s_waitcnt lgkmcnt(6)\n\t"
        "v_mfma_f32_16x16x32_bf16 v[44:47], a[132:135], v[84:87], v[44:47]\n\t"
        "buffer_load_dwordx4 v100, %[sx], %[t0] offen lds\n\t"
        "v_mfma_f32_16x16x32_bf16 v[48:51], a[136:139], v[84:87], v[48:51]\n\t"
        "ds_read_b128 v[80:83], v106 offset:57344\n\t"
        "v_mfma_f32_16x16x32_bf16 v[52:55], v[84:87], a[140:143], v[52:55]\n\t"
        "s_waitcnt lgkmcnt(6)\n\t"
        "v_mfma_f32_16x16x32_bf16 v[32:35], a[144:147], v[56:59], v[32:35]\n\t"
        "v_mfma_f32_16x16x32_bf16 v[36:39], a[148:151], v[56:59], v[36:39]\n\t"
        "ds_read_b128 v[84:87], v106 offset:59392\n\t"
        "v_mfma_f32_16x16x32_bf16 v[40:43], v[56:59], a[152:155], v[40:43]\n\t"
        "s_add_u32 m0, %[ld], 25600\n\t"
        "s_add_u32 %[t0], %[xo], 16768\n\t"
        "s_waitcnt lgkmcnt(6)\n\t"
        "v_mfma_f32_16x16x32_bf16 v[44:47], a[144:147], v[60:63], v[44:47]\n\t"
        "buffer_load_dwordx4 v100, %[sx], %[t0] offen lds\n\t"
        "v_mfma_f32_16x16x32_bf16 v[48:51], a[148:151], v[60:63], v[48:51]\n\t"
        "v_mfma_f32_16x16x32_bf16 v[52:55], v[60:63], a[152:155], v[52:55]\n\t"
        "s_waitcnt lgkmcnt(5)\n\t"
        "v_mfma_f32_16x16x32_bf16 v[32:35], a[156:159], v[64:67], v[32:35]\n\t"
        "s_waitcnt vmcnt(9) lgkmcnt(0)\n\t"
        "s_barrier\n\t"
        "v_mfma_f32_16x16x32_bf16 v[36:39], a[160:163], v[64:67], v[36:39]\n\t"
        "ds_read_b128 v[56:59], v107 offset:0\n\t"
        "v_mfma_f32_16x16x32_bf16 v[40:43], v[64:67], a[164:167], v[40:43]\n\t"
        "ds_read_b128 v[60:63], v107 offset:2048\n\t"
        "s_add_u32 m0, %[ld], 32768\n\t"
        "s_add_u32 %[t0], %[xo], 512\n\t"
        "v_mfma_f32_16x16x32_bf16 v[44:47], a[156:159], v[68:71], v[44:47]\n\t"
        "buffer_load_dwordx4 v100, %[sx], %[t0] offen lds\n\t"
        "v_mfma_f32_16x16x32_bf16 v[48:51], a[160:163], v[68:71], v[48:51]\n\t"
        "ds_read_b128 v[64:67], v108 offset:0\n\t"
        "v_mfma_f32_16x16x32_bf16 v[52:55], v[68:71], a[164:167], v[52:55]\n\t"
        "v_mfma_f32_16x16x32_bf16 v[32:35], a[168:171], v[72:75], v[32:35]\n\t"
        "v_mfma_f32_16x16x32_bf16 v[36:39], a[172:175], v[72:75], v[36:39]\n\t"
        "ds_read_b128 v[68:71], v108 offset:2048\n\t"
        "v_mov_b32_e32 v114, %[cn]\n\t"
        "buffer_store_dword v114, v113, %[sw], 0 offen sc1\n\t"
        "s_add_u32 %[cn], %[cn], 1\n\t"
        "v_mfma_f32_16x16x32_bf16 v[40:43], v[72:75], a[176:179], v[40:43]\n\t"
        "s_add_u32 m0, %[ld], 33792\n\t"
        "s_add_u32 %[t0], %[xo], 16896\n\t"
        "v_mfma_f32_16x16x32_bf16 v[44:47], a[168:171], v[76:79], v[44:47]\n\t"
        "buffer_load_dwordx4 v100, %[sx], %[t0] offen lds\n\t"
        "v_mfma_f32_16x16x32_bf16 v[48:51], a[172:175], v[76:79], v[48:51]\n\t"
        "ds_read_b128 v[72:75], v107 offset:8192\n\t"
        "v_mfma_f32_16x16x32_bf16 v[52:55], v[76:79], a[176:179], v[52:55]\n\t"
        "v_mfma_f32_16x16x32_bf16 v[32:35], a[180:183], v[80:83], v[32:35]\n\t"
        "v_mfma_f32_16x16x32_bf16 v[36:39], a[184:187], v[80:83], v[36:39]\n\t"
        "ds_read_b128 v[76:79], v107 offset:10240\n\t"
        "v_mfma_f32_16x16x32_bf16 v[40:43], v[80:83], a[188:191], v[40:43]\n\t"
        "s_add_u32 m0, %[ld], 40960\n\t"
        "s_add_u32 %[t0], %[xo], 640\n\t"
        "v_mfma_f32_16x16x32_bf16 v[44:47], a[180:183], v[84:87], v[44:47]\n\t"
        "buffer_load_dwordx4 v100, %[sx], %[t0] offen lds\n\t"
        "v_mfma_f32_16x16x32_bf16 v[48:51], a[184:187], v[84:87], v[48:51]\n\t"
        "ds_read_b128 v[80:83], v108 offset:8192\n\t"
        "v_mfma_f32_16x16x32_bf16 v[52:55], v[84:87], a[188:191], v[52:55]\n\t"
        "s_waitcnt lgkmcnt(6)\n\t"
        "v_mfma_f32_16x16x32_bf16 v[32:35], a[192:195], v[56:59], v[32:35]\n\t"
        "v_mfma_f32_16x16x32_bf16 v[36:39], a[196:199], v[56:59], v[36:39]\n\t"
        "ds_read_b128 v[84:87], v108 offset:10240\n\t"
        "v_mfma_f32_16x16x32_bf16 v[40:43], v[56:59], a[200:203], v[40:43]\n\t"
        "s_add_u32 m0, %[ld], 41984\n\t"
        "s_add_u32 %[t0], %[xo], 17024\n\t"
        "s_waitcnt lgkmcnt(6)\n\t"
        "v_mfma_f32_16x16x32_bf16 v[44:47], a[192:195], v[60:63], v[44:47]\n\t"
        "buffer_load_dwordx4 v100, %[sx], %[t0] offen lds\n\t"
        "v_mfma_f32_16x16x32_bf16 v[48:51], a[196:199], v[60:63], v[48:51]\n\t"
        "ds_read_b128 v[56:59], v107 offset:16384\n\t"
        "v_mfma_f32_16x16x32_bf16 v[52:55], v[60:63], a[200:203], v[52:55]\n\t"
        "s_waitcnt lgkmcnt(6)\n\t"
        "v_mfma_f32_16x16x32_bf16 v[32:35], a[204:207], v[64:67], v[32:35]\n\t"
        "v_mfma_f32_16x16x32_bf16 v[36:39], a[208:211], v[64:67], v[36:39]\n\t"
        "ds_read_b128 v[60:63], v107 offset:18432\n\t"
        "v_mfma_f32_16x16x32_bf16 v[40:43], v[64:67], a[212:215], v[40:43]\n\t"
        "s_add_u32 m0, %[ld], 49152\n\t"
        "s_add_u32 %[t0], %[xo], 768\n\t"
        "s_waitcnt lgkmcnt(6)\n\t"
        "v_mfma_f32_16x16x32_bf16 v[44:47], a[204:207], v[68:71], v[44:47]\n\t"
        "buffer_load_dwordx4 v100, %[sx], %[t0] offen lds\n\t"
        "v_mfma_f32_16x16x32_bf16 v[48:51], a[208:211], v[68:71], v[48:51]\n\t"
        "ds_read_b128 v[64:67], v108 offset:16384\n\t"
        "v_mfma_f32_16x16x32_bf16 v[52:55], v[68:71], a[212:215], v[52:55]\n\t"
        "s_waitcnt lgkmcnt(6)\n\t"
        "v_mfma_f32_16x16x32_bf16 v[32:35], a[216:219], v[72:75], v[32:35]\n\t"
        "v_mfma_f32_16x16x32_bf16 v[36:39], a[220:223], v[72:75], v[36:39]\n\t"
        "ds_read_b128 v[68:71], v108 offset:18432\n\t"
        "v_mfma_f32_16x16x32_bf16 v[40:43], v[72:75], a[224:227], v[40:43]\n\t"
        "s_add_u32 m0, %[ld], 50176\n\t"
        "s_add_u32 %[t0], %[xo], 17152\n\t"
        "s_waitcnt lgkmcnt(6)\n\t"
        "v_mfma_f32_16x16x32_bf16 v[44:47], a[216:219], v[76:79], v[44:47]\n\t"
        "buffer_load_dwordx4 v100, %[sx], %[t0] offen lds\n\t"
        "v_mfma_f32_16x16x32_bf16 v[48:51], a[220:223], v[76:79], v[48:51]\n\t"
        "ds_read_b128 v[72:75], v107 offset:24576\n\t"
        "v_mfma_f32_16x16x32_bf16 v[52:55], v[76:79], a[224:227], v[52:55]\n\t"
        "s_waitcnt lgkmcnt(6)\n\t"
        "v_mfma_f32_16x16x32_bf16 v[32:35], a[228:231], v[80:83], v[32:35]\n\t"
        "v_mfma_f32_16x16x32_bf16 v[36:39], a[232:235], v[80:83], v[36:39]\n\t"
        "ds_read_b128 v[76:79], v107 offset:26624\n\t"
        "v_mfma_f32_16x16x32_bf16 v[40:43], v[80:83], a[236:239], v[40:43]\n\t"
        "s_add_u32 m0, %[ld], 57344\n\t"
        "s_add_u32 %[t0], %[xo], 896\n\t"
        "s_waitcnt lgkmcnt(6)\n\t"
        "v_mfma_f32_16x16x32_bf16 v[44:47], a[228:231], v[84:87], v[44:47]\n\t"
        "buffer_load_dwordx4 v100, %[sx], %[t0] offen lds\n\t"
        "v_mfma_f32_16x16x32_bf16 v[48:51], a[232:235], v[84:87], v[48:51]\n\t"
        "ds_read_b128 v[80:83], v108 offset:24576\n\t"
        "v_mfma_f32_16x16x32_bf16 v[52:55], v[84:87], a[236:239], v[52:55]\n\t"
        "s_waitcnt lgkmcnt(6)\n\t"
        "v_mfma_f32_16x16x32_bf16 v[32:35], a[240:243], v[56:59], v[32:35]\n\t"
        "v_mfma_f32_16x16x32_bf16 v[36:39], a[244:247], v[56:59], v[36:39]\n\t"
        "ds_read_b128 v[84:87], v108 offset:26624\n\t"
        "v_mfma_f32_16x16x32_bf16 v[40:43], v[56:59], a[248:251], v[40:43]\n\t"
        "s_add_u32 m0, %[ld], 58368\n\t"
        "s_add_u32 %[t0], %[xo], 17280\n\t"
        "s_waitcnt lgkmcnt(6)\n\t"
        "v_mfma_f32_16x16x32_bf16 v[44:47], a[240:243], v[60:63], v[44:47]\n\t"
        "buffer_load_dwordx4 v100, %[sx], %[t0] offen lds\n\t"
        "v_mfma_f32_16x16x32_bf16 v[48:51], a[244:247], v[60:63], v[48:51]\n\t"
        "v_mfma_f32_16x16x32_bf16 v[52:55], v[60:63], a[248:251], v[52:55]\n\t"
        "s_waitcnt lgkmcnt(5)\n\t"
        "v_mfma_f32_16x16x32_bf16 v[32:35], a[252:255], v[64:67], v[32:35]\n\t"
        "s_waitcnt vmcnt(17) lgkmcnt(0)\n\t"
        "s_barrier\n\t"
        "v_mfma_f32_16x16x32_bf16 v[36:39], v[128:131], v[64:67], v[36:39]\n\t"
        "ds_read_b128 v[56:59], v107 offset:32768\n\t"
        "v_mfma_f32_16x16x32_bf16 v[40:43], v[64:67], v[132:135], v[40:43]\n\t"
        "ds_read_b128 v[60:63], v107 offset:34816\n\t"
        "s_add_u32 m0, %[ld], 65536\n\t"
        "s_add_u32 %[t0], %[xo], 1024\n\t"
        "v_mfma_f32_16x16x32_bf16 v[44:47], a[252:255], v[68:71], v[44:47]\n\t"
        "buffer_load_dwordx4 v100, %[sx], %[t0] offen lds\n\t"
        "v_mfma_f32_16x16x32_bf16 v[48:51], v[128:131], v[68:71], v[48:51]\n\t"
        "ds_read_b128 v[64:67], v108 offset:32768\n\t"
        "v_mfma_f32_16x16x32_bf16 v[52:55], v[68:71], v[132:135], v[52:55]\n\t"
        "v_mfma_f32_16x16x32_bf16 v[32:35], v[136:139], v[72:75], v[32:35]\n\t"
        "v_mfma_f32_16x16x32_bf16 v[36:39], v[140:143], v[72:75], v[36:39]\n\t"
        "ds_read_b128 v[68:71], v108 offset:34816\n\t"
        "v_mfma_f32_16x16x32_bf16 v[40:43], v[72:75], v[144:147], v[40:43]\n\t"
        "s_add_u32 m0, %[ld], 66560\n\t"
        "s_add_u32 %[t0], %[xo], 17408\n\t"
        "v_mfma_f32_16x16x32_bf16 v[44:47], v[136:139], v[76:79], v[44:47]\n\t"
        "buffer_load_dwordx4 v100, %[sx], %[t0] offen lds\n\t"
        "v_mfma_f32_16x16x32_bf16 v[48:51], v[140:143], v[76:79], v[48:51]\n\t"
        "ds_read_b128 v[72:75], v107 offset:40960\n\t"
        "v_mfma_f32_16x16x32_bf16 v[52:55], v[76:79], v[144:147], v[52:55]\n\t"
        "v_mfma_f32_16x16x32_bf16 v[32:35], v[148:151], v[80:83], v[32:35]\n\t"
        "v_mfma_f32_16x16x32_bf16 v[36:39], v[152:155], v[80:83], v[36:39]\n\t"
        "ds_read_b128 v[76:79], v107 offset:43008\n\t"
        "v_mfma_f32_16x16x32_bf16 v[40:43], v[80:83], v[156:159], v[40:43]\n\t"
        "s_add_u32 m0, %[ld], 73728\n\t"
        "s_add_u32 %[t0], %[xo], 1152\n\t"
        "v_mfma_f32_16x16x32_bf16 v[44:47], v[148:151], v[84:87], v[44:47]\n\t"
        "buffer_load_dwordx4 v100, %[sx], %[t0] offen lds\n\t"
        "v_mfma_f32_16x16x32_bf16 v[48:51], v[152:155], v[84:87], v[48:51]\n\t"
        "ds_read_b128 v[80:83], v108 offset:40960\n\t"
        "v_mfma_f32_16x16x32_bf16 v[52:55], v[84:87], v[156:159], v[52:55]\n\t"
        "s_waitcnt lgkmcnt(6)\n\t"
        "v_mfma_f32_16x16x32_bf16 v[32:35], v[160:163], v[56:59], v[32:35]\n\t"
        "v_mfma_f32_16x16x32_bf16 v[36:39], v[164:167], v[56:59], v[36:39]\n\t"
        "ds_read_b128 v[84:87], v108 offset:43008\n\t"
        "v_mfma_f32_16x16x32_bf16 v[40:43], v[56:59], v[168:171], v[40:43]\n\t"
        "s_add_u32 m0, %[ld], 74752\n\t"
        "s_add_u32 %[t0], %[xo], 17536\n\t"
        "s_waitcnt lgkmcnt(6)\n\t"
        "v_mfma_f32_16x16x32_bf16 v[44:47], v[160:163], v[60:63], v[44:47]\n\t"
        "buffer_load_dwordx4 v100, %[sx], %[t0] offen lds\n\t"
        "v_mfma_f32_16x16x32_bf16 v[48:51], v[164:167], v[60:63], v[48:51]\n\t"
        "ds_read_b128 v[56:59], v107 offset:49152\n\t"
        "v_mfma_f32_16x16x32_bf16 v[52:55], v[60:63], v[168:171], v[52:55]\n\t"
        "s_waitcnt lgkmcnt(6)\n\t"
        "v_mfma_f32_16x16x32_bf16 v[32:35], v[172:175], v[64:67], v[32:35]\n\t"
        "v_mfma_f32_16x16x32_bf16 v[36:39], v[176:179], v[64:67], v[36:39]\n\t"
        "ds_read_b128 v[60:63], v107 offset:51200\n\t"
        "v_mfma_f32_16x16x32_bf16 v[40:43], v[64:67], v[180:183], v[40:43]\n\t"
        "s_add_u32 m0, %[ld], 81920\n\t"
        "s_add_u32 %[t0], %[xo], 1280\n\t"
        "s_waitcnt lgkmcnt(6)\n\t"
        "v_mfma_f32_16x16x32_bf16 v[44:47], v[172:175], v[68:71], v[44:47]\n\t"
        "buffer_load_dwordx4 v100, %[sx], %[t0] offen lds\n\t"
        "v_mfma_f32_16x16x32_bf16 v[48:51], v[176:179], v[68:71], v[48:51]\n\t"
        "ds_read_b128 v[64:67], v108 offset:49152\n\t"
        "v_mfma_f32_16x16x32_bf16 v[52:55], v[68:71], v[180:183], v[52:55]\n\t"
        "s_waitcnt lgkmcnt(6)\n\t"
        "v_mfma_f32_16x16x32_bf16 v[32:35], v[184:187], v[72:75], v[32:35]\n\t"
        "v_mfma_f32_16x16x32_bf16 v[36:39], v[188:191], v[72:75], v[36:39]\n\t"
        "ds_read_b128 v[68:71], v108 offset:51200\n\t"
        "v_mfma_f32_16x16x32_bf16 v[40:43], v[72:75], v[192:195], v[40:43]\n\t"
        "s_add_u32 m0, %[ld], 82944\n\t"
        "s_add_u32 %[t0], %[xo], 17664\n\t"
        "s_waitcnt lgkmcnt(6)\n\t"
        "v_mfma_f32_16x16x32_bf16 v[44:47], v[184:187], v[76:79], v[44:47]\n\t"
        "buffer_load_dwordx4 v100, %[sx], %[t0] offen lds\n\t"
        "v_mfma_f32_16x16x32_bf16 v[48:51], v[188:191], v[76:79], v[48:51]\n\t"
        "ds_read_b128 v[72:75], v107 offset:57344\n\t"
        "v_mfma_f32_16x16x32_bf16 v[52:55], v[76:79], v[192:195], v[52:55]\n\t"
        "s_waitcnt lgkmcnt(6)\n\t"
        "v_mfma_f32_16x16x32_bf16 v[32:35], v[196:199], v[80:83], v[32:35]\n\t"
        "v_mfma_f32_16x16x32_bf16 v[36:39], v[200:203], v[80:83], v[36:39]\n\t"
        "ds_read_b128 v[76:79], v107 offset:59392\n\t"
        "v_mfma_f32_16x16x32_bf16 v[40:43], v[80:83], v[204:207], v[40:43]\n\t"
        "s_add_u32 m0, %[ld], 90112\n\t"
        "s_add_u32 %[t0], %[xo], 1408\n\t"
        "s_waitcnt lgkmcnt(6)\n\t"
        "v_mfma_f32_16x16x32_bf16 v[44:47], v[196:199], v[84:87], v[44:47]\n\t"
        "buffer_load_dwordx4 v100, %[sx], %[t0] offen lds\n\t"
        "v_mfma_f32_16x16x32_bf16 v[48:51], v[200:203], v[84:87], v[48:51]\n\t"
        "ds_read_b128 v[80:83], v108 offset:57344\n\t"
        "v_mfma_f32_16x16x32_bf16 v[52:55], v[84:87], v[204:207], v[52:55]\n\t"
        "s_waitcnt lgkmcnt(6)\n\t"
        "v_mfma_f32_16x16x32_bf16 v[32:35], v[208:211], v[56:59], v[32:35]\n\t"
        "v_mfma_f32_16x16x32_bf16 v[36:39], v[212:215], v[56:59], v[36:39]\n\t"
        "ds_read_b128 v[84:87], v108 offset:59392\n\t"
        "v_mfma_f32_16x16x32_bf16 v[40:43], v[56:59], v[216:219], v[40:43]\n\t"
        "s_add_u32 m0, %[ld], 91136\n\t"
        "s_add_u32 %[t0], %[xo], 17792\n\t"
        "s_waitcnt lgkmcnt(6)\n\t"
        "v_mfma_f32_16x16x32_bf16 v[44:47], v[208:211], v[60:63], v[44:47]\n\t"
        "buffer_load_dwordx4 v100, %[sx], %[t0] offen lds\n\t"
        "v_mfma_f32_16x16x32_bf16 v[48:51], v[212:215], v[60:63], v[48:51]\n\t"
        "v_mfma_f32_16x16x32_bf16 v[52:55], v[60:63], v[216:219], v[52:55]\n\t"
        "s_waitcnt lgkmcnt(5)\n\t"
        "v_mfma_f32_16x16x32_bf16 v[32:35], v[220:223], v[64:67], v[32:35]\n\t"
        "s_waitcnt vmcnt(17) lgkmcnt(0)\n\t"
        "s_barrier\n\t"
        "v_mfma_f32_16x16x32_bf16 v[36:39], v[224:227], v[64:67], v[36:39]\n\t"
        "ds_read_b128 v[56:59], v105 offset:0\n\t"
        "v_mfma_f32_16x16x32_bf16 v[40:43], v[64:67], v[228:231], v[40:43]\n\t"
        "ds_read_b128 v[60:63], v105 offset:2048\n\t"
        "s_add_u32 m0, %[ld], 98304\n\t"
        "s_add_u32 %[t0], %[xo], 1536\n\t"
        "v_mfma_f32_16x16x32_bf16 v[44:47], v[220:223], v[68:71], v[44:47]\n\t"
        "buffer_load_dwordx4 v100, %[sx], %[t0] offen lds\n\t"
        "v_mfma_f32_16x16x32_bf16 v[48:51], v[224:227], v[68:71], v[48:51]\n\t"
        "ds_read_b128 v[64:67], v106 offset:0\n\t"
        "v_mfma_f32_16x16x32_bf16 v[52:55], v[68:71], v[228:231], v[52:55]\n\t"
        "v_mfma_f32_16x16x32_bf16 v[32:35], v[232:235], v[72:75], v[32:35]\n\t"
        "v_mfma_f32_16x16x32_bf16 v[36:39], v[236:239], v[72:75], v[36:39]\n\t"
        "ds_read_b128 v[68:71], v106 offset:2048\n\t"
        "v_mfma_f32_16x16x32_bf16 v[40:43], v[72:75], v[240:243], v[40:43]\n\t"
        "s_add_u32 m0, %[ld], 99328\n\t"
        "s_add_u32 %[t0], %[xo], 17920\n\t"
        "v_mfma_f32_16x16x32_bf16 v[44:47], v[232:235], v[76:79], v[44:47]\n\t"
        "buffer_load_dwordx4 v100, %[sx], %[t0] offen lds\n\t"
        "v_mfma_f32_16x16x32_bf16 v[48:51], v[236:239], v[76:79], v[48:51]\n\t"
        "ds_read_b128 v[72:75], v105 offset:8192\n\t"
        "v_mfma_f32_16x16x32_bf16 v[52:55], v[76:79], v[240:243], v[52:55]\n\t"
        "v_mfma_f32_16x16x32_bf16 v[32:35], v[244:247], v[80:83], v[32:35]\n\t"
        "v_mfma_f32_16x16x32_bf16 v[36:39], v[248:251], v[80:83], v[36:39]\n\t"
        "ds_read_b128 v[76:79], v105 offset:10240\n\t"
        "v_mfma_f32_16x16x32_bf16 v[40:43], v[80:83], v[252:255], v[40:43]\n\t"
        "s_add_u32 m0, %[ld], 106496\n\t"
        "s_add_u32 %[t0], %[xo], 1664\n\t"
        "v_mfma_f32_16x16x32_bf16 v[44:47], v[244:247], v[84:87], v[44:47]\n\t"
        "buffer_load_dwordx4 v100, %[sx], %[t0] offen lds\n\t"
        "v_mfma_f32_16x16x32_bf16 v[48:51], v[248:251], v[84:87], v[48:51]\n\t"
        "ds_read_b128 v[80:83], v106 offset:8192\n\t"
        "v_mfma_f32_16x16x32_bf16 v[52:55], v[84:87], v[252:255], v[52:55]\n\t"
        "s_cmp_lt_u32 %[cn], 32\n\t"
        "s_cbranch_scc1 LG_loop%=\n\t"
        "s_nop 7\n\t"
        "v_cvt_pk_bf16_f32 v88, v32, v33\n\t"
        "v_cvt_pk_bf16_f32 v89, v34, v35\n\t"
        "v_cvt_pk_bf16_f32 v90, v36, v37\n\t"
        "v_cvt_pk_bf16_f32 v91, v38, v39\n\t"
        "buffer_store_dwordx4 v[88:91], v109, %[sw], %[so] offen sc1\n\t"
        "v_cvt_pk_bf16_f32 v92, v44, v45\n\t"
        "v_cvt_pk_bf16_f32 v93, v46, v47\n\t"
        "v_cvt_pk_bf16_f32 v94, v48, v49\n\t"
        "v_cvt_pk_bf16_f32 v95, v50, v51\n\t"
        "buffer_store_dwordx4 v[92:95], v110, %[sw], %[so] offen sc1\n\t"
        "v_cvt_pk_bf16_f32 v96, v40, v41\n\t"
        "v_cvt_pk_bf16_f32 v97, v42, v43\n\t"
        "v_cvt_pk_bf16_f32 v98, v52, v53\n\t"
        "v_cvt_pk_bf16_f32 v99, v54, v55\n\t"
        "buffer_store_dwordx4 v[96:99], v111, %[sw], %[so] offen sc1\n\t"
        "s_waitcnt vmcnt(0) lgkmcnt(0)\n\t"
        "v_mov_b32_e32 v114, %[cn]\n\t"
        "buffer_store_dword v114, v113, %[sw], 0 offen sc1\n\t"
        "s_barrier\n\t"
        : [t0] "=&s"(t0), [xo] "=&s"(xo), [so] "=&s"(so), [cn] "=&s"(cn), [ts] "=&s"(ts)
        : [sx] "s"(sx), [sq] "s"(sq), [sk] "s"(sk), [sv] "s"(sv), [sw] "s"(sw), [ld] "s"(lb + w * 2048),
          "{v100}"((16 * w + (l >> 3)) * 2048 + (((l & 7) ^ (l >> 3)) << 4)), "{v101}"((16 * w + (l >> 2)) * 8192 + h * 256 + c * 64 + (l & 3) * 16), "{v102}"(lb + 131072 + w * 1024 + l * 16),
          "{v103}"(lr + tn * 4096 + (n & 3) * 16), "{v104}"(lr + 8192 + tn * 32 + (n & 3) * 8), "{v105}"(lx), "{v106}"(lx ^ 64u), "{v107}"(lx + 65536), "{v108}"((lx ^ 64u) + 65536),
          "{v109}"(aq), "{v110}"(aq + 4096), "{v111}"(W_VF + (tm * 32 + h) * 8192 + (2 * c + tn) * 1024 + l * 16), "{v113}"(l == 0 ? F_KVQ + h * 128 + (c * 4 + w) * 4 : OOB)
        : "memory", "scc", "m0", "v8", "v9", "v10", "v11", "v12", "v13", "v14", "v15", "v16", "v17", "v18", "v19", "v20", "v21", "v22", "v23", "v24", "v25", "v26", "v27", "v28", "v29", "v30", "v31", "v32", "v33", "v34", "v35", "v36", "v37", "v38", "v39", "v40", "v41", "v42", "v43", "v44", "v45", "v46", "v47", "v48", "v49", "v50", "v51", "v52", "v53", "v54", "v55", "v56", "v57", "v58", "v59", "v60", "v61", "v62", "v63", "v64", "v65", "v66", "v67", "v68", "v69", "v70", "v71", "v72", "v73", "v74", "v75", "v76", "v77", "v78", "v79", "v80", "v81", "v82", "v83", "v84", "v85", "v86", "v87", "v88", "v89", "v90", "v91", "v92", "v93", "v94", "v95", "v96", "v97", "v98", "v99", "v112", "v114", "v115", "v116", "v117", "v118", "v119", "v120", "v121", "v122", "v123", "v124", "v125", "v126", "v127", "v128", "v129", "v130", "v131", "v132", "v133", "v134", "v135", "v136", "v137", "v138", "v139", "v140", "v141", "v142", "v143", "v144", "v145", "v146", "v147", "v148", "v149", "v150", "v151", "v152", "v153", "v154", "v155", "v156", "v157", "v158", "v159", "v160", "v161", "v162", "v163", "v164", "v165", "v166", "v167", "v168", "v169", "v170", "v171", "v172", "v173", "v174", "v175", "v176", "v177", "v178", "v179", "v180", "v181", "v182", "v183", "v184", "v185", "v186", "v187", "v188", "v189", "v190", "v191", "v192", "v193", "v194", "v195", "v196", "v197", "v198", "v199", "v200", "v201", "v202", "v203", "v204", "v205", "v206", "v207", "v208", "v209", "v210", "v211", "v212", "v213", "v214", "v215", "v216", "v217", "v218", "v219", "v220", "v221", "v222", "v223", "v224", "v225", "v226", "v227", "v228", "v229", "v230", "v231", "v232", "v233", "v234", "v235", "v236", "v237", "v238", "v239", "v240", "v241", "v242", "v243", "v244", "v245", "v246", "v247", "v248", "v249", "v250", "v251", "v252", "v253", "v254", "v255", "a0", "a1", "a2", "a3", "a4", "a5", "a6", "a7", "a8", "a9", "a10", "a11", "a12", "a13", "a14", "a15", "a16", "a17", "a18", "a19", "a20", "a21", "a22", "a23", "a24", "a25", "a26", "a27", "a28", "a29", "a30", "a31", "a32", "a33", "a34", "a35", "a36", "a37", "a38", "a39", "a40", "a41", "a42", "a43", "a44", "a45", "a46", "a47", "a48", "a49", "a50", "a51", "a52", "a53", "a54", "a55", "a56", "a57", "a58", "a59", "a60", "a61", "a62", "a63", "a64", "a65", "a66", "a67", "a68", "a69", "a70", "a71", "a72", "a73", "a74", "a75", "a76", "a77", "a78", "a79", "a80", "a81", "a82", "a83", "a84", "a85", "a86", "a87", "a88", "a89", "a90", "a91", "a92", "a93", "a94", "a95", "a96", "a97", "a98", "a99", "a100", "a101", "a102", "a103", "a104", "a105", "a106", "a107", "a108", "a109", "a110", "a111", "a112", "a113", "a114", "a115", "a116", "a117", "a118", "a119", "a120", "a121", "a122", "a123", "a124", "a125", "a126", "a127", "a128", "a129", "a130", "a131", "a132", "a133", "a134", "a135", "a136", "a137", "a138", "a139", "a140", "a141", "a142", "a143", "a144", "a145", "a146", "a147", "a148", "a149", "a150", "a151", "a152", "a153", "a154", "a155", "a156", "a157", "a158", "a159", "a160", "a161", "a162", "a163", "a164", "a165", "a166", "a167", "a168", "a169", "a170", "a171", "a172", "a173", "a174", "a175", "a176", "a177", "a178", "a179", "a180", "a181", "a182", "a183", "a184", "a185", "a186", "a187", "a188", "a189", "a190", "a191", "a192", "a193", "a194", "a195", "a196", "a197", "a198", "a199", "a200", "a201", "a202", "a203", "a204", "a205", "a206", "a207", "a208", "a209", "a210", "a211", "a212", "a213", "a214", "a215", "a216", "a217", "a218", "a219", "a220", "a221", "a222", "a223", "a224", "a225", "a226", "a227", "a228", "a229", "a230", "a231", "a232", "a233", "a234", "a235", "a236", "a237", "a238", "a239", "a240", "a241", "a242", "a243", "a244", "a245", "a246", "a247", "a248", "a249", "a250", "a251", "a252", "a253", "a254", "a255");
}

__device__ __forceinline__ unsigned min16(unsigned d) {
    d = min(d, (unsigned)__builtin_amdgcn_update_dpp((int)d, (int)d, 0xB1, 0xF, 0xF, false)); d = min(d, (unsigned)__builtin_amdgcn_update_dpp((int)d, (int)d, 0x4E, 0xF, 0xF, false));
    d = min(d, (unsigned)__builtin_amdgcn_update_dpp((int)d, (int)d, 0x124, 0xF, 0xF, false)); d = min(d, (unsigned)__builtin_amdgcn_update_dpp((int)d, (int)d, 0x128, 0xF, 0xF, false));
    return __builtin_amdgcn_readfirstlane(d); }
constexpr int OP_P = 72;
__device__ __forceinline__ void fin_loads(v4u (&fp)[8], v2u& fa0, v2u& fa1, unsigned& fg, unsigned po, unsigned ao, unsigned go, const v4u& rd) {
    asm volatile("buffer_load_dwordx4 %0, %11, %14, 0 offen sc1\n\tbuffer_load_dwordx4 %1, %11, %14, 0 offen offset:1024 sc1\n\t"
                 "buffer_load_dwordx4 %2, %11, %14, %15 offen sc1\n\tbuffer_load_dwordx4 %3, %11, %14, %15 offen offset:1024 sc1\n\t"
                 "buffer_load_dwordx4 %4, %11, %14, %16 offen sc1\n\tbuffer_load_dwordx4 %5, %11, %14, %16 offen offset:1024 sc1\n\t"
                 "buffer_load_dwordx4 %6, %11, %14, %17 offen sc1\n\tbuffer_load_dwordx4 %7, %11, %14, %17 offen offset:1024 sc1\n\t"
                 "buffer_load_dwordx2 %8, %12, %14, 0 offen sc1\n\tbuffer_load_dwordx2 %9, %12, %14, %18 offen sc1\n\tbuffer_load_dword %10, %13, %14, 0 offen sc1"
                 : "=&v"(fp[0]), "=&v"(fp[1]), "=&v"(fp[2]), "=&v"(fp[3]), "=&v"(fp[4]), "=&v"(fp[5]), "=&v"(fp[6]), "=&v"(fp[7]), "=&v"(fa0), "=&v"(fa1), "=&v"(fg)
                 : "v"(po), "v"(ao), "v"(go), "s"(rd), "s"(262144u), "s"(524288u), "s"(786432u), "s"(32u) : "memory"); }
__device__ __forceinline__ v2u mix(const v4u& p0, const v4u& p1, const v4u& p2, const v4u& p3, const v2u& av, unsigned fg) {
    const float gg = __uint_as_float(fg), og = rbfh(1.f - gg); float o[4];
#pragma unroll
    for (int q = 0; q < 4; ++q) { const unsigned aw = q < 2 ? av.x : av.y; const float aa = __uint_as_float((q & 1) ? (aw & 0xFFFF0000u) : (aw << 16));
        const float bsum = (__uint_as_float(p0[q]) + __uint_as_float(p1[q])) + (__uint_as_float(p2[q]) + __uint_as_float(p3[q]));
        o[q] = rbfh(gg * aa) + rbfh(og * rbfh(bsum)); }
    return (v2u){pack2(o[0], o[1]), pack2(o[2], o[3])}; }
__device__ __forceinline__ void bulk_block(const Params& p, rsrc_t r, unsigned char* lds, const int wv, const int j) {
    const int w = wv;
    BSTAMP(0);
    proj_block(p, lds, wv, (j & 6) * 4 + (j >> 4), ((j >> 2) & 2) | (j & 1), j);
    BSTAMP(2);
    {
        const rsrc_t ro = __builtin_amdgcn_make_buffer_rsrc(p.out, 0, S * D * 2, 0x00020000); const v4u z = {0u, 0u, 0u, 0u}; const int tid = otid();
#pragma unroll
        for (int i = 0; i < 8; ++i) st16(ro, (unsigned)(j * 32768 + i * 4096 + tid * 16), z);
        DRAIN(); __syncthreads();
        if (tid == 0) __hip_atomic_fetch_add((unsigned*)(p.ws + F_CONV), 1u, __ATOMIC_RELAXED, __HIP_MEMORY_SCOPE_AGENT);
    }
    const int tid4 = otid();
    const int kq = (j & 7) >> 1, nt = (j >> 3) * 2 + (j & 1), l = tid4 & 63, n16 = l & 15, g = l >> 4;
    __syncthreads();
    for (int i = tid4; i < 4096; i += 256) { const int row = i >> 2, q4 = i & 3;
        *(v4u*)(lds + row * OP_P + q4 * 16) = *(const v4u*)(p.Wo + (size_t)(kq * 1024 + row) * 1024 + nt * 32 + q4 * 8); }
    __syncthreads();
    BSTAMP(13);
    const unsigned ldsbase = (unsigned)(size_t)lds;
    auto finish = [&](int tt) {
        const int tok = tt * 64 + 16 * w + n16;
        const unsigned po = W_PART + tt * 1048576 + (w * 32 + nt) * 2048 + l * 16;
        v4u pp[4][2];
#pragma unroll
        for (int q = 0; q < 4; ++q)
#pragma unroll
            for (int jn = 0; jn < 2; ++jn) pp[q][jn] = ld16(r, po + q * 262144 + jn * 1024);
        const float gg = __uint_as_float(ld4(r, W_GATE + tok * 4)), og = rbfh(1.f - gg);
#pragma unroll
        for (int jn = 0; jn < 2; ++jn) {
            const int n = nt * 32 + 16 * jn + 4 * g;
            const v2u av = ld8(r, W_A + nt * 131072 + (tok & 31) * 4096 + (tok >> 5) * 64 + jn * 32 + g * 8);
            float o[4];
#pragma unroll
            for (int q = 0; q < 4; ++q) { const unsigned aw = q < 2 ? av.x : av.y; const float aa = __uint_as_float((q & 1) ? (aw & 0xFFFF0000u) : (aw << 16));
                const float bsum = (__uint_as_float(pp[0][jn][q]) + __uint_as_float(pp[1][jn][q])) + (__uint_as_float(pp[2][jn][q]) + __uint_as_float(pp[3][jn][q]));
                o[q] = rbfh(gg * aa) + rbfh(og * rbfh(bsum)); }
            v2u pk = {pack2(o[0], o[1]), pack2(o[2], o[3])}; *(v2u*)(p.out + (size_t)tok * 1024 + n) = pk; } };
    bf16x8 wof[32][2];
#pragma unroll
    for (int ks = 0; ks < 32; ++ks)
#pragma unroll
        for (int jn = 0; jn < 2; ++jn) {
            const unsigned a0 = ldsbase + (32 * ks + 4 * g + (n16 >> 2)) * OP_P + (16 * jn + 4 * (n16 & 3)) * 2;
            const v2u lo = trb(a0), hi = trb(a0 + 16 * OP_P);
            v4u v = {lo.x, lo.y, hi.x, hi.y}; asm volatile("" : "=a"(wof[ks][jn]) : "0"(as_frag(v))); }
    const v4u rd = mk_srd(p.ws, W_TOTAL), ro = mk_srd(p.out, S * D * 2);
    const int pw = w >> 1;
    int op = 0, nev = 0; unsigned idle = 0u; (void)nev;
    bool conv_ok = false; unsigned known = 0u;
    unsigned prog = 0u, tseen = rtnow() - 100000u;
    auto a_off = [&](int t) -> unsigned { return W_OX + ((t * 4 + w) * 32 + 8 * kq) * 4096 + l * 16; };
    const unsigned r_off = F_PROG + (kq * 8 + (l & 7)) * 128;
    auto units = [&](unsigned pg) -> unsigned { const unsigned c = (pg + 1u - pw) >> 1; return c >= 32u ? ~0u : (1u << c) - 1u; };
    while (op < 32) {
        unsigned rdy = known >> op;
        if (!(rdy & 1u)) {
            const unsigned now = rtnow(), est = prog + 1u + (now - tseen) / STEP_MIN;
            if (++idle > (1u << 22)) { st4(r, F_ERR, 128u); return; }
            if (est <= (unsigned)(2 * op + pw)) { __builtin_amdgcn_s_sleep(16); continue; }
            MEMBAR();
            prog = min16(ld4(r, r_off)); tseen = now; known = units(prog); rdy = known >> op;
            MEMBAR();
            if (!(rdy & 1u)) { __builtin_amdgcn_s_sleep(POLL_SLP); continue; }
        }
        idle = 0u;
        if (!conv_ok) { if (!wait_word(r, F_CONV, 160u, 256u)) return; conv_ok = true; }
        EV(128 + op);
        v4u af[32], wm, fp[8]; v2u fa0, fa1; unsigned aold = 0u, cfl = 0u, fg, d0;
        {
            const unsigned a0 = a_off(op);
            if (WARM1) asm volatile("buffer_load_dwordx4 %0, %1, %2, 0 offen sc1\n\ts_waitcnt vmcnt(0)" : "=&v"(wm) : "v"(a0 + nt * 1024), "s"(rd) : "memory");
#pragma unroll
            for (int q = 0; q < 8; ++q)
                asm volatile("buffer_load_dwordx4 %0, %4, %5, %6 offen sc1\n\tbuffer_load_dwordx4 %1, %4, %5, %6 offen offset:1024 sc1\n\t"
                             "buffer_load_dwordx4 %2, %4, %5, %6 offen offset:2048 sc1\n\tbuffer_load_dwordx4 %3, %4, %5, %6 offen offset:3072 sc1"
                             : "=&v"(af[4 * q]), "=&v"(af[4 * q + 1]), "=&v"(af[4 * q + 2]), "=&v"(af[4 * q + 3]) : "v"(a0), "s"(rd), "s"(q * 4096) : "memory");
            asm volatile("buffer_load_dwordx4 %3, %5, %6, 0 offen sc1\n\tbuffer_load_dword %1, %4, %6, 0 offen sc1\n\tbuffer_load_dword %2, %7, %6, 0 offen sc1\n\t"
                         "buffer_load_dword %0, %4, %6, 0 offen sc1\n\tbuffer_load_dword %0, %4, %6, 0 offen sc1\n\tbuffer_load_dword %0, %4, %6, 0 offen sc1\n\tbuffer_load_dword %0, %4, %6, 0 offen sc1"
                         : "=&v"(d0), "=&v"(aold), "=&v"(cfl), "=&v"(wm) : "v"(OOB), "v"((rdy & 2u) ? a_off(op + 1) + nt * 1024 : OOB), "s"(rd), "v"(r_off) : "memory");
            fin_loads(fp, fa0, fa1, fg, OOB, OOB, OOB, rd);
        }
        const int op0 = op; bool fin2 = false; int fpend = -1;
        for (;;) {
            const bool more = (rdy & 2u) != 0u;
            const unsigned an = more ? a_off(op + 1) : OOB;
            f32x4 acc0 = {0.f, 0.f, 0.f, 0.f}, acc1 = {0.f, 0.f, 0.f, 0.f};
            asm volatile("s_nop 1" : "+v"(acc0), "+v"(acc1));
#pragma unroll
            for (int q = 0; q < 8; ++q)
                asm volatile(
                    "s_waitcnt vmcnt(49)\n\tv_mfma_f32_16x16x32_bf16 %4, %6, %0, %4\n\tv_mfma_f32_16x16x32_bf16 %5, %7, %0, %5\n\tbuffer_load_dwordx4 %0, %14, %15, %16 offen sc1\n\t"
                    "s_waitcnt vmcnt(49)\n\tv_mfma_f32_16x16x32_bf16 %4, %8, %1, %4\n\tv_mfma_f32_16x16x32_bf16 %5, %9, %1, %5\n\tbuffer_load_dwordx4 %1, %14, %15, %16 offen offset:1024 sc1\n\t"
                    "s_waitcnt vmcnt(49)\n\tv_mfma_f32_16x16x32_bf16 %4, %10, %2, %4\n\tv_mfma_f32_16x16x32_bf16 %5, %11, %2, %5\n\tbuffer_load_dwordx4 %2, %14, %15, %16 offen offset:2048 sc1\n\t"
                    "s_waitcnt vmcnt(49)\n\tv_mfma_f32_16x16x32_bf16 %4, %12, %3, %4\n\tv_mfma_f32_16x16x32_bf16 %5, %13, %3, %5\n\tbuffer_load_dwordx4 %3, %14, %15, %16 offen offset:3072 sc1"
                    : "+v"(af[4 * q]), "+v"(af[4 * q + 1]), "+v"(af[4 * q + 2]), "+v"(af[4 * q + 3]), "+v"(acc0), "+v"(acc1)
                    : "a"(wof[4 * q][0]), "a"(wof[4 * q][1]), "a"(wof[4 * q + 1][0]), "a"(wof[4 * q + 1][1]), "a"(wof[4 * q + 2][0]), "a"(wof[4 * q + 2][1]), "a"(wof[4 * q + 3][0]), "a"(wof[4 * q + 3][1]),
                      "v"(an), "s"(rd), "s"(q * 4096) : "memory");
            const unsigned po = W_PART + (op * 4 + kq) * 262144 + (w * 32 + nt) * 2048 + l * 16;
            asm volatile("s_nop 7\n\tbuffer_store_dwordx4 %14, %16, %17, 0 offen sc1\n\tbuffer_store_dwordx4 %15, %16, %17, 0 offen offset:1024 sc1\n\ts_waitcnt vmcnt(34)"
                         : "+v"(aold), "+v"(cfl), "+v"(wm), "+v"(fp[0]), "+v"(fp[1]), "+v"(fp[2]), "+v"(fp[3]), "+v"(fp[4]), "+v"(fp[5]), "+v"(fp[6]), "+v"(fp[7]), "+v"(fa0), "+v"(fa1), "+v"(fg)
                         : "v"(acc0), "v"(acc1), "v"(po), "s"(rd) : "memory");
            const unsigned oldu = __builtin_amdgcn_readfirstlane(aold);
            rdy |= units(min16(cfl)) >> op;
            {
                const v2u o0 = mix(fp[0], fp[2], fp[4], fp[6], fa0, fg), o1 = mix(fp[1], fp[3], fp[5], fp[7], fa1, fg);
                const unsigned oo = fpend >= 0 ? (unsigned)(((fpend * 64 + 16 * w + n16) * 1024 + nt * 32 + 4 * g) * 2) : OOB;
                asm volatile("buffer_store_dwordx2 %0, %2, %3, 0 offen\n\tbuffer_store_dwordx2 %1, %2, %3, 0 offen offset:32" :: "v"(o0), "v"(o1), "v"(oo), "s"(ro) : "memory");
            }
            const bool fin = op >= op0 + 2 && oldu == 3u;
            if (!more) {
                if (!(rdy & 2u)) { fin2 = fin; break; }
                const unsigned a1 = a_off(op + 1);
#pragma unroll
                for (int q = 0; q < 8; ++q)
                    asm volatile("buffer_load_dwordx4 %0, %4, %5, %6 offen sc1\n\tbuffer_load_dwordx4 %1, %4, %5, %6 offen offset:1024 sc1\n\t"
                                 "buffer_load_dwordx4 %2, %4, %5, %6 offen offset:2048 sc1\n\tbuffer_load_dwordx4 %3, %4, %5, %6 offen offset:3072 sc1"
                                 : "+v"(af[4 * q]), "+v"(af[4 * q + 1]), "+v"(af[4 * q + 2]), "+v"(af[4 * q + 3]) : "v"(a1), "s"(rd), "s"(q * 4096) : "memory");
                asm volatile("buffer_load_dword %0, %1, %2, 0 offen sc1\n\tbuffer_load_dword %0, %1, %2, 0 offen sc1\n\tbuffer_load_dword %0, %1, %2, 0 offen sc1\n\tbuffer_load_dword %0, %1, %2, 0 offen sc1"
                             : "+v"(d0) : "v"(OOB), "s"(rd) : "memory");
            }
            {
                const unsigned tadr = op > op0 ? F_OP + ((op - 1) * 32 + nt) * 64 + w * 4 : F_DUM + (j * 4 + w) * 16;
                aold = 1u;
                asm volatile("s_mov_b64 exec, 1\n\tbuffer_atomic_add %0, %3, %6, 0 offen sc0\n\ts_mov_b64 exec, -1\n\t"
                             "buffer_load_dword %1, %4, %6, 0 offen sc1\n\tbuffer_load_dwordx4 %2, %5, %6, 0 offen sc1"
                             : "+v"(aold), "=&v"(cfl), "=&v"(wm) : "v"(tadr), "v"((rdy & 8u) ? OOB : r_off), "v"((rdy & 4u) ? a_off(op + 2) + nt * 1024 : OOB), "s"(rd) : "memory");
                fpend = fin ? op - 2 : -1;
                const int tok = (op - 2) * 64 + 16 * w + n16;
                fin_loads(fp, fa0, fa1, fg, fin ? W_PART + (op - 2) * 1048576 + (w * 32 + nt) * 2048 + l * 16 : OOB,
                          fin ? W_A + nt * 131072 + (tok & 31) * 4096 + (tok >> 5) * 64 + g * 8 : OOB, fin ? W_GATE + tok * 4 : OOB, rd);
            }
            if (op == 15) BSTAMP(5);
            rdy >>= 1; ++op;
        }
        asm volatile("s_waitcnt vmcnt(0)" : "+v"(af[0]), "+v"(af[1]), "+v"(af[2]), "+v"(af[3]), "+v"(af[4]), "+v"(af[5]), "+v"(af[6]), "+v"(af[7]), "+v"(af[8]), "+v"(af[9]), "+v"(af[10]), "+v"(af[11]),
                     "+v"(af[12]), "+v"(af[13]), "+v"(af[14]), "+v"(af[15]), "+v"(d0) :: "memory");
        asm volatile("" : "+v"(af[16]), "+v"(af[17]), "+v"(af[18]), "+v"(af[19]), "+v"(af[20]), "+v"(af[21]), "+v"(af[22]), "+v"(af[23]), "+v"(af[24]), "+v"(af[25]), "+v"(af[26]), "+v"(af[27]),
                     "+v"(af[28]), "+v"(af[29]), "+v"(af[30]), "+v"(af[31]) :: "memory");
        unsigned o1 = 0u, o2 = 0u;
        if (l == 0) { o2 = __hip_atomic_fetch_add((unsigned*)(p.ws + F_OP + (op * 32 + nt) * 64 + w * 4), 1u, __ATOMIC_RELAXED, __HIP_MEMORY_SCOPE_AGENT);
            if (op > op0) o1 = __hip_atomic_fetch_add((unsigned*)(p.ws + F_OP + ((op - 1) * 32 + nt) * 64 + w * 4), 1u, __ATOMIC_RELAXED, __HIP_MEMORY_SCOPE_AGENT); }
        {
            const unsigned now = rtnow();
            prog = min16(ld4(r, r_off)); tseen = now; known = units(prog);
        }
        if (fin2) finish(op - 2);
        if (__builtin_amdgcn_readfirstlane(o1) == 3u) finish(op - 1);
        if (__builtin_amdgcn_readfirstlane(o2) == 3u) finish(op);
        if (op == 15) BSTAMP(5);
        EV(192 + op);
        ++op;
    }
    BSTAMP(7);
}

__device__ __forceinline__ void rec_block(const Params& p, rsrc_t r, unsigned char* lds, const int wv, const int b) {
    const int xq = (b & 7) >> 1, t = (b >> 3) * 2 + (b & 1);
    const unsigned lb = __builtin_amdgcn_readfirstlane((unsigned)(size_t)lds);
    int kind, id, rg = 0;
    if (RPL == 0) { kind = t < 8 ? 0 : t < 24 ? 1 : 2; id = t < 8 ? xq * 8 + t : t < 24 ? (xq & 1) * 16 + t - 8 : xq * 8 + t - 24; rg = xq >> 1; }
    else if (RPL == 1) { kind = xq == 0 ? 0 : xq < 3 ? 1 : 2; id = t; rg = xq - 1; }
    else if (RPL == 2) { const int na = xq == 2 ? 10 : 11, q = xq * 21 + t - na; kind = xq == 3 ? 2 : t < na ? 0 : 1; id = xq == 3 ? t : t < na ? xq * 11 + t : q >> 1; rg = q & 1; }
    else { const int na = xq == 2 ? 10 : 11, q = t - na; kind = xq == 3 ? 2 : t < na ? 0 : 1; id = xq == 3 ? t : t < na ? xq * 11 + t : xq < 2 ? q : 21 + (q >> 1); rg = xq < 2 ? xq : q & 1; }
    if (kind == 0) rec_a(p, lb, wv, id, xq);
    else if (kind == 1) rec_b(p, lb, wv, id, rg, xq);
    else {
        const int hi = id, j = hi - 32; (void)j;
        BSTAMP(0);
        rec_helper(p, r, lds, wv, hi);
        BSTAMP(6);
        gate_block(p, r, hi, wv);
        BSTAMP(2);
        conv_block(p, r, lds, wv, hi, j);
        BSTAMP(3);
    }
}

__global__ void __launch_bounds__(256, 1) k_main(Params p) {
    extern __shared__ __attribute__((aligned(16))) unsigned char lds[];
    rsrc_t r = __builtin_amdgcn_make_buffer_rsrc(p.ws, 0, W_TOTAL, 0x00020000);
    const int wv = __builtin_amdgcn_readfirstlane(threadIdx.x >> 6);
    int role = blockIdx.x;
    if (threadIdx.x == 0) {
        unsigned xcc; asm volatile("s_getreg_b32 %0, hwreg(HW_REG_XCC_ID)" : "=s"(xcc)); xcc &= 7u;
        unsigned* tk = (unsigned*)(p.ws + F_TKT);
        unsigned t = __hip_atomic_fetch_add(tk + xcc * 16, 1u, __ATOMIC_RELAXED, __HIP_MEMORY_SCOPE_AGENT);
        if (t >= 32u) {
            unsigned o = __hip_atomic_fetch_add(tk + 128, 1u, __ATOMIC_RELAXED, __HIP_MEMORY_SCOPE_AGENT);
            for (unsigned spin = 0; spin < (1u << 22); ++spin) { unsigned tot = 0u;
                for (int x = 0; x < 8; ++x) tot += __hip_atomic_load(tk + x * 16, __ATOMIC_RELAXED, __HIP_MEMORY_SCOPE_AGENT);
                if (tot >= 256u) break; }
            for (int x = 0; x < 8; ++x) { const unsigned cnt = __hip_atomic_load(tk + x * 16, __ATOMIC_RELAXED, __HIP_MEMORY_SCOPE_AGENT), fr = cnt < 32u ? 32u - cnt : 0u;
                if (o < fr) { xcc = (unsigned)x; t = cnt + o; break; }
                o -= fr; }
        }
        *(volatile int*)lds = (int)((xcc >> 2) * 128u + (t >> 1) * 8u + (xcc & 3u) * 2u + (t & 1u));
    }
    __syncthreads();
    role = __builtin_amdgcn_readfirstlane(*(volatile int*)lds) & 255;
    __syncthreads();
    if (role >= 128) bulk_block(p, r, lds, wv, role - 128); else rec_block(p, r, lds, wv, role);
}

extern "C" void kernel_launch(void* const* d_in, const int* in_sizes, int n_in, void* d_out, int out_size, void* d_ws, size_t ws_size, hipStream_t stream) {
    Params p;
    p.Aq = (const bf16_t*)d_in[0]; p.Ak = (const bf16_t*)d_in[1]; p.Av = (const bf16_t*)d_in[2];
    p.shift_k = (const bf16_t*)d_in[3]; p.ssm_k = (const bf16_t*)d_in[4]; p.ssm_D = (const bf16_t*)d_in[5];
    p.mem0 = (const bf16_t*)d_in[7]; p.Wq = (const bf16_t*)d_in[8]; p.Wk = (const bf16_t*)d_in[9];
    p.Wv = (const bf16_t*)d_in[10]; p.Wo = (const bf16_t*)d_in[11]; p.gate_w = (const bf16_t*)d_in[13];
    p.out = (bf16_t*)d_out; p.ws = (unsigned char*)d_ws;
    unsigned char* ws = (unsigned char*)d_ws;
    (void)hipMemsetAsync(ws, 0, FLG_BYTES, stream);
    (void)hipFuncSetAttribute((const void*)k_main, hipFuncAttributeMaxDynamicSharedMemorySize, L_TOTAL);
    k_main<<<256, 256, L_TOTAL, stream>>>(p);
}
```

```cpp
#define ROT 1
#include <hip/hip_runtime.h>
#include <stdint.h>
#include <type_traits>
typedef unsigned short bf16_t;
using bf16x8 = __attribute__((ext_vector_type(8))) short;
using f32x4 = __attribute__((ext_vector_type(4))) float;
typedef unsigned v4u __attribute__((ext_vector_type(4)));
typedef unsigned v2u __attribute__((ext_vector_type(2)));
typedef __amdgpu_buffer_rsrc_t rsrc_t;

__device__ __forceinline__ float bf2f(bf16_t b) { return __uint_as_float(((unsigned)b) << 16); }
typedef __bf16 hbf2 __attribute__((ext_vector_type(2)));
typedef float f32x2 __attribute__((ext_vector_type(2)));
__device__ __forceinline__ unsigned pack2(float a, float b) { f32x2 v = {a, b}; return __builtin_bit_cast(unsigned, __builtin_convertvector(v, hbf2)); }
__device__ __forceinline__ float rbfh(float f) { return __uint_as_float(pack2(f, 0.f) << 16); }
__device__ __forceinline__ unsigned nosent(unsigned x) {
    typedef unsigned short us2 __attribute__((ext_vector_type(2)));
    const us2 a = __builtin_bit_cast(us2, x), c = {(unsigned short)0xFFFE, (unsigned short)0xFFFE}; return __builtin_bit_cast(unsigned, __builtin_elementwise_min(a, c)); }

constexpr int S = 2048, D = 1024, H = 32, DH = 128, M = 8, SEG = 32, NSEG = 64;

constexpr unsigned W_FLG = 0;
constexpr unsigned F_KVQ = W_FLG;
constexpr unsigned F_CONV = W_FLG + 32768;
constexpr unsigned F_ERR = W_FLG + 32768 + 256;
constexpr unsigned F_PREP = W_FLG + 32768 + 512;
constexpr unsigned F_CAL = W_FLG + 33792;
constexpr unsigned F_TKT = W_FLG + 34816;
constexpr unsigned F_TQ = W_FLG + 36864;
constexpr unsigned F_PROG = W_FLG + 49152;
constexpr unsigned F_DUM = W_FLG + 53248;
constexpr unsigned F_OP = W_FLG + 65536;
constexpr unsigned F_ATT = W_FLG + 131072;
constexpr unsigned F_RDY = W_FLG + 147456;
constexpr unsigned FLG_BYTES = 180224;
constexpr unsigned HO_KVM = W_FLG + FLG_BYTES;
constexpr unsigned W_A = HO_KVM + 64 * 32 * 4096;
constexpr unsigned W_GATE = W_A + S * D * 2;
constexpr unsigned W_QX = W_GATE + 8192;
constexpr unsigned W_KX = W_QX + S * 4096 * 2;
constexpr unsigned W_VF = W_KX + S * 4096 * 2;
constexpr unsigned W_OX = W_VF + 64 * 32 * 8192;
constexpr unsigned W_PART = W_OX + S * 4096 * 2;
constexpr unsigned W_DBG = W_PART + 32 * 4 * 64 * 1024 * 4;
constexpr unsigned W_U = W_DBG + 256 * 64 * 16 * 8;
constexpr unsigned W_TOTAL = W_U + S * D * 2;

__device__ __forceinline__ v4u ld16(rsrc_t r, unsigned off) { return __builtin_amdgcn_raw_buffer_load_b128(r, off, 0, 16); }
__device__ __forceinline__ v2u ld8(rsrc_t r, unsigned off) { return __builtin_amdgcn_raw_buffer_load_b64(r, off, 0, 16); }
__device__ __forceinline__ unsigned ld4(rsrc_t r, unsigned off) { return __builtin_amdgcn_raw_buffer_load_b32(r, off, 0, 16); }
__device__ __forceinline__ void st16(rsrc_t r, unsigned off, v4u v) { __builtin_amdgcn_raw_buffer_store_b128(v, r, off, 0, 16); }
__device__ __forceinline__ void st8(rsrc_t r, unsigned off, v2u v) { __builtin_amdgcn_raw_buffer_store_b64(v, r, off, 0, 16); }
__device__ __forceinline__ void st4(rsrc_t r, unsigned off, unsigned v) { __builtin_amdgcn_raw_buffer_store_b32(v, r, off, 0, 16); }
__device__ __forceinline__ bool okdw(unsigned x) { return ((x & 0xFFFFu) != 0xFFFFu) & ((x >> 16) != 0xFFFFu); }
typedef unsigned short us2_t __attribute__((ext_vector_type(2)));
__device__ __forceinline__ unsigned pkmax(unsigned a, unsigned b) { return __builtin_bit_cast(unsigned, __builtin_elementwise_max(__builtin_bit_cast(us2_t, a), __builtin_bit_cast(us2_t, b))); }
__device__ __forceinline__ unsigned pkmax4(v4u v) { return pkmax(pkmax(v.x, v.y), pkmax(v.z, v.w)); }
__device__ __forceinline__ bool ok16(v4u v) { return okdw(pkmax4(v)); }
__device__ __forceinline__ bool ok8(v2u v) { return okdw(v.x) & okdw(v.y); }
__device__ __forceinline__ bf16x8 as_frag(v4u v) { return __builtin_bit_cast(bf16x8, v); }
#define MFMA(a, b, c) __builtin_amdgcn_mfma_f32_16x16x32_bf16((a), (b), (c), 0, 0, 0)
#define D1 144
#define D2 100
#define D3 76
#define LT_A 4
#define LB0 12
#define LC0 20
#define LA0 24
#define TR_UP 4
#define TR_DN 2
#define L_DEC 3
#define WARM1 0
#define KROT 0
#define STAG 1
#define UP_SH 2
#define EPI_SLP 0
#define SITA 1
#define PAIRX 4
#define DUE_P 1
#define LATE 1
#define EARLY_FROM 56
#define SKEW 0
#define OUT_WARM 1
#define ADAPT 1
#define RPL 3
#define CV_POL 2
#define POLL_SLP 4
#define TOUCH 1
#define XWAIT 0
#define PEN 60
#define MEMBAR() asm volatile("" ::: "memory")
#define FT(i) do { } while (0)
#define ENDPGM() asm volatile("s_endpgm" ::: "memory")
#define DRAIN() asm volatile("s_waitcnt vmcnt(0)" ::: "memory")

__device__ __forceinline__ float xor16_max(float x) { auto r = __builtin_amdgcn_permlane16_swap(__float_as_uint(x), __float_as_uint(x), false, false); return fmaxf(__uint_as_float(r[0]), __uint_as_float(r[1])); }
__device__ __forceinline__ float xor32_max(float x) { auto r = __builtin_amdgcn_permlane32_swap(__float_as_uint(x), __float_as_uint(x), false, false); return fmaxf(__uint_as_float(r[0]), __uint_as_float(r[1])); }
__device__ __forceinline__ float xor16_add(float x) { auto r = __builtin_amdgcn_permlane16_swap(__float_as_uint(x), __float_as_uint(x), false, false); return __uint_as_float(r[0]) + __uint_as_float(r[1]); }
__device__ __forceinline__ float xor32_add(float x) { auto r = __builtin_amdgcn_permlane32_swap(__float_as_uint(x), __float_as_uint(x), false, false); return __uint_as_float(r[0]) + __uint_as_float(r[1]); }
__device__ __forceinline__ v2u tr_read(unsigned lds_addr) { v2u r; asm volatile("ds_read_b64_tr_b16 %0, %1\n\ts_waitcnt lgkmcnt(0)" : "=&v"(r) : "v"(lds_addr) : "memory"); return r; }

typedef short v4i16_t __attribute__((ext_vector_type(4)));
__device__ __forceinline__ v2u trb(unsigned lds_addr) { return __builtin_bit_cast(v2u, __builtin_amdgcn_ds_read_tr16_b64_v4i16((__attribute__((address_space(3))) v4i16_t*)(size_t)lds_addr)); }
__device__ __forceinline__ int otid_(int wv) { int t; asm volatile("v_mbcnt_lo_u32_b32 %0, -1, 0\n\tv_mbcnt_hi_u32_b32 %0, -1, %0" : "=v"(t)); return wv * 64 + t; }
#define otid() otid_(wv)
__device__ __forceinline__ bool wait_word(rsrc_t r, unsigned off, unsigned want, unsigned code) {
    for (unsigned spin = 0;; ++spin) { MEMBAR();
        const unsigned v = __builtin_amdgcn_readfirstlane(ld4(r, off));
        if (v >= want) return true;
        if (spin > (1u << 20)) { st4(r, F_ERR, code); return false; }
        __builtin_amdgcn_s_sleep(8); }
}

__device__ __forceinline__ unsigned rtnow() { return (unsigned)__builtin_amdgcn_s_memrealtime(); }
__device__ __forceinline__ void wait_until(unsigned target) { for (int i = 0; i < 4096; ++i) { if ((int)(rtnow() - target) >= 0) break; } }
__device__ __forceinline__ void wait_long(unsigned target) { for (int i = 0; i < 4096; ++i) { if ((int)(rtnow() - target) >= 0) break; __builtin_amdgcn_s_sleep(8); } }
constexpr int RED_G = 320;
constexpr int RED_T = 4 * RED_G;
constexpr int L_RED = 0;
constexpr int L_KX = 65536;
constexpr int KXP = 272;
constexpr int L_VF = L_KX + 2 * 32 * KXP;
constexpr int L_QKV = L_VF + 16384;
constexpr int L_OFF = L_QKV + 6144;
constexpr int L_STMP = L_OFF + 256;
constexpr int L_TOTAL = 163840;
constexpr unsigned U_A = 0, U_S = 32, U_B = 36, U_INIT = 113, U_C = 128, U_END = 512;
#define PLACE 1
#define CAL_N 5
#define STEP_MIN 200u

struct Params {
    const bf16_t *Aq, *Ak, *Av, *shift_k, *ssm_k, *ssm_D, *mem0, *Wq, *Wk, *Wv, *Wo, *gate_w;
    bf16_t* out; unsigned char* ws;
};
__host__ __device__ __forceinline__ unsigned hx_base(const void* ws) { const size_t a = (size_t)ws; return (unsigned)((((a + W_TOTAL + 0x7FFFFFu) >> 23) << 23) - a); }

#define STAMP(i) do { } while (0)
#define BSTAMP(i) do { } while (0)
#define EV(code) do { } while (0)
#define SPINS(i) do { } while (0)
typedef short v4s __attribute__((ext_vector_type(4)));
typedef unsigned v32u __attribute__((ext_vector_type(32)));
typedef unsigned v16u __attribute__((ext_vector_type(16)));
static_assert(L_VF - L_KX == 16384 + 1024, "fragment read offsets");
#define MFMA16(a, b, c) __builtin_amdgcn_mfma_f32_16x16x16bf16_1k(__builtin_bit_cast(v4s, (a)), __builtin_bit_cast(v4s, (b)), (c), 0, 0, 0)
__device__ __forceinline__ unsigned ror8(unsigned x) { return (unsigned)__builtin_amdgcn_update_dpp(0, (int)x, 0x128, 0xF, 0xF, false); }
__device__ __forceinline__ unsigned qx1(unsigned x) { return (unsigned)__builtin_amdgcn_update_dpp(0, (int)x, 0xB1, 0xF, 0xF, false); }
__device__ __forceinline__ unsigned wave_pkmax(unsigned d) {
    d = pkmax(d, (unsigned)__builtin_amdgcn_update_dpp((int)d, (int)d, 0xB1, 0xF, 0xF, false)); d = pkmax(d, (unsigned)__builtin_amdgcn_update_dpp((int)d, (int)d, 0x4E, 0xF, 0xF, false));
    d = pkmax(d, (unsigned)__builtin_amdgcn_update_dpp((int)d, (int)d, 0x124, 0xF, 0xF, false)); d = pkmax(d, (unsigned)__builtin_amdgcn_update_dpp((int)d, (int)d, 0x128, 0xF, 0xF, false));
    { auto q = __builtin_amdgcn_permlane16_swap(d, d, false, false); d = pkmax(q[0], q[1]); }
    { auto q = __builtin_amdgcn_permlane32_swap(d, d, false, false); d = pkmax(q[0], q[1]); }
    return d; }
__device__ __forceinline__ int wave_max(int d) {
    d = max(d, __builtin_amdgcn_update_dpp(d, d, 0xB1, 0xF, 0xF, false)); d = max(d, __builtin_amdgcn_update_dpp(d, d, 0x4E, 0xF, 0xF, false));
    d = max(d, __builtin_amdgcn_update_dpp(d, d, 0x124, 0xF, 0xF, false)); d = max(d, __builtin_amdgcn_update_dpp(d, d, 0x128, 0xF, 0xF, false));
    { auto q = __builtin_amdgcn_permlane16_swap((unsigned)d, (unsigned)d, false, false); d = max((int)q[0], (int)q[1]); }
    { auto q = __builtin_amdgcn_permlane32_swap((unsigned)d, (unsigned)d, false, false); d = max((int)q[0], (int)q[1]); }
    return d; }

__device__ __forceinline__ v4u mk_srd(const void* p, unsigned bytes) { const size_t a = (size_t)p; v4u d;
    d.x = __builtin_amdgcn_readfirstlane((unsigned)a); d.y = __builtin_amdgcn_readfirstlane((unsigned)(a >> 32) & 0xFFFFu); d.z = __builtin_amdgcn_readfirstlane(bytes); d.w = 0x00020000u; return d; }
__device__ __forceinline__ bf16x8 ldsa(unsigned addr) { return *(const __attribute__((address_space(3))) bf16x8*)(size_t)addr; }
constexpr unsigned HX_BYTES = 2097152, OOB = 0xF0000000u;
constexpr unsigned F_RDY0 = W_FLG + 34304;
constexpr int L_KV = 90112, L_MS = 139264, L_IMG = 155648, IMG_PS = 1056, IMG_MS = 2112, LB_R = 8192, LB_STG = 16384, LB_STGP = 8704;
static_assert(F_KVQ == 0 && F_ERR == 33024 && F_CAL == 33792 && F_RDY0 == 34304 && F_PROG == 49152 && W_QX == 12771328 && W_KX == 29548544 && W_OX == 63102976 && W_VF == 46325760 && W_DBG == 113434624 && L_KV == 90112 && L_MS == 139264 && L_IMG == 155648 && IMG_PS == 1056 && IMG_MS == 2112 && LB_R == 8192 && LB_STG == 16384 && LB_STGP == 8704, "constants of the generated code");
#define REC_S_IN "{s[8:11]}"(sh), "{s[12:15]}"(sw), "{s[16:19]}"(s1), "{s[20:23]}"(s2), "{s29}"(D1), "{s30}"(D2), "{s43}"(w), "{s44}"(id), "{s49}"(rb), "{s50}"(PEN), "{s51}"(LT_A)
__device__ __forceinline__ void rec_a(const Params& p, unsigned lb, const int wv, const int id, const int cp) {
    const int w = wv, l = otid() & 63, n = l & 15, g = l >> 4, h = id, rb = id;
    const v4u sh = mk_srd(p.ws + hx_base(p.ws), HX_BYTES - 8192), sw = mk_srd(p.ws, W_TOTAL), s1 = mk_srd(w < 2 ? p.Wq : p.Wk, 1024 * 4096 * 2), s2 = s1;
    const int r8 = l >> 3, pc = l & 7, pp = n >> 3, bb = (n >> 2) & 1, f = (g & 1) | (pp << 1);
    const unsigned stg = lb + 131072 + w * 8192;
    const unsigned g_w = r8 * 8192 + h * 256 + (w & 1) * 128 + pc * 16;
    const unsigned wb0 = stg + r8 * 128 + (((pc >> 1) ^ (r8 >> 2)) << 5) + (pc & 1) * 16, wb1 = wb0 ^ 64u;
    const unsigned rb0 = stg + (16 * (g >> 1) + 8 * pp + 4 * (g & 1) + bb) * 128 + (f << 5) + (n & 3) * 8;
    const unsigned a_mem = cp * 32768 + (w >> 1) * 16384 + (w & 1) * 4096 + (g >> 1) * 512 + ((n & 7) >> 2) * 256 + ((g & 1) * 2 + pp) * 64 + (n & 3) * 16;
    const unsigned l_ms = lb + L_MS + l * 16;
    const unsigned l_iw = lb + L_IMG + (w >> 1) * IMG_MS + (g & 1) * IMG_PS + (64 * (w & 1) + 16 * (g >> 1) + n) * 8;
    const unsigned l_iq = lb + L_IMG + (n & 1) * IMG_PS + (8 * g + (n >> 2)) * 8;
    const unsigned a_vm = (16 + (h >> 2)) * 16384 + (h & 3) * 2048 + w * 512 + n * 32 + (g & 1) * 16;
    const unsigned a_o = n < 8 ? (8 + (n >> 2) * 4 + (h >> 3)) * 16384 + (h & 7) * 1024 + w * 256 + g * 64 + (n & 3) * 16 : OOB;
    const unsigned a_ox = (((w & 1) * 32 + h) * 4 + w) * 1024 + l * 16;
    asm volatile(
        "s_lshl_b32 s34, s43, 2\n\t"
        "s_add_u32 s34, s34, 0\n\t"
        "s_and_b32 s34, s34, 15\n\t"
        "s_lshl_b32 s34, s34, 19\n\t"
        "buffer_load_dwordx4 v[224:227], v96, s[16:19], s34 offen nt\n\t"
        "s_add_u32 s34, s34, 0x10000\n\t"
        "buffer_load_dwordx4 v[228:231], v96, s[16:19], s34 offen nt\n\t"
        "s_add_u32 s34, s34, 0x10000\n\t"
        "buffer_load_dwordx4 v[232:235], v96, s[16:19], s34 offen nt\n\t"
        "s_add_u32 s34, s34, 0x10000\n\t"
        "buffer_load_dwordx4 v[236:239], v96, s[16:19], s34 offen nt\n\t"
        "s_add_u32 s34, s34, 0x10000\n\t"
        "buffer_load_dwordx4 v[240:243], v96, s[16:19], s34 offen nt\n\t"
        "s_add_u32 s34, s34, 0x10000\n\t"
        "buffer_load_dwordx4 v[244:247], v96, s[16:19], s34 offen nt\n\t"
        "s_add_u32 s34, s34, 0x10000\n\t"
        "buffer_load_dwordx4 v[248:251], v96, s[16:19], s34 offen nt\n\t"
        "s_add_u32 s34, s34, 0x10000\n\t"
        "buffer_load_dwordx4 v[252:255], v96, s[16:19], s34 offen nt\n\t"
        "s_lshl_b32 s34, s43, 2\n\t"
        "s_add_u32 s34, s34, 1\n\t"
        "s_and_b32 s34, s34, 15\n\t"
        "s_lshl_b32 s34, s34, 19\n\t"
        "buffer_load_dwordx4 v[0:3], v96, s[16:19], s34 offen nt\n\t"
        "s_add_u32 s34, s34, 0x10000\n\t"
        "buffer_load_dwordx4 v[4:7], v96, s[16:19], s34 offen nt\n\t"
        "s_add_u32 s34, s34, 0x10000\n\t"
        "buffer_load_dwordx4 v[8:11], v96, s[16:19], s34 offen nt\n\t"
        "s_add_u32 s34, s34, 0x10000\n\t"
        "buffer_load_dwordx4 v[12:15], v96, s[16:19], s34 offen nt\n\t"
        "s_add_u32 s34, s34, 0x10000\n\t"
        "buffer_load_dwordx4 v[16:19], v96, s[16:19], s34 offen nt\n\t"
        "s_add_u32 s34, s34, 0x10000\n\t"
        "buffer_load_dwordx4 v[20:23], v96, s[16:19], s34 offen nt\n\t"
        "s_add_u32 s34, s34, 0x10000\n\t"
        "buffer_load_dwordx4 v[24:27], v96, s[16:19], s34 offen nt\n\t"
        "s_add_u32 s34, s34, 0x10000\n\t"
        "buffer_load_dwordx4 v[28:31], v96, s[16:19], s34 offen nt\n\t"
        "s_lshl_b32 s34, s43, 2\n\t"
        "s_add_u32 s34, s34, 2\n\t"
        "s_and_b32 s34, s34, 15\n\t"
        "s_lshl_b32 s34, s34, 19\n\t"
        "buffer_load_dwordx4 v[32:35], v96, s[16:19], s34 offen nt\n\t"
        "s_add_u32 s34, s34, 0x10000\n\t"
        "buffer_load_dwordx4 v[36:39], v96, s[16:19], s34 offen nt\n\t"
        "s_add_u32 s34, s34, 0x10000\n\t"
        "buffer_load_dwordx4 v[40:43], v96, s[16:19], s34 offen nt\n\t"
        "s_add_u32 s34, s34, 0x10000\n\t"
        "buffer_load_dwordx4 v[44:47], v96, s[16:19], s34 offen nt\n\t"
        "s_add_u32 s34, s34, 0x10000\n\t"
        "buffer_load_dwordx4 v[48:51], v96, s[16:19], s34 offen nt\n\t"
        "s_add_u32 s34, s34, 0x10000\n\t"
        "buffer_load_dwordx4 v[52:55], v96, s[16:19], s34 offen nt\n\t"
        "s_add_u32 s34, s34, 0x10000\n\t"
        "buffer_load_dwordx4 v[56:59], v96, s[16:19], s34 offen nt\n\t"
        "s_add_u32 s34, s34, 0x10000\n\t"
        "buffer_load_dwordx4 v[60:63], v96, s[16:19], s34 offen nt\n\t"
        "s_lshl_b32 s34, s43, 2\n\t"
        "s_add_u32 s34, s34, 3\n\t"
        "s_and_b32 s34, s34, 15\n\t"
        "s_lshl_b32 s34, s34, 19\n\t"
        "buffer_load_dwordx4 v[64:67], v96, s[16:19], s34 offen nt\n\t"
        "s_add_u32 s34, s34, 0x10000\n\t"
        "buffer_load_dwordx4 v[68:71], v96, s[16:19], s34 offen nt\n\t"
        "s_add_u32 s34, s34, 0x10000\n\t"
        "buffer_load_dwordx4 v[72:75], v96, s[16:19], s34 offen nt\n\t"
        "s_add_u32 s34, s34, 0x10000\n\t"
        "buffer_load_dwordx4 v[76:79], v96, s[16:19], s34 offen nt\n\t"
        "s_add_u32 s34, s34, 0x10000\n\t"
        "buffer_load_dwordx4 v[80:83], v96, s[16:19], s34 offen nt\n\t"
        "s_add_u32 s34, s34, 0x10000\n\t"
        "buffer_load_dwordx4 v[84:87], v96, s[16:19], s34 offen nt\n\t"
        "s_add_u32 s34, s34, 0x10000\n\t"
        "buffer_load_dwordx4 v[88:91], v96, s[16:19], s34 offen nt\n\t"
        "s_add_u32 s34, s34, 0x10000\n\t"
        "buffer_load_dwordx4 v[92:95], v96, s[16:19], s34 offen nt\n\t"
        "s_lshl_b32 s34, s43, 2\n\t"
        "s_add_u32 s34, s34, 4\n\t"
        "s_and_b32 s34, s34, 15\n\t"
        "s_lshl_b32 s34, s34, 19\n\t"
        "buffer_load_dwordx4 a[96:99], v96, s[16:19], s34 offen nt\n\t"
        "s_add_u32 s34, s34, 0x10000\n\t"
        "buffer_load_dwordx4 a[100:103], v96, s[16:19], s34 offen nt\n\t"
        "s_add_u32 s34, s34, 0x10000\n\t"
        "buffer_load_dwordx4 a[104:107], v96, s[16:19], s34 offen nt\n\t"
        "s_add_u32 s34, s34, 0x10000\n\t"
        "buffer_load_dwordx4 a[108:111], v96, s[16:19], s34 offen nt\n\t"
        "s_add_u32 s34, s34, 0x10000\n\t"
        "buffer_load_dwordx4 a[112:115], v96, s[16:19], s34 offen nt\n\t"
        "s_add_u32 s34, s34, 0x10000\n\t"
        "buffer_load_dwordx4 a[116:119], v96, s[16:19], s34 offen nt\n\t"
        "s_add_u32 s34, s34, 0x10000\n\t"
        "buffer_load_dwordx4 a[120:123], v96, s[16:19], s34 offen nt\n\t"
        "s_add_u32 s34, s34, 0x10000\n\t"
        "buffer_load_dwordx4 a[124:127], v96, s[16:19], s34 offen nt\n\t"
        "s_lshl_b32 s34, s43, 2\n\t"
        "s_add_u32 s34, s34, 5\n\t"
        "s_and_b32 s34, s34, 15\n\t"
        "s_lshl_b32 s34, s34, 19\n\t"
        "buffer_load_dwordx4 a[128:131], v96, s[16:19], s34 offen nt\n\t"
        "s_add_u32 s34, s34, 0x10000\n\t"
        "buffer_load_dwordx4 a[132:135], v96, s[16:19], s34 offen nt\n\t"
        "s_add_u32 s34, s34, 0x10000\n\t"
        "buffer_load_dwordx4 a[136:139], v96, s[16:19], s34 offen nt\n\t"
        "s_add_u32 s34, s34, 0x10000\n\t"
        "buffer_load_dwordx4 a[140:143], v96, s[16:19], s34 offen nt\n\t"
        "s_add_u32 s34, s34, 0x10000\n\t"
        "buffer_load_dwordx4 a[144:147], v96, s[16:19], s34 offen nt\n\t"
        "s_add_u32 s34, s34, 0x10000\n\t"
        "buffer_load_dwordx4 a[148:151], v96, s[16:19], s34 offen nt\n\t"
        "s_add_u32 s34, s34, 0x10000\n\t"
        "buffer_load_dwordx4 a[152:155], v96, s[16:19], s34 offen nt\n\t"
        "s_add_u32 s34, s34, 0x10000\n\t"
        "buffer_load_dwordx4 a[156:159], v96, s[16:19], s34 offen nt\n\t"
        "s_lshl_b32 s34, s43, 2\n\t"
        "s_add_u32 s34, s34, 6\n\t"
        "s_and_b32 s34, s34, 15\n\t"
        "s_lshl_b32 s34, s34, 19\n\t"
        "buffer_load_dwordx4 a[160:163], v96, s[16:19], s34 offen nt\n\t"
        "s_add_u32 s34, s34, 0x10000\n\t"
        "buffer_load_dwordx4 a[164:167], v96, s[16:19], s34 offen nt\n\t"
        "s_add_u32 s34, s34, 0x10000\n\t"
        "buffer_load_dwordx4 a[168:171], v96, s[16:19], s34 offen nt\n\t"
        "s_add_u32 s34, s34, 0x10000\n\t"
        "buffer_load_dwordx4 a[172:175], v96, s[16:19], s34 offen nt\n\t"
        "s_add_u32 s34, s34, 0x10000\n\t"
        "buffer_load_dwordx4 a[176:179], v96, s[16:19], s34 offen nt\n\t"
        "s_add_u32 s34, s34, 0x10000\n\t"
        "buffer_load_dwordx4 a[180:183], v96, s[16:19], s34 offen nt\n\t"
        "s_add_u32 s34, s34, 0x10000\n\t"
        "buffer_load_dwordx4 a[184:187], v96, s[16:19], s34 offen nt\n\t"
        "s_add_u32 s34, s34, 0x10000\n\t"
        "buffer_load_dwordx4 a[188:191], v96, s[16:19], s34 offen nt\n\t"
        "v_cmp_le_i32_e64 s[60:61], 0, v126\n\t"
        "v_cmp_le_i32_e64 s[62:63], 1, v126\n\t"
        "v_cmp_le_i32_e64 s[64:65], 2, v126\n\t"
        "v_cmp_le_i32_e64 s[66:67], 3, v126\n\t"
        "v_cmp_le_i32_e64 s[68:69], 16, v126\n\t"
        "v_cmp_le_i32_e64 s[70:71], 17, v126\n\t"
        "v_cmp_le_i32_e64 s[72:73], 18, v126\n\t"
        "v_cmp_le_i32_e64 s[74:75], 19, v126\n\t"
        "s_mul_i32 s35, s43, 10240\n\t"
        "v_add_u32_e32 v126, s35, v124\n\t"
        "s_waitcnt vmcnt(48)\n\t"
        "ds_write_b128 v97, v[224:227] offset:0\n\t"
        "ds_write_b128 v98, v[228:231] offset:1024\n\t"
        "ds_write_b128 v97, v[232:235] offset:2048\n\t"
        "ds_write_b128 v98, v[236:239] offset:3072\n\t"
        "ds_write_b128 v97, v[240:243] offset:4096\n\t"
        "ds_write_b128 v98, v[244:247] offset:5120\n\t"
        "ds_write_b128 v97, v[248:251] offset:6144\n\t"
        "ds_write_b128 v98, v[252:255] offset:7168\n\t"
        "ds_read_b64_tr_b16 a[0:1], v99 offset:0\n\t"
        "ds_read_b64_tr_b16 a[2:3], v99 offset:256\n\t"
        "ds_read_b64_tr_b16 a[4:5], v99 offset:4096\n\t"
        "ds_read_b64_tr_b16 a[6:7], v99 offset:4352\n\t"
        "s_waitcnt lgkmcnt(8)\n\t"
        "ds_read_b64_tr_b16 a[8:9], v100 offset:0\n\t"
        "ds_read_b64_tr_b16 a[10:11], v100 offset:256\n\t"
        "ds_read_b64_tr_b16 a[12:13], v100 offset:4096\n\t"
        "ds_read_b64_tr_b16 a[14:15], v100 offset:4352\n\t"
        "s_waitcnt lgkmcnt(8)\n\t"
        "ds_read_b64_tr_b16 a[16:17], v101 offset:0\n\t"
        "ds_read_b64_tr_b16 a[18:19], v101 offset:256\n\t"
        "ds_read_b64_tr_b16 a[20:21], v101 offset:4096\n\t"
        "ds_read_b64_tr_b16 a[22:23], v101 offset:4352\n\t"
        "s_waitcnt lgkmcnt(8)\n\t"
        "ds_read_b64_tr_b16 v[104:105], v102 offset:0\n\t"
        "ds_read_b64_tr_b16 v[106:107], v102 offset:256\n\t"
        "ds_read_b64_tr_b16 v[108:109], v102 offset:4096\n\t"
        "ds_read_b64_tr_b16 v[110:111], v102 offset:4352\n\t"
        "s_waitcnt lgkmcnt(0)\n\t"
        "ds_write_b128 v117, v[104:107] offset:0\n\t"
        "ds_write_b128 v117, v[108:111] offset:1024\n\t"
        "s_lshl_b32 s34, s43, 2\n\t"
        "s_add_u32 s34, s34, 7\n\t"
        "s_and_b32 s34, s34, 15\n\t"
        "s_lshl_b32 s34, s34, 19\n\t"
        "buffer_load_dwordx4 a[192:195], v96, s[16:19], s34 offen nt\n\t"
        "s_add_u32 s34, s34, 0x10000\n\t"
        "buffer_load_dwordx4 a[196:199], v96, s[16:19], s34 offen nt\n\t"
        "s_add_u32 s34, s34, 0x10000\n\t"
        "buffer_load_dwordx4 a[200:203], v96, s[16:19], s34 offen nt\n\t"
        "s_add_u32 s34, s34, 0x10000\n\t"
        "buffer_load_dwordx4 a[204:207], v96, s[16:19], s34 offen nt\n\t"
        "s_add_u32 s34, s34, 0x10000\n\t"
        "buffer_load_dwordx4 a[208:211], v96, s[16:19], s34 offen nt\n\t"
        "s_add_u32 s34, s34, 0x10000\n\t"
        "buffer_load_dwordx4 a[212:215], v96, s[16:19], s34 offen nt\n\t"
        "s_add_u32 s34, s34, 0x10000\n\t"
        "buffer_load_dwordx4 a[216:219], v96, s[16:19], s34 offen nt\n\t"
        "s_add_u32 s34, s34, 0x10000\n\t"
        "buffer_load_dwordx4 a[220:223], v96, s[16:19], s34 offen nt\n\t"
        "s_waitcnt vmcnt(48)\n\t"
        "ds_write_b128 v97, v[0:3] offset:0\n\t"
        "ds_write_b128 v98, v[4:7] offset:1024\n\t"
        "ds_write_b128 v97, v[8:11] offset:2048\n\t"
        "ds_write_b128 v98, v[12:15] offset:3072\n\t"
        "ds_write_b128 v97, v[16:19] offset:4096\n\t"
        "ds_write_b128 v98, v[20:23] offset:5120\n\t"
        "ds_write_b128 v97, v[24:27] offset:6144\n\t"
        "ds_write_b128 v98, v[28:31] offset:7168\n\t"
        "ds_read_b64_tr_b16 a[24:25], v99 offset:0\n\t"
        "ds_read_b64_tr_b16 a[26:27], v99 offset:256\n\t"
        "ds_read_b64_tr_b16 a[28:29], v99 offset:4096\n\t"
        "ds_read_b64_tr_b16 a[30:31], v99 offset:4352\n\t"
        "s_waitcnt lgkmcnt(8)\n\t"
        "ds_read_b64_tr_b16 a[32:33], v100 offset:0\n\t"
        "ds_read_b64_tr_b16 a[34:35], v100 offset:256\n\t"
        "ds_read_b64_tr_b16 a[36:37], v100 offset:4096\n\t"
        "ds_read_b64_tr_b16 a[38:39], v100 offset:4352\n\t"
        "s_waitcnt lgkmcnt(8)\n\t"
        "ds_read_b64_tr_b16 a[40:41], v101 offset:0\n\t"
        "ds_read_b64_tr_b16 a[42:43], v101 offset:256\n\t"
        "ds_read_b64_tr_b16 a[44:45], v101 offset:4096\n\t"
        "ds_read_b64_tr_b16 a[46:47], v101 offset:4352\n\t"
        "s_waitcnt lgkmcnt(8)\n\t"
        "ds_read_b64_tr_b16 v[104:105], v102 offset:0\n\t"
        "ds_read_b64_tr_b16 v[106:107], v102 offset:256\n\t"
        "ds_read_b64_tr_b16 v[108:109], v102 offset:4096\n\t"
        "ds_read_b64_tr_b16 v[110:111], v102 offset:4352\n\t"
        "s_waitcnt lgkmcnt(0)\n\t"
        "ds_write_b128 v117, v[104:107] offset:2048\n\t"
        "ds_write_b128 v117, v[108:111] offset:3072\n\t"
        "s_lshl_b32 s34, s43, 2\n\t"
        "s_add_u32 s34, s34, 8\n\t"
        "s_and_b32 s34, s34, 15\n\t"
        "s_lshl_b32 s34, s34, 19\n\t"
        "buffer_load_dwordx4 a[224:227], v96, s[16:19], s34 offen nt\n\t"
        "s_add_u32 s34, s34, 0x10000\n\t"
        "buffer_load_dwordx4 a[228:231], v96, s[16:19], s34 offen nt\n\t"
        "s_add_u32 s34, s34, 0x10000\n\t"
        "buffer_load_dwordx4 a[232:235], v96, s[16:19], s34 offen nt\n\t"
        "s_add_u32 s34, s34, 0x10000\n\t"
        "buffer_load_dwordx4 a[236:239], v96, s[16:19], s34 offen nt\n\t"
        "s_add_u32 s34, s34, 0x10000\n\t"
        "buffer_load_dwordx4 a[240:243], v96, s[16:19], s34 offen nt\n\t"
        "s_add_u32 s34, s34, 0x10000\n\t"
        "buffer_load_dwordx4 a[244:247], v96, s[16:19], s34 offen nt\n\t"
        "s_add_u32 s34, s34, 0x10000\n\t"
        "buffer_load_dwordx4 a[248:251], v96, s[16:19], s34 offen nt\n\t"
        "s_add_u32 s34, s34, 0x10000\n\t"
        "buffer_load_dwordx4 a[252:255], v96, s[16:19], s34 offen nt\n\t"
        "s_waitcnt vmcnt(48)\n\t"
        "ds_write_b128 v97, v[32:35] offset:0\n\t"
        "ds_write_b128 v98, v[36:39] offset:1024\n\t"
        "ds_write_b128 v97, v[40:43] offset:2048\n\t"
        "ds_write_b128 v98, v[44:47] offset:3072\n\t"
        "ds_write_b128 v97, v[48:51] offset:4096\n\t"
        "ds_write_b128 v98, v[52:55] offset:5120\n\t"
        "ds_write_b128 v97, v[56:59] offset:6144\n\t"
        "ds_write_b128 v98, v[60:63] offset:7168\n\t"
        "ds_read_b64_tr_b16 a[48:49], v99 offset:0\n\t"
        "ds_read_b64_tr_b16 a[50:51], v99 offset:256\n\t"
        "ds_read_b64_tr_b16 a[52:53], v99 offset:4096\n\t"
        "ds_read_b64_tr_b16 a[54:55], v99 offset:4352\n\t"
        "s_waitcnt lgkmcnt(8)\n\t"
        "ds_read_b64_tr_b16 a[56:57], v100 offset:0\n\t"
        "ds_read_b64_tr_b16 a[58:59], v100 offset:256\n\t"
        "ds_read_b64_tr_b16 a[60:61], v100 offset:4096\n\t"
        "ds_read_b64_tr_b16 a[62:63], v100 offset:4352\n\t"
        "s_waitcnt lgkmcnt(8)\n\t"
        "ds_read_b64_tr_b16 a[64:65], v101 offset:0\n\t"
        "ds_read_b64_tr_b16 a[66:67], v101 offset:256\n\t"
        "ds_read_b64_tr_b16 a[68:69], v101 offset:4096\n\t"
        "ds_read_b64_tr_b16 a[70:71], v101 offset:4352\n\t"
        "s_waitcnt lgkmcnt(8)\n\t"
        "ds_read_b64_tr_b16 v[104:105], v102 offset:0\n\t"
        "ds_read_b64_tr_b16 v[106:107], v102 offset:256\n\t"
        "ds_read_b64_tr_b16 v[108:109], v102 offset:4096\n\t"
        "ds_read_b64_tr_b16 v[110:111], v102 offset:4352\n\t"
        "s_waitcnt lgkmcnt(0)\n\t"
        "ds_write_b128 v117, v[104:107] offset:4096\n\t"
        "ds_write_b128 v117, v[108:111] offset:5120\n\t"
        "s_lshl_b32 s34, s43, 2\n\t"
        "s_add_u32 s34, s34, 9\n\t"
        "s_and_b32 s34, s34, 15\n\t"
        "s_lshl_b32 s34, s34, 19\n\t"
        "buffer_load_dwordx4 v[128:131], v96, s[16:19], s34 offen nt\n\t"
        "s_add_u32 s34, s34, 0x10000\n\t"
        "buffer_load_dwordx4 v[132:135], v96, s[16:19], s34 offen nt\n\t"
        "s_add_u32 s34, s34, 0x10000\n\t"
        "buffer_load_dwordx4 v[136:139], v96, s[16:19], s34 offen nt\n\t"
        "s_add_u32 s34, s34, 0x10000\n\t"
        "buffer_load_dwordx4 v[140:143], v96, s[16:19], s34 offen nt\n\t"
        "s_add_u32 s34, s34, 0x10000\n\t"
        "buffer_load_dwordx4 v[144:147], v96, s[16:19], s34 offen nt\n\t"
        "s_add_u32 s34, s34, 0x10000\n\t"
        "buffer_load_dwordx4 v[148:151], v96, s[16:19], s34 offen nt\n\t"
        "s_add_u32 s34, s34, 0x10000\n\t"
        "buffer_load_dwordx4 v[152:155], v96, s[16:19], s34 offen nt\n\t"
        "s_add_u32 s34, s34, 0x10000\n\t"
        "buffer_load_dwordx4 v[156:159], v96, s[16:19], s34 offen nt\n\t"
        "s_waitcnt vmcnt(48)\n\t"
        "ds_write_b128 v97, v[64:67] offset:0\n\t"
        "ds_write_b128 v98, v[68:71] offset:1024\n\t"
        "ds_write_b128 v97, v[72:75] offset:2048\n\t"
        "ds_write_b128 v98, v[76:79] offset:3072\n\t"
        "ds_write_b128 v97, v[80:83] offset:4096\n\t"
        "ds_write_b128 v98, v[84:87] offset:5120\n\t"
        "ds_write_b128 v97, v[88:91] offset:6144\n\t"
        "ds_write_b128 v98, v[92:95] offset:7168\n\t"
        "ds_read_b64_tr_b16 a[72:73], v99 offset:0\n\t"
        "ds_read_b64_tr_b16 a[74:75], v99 offset:256\n\t"
        "ds_read_b64_tr_b16 a[76:77], v99 offset:4096\n\t"
        "ds_read_b64_tr_b16 a[78:79], v99 offset:4352\n\t"
        "s_waitcnt lgkmcnt(8)\n\t"
        "ds_read_b64_tr_b16 a[80:81], v100 offset:0\n\t"
        "ds_read_b64_tr_b16 a[82:83], v100 offset:256\n\t"
        "ds_read_b64_tr_b16 a[84:85], v100 offset:4096\n\t"
        "ds_read_b64_tr_b16 a[86:87], v100 offset:4352\n\t"
        "s_waitcnt lgkmcnt(8)\n\t"
        "ds_read_b64_tr_b16 a[88:89], v101 offset:0\n\t"
        "ds_read_b64_tr_b16 a[90:91], v101 offset:256\n\t"
        "ds_read_b64_tr_b16 a[92:93], v101 offset:4096\n\t"
        "ds_read_b64_tr_b16 a[94:95], v101 offset:4352\n\t"
        "s_waitcnt lgkmcnt(8)\n\t"
        "ds_read_b64_tr_b16 v[104:105], v102 offset:0\n\t"
        "ds_read_b64_tr_b16 v[106:107], v102 offset:256\n\t"
        "ds_read_b64_tr_b16 v[108:109], v102 offset:4096\n\t"
        "ds_read_b64_tr_b16 v[110:111], v102 offset:4352\n\t"
        "s_waitcnt lgkmcnt(0)\n\t"
        "ds_write_b128 v117, v[104:107] offset:6144\n\t"
        "ds_write_b128 v117, v[108:111] offset:7168\n\t"
        "s_lshl_b32 s34, s43, 2\n\t"
        "s_add_u32 s34, s34, 10\n\t"
        "s_and_b32 s34, s34, 15\n\t"
        "s_lshl_b32 s34, s34, 19\n\t"
        "buffer_load_dwordx4 v[160:163], v96, s[16:19], s34 offen nt\n\t"
        "s_add_u32 s34, s34, 0x10000\n\t"
        "buffer_load_dwordx4 v[164:167], v96, s[16:19], s34 offen nt\n\t"
        "s_add_u32 s34, s34, 0x10000\n\t"
        "buffer_load_dwordx4 v[168:171], v96, s[16:19], s34 offen nt\n\t"
        "s_add_u32 s34, s34, 0x10000\n\t"
        "buffer_load_dwordx4 v[172:175], v96, s[16:19], s34 offen nt\n\t"
        "s_add_u32 s34, s34, 0x10000\n\t"
        "buffer_load_dwordx4 v[176:179], v96, s[16:19], s34 offen nt\n\t"
        "s_add_u32 s34, s34, 0x10000\n\t"
        "buffer_load_dwordx4 v[180:183], v96, s[16:19], s34 offen nt\n\t"
        "s_add_u32 s34, s34, 0x10000\n\t"
        "buffer_load_dwordx4 v[184:187], v96, s[16:19], s34 offen nt\n\t"
        "s_add_u32 s34, s34, 0x10000\n\t"
        "buffer_load_dwordx4 v[188:191], v96, s[16:19], s34 offen nt\n\t"
        "s_waitcnt vmcnt(48)\n\t"
        "ds_write_b128 v97, a[96:99] offset:0\n\t"
        "ds_write_b128 v98, a[100:103] offset:1024\n\t"
        "ds_write_b128 v97, a[104:107] offset:2048\n\t"
        "ds_write_b128 v98, a[108:111] offset:3072\n\t"
        "ds_write_b128 v97, a[112:115] offset:4096\n\t"
        "ds_write_b128 v98, a[116:119] offset:5120\n\t"
        "ds_write_b128 v97, a[120:123] offset:6144\n\t"
        "ds_write_b128 v98, a[124:127] offset:7168\n\t"
        "ds_read_b64_tr_b16 a[96:97], v99 offset:0\n\t"
        "ds_read_b64_tr_b16 a[98:99], v99 offset:256\n\t"
        "ds_read_b64_tr_b16 a[100:101], v99 offset:4096\n\t"
        "ds_read_b64_tr_b16 a[102:103], v99 offset:4352\n\t"
        "s_waitcnt lgkmcnt(8)\n\t"
        "ds_read_b64_tr_b16 a[104:105], v100 offset:0\n\t"
        "ds_read_b64_tr_b16 a[106:107], v100 offset:256\n\t"
        "ds_read_b64_tr_b16 a[108:109], v100 offset:4096\n\t"
        "ds_read_b64_tr_b16 a[110:111], v100 offset:4352\n\t"
        "s_waitcnt lgkmcnt(8)\n\t"
        "ds_read_b64_tr_b16 a[112:113], v101 offset:0\n\t"
        "ds_read_b64_tr_b16 a[114:115], v101 offset:256\n\t"
        "ds_read_b64_tr_b16 a[116:117], v101 offset:4096\n\t"
        "ds_read_b64_tr_b16 a[118:119], v101 offset:4352\n\t"
        "s_waitcnt lgkmcnt(8)\n\t"
        "ds_read_b64_tr_b16 v[104:105], v102 offset:0\n\t"
        "ds_read_b64_tr_b16 v[106:107], v102 offset:256\n\t"
        "ds_read_b64_tr_b16 v[108:109], v102 offset:4096\n\t"
        "ds_read_b64_tr_b16 v[110:111], v102 offset:4352\n\t"
        "s_waitcnt lgkmcnt(0)\n\t"
        "ds_write_b128 v117, v[104:107] offset:8192\n\t"
        "ds_write_b128 v117, v[108:111] offset:9216\n\t"
        "s_lshl_b32 s34, s43, 2\n\t"
        "s_add_u32 s34, s34, 11\n\t"
        "s_and_b32 s34, s34, 15\n\t"
        "s_lshl_b32 s34, s34, 19\n\t"
        "buffer_load_dwordx4 v[192:195], v96, s[16:19], s34 offen nt\n\t"
        "s_add_u32 s34, s34, 0x10000\n\t"
        "buffer_load_dwordx4 v[196:199], v96, s[16:19], s34 offen nt\n\t"
        "s_add_u32 s34, s34, 0x10000\n\t"
        "buffer_load_dwordx4 v[200:203], v96, s[16:19], s34 offen nt\n\t"
        "s_add_u32 s34, s34, 0x10000\n\t"
        "buffer_load_dwordx4 v[204:207], v96, s[16:19], s34 offen nt\n\t"
        "s_add_u32 s34, s34, 0x10000\n\t"
        "buffer_load_dwordx4 v[208:211], v96, s[16:19], s34 offen nt\n\t"
        "s_add_u32 s34, s34, 0x10000\n\t"
        "buffer_load_dwordx4 v[212:215], v96, s[16:19], s34 offen nt\n\t"
        "s_add_u32 s34, s34, 0x10000\n\t"
        "buffer_load_dwordx4 v[216:219], v96, s[16:19], s34 offen nt\n\t"
        "s_add_u32 s34, s34, 0x10000\n\t"
        "buffer_load_dwordx4 v[220:223], v96, s[16:19], s34 offen nt\n\t"
        "s_waitcnt vmcnt(48)\n\t"
        "ds_write_b128 v97, a[128:131] offset:0\n\t"
        "ds_write_b128 v98, a[132:135] offset:1024\n\t"
        "ds_write_b128 v97, a[136:139] offset:2048\n\t"
        "ds_write_b128 v98, a[140:143] offset:3072\n\t"
        "ds_write_b128 v97, a[144:147] offset:4096\n\t"
        "ds_write_b128 v98, a[148:151] offset:5120\n\t"
        "ds_write_b128 v97, a[152:155] offset:6144\n\t"
        "ds_write_b128 v98, a[156:159] offset:7168\n\t"
        "ds_read_b64_tr_b16 a[120:121], v99 offset:0\n\t"
        "ds_read_b64_tr_b16 a[122:123], v99 offset:256\n\t"
        "ds_read_b64_tr_b16 a[124:125], v99 offset:4096\n\t"
        "ds_read_b64_tr_b16 a[126:127], v99 offset:4352\n\t"
        "s_waitcnt lgkmcnt(8)\n\t"
        "ds_read_b64_tr_b16 a[128:129], v100 offset:0\n\t"
        "ds_read_b64_tr_b16 a[130:131], v100 offset:256\n\t"
        "ds_read_b64_tr_b16 a[132:133], v100 offset:4096\n\t"
        "ds_read_b64_tr_b16 a[134:135], v100 offset:4352\n\t"
        "s_waitcnt lgkmcnt(8)\n\t"
        "ds_read_b64_tr_b16 a[136:137], v101 offset:0\n\t"
        "ds_read_b64_tr_b16 a[138:139], v101 offset:256\n\t"
        "ds_read_b64_tr_b16 a[140:141], v101 offset:4096\n\t"
        "ds_read_b64_tr_b16 a[142:143], v101 offset:4352\n\t"
        "s_waitcnt lgkmcnt(8)\n\t"
        "ds_read_b64_tr_b16 v[104:105], v102 offset:0\n\t"
        "ds_read_b64_tr_b16 v[106:107], v102 offset:256\n\t"
        "ds_read_b64_tr_b16 v[108:109], v102 offset:4096\n\t"
        "ds_read_b64_tr_b16 v[110:111], v102 offset:4352\n\t"
        "s_waitcnt lgkmcnt(0)\n\t"
        "ds_write_b128 v117, v[104:107] offset:10240\n\t"
        "ds_write_b128 v117, v[108:111] offset:11264\n\t"
        "s_lshl_b32 s34, s43, 2\n\t"
        "s_add_u32 s34, s34, 12\n\t"
        "s_and_b32 s34, s34, 15\n\t"
        "s_lshl_b32 s34, s34, 19\n\t"
        "buffer_load_dwordx4 v[224:227], v96, s[16:19], s34 offen nt\n\t"
        "s_add_u32 s34, s34, 0x10000\n\t"
        "buffer_load_dwordx4 v[228:231], v96, s[16:19], s34 offen nt\n\t"
        "s_add_u32 s34, s34, 0x10000\n\t"
        "buffer_load_dwordx4 v[232:235], v96, s[16:19], s34 offen nt\n\t"
        "s_add_u32 s34, s34, 0x10000\n\t"
        "buffer_load_dwordx4 v[236:239], v96, s[16:19], s34 offen nt\n\t"
        "s_add_u32 s34, s34, 0x10000\n\t"
        "buffer_load_dwordx4 v[240:243], v96, s[16:19], s34 offen nt\n\t"
        "s_add_u32 s34, s34, 0x10000\n\t"
        "buffer_load_dwordx4 v[244:247], v96, s[16:19], s34 offen nt\n\t"
        "s_add_u32 s34, s34, 0x10000\n\t"
        "buffer_load_dwordx4 v[248:251], v96, s[16:19], s34 offen nt\n\t"
        "s_add_u32 s34, s34, 0x10000\n\t"
        "buffer_load_dwordx4 v[252:255], v96, s[16:19], s34 offen nt\n\t"
        "s_waitcnt vmcnt(48)\n\t"
        "ds_write_b128 v97, a[160:163] offset:0\n\t"
        "ds_write_b128 v98, a[164:167] offset:1024\n\t"
        "ds_write_b128 v97, a[168:171] offset:2048\n\t"
        "ds_write_b128 v98, a[172:175] offset:3072\n\t"
        "ds_write_b128 v97, a[176:179] offset:4096\n\t"
        "ds_write_b128 v98, a[180:183] offset:5120\n\t"
        "ds_write_b128 v97, a[184:187] offset:6144\n\t"
        "ds_write_b128 v98, a[188:191] offset:7168\n\t"
        "ds_read_b64_tr_b16 a[144:145], v99 offset:0\n\t"
        "ds_read_b64_tr_b16 a[146:147], v99 offset:256\n\t"
        "ds_read_b64_tr_b16 a[148:149], v99 offset:4096\n\t"
        "ds_read_b64_tr_b16 a[150:151], v99 offset:4352\n\t"
        "s_waitcnt lgkmcnt(8)\n\t"
        "ds_read_b64_tr_b16 a[152:153], v100 offset:0\n\t"
        "ds_read_b64_tr_b16 a[154:155], v100 offset:256\n\t"
        "ds_read_b64_tr_b16 a[156:157], v100 offset:4096\n\t"
        "ds_read_b64_tr_b16 a[158:159], v100 offset:4352\n\t"
        "s_waitcnt lgkmcnt(8)\n\t"
        "ds_read_b64_tr_b16 a[160:161], v101 offset:0\n\t"
        "ds_read_b64_tr_b16 a[162:163], v101 offset:256\n\t"
        "ds_read_b64_tr_b16 a[164:165], v101 offset:4096\n\t"
        "ds_read_b64_tr_b16 a[166:167], v101 offset:4352\n\t"
        "s_waitcnt lgkmcnt(8)\n\t"
        "ds_read_b64_tr_b16 v[104:105], v102 offset:0\n\t"
        "ds_read_b64_tr_b16 v[106:107], v102 offset:256\n\t"
        "ds_read_b64_tr_b16 v[108:109], v102 offset:4096\n\t"
        "ds_read_b64_tr_b16 v[110:111], v102 offset:4352\n\t"
        "s_waitcnt lgkmcnt(0)\n\t"
        "ds_write_b128 v117, v[104:107] offset:12288\n\t"
        "ds_write_b128 v117, v[108:111] offset:13312\n\t"
        "s_lshl_b32 s34, s43, 2\n\t"
        "s_add_u32 s34, s34, 13\n\t"
        "s_and_b32 s34, s34, 15\n\t"
        "s_lshl_b32 s34, s34, 19\n\t"
        "buffer_load_dwordx4 v[0:3], v96, s[16:19], s34 offen nt\n\t"
        "s_add_u32 s34, s34, 0x10000\n\t"
        "buffer_load_dwordx4 v[4:7], v96, s[16:19], s34 offen nt\n\t"
        "s_add_u32 s34, s34, 0x10000\n\t"
        "buffer_load_dwordx4 v[8:11], v96, s[16:19], s34 offen nt\n\t"
        "s_add_u32 s34, s34, 0x10000\n\t"
        "buffer_load_dwordx4 v[12:15], v96, s[16:19], s34 offen nt\n\t"
        "s_add_u32 s34, s34, 0x10000\n\t"
        "buffer_load_dwordx4 v[16:19], v96, s[16:19], s34 offen nt\n\t"
        "s_add_u32 s34, s34, 0x10000\n\t"
        "buffer_load_dwordx4 v[20:23], v96, s[16:19], s34 offen nt\n\t"
        "s_add_u32 s34, s34, 0x10000\n\t"
        "buffer_load_dwordx4 v[24:27], v96, s[16:19], s34 offen nt\n\t"
        "s_add_u32 s34, s34, 0x10000\n\t"
        "buffer_load_dwordx4 v[28:31], v96, s[16:19], s34 offen nt\n\t"
        "s_waitcnt vmcnt(48)\n\t"
        "ds_write_b128 v97, a[192:195] offset:0\n\t"
        "ds_write_b128 v98, a[196:199] offset:1024\n\t"
        "ds_write_b128 v97, a[200:203] offset:2048\n\t"
        "ds_write_b128 v98, a[204:207] offset:3072\n\t"
        "ds_write_b128 v97, a[208:211] offset:4096\n\t"
        "ds_write_b128 v98, a[212:215] offset:5120\n\t"
        "ds_write_b128 v97, a[216:219] offset:6144\n\t"
        "ds_write_b128 v98, a[220:223] offset:7168\n\t"
        "ds_read_b64_tr_b16 a[168:169], v99 offset:0\n\t"
        "ds_read_b64_tr_b16 a[170:171], v99 offset:256\n\t"
        "ds_read_b64_tr_b16 a[172:173], v99 offset:4096\n\t"
        "ds_read_b64_tr_b16 a[174:175], v99 offset:4352\n\t"
        "s_waitcnt lgkmcnt(8)\n\t"
        "ds_read_b64_tr_b16 a[176:177], v100 offset:0\n\t"
        "ds_read_b64_tr_b16 a[178:179], v100 offset:256\n\t"
        "ds_read_b64_tr_b16 a[180:181], v100 offset:4096\n\t"
        "ds_read_b64_tr_b16 a[182:183], v100 offset:4352\n\t"
        "s_waitcnt lgkmcnt(8)\n\t"
        "ds_read_b64_tr_b16 a[184:185], v101 offset:0\n\t"
        "ds_read_b64_tr_b16 a[186:187], v101 offset:256\n\t"
        "ds_read_b64_tr_b16 a[188:189], v101 offset:4096\n\t"
        "ds_read_b64_tr_b16 a[190:191], v101 offset:4352\n\t"
        "s_waitcnt lgkmcnt(8)\n\t"
        "ds_read_b64_tr_b16 v[104:105], v102 offset:0\n\t"
        "ds_read_b64_tr_b16 v[106:107], v102 offset:256\n\t"
        "ds_read_b64_tr_b16 v[108:109], v102 offset:4096\n\t"
        "ds_read_b64_tr_b16 v[110:111], v102 offset:4352\n\t"
        "s_waitcnt lgkmcnt(0)\n\t"
        "ds_write_b128 v117, v[104:107] offset:14336\n\t"
        "ds_write_b128 v117, v[108:111] offset:15360\n\t"
        "s_lshl_b32 s34, s43, 2\n\t"
        "s_add_u32 s34, s34, 14\n\t"
        "s_and_b32 s34, s34, 15\n\t"
        "s_lshl_b32 s34, s34, 19\n\t"
        "buffer_load_dwordx4 v[32:35], v96, s[16:19], s34 offen nt\n\t"
        "s_add_u32 s34, s34, 0x10000\n\t"
        "buffer_load_dwordx4 v[36:39], v96, s[16:19], s34 offen nt\n\t"
        "s_add_u32 s34, s34, 0x10000\n\t"
        "buffer_load_dwordx4 v[40:43], v96, s[16:19], s34 offen nt\n\t"
        "s_add_u32 s34, s34, 0x10000\n\t"
        "buffer_load_dwordx4 v[44:47], v96, s[16:19], s34 offen nt\n\t"
        "s_add_u32 s34, s34, 0x10000\n\t"
        "buffer_load_dwordx4 v[48:51], v96, s[16:19], s34 offen nt\n\t"
        "s_add_u32 s34, s34, 0x10000\n\t"
        "buffer_load_dwordx4 v[52:55], v96, s[16:19], s34 offen nt\n\t"
        "s_add_u32 s34, s34, 0x10000\n\t"
        "buffer_load_dwordx4 v[56:59], v96, s[16:19], s34 offen nt\n\t"
        "s_add_u32 s34, s34, 0x10000\n\t"
        "buffer_load_dwordx4 v[60:63], v96, s[16:19], s34 offen nt\n\t"
        "s_waitcnt vmcnt(48)\n\t"
        "ds_write_b128 v97, a[224:227] offset:0\n\t"
        "ds_write_b128 v98, a[228:231] offset:1024\n\t"
        "ds_write_b128 v97, a[232:235] offset:2048\n\t"
        "ds_write_b128 v98, a[236:239] offset:3072\n\t"
        "ds_write_b128 v97, a[240:243] offset:4096\n\t"
        "ds_write_b128 v98, a[244:247] offset:5120\n\t"
        "ds_write_b128 v97, a[248:251] offset:6144\n\t"
        "ds_write_b128 v98, a[252:255] offset:7168\n\t"
        "ds_read_b64_tr_b16 a[192:193], v99 offset:0\n\t"
        "ds_read_b64_tr_b16 a[194:195], v99 offset:256\n\t"
        "ds_read_b64_tr_b16 a[196:197], v99 offset:4096\n\t"
        "ds_read_b64_tr_b16 a[198:199], v99 offset:4352\n\t"
        "s_waitcnt lgkmcnt(8)\n\t"
        "ds_read_b64_tr_b16 a[200:201], v100 offset:0\n\t"
        "ds_read_b64_tr_b16 a[202:203], v100 offset:256\n\t"
        "ds_read_b64_tr_b16 a[204:205], v100 offset:4096\n\t"
        "ds_read_b64_tr_b16 a[206:207], v100 offset:4352\n\t"
        "s_waitcnt lgkmcnt(8)\n\t"
        "ds_read_b64_tr_b16 a[208:209], v101 offset:0\n\t"
        "ds_read_b64_tr_b16 a[210:211], v101 offset:256\n\t"
        "ds_read_b64_tr_b16 a[212:213], v101 offset:4096\n\t"
        "ds_read_b64_tr_b16 a[214:215], v101 offset:4352\n\t"
        "s_waitcnt lgkmcnt(8)\n\t"
        "ds_read_b64_tr_b16 v[104:105], v102 offset:0\n\t"
        "ds_read_b64_tr_b16 v[106:107], v102 offset:256\n\t"
        "ds_read_b64_tr_b16 v[108:109], v102 offset:4096\n\t"
        "ds_read_b64_tr_b16 v[110:111], v102 offset:4352\n\t"
        "s_waitcnt lgkmcnt(0)\n\t"
        "ds_write_b128 v117, v[104:107] offset:16384\n\t"
        "ds_write_b128 v117, v[108:111] offset:17408\n\t"
        "s_lshl_b32 s34, s43, 2\n\t"
        "s_add_u32 s34, s34, 15\n\t"
        "s_and_b32 s34, s34, 15\n\t"
        "s_lshl_b32 s34, s34, 19\n\t"
        "buffer_load_dwordx4 v[64:67], v96, s[16:19], s34 offen nt\n\t"
        "s_add_u32 s34, s34, 0x10000\n\t"
        "buffer_load_dwordx4 v[68:71], v96, s[16:19], s34 offen nt\n\t"
        "s_add_u32 s34, s34, 0x10000\n\t"
        "buffer_load_dwordx4 v[72:75], v96, s[16:19], s34 offen nt\n\t"
        "s_add_u32 s34, s34, 0x10000\n\t"
        "buffer_load_dwordx4 v[76:79], v96, s[16:19], s34 offen nt\n\t"
        "s_add_u32 s34, s34, 0x10000\n\t"
        "buffer_load_dwordx4 v[80:83], v96, s[16:19], s34 offen nt\n\t"
        "s_add_u32 s34, s34, 0x10000\n\t"
        "buffer_load_dwordx4 v[84:87], v96, s[16:19], s34 offen nt\n\t"
        "s_add_u32 s34, s34, 0x10000\n\t"
        "buffer_load_dwordx4 v[88:91], v96, s[16:19], s34 offen nt\n\t"
        "s_add_u32 s34, s34, 0x10000\n\t"
        "buffer_load_dwordx4 v[92:95], v96, s[16:19], s34 offen nt\n\t"
        "s_waitcnt vmcnt(48)\n\t"
        "ds_write_b128 v97, v[128:131] offset:0\n\t"
        "ds_write_b128 v98, v[132:135] offset:1024\n\t"
        "ds_write_b128 v97, v[136:139] offset:2048\n\t"
        "ds_write_b128 v98, v[140:143] offset:3072\n\t"
        "ds_write_b128 v97, v[144:147] offset:4096\n\t"
        "ds_write_b128 v98, v[148:151] offset:5120\n\t"
        "ds_write_b128 v97, v[152:155] offset:6144\n\t"
        "ds_write_b128 v98, v[156:159] offset:7168\n\t"
        "ds_read_b64_tr_b16 a[216:217], v99 offset:0\n\t"
        "ds_read_b64_tr_b16 a[218:219], v99 offset:256\n\t"
        "ds_read_b64_tr_b16 a[220:221], v99 offset:4096\n\t"
        "ds_read_b64_tr_b16 a[222:223], v99 offset:4352\n\t"
        "s_waitcnt lgkmcnt(8)\n\t"
        "ds_read_b64_tr_b16 a[224:225], v100 offset:0\n\t"
        "ds_read_b64_tr_b16 a[226:227], v100 offset:256\n\t"
        "ds_read_b64_tr_b16 a[228:229], v100 offset:4096\n\t"
        "ds_read_b64_tr_b16 a[230:231], v100 offset:4352\n\t"
        "s_waitcnt lgkmcnt(8)\n\t"
        "ds_read_b64_tr_b16 a[232:233], v101 offset:0\n\t"
        "ds_read_b64_tr_b16 a[234:235], v101 offset:256\n\t"
        "ds_read_b64_tr_b16 a[236:237], v101 offset:4096\n\t"
        "ds_read_b64_tr_b16 a[238:239], v101 offset:4352\n\t"
        "s_waitcnt lgkmcnt(8)\n\t"
        "ds_read_b64_tr_b16 v[104:105], v102 offset:0\n\t"
        "ds_read_b64_tr_b16 v[106:107], v102 offset:256\n\t"
        "ds_read_b64_tr_b16 v[108:109], v102 offset:4096\n\t"
        "ds_read_b64_tr_b16 v[110:111], v102 offset:4352\n\t"
        "s_waitcnt lgkmcnt(0)\n\t"
        "ds_write_b128 v117, v[104:107] offset:18432\n\t"
        "ds_write_b128 v117, v[108:111] offset:19456\n\t"
        "s_waitcnt vmcnt(40)\n\t"
        "ds_write_b128 v97, v[160:163] offset:0\n\t"
        "ds_write_b128 v98, v[164:167] offset:1024\n\t"
        "ds_write_b128 v97, v[168:171] offset:2048\n\t"
        "ds_write_b128 v98, v[172:175] offset:3072\n\t"
        "ds_write_b128 v97, v[176:179] offset:4096\n\t"
        "ds_write_b128 v98, v[180:183] offset:5120\n\t"
        "ds_write_b128 v97, v[184:187] offset:6144\n\t"
        "ds_write_b128 v98, v[188:191] offset:7168\n\t"
        "ds_read_b64_tr_b16 a[240:241], v99 offset:0\n\t"
        "ds_read_b64_tr_b16 a[242:243], v99 offset:256\n\t"
        "ds_read_b64_tr_b16 a[244:245], v99 offset:4096\n\t"
        "ds_read_b64_tr_b16 a[246:247], v99 offset:4352\n\t"
        "s_waitcnt lgkmcnt(8)\n\t"
        "ds_read_b64_tr_b16 a[248:249], v100 offset:0\n\t"
        "ds_read_b64_tr_b16 a[250:251], v100 offset:256\n\t"
        "ds_read_b64_tr_b16 a[252:253], v100 offset:4096\n\t"
        "ds_read_b64_tr_b16 a[254:255], v100 offset:4352\n\t"
        "s_waitcnt lgkmcnt(8)\n\t"
        "ds_read_b64_tr_b16 v[128:129], v101 offset:0\n\t"
        "ds_read_b64_tr_b16 v[130:131], v101 offset:256\n\t"
        "ds_read_b64_tr_b16 v[132:133], v101 offset:4096\n\t"
        "ds_read_b64_tr_b16 v[134:135], v101 offset:4352\n\t"
        "s_waitcnt lgkmcnt(8)\n\t"
        "ds_read_b64_tr_b16 v[104:105], v102 offset:0\n\t"
        "ds_read_b64_tr_b16 v[106:107], v102 offset:256\n\t"
        "ds_read_b64_tr_b16 v[108:109], v102 offset:4096\n\t"
        "ds_read_b64_tr_b16 v[110:111], v102 offset:4352\n\t"
        "s_waitcnt lgkmcnt(0)\n\t"
        "ds_write_b128 v117, v[104:107] offset:20480\n\t"
        "ds_write_b128 v117, v[108:111] offset:21504\n\t"
        "s_waitcnt vmcnt(32)\n\t"
        "ds_write_b128 v97, v[192:195] offset:0\n\t"
        "ds_write_b128 v98, v[196:199] offset:1024\n\t"
        "ds_write_b128 v97, v[200:203] offset:2048\n\t"
        "ds_write_b128 v98, v[204:207] offset:3072\n\t"
        "ds_write_b128 v97, v[208:211] offset:4096\n\t"
        "ds_write_b128 v98, v[212:215] offset:5120\n\t"
        "ds_write_b128 v97, v[216:219] offset:6144\n\t"
        "ds_write_b128 v98, v[220:223] offset:7168\n\t"
        "ds_read_b64_tr_b16 v[136:137], v99 offset:0\n\t"
        "ds_read_b64_tr_b16 v[138:139], v99 offset:256\n\t"
        "ds_read_b64_tr_b16 v[140:141], v99 offset:4096\n\t"
        "ds_read_b64_tr_b16 v[142:143], v99 offset:4352\n\t"
        "s_waitcnt lgkmcnt(8)\n\t"
        "ds_read_b64_tr_b16 v[144:145], v100 offset:0\n\t"
        "ds_read_b64_tr_b16 v[146:147], v100 offset:256\n\t"
        "ds_read_b64_tr_b16 v[148:149], v100 offset:4096\n\t"
        "ds_read_b64_tr_b16 v[150:151], v100 offset:4352\n\t"
        "s_waitcnt lgkmcnt(8)\n\t"
        "ds_read_b64_tr_b16 v[152:153], v101 offset:0\n\t"
        "ds_read_b64_tr_b16 v[154:155], v101 offset:256\n\t"
        "ds_read_b64_tr_b16 v[156:157], v101 offset:4096\n\t"
        "ds_read_b64_tr_b16 v[158:159], v101 offset:4352\n\t"
        "s_waitcnt lgkmcnt(8)\n\t"
        "ds_read_b64_tr_b16 v[104:105], v102 offset:0\n\t"
        "ds_read_b64_tr_b16 v[106:107], v102 offset:256\n\t"
        "ds_read_b64_tr_b16 v[108:109], v102 offset:4096\n\t"
        "ds_read_b64_tr_b16 v[110:111], v102 offset:4352\n\t"
        "s_waitcnt lgkmcnt(0)\n\t"
        "ds_write_b128 v126, v[104:107] offset:0\n\t"
        "ds_write_b128 v126, v[108:111] offset:1024\n\t"
        "s_waitcnt vmcnt(24)\n\t"
        "ds_write_b128 v97, v[224:227] offset:0\n\t"
        "ds_write_b128 v98, v[228:231] offset:1024\n\t"
        "ds_write_b128 v97, v[232:235] offset:2048\n\t"
        "ds_write_b128 v98, v[236:239] offset:3072\n\t"
        "ds_write_b128 v97, v[240:243] offset:4096\n\t"
        "ds_write_b128 v98, v[244:247] offset:5120\n\t"
        "ds_write_b128 v97, v[248:251] offset:6144\n\t"
        "ds_write_b128 v98, v[252:255] offset:7168\n\t"
        "ds_read_b64_tr_b16 v[160:161], v99 offset:0\n\t"
        "ds_read_b64_tr_b16 v[162:163], v99 offset:256\n\t"
        "ds_read_b64_tr_b16 v[164:165], v99 offset:4096\n\t"
        "ds_read_b64_tr_b16 v[166:167], v99 offset:4352\n\t"
        "s_waitcnt lgkmcnt(8)\n\t"
        "ds_read_b64_tr_b16 v[168:169], v100 offset:0\n\t"
        "ds_read_b64_tr_b16 v[170:171], v100 offset:256\n\t"
        "ds_read_b64_tr_b16 v[172:173], v100 offset:4096\n\t"
        "ds_read_b64_tr_b16 v[174:175], v100 offset:4352\n\t"
        "s_waitcnt lgkmcnt(8)\n\t"
        "ds_read_b64_tr_b16 v[176:177], v101 offset:0\n\t"
        "ds_read_b64_tr_b16 v[178:179], v101 offset:256\n\t"
        "ds_read_b64_tr_b16 v[180:181], v101 offset:4096\n\t"
        "ds_read_b64_tr_b16 v[182:183], v101 offset:4352\n\t"
        "s_waitcnt lgkmcnt(8)\n\t"
        "ds_read_b64_tr_b16 v[104:105], v102 offset:0\n\t"
        "ds_read_b64_tr_b16 v[106:107], v102 offset:256\n\t"
        "ds_read_b64_tr_b16 v[108:109], v102 offset:4096\n\t"
        "ds_read_b64_tr_b16 v[110:111], v102 offset:4352\n\t"
        "s_waitcnt lgkmcnt(0)\n\t"
        "ds_write_b128 v126, v[104:107] offset:2048\n\t"
        "ds_write_b128 v126, v[108:111] offset:3072\n\t"
        "s_waitcnt vmcnt(16)\n\t"
        "ds_write_b128 v97, v[0:3] offset:0\n\t"
        "ds_write_b128 v98, v[4:7] offset:1024\n\t"
        "ds_write_b128 v97, v[8:11] offset:2048\n\t"
        "ds_write_b128 v98, v[12:15] offset:3072\n\t"
        "ds_write_b128 v97, v[16:19] offset:4096\n\t"
        "ds_write_b128 v98, v[20:23] offset:5120\n\t"
        "ds_write_b128 v97, v[24:27] offset:6144\n\t"
        "ds_write_b128 v98, v[28:31] offset:7168\n\t"
        "ds_read_b64_tr_b16 v[184:185], v99 offset:0\n\t"
        "ds_read_b64_tr_b16 v[186:187], v99 offset:256\n\t"
        "ds_read_b64_tr_b16 v[188:189], v99 offset:4096\n\t"
        "ds_read_b64_tr_b16 v[190:191], v99 offset:4352\n\t"
        "s_waitcnt lgkmcnt(8)\n\t"
        "ds_read_b64_tr_b16 v[192:193], v100 offset:0\n\t"
        "ds_read_b64_tr_b16 v[194:195], v100 offset:256\n\t"
        "ds_read_b64_tr_b16 v[196:197], v100 offset:4096\n\t"
        "ds_read_b64_tr_b16 v[198:199], v100 offset:4352\n\t"
        "s_waitcnt lgkmcnt(8)\n\t"
        "ds_read_b64_tr_b16 v[200:201], v101 offset:0\n\t"
        "ds_read_b64_tr_b16 v[202:203], v101 offset:256\n\t"
        "ds_read_b64_tr_b16 v[204:205], v101 offset:4096\n\t"
        "ds_read_b64_tr_b16 v[206:207], v101 offset:4352\n\t"
        "s_waitcnt lgkmcnt(8)\n\t"
        "ds_read_b64_tr_b16 v[104:105], v102 offset:0\n\t"
        "ds_read_b64_tr_b16 v[106:107], v102 offset:256\n\t"
        "ds_read_b64_tr_b16 v[108:109], v102 offset:4096\n\t"
        "ds_read_b64_tr_b16 v[110:111], v102 offset:4352\n\t"
        "s_waitcnt lgkmcnt(0)\n\t"
        "ds_write_b128 v126, v[104:107] offset:4096\n\t"
        "ds_write_b128 v126, v[108:111] offset:5120\n\t"
        "s_waitcnt vmcnt(8)\n\t"
        "ds_write_b128 v97, v[32:35] offset:0\n\t"
        "ds_write_b128 v98, v[36:39] offset:1024\n\t"
        "ds_write_b128 v97, v[40:43] offset:2048\n\t"
        "ds_write_b128 v98, v[44:47] offset:3072\n\t"
        "ds_write_b128 v97, v[48:51] offset:4096\n\t"
        "ds_write_b128 v98, v[52:55] offset:5120\n\t"
        "ds_write_b128 v97, v[56:59] offset:6144\n\t"
        "ds_write_b128 v98, v[60:63] offset:7168\n\t"
        "ds_read_b64_tr_b16 v[208:209], v99 offset:0\n\t"
        "ds_read_b64_tr_b16 v[210:211], v99 offset:256\n\t"
        "ds_read_b64_tr_b16 v[212:213], v99 offset:4096\n\t"
        "ds_read_b64_tr_b16 v[214:215], v99 offset:4352\n\t"
        "s_waitcnt lgkmcnt(8)\n\t"
        "ds_read_b64_tr_b16 v[216:217], v100 offset:0\n\t"
        "ds_read_b64_tr_b16 v[218:219], v100 offset:256\n\t"
        "ds_read_b64_tr_b16 v[220:221], v100 offset:4096\n\t"
        "ds_read_b64_tr_b16 v[222:223], v100 offset:4352\n\t"
        "s_waitcnt lgkmcnt(8)\n\t"
        "ds_read_b64_tr_b16 v[224:225], v101 offset:0\n\t"
        "ds_read_b64_tr_b16 v[226:227], v101 offset:256\n\t"
        "ds_read_b64_tr_b16 v[228:229], v101 offset:4096\n\t"
        "ds_read_b64_tr_b16 v[230:231], v101 offset:4352\n\t"
        "s_waitcnt lgkmcnt(8)\n\t"
        "ds_read_b64_tr_b16 v[104:105], v102 offset:0\n\t"
        "ds_read_b64_tr_b16 v[106:107], v102 offset:256\n\t"
        "ds_read_b64_tr_b16 v[108:109], v102 offset:4096\n\t"
        "ds_read_b64_tr_b16 v[110:111], v102 offset:4352\n\t"
        "s_waitcnt lgkmcnt(0)\n\t"
        "ds_write_b128 v126, v[104:107] offset:6144\n\t"
        "ds_write_b128 v126, v[108:111] offset:7168\n\t"
        "s_waitcnt vmcnt(0)\n\t"
        "ds_write_b128 v97, v[64:67] offset:0\n\t"
        "ds_write_b128 v98, v[68:71] offset:1024\n\t"
        "ds_write_b128 v97, v[72:75] offset:2048\n\t"
        "ds_write_b128 v98, v[76:79] offset:3072\n\t"
        "ds_write_b128 v97, v[80:83] offset:4096\n\t"
        "ds_write_b128 v98, v[84:87] offset:5120\n\t"
        "ds_write_b128 v97, v[88:91] offset:6144\n\t"
        "ds_write_b128 v98, v[92:95] offset:7168\n\t"
        "ds_read_b64_tr_b16 v[232:233], v99 offset:0\n\t"
        "ds_read_b64_tr_b16 v[234:235], v99 offset:256\n\t"
        "ds_read_b64_tr_b16 v[236:237], v99 offset:4096\n\t"
        "ds_read_b64_tr_b16 v[238:239], v99 offset:4352\n\t"
        "s_waitcnt lgkmcnt(8)\n\t"
        "ds_read_b64_tr_b16 v[240:241], v100 offset:0\n\t"
        "ds_read_b64_tr_b16 v[242:243], v100 offset:256\n\t"
        "ds_read_b64_tr_b16 v[244:245], v100 offset:4096\n\t"
        "ds_read_b64_tr_b16 v[246:247], v100 offset:4352\n\t"
        "s_waitcnt lgkmcnt(8)\n\t"
        "ds_read_b64_tr_b16 v[248:249], v101 offset:0\n\t"
        "ds_read_b64_tr_b16 v[250:251], v101 offset:256\n\t"
        "ds_read_b64_tr_b16 v[252:253], v101 offset:4096\n\t"
        "ds_read_b64_tr_b16 v[254:255], v101 offset:4352\n\t"
        "s_waitcnt lgkmcnt(8)\n\t"
        "ds_read_b64_tr_b16 v[104:105], v102 offset:0\n\t"
        "ds_read_b64_tr_b16 v[106:107], v102 offset:256\n\t"
        "ds_read_b64_tr_b16 v[108:109], v102 offset:4096\n\t"
        "ds_read_b64_tr_b16 v[110:111], v102 offset:4352\n\t"
        "s_waitcnt lgkmcnt(0)\n\t"
        "ds_write_b128 v126, v[104:107] offset:8192\n\t"
        "ds_write_b128 v126, v[108:111] offset:9216\n\t"
        "s_waitcnt lgkmcnt(0)\n\t"
        "v_mov_b32_e32 v110, v103\n\t"
        "ds_read_b128 v[64:67], v126 offset:0\n\t"
        "ds_read_b128 v[68:71], v126 offset:1024\n\t"
        "ds_read_b128 v[72:75], v126 offset:2048\n\t"
        "ds_read_b128 v[76:79], v126 offset:3072\n\t"
        "ds_read_b128 v[80:83], v126 offset:4096\n\t"
        "ds_read_b128 v[84:87], v126 offset:5120\n\t"
        "ds_read_b128 v[88:91], v126 offset:6144\n\t"
        "ds_read_b128 v[92:95], v126 offset:7168\n\t"
        "ds_read_b128 v[96:99], v126 offset:8192\n\t"
        "ds_read_b128 v[100:103], v126 offset:9216\n\t"
        "s_waitcnt lgkmcnt(0)\n\t"
        "v_mov_b32_e32 v126, 0\n\t"
        "s_barrier\n\t"
        "s_mov_b32 s48, 0\n\t"
        "s_mov_b32 s54, 0\n\t"
        "s_movk_i32 s56, 24576\n\t"
        "s_lshl_b32 s57, s44, 5\n\t"
        "s_add_u32 s57, s57, s43\n\t"
        "s_lshl_b32 s57, s57, 2\n\t"
        "s_add_u32 s57, s57, 458752\n\t"
        "s_lshl_b32 s35, s44, 7\n\t"
        "v_lshrrev_b32_e32 v111, 5, v125\n\t"
        "v_and_b32_e32 v111, 60, v111\n\t"
        "buffer_load_dword v111, v111, s[12:15], s35 offen offset:0 sc1\n\t"
        "s_lshr_b32 s36, s48, 1\n\t"
        "s_cmp_gt_u32 s54, s36\n\t"
        "s_cbranch_scc1 LA_kv0ok%=\n\t"
        "s_mov_b32 s42, 0\n\t"
        "LA_kv0%=:\n\t"
        "s_waitcnt vmcnt(0)\n\t"
        "v_min_u32_dpp v111, v111, v111 quad_perm:[1,0,3,2] row_mask:0xf bank_mask:0xf\n\t"
        "s_nop 1\n\t"
        "v_min_u32_dpp v111, v111, v111 quad_perm:[2,3,0,1] row_mask:0xf bank_mask:0xf\n\t"
        "s_nop 1\n\t"
        "v_min_u32_dpp v111, v111, v111 row_ror:4 row_mask:0xf bank_mask:0xf\n\t"
        "s_nop 1\n\t"
        "v_min_u32_dpp v111, v111, v111 row_ror:8 row_mask:0xf bank_mask:0xf\n\t"
        "s_nop 0\n\t"
        "v_readfirstlane_b32 s54, v111\n\t"
        "s_cmp_gt_u32 s54, s36\n\t"
        "s_cbranch_scc1 LA_kv0ok%=\n\t"
        "s_sleep 4\n\t"
        "s_lshl_b32 s35, s44, 7\n\t"
        "v_lshrrev_b32_e32 v111, 5, v125\n\t"
        "v_and_b32_e32 v111, 60, v111\n\t"
        "buffer_load_dword v111, v111, s[12:15], s35 offen offset:0 sc1\n\t"
        "s_add_u32 s42, s42, 1\n\t"
        "s_cmp_lt_u32 s42, 0x100000\n\t"
        "s_cbranch_scc1 LA_kv0%=\n\t"
        "s_movk_i32 s34, 16\n\t"
        "s_branch LA_die%=\n\t"
        "LA_kv0ok%=:\n\t"
        "s_and_b32 s36, s48, 1\n\t"
        "s_mul_i32 s36, s36, 24576\n\t"
        "s_add_u32 s36, s36, s55\n\t"
        "s_lshl_b32 s37, s48, 18\n\t"
        "s_lshl_b32 s34, s43, 11\n\t"
        "s_add_u32 s37, s37, s34\n\t"
        "s_add_u32 m0, s36, 0\n\t"
        "s_add_u32 s34, s37, 29548544\n\t"
        "buffer_load_dwordx4 v122, s[12:15], s34 offen sc1 lds\n\t"
        "s_add_u32 m0, s36, 1024\n\t"
        "s_add_u32 s34, s37, 29549568\n\t"
        "buffer_load_dwordx4 v122, s[12:15], s34 offen sc1 lds\n\t"
        "s_add_u32 m0, s36, 8192\n\t"
        "s_add_u32 s34, s37, 12771328\n\t"
        "buffer_load_dwordx4 v122, s[12:15], s34 offen sc1 lds\n\t"
        "s_add_u32 m0, s36, 9216\n\t"
        "s_add_u32 s34, s37, 12772352\n\t"
        "buffer_load_dwordx4 v122, s[12:15], s34 offen sc1 lds\n\t"
        "s_add_u32 m0, s36, 16384\n\t"
        "s_add_u32 s34, s37, 46325760\n\t"
        "buffer_load_dwordx4 v122, s[12:15], s34 offen sc1 lds\n\t"
        "s_add_u32 m0, s36, 17408\n\t"
        "s_add_u32 s34, s37, 46326784\n\t"
        "buffer_load_dwordx4 v122, s[12:15], s34 offen sc1 lds\n\t"
        "s_mov_b32 s42, 0\n\t"
        "v_mbcnt_lo_u32_b32 v2, -1, 0\n\t"
        "v_mbcnt_hi_u32_b32 v2, -1, v2\n\t"
        "v_and_b32_e32 v3, 31, v2\n\t"
        "v_lshlrev_b32_e32 v3, 2, v3\n\t"
        "s_mov_b32 s35, 33792\n\t"
        "LA_cal%=:\n\t"
        "buffer_load_dword v0, v3, s[12:15], s35 offen sc1\n\t"
        "s_waitcnt vmcnt(0)\n\t"
        "v_cmp_eq_u32_e32 vcc, 0, v0\n\t"
        "s_cbranch_vccz LA_calok%=\n\t"
        "s_sleep 4\n\t"
        "s_add_u32 s42, s42, 1\n\t"
        "s_cmp_lt_u32 s42, 0x100000\n\t"
        "s_cbranch_scc1 LA_cal%=\n\t"
        "s_movk_i32 s34, 8\n\t"
        "s_branch LA_die%=\n\t"
        "LA_calok%=:\n\t"
        "v_cmp_eq_u32_e32 vcc, 3, v0\n\t"
        "s_nop 0\n\t"
        "s_bcnt1_i32_b64 s34, vcc\n\t"
        "s_cmp_ge_u32 s34, 32\n\t"
        "s_cselect_b32 s34, 8192, 0\n\t"
        "s_add_u32 s8, s8, s34\n\t"
        "s_addc_u32 s9, s9, 0\n\t"
        "s_waitcnt vmcnt(0) lgkmcnt(0)\n\t"
        "s_barrier\n\t"
        "s_memrealtime s[32:33]\n\t"
        "s_lshl_b32 s35, s49, 2\n\t"
        "s_add_u32 s35, s35, 34304\n\t"
        "s_cmp_eq_u32 s43, 0\n\t"
        "s_cselect_b32 s35, s35, 0xf0000000\n\t"
        "s_waitcnt lgkmcnt(0)\n\t"
        "s_or_b32 s25, s32, 1\n\t"
        "v_mov_b32_e32 v0, s25\n\t"
        "s_mov_b64 exec, 1\n\t"
        "buffer_store_dword v0, off, s[12:15], s35 sc1\n\t"
        "s_mov_b64 exec, -1\n\t"
        "v_min_u32_e32 v3, 47, v2\n\t"
        "v_lshlrev_b32_e32 v3, 3, v3\n\t"
        "s_mov_b32 s35, 34304\n\t"
        "s_mov_b32 s42, 0\n\t"
        "LA_rdy%=:\n\t"
        "buffer_load_dwordx2 v[0:1], v3, s[12:15], s35 offen sc1\n\t"
        "s_waitcnt vmcnt(0)\n\t"
        "v_min_u32_e32 v2, v0, v1\n\t"
        "v_cmp_eq_u32_e32 vcc, 0, v2\n\t"
        "s_cbranch_vccz LA_rdyok%=\n\t"
        "s_sleep 3\n\t"
        "s_add_u32 s42, s42, 1\n\t"
        "s_cmp_lt_u32 s42, 0x100000\n\t"
        "s_cbranch_scc1 LA_rdy%=\n\t"
        "s_movk_i32 s34, 9\n\t"
        "s_branch LA_die%=\n\t"
        "LA_rdyok%=:\n\t"
        "v_subrev_u32_e32 v0, s25, v0\n\t"
        "v_subrev_u32_e32 v1, s25, v1\n\t"
        "v_max_i32_e32 v0, v0, v1\n\t"
        "s_nop 1\n\t"
        "v_max_i32_dpp v0, v0, v0 quad_perm:[1,0,3,2] row_mask:0xf bank_mask:0xf\n\t"
        "s_nop 1\n\t"
        "v_max_i32_dpp v0, v0, v0 quad_perm:[2,3,0,1] row_mask:0xf bank_mask:0xf\n\t"
        "s_nop 1\n\t"
        "v_max_i32_dpp v0, v0, v0 row_ror:4 row_mask:0xf bank_mask:0xf\n\t"
        "s_nop 1\n\t"
        "v_max_i32_dpp v0, v0, v0 row_ror:8 row_mask:0xf bank_mask:0xf\n\t"
        "v_mov_b32_e32 v1, v0\n\t"
        "s_nop 1\n\t"
        "v_permlane16_swap_b32_e32 v0, v1\n\t"
        "v_max_i32_e32 v0, v0, v1\n\t"
        "v_mov_b32_e32 v1, v0\n\t"
        "s_nop 1\n\t"
        "v_permlane32_swap_b32_e32 v0, v1\n\t"
        "v_max_i32_e32 v0, v0, v1\n\t"
        "s_nop 0\n\t"
        "v_readfirstlane_b32 s34, v0\n\t"
        "s_nop 0\n\t"
        "s_add_u32 s25, s25, s34\n\t"
        "s_add_u32 s25, s25, 70\n\t"
        "s_mov_b32 s24, 0\n\t"
        "s_mov_b32 s26, 0\n\t"
        "s_mov_b32 s27, 0x80000\n\t"
        "s_mov_b32 s28, 0x180000\n\t"
        "s_mov_b32 s31, 0\n\t"
        "s_mov_b32 s47, 0\n\t"
        "s_mov_b32 s38, 0xff00ff00\n\t"
        "s_mov_b32 s39, 0xff00ff00\n\t"
        "s_mov_b32 s40, 0\n\t"
        "s_mov_b32 s41, -1\n\t"
        "ds_read_b128 v[32:35], v117 offset:0\n\t"
        "ds_read_b128 v[36:39], v117 offset:1024\n\t"
        "ds_read_b128 v[40:43], v117 offset:2048\n\t"
        "ds_read_b128 v[44:47], v117 offset:3072\n\t"
        "ds_read_b128 v[48:51], v117 offset:4096\n\t"
        "ds_read_b128 v[52:55], v117 offset:5120\n\t"
        "ds_read_b128 v[56:59], v117 offset:6144\n\t"
        "ds_read_b128 v[60:63], v117 offset:7168\n\t"
        "v_mov_b64_e32 v[0:1], 0\n\t"
        "v_mov_b64_e32 v[2:3], 0\n\t"
        "v_mov_b64_e32 v[4:5], 0\n\t"
        "v_mov_b64_e32 v[6:7], 0\n\t"
        "v_mov_b64_e32 v[8:9], 0\n\t"
        "v_mov_b64_e32 v[10:11], 0\n\t"
        "v_mov_b64_e32 v[12:13], 0\n\t"
        "v_mov_b64_e32 v[14:15], 0\n\t"
        "LA_loop%=:\n\t"
        "s_mov_b32 s42, 0\n\t"
        "s_sub_u32 s35, s25, 1\n\t"
        "LA_wa%=:\n\t"
        "s_memrealtime s[32:33]\n\t"
        "s_waitcnt lgkmcnt(0)\n\t"
        "s_sub_u32 s34, s35, s32\n\t"
        "s_cmpk_lt_u32 s34, 0x2000\n\t"
        "s_cbranch_scc1 LA_wa%=\n\t"
        "LA_rd%=:\n\t"
        "buffer_load_dwordx4 v[16:19], v112, s[8:11], s26 offen offset:0 sc1\n\t"
        "s_nop 9\n\t"
        "buffer_load_dwordx4 v[20:23], v112, s[8:11], s26 offen offset:1024 sc1\n\t"
        "s_nop 9\n\t"
        "buffer_load_dwordx4 v[24:27], v112, s[8:11], s26 offen offset:2048 sc1\n\t"
        "s_nop 9\n\t"
        "buffer_load_dwordx4 v[28:31], v112, s[8:11], s26 offen offset:3072 sc1\n\t"
        "LA_go%=:\n\t"
        "s_waitcnt vmcnt(3)\n\t"
        "v_smfmac_f32_16x16x64_bf16 v[12:15], v[16:19], v[32:39], v110\n\t"
        "ds_write_b128 v113, v[16:19] offset:0\n\t"
        "v_smfmac_f32_16x16x64_bf16 v[0:3], v[16:19], a[0:7], v110\n\t"
        "ds_read_b128 v[32:35], v117 offset:8192\n\t"
        "v_max3_u32 v111, v16, v17, v18\n\t"
        "v_smfmac_f32_16x16x64_bf16 v[4:7], v[16:19], a[8:15], v110\n\t"
        "ds_read_b128 v[36:39], v117 offset:9216\n\t"
        "v_max_u32_e32 v111, v19, v111\n\t"
        "v_smfmac_f32_16x16x64_bf16 v[8:11], v[16:19], a[16:23], v110\n\t"
        "s_waitcnt vmcnt(2)\n\t"
        "v_smfmac_f32_16x16x64_bf16 v[12:15], v[20:23], v[40:47], v110\n\t"
        "ds_write_b128 v113, v[20:23] offset:1024\n\t"
        "v_smfmac_f32_16x16x64_bf16 v[0:3], v[20:23], a[24:31], v110\n\t"
        "ds_read_b128 v[40:43], v117 offset:10240\n\t"
        "v_max3_u32 v111, v20, v21, v111\n\t"
        "v_smfmac_f32_16x16x64_bf16 v[4:7], v[20:23], a[32:39], v110\n\t"
        "ds_read_b128 v[44:47], v117 offset:11264\n\t"
        "v_max3_u32 v111, v22, v23, v111\n\t"
        "v_smfmac_f32_16x16x64_bf16 v[8:11], v[20:23], a[40:47], v110\n\t"
        "s_waitcnt vmcnt(1)\n\t"
        "v_smfmac_f32_16x16x64_bf16 v[12:15], v[24:27], v[48:55], v110\n\t"
        "ds_write_b128 v113, v[24:27] offset:2048\n\t"
        "v_smfmac_f32_16x16x64_bf16 v[0:3], v[24:27], a[48:55], v110\n\t"
        "ds_read_b128 v[48:51], v117 offset:12288\n\t"
        "v_max3_u32 v111, v24, v25, v111\n\t"
        "v_smfmac_f32_16x16x64_bf16 v[4:7], v[24:27], a[56:63], v110\n\t"
        "ds_read_b128 v[52:55], v117 offset:13312\n\t"
        "v_max3_u32 v111, v26, v27, v111\n\t"
        "v_smfmac_f32_16x16x64_bf16 v[8:11], v[24:27], a[64:71], v110\n\t"
        "s_waitcnt vmcnt(0)\n\t"
        "ds_write_b128 v113, v[28:31] offset:3072\n\t"
        "v_max3_u32 v111, v28, v29, v111\n\t"
        "v_max3_u32 v111, v30, v31, v111\n\t"
        "v_cmp_eq_u32_e32 vcc, -1, v111\n\t"
        "s_waitcnt lgkmcnt(0)\n\t"
        "s_cbranch_vccnz LA_rdfail%=\n\t"
        "s_barrier\n\t"
        "ds_read_b128 v[16:19], v114 offset:0\n\t"
        "ds_read_b128 v[20:23], v114 offset:1024\n\t"
        "ds_read_b128 v[24:27], v114 offset:2048\n\t"
        "v_smfmac_f32_16x16x64_bf16 v[12:15], v[28:31], v[56:63], v110\n\t"
        "ds_read_b128 v[56:59], v117 offset:14336\n\t"
        "v_smfmac_f32_16x16x64_bf16 v[0:3], v[28:31], a[72:79], v110\n\t"
        "ds_read_b128 v[60:63], v117 offset:15360\n\t"
        "v_smfmac_f32_16x16x64_bf16 v[4:7], v[28:31], a[80:87], v110\n\t"
        "v_smfmac_f32_16x16x64_bf16 v[8:11], v[28:31], a[88:95], v110\n\t"
        "ds_read_b128 v[28:31], v114 offset:3072\n\t"
        "s_waitcnt lgkmcnt(5)\n\t"
        "v_smfmac_f32_16x16x64_bf16 v[12:15], v[16:19], v[32:39], v110\n\t"
        "ds_read_b128 v[32:35], v117 offset:16384\n\t"
        "v_smfmac_f32_16x16x64_bf16 v[0:3], v[16:19], a[96:103], v110\n\t"
        "ds_read_b128 v[36:39], v117 offset:17408\n\t"
        "v_smfmac_f32_16x16x64_bf16 v[4:7], v[16:19], a[104:111], v110\n\t"
        "v_smfmac_f32_16x16x64_bf16 v[8:11], v[16:19], a[112:119], v110\n\t"
        "ds_read_b128 v[16:19], v115 offset:0\n\t"
        "s_waitcnt lgkmcnt(7)\n\t"
        "v_smfmac_f32_16x16x64_bf16 v[12:15], v[20:23], v[40:47], v110\n\t"
        "ds_read_b128 v[40:43], v117 offset:18432\n\t"
        "v_smfmac_f32_16x16x64_bf16 v[0:3], v[20:23], a[120:127], v110\n\t"
        "ds_read_b128 v[44:47], v117 offset:19456\n\t"
        "v_smfmac_f32_16x16x64_bf16 v[4:7], v[20:23], a[128:135], v110\n\t"
        "v_smfmac_f32_16x16x64_bf16 v[8:11], v[20:23], a[136:143], v110\n\t"
        "ds_read_b128 v[20:23], v115 offset:1024\n\t"
        "s_waitcnt lgkmcnt(9)\n\t"
        "v_smfmac_f32_16x16x64_bf16 v[12:15], v[24:27], v[48:55], v110\n\t"
        "ds_read_b128 v[48:51], v117 offset:20480\n\t"
        "v_smfmac_f32_16x16x64_bf16 v[0:3], v[24:27], a[144:151], v110\n\t"
        "ds_read_b128 v[52:55], v117 offset:21504\n\t"
        "v_smfmac_f32_16x16x64_bf16 v[4:7], v[24:27], a[152:159], v110\n\t"
        "v_smfmac_f32_16x16x64_bf16 v[8:11], v[24:27], a[160:167], v110\n\t"
        "ds_read_b128 v[24:27], v115 offset:2048\n\t"
        "s_waitcnt lgkmcnt(9)\n\t"
        "v_smfmac_f32_16x16x64_bf16 v[12:15], v[28:31], v[56:63], v110\n\t"
        "ds_read_b128 v[56:59], v124 offset:3072\n\t"
        "v_smfmac_f32_16x16x64_bf16 v[0:3], v[28:31], a[168:175], v110\n\t"
        "ds_read_b128 v[60:63], v124 offset:7168\n\t"
        "v_smfmac_f32_16x16x64_bf16 v[4:7], v[28:31], a[176:183], v110\n\t"
        "v_smfmac_f32_16x16x64_bf16 v[8:11], v[28:31], a[184:191], v110\n\t"
        "ds_read_b128 v[28:31], v115 offset:3072\n\t"
        "s_waitcnt lgkmcnt(9)\n\t"
        "v_smfmac_f32_16x16x64_bf16 v[12:15], v[16:19], v[32:39], v110\n\t"
        "ds_read_b128 v[32:35], v124 offset:0\n\t"
        "v_smfmac_f32_16x16x64_bf16 v[0:3], v[16:19], a[192:199], v110\n\t"
        "ds_read_b128 v[36:39], v124 offset:4096\n\t"
        "v_smfmac_f32_16x16x64_bf16 v[4:7], v[16:19], a[200:207], v110\n\t"
        "v_smfmac_f32_16x16x64_bf16 v[8:11], v[16:19], a[208:215], v110\n\t"
        "ds_read_b128 v[16:19], v116 offset:0\n\t"
        "s_waitcnt lgkmcnt(9)\n\t"
        "v_smfmac_f32_16x16x64_bf16 v[12:15], v[20:23], v[40:47], v110\n\t"
        "ds_read_b128 v[40:43], v124 offset:1024\n\t"
        "v_smfmac_f32_16x16x64_bf16 v[0:3], v[20:23], a[216:223], v110\n\t"
        "ds_read_b128 v[44:47], v124 offset:5120\n\t"
        "v_smfmac_f32_16x16x64_bf16 v[4:7], v[20:23], a[224:231], v110\n\t"
        "v_smfmac_f32_16x16x64_bf16 v[8:11], v[20:23], a[232:239], v110\n\t"
        "ds_read_b128 v[20:23], v116 offset:1024\n\t"
        "s_waitcnt lgkmcnt(9)\n\t"
        "v_smfmac_f32_16x16x64_bf16 v[12:15], v[24:27], v[48:55], v110\n\t"
        "ds_read_b128 v[48:51], v124 offset:2048\n\t"
        "v_smfmac_f32_16x16x64_bf16 v[0:3], v[24:27], a[240:247], v110\n\t"
        "ds_read_b128 v[52:55], v124 offset:6144\n\t"
        "v_smfmac_f32_16x16x64_bf16 v[4:7], v[24:27], a[248:255], v110\n\t"
        "v_smfmac_f32_16x16x64_bf16 v[8:11], v[24:27], v[128:135], v110\n\t"
        "ds_read_b128 v[24:27], v116 offset:2048\n\t"
        "s_waitcnt lgkmcnt(9)\n\t"
        "v_smfmac_f32_16x16x64_bf16 v[12:15], v[28:31], v[64:71], v110\n\t"
        "v_smfmac_f32_16x16x64_bf16 v[0:3], v[28:31], v[136:143], v110\n\t"
        "v_smfmac_f32_16x16x64_bf16 v[4:7], v[28:31], v[144:151], v110\n\t"
        "v_smfmac_f32_16x16x64_bf16 v[8:11], v[28:31], v[152:159], v110\n\t"
        "ds_read_b128 v[28:31], v116 offset:3072\n\t"
        "s_waitcnt lgkmcnt(7)\n\t"
        "v_smfmac_f32_16x16x64_bf16 v[12:15], v[16:19], v[72:79], v110\n\t"
        "v_smfmac_f32_16x16x64_bf16 v[0:3], v[16:19], v[160:167], v110\n\t"
        "v_smfmac_f32_16x16x64_bf16 v[4:7], v[16:19], v[168:175], v110\n\t"
        "v_smfmac_f32_16x16x64_bf16 v[8:11], v[16:19], v[176:183], v110\n\t"
        "s_waitcnt lgkmcnt(4)\n\t"
        "v_smfmac_f32_16x16x64_bf16 v[0:3], v[20:23], v[184:191], v110\n\t"
        "v_smfmac_f32_16x16x64_bf16 v[4:7], v[20:23], v[192:199], v110\n\t"
        "s_waitcnt lgkmcnt(1)\n\t"
        "v_smfmac_f32_16x16x64_bf16 v[0:3], v[24:27], v[208:215], v110\n\t"
        "v_smfmac_f32_16x16x64_bf16 v[4:7], v[24:27], v[216:223], v110\n\t"
        "s_waitcnt lgkmcnt(0)\n\t"
        "v_smfmac_f32_16x16x64_bf16 v[0:3], v[28:31], v[232:239], v110\n\t"
        "v_smfmac_f32_16x16x64_bf16 v[4:7], v[28:31], v[240:247], v110\n\t"
        "v_smfmac_f32_16x16x64_bf16 v[12:15], v[20:23], v[80:87], v110\n\t"
        "s_nop 1\n\t"
        "v_smfmac_f32_16x16x64_bf16 v[8:11], v[20:23], v[200:207], v110\n\t"
        "s_nop 1\n\t"
        "v_smfmac_f32_16x16x64_bf16 v[12:15], v[24:27], v[88:95], v110\n\t"
        "s_nop 0\n\t"
        "v_permlane32_swap_b32_e32 v0, v4\n\t"
        "v_permlane32_swap_b32_e32 v1, v5\n\t"
        "v_permlane32_swap_b32_e32 v2, v6\n\t"
        "v_smfmac_f32_16x16x64_bf16 v[8:11], v[24:27], v[224:231], v110\n\t"
        "v_permlane32_swap_b32_e32 v3, v7\n\t"
        "v_add_f32_e32 v0, v0, v4\n\t"
        "v_add_f32_e32 v1, v1, v5\n\t"
        "v_smfmac_f32_16x16x64_bf16 v[12:15], v[28:31], v[96:103], v110\n\t"
        "v_add_f32_e32 v2, v2, v6\n\t"
        "v_add_f32_e32 v3, v3, v7\n\t"
        "v_cvt_pk_bf16_f32 v4, v0, v1\n\t"
        "v_smfmac_f32_16x16x64_bf16 v[8:11], v[28:31], v[248:255], v110\n\t"
        "v_cvt_pk_bf16_f32 v5, v2, v3\n\t"
        "ds_write_b64 v118, v[4:5]\n\t"
        "s_lshl_b32 s34, s43, 11\n\t"
        "v_add_u32_e32 v108, s34, v124\n\t"
        "s_nop 3\n\t"
        "v_permlane32_swap_b32_e32 v8, v12\n\t"
        "v_permlane32_swap_b32_e32 v9, v13\n\t"
        "v_permlane32_swap_b32_e32 v10, v14\n\t"
        "v_permlane32_swap_b32_e32 v11, v15\n\t"
        "v_add_f32_e32 v8, v8, v12\n\t"
        "v_add_f32_e32 v9, v9, v13\n\t"
        "v_add_f32_e32 v10, v10, v14\n\t"
        "v_add_f32_e32 v11, v11, v15\n\t"
        "v_cvt_pk_bf16_f32 v12, v8, v9\n\t"
        "v_cvt_pk_bf16_f32 v13, v10, v11\n\t"
        "ds_write_b64 v118, v[12:13] offset:256\n\t"
        "buffer_load_dwordx4 v[104:107], v120, s[8:11], s26 offen sc1\n\t"
        "s_waitcnt lgkmcnt(0)\n\t"
        "s_barrier\n\t"
        "ds_read_b64_tr_b16 v[16:17], v119 offset:0\n\t"
        "ds_read_b64_tr_b16 v[18:19], v119 offset:32\n\t"
        "ds_read_b64_tr_b16 v[20:21], v119 offset:256\n\t"
        "ds_read_b64_tr_b16 v[22:23], v119 offset:288\n\t"
        "ds_read_b64_tr_b16 v[24:25], v119 offset:512\n\t"
        "ds_read_b64_tr_b16 v[26:27], v119 offset:544\n\t"
        "ds_read_b64_tr_b16 v[28:29], v119 offset:768\n\t"
        "ds_read_b64_tr_b16 v[30:31], v119 offset:800\n\t"
        "buffer_store_dword v126, v127, s[12:15], 0 offen sc1\n\t"
        "s_waitcnt lgkmcnt(6)\n\t"
        "v_mfma_f32_16x16x32_bf16 v[0:3], v[32:35], v[16:19], 0\n\t"
        "ds_read_b64_tr_b16 v[32:33], v119 offset:2112\n\t"
        "ds_read_b64_tr_b16 v[34:35], v119 offset:2144\n\t"
        "v_mfma_f32_16x16x32_bf16 v[4:7], v[36:39], v[16:19], 0\n\t"
        "ds_read_b64_tr_b16 v[36:37], v119 offset:2368\n\t"
        "ds_read_b64_tr_b16 v[38:39], v119 offset:2400\n\t"
        "s_waitcnt lgkmcnt(8)\n\t"
        "v_mfma_f32_16x16x32_bf16 v[0:3], v[40:43], v[20:23], v[0:3]\n\t"
        "ds_read_b64_tr_b16 v[40:41], v119 offset:2624\n\t"
        "ds_read_b64_tr_b16 v[42:43], v119 offset:2656\n\t"
        "v_mfma_f32_16x16x32_bf16 v[4:7], v[44:47], v[20:23], v[4:7]\n\t"
        "ds_read_b64_tr_b16 v[44:45], v119 offset:2880\n\t"
        "ds_read_b64_tr_b16 v[46:47], v119 offset:2912\n\t"
        "s_waitcnt lgkmcnt(10)\n\t"
        "v_mfma_f32_16x16x32_bf16 v[0:3], v[48:51], v[24:27], v[0:3]\n\t"
        "ds_read_b128 v[48:51], v108 offset:16384\n\t"
        "v_mfma_f32_16x16x32_bf16 v[4:7], v[52:55], v[24:27], v[4:7]\n\t"
        "ds_read_b128 v[52:55], v108 offset:17408\n\t"
        "s_waitcnt lgkmcnt(10)\n\t"
        "v_mfma_f32_16x16x32_bf16 v[0:3], v[56:59], v[28:31], v[0:3]\n\t"
        "v_mfma_f32_16x16x32_bf16 v[4:7], v[60:63], v[28:31], v[4:7]\n\t"
        "s_waitcnt lgkmcnt(2)\n\t"
        "v_mfma_f32_16x16x32_bf16 v[8:11], v[32:35], v[16:19], 0\n\t"
        "s_nop 1\n\t"
        "v_mfma_f32_16x16x32_bf16 v[8:11], v[36:39], v[20:23], v[8:11]\n\t"
        "s_nop 1\n\t"
        "v_mfma_f32_16x16x32_bf16 v[8:11], v[40:43], v[24:27], v[8:11]\n\t"
        "v_max3_f32 v59, v0, v1, v2\n\t"
        "v_max3_f32 v59, v59, v3, v4\n\t"
        "v_mfma_f32_16x16x32_bf16 v[8:11], v[44:47], v[28:31], v[8:11]\n\t"
        "v_max3_f32 v59, v59, v5, v6\n\t"
        "v_max_f32_e32 v60, v59, v7\n\t"
        "v_max_f32_e32 v59, v59, v7\n\t"
        "v_cndmask_b32_e64 v0, v0, v4, s[38:39]\n\t"
        "v_cndmask_b32_e64 v1, v1, v5, s[38:39]\n\t"
        "v_permlane32_swap_b32_e32 v59, v60\n\t"
        "v_cndmask_b32_e64 v2, v2, v6, s[38:39]\n\t"
        "v_cndmask_b32_e64 v3, v3, v7, s[38:39]\n\t"
        "v_max3_f32 v59, v59, v60, v8\n\t"
        "v_max3_f32 v59, v59, v9, v10\n\t"
        "v_max_f32_e32 v60, v59, v11\n\t"
        "v_max_f32_e32 v59, v59, v11\n\t"
        "v_cndmask_b32_e64 v8, v8, v9, s[38:39]\n\t"
        "v_cndmask_b32_e64 v10, v10, v11, s[38:39]\n\t"
        "v_permlane16_swap_b32_e32 v59, v60\n\t"
        "v_max_f32_e32 v59, v59, v60\n\t"
        "v_mul_f32_e32 v59, 0xbe02940c, v59\n\t"
        "v_cndmask_b32_e64 v58, v8, v10, s[40:41]\n\t"
        "v_fmamk_f32 v0, v0, 0x3e02940c, v59\n\t"
        "v_exp_f32_e32 v0, v0\n\t"
        "v_fmamk_f32 v1, v1, 0x3e02940c, v59\n\t"
        "v_exp_f32_e32 v1, v1\n\t"
        "v_fmamk_f32 v2, v2, 0x3e02940c, v59\n\t"
        "v_exp_f32_e32 v2, v2\n\t"
        "v_fmamk_f32 v3, v3, 0x3e02940c, v59\n\t"
        "v_exp_f32_e32 v3, v3\n\t"
        "v_fmamk_f32 v58, v58, 0x3e02940c, v59\n\t"
        "v_cvt_pk_bf16_f32 v12, v0, v1\n\t"
        "v_cvt_pk_bf16_f32 v13, v2, v3\n\t"
        "v_exp_f32_e32 v58, v58\n\t"
        "v_add_f32_e32 v60, v0, v1\n\t"
        "v_mov_b32_dpp v14, v12 row_ror:8 row_mask:0xf bank_mask:0xf\n\t"
        "v_mov_b32_dpp v15, v13 row_ror:8 row_mask:0xf bank_mask:0xf\n\t"
        "v_mov_b32_dpp v59, v58 row_ror:8 row_mask:0xf bank_mask:0xf\n\t"
        "v_add_f32_e32 v61, v2, v3\n\t"
        "s_waitcnt lgkmcnt(0)\n\t"
        "v_mfma_f32_16x16x32_bf16 v[4:7], v[48:51], v[12:15], 0\n\t"
        "v_cvt_pk_bf16_f32 v59, v58, v59\n\t"
        "v_add_f32_e32 v60, v60, v58\n\t"
        "v_mfma_f32_16x16x32_bf16 v[8:11], v[52:55], v[12:15], 0\n\t"
        "v_cndmask_b32_e64 v56, v59, 0, s[40:41]\n\t"
        "v_cndmask_b32_e64 v57, 0, v59, s[40:41]\n\t"
        "v_add_f32_e32 v59, v60, v61\n\t"
        "s_waitcnt vmcnt(1)\n\t"
        "v_max3_u32 v111, v104, v105, v106\n\t"
        "v_mfma_f32_16x16x16_bf16 v[4:7], v[104:105], v[56:57], v[4:7]\n\t"
        "v_add_f32_dpp v60, v59, v59 row_ror:8 row_mask:0xf bank_mask:0xf\n\t"
        "v_add_f32_dpp v61, v59, v59 row_ror:8 row_mask:0xf bank_mask:0xf\n\t"
        "v_mfma_f32_16x16x16_bf16 v[8:11], v[106:107], v[56:57], v[8:11]\n\t"
        "v_max_u32_e32 v111, v111, v107\n\t"
        "v_permlane16_swap_b32_e32 v60, v61\n\t"
        "v_add_f32_e32 v59, v60, v61\n\t"
        "v_add_f32_e32 v60, v60, v61\n\t"
        "v_cmp_eq_u32_e32 vcc, -1, v111\n\t"
        "s_nop 0\n\t"
        "v_permlane32_swap_b32_e32 v59, v60\n\t"
        "v_add_f32_e32 v59, v59, v60\n\t"
        "v_rcp_f32_e32 v59, v59\n\t"
        "s_cbranch_vccnz LA_vmfail%=\n\t"
        "LA_vmok%=:\n\t"
        "v_mul_f32_e32 v4, v4, v59\n\t"
        "v_mul_f32_e32 v5, v5, v59\n\t"
        "v_cvt_pk_bf16_f32 v0, v4, v5\n\t"
        "v_min_u32_e32 v0, -2, v0\n\t"
        "v_mul_f32_e32 v6, v6, v59\n\t"
        "v_mul_f32_e32 v7, v7, v59\n\t"
        "v_cvt_pk_bf16_f32 v1, v6, v7\n\t"
        "v_min_u32_e32 v1, -2, v1\n\t"
        "v_mul_f32_e32 v8, v8, v59\n\t"
        "v_mul_f32_e32 v9, v9, v59\n\t"
        "v_cvt_pk_bf16_f32 v2, v8, v9\n\t"
        "v_min_u32_e32 v2, -2, v2\n\t"
        "v_mul_f32_e32 v10, v10, v59\n\t"
        "v_mul_f32_e32 v11, v11, v59\n\t"
        "v_cvt_pk_bf16_f32 v3, v10, v11\n\t"
        "v_min_u32_e32 v3, -2, v3\n\t"
        "buffer_store_dwordx4 v[0:3], v121, s[8:11], s26 offen sc1\n\t"
        "s_memrealtime s[52:53]\n\t"
        "s_and_b32 s35, s43, 1\n\t"
        "s_lshl_b32 s35, s35, 12\n\t"
        "v_add_u32_e32 v109, s35, v124\n\t"
        "ds_read_b128 v[16:19], v109 offset:8192\n\t"
        "ds_read_b128 v[20:23], v109 offset:9216\n\t"
        "ds_read_b128 v[24:27], v109 offset:10240\n\t"
        "ds_read_b128 v[28:31], v109 offset:11264\n\t"
        "ds_read_b128 v[56:59], v124 offset:0\n\t"
        "ds_read_b128 v[60:63], v124 offset:4096\n\t"
        "ds_read_b128 v[12:15], v124 offset:1024\n\t"
        "v_xor_b32_e32 v7, 0x800, v108\n\t"
        "s_add_u32 s48, s24, 1\n\t"
        "s_min_u32 s48, s48, 63\n\t"
        "buffer_load_dwordx2 v[108:109], v125, s[8:11], s26 offen sc1\n\t"
        "s_lshl_b32 s35, s44, 7\n\t"
        "v_lshrrev_b32_e32 v111, 5, v125\n\t"
        "v_and_b32_e32 v111, 60, v111\n\t"
        "buffer_load_dword v111, v111, s[12:15], s35 offen offset:0 sc1\n\t"
        "s_lshr_b32 s36, s48, 1\n\t"
        "s_cmp_gt_u32 s54, s36\n\t"
        "s_cbranch_scc1 LA_kvok%=\n\t"
        "s_mov_b32 s42, 0\n\t"
        "LA_kv%=:\n\t"
        "s_waitcnt vmcnt(0)\n\t"
        "v_min_u32_dpp v111, v111, v111 quad_perm:[1,0,3,2] row_mask:0xf bank_mask:0xf\n\t"
        "s_nop 1\n\t"
        "v_min_u32_dpp v111, v111, v111 quad_perm:[2,3,0,1] row_mask:0xf bank_mask:0xf\n\t"
        "s_nop 1\n\t"
        "v_min_u32_dpp v111, v111, v111 row_ror:4 row_mask:0xf bank_mask:0xf\n\t"
        "s_nop 1\n\t"
        "v_min_u32_dpp v111, v111, v111 row_ror:8 row_mask:0xf bank_mask:0xf\n\t"
        "s_nop 0\n\t"
        "v_readfirstlane_b32 s54, v111\n\t"
        "s_cmp_gt_u32 s54, s36\n\t"
        "s_cbranch_scc1 LA_kvok%=\n\t"
        "s_sleep 4\n\t"
        "s_lshl_b32 s35, s44, 7\n\t"
        "v_lshrrev_b32_e32 v111, 5, v125\n\t"
        "v_and_b32_e32 v111, 60, v111\n\t"
        "buffer_load_dword v111, v111, s[12:15], s35 offen offset:0 sc1\n\t"
        "s_add_u32 s42, s42, 1\n\t"
        "s_cmp_lt_u32 s42, 0x100000\n\t"
        "s_cbranch_scc1 LA_kv%=\n\t"
        "s_movk_i32 s34, 16\n\t"
        "s_branch LA_die%=\n\t"
        "LA_kvok%=:\n\t"
        "s_add_u32 s35, s24, 1\n\t"
        "s_and_b32 s36, s35, 1\n\t"
        "s_mul_i32 s36, s36, 24576\n\t"
        "s_add_u32 s36, s36, s55\n\t"
        "s_lshl_b32 s37, s48, 18\n\t"
        "s_lshl_b32 s34, s43, 11\n\t"
        "s_add_u32 s37, s37, s34\n\t"
        "s_waitcnt lgkmcnt(3)\n\t"
        "v_mfma_f32_16x16x32_bf16 v[8:11], v[32:35], v[16:19], 0\n\t"
        "v_mfma_f32_16x16x32_bf16 v[8:11], v[36:39], v[20:23], v[8:11]\n\t"
        "v_mfma_f32_16x16x32_bf16 v[8:11], v[40:43], v[24:27], v[8:11]\n\t"
        "v_mfma_f32_16x16x32_bf16 v[8:11], v[44:47], v[28:31], v[8:11]\n\t"
        "v_mov_b32_e32 v43, v7\n\t"
        "s_waitcnt lgkmcnt(2)\n\t"
        "v_mfma_f32_16x16x32_bf16 v[0:3], v[56:59], v[16:19], 0\n\t"
        "ds_read_b128 v[56:59], v124 offset:5120\n\t"
        "s_waitcnt lgkmcnt(2)\n\t"
        "v_mfma_f32_16x16x32_bf16 v[4:7], v[60:63], v[16:19], 0\n\t"
        "ds_read_b128 v[60:63], v124 offset:2048\n\t"
        "s_waitcnt lgkmcnt(2)\n\t"
        "v_mfma_f32_16x16x32_bf16 v[0:3], v[12:15], v[20:23], v[0:3]\n\t"
        "ds_read_b128 v[12:15], v124 offset:6144\n\t"
        "s_waitcnt lgkmcnt(2)\n\t"
        "v_mfma_f32_16x16x32_bf16 v[4:7], v[56:59], v[20:23], v[4:7]\n\t"
        "ds_read_b128 v[56:59], v124 offset:3072\n\t"
        "s_waitcnt lgkmcnt(2)\n\t"
        "v_mfma_f32_16x16x32_bf16 v[0:3], v[60:63], v[24:27], v[0:3]\n\t"
        "ds_read_b128 v[60:63], v124 offset:7168\n\t"
        "s_waitcnt lgkmcnt(2)\n\t"
        "v_mfma_f32_16x16x32_bf16 v[4:7], v[12:15], v[24:27], v[4:7]\n\t"
        "v_xor_b32_e32 v42, 0x200, v120\n\t"
        "buffer_load_dwordx4 v[12:15], v42, s[8:11], s26 offen sc1\n\t"
        "s_waitcnt lgkmcnt(1)\n\t"
        "v_mfma_f32_16x16x32_bf16 v[0:3], v[56:59], v[28:31], v[0:3]\n\t"
        "s_waitcnt lgkmcnt(0)\n\t"
        "v_mfma_f32_16x16x32_bf16 v[4:7], v[60:63], v[28:31], v[4:7]\n\t"
        "ds_read_b128 v[56:59], v43 offset:16384\n\t"
        "ds_read_b128 v[60:63], v43 offset:17408\n\t"
        "v_mov_b64_e32 v[44:45], -1\n\t"
        "v_mov_b64_e32 v[46:47], -1\n\t"
        "v_mov_b32_e32 v40, 0xce6e6b28\n\t"
        "s_add_u32 m0, s36, 0\n\t"
        "s_add_u32 s34, s37, 29548544\n\t"
        "buffer_load_dwordx4 v122, s[12:15], s34 offen sc1 lds\n\t"
        "v_cvt_pk_bf16_f32 v8, 0, v8\n\t"
        "v_cvt_pk_bf16_f32 v9, 0, v9\n\t"
        "v_cvt_pk_bf16_f32 v10, 0, v10\n\t"
        "v_cvt_pk_bf16_f32 v11, 0, v11\n\t"
        "v_cvt_pk_bf16_f32 v0, 0, v0\n\t"
        "v_cvt_pk_bf16_f32 v1, 0, v1\n\t"
        "s_add_u32 m0, s36, 1024\n\t"
        "s_add_u32 s34, s37, 29549568\n\t"
        "buffer_load_dwordx4 v122, s[12:15], s34 offen sc1 lds\n\t"
        "v_cvt_pk_bf16_f32 v2, 0, v2\n\t"
        "v_cvt_pk_bf16_f32 v3, 0, v3\n\t"
        "v_cvt_pk_bf16_f32 v4, 0, v4\n\t"
        "v_cvt_pk_bf16_f32 v5, 0, v5\n\t"
        "v_cvt_pk_bf16_f32 v6, 0, v6\n\t"
        "v_cvt_pk_bf16_f32 v7, 0, v7\n\t"
        "s_add_u32 m0, s36, 8192\n\t"
        "s_add_u32 s34, s37, 12771328\n\t"
        "buffer_load_dwordx4 v122, s[12:15], s34 offen sc1 lds\n\t"
        "v_mul_f32_e32 v8, 0x3db504f3, v8\n\t"
        "v_mul_f32_e32 v9, 0x3db504f3, v9\n\t"
        "v_mul_f32_e32 v10, 0x3db504f3, v10\n\t"
        "v_mul_f32_e32 v11, 0x3db504f3, v11\n\t"
        "v_mul_f32_e32 v0, 0x3db504f3, v0\n\t"
        "v_mul_f32_e32 v1, 0x3db504f3, v1\n\t"
        "s_add_u32 m0, s36, 9216\n\t"
        "s_add_u32 s34, s37, 12772352\n\t"
        "buffer_load_dwordx4 v122, s[12:15], s34 offen sc1 lds\n\t"
        "v_mul_f32_e32 v2, 0x3db504f3, v2\n\t"
        "v_mul_f32_e32 v3, 0x3db504f3, v3\n\t"
        "v_mul_f32_e32 v4, 0x3db504f3, v4\n\t"
        "v_mul_f32_e32 v5, 0x3db504f3, v5\n\t"
        "v_mul_f32_e32 v6, 0x3db504f3, v6\n\t"
        "v_mul_f32_e32 v7, 0x3db504f3, v7\n\t"
        "s_add_u32 m0, s36, 16384\n\t"
        "s_add_u32 s34, s37, 46325760\n\t"
        "buffer_load_dwordx4 v122, s[12:15], s34 offen sc1 lds\n\t"
        "v_cvt_pk_bf16_f32 v8, 0, v8\n\t"
        "v_cvt_pk_bf16_f32 v9, 0, v9\n\t"
        "v_cvt_pk_bf16_f32 v10, 0, v10\n\t"
        "v_cvt_pk_bf16_f32 v11, 0, v11\n\t"
        "v_cvt_pk_bf16_f32 v0, 0, v0\n\t"
        "v_cvt_pk_bf16_f32 v1, 0, v1\n\t"
        "s_add_u32 m0, s36, 17408\n\t"
        "s_add_u32 s34, s37, 46326784\n\t"
        "buffer_load_dwordx4 v122, s[12:15], s34 offen sc1 lds\n\t"
        "v_cvt_pk_bf16_f32 v2, 0, v2\n\t"
        "v_cvt_pk_bf16_f32 v3, 0, v3\n\t"
        "v_cvt_pk_bf16_f32 v4, 0, v4\n\t"
        "v_cvt_pk_bf16_f32 v5, 0, v5\n\t"
        "v_cvt_pk_bf16_f32 v6, 0, v6\n\t"
        "v_cvt_pk_bf16_f32 v7, 0, v7\n\t"
        "buffer_store_dwordx4 v[44:47], v121, s[8:11], s28 offen sc1\n\t"
        "s_pack_ll_b32_b16 s34, s52, s24\n\t"
        "v_mov_b32_e32 v42, s34\n\t"
        "s_add_u32 s35, s26, s57\n\t"
        "s_mov_b64 exec, 1\n\t"
        "buffer_store_dword v42, off, s[8:11], s35 sc1\n\t"
        "s_mov_b64 exec, -1\n\t"
        "v_cndmask_b32_e64 v0, v40, v0, s[60:61]\n\t"
        "v_cndmask_b32_e64 v1, v40, v1, s[62:63]\n\t"
        "v_cndmask_b32_e64 v2, v40, v2, s[64:65]\n\t"
        "v_cndmask_b32_e64 v3, v40, v3, s[66:67]\n\t"
        "v_cndmask_b32_e64 v4, v40, v4, s[68:69]\n\t"
        "v_cndmask_b32_e64 v5, v40, v5, s[70:71]\n\t"
        "v_cndmask_b32_e64 v6, v40, v6, s[72:73]\n\t"
        "v_cndmask_b32_e64 v7, v40, v7, s[74:75]\n\t"
        "v_max3_f32 v41, v0, v1, v2\n\t"
        "v_max3_f32 v41, v41, v3, v4\n\t"
        "v_max3_f32 v41, v41, v5, v6\n\t"
        "v_max3_f32 v41, v41, v7, v8\n\t"
        "v_max3_f32 v41, v41, v9, v10\n\t"
        "v_max_f32_e32 v42, v41, v11\n\t"
        "v_max_f32_e32 v41, v41, v11\n\t"
        "s_nop 1\n\t"
        "v_permlane16_swap_b32_e32 v41, v42\n\t"
        "v_max_f32_e32 v43, v41, v42\n\t"
        "v_max_f32_e32 v41, v41, v42\n\t"
        "s_nop 1\n\t"
        "v_permlane32_swap_b32_e32 v41, v43\n\t"
        "v_max_f32_e32 v41, v41, v43\n\t"
        "v_mul_f32_e32 v41, 0xbfb8aa3b, v41\n\t"
        "v_fmamk_f32 v0, v0, 0x3fb8aa3b, v41\n\t"
        "v_exp_f32_e32 v0, v0\n\t"
        "v_fmamk_f32 v1, v1, 0x3fb8aa3b, v41\n\t"
        "v_exp_f32_e32 v1, v1\n\t"
        "v_fmamk_f32 v2, v2, 0x3fb8aa3b, v41\n\t"
        "v_exp_f32_e32 v2, v2\n\t"
        "v_fmamk_f32 v3, v3, 0x3fb8aa3b, v41\n\t"
        "v_exp_f32_e32 v3, v3\n\t"
        "v_fmamk_f32 v4, v4, 0x3fb8aa3b, v41\n\t"
        "v_exp_f32_e32 v4, v4\n\t"
        "v_fmamk_f32 v5, v5, 0x3fb8aa3b, v41\n\t"
        "v_exp_f32_e32 v5, v5\n\t"
        "v_fmamk_f32 v6, v6, 0x3fb8aa3b, v41\n\t"
        "v_exp_f32_e32 v6, v6\n\t"
        "v_fmamk_f32 v7, v7, 0x3fb8aa3b, v41\n\t"
        "v_exp_f32_e32 v7, v7\n\t"
        "v_fmamk_f32 v8, v8, 0x3fb8aa3b, v41\n\t"
        "v_exp_f32_e32 v8, v8\n\t"
        "v_fmamk_f32 v9, v9, 0x3fb8aa3b, v41\n\t"
        "v_exp_f32_e32 v9, v9\n\t"
        "v_fmamk_f32 v10, v10, 0x3fb8aa3b, v41\n\t"
        "v_exp_f32_e32 v10, v10\n\t"
        "v_fmamk_f32 v11, v11, 0x3fb8aa3b, v41\n\t"
        "v_exp_f32_e32 v11, v11\n\t"
        "v_cndmask_b32_e64 v8, v8, 0, s[40:41]\n\t"
        "v_cndmask_b32_e64 v9, v9, 0, s[40:41]\n\t"
        "v_cndmask_b32_e64 v10, v10, 0, s[40:41]\n\t"
        "v_cndmask_b32_e64 v11, v11, 0, s[40:41]\n\t"
        "v_add_f32_e32 v41, v0, v1\n\t"
        "v_add_f32_e32 v41, v41, v2\n\t"
        "v_add_f32_e32 v41, v41, v3\n\t"
        "v_add_f32_e32 v41, v41, v4\n\t"
        "v_add_f32_e32 v41, v41, v5\n\t"
        "v_add_f32_e32 v41, v41, v6\n\t"
        "v_add_f32_e32 v41, v41, v7\n\t"
        "v_add_f32_e32 v41, v41, v8\n\t"
        "v_add_f32_e32 v41, v41, v9\n\t"
        "v_add_f32_e32 v41, v41, v10\n\t"
        "v_add_f32_e32 v42, v41, v11\n\t"
        "v_add_f32_e32 v41, v41, v11\n\t"
        "s_nop 1\n\t"
        "v_permlane16_swap_b32_e32 v41, v42\n\t"
        "v_add_f32_e32 v43, v41, v42\n\t"
        "v_add_f32_e32 v41, v41, v42\n\t"
        "s_nop 1\n\t"
        "v_permlane32_swap_b32_e32 v41, v43\n\t"
        "v_add_f32_e32 v41, v41, v43\n\t"
        "v_rcp_f32_e32 v41, v41\n\t"
        "s_nop 0\n\t"
        "v_mul_f32_e32 v0, v0, v41\n\t"
        "v_mul_f32_e32 v1, v1, v41\n\t"
        "v_cvt_pk_bf16_f32 v32, v0, v1\n\t"
        "v_mul_f32_e32 v2, v2, v41\n\t"
        "v_mul_f32_e32 v3, v3, v41\n\t"
        "v_cvt_pk_bf16_f32 v33, v2, v3\n\t"
        "v_mul_f32_e32 v4, v4, v41\n\t"
        "v_mul_f32_e32 v5, v5, v41\n\t"
        "v_cvt_pk_bf16_f32 v34, v4, v5\n\t"
        "v_mul_f32_e32 v6, v6, v41\n\t"
        "v_mul_f32_e32 v7, v7, v41\n\t"
        "v_cvt_pk_bf16_f32 v35, v6, v7\n\t"
        "v_mul_f32_e32 v8, v8, v41\n\t"
        "v_mul_f32_e32 v9, v9, v41\n\t"
        "v_cvt_pk_bf16_f32 v36, v8, v9\n\t"
        "v_mul_f32_e32 v10, v10, v41\n\t"
        "v_mul_f32_e32 v11, v11, v41\n\t"
        "v_cvt_pk_bf16_f32 v37, v10, v11\n\t"
        "s_waitcnt lgkmcnt(0)\n\t"
        "v_mfma_f32_16x16x32_bf16 v[16:19], v[48:51], v[32:35], 0\n\t"
        "v_mfma_f32_16x16x32_bf16 v[20:23], v[52:55], v[32:35], 0\n\t"
        "v_mfma_f32_16x16x32_bf16 v[24:27], v[56:59], v[32:35], 0\n\t"
        "v_mfma_f32_16x16x32_bf16 v[28:31], v[60:63], v[32:35], 0\n\t"
        "s_waitcnt vmcnt(8)\n\t"
        "v_max3_u32 v43, v12, v13, v14\n\t"
        "v_max_u32_e32 v43, v43, v15\n\t"
        "v_cmp_eq_u32_e32 vcc, -1, v43\n\t"
        "s_nop 0\n\t"
        "s_cbranch_vccnz LA_v2fail%=\n\t"
        "LA_v2ok%=:\n\t"
        "v_mfma_f32_16x16x16_bf16 v[16:19], v[104:105], v[36:37], v[16:19]\n\t"
        "v_mfma_f32_16x16x16_bf16 v[20:23], v[106:107], v[36:37], v[20:23]\n\t"
        "v_mfma_f32_16x16x16_bf16 v[24:27], v[12:13], v[36:37], v[24:27]\n\t"
        "v_mfma_f32_16x16x16_bf16 v[28:31], v[14:15], v[36:37], v[28:31]\n\t"
        "s_lshl_b32 s34, s24, 18\n\t"
        "s_add_u32 s34, s34, 63102976\n\t"
        "v_xor_b32_e32 v43, 0x400, v123\n\t"
        "s_nop 1\n\t"
        "v_cvt_pk_bf16_f32 v0, v16, v17\n\t"
        "v_cvt_pk_bf16_f32 v1, v18, v19\n\t"
        "v_cvt_pk_bf16_f32 v2, v20, v21\n\t"
        "v_cvt_pk_bf16_f32 v3, v22, v23\n\t"
        "buffer_store_dwordx4 v[0:3], v123, s[12:15], s34 offen sc1\n\t"
        "v_cvt_pk_bf16_f32 v4, v24, v25\n\t"
        "v_cvt_pk_bf16_f32 v5, v26, v27\n\t"
        "v_cvt_pk_bf16_f32 v6, v28, v29\n\t"
        "v_cvt_pk_bf16_f32 v7, v30, v31\n\t"
        "buffer_store_dwordx4 v[4:7], v43, s[12:15], s34 offen sc1\n\t"
        "s_add_u32 s34, s24, 1\n\t"
        "v_mov_b32_e32 v126, s34\n\t"
        "s_cmp_eq_u32 s24, 63\n\t"
        "s_cbranch_scc1 LA_last%=\n\t"
        "v_add_u32_e32 v124, s56, v124\n\t"
        "s_sub_u32 s56, 0, s56\n\t"
        "ds_read_b128 v[32:35], v117 offset:0\n\t"
        "ds_read_b128 v[36:39], v117 offset:1024\n\t"
        "ds_read_b128 v[40:43], v117 offset:2048\n\t"
        "ds_read_b128 v[44:47], v117 offset:3072\n\t"
        "ds_read_b128 v[48:51], v117 offset:4096\n\t"
        "ds_read_b128 v[52:55], v117 offset:5120\n\t"
        "ds_read_b128 v[56:59], v117 offset:6144\n\t"
        "ds_read_b128 v[60:63], v117 offset:7168\n\t"
        "v_mov_b64_e32 v[0:1], 0\n\t"
        "v_mov_b64_e32 v[2:3], 0\n\t"
        "v_mov_b64_e32 v[4:5], 0\n\t"
        "v_mov_b64_e32 v[6:7], 0\n\t"
        "v_mov_b64_e32 v[8:9], 0\n\t"
        "v_mov_b64_e32 v[10:11], 0\n\t"
        "v_mov_b64_e32 v[12:13], 0\n\t"
        "v_mov_b64_e32 v[14:15], 0\n\t"
        "v_min_u32_dpp v111, v111, v111 quad_perm:[1,0,3,2] row_mask:0xf bank_mask:0xf\n\t"
        "s_nop 1\n\t"
        "v_min_u32_dpp v111, v111, v111 quad_perm:[2,3,0,1] row_mask:0xf bank_mask:0xf\n\t"
        "s_nop 1\n\t"
        "v_min_u32_dpp v111, v111, v111 row_ror:4 row_mask:0xf bank_mask:0xf\n\t"
        "s_nop 1\n\t"
        "v_min_u32_dpp v111, v111, v111 row_ror:8 row_mask:0xf bank_mask:0xf\n\t"
        "s_nop 0\n\t"
        "v_readfirstlane_b32 s54, v111\n\t"
        "v_and_b32_e32 v30, 0xff, v109\n\t"
        "v_cmp_ne_u32_e32 vcc, s24, v30\n\t"
        "s_cbranch_vccz LA_stok%=\n\t"
        "s_mov_b32 s42, 0\n\t"
        "LA_stre%=:\n\t"
        "buffer_load_dwordx2 v[108:109], v125, s[8:11], s26 offen sc1\n\t"
        "s_waitcnt vmcnt(0)\n\t"
        "v_and_b32_e32 v30, 0xff, v109\n\t"
        "v_cmp_ne_u32_e32 vcc, s24, v30\n\t"
        "s_cbranch_vccz LA_stok%=\n\t"
        "s_add_u32 s42, s42, 1\n\t"
        "s_cmp_lt_u32 s42, 0x100000\n\t"
        "s_cbranch_scc1 LA_stre%=\n\t"
        "s_movk_i32 s34, 5\n\t"
        "s_branch LA_die%=\n\t"
        "LA_stok%=:\n\t"
        "v_readfirstlane_b32 s35, v109\n\t"
        "v_subrev_u32_e32 v108, s25, v108\n\t"
        "s_bfe_u32 s29, s35, 0xc0008\n\t"
        "s_lshr_b32 s30, s35, 20\n\t"
        "v_max_i32_dpp v108, v108, v108 quad_perm:[1,0,3,2] row_mask:0xf bank_mask:0xf\n\t"
        "s_nop 1\n\t"
        "v_max_i32_dpp v108, v108, v108 quad_perm:[2,3,0,1] row_mask:0xf bank_mask:0xf\n\t"
        "s_nop 1\n\t"
        "v_max_i32_dpp v108, v108, v108 row_ror:4 row_mask:0xf bank_mask:0xf\n\t"
        "s_nop 1\n\t"
        "v_max_i32_dpp v108, v108, v108 row_ror:8 row_mask:0xf bank_mask:0xf\n\t"
        "v_mov_b32_e32 v109, v108\n\t"
        "s_nop 1\n\t"
        "v_permlane16_swap_b32_e32 v108, v109\n\t"
        "v_max_i32_e32 v108, v108, v109\n\t"
        "v_mov_b32_e32 v109, v108\n\t"
        "s_nop 1\n\t"
        "v_permlane32_swap_b32_e32 v108, v109\n\t"
        "v_max_i32_e32 v108, v108, v109\n\t"
        "s_nop 0\n\t"
        "v_readfirstlane_b32 s36, v108\n\t"
        "s_add_u32 s35, s31, s50\n\t"
        "s_mul_i32 s37, s50, 3\n\t"
        "s_min_u32 s35, s35, s37\n\t"
        "s_add_i32 s34, s36, s51\n\t"
        "s_cmp_gt_i32 s34, 0\n\t"
        "s_cselect_b32 s31, s35, 0\n\t"
        "s_cselect_b32 s34, s34, 0\n\t"
        "s_add_u32 s34, s34, s31\n\t"
        "s_add_u32 s37, s25, s34\n\t"
        "s_add_u32 s45, s37, s29\n\t"
        "s_add_u32 s25, s45, s30\n\t"
        "s_add_u32 s24, s24, 1\n\t"
        "s_mov_b32 s28, s26\n\t"
        "s_mov_b32 s26, s27\n\t"
        "s_add_u32 s27, s27, 0x80000\n\t"
        "s_and_b32 s27, s27, 0x1fffff\n\t"
        "s_branch LA_loop%=\n\t"
        "LA_last%=:\n\t"
        "s_waitcnt vmcnt(0)\n\t"
        "s_barrier\n\t"
        "buffer_store_dword v126, v127, s[12:15], 0 offen sc1\n\t"
        "s_endpgm\n\t"
        "LA_v2fail%=:\n\t"
        "s_mov_b32 s42, 0\n\t"
        "LA_v2re%=:\n\t"
        "v_xor_b32_e32 v43, 0x200, v120\n\t"
        "buffer_load_dwordx4 v[12:15], v43, s[8:11], s26 offen sc1\n\t"
        "s_waitcnt vmcnt(0)\n\t"
        "v_max3_u32 v43, v12, v13, v14\n\t"
        "v_max_u32_e32 v43, v43, v15\n\t"
        "v_cmp_eq_u32_e32 vcc, -1, v43\n\t"
        "s_nop 0\n\t"
        "s_cbranch_vccz LA_v2ok%=\n\t"
        "s_add_u32 s42, s42, 1\n\t"
        "s_cmp_lt_u32 s42, 0x100000\n\t"
        "s_cbranch_scc1 LA_v2re%=\n\t"
        "s_movk_i32 s34, 6\n\t"
        "s_branch LA_die%=\n\t"
        "LA_rdfail%=:\n\t"
        "s_or_b32 s47, s47, 1\n\t"
        "buffer_load_dwordx4 v[16:19], v112, s[8:11], s26 offen offset:0 sc1\n\t"
        "buffer_load_dwordx4 v[20:23], v112, s[8:11], s26 offen offset:1024 sc1\n\t"
        "buffer_load_dwordx4 v[24:27], v112, s[8:11], s26 offen offset:2048 sc1\n\t"
        "buffer_load_dwordx4 v[28:31], v112, s[8:11], s26 offen offset:3072 sc1\n\t"
        "s_nop 7\n\t"
        "ds_read_b128 v[32:35], v117 offset:0\n\t"
        "ds_read_b128 v[36:39], v117 offset:1024\n\t"
        "ds_read_b128 v[40:43], v117 offset:2048\n\t"
        "ds_read_b128 v[44:47], v117 offset:3072\n\t"
        "ds_read_b128 v[48:51], v117 offset:4096\n\t"
        "ds_read_b128 v[52:55], v117 offset:5120\n\t"
        "ds_read_b128 v[56:59], v117 offset:6144\n\t"
        "ds_read_b128 v[60:63], v117 offset:7168\n\t"
        "v_mov_b64_e32 v[0:1], 0\n\t"
        "v_mov_b64_e32 v[2:3], 0\n\t"
        "v_mov_b64_e32 v[4:5], 0\n\t"
        "v_mov_b64_e32 v[6:7], 0\n\t"
        "v_mov_b64_e32 v[8:9], 0\n\t"
        "v_mov_b64_e32 v[10:11], 0\n\t"
        "v_mov_b64_e32 v[12:13], 0\n\t"
        "v_mov_b64_e32 v[14:15], 0\n\t"
        "s_branch LA_rdre%=w\n\t"
        "LA_rdre%=:\n\t"
        "buffer_load_dwordx4 v[16:19], v112, s[8:11], s26 offen offset:0 sc1\n\t"
        "buffer_load_dwordx4 v[20:23], v112, s[8:11], s26 offen offset:1024 sc1\n\t"
        "buffer_load_dwordx4 v[24:27], v112, s[8:11], s26 offen offset:2048 sc1\n\t"
        "buffer_load_dwordx4 v[28:31], v112, s[8:11], s26 offen offset:3072 sc1\n\t"
        "LA_rdre%=w:\n\t"
        "s_waitcnt vmcnt(0) lgkmcnt(0)\n\t"
        "v_max3_u32 v111, v16, v17, v18\n\t"
        "v_max3_u32 v111, v111, v19, v20\n\t"
        "v_max3_u32 v111, v111, v21, v22\n\t"
        "v_max3_u32 v111, v111, v23, v24\n\t"
        "v_max3_u32 v111, v111, v25, v26\n\t"
        "v_max3_u32 v111, v111, v27, v28\n\t"
        "v_max3_u32 v111, v111, v29, v30\n\t"
        "v_max_u32_e32 v111, v111, v31\n\t"
        "v_cmp_eq_u32_e32 vcc, -1, v111\n\t"
        "s_nop 0\n\t"
        "s_cbranch_vccz LA_go%=\n\t"
        "s_add_u32 s42, s42, 1\n\t"
        "s_cmp_lt_u32 s42, 0x100000\n\t"
        "s_cbranch_scc1 LA_rdre%=\n\t"
        "s_movk_i32 s34, 1\n\t"
        "s_branch LA_die%=\n\t"
        "LA_vmfail%=:\n\t"
        "s_or_b32 s47, s47, 2\n\t"
        "s_mov_b32 s42, 0\n\t"
        "LA_vmre%=:\n\t"
        "buffer_load_dwordx4 v[104:107], v120, s[8:11], s26 offen sc1\n\t"
        "v_mfma_f32_16x16x32_bf16 v[4:7], v[48:51], v[12:15], 0\n\t"
        "v_mfma_f32_16x16x32_bf16 v[8:11], v[52:55], v[12:15], 0\n\t"
        "s_nop 7\n\t"
        "s_waitcnt vmcnt(0)\n\t"
        "v_mfma_f32_16x16x16_bf16 v[4:7], v[104:105], v[56:57], v[4:7]\n\t"
        "v_mfma_f32_16x16x16_bf16 v[8:11], v[106:107], v[56:57], v[8:11]\n\t"
        "v_max3_u32 v111, v104, v105, v106\n\t"
        "v_max_u32_e32 v111, v111, v107\n\t"
        "v_cmp_eq_u32_e32 vcc, -1, v111\n\t"
        "s_nop 7\n\t"
        "s_cbranch_vccz LA_vmok%=\n\t"
        "s_add_u32 s42, s42, 1\n\t"
        "s_cmp_lt_u32 s42, 0x100000\n\t"
        "s_cbranch_scc1 LA_vmre%=\n\t"
        "s_movk_i32 s34, 2\n\t"
        "s_branch LA_die%=\n\t"
        "LA_die%=:\n\t"
        "v_mov_b32_e32 v30, s34\n\t"
        "s_mov_b32 s35, 33024\n\t"
        "s_mov_b64 exec, 1\n\t"
        "buffer_store_dword v30, off, s[12:15], s35 sc1\n\t"
        "s_endpgm\n\t"
        :: REC_S_IN, "{v96}"(g_w), "{v97}"(wb0), "{v98}"(wb1), "{v99}"(rb0), "{v100}"(rb0 ^ 32u), "{v101}"(rb0 ^ 64u), "{v102}"(rb0 ^ 96u),
           "{v112}"(a_mem), "{v113}"(l_ms + w * 4096), "{v114}"(l_ms + ((w + 1) & 3) * 4096), "{v115}"(l_ms + ((w + 2) & 3) * 4096), "{v116}"(l_ms + ((w + 3) & 3) * 4096),
           "{v117}"(lb + w * 22528 + l * 16), "{v118}"(l_iw), "{v119}"(l_iq), "{v120}"(a_vm), "{v121}"(a_o), "{v122}"(h * 8192 + l * 16), "{v123}"(a_ox), "{v124}"(lb + L_KV + l * 16), "{s55}"(lb + L_KV + w * 2048),
           "{v125}"((24 + cp) * 16384 + l * 128), "{v126}"(16 * (w & 1) + n - 4 * g), "{v103}"(pp ? 0xEEEEEEEEu : 0x44444444u),
           "{v127}"((l == 0 && w == 0) ? F_PROG + h * 128 : OOB)
        : "memory", "vcc", "scc", "v0", "v255", "a0", "a255", "s101");
    __builtin_unreachable();
}
__device__ __forceinline__ void rec_b(const Params& p, unsigned lb, const int wv, const int id, const int rg, const int cp, const int rot, const int bq) {
    const int w = wv, l = otid() & 63, n = l & 15, g = l >> 4, cs = id, rb = 32 + id * 2 + rg;
    const v4u sh = mk_srd(p.ws + hx_base(p.ws), HX_BYTES - 8192), sw = mk_srd(p.ws, W_TOTAL), s1 = mk_srd(p.Wv, 1024 * 4096 * 2), s2 = mk_srd(p.Wo, 1024 * 4096 * 2);
    const int r8 = l >> 3, pc = l & 7, pp = n >> 3, bb = (n >> 2) & 1, f = (g & 1) | (pp << 1);
    const unsigned stg = lb + LB_STG + w * LB_STGP;
    const unsigned g_v = r8 * 8192 + cs * 256 + rg * 128 + pc * 16, g_o = (l >> 2) * 2048 + cs * 64 + (l & 3) * 16;
    const unsigned wb0 = stg + r8 * 128 + (((pc >> 1) ^ (r8 >> 2)) << 5) + (pc & 1) * 16, wb1 = wb0 ^ 64u;
    const unsigned rb0 = stg + (16 * (g >> 1) + 8 * pp + 4 * (g & 1) + bb) * 128 + (f << 5) + (n & 3) * 8;
    const unsigned o_r = stg + pp * (2048 + 128) + (g & 1) * (1024 + 32) + (bb + 4 * (g >> 1)) * 64 + (n & 3) * 8;
    const unsigned a_mem = cp * 32768 + (w >> 1) * 16384 + (w & 1) * 4096 + (g >> 1) * 512 + ((n & 7) >> 2) * 256 + ((g & 1) * 2 + pp) * 64 + (n & 3) * 16;
    const unsigned a_vmw = w < 2 ? (16 + (cs >> 2)) * 16384 + (cs & 3) * 2048 + (2 * rg + w) * 512 + n * 32 + (g & 1) * 16 + (g >> 1) * 8 : OOB;
    const unsigned a_or = (8 + rg * 4 + w) * 16384 + (n >> 2) * 256 + g * 64 + (n & 3) * 16;
    const unsigned a_mw = (RPL != 0 && w == 3) ? OOB : w * 32768 + (cs >> 4) * 16384 + ((cs >> 1) & 7) * 1024 + (cs & 1) * 512 + rg * 256 + l * 4;
    const unsigned a_sw = (w == 0 && l < 4) ? (24 + l) * 16384 + (id * 2 + rg) * 128 : OOB;
    const bf16_t* m0p = p.mem0 + (4 * rg + ((l >> 2) & 3)) * 1024 + cs * 32 + g * 8 + (l & 3) * 2;
    const int tk = bq * 4 + w, th = ((cp * 10 + 2) & ~3) + 1 + 4 * (tk / 18), tm = (tk / 9) & 1;
    const bool ton = TOUCH && bq < 99 && th < (cp == 2 ? 32 : cp * 10 + 10);
    const bf16_t* tp = ton ? (tm ? p.Wk : p.Wq) + (size_t)((((tm * 2 + 1) * 4 + 7 + tk % 9) & 15) * 64 + l) * 4096 + th * 128 + 64 : p.mem0;
    asm volatile(
        "global_load_dword v104, v[104:105], off\n\t"
        "global_load_dword v109, v[108:109], off\n\t"
        "s_add_u32 s35, s68, 0\n\t"
        "s_and_b32 s35, s35, 7\n\t"
        "s_lshl_b32 s69, s35, 10\n\t"
        "s_add_u32 s35, s68, 2\n\t"
        "s_and_b32 s35, s35, 7\n\t"
        "s_lshl_b32 s70, s35, 10\n\t"
        "s_add_u32 s35, s68, 4\n\t"
        "s_and_b32 s35, s35, 7\n\t"
        "s_lshl_b32 s71, s35, 10\n\t"
        "s_add_u32 s35, s68, 6\n\t"
        "s_and_b32 s35, s35, 7\n\t"
        "s_lshl_b32 s72, s35, 10\n\t"
        "s_lshl_b32 s34, s43, 21\n\t"
        "s_add_u32 s34, s34, 0\n\t"
        "buffer_load_dwordx4 v[128:131], v96, s[16:19], s34 offen nt\n\t"
        "s_add_u32 s34, s34, 0x10000\n\t"
        "buffer_load_dwordx4 v[132:135], v96, s[16:19], s34 offen nt\n\t"
        "s_add_u32 s34, s34, 0x10000\n\t"
        "buffer_load_dwordx4 v[136:139], v96, s[16:19], s34 offen nt\n\t"
        "s_add_u32 s34, s34, 0x10000\n\t"
        "buffer_load_dwordx4 v[140:143], v96, s[16:19], s34 offen nt\n\t"
        "s_add_u32 s34, s34, 0x10000\n\t"
        "buffer_load_dwordx4 v[144:147], v96, s[16:19], s34 offen nt\n\t"
        "s_add_u32 s34, s34, 0x10000\n\t"
        "buffer_load_dwordx4 v[148:151], v96, s[16:19], s34 offen nt\n\t"
        "s_add_u32 s34, s34, 0x10000\n\t"
        "buffer_load_dwordx4 v[152:155], v96, s[16:19], s34 offen nt\n\t"
        "s_add_u32 s34, s34, 0x10000\n\t"
        "buffer_load_dwordx4 v[156:159], v96, s[16:19], s34 offen nt\n\t"
        "s_lshl_b32 s34, s43, 21\n\t"
        "s_add_u32 s34, s34, 524288\n\t"
        "buffer_load_dwordx4 v[160:163], v96, s[16:19], s34 offen nt\n\t"
        "s_add_u32 s34, s34, 0x10000\n\t"
        "buffer_load_dwordx4 v[164:167], v96, s[16:19], s34 offen nt\n\t"
        "s_add_u32 s34, s34, 0x10000\n\t"
        "buffer_load_dwordx4 v[168:171], v96, s[16:19], s34 offen nt\n\t"
        "s_add_u32 s34, s34, 0x10000\n\t"
        "buffer_load_dwordx4 v[172:175], v96, s[16:19], s34 offen nt\n\t"
        "s_add_u32 s34, s34, 0x10000\n\t"
        "buffer_load_dwordx4 v[176:179], v96, s[16:19], s34 offen nt\n\t"
        "s_add_u32 s34, s34, 0x10000\n\t"
        "buffer_load_dwordx4 v[180:183], v96, s[16:19], s34 offen nt\n\t"
        "s_add_u32 s34, s34, 0x10000\n\t"
        "buffer_load_dwordx4 v[184:187], v96, s[16:19], s34 offen nt\n\t"
        "s_add_u32 s34, s34, 0x10000\n\t"
        "buffer_load_dwordx4 v[188:191], v96, s[16:19], s34 offen nt\n\t"
        "s_lshl_b32 s34, s43, 21\n\t"
        "s_add_u32 s34, s34, 1048576\n\t"
        "buffer_load_dwordx4 v[192:195], v96, s[16:19], s34 offen nt\n\t"
        "s_add_u32 s34, s34, 0x10000\n\t"
        "buffer_load_dwordx4 v[196:199], v96, s[16:19], s34 offen nt\n\t"
        "s_add_u32 s34, s34, 0x10000\n\t"
        "buffer_load_dwordx4 v[200:203], v96, s[16:19], s34 offen nt\n\t"
        "s_add_u32 s34, s34, 0x10000\n\t"
        "buffer_load_dwordx4 v[204:207], v96, s[16:19], s34 offen nt\n\t"
        "s_add_u32 s34, s34, 0x10000\n\t"
        "buffer_load_dwordx4 v[208:211], v96, s[16:19], s34 offen nt\n\t"
        "s_add_u32 s34, s34, 0x10000\n\t"
        "buffer_load_dwordx4 v[212:215], v96, s[16:19], s34 offen nt\n\t"
        "s_add_u32 s34, s34, 0x10000\n\t"
        "buffer_load_dwordx4 v[216:219], v96, s[16:19], s34 offen nt\n\t"
        "s_add_u32 s34, s34, 0x10000\n\t"
        "buffer_load_dwordx4 v[220:223], v96, s[16:19], s34 offen nt\n\t"
        "s_lshl_b32 s34, s43, 21\n\t"
        "s_add_u32 s34, s34, 1572864\n\t"
        "buffer_load_dwordx4 v[224:227], v96, s[16:19], s34 offen nt\n\t"
        "s_add_u32 s34, s34, 0x10000\n\t"
        "buffer_load_dwordx4 v[228:231], v96, s[16:19], s34 offen nt\n\t"
        "s_add_u32 s34, s34, 0x10000\n\t"
        "buffer_load_dwordx4 v[232:235], v96, s[16:19], s34 offen nt\n\t"
        "s_add_u32 s34, s34, 0x10000\n\t"
        "buffer_load_dwordx4 v[236:239], v96, s[16:19], s34 offen nt\n\t"
        "s_add_u32 s34, s34, 0x10000\n\t"
        "buffer_load_dwordx4 v[240:243], v96, s[16:19], s34 offen nt\n\t"
        "s_add_u32 s34, s34, 0x10000\n\t"
        "buffer_load_dwordx4 v[244:247], v96, s[16:19], s34 offen nt\n\t"
        "s_add_u32 s34, s34, 0x10000\n\t"
        "buffer_load_dwordx4 v[248:251], v96, s[16:19], s34 offen nt\n\t"
        "s_add_u32 s34, s34, 0x10000\n\t"
        "buffer_load_dwordx4 v[252:255], v96, s[16:19], s34 offen nt\n\t"
        "s_add_u32 s35, s68, 0\n\t"
        "s_and_b32 s35, s35, 7\n\t"
        "s_lshl_b32 s35, s35, 18\n\t"
        "s_lshl_b32 s34, s43, 21\n\t"
        "s_add_u32 s34, s34, s35\n\t"
        "buffer_load_dwordx4 a[0:3], v97, s[20:23], s34 offen\n\t"
        "s_add_u32 s34, s34, 0x8000\n\t"
        "buffer_load_dwordx4 a[4:7], v97, s[20:23], s34 offen\n\t"
        "s_add_u32 s34, s34, 0x8000\n\t"
        "buffer_load_dwordx4 a[8:11], v97, s[20:23], s34 offen\n\t"
        "s_add_u32 s34, s34, 0x8000\n\t"
        "buffer_load_dwordx4 a[12:15], v97, s[20:23], s34 offen\n\t"
        "s_add_u32 s34, s34, 0x8000\n\t"
        "buffer_load_dwordx4 a[16:19], v97, s[20:23], s34 offen\n\t"
        "s_add_u32 s34, s34, 0x8000\n\t"
        "buffer_load_dwordx4 a[20:23], v97, s[20:23], s34 offen\n\t"
        "s_add_u32 s34, s34, 0x8000\n\t"
        "buffer_load_dwordx4 a[24:27], v97, s[20:23], s34 offen\n\t"
        "s_add_u32 s34, s34, 0x8000\n\t"
        "buffer_load_dwordx4 a[28:31], v97, s[20:23], s34 offen\n\t"
        "s_add_u32 s35, s68, 1\n\t"
        "s_and_b32 s35, s35, 7\n\t"
        "s_lshl_b32 s35, s35, 18\n\t"
        "s_lshl_b32 s34, s43, 21\n\t"
        "s_add_u32 s34, s34, s35\n\t"
        "buffer_load_dwordx4 a[32:35], v97, s[20:23], s34 offen\n\t"
        "s_add_u32 s34, s34, 0x8000\n\t"
        "buffer_load_dwordx4 a[36:39], v97, s[20:23], s34 offen\n\t"
        "s_add_u32 s34, s34, 0x8000\n\t"
        "buffer_load_dwordx4 a[40:43], v97, s[20:23], s34 offen\n\t"
        "s_add_u32 s34, s34, 0x8000\n\t"
        "buffer_load_dwordx4 a[44:47], v97, s[20:23], s34 offen\n\t"
        "s_add_u32 s34, s34, 0x8000\n\t"
        "buffer_load_dwordx4 a[48:51], v97, s[20:23], s34 offen\n\t"
        "s_add_u32 s34, s34, 0x8000\n\t"
        "buffer_load_dwordx4 a[52:55], v97, s[20:23], s34 offen\n\t"
        "s_add_u32 s34, s34, 0x8000\n\t"
        "buffer_load_dwordx4 a[56:59], v97, s[20:23], s34 offen\n\t"
        "s_add_u32 s34, s34, 0x8000\n\t"
        "buffer_load_dwordx4 a[60:63], v97, s[20:23], s34 offen\n\t"
        "s_add_u32 s35, s68, 2\n\t"
        "s_and_b32 s35, s35, 7\n\t"
        "s_lshl_b32 s35, s35, 18\n\t"
        "s_lshl_b32 s34, s43, 21\n\t"
        "s_add_u32 s34, s34, s35\n\t"
        "buffer_load_dwordx4 a[64:67], v97, s[20:23], s34 offen\n\t"
        "s_add_u32 s34, s34, 0x8000\n\t"
        "buffer_load_dwordx4 a[68:71], v97, s[20:23], s34 offen\n\t"
        "s_add_u32 s34, s34, 0x8000\n\t"
        "buffer_load_dwordx4 a[72:75], v97, s[20:23], s34 offen\n\t"
        "s_add_u32 s34, s34, 0x8000\n\t"
        "buffer_load_dwordx4 a[76:79], v97, s[20:23], s34 offen\n\t"
        "s_add_u32 s34, s34, 0x8000\n\t"
        "buffer_load_dwordx4 a[80:83], v97, s[20:23], s34 offen\n\t"
        "s_add_u32 s34, s34, 0x8000\n\t"
        "buffer_load_dwordx4 a[84:87], v97, s[20:23], s34 offen\n\t"
        "s_add_u32 s34, s34, 0x8000\n\t"
        "buffer_load_dwordx4 a[88:91], v97, s[20:23], s34 offen\n\t"
        "s_add_u32 s34, s34, 0x8000\n\t"
        "buffer_load_dwordx4 a[92:95], v97, s[20:23], s34 offen\n\t"
        "s_waitcnt vmcnt(48)\n\t"
        "s_add_u32 s35, s68, 3\n\t"
        "s_and_b32 s35, s35, 7\n\t"
        "s_lshl_b32 s35, s35, 18\n\t"
        "s_lshl_b32 s34, s43, 21\n\t"
        "s_add_u32 s34, s34, s35\n\t"
        "buffer_load_dwordx4 a[96:99], v97, s[20:23], s34 offen\n\t"
        "s_add_u32 s34, s34, 0x8000\n\t"
        "buffer_load_dwordx4 a[100:103], v97, s[20:23], s34 offen\n\t"
        "s_add_u32 s34, s34, 0x8000\n\t"
        "buffer_load_dwordx4 a[104:107], v97, s[20:23], s34 offen\n\t"
        "s_add_u32 s34, s34, 0x8000\n\t"
        "buffer_load_dwordx4 a[108:111], v97, s[20:23], s34 offen\n\t"
        "s_add_u32 s34, s34, 0x8000\n\t"
        "buffer_load_dwordx4 a[112:115], v97, s[20:23], s34 offen\n\t"
        "s_add_u32 s34, s34, 0x8000\n\t"
        "buffer_load_dwordx4 a[116:119], v97, s[20:23], s34 offen\n\t"
        "s_add_u32 s34, s34, 0x8000\n\t"
        "buffer_load_dwordx4 a[120:123], v97, s[20:23], s34 offen\n\t"
        "s_add_u32 s34, s34, 0x8000\n\t"
        "buffer_load_dwordx4 a[124:127], v97, s[20:23], s34 offen\n\t"
        "ds_write_b128 v98, v[128:131] offset:0\n\t"
        "ds_write_b128 v99, v[132:135] offset:1024\n\t"
        "ds_write_b128 v98, v[136:139] offset:2048\n\t"
        "ds_write_b128 v99, v[140:143] offset:3072\n\t"
        "ds_write_b128 v98, v[144:147] offset:4096\n\t"
        "ds_write_b128 v99, v[148:151] offset:5120\n\t"
        "ds_write_b128 v98, v[152:155] offset:6144\n\t"
        "ds_write_b128 v99, v[156:159] offset:7168\n\t"
        "ds_read_b64_tr_b16 v[128:129], v100 offset:0\n\t"
        "ds_read_b64_tr_b16 v[130:131], v100 offset:256\n\t"
        "ds_read_b64_tr_b16 v[132:133], v100 offset:4096\n\t"
        "ds_read_b64_tr_b16 v[134:135], v100 offset:4352\n\t"
        "s_waitcnt lgkmcnt(8)\n\t"
        "ds_read_b64_tr_b16 v[136:137], v101 offset:0\n\t"
        "ds_read_b64_tr_b16 v[138:139], v101 offset:256\n\t"
        "ds_read_b64_tr_b16 v[140:141], v101 offset:4096\n\t"
        "ds_read_b64_tr_b16 v[142:143], v101 offset:4352\n\t"
        "s_waitcnt lgkmcnt(8)\n\t"
        "ds_read_b64_tr_b16 v[144:145], v102 offset:0\n\t"
        "ds_read_b64_tr_b16 v[146:147], v102 offset:256\n\t"
        "ds_read_b64_tr_b16 v[148:149], v102 offset:4096\n\t"
        "ds_read_b64_tr_b16 v[150:151], v102 offset:4352\n\t"
        "s_waitcnt lgkmcnt(8)\n\t"
        "ds_read_b64_tr_b16 v[152:153], v103 offset:0\n\t"
        "ds_read_b64_tr_b16 v[154:155], v103 offset:256\n\t"
        "ds_read_b64_tr_b16 v[156:157], v103 offset:4096\n\t"
        "ds_read_b64_tr_b16 v[158:159], v103 offset:4352\n\t"
        "s_waitcnt lgkmcnt(0)\n\t"
        "s_waitcnt vmcnt(48)\n\t"
        "s_add_u32 s35, s68, 4\n\t"
        "s_and_b32 s35, s35, 7\n\t"
        "s_lshl_b32 s35, s35, 18\n\t"
        "s_lshl_b32 s34, s43, 21\n\t"
        "s_add_u32 s34, s34, s35\n\t"
        "buffer_load_dwordx4 a[128:131], v97, s[20:23], s34 offen\n\t"
        "s_add_u32 s34, s34, 0x8000\n\t"
        "buffer_load_dwordx4 a[132:135], v97, s[20:23], s34 offen\n\t"
        "s_add_u32 s34, s34, 0x8000\n\t"
        "buffer_load_dwordx4 a[136:139], v97, s[20:23], s34 offen\n\t"
        "s_add_u32 s34, s34, 0x8000\n\t"
        "buffer_load_dwordx4 a[140:143], v97, s[20:23], s34 offen\n\t"
        "s_add_u32 s34, s34, 0x8000\n\t"
        "buffer_load_dwordx4 a[144:147], v97, s[20:23], s34 offen\n\t"
        "s_add_u32 s34, s34, 0x8000\n\t"
        "buffer_load_dwordx4 a[148:151], v97, s[20:23], s34 offen\n\t"
        "s_add_u32 s34, s34, 0x8000\n\t"
        "buffer_load_dwordx4 a[152:155], v97, s[20:23], s34 offen\n\t"
        "s_add_u32 s34, s34, 0x8000\n\t"
        "buffer_load_dwordx4 a[156:159], v97, s[20:23], s34 offen\n\t"
        "ds_write_b128 v98, v[160:163] offset:0\n\t"
        "ds_write_b128 v99, v[164:167] offset:1024\n\t"
        "ds_write_b128 v98, v[168:171] offset:2048\n\t"
        "ds_write_b128 v99, v[172:175] offset:3072\n\t"
        "ds_write_b128 v98, v[176:179] offset:4096\n\t"
        "ds_write_b128 v99, v[180:183] offset:5120\n\t"
        "ds_write_b128 v98, v[184:187] offset:6144\n\t"
        "ds_write_b128 v99, v[188:191] offset:7168\n\t"
        "ds_read_b64_tr_b16 v[160:161], v100 offset:0\n\t"
        "ds_read_b64_tr_b16 v[162:163], v100 offset:256\n\t"
        "ds_read_b64_tr_b16 v[164:165], v100 offset:4096\n\t"
        "ds_read_b64_tr_b16 v[166:167], v100 offset:4352\n\t"
        "s_waitcnt lgkmcnt(8)\n\t"
        "ds_read_b64_tr_b16 v[168:169], v101 offset:0\n\t"
        "ds_read_b64_tr_b16 v[170:171], v101 offset:256\n\t"
        "ds_read_b64_tr_b16 v[172:173], v101 offset:4096\n\t"
        "ds_read_b64_tr_b16 v[174:175], v101 offset:4352\n\t"
        "s_waitcnt lgkmcnt(8)\n\t"
        "ds_read_b64_tr_b16 v[176:177], v102 offset:0\n\t"
        "ds_read_b64_tr_b16 v[178:179], v102 offset:256\n\t"
        "ds_read_b64_tr_b16 v[180:181], v102 offset:4096\n\t"
        "ds_read_b64_tr_b16 v[182:183], v102 offset:4352\n\t"
        "s_waitcnt lgkmcnt(8)\n\t"
        "ds_read_b64_tr_b16 v[184:185], v103 offset:0\n\t"
        "ds_read_b64_tr_b16 v[186:187], v103 offset:256\n\t"
        "ds_read_b64_tr_b16 v[188:189], v103 offset:4096\n\t"
        "ds_read_b64_tr_b16 v[190:191], v103 offset:4352\n\t"
        "s_waitcnt lgkmcnt(0)\n\t"
        "s_waitcnt vmcnt(48)\n\t"
        "s_add_u32 s35, s68, 5\n\t"
        "s_and_b32 s35, s35, 7\n\t"
        "s_lshl_b32 s35, s35, 18\n\t"
        "s_lshl_b32 s34, s43, 21\n\t"
        "s_add_u32 s34, s34, s35\n\t"
        "buffer_load_dwordx4 a[160:163], v97, s[20:23], s34 offen\n\t"
        "s_add_u32 s34, s34, 0x8000\n\t"
        "buffer_load_dwordx4 a[164:167], v97, s[20:23], s34 offen\n\t"
        "s_add_u32 s34, s34, 0x8000\n\t"
        "buffer_load_dwordx4 a[168:171], v97, s[20:23], s34 offen\n\t"
        "s_add_u32 s34, s34, 0x8000\n\t"
        "buffer_load_dwordx4 a[172:175], v97, s[20:23], s34 offen\n\t"
        "s_add_u32 s34, s34, 0x8000\n\t"
        "buffer_load_dwordx4 a[176:179], v97, s[20:23], s34 offen\n\t"
        "s_add_u32 s34, s34, 0x8000\n\t"
        "buffer_load_dwordx4 a[180:183], v97, s[20:23], s34 offen\n\t"
        "s_add_u32 s34, s34, 0x8000\n\t"
        "buffer_load_dwordx4 a[184:187], v97, s[20:23], s34 offen\n\t"
        "s_add_u32 s34, s34, 0x8000\n\t"
        "buffer_load_dwordx4 a[188:191], v97, s[20:23], s34 offen\n\t"
        "ds_write_b128 v98, v[192:195] offset:0\n\t"
        "ds_write_b128 v99, v[196:199] offset:1024\n\t"
        "ds_write_b128 v98, v[200:203] offset:2048\n\t"
        "ds_write_b128 v99, v[204:207] offset:3072\n\t"
        "ds_write_b128 v98, v[208:211] offset:4096\n\t"
        "ds_write_b128 v99, v[212:215] offset:5120\n\t"
        "ds_write_b128 v98, v[216:219] offset:6144\n\t"
        "ds_write_b128 v99, v[220:223] offset:7168\n\t"
        "ds_read_b64_tr_b16 v[192:193], v100 offset:0\n\t"
        "ds_read_b64_tr_b16 v[194:195], v100 offset:256\n\t"
        "ds_read_b64_tr_b16 v[196:197], v100 offset:4096\n\t"
        "ds_read_b64_tr_b16 v[198:199], v100 offset:4352\n\t"
        "s_waitcnt lgkmcnt(8)\n\t"
        "ds_read_b64_tr_b16 v[200:201], v101 offset:0\n\t"
        "ds_read_b64_tr_b16 v[202:203], v101 offset:256\n\t"
        "ds_read_b64_tr_b16 v[204:205], v101 offset:4096\n\t"
        "ds_read_b64_tr_b16 v[206:207], v101 offset:4352\n\t"
        "s_waitcnt lgkmcnt(8)\n\t"
        "ds_read_b64_tr_b16 v[208:209], v102 offset:0\n\t"
        "ds_read_b64_tr_b16 v[210:211], v102 offset:256\n\t"
        "ds_read_b64_tr_b16 v[212:213], v102 offset:4096\n\t"
        "ds_read_b64_tr_b16 v[214:215], v102 offset:4352\n\t"
        "s_waitcnt lgkmcnt(8)\n\t"
        "ds_read_b64_tr_b16 v[216:217], v103 offset:0\n\t"
        "ds_read_b64_tr_b16 v[218:219], v103 offset:256\n\t"
        "ds_read_b64_tr_b16 v[220:221], v103 offset:4096\n\t"
        "ds_read_b64_tr_b16 v[222:223], v103 offset:4352\n\t"
        "s_waitcnt lgkmcnt(0)\n\t"
        "s_waitcnt vmcnt(48)\n\t"
        "s_add_u32 s35, s68, 6\n\t"
        "s_and_b32 s35, s35, 7\n\t"
        "s_lshl_b32 s35, s35, 18\n\t"
        "s_lshl_b32 s34, s43, 21\n\t"
        "s_add_u32 s34, s34, s35\n\t"
        "buffer_load_dwordx4 a[192:195], v97, s[20:23], s34 offen\n\t"
        "s_add_u32 s34, s34, 0x8000\n\t"
        "buffer_load_dwordx4 a[196:199], v97, s[20:23], s34 offen\n\t"
        "s_add_u32 s34, s34, 0x8000\n\t"
        "buffer_load_dwordx4 a[200:203], v97, s[20:23], s34 offen\n\t"
        "s_add_u32 s34, s34, 0x8000\n\t"
        "buffer_load_dwordx4 a[204:207], v97, s[20:23], s34 offen\n\t"
        "s_add_u32 s34, s34, 0x8000\n\t"
        "buffer_load_dwordx4 a[208:211], v97, s[20:23], s34 offen\n\t"
        "s_add_u32 s34, s34, 0x8000\n\t"
        "buffer_load_dwordx4 a[212:215], v97, s[20:23], s34 offen\n\t"
        "s_add_u32 s34, s34, 0x8000\n\t"
        "buffer_load_dwordx4 a[216:219], v97, s[20:23], s34 offen\n\t"
        "s_add_u32 s34, s34, 0x8000\n\t"
        "buffer_load_dwordx4 a[220:223], v97, s[20:23], s34 offen\n\t"
        "ds_write_b128 v98, v[224:227] offset:0\n\t"
        "ds_write_b128 v99, v[228:231] offset:1024\n\t"
        "ds_write_b128 v98, v[232:235] offset:2048\n\t"
        "ds_write_b128 v99, v[236:239] offset:3072\n\t"
        "ds_write_b128 v98, v[240:243] offset:4096\n\t"
        "ds_write_b128 v99, v[244:247] offset:5120\n\t"
        "ds_write_b128 v98, v[248:251] offset:6144\n\t"
        "ds_write_b128 v99, v[252:255] offset:7168\n\t"
        "ds_read_b64_tr_b16 v[224:225], v100 offset:0\n\t"
        "ds_read_b64_tr_b16 v[226:227], v100 offset:256\n\t"
        "ds_read_b64_tr_b16 v[228:229], v100 offset:4096\n\t"
        "ds_read_b64_tr_b16 v[230:231], v100 offset:4352\n\t"
        "s_waitcnt lgkmcnt(8)\n\t"
        "ds_read_b64_tr_b16 v[232:233], v101 offset:0\n\t"
        "ds_read_b64_tr_b16 v[234:235], v101 offset:256\n\t"
        "ds_read_b64_tr_b16 v[236:237], v101 offset:4096\n\t"
        "ds_read_b64_tr_b16 v[238:239], v101 offset:4352\n\t"
        "s_waitcnt lgkmcnt(8)\n\t"
        "ds_read_b64_tr_b16 v[240:241], v102 offset:0\n\t"
        "ds_read_b64_tr_b16 v[242:243], v102 offset:256\n\t"
        "ds_read_b64_tr_b16 v[244:245], v102 offset:4096\n\t"
        "ds_read_b64_tr_b16 v[246:247], v102 offset:4352\n\t"
        "s_waitcnt lgkmcnt(8)\n\t"
        "ds_read_b64_tr_b16 v[248:249], v103 offset:0\n\t"
        "ds_read_b64_tr_b16 v[250:251], v103 offset:256\n\t"
        "ds_read_b64_tr_b16 v[252:253], v103 offset:4096\n\t"
        "ds_read_b64_tr_b16 v[254:255], v103 offset:4352\n\t"
        "s_waitcnt lgkmcnt(0)\n\t"
        "s_waitcnt vmcnt(48)\n\t"
        "s_add_u32 s35, s68, 7\n\t"
        "s_and_b32 s35, s35, 7\n\t"
        "s_lshl_b32 s35, s35, 18\n\t"
        "s_lshl_b32 s34, s43, 21\n\t"
        "s_add_u32 s34, s34, s35\n\t"
        "buffer_load_dwordx4 a[224:227], v97, s[20:23], s34 offen\n\t"
        "s_add_u32 s34, s34, 0x8000\n\t"
        "buffer_load_dwordx4 a[228:231], v97, s[20:23], s34 offen\n\t"
        "s_add_u32 s34, s34, 0x8000\n\t"
        "buffer_load_dwordx4 a[232:235], v97, s[20:23], s34 offen\n\t"
        "s_add_u32 s34, s34, 0x8000\n\t"
        "buffer_load_dwordx4 a[236:239], v97, s[20:23], s34 offen\n\t"
        "s_add_u32 s34, s34, 0x8000\n\t"
        "buffer_load_dwordx4 a[240:243], v97, s[20:23], s34 offen\n\t"
        "s_add_u32 s34, s34, 0x8000\n\t"
        "buffer_load_dwordx4 a[244:247], v97, s[20:23], s34 offen\n\t"
        "s_add_u32 s34, s34, 0x8000\n\t"
        "buffer_load_dwordx4 a[248:251], v97, s[20:23], s34 offen\n\t"
        "s_add_u32 s34, s34, 0x8000\n\t"
        "buffer_load_dwordx4 a[252:255], v97, s[20:23], s34 offen\n\t"
        "ds_write_b128 v122, a[0:3] offset:0\n\t"
        "ds_write_b128 v122, a[4:7] offset:1056\n\t"
        "ds_write_b128 v122, a[8:11] offset:2176\n\t"
        "ds_write_b128 v122, a[12:15] offset:3232\n\t"
        "ds_write_b128 v122, a[16:19] offset:4352\n\t"
        "ds_write_b128 v122, a[20:23] offset:5408\n\t"
        "ds_write_b128 v122, a[24:27] offset:6528\n\t"
        "ds_write_b128 v122, a[28:31] offset:7584\n\t"
        "ds_read_b64_tr_b16 a[0:1], v123 offset:0\n\t"
        "ds_read_b64_tr_b16 a[2:3], v123 offset:128\n\t"
        "ds_read_b64_tr_b16 a[4:5], v123 offset:512\n\t"
        "ds_read_b64_tr_b16 a[6:7], v123 offset:640\n\t"
        "s_waitcnt lgkmcnt(8)\n\t"
        "ds_read_b64_tr_b16 a[8:9], v123 offset:32\n\t"
        "ds_read_b64_tr_b16 a[10:11], v123 offset:160\n\t"
        "ds_read_b64_tr_b16 a[12:13], v123 offset:544\n\t"
        "ds_read_b64_tr_b16 a[14:15], v123 offset:672\n\t"
        "s_waitcnt lgkmcnt(8)\n\t"
        "ds_read_b64_tr_b16 a[16:17], v123 offset:4352\n\t"
        "ds_read_b64_tr_b16 a[18:19], v123 offset:4480\n\t"
        "ds_read_b64_tr_b16 a[20:21], v123 offset:4864\n\t"
        "ds_read_b64_tr_b16 a[22:23], v123 offset:4992\n\t"
        "s_waitcnt lgkmcnt(8)\n\t"
        "ds_read_b64_tr_b16 a[24:25], v123 offset:4384\n\t"
        "ds_read_b64_tr_b16 a[26:27], v123 offset:4512\n\t"
        "ds_read_b64_tr_b16 a[28:29], v123 offset:4896\n\t"
        "ds_read_b64_tr_b16 a[30:31], v123 offset:5024\n\t"
        "s_waitcnt lgkmcnt(8)\n\t"
        "s_waitcnt lgkmcnt(0)\n\t"
        "s_waitcnt vmcnt(48)\n\t"
        "ds_write_b128 v122, a[32:35] offset:0\n\t"
        "ds_write_b128 v122, a[36:39] offset:1056\n\t"
        "ds_write_b128 v122, a[40:43] offset:2176\n\t"
        "ds_write_b128 v122, a[44:47] offset:3232\n\t"
        "ds_write_b128 v122, a[48:51] offset:4352\n\t"
        "ds_write_b128 v122, a[52:55] offset:5408\n\t"
        "ds_write_b128 v122, a[56:59] offset:6528\n\t"
        "ds_write_b128 v122, a[60:63] offset:7584\n\t"
        "ds_read_b64_tr_b16 a[32:33], v123 offset:0\n\t"
        "ds_read_b64_tr_b16 a[34:35], v123 offset:128\n\t"
        "ds_read_b64_tr_b16 a[36:37], v123 offset:512\n\t"
        "ds_read_b64_tr_b16 a[38:39], v123 offset:640\n\t"
        "s_waitcnt lgkmcnt(8)\n\t"
        "ds_read_b64_tr_b16 a[40:41], v123 offset:32\n\t"
        "ds_read_b64_tr_b16 a[42:43], v123 offset:160\n\t"
        "ds_read_b64_tr_b16 a[44:45], v123 offset:544\n\t"
        "ds_read_b64_tr_b16 a[46:47], v123 offset:672\n\t"
        "s_waitcnt lgkmcnt(8)\n\t"
        "ds_read_b64_tr_b16 a[48:49], v123 offset:4352\n\t"
        "ds_read_b64_tr_b16 a[50:51], v123 offset:4480\n\t"
        "ds_read_b64_tr_b16 a[52:53], v123 offset:4864\n\t"
        "ds_read_b64_tr_b16 a[54:55], v123 offset:4992\n\t"
        "s_waitcnt lgkmcnt(8)\n\t"
        "ds_read_b64_tr_b16 a[56:57], v123 offset:4384\n\t"
        "ds_read_b64_tr_b16 a[58:59], v123 offset:4512\n\t"
        "ds_read_b64_tr_b16 a[60:61], v123 offset:4896\n\t"
        "ds_read_b64_tr_b16 a[62:63], v123 offset:5024\n\t"
        "s_waitcnt lgkmcnt(8)\n\t"
        "s_waitcnt lgkmcnt(0)\n\t"
        "s_waitcnt vmcnt(40)\n\t"
        "ds_write_b128 v122, a[64:67] offset:0\n\t"
        "ds_write_b128 v122, a[68:71] offset:1056\n\t"
        "ds_write_b128 v122, a[72:75] offset:2176\n\t"
        "ds_write_b128 v122, a[76:79] offset:3232\n\t"
        "ds_write_b128 v122, a[80:83] offset:4352\n\t"
        "ds_write_b128 v122, a[84:87] offset:5408\n\t"
        "ds_write_b128 v122, a[88:91] offset:6528\n\t"
        "ds_write_b128 v122, a[92:95] offset:7584\n\t"
        "ds_read_b64_tr_b16 a[64:65], v123 offset:0\n\t"
        "ds_read_b64_tr_b16 a[66:67], v123 offset:128\n\t"
        "ds_read_b64_tr_b16 a[68:69], v123 offset:512\n\t"
        "ds_read_b64_tr_b16 a[70:71], v123 offset:640\n\t"
        "s_waitcnt lgkmcnt(8)\n\t"
        "ds_read_b64_tr_b16 a[72:73], v123 offset:32\n\t"
        "ds_read_b64_tr_b16 a[74:75], v123 offset:160\n\t"
        "ds_read_b64_tr_b16 a[76:77], v123 offset:544\n\t"
        "ds_read_b64_tr_b16 a[78:79], v123 offset:672\n\t"
        "s_waitcnt lgkmcnt(8)\n\t"
        "ds_read_b64_tr_b16 a[80:81], v123 offset:4352\n\t"
        "ds_read_b64_tr_b16 a[82:83], v123 offset:4480\n\t"
        "ds_read_b64_tr_b16 a[84:85], v123 offset:4864\n\t"
        "ds_read_b64_tr_b16 a[86:87], v123 offset:4992\n\t"
        "s_waitcnt lgkmcnt(8)\n\t"
        "ds_read_b64_tr_b16 a[88:89], v123 offset:4384\n\t"
        "ds_read_b64_tr_b16 a[90:91], v123 offset:4512\n\t"
        "ds_read_b64_tr_b16 a[92:93], v123 offset:4896\n\t"
        "ds_read_b64_tr_b16 a[94:95], v123 offset:5024\n\t"
        "s_waitcnt lgkmcnt(8)\n\t"
        "s_waitcnt lgkmcnt(0)\n\t"
        "s_waitcnt vmcnt(32)\n\t"
        "ds_write_b128 v122, a[96:99] offset:0\n\t"
        "ds_write_b128 v122, a[100:103] offset:1056\n\t"
        "ds_write_b128 v122, a[104:107] offset:2176\n\t"
        "ds_write_b128 v122, a[108:111] offset:3232\n\t"
        "ds_write_b128 v122, a[112:115] offset:4352\n\t"
        "ds_write_b128 v122, a[116:119] offset:5408\n\t"
        "ds_write_b128 v122, a[120:123] offset:6528\n\t"
        "ds_write_b128 v122, a[124:127] offset:7584\n\t"
        "ds_read_b64_tr_b16 a[96:97], v123 offset:0\n\t"
        "ds_read_b64_tr_b16 a[98:99], v123 offset:128\n\t"
        "ds_read_b64_tr_b16 a[100:101], v123 offset:512\n\t"
        "ds_read_b64_tr_b16 a[102:103], v123 offset:640\n\t"
        "s_waitcnt lgkmcnt(8)\n\t"
        "ds_read_b64_tr_b16 a[104:105], v123 offset:32\n\t"
        "ds_read_b64_tr_b16 a[106:107], v123 offset:160\n\t"
        "ds_read_b64_tr_b16 a[108:109], v123 offset:544\n\t"
        "ds_read_b64_tr_b16 a[110:111], v123 offset:672\n\t"
        "s_waitcnt lgkmcnt(8)\n\t"
        "ds_read_b64_tr_b16 a[112:113], v123 offset:4352\n\t"
        "ds_read_b64_tr_b16 a[114:115], v123 offset:4480\n\t"
        "ds_read_b64_tr_b16 a[116:117], v123 offset:4864\n\t"
        "ds_read_b64_tr_b16 a[118:119], v123 offset:4992\n\t"
        "s_waitcnt lgkmcnt(8)\n\t"
        "ds_read_b64_tr_b16 a[120:121], v123 offset:4384\n\t"
        "ds_read_b64_tr_b16 a[122:123], v123 offset:4512\n\t"
        "ds_read_b64_tr_b16 a[124:125], v123 offset:4896\n\t"
        "ds_read_b64_tr_b16 a[126:127], v123 offset:5024\n\t"
        "s_waitcnt lgkmcnt(8)\n\t"
        "s_waitcnt lgkmcnt(0)\n\t"
        "s_waitcnt vmcnt(24)\n\t"
        "ds_write_b128 v122, a[128:131] offset:0\n\t"
        "ds_write_b128 v122, a[132:135] offset:1056\n\t"
        "ds_write_b128 v122, a[136:139] offset:2176\n\t"
        "ds_write_b128 v122, a[140:143] offset:3232\n\t"
        "ds_write_b128 v122, a[144:147] offset:4352\n\t"
        "ds_write_b128 v122, a[148:151] offset:5408\n\t"
        "ds_write_b128 v122, a[152:155] offset:6528\n\t"
        "ds_write_b128 v122, a[156:159] offset:7584\n\t"
        "ds_read_b64_tr_b16 a[128:129], v123 offset:0\n\t"
        "ds_read_b64_tr_b16 a[130:131], v123 offset:128\n\t"
        "ds_read_b64_tr_b16 a[132:133], v123 offset:512\n\t"
        "ds_read_b64_tr_b16 a[134:135], v123 offset:640\n\t"
        "s_waitcnt lgkmcnt(8)\n\t"
        "ds_read_b64_tr_b16 a[136:137], v123 offset:32\n\t"
        "ds_read_b64_tr_b16 a[138:139], v123 offset:160\n\t"
        "ds_read_b64_tr_b16 a[140:141], v123 offset:544\n\t"
        "ds_read_b64_tr_b16 a[142:143], v123 offset:672\n\t"
        "s_waitcnt lgkmcnt(8)\n\t"
        "ds_read_b64_tr_b16 a[144:145], v123 offset:4352\n\t"
        "ds_read_b64_tr_b16 a[146:147], v123 offset:4480\n\t"
        "ds_read_b64_tr_b16 a[148:149], v123 offset:4864\n\t"
        "ds_read_b64_tr_b16 a[150:151], v123 offset:4992\n\t"
        "s_waitcnt lgkmcnt(8)\n\t"
        "ds_read_b64_tr_b16 a[152:153], v123 offset:4384\n\t"
        "ds_read_b64_tr_b16 a[154:155], v123 offset:4512\n\t"
        "ds_read_b64_tr_b16 a[156:157], v123 offset:4896\n\t"
        "ds_read_b64_tr_b16 a[158:159], v123 offset:5024\n\t"
        "s_waitcnt lgkmcnt(8)\n\t"
        "s_waitcnt lgkmcnt(0)\n\t"
        "s_waitcnt vmcnt(16)\n\t"
        "ds_write_b128 v122, a[160:163] offset:0\n\t"
        "ds_write_b128 v122, a[164:167] offset:1056\n\t"
        "ds_write_b128 v122, a[168:171] offset:2176\n\t"
        "ds_write_b128 v122, a[172:175] offset:3232\n\t"
        "ds_write_b128 v122, a[176:179] offset:4352\n\t"
        "ds_write_b128 v122, a[180:183] offset:5408\n\t"
        "ds_write_b128 v122, a[184:187] offset:6528\n\t"
        "ds_write_b128 v122, a[188:191] offset:7584\n\t"
        "ds_read_b64_tr_b16 a[160:161], v123 offset:0\n\t"
        "ds_read_b64_tr_b16 a[162:163], v123 offset:128\n\t"
        "ds_read_b64_tr_b16 a[164:165], v123 offset:512\n\t"
        "ds_read_b64_tr_b16 a[166:167], v123 offset:640\n\t"
        "s_waitcnt lgkmcnt(8)\n\t"
        "ds_read_b64_tr_b16 a[168:169], v123 offset:32\n\t"
        "ds_read_b64_tr_b16 a[170:171], v123 offset:160\n\t"
        "ds_read_b64_tr_b16 a[172:173], v123 offset:544\n\t"
        "ds_read_b64_tr_b16 a[174:175], v123 offset:672\n\t"
        "s_waitcnt lgkmcnt(8)\n\t"
        "ds_read_b64_tr_b16 a[176:177], v123 offset:4352\n\t"
        "ds_read_b64_tr_b16 a[178:179], v123 offset:4480\n\t"
        "ds_read_b64_tr_b16 a[180:181], v123 offset:4864\n\t"
        "ds_read_b64_tr_b16 a[182:183], v123 offset:4992\n\t"
        "s_waitcnt lgkmcnt(8)\n\t"
        "ds_read_b64_tr_b16 a[184:185], v123 offset:4384\n\t"
        "ds_read_b64_tr_b16 a[186:187], v123 offset:4512\n\t"
        "ds_read_b64_tr_b16 a[188:189], v123 offset:4896\n\t"
        "ds_read_b64_tr_b16 a[190:191], v123 offset:5024\n\t"
        "s_waitcnt lgkmcnt(8)\n\t"
        "s_waitcnt lgkmcnt(0)\n\t"
        "s_waitcnt vmcnt(8)\n\t"
        "ds_write_b128 v122, a[192:195] offset:0\n\t"
        "ds_write_b128 v122, a[196:199] offset:1056\n\t"
        "ds_write_b128 v122, a[200:203] offset:2176\n\t"
        "ds_write_b128 v122, a[204:207] offset:3232\n\t"
        "ds_write_b128 v122, a[208:211] offset:4352\n\t"
        "ds_write_b128 v122, a[212:215] offset:5408\n\t"
        "ds_write_b128 v122, a[216:219] offset:6528\n\t"
        "ds_write_b128 v122, a[220:223] offset:7584\n\t"
        "ds_read_b64_tr_b16 a[192:193], v123 offset:0\n\t"
        "ds_read_b64_tr_b16 a[194:195], v123 offset:128\n\t"
        "ds_read_b64_tr_b16 a[196:197], v123 offset:512\n\t"
        "ds_read_b64_tr_b16 a[198:199], v123 offset:640\n\t"
        "s_waitcnt lgkmcnt(8)\n\t"
        "ds_read_b64_tr_b16 a[200:201], v123 offset:32\n\t"
        "ds_read_b64_tr_b16 a[202:203], v123 offset:160\n\t"
        "ds_read_b64_tr_b16 a[204:205], v123 offset:544\n\t"
        "ds_read_b64_tr_b16 a[206:207], v123 offset:672\n\t"
        "s_waitcnt lgkmcnt(8)\n\t"
        "ds_read_b64_tr_b16 a[208:209], v123 offset:4352\n\t"
        "ds_read_b64_tr_b16 a[210:211], v123 offset:4480\n\t"
        "ds_read_b64_tr_b16 a[212:213], v123 offset:4864\n\t"
        "ds_read_b64_tr_b16 a[214:215], v123 offset:4992\n\t"
        "s_waitcnt lgkmcnt(8)\n\t"
        "ds_read_b64_tr_b16 a[216:217], v123 offset:4384\n\t"
        "ds_read_b64_tr_b16 a[218:219], v123 offset:4512\n\t"
        "ds_read_b64_tr_b16 a[220:221], v123 offset:4896\n\t"
        "ds_read_b64_tr_b16 a[222:223], v123 offset:5024\n\t"
        "s_waitcnt lgkmcnt(8)\n\t"
        "s_waitcnt lgkmcnt(0)\n\t"
        "s_waitcnt vmcnt(0)\n\t"
        "ds_write_b128 v122, a[224:227] offset:0\n\t"
        "ds_write_b128 v122, a[228:231] offset:1056\n\t"
        "ds_write_b128 v122, a[232:235] offset:2176\n\t"
        "ds_write_b128 v122, a[236:239] offset:3232\n\t"
        "ds_write_b128 v122, a[240:243] offset:4352\n\t"
        "ds_write_b128 v122, a[244:247] offset:5408\n\t"
        "ds_write_b128 v122, a[248:251] offset:6528\n\t"
        "ds_write_b128 v122, a[252:255] offset:7584\n\t"
        "ds_read_b64_tr_b16 a[224:225], v123 offset:0\n\t"
        "ds_read_b64_tr_b16 a[226:227], v123 offset:128\n\t"
        "ds_read_b64_tr_b16 a[228:229], v123 offset:512\n\t"
        "ds_read_b64_tr_b16 a[230:231], v123 offset:640\n\t"
        "s_waitcnt lgkmcnt(8)\n\t"
        "ds_read_b64_tr_b16 a[232:233], v123 offset:32\n\t"
        "ds_read_b64_tr_b16 a[234:235], v123 offset:160\n\t"
        "ds_read_b64_tr_b16 a[236:237], v123 offset:544\n\t"
        "ds_read_b64_tr_b16 a[238:239], v123 offset:672\n\t"
        "s_waitcnt lgkmcnt(8)\n\t"
        "ds_read_b64_tr_b16 a[240:241], v123 offset:4352\n\t"
        "ds_read_b64_tr_b16 a[242:243], v123 offset:4480\n\t"
        "ds_read_b64_tr_b16 a[244:245], v123 offset:4864\n\t"
        "ds_read_b64_tr_b16 a[246:247], v123 offset:4992\n\t"
        "s_waitcnt lgkmcnt(8)\n\t"
        "ds_read_b64_tr_b16 a[248:249], v123 offset:4384\n\t"
        "ds_read_b64_tr_b16 a[250:251], v123 offset:4512\n\t"
        "ds_read_b64_tr_b16 a[252:253], v123 offset:4896\n\t"
        "ds_read_b64_tr_b16 a[254:255], v123 offset:5024\n\t"
        "s_waitcnt lgkmcnt(8)\n\t"
        "s_waitcnt lgkmcnt(0)\n\t"
        "s_mov_b32 s42, 0\n\t"
        "v_mbcnt_lo_u32_b32 v2, -1, 0\n\t"
        "v_mbcnt_hi_u32_b32 v2, -1, v2\n\t"
        "v_and_b32_e32 v3, 31, v2\n\t"
        "v_lshlrev_b32_e32 v3, 2, v3\n\t"
        "s_mov_b32 s35, 33792\n\t"
        "LB_cal%=:\n\t"
        "buffer_load_dword v0, v3, s[12:15], s35 offen sc1\n\t"
        "s_waitcnt vmcnt(0)\n\t"
        "v_cmp_eq_u32_e32 vcc, 0, v0\n\t"
        "s_cbranch_vccz LB_calok%=\n\t"
        "s_sleep 4\n\t"
        "s_add_u32 s42, s42, 1\n\t"
        "s_cmp_lt_u32 s42, 0x100000\n\t"
        "s_cbranch_scc1 LB_cal%=\n\t"
        "s_movk_i32 s34, 8\n\t"
        "s_branch LB_die%=\n\t"
        "LB_calok%=:\n\t"
        "v_cmp_eq_u32_e32 vcc, 3, v0\n\t"
        "s_nop 0\n\t"
        "s_bcnt1_i32_b64 s34, vcc\n\t"
        "s_cmp_ge_u32 s34, 32\n\t"
        "s_cselect_b32 s34, 8192, 0\n\t"
        "s_add_u32 s8, s8, s34\n\t"
        "s_addc_u32 s9, s9, 0\n\t"
        "v_min_u32_e32 v109, -2, v109\n\t"
        "buffer_store_dword v109, v116, s[8:11], 0 offen sc1\n\t"
        "s_memrealtime s[32:33]\n\t"
        "s_waitcnt lgkmcnt(0)\n\t"
        "v_mov_b32_e32 v110, s32\n\t"
        "s_lshl_b32 s34, s29, 8\n\t"
        "s_lshl_b32 s35, s30, 20\n\t"
        "s_or_b32 s54, s34, s35\n\t"
        "v_mov_b32_e32 v111, s54\n\t"
        "s_waitcnt vmcnt(0) lgkmcnt(0)\n\t"
        "s_barrier\n\t"
        "s_memrealtime s[32:33]\n\t"
        "s_lshl_b32 s35, s49, 2\n\t"
        "s_add_u32 s35, s35, 34304\n\t"
        "s_cmp_eq_u32 s43, 0\n\t"
        "s_cselect_b32 s35, s35, 0xf0000000\n\t"
        "s_waitcnt lgkmcnt(0)\n\t"
        "s_or_b32 s25, s32, 1\n\t"
        "v_mov_b32_e32 v0, s25\n\t"
        "s_mov_b64 exec, 1\n\t"
        "buffer_store_dword v0, off, s[12:15], s35 sc1\n\t"
        "s_mov_b64 exec, -1\n\t"
        "v_min_u32_e32 v3, 47, v2\n\t"
        "v_lshlrev_b32_e32 v3, 3, v3\n\t"
        "s_mov_b32 s35, 34304\n\t"
        "s_mov_b32 s42, 0\n\t"
        "LB_rdy%=:\n\t"
        "buffer_load_dwordx2 v[0:1], v3, s[12:15], s35 offen sc1\n\t"
        "s_waitcnt vmcnt(0)\n\t"
        "v_min_u32_e32 v2, v0, v1\n\t"
        "v_cmp_eq_u32_e32 vcc, 0, v2\n\t"
        "s_cbranch_vccz LB_rdyok%=\n\t"
        "s_sleep 3\n\t"
        "s_add_u32 s42, s42, 1\n\t"
        "s_cmp_lt_u32 s42, 0x100000\n\t"
        "s_cbranch_scc1 LB_rdy%=\n\t"
        "s_movk_i32 s34, 9\n\t"
        "s_branch LB_die%=\n\t"
        "LB_rdyok%=:\n\t"
        "v_subrev_u32_e32 v0, s25, v0\n\t"
        "v_subrev_u32_e32 v1, s25, v1\n\t"
        "v_max_i32_e32 v0, v0, v1\n\t"
        "s_nop 1\n\t"
        "v_max_i32_dpp v0, v0, v0 quad_perm:[1,0,3,2] row_mask:0xf bank_mask:0xf\n\t"
        "s_nop 1\n\t"
        "v_max_i32_dpp v0, v0, v0 quad_perm:[2,3,0,1] row_mask:0xf bank_mask:0xf\n\t"
        "s_nop 1\n\t"
        "v_max_i32_dpp v0, v0, v0 row_ror:4 row_mask:0xf bank_mask:0xf\n\t"
        "s_nop 1\n\t"
        "v_max_i32_dpp v0, v0, v0 row_ror:8 row_mask:0xf bank_mask:0xf\n\t"
        "v_mov_b32_e32 v1, v0\n\t"
        "s_nop 1\n\t"
        "v_permlane16_swap_b32_e32 v0, v1\n\t"
        "v_max_i32_e32 v0, v0, v1\n\t"
        "v_mov_b32_e32 v1, v0\n\t"
        "s_nop 1\n\t"
        "v_permlane32_swap_b32_e32 v0, v1\n\t"
        "v_max_i32_e32 v0, v0, v1\n\t"
        "s_nop 0\n\t"
        "v_readfirstlane_b32 s34, v0\n\t"
        "s_nop 0\n\t"
        "s_add_u32 s25, s25, s34\n\t"
        "s_add_u32 s25, s25, 70\n\t"
        "s_mov_b32 s24, 0\n\t"
        "s_mov_b32 s26, 0\n\t"
        "s_mov_b32 s27, 0x80000\n\t"
        "s_mov_b32 s28, 0x180000\n\t"
        "s_mov_b32 s31, 0\n\t"
        "s_mov_b32 s47, 0\n\t"
        "s_mov_b32 s38, 0xff00ff00\n\t"
        "s_mov_b32 s39, 0xff00ff00\n\t"
        "s_mov_b32 s40, 0\n\t"
        "s_mov_b32 s41, -1\n\t"
        "v_mov_b64_e32 v[0:1], 0\n\t"
        "v_mov_b64_e32 v[2:3], 0\n\t"
        "v_mov_b64_e32 v[4:5], 0\n\t"
        "v_mov_b64_e32 v[6:7], 0\n\t"
        "v_mov_b64_e32 v[8:9], 0\n\t"
        "v_mov_b64_e32 v[10:11], 0\n\t"
        "v_mov_b64_e32 v[12:13], 0\n\t"
        "v_mov_b64_e32 v[14:15], 0\n\t"
        "LB_loop%=:\n\t"
        "s_mov_b32 s42, 0\n\t"
        "s_sub_u32 s35, s25, 1\n\t"
        "LB_wa%=:\n\t"
        "s_memrealtime s[32:33]\n\t"
        "s_waitcnt lgkmcnt(0)\n\t"
        "s_sub_u32 s34, s35, s32\n\t"
        "s_cmpk_lt_u32 s34, 0x2000\n\t"
        "s_cbranch_scc1 LB_wa%=\n\t"
        "LB_rd%=:\n\t"
        "buffer_load_dwordx4 v[16:19], v112, s[8:11], s26 offen offset:0 sc1\n\t"
        "s_nop 9\n\t"
        "buffer_load_dwordx4 v[20:23], v112, s[8:11], s26 offen offset:1024 sc1\n\t"
        "s_nop 9\n\t"
        "buffer_load_dwordx4 v[24:27], v112, s[8:11], s26 offen offset:2048 sc1\n\t"
        "s_nop 9\n\t"
        "buffer_load_dwordx4 v[28:31], v112, s[8:11], s26 offen offset:3072 sc1\n\t"
        "LB_go%=:\n\t"
        "s_waitcnt vmcnt(3)\n\t"
        "v_smfmac_f32_16x16x64_bf16 v[0:3], v[16:19], v[128:135], v106\n\t"
        "v_max3_u32 v108, v16, v17, v18\n\t"
        "v_max_u32_e32 v108, v19, v108\n\t"
        "v_smfmac_f32_16x16x64_bf16 v[4:7], v[16:19], v[136:143], v106\n\t"
        "v_smfmac_f32_16x16x64_bf16 v[8:11], v[16:19], v[144:151], v106\n\t"
        "v_smfmac_f32_16x16x64_bf16 v[12:15], v[16:19], v[152:159], v106\n\t"
        "s_waitcnt vmcnt(2)\n\t"
        "v_smfmac_f32_16x16x64_bf16 v[0:3], v[20:23], v[160:167], v106\n\t"
        "v_max3_u32 v108, v20, v21, v108\n\t"
        "v_max3_u32 v108, v22, v23, v108\n\t"
        "v_smfmac_f32_16x16x64_bf16 v[4:7], v[20:23], v[168:175], v106\n\t"
        "v_smfmac_f32_16x16x64_bf16 v[8:11], v[20:23], v[176:183], v106\n\t"
        "v_smfmac_f32_16x16x64_bf16 v[12:15], v[20:23], v[184:191], v106\n\t"
        "s_waitcnt vmcnt(1)\n\t"
        "v_smfmac_f32_16x16x64_bf16 v[0:3], v[24:27], v[192:199], v106\n\t"
        "v_max3_u32 v108, v24, v25, v108\n\t"
        "v_max3_u32 v108, v26, v27, v108\n\t"
        "v_smfmac_f32_16x16x64_bf16 v[4:7], v[24:27], v[200:207], v106\n\t"
        "v_smfmac_f32_16x16x64_bf16 v[8:11], v[24:27], v[208:215], v106\n\t"
        "v_smfmac_f32_16x16x64_bf16 v[12:15], v[24:27], v[216:223], v106\n\t"
        "s_waitcnt vmcnt(0)\n\t"
        "v_smfmac_f32_16x16x64_bf16 v[0:3], v[28:31], v[224:231], v106\n\t"
        "v_max3_u32 v108, v28, v29, v108\n\t"
        "v_max3_u32 v108, v30, v31, v108\n\t"
        "v_smfmac_f32_16x16x64_bf16 v[4:7], v[28:31], v[232:239], v106\n\t"
        "v_cmp_eq_u32_e32 vcc, -1, v108\n\t"
        "v_smfmac_f32_16x16x64_bf16 v[8:11], v[28:31], v[240:247], v106\n\t"
        "v_smfmac_f32_16x16x64_bf16 v[12:15], v[28:31], v[248:255], v106\n\t"
        "s_cbranch_vccnz LB_rdfail%=\n\t"
        "s_nop 3\n\t"
        "v_permlane32_swap_b32_e32 v0, v4\n\t"
        "v_permlane32_swap_b32_e32 v1, v5\n\t"
        "v_permlane32_swap_b32_e32 v2, v6\n\t"
        "v_permlane32_swap_b32_e32 v3, v7\n\t"
        "v_pk_add_f32 v[0:1], v[0:1], v[4:5]\n\t"
        "v_pk_add_f32 v[2:3], v[2:3], v[6:7]\n\t"
        "ds_write_b128 v118, v[0:3] offset:0\n\t"
        "v_permlane32_swap_b32_e32 v8, v12\n\t"
        "v_permlane32_swap_b32_e32 v9, v13\n\t"
        "v_permlane32_swap_b32_e32 v10, v14\n\t"
        "v_permlane32_swap_b32_e32 v11, v15\n\t"
        "v_pk_add_f32 v[8:9], v[8:9], v[12:13]\n\t"
        "v_pk_add_f32 v[10:11], v[10:11], v[14:15]\n\t"
        "ds_write_b128 v118, v[8:11] offset:1024\n\t"
        "buffer_store_dwordx2 v[110:111], v117, s[8:11], s26 offen sc1\n\t"
        "s_waitcnt lgkmcnt(0)\n\t"
        "s_barrier\n\t"
        "ds_read_b128 v[16:19], v119 offset:0\n\t"
        "ds_read_b128 v[20:23], v119 offset:2048\n\t"
        "ds_read_b128 v[24:27], v119 offset:4096\n\t"
        "ds_read_b128 v[28:31], v119 offset:6144\n\t"
        "buffer_load_dwordx4 v[64:67], v124, s[8:11], s28 offen sc1\n\t"
        "s_waitcnt lgkmcnt(2)\n\t"
        "v_pk_add_f32 v[16:17], v[16:17], v[20:21]\n\t"
        "v_pk_add_f32 v[18:19], v[18:19], v[22:23]\n\t"
        "s_waitcnt lgkmcnt(0)\n\t"
        "v_pk_add_f32 v[24:25], v[24:25], v[28:29]\n\t"
        "v_pk_add_f32 v[26:27], v[26:27], v[30:31]\n\t"
        "v_pk_add_f32 v[16:17], v[16:17], v[24:25]\n\t"
        "v_pk_add_f32 v[18:19], v[18:19], v[26:27]\n\t"
        "v_cvt_pk_bf16_f32 v20, v16, v17\n\t"
        "v_min_u32_e32 v20, -2, v20\n\t"
        "v_cvt_pk_bf16_f32 v21, v18, v19\n\t"
        "v_min_u32_e32 v21, -2, v21\n\t"
        "buffer_store_dwordx2 v[20:21], v114, s[8:11], s26 offen sc1\n\t"
        "s_cmp_eq_u32 s24, 63\n\t"
        "s_cbranch_scc1 LB_end%=\n\t"
        "s_memrealtime s[52:53]\n\t"
        "v_mov_b64_e32 v[24:25], -1\n\t"
        "buffer_store_dword v24, v116, s[8:11], s28 offen sc1\n\t"
        "s_xor_b32 s34, s26, 0x100000\n\t"
        "buffer_store_dwordx2 v[24:25], v114, s[8:11], s34 offen sc1\n\t"
        "v_mov_b64_e32 v[32:33], 0\n\t"
        "v_mov_b64_e32 v[34:35], 0\n\t"
        "v_mov_b64_e32 v[36:37], 0\n\t"
        "v_mov_b64_e32 v[38:39], 0\n\t"
        "v_mov_b64_e32 v[40:41], 0\n\t"
        "v_mov_b64_e32 v[42:43], 0\n\t"
        "v_mov_b64_e32 v[44:45], 0\n\t"
        "v_mov_b64_e32 v[46:47], 0\n\t"
        "v_mov_b64_e32 v[0:1], 0\n\t"
        "v_mov_b64_e32 v[2:3], 0\n\t"
        "v_mov_b64_e32 v[4:5], 0\n\t"
        "v_mov_b64_e32 v[6:7], 0\n\t"
        "v_mov_b64_e32 v[8:9], 0\n\t"
        "v_mov_b64_e32 v[10:11], 0\n\t"
        "v_mov_b64_e32 v[12:13], 0\n\t"
        "v_mov_b64_e32 v[14:15], 0\n\t"
        "s_add_u32 s46, s26, s69\n\t"
        "s_add_u32 s48, s26, s70\n\t"
        "s_add_u32 s73, s26, s71\n\t"
        "s_add_u32 s74, s26, s72\n\t"
        "s_sleep 1\n\t"
        "buffer_load_dwordx2 v[104:105], v113, s[8:11], s26 offen sc1\n\t"
        "s_add_u32 s66, s49, s43\n\t"
        "s_cmp_lg_u32 s66, 32\n\t"
        "s_cbranch_scc1 LB_norep%=\n\t"
        "s_waitcnt vmcnt(4)\n\t"
        "s_sub_u32 s66, s24, 1\n\t"
        "v_lshrrev_b32_e32 v68, 16, v64\n\t"
        "v_cmp_eq_u32_e32 vcc, s66, v68\n\t"
        "v_subrev_u32_e32 v64, s59, v64\n\t"
        "v_bfe_i32 v64, v64, 0, 16\n\t"
        "v_cndmask_b32_e32 v64, 0, v64, vcc\n\t"
        "v_lshrrev_b32_e32 v68, 16, v65\n\t"
        "v_cmp_eq_u32_e32 vcc, s66, v68\n\t"
        "v_subrev_u32_e32 v65, s59, v65\n\t"
        "v_bfe_i32 v65, v65, 0, 16\n\t"
        "v_cndmask_b32_e32 v65, 0, v65, vcc\n\t"
        "v_lshrrev_b32_e32 v68, 16, v66\n\t"
        "v_cmp_eq_u32_e32 vcc, s66, v68\n\t"
        "v_subrev_u32_e32 v66, s59, v66\n\t"
        "v_bfe_i32 v66, v66, 0, 16\n\t"
        "v_cndmask_b32_e32 v66, 0, v66, vcc\n\t"
        "v_lshrrev_b32_e32 v68, 16, v67\n\t"
        "v_cmp_eq_u32_e32 vcc, s66, v68\n\t"
        "v_subrev_u32_e32 v67, s59, v67\n\t"
        "v_bfe_i32 v67, v67, 0, 16\n\t"
        "v_cndmask_b32_e32 v67, 0, v67, vcc\n\t"
        "v_max3_i32 v64, v64, v65, v66\n\t"
        "v_max_i32_e32 v64, v64, v67\n\t"
        "s_nop 1\n\t"
        "v_max_i32_dpp v64, v64, v64 quad_perm:[1,0,3,2] row_mask:0xf bank_mask:0xf\n\t"
        "s_nop 1\n\t"
        "v_max_i32_dpp v64, v64, v64 quad_perm:[2,3,0,1] row_mask:0xf bank_mask:0xf\n\t"
        "s_nop 1\n\t"
        "v_max_i32_dpp v64, v64, v64 row_ror:4 row_mask:0xf bank_mask:0xf\n\t"
        "s_nop 1\n\t"
        "v_max_i32_dpp v64, v64, v64 row_ror:8 row_mask:0xf bank_mask:0xf\n\t"
        "v_mov_b32_e32 v65, v64\n\t"
        "s_nop 1\n\t"
        "v_permlane16_swap_b32_e32 v64, v65\n\t"
        "v_max_i32_e32 v64, v64, v65\n\t"
        "v_mov_b32_e32 v65, v64\n\t"
        "s_nop 1\n\t"
        "v_permlane32_swap_b32_e32 v64, v65\n\t"
        "v_max_i32_e32 v64, v64, v65\n\t"
        "s_nop 0\n\t"
        "v_readfirstlane_b32 s64, v64\n\t"
        "LB_norep%=:\n\t"
        "s_waitcnt vmcnt(0) lgkmcnt(0)\n\t"
        "s_mov_b32 s61, s29\n\t"
        "s_mov_b32 s62, s30\n\t"
        "s_mov_b32 s63, s31\n\t"
        "v_and_b32_e32 v30, 0xff, v105\n\t"
        "v_cmp_ne_u32_e32 vcc, s24, v30\n\t"
        "s_cbranch_vccz LB_stok%=\n\t"
        "s_mov_b32 s42, 0\n\t"
        "LB_stre%=:\n\t"
        "buffer_load_dwordx2 v[104:105], v113, s[8:11], s26 offen sc1\n\t"
        "s_waitcnt vmcnt(0)\n\t"
        "v_and_b32_e32 v30, 0xff, v105\n\t"
        "v_cmp_ne_u32_e32 vcc, s24, v30\n\t"
        "s_cbranch_vccz LB_stok%=\n\t"
        "s_add_u32 s42, s42, 1\n\t"
        "s_cmp_lt_u32 s42, 0x100000\n\t"
        "s_cbranch_scc1 LB_stre%=\n\t"
        "s_movk_i32 s34, 5\n\t"
        "s_branch LB_die%=\n\t"
        "LB_stok%=:\n\t"
        "v_readfirstlane_b32 s35, v105\n\t"
        "v_subrev_u32_e32 v104, s25, v104\n\t"
        "s_bfe_u32 s29, s35, 0xc0008\n\t"
        "s_lshr_b32 s30, s35, 20\n\t"
        "v_max_i32_dpp v104, v104, v104 quad_perm:[1,0,3,2] row_mask:0xf bank_mask:0xf\n\t"
        "s_nop 1\n\t"
        "v_max_i32_dpp v104, v104, v104 quad_perm:[2,3,0,1] row_mask:0xf bank_mask:0xf\n\t"
        "s_nop 1\n\t"
        "v_max_i32_dpp v104, v104, v104 row_ror:4 row_mask:0xf bank_mask:0xf\n\t"
        "s_nop 1\n\t"
        "v_max_i32_dpp v104, v104, v104 row_ror:8 row_mask:0xf bank_mask:0xf\n\t"
        "v_mov_b32_e32 v105, v104\n\t"
        "s_nop 1\n\t"
        "v_permlane16_swap_b32_e32 v104, v105\n\t"
        "v_max_i32_e32 v104, v104, v105\n\t"
        "v_mov_b32_e32 v105, v104\n\t"
        "s_nop 1\n\t"
        "v_permlane32_swap_b32_e32 v104, v105\n\t"
        "v_max_i32_e32 v104, v104, v105\n\t"
        "s_nop 0\n\t"
        "v_readfirstlane_b32 s36, v104\n\t"
        "s_add_u32 s35, s31, s50\n\t"
        "s_mul_i32 s37, s50, 3\n\t"
        "s_min_u32 s35, s35, s37\n\t"
        "s_add_i32 s34, s36, s51\n\t"
        "s_cmp_gt_i32 s34, 0\n\t"
        "s_cselect_b32 s31, s35, 0\n\t"
        "s_cselect_b32 s34, s34, 0\n\t"
        "s_add_u32 s34, s34, s31\n\t"
        "s_add_u32 s37, s25, s34\n\t"
        "s_add_u32 s45, s37, s29\n\t"
        "s_add_u32 s25, s45, s30\n\t"
        "s_add_u32 s66, s49, s43\n\t"
        "s_cmp_lg_u32 s66, 32\n\t"
        "s_cbranch_scc1 LB_noctl%=\n\t"
        "s_cmp_eq_u32 s24, 0\n\t"
        "s_cbranch_scc1 LB_ctl0%=\n\t"
        "s_add_i32 s65, s36, s62\n\t"
        "s_lshl_b32 s66, s65, 1\n\t"
        "s_sub_i32 s66, s66, s56\n\t"
        "s_cmp_gt_i32 s66, 56\n\t"
        "s_cbranch_scc1 LB_c1%=\n\t"
        "s_lshl_b32 s66, s65, 1\n\t"
        "s_add_u32 s34, s56, 16\n\t"
        "s_min_i32 s34, s34, s66\n\t"
        "s_sub_u32 s35, s56, s66\n\t"
        "s_ashr_i32 s35, s35, 3\n\t"
        "s_max_i32 s35, s35, 1\n\t"
        "s_sub_u32 s35, s56, s35\n\t"
        "s_max_i32 s56, s34, s35\n\t"
        "s_branch LB_c2%=\n\t"
        "LB_c1%=:\n\t"
        "s_add_u32 s56, s56, 2\n\t"
        "s_add_i32 s66, s64, s57\n\t"
        "s_cmp_gt_i32 s66, s61\n\t"
        "s_cbranch_scc1 LB_c2%=\n\t"
        "s_add_u32 s57, s57, 4\n\t"
        "s_min_u32 s57, s57, 26\n\t"
        "LB_c2%=:\n\t"
        "s_cmp_lg_u32 s63, 0\n\t"
        "s_cbranch_scc1 LB_c4%=\n\t"
        "s_lshl_b32 s66, s64, 1\n\t"
        "s_sub_i32 s66, s66, s55\n\t"
        "s_cmp_gt_i32 s66, 56\n\t"
        "s_cbranch_scc1 LB_c3%=\n\t"
        "s_lshl_b32 s66, s64, 1\n\t"
        "s_add_u32 s34, s55, 16\n\t"
        "s_min_i32 s34, s34, s66\n\t"
        "s_sub_u32 s35, s55, s66\n\t"
        "s_ashr_i32 s35, s35, 3\n\t"
        "s_max_i32 s35, s35, 1\n\t"
        "s_sub_u32 s35, s55, s35\n\t"
        "s_max_i32 s55, s34, s35\n\t"
        "s_branch LB_c4%=\n\t"
        "LB_c3%=:\n\t"
        "s_add_u32 s55, s55, 2\n\t"
        "s_cmp_lt_i32 s60, s58\n\t"
        "s_cbranch_scc1 LB_c4%=\n\t"
        "s_add_u32 s66, s58, 8\n\t"
        "s_cmp_gt_i32 s60, s66\n\t"
        "s_cbranch_scc1 LB_c4%=\n\t"
        "s_add_u32 s58, s58, 4\n\t"
        "s_min_u32 s58, s58, 24\n\t"
        "LB_c4%=:\n\t"
        "s_and_b32 s66, s24, 3\n\t"
        "s_cmp_eq_u32 s66, 0\n\t"
        "s_cselect_b32 s66, 1, 0\n\t"
        "s_sub_u32 s57, s57, s66\n\t"
        "s_max_u32 s57, s57, 10\n\t"
        "s_sub_u32 s58, s58, s66\n\t"
        "s_max_u32 s58, s58, 8\n\t"
        "s_add_u32 s34, s55, 1\n\t"
        "s_lshr_b32 s34, s34, 1\n\t"
        "s_add_u32 s34, s34, s57\n\t"
        "s_min_u32 s34, s34, 1000\n\t"
        "s_lshl_b32 s34, s34, 8\n\t"
        "s_add_u32 s35, s56, 1\n\t"
        "s_lshr_b32 s35, s35, 1\n\t"
        "s_add_u32 s35, s35, s58\n\t"
        "s_min_u32 s35, s35, 1000\n\t"
        "s_lshl_b32 s35, s35, 20\n\t"
        "s_or_b32 s54, s34, s35\n\t"
        "s_branch LB_ctl1%=\n\t"
        "LB_ctl0%=:\n\t"
        "s_movk_i32 s57, 10\n\t"
        "s_movk_i32 s58, 8\n\t"
        "s_sub_u32 s55, s29, s57\n\t"
        "s_lshl_b32 s55, s55, 1\n\t"
        "s_sub_u32 s56, s30, s58\n\t"
        "s_lshl_b32 s56, s56, 1\n\t"
        "LB_ctl1%=:\n\t"
        "s_mov_b32 s59, s37\n\t"
        "s_sub_u32 s60, 0, s36\n\t"
        "LB_noctl%=:\n\t"
        "s_add_u32 s34, s24, 1\n\t"
        "s_or_b32 s34, s34, s54\n\t"
        "v_mov_b32_e32 v111, s34\n\t"
        "s_mov_b32 s42, 0\n\t"
        "s_sub_u32 s35, s45, 1\n\t"
        "LB_wo%=:\n\t"
        "s_memrealtime s[32:33]\n\t"
        "s_waitcnt lgkmcnt(0)\n\t"
        "s_sub_u32 s34, s35, s32\n\t"
        "s_cmpk_lt_u32 s34, 0x2000\n\t"
        "s_cbranch_scc1 LB_wo%=\n\t"
        "LB_ro%=:\n\t"
        "buffer_load_dwordx4 v[64:67], v115, s[8:11], s46 offen offset:0 sc1\n\t"
        "s_nop 12\n\t"
        "buffer_load_dwordx4 v[68:71], v115, s[8:11], s46 offen offset:1024 sc1\n\t"
        "s_nop 12\n\t"
        "buffer_load_dwordx4 v[72:75], v115, s[8:11], s48 offen offset:0 sc1\n\t"
        "s_nop 12\n\t"
        "buffer_load_dwordx4 v[76:79], v115, s[8:11], s48 offen offset:1024 sc1\n\t"
        "s_nop 12\n\t"
        "buffer_load_dwordx4 v[80:83], v115, s[8:11], s73 offen offset:0 sc1\n\t"
        "s_nop 12\n\t"
        "buffer_load_dwordx4 v[84:87], v115, s[8:11], s73 offen offset:1024 sc1\n\t"
        "s_nop 12\n\t"
        "buffer_load_dwordx4 v[88:91], v115, s[8:11], s74 offen offset:0 sc1\n\t"
        "s_nop 12\n\t"
        "buffer_load_dwordx4 v[92:95], v115, s[8:11], s74 offen offset:1024 sc1\n\t"
        "LB_go2%=:\n\t"
        "s_waitcnt vmcnt(7)\n\t"
        "v_smfmac_f32_16x16x64_bf16 v[40:43], v[64:67], a[16:23], v107\n\t"
        "v_max3_u32 v108, v64, v65, v66\n\t"
        "v_max_u32_e32 v108, v67, v108\n\t"
        "v_smfmac_f32_16x16x64_bf16 v[36:39], v[64:67], a[8:15], v107\n\t"
        "v_smfmac_f32_16x16x64_bf16 v[32:35], v[64:67], a[0:7], v107\n\t"
        "v_smfmac_f32_16x16x64_bf16 v[44:47], v[64:67], a[24:31], v107\n\t"
        "s_waitcnt vmcnt(6)\n\t"
        "v_smfmac_f32_16x16x64_bf16 v[40:43], v[68:71], a[48:55], v107\n\t"
        "v_max3_u32 v108, v68, v69, v108\n\t"
        "v_max3_u32 v108, v70, v71, v108\n\t"
        "v_smfmac_f32_16x16x64_bf16 v[36:39], v[68:71], a[40:47], v107\n\t"
        "v_smfmac_f32_16x16x64_bf16 v[32:35], v[68:71], a[32:39], v107\n\t"
        "v_smfmac_f32_16x16x64_bf16 v[44:47], v[68:71], a[56:63], v107\n\t"
        "s_waitcnt vmcnt(5)\n\t"
        "v_smfmac_f32_16x16x64_bf16 v[40:43], v[72:75], a[80:87], v107\n\t"
        "v_max3_u32 v108, v72, v73, v108\n\t"
        "v_max3_u32 v108, v74, v75, v108\n\t"
        "v_smfmac_f32_16x16x64_bf16 v[36:39], v[72:75], a[72:79], v107\n\t"
        "v_smfmac_f32_16x16x64_bf16 v[32:35], v[72:75], a[64:71], v107\n\t"
        "v_smfmac_f32_16x16x64_bf16 v[44:47], v[72:75], a[88:95], v107\n\t"
        "s_waitcnt vmcnt(4)\n\t"
        "v_smfmac_f32_16x16x64_bf16 v[40:43], v[76:79], a[112:119], v107\n\t"
        "v_max3_u32 v108, v76, v77, v108\n\t"
        "v_max3_u32 v108, v78, v79, v108\n\t"
        "v_smfmac_f32_16x16x64_bf16 v[36:39], v[76:79], a[104:111], v107\n\t"
        "v_smfmac_f32_16x16x64_bf16 v[32:35], v[76:79], a[96:103], v107\n\t"
        "v_smfmac_f32_16x16x64_bf16 v[44:47], v[76:79], a[120:127], v107\n\t"
        "s_waitcnt vmcnt(3)\n\t"
        "v_smfmac_f32_16x16x64_bf16 v[40:43], v[80:83], a[144:151], v107\n\t"
        "v_max3_u32 v108, v80, v81, v108\n\t"
        "v_max3_u32 v108, v82, v83, v108\n\t"
        "v_smfmac_f32_16x16x64_bf16 v[36:39], v[80:83], a[136:143], v107\n\t"
        "v_smfmac_f32_16x16x64_bf16 v[32:35], v[80:83], a[128:135], v107\n\t"
        "v_smfmac_f32_16x16x64_bf16 v[44:47], v[80:83], a[152:159], v107\n\t"
        "s_waitcnt vmcnt(2)\n\t"
        "v_smfmac_f32_16x16x64_bf16 v[40:43], v[84:87], a[176:183], v107\n\t"
        "v_max3_u32 v108, v84, v85, v108\n\t"
        "v_max3_u32 v108, v86, v87, v108\n\t"
        "v_smfmac_f32_16x16x64_bf16 v[36:39], v[84:87], a[168:175], v107\n\t"
        "v_smfmac_f32_16x16x64_bf16 v[32:35], v[84:87], a[160:167], v107\n\t"
        "v_smfmac_f32_16x16x64_bf16 v[44:47], v[84:87], a[184:191], v107\n\t"
        "s_waitcnt vmcnt(1)\n\t"
        "v_smfmac_f32_16x16x64_bf16 v[40:43], v[88:91], a[208:215], v107\n\t"
        "v_max3_u32 v108, v88, v89, v108\n\t"
        "v_max3_u32 v108, v90, v91, v108\n\t"
        "v_smfmac_f32_16x16x64_bf16 v[36:39], v[88:91], a[200:207], v107\n\t"
        "v_smfmac_f32_16x16x64_bf16 v[32:35], v[88:91], a[192:199], v107\n\t"
        "v_smfmac_f32_16x16x64_bf16 v[44:47], v[88:91], a[216:223], v107\n\t"
        "s_waitcnt vmcnt(0)\n\t"
        "v_smfmac_f32_16x16x64_bf16 v[40:43], v[92:95], a[240:247], v107\n\t"
        "v_max3_u32 v108, v92, v93, v108\n\t"
        "v_max3_u32 v108, v94, v95, v108\n\t"
        "v_smfmac_f32_16x16x64_bf16 v[36:39], v[92:95], a[232:239], v107\n\t"
        "v_cmp_eq_u32_e32 vcc, -1, v108\n\t"
        "v_smfmac_f32_16x16x64_bf16 v[32:35], v[92:95], a[224:231], v107\n\t"
        "v_smfmac_f32_16x16x64_bf16 v[44:47], v[92:95], a[248:255], v107\n\t"
        "s_cbranch_vccnz LB_rofail%=\n\t"
        "s_nop 3\n\t"
        "v_permlane32_swap_b32_e32 v40, v36\n\t"
        "v_permlane32_swap_b32_e32 v41, v37\n\t"
        "v_permlane32_swap_b32_e32 v42, v38\n\t"
        "v_permlane32_swap_b32_e32 v43, v39\n\t"
        "v_pk_add_f32 v[32:33], v[32:33], v[36:37]\n\t"
        "v_pk_add_f32 v[34:35], v[34:35], v[38:39]\n\t"
        "s_nop 0\n\t"
        "v_add_f32_dpp v32, v40, v44 quad_perm:[0,1,2,3] row_mask:0xc bank_mask:0xf\n\t"
        "v_add_f32_dpp v33, v41, v45 quad_perm:[0,1,2,3] row_mask:0xc bank_mask:0xf\n\t"
        "v_add_f32_dpp v34, v42, v46 quad_perm:[0,1,2,3] row_mask:0xc bank_mask:0xf\n\t"
        "v_add_f32_dpp v35, v43, v47 quad_perm:[0,1,2,3] row_mask:0xc bank_mask:0xf\n\t"
        "v_permlane16_swap_b32_e32 v32, v33\n\t"
        "s_nop 0\n\t"
        "v_permlane16_swap_b32_e32 v34, v35\n\t"
        "v_add_f32_e32 v32, v32, v33\n\t"
        "v_add_f32_e32 v34, v34, v35\n\t"
        "ds_write2_b32 v120, v32, v34 offset0:0 offset1:64\n\t"
        "s_waitcnt lgkmcnt(0)\n\t"
        "s_barrier\n\t"
        "ds_read2st64_b64 v[64:67], v121 offset0:0 offset1:1\n\t"
        "ds_read2st64_b64 v[68:71], v121 offset0:2 offset1:3\n\t"
        "s_waitcnt lgkmcnt(1)\n\t"
        "v_pk_add_f32 v[64:65], v[64:65], v[66:67]\n\t"
        "s_waitcnt lgkmcnt(0)\n\t"
        "v_pk_add_f32 v[68:69], v[68:69], v[70:71]\n\t"
        "v_pk_add_f32 v[64:65], v[64:65], v[68:69]\n\t"
        "v_cvt_pk_bf16_f32 v64, v64, v65\n\t"
        "v_min_u32_e32 v64, -2, v64\n\t"
        "buffer_store_dword v64, v116, s[8:11], s27 offen sc1\n\t"
        "s_memrealtime s[32:33]\n\t"
        "s_waitcnt lgkmcnt(0)\n\t"
        "v_mov_b32_e32 v110, s32\n\t"
        "s_add_u32 s24, s24, 1\n\t"
        "s_mov_b32 s28, s26\n\t"
        "s_mov_b32 s26, s27\n\t"
        "s_add_u32 s27, s27, 0x80000\n\t"
        "s_and_b32 s27, s27, 0x1fffff\n\t"
        "s_branch LB_loop%=\n\t"
        "LB_end%=:\n\t"
        "s_endpgm\n\t"
        "LB_rdfail%=:\n\t"
        "s_or_b32 s47, s47, 1\n\t"
        "buffer_load_dwordx4 v[16:19], v112, s[8:11], s26 offen offset:0 sc1\n\t"
        "buffer_load_dwordx4 v[20:23], v112, s[8:11], s26 offen offset:1024 sc1\n\t"
        "buffer_load_dwordx4 v[24:27], v112, s[8:11], s26 offen offset:2048 sc1\n\t"
        "buffer_load_dwordx4 v[28:31], v112, s[8:11], s26 offen offset:3072 sc1\n\t"
        "s_nop 7\n\t"
        "v_mov_b64_e32 v[0:1], 0\n\t"
        "v_mov_b64_e32 v[2:3], 0\n\t"
        "v_mov_b64_e32 v[4:5], 0\n\t"
        "v_mov_b64_e32 v[6:7], 0\n\t"
        "v_mov_b64_e32 v[8:9], 0\n\t"
        "v_mov_b64_e32 v[10:11], 0\n\t"
        "v_mov_b64_e32 v[12:13], 0\n\t"
        "v_mov_b64_e32 v[14:15], 0\n\t"
        "s_branch LB_rdre%=w\n\t"
        "LB_rdre%=:\n\t"
        "buffer_load_dwordx4 v[16:19], v112, s[8:11], s26 offen offset:0 sc1\n\t"
        "buffer_load_dwordx4 v[20:23], v112, s[8:11], s26 offen offset:1024 sc1\n\t"
        "buffer_load_dwordx4 v[24:27], v112, s[8:11], s26 offen offset:2048 sc1\n\t"
        "buffer_load_dwordx4 v[28:31], v112, s[8:11], s26 offen offset:3072 sc1\n\t"
        "LB_rdre%=w:\n\t"
        "s_waitcnt vmcnt(0) lgkmcnt(0)\n\t"
        "v_max3_u32 v108, v16, v17, v18\n\t"
        "v_max3_u32 v108, v108, v19, v20\n\t"
        "v_max3_u32 v108, v108, v21, v22\n\t"
        "v_max3_u32 v108, v108, v23, v24\n\t"
        "v_max3_u32 v108, v108, v25, v26\n\t"
        "v_max3_u32 v108, v108, v27, v28\n\t"
        "v_max3_u32 v108, v108, v29, v30\n\t"
        "v_max_u32_e32 v108, v108, v31\n\t"
        "v_cmp_eq_u32_e32 vcc, -1, v108\n\t"
        "s_nop 0\n\t"
        "s_cbranch_vccz LB_go%=\n\t"
        "s_add_u32 s42, s42, 1\n\t"
        "s_cmp_lt_u32 s42, 0x100000\n\t"
        "s_cbranch_scc1 LB_rdre%=\n\t"
        "s_movk_i32 s34, 3\n\t"
        "s_branch LB_die%=\n\t"
        "LB_rofail%=:\n\t"
        "s_or_b32 s47, s47, 4\n\t"
        "buffer_load_dwordx4 v[64:67], v115, s[8:11], s46 offen offset:0 sc1\n\t"
        "buffer_load_dwordx4 v[68:71], v115, s[8:11], s46 offen offset:1024 sc1\n\t"
        "buffer_load_dwordx4 v[72:75], v115, s[8:11], s48 offen offset:0 sc1\n\t"
        "buffer_load_dwordx4 v[76:79], v115, s[8:11], s48 offen offset:1024 sc1\n\t"
        "buffer_load_dwordx4 v[80:83], v115, s[8:11], s73 offen offset:0 sc1\n\t"
        "buffer_load_dwordx4 v[84:87], v115, s[8:11], s73 offen offset:1024 sc1\n\t"
        "buffer_load_dwordx4 v[88:91], v115, s[8:11], s74 offen offset:0 sc1\n\t"
        "buffer_load_dwordx4 v[92:95], v115, s[8:11], s74 offen offset:1024 sc1\n\t"
        "s_nop 7\n\t"
        "v_mov_b64_e32 v[32:33], 0\n\t"
        "v_mov_b64_e32 v[34:35], 0\n\t"
        "v_mov_b64_e32 v[36:37], 0\n\t"
        "v_mov_b64_e32 v[38:39], 0\n\t"
        "v_mov_b64_e32 v[40:41], 0\n\t"
        "v_mov_b64_e32 v[42:43], 0\n\t"
        "v_mov_b64_e32 v[44:45], 0\n\t"
        "v_mov_b64_e32 v[46:47], 0\n\t"
        "s_branch LB_rore%=w\n\t"
        "LB_rore%=:\n\t"
        "buffer_load_dwordx4 v[64:67], v115, s[8:11], s46 offen offset:0 sc1\n\t"
        "buffer_load_dwordx4 v[68:71], v115, s[8:11], s46 offen offset:1024 sc1\n\t"
        "buffer_load_dwordx4 v[72:75], v115, s[8:11], s48 offen offset:0 sc1\n\t"
        "buffer_load_dwordx4 v[76:79], v115, s[8:11], s48 offen offset:1024 sc1\n\t"
        "buffer_load_dwordx4 v[80:83], v115, s[8:11], s73 offen offset:0 sc1\n\t"
        "buffer_load_dwordx4 v[84:87], v115, s[8:11], s73 offen offset:1024 sc1\n\t"
        "buffer_load_dwordx4 v[88:91], v115, s[8:11], s74 offen offset:0 sc1\n\t"
        "buffer_load_dwordx4 v[92:95], v115, s[8:11], s74 offen offset:1024 sc1\n\t"
        "LB_rore%=w:\n\t"
        "s_waitcnt vmcnt(0) lgkmcnt(0)\n\t"
        "v_max3_u32 v108, v64, v65, v66\n\t"
        "v_max3_u32 v108, v108, v67, v68\n\t"
        "v_max3_u32 v108, v108, v69, v70\n\t"
        "v_max3_u32 v108, v108, v71, v72\n\t"
        "v_max3_u32 v108, v108, v73, v74\n\t"
        "v_max3_u32 v108, v108, v75, v76\n\t"
        "v_max3_u32 v108, v108, v77, v78\n\t"
        "v_max3_u32 v108, v108, v79, v80\n\t"
        "v_max3_u32 v108, v108, v81, v82\n\t"
        "v_max3_u32 v108, v108, v83, v84\n\t"
        "v_max3_u32 v108, v108, v85, v86\n\t"
        "v_max3_u32 v108, v108, v87, v88\n\t"
        "v_max3_u32 v108, v108, v89, v90\n\t"
        "v_max3_u32 v108, v108, v91, v92\n\t"
        "v_max3_u32 v108, v108, v93, v94\n\t"
        "v_max_u32_e32 v108, v108, v95\n\t"
        "v_cmp_eq_u32_e32 vcc, -1, v108\n\t"
        "s_nop 0\n\t"
        "s_cbranch_vccz LB_go2%=\n\t"
        "s_add_u32 s42, s42, 1\n\t"
        "s_cmp_lt_u32 s42, 0x100000\n\t"
        "s_cbranch_scc1 LB_rore%=\n\t"
        "s_movk_i32 s34, 4\n\t"
        "s_branch LB_die%=\n\t"
        "LB_die%=:\n\t"
        "v_mov_b32_e32 v30, s34\n\t"
        "s_mov_b32 s35, 33024\n\t"
        "s_mov_b64 exec, 1\n\t"
        "buffer_store_dword v30, off, s[12:15], s35 sc1\n\t"
        "s_endpgm\n\t"
        :: REC_S_IN, "{v96}"(g_v), "{v97}"(g_o), "{v98}"(wb0), "{v99}"(wb1), "{v100}"(rb0), "{v101}"(rb0 ^ 32u), "{v102}"(rb0 ^ 64u), "{v103}"(rb0 ^ 96u),
           "{v106}"(pp ? 0xEEEEEEEEu : 0x44444444u), "{v107}"(bb ? 0xEEEEEEEEu : 0x44444444u), "{v[108:109]}"(m0p), "{v[104:105]}"(tp), "{s68}"(rot),
           "{v112}"(a_mem), "{v113}"((24 + cp) * 16384 + l * 128), "{v114}"(a_vmw), "{v115}"(a_or), "{v116}"(a_mw), "{v117}"(a_sw),
           "{v118}"(lb + w * 2048 + l * 16), "{v119}"(lb + (w & 1) * 1024 + l * 16), "{v120}"(lb + LB_R + w * 512 + (g & 1) * 128 + ((g >> 1) * 16 + n) * 4),
           "{v121}"(lb + LB_R + ((l >> 2) & 3) * 128 + g * 32 + (l & 3) * 8), "{v122}"(stg + l * 16), "{v123}"(o_r), "{v124}"(rb + w == 32 ? 28 * 16384 + (l & 31) * 128 : OOB)
        : "memory", "vcc", "scc", "v0", "v255", "a0", "a255", "s101");
    __builtin_unreachable();
}
__device__ __forceinline__ void rec_helper(const Params& p, rsrc_t r, unsigned char* lds, const int wv, const int hi) {
    const int tid = otid(), l = tid & 63;
    const rsrc_t rh = __builtin_amdgcn_make_buffer_rsrc(p.ws + hx_base(p.ws), 0, HX_BYTES, 0x00020000);
    const v4u ones = {0xFFFFFFFFu, 0xFFFFFFFFu, 0xFFFFFFFFu, 0xFFFFFFFFu};
    const unsigned ca = l >= 8 ? OOB : (31 + wv * 32) * 16384 + hi * 256 + l * 16;
    unsigned best0 = ~0u, best1 = ~0u;
    for (int i = 0; i < CAL_N; ++i) {
        const unsigned t0 = (unsigned)__builtin_amdgcn_s_memtime(); __builtin_amdgcn_s_waitcnt(0);
        st16(rh, ca, ones); DRAIN();
        const unsigned t1 = (unsigned)__builtin_amdgcn_s_memtime(); __builtin_amdgcn_s_waitcnt(0);
        st16(rh, ca + 8192, ones); DRAIN();
        const unsigned t2 = (unsigned)__builtin_amdgcn_s_memtime(); __builtin_amdgcn_s_waitcnt(0);
        best0 = min(best0, t1 - t0); best1 = min(best1, t2 - t1); }
    if (l == 0) *(v2u*)(lds + wv * 8) = (v2u){best0, best1};
#pragma unroll
    for (int i = 0; i < 16; ++i) st16(rh, hi * 65536 + i * 4096 + tid * 16, ones);
    DRAIN();
    __syncthreads();
    if (tid == 0) { const v4u a = *(const v4u*)lds, b = *(const v4u*)(lds + 16);
        best0 = min(min(a.x, a.z), min(b.x, b.z)); best1 = min(min(a.y, a.w), min(b.y, b.w));
        st4(r, F_CAL + hi * 4, best1 < best0 ? 3u : 2u); }
    __syncthreads();
}
using f32x16 = __attribute__((ext_vector_type(16))) float;
constexpr int CV_RP = 80;
constexpr int CV_U = 96 * CV_RP;
constexpr int CV_KP = 4384;
constexpr int CV_K0 = 8 * CV_U;
__device__ __forceinline__ float blo(unsigned x) { return __uint_as_float(x << 16); }
__device__ __forceinline__ float bhi(unsigned x) { return __uint_as_float(x & 0xFFFF0000u); }
__device__ __forceinline__ void gate_block(const Params& p, rsrc_t r, const int hi, const int wv) {
    const int l = otid() & 63, t0 = hi * 64 + wv * 16;
    v4u xg[16][2], gw[2];
#pragma unroll
    for (int q = 0; q < 2; ++q) gw[q] = *(const v4u*)(p.gate_w + q * 512 + l * 8);
#pragma unroll
    for (int i = 0; i < 16; ++i)
#pragma unroll
        for (int q = 0; q < 2; ++q) xg[i][q] = *(const v4u*)(p.Aq + (size_t)(t0 + i) * D + q * 512 + l * 8);
    float mine = 0.f;
#pragma unroll
    for (int i = 0; i < 16; ++i) { float acc = 0.f;
#pragma unroll
        for (int q = 0; q < 2; ++q)
#pragma unroll
            for (int e = 0; e < 4; ++e) { const unsigned pr = pack2(blo(xg[i][q][e]) * blo(gw[q][e]), bhi(xg[i][q][e]) * bhi(gw[q][e])); acc += blo(pr); acc += bhi(pr); }
        for (int o = 32; o > 0; o >>= 1) acc += __shfl_xor(acc, o);
        if (l == i) mine = acc; }
    if (l < 16) { const float z = rbfh(mine); st4(r, W_GATE + (t0 + l) * 4, __float_as_uint(rbfh(1.f / (1.f + expf(-z))))); }
}
__device__ __forceinline__ void conv_block(const Params& p, rsrc_t r, unsigned char* lds, const int wv, const int hi, const int j) {
    (void)j;
    const int tid = otid(), w = wv, l = tid & 63, Q = tid >> 2, cg = tid & 3, ch0 = hi * 32 + cg * 8;
    for (int i = tid; i < 8 * 640; i += 256) *(unsigned*)(lds + (i / 640) * CV_U + (i % 640) * 4) = 0u;
    unsigned char* kc = lds + CV_K0 + w * 4 * CV_KP;
    if (l < 32) {
#pragma unroll
        for (int q = 0; q < 4; ++q) { *(unsigned*)(kc + q * CV_KP + l * 4) = 0u; *(unsigned*)(kc + q * CV_KP + 4224 + l * 4) = 0u; } }
    v4u u[32];
    {
        v4u sk[4]; float sv[8][4];
#pragma unroll
        for (int q = 0; q < 4; ++q) sk[q] = *(const v4u*)(p.shift_k + ch0 * 4 + q * 8);
        const rsrc_t rk = __builtin_amdgcn_make_buffer_rsrc((void*)p.Ak, 0, S * D * 2, 0x00020000), rv = __builtin_amdgcn_make_buffer_rsrc((void*)p.Av, 0, S * D * 2, 0x00020000);
        const unsigned ro = (unsigned)(Q * 32 * D + ch0) * 2u;
        v4u kr[19], kn[16];
#pragma unroll
        for (int i = 0; i < 19; ++i) kr[i] = __builtin_amdgcn_raw_buffer_load_b128(rk, (i < 3 && Q == 0) ? OOB : ro + (i - 3) * 2048, 0, CV_POL);
#pragma unroll
        for (int i = 0; i < 16; ++i) u[i] = __builtin_amdgcn_raw_buffer_load_b128(rv, ro + i * 2048, 0, CV_POL);
#pragma unroll
        for (int i = 0; i < 16; ++i) kn[i] = __builtin_amdgcn_raw_buffer_load_b128(rk, ro + (16 + i) * 2048, 0, CV_POL);
#pragma unroll
        for (int c8 = 0; c8 < 8; ++c8)
#pragma unroll
            for (int tp = 0; tp < 4; ++tp) { const unsigned sw = sk[c8 >> 1][(c8 & 1) * 2 + (tp >> 1)]; sv[c8][tp] = (tp & 1) ? bhi(sw) : blo(sw); }
        auto row = [&](int i, const v4u& k0, const v4u& k1, const v4u& k2, const v4u& k3) {
#pragma unroll
            for (int q = 0; q < 4; ++q) {
                const float a = fmaf(blo(k3[q]), sv[2 * q][0], fmaf(blo(k2[q]), sv[2 * q][1], fmaf(blo(k1[q]), sv[2 * q][2], blo(k0[q]) * sv[2 * q][3])));
                const float b = fmaf(bhi(k3[q]), sv[2 * q + 1][0], fmaf(bhi(k2[q]), sv[2 * q + 1][1], fmaf(bhi(k1[q]), sv[2 * q + 1][2], bhi(k0[q]) * sv[2 * q + 1][3])));
                const unsigned ks = pack2(a, b);
                u[i][q] = pack2(blo(ks) * blo(u[i][q]), bhi(ks) * bhi(u[i][q])); } };
        __builtin_amdgcn_sched_barrier(0);
#pragma unroll
        for (int i = 0; i < 16; ++i) { row(i, kr[i], kr[i + 1], kr[i + 2], kr[i + 3]); u[16 + i] = __builtin_amdgcn_raw_buffer_load_b128(rv, ro + (16 + i) * 2048, 0, CV_POL); __builtin_amdgcn_sched_barrier(0); }
#pragma unroll
        for (int i = 0; i < 16; ++i) row(16 + i, i < 3 ? kr[16 + i] : kn[i - 3], i < 2 ? kr[17 + i] : kn[i - 2], i < 1 ? kr[18] : kn[i - 1], kn[i]);
    }
    BSTAMP(8);
    const rsrc_t rs = __builtin_amdgcn_make_buffer_rsrc((void*)p.ssm_k, 0, S * D * 2, 0x00020000);
    const int b = l & 31, kg = l >> 5, cc = (b + 1) & 3;
    v4u kraw[4]; v2u kext[4];
    auto kload = [&](int n) { const unsigned o = (unsigned)((hi * 32 + w * 8 + n) * S + l * 8) * 2u;
#pragma unroll
        for (int i = 0; i < 4; ++i) { kraw[i] = __builtin_amdgcn_raw_buffer_load_b128(rs, o + i * 1024, 0, CV_POL);
            kext[i] = __builtin_amdgcn_raw_buffer_load_b64(rs, (i == 3 && l == 63) ? OOB : o + i * 1024 + 16, 0, 0); } };
    kload(0);
    const v4u dv = *(const v4u*)(p.ssm_D + ch0);
    unsigned char* const urow = lds + 2 * cg * CV_U + (32 + Q) * CV_RP;
#pragma unroll
    for (int rd = 0; rd < 4; ++rd) {
#pragma unroll
        for (int G = 0; G < 4; ++G) { v4u lo, hh;
#pragma unroll
            for (int q = 0; q < 4; ++q) { const unsigned x = u[8 * G + 7 - 2 * q][rd], y = u[8 * G + 6 - 2 * q][rd];
                lo[q] = __builtin_amdgcn_perm(y, x, 0x05040100u); hh[q] = __builtin_amdgcn_perm(y, x, 0x07060302u); }
            *(v4u*)(urow + G * 16) = lo; *(v4u*)(urow + CV_U + G * 16) = hh; }
        __syncthreads();
#pragma unroll 1
        for (int ci = 0; ci < 2; ++ci) {
            const int c8 = 2 * w + ci;
#pragma unroll
            for (int i = 0; i < 4; ++i) { const v4u e = kraw[i]; const v2u f = kext[i]; unsigned char* q = kc + 128 + (i * 64 + l) * 16;
                const unsigned s0 = __builtin_amdgcn_alignbit(e.y, e.x, 16), s1 = __builtin_amdgcn_alignbit(e.z, e.y, 16), s2 = __builtin_amdgcn_alignbit(e.w, e.z, 16),
                               s3 = __builtin_amdgcn_alignbit(f.x, e.w, 16), s4 = __builtin_amdgcn_alignbit(f.y, f.x, 16);
                *(v4u*)q = e; *(v4u*)(q + CV_KP) = (v4u){s0, s1, s2, s3}; *(v4u*)(q + 2 * CV_KP) = (v4u){e.y, e.z, e.w, f.x}; *(v4u*)(q + 3 * CV_KP) = (v4u){s1, s2, s3, s4};
                if (i == 0 && l == 0) {
                    *(v2u*)(q + CV_KP - 8) = (v2u){0u, e.x << 16}; *(v2u*)(q + 2 * CV_KP - 8) = (v2u){0u, e.x}; *(v2u*)(q + 3 * CV_KP - 8) = (v2u){e.x << 16, s0}; } }
            if (2 * rd + ci < 7) kload(2 * rd + ci + 1);
            __builtin_amdgcn_wave_barrier();
            f32x16 acc0, acc1;
#pragma unroll
            for (int i = 0; i < 16; ++i) { acc0[i] = 0.f; acc1[i] = 0.f; }
            const unsigned char* kb = kc + cc * CV_KP + (b - 8 * kg - 7 + 64 - cc) * 2;
            const unsigned char* ub = lds + c8 * CV_U + (32 + b) * CV_RP + kg * 16;
            v2u ka[4][2], kz[4][2]; bf16x8 fa[4], fz[4];
            auto ld_k = [&](int set, int un, int hi2) { const int dl = un >> 1, kh = un & 1; const unsigned char* q = kb + (32 * dl - 16 * kh) * 2 + hi2 * 2048;
                if (hi2) { kz[set][0] = *(const v2u*)q; kz[set][1] = *(const v2u*)(q + 8); } else { ka[set][0] = *(const v2u*)q; ka[set][1] = *(const v2u*)(q + 8); } };
            auto ld_f = [&](int set, int un) { const int dl = un >> 1, kh = un & 1;
                fa[set] = *(const bf16x8*)(ub + (32 - dl) * CV_RP + kh * 32); fz[set] = *(const bf16x8*)(ub - dl * CV_RP + kh * 32); };
#pragma unroll
            for (int q = 0; q < 2; ++q) { ld_k(q, q, 0); ld_f(q, q); ld_k(q, q, 1); }
#pragma unroll 1
            for (int u0 = 0; u0 < 64; u0 += 4) {
#pragma unroll
                for (int q = 0; q < 4; ++q) {
                    const int un = min(u0 + q + 2, 63), ns = (q + 2) & 3;
                    const v4u kav = {ka[q][0].x, ka[q][0].y, ka[q][1].x, ka[q][1].y}, kzv = {kz[q][0].x, kz[q][0].y, kz[q][1].x, kz[q][1].y};
                    __builtin_amdgcn_sched_barrier(0);
                    asm volatile("s_nop 1\n\tv_mfma_f32_32x32x16_bf16 %0, %2, %1, %0" : "+v"(acc1) : "v"(fa[q]), "v"(kav));
                    ld_k(ns, un, 0);
                    __builtin_amdgcn_sched_barrier(0);
                    asm volatile("v_mfma_f32_32x32x16_bf16 %0, %2, %1, %0" : "+v"(acc0) : "v"(fz[q]), "v"(kav));
                    ld_f(ns, un);
                    __builtin_amdgcn_sched_barrier(0);
                    asm volatile("v_mfma_f32_32x32x16_bf16 %0, %2, %1, %0" : "+v"(acc1) : "v"(fz[q]), "v"(kzv));
                    ld_k(ns, un, 1);
                }
            }
            asm volatile("s_waitcnt lgkmcnt(0)\n\ts_nop 7\n\ts_nop 7" : "+v"(acc0), "+v"(acc1) :: "memory");
            unsigned char* uc = lds + c8 * CV_U + (32 + b) * CV_RP + kg * 8;
#pragma unroll
            for (int q = 0; q < 4; ++q) {
                *(v2u*)(uc + q * 16) = (v2u){pack2(acc0[4 * q], acc0[4 * q + 1]), pack2(acc0[4 * q + 2], acc0[4 * q + 3])};
                *(v2u*)(uc + 32 * CV_RP + q * 16) = (v2u){pack2(acc1[4 * q], acc1[4 * q + 1]), pack2(acc1[4 * q + 2], acc1[4 * q + 3])}; }
            __builtin_amdgcn_wave_barrier();
        }
        __syncthreads();
        const float d0 = blo(dv[rd]), d1 = bhi(dv[rd]);
#pragma unroll
        for (int G = 0; G < 4; ++G) { const v4u lo = *(const v4u*)(urow + G * 16), hh = *(const v4u*)(urow + CV_U + G * 16);
#pragma unroll
            for (int q = 0; q < 8; ++q) { const unsigned x = u[8 * G + q][rd], ud = pack2(blo(x) * d0, bhi(x) * d1);
                u[8 * G + q][rd] = pack2(((q & 1) ? bhi(lo[q >> 1]) : blo(lo[q >> 1])) + blo(ud), ((q & 1) ? bhi(hh[q >> 1]) : blo(hh[q >> 1])) + bhi(ud)); } }
        if (rd == 0) BSTAMP(9);
    }
    BSTAMP(11);
#pragma unroll
    for (int hf = 0; hf < 2; ++hf) { v4u qv[16];
#pragma unroll
        for (int i = 0; i < 16; ++i) qv[i] = *(const v4u*)(p.Aq + (size_t)(Q * 32 + hf * 16 + i) * D + ch0);
#pragma unroll
        for (int i = 0; i < 16; ++i) { v4u o;
#pragma unroll
            for (int q = 0; q < 4; ++q) o[q] = pack2(blo(qv[i][q]) * blo(u[hf * 16 + i][q]), bhi(qv[i][q]) * bhi(u[hf * 16 + i][q]));
            st16(r, W_A + hi * 131072 + (hf * 16 + i) * 4096 + tid * 16, o); } }
    DRAIN(); __syncthreads();
    if (tid == 0) __hip_atomic_fetch_add((unsigned*)(p.ws + F_CONV), 1u, __ATOMIC_RELAXED, __HIP_MEMORY_SCOPE_AGENT);
}

__device__ __forceinline__ void proj_block(const Params& p, unsigned char* lds, const int wv, const int h, const int c, const int j) {
    const int w = wv, l = otid() & 63, n = l & 15, g = l >> 4, tm = w >> 1, tn = w & 1;
    const unsigned lb = __builtin_amdgcn_readfirstlane((unsigned)(size_t)lds);
    const v4u sx = mk_srd(p.Aq, S * D * 2), sq = mk_srd(c < 2 ? p.Wq : p.Wk, 1024 * 4096 * 2), sv = mk_srd(p.Wv, 1024 * 4096 * 2), sw = mk_srd(p.ws, W_TOTAL);
    const unsigned lx = lb + (32 * tm + n) * 128 + ((g ^ (n & 7)) << 4), lr = lb + 131072, rr = 8 * g + (n >> 2);
    const int cv = ((c & 1) ^ 1) * 2 + (c >> 1);
    const unsigned aq = (c < 2 ? W_QX : W_KX) + (tm * 32 + h) * 8192 + (2 * (c & 1) + tn) * 1024 + l * 16;
    unsigned t0, xo, so, cn; unsigned long long ts;
    asm volatile(
        "s_mov_b32 %[xo], 0\n\t"
        "s_mov_b32 %[so], 0xf0000000\n\t"
        "s_mov_b32 %[cn], 0\n\t"
        "s_add_u32 m0, %[ld], 0\n\t"
        "s_add_u32 %[t0], %[xo], 0\n\t"
        "buffer_load_dwordx4 v100, %[sx], %[t0] offen lds\n\t"
        "s_add_u32 m0, %[ld], 1024\n\t"
        "s_add_u32 %[t0], %[xo], 16384\n\t"
        "buffer_load_dwordx4 v100, %[sx], %[t0] offen lds\n\t"
        "s_add_u32 m0, %[ld], 8192\n\t"
        "s_add_u32 %[t0], %[xo], 128\n\t"
        "buffer_load_dwordx4 v100, %[sx], %[t0] offen lds\n\t"
        "s_add_u32 m0, %[ld], 9216\n\t"
        "s_add_u32 %[t0], %[xo], 16512\n\t"
        "buffer_load_dwordx4 v100, %[sx], %[t0] offen lds\n\t"
        "s_add_u32 m0, %[ld], 16384\n\t"
        "s_add_u32 %[t0], %[xo], 256\n\t"
        "buffer_load_dwordx4 v100, %[sx], %[t0] offen lds\n\t"
        "s_add_u32 m0, %[ld], 17408\n\t"
        "s_add_u32 %[t0], %[xo], 16640\n\t"
        "buffer_load_dwordx4 v100, %[sx], %[t0] offen lds\n\t"
        "s_add_u32 m0, %[ld], 24576\n\t"
        "s_add_u32 %[t0], %[xo], 384\n\t"
        "buffer_load_dwordx4 v100, %[sx], %[t0] offen lds\n\t"
        "s_add_u32 m0, %[ld], 25600\n\t"
        "s_add_u32 %[t0], %[xo], 16768\n\t"
        "buffer_load_dwordx4 v100, %[sx], %[t0] offen lds\n\t"
        "s_mov_b32 %[t0], 0\n\t"
        "buffer_load_dwordx4 a[192:195], v101, %[sq], %[t0] offen nt\n\t"
        "buffer_load_dwordx4 a[200:203], v112, %[sv], %[t0] offen nt\n\t"
        "s_mov_b32 %[t0], 262144\n\t"
        "buffer_load_dwordx4 a[196:199], v101, %[sq], %[t0] offen nt\n\t"
        "s_mov_b32 %[t0], 524288\n\t"
        "buffer_load_dwordx4 a[204:207], v101, %[sq], %[t0] offen nt\n\t"
        "buffer_load_dwordx4 a[212:215], v112, %[sv], %[t0] offen nt\n\t"
        "s_mov_b32 %[t0], 786432\n\t"
        "buffer_load_dwordx4 a[208:211], v101, %[sq], %[t0] offen nt\n\t"
        "s_mov_b32 %[t0], 1048576\n\t"
        "buffer_load_dwordx4 a[216:219], v101, %[sq], %[t0] offen nt\n\t"
        "buffer_load_dwordx4 a[224:227], v112, %[sv], %[t0] offen nt\n\t"
        "s_mov_b32 %[t0], 1310720\n\t"
        "buffer_load_dwordx4 a[220:223], v101, %[sq], %[t0] offen nt\n\t"
        "s_mov_b32 %[t0], 1572864\n\t"
        "buffer_load_dwordx4 a[228:231], v101, %[sq], %[t0] offen nt\n\t"
        "buffer_load_dwordx4 a[236:239], v112, %[sv], %[t0] offen nt\n\t"
        "s_mov_b32 %[t0], 1835008\n\t"
        "buffer_load_dwordx4 a[232:235], v101, %[sq], %[t0] offen nt\n\t"
        "s_mov_b32 %[t0], 2097152\n\t"
        "buffer_load_dwordx4 a[240:243], v101, %[sq], %[t0] offen nt\n\t"
        "buffer_load_dwordx4 a[248:251], v112, %[sv], %[t0] offen nt\n\t"
        "s_mov_b32 %[t0], 2359296\n\t"
        "buffer_load_dwordx4 a[244:247], v101, %[sq], %[t0] offen nt\n\t"
        "s_mov_b32 %[t0], 2621440\n\t"
        "buffer_load_dwordx4 a[252:255], v101, %[sq], %[t0] offen nt\n\t"
        "buffer_load_dwordx4 v[132:135], v112, %[sv], %[t0] offen nt\n\t"
        "s_mov_b32 %[t0], 2883584\n\t"
        "buffer_load_dwordx4 v[128:131], v101, %[sq], %[t0] offen nt\n\t"
        "s_mov_b32 %[t0], 3145728\n\t"
        "buffer_load_dwordx4 v[136:139], v101, %[sq], %[t0] offen nt\n\t"
        "buffer_load_dwordx4 v[144:147], v112, %[sv], %[t0] offen nt\n\t"
        "s_mov_b32 %[t0], 3407872\n\t"
        "buffer_load_dwordx4 v[140:143], v101, %[sq], %[t0] offen nt\n\t"
        "s_mov_b32 %[t0], 3670016\n\t"
        "buffer_load_dwordx4 v[148:151], v101, %[sq], %[t0] offen nt\n\t"
        "buffer_load_dwordx4 v[156:159], v112, %[sv], %[t0] offen nt\n\t"
        "s_mov_b32 %[t0], 3932160\n\t"
        "buffer_load_dwordx4 v[152:155], v101, %[sq], %[t0] offen nt\n\t"
        "s_mov_b32 %[t0], 4194304\n\t"
        "buffer_load_dwordx4 v[160:163], v101, %[sq], %[t0] offen nt\n\t"
        "buffer_load_dwordx4 v[168:171], v112, %[sv], %[t0] offen nt\n\t"
        "s_mov_b32 %[t0], 4456448\n\t"
        "buffer_load_dwordx4 v[164:167], v101, %[sq], %[t0] offen nt\n\t"
        "s_mov_b32 %[t0], 4718592\n\t"
        "buffer_load_dwordx4 v[172:175], v101, %[sq], %[t0] offen nt\n\t"
        "buffer_load_dwordx4 v[180:183], v112, %[sv], %[t0] offen nt\n\t"
        "s_mov_b32 %[t0], 4980736\n\t"
        "buffer_load_dwordx4 v[176:179], v101, %[sq], %[t0] offen nt\n\t"
        "s_mov_b32 %[t0], 5242880\n\t"
        "buffer_load_dwordx4 v[184:187], v101, %[sq], %[t0] offen nt\n\t"
        "buffer_load_dwordx4 v[192:195], v112, %[sv], %[t0] offen nt\n\t"
        "s_mov_b32 %[t0], 5505024\n\t"
        "buffer_load_dwordx4 v[188:191], v101, %[sq], %[t0] offen nt\n\t"
        "s_mov_b32 %[t0], 5767168\n\t"
        "buffer_load_dwordx4 v[196:199], v101, %[sq], %[t0] offen nt\n\t"
        "buffer_load_dwordx4 v[204:207], v112, %[sv], %[t0] offen nt\n\t"
        "s_mov_b32 %[t0], 6029312\n\t"
        "buffer_load_dwordx4 v[200:203], v101, %[sq], %[t0] offen nt\n\t"
        "s_mov_b32 %[t0], 6291456\n\t"
        "buffer_load_dwordx4 v[208:211], v101, %[sq], %[t0] offen nt\n\t"
        "buffer_load_dwordx4 v[216:219], v112, %[sv], %[t0] offen nt\n\t"
        "s_mov_b32 %[t0], 6553600\n\t"
        "buffer_load_dwordx4 v[212:215], v101, %[sq], %[t0] offen nt\n\t"
        "s_mov_b32 %[t0], 6815744\n\t"
        "buffer_load_dwordx4 v[220:223], v101, %[sq], %[t0] offen nt\n\t"
        "buffer_load_dwordx4 v[228:231], v112, %[sv], %[t0] offen nt\n\t"
        "s_mov_b32 %[t0], 7077888\n\t"
        "buffer_load_dwordx4 v[224:227], v101, %[sq], %[t0] offen nt\n\t"
        "s_mov_b32 %[t0], 7340032\n\t"
        "buffer_load_dwordx4 v[232:235], v101, %[sq], %[t0] offen nt\n\t"
        "buffer_load_dwordx4 v[240:243], v112, %[sv], %[t0] offen nt\n\t"
        "s_mov_b32 %[t0], 7602176\n\t"
        "buffer_load_dwordx4 v[236:239], v101, %[sq], %[t0] offen nt\n\t"
        "s_mov_b32 %[t0], 7864320\n\t"
        "buffer_load_dwordx4 v[244:247], v101, %[sq], %[t0] offen nt\n\t"
        "buffer_load_dwordx4 v[252:255], v112, %[sv], %[t0] offen nt\n\t"
        "s_mov_b32 %[t0], 8126464\n\t"
        "buffer_load_dwordx4 v[248:251], v101, %[sq], %[t0] offen nt\n\t"
        "s_waitcnt vmcnt(45)\n\t"
        "ds_write_b128 v102, a[192:195] offset:0\n\t"
        "ds_write_b128 v102, a[196:199] offset:4096\n\t"
        "ds_write_b128 v102, a[200:203] offset:8192\n\t"
        "s_waitcnt lgkmcnt(0)\n\t"
        "s_barrier\n\t"
        "ds_read_b64_tr_b16 a[0:1], v103 offset:0\n\t"
        "ds_read_b64_tr_b16 a[2:3], v103 offset:512\n\t"
        "ds_read_b64_tr_b16 a[4:5], v103 offset:8\n\t"
        "ds_read_b64_tr_b16 a[6:7], v103 offset:520\n\t"
        "ds_read_b64_tr_b16 a[8:9], v104 offset:0\n\t"
        "ds_read_b64_tr_b16 a[10:11], v104 offset:256\n\t"
        "ds_read_b64_tr_b16 a[12:13], v103 offset:4096\n\t"
        "ds_read_b64_tr_b16 a[14:15], v103 offset:4608\n\t"
        "ds_read_b64_tr_b16 a[16:17], v103 offset:4104\n\t"
        "ds_read_b64_tr_b16 a[18:19], v103 offset:4616\n\t"
        "ds_read_b64_tr_b16 a[20:21], v104 offset:2048\n\t"
        "ds_read_b64_tr_b16 a[22:23], v104 offset:2304\n\t"
        "s_waitcnt vmcnt(42)\n\t"
        "ds_write_b128 v102, a[204:207] offset:16384\n\t"
        "ds_write_b128 v102, a[208:211] offset:20480\n\t"
        "ds_write_b128 v102, a[212:215] offset:24576\n\t"
        "s_waitcnt lgkmcnt(0)\n\t"
        "s_barrier\n\t"
        "ds_read_b64_tr_b16 a[24:25], v103 offset:16384\n\t"
        "ds_read_b64_tr_b16 a[26:27], v103 offset:16896\n\t"
        "ds_read_b64_tr_b16 a[28:29], v103 offset:16392\n\t"
        "ds_read_b64_tr_b16 a[30:31], v103 offset:16904\n\t"
        "ds_read_b64_tr_b16 a[32:33], v104 offset:16384\n\t"
        "ds_read_b64_tr_b16 a[34:35], v104 offset:16640\n\t"
        "ds_read_b64_tr_b16 a[36:37], v103 offset:20480\n\t"
        "ds_read_b64_tr_b16 a[38:39], v103 offset:20992\n\t"
        "ds_read_b64_tr_b16 a[40:41], v103 offset:20488\n\t"
        "ds_read_b64_tr_b16 a[42:43], v103 offset:21000\n\t"
        "ds_read_b64_tr_b16 a[44:45], v104 offset:18432\n\t"
        "ds_read_b64_tr_b16 a[46:47], v104 offset:18688\n\t"
        "s_waitcnt vmcnt(39)\n\t"
        "ds_write_b128 v102, a[216:219] offset:0\n\t"
        "ds_write_b128 v102, a[220:223] offset:4096\n\t"
        "ds_write_b128 v102, a[224:227] offset:8192\n\t"
        "s_add_u32 m0, %[ld], 32768\n\t"
        "s_add_u32 %[t0], %[xo], 512\n\t"
        "buffer_load_dwordx4 v100, %[sx], %[t0] offen lds\n\t"
        "s_add_u32 m0, %[ld], 33792\n\t"
        "s_add_u32 %[t0], %[xo], 16896\n\t"
        "buffer_load_dwordx4 v100, %[sx], %[t0] offen lds\n\t"
        "s_add_u32 m0, %[ld], 40960\n\t"
        "s_add_u32 %[t0], %[xo], 640\n\t"
        "buffer_load_dwordx4 v100, %[sx], %[t0] offen lds\n\t"
        "s_add_u32 m0, %[ld], 41984\n\t"
        "s_add_u32 %[t0], %[xo], 17024\n\t"
        "buffer_load_dwordx4 v100, %[sx], %[t0] offen lds\n\t"
        "s_add_u32 m0, %[ld], 49152\n\t"
        "s_add_u32 %[t0], %[xo], 768\n\t"
        "buffer_load_dwordx4 v100, %[sx], %[t0] offen lds\n\t"
        "s_add_u32 m0, %[ld], 50176\n\t"
        "s_add_u32 %[t0], %[xo], 17152\n\t"
        "buffer_load_dwordx4 v100, %[sx], %[t0] offen lds\n\t"
        "s_add_u32 m0, %[ld], 57344\n\t"
        "s_add_u32 %[t0], %[xo], 896\n\t"
        "buffer_load_dwordx4 v100, %[sx], %[t0] offen lds\n\t"
        "s_add_u32 m0, %[ld], 58368\n\t"
        "s_add_u32 %[t0], %[xo], 17280\n\t"
        "buffer_load_dwordx4 v100, %[sx], %[t0] offen lds\n\t"
        "s_waitcnt lgkmcnt(0)\n\t"
        "s_barrier\n\t"
        "ds_read_b64_tr_b16 a[48:49], v103 offset:0\n\t"
        "ds_read_b64_tr_b16 a[50:51], v103 offset:512\n\t"
        "ds_read_b64_tr_b16 a[52:53], v103 offset:8\n\t"
        "ds_read_b64_tr_b16 a[54:55], v103 offset:520\n\t"
        "ds_read_b64_tr_b16 a[56:57], v104 offset:0\n\t"
        "ds_read_b64_tr_b16 a[58:59], v104 offset:256\n\t"
        "ds_read_b64_tr_b16 a[60:61], v103 offset:4096\n\t"
        "ds_read_b64_tr_b16 a[62:63], v103 offset:4608\n\t"
        "ds_read_b64_tr_b16 a[64:65], v103 offset:4104\n\t"
        "ds_read_b64_tr_b16 a[66:67], v103 offset:4616\n\t"
        "ds_read_b64_tr_b16 a[68:69], v104 offset:2048\n\t"
        "ds_read_b64_tr_b16 a[70:71], v104 offset:2304\n\t"
        "s_waitcnt vmcnt(44)\n\t"
        "ds_write_b128 v102, a[228:231] offset:16384\n\t"
        "ds_write_b128 v102, a[232:235] offset:20480\n\t"
        "ds_write_b128 v102, a[236:239] offset:24576\n\t"
        "s_waitcnt lgkmcnt(0)\n\t"
        "s_barrier\n\t"
        "ds_read_b64_tr_b16 a[72:73], v103 offset:16384\n\t"
        "ds_read_b64_tr_b16 a[74:75], v103 offset:16896\n\t"
        "ds_read_b64_tr_b16 a[76:77], v103 offset:16392\n\t"
        "ds_read_b64_tr_b16 a[78:79], v103 offset:16904\n\t"
        "ds_read_b64_tr_b16 a[80:81], v104 offset:16384\n\t"
        "ds_read_b64_tr_b16 a[82:83], v104 offset:16640\n\t"
        "ds_read_b64_tr_b16 a[84:85], v103 offset:20480\n\t"
        "ds_read_b64_tr_b16 a[86:87], v103 offset:20992\n\t"
        "ds_read_b64_tr_b16 a[88:89], v103 offset:20488\n\t"
        "ds_read_b64_tr_b16 a[90:91], v103 offset:21000\n\t"
        "ds_read_b64_tr_b16 a[92:93], v104 offset:18432\n\t"
        "ds_read_b64_tr_b16 a[94:95], v104 offset:18688\n\t"
        "s_waitcnt vmcnt(41)\n\t"
        "ds_write_b128 v102, a[240:243] offset:0\n\t"
        "ds_write_b128 v102, a[244:247] offset:4096\n\t"
        "ds_write_b128 v102, a[248:251] offset:8192\n\t"
        "s_waitcnt lgkmcnt(0)\n\t"
        "s_barrier\n\t"
        "ds_read_b64_tr_b16 a[96:97], v103 offset:0\n\t"
        "ds_read_b64_tr_b16 a[98:99], v103 offset:512\n\t"
        "ds_read_b64_tr_b16 a[100:101], v103 offset:8\n\t"
        "ds_read_b64_tr_b16 a[102:103], v103 offset:520\n\t"
        "ds_read_b64_tr_b16 a[104:105], v104 offset:0\n\t"
        "ds_read_b64_tr_b16 a[106:107], v104 offset:256\n\t"
        "ds_read_b64_tr_b16 a[108:109], v103 offset:4096\n\t"
        "ds_read_b64_tr_b16 a[110:111], v103 offset:4608\n\t"
        "ds_read_b64_tr_b16 a[112:113], v103 offset:4104\n\t"
        "ds_read_b64_tr_b16 a[114:115], v103 offset:4616\n\t"
        "ds_read_b64_tr_b16 a[116:117], v104 offset:2048\n\t"
        "ds_read_b64_tr_b16 a[118:119], v104 offset:2304\n\t"
        "s_waitcnt vmcnt(38)\n\t"
        "ds_write_b128 v102, a[252:255] offset:16384\n\t"
        "ds_write_b128 v102, v[128:131] offset:20480\n\t"
        "ds_write_b128 v102, v[132:135] offset:24576\n\t"
        "s_add_u32 m0, %[ld], 65536\n\t"
        "s_add_u32 %[t0], %[xo], 1024\n\t"
        "buffer_load_dwordx4 v100, %[sx], %[t0] offen lds\n\t"
        "s_add_u32 m0, %[ld], 66560\n\t"
        "s_add_u32 %[t0], %[xo], 17408\n\t"
        "buffer_load_dwordx4 v100, %[sx], %[t0] offen lds\n\t"
        "s_add_u32 m0, %[ld], 73728\n\t"
        "s_add_u32 %[t0], %[xo], 1152\n\t"
        "buffer_load_dwordx4 v100, %[sx], %[t0] offen lds\n\t"
        "s_add_u32 m0, %[ld], 74752\n\t"
        "s_add_u32 %[t0], %[xo], 17536\n\t"
        "buffer_load_dwordx4 v100, %[sx], %[t0] offen lds\n\t"
        "s_add_u32 m0, %[ld], 81920\n\t"
        "s_add_u32 %[t0], %[xo], 1280\n\t"
        "buffer_load_dwordx4 v100, %[sx], %[t0] offen lds\n\t"
        "s_add_u32 m0, %[ld], 82944\n\t"
        "s_add_u32 %[t0], %[xo], 17664\n\t"
        "buffer_load_dwordx4 v100, %[sx], %[t0] offen lds\n\t"
        "s_add_u32 m0, %[ld], 90112\n\t"
        "s_add_u32 %[t0], %[xo], 1408\n\t"
        "buffer_load_dwordx4 v100, %[sx], %[t0] offen lds\n\t"
        "s_add_u32 m0, %[ld], 91136\n\t"
        "s_add_u32 %[t0], %[xo], 17792\n\t"
        "buffer_load_dwordx4 v100, %[sx], %[t0] offen lds\n\t"
        "s_waitcnt lgkmcnt(0)\n\t"
        "s_barrier\n\t"
        "ds_read_b64_tr_b16 a[120:121], v103 offset:16384\n\t"
        "ds_read_b64_tr_b16 a[122:123], v103 offset:16896\n\t"
        "ds_read_b64_tr_b16 a[124:125], v103 offset:16392\n\t"
        "ds_read_b64_tr_b16 a[126:127], v103 offset:16904\n\t"
        "ds_read_b64_tr_b16 a[128:129], v104 offset:16384\n\t"
        "ds_read_b64_tr_b16 a[130:131], v104 offset:16640\n\t"
        "ds_read_b64_tr_b16 a[132:133], v103 offset:20480\n\t"
        "ds_read_b64_tr_b16 a[134:135], v103 offset:20992\n\t"
        "ds_read_b64_tr_b16 a[136:137], v103 offset:20488\n\t"
        "ds_read_b64_tr_b16 a[138:139], v103 offset:21000\n\t"
        "ds_read_b64_tr_b16 a[140:141], v104 offset:18432\n\t"
        "ds_read_b64_tr_b16 a[142:143], v104 offset:18688\n\t"
        "s_waitcnt vmcnt(43)\n\t"
        "ds_write_b128 v102, v[136:139] offset:0\n\t"
        "ds_write_b128 v102, v[140:143] offset:4096\n\t"
        "ds_write_b128 v102, v[144:147] offset:8192\n\t"
        "s_waitcnt lgkmcnt(0)\n\t"
        "s_barrier\n\t"
        "ds_read_b64_tr_b16 a[144:145], v103 offset:0\n\t"
        "ds_read_b64_tr_b16 a[146:147], v103 offset:512\n\t"
        "ds_read_b64_tr_b16 a[148:149], v103 offset:8\n\t"
        "ds_read_b64_tr_b16 a[150:151], v103 offset:520\n\t"
        "ds_read_b64_tr_b16 a[152:153], v104 offset:0\n\t"
        "ds_read_b64_tr_b16 a[154:155], v104 offset:256\n\t"
        "ds_read_b64_tr_b16 a[156:157], v103 offset:4096\n\t"
        "ds_read_b64_tr_b16 a[158:159], v103 offset:4608\n\t"
        "ds_read_b64_tr_b16 a[160:161], v103 offset:4104\n\t"
        "ds_read_b64_tr_b16 a[162:163], v103 offset:4616\n\t"
        "ds_read_b64_tr_b16 a[164:165], v104 offset:2048\n\t"
        "ds_read_b64_tr_b16 a[166:167], v104 offset:2304\n\t"
        "s_waitcnt vmcnt(40)\n\t"
        "ds_write_b128 v102, v[148:151] offset:16384\n\t"
        "ds_write_b128 v102, v[152:155] offset:20480\n\t"
        "ds_write_b128 v102, v[156:159] offset:24576\n\t"
        "s_waitcnt lgkmcnt(0)\n\t"
        "s_barrier\n\t"
        "ds_read_b64_tr_b16 a[168:169], v103 offset:16384\n\t"
        "ds_read_b64_tr_b16 a[170:171], v103 offset:16896\n\t"
        "ds_read_b64_tr_b16 a[172:173], v103 offset:16392\n\t"
        "ds_read_b64_tr_b16 a[174:175], v103 offset:16904\n\t"
        "ds_read_b64_tr_b16 a[176:177], v104 offset:16384\n\t"
        "ds_read_b64_tr_b16 a[178:179], v104 offset:16640\n\t"
        "ds_read_b64_tr_b16 a[180:181], v103 offset:20480\n\t"
        "ds_read_b64_tr_b16 a[182:183], v103 offset:20992\n\t"
        "ds_read_b64_tr_b16 a[184:185], v103 offset:20488\n\t"
        "ds_read_b64_tr_b16 a[186:187], v103 offset:21000\n\t"
        "ds_read_b64_tr_b16 a[188:189], v104 offset:18432\n\t"
        "ds_read_b64_tr_b16 a[190:191], v104 offset:18688\n\t"
        "s_waitcnt vmcnt(37)\n\t"
        "ds_write_b128 v102, v[160:163] offset:0\n\t"
        "ds_write_b128 v102, v[164:167] offset:4096\n\t"
        "ds_write_b128 v102, v[168:171] offset:8192\n\t"
        "s_add_u32 m0, %[ld], 98304\n\t"
        "s_add_u32 %[t0], %[xo], 1536\n\t"
        "buffer_load_dwordx4 v100, %[sx], %[t0] offen lds\n\t"
        "s_add_u32 m0, %[ld], 99328\n\t"
        "s_add_u32 %[t0], %[xo], 17920\n\t"
        "buffer_load_dwordx4 v100, %[sx], %[t0] offen lds\n\t"
        "s_add_u32 m0, %[ld], 106496\n\t"
        "s_add_u32 %[t0], %[xo], 1664\n\t"
        "buffer_load_dwordx4 v100, %[sx], %[t0] offen lds\n\t"
        "s_add_u32 m0, %[ld], 107520\n\t"
        "s_add_u32 %[t0], %[xo], 18048\n\t"
        "buffer_load_dwordx4 v100, %[sx], %[t0] offen lds\n\t"
        "s_add_u32 m0, %[ld], 114688\n\t"
        "s_add_u32 %[t0], %[xo], 1792\n\t"
        "buffer_load_dwordx4 v100, %[sx], %[t0] offen lds\n\t"
        "s_add_u32 m0, %[ld], 115712\n\t"
        "s_add_u32 %[t0], %[xo], 18176\n\t"
        "buffer_load_dwordx4 v100, %[sx], %[t0] offen lds\n\t"
        "s_add_u32 m0, %[ld], 122880\n\t"
        "s_add_u32 %[t0], %[xo], 1920\n\t"
        "buffer_load_dwordx4 v100, %[sx], %[t0] offen lds\n\t"
        "s_add_u32 m0, %[ld], 123904\n\t"
        "s_add_u32 %[t0], %[xo], 18304\n\t"
        "buffer_load_dwordx4 v100, %[sx], %[t0] offen lds\n\t"
        "s_waitcnt lgkmcnt(0)\n\t"
        "s_barrier\n\t"
        "ds_read_b64_tr_b16 a[192:193], v103 offset:0\n\t"
        "ds_read_b64_tr_b16 a[194:195], v103 offset:512\n\t"
        "ds_read_b64_tr_b16 a[196:197], v103 offset:8\n\t"
        "ds_read_b64_tr_b16 a[198:199], v103 offset:520\n\t"
        "ds_read_b64_tr_b16 a[200:201], v104 offset:0\n\t"
        "ds_read_b64_tr_b16 a[202:203], v104 offset:256\n\t"
        "ds_read_b64_tr_b16 a[204:205], v103 offset:4096\n\t"
        "ds_read_b64_tr_b16 a[206:207], v103 offset:4608\n\t"
        "ds_read_b64_tr_b16 a[208:209], v103 offset:4104\n\t"
        "ds_read_b64_tr_b16 a[210:211], v103 offset:4616\n\t"
        "ds_read_b64_tr_b16 a[212:213], v104 offset:2048\n\t"
        "ds_read_b64_tr_b16 a[214:215], v104 offset:2304\n\t"
        "s_waitcnt vmcnt(42)\n\t"
        "ds_write_b128 v102, v[172:175] offset:16384\n\t"
        "ds_write_b128 v102, v[176:179] offset:20480\n\t"
        "ds_write_b128 v102, v[180:183] offset:24576\n\t"
        "s_waitcnt lgkmcnt(0)\n\t"
        "s_barrier\n\t"
        "ds_read_b64_tr_b16 a[216:217], v103 offset:16384\n\t"
        "ds_read_b64_tr_b16 a[218:219], v103 offset:16896\n\t"
        "ds_read_b64_tr_b16 a[220:221], v103 offset:16392\n\t"
        "ds_read_b64_tr_b16 a[222:223], v103 offset:16904\n\t"
        "ds_read_b64_tr_b16 a[224:225], v104 offset:16384\n\t"
        "ds_read_b64_tr_b16 a[226:227], v104 offset:16640\n\t"
        "ds_read_b64_tr_b16 a[228:229], v103 offset:20480\n\t"
        "ds_read_b64_tr_b16 a[230:231], v103 offset:20992\n\t"
        "ds_read_b64_tr_b16 a[232:233], v103 offset:20488\n\t"
        "ds_read_b64_tr_b16 a[234:235], v103 offset:21000\n\t"
        "ds_read_b64_tr_b16 a[236:237], v104 offset:18432\n\t"
        "ds_read_b64_tr_b16 a[238:239], v104 offset:18688\n\t"
        "s_waitcnt vmcnt(39)\n\t"
        "ds_write_b128 v102, v[184:187] offset:0\n\t"
        "ds_write_b128 v102, v[188:191] offset:4096\n\t"
        "ds_write_b128 v102, v[192:195] offset:8192\n\t"
        "s_waitcnt lgkmcnt(0)\n\t"
        "s_barrier\n\t"
        "ds_read_b64_tr_b16 a[240:241], v103 offset:0\n\t"
        "ds_read_b64_tr_b16 a[242:243], v103 offset:512\n\t"
        "ds_read_b64_tr_b16 a[244:245], v103 offset:8\n\t"
        "ds_read_b64_tr_b16 a[246:247], v103 offset:520\n\t"
        "ds_read_b64_tr_b16 a[248:249], v104 offset:0\n\t"
        "ds_read_b64_tr_b16 a[250:251], v104 offset:256\n\t"
        "ds_read_b64_tr_b16 a[252:253], v103 offset:4096\n\t"
        "ds_read_b64_tr_b16 a[254:255], v103 offset:4608\n\t"
        "ds_read_b64_tr_b16 v[128:129], v103 offset:4104\n\t"
        "ds_read_b64_tr_b16 v[130:131], v103 offset:4616\n\t"
        "ds_read_b64_tr_b16 v[132:133], v104 offset:2048\n\t"
        "ds_read_b64_tr_b16 v[134:135], v104 offset:2304\n\t"
        "s_waitcnt vmcnt(36)\n\t"
        "ds_write_b128 v102, v[196:199] offset:16384\n\t"
        "ds_write_b128 v102, v[200:203] offset:20480\n\t"
        "ds_write_b128 v102, v[204:207] offset:24576\n\t"
        "s_waitcnt lgkmcnt(0)\n\t"
        "s_barrier\n\t"
        "ds_read_b64_tr_b16 v[136:137], v103 offset:16384\n\t"
        "ds_read_b64_tr_b16 v[138:139], v103 offset:16896\n\t"
        "ds_read_b64_tr_b16 v[140:141], v103 offset:16392\n\t"
        "ds_read_b64_tr_b16 v[142:143], v103 offset:16904\n\t"
        "ds_read_b64_tr_b16 v[144:145], v104 offset:16384\n\t"
        "ds_read_b64_tr_b16 v[146:147], v104 offset:16640\n\t"
        "ds_read_b64_tr_b16 v[148:149], v103 offset:20480\n\t"
        "ds_read_b64_tr_b16 v[150:151], v103 offset:20992\n\t"
        "ds_read_b64_tr_b16 v[152:153], v103 offset:20488\n\t"
        "ds_read_b64_tr_b16 v[154:155], v103 offset:21000\n\t"
        "ds_read_b64_tr_b16 v[156:157], v104 offset:18432\n\t"
        "ds_read_b64_tr_b16 v[158:159], v104 offset:18688\n\t"
        "s_waitcnt vmcnt(33)\n\t"
        "ds_write_b128 v102, v[208:211] offset:0\n\t"
        "ds_write_b128 v102, v[212:215] offset:4096\n\t"
        "ds_write_b128 v102, v[216:219] offset:8192\n\t"
        "s_waitcnt lgkmcnt(0)\n\t"
        "s_barrier\n\t"
        "ds_read_b64_tr_b16 v[160:161], v103 offset:0\n\t"
        "ds_read_b64_tr_b16 v[162:163], v103 offset:512\n\t"
        "ds_read_b64_tr_b16 v[164:165], v103 offset:8\n\t"
        "ds_read_b64_tr_b16 v[166:167], v103 offset:520\n\t"
        "ds_read_b64_tr_b16 v[168:169], v104 offset:0\n\t"
        "ds_read_b64_tr_b16 v[170:171], v104 offset:256\n\t"
        "ds_read_b64_tr_b16 v[172:173], v103 offset:4096\n\t"
        "ds_read_b64_tr_b16 v[174:175], v103 offset:4608\n\t"
        "ds_read_b64_tr_b16 v[176:177], v103 offset:4104\n\t"
        "ds_read_b64_tr_b16 v[178:179], v103 offset:4616\n\t"
        "ds_read_b64_tr_b16 v[180:181], v104 offset:2048\n\t"
        "ds_read_b64_tr_b16 v[182:183], v104 offset:2304\n\t"
        "s_waitcnt vmcnt(30)\n\t"
        "ds_write_b128 v102, v[220:223] offset:16384\n\t"
        "ds_write_b128 v102, v[224:227] offset:20480\n\t"
        "ds_write_b128 v102, v[228:231] offset:24576\n\t"
        "s_waitcnt lgkmcnt(0)\n\t"
        "s_barrier\n\t"
        "ds_read_b64_tr_b16 v[184:185], v103 offset:16384\n\t"
        "ds_read_b64_tr_b16 v[186:187], v103 offset:16896\n\t"
        "ds_read_b64_tr_b16 v[188:189], v103 offset:16392\n\t"
        "ds_read_b64_tr_b16 v[190:191], v103 offset:16904\n\t"
        "ds_read_b64_tr_b16 v[192:193], v104 offset:16384\n\t"
        "ds_read_b64_tr_b16 v[194:195], v104 offset:16640\n\t"
        "ds_read_b64_tr_b16 v[196:197], v103 offset:20480\n\t"
        "ds_read_b64_tr_b16 v[198:199], v103 offset:20992\n\t"
        "ds_read_b64_tr_b16 v[200:201], v103 offset:20488\n\t"
        "ds_read_b64_tr_b16 v[202:203], v103 offset:21000\n\t"
        "ds_read_b64_tr_b16 v[204:205], v104 offset:18432\n\t"
        "ds_read_b64_tr_b16 v[206:207], v104 offset:18688\n\t"
        "s_waitcnt vmcnt(27)\n\t"
        "ds_write_b128 v102, v[232:235] offset:0\n\t"
        "ds_write_b128 v102, v[236:239] offset:4096\n\t"
        "ds_write_b128 v102, v[240:243] offset:8192\n\t"
        "s_waitcnt lgkmcnt(0)\n\t"
        "s_barrier\n\t"
        "ds_read_b64_tr_b16 v[208:209], v103 offset:0\n\t"
        "ds_read_b64_tr_b16 v[210:211], v103 offset:512\n\t"
        "ds_read_b64_tr_b16 v[212:213], v103 offset:8\n\t"
        "ds_read_b64_tr_b16 v[214:215], v103 offset:520\n\t"
        "ds_read_b64_tr_b16 v[216:217], v104 offset:0\n\t"
        "ds_read_b64_tr_b16 v[218:219], v104 offset:256\n\t"
        "ds_read_b64_tr_b16 v[220:221], v103 offset:4096\n\t"
        "ds_read_b64_tr_b16 v[222:223], v103 offset:4608\n\t"
        "ds_read_b64_tr_b16 v[224:225], v103 offset:4104\n\t"
        "ds_read_b64_tr_b16 v[226:227], v103 offset:4616\n\t"
        "ds_read_b64_tr_b16 v[228:229], v104 offset:2048\n\t"
        "ds_read_b64_tr_b16 v[230:231], v104 offset:2304\n\t"
        "s_waitcnt vmcnt(24)\n\t"
        "ds_write_b128 v102, v[244:247] offset:16384\n\t"
        "ds_write_b128 v102, v[248:251] offset:20480\n\t"
        "ds_write_b128 v102, v[252:255] offset:24576\n\t"
        "s_waitcnt lgkmcnt(0)\n\t"
        "s_barrier\n\t"
        "ds_read_b64_tr_b16 v[232:233], v103 offset:16384\n\t"
        "ds_read_b64_tr_b16 v[234:235], v103 offset:16896\n\t"
        "ds_read_b64_tr_b16 v[236:237], v103 offset:16392\n\t"
        "ds_read_b64_tr_b16 v[238:239], v103 offset:16904\n\t"
        "ds_read_b64_tr_b16 v[240:241], v104 offset:16384\n\t"
        "ds_read_b64_tr_b16 v[242:243], v104 offset:16640\n\t"
        "ds_read_b64_tr_b16 v[244:245], v103 offset:20480\n\t"
        "ds_read_b64_tr_b16 v[246:247], v103 offset:20992\n\t"
        "ds_read_b64_tr_b16 v[248:249], v103 offset:20488\n\t"
        "ds_read_b64_tr_b16 v[250:251], v103 offset:21000\n\t"
        "ds_read_b64_tr_b16 v[252:253], v104 offset:18432\n\t"
        "ds_read_b64_tr_b16 v[254:255], v104 offset:18688\n\t"
        "s_waitcnt vmcnt(0) lgkmcnt(0)\n\t"
        "s_barrier\n\t"
        "ds_read_b128 v[56:59], v105 offset:0\n\t"
        "ds_read_b128 v[60:63], v105 offset:2048\n\t"
        "ds_read_b128 v[64:67], v106 offset:0\n\t"
        "ds_read_b128 v[68:71], v106 offset:2048\n\t"
        "ds_read_b128 v[72:75], v105 offset:8192\n\t"
        "ds_read_b128 v[76:79], v105 offset:10240\n\t"
        "ds_read_b128 v[80:83], v106 offset:8192\n\t"
        "LG_loop%=:\n\t"
        "s_waitcnt lgkmcnt(6)\n\t"
        "v_mfma_f32_16x16x32_bf16 v[8:11], a[0:3], v[56:59], 0\n\t"
        "v_mfma_f32_16x16x32_bf16 v[12:15], a[4:7], v[56:59], 0\n\t"
        "ds_read_b128 v[84:87], v106 offset:10240\n\t"
        "v_mfma_f32_16x16x32_bf16 v[16:19], v[56:59], a[8:11], 0\n\t"
        "s_add_u32 m0, %[ld], 107520\n\t"
        "s_add_u32 %[t0], %[xo], 18048\n\t"
        "s_waitcnt lgkmcnt(6)\n\t"
        "v_mfma_f32_16x16x32_bf16 v[20:23], a[0:3], v[60:63], 0\n\t"
        "buffer_load_dwordx4 v100, %[sx], %[t0] offen lds\n\t"
        "v_mfma_f32_16x16x32_bf16 v[24:27], a[4:7], v[60:63], 0\n\t"
        "ds_read_b128 v[56:59], v105 offset:16384\n\t"
        "v_mfma_f32_16x16x32_bf16 v[28:31], v[60:63], a[8:11], 0\n\t"
        "s_waitcnt lgkmcnt(6)\n\t"
        "v_mfma_f32_16x16x32_bf16 v[8:11], a[12:15], v[64:67], v[8:11]\n\t"
        "v_cvt_pk_bf16_f32 v88, v32, v33\n\t"
        "v_cvt_pk_bf16_f32 v89, v34, v35\n\t"
        "v_mfma_f32_16x16x32_bf16 v[12:15], a[16:19], v[64:67], v[12:15]\n\t"
        "ds_read_b128 v[60:63], v105 offset:18432\n\t"
        "v_cvt_pk_bf16_f32 v90, v36, v37\n\t"
        "v_mfma_f32_16x16x32_bf16 v[16:19], v[64:67], a[20:23], v[16:19]\n\t"
        "s_add_u32 m0, %[ld], 114688\n\t"
        "s_add_u32 %[t0], %[xo], 1792\n\t"
        "s_waitcnt lgkmcnt(6)\n\t"
        "v_mfma_f32_16x16x32_bf16 v[20:23], a[12:15], v[68:71], v[20:23]\n\t"
        "buffer_load_dwordx4 v100, %[sx], %[t0] offen lds\n\t"
        "v_mfma_f32_16x16x32_bf16 v[24:27], a[16:19], v[68:71], v[24:27]\n\t"
        "ds_read_b128 v[64:67], v106 offset:16384\n\t"
        "v_cvt_pk_bf16_f32 v91, v38, v39\n\t"
        "v_mfma_f32_16x16x32_bf16 v[28:31], v[68:71], a[20:23], v[28:31]\n\t"
        "buffer_store_dwordx4 v[88:91], v109, %[sw], %[so] offen sc1\n\t"
        "s_waitcnt lgkmcnt(6)\n\t"
        "v_mfma_f32_16x16x32_bf16 v[8:11], a[24:27], v[72:75], v[8:11]\n\t"
        "v_cvt_pk_bf16_f32 v92, v44, v45\n\t"
        "v_cvt_pk_bf16_f32 v93, v46, v47\n\t"
        "v_mfma_f32_16x16x32_bf16 v[12:15], a[28:31], v[72:75], v[12:15]\n\t"
        "ds_read_b128 v[68:71], v106 offset:18432\n\t"
        "v_cvt_pk_bf16_f32 v94, v48, v49\n\t"
        "v_mfma_f32_16x16x32_bf16 v[16:19], v[72:75], a[32:35], v[16:19]\n\t"
        "s_add_u32 m0, %[ld], 115712\n\t"
        "s_add_u32 %[t0], %[xo], 18176\n\t"
        "s_waitcnt lgkmcnt(6)\n\t"
        "v_mfma_f32_16x16x32_bf16 v[20:23], a[24:27], v[76:79], v[20:23]\n\t"
        "buffer_load_dwordx4 v100, %[sx], %[t0] offen lds\n\t"
        "v_mfma_f32_16x16x32_bf16 v[24:27], a[28:31], v[76:79], v[24:27]\n\t"
        "ds_read_b128 v[72:75], v105 offset:24576\n\t"
        "v_cvt_pk_bf16_f32 v95, v50, v51\n\t"
        "v_mfma_f32_16x16x32_bf16 v[28:31], v[76:79], a[32:35], v[28:31]\n\t"
        "buffer_store_dwordx4 v[92:95], v110, %[sw], %[so] offen sc1\n\t"
        "s_waitcnt lgkmcnt(6)\n\t"
        "v_mfma_f32_16x16x32_bf16 v[8:11], a[36:39], v[80:83], v[8:11]\n\t"
        "v_cvt_pk_bf16_f32 v96, v40, v41\n\t"
        "v_cvt_pk_bf16_f32 v97, v42, v43\n\t"
        "v_mfma_f32_16x16x32_bf16 v[12:15], a[40:43], v[80:83], v[12:15]\n\t"
        "ds_read_b128 v[76:79], v105 offset:26624\n\t"
        "v_cvt_pk_bf16_f32 v98, v52, v53\n\t"
        "v_mfma_f32_16x16x32_bf16 v[16:19], v[80:83], a[44:47], v[16:19]\n\t"
        "s_add_u32 m0, %[ld], 122880\n\t"
        "s_add_u32 %[t0], %[xo], 1920\n\t"
        "s_waitcnt lgkmcnt(6)\n\t"
        "v_mfma_f32_16x16x32_bf16 v[20:23], a[36:39], v[84:87], v[20:23]\n\t"
        "buffer_load_dwordx4 v100, %[sx], %[t0] offen lds\n\t"
        "v_mfma_f32_16x16x32_bf16 v[24:27], a[40:43], v[84:87], v[24:27]\n\t"
        "ds_read_b128 v[80:83], v106 offset:24576\n\t"
        "v_cvt_pk_bf16_f32 v99, v54, v55\n\t"
        "v_mfma_f32_16x16x32_bf16 v[28:31], v[84:87], a[44:47], v[28:31]\n\t"
        "buffer_store_dwordx4 v[96:99], v111, %[sw], %[so] offen sc1\n\t"
        "s_waitcnt lgkmcnt(6)\n\t"
        "v_mfma_f32_16x16x32_bf16 v[8:11], a[48:51], v[56:59], v[8:11]\n\t"
        "s_lshl_b32 %[so], %[cn], 19\n\t"
        "v_mfma_f32_16x16x32_bf16 v[12:15], a[52:55], v[56:59], v[12:15]\n\t"
        "ds_read_b128 v[84:87], v106 offset:26624\n\t"
        "v_mfma_f32_16x16x32_bf16 v[16:19], v[56:59], a[56:59], v[16:19]\n\t"
        "s_add_u32 m0, %[ld], 123904\n\t"
        "s_add_u32 %[t0], %[xo], 18304\n\t"
        "s_waitcnt lgkmcnt(6)\n\t"
        "v_mfma_f32_16x16x32_bf16 v[20:23], a[48:51], v[60:63], v[20:23]\n\t"
        "buffer_load_dwordx4 v100, %[sx], %[t0] offen lds\n\t"
        "v_mfma_f32_16x16x32_bf16 v[24:27], a[52:55], v[60:63], v[24:27]\n\t"
        "v_mfma_f32_16x16x32_bf16 v[28:31], v[60:63], a[56:59], v[28:31]\n\t"
        "s_waitcnt lgkmcnt(5)\n\t"
        "v_mfma_f32_16x16x32_bf16 v[8:11], a[60:63], v[64:67], v[8:11]\n\t"
        "s_waitcnt vmcnt(19) lgkmcnt(0)\n\t"
        "s_barrier\n\t"
        "s_add_u32 %[xo], %[xo], 0x20000\n\t"
        "v_mfma_f32_16x16x32_bf16 v[12:15], a[64:67], v[64:67], v[12:15]\n\t"
        "ds_read_b128 v[56:59], v105 offset:32768\n\t"
        "v_mfma_f32_16x16x32_bf16 v[16:19], v[64:67], a[68:71], v[16:19]\n\t"
        "ds_read_b128 v[60:63], v105 offset:34816\n\t"
        "s_add_u32 m0, %[ld], 0\n\t"
        "s_add_u32 %[t0], %[xo], 0\n\t"
        "v_mfma_f32_16x16x32_bf16 v[20:23], a[60:63], v[68:71], v[20:23]\n\t"
        "buffer_load_dwordx4 v100, %[sx], %[t0] offen lds\n\t"
        "v_mfma_f32_16x16x32_bf16 v[24:27], a[64:67], v[68:71], v[24:27]\n\t"
        "ds_read_b128 v[64:67], v106 offset:32768\n\t"
        "v_mfma_f32_16x16x32_bf16 v[28:31], v[68:71], a[68:71], v[28:31]\n\t"
        "v_mfma_f32_16x16x32_bf16 v[8:11], a[72:75], v[72:75], v[8:11]\n\t"
        "v_mfma_f32_16x16x32_bf16 v[12:15], a[76:79], v[72:75], v[12:15]\n\t"
        "ds_read_b128 v[68:71], v106 offset:34816\n\t"
        "v_mfma_f32_16x16x32_bf16 v[16:19], v[72:75], a[80:83], v[16:19]\n\t"
        "s_add_u32 m0, %[ld], 1024\n\t"
        "s_add_u32 %[t0], %[xo], 16384\n\t"
        "v_mfma_f32_16x16x32_bf16 v[20:23], a[72:75], v[76:79], v[20:23]\n\t"
        "buffer_load_dwordx4 v100, %[sx], %[t0] offen lds\n\t"
        "v_mfma_f32_16x16x32_bf16 v[24:27], a[76:79], v[76:79], v[24:27]\n\t"
        "ds_read_b128 v[72:75], v105 offset:40960\n\t"
        "v_mfma_f32_16x16x32_bf16 v[28:31], v[76:79], a[80:83], v[28:31]\n\t"
        "v_mfma_f32_16x16x32_bf16 v[8:11], a[84:87], v[80:83], v[8:11]\n\t"
        "v_mfma_f32_16x16x32_bf16 v[12:15], a[88:91], v[80:83], v[12:15]\n\t"
        "ds_read_b128 v[76:79], v105 offset:43008\n\t"
        "v_mfma_f32_16x16x32_bf16 v[16:19], v[80:83], a[92:95], v[16:19]\n\t"
        "s_add_u32 m0, %[ld], 8192\n\t"
        "s_add_u32 %[t0], %[xo], 128\n\t"
        "v_mfma_f32_16x16x32_bf16 v[20:23], a[84:87], v[84:87], v[20:23]\n\t"
        "buffer_load_dwordx4 v100, %[sx], %[t0] offen lds\n\t"
        "v_mfma_f32_16x16x32_bf16 v[24:27], a[88:91], v[84:87], v[24:27]\n\t"
        "ds_read_b128 v[80:83], v106 offset:40960\n\t"
        "v_mfma_f32_16x16x32_bf16 v[28:31], v[84:87], a[92:95], v[28:31]\n\t"
        "s_waitcnt lgkmcnt(6)\n\t"
        "v_mfma_f32_16x16x32_bf16 v[8:11], a[96:99], v[56:59], v[8:11]\n\t"
        "v_mfma_f32_16x16x32_bf16 v[12:15], a[100:103], v[56:59], v[12:15]\n\t"
        "ds_read_b128 v[84:87], v106 offset:43008\n\t"
        "v_mfma_f32_16x16x32_bf16 v[16:19], v[56:59], a[104:107], v[16:19]\n\t"
        "s_add_u32 m0, %[ld], 9216\n\t"
        "s_add_u32 %[t0], %[xo], 16512\n\t"
        "s_waitcnt lgkmcnt(6)\n\t"
        "v_mfma_f32_16x16x32_bf16 v[20:23], a[96:99], v[60:63], v[20:23]\n\t"
        "buffer_load_dwordx4 v100, %[sx], %[t0] offen lds\n\t"
        "v_mfma_f32_16x16x32_bf16 v[24:27], a[100:103], v[60:63], v[24:27]\n\t"
        "ds_read_b128 v[56:59], v105 offset:49152\n\t"
        "v_mfma_f32_16x16x32_bf16 v[28:31], v[60:63], a[104:107], v[28:31]\n\t"
        "s_waitcnt lgkmcnt(6)\n\t"
        "v_mfma_f32_16x16x32_bf16 v[8:11], a[108:111], v[64:67], v[8:11]\n\t"
        "v_mfma_f32_16x16x32_bf16 v[12:15], a[112:115], v[64:67], v[12:15]\n\t"
        "ds_read_b128 v[60:63], v105 offset:51200\n\t"
        "v_mfma_f32_16x16x32_bf16 v[16:19], v[64:67], a[116:119], v[16:19]\n\t"
        "s_add_u32 m0, %[ld], 16384\n\t"
        "s_add_u32 %[t0], %[xo], 256\n\t"
        "s_waitcnt lgkmcnt(6)\n\t"
        "v_mfma_f32_16x16x32_bf16 v[20:23], a[108:111], v[68:71], v[20:23]\n\t"
        "buffer_load_dwordx4 v100, %[sx], %[t0] offen lds\n\t"
        "v_mfma_f32_16x16x32_bf16 v[24:27], a[112:115], v[68:71], v[24:27]\n\t"
        "ds_read_b128 v[64:67], v106 offset:49152\n\t"
        "v_mfma_f32_16x16x32_bf16 v[28:31], v[68:71], a[116:119], v[28:31]\n\t"
        "s_waitcnt lgkmcnt(6)\n\t"
        "v_mfma_f32_16x16x32_bf16 v[8:11], a[120:123], v[72:75], v[8:11]\n\t"
        "v_mfma_f32_16x16x32_bf16 v[12:15], a[124:127], v[72:75], v[12:15]\n\t"
        "ds_read_b128 v[68:71], v106 offset:51200\n\t"
        "v_mfma_f32_16x16x32_bf16 v[16:19], v[72:75], a[128:131], v[16:19]\n\t"
        "s_add_u32 m0, %[ld], 17408\n\t"
        "s_add_u32 %[t0], %[xo], 16640\n\t"
        "s_waitcnt lgkmcnt(6)\n\t"
        "v_mfma_f32_16x16x32_bf16 v[20:23], a[120:123], v[76:79], v[20:23]\n\t"
        "buffer_load_dwordx4 v100, %[sx], %[t0] offen lds\n\t"
        "v_mfma_f32_16x16x32_bf16 v[24:27], a[124:127], v[76:79], v[24:27]\n\t"
        "ds_read_b128 v[72:75], v105 offset:57344\n\t"
        "v_mfma_f32_16x16x32_bf16 v[28:31], v[76:79], a[128:131], v[28:31]\n\t"
        "s_waitcnt lgkmcnt(6)\n\t"
        "v_mfma_f32_16x16x32_bf16 v[8:11], a[132:135], v[80:83], v[8:11]\n\t"
        "v_mfma_f32_16x16x32_bf16 v[12:15], a[136:139], v[80:83], v[12:15]\n\t"
        "ds_read_b128 v[76:79], v105 offset:59392\n\t"
        "v_mfma_f32_16x16x32_bf16 v[16:19], v[80:83], a[140:143], v[16:19]\n\t"
        "s_add_u32 m0, %[ld], 24576\n\t"
        "s_add_u32 %[t0], %[xo], 384\n\t"
        "s_waitcnt lgkmcnt(6)\n\t"
        "v_mfma_f32_16x16x32_bf16 v[20:23], a[132:135], v[84:87], v[20:23]\n\t"
        "buffer_load_dwordx4 v100, %[sx], %[t0] offen lds\n\t"
        "v_mfma_f32_16x16x32_bf16 v[24:27], a[136:139], v[84:87], v[24:27]\n\t"
        "ds_read_b128 v[80:83], v106 offset:57344\n\t"
        "v_mfma_f32_16x16x32_bf16 v[28:31], v[84:87], a[140:143], v[28:31]\n\t"
        "s_waitcnt lgkmcnt(6)\n\t"
        "v_mfma_f32_16x16x32_bf16 v[8:11], a[144:147], v[56:59], v[8:11]\n\t"
        "v_mfma_f32_16x16x32_bf16 v[12:15], a[148:151], v[56:59], v[12:15]\n\t"
        "ds_read_b128 v[84:87], v106 offset:59392\n\t"
        "v_mfma_f32_16x16x32_bf16 v[16:19], v[56:59], a[152:155], v[16:19]\n\t"
        "s_add_u32 m0, %[ld], 25600\n\t"
        "s_add_u32 %[t0], %[xo], 16768\n\t"
        "s_waitcnt lgkmcnt(6)\n\t"
        "v_mfma_f32_16x16x32_bf16 v[20:23], a[144:147], v[60:63], v[20:23]\n\t"
        "buffer_load_dwordx4 v100, %[sx], %[t0] offen lds\n\t"
        "v_mfma_f32_16x16x32_bf16 v[24:27], a[148:151], v[60:63], v[24:27]\n\t"
        "v_mfma_f32_16x16x32_bf16 v[28:31], v[60:63], a[152:155], v[28:31]\n\t"
        "s_waitcnt lgkmcnt(5)\n\t"
        "v_mfma_f32_16x16x32_bf16 v[8:11], a[156:159], v[64:67], v[8:11]\n\t"
        "s_waitcnt vmcnt(9) lgkmcnt(0)\n\t"
        "s_barrier\n\t"
        "v_mfma_f32_16x16x32_bf16 v[12:15], a[160:163], v[64:67], v[12:15]\n\t"
        "ds_read_b128 v[56:59], v107 offset:0\n\t"
        "v_mfma_f32_16x16x32_bf16 v[16:19], v[64:67], a[164:167], v[16:19]\n\t"
        "ds_read_b128 v[60:63], v107 offset:2048\n\t"
        "s_add_u32 m0, %[ld], 32768\n\t"
        "s_add_u32 %[t0], %[xo], 512\n\t"
        "v_mfma_f32_16x16x32_bf16 v[20:23], a[156:159], v[68:71], v[20:23]\n\t"
        "buffer_load_dwordx4 v100, %[sx], %[t0] offen lds\n\t"
        "v_mfma_f32_16x16x32_bf16 v[24:27], a[160:163], v[68:71], v[24:27]\n\t"
        "ds_read_b128 v[64:67], v108 offset:0\n\t"
        "v_mfma_f32_16x16x32_bf16 v[28:31], v[68:71], a[164:167], v[28:31]\n\t"
        "v_mfma_f32_16x16x32_bf16 v[8:11], a[168:171], v[72:75], v[8:11]\n\t"
        "v_mfma_f32_16x16x32_bf16 v[12:15], a[172:175], v[72:75], v[12:15]\n\t"
        "ds_read_b128 v[68:71], v108 offset:2048\n\t"
        "v_mov_b32_e32 v114, %[cn]\n\t"
        "buffer_store_dword v114, v113, %[sw], 0 offen sc1\n\t"
        "s_add_u32 %[cn], %[cn], 1\n\t"
        "v_mfma_f32_16x16x32_bf16 v[16:19], v[72:75], a[176:179], v[16:19]\n\t"
        "s_add_u32 m0, %[ld], 33792\n\t"
        "s_add_u32 %[t0], %[xo], 16896\n\t"
        "v_mfma_f32_16x16x32_bf16 v[20:23], a[168:171], v[76:79], v[20:23]\n\t"
        "buffer_load_dwordx4 v100, %[sx], %[t0] offen lds\n\t"
        "v_mfma_f32_16x16x32_bf16 v[24:27], a[172:175], v[76:79], v[24:27]\n\t"
        "ds_read_b128 v[72:75], v107 offset:8192\n\t"
        "v_mfma_f32_16x16x32_bf16 v[28:31], v[76:79], a[176:179], v[28:31]\n\t"
        "v_mfma_f32_16x16x32_bf16 v[8:11], a[180:183], v[80:83], v[8:11]\n\t"
        "v_mfma_f32_16x16x32_bf16 v[12:15], a[184:187], v[80:83], v[12:15]\n\t"
        "ds_read_b128 v[76:79], v107 offset:10240\n\t"
        "v_mfma_f32_16x16x32_bf16 v[16:19], v[80:83], a[188:191], v[16:19]\n\t"
        "s_add_u32 m0, %[ld], 40960\n\t"
        "s_add_u32 %[t0], %[xo], 640\n\t"
        "v_mfma_f32_16x16x32_bf16 v[20:23], a[180:183], v[84:87], v[20:23]\n\t"
        "buffer_load_dwordx4 v100, %[sx], %[t0] offen lds\n\t"
        "v_mfma_f32_16x16x32_bf16 v[24:27], a[184:187], v[84:87], v[24:27]\n\t"
        "ds_read_b128 v[80:83], v108 offset:8192\n\t"
        "v_mfma_f32_16x16x32_bf16 v[28:31], v[84:87], a[188:191], v[28:31]\n\t"
        "s_waitcnt lgkmcnt(6)\n\t"
        "v_mfma_f32_16x16x32_bf16 v[8:11], a[192:195], v[56:59], v[8:11]\n\t"
        "v_mfma_f32_16x16x32_bf16 v[12:15], a[196:199], v[56:59], v[12:15]\n\t"
        "ds_read_b128 v[84:87], v108 offset:10240\n\t"
        "v_mfma_f32_16x16x32_bf16 v[16:19], v[56:59], a[200:203], v[16:19]\n\t"
        "s_add_u32 m0, %[ld], 41984\n\t"
        "s_add_u32 %[t0], %[xo], 17024\n\t"
        "s_waitcnt lgkmcnt(6)\n\t"
        "v_mfma_f32_16x16x32_bf16 v[20:23], a[192:195], v[60:63], v[20:23]\n\t"
        "buffer_load_dwordx4 v100, %[sx], %[t0] offen lds\n\t"
        "v_mfma_f32_16x16x32_bf16 v[24:27], a[196:199], v[60:63], v[24:27]\n\t"
        "ds_read_b128 v[56:59], v107 offset:16384\n\t"
        "v_mfma_f32_16x16x32_bf16 v[28:31], v[60:63], a[200:203], v[28:31]\n\t"
        "s_waitcnt lgkmcnt(6)\n\t"
        "v_mfma_f32_16x16x32_bf16 v[8:11], a[204:207], v[64:67], v[8:11]\n\t"
        "v_mfma_f32_16x16x32_bf16 v[12:15], a[208:211], v[64:67], v[12:15]\n\t"
        "ds_read_b128 v[60:63], v107 offset:18432\n\t"
        "v_mfma_f32_16x16x32_bf16 v[16:19], v[64:67], a[212:215], v[16:19]\n\t"
        "s_add_u32 m0, %[ld], 49152\n\t"
        "s_add_u32 %[t0], %[xo], 768\n\t"
        "s_waitcnt lgkmcnt(6)\n\t"
        "v_mfma_f32_16x16x32_bf16 v[20:23], a[204:207], v[68:71], v[20:23]\n\t"
        "buffer_load_dwordx4 v100, %[sx], %[t0] offen lds\n\t"
        "v_mfma_f32_16x16x32_bf16 v[24:27], a[208:211], v[68:71], v[24:27]\n\t"
        "ds_read_b128 v[64:67], v108 offset:16384\n\t"
        "v_mfma_f32_16x16x32_bf16 v[28:31], v[68:71], a[212:215], v[28:31]\n\t"
        "s_waitcnt lgkmcnt(6)\n\t"
        "v_mfma_f32_16x16x32_bf16 v[8:11], a[216:219], v[72:75], v[8:11]\n\t"
        "v_mfma_f32_16x16x32_bf16 v[12:15], a[220:223], v[72:75], v[12:15]\n\t"
        "ds_read_b128 v[68:71], v108 offset:18432\n\t"
        "v_mfma_f32_16x16x32_bf16 v[16:19], v[72:75], a[224:227], v[16:19]\n\t"
        "s_add_u32 m0, %[ld], 50176\n\t"
        "s_add_u32 %[t0], %[xo], 17152\n\t"
        "s_waitcnt lgkmcnt(6)\n\t"
        "v_mfma_f32_16x16x32_bf16 v[20:23], a[216:219], v[76:79], v[20:23]\n\t"
        "buffer_load_dwordx4 v100, %[sx], %[t0] offen lds\n\t"
        "v_mfma_f32_16x16x32_bf16 v[24:27], a[220:223], v[76:79], v[24:27]\n\t"
        "ds_read_b128 v[72:75], v107 offset:24576\n\t"
        "v_mfma_f32_16x16x32_bf16 v[28:31], v[76:79], a[224:227], v[28:31]\n\t"
        "s_waitcnt lgkmcnt(6)\n\t"
        "v_mfma_f32_16x16x32_bf16 v[8:11], a[228:231], v[80:83], v[8:11]\n\t"
        "v_mfma_f32_16x16x32_bf16 v[12:15], a[232:235], v[80:83], v[12:15]\n\t"
        "ds_read_b128 v[76:79], v107 offset:26624\n\t"
        "v_mfma_f32_16x16x32_bf16 v[16:19], v[80:83], a[236:239], v[16:19]\n\t"
        "s_add_u32 m0, %[ld], 57344\n\t"
        "s_add_u32 %[t0], %[xo], 896\n\t"
        "s_waitcnt lgkmcnt(6)\n\t"
        "v_mfma_f32_16x16x32_bf16 v[20:23], a[228:231], v[84:87], v[20:23]\n\t"
        "buffer_load_dwordx4 v100, %[sx], %[t0] offen lds\n\t"
        "v_mfma_f32_16x16x32_bf16 v[24:27], a[232:235], v[84:87], v[24:27]\n\t"
        "ds_read_b128 v[80:83], v108 offset:24576\n\t"
        "v_mfma_f32_16x16x32_bf16 v[28:31], v[84:87], a[236:239], v[28:31]\n\t"
        "s_waitcnt lgkmcnt(6)\n\t"
        "v_mfma_f32_16x16x32_bf16 v[8:11], a[240:243], v[56:59], v[8:11]\n\t"
        "v_mfma_f32_16x16x32_bf16 v[12:15], a[244:247], v[56:59], v[12:15]\n\t"
        "ds_read_b128 v[84:87], v108 offset:26624\n\t"
        "v_mfma_f32_16x16x32_bf16 v[16:19], v[56:59], a[248:251], v[16:19]\n\t"
        "s_add_u32 m0, %[ld], 58368\n\t"
        "s_add_u32 %[t0], %[xo], 17280\n\t"
        "s_waitcnt lgkmcnt(6)\n\t"
        "v_mfma_f32_16x16x32_bf16 v[20:23], a[240:243], v[60:63], v[20:23]\n\t"
        "buffer_load_dwordx4 v100, %[sx], %[t0] offen lds\n\t"
        "v_mfma_f32_16x16x32_bf16 v[24:27], a[244:247], v[60:63], v[24:27]\n\t"
        "v_mfma_f32_16x16x32_bf16 v[28:31], v[60:63], a[248:251], v[28:31]\n\t"
        "s_waitcnt lgkmcnt(5)\n\t"
        "v_mfma_f32_16x16x32_bf16 v[8:11], a[252:255], v[64:67], v[8:11]\n\t"
        "s_waitcnt vmcnt(17) lgkmcnt(0)\n\t"
        "s_barrier\n\t"
        "v_mfma_f32_16x16x32_bf16 v[12:15], v[128:131], v[64:67], v[12:15]\n\t"
        "ds_read_b128 v[56:59], v107 offset:32768\n\t"
        "v_mfma_f32_16x16x32_bf16 v[16:19], v[64:67], v[132:135], v[16:19]\n\t"
        "ds_read_b128 v[60:63], v107 offset:34816\n\t"
        "s_add_u32 m0, %[ld], 65536\n\t"
        "s_add_u32 %[t0], %[xo], 1024\n\t"
        "v_mfma_f32_16x16x32_bf16 v[20:23], a[252:255], v[68:71], v[20:23]\n\t"
        "buffer_load_dwordx4 v100, %[sx], %[t0] offen lds\n\t"
        "v_mfma_f32_16x16x32_bf16 v[24:27], v[128:131], v[68:71], v[24:27]\n\t"
        "ds_read_b128 v[64:67], v108 offset:32768\n\t"
        "v_mfma_f32_16x16x32_bf16 v[28:31], v[68:71], v[132:135], v[28:31]\n\t"
        "v_mfma_f32_16x16x32_bf16 v[8:11], v[136:139], v[72:75], v[8:11]\n\t"
        "v_mfma_f32_16x16x32_bf16 v[12:15], v[140:143], v[72:75], v[12:15]\n\t"
        "ds_read_b128 v[68:71], v108 offset:34816\n\t"
        "v_mfma_f32_16x16x32_bf16 v[16:19], v[72:75], v[144:147], v[16:19]\n\t"
        "s_add_u32 m0, %[ld], 66560\n\t"
        "s_add_u32 %[t0], %[xo], 17408\n\t"
        "v_mfma_f32_16x16x32_bf16 v[20:23], v[136:139], v[76:79], v[20:23]\n\t"
        "buffer_load_dwordx4 v100, %[sx], %[t0] offen lds\n\t"
        "v_mfma_f32_16x16x32_bf16 v[24:27], v[140:143], v[76:79], v[24:27]\n\t"
        "ds_read_b128 v[72:75], v107 offset:40960\n\t"
        "v_mfma_f32_16x16x32_bf16 v[28:31], v[76:79], v[144:147], v[28:31]\n\t"
        "v_mfma_f32_16x16x32_bf16 v[8:11], v[148:151], v[80:83], v[8:11]\n\t"
        "v_mfma_f32_16x16x32_bf16 v[12:15], v[152:155], v[80:83], v[12:15]\n\t"
        "ds_read_b128 v[76:79], v107 offset:43008\n\t"
        "v_mfma_f32_16x16x32_bf16 v[16:19], v[80:83], v[156:159], v[16:19]\n\t"
        "s_add_u32 m0, %[ld], 73728\n\t"
        "s_add_u32 %[t0], %[xo], 1152\n\t"
        "v_mfma_f32_16x16x32_bf16 v[20:23], v[148:151], v[84:87], v[20:23]\n\t"
        "buffer_load_dwordx4 v100, %[sx], %[t0] offen lds\n\t"
        "v_mfma_f32_16x16x32_bf16 v[24:27], v[152:155], v[84:87], v[24:27]\n\t"
        "ds_read_b128 v[80:83], v108 offset:40960\n\t"
        "v_mfma_f32_16x16x32_bf16 v[28:31], v[84:87], v[156:159], v[28:31]\n\t"
        "s_waitcnt lgkmcnt(6)\n\t"
        "v_mfma_f32_16x16x32_bf16 v[8:11], v[160:163], v[56:59], v[8:11]\n\t"
        "v_mfma_f32_16x16x32_bf16 v[12:15], v[164:167], v[56:59], v[12:15]\n\t"
        "ds_read_b128 v[84:87], v108 offset:43008\n\t"
        "v_mfma_f32_16x16x32_bf16 v[16:19], v[56:59], v[168:171], v[16:19]\n\t"
        "s_add_u32 m0, %[ld], 74752\n\t"
        "s_add_u32 %[t0], %[xo], 17536\n\t"
        "s_waitcnt lgkmcnt(6)\n\t"
        "v_mfma_f32_16x16x32_bf16 v[20:23], v[160:163], v[60:63], v[20:23]\n\t"
        "buffer_load_dwordx4 v100, %[sx], %[t0] offen lds\n\t"
        "v_mfma_f32_16x16x32_bf16 v[24:27], v[164:167], v[60:63], v[24:27]\n\t"
        "ds_read_b128 v[56:59], v107 offset:49152\n\t"
        "v_mfma_f32_16x16x32_bf16 v[28:31], v[60:63], v[168:171], v[28:31]\n\t"
        "s_waitcnt lgkmcnt(6)\n\t"
        "v_mfma_f32_16x16x32_bf16 v[8:11], v[172:175], v[64:67], v[8:11]\n\t"
        "v_mfma_f32_16x16x32_bf16 v[12:15], v[176:179], v[64:67], v[12:15]\n\t"
        "ds_read_b128 v[60:63], v107 offset:51200\n\t"
        "v_mfma_f32_16x16x32_bf16 v[16:19], v[64:67], v[180:183], v[16:19]\n\t"
        "s_add_u32 m0, %[ld], 81920\n\t"
        "s_add_u32 %[t0], %[xo], 1280\n\t"
        "s_waitcnt lgkmcnt(6)\n\t"
        "v_mfma_f32_16x16x32_bf16 v[20:23], v[172:175], v[68:71], v[20:23]\n\t"
        "buffer_load_dwordx4 v100, %[sx], %[t0] offen lds\n\t"
        "v_mfma_f32_16x16x32_bf16 v[24:27], v[176:179], v[68:71], v[24:27]\n\t"
        "ds_read_b128 v[64:67], v108 offset:49152\n\t"
        "v_mfma_f32_16x16x32_bf16 v[28:31], v[68:71], v[180:183], v[28:31]\n\t"
        "s_waitcnt lgkmcnt(6)\n\t"
        "v_mfma_f32_16x16x32_bf16 v[8:11], v[184:187], v[72:75], v[8:11]\n\t"
        "v_mfma_f32_16x16x32_bf16 v[12:15], v[188:191], v[72:75], v[12:15]\n\t"
        "ds_read_b128 v[68:71], v108 offset:51200\n\t"
        "v_mfma_f32_16x16x32_bf16 v[16:19], v[72:75], v[192:195], v[16:19]\n\t"
        "s_add_u32 m0, %[ld], 82944\n\t"
        "s_add_u32 %[t0], %[xo], 17664\n\t"
        "s_waitcnt lgkmcnt(6)\n\t"
        "v_mfma_f32_16x16x32_bf16 v[20:23], v[184:187], v[76:79], v[20:23]\n\t"
        "buffer_load_dwordx4 v100, %[sx], %[t0] offen lds\n\t"
        "v_mfma_f32_16x16x32_bf16 v[24:27], v[188:191], v[76:79], v[24:27]\n\t"
        "ds_read_b128 v[72:75], v107 offset:57344\n\t"
        "v_mfma_f32_16x16x32_bf16 v[28:31], v[76:79], v[192:195], v[28:31]\n\t"
        "s_waitcnt lgkmcnt(6)\n\t"
        "v_mfma_f32_16x16x32_bf16 v[8:11], v[196:199], v[80:83], v[8:11]\n\t"
        "v_mfma_f32_16x16x32_bf16 v[12:15], v[200:203], v[80:83], v[12:15]\n\t"
        "ds_read_b128 v[76:79], v107 offset:59392\n\t"
        "v_mfma_f32_16x16x32_bf16 v[16:19], v[80:83], v[204:207], v[16:19]\n\t"
        "s_add_u32 m0, %[ld], 90112\n\t"
        "s_add_u32 %[t0], %[xo], 1408\n\t"
        "s_waitcnt lgkmcnt(6)\n\t"
        "v_mfma_f32_16x16x32_bf16 v[20:23], v[196:199], v[84:87], v[20:23]\n\t"
        "buffer_load_dwordx4 v100, %[sx], %[t0] offen lds\n\t"
        "v_mfma_f32_16x16x32_bf16 v[24:27], v[200:203], v[84:87], v[24:27]\n\t"
        "ds_read_b128 v[80:83], v108 offset:57344\n\t"
        "v_mfma_f32_16x16x32_bf16 v[28:31], v[84:87], v[204:207], v[28:31]\n\t"
        "s_waitcnt lgkmcnt(6)\n\t"
        "v_mfma_f32_16x16x32_bf16 v[8:11], v[208:211], v[56:59], v[8:11]\n\t"
        "v_mfma_f32_16x16x32_bf16 v[12:15], v[212:215], v[56:59], v[12:15]\n\t"
        "ds_read_b128 v[84:87], v108 offset:59392\n\t"
        "v_mfma_f32_16x16x32_bf16 v[16:19], v[56:59], v[216:219], v[16:19]\n\t"
        "s_add_u32 m0, %[ld], 91136\n\t"
        "s_add_u32 %[t0], %[xo], 17792\n\t"
        "s_waitcnt lgkmcnt(6)\n\t"
        "v_mfma_f32_16x16x32_bf16 v[20:23], v[208:211], v[60:63], v[20:23]\n\t"
        "buffer_load_dwordx4 v100, %[sx], %[t0] offen lds\n\t"
        "v_mfma_f32_16x16x32_bf16 v[24:27], v[212:215], v[60:63], v[24:27]\n\t"
        "v_mfma_f32_16x16x32_bf16 v[28:31], v[60:63], v[216:219], v[28:31]\n\t"
        "s_waitcnt lgkmcnt(5)\n\t"
        "v_mfma_f32_16x16x32_bf16 v[8:11], v[220:223], v[64:67], v[8:11]\n\t"
        "s_waitcnt vmcnt(17) lgkmcnt(0)\n\t"
        "s_barrier\n\t"
        "v_mfma_f32_16x16x32_bf16 v[12:15], v[224:227], v[64:67], v[12:15]\n\t"
        "ds_read_b128 v[56:59], v105 offset:0\n\t"
        "v_mfma_f32_16x16x32_bf16 v[16:19], v[64:67], v[228:231], v[16:19]\n\t"
        "ds_read_b128 v[60:63], v105 offset:2048\n\t"
        "s_add_u32 m0, %[ld], 98304\n\t"
        "s_add_u32 %[t0], %[xo], 1536\n\t"
        "v_mfma_f32_16x16x32_bf16 v[20:23], v[220:223], v[68:71], v[20:23]\n\t"
        "buffer_load_dwordx4 v100, %[sx], %[t0] offen lds\n\t"
        "v_mfma_f32_16x16x32_bf16 v[24:27], v[224:227], v[68:71], v[24:27]\n\t"
        "ds_read_b128 v[64:67], v106 offset:0\n\t"
        "v_mfma_f32_16x16x32_bf16 v[28:31], v[68:71], v[228:231], v[28:31]\n\t"
        "v_mfma_f32_16x16x32_bf16 v[8:11], v[232:235], v[72:75], v[8:11]\n\t"
        "v_mfma_f32_16x16x32_bf16 v[12:15], v[236:239], v[72:75], v[12:15]\n\t"
        "ds_read_b128 v[68:71], v106 offset:2048\n\t"
        "v_mfma_f32_16x16x32_bf16 v[16:19], v[72:75], v[240:243], v[16:19]\n\t"
        "s_add_u32 m0, %[ld], 99328\n\t"
        "s_add_u32 %[t0], %[xo], 17920\n\t"
        "v_mfma_f32_16x16x32_bf16 v[20:23], v[232:235], v[76:79], v[20:23]\n\t"
        "buffer_load_dwordx4 v100, %[sx], %[t0] offen lds\n\t"
        "v_mfma_f32_16x16x32_bf16 v[24:27], v[236:239], v[76:79], v[24:27]\n\t"
        "ds_read_b128 v[72:75], v105 offset:8192\n\t"
        "v_mfma_f32_16x16x32_bf16 v[28:31], v[76:79], v[240:243], v[28:31]\n\t"
        "v_mfma_f32_16x16x32_bf16 v[8:11], v[244:247], v[80:83], v[8:11]\n\t"
        "v_mfma_f32_16x16x32_bf16 v[12:15], v[248:251], v[80:83], v[12:15]\n\t"
        "ds_read_b128 v[76:79], v105 offset:10240\n\t"
        "v_mfma_f32_16x16x32_bf16 v[16:19], v[80:83], v[252:255], v[16:19]\n\t"
        "s_add_u32 m0, %[ld], 106496\n\t"
        "s_add_u32 %[t0], %[xo], 1664\n\t"
        "v_mfma_f32_16x16x32_bf16 v[20:23], v[244:247], v[84:87], v[20:23]\n\t"
        "buffer_load_dwordx4 v100, %[sx], %[t0] offen lds\n\t"
        "v_mfma_f32_16x16x32_bf16 v[24:27], v[248:251], v[84:87], v[24:27]\n\t"
        "ds_read_b128 v[80:83], v106 offset:8192\n\t"
        "v_mfma_f32_16x16x32_bf16 v[28:31], v[84:87], v[252:255], v[28:31]\n\t"
        "s_waitcnt lgkmcnt(6)\n\t"
        "v_mfma_f32_16x16x32_bf16 v[32:35], a[0:3], v[56:59], 0\n\t"
        "v_mfma_f32_16x16x32_bf16 v[36:39], a[4:7], v[56:59], 0\n\t"
        "ds_read_b128 v[84:87], v106 offset:10240\n\t"
        "v_mfma_f32_16x16x32_bf16 v[40:43], v[56:59], a[8:11], 0\n\t"
        "s_add_u32 m0, %[ld], 107520\n\t"
        "s_add_u32 %[t0], %[xo], 18048\n\t"
        "s_waitcnt lgkmcnt(6)\n\t"
        "v_mfma_f32_16x16x32_bf16 v[44:47], a[0:3], v[60:63], 0\n\t"
        "buffer_load_dwordx4 v100, %[sx], %[t0] offen lds\n\t"
        "v_mfma_f32_16x16x32_bf16 v[48:51], a[4:7], v[60:63], 0\n\t"
        "ds_read_b128 v[56:59], v105 offset:16384\n\t"
        "v_mfma_f32_16x16x32_bf16 v[52:55], v[60:63], a[8:11], 0\n\t"
        "s_waitcnt lgkmcnt(6)\n\t"
        "v_mfma_f32_16x16x32_bf16 v[32:35], a[12:15], v[64:67], v[32:35]\n\t"
        "v_cvt_pk_bf16_f32 v88, v8, v9\n\t"
        "v_cvt_pk_bf16_f32 v89, v10, v11\n\t"
        "v_mfma_f32_16x16x32_bf16 v[36:39], a[16:19], v[64:67], v[36:39]\n\t"
        "ds_read_b128 v[60:63], v105 offset:18432\n\t"
        "v_cvt_pk_bf16_f32 v90, v12, v13\n\t"
        "v_mfma_f32_16x16x32_bf16 v[40:43], v[64:67], a[20:23], v[40:43]\n\t"
        "s_add_u32 m0, %[ld], 114688\n\t"
        "s_add_u32 %[t0], %[xo], 1792\n\t"
        "s_waitcnt lgkmcnt(6)\n\t"
        "v_mfma_f32_16x16x32_bf16 v[44:47], a[12:15], v[68:71], v[44:47]\n\t"
        "buffer_load_dwordx4 v100, %[sx], %[t0] offen lds\n\t"
        "v_mfma_f32_16x16x32_bf16 v[48:51], a[16:19], v[68:71], v[48:51]\n\t"
        "ds_read_b128 v[64:67], v106 offset:16384\n\t"
        "v_cvt_pk_bf16_f32 v91, v14, v15\n\t"
        "v_mfma_f32_16x16x32_bf16 v[52:55], v[68:71], a[20:23], v[52:55]\n\t"
        "buffer_store_dwordx4 v[88:91], v109, %[sw], %[so] offen sc1\n\t"
        "s_waitcnt lgkmcnt(6)\n\t"
        "v_mfma_f32_16x16x32_bf16 v[32:35], a[24:27], v[72:75], v[32:35]\n\t"
        "v_cvt_pk_bf16_f32 v92, v20, v21\n\t"
        "v_cvt_pk_bf16_f32 v93, v22, v23\n\t"
        "v_mfma_f32_16x16x32_bf16 v[36:39], a[28:31], v[72:75], v[36:39]\n\t"
        "ds_read_b128 v[68:71], v106 offset:18432\n\t"
        "v_cvt_pk_bf16_f32 v94, v24, v25\n\t"
        "v_mfma_f32_16x16x32_bf16 v[40:43], v[72:75], a[32:35], v[40:43]\n\t"
        "s_add_u32 m0, %[ld], 115712\n\t"
        "s_add_u32 %[t0], %[xo], 18176\n\t"
        "s_waitcnt lgkmcnt(6)\n\t"
        "v_mfma_f32_16x16x32_bf16 v[44:47], a[24:27], v[76:79], v[44:47]\n\t"
        "buffer_load_dwordx4 v100, %[sx], %[t0] offen lds\n\t"
        "v_mfma_f32_16x16x32_bf16 v[48:51], a[28:31], v[76:79], v[48:51]\n\t"
        "ds_read_b128 v[72:75], v105 offset:24576\n\t"
        "v_cvt_pk_bf16_f32 v95, v26, v27\n\t"
        "v_mfma_f32_16x16x32_bf16 v[52:55], v[76:79], a[32:35], v[52:55]\n\t"
        "buffer_store_dwordx4 v[92:95], v110, %[sw], %[so] offen sc1\n\t"
        "s_waitcnt lgkmcnt(6)\n\t"
        "v_mfma_f32_16x16x32_bf16 v[32:35], a[36:39], v[80:83], v[32:35]\n\t"
        "v_cvt_pk_bf16_f32 v96, v16, v17\n\t"
        "v_cvt_pk_bf16_f32 v97, v18, v19\n\t"
        "v_mfma_f32_16x16x32_bf16 v[36:39], a[40:43], v[80:83], v[36:39]\n\t"
        "ds_read_b128 v[76:79], v105 offset:26624\n\t"
        "v_cvt_pk_bf16_f32 v98, v28, v29\n\t"
        "v_mfma_f32_16x16x32_bf16 v[40:43], v[80:83], a[44:47], v[40:43]\n\t"
        "s_add_u32 m0, %[ld], 122880\n\t"
        "s_add_u32 %[t0], %[xo], 1920\n\t"
        "s_waitcnt lgkmcnt(6)\n\t"
        "v_mfma_f32_16x16x32_bf16 v[44:47], a[36:39], v[84:87], v[44:47]\n\t"
        "buffer_load_dwordx4 v100, %[sx], %[t0] offen lds\n\t"
        "v_mfma_f32_16x16x32_bf16 v[48:51], a[40:43], v[84:87], v[48:51]\n\t"
        "ds_read_b128 v[80:83], v106 offset:24576\n\t"
        "v_cvt_pk_bf16_f32 v99, v30, v31\n\t"
        "v_mfma_f32_16x16x32_bf16 v[52:55], v[84:87], a[44:47], v[52:55]\n\t"
        "buffer_store_dwordx4 v[96:99], v111, %[sw], %[so] offen sc1\n\t"
        "s_waitcnt lgkmcnt(6)\n\t"
        "v_mfma_f32_16x16x32_bf16 v[32:35], a[48:51], v[56:59], v[32:35]\n\t"
        "s_lshl_b32 %[so], %[cn], 19\n\t"
        "v_mfma_f32_16x16x32_bf16 v[36:39], a[52:55], v[56:59], v[36:39]\n\t"
        "ds_read_b128 v[84:87], v106 offset:26624\n\t"
        "v_mfma_f32_16x16x32_bf16 v[40:43], v[56:59], a[56:59], v[40:43]\n\t"
        "s_add_u32 m0, %[ld], 123904\n\t"
        "s_add_u32 %[t0], %[xo], 18304\n\t"
        "s_waitcnt lgkmcnt(6)\n\t"
        "v_mfma_f32_16x16x32_bf16 v[44:47], a[48:51], v[60:63], v[44:47]\n\t"
        "buffer_load_dwordx4 v100, %[sx], %[t0] offen lds\n\t"
        "v_mfma_f32_16x16x32_bf16 v[48:51], a[52:55], v[60:63], v[48:51]\n\t"
        "v_mfma_f32_16x16x32_bf16 v[52:55], v[60:63], a[56:59], v[52:55]\n\t"
        "s_waitcnt lgkmcnt(5)\n\t"
        "v_mfma_f32_16x16x32_bf16 v[32:35], a[60:63], v[64:67], v[32:35]\n\t"
        "s_waitcnt vmcnt(19) lgkmcnt(0)\n\t"
        "s_barrier\n\t"
        "s_add_u32 %[xo], %[xo], 0x20000\n\t"
        "v_mfma_f32_16x16x32_bf16 v[36:39], a[64:67], v[64:67], v[36:39]\n\t"
        "ds_read_b128 v[56:59], v105 offset:32768\n\t"
        "v_mfma_f32_16x16x32_bf16 v[40:43], v[64:67], a[68:71], v[40:43]\n\t"
        "ds_read_b128 v[60:63], v105 offset:34816\n\t"
        "s_add_u32 m0, %[ld], 0\n\t"
        "s_add_u32 %[t0], %[xo], 0\n\t"
        "v_mfma_f32_16x16x32_bf16 v[44:47], a[60:63], v[68:71], v[44:47]\n\t"
        "buffer_load_dwordx4 v100, %[sx], %[t0] offen lds\n\t"
        "v_mfma_f32_16x16x32_bf16 v[48:51], a[64:67], v[68:71], v[48:51]\n\t"
        "ds_read_b128 v[64:67], v106 offset:32768\n\t"
        "v_mfma_f32_16x16x32_bf16 v[52:55], v[68:71], a[68:71], v[52:55]\n\t"
        "v_mfma_f32_16x16x32_bf16 v[32:35], a[72:75], v[72:75], v[32:35]\n\t"
        "v_mfma_f32_16x16x32_bf16 v[36:39], a[76:79], v[72:75], v[36:39]\n\t"
        "ds_read_b128 v[68:71], v106 offset:34816\n\t"
        "v_mfma_f32_16x16x32_bf16 v[40:43], v[72:75], a[80:83], v[40:43]\n\t"
        "s_add_u32 m0, %[ld], 1024\n\t"
        "s_add_u32 %[t0], %[xo], 16384\n\t"
        "v_mfma_f32_16x16x32_bf16 v[44:47], a[72:75], v[76:79], v[44:47]\n\t"
        "buffer_load_dwordx4 v100, %[sx], %[t0] offen lds\n\t"
        "v_mfma_f32_16x16x32_bf16 v[48:51], a[76:79], v[76:79], v[48:51]\n\t"
        "ds_read_b128 v[72:75], v105 offset:40960\n\t"
        "v_mfma_f32_16x16x32_bf16 v[52:55], v[76:79], a[80:83], v[52:55]\n\t"
        "v_mfma_f32_16x16x32_bf16 v[32:35], a[84:87], v[80:83], v[32:35]\n\t"
        "v_mfma_f32_16x16x32_bf16 v[36:39], a[88:91], v[80:83], v[36:39]\n\t"
        "ds_read_b128 v[76:79], v105 offset:43008\n\t"
        "v_mfma_f32_16x16x32_bf16 v[40:43], v[80:83], a[92:95], v[40:43]\n\t"
        "s_add_u32 m0, %[ld], 8192\n\t"
        "s_add_u32 %[t0], %[xo], 128\n\t"
        "v_mfma_f32_16x16x32_bf16 v[44:47], a[84:87], v[84:87], v[44:47]\n\t"
        "buffer_load_dwordx4 v100, %[sx], %[t0] offen lds\n\t"
        "v_mfma_f32_16x16x32_bf16 v[48:51], a[88:91], v[84:87], v[48:51]\n\t"
        "ds_read_b128 v[80:83], v106 offset:40960\n\t"
        "v_mfma_f32_16x16x32_bf16 v[52:55], v[84:87], a[92:95], v[52:55]\n\t"
        "s_waitcnt lgkmcnt(6)\n\t"
        "v_mfma_f32_16x16x32_bf16 v[32:35], a[96:99], v[56:59], v[32:35]\n\t"
        "v_mfma_f32_16x16x32_bf16 v[36:39], a[100:103], v[56:59], v[36:39]\n\t"
        "ds_read_b128 v[84:87], v106 offset:43008\n\t"
        "v_mfma_f32_16x16x32_bf16 v[40:43], v[56:59], a[104:107], v[40:43]\n\t"
        "s_add_u32 m0, %[ld], 9216\n\t"
        "s_add_u32 %[t0], %[xo], 16512\n\t"
        "s_waitcnt lgkmcnt(6)\n\t"
        "v_mfma_f32_16x16x32_bf16 v[44:47], a[96:99], v[60:63], v[44:47]\n\t"
        "buffer_load_dwordx4 v100, %[sx], %[t0] offen lds\n\t"
        "v_mfma_f32_16x16x32_bf16 v[48:51], a[100:103], v[60:63], v[48:51]\n\t"
        "ds_read_b128 v[56:59], v105 offset:49152\n\t"
        "v_mfma_f32_16x16x32_bf16 v[52:55], v[60:63], a[104:107], v[52:55]\n\t"
        "s_waitcnt lgkmcnt(6)\n\t"
        "v_mfma_f32_16x16x32_bf16 v[32:35], a[108:111], v[64:67], v[32:35]\n\t"
        "v_mfma_f32_16x16x32_bf16 v[36:39], a[112:115], v[64:67], v[36:39]\n\t"
        "ds_read_b128 v[60:63], v105 offset:51200\n\t"
        "v_mfma_f32_16x16x32_bf16 v[40:43], v[64:67], a[116:119], v[40:43]\n\t"
        "s_add_u32 m0, %[ld], 16384\n\t"
        "s_add_u32 %[t0], %[xo], 256\n\t"
        "s_waitcnt lgkmcnt(6)\n\t"
        "v_mfma_f32_16x16x32_bf16 v[44:47], a[108:111], v[68:71], v[44:47]\n\t"
        "buffer_load_dwordx4 v100, %[sx], %[t0] offen lds\n\t"
        "v_mfma_f32_16x16x32_bf16 v[48:51], a[112:115], v[68:71], v[48:51]\n\t"
        "ds_read_b128 v[64:67], v106 offset:49152\n\t"
        "v_mfma_f32_16x16x32_bf16 v[52:55], v[68:71], a[116:119], v[52:55]\n\t"
        "s_waitcnt lgkmcnt(6)\n\t"
        "v_mfma_f32_16x16x32_bf16 v[32:35], a[120:123], v[72:75], v[32:35]\n\t"
        "v_mfma_f32_16x16x32_bf16 v[36:39], a[124:127], v[72:75], v[36:39]\n\t"
        "ds_read_b128 v[68:71], v106 offset:51200\n\t"
        "v_mfma_f32_16x16x32_bf16 v[40:43], v[72:75], a[128:131], v[40:43]\n\t"
        "s_add_u32 m0, %[ld], 17408\n\t"
        "s_add_u32 %[t0], %[xo], 16640\n\t"
        "s_waitcnt lgkmcnt(6)\n\t"
        "v_mfma_f32_16x16x32_bf16 v[44:47], a[120:123], v[76:79], v[44:47]\n\t"
        "buffer_load_dwordx4 v100, %[sx], %[t0] offen lds\n\t"
        "v_mfma_f32_16x16x32_bf16 v[48:51], a[124:127], v[76:79], v[48:51]\n\t"
        "ds_read_b128 v[72:75], v105 offset:57344\n\t"
        "v_mfma_f32_16x16x32_bf16 v[52:55], v[76:79], a[128:131], v[52:55]\n\t"
        "s_waitcnt lgkmcnt(6)\n\t"
        "v_mfma_f32_16x16x32_bf16 v[32:35], a[132:135], v[80:83], v[32:35]\n\t"
        "v_mfma_f32_16x16x32_bf16 v[36:39], a[136:139], v[80:83], v[36:39]\n\t"
        "ds_read_b128 v[76:79], v105 offset:59392\n\t"
        "v_mfma_f32_16x16x32_bf16 v[40:43], v[80:83], a[140:143], v[40:43]\n\t"
        "s_add_u32 m0, %[ld], 24576\n\t"
        "s_add_u32 %[t0], %[xo], 384\n\t"
        "s_waitcnt lgkmcnt(6)\n\t"
        "v_mfma_f32_16x16x32_bf16 v[44:47], a[132:135], v[84:87], v[44:47]\n\t"
        "buffer_load_dwordx4 v100, %[sx], %[t0] offen lds\n\t"
        "v_mfma_f32_16x16x32_bf16 v[48:51], a[136:139], v[84:87], v[48:51]\n\t"
        "ds_read_b128 v[80:83], v106 offset:57344\n\t"
        "v_mfma_f32_16x16x32_bf16 v[52:55], v[84:87], a[140:143], v[52:55]\n\t"
        "s_waitcnt lgkmcnt(6)\n\t"
        "v_mfma_f32_16x16x32_bf16 v[32:35], a[144:147], v[56:59], v[32:35]\n\t"
        "v_mfma_f32_16x16x32_bf16 v[36:39], a[148:151], v[56:59], v[36:39]\n\t"
        "ds_read_b128 v[84:87], v106 offset:59392\n\t"
        "v_mfma_f32_16x16x32_bf16 v[40:43], v[56:59], a[152:155], v[40:43]\n\t"
        "s_add_u32 m0, %[ld], 25600\n\t"
        "s_add_u32 %[t0], %[xo], 16768\n\t"
        "s_waitcnt lgkmcnt(6)\n\t"
        "v_mfma_f32_16x16x32_bf16 v[44:47], a[144:147], v[60:63], v[44:47]\n\t"
        "buffer_load_dwordx4 v100, %[sx], %[t0] offen lds\n\t"
        "v_mfma_f32_16x16x32_bf16 v[48:51], a[148:151], v[60:63], v[48:51]\n\t"
        "v_mfma_f32_16x16x32_bf16 v[52:55], v[60:63], a[152:155], v[52:55]\n\t"
        "s_waitcnt lgkmcnt(5)\n\t"
        "v_mfma_f32_16x16x32_bf16 v[32:35], a[156:159], v[64:67], v[32:35]\n\t"
        "s_waitcnt vmcnt(9) lgkmcnt(0)\n\t"
        "s_barrier\n\t"
        "v_mfma_f32_16x16x32_bf16 v[36:39], a[160:163], v[64:67], v[36:39]\n\t"
        "ds_read_b128 v[56:59], v107 offset:0\n\t"
        "v_mfma_f32_16x16x32_bf16 v[40:43], v[64:67], a[164:167], v[40:43]\n\t"
        "ds_read_b128 v[60:63], v107 offset:2048\n\t"
        "s_add_u32 m0, %[ld], 32768\n\t"
        "s_add_u32 %[t0], %[xo], 512\n\t"
        "v_mfma_f32_16x16x32_bf16 v[44:47], a[156:159], v[68:71], v[44:47]\n\t"
        "buffer_load_dwordx4 v100, %[sx], %[t0] offen lds\n\t"
        "v_mfma_f32_16x16x32_bf16 v[48:51], a[160:163], v[68:71], v[48:51]\n\t"
        "ds_read_b128 v[64:67], v108 offset:0\n\t"
        "v_mfma_f32_16x16x32_bf16 v[52:55], v[68:71], a[164:167], v[52:55]\n\t"
        "v_mfma_f32_16x16x32_bf16 v[32:35], a[168:171], v[72:75], v[32:35]\n\t"
        "v_mfma_f32_16x16x32_bf16 v[36:39], a[172:175], v[72:75], v[36:39]\n\t"
        "ds_read_b128 v[68:71], v108 offset:2048\n\t"
        "v_mov_b32_e32 v114, %[cn]\n\t"
        "buffer_store_dword v114, v113, %[sw], 0 offen sc1\n\t"
        "s_add_u32 %[cn], %[cn], 1\n\t"
        "v_mfma_f32_16x16x32_bf16 v[40:43], v[72:75], a[176:179], v[40:43]\n\t"
        "s_add_u32 m0, %[ld], 33792\n\t"
        "s_add_u32 %[t0], %[xo], 16896\n\t"
        "v_mfma_f32_16x16x32_bf16 v[44:47], a[168:171], v[76:79], v[44:47]\n\t"
        "buffer_load_dwordx4 v100, %[sx], %[t0] offen lds\n\t"
        "v_mfma_f32_16x16x32_bf16 v[48:51], a[172:175], v[76:79], v[48:51]\n\t"
        "ds_read_b128 v[72:75], v107 offset:8192\n\t"
        "v_mfma_f32_16x16x32_bf16 v[52:55], v[76:79], a[176:179], v[52:55]\n\t"
        "v_mfma_f32_16x16x32_bf16 v[32:35], a[180:183], v[80:83], v[32:35]\n\t"
        "v_mfma_f32_16x16x32_bf16 v[36:39], a[184:187], v[80:83], v[36:39]\n\t"
        "ds_read_b128 v[76:79], v107 offset:10240\n\t"
        "v_mfma_f32_16x16x32_bf16 v[40:43], v[80:83], a[188:191], v[40:43]\n\t"
        "s_add_u32 m0, %[ld], 40960\n\t"
        "s_add_u32 %[t0], %[xo], 640\n\t"
        "v_mfma_f32_16x16x32_bf16 v[44:47], a[180:183], v[84:87], v[44:47]\n\t"
        "buffer_load_dwordx4 v100, %[sx], %[t0] offen lds\n\t"
        "v_mfma_f32_16x16x32_bf16 v[48:51], a[184:187], v[84:87], v[48:51]\n\t"
        "ds_read_b128 v[80:83], v108 offset:8192\n\t"
        "v_mfma_f32_16x16x32_bf16 v[52:55], v[84:87], a[188:191], v[52:55]\n\t"
        "s_waitcnt lgkmcnt(6)\n\t"
        "v_mfma_f32_16x16x32_bf16 v[32:35], a[192:195], v[56:59], v[32:35]\n\t"
        "v_mfma_f32_16x16x32_bf16 v[36:39], a[196:199], v[56:59], v[36:39]\n\t"
        "ds_read_b128 v[84:87], v108 offset:10240\n\t"
        "v_mfma_f32_16x16x32_bf16 v[40:43], v[56:59], a[200:203], v[40:43]\n\t"
        "s_add_u32 m0, %[ld], 41984\n\t"
        "s_add_u32 %[t0], %[xo], 17024\n\t"
        "s_waitcnt lgkmcnt(6)\n\t"
        "v_mfma_f32_16x16x32_bf16 v[44:47], a[192:195], v[60:63], v[44:47]\n\t"
        "buffer_load_dwordx4 v100, %[sx], %[t0] offen lds\n\t"
        "v_mfma_f32_16x16x32_bf16 v[48:51], a[196:199], v[60:63], v[48:51]\n\t"
        "ds_read_b128 v[56:59], v107 offset:16384\n\t"
        "v_mfma_f32_16x16x32_bf16 v[52:55], v[60:63], a[200:203], v[52:55]\n\t"
        "s_waitcnt lgkmcnt(6)\n\t"
        "v_mfma_f32_16x16x32_bf16 v[32:35], a[204:207], v[64:67], v[32:35]\n\t"
        "v_mfma_f32_16x16x32_bf16 v[36:39], a[208:211], v[64:67], v[36:39]\n\t"
        "ds_read_b128 v[60:63], v107 offset:18432\n\t"
        "v_mfma_f32_16x16x32_bf16 v[40:43], v[64:67], a[212:215], v[40:43]\n\t"
        "s_add_u32 m0, %[ld], 49152\n\t"
        "s_add_u32 %[t0], %[xo], 768\n\t"
        "s_waitcnt lgkmcnt(6)\n\t"
        "v_mfma_f32_16x16x32_bf16 v[44:47], a[204:207], v[68:71], v[44:47]\n\t"
        "buffer_load_dwordx4 v100, %[sx], %[t0] offen lds\n\t"
        "v_mfma_f32_16x16x32_bf16 v[48:51], a[208:211], v[68:71], v[48:51]\n\t"
        "ds_read_b128 v[64:67], v108 offset:16384\n\t"
        "v_mfma_f32_16x16x32_bf16 v[52:55], v[68:71], a[212:215], v[52:55]\n\t"
        "s_waitcnt lgkmcnt(6)\n\t"
        "v_mfma_f32_16x16x32_bf16 v[32:35], a[216:219], v[72:75], v[32:35]\n\t"
        "v_mfma_f32_16x16x32_bf16 v[36:39], a[220:223], v[72:75], v[36:39]\n\t"
        "ds_read_b128 v[68:71], v108 offset:18432\n\t"
        "v_mfma_f32_16x16x32_bf16 v[40:43], v[72:75], a[224:227], v[40:43]\n\t"
        "s_add_u32 m0, %[ld], 50176\n\t"
        "s_add_u32 %[t0], %[xo], 17152\n\t"
        "s_waitcnt lgkmcnt(6)\n\t"
        "v_mfma_f32_16x16x32_bf16 v[44:47], a[216:219], v[76:79], v[44:47]\n\t"
        "buffer_load_dwordx4 v100, %[sx], %[t0] offen lds\n\t"
        "v_mfma_f32_16x16x32_bf16 v[48:51], a[220:223], v[76:79], v[48:51]\n\t"
        "ds_read_b128 v[72:75], v107 offset:24576\n\t"
        "v_mfma_f32_16x16x32_bf16 v[52:55], v[76:79], a[224:227], v[52:55]\n\t"
        "s_waitcnt lgkmcnt(6)\n\t"
        "v_mfma_f32_16x16x32_bf16 v[32:35], a[228:231], v[80:83], v[32:35]\n\t"
        "v_mfma_f32_16x16x32_bf16 v[36:39], a[232:235], v[80:83], v[36:39]\n\t"
        "ds_read_b128 v[76:79], v107 offset:26624\n\t"
        "v_mfma_f32_16x16x32_bf16 v[40:43], v[80:83], a[236:239], v[40:43]\n\t"
        "s_add_u32 m0, %[ld], 57344\n\t"
        "s_add_u32 %[t0], %[xo], 896\n\t"
        "s_waitcnt lgkmcnt(6)\n\t"
        "v_mfma_f32_16x16x32_bf16 v[44:47], a[228:231], v[84:87], v[44:47]\n\t"
        "buffer_load_dwordx4 v100, %[sx], %[t0] offen lds\n\t"
        "v_mfma_f32_16x16x32_bf16 v[48:51], a[232:235], v[84:87], v[48:51]\n\t"
        "ds_read_b128 v[80:83], v108 offset:24576\n\t"
        "v_mfma_f32_16x16x32_bf16 v[52:55], v[84:87], a[236:239], v[52:55]\n\t"
        "s_waitcnt lgkmcnt(6)\n\t"
        "v_mfma_f32_16x16x32_bf16 v[32:35], a[240:243], v[56:59], v[32:35]\n\t"
        "v_mfma_f32_16x16x32_bf16 v[36:39], a[244:247], v[56:59], v[36:39]\n\t"
        "ds_read_b128 v[84:87], v108 offset:26624\n\t"
        "v_mfma_f32_16x16x32_bf16 v[40:43], v[56:59], a[248:251], v[40:43]\n\t"
        "s_add_u32 m0, %[ld], 58368\n\t"
        "s_add_u32 %[t0], %[xo], 17280\n\t"
        "s_waitcnt lgkmcnt(6)\n\t"
        "v_mfma_f32_16x16x32_bf16 v[44:47], a[240:243], v[60:63], v[44:47]\n\t"
        "buffer_load_dwordx4 v100, %[sx], %[t0] offen lds\n\t"
        "v_mfma_f32_16x16x32_bf16 v[48:51], a[244:247], v[60:63], v[48:51]\n\t"
        "v_mfma_f32_16x16x32_bf16 v[52:55], v[60:63], a[248:251], v[52:55]\n\t"
        "s_waitcnt lgkmcnt(5)\n\t"
        "v_mfma_f32_16x16x32_bf16 v[32:35], a[252:255], v[64:67], v[32:35]\n\t"
        "s_waitcnt vmcnt(17) lgkmcnt(0)\n\t"
        "s_barrier\n\t"
        "v_mfma_f32_16x16x32_bf16 v[36:39], v[128:131], v[64:67], v[36:39]\n\t"
        "ds_read_b128 v[56:59], v107 offset:32768\n\t"
        "v_mfma_f32_16x16x32_bf16 v[40:43], v[64:67], v[132:135], v[40:43]\n\t"
        "ds_read_b128 v[60:63], v107 offset:34816\n\t"
        "s_add_u32 m0, %[ld], 65536\n\t"
        "s_add_u32 %[t0], %[xo], 1024\n\t"
        "v_mfma_f32_16x16x32_bf16 v[44:47], a[252:255], v[68:71], v[44:47]\n\t"
        "buffer_load_dwordx4 v100, %[sx], %[t0] offen lds\n\t"
        "v_mfma_f32_16x16x32_bf16 v[48:51], v[128:131], v[68:71], v[48:51]\n\t"
        "ds_read_b128 v[64:67], v108 offset:32768\n\t"
        "v_mfma_f32_16x16x32_bf16 v[52:55], v[68:71], v[132:135], v[52:55]\n\t"
        "v_mfma_f32_16x16x32_bf16 v[32:35], v[136:139], v[72:75], v[32:35]\n\t"
        "v_mfma_f32_16x16x32_bf16 v[36:39], v[140:143], v[72:75], v[36:39]\n\t"
        "ds_read_b128 v[68:71], v108 offset:34816\n\t"
        "v_mfma_f32_16x16x32_bf16 v[40:43], v[72:75], v[144:147], v[40:43]\n\t"
        "s_add_u32 m0, %[ld], 66560\n\t"
        "s_add_u32 %[t0], %[xo], 17408\n\t"
        "v_mfma_f32_16x16x32_bf16 v[44:47], v[136:139], v[76:79], v[44:47]\n\t"
        "buffer_load_dwordx4 v100, %[sx], %[t0] offen lds\n\t"
        "v_mfma_f32_16x16x32_bf16 v[48:51], v[140:143], v[76:79], v[48:51]\n\t"
        "ds_read_b128 v[72:75], v107 offset:40960\n\t"
        "v_mfma_f32_16x16x32_bf16 v[52:55], v[76:79], v[144:147], v[52:55]\n\t"
        "v_mfma_f32_16x16x32_bf16 v[32:35], v[148:151], v[80:83], v[32:35]\n\t"
        "v_mfma_f32_16x16x32_bf16 v[36:39], v[152:155], v[80:83], v[36:39]\n\t"
        "ds_read_b128 v[76:79], v107 offset:43008\n\t"
        "v_mfma_f32_16x16x32_bf16 v[40:43], v[80:83], v[156:159], v[40:43]\n\t"
        "s_add_u32 m0, %[ld], 73728\n\t"
        "s_add_u32 %[t0], %[xo], 1152\n\t"
        "v_mfma_f32_16x16x32_bf16 v[44:47], v[148:151], v[84:87], v[44:47]\n\t"
        "buffer_load_dwordx4 v100, %[sx], %[t0] offen lds\n\t"
        "v_mfma_f32_16x16x32_bf16 v[48:51], v[152:155], v[84:87], v[48:51]\n\t"
        "ds_read_b128 v[80:83], v108 offset:40960\n\t"
        "v_mfma_f32_16x16x32_bf16 v[52:55], v[84:87], v[156:159], v[52:55]\n\t"
        "s_waitcnt lgkmcnt(6)\n\t"
        "v_mfma_f32_16x16x32_bf16 v[32:35], v[160:163], v[56:59], v[32:35]\n\t"
        "v_mfma_f32_16x16x32_bf16 v[36:39], v[164:167], v[56:59], v[36:39]\n\t"
        "ds_read_b128 v[84:87], v108 offset:43008\n\t"
        "v_mfma_f32_16x16x32_bf16 v[40:43], v[56:59], v[168:171], v[40:43]\n\t"
        "s_add_u32 m0, %[ld], 74752\n\t"
        "s_add_u32 %[t0], %[xo], 17536\n\t"
        "s_waitcnt lgkmcnt(6)\n\t"
        "v_mfma_f32_16x16x32_bf16 v[44:47], v[160:163], v[60:63], v[44:47]\n\t"
        "buffer_load_dwordx4 v100, %[sx], %[t0] offen lds\n\t"
        "v_mfma_f32_16x16x32_bf16 v[48:51], v[164:167], v[60:63], v[48:51]\n\t"
        "ds_read_b128 v[56:59], v107 offset:49152\n\t"
        "v_mfma_f32_16x16x32_bf16 v[52:55], v[60:63], v[168:171], v[52:55]\n\t"
        "s_waitcnt lgkmcnt(6)\n\t"
        "v_mfma_f32_16x16x32_bf16 v[32:35], v[172:175], v[64:67], v[32:35]\n\t"
        "v_mfma_f32_16x16x32_bf16 v[36:39], v[176:179], v[64:67], v[36:39]\n\t"
        "ds_read_b128 v[60:63], v107 offset:51200\n\t"
        "v_mfma_f32_16x16x32_bf16 v[40:43], v[64:67], v[180:183], v[40:43]\n\t"
        "s_add_u32 m0, %[ld], 81920\n\t"
        "s_add_u32 %[t0], %[xo], 1280\n\t"
        "s_waitcnt lgkmcnt(6)\n\t"
        "v_mfma_f32_16x16x32_bf16 v[44:47], v[172:175], v[68:71], v[44:47]\n\t"
        "buffer_load_dwordx4 v100, %[sx], %[t0] offen lds\n\t"
        "v_mfma_f32_16x16x32_bf16 v[48:51], v[176:179], v[68:71], v[48:51]\n\t"
        "ds_read_b128 v[64:67], v108 offset:49152\n\t"
        "v_mfma_f32_16x16x32_bf16 v[52:55], v[68:71], v[180:183], v[52:55]\n\t"
        "s_waitcnt lgkmcnt(6)\n\t"
        "v_mfma_f32_16x16x32_bf16 v[32:35], v[184:187], v[72:75], v[32:35]\n\t"
        "v_mfma_f32_16x16x32_bf16 v[36:39], v[188:191], v[72:75], v[36:39]\n\t"
        "ds_read_b128 v[68:71], v108 offset:51200\n\t"
        "v_mfma_f32_16x16x32_bf16 v[40:43], v[72:75], v[192:195], v[40:43]\n\t"
        "s_add_u32 m0, %[ld], 82944\n\t"
        "s_add_u32 %[t0], %[xo], 17664\n\t"
        "s_waitcnt lgkmcnt(6)\n\t"
        "v_mfma_f32_16x16x32_bf16 v[44:47], v[184:187], v[76:79], v[44:47]\n\t"
        "buffer_load_dwordx4 v100, %[sx], %[t0] offen lds\n\t"
        "v_mfma_f32_16x16x32_bf16 v[48:51], v[188:191], v[76:79], v[48:51]\n\t"
        "ds_read_b128 v[72:75], v107 offset:57344\n\t"
        "v_mfma_f32_16x16x32_bf16 v[52:55], v[76:79], v[192:195], v[52:55]\n\t"
        "s_waitcnt lgkmcnt(6)\n\t"
        "v_mfma_f32_16x16x32_bf16 v[32:35], v[196:199], v[80:83], v[32:35]\n\t"
        "v_mfma_f32_16x16x32_bf16 v[36:39], v[200:203], v[80:83], v[36:39]\n\t"
        "ds_read_b128 v[76:79], v107 offset:59392\n\t"
        "v_mfma_f32_16x16x32_bf16 v[40:43], v[80:83], v[204:207], v[40:43]\n\t"
        "s_add_u32 m0, %[ld], 90112\n\t"
        "s_add_u32 %[t0], %[xo], 1408\n\t"
        "s_waitcnt lgkmcnt(6)\n\t"
        "v_mfma_f32_16x16x32_bf16 v[44:47], v[196:199], v[84:87], v[44:47]\n\t"
        "buffer_load_dwordx4 v100, %[sx], %[t0] offen lds\n\t"
        "v_mfma_f32_16x16x32_bf16 v[48:51], v[200:203], v[84:87], v[48:51]\n\t"
        "ds_read_b128 v[80:83], v108 offset:57344\n\t"
        "v_mfma_f32_16x16x32_bf16 v[52:55], v[84:87], v[204:207], v[52:55]\n\t"
        "s_waitcnt lgkmcnt(6)\n\t"
        "v_mfma_f32_16x16x32_bf16 v[32:35], v[208:211], v[56:59], v[32:35]\n\t"
        "v_mfma_f32_16x16x32_bf16 v[36:39], v[212:215], v[56:59], v[36:39]\n\t"
        "ds_read_b128 v[84:87], v108 offset:59392\n\t"
        "v_mfma_f32_16x16x32_bf16 v[40:43], v[56:59], v[216:219], v[40:43]\n\t"
        "s_add_u32 m0, %[ld], 91136\n\t"
        "s_add_u32 %[t0], %[xo], 17792\n\t"
        "s_waitcnt lgkmcnt(6)\n\t"
        "v_mfma_f32_16x16x32_bf16 v[44:47], v[208:211], v[60:63], v[44:47]\n\t"
        "buffer_load_dwordx4 v100, %[sx], %[t0] offen lds\n\t"
        "v_mfma_f32_16x16x32_bf16 v[48:51], v[212:215], v[60:63], v[48:51]\n\t"
        "v_mfma_f32_16x16x32_bf16 v[52:55], v[60:63], v[216:219], v[52:55]\n\t"
        "s_waitcnt lgkmcnt(5)\n\t"
        "v_mfma_f32_16x16x32_bf16 v[32:35], v[220:223], v[64:67], v[32:35]\n\t"
        "s_waitcnt vmcnt(17) lgkmcnt(0)\n\t"
        "s_barrier\n\t"
        "v_mfma_f32_16x16x32_bf16 v[36:39], v[224:227], v[64:67], v[36:39]\n\t"
        "ds_read_b128 v[56:59], v105 offset:0\n\t"
        "v_mfma_f32_16x16x32_bf16 v[40:43], v[64:67], v[228:231], v[40:43]\n\t"
        "ds_read_b128 v[60:63], v105 offset:2048\n\t"
        "s_add_u32 m0, %[ld], 98304\n\t"
        "s_add_u32 %[t0], %[xo], 1536\n\t"
        "v_mfma_f32_16x16x32_bf16 v[44:47], v[220:223], v[68:71], v[44:47]\n\t"
        "buffer_load_dwordx4 v100, %[sx], %[t0] offen lds\n\t"
        "v_mfma_f32_16x16x32_bf16 v[48:51], v[224:227], v[68:71], v[48:51]\n\t"
        "ds_read_b128 v[64:67], v106 offset:0\n\t"
        "v_mfma_f32_16x16x32_bf16 v[52:55], v[68:71], v[228:231], v[52:55]\n\t"
        "v_mfma_f32_16x16x32_bf16 v[32:35], v[232:235], v[72:75], v[32:35]\n\t"
        "v_mfma_f32_16x16x32_bf16 v[36:39], v[236:239], v[72:75], v[36:39]\n\t"
        "ds_read_b128 v[68:71], v106 offset:2048\n\t"
        "v_mfma_f32_16x16x32_bf16 v[40:43], v[72:75], v[240:243], v[40:43]\n\t"
        "s_add_u32 m0, %[ld], 99328\n\t"
        "s_add_u32 %[t0], %[xo], 17920\n\t"
        "v_mfma_f32_16x16x32_bf16 v[44:47], v[232:235], v[76:79], v[44:47]\n\t"
        "buffer_load_dwordx4 v100, %[sx], %[t0] offen lds\n\t"
        "v_mfma_f32_16x16x32_bf16 v[48:51], v[236:239], v[76:79], v[48:51]\n\t"
        "ds_read_b128 v[72:75], v105 offset:8192\n\t"
        "v_mfma_f32_16x16x32_bf16 v[52:55], v[76:79], v[240:243], v[52:55]\n\t"
        "v_mfma_f32_16x16x32_bf16 v[32:35], v[244:247], v[80:83], v[32:35]\n\t"
        "v_mfma_f32_16x16x32_bf16 v[36:39], v[248:251], v[80:83], v[36:39]\n\t"
        "ds_read_b128 v[76:79], v105 offset:10240\n\t"
        "v_mfma_f32_16x16x32_bf16 v[40:43], v[80:83], v[252:255], v[40:43]\n\t"
        "s_add_u32 m0, %[ld], 106496\n\t"
        "s_add_u32 %[t0], %[xo], 1664\n\t"
        "v_mfma_f32_16x16x32_bf16 v[44:47], v[244:247], v[84:87], v[44:47]\n\t"
        "buffer_load_dwordx4 v100, %[sx], %[t0] offen lds\n\t"
        "v_mfma_f32_16x16x32_bf16 v[48:51], v[248:251], v[84:87], v[48:51]\n\t"
        "ds_read_b128 v[80:83], v106 offset:8192\n\t"
        "v_mfma_f32_16x16x32_bf16 v[52:55], v[84:87], v[252:255], v[52:55]\n\t"
        "s_cmp_lt_u32 %[cn], 32\n\t"
        "s_cbranch_scc1 LG_loop%=\n\t"
        "s_nop 7\n\t"
        "v_cvt_pk_bf16_f32 v88, v32, v33\n\t"
        "v_cvt_pk_bf16_f32 v89, v34, v35\n\t"
        "v_cvt_pk_bf16_f32 v90, v36, v37\n\t"
        "v_cvt_pk_bf16_f32 v91, v38, v39\n\t"
        "buffer_store_dwordx4 v[88:91], v109, %[sw], %[so] offen sc1\n\t"
        "v_cvt_pk_bf16_f32 v92, v44, v45\n\t"
        "v_cvt_pk_bf16_f32 v93, v46, v47\n\t"
        "v_cvt_pk_bf16_f32 v94, v48, v49\n\t"
        "v_cvt_pk_bf16_f32 v95, v50, v51\n\t"
        "buffer_store_dwordx4 v[92:95], v110, %[sw], %[so] offen sc1\n\t"
        "v_cvt_pk_bf16_f32 v96, v40, v41\n\t"
        "v_cvt_pk_bf16_f32 v97, v42, v43\n\t"
        "v_cvt_pk_bf16_f32 v98, v52, v53\n\t"
        "v_cvt_pk_bf16_f32 v99, v54, v55\n\t"
        "buffer_store_dwordx4 v[96:99], v111, %[sw], %[so] offen sc1\n\t"
        "s_waitcnt vmcnt(0) lgkmcnt(0)\n\t"
        "v_mov_b32_e32 v114, %[cn]\n\t"
        "buffer_store_dword v114, v113, %[sw], 0 offen sc1\n\t"
        "s_barrier\n\t"
        : [t0] "=&s"(t0), [xo] "=&s"(xo), [so] "=&s"(so), [cn] "=&s"(cn), [ts] "=&s"(ts)
        : [sx] "s"(sx), [sq] "s"(sq), [sv] "s"(sv), [sw] "s"(sw), [ld] "s"(lb + w * 2048),
          "{v100}"((16 * w + (l >> 3)) * 2048 + (((l & 7) ^ (l >> 3)) << 4)), "{v101}"((8 * w + (l >> 3)) * 8192 + h * 256 + (c & 1) * 128 + (l & 7) * 16),
          "{v112}"((16 * w + (l >> 2)) * 8192 + h * 256 + cv * 64 + (l & 3) * 16), "{v102}"(lb + 131072 + w * 1024 + l * 16),
          "{v103}"(lr + rr * 128 + tn * 64 + (n & 3) * 16), "{v104}"(lr + 8192 + rr * 64 + tn * 32 + (n & 3) * 8), "{v105}"(lx), "{v106}"(lx ^ 64u), "{v107}"(lx + 65536), "{v108}"((lx ^ 64u) + 65536),
          "{v109}"(aq), "{v110}"(aq + 4096), "{v111}"(W_VF + (tm * 32 + h) * 8192 + (2 * cv + tn) * 1024 + l * 16), "{v113}"(l == 0 ? F_KVQ + h * 128 + (c * 4 + w) * 4 : OOB)
        : "memory", "scc", "m0", "v8", "v9", "v10", "v11", "v12", "v13", "v14", "v15", "v16", "v17", "v18", "v19", "v20", "v21", "v22", "v23", "v24", "v25", "v26", "v27", "v28", "v29", "v30", "v31", "v32", "v33", "v34", "v35", "v36", "v37", "v38", "v39", "v40", "v41", "v42", "v43", "v44", "v45", "v46", "v47", "v48", "v49", "v50", "v51", "v52", "v53", "v54", "v55", "v56", "v57", "v58", "v59", "v60", "v61", "v62", "v63", "v64", "v65", "v66", "v67", "v68", "v69", "v70", "v71", "v72", "v73", "v74", "v75", "v76", "v77", "v78", "v79", "v80", "v81", "v82", "v83", "v84", "v85", "v86", "v87", "v88", "v89", "v90", "v91", "v92", "v93", "v94", "v95", "v96", "v97", "v98", "v99", "v114", "v115", "v116", "v117", "v118", "v119", "v120", "v121", "v122", "v123", "v124", "v125", "v126", "v127", "v128", "v129", "v130", "v131", "v132", "v133", "v134", "v135", "v136", "v137", "v138", "v139", "v140", "v141", "v142", "v143", "v144", "v145", "v146", "v147", "v148", "v149", "v150", "v151", "v152", "v153", "v154", "v155", "v156", "v157", "v158", "v159", "v160", "v161", "v162", "v163", "v164", "v165", "v166", "v167", "v168", "v169", "v170", "v171", "v172", "v173", "v174", "v175", "v176", "v177", "v178", "v179", "v180", "v181", "v182", "v183", "v184", "v185", "v186", "v187", "v188", "v189", "v190", "v191", "v192", "v193", "v194", "v195", "v196", "v197", "v198", "v199", "v200", "v201", "v202", "v203", "v204", "v205", "v206", "v207", "v208", "v209", "v210", "v211", "v212", "v213", "v214", "v215", "v216", "v217", "v218", "v219", "v220", "v221", "v222", "v223", "v224", "v225", "v226", "v227", "v228", "v229", "v230", "v231", "v232", "v233", "v234", "v235", "v236", "v237", "v238", "v239", "v240", "v241", "v242", "v243", "v244", "v245", "v246", "v247", "v248", "v249", "v250", "v251", "v252", "v253", "v254", "v255", "a0", "a1", "a2", "a3", "a4", "a5", "a6", "a7", "a8", "a9", "a10", "a11", "a12", "a13", "a14", "a15", "a16", "a17", "a18", "a19", "a20", "a21", "a22", "a23", "a24", "a25", "a26", "a27", "a28", "a29", "a30", "a31", "a32", "a33", "a34", "a35", "a36", "a37", "a38", "a39", "a40", "a41", "a42", "a43", "a44", "a45", "a46", "a47", "a48", "a49", "a50", "a51", "a52", "a53", "a54", "a55", "a56", "a57", "a58", "a59", "a60", "a61", "a62", "a63", "a64", "a65", "a66", "a67", "a68", "a69", "a70", "a71", "a72", "a73", "a74", "a75", "a76", "a77", "a78", "a79", "a80", "a81", "a82", "a83", "a84", "a85", "a86", "a87", "a88", "a89", "a90", "a91", "a92", "a93", "a94", "a95", "a96", "a97", "a98", "a99", "a100", "a101", "a102", "a103", "a104", "a105", "a106", "a107", "a108", "a109", "a110", "a111", "a112", "a113", "a114", "a115", "a116", "a117", "a118", "a119", "a120", "a121", "a122", "a123", "a124", "a125", "a126", "a127", "a128", "a129", "a130", "a131", "a132", "a133", "a134", "a135", "a136", "a137", "a138", "a139", "a140", "a141", "a142", "a143", "a144", "a145", "a146", "a147", "a148", "a149", "a150", "a151", "a152", "a153", "a154", "a155", "a156", "a157", "a158", "a159", "a160", "a161", "a162", "a163", "a164", "a165", "a166", "a167", "a168", "a169", "a170", "a171", "a172", "a173", "a174", "a175", "a176", "a177", "a178", "a179", "a180", "a181", "a182", "a183", "a184", "a185", "a186", "a187", "a188", "a189", "a190", "a191", "a192", "a193", "a194", "a195", "a196", "a197", "a198", "a199", "a200", "a201", "a202", "a203", "a204", "a205", "a206", "a207", "a208", "a209", "a210", "a211", "a212", "a213", "a214", "a215", "a216", "a217", "a218", "a219", "a220", "a221", "a222", "a223", "a224", "a225", "a226", "a227", "a228", "a229", "a230", "a231", "a232", "a233", "a234", "a235", "a236", "a237", "a238", "a239", "a240", "a241", "a242", "a243", "a244", "a245", "a246", "a247", "a248", "a249", "a250", "a251", "a252", "a253", "a254", "a255");
}

__device__ __forceinline__ unsigned min16(unsigned d) {
    d = min(d, (unsigned)__builtin_amdgcn_update_dpp((int)d, (int)d, 0xB1, 0xF, 0xF, false)); d = min(d, (unsigned)__builtin_amdgcn_update_dpp((int)d, (int)d, 0x4E, 0xF, 0xF, false));
    d = min(d, (unsigned)__builtin_amdgcn_update_dpp((int)d, (int)d, 0x124, 0xF, 0xF, false)); d = min(d, (unsigned)__builtin_amdgcn_update_dpp((int)d, (int)d, 0x128, 0xF, 0xF, false));
    return __builtin_amdgcn_readfirstlane(d); }
constexpr int OP_P = 72;
__device__ __forceinline__ void fin_loads(v4u (&fp)[8], v2u& fa0, v2u& fa1, unsigned& fg, unsigned po, unsigned ao, unsigned go, const v4u& rd) {
    asm volatile("buffer_load_dwordx4 %0, %11, %14, 0 offen sc1\n\tbuffer_load_dwordx4 %1, %11, %14, 0 offen offset:1024 sc1\n\t"
                 "buffer_load_dwordx4 %2, %11, %14, %15 offen sc1\n\tbuffer_load_dwordx4 %3, %11, %14, %15 offen offset:1024 sc1\n\t"
                 "buffer_load_dwordx4 %4, %11, %14, %16 offen sc1\n\tbuffer_load_dwordx4 %5, %11, %14, %16 offen offset:1024 sc1\n\t"
                 "buffer_load_dwordx4 %6, %11, %14, %17 offen sc1\n\tbuffer_load_dwordx4 %7, %11, %14, %17 offen offset:1024 sc1\n\t"
                 "buffer_load_dwordx2 %8, %12, %14, 0 offen sc1\n\tbuffer_load_dwordx2 %9, %12, %14, %18 offen sc1\n\tbuffer_load_dword %10, %13, %14, 0 offen sc1"
                 : "=&v"(fp[0]), "=&v"(fp[1]), "=&v"(fp[2]), "=&v"(fp[3]), "=&v"(fp[4]), "=&v"(fp[5]), "=&v"(fp[6]), "=&v"(fp[7]), "=&v"(fa0), "=&v"(fa1), "=&v"(fg)
                 : "v"(po), "v"(ao), "v"(go), "s"(rd), "s"(262144u), "s"(524288u), "s"(786432u), "s"(32u) : "memory"); }
__device__ __forceinline__ v2u mix(const v4u& p0, const v4u& p1, const v4u& p2, const v4u& p3, const v2u& av, unsigned fg) {
    const float gg = __uint_as_float(fg), og = rbfh(1.f - gg); float o[4];
#pragma unroll
    for (int q = 0; q < 4; ++q) { const unsigned aw = q < 2 ? av.x : av.y; const float aa = __uint_as_float((q & 1) ? (aw & 0xFFFF0000u) : (aw << 16));
        const float bsum = (__uint_as_float(p0[q]) + __uint_as_float(p1[q])) + (__uint_as_float(p2[q]) + __uint_as_float(p3[q]));
        o[q] = rbfh(gg * aa) + rbfh(og * rbfh(bsum)); }
    return (v2u){pack2(o[0], o[1]), pack2(o[2], o[3])}; }
__device__ __forceinline__ void bulk_block(const Params& p, rsrc_t r, unsigned char* lds, const int wv, const int j) {
    const int w = wv;
    BSTAMP(0);
    proj_block(p, lds, wv, (j & 6) * 4 + (j >> 4), ((j >> 2) & 2) | (j & 1), j);
    BSTAMP(2);
    {
        const rsrc_t ro = __builtin_amdgcn_make_buffer_rsrc(p.out, 0, S * D * 2, 0x00020000); const v4u z = {0u, 0u, 0u, 0u}; const int tid = otid();
#pragma unroll
        for (int i = 0; i < 8; ++i) st16(ro, (unsigned)(j * 32768 + i * 4096 + tid * 16), z);
        DRAIN(); __syncthreads();
        if (tid == 0) __hip_atomic_fetch_add((unsigned*)(p.ws + F_CONV), 1u, __ATOMIC_RELAXED, __HIP_MEMORY_SCOPE_AGENT);
    }
    const int tid4 = otid();
    const int kq = (j & 7) >> 1, nt = (j >> 3) * 2 + (j & 1), l = tid4 & 63, n16 = l & 15, g = l >> 4;
    __syncthreads();
    for (int i = tid4; i < 4096; i += 256) { const int row = i >> 2, q4 = i & 3;
        *(v4u*)(lds + row * OP_P + q4 * 16) = *(const v4u*)(p.Wo + (size_t)(kq * 1024 + row) * 1024 + nt * 32 + q4 * 8); }
    __syncthreads();
    BSTAMP(13);
    const unsigned ldsbase = (unsigned)(size_t)lds;
    auto finish = [&](int tt) {
        const int tok = tt * 64 + 16 * w + n16;
        const unsigned po = W_PART + tt * 1048576 + (w * 32 + nt) * 2048 + l * 16;
        v4u pp[4][2];
#pragma unroll
        for (int q = 0; q < 4; ++q)
#pragma unroll
            for (int jn = 0; jn < 2; ++jn) pp[q][jn] = ld16(r, po + q * 262144 + jn * 1024);
        const float gg = __uint_as_float(ld4(r, W_GATE + tok * 4)), og = rbfh(1.f - gg);
#pragma unroll
        for (int jn = 0; jn < 2; ++jn) {
            const int n = nt * 32 + 16 * jn + 4 * g;
            const v2u av = ld8(r, W_A + nt * 131072 + (tok & 31) * 4096 + (tok >> 5) * 64 + jn * 32 + g * 8);
            float o[4];
#pragma unroll
            for (int q = 0; q < 4; ++q) { const unsigned aw = q < 2 ? av.x : av.y; const float aa = __uint_as_float((q & 1) ? (aw & 0xFFFF0000u) : (aw << 16));
                const float bsum = (__uint_as_float(pp[0][jn][q]) + __uint_as_float(pp[1][jn][q])) + (__uint_as_float(pp[2][jn][q]) + __uint_as_float(pp[3][jn][q]));
                o[q] = rbfh(gg * aa) + rbfh(og * rbfh(bsum)); }
            v2u pk = {pack2(o[0], o[1]), pack2(o[2], o[3])}; *(v2u*)(p.out + (size_t)tok * 1024 + n) = pk; } };
    bf16x8 wof[32][2];
#pragma unroll
    for (int ks = 0; ks < 32; ++ks)
#pragma unroll
        for (int jn = 0; jn < 2; ++jn) {
            const unsigned a0 = ldsbase + (32 * ks + 4 * g + (n16 >> 2)) * OP_P + (16 * jn + 4 * (n16 & 3)) * 2;
            const v2u lo = trb(a0), hi = trb(a0 + 16 * OP_P);
            v4u v = {lo.x, lo.y, hi.x, hi.y}; asm volatile("" : "=a"(wof[ks][jn]) : "0"(as_frag(v))); }
    const v4u rd = mk_srd(p.ws, W_TOTAL), ro = mk_srd(p.out, S * D * 2);
    const int pw = w >> 1;
    int op = 0, nev = 0; unsigned idle = 0u; (void)nev;
    bool conv_ok = false; unsigned known = 0u;
    unsigned prog = 0u, tseen = rtnow() - 100000u;
    auto a_off = [&](int t) -> unsigned { return W_OX + ((t * 4 + w) * 32 + 8 * kq) * 4096 + l * 16; };
    const unsigned r_off = F_PROG + (kq * 8 + (l & 7)) * 128;
    auto units = [&](unsigned pg) -> unsigned { const unsigned c = (pg + 1u - pw) >> 1; return c >= 32u ? ~0u : (1u << c) - 1u; };
    while (op < 32) {
        unsigned rdy = known >> op;
        if (!(rdy & 1u)) {
            const unsigned now = rtnow(), est = prog + 1u + (now - tseen) / STEP_MIN;
            if (++idle > (1u << 22)) { st4(r, F_ERR, 128u); return; }
            if (est <= (unsigned)(2 * op + pw)) { __builtin_amdgcn_s_sleep(16); continue; }
            MEMBAR();
            prog = min16(ld4(r, r_off)); tseen = now; known = units(prog); rdy = known >> op;
            MEMBAR();
            if (!(rdy & 1u)) { __builtin_amdgcn_s_sleep(POLL_SLP); continue; }
        }
        idle = 0u;
        if (!conv_ok) { if (!wait_word(r, F_CONV, 160u, 256u)) return; conv_ok = true; }
        EV(128 + op);
        v4u af[32], wm, fp[8]; v2u fa0, fa1; unsigned aold = 0u, cfl = 0u, fg, d0;
        {
            const unsigned a0 = a_off(op);
            if (WARM1) asm volatile("buffer_load_dwordx4 %0, %1, %2, 0 offen sc1\n\ts_waitcnt vmcnt(0)" : "=&v"(wm) : "v"(a0 + nt * 1024), "s"(rd) : "memory");
#pragma unroll
            for (int q = 0; q < 8; ++q)
                asm volatile("buffer_load_dwordx4 %0, %4, %5, %6 offen sc1\n\tbuffer_load_dwordx4 %1, %4, %5, %6 offen offset:1024 sc1\n\t"
                             "buffer_load_dwordx4 %2, %4, %5, %6 offen offset:2048 sc1\n\tbuffer_load_dwordx4 %3, %4, %5, %6 offen offset:3072 sc1"
                             : "=&v"(af[4 * q]), "=&v"(af[4 * q + 1]), "=&v"(af[4 * q + 2]), "=&v"(af[4 * q + 3]) : "v"(a0), "s"(rd), "s"(q * 4096) : "memory");
            asm volatile("buffer_load_dwordx4 %3, %5, %6, 0 offen sc1\n\tbuffer_load_dword %1, %4, %6, 0 offen sc1\n\tbuffer_load_dword %2, %7, %6, 0 offen sc1\n\t"
                         "buffer_load_dword %0, %4, %6, 0 offen sc1\n\tbuffer_load_dword %0, %4, %6, 0 offen sc1\n\tbuffer_load_dword %0, %4, %6, 0 offen sc1\n\tbuffer_load_dword %0, %4, %6, 0 offen sc1"
                         : "=&v"(d0), "=&v"(aold), "=&v"(cfl), "=&v"(wm) : "v"(OOB), "v"((rdy & 2u) ? a_off(op + 1) + nt * 1024 : OOB), "s"(rd), "v"(r_off) : "memory");
            fin_loads(fp, fa0, fa1, fg, OOB, OOB, OOB, rd);
        }
        const int op0 = op; bool fin2 = false; int fpend = -1;
        for (;;) {
            const bool more = (rdy & 2u) != 0u;
            const unsigned an = more ? a_off(op + 1) : OOB;
            f32x4 acc0 = {0.f, 0.f, 0.f, 0.f}, acc1 = {0.f, 0.f, 0.f, 0.f};
            asm volatile("s_nop 1" : "+v"(acc0), "+v"(acc1));
#pragma unroll
            for (int q = 0; q < 8; ++q)
                asm volatile(
                    "s_waitcnt vmcnt(49)\n\tv_mfma_f32_16x16x32_bf16 %4, %6, %0, %4\n\tv_mfma_f32_16x16x32_bf16 %5, %7, %0, %5\n\tbuffer_load_dwordx4 %0, %14, %15, %16 offen sc1\n\t"
                    "s_waitcnt vmcnt(49)\n\tv_mfma_f32_16x16x32_bf16 %4, %8, %1, %4\n\tv_mfma_f32_16x16x32_bf16 %5, %9, %1, %5\n\tbuffer_load_dwordx4 %1, %14, %15, %16 offen offset:1024 sc1\n\t"
                    "s_waitcnt vmcnt(49)\n\tv_mfma_f32_16x16x32_bf16 %4, %10, %2, %4\n\tv_mfma_f32_16x16x32_bf16 %5, %11, %2, %5\n\tbuffer_load_dwordx4 %2, %14, %15, %16 offen offset:2048 sc1\n\t"
                    "s_waitcnt vmcnt(49)\n\tv_mfma_f32_16x16x32_bf16 %4, %12, %3, %4\n\tv_mfma_f32_16x16x32_bf16 %5, %13, %3, %5\n\tbuffer_load_dwordx4 %3, %14, %15, %16 offen offset:3072 sc1"
                    : "+v"(af[4 * q]), "+v"(af[4 * q + 1]), "+v"(af[4 * q + 2]), "+v"(af[4 * q + 3]), "+v"(acc0), "+v"(acc1)
                    : "a"(wof[4 * q][0]), "a"(wof[4 * q][1]), "a"(wof[4 * q + 1][0]), "a"(wof[4 * q + 1][1]), "a"(wof[4 * q + 2][0]), "a"(wof[4 * q + 2][1]), "a"(wof[4 * q + 3][0]), "a"(wof[4 * q + 3][1]),
                      "v"(an), "s"(rd), "s"(q * 4096) : "memory");
            const unsigned po = W_PART + (op * 4 + kq) * 262144 + (w * 32 + nt) * 2048 + l * 16;
            asm volatile("s_nop 7\n\tbuffer_store_dwordx4 %14, %16, %17, 0 offen sc1\n\tbuffer_store_dwordx4 %15, %16, %17, 0 offen offset:1024 sc1\n\ts_waitcnt vmcnt(34)"
                         : "+v"(aold), "+v"(cfl), "+v"(wm), "+v"(fp[0]), "+v"(fp[1]), "+v"(fp[2]), "+v"(fp[3]), "+v"(fp[4]), "+v"(fp[5]), "+v"(fp[6]), "+v"(fp[7]), "+v"(fa0), "+v"(fa1), "+v"(fg)
                         : "v"(acc0), "v"(acc1), "v"(po), "s"(rd) : "memory");
            const unsigned oldu = __builtin_amdgcn_readfirstlane(aold);
            rdy |= units(min16(cfl)) >> op;
            {
                const v2u o0 = mix(fp[0], fp[2], fp[4], fp[6], fa0, fg), o1 = mix(fp[1], fp[3], fp[5], fp[7], fa1, fg);
                const unsigned oo = fpend >= 0 ? (unsigned)(((fpend * 64 + 16 * w + n16) * 1024 + nt * 32 + 4 * g) * 2) : OOB;
                asm volatile("buffer_store_dwordx2 %0, %2, %3, 0 offen\n\tbuffer_store_dwordx2 %1, %2, %3, 0 offen offset:32" :: "v"(o0), "v"(o1), "v"(oo), "s"(ro) : "memory");
            }
            const bool fin = op >= op0 + 2 && oldu == 3u;
            if (!more) {
                if (!(rdy & 2u)) { fin2 = fin; break; }
                const unsigned a1 = a_off(op + 1);
#pragma unroll
                for (int q = 0; q < 8; ++q)
                    asm volatile("buffer_load_dwordx4 %0, %4, %5, %6 offen sc1\n\tbuffer_load_dwordx4 %1, %4, %5, %6 offen offset:1024 sc1\n\t"
                                 "buffer_load_dwordx4 %2, %4, %5, %6 offen offset:2048 sc1\n\tbuffer_load_dwordx4 %3, %4, %5, %6 offen offset:3072 sc1"
                                 : "+v"(af[4 * q]), "+v"(af[4 * q + 1]), "+v"(af[4 * q + 2]), "+v"(af[4 * q + 3]) : "v"(a1), "s"(rd), "s"(q * 4096) : "memory");
                asm volatile("buffer_load_dword %0, %1, %2, 0 offen sc1\n\tbuffer_load_dword %0, %1, %2, 0 offen sc1\n\tbuffer_load_dword %0, %1, %2, 0 offen sc1\n\tbuffer_load_dword %0, %1, %2, 0 offen sc1"
                             : "+v"(d0) : "v"(OOB), "s"(rd) : "memory");
            }
            {
                const unsigned tadr = op > op0 ? F_OP + ((op - 1) * 32 + nt) * 64 + w * 4 : F_DUM + (j * 4 + w) * 16;
                aold = 1u;
                asm volatile("s_mov_b64 exec, 1\n\tbuffer_atomic_add %0, %3, %6, 0 offen sc0\n\ts_mov_b64 exec, -1\n\t"
                             "buffer_load_dword %1, %4, %6, 0 offen sc1\n\tbuffer_load_dwordx4 %2, %5, %6, 0 offen sc1"
                             : "+v"(aold), "=&v"(cfl), "=&v"(wm) : "v"(tadr), "v"((rdy & 8u) ? OOB : r_off), "v"((rdy & 4u) ? a_off(op + 2) + nt * 1024 : OOB), "s"(rd) : "memory");
                fpend = fin ? op - 2 : -1;
                const int tok = (op - 2) * 64 + 16 * w + n16;
                fin_loads(fp, fa0, fa1, fg, fin ? W_PART + (op - 2) * 1048576 + (w * 32 + nt) * 2048 + l * 16 : OOB,
                          fin ? W_A + nt * 131072 + (tok & 31) * 4096 + (tok >> 5) * 64 + g * 8 : OOB, fin ? W_GATE + tok * 4 : OOB, rd);
            }
            if (op == 15) BSTAMP(5);
            rdy >>= 1; ++op;
        }
        asm volatile("s_waitcnt vmcnt(0)" : "+v"(af[0]), "+v"(af[1]), "+v"(af[2]), "+v"(af[3]), "+v"(af[4]), "+v"(af[5]), "+v"(af[6]), "+v"(af[7]), "+v"(af[8]), "+v"(af[9]), "+v"(af[10]), "+v"(af[11]),
                     "+v"(af[12]), "+v"(af[13]), "+v"(af[14]), "+v"(af[15]), "+v"(d0) :: "memory");
        asm volatile("" : "+v"(af[16]), "+v"(af[17]), "+v"(af[18]), "+v"(af[19]), "+v"(af[20]), "+v"(af[21]), "+v"(af[22]), "+v"(af[23]), "+v"(af[24]), "+v"(af[25]), "+v"(af[26]), "+v"(af[27]),
                     "+v"(af[28]), "+v"(af[29]), "+v"(af[30]), "+v"(af[31]) :: "memory");
        unsigned o1 = 0u, o2 = 0u;
        if (l == 0) { o2 = __hip_atomic_fetch_add((unsigned*)(p.ws + F_OP + (op * 32 + nt) * 64 + w * 4), 1u, __ATOMIC_RELAXED, __HIP_MEMORY_SCOPE_AGENT);
            if (op > op0) o1 = __hip_atomic_fetch_add((unsigned*)(p.ws + F_OP + ((op - 1) * 32 + nt) * 64 + w * 4), 1u, __ATOMIC_RELAXED, __HIP_MEMORY_SCOPE_AGENT); }
        {
            const unsigned now = rtnow();
            prog = min16(ld4(r, r_off)); tseen = now; known = units(prog);
        }
        if (fin2) finish(op - 2);
        if (__builtin_amdgcn_readfirstlane(o1) == 3u) finish(op - 1);
        if (__builtin_amdgcn_readfirstlane(o2) == 3u) finish(op);
        if (op == 15) BSTAMP(5);
        EV(192 + op);
        ++op;
    }
    BSTAMP(7);
}

__device__ __forceinline__ void rec_block(const Params& p, rsrc_t r, unsigned char* lds, const int wv, const int b) {
    const int xq = (b & 7) >> 1, t = (b >> 3) * 2 + (b & 1);
    const unsigned lb = __builtin_amdgcn_readfirstlane((unsigned)(size_t)lds);
    int kind, id, rg = 0, rot = 0, bq = 99;
    if (RPL == 0) { kind = t < 8 ? 0 : t < 24 ? 1 : 2; id = t < 8 ? xq * 8 + t : t < 24 ? (xq & 1) * 16 + t - 8 : xq * 8 + t - 24; rg = xq >> 1; }
    else if (RPL == 1) { kind = xq == 0 ? 0 : xq < 3 ? 1 : 2; id = t; rg = xq - 1; }
    else if (RPL == 2) { const int na = xq == 2 ? 10 : 11, q = xq * 21 + t - na; kind = xq == 3 ? 2 : t < na ? 0 : 1; id = xq == 3 ? t : t < na ? xq * 11 + t : q >> 1; rg = q & 1; }
    else { const int na = xq == 2 ? 12 : 10, q = t - na; kind = xq == 3 ? 2 : t < na ? 0 : 1; rg = xq < 2 ? xq : q & 1; bq = q;
        const int c2 = q >> 1, cs = xq < 2 ? (q < 6 ? q : q < 20 ? q + 2 : q + 4) : (c2 < 2 ? 6 + c2 : c2 < 4 ? 20 + c2 : 22 + c2);
        id = xq == 3 ? t : t < na ? xq * 10 + t : cs; rot = !ROT ? 0 : xq < 2 ? (cs & 1) * 4 : ((cs & 1) * 2 + rg) * 2; }
    if (kind == 0) rec_a(p, lb, wv, id, xq);
    else if (kind == 1) rec_b(p, lb, wv, id, rg, xq, rot, bq);
    else {
        const int hi = id, j = hi - 32; (void)j;
        BSTAMP(0);
        rec_helper(p, r, lds, wv, hi);
        BSTAMP(6);
        gate_block(p, r, hi, wv);
        BSTAMP(2);
        conv_block(p, r, lds, wv, hi, j);
        BSTAMP(3);
    }
}

__global__ void __launch_bounds__(256, 1) k_main(Params p) {
    extern __shared__ __attribute__((aligned(16))) unsigned char lds[];
    rsrc_t r = __builtin_amdgcn_make_buffer_rsrc(p.ws, 0, W_TOTAL, 0x00020000);
    const int wv = __builtin_amdgcn_readfirstlane(threadIdx.x >> 6);
    int role = blockIdx.x;
    if (threadIdx.x == 0) {
        unsigned xcc; asm volatile("s_getreg_b32 %0, hwreg(HW_REG_XCC_ID)" : "=s"(xcc)); xcc &= 7u;
        unsigned* tk = (unsigned*)(p.ws + F_TKT);
        unsigned t = __hip_atomic_fetch_add(tk + xcc * 16, 1u, __ATOMIC_RELAXED, __HIP_MEMORY_SCOPE_AGENT);
        if (t >= 32u) {
            unsigned o = __hip_atomic_fetch_add(tk + 128, 1u, __ATOMIC_RELAXED, __HIP_MEMORY_SCOPE_AGENT);
            for (unsigned spin = 0; spin < (1u << 22); ++spin) { unsigned tot = 0u;
                for (int x = 0; x < 8; ++x) tot += __hip_atomic_load(tk + x * 16, __ATOMIC_RELAXED, __HIP_MEMORY_SCOPE_AGENT);
                if (tot >= 256u) break; }
            for (int x = 0; x < 8; ++x) { const unsigned cnt = __hip_atomic_load(tk + x * 16, __ATOMIC_RELAXED, __HIP_MEMORY_SCOPE_AGENT), fr = cnt < 32u ? 32u - cnt : 0u;
                if (o < fr) { xcc = (unsigned)x; t = cnt + o; break; }
                o -= fr; }
        }
        *(volatile int*)lds = (int)((xcc >> 2) * 128u + (t >> 1) * 8u + (xcc & 3u) * 2u + (t & 1u));
    }
    __syncthreads();
    role = __builtin_amdgcn_readfirstlane(*(volatile int*)lds) & 255;
    __syncthreads();
    if (role >= 128) bulk_block(p, r, lds, wv, role - 128); else rec_block(p, r, lds, wv, role);
}

extern "C" void kernel_launch(void* const* d_in, const int* in_sizes, int n_in, void* d_out, int out_size, void* d_ws, size_t ws_size, hipStream_t stream) {
    Params p;
    p.Aq = (const bf16_t*)d_in[0]; p.Ak = (const bf16_t*)d_in[1]; p.Av = (const bf16_t*)d_in[2];
    p.shift_k = (const bf16_t*)d_in[3]; p.ssm_k = (const bf16_t*)d_in[4]; p.ssm_D = (const bf16_t*)d_in[5];
    p.mem0 = (const bf16_t*)d_in[7]; p.Wq = (const bf16_t*)d_in[8]; p.Wk = (const bf16_t*)d_in[9];
    p.Wv = (const bf16_t*)d_in[10]; p.Wo = (const bf16_t*)d_in[11]; p.gate_w = (const bf16_t*)d_in[13];
    p.out = (bf16_t*)d_out; p.ws = (unsigned char*)d_ws;
    unsigned char* ws = (unsigned char*)d_ws;
    (void)hipMemsetAsync(ws, 0, FLG_BYTES, stream);
    (void)hipFuncSetAttribute((const void*)k_main, hipFuncAttributeMaxDynamicSharedMemorySize, L_TOTAL);
    k_main<<<256, 256, L_TOTAL, stream>>>(p);
}
```

```cpp
#include <hip/hip_runtime.h>
#include <stdint.h>
#include <type_traits>
typedef unsigned short bf16_t;
using bf16x8 = __attribute__((ext_vector_type(8))) short;
using f32x4 = __attribute__((ext_vector_type(4))) float;
typedef unsigned v4u __attribute__((ext_vector_type(4)));
typedef unsigned v2u __attribute__((ext_vector_type(2)));
typedef __amdgpu_buffer_rsrc_t rsrc_t;

__device__ __forceinline__ float bf2f(bf16_t b) { return __uint_as_float(((unsigned)b) << 16); }
typedef __bf16 hbf2 __attribute__((ext_vector_type(2)));
typedef float f32x2 __attribute__((ext_vector_type(2)));
__device__ __forceinline__ unsigned pack2(float a, float b) { f32x2 v = {a, b}; return __builtin_bit_cast(unsigned, __builtin_convertvector(v, hbf2)); }
__device__ __forceinline__ float rbfh(float f) { return __uint_as_float(pack2(f, 0.f) << 16); }
__device__ __forceinline__ unsigned nosent(unsigned x) {
    typedef unsigned short us2 __attribute__((ext_vector_type(2)));
    const us2 a = __builtin_bit_cast(us2, x), c = {(unsigned short)0xFFFE, (unsigned short)0xFFFE}; return __builtin_bit_cast(unsigned, __builtin_elementwise_min(a, c)); }

constexpr int S = 2048, D = 1024, H = 32, DH = 128, M = 8, SEG = 32, NSEG = 64;

constexpr unsigned W_FLG = 0;
constexpr unsigned F_KVQ = W_FLG;
constexpr unsigned F_CONV = W_FLG + 32768;
constexpr unsigned F_ERR = W_FLG + 32768 + 256;
constexpr unsigned F_PREP = W_FLG + 32768 + 512;
constexpr unsigned F_CAL = W_FLG + 33792;
constexpr unsigned F_TKT = W_FLG + 34816;
constexpr unsigned F_PROG = W_FLG + 49152;
constexpr unsigned F_DUM = W_FLG + 53248;
constexpr unsigned F_OP = W_FLG + 65536;
constexpr unsigned F_ATT = W_FLG + 131072;
constexpr unsigned F_RDY = W_FLG + 147456;
constexpr unsigned FLG_BYTES = 180224;
constexpr unsigned HO_KVM = W_FLG + FLG_BYTES;
constexpr unsigned W_A = HO_KVM + 64 * 32 * 4096;
constexpr unsigned W_GATE = W_A + S * D * 2;
constexpr unsigned W_QX = W_GATE + 8192;
constexpr unsigned W_KX = W_QX + S * 4096 * 2;
constexpr unsigned W_VF = W_KX + S * 4096 * 2;
constexpr unsigned W_OX = W_VF + 64 * 32 * 8192;
constexpr unsigned W_PART = W_OX + S * 4096 * 2;
constexpr unsigned W_DBG = W_PART + 32 * 4 * 64 * 1024 * 4;
constexpr unsigned W_U = W_DBG + 256 * 64 * 16 * 8;
constexpr unsigned W_TOTAL = W_U + S * D * 2;

__device__ __forceinline__ v4u ld16(rsrc_t r, unsigned off) { return __builtin_amdgcn_raw_buffer_load_b128(r, off, 0, 16); }
__device__ __forceinline__ v2u ld8(rsrc_t r, unsigned off) { return __builtin_amdgcn_raw_buffer_load_b64(r, off, 0, 16); }
__device__ __forceinline__ unsigned ld4(rsrc_t r, unsigned off) { return __builtin_amdgcn_raw_buffer_load_b32(r, off, 0, 16); }
__device__ __forceinline__ void st16(rsrc_t r, unsigned off, v4u v) { __builtin_amdgcn_raw_buffer_store_b128(v, r, off, 0, 16); }
__device__ __forceinline__ void st8(rsrc_t r, unsigned off, v2u v) { __builtin_amdgcn_raw_buffer_store_b64(v, r, off, 0, 16); }
__device__ __forceinline__ void st4(rsrc_t r, unsigned off, unsigned v) { __builtin_amdgcn_raw_buffer_store_b32(v, r, off, 0, 16); }
__device__ __forceinline__ bool okdw(unsigned x) { return ((x & 0xFFFFu) != 0xFFFFu) & ((x >> 16) != 0xFFFFu); }
typedef unsigned short us2_t __attribute__((ext_vector_type(2)));
__device__ __forceinline__ unsigned pkmax(unsigned a, unsigned b) { return __builtin_bit_cast(unsigned, __builtin_elementwise_max(__builtin_bit_cast(us2_t, a), __builtin_bit_cast(us2_t, b))); }
__device__ __forceinline__ unsigned pkmax4(v4u v) { return pkmax(pkmax(v.x, v.y), pkmax(v.z, v.w)); }
__device__ __forceinline__ bool ok16(v4u v) { return okdw(pkmax4(v)); }
__device__ __forceinline__ bool ok8(v2u v) { return okdw(v.x) & okdw(v.y); }
__device__ __forceinline__ bf16x8 as_frag(v4u v) { return __builtin_bit_cast(bf16x8, v); }
#define MFMA(a, b, c) __builtin_amdgcn_mfma_f32_16x16x32_bf16((a), (b), (c), 0, 0, 0)
#define D1 142
#define D2 94
#define D3 76
#define LT_A 4
#define LB0 12
#define LC0 20
#define LA0 24
#define TR_UP 4
#define TR_DN 2
#define L_DEC 3
#define WARM1 0
#define KROT 0
#define STAG 1
#define UP_SH 2
#define EPI_SLP 0
#define SITA 1
#define PAIRX 4
#define DUE_P 1
#define LATE 1
#define EARLY_FROM 56
#define SKEW 0
#define OUT_WARM 1
#define ADAPT 1
#define RPL 0
#define CV_POL 2
#define POLL_SLP 4
#define TOUCH 1
#define ROT 0
#define CODE_T 1
#define CAL_T 450u
#define GTOUCH 0
#define XTOUCH 32
#define XWAIT 1
#define PEN 60
#define MEMBAR() asm volatile("" ::: "memory")
#define FT(i) do { } while (0)
#define ENDPGM() asm volatile("s_endpgm" ::: "memory")
#define DRAIN() asm volatile("s_waitcnt vmcnt(0)" ::: "memory")

__device__ __forceinline__ float xor16_max(float x) { auto r = __builtin_amdgcn_permlane16_swap(__float_as_uint(x), __float_as_uint(x), false, false); return fmaxf(__uint_as_float(r[0]), __uint_as_float(r[1])); }
__device__ __forceinline__ float xor32_max(float x) { auto r = __builtin_amdgcn_permlane32_swap(__float_as_uint(x), __float_as_uint(x), false, false); return fmaxf(__uint_as_float(r[0]), __uint_as_float(r[1])); }
__device__ __forceinline__ float xor16_add(float x) { auto r = __builtin_amdgcn_permlane16_swap(__float_as_uint(x), __float_as_uint(x), false, false); return __uint_as_float(r[0]) + __uint_as_float(r[1]); }
__device__ __forceinline__ float xor32_add(float x) { auto r = __builtin_amdgcn_permlane32_swap(__float_as_uint(x), __float_as_uint(x), false, false); return __uint_as_float(r[0]) + __uint_as_float(r[1]); }
__device__ __forceinline__ v2u tr_read(unsigned lds_addr) { v2u r; asm volatile("ds_read_b64_tr_b16 %0, %1\n\ts_waitcnt lgkmcnt(0)" : "=&v"(r) : "v"(lds_addr) : "memory"); return r; }

typedef short v4i16_t __attribute__((ext_vector_type(4)));
__device__ __forceinline__ v2u trb(unsigned lds_addr) { return __builtin_bit_cast(v2u, __builtin_amdgcn_ds_read_tr16_b64_v4i16((__attribute__((address_space(3))) v4i16_t*)(size_t)lds_addr)); }
__device__ __forceinline__ int otid_(int wv) { int t; asm volatile("v_mbcnt_lo_u32_b32 %0, -1, 0\n\tv_mbcnt_hi_u32_b32 %0, -1, %0" : "=v"(t)); return wv * 64 + t; }
#define otid() otid_(wv)
__device__ __forceinline__ bool wait_word(rsrc_t r, unsigned off, unsigned want, unsigned code) {
    for (unsigned spin = 0;; ++spin) { MEMBAR();
        const unsigned v = __builtin_amdgcn_readfirstlane(ld4(r, off));
        if (v >= want) return true;
        if (spin > (1u << 20)) { st4(r, F_ERR, code); return false; }
        __builtin_amdgcn_s_sleep(8); }
}

__device__ __forceinline__ unsigned rtnow() { return (unsigned)__builtin_amdgcn_s_memrealtime(); }
__device__ __forceinline__ void wait_until(unsigned target) { for (int i = 0; i < 4096; ++i) { if ((int)(rtnow() - target) >= 0) break; } }
__device__ __forceinline__ void wait_long(unsigned target) { for (int i = 0; i < 4096; ++i) { if ((int)(rtnow() - target) >= 0) break; __builtin_amdgcn_s_sleep(8); } }
constexpr int RED_G = 320;
constexpr int RED_T = 4 * RED_G;
constexpr int L_RED = 0;
constexpr int L_KX = 65536;
constexpr int KXP = 272;
constexpr int L_VF = L_KX + 2 * 32 * KXP;
constexpr int L_QKV = L_VF + 16384;
constexpr int L_OFF = L_QKV + 6144;
constexpr int L_STMP = L_OFF + 256;
constexpr int L_TOTAL = 163840;
constexpr unsigned U_A = 0, U_S = 32, U_B = 36, U_INIT = 113, U_C = 128, U_END = 512;
#define PLACE 1
#define CAL_N 5
#define STEP_MIN 200u

struct Params {
    const bf16_t *Aq, *Ak, *Av, *shift_k, *ssm_k, *ssm_D, *mem0, *Wq, *Wk, *Wv, *Wo, *gate_w;
    bf16_t* out; unsigned char* ws;
};
__host__ __device__ __forceinline__ unsigned hx_base(const void* ws) { const size_t a = (size_t)ws; return (unsigned)((((a + W_TOTAL + 0x7FFFFFu) >> 23) << 23) - a); }

#define STAMP(i) do { } while (0)
#define BSTAMP(i) do { } while (0)
#define EV(code) do { } while (0)
#define SPINS(i) do { } while (0)
typedef short v4s __attribute__((ext_vector_type(4)));
typedef unsigned v32u __attribute__((ext_vector_type(32)));
typedef unsigned v16u __attribute__((ext_vector_type(16)));
static_assert(L_VF - L_KX == 16384 + 1024, "fragment read offsets");
#define MFMA16(a, b, c) __builtin_amdgcn_mfma_f32_16x16x16bf16_1k(__builtin_bit_cast(v4s, (a)), __builtin_bit_cast(v4s, (b)), (c), 0, 0, 0)
__device__ __forceinline__ unsigned ror8(unsigned x) { return (unsigned)__builtin_amdgcn_update_dpp(0, (int)x, 0x128, 0xF, 0xF, false); }
__device__ __forceinline__ unsigned qx1(unsigned x) { return (unsigned)__builtin_amdgcn_update_dpp(0, (int)x, 0xB1, 0xF, 0xF, false); }
__device__ __forceinline__ unsigned wave_pkmax(unsigned d) {
    d = pkmax(d, (unsigned)__builtin_amdgcn_update_dpp((int)d, (int)d, 0xB1, 0xF, 0xF, false)); d = pkmax(d, (unsigned)__builtin_amdgcn_update_dpp((int)d, (int)d, 0x4E, 0xF, 0xF, false));
    d = pkmax(d, (unsigned)__builtin_amdgcn_update_dpp((int)d, (int)d, 0x124, 0xF, 0xF, false)); d = pkmax(d, (unsigned)__builtin_amdgcn_update_dpp((int)d, (int)d, 0x128, 0xF, 0xF, false));
    { auto q = __builtin_amdgcn_permlane16_swap(d, d, false, false); d = pkmax(q[0], q[1]); }
    { auto q = __builtin_amdgcn_permlane32_swap(d, d, false, false); d = pkmax(q[0], q[1]); }
    return d; }
__device__ __forceinline__ int wave_max(int d) {
    d = max(d, __builtin_amdgcn_update_dpp(d, d, 0xB1, 0xF, 0xF, false)); d = max(d, __builtin_amdgcn_update_dpp(d, d, 0x4E, 0xF, 0xF, false));
    d = max(d, __builtin_amdgcn_update_dpp(d, d, 0x124, 0xF, 0xF, false)); d = max(d, __builtin_amdgcn_update_dpp(d, d, 0x128, 0xF, 0xF, false));
    { auto q = __builtin_amdgcn_permlane16_swap((unsigned)d, (unsigned)d, false, false); d = max((int)q[0], (int)q[1]); }
    { auto q = __builtin_amdgcn_permlane32_swap((unsigned)d, (unsigned)d, false, false); d = max((int)q[0], (int)q[1]); }
    return d; }

__device__ __forceinline__ v4u mk_srd(const void* p, unsigned bytes) { const size_t a = (size_t)p; v4u d;
    d.x = __builtin_amdgcn_readfirstlane((unsigned)a); d.y = __builtin_amdgcn_readfirstlane((unsigned)(a >> 32) & 0xFFFFu); d.z = __builtin_amdgcn_readfirstlane(bytes); d.w = 0x00020000u; return d; }
__device__ __forceinline__ bf16x8 ldsa(unsigned addr) { return *(const __attribute__((address_space(3))) bf16x8*)(size_t)addr; }
constexpr unsigned HX_BYTES = 2097152, OOB = 0xF0000000u;
constexpr unsigned F_RDY0 = W_FLG + 34304;
constexpr unsigned F_GO = W_FLG + 36864;
constexpr int L_KV = 90112, L_MS = 139264, L_IMG = 155648, IMG_PS = 1056, IMG_MS = 2112, LB_R = 8192, LB_STG = 16384, LB_STGP = 8704;
static_assert(F_KVQ == 0 && F_ERR == 33024 && F_CAL == 33792 && F_RDY0 == 34304 && F_GO == 36864 && F_PROG == 49152 && W_QX == 12771328 && W_KX == 29548544 && W_OX == 63102976 && W_VF == 46325760 && W_DBG == 113434624 && L_KV == 90112 && L_MS == 139264 && L_IMG == 155648 && IMG_PS == 1056 && IMG_MS == 2112 && LB_R == 8192 && LB_STG == 16384 && LB_STGP == 8704, "constants of the generated code");
#define REC_S_IN "{s[8:11]}"(sh), "{s[12:15]}"(sw), "{s[16:19]}"(s1), "{s[20:23]}"(s2), "{s29}"(D1), "{s30}"(D2), "{s43}"(w), "{s44}"(id), "{s49}"(rb), "{s50}"(PEN), "{s51}"(LT_A), "{s45}"(F_GO + (cp * 4 + w) * 128)
__device__ __forceinline__ void rec_a(const Params& p, unsigned lb, const int wv, const int id, const int cp) {
    const int w = wv, l = otid() & 63, n = l & 15, g = l >> 4, h = id, rb = id;
    const v4u sh = mk_srd(p.ws + hx_base(p.ws), HX_BYTES - 8192), sw = mk_srd(p.ws, W_TOTAL), s1 = mk_srd(w < 2 ? p.Wq : p.Wk, 1024 * 4096 * 2), s2 = s1;
    const int r8 = l >> 3, pc = l & 7, pp = n >> 3, bb = (n >> 2) & 1, f = (g & 1) | (pp << 1);
    const unsigned stg = lb + 131072 + w * 8192;
    const unsigned g_w = r8 * 8192 + h * 256 + (w & 1) * 128 + pc * 16;
    const unsigned wb0 = stg + r8 * 128 + (((pc >> 1) ^ (r8 >> 2)) << 5) + (pc & 1) * 16, wb1 = wb0 ^ 64u;
    const unsigned rb0 = stg + (16 * (g >> 1) + 8 * pp + 4 * (g & 1) + bb) * 128 + (f << 5) + (n & 3) * 8;
    const unsigned a_mem = cp * 32768 + (w >> 1) * 16384 + (w & 1) * 4096 + (g >> 1) * 512 + ((n & 7) >> 2) * 256 + ((g & 1) * 2 + pp) * 64 + (n & 3) * 16;
    const unsigned l_ms = lb + L_MS + l * 16;
    const unsigned l_iw = lb + L_IMG + (w >> 1) * IMG_MS + (g & 1) * IMG_PS + (64 * (w & 1) + 16 * (g >> 1) + n) * 8;
    const unsigned l_iq = lb + L_IMG + (n & 1) * IMG_PS + (8 * g + (n >> 2)) * 8;
    const unsigned a_vm = (16 + (h >> 2)) * 16384 + (h & 3) * 2048 + w * 512 + n * 32 + (g & 1) * 16;
    const unsigned a_o = n < 8 ? (8 + (n >> 2) * 4 + (h >> 3)) * 16384 + (h & 7) * 1024 + w * 256 + g * 64 + (n & 3) * 16 : OOB;
    const unsigned a_ox = (((w & 1) * 32 + h) * 4 + w) * 1024 + l * 16;
    asm volatile(
        "s_lshl_b32 s34, s43, 2\n\t"
        "s_add_u32 s34, s34, 0\n\t"
        "s_and_b32 s34, s34, 15\n\t"
        "s_lshl_b32 s34, s34, 19\n\t"
        "buffer_load_dwordx4 v[224:227], v96, s[16:19], s34 offen nt\n\t"
        "s_add_u32 s34, s34, 0x10000\n\t"
        "buffer_load_dwordx4 v[228:231], v96, s[16:19], s34 offen nt\n\t"
        "s_add_u32 s34, s34, 0x10000\n\t"
        "buffer_load_dwordx4 v[232:235], v96, s[16:19], s34 offen nt\n\t"
        "s_add_u32 s34, s34, 0x10000\n\t"
        "buffer_load_dwordx4 v[236:239], v96, s[16:19], s34 offen nt\n\t"
        "s_add_u32 s34, s34, 0x10000\n\t"
        "buffer_load_dwordx4 v[240:243], v96, s[16:19], s34 offen nt\n\t"
        "s_add_u32 s34, s34, 0x10000\n\t"
        "buffer_load_dwordx4 v[244:247], v96, s[16:19], s34 offen nt\n\t"
        "s_add_u32 s34, s34, 0x10000\n\t"
        "buffer_load_dwordx4 v[248:251], v96, s[16:19], s34 offen nt\n\t"
        "s_add_u32 s34, s34, 0x10000\n\t"
        "buffer_load_dwordx4 v[252:255], v96, s[16:19], s34 offen nt\n\t"
        "s_lshl_b32 s34, s43, 2\n\t"
        "s_add_u32 s34, s34, 1\n\t"
        "s_and_b32 s34, s34, 15\n\t"
        "s_lshl_b32 s34, s34, 19\n\t"
        "buffer_load_dwordx4 v[0:3], v96, s[16:19], s34 offen nt\n\t"
        "s_add_u32 s34, s34, 0x10000\n\t"
        "buffer_load_dwordx4 v[4:7], v96, s[16:19], s34 offen nt\n\t"
        "s_add_u32 s34, s34, 0x10000\n\t"
        "buffer_load_dwordx4 v[8:11], v96, s[16:19], s34 offen nt\n\t"
        "s_add_u32 s34, s34, 0x10000\n\t"
        "buffer_load_dwordx4 v[12:15], v96, s[16:19], s34 offen nt\n\t"
        "s_add_u32 s34, s34, 0x10000\n\t"
        "buffer_load_dwordx4 v[16:19], v96, s[16:19], s34 offen nt\n\t"
        "s_add_u32 s34, s34, 0x10000\n\t"
        "buffer_load_dwordx4 v[20:23], v96, s[16:19], s34 offen nt\n\t"
        "s_add_u32 s34, s34, 0x10000\n\t"
        "buffer_load_dwordx4 v[24:27], v96, s[16:19], s34 offen nt\n\t"
        "s_add_u32 s34, s34, 0x10000\n\t"
        "buffer_load_dwordx4 v[28:31], v96, s[16:19], s34 offen nt\n\t"
        "s_lshl_b32 s34, s43, 2\n\t"
        "s_add_u32 s34, s34, 2\n\t"
        "s_and_b32 s34, s34, 15\n\t"
        "s_lshl_b32 s34, s34, 19\n\t"
        "buffer_load_dwordx4 v[32:35], v96, s[16:19], s34 offen nt\n\t"
        "s_add_u32 s34, s34, 0x10000\n\t"
        "buffer_load_dwordx4 v[36:39], v96, s[16:19], s34 offen nt\n\t"
        "s_add_u32 s34, s34, 0x10000\n\t"
        "buffer_load_dwordx4 v[40:43], v96, s[16:19], s34 offen nt\n\t"
        "s_add_u32 s34, s34, 0x10000\n\t"
        "buffer_load_dwordx4 v[44:47], v96, s[16:19], s34 offen nt\n\t"
        "s_add_u32 s34, s34, 0x10000\n\t"
        "buffer_load_dwordx4 v[48:51], v96, s[16:19], s34 offen nt\n\t"
        "s_add_u32 s34, s34, 0x10000\n\t"
        "buffer_load_dwordx4 v[52:55], v96, s[16:19], s34 offen nt\n\t"
        "s_add_u32 s34, s34, 0x10000\n\t"
        "buffer_load_dwordx4 v[56:59], v96, s[16:19], s34 offen nt\n\t"
        "s_add_u32 s34, s34, 0x10000\n\t"
        "buffer_load_dwordx4 v[60:63], v96, s[16:19], s34 offen nt\n\t"
        "v_cmp_le_i32_e64 s[60:61], 0, v126\n\t"
        "v_cmp_le_i32_e64 s[62:63], 1, v126\n\t"
        "v_cmp_le_i32_e64 s[64:65], 2, v126\n\t"
        "v_cmp_le_i32_e64 s[66:67], 3, v126\n\t"
        "v_cmp_le_i32_e64 s[68:69], 16, v126\n\t"
        "v_cmp_le_i32_e64 s[70:71], 17, v126\n\t"
        "v_cmp_le_i32_e64 s[72:73], 18, v126\n\t"
        "v_cmp_le_i32_e64 s[74:75], 19, v126\n\t"
        "s_mul_i32 s35, s43, 10240\n\t"
        "v_add_u32_e32 v126, s35, v124\n\t"
        "s_waitcnt vmcnt(16)\n\t"
        "ds_write_b128 v97, v[224:227] offset:0\n\t"
        "ds_write_b128 v98, v[228:231] offset:1024\n\t"
        "ds_write_b128 v97, v[232:235] offset:2048\n\t"
        "ds_write_b128 v98, v[236:239] offset:3072\n\t"
        "ds_write_b128 v97, v[240:243] offset:4096\n\t"
        "ds_write_b128 v98, v[244:247] offset:5120\n\t"
        "ds_write_b128 v97, v[248:251] offset:6144\n\t"
        "ds_write_b128 v98, v[252:255] offset:7168\n\t"
        "ds_read_b64_tr_b16 a[0:1], v99 offset:0\n\t"
        "ds_read_b64_tr_b16 a[2:3], v99 offset:256\n\t"
        "ds_read_b64_tr_b16 a[4:5], v99 offset:4096\n\t"
        "ds_read_b64_tr_b16 a[6:7], v99 offset:4352\n\t"
        "s_waitcnt lgkmcnt(8)\n\t"
        "ds_read_b64_tr_b16 a[8:9], v100 offset:0\n\t"
        "ds_read_b64_tr_b16 a[10:11], v100 offset:256\n\t"
        "ds_read_b64_tr_b16 a[12:13], v100 offset:4096\n\t"
        "ds_read_b64_tr_b16 a[14:15], v100 offset:4352\n\t"
        "s_waitcnt lgkmcnt(8)\n\t"
        "ds_read_b64_tr_b16 a[16:17], v101 offset:0\n\t"
        "ds_read_b64_tr_b16 a[18:19], v101 offset:256\n\t"
        "ds_read_b64_tr_b16 a[20:21], v101 offset:4096\n\t"
        "ds_read_b64_tr_b16 a[22:23], v101 offset:4352\n\t"
        "s_waitcnt lgkmcnt(8)\n\t"
        "ds_read_b64_tr_b16 v[104:105], v102 offset:0\n\t"
        "ds_read_b64_tr_b16 v[106:107], v102 offset:256\n\t"
        "ds_read_b64_tr_b16 v[108:109], v102 offset:4096\n\t"
        "ds_read_b64_tr_b16 v[110:111], v102 offset:4352\n\t"
        "s_lshl_b32 s34, s43, 2\n\t"
        "s_add_u32 s34, s34, 3\n\t"
        "s_and_b32 s34, s34, 15\n\t"
        "s_lshl_b32 s34, s34, 19\n\t"
        "buffer_load_dwordx4 v[64:67], v96, s[16:19], s34 offen nt\n\t"
        "s_add_u32 s34, s34, 0x10000\n\t"
        "buffer_load_dwordx4 v[68:71], v96, s[16:19], s34 offen nt\n\t"
        "s_add_u32 s34, s34, 0x10000\n\t"
        "buffer_load_dwordx4 v[72:75], v96, s[16:19], s34 offen nt\n\t"
        "s_add_u32 s34, s34, 0x10000\n\t"
        "buffer_load_dwordx4 v[76:79], v96, s[16:19], s34 offen nt\n\t"
        "s_add_u32 s34, s34, 0x10000\n\t"
        "buffer_load_dwordx4 v[80:83], v96, s[16:19], s34 offen nt\n\t"
        "s_add_u32 s34, s34, 0x10000\n\t"
        "buffer_load_dwordx4 v[84:87], v96, s[16:19], s34 offen nt\n\t"
        "s_add_u32 s34, s34, 0x10000\n\t"
        "buffer_load_dwordx4 v[88:91], v96, s[16:19], s34 offen nt\n\t"
        "s_add_u32 s34, s34, 0x10000\n\t"
        "buffer_load_dwordx4 v[92:95], v96, s[16:19], s34 offen nt\n\t"
        "s_waitcnt lgkmcnt(0)\n\t"
        "ds_write_b128 v117, v[104:107] offset:0\n\t"
        "ds_write_b128 v117, v[108:111] offset:1024\n\t"
        "s_waitcnt vmcnt(16)\n\t"
        "ds_write_b128 v97, v[0:3] offset:0\n\t"
        "ds_write_b128 v98, v[4:7] offset:1024\n\t"
        "ds_write_b128 v97, v[8:11] offset:2048\n\t"
        "ds_write_b128 v98, v[12:15] offset:3072\n\t"
        "ds_write_b128 v97, v[16:19] offset:4096\n\t"
        "ds_write_b128 v98, v[20:23] offset:5120\n\t"
        "ds_write_b128 v97, v[24:27] offset:6144\n\t"
        "ds_write_b128 v98, v[28:31] offset:7168\n\t"
        "ds_read_b64_tr_b16 a[24:25], v99 offset:0\n\t"
        "ds_read_b64_tr_b16 a[26:27], v99 offset:256\n\t"
        "ds_read_b64_tr_b16 a[28:29], v99 offset:4096\n\t"
        "ds_read_b64_tr_b16 a[30:31], v99 offset:4352\n\t"
        "s_waitcnt lgkmcnt(8)\n\t"
        "ds_read_b64_tr_b16 a[32:33], v100 offset:0\n\t"
        "ds_read_b64_tr_b16 a[34:35], v100 offset:256\n\t"
        "ds_read_b64_tr_b16 a[36:37], v100 offset:4096\n\t"
        "ds_read_b64_tr_b16 a[38:39], v100 offset:4352\n\t"
        "s_waitcnt lgkmcnt(8)\n\t"
        "ds_read_b64_tr_b16 a[40:41], v101 offset:0\n\t"
        "ds_read_b64_tr_b16 a[42:43], v101 offset:256\n\t"
        "ds_read_b64_tr_b16 a[44:45], v101 offset:4096\n\t"
        "ds_read_b64_tr_b16 a[46:47], v101 offset:4352\n\t"
        "s_waitcnt lgkmcnt(8)\n\t"
        "ds_read_b64_tr_b16 v[104:105], v102 offset:0\n\t"
        "ds_read_b64_tr_b16 v[106:107], v102 offset:256\n\t"
        "ds_read_b64_tr_b16 v[108:109], v102 offset:4096\n\t"
        "ds_read_b64_tr_b16 v[110:111], v102 offset:4352\n\t"
        "s_lshl_b32 s34, s43, 2\n\t"
        "s_add_u32 s34, s34, 4\n\t"
        "s_and_b32 s34, s34, 15\n\t"
        "s_lshl_b32 s34, s34, 19\n\t"
        "buffer_load_dwordx4 a[96:99], v96, s[16:19], s34 offen nt\n\t"
        "s_add_u32 s34, s34, 0x10000\n\t"
        "buffer_load_dwordx4 a[100:103], v96, s[16:19], s34 offen nt\n\t"
        "s_add_u32 s34, s34, 0x10000\n\t"
        "buffer_load_dwordx4 a[104:107], v96, s[16:19], s34 offen nt\n\t"
        "s_add_u32 s34, s34, 0x10000\n\t"
        "buffer_load_dwordx4 a[108:111], v96, s[16:19], s34 offen nt\n\t"
        "s_add_u32 s34, s34, 0x10000\n\t"
        "buffer_load_dwordx4 a[112:115], v96, s[16:19], s34 offen nt\n\t"
        "s_add_u32 s34, s34, 0x10000\n\t"
        "buffer_load_dwordx4 a[116:119], v96, s[16:19], s34 offen nt\n\t"
        "s_add_u32 s34, s34, 0x10000\n\t"
        "buffer_load_dwordx4 a[120:123], v96, s[16:19], s34 offen nt\n\t"
        "s_add_u32 s34, s34, 0x10000\n\t"
        "buffer_load_dwordx4 a[124:127], v96, s[16:19], s34 offen nt\n\t"
        "s_waitcnt lgkmcnt(0)\n\t"
        "ds_write_b128 v117, v[104:107] offset:2048\n\t"
        "ds_write_b128 v117, v[108:111] offset:3072\n\t"
        "s_waitcnt vmcnt(16)\n\t"
        "ds_write_b128 v97, v[32:35] offset:0\n\t"
        "ds_write_b128 v98, v[36:39] offset:1024\n\t"
        "ds_write_b128 v97, v[40:43] offset:2048\n\t"
        "ds_write_b128 v98, v[44:47] offset:3072\n\t"
        "ds_write_b128 v97, v[48:51] offset:4096\n\t"
        "ds_write_b128 v98, v[52:55] offset:5120\n\t"
        "ds_write_b128 v97, v[56:59] offset:6144\n\t"
        "ds_write_b128 v98, v[60:63] offset:7168\n\t"
        "ds_read_b64_tr_b16 a[48:49], v99 offset:0\n\t"
        "ds_read_b64_tr_b16 a[50:51], v99 offset:256\n\t"
        "ds_read_b64_tr_b16 a[52:53], v99 offset:4096\n\t"
        "ds_read_b64_tr_b16 a[54:55], v99 offset:4352\n\t"
        "s_waitcnt lgkmcnt(8)\n\t"
        "ds_read_b64_tr_b16 a[56:57], v100 offset:0\n\t"
        "ds_read_b64_tr_b16 a[58:59], v100 offset:256\n\t"
        "ds_read_b64_tr_b16 a[60:61], v100 offset:4096\n\t"
        "ds_read_b64_tr_b16 a[62:63], v100 offset:4352\n\t"
        "s_waitcnt lgkmcnt(8)\n\t"
        "ds_read_b64_tr_b16 a[64:65], v101 offset:0\n\t"
        "ds_read_b64_tr_b16 a[66:67], v101 offset:256\n\t"
        "ds_read_b64_tr_b16 a[68:69], v101 offset:4096\n\t"
        "ds_read_b64_tr_b16 a[70:71], v101 offset:4352\n\t"
        "s_waitcnt lgkmcnt(8)\n\t"
        "ds_read_b64_tr_b16 v[104:105], v102 offset:0\n\t"
        "ds_read_b64_tr_b16 v[106:107], v102 offset:256\n\t"
        "ds_read_b64_tr_b16 v[108:109], v102 offset:4096\n\t"
        "ds_read_b64_tr_b16 v[110:111], v102 offset:4352\n\t"
        "s_lshl_b32 s34, s43, 2\n\t"
        "s_add_u32 s34, s34, 5\n\t"
        "s_and_b32 s34, s34, 15\n\t"
        "s_lshl_b32 s34, s34, 19\n\t"
        "buffer_load_dwordx4 a[128:131], v96, s[16:19], s34 offen nt\n\t"
        "s_add_u32 s34, s34, 0x10000\n\t"
        "buffer_load_dwordx4 a[132:135], v96, s[16:19], s34 offen nt\n\t"
        "s_add_u32 s34, s34, 0x10000\n\t"
        "buffer_load_dwordx4 a[136:139], v96, s[16:19], s34 offen nt\n\t"
        "s_add_u32 s34, s34, 0x10000\n\t"
        "buffer_load_dwordx4 a[140:143], v96, s[16:19], s34 offen nt\n\t"
        "s_add_u32 s34, s34, 0x10000\n\t"
        "buffer_load_dwordx4 a[144:147], v96, s[16:19], s34 offen nt\n\t"
        "s_add_u32 s34, s34, 0x10000\n\t"
        "buffer_load_dwordx4 a[148:151], v96, s[16:19], s34 offen nt\n\t"
        "s_add_u32 s34, s34, 0x10000\n\t"
        "buffer_load_dwordx4 a[152:155], v96, s[16:19], s34 offen nt\n\t"
        "s_add_u32 s34, s34, 0x10000\n\t"
        "buffer_load_dwordx4 a[156:159], v96, s[16:19], s34 offen nt\n\t"
        "s_waitcnt lgkmcnt(0)\n\t"
        "ds_write_b128 v117, v[104:107] offset:4096\n\t"
        "ds_write_b128 v117, v[108:111] offset:5120\n\t"
        "s_waitcnt vmcnt(16)\n\t"
        "ds_write_b128 v97, v[64:67] offset:0\n\t"
        "ds_write_b128 v98, v[68:71] offset:1024\n\t"
        "ds_write_b128 v97, v[72:75] offset:2048\n\t"
        "ds_write_b128 v98, v[76:79] offset:3072\n\t"
        "ds_write_b128 v97, v[80:83] offset:4096\n\t"
        "ds_write_b128 v98, v[84:87] offset:5120\n\t"
        "ds_write_b128 v97, v[88:91] offset:6144\n\t"
        "ds_write_b128 v98, v[92:95] offset:7168\n\t"
        "ds_read_b64_tr_b16 a[72:73], v99 offset:0\n\t"
        "ds_read_b64_tr_b16 a[74:75], v99 offset:256\n\t"
        "ds_read_b64_tr_b16 a[76:77], v99 offset:4096\n\t"
        "ds_read_b64_tr_b16 a[78:79], v99 offset:4352\n\t"
        "s_waitcnt lgkmcnt(8)\n\t"
        "ds_read_b64_tr_b16 a[80:81], v100 offset:0\n\t"
        "ds_read_b64_tr_b16 a[82:83], v100 offset:256\n\t"
        "ds_read_b64_tr_b16 a[84:85], v100 offset:4096\n\t"
        "ds_read_b64_tr_b16 a[86:87], v100 offset:4352\n\t"
        "s_waitcnt lgkmcnt(8)\n\t"
        "ds_read_b64_tr_b16 a[88:89], v101 offset:0\n\t"
        "ds_read_b64_tr_b16 a[90:91], v101 offset:256\n\t"
        "ds_read_b64_tr_b16 a[92:93], v101 offset:4096\n\t"
        "ds_read_b64_tr_b16 a[94:95], v101 offset:4352\n\t"
        "s_waitcnt lgkmcnt(8)\n\t"
        "ds_read_b64_tr_b16 v[104:105], v102 offset:0\n\t"
        "ds_read_b64_tr_b16 v[106:107], v102 offset:256\n\t"
        "ds_read_b64_tr_b16 v[108:109], v102 offset:4096\n\t"
        "ds_read_b64_tr_b16 v[110:111], v102 offset:4352\n\t"
        "s_lshl_b32 s34, s43, 2\n\t"
        "s_add_u32 s34, s34, 6\n\t"
        "s_and_b32 s34, s34, 15\n\t"
        "s_lshl_b32 s34, s34, 19\n\t"
        "buffer_load_dwordx4 a[160:163], v96, s[16:19], s34 offen nt\n\t"
        "s_add_u32 s34, s34, 0x10000\n\t"
        "buffer_load_dwordx4 a[164:167], v96, s[16:19], s34 offen nt\n\t"
        "s_add_u32 s34, s34, 0x10000\n\t"
        "buffer_load_dwordx4 a[168:171], v96, s[16:19], s34 offen nt\n\t"
        "s_add_u32 s34, s34, 0x10000\n\t"
        "buffer_load_dwordx4 a[172:175], v96, s[16:19], s34 offen nt\n\t"
        "s_add_u32 s34, s34, 0x10000\n\t"
        "buffer_load_dwordx4 a[176:179], v96, s[16:19], s34 offen nt\n\t"
        "s_add_u32 s34, s34, 0x10000\n\t"
        "buffer_load_dwordx4 a[180:183], v96, s[16:19], s34 offen nt\n\t"
        "s_add_u32 s34, s34, 0x10000\n\t"
        "buffer_load_dwordx4 a[184:187], v96, s[16:19], s34 offen nt\n\t"
        "s_add_u32 s34, s34, 0x10000\n\t"
        "buffer_load_dwordx4 a[188:191], v96, s[16:19], s34 offen nt\n\t"
        "s_waitcnt lgkmcnt(0)\n\t"
        "ds_write_b128 v117, v[104:107] offset:6144\n\t"
        "ds_write_b128 v117, v[108:111] offset:7168\n\t"
        "s_waitcnt vmcnt(16)\n\t"
        "ds_write_b128 v97, a[96:99] offset:0\n\t"
        "ds_write_b128 v98, a[100:103] offset:1024\n\t"
        "ds_write_b128 v97, a[104:107] offset:2048\n\t"
        "ds_write_b128 v98, a[108:111] offset:3072\n\t"
        "ds_write_b128 v97, a[112:115] offset:4096\n\t"
        "ds_write_b128 v98, a[116:119] offset:5120\n\t"
        "ds_write_b128 v97, a[120:123] offset:6144\n\t"
        "ds_write_b128 v98, a[124:127] offset:7168\n\t"
        "ds_read_b64_tr_b16 a[96:97], v99 offset:0\n\t"
        "ds_read_b64_tr_b16 a[98:99], v99 offset:256\n\t"
        "ds_read_b64_tr_b16 a[100:101], v99 offset:4096\n\t"
        "ds_read_b64_tr_b16 a[102:103], v99 offset:4352\n\t"
        "s_waitcnt lgkmcnt(8)\n\t"
        "ds_read_b64_tr_b16 a[104:105], v100 offset:0\n\t"
        "ds_read_b64_tr_b16 a[106:107], v100 offset:256\n\t"
        "ds_read_b64_tr_b16 a[108:109], v100 offset:4096\n\t"
        "ds_read_b64_tr_b16 a[110:111], v100 offset:4352\n\t"
        "s_waitcnt lgkmcnt(8)\n\t"
        "ds_read_b64_tr_b16 a[112:113], v101 offset:0\n\t"
        "ds_read_b64_tr_b16 a[114:115], v101 offset:256\n\t"
        "ds_read_b64_tr_b16 a[116:117], v101 offset:4096\n\t"
        "ds_read_b64_tr_b16 a[118:119], v101 offset:4352\n\t"
        "s_waitcnt lgkmcnt(8)\n\t"
        "ds_read_b64_tr_b16 v[104:105], v102 offset:0\n\t"
        "ds_read_b64_tr_b16 v[106:107], v102 offset:256\n\t"
        "ds_read_b64_tr_b16 v[108:109], v102 offset:4096\n\t"
        "ds_read_b64_tr_b16 v[110:111], v102 offset:4352\n\t"
        "s_lshl_b32 s34, s43, 2\n\t"
        "s_add_u32 s34, s34, 7\n\t"
        "s_and_b32 s34, s34, 15\n\t"
        "s_lshl_b32 s34, s34, 19\n\t"
        "buffer_load_dwordx4 a[192:195], v96, s[16:19], s34 offen nt\n\t"
        "s_add_u32 s34, s34, 0x10000\n\t"
        "buffer_load_dwordx4 a[196:199], v96, s[16:19], s34 offen nt\n\t"
        "s_add_u32 s34, s34, 0x10000\n\t"
        "buffer_load_dwordx4 a[200:203], v96, s[16:19], s34 offen nt\n\t"
        "s_add_u32 s34, s34, 0x10000\n\t"
        "buffer_load_dwordx4 a[204:207], v96, s[16:19], s34 offen nt\n\t"
        "s_add_u32 s34, s34, 0x10000\n\t"
        "buffer_load_dwordx4 a[208:211], v96, s[16:19], s34 offen nt\n\t"
        "s_add_u32 s34, s34, 0x10000\n\t"
        "buffer_load_dwordx4 a[212:215], v96, s[16:19], s34 offen nt\n\t"
        "s_add_u32 s34, s34, 0x10000\n\t"
        "buffer_load_dwordx4 a[216:219], v96, s[16:19], s34 offen nt\n\t"
        "s_add_u32 s34, s34, 0x10000\n\t"
        "buffer_load_dwordx4 a[220:223], v96, s[16:19], s34 offen nt\n\t"
        "s_waitcnt lgkmcnt(0)\n\t"
        "ds_write_b128 v117, v[104:107] offset:8192\n\t"
        "ds_write_b128 v117, v[108:111] offset:9216\n\t"
        "s_waitcnt vmcnt(16)\n\t"
        "ds_write_b128 v97, a[128:131] offset:0\n\t"
        "ds_write_b128 v98, a[132:135] offset:1024\n\t"
        "ds_write_b128 v97, a[136:139] offset:2048\n\t"
        "ds_write_b128 v98, a[140:143] offset:3072\n\t"
        "ds_write_b128 v97, a[144:147] offset:4096\n\t"
        "ds_write_b128 v98, a[148:151] offset:5120\n\t"
        "ds_write_b128 v97, a[152:155] offset:6144\n\t"
        "ds_write_b128 v98, a[156:159] offset:7168\n\t"
        "ds_read_b64_tr_b16 a[120:121], v99 offset:0\n\t"
        "ds_read_b64_tr_b16 a[122:123], v99 offset:256\n\t"
        "ds_read_b64_tr_b16 a[124:125], v99 offset:4096\n\t"
        "ds_read_b64_tr_b16 a[126:127], v99 offset:4352\n\t"
        "s_waitcnt lgkmcnt(8)\n\t"
        "ds_read_b64_tr_b16 a[128:129], v100 offset:0\n\t"
        "ds_read_b64_tr_b16 a[130:131], v100 offset:256\n\t"
        "ds_read_b64_tr_b16 a[132:133], v100 offset:4096\n\t"
        "ds_read_b64_tr_b16 a[134:135], v100 offset:4352\n\t"
        "s_waitcnt lgkmcnt(8)\n\t"
        "ds_read_b64_tr_b16 a[136:137], v101 offset:0\n\t"
        "ds_read_b64_tr_b16 a[138:139], v101 offset:256\n\t"
        "ds_read_b64_tr_b16 a[140:141], v101 offset:4096\n\t"
        "ds_read_b64_tr_b16 a[142:143], v101 offset:4352\n\t"
        "s_waitcnt lgkmcnt(8)\n\t"
        "ds_read_b64_tr_b16 v[104:105], v102 offset:0\n\t"
        "ds_read_b64_tr_b16 v[106:107], v102 offset:256\n\t"
        "ds_read_b64_tr_b16 v[108:109], v102 offset:4096\n\t"
        "ds_read_b64_tr_b16 v[110:111], v102 offset:4352\n\t"
        "s_lshl_b32 s34, s43, 2\n\t"
        "s_add_u32 s34, s34, 8\n\t"
        "s_and_b32 s34, s34, 15\n\t"
        "s_lshl_b32 s34, s34, 19\n\t"
        "buffer_load_dwordx4 a[224:227], v96, s[16:19], s34 offen nt\n\t"
        "s_add_u32 s34, s34, 0x10000\n\t"
        "buffer_load_dwordx4 a[228:231], v96, s[16:19], s34 offen nt\n\t"
        "s_add_u32 s34, s34, 0x10000\n\t"
        "buffer_load_dwordx4 a[232:235], v96, s[16:19], s34 offen nt\n\t"
        "s_add_u32 s34, s34, 0x10000\n\t"
        "buffer_load_dwordx4 a[236:239], v96, s[16:19], s34 offen nt\n\t"
        "s_add_u32 s34, s34, 0x10000\n\t"
        "buffer_load_dwordx4 a[240:243], v96, s[16:19], s34 offen nt\n\t"
        "s_add_u32 s34, s34, 0x10000\n\t"
        "buffer_load_dwordx4 a[244:247], v96, s[16:19], s34 offen nt\n\t"
        "s_add_u32 s34, s34, 0x10000\n\t"
        "buffer_load_dwordx4 a[248:251], v96, s[16:19], s34 offen nt\n\t"
        "s_add_u32 s34, s34, 0x10000\n\t"
        "buffer_load_dwordx4 a[252:255], v96, s[16:19], s34 offen nt\n\t"
        "s_waitcnt lgkmcnt(0)\n\t"
        "ds_write_b128 v117, v[104:107] offset:10240\n\t"
        "ds_write_b128 v117, v[108:111] offset:11264\n\t"
        "s_waitcnt vmcnt(16)\n\t"
        "ds_write_b128 v97, a[160:163] offset:0\n\t"
        "ds_write_b128 v98, a[164:167] offset:1024\n\t"
        "ds_write_b128 v97, a[168:171] offset:2048\n\t"
        "ds_write_b128 v98, a[172:175] offset:3072\n\t"
        "ds_write_b128 v97, a[176:179] offset:4096\n\t"
        "ds_write_b128 v98, a[180:183] offset:5120\n\t"
        "ds_write_b128 v97, a[184:187] offset:6144\n\t"
        "ds_write_b128 v98, a[188:191] offset:7168\n\t"
        "ds_read_b64_tr_b16 a[144:145], v99 offset:0\n\t"
        "ds_read_b64_tr_b16 a[146:147], v99 offset:256\n\t"
        "ds_read_b64_tr_b16 a[148:149], v99 offset:4096\n\t"
        "ds_read_b64_tr_b16 a[150:151], v99 offset:4352\n\t"
        "s_waitcnt lgkmcnt(8)\n\t"
        "ds_read_b64_tr_b16 a[152:153], v100 offset:0\n\t"
        "ds_read_b64_tr_b16 a[154:155], v100 offset:256\n\t"
        "ds_read_b64_tr_b16 a[156:157], v100 offset:4096\n\t"
        "ds_read_b64_tr_b16 a[158:159], v100 offset:4352\n\t"
        "s_waitcnt lgkmcnt(8)\n\t"
        "ds_read_b64_tr_b16 a[160:161], v101 offset:0\n\t"
        "ds_read_b64_tr_b16 a[162:163], v101 offset:256\n\t"
        "ds_read_b64_tr_b16 a[164:165], v101 offset:4096\n\t"
        "ds_read_b64_tr_b16 a[166:167], v101 offset:4352\n\t"
        "s_waitcnt lgkmcnt(8)\n\t"
        "ds_read_b64_tr_b16 v[104:105], v102 offset:0\n\t"
        "ds_read_b64_tr_b16 v[106:107], v102 offset:256\n\t"
        "ds_read_b64_tr_b16 v[108:109], v102 offset:4096\n\t"
        "ds_read_b64_tr_b16 v[110:111], v102 offset:4352\n\t"
        "s_lshl_b32 s34, s43, 2\n\t"
        "s_add_u32 s34, s34, 9\n\t"
        "s_and_b32 s34, s34, 15\n\t"
        "s_lshl_b32 s34, s34, 19\n\t"
        "buffer_load_dwordx4 v[128:131], v96, s[16:19], s34 offen nt\n\t"
        "s_add_u32 s34, s34, 0x10000\n\t"
        "buffer_load_dwordx4 v[132:135], v96, s[16:19], s34 offen nt\n\t"
        "s_add_u32 s34, s34, 0x10000\n\t"
        "buffer_load_dwordx4 v[136:139], v96, s[16:19], s34 offen nt\n\t"
        "s_add_u32 s34, s34, 0x10000\n\t"
        "buffer_load_dwordx4 v[140:143], v96, s[16:19], s34 offen nt\n\t"
        "s_add_u32 s34, s34, 0x10000\n\t"
        "buffer_load_dwordx4 v[144:147], v96, s[16:19], s34 offen nt\n\t"
        "s_add_u32 s34, s34, 0x10000\n\t"
        "buffer_load_dwordx4 v[148:151], v96, s[16:19], s34 offen nt\n\t"
        "s_add_u32 s34, s34, 0x10000\n\t"
        "buffer_load_dwordx4 v[152:155], v96, s[16:19], s34 offen nt\n\t"
        "s_add_u32 s34, s34, 0x10000\n\t"
        "buffer_load_dwordx4 v[156:159], v96, s[16:19], s34 offen nt\n\t"
        "s_waitcnt lgkmcnt(0)\n\t"
        "ds_write_b128 v117, v[104:107] offset:12288\n\t"
        "ds_write_b128 v117, v[108:111] offset:13312\n\t"
        "s_waitcnt vmcnt(16)\n\t"
        "ds_write_b128 v97, a[192:195] offset:0\n\t"
        "ds_write_b128 v98, a[196:199] offset:1024\n\t"
        "ds_write_b128 v97, a[200:203] offset:2048\n\t"
        "ds_write_b128 v98, a[204:207] offset:3072\n\t"
        "ds_write_b128 v97, a[208:211] offset:4096\n\t"
        "ds_write_b128 v98, a[212:215] offset:5120\n\t"
        "ds_write_b128 v97, a[216:219] offset:6144\n\t"
        "ds_write_b128 v98, a[220:223] offset:7168\n\t"
        "ds_read_b64_tr_b16 a[168:169], v99 offset:0\n\t"
        "ds_read_b64_tr_b16 a[170:171], v99 offset:256\n\t"
        "ds_read_b64_tr_b16 a[172:173], v99 offset:4096\n\t"
        "ds_read_b64_tr_b16 a[174:175], v99 offset:4352\n\t"
        "s_waitcnt lgkmcnt(8)\n\t"
        "ds_read_b64_tr_b16 a[176:177], v100 offset:0\n\t"
        "ds_read_b64_tr_b16 a[178:179], v100 offset:256\n\t"
        "ds_read_b64_tr_b16 a[180:181], v100 offset:4096\n\t"
        "ds_read_b64_tr_b16 a[182:183], v100 offset:4352\n\t"
        "s_waitcnt lgkmcnt(8)\n\t"
        "ds_read_b64_tr_b16 a[184:185], v101 offset:0\n\t"
        "ds_read_b64_tr_b16 a[186:187], v101 offset:256\n\t"
        "ds_read_b64_tr_b16 a[188:189], v101 offset:4096\n\t"
        "ds_read_b64_tr_b16 a[190:191], v101 offset:4352\n\t"
        "s_waitcnt lgkmcnt(8)\n\t"
        "ds_read_b64_tr_b16 v[104:105], v102 offset:0\n\t"
        "ds_read_b64_tr_b16 v[106:107], v102 offset:256\n\t"
        "ds_read_b64_tr_b16 v[108:109], v102 offset:4096\n\t"
        "ds_read_b64_tr_b16 v[110:111], v102 offset:4352\n\t"
        "s_lshl_b32 s34, s43, 2\n\t"
        "s_add_u32 s34, s34, 10\n\t"
        "s_and_b32 s34, s34, 15\n\t"
        "s_lshl_b32 s34, s34, 19\n\t"
        "buffer_load_dwordx4 v[160:163], v96, s[16:19], s34 offen nt\n\t"
        "s_add_u32 s34, s34, 0x10000\n\t"
        "buffer_load_dwordx4 v[164:167], v96, s[16:19], s34 offen nt\n\t"
        "s_add_u32 s34, s34, 0x10000\n\t"
        "buffer_load_dwordx4 v[168:171], v96, s[16:19], s34 offen nt\n\t"
        "s_add_u32 s34, s34, 0x10000\n\t"
        "buffer_load_dwordx4 v[172:175], v96, s[16:19], s34 offen nt\n\t"
        "s_add_u32 s34, s34, 0x10000\n\t"
        "buffer_load_dwordx4 v[176:179], v96, s[16:19], s34 offen nt\n\t"
        "s_add_u32 s34, s34, 0x10000\n\t"
        "buffer_load_dwordx4 v[180:183], v96, s[16:19], s34 offen nt\n\t"
        "s_add_u32 s34, s34, 0x10000\n\t"
        "buffer_load_dwordx4 v[184:187], v96, s[16:19], s34 offen nt\n\t"
        "s_add_u32 s34, s34, 0x10000\n\t"
        "buffer_load_dwordx4 v[188:191], v96, s[16:19], s34 offen nt\n\t"
        "s_waitcnt lgkmcnt(0)\n\t"
        "ds_write_b128 v117, v[104:107] offset:14336\n\t"
        "ds_write_b128 v117, v[108:111] offset:15360\n\t"
        "s_waitcnt vmcnt(16)\n\t"
        "ds_write_b128 v97, a[224:227] offset:0\n\t"
        "ds_write_b128 v98, a[228:231] offset:1024\n\t"
        "ds_write_b128 v97, a[232:235] offset:2048\n\t"
        "ds_write_b128 v98, a[236:239] offset:3072\n\t"
        "ds_write_b128 v97, a[240:243] offset:4096\n\t"
        "ds_write_b128 v98, a[244:247] offset:5120\n\t"
        "ds_write_b128 v97, a[248:251] offset:6144\n\t"
        "ds_write_b128 v98, a[252:255] offset:7168\n\t"
        "ds_read_b64_tr_b16 a[192:193], v99 offset:0\n\t"
        "ds_read_b64_tr_b16 a[194:195], v99 offset:256\n\t"
        "ds_read_b64_tr_b16 a[196:197], v99 offset:4096\n\t"
        "ds_read_b64_tr_b16 a[198:199], v99 offset:4352\n\t"
        "s_waitcnt lgkmcnt(8)\n\t"
        "ds_read_b64_tr_b16 a[200:201], v100 offset:0\n\t"
        "ds_read_b64_tr_b16 a[202:203], v100 offset:256\n\t"
        "ds_read_b64_tr_b16 a[204:205], v100 offset:4096\n\t"
        "ds_read_b64_tr_b16 a[206:207], v100 offset:4352\n\t"
        "s_waitcnt lgkmcnt(8)\n\t"
        "ds_read_b64_tr_b16 a[208:209], v101 offset:0\n\t"
        "ds_read_b64_tr_b16 a[210:211], v101 offset:256\n\t"
        "ds_read_b64_tr_b16 a[212:213], v101 offset:4096\n\t"
        "ds_read_b64_tr_b16 a[214:215], v101 offset:4352\n\t"
        "s_waitcnt lgkmcnt(8)\n\t"
        "ds_read_b64_tr_b16 v[104:105], v102 offset:0\n\t"
        "ds_read_b64_tr_b16 v[106:107], v102 offset:256\n\t"
        "ds_read_b64_tr_b16 v[108:109], v102 offset:4096\n\t"
        "ds_read_b64_tr_b16 v[110:111], v102 offset:4352\n\t"
        "s_lshl_b32 s34, s43, 2\n\t"
        "s_add_u32 s34, s34, 11\n\t"
        "s_and_b32 s34, s34, 15\n\t"
        "s_lshl_b32 s34, s34, 19\n\t"
        "buffer_load_dwordx4 v[192:195], v96, s[16:19], s34 offen nt\n\t"
        "s_add_u32 s34, s34, 0x10000\n\t"
        "buffer_load_dwordx4 v[196:199], v96, s[16:19], s34 offen nt\n\t"
        "s_add_u32 s34, s34, 0x10000\n\t"
        "buffer_load_dwordx4 v[200:203], v96, s[16:19], s34 offen nt\n\t"
        "s_add_u32 s34, s34, 0x10000\n\t"
        "buffer_load_dwordx4 v[204:207], v96, s[16:19], s34 offen nt\n\t"
        "s_add_u32 s34, s34, 0x10000\n\t"
        "buffer_load_dwordx4 v[208:211], v96, s[16:19], s34 offen nt\n\t"
        "s_add_u32 s34, s34, 0x10000\n\t"
        "buffer_load_dwordx4 v[212:215], v96, s[16:19], s34 offen nt\n\t"
        "s_add_u32 s34, s34, 0x10000\n\t"
        "buffer_load_dwordx4 v[216:219], v96, s[16:19], s34 offen nt\n\t"
        "s_add_u32 s34, s34, 0x10000\n\t"
        "buffer_load_dwordx4 v[220:223], v96, s[16:19], s34 offen nt\n\t"
        "s_waitcnt lgkmcnt(0)\n\t"
        "ds_write_b128 v117, v[104:107] offset:16384\n\t"
        "ds_write_b128 v117, v[108:111] offset:17408\n\t"
        "s_waitcnt vmcnt(16)\n\t"
        "ds_write_b128 v97, v[128:131] offset:0\n\t"
        "ds_write_b128 v98, v[132:135] offset:1024\n\t"
        "ds_write_b128 v97, v[136:139] offset:2048\n\t"
        "ds_write_b128 v98, v[140:143] offset:3072\n\t"
        "ds_write_b128 v97, v[144:147] offset:4096\n\t"
        "ds_write_b128 v98, v[148:151] offset:5120\n\t"
        "ds_write_b128 v97, v[152:155] offset:6144\n\t"
        "ds_write_b128 v98, v[156:159] offset:7168\n\t"
        "ds_read_b64_tr_b16 a[216:217], v99 offset:0\n\t"
        "ds_read_b64_tr_b16 a[218:219], v99 offset:256\n\t"
        "ds_read_b64_tr_b16 a[220:221], v99 offset:4096\n\t"
        "ds_read_b64_tr_b16 a[222:223], v99 offset:4352\n\t"
        "s_waitcnt lgkmcnt(8)\n\t"
        "ds_read_b64_tr_b16 a[224:225], v100 offset:0\n\t"
        "ds_read_b64_tr_b16 a[226:227], v100 offset:256\n\t"
        "ds_read_b64_tr_b16 a[228:229], v100 offset:4096\n\t"
        "ds_read_b64_tr_b16 a[230:231], v100 offset:4352\n\t"
        "s_waitcnt lgkmcnt(8)\n\t"
        "ds_read_b64_tr_b16 a[232:233], v101 offset:0\n\t"
        "ds_read_b64_tr_b16 a[234:235], v101 offset:256\n\t"
        "ds_read_b64_tr_b16 a[236:237], v101 offset:4096\n\t"
        "ds_read_b64_tr_b16 a[238:239], v101 offset:4352\n\t"
        "s_waitcnt lgkmcnt(8)\n\t"
        "ds_read_b64_tr_b16 v[104:105], v102 offset:0\n\t"
        "ds_read_b64_tr_b16 v[106:107], v102 offset:256\n\t"
        "ds_read_b64_tr_b16 v[108:109], v102 offset:4096\n\t"
        "ds_read_b64_tr_b16 v[110:111], v102 offset:4352\n\t"
        "s_lshl_b32 s34, s43, 2\n\t"
        "s_add_u32 s34, s34, 12\n\t"
        "s_and_b32 s34, s34, 15\n\t"
        "s_lshl_b32 s34, s34, 19\n\t"
        "buffer_load_dwordx4 v[224:227], v96, s[16:19], s34 offen nt\n\t"
        "s_add_u32 s34, s34, 0x10000\n\t"
        "buffer_load_dwordx4 v[228:231], v96, s[16:19], s34 offen nt\n\t"
        "s_add_u32 s34, s34, 0x10000\n\t"
        "buffer_load_dwordx4 v[232:235], v96, s[16:19], s34 offen nt\n\t"
        "s_add_u32 s34, s34, 0x10000\n\t"
        "buffer_load_dwordx4 v[236:239], v96, s[16:19], s34 offen nt\n\t"
        "s_add_u32 s34, s34, 0x10000\n\t"
        "buffer_load_dwordx4 v[240:243], v96, s[16:19], s34 offen nt\n\t"
        "s_add_u32 s34, s34, 0x10000\n\t"
        "buffer_load_dwordx4 v[244:247], v96, s[16:19], s34 offen nt\n\t"
        "s_add_u32 s34, s34, 0x10000\n\t"
        "buffer_load_dwordx4 v[248:251], v96, s[16:19], s34 offen nt\n\t"
        "s_add_u32 s34, s34, 0x10000\n\t"
        "buffer_load_dwordx4 v[252:255], v96, s[16:19], s34 offen nt\n\t"
        "s_waitcnt lgkmcnt(0)\n\t"
        "ds_write_b128 v117, v[104:107] offset:18432\n\t"
        "ds_write_b128 v117, v[108:111] offset:19456\n\t"
        "s_waitcnt vmcnt(16)\n\t"
        "ds_write_b128 v97, v[160:163] offset:0\n\t"
        "ds_write_b128 v98, v[164:167] offset:1024\n\t"
        "ds_write_b128 v97, v[168:171] offset:2048\n\t"
        "ds_write_b128 v98, v[172:175] offset:3072\n\t"
        "ds_write_b128 v97, v[176:179] offset:4096\n\t"
        "ds_write_b128 v98, v[180:183] offset:5120\n\t"
        "ds_write_b128 v97, v[184:187] offset:6144\n\t"
        "ds_write_b128 v98, v[188:191] offset:7168\n\t"
        "ds_read_b64_tr_b16 a[240:241], v99 offset:0\n\t"
        "ds_read_b64_tr_b16 a[242:243], v99 offset:256\n\t"
        "ds_read_b64_tr_b16 a[244:245], v99 offset:4096\n\t"
        "ds_read_b64_tr_b16 a[246:247], v99 offset:4352\n\t"
        "s_waitcnt lgkmcnt(8)\n\t"
        "ds_read_b64_tr_b16 a[248:249], v100 offset:0\n\t"
        "ds_read_b64_tr_b16 a[250:251], v100 offset:256\n\t"
        "ds_read_b64_tr_b16 a[252:253], v100 offset:4096\n\t"
        "ds_read_b64_tr_b16 a[254:255], v100 offset:4352\n\t"
        "s_waitcnt lgkmcnt(8)\n\t"
        "ds_read_b64_tr_b16 v[128:129], v101 offset:0\n\t"
        "ds_read_b64_tr_b16 v[130:131], v101 offset:256\n\t"
        "ds_read_b64_tr_b16 v[132:133], v101 offset:4096\n\t"
        "ds_read_b64_tr_b16 v[134:135], v101 offset:4352\n\t"
        "s_waitcnt lgkmcnt(8)\n\t"
        "ds_read_b64_tr_b16 v[104:105], v102 offset:0\n\t"
        "ds_read_b64_tr_b16 v[106:107], v102 offset:256\n\t"
        "ds_read_b64_tr_b16 v[108:109], v102 offset:4096\n\t"
        "ds_read_b64_tr_b16 v[110:111], v102 offset:4352\n\t"
        "s_lshl_b32 s34, s43, 2\n\t"
        "s_add_u32 s34, s34, 13\n\t"
        "s_and_b32 s34, s34, 15\n\t"
        "s_lshl_b32 s34, s34, 19\n\t"
        "buffer_load_dwordx4 v[0:3], v96, s[16:19], s34 offen nt\n\t"
        "s_add_u32 s34, s34, 0x10000\n\t"
        "buffer_load_dwordx4 v[4:7], v96, s[16:19], s34 offen nt\n\t"
        "s_add_u32 s34, s34, 0x10000\n\t"
        "buffer_load_dwordx4 v[8:11], v96, s[16:19], s34 offen nt\n\t"
        "s_add_u32 s34, s34, 0x10000\n\t"
        "buffer_load_dwordx4 v[12:15], v96, s[16:19], s34 offen nt\n\t"
        "s_add_u32 s34, s34, 0x10000\n\t"
        "buffer_load_dwordx4 v[16:19], v96, s[16:19], s34 offen nt\n\t"
        "s_add_u32 s34, s34, 0x10000\n\t"
        "buffer_load_dwordx4 v[20:23], v96, s[16:19], s34 offen nt\n\t"
        "s_add_u32 s34, s34, 0x10000\n\t"
        "buffer_load_dwordx4 v[24:27], v96, s[16:19], s34 offen nt\n\t"
        "s_add_u32 s34, s34, 0x10000\n\t"
        "buffer_load_dwordx4 v[28:31], v96, s[16:19], s34 offen nt\n\t"
        "s_waitcnt lgkmcnt(0)\n\t"
        "ds_write_b128 v117, v[104:107] offset:20480\n\t"
        "ds_write_b128 v117, v[108:111] offset:21504\n\t"
        "s_waitcnt vmcnt(16)\n\t"
        "ds_write_b128 v97, v[192:195] offset:0\n\t"
        "ds_write_b128 v98, v[196:199] offset:1024\n\t"
        "ds_write_b128 v97, v[200:203] offset:2048\n\t"
        "ds_write_b128 v98, v[204:207] offset:3072\n\t"
        "ds_write_b128 v97, v[208:211] offset:4096\n\t"
        "ds_write_b128 v98, v[212:215] offset:5120\n\t"
        "ds_write_b128 v97, v[216:219] offset:6144\n\t"
        "ds_write_b128 v98, v[220:223] offset:7168\n\t"
        "ds_read_b64_tr_b16 v[136:137], v99 offset:0\n\t"
        "ds_read_b64_tr_b16 v[138:139], v99 offset:256\n\t"
        "ds_read_b64_tr_b16 v[140:141], v99 offset:4096\n\t"
        "ds_read_b64_tr_b16 v[142:143], v99 offset:4352\n\t"
        "s_waitcnt lgkmcnt(8)\n\t"
        "ds_read_b64_tr_b16 v[144:145], v100 offset:0\n\t"
        "ds_read_b64_tr_b16 v[146:147], v100 offset:256\n\t"
        "ds_read_b64_tr_b16 v[148:149], v100 offset:4096\n\t"
        "ds_read_b64_tr_b16 v[150:151], v100 offset:4352\n\t"
        "s_waitcnt lgkmcnt(8)\n\t"
        "ds_read_b64_tr_b16 v[152:153], v101 offset:0\n\t"
        "ds_read_b64_tr_b16 v[154:155], v101 offset:256\n\t"
        "ds_read_b64_tr_b16 v[156:157], v101 offset:4096\n\t"
        "ds_read_b64_tr_b16 v[158:159], v101 offset:4352\n\t"
        "s_waitcnt lgkmcnt(8)\n\t"
        "ds_read_b64_tr_b16 v[104:105], v102 offset:0\n\t"
        "ds_read_b64_tr_b16 v[106:107], v102 offset:256\n\t"
        "ds_read_b64_tr_b16 v[108:109], v102 offset:4096\n\t"
        "ds_read_b64_tr_b16 v[110:111], v102 offset:4352\n\t"
        "s_lshl_b32 s34, s43, 2\n\t"
        "s_add_u32 s34, s34, 14\n\t"
        "s_and_b32 s34, s34, 15\n\t"
        "s_lshl_b32 s34, s34, 19\n\t"
        "buffer_load_dwordx4 v[32:35], v96, s[16:19], s34 offen nt\n\t"
        "s_add_u32 s34, s34, 0x10000\n\t"
        "buffer_load_dwordx4 v[36:39], v96, s[16:19], s34 offen nt\n\t"
        "s_add_u32 s34, s34, 0x10000\n\t"
        "buffer_load_dwordx4 v[40:43], v96, s[16:19], s34 offen nt\n\t"
        "s_add_u32 s34, s34, 0x10000\n\t"
        "buffer_load_dwordx4 v[44:47], v96, s[16:19], s34 offen nt\n\t"
        "s_add_u32 s34, s34, 0x10000\n\t"
        "buffer_load_dwordx4 v[48:51], v96, s[16:19], s34 offen nt\n\t"
        "s_add_u32 s34, s34, 0x10000\n\t"
        "buffer_load_dwordx4 v[52:55], v96, s[16:19], s34 offen nt\n\t"
        "s_add_u32 s34, s34, 0x10000\n\t"
        "buffer_load_dwordx4 v[56:59], v96, s[16:19], s34 offen nt\n\t"
        "s_add_u32 s34, s34, 0x10000\n\t"
        "buffer_load_dwordx4 v[60:63], v96, s[16:19], s34 offen nt\n\t"
        "s_waitcnt lgkmcnt(0)\n\t"
        "ds_write_b128 v126, v[104:107] offset:0\n\t"
        "ds_write_b128 v126, v[108:111] offset:1024\n\t"
        "s_waitcnt vmcnt(16)\n\t"
        "ds_write_b128 v97, v[224:227] offset:0\n\t"
        "ds_write_b128 v98, v[228:231] offset:1024\n\t"
        "ds_write_b128 v97, v[232:235] offset:2048\n\t"
        "ds_write_b128 v98, v[236:239] offset:3072\n\t"
        "ds_write_b128 v97, v[240:243] offset:4096\n\t"
        "ds_write_b128 v98, v[244:247] offset:5120\n\t"
        "ds_write_b128 v97, v[248:251] offset:6144\n\t"
        "ds_write_b128 v98, v[252:255] offset:7168\n\t"
        "ds_read_b64_tr_b16 v[160:161], v99 offset:0\n\t"
        "ds_read_b64_tr_b16 v[162:163], v99 offset:256\n\t"
        "ds_read_b64_tr_b16 v[164:165], v99 offset:4096\n\t"
        "ds_read_b64_tr_b16 v[166:167], v99 offset:4352\n\t"
        "s_waitcnt lgkmcnt(8)\n\t"
        "ds_read_b64_tr_b16 v[168:169], v100 offset:0\n\t"
        "ds_read_b64_tr_b16 v[170:171], v100 offset:256\n\t"
        "ds_read_b64_tr_b16 v[172:173], v100 offset:4096\n\t"
        "ds_read_b64_tr_b16 v[174:175], v100 offset:4352\n\t"
        "s_waitcnt lgkmcnt(8)\n\t"
        "ds_read_b64_tr_b16 v[176:177], v101 offset:0\n\t"
        "ds_read_b64_tr_b16 v[178:179], v101 offset:256\n\t"
        "ds_read_b64_tr_b16 v[180:181], v101 offset:4096\n\t"
        "ds_read_b64_tr_b16 v[182:183], v101 offset:4352\n\t"
        "s_waitcnt lgkmcnt(8)\n\t"
        "ds_read_b64_tr_b16 v[104:105], v102 offset:0\n\t"
        "ds_read_b64_tr_b16 v[106:107], v102 offset:256\n\t"
        "ds_read_b64_tr_b16 v[108:109], v102 offset:4096\n\t"
        "ds_read_b64_tr_b16 v[110:111], v102 offset:4352\n\t"
        "s_lshl_b32 s34, s43, 2\n\t"
        "s_add_u32 s34, s34, 15\n\t"
        "s_and_b32 s34, s34, 15\n\t"
        "s_lshl_b32 s34, s34, 19\n\t"
        "buffer_load_dwordx4 v[64:67], v96, s[16:19], s34 offen nt\n\t"
        "s_add_u32 s34, s34, 0x10000\n\t"
        "buffer_load_dwordx4 v[68:71], v96, s[16:19], s34 offen nt\n\t"
        "s_add_u32 s34, s34, 0x10000\n\t"
        "buffer_load_dwordx4 v[72:75], v96, s[16:19], s34 offen nt\n\t"
        "s_add_u32 s34, s34, 0x10000\n\t"
        "buffer_load_dwordx4 v[76:79], v96, s[16:19], s34 offen nt\n\t"
        "s_add_u32 s34, s34, 0x10000\n\t"
        "buffer_load_dwordx4 v[80:83], v96, s[16:19], s34 offen nt\n\t"
        "s_add_u32 s34, s34, 0x10000\n\t"
        "buffer_load_dwordx4 v[84:87], v96, s[16:19], s34 offen nt\n\t"
        "s_add_u32 s34, s34, 0x10000\n\t"
        "buffer_load_dwordx4 v[88:91], v96, s[16:19], s34 offen nt\n\t"
        "s_add_u32 s34, s34, 0x10000\n\t"
        "buffer_load_dwordx4 v[92:95], v96, s[16:19], s34 offen nt\n\t"
        "s_waitcnt lgkmcnt(0)\n\t"
        "ds_write_b128 v126, v[104:107] offset:2048\n\t"
        "ds_write_b128 v126, v[108:111] offset:3072\n\t"
        "s_waitcnt vmcnt(16)\n\t"
        "ds_write_b128 v97, v[0:3] offset:0\n\t"
        "ds_write_b128 v98, v[4:7] offset:1024\n\t"
        "ds_write_b128 v97, v[8:11] offset:2048\n\t"
        "ds_write_b128 v98, v[12:15] offset:3072\n\t"
        "ds_write_b128 v97, v[16:19] offset:4096\n\t"
        "ds_write_b128 v98, v[20:23] offset:5120\n\t"
        "ds_write_b128 v97, v[24:27] offset:6144\n\t"
        "ds_write_b128 v98, v[28:31] offset:7168\n\t"
        "ds_read_b64_tr_b16 v[184:185], v99 offset:0\n\t"
        "ds_read_b64_tr_b16 v[186:187], v99 offset:256\n\t"
        "ds_read_b64_tr_b16 v[188:189], v99 offset:4096\n\t"
        "ds_read_b64_tr_b16 v[190:191], v99 offset:4352\n\t"
        "s_waitcnt lgkmcnt(8)\n\t"
        "ds_read_b64_tr_b16 v[192:193], v100 offset:0\n\t"
        "ds_read_b64_tr_b16 v[194:195], v100 offset:256\n\t"
        "ds_read_b64_tr_b16 v[196:197], v100 offset:4096\n\t"
        "ds_read_b64_tr_b16 v[198:199], v100 offset:4352\n\t"
        "s_waitcnt lgkmcnt(8)\n\t"
        "ds_read_b64_tr_b16 v[200:201], v101 offset:0\n\t"
        "ds_read_b64_tr_b16 v[202:203], v101 offset:256\n\t"
        "ds_read_b64_tr_b16 v[204:205], v101 offset:4096\n\t"
        "ds_read_b64_tr_b16 v[206:207], v101 offset:4352\n\t"
        "s_waitcnt lgkmcnt(8)\n\t"
        "ds_read_b64_tr_b16 v[104:105], v102 offset:0\n\t"
        "ds_read_b64_tr_b16 v[106:107], v102 offset:256\n\t"
        "ds_read_b64_tr_b16 v[108:109], v102 offset:4096\n\t"
        "ds_read_b64_tr_b16 v[110:111], v102 offset:4352\n\t"
        "s_waitcnt lgkmcnt(0)\n\t"
        "ds_write_b128 v126, v[104:107] offset:4096\n\t"
        "ds_write_b128 v126, v[108:111] offset:5120\n\t"
        "s_waitcnt vmcnt(8)\n\t"
        "ds_write_b128 v97, v[32:35] offset:0\n\t"
        "ds_write_b128 v98, v[36:39] offset:1024\n\t"
        "ds_write_b128 v97, v[40:43] offset:2048\n\t"
        "ds_write_b128 v98, v[44:47] offset:3072\n\t"
        "ds_write_b128 v97, v[48:51] offset:4096\n\t"
        "ds_write_b128 v98, v[52:55] offset:5120\n\t"
        "ds_write_b128 v97, v[56:59] offset:6144\n\t"
        "ds_write_b128 v98, v[60:63] offset:7168\n\t"
        "ds_read_b64_tr_b16 v[208:209], v99 offset:0\n\t"
        "ds_read_b64_tr_b16 v[210:211], v99 offset:256\n\t"
        "ds_read_b64_tr_b16 v[212:213], v99 offset:4096\n\t"
        "ds_read_b64_tr_b16 v[214:215], v99 offset:4352\n\t"
        "s_waitcnt lgkmcnt(8)\n\t"
        "ds_read_b64_tr_b16 v[216:217], v100 offset:0\n\t"
        "ds_read_b64_tr_b16 v[218:219], v100 offset:256\n\t"
        "ds_read_b64_tr_b16 v[220:221], v100 offset:4096\n\t"
        "ds_read_b64_tr_b16 v[222:223], v100 offset:4352\n\t"
        "s_waitcnt lgkmcnt(8)\n\t"
        "ds_read_b64_tr_b16 v[224:225], v101 offset:0\n\t"
        "ds_read_b64_tr_b16 v[226:227], v101 offset:256\n\t"
        "ds_read_b64_tr_b16 v[228:229], v101 offset:4096\n\t"
        "ds_read_b64_tr_b16 v[230:231], v101 offset:4352\n\t"
        "s_waitcnt lgkmcnt(8)\n\t"
        "ds_read_b64_tr_b16 v[104:105], v102 offset:0\n\t"
        "ds_read_b64_tr_b16 v[106:107], v102 offset:256\n\t"
        "ds_read_b64_tr_b16 v[108:109], v102 offset:4096\n\t"
        "ds_read_b64_tr_b16 v[110:111], v102 offset:4352\n\t"
        "s_waitcnt lgkmcnt(0)\n\t"
        "ds_write_b128 v126, v[104:107] offset:6144\n\t"
        "ds_write_b128 v126, v[108:111] offset:7168\n\t"
        "s_waitcnt vmcnt(0)\n\t"
        "ds_write_b128 v97, v[64:67] offset:0\n\t"
        "ds_write_b128 v98, v[68:71] offset:1024\n\t"
        "ds_write_b128 v97, v[72:75] offset:2048\n\t"
        "ds_write_b128 v98, v[76:79] offset:3072\n\t"
        "ds_write_b128 v97, v[80:83] offset:4096\n\t"
        "ds_write_b128 v98, v[84:87] offset:5120\n\t"
        "ds_write_b128 v97, v[88:91] offset:6144\n\t"
        "ds_write_b128 v98, v[92:95] offset:7168\n\t"
        "ds_read_b64_tr_b16 v[232:233], v99 offset:0\n\t"
        "ds_read_b64_tr_b16 v[234:235], v99 offset:256\n\t"
        "ds_read_b64_tr_b16 v[236:237], v99 offset:4096\n\t"
        "ds_read_b64_tr_b16 v[238:239], v99 offset:4352\n\t"
        "s_waitcnt lgkmcnt(8)\n\t"
        "ds_read_b64_tr_b16 v[240:241], v100 offset:0\n\t"
        "ds_read_b64_tr_b16 v[242:243], v100 offset:256\n\t"
        "ds_read_b64_tr_b16 v[244:245], v100 offset:4096\n\t"
        "ds_read_b64_tr_b16 v[246:247], v100 offset:4352\n\t"
        "s_waitcnt lgkmcnt(8)\n\t"
        "ds_read_b64_tr_b16 v[248:249], v101 offset:0\n\t"
        "ds_read_b64_tr_b16 v[250:251], v101 offset:256\n\t"
        "ds_read_b64_tr_b16 v[252:253], v101 offset:4096\n\t"
        "ds_read_b64_tr_b16 v[254:255], v101 offset:4352\n\t"
        "s_waitcnt lgkmcnt(8)\n\t"
        "ds_read_b64_tr_b16 v[104:105], v102 offset:0\n\t"
        "ds_read_b64_tr_b16 v[106:107], v102 offset:256\n\t"
        "ds_read_b64_tr_b16 v[108:109], v102 offset:4096\n\t"
        "ds_read_b64_tr_b16 v[110:111], v102 offset:4352\n\t"
        "s_mov_b32 s35, 33792\n\t"
        "buffer_load_dword v0, off, s[12:15], s35 sc1\n\t"
        "s_lshl_b32 s35, s44, 10\n\t"
        "v_lshrrev_b32_e32 v1, 1, v125\n\t"
        "v_and_b32_e32 v1, 0x3c0, v1\n\t"
        "buffer_load_dword v1, v1, s[12:15], s35 offen offset:0 sc1\n\t"
        "s_waitcnt lgkmcnt(0)\n\t"
        "ds_write_b128 v126, v[104:107] offset:8192\n\t"
        "ds_write_b128 v126, v[108:111] offset:9216\n\t"
        "s_waitcnt lgkmcnt(0)\n\t"
        "v_mov_b32_e32 v110, v103\n\t"
        "ds_read_b128 v[64:67], v126 offset:0\n\t"
        "ds_read_b128 v[68:71], v126 offset:1024\n\t"
        "ds_read_b128 v[72:75], v126 offset:2048\n\t"
        "ds_read_b128 v[76:79], v126 offset:3072\n\t"
        "ds_read_b128 v[80:83], v126 offset:4096\n\t"
        "ds_read_b128 v[84:87], v126 offset:5120\n\t"
        "ds_read_b128 v[88:91], v126 offset:6144\n\t"
        "ds_read_b128 v[92:95], v126 offset:7168\n\t"
        "ds_read_b128 v[96:99], v126 offset:8192\n\t"
        "ds_read_b128 v[100:103], v126 offset:9216\n\t"
        "s_waitcnt lgkmcnt(0)\n\t"
        "v_mov_b32_e32 v126, 0\n\t"
        "s_barrier\n\t"
        "s_mov_b32 s48, 0\n\t"
        "s_mov_b32 s54, 0\n\t"
        "s_movk_i32 s56, 24576\n\t"
        "s_lshl_b32 s57, s44, 5\n\t"
        "s_add_u32 s57, s57, s43\n\t"
        "s_lshl_b32 s57, s57, 2\n\t"
        "s_add_u32 s57, s57, 458752\n\t"
        "s_waitcnt vmcnt(0)\n\t"
        "v_mov_b32_e32 v111, v1\n\t"
        "s_lshr_b32 s36, s48, 1\n\t"
        "s_cmp_gt_u32 s54, s36\n\t"
        "s_cbranch_scc1 LA_kv0ok%=\n\t"
        "s_mov_b32 s42, 0\n\t"
        "LA_kv0%=:\n\t"
        "s_waitcnt vmcnt(0)\n\t"
        "v_min_u32_dpp v111, v111, v111 quad_perm:[1,0,3,2] row_mask:0xf bank_mask:0xf\n\t"
        "s_nop 1\n\t"
        "v_min_u32_dpp v111, v111, v111 quad_perm:[2,3,0,1] row_mask:0xf bank_mask:0xf\n\t"
        "s_nop 1\n\t"
        "v_min_u32_dpp v111, v111, v111 row_ror:4 row_mask:0xf bank_mask:0xf\n\t"
        "s_nop 1\n\t"
        "v_min_u32_dpp v111, v111, v111 row_ror:8 row_mask:0xf bank_mask:0xf\n\t"
        "s_nop 0\n\t"
        "v_readfirstlane_b32 s54, v111\n\t"
        "s_nop 0\n\t"
        "s_cmp_gt_u32 s54, s36\n\t"
        "s_cbranch_scc1 LA_kv0ok%=\n\t"
        "s_sleep 4\n\t"
        "s_lshl_b32 s35, s44, 10\n\t"
        "v_lshrrev_b32_e32 v111, 1, v125\n\t"
        "v_and_b32_e32 v111, 0x3c0, v111\n\t"
        "buffer_load_dword v111, v111, s[12:15], s35 offen offset:0 sc1\n\t"
        "s_add_u32 s42, s42, 1\n\t"
        "s_cmp_lt_u32 s42, 0x100000\n\t"
        "s_cbranch_scc1 LA_kv0%=\n\t"
        "s_movk_i32 s34, 16\n\t"
        "s_branch LA_die%=\n\t"
        "LA_kv0ok%=:\n\t"
        "s_and_b32 s36, s48, 1\n\t"
        "s_mul_i32 s36, s36, 24576\n\t"
        "s_add_u32 s36, s36, s55\n\t"
        "s_lshl_b32 s37, s48, 18\n\t"
        "s_lshl_b32 s34, s43, 11\n\t"
        "s_add_u32 s37, s37, s34\n\t"
        "s_add_u32 m0, s36, 0\n\t"
        "s_add_u32 s34, s37, 29548544\n\t"
        "buffer_load_dwordx4 v122, s[12:15], s34 offen sc1 lds\n\t"
        "s_add_u32 m0, s36, 1024\n\t"
        "s_add_u32 s34, s37, 29549568\n\t"
        "buffer_load_dwordx4 v122, s[12:15], s34 offen sc1 lds\n\t"
        "s_add_u32 m0, s36, 8192\n\t"
        "s_add_u32 s34, s37, 12771328\n\t"
        "buffer_load_dwordx4 v122, s[12:15], s34 offen sc1 lds\n\t"
        "s_add_u32 m0, s36, 9216\n\t"
        "s_add_u32 s34, s37, 12772352\n\t"
        "buffer_load_dwordx4 v122, s[12:15], s34 offen sc1 lds\n\t"
        "s_add_u32 m0, s36, 16384\n\t"
        "s_add_u32 s34, s37, 46325760\n\t"
        "buffer_load_dwordx4 v122, s[12:15], s34 offen sc1 lds\n\t"
        "s_add_u32 m0, s36, 17408\n\t"
        "s_add_u32 s34, s37, 46326784\n\t"
        "buffer_load_dwordx4 v122, s[12:15], s34 offen sc1 lds\n\t"
        "s_mov_b32 s42, 0\n\t"
        "LA_cal%=:\n\t"
        "v_readfirstlane_b32 s34, v0\n\t"
        "s_nop 0\n\t"
        "s_cmp_lg_u32 s34, 0\n\t"
        "s_cbranch_scc1 LA_calok%=\n\t"
        "s_sleep 4\n\t"
        "s_mov_b32 s35, 33792\n\t"
        "buffer_load_dword v0, off, s[12:15], s35 sc1\n\t"
        "s_waitcnt vmcnt(0)\n\t"
        "s_add_u32 s42, s42, 1\n\t"
        "s_cmp_lt_u32 s42, 0x100000\n\t"
        "s_cbranch_scc1 LA_cal%=\n\t"
        "s_movk_i32 s34, 8\n\t"
        "s_branch LA_die%=\n\t"
        "LA_calok%=:\n\t"
        "s_and_b32 s34, s34, 8192\n\t"
        "s_add_u32 s8, s8, s34\n\t"
        "s_addc_u32 s9, s9, 0\n\t"
        "s_barrier\n\t"
        "s_cmp_lg_u32 s43, 0\n\t"
        "s_cbranch_scc1 LA_st%=\n\t"
        "v_mov_b32_e32 v2, 1\n\t"
        "s_mov_b32 s35, 34304\n\t"
        "s_mov_b64 exec, 1\n\t"
        "buffer_atomic_add v2, off, s[12:15], s35 sc0\n\t"
        "s_mov_b64 exec, -1\n\t"
        "s_waitcnt vmcnt(0)\n\t"
        "v_readfirstlane_b32 s34, v2\n\t"
        "s_nop 0\n\t"
        "s_cmp_lg_u32 s34, 95\n\t"
        "s_cbranch_scc1 LA_st%=\n\t"
        "s_memrealtime s[32:33]\n\t"
        "v_mbcnt_lo_u32_b32 v3, -1, 0\n\t"
        "v_lshlrev_b32_e32 v3, 7, v3\n\t"
        "s_waitcnt lgkmcnt(0)\n\t"
        "s_add_u32 s34, s32, 50\n\t"
        "s_or_b32 s34, s34, 1\n\t"
        "v_mov_b32_e32 v2, s34\n\t"
        "s_mov_b32 s35, 36864\n\t"
        "s_mov_b64 exec, 0xffff\n\t"
        "buffer_store_dword v2, v3, s[12:15], s35 offen sc1\n\t"
        "s_mov_b64 exec, -1\n\t"
        "LA_st%=:\n\t"
        "s_mov_b32 s24, 0\n\t"
        "s_mov_b32 s26, 0\n\t"
        "s_mov_b32 s27, 0x80000\n\t"
        "s_mov_b32 s28, 0x180000\n\t"
        "s_mov_b32 s31, 0\n\t"
        "s_mov_b32 s47, 0\n\t"
        "s_mov_b32 s38, 0xff00ff00\n\t"
        "s_mov_b32 s39, 0xff00ff00\n\t"
        "s_mov_b32 s40, 0\n\t"
        "s_mov_b32 s41, -1\n\t"
        "ds_read_b128 v[32:35], v117 offset:0\n\t"
        "ds_read_b128 v[36:39], v117 offset:1024\n\t"
        "ds_read_b128 v[40:43], v117 offset:2048\n\t"
        "ds_read_b128 v[44:47], v117 offset:3072\n\t"
        "ds_read_b128 v[48:51], v117 offset:4096\n\t"
        "ds_read_b128 v[52:55], v117 offset:5120\n\t"
        "ds_read_b128 v[56:59], v117 offset:6144\n\t"
        "ds_read_b128 v[60:63], v117 offset:7168\n\t"
        "v_mov_b64_e32 v[0:1], 0\n\t"
        "v_mov_b64_e32 v[2:3], 0\n\t"
        "v_mov_b64_e32 v[4:5], 0\n\t"
        "v_mov_b64_e32 v[6:7], 0\n\t"
        "v_mov_b64_e32 v[8:9], 0\n\t"
        "v_mov_b64_e32 v[10:11], 0\n\t"
        "v_mov_b64_e32 v[12:13], 0\n\t"
        "v_mov_b64_e32 v[14:15], 0\n\t"
        "s_mov_b32 s42, 0\n\t"
        "LA_gw%=:\n\t"
        "buffer_load_dword v16, off, s[12:15], s45 sc1\n\t"
        "s_waitcnt vmcnt(0)\n\t"
        "v_readfirstlane_b32 s25, v16\n\t"
        "s_nop 0\n\t"
        "s_cmp_lg_u32 s25, 0\n\t"
        "s_cbranch_scc1 LA_loop%=\n\t"
        "s_sleep 2\n\t"
        "s_add_u32 s42, s42, 1\n\t"
        "s_cmp_lt_u32 s42, 0x100000\n\t"
        "s_cbranch_scc1 LA_gw%=\n\t"
        "s_movk_i32 s34, 9\n\t"
        "s_branch LA_die%=\n\t"
        "LA_loop%=:\n\t"
        "s_mov_b32 s42, 0\n\t"
        "s_sub_u32 s35, s25, 1\n\t"
        "LA_wa%=:\n\t"
        "s_memrealtime s[32:33]\n\t"
        "s_waitcnt lgkmcnt(0)\n\t"
        "s_sub_u32 s34, s35, s32\n\t"
        "s_cmpk_lt_u32 s34, 0x2000\n\t"
        "s_cbranch_scc1 LA_wa%=\n\t"
        "LA_rd%=:\n\t"
        "buffer_load_dwordx4 v[16:19], v112, s[8:11], s26 offen offset:0 sc1\n\t"
        "s_nop 9\n\t"
        "buffer_load_dwordx4 v[20:23], v112, s[8:11], s26 offen offset:1024 sc1\n\t"
        "s_nop 9\n\t"
        "buffer_load_dwordx4 v[24:27], v112, s[8:11], s26 offen offset:2048 sc1\n\t"
        "s_nop 9\n\t"
        "buffer_load_dwordx4 v[28:31], v112, s[8:11], s26 offen offset:3072 sc1\n\t"
        "LA_go%=:\n\t"
        "s_waitcnt vmcnt(3)\n\t"
        "v_smfmac_f32_16x16x64_bf16 v[12:15], v[16:19], v[32:39], v110\n\t"
        "ds_write_b128 v113, v[16:19] offset:0\n\t"
        "v_smfmac_f32_16x16x64_bf16 v[0:3], v[16:19], a[0:7], v110\n\t"
        "ds_read_b128 v[32:35], v117 offset:8192\n\t"
        "v_max3_u32 v111, v16, v17, v18\n\t"
        "v_smfmac_f32_16x16x64_bf16 v[4:7], v[16:19], a[8:15], v110\n\t"
        "ds_read_b128 v[36:39], v117 offset:9216\n\t"
        "v_max_u32_e32 v111, v19, v111\n\t"
        "v_smfmac_f32_16x16x64_bf16 v[8:11], v[16:19], a[16:23], v110\n\t"
        "s_waitcnt vmcnt(2)\n\t"
        "v_smfmac_f32_16x16x64_bf16 v[12:15], v[20:23], v[40:47], v110\n\t"
        "ds_write_b128 v113, v[20:23] offset:1024\n\t"
        "v_smfmac_f32_16x16x64_bf16 v[0:3], v[20:23], a[24:31], v110\n\t"
        "ds_read_b128 v[40:43], v117 offset:10240\n\t"
        "v_max3_u32 v111, v20, v21, v111\n\t"
        "v_smfmac_f32_16x16x64_bf16 v[4:7], v[20:23], a[32:39], v110\n\t"
        "ds_read_b128 v[44:47], v117 offset:11264\n\t"
        "v_max3_u32 v111, v22, v23, v111\n\t"
        "v_smfmac_f32_16x16x64_bf16 v[8:11], v[20:23], a[40:47], v110\n\t"
        "s_waitcnt vmcnt(1)\n\t"
        "v_smfmac_f32_16x16x64_bf16 v[12:15], v[24:27], v[48:55], v110\n\t"
        "ds_write_b128 v113, v[24:27] offset:2048\n\t"
        "v_smfmac_f32_16x16x64_bf16 v[0:3], v[24:27], a[48:55], v110\n\t"
        "ds_read_b128 v[48:51], v117 offset:12288\n\t"
        "v_max3_u32 v111, v24, v25, v111\n\t"
        "v_smfmac_f32_16x16x64_bf16 v[4:7], v[24:27], a[56:63], v110\n\t"
        "ds_read_b128 v[52:55], v117 offset:13312\n\t"
        "v_max3_u32 v111, v26, v27, v111\n\t"
        "v_smfmac_f32_16x16x64_bf16 v[8:11], v[24:27], a[64:71], v110\n\t"
        "s_waitcnt vmcnt(0)\n\t"
        "ds_write_b128 v113, v[28:31] offset:3072\n\t"
        "v_max3_u32 v111, v28, v29, v111\n\t"
        "v_max3_u32 v111, v30, v31, v111\n\t"
        "v_cmp_eq_u32_e32 vcc, -1, v111\n\t"
        "s_waitcnt lgkmcnt(0)\n\t"
        "s_cbranch_vccnz LA_rdfail%=\n\t"
        "s_barrier\n\t"
        "ds_read_b128 v[16:19], v114 offset:0\n\t"
        "ds_read_b128 v[20:23], v114 offset:1024\n\t"
        "ds_read_b128 v[24:27], v114 offset:2048\n\t"
        "v_smfmac_f32_16x16x64_bf16 v[12:15], v[28:31], v[56:63], v110\n\t"
        "ds_read_b128 v[56:59], v117 offset:14336\n\t"
        "v_smfmac_f32_16x16x64_bf16 v[0:3], v[28:31], a[72:79], v110\n\t"
        "ds_read_b128 v[60:63], v117 offset:15360\n\t"
        "v_smfmac_f32_16x16x64_bf16 v[4:7], v[28:31], a[80:87], v110\n\t"
        "v_smfmac_f32_16x16x64_bf16 v[8:11], v[28:31], a[88:95], v110\n\t"
        "ds_read_b128 v[28:31], v114 offset:3072\n\t"
        "s_waitcnt lgkmcnt(5)\n\t"
        "v_smfmac_f32_16x16x64_bf16 v[12:15], v[16:19], v[32:39], v110\n\t"
        "ds_read_b128 v[32:35], v117 offset:16384\n\t"
        "v_smfmac_f32_16x16x64_bf16 v[0:3], v[16:19], a[96:103], v110\n\t"
        "ds_read_b128 v[36:39], v117 offset:17408\n\t"
        "v_smfmac_f32_16x16x64_bf16 v[4:7], v[16:19], a[104:111], v110\n\t"
        "v_smfmac_f32_16x16x64_bf16 v[8:11], v[16:19], a[112:119], v110\n\t"
        "ds_read_b128 v[16:19], v115 offset:0\n\t"
        "s_waitcnt lgkmcnt(7)\n\t"
        "v_smfmac_f32_16x16x64_bf16 v[12:15], v[20:23], v[40:47], v110\n\t"
        "ds_read_b128 v[40:43], v117 offset:18432\n\t"
        "v_smfmac_f32_16x16x64_bf16 v[0:3], v[20:23], a[120:127], v110\n\t"
        "ds_read_b128 v[44:47], v117 offset:19456\n\t"
        "v_smfmac_f32_16x16x64_bf16 v[4:7], v[20:23], a[128:135], v110\n\t"
        "v_smfmac_f32_16x16x64_bf16 v[8:11], v[20:23], a[136:143], v110\n\t"
        "ds_read_b128 v[20:23], v115 offset:1024\n\t"
        "s_waitcnt lgkmcnt(9)\n\t"
        "v_smfmac_f32_16x16x64_bf16 v[12:15], v[24:27], v[48:55], v110\n\t"
        "ds_read_b128 v[48:51], v117 offset:20480\n\t"
        "v_smfmac_f32_16x16x64_bf16 v[0:3], v[24:27], a[144:151], v110\n\t"
        "ds_read_b128 v[52:55], v117 offset:21504\n\t"
        "v_smfmac_f32_16x16x64_bf16 v[4:7], v[24:27], a[152:159], v110\n\t"
        "v_smfmac_f32_16x16x64_bf16 v[8:11], v[24:27], a[160:167], v110\n\t"
        "ds_read_b128 v[24:27], v115 offset:2048\n\t"
        "s_waitcnt lgkmcnt(9)\n\t"
        "v_smfmac_f32_16x16x64_bf16 v[12:15], v[28:31], v[56:63], v110\n\t"
        "ds_read_b128 v[56:59], v124 offset:3072\n\t"
        "v_smfmac_f32_16x16x64_bf16 v[0:3], v[28:31], a[168:175], v110\n\t"
        "ds_read_b128 v[60:63], v124 offset:7168\n\t"
        "v_smfmac_f32_16x16x64_bf16 v[4:7], v[28:31], a[176:183], v110\n\t"
        "v_smfmac_f32_16x16x64_bf16 v[8:11], v[28:31], a[184:191], v110\n\t"
        "ds_read_b128 v[28:31], v115 offset:3072\n\t"
        "s_waitcnt lgkmcnt(9)\n\t"
        "v_smfmac_f32_16x16x64_bf16 v[12:15], v[16:19], v[32:39], v110\n\t"
        "ds_read_b128 v[32:35], v124 offset:0\n\t"
        "v_smfmac_f32_16x16x64_bf16 v[0:3], v[16:19], a[192:199], v110\n\t"
        "ds_read_b128 v[36:39], v124 offset:4096\n\t"
        "v_smfmac_f32_16x16x64_bf16 v[4:7], v[16:19], a[200:207], v110\n\t"
        "v_smfmac_f32_16x16x64_bf16 v[8:11], v[16:19], a[208:215], v110\n\t"
        "ds_read_b128 v[16:19], v116 offset:0\n\t"
        "s_waitcnt lgkmcnt(9)\n\t"
        "v_smfmac_f32_16x16x64_bf16 v[12:15], v[20:23], v[40:47], v110\n\t"
        "ds_read_b128 v[40:43], v124 offset:1024\n\t"
        "v_smfmac_f32_16x16x64_bf16 v[0:3], v[20:23], a[216:223], v110\n\t"
        "ds_read_b128 v[44:47], v124 offset:5120\n\t"
        "v_smfmac_f32_16x16x64_bf16 v[4:7], v[20:23], a[224:231], v110\n\t"
        "v_smfmac_f32_16x16x64_bf16 v[8:11], v[20:23], a[232:239], v110\n\t"
        "ds_read_b128 v[20:23], v116 offset:1024\n\t"
        "s_waitcnt lgkmcnt(9)\n\t"
        "v_smfmac_f32_16x16x64_bf16 v[12:15], v[24:27], v[48:55], v110\n\t"
        "ds_read_b128 v[48:51], v124 offset:2048\n\t"
        "v_smfmac_f32_16x16x64_bf16 v[0:3], v[24:27], a[240:247], v110\n\t"
        "ds_read_b128 v[52:55], v124 offset:6144\n\t"
        "v_smfmac_f32_16x16x64_bf16 v[4:7], v[24:27], a[248:255], v110\n\t"
        "v_smfmac_f32_16x16x64_bf16 v[8:11], v[24:27], v[128:135], v110\n\t"
        "ds_read_b128 v[24:27], v116 offset:2048\n\t"
        "s_waitcnt lgkmcnt(9)\n\t"
        "v_smfmac_f32_16x16x64_bf16 v[12:15], v[28:31], v[64:71], v110\n\t"
        "v_smfmac_f32_16x16x64_bf16 v[0:3], v[28:31], v[136:143], v110\n\t"
        "v_smfmac_f32_16x16x64_bf16 v[4:7], v[28:31], v[144:151], v110\n\t"
        "v_smfmac_f32_16x16x64_bf16 v[8:11], v[28:31], v[152:159], v110\n\t"
        "ds_read_b128 v[28:31], v116 offset:3072\n\t"
        "s_waitcnt lgkmcnt(7)\n\t"
        "v_smfmac_f32_16x16x64_bf16 v[12:15], v[16:19], v[72:79], v110\n\t"
        "v_smfmac_f32_16x16x64_bf16 v[0:3], v[16:19], v[160:167], v110\n\t"
        "v_smfmac_f32_16x16x64_bf16 v[4:7], v[16:19], v[168:175], v110\n\t"
        "v_smfmac_f32_16x16x64_bf16 v[8:11], v[16:19], v[176:183], v110\n\t"
        "s_waitcnt lgkmcnt(4)\n\t"
        "v_smfmac_f32_16x16x64_bf16 v[0:3], v[20:23], v[184:191], v110\n\t"
        "v_smfmac_f32_16x16x64_bf16 v[4:7], v[20:23], v[192:199], v110\n\t"
        "s_waitcnt lgkmcnt(1)\n\t"
        "v_smfmac_f32_16x16x64_bf16 v[0:3], v[24:27], v[208:215], v110\n\t"
        "v_smfmac_f32_16x16x64_bf16 v[4:7], v[24:27], v[216:223], v110\n\t"
        "s_waitcnt lgkmcnt(0)\n\t"
        "v_smfmac_f32_16x16x64_bf16 v[0:3], v[28:31], v[232:239], v110\n\t"
        "v_smfmac_f32_16x16x64_bf16 v[4:7], v[28:31], v[240:247], v110\n\t"
        "v_smfmac_f32_16x16x64_bf16 v[12:15], v[20:23], v[80:87], v110\n\t"
        "s_nop 1\n\t"
        "v_smfmac_f32_16x16x64_bf16 v[8:11], v[20:23], v[200:207], v110\n\t"
        "s_nop 1\n\t"
        "v_smfmac_f32_16x16x64_bf16 v[12:15], v[24:27], v[88:95], v110\n\t"
        "s_nop 0\n\t"
        "v_permlane32_swap_b32_e32 v0, v4\n\t"
        "v_permlane32_swap_b32_e32 v1, v5\n\t"
        "v_permlane32_swap_b32_e32 v2, v6\n\t"
        "v_smfmac_f32_16x16x64_bf16 v[8:11], v[24:27], v[224:231], v110\n\t"
        "v_permlane32_swap_b32_e32 v3, v7\n\t"
        "v_add_f32_e32 v0, v0, v4\n\t"
        "v_add_f32_e32 v1, v1, v5\n\t"
        "v_smfmac_f32_16x16x64_bf16 v[12:15], v[28:31], v[96:103], v110\n\t"
        "v_add_f32_e32 v2, v2, v6\n\t"
        "v_add_f32_e32 v3, v3, v7\n\t"
        "v_cvt_pk_bf16_f32 v4, v0, v1\n\t"
        "v_smfmac_f32_16x16x64_bf16 v[8:11], v[28:31], v[248:255], v110\n\t"
        "v_cvt_pk_bf16_f32 v5, v2, v3\n\t"
        "ds_write_b64 v118, v[4:5]\n\t"
        "s_lshl_b32 s34, s43, 11\n\t"
        "v_add_u32_e32 v108, s34, v124\n\t"
        "s_nop 3\n\t"
        "v_permlane32_swap_b32_e32 v8, v12\n\t"
        "v_permlane32_swap_b32_e32 v9, v13\n\t"
        "v_permlane32_swap_b32_e32 v10, v14\n\t"
        "v_permlane32_swap_b32_e32 v11, v15\n\t"
        "v_add_f32_e32 v8, v8, v12\n\t"
        "v_add_f32_e32 v9, v9, v13\n\t"
        "v_add_f32_e32 v10, v10, v14\n\t"
        "v_add_f32_e32 v11, v11, v15\n\t"
        "v_cvt_pk_bf16_f32 v12, v8, v9\n\t"
        "v_cvt_pk_bf16_f32 v13, v10, v11\n\t"
        "ds_write_b64 v118, v[12:13] offset:256\n\t"
        "buffer_load_dwordx4 v[104:107], v120, s[8:11], s26 offen sc1\n\t"
        "s_waitcnt lgkmcnt(0)\n\t"
        "s_barrier\n\t"
        "ds_read_b64_tr_b16 v[16:17], v119 offset:0\n\t"
        "ds_read_b64_tr_b16 v[18:19], v119 offset:32\n\t"
        "ds_read_b64_tr_b16 v[20:21], v119 offset:256\n\t"
        "ds_read_b64_tr_b16 v[22:23], v119 offset:288\n\t"
        "ds_read_b64_tr_b16 v[24:25], v119 offset:512\n\t"
        "ds_read_b64_tr_b16 v[26:27], v119 offset:544\n\t"
        "ds_read_b64_tr_b16 v[28:29], v119 offset:768\n\t"
        "ds_read_b64_tr_b16 v[30:31], v119 offset:800\n\t"
        "buffer_store_dword v126, v127, s[12:15], 0 offen sc1\n\t"
        "s_waitcnt lgkmcnt(6)\n\t"
        "v_mfma_f32_16x16x32_bf16 v[0:3], v[32:35], v[16:19], 0\n\t"
        "ds_read_b64_tr_b16 v[32:33], v119 offset:2112\n\t"
        "ds_read_b64_tr_b16 v[34:35], v119 offset:2144\n\t"
        "v_mfma_f32_16x16x32_bf16 v[4:7], v[36:39], v[16:19], 0\n\t"
        "ds_read_b64_tr_b16 v[36:37], v119 offset:2368\n\t"
        "ds_read_b64_tr_b16 v[38:39], v119 offset:2400\n\t"
        "s_waitcnt lgkmcnt(8)\n\t"
        "v_mfma_f32_16x16x32_bf16 v[0:3], v[40:43], v[20:23], v[0:3]\n\t"
        "ds_read_b64_tr_b16 v[40:41], v119 offset:2624\n\t"
        "ds_read_b64_tr_b16 v[42:43], v119 offset:2656\n\t"
        "v_mfma_f32_16x16x32_bf16 v[4:7], v[44:47], v[20:23], v[4:7]\n\t"
        "ds_read_b64_tr_b16 v[44:45], v119 offset:2880\n\t"
        "ds_read_b64_tr_b16 v[46:47], v119 offset:2912\n\t"
        "s_waitcnt lgkmcnt(10)\n\t"
        "v_mfma_f32_16x16x32_bf16 v[0:3], v[48:51], v[24:27], v[0:3]\n\t"
        "ds_read_b128 v[48:51], v108 offset:16384\n\t"
        "v_mfma_f32_16x16x32_bf16 v[4:7], v[52:55], v[24:27], v[4:7]\n\t"
        "ds_read_b128 v[52:55], v108 offset:17408\n\t"
        "s_waitcnt lgkmcnt(10)\n\t"
        "v_mfma_f32_16x16x32_bf16 v[0:3], v[56:59], v[28:31], v[0:3]\n\t"
        "v_mfma_f32_16x16x32_bf16 v[4:7], v[60:63], v[28:31], v[4:7]\n\t"
        "s_waitcnt lgkmcnt(2)\n\t"
        "v_mfma_f32_16x16x32_bf16 v[8:11], v[32:35], v[16:19], 0\n\t"
        "s_nop 1\n\t"
        "v_mfma_f32_16x16x32_bf16 v[8:11], v[36:39], v[20:23], v[8:11]\n\t"
        "s_nop 1\n\t"
        "v_mfma_f32_16x16x32_bf16 v[8:11], v[40:43], v[24:27], v[8:11]\n\t"
        "v_max3_f32 v59, v0, v1, v2\n\t"
        "v_max3_f32 v59, v59, v3, v4\n\t"
        "v_mfma_f32_16x16x32_bf16 v[8:11], v[44:47], v[28:31], v[8:11]\n\t"
        "v_max3_f32 v59, v59, v5, v6\n\t"
        "v_max_f32_e32 v60, v59, v7\n\t"
        "v_max_f32_e32 v59, v59, v7\n\t"
        "v_cndmask_b32_e64 v0, v0, v4, s[38:39]\n\t"
        "v_cndmask_b32_e64 v1, v1, v5, s[38:39]\n\t"
        "v_permlane32_swap_b32_e32 v59, v60\n\t"
        "v_cndmask_b32_e64 v2, v2, v6, s[38:39]\n\t"
        "v_cndmask_b32_e64 v3, v3, v7, s[38:39]\n\t"
        "v_max3_f32 v59, v59, v60, v8\n\t"
        "v_max3_f32 v59, v59, v9, v10\n\t"
        "v_max_f32_e32 v60, v59, v11\n\t"
        "v_max_f32_e32 v59, v59, v11\n\t"
        "v_cndmask_b32_e64 v8, v8, v9, s[38:39]\n\t"
        "v_cndmask_b32_e64 v10, v10, v11, s[38:39]\n\t"
        "v_permlane16_swap_b32_e32 v59, v60\n\t"
        "v_max_f32_e32 v59, v59, v60\n\t"
        "v_mul_f32_e32 v59, 0xbe02940c, v59\n\t"
        "v_cndmask_b32_e64 v58, v8, v10, s[40:41]\n\t"
        "v_fmamk_f32 v0, v0, 0x3e02940c, v59\n\t"
        "v_exp_f32_e32 v0, v0\n\t"
        "v_fmamk_f32 v1, v1, 0x3e02940c, v59\n\t"
        "v_exp_f32_e32 v1, v1\n\t"
        "v_fmamk_f32 v2, v2, 0x3e02940c, v59\n\t"
        "v_exp_f32_e32 v2, v2\n\t"
        "v_fmamk_f32 v3, v3, 0x3e02940c, v59\n\t"
        "v_exp_f32_e32 v3, v3\n\t"
        "v_fmamk_f32 v58, v58, 0x3e02940c, v59\n\t"
        "v_cvt_pk_bf16_f32 v12, v0, v1\n\t"
        "v_cvt_pk_bf16_f32 v13, v2, v3\n\t"
        "v_exp_f32_e32 v58, v58\n\t"
        "v_add_f32_e32 v60, v0, v1\n\t"
        "v_mov_b32_dpp v14, v12 row_ror:8 row_mask:0xf bank_mask:0xf\n\t"
        "v_mov_b32_dpp v15, v13 row_ror:8 row_mask:0xf bank_mask:0xf\n\t"
        "v_mov_b32_dpp v59, v58 row_ror:8 row_mask:0xf bank_mask:0xf\n\t"
        "v_add_f32_e32 v61, v2, v3\n\t"
        "s_waitcnt lgkmcnt(0)\n\t"
        "v_mfma_f32_16x16x32_bf16 v[4:7], v[48:51], v[12:15], 0\n\t"
        "v_cvt_pk_bf16_f32 v59, v58, v59\n\t"
        "v_add_f32_e32 v60, v60, v58\n\t"
        "v_mfma_f32_16x16x32_bf16 v[8:11], v[52:55], v[12:15], 0\n\t"
        "v_cndmask_b32_e64 v56, v59, 0, s[40:41]\n\t"
        "v_cndmask_b32_e64 v57, 0, v59, s[40:41]\n\t"
        "v_add_f32_e32 v59, v60, v61\n\t"
        "s_waitcnt vmcnt(1)\n\t"
        "v_max3_u32 v111, v104, v105, v106\n\t"
        "v_mfma_f32_16x16x16_bf16 v[4:7], v[104:105], v[56:57], v[4:7]\n\t"
        "v_add_f32_dpp v60, v59, v59 row_ror:8 row_mask:0xf bank_mask:0xf\n\t"
        "v_add_f32_dpp v61, v59, v59 row_ror:8 row_mask:0xf bank_mask:0xf\n\t"
        "v_mfma_f32_16x16x16_bf16 v[8:11], v[106:107], v[56:57], v[8:11]\n\t"
        "v_max_u32_e32 v111, v111, v107\n\t"
        "v_permlane16_swap_b32_e32 v60, v61\n\t"
        "v_add_f32_e32 v59, v60, v61\n\t"
        "v_add_f32_e32 v60, v60, v61\n\t"
        "v_cmp_eq_u32_e32 vcc, -1, v111\n\t"
        "s_nop 0\n\t"
        "v_permlane32_swap_b32_e32 v59, v60\n\t"
        "v_add_f32_e32 v59, v59, v60\n\t"
        "v_rcp_f32_e32 v59, v59\n\t"
        "s_cbranch_vccnz LA_vmfail%=\n\t"
        "LA_vmok%=:\n\t"
        "v_mul_f32_e32 v4, v4, v59\n\t"
        "v_mul_f32_e32 v5, v5, v59\n\t"
        "v_cvt_pk_bf16_f32 v0, v4, v5\n\t"
        "v_min_u32_e32 v0, -2, v0\n\t"
        "v_mul_f32_e32 v6, v6, v59\n\t"
        "v_mul_f32_e32 v7, v7, v59\n\t"
        "v_cvt_pk_bf16_f32 v1, v6, v7\n\t"
        "v_min_u32_e32 v1, -2, v1\n\t"
        "v_mul_f32_e32 v8, v8, v59\n\t"
        "v_mul_f32_e32 v9, v9, v59\n\t"
        "v_cvt_pk_bf16_f32 v2, v8, v9\n\t"
        "v_min_u32_e32 v2, -2, v2\n\t"
        "v_mul_f32_e32 v10, v10, v59\n\t"
        "v_mul_f32_e32 v11, v11, v59\n\t"
        "v_cvt_pk_bf16_f32 v3, v10, v11\n\t"
        "v_min_u32_e32 v3, -2, v3\n\t"
        "buffer_store_dwordx4 v[0:3], v121, s[8:11], s26 offen sc1\n\t"
        "s_memrealtime s[52:53]\n\t"
        "s_and_b32 s35, s43, 1\n\t"
        "s_lshl_b32 s35, s35, 12\n\t"
        "v_add_u32_e32 v109, s35, v124\n\t"
        "ds_read_b128 v[16:19], v109 offset:8192\n\t"
        "ds_read_b128 v[20:23], v109 offset:9216\n\t"
        "ds_read_b128 v[24:27], v109 offset:10240\n\t"
        "ds_read_b128 v[28:31], v109 offset:11264\n\t"
        "ds_read_b128 v[56:59], v124 offset:0\n\t"
        "ds_read_b128 v[60:63], v124 offset:4096\n\t"
        "ds_read_b128 v[12:15], v124 offset:1024\n\t"
        "v_xor_b32_e32 v7, 0x800, v108\n\t"
        "s_add_u32 s48, s24, 1\n\t"
        "s_min_u32 s48, s48, 63\n\t"
        "buffer_load_dwordx2 v[108:109], v125, s[8:11], s26 offen sc1\n\t"
        "s_lshl_b32 s35, s44, 10\n\t"
        "v_lshrrev_b32_e32 v111, 1, v125\n\t"
        "v_and_b32_e32 v111, 0x3c0, v111\n\t"
        "buffer_load_dword v111, v111, s[12:15], s35 offen offset:0 sc1\n\t"
        "s_lshr_b32 s36, s48, 1\n\t"
        "s_cmp_gt_u32 s54, s36\n\t"
        "s_cbranch_scc1 LA_kvok%=\n\t"
        "s_mov_b32 s42, 0\n\t"
        "LA_kv%=:\n\t"
        "s_waitcnt vmcnt(0)\n\t"
        "v_min_u32_dpp v111, v111, v111 quad_perm:[1,0,3,2] row_mask:0xf bank_mask:0xf\n\t"
        "s_nop 1\n\t"
        "v_min_u32_dpp v111, v111, v111 quad_perm:[2,3,0,1] row_mask:0xf bank_mask:0xf\n\t"
        "s_nop 1\n\t"
        "v_min_u32_dpp v111, v111, v111 row_ror:4 row_mask:0xf bank_mask:0xf\n\t"
        "s_nop 1\n\t"
        "v_min_u32_dpp v111, v111, v111 row_ror:8 row_mask:0xf bank_mask:0xf\n\t"
        "s_nop 0\n\t"
        "v_readfirstlane_b32 s54, v111\n\t"
        "s_nop 0\n\t"
        "s_cmp_gt_u32 s54, s36\n\t"
        "s_cbranch_scc1 LA_kvok%=\n\t"
        "s_sleep 4\n\t"
        "s_lshl_b32 s35, s44, 10\n\t"
        "v_lshrrev_b32_e32 v111, 1, v125\n\t"
        "v_and_b32_e32 v111, 0x3c0, v111\n\t"
        "buffer_load_dword v111, v111, s[12:15], s35 offen offset:0 sc1\n\t"
        "s_add_u32 s42, s42, 1\n\t"
        "s_cmp_lt_u32 s42, 0x100000\n\t"
        "s_cbranch_scc1 LA_kv%=\n\t"
        "s_movk_i32 s34, 16\n\t"
        "s_branch LA_die%=\n\t"
        "LA_kvok%=:\n\t"
        "s_add_u32 s35, s24, 1\n\t"
        "s_and_b32 s36, s35, 1\n\t"
        "s_mul_i32 s36, s36, 24576\n\t"
        "s_add_u32 s36, s36, s55\n\t"
        "s_lshl_b32 s37, s48, 18\n\t"
        "s_lshl_b32 s34, s43, 11\n\t"
        "s_add_u32 s37, s37, s34\n\t"
        "s_waitcnt lgkmcnt(3)\n\t"
        "v_mfma_f32_16x16x32_bf16 v[8:11], v[32:35], v[16:19], 0\n\t"
        "v_mfma_f32_16x16x32_bf16 v[8:11], v[36:39], v[20:23], v[8:11]\n\t"
        "v_mfma_f32_16x16x32_bf16 v[8:11], v[40:43], v[24:27], v[8:11]\n\t"
        "v_mfma_f32_16x16x32_bf16 v[8:11], v[44:47], v[28:31], v[8:11]\n\t"
        "v_mov_b32_e32 v43, v7\n\t"
        "s_waitcnt lgkmcnt(2)\n\t"
        "v_mfma_f32_16x16x32_bf16 v[0:3], v[56:59], v[16:19], 0\n\t"
        "ds_read_b128 v[56:59], v124 offset:5120\n\t"
        "s_waitcnt lgkmcnt(2)\n\t"
        "v_mfma_f32_16x16x32_bf16 v[4:7], v[60:63], v[16:19], 0\n\t"
        "ds_read_b128 v[60:63], v124 offset:2048\n\t"
        "s_waitcnt lgkmcnt(2)\n\t"
        "v_mfma_f32_16x16x32_bf16 v[0:3], v[12:15], v[20:23], v[0:3]\n\t"
        "ds_read_b128 v[12:15], v124 offset:6144\n\t"
        "s_waitcnt lgkmcnt(2)\n\t"
        "v_mfma_f32_16x16x32_bf16 v[4:7], v[56:59], v[20:23], v[4:7]\n\t"
        "ds_read_b128 v[56:59], v124 offset:3072\n\t"
        "s_waitcnt lgkmcnt(2)\n\t"
        "v_mfma_f32_16x16x32_bf16 v[0:3], v[60:63], v[24:27], v[0:3]\n\t"
        "ds_read_b128 v[60:63], v124 offset:7168\n\t"
        "s_waitcnt lgkmcnt(2)\n\t"
        "v_mfma_f32_16x16x32_bf16 v[4:7], v[12:15], v[24:27], v[4:7]\n\t"
        "v_xor_b32_e32 v42, 0x200, v120\n\t"
        "buffer_load_dwordx4 v[12:15], v42, s[8:11], s26 offen sc1\n\t"
        "s_waitcnt lgkmcnt(1)\n\t"
        "v_mfma_f32_16x16x32_bf16 v[0:3], v[56:59], v[28:31], v[0:3]\n\t"
        "s_waitcnt lgkmcnt(0)\n\t"
        "v_mfma_f32_16x16x32_bf16 v[4:7], v[60:63], v[28:31], v[4:7]\n\t"
        "ds_read_b128 v[56:59], v43 offset:16384\n\t"
        "ds_read_b128 v[60:63], v43 offset:17408\n\t"
        "v_mov_b64_e32 v[44:45], -1\n\t"
        "v_mov_b64_e32 v[46:47], -1\n\t"
        "v_mov_b32_e32 v40, 0xce6e6b28\n\t"
        "s_add_u32 m0, s36, 0\n\t"
        "s_add_u32 s34, s37, 29548544\n\t"
        "buffer_load_dwordx4 v122, s[12:15], s34 offen sc1 lds\n\t"
        "v_cvt_pk_bf16_f32 v8, 0, v8\n\t"
        "v_cvt_pk_bf16_f32 v9, 0, v9\n\t"
        "v_cvt_pk_bf16_f32 v10, 0, v10\n\t"
        "v_cvt_pk_bf16_f32 v11, 0, v11\n\t"
        "v_cvt_pk_bf16_f32 v0, 0, v0\n\t"
        "v_cvt_pk_bf16_f32 v1, 0, v1\n\t"
        "s_add_u32 m0, s36, 1024\n\t"
        "s_add_u32 s34, s37, 29549568\n\t"
        "buffer_load_dwordx4 v122, s[12:15], s34 offen sc1 lds\n\t"
        "v_cvt_pk_bf16_f32 v2, 0, v2\n\t"
        "v_cvt_pk_bf16_f32 v3, 0, v3\n\t"
        "v_cvt_pk_bf16_f32 v4, 0, v4\n\t"
        "v_cvt_pk_bf16_f32 v5, 0, v5\n\t"
        "v_cvt_pk_bf16_f32 v6, 0, v6\n\t"
        "v_cvt_pk_bf16_f32 v7, 0, v7\n\t"
        "s_add_u32 m0, s36, 8192\n\t"
        "s_add_u32 s34, s37, 12771328\n\t"
        "buffer_load_dwordx4 v122, s[12:15], s34 offen sc1 lds\n\t"
        "v_mul_f32_e32 v8, 0x3db504f3, v8\n\t"
        "v_mul_f32_e32 v9, 0x3db504f3, v9\n\t"
        "v_mul_f32_e32 v10, 0x3db504f3, v10\n\t"
        "v_mul_f32_e32 v11, 0x3db504f3, v11\n\t"
        "v_mul_f32_e32 v0, 0x3db504f3, v0\n\t"
        "v_mul_f32_e32 v1, 0x3db504f3, v1\n\t"
        "s_add_u32 m0, s36, 9216\n\t"
        "s_add_u32 s34, s37, 12772352\n\t"
        "buffer_load_dwordx4 v122, s[12:15], s34 offen sc1 lds\n\t"
        "v_mul_f32_e32 v2, 0x3db504f3, v2\n\t"
        "v_mul_f32_e32 v3, 0x3db504f3, v3\n\t"
        "v_mul_f32_e32 v4, 0x3db504f3, v4\n\t"
        "v_mul_f32_e32 v5, 0x3db504f3, v5\n\t"
        "v_mul_f32_e32 v6, 0x3db504f3, v6\n\t"
        "v_mul_f32_e32 v7, 0x3db504f3, v7\n\t"
        "s_add_u32 m0, s36, 16384\n\t"
        "s_add_u32 s34, s37, 46325760\n\t"
        "buffer_load_dwordx4 v122, s[12:15], s34 offen sc1 lds\n\t"
        "v_cvt_pk_bf16_f32 v8, 0, v8\n\t"
        "v_cvt_pk_bf16_f32 v9, 0, v9\n\t"
        "v_cvt_pk_bf16_f32 v10, 0, v10\n\t"
        "v_cvt_pk_bf16_f32 v11, 0, v11\n\t"
        "v_cvt_pk_bf16_f32 v0, 0, v0\n\t"
        "v_cvt_pk_bf16_f32 v1, 0, v1\n\t"
        "s_add_u32 m0, s36, 17408\n\t"
        "s_add_u32 s34, s37, 46326784\n\t"
        "buffer_load_dwordx4 v122, s[12:15], s34 offen sc1 lds\n\t"
        "v_cvt_pk_bf16_f32 v2, 0, v2\n\t"
        "v_cvt_pk_bf16_f32 v3, 0, v3\n\t"
        "v_cvt_pk_bf16_f32 v4, 0, v4\n\t"
        "v_cvt_pk_bf16_f32 v5, 0, v5\n\t"
        "v_cvt_pk_bf16_f32 v6, 0, v6\n\t"
        "v_cvt_pk_bf16_f32 v7, 0, v7\n\t"
        "buffer_store_dwordx4 v[44:47], v121, s[8:11], s28 offen sc1\n\t"
        "s_pack_ll_b32_b16 s34, s52, s24\n\t"
        "v_mov_b32_e32 v42, s34\n\t"
        "s_add_u32 s35, s26, s57\n\t"
        "s_mov_b64 exec, 1\n\t"
        "buffer_store_dword v42, off, s[8:11], s35 sc1\n\t"
        "s_mov_b64 exec, -1\n\t"
        "v_cndmask_b32_e64 v0, v40, v0, s[60:61]\n\t"
        "v_cndmask_b32_e64 v1, v40, v1, s[62:63]\n\t"
        "v_cndmask_b32_e64 v2, v40, v2, s[64:65]\n\t"
        "v_cndmask_b32_e64 v3, v40, v3, s[66:67]\n\t"
        "v_cndmask_b32_e64 v4, v40, v4, s[68:69]\n\t"
        "v_cndmask_b32_e64 v5, v40, v5, s[70:71]\n\t"
        "v_cndmask_b32_e64 v6, v40, v6, s[72:73]\n\t"
        "v_cndmask_b32_e64 v7, v40, v7, s[74:75]\n\t"
        "v_max3_f32 v41, v0, v1, v2\n\t"
        "v_max3_f32 v41, v41, v3, v4\n\t"
        "v_max3_f32 v41, v41, v5, v6\n\t"
        "v_max3_f32 v41, v41, v7, v8\n\t"
        "v_max3_f32 v41, v41, v9, v10\n\t"
        "v_max_f32_e32 v42, v41, v11\n\t"
        "v_max_f32_e32 v41, v41, v11\n\t"
        "s_nop 1\n\t"
        "v_permlane16_swap_b32_e32 v41, v42\n\t"
        "v_max_f32_e32 v43, v41, v42\n\t"
        "v_max_f32_e32 v41, v41, v42\n\t"
        "s_nop 1\n\t"
        "v_permlane32_swap_b32_e32 v41, v43\n\t"
        "v_max_f32_e32 v41, v41, v43\n\t"
        "v_mul_f32_e32 v41, 0xbfb8aa3b, v41\n\t"
        "v_fmamk_f32 v0, v0, 0x3fb8aa3b, v41\n\t"
        "v_exp_f32_e32 v0, v0\n\t"
        "v_fmamk_f32 v1, v1, 0x3fb8aa3b, v41\n\t"
        "v_exp_f32_e32 v1, v1\n\t"
        "v_fmamk_f32 v2, v2, 0x3fb8aa3b, v41\n\t"
        "v_exp_f32_e32 v2, v2\n\t"
        "v_fmamk_f32 v3, v3, 0x3fb8aa3b, v41\n\t"
        "v_exp_f32_e32 v3, v3\n\t"
        "v_fmamk_f32 v4, v4, 0x3fb8aa3b, v41\n\t"
        "v_exp_f32_e32 v4, v4\n\t"
        "v_fmamk_f32 v5, v5, 0x3fb8aa3b, v41\n\t"
        "v_exp_f32_e32 v5, v5\n\t"
        "v_fmamk_f32 v6, v6, 0x3fb8aa3b, v41\n\t"
        "v_exp_f32_e32 v6, v6\n\t"
        "v_fmamk_f32 v7, v7, 0x3fb8aa3b, v41\n\t"
        "v_exp_f32_e32 v7, v7\n\t"
        "v_fmamk_f32 v8, v8, 0x3fb8aa3b, v41\n\t"
        "v_exp_f32_e32 v8, v8\n\t"
        "v_fmamk_f32 v9, v9, 0x3fb8aa3b, v41\n\t"
        "v_exp_f32_e32 v9, v9\n\t"
        "v_fmamk_f32 v10, v10, 0x3fb8aa3b, v41\n\t"
        "v_exp_f32_e32 v10, v10\n\t"
        "v_fmamk_f32 v11, v11, 0x3fb8aa3b, v41\n\t"
        "v_exp_f32_e32 v11, v11\n\t"
        "v_cndmask_b32_e64 v8, v8, 0, s[40:41]\n\t"
        "v_cndmask_b32_e64 v9, v9, 0, s[40:41]\n\t"
        "v_cndmask_b32_e64 v10, v10, 0, s[40:41]\n\t"
        "v_cndmask_b32_e64 v11, v11, 0, s[40:41]\n\t"
        "v_add_f32_e32 v41, v0, v1\n\t"
        "v_add_f32_e32 v41, v41, v2\n\t"
        "v_add_f32_e32 v41, v41, v3\n\t"
        "v_add_f32_e32 v41, v41, v4\n\t"
        "v_add_f32_e32 v41, v41, v5\n\t"
        "v_add_f32_e32 v41, v41, v6\n\t"
        "v_add_f32_e32 v41, v41, v7\n\t"
        "v_add_f32_e32 v41, v41, v8\n\t"
        "v_add_f32_e32 v41, v41, v9\n\t"
        "v_add_f32_e32 v41, v41, v10\n\t"
        "v_add_f32_e32 v42, v41, v11\n\t"
        "v_add_f32_e32 v41, v41, v11\n\t"
        "s_nop 1\n\t"
        "v_permlane16_swap_b32_e32 v41, v42\n\t"
        "v_add_f32_e32 v43, v41, v42\n\t"
        "v_add_f32_e32 v41, v41, v42\n\t"
        "s_nop 1\n\t"
        "v_permlane32_swap_b32_e32 v41, v43\n\t"
        "v_add_f32_e32 v41, v41, v43\n\t"
        "v_rcp_f32_e32 v41, v41\n\t"
        "s_nop 0\n\t"
        "v_mul_f32_e32 v0, v0, v41\n\t"
        "v_mul_f32_e32 v1, v1, v41\n\t"
        "v_cvt_pk_bf16_f32 v32, v0, v1\n\t"
        "v_mul_f32_e32 v2, v2, v41\n\t"
        "v_mul_f32_e32 v3, v3, v41\n\t"
        "v_cvt_pk_bf16_f32 v33, v2, v3\n\t"
        "v_mul_f32_e32 v4, v4, v41\n\t"
        "v_mul_f32_e32 v5, v5, v41\n\t"
        "v_cvt_pk_bf16_f32 v34, v4, v5\n\t"
        "v_mul_f32_e32 v6, v6, v41\n\t"
        "v_mul_f32_e32 v7, v7, v41\n\t"
        "v_cvt_pk_bf16_f32 v35, v6, v7\n\t"
        "v_mul_f32_e32 v8, v8, v41\n\t"
        "v_mul_f32_e32 v9, v9, v41\n\t"
        "v_cvt_pk_bf16_f32 v36, v8, v9\n\t"
        "v_mul_f32_e32 v10, v10, v41\n\t"
        "v_mul_f32_e32 v11, v11, v41\n\t"
        "v_cvt_pk_bf16_f32 v37, v10, v11\n\t"
        "s_waitcnt lgkmcnt(0)\n\t"
        "v_mfma_f32_16x16x32_bf16 v[16:19], v[48:51], v[32:35], 0\n\t"
        "v_mfma_f32_16x16x32_bf16 v[20:23], v[52:55], v[32:35], 0\n\t"
        "v_mfma_f32_16x16x32_bf16 v[24:27], v[56:59], v[32:35], 0\n\t"
        "v_mfma_f32_16x16x32_bf16 v[28:31], v[60:63], v[32:35], 0\n\t"
        "s_waitcnt vmcnt(8)\n\t"
        "v_max3_u32 v43, v12, v13, v14\n\t"
        "v_max_u32_e32 v43, v43, v15\n\t"
        "v_cmp_eq_u32_e32 vcc, -1, v43\n\t"
        "s_nop 0\n\t"
        "s_cbranch_vccnz LA_v2fail%=\n\t"
        "LA_v2ok%=:\n\t"
        "v_mfma_f32_16x16x16_bf16 v[16:19], v[104:105], v[36:37], v[16:19]\n\t"
        "v_mfma_f32_16x16x16_bf16 v[20:23], v[106:107], v[36:37], v[20:23]\n\t"
        "v_mfma_f32_16x16x16_bf16 v[24:27], v[12:13], v[36:37], v[24:27]\n\t"
        "v_mfma_f32_16x16x16_bf16 v[28:31], v[14:15], v[36:37], v[28:31]\n\t"
        "s_lshl_b32 s34, s24, 18\n\t"
        "s_add_u32 s34, s34, 63102976\n\t"
        "v_xor_b32_e32 v43, 0x400, v123\n\t"
        "s_nop 1\n\t"
        "v_cvt_pk_bf16_f32 v0, v16, v17\n\t"
        "v_cvt_pk_bf16_f32 v1, v18, v19\n\t"
        "v_cvt_pk_bf16_f32 v2, v20, v21\n\t"
        "v_cvt_pk_bf16_f32 v3, v22, v23\n\t"
        "buffer_store_dwordx4 v[0:3], v123, s[12:15], s34 offen sc1\n\t"
        "v_cvt_pk_bf16_f32 v4, v24, v25\n\t"
        "v_cvt_pk_bf16_f32 v5, v26, v27\n\t"
        "v_cvt_pk_bf16_f32 v6, v28, v29\n\t"
        "v_cvt_pk_bf16_f32 v7, v30, v31\n\t"
        "buffer_store_dwordx4 v[4:7], v43, s[12:15], s34 offen sc1\n\t"
        "s_add_u32 s34, s24, 1\n\t"
        "v_mov_b32_e32 v126, s34\n\t"
        "s_cmp_eq_u32 s24, 63\n\t"
        "s_cbranch_scc1 LA_last%=\n\t"
        "v_add_u32_e32 v124, s56, v124\n\t"
        "s_sub_u32 s56, 0, s56\n\t"
        "ds_read_b128 v[32:35], v117 offset:0\n\t"
        "ds_read_b128 v[36:39], v117 offset:1024\n\t"
        "ds_read_b128 v[40:43], v117 offset:2048\n\t"
        "ds_read_b128 v[44:47], v117 offset:3072\n\t"
        "ds_read_b128 v[48:51], v117 offset:4096\n\t"
        "ds_read_b128 v[52:55], v117 offset:5120\n\t"
        "ds_read_b128 v[56:59], v117 offset:6144\n\t"
        "ds_read_b128 v[60:63], v117 offset:7168\n\t"
        "v_mov_b64_e32 v[0:1], 0\n\t"
        "v_mov_b64_e32 v[2:3], 0\n\t"
        "v_mov_b64_e32 v[4:5], 0\n\t"
        "v_mov_b64_e32 v[6:7], 0\n\t"
        "v_mov_b64_e32 v[8:9], 0\n\t"
        "v_mov_b64_e32 v[10:11], 0\n\t"
        "v_mov_b64_e32 v[12:13], 0\n\t"
        "v_mov_b64_e32 v[14:15], 0\n\t"
        "v_min_u32_dpp v111, v111, v111 quad_perm:[1,0,3,2] row_mask:0xf bank_mask:0xf\n\t"
        "s_nop 1\n\t"
        "v_min_u32_dpp v111, v111, v111 quad_perm:[2,3,0,1] row_mask:0xf bank_mask:0xf\n\t"
        "s_nop 1\n\t"
        "v_min_u32_dpp v111, v111, v111 row_ror:4 row_mask:0xf bank_mask:0xf\n\t"
        "s_nop 1\n\t"
        "v_min_u32_dpp v111, v111, v111 row_ror:8 row_mask:0xf bank_mask:0xf\n\t"
        "s_nop 0\n\t"
        "v_readfirstlane_b32 s54, v111\n\t"
        "v_and_b32_e32 v30, 0xff, v109\n\t"
        "v_cmp_ne_u32_e32 vcc, s24, v30\n\t"
        "s_cbranch_vccz LA_stok%=\n\t"
        "s_mov_b32 s42, 0\n\t"
        "LA_stre%=:\n\t"
        "buffer_load_dwordx2 v[108:109], v125, s[8:11], s26 offen sc1\n\t"
        "s_waitcnt vmcnt(0)\n\t"
        "v_and_b32_e32 v30, 0xff, v109\n\t"
        "v_cmp_ne_u32_e32 vcc, s24, v30\n\t"
        "s_cbranch_vccz LA_stok%=\n\t"
        "s_add_u32 s42, s42, 1\n\t"
        "s_cmp_lt_u32 s42, 0x100000\n\t"
        "s_cbranch_scc1 LA_stre%=\n\t"
        "s_movk_i32 s34, 5\n\t"
        "s_branch LA_die%=\n\t"
        "LA_stok%=:\n\t"
        "v_readfirstlane_b32 s35, v109\n\t"
        "v_subrev_u32_e32 v108, s25, v108\n\t"
        "s_bfe_u32 s29, s35, 0xc0008\n\t"
        "s_lshr_b32 s30, s35, 20\n\t"
        "v_max_i32_dpp v108, v108, v108 quad_perm:[1,0,3,2] row_mask:0xf bank_mask:0xf\n\t"
        "s_nop 1\n\t"
        "v_max_i32_dpp v108, v108, v108 quad_perm:[2,3,0,1] row_mask:0xf bank_mask:0xf\n\t"
        "s_nop 1\n\t"
        "v_max_i32_dpp v108, v108, v108 row_ror:4 row_mask:0xf bank_mask:0xf\n\t"
        "s_nop 1\n\t"
        "v_max_i32_dpp v108, v108, v108 row_ror:8 row_mask:0xf bank_mask:0xf\n\t"
        "v_mov_b32_e32 v109, v108\n\t"
        "s_nop 1\n\t"
        "v_permlane16_swap_b32_e32 v108, v109\n\t"
        "v_max_i32_e32 v108, v108, v109\n\t"
        "v_mov_b32_e32 v109, v108\n\t"
        "s_nop 1\n\t"
        "v_permlane32_swap_b32_e32 v108, v109\n\t"
        "v_max_i32_e32 v108, v108, v109\n\t"
        "s_nop 0\n\t"
        "v_readfirstlane_b32 s36, v108\n\t"
        "s_add_u32 s35, s31, s50\n\t"
        "s_mul_i32 s37, s50, 3\n\t"
        "s_min_u32 s35, s35, s37\n\t"
        "s_add_i32 s34, s36, s51\n\t"
        "s_cmp_gt_i32 s34, 0\n\t"
        "s_cselect_b32 s31, s35, 0\n\t"
        "s_cselect_b32 s34, s34, 0\n\t"
        "s_add_u32 s34, s34, s31\n\t"
        "s_add_u32 s37, s25, s34\n\t"
        "s_add_u32 s45, s37, s29\n\t"
        "s_add_u32 s25, s45, s30\n\t"
        "s_add_u32 s24, s24, 1\n\t"
        "s_mov_b32 s28, s26\n\t"
        "s_mov_b32 s26, s27\n\t"
        "s_add_u32 s27, s27, 0x80000\n\t"
        "s_and_b32 s27, s27, 0x1fffff\n\t"
        "s_branch LA_loop%=\n\t"
        "LA_last%=:\n\t"
        "s_waitcnt vmcnt(0)\n\t"
        "s_barrier\n\t"
        "buffer_store_dword v126, v127, s[12:15], 0 offen sc1\n\t"
        "s_endpgm\n\t"
        "LA_v2fail%=:\n\t"
        "s_mov_b32 s42, 0\n\t"
        "LA_v2re%=:\n\t"
        "v_xor_b32_e32 v43, 0x200, v120\n\t"
        "buffer_load_dwordx4 v[12:15], v43, s[8:11], s26 offen sc1\n\t"
        "s_waitcnt vmcnt(0)\n\t"
        "v_max3_u32 v43, v12, v13, v14\n\t"
        "v_max_u32_e32 v43, v43, v15\n\t"
        "v_cmp_eq_u32_e32 vcc, -1, v43\n\t"
        "s_nop 0\n\t"
        "s_cbranch_vccz LA_v2ok%=\n\t"
        "s_add_u32 s42, s42, 1\n\t"
        "s_cmp_lt_u32 s42, 0x100000\n\t"
        "s_cbranch_scc1 LA_v2re%=\n\t"
        "s_movk_i32 s34, 6\n\t"
        "s_branch LA_die%=\n\t"
        "LA_rdfail%=:\n\t"
        "s_or_b32 s47, s47, 1\n\t"
        "buffer_load_dwordx4 v[16:19], v112, s[8:11], s26 offen offset:0 sc1\n\t"
        "buffer_load_dwordx4 v[20:23], v112, s[8:11], s26 offen offset:1024 sc1\n\t"
        "buffer_load_dwordx4 v[24:27], v112, s[8:11], s26 offen offset:2048 sc1\n\t"
        "buffer_load_dwordx4 v[28:31], v112, s[8:11], s26 offen offset:3072 sc1\n\t"
        "s_nop 7\n\t"
        "ds_read_b128 v[32:35], v117 offset:0\n\t"
        "ds_read_b128 v[36:39], v117 offset:1024\n\t"
        "ds_read_b128 v[40:43], v117 offset:2048\n\t"
        "ds_read_b128 v[44:47], v117 offset:3072\n\t"
        "ds_read_b128 v[48:51], v117 offset:4096\n\t"
        "ds_read_b128 v[52:55], v117 offset:5120\n\t"
        "ds_read_b128 v[56:59], v117 offset:6144\n\t"
        "ds_read_b128 v[60:63], v117 offset:7168\n\t"
        "v_mov_b64_e32 v[0:1], 0\n\t"
        "v_mov_b64_e32 v[2:3], 0\n\t"
        "v_mov_b64_e32 v[4:5], 0\n\t"
        "v_mov_b64_e32 v[6:7], 0\n\t"
        "v_mov_b64_e32 v[8:9], 0\n\t"
        "v_mov_b64_e32 v[10:11], 0\n\t"
        "v_mov_b64_e32 v[12:13], 0\n\t"
        "v_mov_b64_e32 v[14:15], 0\n\t"
        "s_branch LA_rdre%=w\n\t"
        "LA_rdre%=:\n\t"
        "buffer_load_dwordx4 v[16:19], v112, s[8:11], s26 offen offset:0 sc1\n\t"
        "buffer_load_dwordx4 v[20:23], v112, s[8:11], s26 offen offset:1024 sc1\n\t"
        "buffer_load_dwordx4 v[24:27], v112, s[8:11], s26 offen offset:2048 sc1\n\t"
        "buffer_load_dwordx4 v[28:31], v112, s[8:11], s26 offen offset:3072 sc1\n\t"
        "LA_rdre%=w:\n\t"
        "s_waitcnt vmcnt(0) lgkmcnt(0)\n\t"
        "v_max3_u32 v111, v16, v17, v18\n\t"
        "v_max3_u32 v111, v111, v19, v20\n\t"
        "v_max3_u32 v111, v111, v21, v22\n\t"
        "v_max3_u32 v111, v111, v23, v24\n\t"
        "v_max3_u32 v111, v111, v25, v26\n\t"
        "v_max3_u32 v111, v111, v27, v28\n\t"
        "v_max3_u32 v111, v111, v29, v30\n\t"
        "v_max_u32_e32 v111, v111, v31\n\t"
        "v_cmp_eq_u32_e32 vcc, -1, v111\n\t"
        "s_nop 0\n\t"
        "s_cbranch_vccz LA_go%=\n\t"
        "s_add_u32 s42, s42, 1\n\t"
        "s_cmp_lt_u32 s42, 0x100000\n\t"
        "s_cbranch_scc1 LA_rdre%=\n\t"
        "s_movk_i32 s34, 1\n\t"
        "s_branch LA_die%=\n\t"
        "LA_vmfail%=:\n\t"
        "s_or_b32 s47, s47, 2\n\t"
        "s_mov_b32 s42, 0\n\t"
        "LA_vmre%=:\n\t"
        "buffer_load_dwordx4 v[104:107], v120, s[8:11], s26 offen sc1\n\t"
        "v_mfma_f32_16x16x32_bf16 v[4:7], v[48:51], v[12:15], 0\n\t"
        "v_mfma_f32_16x16x32_bf16 v[8:11], v[52:55], v[12:15], 0\n\t"
        "s_nop 7\n\t"
        "s_waitcnt vmcnt(0)\n\t"
        "v_mfma_f32_16x16x16_bf16 v[4:7], v[104:105], v[56:57], v[4:7]\n\t"
        "v_mfma_f32_16x16x16_bf16 v[8:11], v[106:107], v[56:57], v[8:11]\n\t"
        "v_max3_u32 v111, v104, v105, v106\n\t"
        "v_max_u32_e32 v111, v111, v107\n\t"
        "v_cmp_eq_u32_e32 vcc, -1, v111\n\t"
        "s_nop 7\n\t"
        "s_cbranch_vccz LA_vmok%=\n\t"
        "s_add_u32 s42, s42, 1\n\t"
        "s_cmp_lt_u32 s42, 0x100000\n\t"
        "s_cbranch_scc1 LA_vmre%=\n\t"
        "s_movk_i32 s34, 2\n\t"
        "s_branch LA_die%=\n\t"
        "LA_die%=:\n\t"
        "v_mov_b32_e32 v30, s34\n\t"
        "s_mov_b32 s35, 33024\n\t"
        "s_mov_b64 exec, 1\n\t"
        "buffer_store_dword v30, off, s[12:15], s35 sc1\n\t"
        "s_endpgm\n\t"
        ".Lk_end_A:\n\t"
        :: REC_S_IN, "{v96}"(g_w), "{v97}"(wb0), "{v98}"(wb1), "{v99}"(rb0), "{v100}"(rb0 ^ 32u), "{v101}"(rb0 ^ 64u), "{v102}"(rb0 ^ 96u),
           "{v112}"(a_mem), "{v113}"(l_ms + w * 4096), "{v114}"(l_ms + ((w + 1) & 3) * 4096), "{v115}"(l_ms + ((w + 2) & 3) * 4096), "{v116}"(l_ms + ((w + 3) & 3) * 4096),
           "{v117}"(lb + w * 22528 + l * 16), "{v118}"(l_iw), "{v119}"(l_iq), "{v120}"(a_vm), "{v121}"(a_o), "{v122}"(h * 8192 + l * 16), "{v123}"(a_ox), "{v124}"(lb + L_KV + l * 16), "{s55}"(lb + L_KV + w * 2048),
           "{v125}"((24 + cp) * 16384 + l * 128), "{v126}"(16 * (w & 1) + n - 4 * g), "{v103}"(pp ? 0xEEEEEEEEu : 0x44444444u),
           "{v127}"((l == 0 && w == 0) ? F_PROG + h * 128 : OOB)
        : "memory", "vcc", "scc", "v0", "v255", "a0", "a255", "s101");
    __builtin_unreachable();
}
__device__ __forceinline__ void rec_b(const Params& p, unsigned lb, const int wv, const int id, const int rg, const int cp, const int rot, const int bq) {
    const int w = wv, l = otid() & 63, n = l & 15, g = l >> 4, cs = id, rb = 32 + id * 2 + rg;
    const v4u sh = mk_srd(p.ws + hx_base(p.ws), HX_BYTES - 8192), sw = mk_srd(p.ws, W_TOTAL), s1 = mk_srd(p.Wv, 1024 * 4096 * 2), s2 = mk_srd(p.Wo, 1024 * 4096 * 2);
    const int r8 = l >> 3, pc = l & 7, pp = n >> 3, bb = (n >> 2) & 1, f = (g & 1) | (pp << 1);
    const unsigned stg = lb + LB_STG + w * LB_STGP;
    const unsigned g_v = r8 * 8192 + cs * 256 + rg * 128 + pc * 16, g_o = (l >> 2) * 2048 + cs * 64 + (l & 3) * 16;
    const unsigned wb0 = stg + r8 * 128 + (((pc >> 1) ^ (r8 >> 2)) << 5) + (pc & 1) * 16, wb1 = wb0 ^ 64u;
    const unsigned rb0 = stg + (16 * (g >> 1) + 8 * pp + 4 * (g & 1) + bb) * 128 + (f << 5) + (n & 3) * 8;
    const unsigned o_r = stg + pp * (2048 + 128) + (g & 1) * (1024 + 32) + (bb + 4 * (g >> 1)) * 64 + (n & 3) * 8;
    const unsigned a_mem = cp * 32768 + (w >> 1) * 16384 + (w & 1) * 4096 + (g >> 1) * 512 + ((n & 7) >> 2) * 256 + ((g & 1) * 2 + pp) * 64 + (n & 3) * 16;
    const unsigned a_vmw = w < 2 ? (16 + (cs >> 2)) * 16384 + (cs & 3) * 2048 + (2 * rg + w) * 512 + n * 32 + (g & 1) * 16 + (g >> 1) * 8 : OOB;
    const unsigned a_or = (8 + rg * 4 + w) * 16384 + (n >> 2) * 256 + g * 64 + (n & 3) * 16;
    const unsigned a_mw = (RPL != 0 && RPL != 4 && w == 3) ? OOB : w * 32768 + (cs >> 4) * 16384 + ((cs >> 1) & 7) * 1024 + (cs & 1) * 512 + rg * 256 + l * 4;
    const unsigned a_sw = (w == 0 && l < 4) ? (24 + l) * 16384 + (id * 2 + rg) * 128 : OOB;
    const bf16_t* m0p = p.mem0 + (4 * rg + ((l >> 2) & 3)) * 1024 + cs * 32 + g * 8 + (l & 3) * 2;
    asm volatile(
        "global_load_dword v109, v[108:109], off\n\t"
        "s_add_u32 s35, s68, 0\n\t"
        "s_and_b32 s35, s35, 7\n\t"
        "s_lshl_b32 s69, s35, 10\n\t"
        "s_add_u32 s35, s68, 2\n\t"
        "s_and_b32 s35, s35, 7\n\t"
        "s_lshl_b32 s70, s35, 10\n\t"
        "s_add_u32 s35, s68, 4\n\t"
        "s_and_b32 s35, s35, 7\n\t"
        "s_lshl_b32 s71, s35, 10\n\t"
        "s_add_u32 s35, s68, 6\n\t"
        "s_and_b32 s35, s35, 7\n\t"
        "s_lshl_b32 s72, s35, 10\n\t"
        "s_lshl_b32 s34, s43, 21\n\t"
        "s_add_u32 s34, s34, 0\n\t"
        "buffer_load_dwordx4 v[128:131], v96, s[16:19], s34 offen nt\n\t"
        "s_add_u32 s34, s34, 0x10000\n\t"
        "buffer_load_dwordx4 v[132:135], v96, s[16:19], s34 offen nt\n\t"
        "s_add_u32 s34, s34, 0x10000\n\t"
        "buffer_load_dwordx4 v[136:139], v96, s[16:19], s34 offen nt\n\t"
        "s_add_u32 s34, s34, 0x10000\n\t"
        "buffer_load_dwordx4 v[140:143], v96, s[16:19], s34 offen nt\n\t"
        "s_add_u32 s34, s34, 0x10000\n\t"
        "buffer_load_dwordx4 v[144:147], v96, s[16:19], s34 offen nt\n\t"
        "s_add_u32 s34, s34, 0x10000\n\t"
        "buffer_load_dwordx4 v[148:151], v96, s[16:19], s34 offen nt\n\t"
        "s_add_u32 s34, s34, 0x10000\n\t"
        "buffer_load_dwordx4 v[152:155], v96, s[16:19], s34 offen nt\n\t"
        "s_add_u32 s34, s34, 0x10000\n\t"
        "buffer_load_dwordx4 v[156:159], v96, s[16:19], s34 offen nt\n\t"
        "s_lshl_b32 s34, s43, 21\n\t"
        "s_add_u32 s34, s34, 524288\n\t"
        "buffer_load_dwordx4 v[160:163], v96, s[16:19], s34 offen nt\n\t"
        "s_add_u32 s34, s34, 0x10000\n\t"
        "buffer_load_dwordx4 v[164:167], v96, s[16:19], s34 offen nt\n\t"
        "s_add_u32 s34, s34, 0x10000\n\t"
        "buffer_load_dwordx4 v[168:171], v96, s[16:19], s34 offen nt\n\t"
        "s_add_u32 s34, s34, 0x10000\n\t"
        "buffer_load_dwordx4 v[172:175], v96, s[16:19], s34 offen nt\n\t"
        "s_add_u32 s34, s34, 0x10000\n\t"
        "buffer_load_dwordx4 v[176:179], v96, s[16:19], s34 offen nt\n\t"
        "s_add_u32 s34, s34, 0x10000\n\t"
        "buffer_load_dwordx4 v[180:183], v96, s[16:19], s34 offen nt\n\t"
        "s_add_u32 s34, s34, 0x10000\n\t"
        "buffer_load_dwordx4 v[184:187], v96, s[16:19], s34 offen nt\n\t"
        "s_add_u32 s34, s34, 0x10000\n\t"
        "buffer_load_dwordx4 v[188:191], v96, s[16:19], s34 offen nt\n\t"
        "s_lshl_b32 s34, s43, 21\n\t"
        "s_add_u32 s34, s34, 1048576\n\t"
        "buffer_load_dwordx4 v[192:195], v96, s[16:19], s34 offen nt\n\t"
        "s_add_u32 s34, s34, 0x10000\n\t"
        "buffer_load_dwordx4 v[196:199], v96, s[16:19], s34 offen nt\n\t"
        "s_add_u32 s34, s34, 0x10000\n\t"
        "buffer_load_dwordx4 v[200:203], v96, s[16:19], s34 offen nt\n\t"
        "s_add_u32 s34, s34, 0x10000\n\t"
        "buffer_load_dwordx4 v[204:207], v96, s[16:19], s34 offen nt\n\t"
        "s_add_u32 s34, s34, 0x10000\n\t"
        "buffer_load_dwordx4 v[208:211], v96, s[16:19], s34 offen nt\n\t"
        "s_add_u32 s34, s34, 0x10000\n\t"
        "buffer_load_dwordx4 v[212:215], v96, s[16:19], s34 offen nt\n\t"
        "s_add_u32 s34, s34, 0x10000\n\t"
        "buffer_load_dwordx4 v[216:219], v96, s[16:19], s34 offen nt\n\t"
        "s_add_u32 s34, s34, 0x10000\n\t"
        "buffer_load_dwordx4 v[220:223], v96, s[16:19], s34 offen nt\n\t"
        "s_waitcnt vmcnt(16)\n\t"
        "ds_write_b128 v98, v[128:131] offset:0\n\t"
        "ds_write_b128 v99, v[132:135] offset:1024\n\t"
        "ds_write_b128 v98, v[136:139] offset:2048\n\t"
        "ds_write_b128 v99, v[140:143] offset:3072\n\t"
        "ds_write_b128 v98, v[144:147] offset:4096\n\t"
        "ds_write_b128 v99, v[148:151] offset:5120\n\t"
        "ds_write_b128 v98, v[152:155] offset:6144\n\t"
        "ds_write_b128 v99, v[156:159] offset:7168\n\t"
        "ds_read_b64_tr_b16 v[128:129], v100 offset:0\n\t"
        "ds_read_b64_tr_b16 v[130:131], v100 offset:256\n\t"
        "ds_read_b64_tr_b16 v[132:133], v100 offset:4096\n\t"
        "ds_read_b64_tr_b16 v[134:135], v100 offset:4352\n\t"
        "s_waitcnt lgkmcnt(8)\n\t"
        "ds_read_b64_tr_b16 v[136:137], v101 offset:0\n\t"
        "ds_read_b64_tr_b16 v[138:139], v101 offset:256\n\t"
        "ds_read_b64_tr_b16 v[140:141], v101 offset:4096\n\t"
        "ds_read_b64_tr_b16 v[142:143], v101 offset:4352\n\t"
        "s_waitcnt lgkmcnt(8)\n\t"
        "ds_read_b64_tr_b16 v[144:145], v102 offset:0\n\t"
        "ds_read_b64_tr_b16 v[146:147], v102 offset:256\n\t"
        "ds_read_b64_tr_b16 v[148:149], v102 offset:4096\n\t"
        "ds_read_b64_tr_b16 v[150:151], v102 offset:4352\n\t"
        "s_waitcnt lgkmcnt(8)\n\t"
        "ds_read_b64_tr_b16 v[152:153], v103 offset:0\n\t"
        "ds_read_b64_tr_b16 v[154:155], v103 offset:256\n\t"
        "ds_read_b64_tr_b16 v[156:157], v103 offset:4096\n\t"
        "ds_read_b64_tr_b16 v[158:159], v103 offset:4352\n\t"
        "s_lshl_b32 s34, s43, 21\n\t"
        "s_add_u32 s34, s34, 1572864\n\t"
        "buffer_load_dwordx4 v[224:227], v96, s[16:19], s34 offen nt\n\t"
        "s_add_u32 s34, s34, 0x10000\n\t"
        "buffer_load_dwordx4 v[228:231], v96, s[16:19], s34 offen nt\n\t"
        "s_add_u32 s34, s34, 0x10000\n\t"
        "buffer_load_dwordx4 v[232:235], v96, s[16:19], s34 offen nt\n\t"
        "s_add_u32 s34, s34, 0x10000\n\t"
        "buffer_load_dwordx4 v[236:239], v96, s[16:19], s34 offen nt\n\t"
        "s_add_u32 s34, s34, 0x10000\n\t"
        "buffer_load_dwordx4 v[240:243], v96, s[16:19], s34 offen nt\n\t"
        "s_add_u32 s34, s34, 0x10000\n\t"
        "buffer_load_dwordx4 v[244:247], v96, s[16:19], s34 offen nt\n\t"
        "s_add_u32 s34, s34, 0x10000\n\t"
        "buffer_load_dwordx4 v[248:251], v96, s[16:19], s34 offen nt\n\t"
        "s_add_u32 s34, s34, 0x10000\n\t"
        "buffer_load_dwordx4 v[252:255], v96, s[16:19], s34 offen nt\n\t"
        "s_waitcnt lgkmcnt(0)\n\t"
        "s_waitcnt vmcnt(16)\n\t"
        "ds_write_b128 v98, v[160:163] offset:0\n\t"
        "ds_write_b128 v99, v[164:167] offset:1024\n\t"
        "ds_write_b128 v98, v[168:171] offset:2048\n\t"
        "ds_write_b128 v99, v[172:175] offset:3072\n\t"
        "ds_write_b128 v98, v[176:179] offset:4096\n\t"
        "ds_write_b128 v99, v[180:183] offset:5120\n\t"
        "ds_write_b128 v98, v[184:187] offset:6144\n\t"
        "ds_write_b128 v99, v[188:191] offset:7168\n\t"
        "ds_read_b64_tr_b16 v[160:161], v100 offset:0\n\t"
        "ds_read_b64_tr_b16 v[162:163], v100 offset:256\n\t"
        "ds_read_b64_tr_b16 v[164:165], v100 offset:4096\n\t"
        "ds_read_b64_tr_b16 v[166:167], v100 offset:4352\n\t"
        "s_waitcnt lgkmcnt(8)\n\t"
        "ds_read_b64_tr_b16 v[168:169], v101 offset:0\n\t"
        "ds_read_b64_tr_b16 v[170:171], v101 offset:256\n\t"
        "ds_read_b64_tr_b16 v[172:173], v101 offset:4096\n\t"
        "ds_read_b64_tr_b16 v[174:175], v101 offset:4352\n\t"
        "s_waitcnt lgkmcnt(8)\n\t"
        "ds_read_b64_tr_b16 v[176:177], v102 offset:0\n\t"
        "ds_read_b64_tr_b16 v[178:179], v102 offset:256\n\t"
        "ds_read_b64_tr_b16 v[180:181], v102 offset:4096\n\t"
        "ds_read_b64_tr_b16 v[182:183], v102 offset:4352\n\t"
        "s_waitcnt lgkmcnt(8)\n\t"
        "ds_read_b64_tr_b16 v[184:185], v103 offset:0\n\t"
        "ds_read_b64_tr_b16 v[186:187], v103 offset:256\n\t"
        "ds_read_b64_tr_b16 v[188:189], v103 offset:4096\n\t"
        "ds_read_b64_tr_b16 v[190:191], v103 offset:4352\n\t"
        "s_add_u32 s35, s68, 0\n\t"
        "s_and_b32 s35, s35, 7\n\t"
        "s_lshl_b32 s35, s35, 18\n\t"
        "s_lshl_b32 s34, s43, 21\n\t"
        "s_add_u32 s34, s34, s35\n\t"
        "buffer_load_dwordx4 a[0:3], v97, s[20:23], s34 offen\n\t"
        "s_add_u32 s34, s34, 0x8000\n\t"
        "buffer_load_dwordx4 a[4:7], v97, s[20:23], s34 offen\n\t"
        "s_add_u32 s34, s34, 0x8000\n\t"
        "buffer_load_dwordx4 a[8:11], v97, s[20:23], s34 offen\n\t"
        "s_add_u32 s34, s34, 0x8000\n\t"
        "buffer_load_dwordx4 a[12:15], v97, s[20:23], s34 offen\n\t"
        "s_add_u32 s34, s34, 0x8000\n\t"
        "buffer_load_dwordx4 a[16:19], v97, s[20:23], s34 offen\n\t"
        "s_add_u32 s34, s34, 0x8000\n\t"
        "buffer_load_dwordx4 a[20:23], v97, s[20:23], s34 offen\n\t"
        "s_add_u32 s34, s34, 0x8000\n\t"
        "buffer_load_dwordx4 a[24:27], v97, s[20:23], s34 offen\n\t"
        "s_add_u32 s34, s34, 0x8000\n\t"
        "buffer_load_dwordx4 a[28:31], v97, s[20:23], s34 offen\n\t"
        "s_waitcnt lgkmcnt(0)\n\t"
        "s_waitcnt vmcnt(16)\n\t"
        "ds_write_b128 v98, v[192:195] offset:0\n\t"
        "ds_write_b128 v99, v[196:199] offset:1024\n\t"
        "ds_write_b128 v98, v[200:203] offset:2048\n\t"
        "ds_write_b128 v99, v[204:207] offset:3072\n\t"
        "ds_write_b128 v98, v[208:211] offset:4096\n\t"
        "ds_write_b128 v99, v[212:215] offset:5120\n\t"
        "ds_write_b128 v98, v[216:219] offset:6144\n\t"
        "ds_write_b128 v99, v[220:223] offset:7168\n\t"
        "ds_read_b64_tr_b16 v[192:193], v100 offset:0\n\t"
        "ds_read_b64_tr_b16 v[194:195], v100 offset:256\n\t"
        "ds_read_b64_tr_b16 v[196:197], v100 offset:4096\n\t"
        "ds_read_b64_tr_b16 v[198:199], v100 offset:4352\n\t"
        "s_waitcnt lgkmcnt(8)\n\t"
        "ds_read_b64_tr_b16 v[200:201], v101 offset:0\n\t"
        "ds_read_b64_tr_b16 v[202:203], v101 offset:256\n\t"
        "ds_read_b64_tr_b16 v[204:205], v101 offset:4096\n\t"
        "ds_read_b64_tr_b16 v[206:207], v101 offset:4352\n\t"
        "s_waitcnt lgkmcnt(8)\n\t"
        "ds_read_b64_tr_b16 v[208:209], v102 offset:0\n\t"
        "ds_read_b64_tr_b16 v[210:211], v102 offset:256\n\t"
        "ds_read_b64_tr_b16 v[212:213], v102 offset:4096\n\t"
        "ds_read_b64_tr_b16 v[214:215], v102 offset:4352\n\t"
        "s_waitcnt lgkmcnt(8)\n\t"
        "ds_read_b64_tr_b16 v[216:217], v103 offset:0\n\t"
        "ds_read_b64_tr_b16 v[218:219], v103 offset:256\n\t"
        "ds_read_b64_tr_b16 v[220:221], v103 offset:4096\n\t"
        "ds_read_b64_tr_b16 v[222:223], v103 offset:4352\n\t"
        "s_add_u32 s35, s68, 1\n\t"
        "s_and_b32 s35, s35, 7\n\t"
        "s_lshl_b32 s35, s35, 18\n\t"
        "s_lshl_b32 s34, s43, 21\n\t"
        "s_add_u32 s34, s34, s35\n\t"
        "buffer_load_dwordx4 a[32:35], v97, s[20:23], s34 offen\n\t"
        "s_add_u32 s34, s34, 0x8000\n\t"
        "buffer_load_dwordx4 a[36:39], v97, s[20:23], s34 offen\n\t"
        "s_add_u32 s34, s34, 0x8000\n\t"
        "buffer_load_dwordx4 a[40:43], v97, s[20:23], s34 offen\n\t"
        "s_add_u32 s34, s34, 0x8000\n\t"
        "buffer_load_dwordx4 a[44:47], v97, s[20:23], s34 offen\n\t"
        "s_add_u32 s34, s34, 0x8000\n\t"
        "buffer_load_dwordx4 a[48:51], v97, s[20:23], s34 offen\n\t"
        "s_add_u32 s34, s34, 0x8000\n\t"
        "buffer_load_dwordx4 a[52:55], v97, s[20:23], s34 offen\n\t"
        "s_add_u32 s34, s34, 0x8000\n\t"
        "buffer_load_dwordx4 a[56:59], v97, s[20:23], s34 offen\n\t"
        "s_add_u32 s34, s34, 0x8000\n\t"
        "buffer_load_dwordx4 a[60:63], v97, s[20:23], s34 offen\n\t"
        "s_waitcnt lgkmcnt(0)\n\t"
        "s_waitcnt vmcnt(16)\n\t"
        "ds_write_b128 v98, v[224:227] offset:0\n\t"
        "ds_write_b128 v99, v[228:231] offset:1024\n\t"
        "ds_write_b128 v98, v[232:235] offset:2048\n\t"
        "ds_write_b128 v99, v[236:239] offset:3072\n\t"
        "ds_write_b128 v98, v[240:243] offset:4096\n\t"
        "ds_write_b128 v99, v[244:247] offset:5120\n\t"
        "ds_write_b128 v98, v[248:251] offset:6144\n\t"
        "ds_write_b128 v99, v[252:255] offset:7168\n\t"
        "ds_read_b64_tr_b16 v[224:225], v100 offset:0\n\t"
        "ds_read_b64_tr_b16 v[226:227], v100 offset:256\n\t"
        "ds_read_b64_tr_b16 v[228:229], v100 offset:4096\n\t"
        "ds_read_b64_tr_b16 v[230:231], v100 offset:4352\n\t"
        "s_waitcnt lgkmcnt(8)\n\t"
        "ds_read_b64_tr_b16 v[232:233], v101 offset:0\n\t"
        "ds_read_b64_tr_b16 v[234:235], v101 offset:256\n\t"
        "ds_read_b64_tr_b16 v[236:237], v101 offset:4096\n\t"
        "ds_read_b64_tr_b16 v[238:239], v101 offset:4352\n\t"
        "s_waitcnt lgkmcnt(8)\n\t"
        "ds_read_b64_tr_b16 v[240:241], v102 offset:0\n\t"
        "ds_read_b64_tr_b16 v[242:243], v102 offset:256\n\t"
        "ds_read_b64_tr_b16 v[244:245], v102 offset:4096\n\t"
        "ds_read_b64_tr_b16 v[246:247], v102 offset:4352\n\t"
        "s_waitcnt lgkmcnt(8)\n\t"
        "ds_read_b64_tr_b16 v[248:249], v103 offset:0\n\t"
        "ds_read_b64_tr_b16 v[250:251], v103 offset:256\n\t"
        "ds_read_b64_tr_b16 v[252:253], v103 offset:4096\n\t"
        "ds_read_b64_tr_b16 v[254:255], v103 offset:4352\n\t"
        "s_add_u32 s35, s68, 2\n\t"
        "s_and_b32 s35, s35, 7\n\t"
        "s_lshl_b32 s35, s35, 18\n\t"
        "s_lshl_b32 s34, s43, 21\n\t"
        "s_add_u32 s34, s34, s35\n\t"
        "buffer_load_dwordx4 a[64:67], v97, s[20:23], s34 offen\n\t"
        "s_add_u32 s34, s34, 0x8000\n\t"
        "buffer_load_dwordx4 a[68:71], v97, s[20:23], s34 offen\n\t"
        "s_add_u32 s34, s34, 0x8000\n\t"
        "buffer_load_dwordx4 a[72:75], v97, s[20:23], s34 offen\n\t"
        "s_add_u32 s34, s34, 0x8000\n\t"
        "buffer_load_dwordx4 a[76:79], v97, s[20:23], s34 offen\n\t"
        "s_add_u32 s34, s34, 0x8000\n\t"
        "buffer_load_dwordx4 a[80:83], v97, s[20:23], s34 offen\n\t"
        "s_add_u32 s34, s34, 0x8000\n\t"
        "buffer_load_dwordx4 a[84:87], v97, s[20:23], s34 offen\n\t"
        "s_add_u32 s34, s34, 0x8000\n\t"
        "buffer_load_dwordx4 a[88:91], v97, s[20:23], s34 offen\n\t"
        "s_add_u32 s34, s34, 0x8000\n\t"
        "buffer_load_dwordx4 a[92:95], v97, s[20:23], s34 offen\n\t"
        "s_waitcnt lgkmcnt(0)\n\t"
        "s_waitcnt vmcnt(16)\n\t"
        "ds_write_b128 v122, a[0:3] offset:0\n\t"
        "ds_write_b128 v122, a[4:7] offset:1056\n\t"
        "ds_write_b128 v122, a[8:11] offset:2176\n\t"
        "ds_write_b128 v122, a[12:15] offset:3232\n\t"
        "ds_write_b128 v122, a[16:19] offset:4352\n\t"
        "ds_write_b128 v122, a[20:23] offset:5408\n\t"
        "ds_write_b128 v122, a[24:27] offset:6528\n\t"
        "ds_write_b128 v122, a[28:31] offset:7584\n\t"
        "ds_read_b64_tr_b16 a[0:1], v123 offset:0\n\t"
        "ds_read_b64_tr_b16 a[2:3], v123 offset:128\n\t"
        "ds_read_b64_tr_b16 a[4:5], v123 offset:512\n\t"
        "ds_read_b64_tr_b16 a[6:7], v123 offset:640\n\t"
        "s_waitcnt lgkmcnt(8)\n\t"
        "ds_read_b64_tr_b16 a[8:9], v123 offset:32\n\t"
        "ds_read_b64_tr_b16 a[10:11], v123 offset:160\n\t"
        "ds_read_b64_tr_b16 a[12:13], v123 offset:544\n\t"
        "ds_read_b64_tr_b16 a[14:15], v123 offset:672\n\t"
        "s_waitcnt lgkmcnt(8)\n\t"
        "ds_read_b64_tr_b16 a[16:17], v123 offset:4352\n\t"
        "ds_read_b64_tr_b16 a[18:19], v123 offset:4480\n\t"
        "ds_read_b64_tr_b16 a[20:21], v123 offset:4864\n\t"
        "ds_read_b64_tr_b16 a[22:23], v123 offset:4992\n\t"
        "s_waitcnt lgkmcnt(8)\n\t"
        "ds_read_b64_tr_b16 a[24:25], v123 offset:4384\n\t"
        "ds_read_b64_tr_b16 a[26:27], v123 offset:4512\n\t"
        "ds_read_b64_tr_b16 a[28:29], v123 offset:4896\n\t"
        "ds_read_b64_tr_b16 a[30:31], v123 offset:5024\n\t"
        "s_waitcnt lgkmcnt(8)\n\t"
        "s_add_u32 s35, s68, 3\n\t"
        "s_and_b32 s35, s35, 7\n\t"
        "s_lshl_b32 s35, s35, 18\n\t"
        "s_lshl_b32 s34, s43, 21\n\t"
        "s_add_u32 s34, s34, s35\n\t"
        "buffer_load_dwordx4 a[96:99], v97, s[20:23], s34 offen\n\t"
        "s_add_u32 s34, s34, 0x8000\n\t"
        "buffer_load_dwordx4 a[100:103], v97, s[20:23], s34 offen\n\t"
        "s_add_u32 s34, s34, 0x8000\n\t"
        "buffer_load_dwordx4 a[104:107], v97, s[20:23], s34 offen\n\t"
        "s_add_u32 s34, s34, 0x8000\n\t"
        "buffer_load_dwordx4 a[108:111], v97, s[20:23], s34 offen\n\t"
        "s_add_u32 s34, s34, 0x8000\n\t"
        "buffer_load_dwordx4 a[112:115], v97, s[20:23], s34 offen\n\t"
        "s_add_u32 s34, s34, 0x8000\n\t"
        "buffer_load_dwordx4 a[116:119], v97, s[20:23], s34 offen\n\t"
        "s_add_u32 s34, s34, 0x8000\n\t"
        "buffer_load_dwordx4 a[120:123], v97, s[20:23], s34 offen\n\t"
        "s_add_u32 s34, s34, 0x8000\n\t"
        "buffer_load_dwordx4 a[124:127], v97, s[20:23], s34 offen\n\t"
        "s_waitcnt lgkmcnt(0)\n\t"
        "s_waitcnt vmcnt(16)\n\t"
        "ds_write_b128 v122, a[32:35] offset:0\n\t"
        "ds_write_b128 v122, a[36:39] offset:1056\n\t"
        "ds_write_b128 v122, a[40:43] offset:2176\n\t"
        "ds_write_b128 v122, a[44:47] offset:3232\n\t"
        "ds_write_b128 v122, a[48:51] offset:4352\n\t"
        "ds_write_b128 v122, a[52:55] offset:5408\n\t"
        "ds_write_b128 v122, a[56:59] offset:6528\n\t"
        "ds_write_b128 v122, a[60:63] offset:7584\n\t"
        "ds_read_b64_tr_b16 a[32:33], v123 offset:0\n\t"
        "ds_read_b64_tr_b16 a[34:35], v123 offset:128\n\t"
        "ds_read_b64_tr_b16 a[36:37], v123 offset:512\n\t"
        "ds_read_b64_tr_b16 a[38:39], v123 offset:640\n\t"
        "s_waitcnt lgkmcnt(8)\n\t"
        "ds_read_b64_tr_b16 a[40:41], v123 offset:32\n\t"
        "ds_read_b64_tr_b16 a[42:43], v123 offset:160\n\t"
        "ds_read_b64_tr_b16 a[44:45], v123 offset:544\n\t"
        "ds_read_b64_tr_b16 a[46:47], v123 offset:672\n\t"
        "s_waitcnt lgkmcnt(8)\n\t"
        "ds_read_b64_tr_b16 a[48:49], v123 offset:4352\n\t"
        "ds_read_b64_tr_b16 a[50:51], v123 offset:4480\n\t"
        "ds_read_b64_tr_b16 a[52:53], v123 offset:4864\n\t"
        "ds_read_b64_tr_b16 a[54:55], v123 offset:4992\n\t"
        "s_waitcnt lgkmcnt(8)\n\t"
        "ds_read_b64_tr_b16 a[56:57], v123 offset:4384\n\t"
        "ds_read_b64_tr_b16 a[58:59], v123 offset:4512\n\t"
        "ds_read_b64_tr_b16 a[60:61], v123 offset:4896\n\t"
        "ds_read_b64_tr_b16 a[62:63], v123 offset:5024\n\t"
        "s_waitcnt lgkmcnt(8)\n\t"
        "s_add_u32 s35, s68, 4\n\t"
        "s_and_b32 s35, s35, 7\n\t"
        "s_lshl_b32 s35, s35, 18\n\t"
        "s_lshl_b32 s34, s43, 21\n\t"
        "s_add_u32 s34, s34, s35\n\t"
        "buffer_load_dwordx4 a[128:131], v97, s[20:23], s34 offen\n\t"
        "s_add_u32 s34, s34, 0x8000\n\t"
        "buffer_load_dwordx4 a[132:135], v97, s[20:23], s34 offen\n\t"
        "s_add_u32 s34, s34, 0x8000\n\t"
        "buffer_load_dwordx4 a[136:139], v97, s[20:23], s34 offen\n\t"
        "s_add_u32 s34, s34, 0x8000\n\t"
        "buffer_load_dwordx4 a[140:143], v97, s[20:23], s34 offen\n\t"
        "s_add_u32 s34, s34, 0x8000\n\t"
        "buffer_load_dwordx4 a[144:147], v97, s[20:23], s34 offen\n\t"
        "s_add_u32 s34, s34, 0x8000\n\t"
        "buffer_load_dwordx4 a[148:151], v97, s[20:23], s34 offen\n\t"
        "s_add_u32 s34, s34, 0x8000\n\t"
        "buffer_load_dwordx4 a[152:155], v97, s[20:23], s34 offen\n\t"
        "s_add_u32 s34, s34, 0x8000\n\t"
        "buffer_load_dwordx4 a[156:159], v97, s[20:23], s34 offen\n\t"
        "s_waitcnt lgkmcnt(0)\n\t"
        "s_waitcnt vmcnt(16)\n\t"
        "ds_write_b128 v122, a[64:67] offset:0\n\t"
        "ds_write_b128 v122, a[68:71] offset:1056\n\t"
        "ds_write_b128 v122, a[72:75] offset:2176\n\t"
        "ds_write_b128 v122, a[76:79] offset:3232\n\t"
        "ds_write_b128 v122, a[80:83] offset:4352\n\t"
        "ds_write_b128 v122, a[84:87] offset:5408\n\t"
        "ds_write_b128 v122, a[88:91] offset:6528\n\t"
        "ds_write_b128 v122, a[92:95] offset:7584\n\t"
        "ds_read_b64_tr_b16 a[64:65], v123 offset:0\n\t"
        "ds_read_b64_tr_b16 a[66:67], v123 offset:128\n\t"
        "ds_read_b64_tr_b16 a[68:69], v123 offset:512\n\t"
        "ds_read_b64_tr_b16 a[70:71], v123 offset:640\n\t"
        "s_waitcnt lgkmcnt(8)\n\t"
        "ds_read_b64_tr_b16 a[72:73], v123 offset:32\n\t"
        "ds_read_b64_tr_b16 a[74:75], v123 offset:160\n\t"
        "ds_read_b64_tr_b16 a[76:77], v123 offset:544\n\t"
        "ds_read_b64_tr_b16 a[78:79], v123 offset:672\n\t"
        "s_waitcnt lgkmcnt(8)\n\t"
        "ds_read_b64_tr_b16 a[80:81], v123 offset:4352\n\t"
        "ds_read_b64_tr_b16 a[82:83], v123 offset:4480\n\t"
        "ds_read_b64_tr_b16 a[84:85], v123 offset:4864\n\t"
        "ds_read_b64_tr_b16 a[86:87], v123 offset:4992\n\t"
        "s_waitcnt lgkmcnt(8)\n\t"
        "ds_read_b64_tr_b16 a[88:89], v123 offset:4384\n\t"
        "ds_read_b64_tr_b16 a[90:91], v123 offset:4512\n\t"
        "ds_read_b64_tr_b16 a[92:93], v123 offset:4896\n\t"
        "ds_read_b64_tr_b16 a[94:95], v123 offset:5024\n\t"
        "s_waitcnt lgkmcnt(8)\n\t"
        "s_add_u32 s35, s68, 5\n\t"
        "s_and_b32 s35, s35, 7\n\t"
        "s_lshl_b32 s35, s35, 18\n\t"
        "s_lshl_b32 s34, s43, 21\n\t"
        "s_add_u32 s34, s34, s35\n\t"
        "buffer_load_dwordx4 a[160:163], v97, s[20:23], s34 offen\n\t"
        "s_add_u32 s34, s34, 0x8000\n\t"
        "buffer_load_dwordx4 a[164:167], v97, s[20:23], s34 offen\n\t"
        "s_add_u32 s34, s34, 0x8000\n\t"
        "buffer_load_dwordx4 a[168:171], v97, s[20:23], s34 offen\n\t"
        "s_add_u32 s34, s34, 0x8000\n\t"
        "buffer_load_dwordx4 a[172:175], v97, s[20:23], s34 offen\n\t"
        "s_add_u32 s34, s34, 0x8000\n\t"
        "buffer_load_dwordx4 a[176:179], v97, s[20:23], s34 offen\n\t"
        "s_add_u32 s34, s34, 0x8000\n\t"
        "buffer_load_dwordx4 a[180:183], v97, s[20:23], s34 offen\n\t"
        "s_add_u32 s34, s34, 0x8000\n\t"
        "buffer_load_dwordx4 a[184:187], v97, s[20:23], s34 offen\n\t"
        "s_add_u32 s34, s34, 0x8000\n\t"
        "buffer_load_dwordx4 a[188:191], v97, s[20:23], s34 offen\n\t"
        "s_waitcnt lgkmcnt(0)\n\t"
        "s_waitcnt vmcnt(16)\n\t"
        "ds_write_b128 v122, a[96:99] offset:0\n\t"
        "ds_write_b128 v122, a[100:103] offset:1056\n\t"
        "ds_write_b128 v122, a[104:107] offset:2176\n\t"
        "ds_write_b128 v122, a[108:111] offset:3232\n\t"
        "ds_write_b128 v122, a[112:115] offset:4352\n\t"
        "ds_write_b128 v122, a[116:119] offset:5408\n\t"
        "ds_write_b128 v122, a[120:123] offset:6528\n\t"
        "ds_write_b128 v122, a[124:127] offset:7584\n\t"
        "ds_read_b64_tr_b16 a[96:97], v123 offset:0\n\t"
        "ds_read_b64_tr_b16 a[98:99], v123 offset:128\n\t"
        "ds_read_b64_tr_b16 a[100:101], v123 offset:512\n\t"
        "ds_read_b64_tr_b16 a[102:103], v123 offset:640\n\t"
        "s_waitcnt lgkmcnt(8)\n\t"
        "ds_read_b64_tr_b16 a[104:105], v123 offset:32\n\t"
        "ds_read_b64_tr_b16 a[106:107], v123 offset:160\n\t"
        "ds_read_b64_tr_b16 a[108:109], v123 offset:544\n\t"
        "ds_read_b64_tr_b16 a[110:111], v123 offset:672\n\t"
        "s_waitcnt lgkmcnt(8)\n\t"
        "ds_read_b64_tr_b16 a[112:113], v123 offset:4352\n\t"
        "ds_read_b64_tr_b16 a[114:115], v123 offset:4480\n\t"
        "ds_read_b64_tr_b16 a[116:117], v123 offset:4864\n\t"
        "ds_read_b64_tr_b16 a[118:119], v123 offset:4992\n\t"
        "s_waitcnt lgkmcnt(8)\n\t"
        "ds_read_b64_tr_b16 a[120:121], v123 offset:4384\n\t"
        "ds_read_b64_tr_b16 a[122:123], v123 offset:4512\n\t"
        "ds_read_b64_tr_b16 a[124:125], v123 offset:4896\n\t"
        "ds_read_b64_tr_b16 a[126:127], v123 offset:5024\n\t"
        "s_waitcnt lgkmcnt(8)\n\t"
        "s_add_u32 s35, s68, 6\n\t"
        "s_and_b32 s35, s35, 7\n\t"
        "s_lshl_b32 s35, s35, 18\n\t"
        "s_lshl_b32 s34, s43, 21\n\t"
        "s_add_u32 s34, s34, s35\n\t"
        "buffer_load_dwordx4 a[192:195], v97, s[20:23], s34 offen\n\t"
        "s_add_u32 s34, s34, 0x8000\n\t"
        "buffer_load_dwordx4 a[196:199], v97, s[20:23], s34 offen\n\t"
        "s_add_u32 s34, s34, 0x8000\n\t"
        "buffer_load_dwordx4 a[200:203], v97, s[20:23], s34 offen\n\t"
        "s_add_u32 s34, s34, 0x8000\n\t"
        "buffer_load_dwordx4 a[204:207], v97, s[20:23], s34 offen\n\t"
        "s_add_u32 s34, s34, 0x8000\n\t"
        "buffer_load_dwordx4 a[208:211], v97, s[20:23], s34 offen\n\t"
        "s_add_u32 s34, s34, 0x8000\n\t"
        "buffer_load_dwordx4 a[212:215], v97, s[20:23], s34 offen\n\t"
        "s_add_u32 s34, s34, 0x8000\n\t"
        "buffer_load_dwordx4 a[216:219], v97, s[20:23], s34 offen\n\t"
        "s_add_u32 s34, s34, 0x8000\n\t"
        "buffer_load_dwordx4 a[220:223], v97, s[20:23], s34 offen\n\t"
        "s_waitcnt lgkmcnt(0)\n\t"
        "s_waitcnt vmcnt(16)\n\t"
        "ds_write_b128 v122, a[128:131] offset:0\n\t"
        "ds_write_b128 v122, a[132:135] offset:1056\n\t"
        "ds_write_b128 v122, a[136:139] offset:2176\n\t"
        "ds_write_b128 v122, a[140:143] offset:3232\n\t"
        "ds_write_b128 v122, a[144:147] offset:4352\n\t"
        "ds_write_b128 v122, a[148:151] offset:5408\n\t"
        "ds_write_b128 v122, a[152:155] offset:6528\n\t"
        "ds_write_b128 v122, a[156:159] offset:7584\n\t"
        "ds_read_b64_tr_b16 a[128:129], v123 offset:0\n\t"
        "ds_read_b64_tr_b16 a[130:131], v123 offset:128\n\t"
        "ds_read_b64_tr_b16 a[132:133], v123 offset:512\n\t"
        "ds_read_b64_tr_b16 a[134:135], v123 offset:640\n\t"
        "s_waitcnt lgkmcnt(8)\n\t"
        "ds_read_b64_tr_b16 a[136:137], v123 offset:32\n\t"
        "ds_read_b64_tr_b16 a[138:139], v123 offset:160\n\t"
        "ds_read_b64_tr_b16 a[140:141], v123 offset:544\n\t"
        "ds_read_b64_tr_b16 a[142:143], v123 offset:672\n\t"
        "s_waitcnt lgkmcnt(8)\n\t"
        "ds_read_b64_tr_b16 a[144:145], v123 offset:4352\n\t"
        "ds_read_b64_tr_b16 a[146:147], v123 offset:4480\n\t"
        "ds_read_b64_tr_b16 a[148:149], v123 offset:4864\n\t"
        "ds_read_b64_tr_b16 a[150:151], v123 offset:4992\n\t"
        "s_waitcnt lgkmcnt(8)\n\t"
        "ds_read_b64_tr_b16 a[152:153], v123 offset:4384\n\t"
        "ds_read_b64_tr_b16 a[154:155], v123 offset:4512\n\t"
        "ds_read_b64_tr_b16 a[156:157], v123 offset:4896\n\t"
        "ds_read_b64_tr_b16 a[158:159], v123 offset:5024\n\t"
        "s_waitcnt lgkmcnt(8)\n\t"
        "s_add_u32 s35, s68, 7\n\t"
        "s_and_b32 s35, s35, 7\n\t"
        "s_lshl_b32 s35, s35, 18\n\t"
        "s_lshl_b32 s34, s43, 21\n\t"
        "s_add_u32 s34, s34, s35\n\t"
        "buffer_load_dwordx4 a[224:227], v97, s[20:23], s34 offen\n\t"
        "s_add_u32 s34, s34, 0x8000\n\t"
        "buffer_load_dwordx4 a[228:231], v97, s[20:23], s34 offen\n\t"
        "s_add_u32 s34, s34, 0x8000\n\t"
        "buffer_load_dwordx4 a[232:235], v97, s[20:23], s34 offen\n\t"
        "s_add_u32 s34, s34, 0x8000\n\t"
        "buffer_load_dwordx4 a[236:239], v97, s[20:23], s34 offen\n\t"
        "s_add_u32 s34, s34, 0x8000\n\t"
        "buffer_load_dwordx4 a[240:243], v97, s[20:23], s34 offen\n\t"
        "s_add_u32 s34, s34, 0x8000\n\t"
        "buffer_load_dwordx4 a[244:247], v97, s[20:23], s34 offen\n\t"
        "s_add_u32 s34, s34, 0x8000\n\t"
        "buffer_load_dwordx4 a[248:251], v97, s[20:23], s34 offen\n\t"
        "s_add_u32 s34, s34, 0x8000\n\t"
        "buffer_load_dwordx4 a[252:255], v97, s[20:23], s34 offen\n\t"
        "s_waitcnt lgkmcnt(0)\n\t"
        "s_waitcnt vmcnt(16)\n\t"
        "ds_write_b128 v122, a[160:163] offset:0\n\t"
        "ds_write_b128 v122, a[164:167] offset:1056\n\t"
        "ds_write_b128 v122, a[168:171] offset:2176\n\t"
        "ds_write_b128 v122, a[172:175] offset:3232\n\t"
        "ds_write_b128 v122, a[176:179] offset:4352\n\t"
        "ds_write_b128 v122, a[180:183] offset:5408\n\t"
        "ds_write_b128 v122, a[184:187] offset:6528\n\t"
        "ds_write_b128 v122, a[188:191] offset:7584\n\t"
        "ds_read_b64_tr_b16 a[160:161], v123 offset:0\n\t"
        "ds_read_b64_tr_b16 a[162:163], v123 offset:128\n\t"
        "ds_read_b64_tr_b16 a[164:165], v123 offset:512\n\t"
        "ds_read_b64_tr_b16 a[166:167], v123 offset:640\n\t"
        "s_waitcnt lgkmcnt(8)\n\t"
        "ds_read_b64_tr_b16 a[168:169], v123 offset:32\n\t"
        "ds_read_b64_tr_b16 a[170:171], v123 offset:160\n\t"
        "ds_read_b64_tr_b16 a[172:173], v123 offset:544\n\t"
        "ds_read_b64_tr_b16 a[174:175], v123 offset:672\n\t"
        "s_waitcnt lgkmcnt(8)\n\t"
        "ds_read_b64_tr_b16 a[176:177], v123 offset:4352\n\t"
        "ds_read_b64_tr_b16 a[178:179], v123 offset:4480\n\t"
        "ds_read_b64_tr_b16 a[180:181], v123 offset:4864\n\t"
        "ds_read_b64_tr_b16 a[182:183], v123 offset:4992\n\t"
        "s_waitcnt lgkmcnt(8)\n\t"
        "ds_read_b64_tr_b16 a[184:185], v123 offset:4384\n\t"
        "ds_read_b64_tr_b16 a[186:187], v123 offset:4512\n\t"
        "ds_read_b64_tr_b16 a[188:189], v123 offset:4896\n\t"
        "ds_read_b64_tr_b16 a[190:191], v123 offset:5024\n\t"
        "s_waitcnt lgkmcnt(8)\n\t"
        "s_waitcnt lgkmcnt(0)\n\t"
        "s_waitcnt vmcnt(8)\n\t"
        "ds_write_b128 v122, a[192:195] offset:0\n\t"
        "ds_write_b128 v122, a[196:199] offset:1056\n\t"
        "ds_write_b128 v122, a[200:203] offset:2176\n\t"
        "ds_write_b128 v122, a[204:207] offset:3232\n\t"
        "ds_write_b128 v122, a[208:211] offset:4352\n\t"
        "ds_write_b128 v122, a[212:215] offset:5408\n\t"
        "ds_write_b128 v122, a[216:219] offset:6528\n\t"
        "ds_write_b128 v122, a[220:223] offset:7584\n\t"
        "ds_read_b64_tr_b16 a[192:193], v123 offset:0\n\t"
        "ds_read_b64_tr_b16 a[194:195], v123 offset:128\n\t"
        "ds_read_b64_tr_b16 a[196:197], v123 offset:512\n\t"
        "ds_read_b64_tr_b16 a[198:199], v123 offset:640\n\t"
        "s_waitcnt lgkmcnt(8)\n\t"
        "ds_read_b64_tr_b16 a[200:201], v123 offset:32\n\t"
        "ds_read_b64_tr_b16 a[202:203], v123 offset:160\n\t"
        "ds_read_b64_tr_b16 a[204:205], v123 offset:544\n\t"
        "ds_read_b64_tr_b16 a[206:207], v123 offset:672\n\t"
        "s_waitcnt lgkmcnt(8)\n\t"
        "ds_read_b64_tr_b16 a[208:209], v123 offset:4352\n\t"
        "ds_read_b64_tr_b16 a[210:211], v123 offset:4480\n\t"
        "ds_read_b64_tr_b16 a[212:213], v123 offset:4864\n\t"
        "ds_read_b64_tr_b16 a[214:215], v123 offset:4992\n\t"
        "s_waitcnt lgkmcnt(8)\n\t"
        "ds_read_b64_tr_b16 a[216:217], v123 offset:4384\n\t"
        "ds_read_b64_tr_b16 a[218:219], v123 offset:4512\n\t"
        "ds_read_b64_tr_b16 a[220:221], v123 offset:4896\n\t"
        "ds_read_b64_tr_b16 a[222:223], v123 offset:5024\n\t"
        "s_waitcnt lgkmcnt(8)\n\t"
        "s_waitcnt lgkmcnt(0)\n\t"
        "s_waitcnt vmcnt(0)\n\t"
        "ds_write_b128 v122, a[224:227] offset:0\n\t"
        "ds_write_b128 v122, a[228:231] offset:1056\n\t"
        "ds_write_b128 v122, a[232:235] offset:2176\n\t"
        "ds_write_b128 v122, a[236:239] offset:3232\n\t"
        "ds_write_b128 v122, a[240:243] offset:4352\n\t"
        "ds_write_b128 v122, a[244:247] offset:5408\n\t"
        "ds_write_b128 v122, a[248:251] offset:6528\n\t"
        "ds_write_b128 v122, a[252:255] offset:7584\n\t"
        "ds_read_b64_tr_b16 a[224:225], v123 offset:0\n\t"
        "ds_read_b64_tr_b16 a[226:227], v123 offset:128\n\t"
        "ds_read_b64_tr_b16 a[228:229], v123 offset:512\n\t"
        "ds_read_b64_tr_b16 a[230:231], v123 offset:640\n\t"
        "s_waitcnt lgkmcnt(8)\n\t"
        "ds_read_b64_tr_b16 a[232:233], v123 offset:32\n\t"
        "ds_read_b64_tr_b16 a[234:235], v123 offset:160\n\t"
        "ds_read_b64_tr_b16 a[236:237], v123 offset:544\n\t"
        "ds_read_b64_tr_b16 a[238:239], v123 offset:672\n\t"
        "s_waitcnt lgkmcnt(8)\n\t"
        "ds_read_b64_tr_b16 a[240:241], v123 offset:4352\n\t"
        "ds_read_b64_tr_b16 a[242:243], v123 offset:4480\n\t"
        "ds_read_b64_tr_b16 a[244:245], v123 offset:4864\n\t"
        "ds_read_b64_tr_b16 a[246:247], v123 offset:4992\n\t"
        "s_waitcnt lgkmcnt(8)\n\t"
        "ds_read_b64_tr_b16 a[248:249], v123 offset:4384\n\t"
        "ds_read_b64_tr_b16 a[250:251], v123 offset:4512\n\t"
        "ds_read_b64_tr_b16 a[252:253], v123 offset:4896\n\t"
        "ds_read_b64_tr_b16 a[254:255], v123 offset:5024\n\t"
        "s_waitcnt lgkmcnt(8)\n\t"
        "s_mov_b32 s35, 33792\n\t"
        "buffer_load_dword v0, off, s[12:15], s35 sc1\n\t"
        "s_waitcnt lgkmcnt(0)\n\t"
        "s_waitcnt vmcnt(0)\n\t"
        "s_mov_b32 s42, 0\n\t"
        "LB_cal%=:\n\t"
        "v_readfirstlane_b32 s34, v0\n\t"
        "s_nop 0\n\t"
        "s_cmp_lg_u32 s34, 0\n\t"
        "s_cbranch_scc1 LB_calok%=\n\t"
        "s_sleep 4\n\t"
        "s_mov_b32 s35, 33792\n\t"
        "buffer_load_dword v0, off, s[12:15], s35 sc1\n\t"
        "s_waitcnt vmcnt(0)\n\t"
        "s_add_u32 s42, s42, 1\n\t"
        "s_cmp_lt_u32 s42, 0x100000\n\t"
        "s_cbranch_scc1 LB_cal%=\n\t"
        "s_movk_i32 s34, 8\n\t"
        "s_branch LB_die%=\n\t"
        "LB_calok%=:\n\t"
        "s_and_b32 s34, s34, 8192\n\t"
        "s_add_u32 s8, s8, s34\n\t"
        "s_addc_u32 s9, s9, 0\n\t"
        "v_min_u32_e32 v109, -2, v109\n\t"
        "buffer_store_dword v109, v116, s[8:11], 0 offen sc1\n\t"
        "s_memrealtime s[32:33]\n\t"
        "s_waitcnt lgkmcnt(0)\n\t"
        "v_mov_b32_e32 v110, s32\n\t"
        "s_lshl_b32 s34, s29, 8\n\t"
        "s_lshl_b32 s35, s30, 20\n\t"
        "s_or_b32 s54, s34, s35\n\t"
        "s_add_u32 s34, s54, 12589056\n\t"
        "v_mov_b32_e32 v111, s34\n\t"
        "s_barrier\n\t"
        "s_cmp_lg_u32 s43, 0\n\t"
        "s_cbranch_scc1 LB_st%=\n\t"
        "v_mov_b32_e32 v2, 1\n\t"
        "s_mov_b32 s35, 34304\n\t"
        "s_mov_b64 exec, 1\n\t"
        "buffer_atomic_add v2, off, s[12:15], s35 sc0\n\t"
        "s_mov_b64 exec, -1\n\t"
        "s_waitcnt vmcnt(0)\n\t"
        "v_readfirstlane_b32 s34, v2\n\t"
        "s_nop 0\n\t"
        "s_cmp_lg_u32 s34, 95\n\t"
        "s_cbranch_scc1 LB_st%=\n\t"
        "s_memrealtime s[32:33]\n\t"
        "v_mbcnt_lo_u32_b32 v3, -1, 0\n\t"
        "v_lshlrev_b32_e32 v3, 7, v3\n\t"
        "s_waitcnt lgkmcnt(0)\n\t"
        "s_add_u32 s34, s32, 50\n\t"
        "s_or_b32 s34, s34, 1\n\t"
        "v_mov_b32_e32 v2, s34\n\t"
        "s_mov_b32 s35, 36864\n\t"
        "s_mov_b64 exec, 0xffff\n\t"
        "buffer_store_dword v2, v3, s[12:15], s35 offen sc1\n\t"
        "s_mov_b64 exec, -1\n\t"
        "LB_st%=:\n\t"
        "s_mov_b32 s24, 0\n\t"
        "s_mov_b32 s26, 0\n\t"
        "s_mov_b32 s27, 0x80000\n\t"
        "s_mov_b32 s28, 0x180000\n\t"
        "s_mov_b32 s31, 0\n\t"
        "s_mov_b32 s47, 0\n\t"
        "s_mov_b32 s38, 0xff00ff00\n\t"
        "s_mov_b32 s39, 0xff00ff00\n\t"
        "s_mov_b32 s40, 0\n\t"
        "s_mov_b32 s41, -1\n\t"
        "v_mov_b64_e32 v[0:1], 0\n\t"
        "v_mov_b64_e32 v[2:3], 0\n\t"
        "v_mov_b64_e32 v[4:5], 0\n\t"
        "v_mov_b64_e32 v[6:7], 0\n\t"
        "v_mov_b64_e32 v[8:9], 0\n\t"
        "v_mov_b64_e32 v[10:11], 0\n\t"
        "v_mov_b64_e32 v[12:13], 0\n\t"
        "v_mov_b64_e32 v[14:15], 0\n\t"
        "s_mov_b32 s42, 0\n\t"
        "LB_gw%=:\n\t"
        "buffer_load_dword v16, off, s[12:15], s45 sc1\n\t"
        "s_waitcnt vmcnt(0)\n\t"
        "v_readfirstlane_b32 s25, v16\n\t"
        "s_nop 0\n\t"
        "s_cmp_lg_u32 s25, 0\n\t"
        "s_cbranch_scc1 LB_loop%=\n\t"
        "s_sleep 2\n\t"
        "s_add_u32 s42, s42, 1\n\t"
        "s_cmp_lt_u32 s42, 0x100000\n\t"
        "s_cbranch_scc1 LB_gw%=\n\t"
        "s_movk_i32 s34, 9\n\t"
        "s_branch LB_die%=\n\t"
        "LB_loop%=:\n\t"
        "s_mov_b32 s42, 0\n\t"
        "s_sub_u32 s35, s25, 1\n\t"
        "LB_wa%=:\n\t"
        "s_memrealtime s[32:33]\n\t"
        "s_waitcnt lgkmcnt(0)\n\t"
        "s_sub_u32 s34, s35, s32\n\t"
        "s_cmpk_lt_u32 s34, 0x2000\n\t"
        "s_cbranch_scc1 LB_wa%=\n\t"
        "LB_rd%=:\n\t"
        "buffer_load_dwordx4 v[16:19], v112, s[8:11], s26 offen offset:0 sc1\n\t"
        "s_nop 9\n\t"
        "buffer_load_dwordx4 v[20:23], v112, s[8:11], s26 offen offset:1024 sc1\n\t"
        "s_nop 9\n\t"
        "buffer_load_dwordx4 v[24:27], v112, s[8:11], s26 offen offset:2048 sc1\n\t"
        "s_nop 9\n\t"
        "buffer_load_dwordx4 v[28:31], v112, s[8:11], s26 offen offset:3072 sc1\n\t"
        "LB_go%=:\n\t"
        "s_waitcnt vmcnt(3)\n\t"
        "v_smfmac_f32_16x16x64_bf16 v[0:3], v[16:19], v[128:135], v106\n\t"
        "v_max3_u32 v108, v16, v17, v18\n\t"
        "v_max_u32_e32 v108, v19, v108\n\t"
        "v_smfmac_f32_16x16x64_bf16 v[4:7], v[16:19], v[136:143], v106\n\t"
        "v_smfmac_f32_16x16x64_bf16 v[8:11], v[16:19], v[144:151], v106\n\t"
        "v_smfmac_f32_16x16x64_bf16 v[12:15], v[16:19], v[152:159], v106\n\t"
        "s_waitcnt vmcnt(2)\n\t"
        "v_smfmac_f32_16x16x64_bf16 v[0:3], v[20:23], v[160:167], v106\n\t"
        "v_max3_u32 v108, v20, v21, v108\n\t"
        "v_max3_u32 v108, v22, v23, v108\n\t"
        "v_smfmac_f32_16x16x64_bf16 v[4:7], v[20:23], v[168:175], v106\n\t"
        "v_smfmac_f32_16x16x64_bf16 v[8:11], v[20:23], v[176:183], v106\n\t"
        "v_smfmac_f32_16x16x64_bf16 v[12:15], v[20:23], v[184:191], v106\n\t"
        "s_waitcnt vmcnt(1)\n\t"
        "v_smfmac_f32_16x16x64_bf16 v[0:3], v[24:27], v[192:199], v106\n\t"
        "v_max3_u32 v108, v24, v25, v108\n\t"
        "v_max3_u32 v108, v26, v27, v108\n\t"
        "v_smfmac_f32_16x16x64_bf16 v[4:7], v[24:27], v[200:207], v106\n\t"
        "v_smfmac_f32_16x16x64_bf16 v[8:11], v[24:27], v[208:215], v106\n\t"
        "v_smfmac_f32_16x16x64_bf16 v[12:15], v[24:27], v[216:223], v106\n\t"
        "s_waitcnt vmcnt(0)\n\t"
        "v_smfmac_f32_16x16x64_bf16 v[0:3], v[28:31], v[224:231], v106\n\t"
        "v_max3_u32 v108, v28, v29, v108\n\t"
        "v_max3_u32 v108, v30, v31, v108\n\t"
        "v_smfmac_f32_16x16x64_bf16 v[4:7], v[28:31], v[232:239], v106\n\t"
        "v_cmp_eq_u32_e32 vcc, -1, v108\n\t"
        "v_smfmac_f32_16x16x64_bf16 v[8:11], v[28:31], v[240:247], v106\n\t"
        "v_smfmac_f32_16x16x64_bf16 v[12:15], v[28:31], v[248:255], v106\n\t"
        "s_cbranch_vccnz LB_rdfail%=\n\t"
        "s_nop 3\n\t"
        "v_permlane32_swap_b32_e32 v0, v4\n\t"
        "v_permlane32_swap_b32_e32 v1, v5\n\t"
        "v_permlane32_swap_b32_e32 v2, v6\n\t"
        "v_permlane32_swap_b32_e32 v3, v7\n\t"
        "v_pk_add_f32 v[0:1], v[0:1], v[4:5]\n\t"
        "v_pk_add_f32 v[2:3], v[2:3], v[6:7]\n\t"
        "ds_write_b128 v118, v[0:3] offset:0\n\t"
        "v_permlane32_swap_b32_e32 v8, v12\n\t"
        "v_permlane32_swap_b32_e32 v9, v13\n\t"
        "v_permlane32_swap_b32_e32 v10, v14\n\t"
        "v_permlane32_swap_b32_e32 v11, v15\n\t"
        "v_pk_add_f32 v[8:9], v[8:9], v[12:13]\n\t"
        "v_pk_add_f32 v[10:11], v[10:11], v[14:15]\n\t"
        "ds_write_b128 v118, v[8:11] offset:1024\n\t"
        "buffer_store_dwordx2 v[110:111], v117, s[8:11], s26 offen sc1\n\t"
        "s_waitcnt lgkmcnt(0)\n\t"
        "s_barrier\n\t"
        "ds_read_b128 v[16:19], v119 offset:0\n\t"
        "ds_read_b128 v[20:23], v119 offset:2048\n\t"
        "ds_read_b128 v[24:27], v119 offset:4096\n\t"
        "ds_read_b128 v[28:31], v119 offset:6144\n\t"
        "buffer_load_dwordx4 v[64:67], v124, s[8:11], s28 offen sc1\n\t"
        "s_waitcnt lgkmcnt(2)\n\t"
        "v_pk_add_f32 v[16:17], v[16:17], v[20:21]\n\t"
        "v_pk_add_f32 v[18:19], v[18:19], v[22:23]\n\t"
        "s_waitcnt lgkmcnt(0)\n\t"
        "v_pk_add_f32 v[24:25], v[24:25], v[28:29]\n\t"
        "v_pk_add_f32 v[26:27], v[26:27], v[30:31]\n\t"
        "v_pk_add_f32 v[16:17], v[16:17], v[24:25]\n\t"
        "v_pk_add_f32 v[18:19], v[18:19], v[26:27]\n\t"
        "v_cvt_pk_bf16_f32 v20, v16, v17\n\t"
        "v_min_u32_e32 v20, -2, v20\n\t"
        "v_cvt_pk_bf16_f32 v21, v18, v19\n\t"
        "v_min_u32_e32 v21, -2, v21\n\t"
        "buffer_store_dwordx2 v[20:21], v114, s[8:11], s26 offen sc1\n\t"
        "s_cmp_eq_u32 s24, 63\n\t"
        "s_cbranch_scc1 LB_end%=\n\t"
        "s_memrealtime s[52:53]\n\t"
        "v_mov_b64_e32 v[24:25], -1\n\t"
        "buffer_store_dword v24, v116, s[8:11], s28 offen sc1\n\t"
        "s_xor_b32 s34, s26, 0x100000\n\t"
        "buffer_store_dwordx2 v[24:25], v114, s[8:11], s34 offen sc1\n\t"
        "v_mov_b64_e32 v[32:33], 0\n\t"
        "v_mov_b64_e32 v[34:35], 0\n\t"
        "v_mov_b64_e32 v[36:37], 0\n\t"
        "v_mov_b64_e32 v[38:39], 0\n\t"
        "v_mov_b64_e32 v[40:41], 0\n\t"
        "v_mov_b64_e32 v[42:43], 0\n\t"
        "v_mov_b64_e32 v[44:45], 0\n\t"
        "v_mov_b64_e32 v[46:47], 0\n\t"
        "v_mov_b64_e32 v[0:1], 0\n\t"
        "v_mov_b64_e32 v[2:3], 0\n\t"
        "v_mov_b64_e32 v[4:5], 0\n\t"
        "v_mov_b64_e32 v[6:7], 0\n\t"
        "v_mov_b64_e32 v[8:9], 0\n\t"
        "v_mov_b64_e32 v[10:11], 0\n\t"
        "v_mov_b64_e32 v[12:13], 0\n\t"
        "v_mov_b64_e32 v[14:15], 0\n\t"
        "s_add_u32 s46, s26, s69\n\t"
        "s_add_u32 s48, s26, s70\n\t"
        "s_add_u32 s73, s26, s71\n\t"
        "s_add_u32 s74, s26, s72\n\t"
        "s_sleep 1\n\t"
        "buffer_load_dwordx2 v[104:105], v113, s[8:11], s26 offen sc1\n\t"
        "s_add_u32 s66, s49, s43\n\t"
        "s_cmp_lg_u32 s66, 32\n\t"
        "s_cbranch_scc1 LB_norep%=\n\t"
        "s_waitcnt vmcnt(4)\n\t"
        "s_sub_u32 s66, s24, 1\n\t"
        "v_lshrrev_b32_e32 v68, 16, v64\n\t"
        "v_cmp_eq_u32_e32 vcc, s66, v68\n\t"
        "v_subrev_u32_e32 v64, s59, v64\n\t"
        "v_bfe_i32 v64, v64, 0, 16\n\t"
        "v_cndmask_b32_e32 v64, 0, v64, vcc\n\t"
        "v_lshrrev_b32_e32 v68, 16, v65\n\t"
        "v_cmp_eq_u32_e32 vcc, s66, v68\n\t"
        "v_subrev_u32_e32 v65, s59, v65\n\t"
        "v_bfe_i32 v65, v65, 0, 16\n\t"
        "v_cndmask_b32_e32 v65, 0, v65, vcc\n\t"
        "v_lshrrev_b32_e32 v68, 16, v66\n\t"
        "v_cmp_eq_u32_e32 vcc, s66, v68\n\t"
        "v_subrev_u32_e32 v66, s59, v66\n\t"
        "v_bfe_i32 v66, v66, 0, 16\n\t"
        "v_cndmask_b32_e32 v66, 0, v66, vcc\n\t"
        "v_lshrrev_b32_e32 v68, 16, v67\n\t"
        "v_cmp_eq_u32_e32 vcc, s66, v68\n\t"
        "v_subrev_u32_e32 v67, s59, v67\n\t"
        "v_bfe_i32 v67, v67, 0, 16\n\t"
        "v_cndmask_b32_e32 v67, 0, v67, vcc\n\t"
        "v_max3_i32 v64, v64, v65, v66\n\t"
        "v_max_i32_e32 v64, v64, v67\n\t"
        "s_nop 1\n\t"
        "v_max_i32_dpp v64, v64, v64 quad_perm:[1,0,3,2] row_mask:0xf bank_mask:0xf\n\t"
        "s_nop 1\n\t"
        "v_max_i32_dpp v64, v64, v64 quad_perm:[2,3,0,1] row_mask:0xf bank_mask:0xf\n\t"
        "s_nop 1\n\t"
        "v_max_i32_dpp v64, v64, v64 row_ror:4 row_mask:0xf bank_mask:0xf\n\t"
        "s_nop 1\n\t"
        "v_max_i32_dpp v64, v64, v64 row_ror:8 row_mask:0xf bank_mask:0xf\n\t"
        "v_mov_b32_e32 v65, v64\n\t"
        "s_nop 1\n\t"
        "v_permlane16_swap_b32_e32 v64, v65\n\t"
        "v_max_i32_e32 v64, v64, v65\n\t"
        "v_mov_b32_e32 v65, v64\n\t"
        "s_nop 1\n\t"
        "v_permlane32_swap_b32_e32 v64, v65\n\t"
        "v_max_i32_e32 v64, v64, v65\n\t"
        "s_nop 0\n\t"
        "v_readfirstlane_b32 s64, v64\n\t"
        "LB_norep%=:\n\t"
        "s_waitcnt vmcnt(0) lgkmcnt(0)\n\t"
        "s_mov_b32 s61, s29\n\t"
        "s_mov_b32 s62, s30\n\t"
        "s_mov_b32 s63, s31\n\t"
        "v_and_b32_e32 v30, 0xff, v105\n\t"
        "v_cmp_ne_u32_e32 vcc, s24, v30\n\t"
        "s_cbranch_vccz LB_stok%=\n\t"
        "s_mov_b32 s42, 0\n\t"
        "LB_stre%=:\n\t"
        "buffer_load_dwordx2 v[104:105], v113, s[8:11], s26 offen sc1\n\t"
        "s_waitcnt vmcnt(0)\n\t"
        "v_and_b32_e32 v30, 0xff, v105\n\t"
        "v_cmp_ne_u32_e32 vcc, s24, v30\n\t"
        "s_cbranch_vccz LB_stok%=\n\t"
        "s_add_u32 s42, s42, 1\n\t"
        "s_cmp_lt_u32 s42, 0x100000\n\t"
        "s_cbranch_scc1 LB_stre%=\n\t"
        "s_movk_i32 s34, 5\n\t"
        "s_branch LB_die%=\n\t"
        "LB_stok%=:\n\t"
        "v_readfirstlane_b32 s35, v105\n\t"
        "v_subrev_u32_e32 v104, s25, v104\n\t"
        "s_bfe_u32 s29, s35, 0xc0008\n\t"
        "s_lshr_b32 s30, s35, 20\n\t"
        "v_max_i32_dpp v104, v104, v104 quad_perm:[1,0,3,2] row_mask:0xf bank_mask:0xf\n\t"
        "s_nop 1\n\t"
        "v_max_i32_dpp v104, v104, v104 quad_perm:[2,3,0,1] row_mask:0xf bank_mask:0xf\n\t"
        "s_nop 1\n\t"
        "v_max_i32_dpp v104, v104, v104 row_ror:4 row_mask:0xf bank_mask:0xf\n\t"
        "s_nop 1\n\t"
        "v_max_i32_dpp v104, v104, v104 row_ror:8 row_mask:0xf bank_mask:0xf\n\t"
        "v_mov_b32_e32 v105, v104\n\t"
        "s_nop 1\n\t"
        "v_permlane16_swap_b32_e32 v104, v105\n\t"
        "v_max_i32_e32 v104, v104, v105\n\t"
        "v_mov_b32_e32 v105, v104\n\t"
        "s_nop 1\n\t"
        "v_permlane32_swap_b32_e32 v104, v105\n\t"
        "v_max_i32_e32 v104, v104, v105\n\t"
        "s_nop 0\n\t"
        "v_readfirstlane_b32 s36, v104\n\t"
        "s_add_u32 s35, s31, s50\n\t"
        "s_mul_i32 s37, s50, 3\n\t"
        "s_min_u32 s35, s35, s37\n\t"
        "s_add_i32 s34, s36, s51\n\t"
        "s_cmp_gt_i32 s34, 0\n\t"
        "s_cselect_b32 s31, s35, 0\n\t"
        "s_cselect_b32 s34, s34, 0\n\t"
        "s_add_u32 s34, s34, s31\n\t"
        "s_add_u32 s37, s25, s34\n\t"
        "s_add_u32 s45, s37, s29\n\t"
        "s_add_u32 s25, s45, s30\n\t"
        "s_add_u32 s66, s49, s43\n\t"
        "s_cmp_lg_u32 s66, 32\n\t"
        "s_cbranch_scc1 LB_noctl%=\n\t"
        "s_cmp_eq_u32 s24, 0\n\t"
        "s_cbranch_scc1 LB_ctl0%=\n\t"
        "s_cmp_eq_u32 s24, 1\n\t"
        "s_cbranch_scc1 LB_ctl1%=\n\t"
        "s_add_i32 s65, s36, s62\n\t"
        "s_lshl_b32 s66, s65, 1\n\t"
        "s_sub_i32 s66, s66, s56\n\t"
        "s_cmp_gt_i32 s66, 56\n\t"
        "s_cbranch_scc1 LB_c1%=\n\t"
        "s_lshl_b32 s66, s65, 1\n\t"
        "s_add_u32 s34, s56, 16\n\t"
        "s_min_i32 s34, s34, s66\n\t"
        "s_sub_u32 s35, s56, s66\n\t"
        "s_ashr_i32 s35, s35, 2\n\t"
        "s_max_i32 s35, s35, 1\n\t"
        "s_sub_u32 s35, s56, s35\n\t"
        "s_max_i32 s56, s34, s35\n\t"
        "s_branch LB_c2%=\n\t"
        "LB_c1%=:\n\t"
        "s_add_u32 s56, s56, 2\n\t"
        "s_add_i32 s66, s64, s57\n\t"
        "s_cmp_gt_i32 s66, s61\n\t"
        "s_cbranch_scc1 LB_c2%=\n\t"
        "s_add_u32 s57, s57, 4\n\t"
        "s_min_u32 s57, s57, 25\n\t"
        "LB_c2%=:\n\t"
        "s_cmp_lg_u32 s63, 0\n\t"
        "s_cbranch_scc1 LB_c4%=\n\t"
        "s_lshl_b32 s66, s64, 1\n\t"
        "s_sub_i32 s66, s66, s55\n\t"
        "s_cmp_gt_i32 s66, 56\n\t"
        "s_cbranch_scc1 LB_c3%=\n\t"
        "s_lshl_b32 s66, s64, 1\n\t"
        "s_add_u32 s34, s55, 16\n\t"
        "s_min_i32 s34, s34, s66\n\t"
        "s_sub_u32 s35, s55, s66\n\t"
        "s_ashr_i32 s35, s35, 2\n\t"
        "s_max_i32 s35, s35, 1\n\t"
        "s_sub_u32 s35, s55, s35\n\t"
        "s_max_i32 s55, s34, s35\n\t"
        "s_branch LB_c4%=\n\t"
        "LB_c3%=:\n\t"
        "s_add_u32 s55, s55, 2\n\t"
        "s_cmp_lt_i32 s60, s58\n\t"
        "s_cbranch_scc1 LB_c4%=\n\t"
        "s_add_u32 s66, s58, 8\n\t"
        "s_cmp_gt_i32 s60, s66\n\t"
        "s_cbranch_scc1 LB_c4%=\n\t"
        "s_add_u32 s58, s58, 4\n\t"
        "s_min_u32 s58, s58, 22\n\t"
        "LB_c4%=:\n\t"
        "s_and_b32 s66, s24, 3\n\t"
        "s_cmp_eq_u32 s66, 0\n\t"
        "s_cselect_b32 s66, 1, 0\n\t"
        "s_sub_u32 s57, s57, s66\n\t"
        "s_max_u32 s57, s57, 9\n\t"
        "s_sub_u32 s58, s58, s66\n\t"
        "s_max_u32 s58, s58, 6\n\t"
        "s_add_u32 s34, s55, 1\n\t"
        "s_lshr_b32 s34, s34, 1\n\t"
        "s_add_u32 s34, s34, s57\n\t"
        "s_min_u32 s34, s34, 1000\n\t"
        "s_lshl_b32 s34, s34, 8\n\t"
        "s_add_u32 s35, s56, 1\n\t"
        "s_lshr_b32 s35, s35, 1\n\t"
        "s_add_u32 s35, s35, s58\n\t"
        "s_min_u32 s35, s35, 1000\n\t"
        "s_lshl_b32 s35, s35, 20\n\t"
        "s_or_b32 s54, s34, s35\n\t"
        "s_branch LB_ctl1%=\n\t"
        "LB_ctl0%=:\n\t"
        "s_movk_i32 s57, 9\n\t"
        "s_movk_i32 s58, 6\n\t"
        "s_sub_u32 s55, s29, 33\n\t"
        "s_lshl_b32 s55, s55, 1\n\t"
        "s_sub_u32 s56, s30, 18\n\t"
        "s_lshl_b32 s56, s56, 1\n\t"
        "LB_ctl1%=:\n\t"
        "s_mov_b32 s59, s37\n\t"
        "s_sub_u32 s60, 0, s36\n\t"
        "LB_noctl%=:\n\t"
        "s_add_u32 s34, s24, 1\n\t"
        "s_or_b32 s34, s34, s54\n\t"
        "v_mov_b32_e32 v111, s34\n\t"
        "s_mov_b32 s42, 0\n\t"
        "s_sub_u32 s35, s45, 1\n\t"
        "LB_wo%=:\n\t"
        "s_memrealtime s[32:33]\n\t"
        "s_waitcnt lgkmcnt(0)\n\t"
        "s_sub_u32 s34, s35, s32\n\t"
        "s_cmpk_lt_u32 s34, 0x2000\n\t"
        "s_cbranch_scc1 LB_wo%=\n\t"
        "LB_ro%=:\n\t"
        "buffer_load_dwordx4 v[64:67], v115, s[8:11], s46 offen offset:0 sc1\n\t"
        "s_nop 12\n\t"
        "buffer_load_dwordx4 v[68:71], v115, s[8:11], s46 offen offset:1024 sc1\n\t"
        "s_nop 12\n\t"
        "buffer_load_dwordx4 v[72:75], v115, s[8:11], s48 offen offset:0 sc1\n\t"
        "s_nop 12\n\t"
        "buffer_load_dwordx4 v[76:79], v115, s[8:11], s48 offen offset:1024 sc1\n\t"
        "s_nop 12\n\t"
        "buffer_load_dwordx4 v[80:83], v115, s[8:11], s73 offen offset:0 sc1\n\t"
        "s_nop 12\n\t"
        "buffer_load_dwordx4 v[84:87], v115, s[8:11], s73 offen offset:1024 sc1\n\t"
        "s_nop 12\n\t"
        "buffer_load_dwordx4 v[88:91], v115, s[8:11], s74 offen offset:0 sc1\n\t"
        "s_nop 12\n\t"
        "buffer_load_dwordx4 v[92:95], v115, s[8:11], s74 offen offset:1024 sc1\n\t"
        "LB_go2%=:\n\t"
        "s_waitcnt vmcnt(7)\n\t"
        "v_smfmac_f32_16x16x64_bf16 v[40:43], v[64:67], a[16:23], v107\n\t"
        "v_max3_u32 v108, v64, v65, v66\n\t"
        "v_max_u32_e32 v108, v67, v108\n\t"
        "v_smfmac_f32_16x16x64_bf16 v[36:39], v[64:67], a[8:15], v107\n\t"
        "v_smfmac_f32_16x16x64_bf16 v[32:35], v[64:67], a[0:7], v107\n\t"
        "v_smfmac_f32_16x16x64_bf16 v[44:47], v[64:67], a[24:31], v107\n\t"
        "s_waitcnt vmcnt(6)\n\t"
        "v_smfmac_f32_16x16x64_bf16 v[40:43], v[68:71], a[48:55], v107\n\t"
        "v_max3_u32 v108, v68, v69, v108\n\t"
        "v_max3_u32 v108, v70, v71, v108\n\t"
        "v_smfmac_f32_16x16x64_bf16 v[36:39], v[68:71], a[40:47], v107\n\t"
        "v_smfmac_f32_16x16x64_bf16 v[32:35], v[68:71], a[32:39], v107\n\t"
        "v_smfmac_f32_16x16x64_bf16 v[44:47], v[68:71], a[56:63], v107\n\t"
        "s_waitcnt vmcnt(5)\n\t"
        "v_smfmac_f32_16x16x64_bf16 v[40:43], v[72:75], a[80:87], v107\n\t"
        "v_max3_u32 v108, v72, v73, v108\n\t"
        "v_max3_u32 v108, v74, v75, v108\n\t"
        "v_smfmac_f32_16x16x64_bf16 v[36:39], v[72:75], a[72:79], v107\n\t"
        "v_smfmac_f32_16x16x64_bf16 v[32:35], v[72:75], a[64:71], v107\n\t"
        "v_smfmac_f32_16x16x64_bf16 v[44:47], v[72:75], a[88:95], v107\n\t"
        "s_waitcnt vmcnt(4)\n\t"
        "v_smfmac_f32_16x16x64_bf16 v[40:43], v[76:79], a[112:119], v107\n\t"
        "v_max3_u32 v108, v76, v77, v108\n\t"
        "v_max3_u32 v108, v78, v79, v108\n\t"
        "v_smfmac_f32_16x16x64_bf16 v[36:39], v[76:79], a[104:111], v107\n\t"
        "v_smfmac_f32_16x16x64_bf16 v[32:35], v[76:79], a[96:103], v107\n\t"
        "v_smfmac_f32_16x16x64_bf16 v[44:47], v[76:79], a[120:127], v107\n\t"
        "s_waitcnt vmcnt(3)\n\t"
        "v_smfmac_f32_16x16x64_bf16 v[40:43], v[80:83], a[144:151], v107\n\t"
        "v_max3_u32 v108, v80, v81, v108\n\t"
        "v_max3_u32 v108, v82, v83, v108\n\t"
        "v_smfmac_f32_16x16x64_bf16 v[36:39], v[80:83], a[136:143], v107\n\t"
        "v_smfmac_f32_16x16x64_bf16 v[32:35], v[80:83], a[128:135], v107\n\t"
        "v_smfmac_f32_16x16x64_bf16 v[44:47], v[80:83], a[152:159], v107\n\t"
        "s_waitcnt vmcnt(2)\n\t"
        "v_smfmac_f32_16x16x64_bf16 v[40:43], v[84:87], a[176:183], v107\n\t"
        "v_max3_u32 v108, v84, v85, v108\n\t"
        "v_max3_u32 v108, v86, v87, v108\n\t"
        "v_smfmac_f32_16x16x64_bf16 v[36:39], v[84:87], a[168:175], v107\n\t"
        "v_smfmac_f32_16x16x64_bf16 v[32:35], v[84:87], a[160:167], v107\n\t"
        "v_smfmac_f32_16x16x64_bf16 v[44:47], v[84:87], a[184:191], v107\n\t"
        "s_waitcnt vmcnt(1)\n\t"
        "v_smfmac_f32_16x16x64_bf16 v[40:43], v[88:91], a[208:215], v107\n\t"
        "v_max3_u32 v108, v88, v89, v108\n\t"
        "v_max3_u32 v108, v90, v91, v108\n\t"
        "v_smfmac_f32_16x16x64_bf16 v[36:39], v[88:91], a[200:207], v107\n\t"
        "v_smfmac_f32_16x16x64_bf16 v[32:35], v[88:91], a[192:199], v107\n\t"
        "v_smfmac_f32_16x16x64_bf16 v[44:47], v[88:91], a[216:223], v107\n\t"
        "s_waitcnt vmcnt(0)\n\t"
        "v_smfmac_f32_16x16x64_bf16 v[40:43], v[92:95], a[240:247], v107\n\t"
        "v_max3_u32 v108, v92, v93, v108\n\t"
        "v_max3_u32 v108, v94, v95, v108\n\t"
        "v_smfmac_f32_16x16x64_bf16 v[36:39], v[92:95], a[232:239], v107\n\t"
        "v_cmp_eq_u32_e32 vcc, -1, v108\n\t"
        "v_smfmac_f32_16x16x64_bf16 v[32:35], v[92:95], a[224:231], v107\n\t"
        "v_smfmac_f32_16x16x64_bf16 v[44:47], v[92:95], a[248:255], v107\n\t"
        "s_cbranch_vccnz LB_rofail%=\n\t"
        "s_nop 3\n\t"
        "v_permlane32_swap_b32_e32 v40, v36\n\t"
        "v_permlane32_swap_b32_e32 v41, v37\n\t"
        "v_permlane32_swap_b32_e32 v42, v38\n\t"
        "v_permlane32_swap_b32_e32 v43, v39\n\t"
        "v_pk_add_f32 v[32:33], v[32:33], v[36:37]\n\t"
        "v_pk_add_f32 v[34:35], v[34:35], v[38:39]\n\t"
        "s_nop 0\n\t"
        "v_add_f32_dpp v32, v40, v44 quad_perm:[0,1,2,3] row_mask:0xc bank_mask:0xf\n\t"
        "v_add_f32_dpp v33, v41, v45 quad_perm:[0,1,2,3] row_mask:0xc bank_mask:0xf\n\t"
        "v_add_f32_dpp v34, v42, v46 quad_perm:[0,1,2,3] row_mask:0xc bank_mask:0xf\n\t"
        "v_add_f32_dpp v35, v43, v47 quad_perm:[0,1,2,3] row_mask:0xc bank_mask:0xf\n\t"
        "v_permlane16_swap_b32_e32 v32, v33\n\t"
        "s_nop 0\n\t"
        "v_permlane16_swap_b32_e32 v34, v35\n\t"
        "v_add_f32_e32 v32, v32, v33\n\t"
        "v_add_f32_e32 v34, v34, v35\n\t"
        "ds_write2_b32 v120, v32, v34 offset0:0 offset1:64\n\t"
        "s_waitcnt lgkmcnt(0)\n\t"
        "s_barrier\n\t"
        "ds_read2st64_b64 v[64:67], v121 offset0:0 offset1:1\n\t"
        "ds_read2st64_b64 v[68:71], v121 offset0:2 offset1:3\n\t"
        "s_waitcnt lgkmcnt(1)\n\t"
        "v_pk_add_f32 v[64:65], v[64:65], v[66:67]\n\t"
        "s_waitcnt lgkmcnt(0)\n\t"
        "v_pk_add_f32 v[68:69], v[68:69], v[70:71]\n\t"
        "v_pk_add_f32 v[64:65], v[64:65], v[68:69]\n\t"
        "v_cvt_pk_bf16_f32 v64, v64, v65\n\t"
        "v_min_u32_e32 v64, -2, v64\n\t"
        "buffer_store_dword v64, v116, s[8:11], s27 offen sc1\n\t"
        "s_memrealtime s[32:33]\n\t"
        "s_waitcnt lgkmcnt(0)\n\t"
        "v_mov_b32_e32 v110, s32\n\t"
        "s_add_u32 s24, s24, 1\n\t"
        "s_mov_b32 s28, s26\n\t"
        "s_mov_b32 s26, s27\n\t"
        "s_add_u32 s27, s27, 0x80000\n\t"
        "s_and_b32 s27, s27, 0x1fffff\n\t"
        "s_branch LB_loop%=\n\t"
        "LB_end%=:\n\t"
        "s_endpgm\n\t"
        "LB_rdfail%=:\n\t"
        "s_or_b32 s47, s47, 1\n\t"
        "buffer_load_dwordx4 v[16:19], v112, s[8:11], s26 offen offset:0 sc1\n\t"
        "buffer_load_dwordx4 v[20:23], v112, s[8:11], s26 offen offset:1024 sc1\n\t"
        "buffer_load_dwordx4 v[24:27], v112, s[8:11], s26 offen offset:2048 sc1\n\t"
        "buffer_load_dwordx4 v[28:31], v112, s[8:11], s26 offen offset:3072 sc1\n\t"
        "s_nop 7\n\t"
        "v_mov_b64_e32 v[0:1], 0\n\t"
        "v_mov_b64_e32 v[2:3], 0\n\t"
        "v_mov_b64_e32 v[4:5], 0\n\t"
        "v_mov_b64_e32 v[6:7], 0\n\t"
        "v_mov_b64_e32 v[8:9], 0\n\t"
        "v_mov_b64_e32 v[10:11], 0\n\t"
        "v_mov_b64_e32 v[12:13], 0\n\t"
        "v_mov_b64_e32 v[14:15], 0\n\t"
        "s_branch LB_rdre%=w\n\t"
        "LB_rdre%=:\n\t"
        "buffer_load_dwordx4 v[16:19], v112, s[8:11], s26 offen offset:0 sc1\n\t"
        "buffer_load_dwordx4 v[20:23], v112, s[8:11], s26 offen offset:1024 sc1\n\t"
        "buffer_load_dwordx4 v[24:27], v112, s[8:11], s26 offen offset:2048 sc1\n\t"
        "buffer_load_dwordx4 v[28:31], v112, s[8:11], s26 offen offset:3072 sc1\n\t"
        "LB_rdre%=w:\n\t"
        "s_waitcnt vmcnt(0) lgkmcnt(0)\n\t"
        "v_max3_u32 v108, v16, v17, v18\n\t"
        "v_max3_u32 v108, v108, v19, v20\n\t"
        "v_max3_u32 v108, v108, v21, v22\n\t"
        "v_max3_u32 v108, v108, v23, v24\n\t"
        "v_max3_u32 v108, v108, v25, v26\n\t"
        "v_max3_u32 v108, v108, v27, v28\n\t"
        "v_max3_u32 v108, v108, v29, v30\n\t"
        "v_max_u32_e32 v108, v108, v31\n\t"
        "v_cmp_eq_u32_e32 vcc, -1, v108\n\t"
        "s_nop 0\n\t"
        "s_cbranch_vccz LB_go%=\n\t"
        "s_add_u32 s42, s42, 1\n\t"
        "s_cmp_lt_u32 s42, 0x100000\n\t"
        "s_cbranch_scc1 LB_rdre%=\n\t"
        "s_movk_i32 s34, 3\n\t"
        "s_branch LB_die%=\n\t"
        "LB_rofail%=:\n\t"
        "s_or_b32 s47, s47, 4\n\t"
        "buffer_load_dwordx4 v[64:67], v115, s[8:11], s46 offen offset:0 sc1\n\t"
        "buffer_load_dwordx4 v[68:71], v115, s[8:11], s46 offen offset:1024 sc1\n\t"
        "buffer_load_dwordx4 v[72:75], v115, s[8:11], s48 offen offset:0 sc1\n\t"
        "buffer_load_dwordx4 v[76:79], v115, s[8:11], s48 offen offset:1024 sc1\n\t"
        "buffer_load_dwordx4 v[80:83], v115, s[8:11], s73 offen offset:0 sc1\n\t"
        "buffer_load_dwordx4 v[84:87], v115, s[8:11], s73 offen offset:1024 sc1\n\t"
        "buffer_load_dwordx4 v[88:91], v115, s[8:11], s74 offen offset:0 sc1\n\t"
        "buffer_load_dwordx4 v[92:95], v115, s[8:11], s74 offen offset:1024 sc1\n\t"
        "s_nop 7\n\t"
        "v_mov_b64_e32 v[32:33], 0\n\t"
        "v_mov_b64_e32 v[34:35], 0\n\t"
        "v_mov_b64_e32 v[36:37], 0\n\t"
        "v_mov_b64_e32 v[38:39], 0\n\t"
        "v_mov_b64_e32 v[40:41], 0\n\t"
        "v_mov_b64_e32 v[42:43], 0\n\t"
        "v_mov_b64_e32 v[44:45], 0\n\t"
        "v_mov_b64_e32 v[46:47], 0\n\t"
        "s_branch LB_rore%=w\n\t"
        "LB_rore%=:\n\t"
        "buffer_load_dwordx4 v[64:67], v115, s[8:11], s46 offen offset:0 sc1\n\t"
        "buffer_load_dwordx4 v[68:71], v115, s[8:11], s46 offen offset:1024 sc1\n\t"
        "buffer_load_dwordx4 v[72:75], v115, s[8:11], s48 offen offset:0 sc1\n\t"
        "buffer_load_dwordx4 v[76:79], v115, s[8:11], s48 offen offset:1024 sc1\n\t"
        "buffer_load_dwordx4 v[80:83], v115, s[8:11], s73 offen offset:0 sc1\n\t"
        "buffer_load_dwordx4 v[84:87], v115, s[8:11], s73 offen offset:1024 sc1\n\t"
        "buffer_load_dwordx4 v[88:91], v115, s[8:11], s74 offen offset:0 sc1\n\t"
        "buffer_load_dwordx4 v[92:95], v115, s[8:11], s74 offen offset:1024 sc1\n\t"
        "LB_rore%=w:\n\t"
        "s_waitcnt vmcnt(0) lgkmcnt(0)\n\t"
        "v_max3_u32 v108, v64, v65, v66\n\t"
        "v_max3_u32 v108, v108, v67, v68\n\t"
        "v_max3_u32 v108, v108, v69, v70\n\t"
        "v_max3_u32 v108, v108, v71, v72\n\t"
        "v_max3_u32 v108, v108, v73, v74\n\t"
        "v_max3_u32 v108, v108, v75, v76\n\t"
        "v_max3_u32 v108, v108, v77, v78\n\t"
        "v_max3_u32 v108, v108, v79, v80\n\t"
        "v_max3_u32 v108, v108, v81, v82\n\t"
        "v_max3_u32 v108, v108, v83, v84\n\t"
        "v_max3_u32 v108, v108, v85, v86\n\t"
        "v_max3_u32 v108, v108, v87, v88\n\t"
        "v_max3_u32 v108, v108, v89, v90\n\t"
        "v_max3_u32 v108, v108, v91, v92\n\t"
        "v_max3_u32 v108, v108, v93, v94\n\t"
        "v_max_u32_e32 v108, v108, v95\n\t"
        "v_cmp_eq_u32_e32 vcc, -1, v108\n\t"
        "s_nop 0\n\t"
        "s_cbranch_vccz LB_go2%=\n\t"
        "s_add_u32 s42, s42, 1\n\t"
        "s_cmp_lt_u32 s42, 0x100000\n\t"
        "s_cbranch_scc1 LB_rore%=\n\t"
        "s_movk_i32 s34, 4\n\t"
        "s_branch LB_die%=\n\t"
        "LB_die%=:\n\t"
        "v_mov_b32_e32 v30, s34\n\t"
        "s_mov_b32 s35, 33024\n\t"
        "s_mov_b64 exec, 1\n\t"
        "buffer_store_dword v30, off, s[12:15], s35 sc1\n\t"
        "s_endpgm\n\t"
        ".Lk_end_B:\n\t"
        :: REC_S_IN, "{v96}"(g_v), "{v97}"(g_o), "{v98}"(wb0), "{v99}"(wb1), "{v100}"(rb0), "{v101}"(rb0 ^ 32u), "{v102}"(rb0 ^ 64u), "{v103}"(rb0 ^ 96u),
           "{v106}"(pp ? 0xEEEEEEEEu : 0x44444444u), "{v107}"(bb ? 0xEEEEEEEEu : 0x44444444u), "{v[108:109]}"(m0p), "{s68}"(rot),
           "{v112}"(a_mem), "{v113}"((24 + cp) * 16384 + l * 128), "{v114}"(a_vmw), "{v115}"(a_or), "{v116}"(a_mw), "{v117}"(a_sw),
           "{v118}"(lb + w * 2048 + l * 16), "{v119}"(lb + (w & 1) * 1024 + l * 16), "{v120}"(lb + LB_R + w * 512 + (g & 1) * 128 + ((g >> 1) * 16 + n) * 4),
           "{v121}"(lb + LB_R + ((l >> 2) & 3) * 128 + g * 32 + (l & 3) * 8), "{v122}"(stg + l * 16), "{v123}"(o_r), "{v124}"(rb + w == 32 ? 28 * 16384 + (l & 31) * 128 : OOB)
        : "memory", "vcc", "scc", "v0", "v255", "a0", "a255", "s101");
    __builtin_unreachable();
}
__device__ __forceinline__ void rec_helper(const Params& p, rsrc_t r, unsigned char* lds, const int wv, const int hi) {
    const int tid = otid(), l = tid & 63;
    const rsrc_t rh = __builtin_amdgcn_make_buffer_rsrc(p.ws + hx_base(p.ws), 0, HX_BYTES, 0x00020000);
    const v4u ones = {0xFFFFFFFFu, 0xFFFFFFFFu, 0xFFFFFFFFu, 0xFFFFFFFFu};
    unsigned best0 = ~0u, best1 = ~0u, d[5] = {0u, 0u, 0u, 0u, 0u};
    const bool meas = (hi & 7) < 2;
    if (meas) {
        const unsigned ca = l >= 8 ? OOB : (31 + wv * 32) * 16384 + hi * 256 + l * 16;
        const unsigned tend = rtnow() + CAL_T;
#pragma unroll 1
        for (int i = 0; i < 64 && (i < 3 || (int)(rtnow() - tend) < 0); ++i) {
            const unsigned t0 = (unsigned)__builtin_amdgcn_s_memtime(); __builtin_amdgcn_s_waitcnt(0);
            st16(rh, ca, ones); DRAIN();
            const unsigned t1 = (unsigned)__builtin_amdgcn_s_memtime(); __builtin_amdgcn_s_waitcnt(0);
            st16(rh, ca + 8192, ones); DRAIN();
            const unsigned t2 = (unsigned)__builtin_amdgcn_s_memtime(); __builtin_amdgcn_s_waitcnt(0);
            best0 = min(best0, t1 - t0); best1 = min(best1, t2 - t1); }
        if (l < 2) __hip_atomic_fetch_max((unsigned*)(p.ws + F_CAL + 128) + l, 65535u - min(l ? best1 : best0, 65535u), __ATOMIC_RELAXED, __HIP_MEMORY_SCOPE_AGENT);
    } else if (TOUCH) {
        const int xq = hi >> 3, k = ((hi & 7) - 2) * 4 + wv;
#pragma unroll
        for (int i = 0; i < 5; ++i) { const bf16_t* q;
            if (i & 1) { const int z = (i >> 1) * 24 + k, st = z & 3, c = 4 + (z >> 2);
                q = ((st & 1) ? p.Wk : p.Wq) + (size_t)(((((st & 1) * 2 + 1) * 4 + c) & 15) * 64 + l) * 4096 + (xq * 8 + 1 + (st >> 1) * 4) * 128 + 64; }
            else { const int z = min((i >> 1) * 24 + k, 63);
                q = p.Wo + (size_t)(((z & 3) * 8 + (z >> 3)) * 128 + ((z >> 2) & 1) * 64 + l) * 1024 + (RPL == 4 ? (xq >> 1) : (xq & 1)) * 512 + 192; if (RPL == 4 && (xq & 1)) q = p.gate_w; }
            asm volatile("global_load_dword %0, %1, off" : "+v"(d[i]) : "v"(q) : "memory"); }
    }
#pragma unroll
    for (int i = 0; i < 16; ++i) st16(rh, hi * 65536 + i * 4096 + tid * 16, ones);
    asm volatile("s_waitcnt vmcnt(0)" : "+v"(d[0]), "+v"(d[1]), "+v"(d[2]), "+v"(d[3]), "+v"(d[4]) :: "memory");
    __syncthreads();
    if (tid == 0 && __hip_atomic_fetch_add((unsigned*)(p.ws + F_CAL + 256), 1u, __ATOMIC_RELAXED, __HIP_MEMORY_SCOPE_AGENT) == 31u) {
        const v2u c = ld8(r, F_CAL + 128); st4(r, F_CAL, c.x < c.y ? 8193u : 1u); }
}
using f32x16 = __attribute__((ext_vector_type(16))) float;
constexpr int CV_RP = 80;
constexpr int CV_U = 96 * CV_RP;
constexpr int CV_KP = 4416;
constexpr int CV_K0 = 8 * CV_U;
__device__ __forceinline__ float blo(unsigned x) { return __uint_as_float(x << 16); }
__device__ __forceinline__ float bhi(unsigned x) { return __uint_as_float(x & 0xFFFF0000u); }
__device__ __forceinline__ void gate_block(const Params& p, rsrc_t r, const int hi, const int wv) {
    const int l = otid() & 63, t0 = hi * 64 + wv * 16;
    v4u xg[16][2], gw[2];
#pragma unroll
    for (int q = 0; q < 2; ++q) gw[q] = *(const v4u*)(p.gate_w + q * 512 + l * 8);
#pragma unroll
    for (int i = 0; i < 16; ++i)
#pragma unroll
        for (int q = 0; q < 2; ++q) xg[i][q] = *(const v4u*)(p.Aq + (size_t)(t0 + i) * D + q * 512 + l * 8);
    float mine = 0.f;
#pragma unroll
    for (int i = 0; i < 16; ++i) { float acc = 0.f;
#pragma unroll
        for (int q = 0; q < 2; ++q)
#pragma unroll
            for (int e = 0; e < 4; ++e) { const unsigned pr = pack2(blo(xg[i][q][e]) * blo(gw[q][e]), bhi(xg[i][q][e]) * bhi(gw[q][e])); acc += blo(pr); acc += bhi(pr); }
        for (int o = 32; o > 0; o >>= 1) acc += __shfl_xor(acc, o);
        if (l == i) mine = acc; }
    if (l < 16) { const float z = rbfh(mine); st4(r, W_GATE + (t0 + l) * 4, __float_as_uint(rbfh(1.f / (1.f + expf(-z))))); }
}
__device__ __forceinline__ void conv_block(const Params& p, rsrc_t r, unsigned char* lds, const int wv, const int hi, const int j) {
    (void)j;
    const int tid = otid(), w = wv, l = tid & 63, Q = tid >> 2, cg = tid & 3, ch0 = hi * 32 + cg * 8;
    for (int i = tid; i < 8 * 640; i += 256) *(unsigned*)(lds + (i / 640) * CV_U + (i % 640) * 4) = 0u;
    unsigned char* kc = lds + CV_K0 + w * 4 * CV_KP;
    if (l < 32) {
#pragma unroll
        for (int q = 0; q < 4; ++q) *(unsigned*)(kc + q * CV_KP + l * 4) = 0u; }
    v4u u[32];
    {
        v4u sk[4]; float sv[8][4];
#pragma unroll
        for (int q = 0; q < 4; ++q) sk[q] = *(const v4u*)(p.shift_k + ch0 * 4 + q * 8);
        const rsrc_t rk = __builtin_amdgcn_make_buffer_rsrc((void*)p.Ak, 0, S * D * 2, 0x00020000), rv = __builtin_amdgcn_make_buffer_rsrc((void*)p.Av, 0, S * D * 2, 0x00020000);
        const unsigned ro = (unsigned)(Q * 32 * D + ch0) * 2u;
        v4u kr[19], kn[16];
#pragma unroll
        for (int i = 0; i < 19; ++i) kr[i] = __builtin_amdgcn_raw_buffer_load_b128(rk, (i < 3 && Q == 0) ? OOB : ro + (i - 3) * 2048, 0, CV_POL);
#pragma unroll
        for (int i = 0; i < 16; ++i) u[i] = __builtin_amdgcn_raw_buffer_load_b128(rv, ro + i * 2048, 0, CV_POL);
#pragma unroll
        for (int i = 0; i < 16; ++i) kn[i] = __builtin_amdgcn_raw_buffer_load_b128(rk, ro + (16 + i) * 2048, 0, CV_POL);
#pragma unroll
        for (int c8 = 0; c8 < 8; ++c8)
#pragma unroll
            for (int tp = 0; tp < 4; ++tp) { const unsigned sw = sk[c8 >> 1][(c8 & 1) * 2 + (tp >> 1)]; sv[c8][tp] = (tp & 1) ? bhi(sw) : blo(sw); }
        auto row = [&](int i, const v4u& k0, const v4u& k1, const v4u& k2, const v4u& k3) {
#pragma unroll
            for (int q = 0; q < 4; ++q) {
                unsigned ks = pack2(blo(k0[q]) * sv[2 * q][3], bhi(k0[q]) * sv[2 * q + 1][3]), pr;
                pr = pack2(blo(k1[q]) * sv[2 * q][2], bhi(k1[q]) * sv[2 * q + 1][2]); ks = pack2(blo(ks) + blo(pr), bhi(ks) + bhi(pr));
                pr = pack2(blo(k2[q]) * sv[2 * q][1], bhi(k2[q]) * sv[2 * q + 1][1]); ks = pack2(blo(ks) + blo(pr), bhi(ks) + bhi(pr));
                pr = pack2(blo(k3[q]) * sv[2 * q][0], bhi(k3[q]) * sv[2 * q + 1][0]); ks = pack2(blo(ks) + blo(pr), bhi(ks) + bhi(pr));
                u[i][q] = pack2(blo(ks) * blo(u[i][q]), bhi(ks) * bhi(u[i][q])); } };
        __builtin_amdgcn_sched_barrier(0);
#pragma unroll
        for (int i = 0; i < 16; ++i) { row(i, kr[i], kr[i + 1], kr[i + 2], kr[i + 3]); u[16 + i] = __builtin_amdgcn_raw_buffer_load_b128(rv, ro + (16 + i) * 2048, 0, CV_POL); __builtin_amdgcn_sched_barrier(0); }
#pragma unroll
        for (int i = 0; i < 16; ++i) row(16 + i, i < 3 ? kr[16 + i] : kn[i - 3], i < 2 ? kr[17 + i] : kn[i - 2], i < 1 ? kr[18] : kn[i - 1], kn[i]);
    }
    BSTAMP(8);
    const rsrc_t rs = __builtin_amdgcn_make_buffer_rsrc((void*)p.ssm_k, 0, S * D * 2, 0x00020000);
    const int b = l & 31, kg = l >> 5;
    const unsigned kb = (unsigned)(size_t)kc + (b & 3) * CV_KP + ((b & 28) - 8 * kg + 56 - 16) * 2, ub0 = (unsigned)(size_t)lds + (1 + b) * CV_RP + kg * 16;
    v4u kraw[4]; v2u kext[4];
    auto kload = [&](int n) { const unsigned o = (unsigned)((hi * 32 + w * 8 + n) * S + l * 8) * 2u;
#pragma unroll
        for (int i = 0; i < 4; ++i) { kraw[i] = __builtin_amdgcn_raw_buffer_load_b128(rs, o + i * 1024, 0, CV_POL);
            kext[i] = __builtin_amdgcn_raw_buffer_load_b64(rs, (i == 3 && l == 63) ? OOB : o + i * 1024 + 16, 0, 0); } };
    kload(0);
    const v4u dv = *(const v4u*)(p.ssm_D + ch0);
    unsigned char* const urow = lds + 2 * cg * CV_U + (32 + Q) * CV_RP;
#pragma unroll
    for (int rd = 0; rd < 4; ++rd) {
#pragma unroll
        for (int G = 0; G < 4; ++G) { v4u lo, hh;
#pragma unroll
            for (int q = 0; q < 4; ++q) { const unsigned x = u[8 * G + 7 - 2 * q][rd], y = u[8 * G + 6 - 2 * q][rd];
                lo[q] = __builtin_amdgcn_perm(y, x, 0x05040100u); hh[q] = __builtin_amdgcn_perm(y, x, 0x07060302u); }
            *(v4u*)(urow + G * 16) = lo; *(v4u*)(urow + CV_U + G * 16) = hh; }
        __syncthreads();
#pragma unroll 1
        for (int ci = 0; ci < 2; ++ci) {
            const int c8 = 2 * w + ci;
#pragma unroll
            for (int i = 0; i < 4; ++i) { const v4u e = kraw[i]; const v2u f = kext[i]; unsigned char* q = kc + 128 + (i * 64 + l) * 16;
                const unsigned s0 = __builtin_amdgcn_alignbit(e.y, e.x, 16), s1 = __builtin_amdgcn_alignbit(e.z, e.y, 16), s2 = __builtin_amdgcn_alignbit(e.w, e.z, 16),
                               s3 = __builtin_amdgcn_alignbit(f.x, e.w, 16), s4 = __builtin_amdgcn_alignbit(f.y, f.x, 16);
                *(v4u*)q = (v4u){s0, s1, s2, s3}; *(v4u*)(q + CV_KP) = (v4u){e.y, e.z, e.w, f.x}; *(v4u*)(q + 2 * CV_KP) = (v4u){s1, s2, s3, s4}; *(v4u*)(q + 3 * CV_KP) = (v4u){e.z, e.w, f.x, f.y};
                if (i == 0 && l == 0) {
                    *(v2u*)(q - 8) = (v2u){0u, e.x << 16}; *(v2u*)(q + CV_KP - 8) = (v2u){0u, e.x}; *(v2u*)(q + 2 * CV_KP - 8) = (v2u){e.x << 16, s0}; *(v2u*)(q + 3 * CV_KP - 8) = (v2u){e.x, e.y}; } }
            if (2 * rd + ci < 7) kload(2 * rd + ci + 1);
            __builtin_amdgcn_wave_barrier();
            f32x16 acc0, acc1;
            if (rd == 1) BSTAMP(ci == 0 ? 10 : 13);
            asm volatile(
                "ds_read_b64 v[192:193], %[kb] offset:32\n\t"
                "ds_read_b64 v[194:195], %[kb] offset:40\n\t"
                "ds_read_b128 v[196:199], %[ub] offset:5040\n\t"
                "ds_read_b128 v[200:203], %[ub] offset:2480\n\t"
                "ds_read_b64 v[204:205], %[kb] offset:2080\n\t"
                "ds_read_b64 v[206:207], %[kb] offset:2088\n\t"
                "ds_read_b64 v[208:209], %[kb] offset:0\n\t"
                "ds_read_b64 v[210:211], %[kb] offset:8\n\t"
                "ds_read_b128 v[212:215], %[ub] offset:5072\n\t"
                "ds_read_b128 v[216:219], %[ub] offset:2512\n\t"
                "ds_read_b64 v[220:221], %[kb] offset:2048\n\t"
                "ds_read_b64 v[222:223], %[kb] offset:2056\n\t"
                "s_waitcnt lgkmcnt(9)\n\t"
                "v_mfma_f32_32x32x16_bf16 %[a1], v[192:195], v[196:199], 0\n\t"
                "ds_read_b64 v[224:225], %[kb] offset:96\n\t"
                "ds_read_b64 v[226:227], %[kb] offset:104\n\t"
                "ds_read_b128 v[228:231], %[ub] offset:4960\n\t"
                "s_waitcnt lgkmcnt(11)\n\t"
                "v_mfma_f32_32x32x16_bf16 %[a0], v[192:195], v[200:203], 0\n\t"
                "ds_read_b128 v[232:235], %[ub] offset:2400\n\t"
                "s_waitcnt lgkmcnt(10)\n\t"
                "v_mfma_f32_32x32x16_bf16 %[a1], v[204:207], v[200:203], %[a1]\n\t"
                "ds_read_b64 v[236:237], %[kb] offset:2144\n\t"
                "ds_read_b64 v[238:239], %[kb] offset:2152\n\t"
                "s_waitcnt lgkmcnt(9)\n\t"
                "v_mfma_f32_32x32x16_bf16 %[a1], v[208:211], v[212:215], %[a1]\n\t"
                "ds_read_b64 v[240:241], %[kb] offset:64\n\t"
                "ds_read_b64 v[242:243], %[kb] offset:72\n\t"
                "ds_read_b128 v[244:247], %[ub] offset:4992\n\t"
                "s_waitcnt lgkmcnt(11)\n\t"
                "v_mfma_f32_32x32x16_bf16 %[a0], v[208:211], v[216:219], %[a0]\n\t"
                "ds_read_b128 v[248:251], %[ub] offset:2432\n\t"
                "s_waitcnt lgkmcnt(10)\n\t"
                "v_mfma_f32_32x32x16_bf16 %[a1], v[220:223], v[216:219], %[a1]\n\t"
                "ds_read_b64 v[252:253], %[kb] offset:2112\n\t"
                "ds_read_b64 v[254:255], %[kb] offset:2120\n\t"
                "s_waitcnt lgkmcnt(9)\n\t"
                "v_mfma_f32_32x32x16_bf16 %[a1], v[224:227], v[228:231], %[a1]\n\t"
                "ds_read_b64 v[192:193], %[kb] offset:160\n\t"
                "ds_read_b64 v[194:195], %[kb] offset:168\n\t"
                "ds_read_b128 v[196:199], %[ub] offset:4880\n\t"
                "s_waitcnt lgkmcnt(11)\n\t"
                "v_mfma_f32_32x32x16_bf16 %[a0], v[224:227], v[232:235], %[a0]\n\t"
                "ds_read_b128 v[200:203], %[ub] offset:2320\n\t"
                "s_waitcnt lgkmcnt(10)\n\t"
                "v_mfma_f32_32x32x16_bf16 %[a1], v[236:239], v[232:235], %[a1]\n\t"
                "ds_read_b64 v[204:205], %[kb] offset:2208\n\t"
                "ds_read_b64 v[206:207], %[kb] offset:2216\n\t"
                "s_waitcnt lgkmcnt(9)\n\t"
                "v_mfma_f32_32x32x16_bf16 %[a1], v[240:243], v[244:247], %[a1]\n\t"
                "ds_read_b64 v[208:209], %[kb] offset:128\n\t"
                "ds_read_b64 v[210:211], %[kb] offset:136\n\t"
                "ds_read_b128 v[212:215], %[ub] offset:4912\n\t"
                "s_waitcnt lgkmcnt(11)\n\t"
                "v_mfma_f32_32x32x16_bf16 %[a0], v[240:243], v[248:251], %[a0]\n\t"
                "ds_read_b128 v[216:219], %[ub] offset:2352\n\t"
                "s_waitcnt lgkmcnt(10)\n\t"
                "v_mfma_f32_32x32x16_bf16 %[a1], v[252:255], v[248:251], %[a1]\n\t"
                "ds_read_b64 v[220:221], %[kb] offset:2176\n\t"
                "ds_read_b64 v[222:223], %[kb] offset:2184\n\t"
                "s_waitcnt lgkmcnt(9)\n\t"
                "v_mfma_f32_32x32x16_bf16 %[a1], v[192:195], v[196:199], %[a1]\n\t"
                "ds_read_b64 v[224:225], %[kb] offset:224\n\t"
                "ds_read_b64 v[226:227], %[kb] offset:232\n\t"
                "ds_read_b128 v[228:231], %[ub] offset:4800\n\t"
                "s_waitcnt lgkmcnt(11)\n\t"
                "v_mfma_f32_32x32x16_bf16 %[a0], v[192:195], v[200:203], %[a0]\n\t"
                "ds_read_b128 v[232:235], %[ub] offset:2240\n\t"
                "s_waitcnt lgkmcnt(10)\n\t"
                "v_mfma_f32_32x32x16_bf16 %[a1], v[204:207], v[200:203], %[a1]\n\t"
                "ds_read_b64 v[236:237], %[kb] offset:2272\n\t"
                "ds_read_b64 v[238:239], %[kb] offset:2280\n\t"
                "s_waitcnt lgkmcnt(9)\n\t"
                "v_mfma_f32_32x32x16_bf16 %[a1], v[208:211], v[212:215], %[a1]\n\t"
                "ds_read_b64 v[240:241], %[kb] offset:192\n\t"
                "ds_read_b64 v[242:243], %[kb] offset:200\n\t"
                "ds_read_b128 v[244:247], %[ub] offset:4832\n\t"
                "s_waitcnt lgkmcnt(11)\n\t"
                "v_mfma_f32_32x32x16_bf16 %[a0], v[208:211], v[216:219], %[a0]\n\t"
                "ds_read_b128 v[248:251], %[ub] offset:2272\n\t"
                "s_waitcnt lgkmcnt(10)\n\t"
                "v_mfma_f32_32x32x16_bf16 %[a1], v[220:223], v[216:219], %[a1]\n\t"
                "ds_read_b64 v[252:253], %[kb] offset:2240\n\t"
                "ds_read_b64 v[254:255], %[kb] offset:2248\n\t"
                "s_waitcnt lgkmcnt(9)\n\t"
                "v_mfma_f32_32x32x16_bf16 %[a1], v[224:227], v[228:231], %[a1]\n\t"
                "ds_read_b64 v[192:193], %[kb] offset:288\n\t"
                "ds_read_b64 v[194:195], %[kb] offset:296\n\t"
                "ds_read_b128 v[196:199], %[ub] offset:4720\n\t"
                "s_waitcnt lgkmcnt(11)\n\t"
                "v_mfma_f32_32x32x16_bf16 %[a0], v[224:227], v[232:235], %[a0]\n\t"
                "ds_read_b128 v[200:203], %[ub] offset:2160\n\t"
                "s_waitcnt lgkmcnt(10)\n\t"
                "v_mfma_f32_32x32x16_bf16 %[a1], v[236:239], v[232:235], %[a1]\n\t"
                "ds_read_b64 v[204:205], %[kb] offset:2336\n\t"
                "ds_read_b64 v[206:207], %[kb] offset:2344\n\t"
                "s_waitcnt lgkmcnt(9)\n\t"
                "v_mfma_f32_32x32x16_bf16 %[a1], v[240:243], v[244:247], %[a1]\n\t"
                "ds_read_b64 v[208:209], %[kb] offset:256\n\t"
                "ds_read_b64 v[210:211], %[kb] offset:264\n\t"
                "ds_read_b128 v[212:215], %[ub] offset:4752\n\t"
                "s_waitcnt lgkmcnt(11)\n\t"
                "v_mfma_f32_32x32x16_bf16 %[a0], v[240:243], v[248:251], %[a0]\n\t"
                "ds_read_b128 v[216:219], %[ub] offset:2192\n\t"
                "s_waitcnt lgkmcnt(10)\n\t"
                "v_mfma_f32_32x32x16_bf16 %[a1], v[252:255], v[248:251], %[a1]\n\t"
                "ds_read_b64 v[220:221], %[kb] offset:2304\n\t"
                "ds_read_b64 v[222:223], %[kb] offset:2312\n\t"
                "s_waitcnt lgkmcnt(9)\n\t"
                "v_mfma_f32_32x32x16_bf16 %[a1], v[192:195], v[196:199], %[a1]\n\t"
                "ds_read_b64 v[224:225], %[kb] offset:352\n\t"
                "ds_read_b64 v[226:227], %[kb] offset:360\n\t"
                "ds_read_b128 v[228:231], %[ub] offset:4640\n\t"
                "s_waitcnt lgkmcnt(11)\n\t"
                "v_mfma_f32_32x32x16_bf16 %[a0], v[192:195], v[200:203], %[a0]\n\t"
                "ds_read_b128 v[232:235], %[ub] offset:2080\n\t"
                "s_waitcnt lgkmcnt(10)\n\t"
                "v_mfma_f32_32x32x16_bf16 %[a1], v[204:207], v[200:203], %[a1]\n\t"
                "ds_read_b64 v[236:237], %[kb] offset:2400\n\t"
                "ds_read_b64 v[238:239], %[kb] offset:2408\n\t"
                "s_waitcnt lgkmcnt(9)\n\t"
                "v_mfma_f32_32x32x16_bf16 %[a1], v[208:211], v[212:215], %[a1]\n\t"
                "ds_read_b64 v[240:241], %[kb] offset:320\n\t"
                "ds_read_b64 v[242:243], %[kb] offset:328\n\t"
                "ds_read_b128 v[244:247], %[ub] offset:4672\n\t"
                "s_waitcnt lgkmcnt(11)\n\t"
                "v_mfma_f32_32x32x16_bf16 %[a0], v[208:211], v[216:219], %[a0]\n\t"
                "ds_read_b128 v[248:251], %[ub] offset:2112\n\t"
                "s_waitcnt lgkmcnt(10)\n\t"
                "v_mfma_f32_32x32x16_bf16 %[a1], v[220:223], v[216:219], %[a1]\n\t"
                "ds_read_b64 v[252:253], %[kb] offset:2368\n\t"
                "ds_read_b64 v[254:255], %[kb] offset:2376\n\t"
                "s_waitcnt lgkmcnt(9)\n\t"
                "v_mfma_f32_32x32x16_bf16 %[a1], v[224:227], v[228:231], %[a1]\n\t"
                "ds_read_b64 v[192:193], %[kb] offset:416\n\t"
                "ds_read_b64 v[194:195], %[kb] offset:424\n\t"
                "ds_read_b128 v[196:199], %[ub] offset:4560\n\t"
                "s_waitcnt lgkmcnt(11)\n\t"
                "v_mfma_f32_32x32x16_bf16 %[a0], v[224:227], v[232:235], %[a0]\n\t"
                "ds_read_b128 v[200:203], %[ub] offset:2000\n\t"
                "s_waitcnt lgkmcnt(10)\n\t"
                "v_mfma_f32_32x32x16_bf16 %[a1], v[236:239], v[232:235], %[a1]\n\t"
                "ds_read_b64 v[204:205], %[kb] offset:2464\n\t"
                "ds_read_b64 v[206:207], %[kb] offset:2472\n\t"
                "s_waitcnt lgkmcnt(9)\n\t"
                "v_mfma_f32_32x32x16_bf16 %[a1], v[240:243], v[244:247], %[a1]\n\t"
                "ds_read_b64 v[208:209], %[kb] offset:384\n\t"
                "ds_read_b64 v[210:211], %[kb] offset:392\n\t"
                "ds_read_b128 v[212:215], %[ub] offset:4592\n\t"
                "s_waitcnt lgkmcnt(11)\n\t"
                "v_mfma_f32_32x32x16_bf16 %[a0], v[240:243], v[248:251], %[a0]\n\t"
                "ds_read_b128 v[216:219], %[ub] offset:2032\n\t"
                "s_waitcnt lgkmcnt(10)\n\t"
                "v_mfma_f32_32x32x16_bf16 %[a1], v[252:255], v[248:251], %[a1]\n\t"
                "ds_read_b64 v[220:221], %[kb] offset:2432\n\t"
                "ds_read_b64 v[222:223], %[kb] offset:2440\n\t"
                "s_waitcnt lgkmcnt(9)\n\t"
                "v_mfma_f32_32x32x16_bf16 %[a1], v[192:195], v[196:199], %[a1]\n\t"
                "ds_read_b64 v[224:225], %[kb] offset:480\n\t"
                "ds_read_b64 v[226:227], %[kb] offset:488\n\t"
                "ds_read_b128 v[228:231], %[ub] offset:4480\n\t"
                "s_waitcnt lgkmcnt(11)\n\t"
                "v_mfma_f32_32x32x16_bf16 %[a0], v[192:195], v[200:203], %[a0]\n\t"
                "ds_read_b128 v[232:235], %[ub] offset:1920\n\t"
                "s_waitcnt lgkmcnt(10)\n\t"
                "v_mfma_f32_32x32x16_bf16 %[a1], v[204:207], v[200:203], %[a1]\n\t"
                "ds_read_b64 v[236:237], %[kb] offset:2528\n\t"
                "ds_read_b64 v[238:239], %[kb] offset:2536\n\t"
                "s_waitcnt lgkmcnt(9)\n\t"
                "v_mfma_f32_32x32x16_bf16 %[a1], v[208:211], v[212:215], %[a1]\n\t"
                "ds_read_b64 v[240:241], %[kb] offset:448\n\t"
                "ds_read_b64 v[242:243], %[kb] offset:456\n\t"
                "ds_read_b128 v[244:247], %[ub] offset:4512\n\t"
                "s_waitcnt lgkmcnt(11)\n\t"
                "v_mfma_f32_32x32x16_bf16 %[a0], v[208:211], v[216:219], %[a0]\n\t"
                "ds_read_b128 v[248:251], %[ub] offset:1952\n\t"
                "s_waitcnt lgkmcnt(10)\n\t"
                "v_mfma_f32_32x32x16_bf16 %[a1], v[220:223], v[216:219], %[a1]\n\t"
                "ds_read_b64 v[252:253], %[kb] offset:2496\n\t"
                "ds_read_b64 v[254:255], %[kb] offset:2504\n\t"
                "s_waitcnt lgkmcnt(9)\n\t"
                "v_mfma_f32_32x32x16_bf16 %[a1], v[224:227], v[228:231], %[a1]\n\t"
                "ds_read_b64 v[192:193], %[kb] offset:544\n\t"
                "ds_read_b64 v[194:195], %[kb] offset:552\n\t"
                "ds_read_b128 v[196:199], %[ub] offset:4400\n\t"
                "s_waitcnt lgkmcnt(11)\n\t"
                "v_mfma_f32_32x32x16_bf16 %[a0], v[224:227], v[232:235], %[a0]\n\t"
                "ds_read_b128 v[200:203], %[ub] offset:1840\n\t"
                "s_waitcnt lgkmcnt(10)\n\t"
                "v_mfma_f32_32x32x16_bf16 %[a1], v[236:239], v[232:235], %[a1]\n\t"
                "ds_read_b64 v[204:205], %[kb] offset:2592\n\t"
                "ds_read_b64 v[206:207], %[kb] offset:2600\n\t"
                "s_waitcnt lgkmcnt(9)\n\t"
                "v_mfma_f32_32x32x16_bf16 %[a1], v[240:243], v[244:247], %[a1]\n\t"
                "ds_read_b64 v[208:209], %[kb] offset:512\n\t"
                "ds_read_b64 v[210:211], %[kb] offset:520\n\t"
                "ds_read_b128 v[212:215], %[ub] offset:4432\n\t"
                "s_waitcnt lgkmcnt(11)\n\t"
                "v_mfma_f32_32x32x16_bf16 %[a0], v[240:243], v[248:251], %[a0]\n\t"
                "ds_read_b128 v[216:219], %[ub] offset:1872\n\t"
                "s_waitcnt lgkmcnt(10)\n\t"
                "v_mfma_f32_32x32x16_bf16 %[a1], v[252:255], v[248:251], %[a1]\n\t"
                "ds_read_b64 v[220:221], %[kb] offset:2560\n\t"
                "ds_read_b64 v[222:223], %[kb] offset:2568\n\t"
                "s_waitcnt lgkmcnt(9)\n\t"
                "v_mfma_f32_32x32x16_bf16 %[a1], v[192:195], v[196:199], %[a1]\n\t"
                "ds_read_b64 v[224:225], %[kb] offset:608\n\t"
                "ds_read_b64 v[226:227], %[kb] offset:616\n\t"
                "ds_read_b128 v[228:231], %[ub] offset:4320\n\t"
                "s_waitcnt lgkmcnt(11)\n\t"
                "v_mfma_f32_32x32x16_bf16 %[a0], v[192:195], v[200:203], %[a0]\n\t"
                "ds_read_b128 v[232:235], %[ub] offset:1760\n\t"
                "s_waitcnt lgkmcnt(10)\n\t"
                "v_mfma_f32_32x32x16_bf16 %[a1], v[204:207], v[200:203], %[a1]\n\t"
                "ds_read_b64 v[236:237], %[kb] offset:2656\n\t"
                "ds_read_b64 v[238:239], %[kb] offset:2664\n\t"
                "s_waitcnt lgkmcnt(9)\n\t"
                "v_mfma_f32_32x32x16_bf16 %[a1], v[208:211], v[212:215], %[a1]\n\t"
                "ds_read_b64 v[240:241], %[kb] offset:576\n\t"
                "ds_read_b64 v[242:243], %[kb] offset:584\n\t"
                "ds_read_b128 v[244:247], %[ub] offset:4352\n\t"
                "s_waitcnt lgkmcnt(11)\n\t"
                "v_mfma_f32_32x32x16_bf16 %[a0], v[208:211], v[216:219], %[a0]\n\t"
                "ds_read_b128 v[248:251], %[ub] offset:1792\n\t"
                "s_waitcnt lgkmcnt(10)\n\t"
                "v_mfma_f32_32x32x16_bf16 %[a1], v[220:223], v[216:219], %[a1]\n\t"
                "ds_read_b64 v[252:253], %[kb] offset:2624\n\t"
                "ds_read_b64 v[254:255], %[kb] offset:2632\n\t"
                "s_waitcnt lgkmcnt(9)\n\t"
                "v_mfma_f32_32x32x16_bf16 %[a1], v[224:227], v[228:231], %[a1]\n\t"
                "ds_read_b64 v[192:193], %[kb] offset:672\n\t"
                "ds_read_b64 v[194:195], %[kb] offset:680\n\t"
                "ds_read_b128 v[196:199], %[ub] offset:4240\n\t"
                "s_waitcnt lgkmcnt(11)\n\t"
                "v_mfma_f32_32x32x16_bf16 %[a0], v[224:227], v[232:235], %[a0]\n\t"
                "ds_read_b128 v[200:203], %[ub] offset:1680\n\t"
                "s_waitcnt lgkmcnt(10)\n\t"
                "v_mfma_f32_32x32x16_bf16 %[a1], v[236:239], v[232:235], %[a1]\n\t"
                "ds_read_b64 v[204:205], %[kb] offset:2720\n\t"
                "ds_read_b64 v[206:207], %[kb] offset:2728\n\t"
                "s_waitcnt lgkmcnt(9)\n\t"
                "v_mfma_f32_32x32x16_bf16 %[a1], v[240:243], v[244:247], %[a1]\n\t"
                "ds_read_b64 v[208:209], %[kb] offset:640\n\t"
                "ds_read_b64 v[210:211], %[kb] offset:648\n\t"
                "ds_read_b128 v[212:215], %[ub] offset:4272\n\t"
                "s_waitcnt lgkmcnt(11)\n\t"
                "v_mfma_f32_32x32x16_bf16 %[a0], v[240:243], v[248:251], %[a0]\n\t"
                "ds_read_b128 v[216:219], %[ub] offset:1712\n\t"
                "s_waitcnt lgkmcnt(10)\n\t"
                "v_mfma_f32_32x32x16_bf16 %[a1], v[252:255], v[248:251], %[a1]\n\t"
                "ds_read_b64 v[220:221], %[kb] offset:2688\n\t"
                "ds_read_b64 v[222:223], %[kb] offset:2696\n\t"
                "s_waitcnt lgkmcnt(9)\n\t"
                "v_mfma_f32_32x32x16_bf16 %[a1], v[192:195], v[196:199], %[a1]\n\t"
                "ds_read_b64 v[224:225], %[kb] offset:736\n\t"
                "ds_read_b64 v[226:227], %[kb] offset:744\n\t"
                "ds_read_b128 v[228:231], %[ub] offset:4160\n\t"
                "s_waitcnt lgkmcnt(11)\n\t"
                "v_mfma_f32_32x32x16_bf16 %[a0], v[192:195], v[200:203], %[a0]\n\t"
                "ds_read_b128 v[232:235], %[ub] offset:1600\n\t"
                "s_waitcnt lgkmcnt(10)\n\t"
                "v_mfma_f32_32x32x16_bf16 %[a1], v[204:207], v[200:203], %[a1]\n\t"
                "ds_read_b64 v[236:237], %[kb] offset:2784\n\t"
                "ds_read_b64 v[238:239], %[kb] offset:2792\n\t"
                "s_waitcnt lgkmcnt(9)\n\t"
                "v_mfma_f32_32x32x16_bf16 %[a1], v[208:211], v[212:215], %[a1]\n\t"
                "ds_read_b64 v[240:241], %[kb] offset:704\n\t"
                "ds_read_b64 v[242:243], %[kb] offset:712\n\t"
                "ds_read_b128 v[244:247], %[ub] offset:4192\n\t"
                "s_waitcnt lgkmcnt(11)\n\t"
                "v_mfma_f32_32x32x16_bf16 %[a0], v[208:211], v[216:219], %[a0]\n\t"
                "ds_read_b128 v[248:251], %[ub] offset:1632\n\t"
                "s_waitcnt lgkmcnt(10)\n\t"
                "v_mfma_f32_32x32x16_bf16 %[a1], v[220:223], v[216:219], %[a1]\n\t"
                "ds_read_b64 v[252:253], %[kb] offset:2752\n\t"
                "ds_read_b64 v[254:255], %[kb] offset:2760\n\t"
                "s_waitcnt lgkmcnt(9)\n\t"
                "v_mfma_f32_32x32x16_bf16 %[a1], v[224:227], v[228:231], %[a1]\n\t"
                "ds_read_b64 v[192:193], %[kb] offset:800\n\t"
                "ds_read_b64 v[194:195], %[kb] offset:808\n\t"
                "ds_read_b128 v[196:199], %[ub] offset:4080\n\t"
                "s_waitcnt lgkmcnt(11)\n\t"
                "v_mfma_f32_32x32x16_bf16 %[a0], v[224:227], v[232:235], %[a0]\n\t"
                "ds_read_b128 v[200:203], %[ub] offset:1520\n\t"
                "s_waitcnt lgkmcnt(10)\n\t"
                "v_mfma_f32_32x32x16_bf16 %[a1], v[236:239], v[232:235], %[a1]\n\t"
                "ds_read_b64 v[204:205], %[kb] offset:2848\n\t"
                "ds_read_b64 v[206:207], %[kb] offset:2856\n\t"
                "s_waitcnt lgkmcnt(9)\n\t"
                "v_mfma_f32_32x32x16_bf16 %[a1], v[240:243], v[244:247], %[a1]\n\t"
                "ds_read_b64 v[208:209], %[kb] offset:768\n\t"
                "ds_read_b64 v[210:211], %[kb] offset:776\n\t"
                "ds_read_b128 v[212:215], %[ub] offset:4112\n\t"
                "s_waitcnt lgkmcnt(11)\n\t"
                "v_mfma_f32_32x32x16_bf16 %[a0], v[240:243], v[248:251], %[a0]\n\t"
                "ds_read_b128 v[216:219], %[ub] offset:1552\n\t"
                "s_waitcnt lgkmcnt(10)\n\t"
                "v_mfma_f32_32x32x16_bf16 %[a1], v[252:255], v[248:251], %[a1]\n\t"
                "ds_read_b64 v[220:221], %[kb] offset:2816\n\t"
                "ds_read_b64 v[222:223], %[kb] offset:2824\n\t"
                "s_waitcnt lgkmcnt(9)\n\t"
                "v_mfma_f32_32x32x16_bf16 %[a1], v[192:195], v[196:199], %[a1]\n\t"
                "ds_read_b64 v[224:225], %[kb] offset:864\n\t"
                "ds_read_b64 v[226:227], %[kb] offset:872\n\t"
                "ds_read_b128 v[228:231], %[ub] offset:4000\n\t"
                "s_waitcnt lgkmcnt(11)\n\t"
                "v_mfma_f32_32x32x16_bf16 %[a0], v[192:195], v[200:203], %[a0]\n\t"
                "ds_read_b128 v[232:235], %[ub] offset:1440\n\t"
                "s_waitcnt lgkmcnt(10)\n\t"
                "v_mfma_f32_32x32x16_bf16 %[a1], v[204:207], v[200:203], %[a1]\n\t"
                "ds_read_b64 v[236:237], %[kb] offset:2912\n\t"
                "ds_read_b64 v[238:239], %[kb] offset:2920\n\t"
                "s_waitcnt lgkmcnt(9)\n\t"
                "v_mfma_f32_32x32x16_bf16 %[a1], v[208:211], v[212:215], %[a1]\n\t"
                "ds_read_b64 v[240:241], %[kb] offset:832\n\t"
                "ds_read_b64 v[242:243], %[kb] offset:840\n\t"
                "ds_read_b128 v[244:247], %[ub] offset:4032\n\t"
                "s_waitcnt lgkmcnt(11)\n\t"
                "v_mfma_f32_32x32x16_bf16 %[a0], v[208:211], v[216:219], %[a0]\n\t"
                "ds_read_b128 v[248:251], %[ub] offset:1472\n\t"
                "s_waitcnt lgkmcnt(10)\n\t"
                "v_mfma_f32_32x32x16_bf16 %[a1], v[220:223], v[216:219], %[a1]\n\t"
                "ds_read_b64 v[252:253], %[kb] offset:2880\n\t"
                "ds_read_b64 v[254:255], %[kb] offset:2888\n\t"
                "s_waitcnt lgkmcnt(9)\n\t"
                "v_mfma_f32_32x32x16_bf16 %[a1], v[224:227], v[228:231], %[a1]\n\t"
                "ds_read_b64 v[192:193], %[kb] offset:928\n\t"
                "ds_read_b64 v[194:195], %[kb] offset:936\n\t"
                "ds_read_b128 v[196:199], %[ub] offset:3920\n\t"
                "s_waitcnt lgkmcnt(11)\n\t"
                "v_mfma_f32_32x32x16_bf16 %[a0], v[224:227], v[232:235], %[a0]\n\t"
                "ds_read_b128 v[200:203], %[ub] offset:1360\n\t"
                "s_waitcnt lgkmcnt(10)\n\t"
                "v_mfma_f32_32x32x16_bf16 %[a1], v[236:239], v[232:235], %[a1]\n\t"
                "ds_read_b64 v[204:205], %[kb] offset:2976\n\t"
                "ds_read_b64 v[206:207], %[kb] offset:2984\n\t"
                "s_waitcnt lgkmcnt(9)\n\t"
                "v_mfma_f32_32x32x16_bf16 %[a1], v[240:243], v[244:247], %[a1]\n\t"
                "ds_read_b64 v[208:209], %[kb] offset:896\n\t"
                "ds_read_b64 v[210:211], %[kb] offset:904\n\t"
                "ds_read_b128 v[212:215], %[ub] offset:3952\n\t"
                "s_waitcnt lgkmcnt(11)\n\t"
                "v_mfma_f32_32x32x16_bf16 %[a0], v[240:243], v[248:251], %[a0]\n\t"
                "ds_read_b128 v[216:219], %[ub] offset:1392\n\t"
                "s_waitcnt lgkmcnt(10)\n\t"
                "v_mfma_f32_32x32x16_bf16 %[a1], v[252:255], v[248:251], %[a1]\n\t"
                "ds_read_b64 v[220:221], %[kb] offset:2944\n\t"
                "ds_read_b64 v[222:223], %[kb] offset:2952\n\t"
                "s_waitcnt lgkmcnt(9)\n\t"
                "v_mfma_f32_32x32x16_bf16 %[a1], v[192:195], v[196:199], %[a1]\n\t"
                "ds_read_b64 v[224:225], %[kb] offset:992\n\t"
                "ds_read_b64 v[226:227], %[kb] offset:1000\n\t"
                "ds_read_b128 v[228:231], %[ub] offset:3840\n\t"
                "s_waitcnt lgkmcnt(11)\n\t"
                "v_mfma_f32_32x32x16_bf16 %[a0], v[192:195], v[200:203], %[a0]\n\t"
                "ds_read_b128 v[232:235], %[ub] offset:1280\n\t"
                "s_waitcnt lgkmcnt(10)\n\t"
                "v_mfma_f32_32x32x16_bf16 %[a1], v[204:207], v[200:203], %[a1]\n\t"
                "ds_read_b64 v[236:237], %[kb] offset:3040\n\t"
                "ds_read_b64 v[238:239], %[kb] offset:3048\n\t"
                "s_waitcnt lgkmcnt(9)\n\t"
                "v_mfma_f32_32x32x16_bf16 %[a1], v[208:211], v[212:215], %[a1]\n\t"
                "ds_read_b64 v[240:241], %[kb] offset:960\n\t"
                "ds_read_b64 v[242:243], %[kb] offset:968\n\t"
                "ds_read_b128 v[244:247], %[ub] offset:3872\n\t"
                "s_waitcnt lgkmcnt(11)\n\t"
                "v_mfma_f32_32x32x16_bf16 %[a0], v[208:211], v[216:219], %[a0]\n\t"
                "ds_read_b128 v[248:251], %[ub] offset:1312\n\t"
                "s_waitcnt lgkmcnt(10)\n\t"
                "v_mfma_f32_32x32x16_bf16 %[a1], v[220:223], v[216:219], %[a1]\n\t"
                "ds_read_b64 v[252:253], %[kb] offset:3008\n\t"
                "ds_read_b64 v[254:255], %[kb] offset:3016\n\t"
                "s_waitcnt lgkmcnt(9)\n\t"
                "v_mfma_f32_32x32x16_bf16 %[a1], v[224:227], v[228:231], %[a1]\n\t"
                "ds_read_b64 v[192:193], %[kb] offset:1056\n\t"
                "ds_read_b64 v[194:195], %[kb] offset:1064\n\t"
                "ds_read_b128 v[196:199], %[ub] offset:3760\n\t"
                "s_waitcnt lgkmcnt(11)\n\t"
                "v_mfma_f32_32x32x16_bf16 %[a0], v[224:227], v[232:235], %[a0]\n\t"
                "ds_read_b128 v[200:203], %[ub] offset:1200\n\t"
                "s_waitcnt lgkmcnt(10)\n\t"
                "v_mfma_f32_32x32x16_bf16 %[a1], v[236:239], v[232:235], %[a1]\n\t"
                "ds_read_b64 v[204:205], %[kb] offset:3104\n\t"
                "ds_read_b64 v[206:207], %[kb] offset:3112\n\t"
                "s_waitcnt lgkmcnt(9)\n\t"
                "v_mfma_f32_32x32x16_bf16 %[a1], v[240:243], v[244:247], %[a1]\n\t"
                "ds_read_b64 v[208:209], %[kb] offset:1024\n\t"
                "ds_read_b64 v[210:211], %[kb] offset:1032\n\t"
                "ds_read_b128 v[212:215], %[ub] offset:3792\n\t"
                "s_waitcnt lgkmcnt(11)\n\t"
                "v_mfma_f32_32x32x16_bf16 %[a0], v[240:243], v[248:251], %[a0]\n\t"
                "ds_read_b128 v[216:219], %[ub] offset:1232\n\t"
                "s_waitcnt lgkmcnt(10)\n\t"
                "v_mfma_f32_32x32x16_bf16 %[a1], v[252:255], v[248:251], %[a1]\n\t"
                "ds_read_b64 v[220:221], %[kb] offset:3072\n\t"
                "ds_read_b64 v[222:223], %[kb] offset:3080\n\t"
                "s_waitcnt lgkmcnt(9)\n\t"
                "v_mfma_f32_32x32x16_bf16 %[a1], v[192:195], v[196:199], %[a1]\n\t"
                "ds_read_b64 v[224:225], %[kb] offset:1120\n\t"
                "ds_read_b64 v[226:227], %[kb] offset:1128\n\t"
                "ds_read_b128 v[228:231], %[ub] offset:3680\n\t"
                "s_waitcnt lgkmcnt(11)\n\t"
                "v_mfma_f32_32x32x16_bf16 %[a0], v[192:195], v[200:203], %[a0]\n\t"
                "ds_read_b128 v[232:235], %[ub] offset:1120\n\t"
                "s_waitcnt lgkmcnt(10)\n\t"
                "v_mfma_f32_32x32x16_bf16 %[a1], v[204:207], v[200:203], %[a1]\n\t"
                "ds_read_b64 v[236:237], %[kb] offset:3168\n\t"
                "ds_read_b64 v[238:239], %[kb] offset:3176\n\t"
                "s_waitcnt lgkmcnt(9)\n\t"
                "v_mfma_f32_32x32x16_bf16 %[a1], v[208:211], v[212:215], %[a1]\n\t"
                "ds_read_b64 v[240:241], %[kb] offset:1088\n\t"
                "ds_read_b64 v[242:243], %[kb] offset:1096\n\t"
                "ds_read_b128 v[244:247], %[ub] offset:3712\n\t"
                "s_waitcnt lgkmcnt(11)\n\t"
                "v_mfma_f32_32x32x16_bf16 %[a0], v[208:211], v[216:219], %[a0]\n\t"
                "ds_read_b128 v[248:251], %[ub] offset:1152\n\t"
                "s_waitcnt lgkmcnt(10)\n\t"
                "v_mfma_f32_32x32x16_bf16 %[a1], v[220:223], v[216:219], %[a1]\n\t"
                "ds_read_b64 v[252:253], %[kb] offset:3136\n\t"
                "ds_read_b64 v[254:255], %[kb] offset:3144\n\t"
                "s_waitcnt lgkmcnt(9)\n\t"
                "v_mfma_f32_32x32x16_bf16 %[a1], v[224:227], v[228:231], %[a1]\n\t"
                "ds_read_b64 v[192:193], %[kb] offset:1184\n\t"
                "ds_read_b64 v[194:195], %[kb] offset:1192\n\t"
                "ds_read_b128 v[196:199], %[ub] offset:3600\n\t"
                "s_waitcnt lgkmcnt(11)\n\t"
                "v_mfma_f32_32x32x16_bf16 %[a0], v[224:227], v[232:235], %[a0]\n\t"
                "ds_read_b128 v[200:203], %[ub] offset:1040\n\t"
                "s_waitcnt lgkmcnt(10)\n\t"
                "v_mfma_f32_32x32x16_bf16 %[a1], v[236:239], v[232:235], %[a1]\n\t"
                "ds_read_b64 v[204:205], %[kb] offset:3232\n\t"
                "ds_read_b64 v[206:207], %[kb] offset:3240\n\t"
                "s_waitcnt lgkmcnt(9)\n\t"
                "v_mfma_f32_32x32x16_bf16 %[a1], v[240:243], v[244:247], %[a1]\n\t"
                "ds_read_b64 v[208:209], %[kb] offset:1152\n\t"
                "ds_read_b64 v[210:211], %[kb] offset:1160\n\t"
                "ds_read_b128 v[212:215], %[ub] offset:3632\n\t"
                "s_waitcnt lgkmcnt(11)\n\t"
                "v_mfma_f32_32x32x16_bf16 %[a0], v[240:243], v[248:251], %[a0]\n\t"
                "ds_read_b128 v[216:219], %[ub] offset:1072\n\t"
                "s_waitcnt lgkmcnt(10)\n\t"
                "v_mfma_f32_32x32x16_bf16 %[a1], v[252:255], v[248:251], %[a1]\n\t"
                "ds_read_b64 v[220:221], %[kb] offset:3200\n\t"
                "ds_read_b64 v[222:223], %[kb] offset:3208\n\t"
                "s_waitcnt lgkmcnt(9)\n\t"
                "v_mfma_f32_32x32x16_bf16 %[a1], v[192:195], v[196:199], %[a1]\n\t"
                "ds_read_b64 v[224:225], %[kb] offset:1248\n\t"
                "ds_read_b64 v[226:227], %[kb] offset:1256\n\t"
                "ds_read_b128 v[228:231], %[ub] offset:3520\n\t"
                "s_waitcnt lgkmcnt(11)\n\t"
                "v_mfma_f32_32x32x16_bf16 %[a0], v[192:195], v[200:203], %[a0]\n\t"
                "ds_read_b128 v[232:235], %[ub] offset:960\n\t"
                "s_waitcnt lgkmcnt(10)\n\t"
                "v_mfma_f32_32x32x16_bf16 %[a1], v[204:207], v[200:203], %[a1]\n\t"
                "ds_read_b64 v[236:237], %[kb] offset:3296\n\t"
                "ds_read_b64 v[238:239], %[kb] offset:3304\n\t"
                "s_waitcnt lgkmcnt(9)\n\t"
                "v_mfma_f32_32x32x16_bf16 %[a1], v[208:211], v[212:215], %[a1]\n\t"
                "ds_read_b64 v[240:241], %[kb] offset:1216\n\t"
                "ds_read_b64 v[242:243], %[kb] offset:1224\n\t"
                "ds_read_b128 v[244:247], %[ub] offset:3552\n\t"
                "s_waitcnt lgkmcnt(11)\n\t"
                "v_mfma_f32_32x32x16_bf16 %[a0], v[208:211], v[216:219], %[a0]\n\t"
                "ds_read_b128 v[248:251], %[ub] offset:992\n\t"
                "s_waitcnt lgkmcnt(10)\n\t"
                "v_mfma_f32_32x32x16_bf16 %[a1], v[220:223], v[216:219], %[a1]\n\t"
                "ds_read_b64 v[252:253], %[kb] offset:3264\n\t"
                "ds_read_b64 v[254:255], %[kb] offset:3272\n\t"
                "s_waitcnt lgkmcnt(9)\n\t"
                "v_mfma_f32_32x32x16_bf16 %[a1], v[224:227], v[228:231], %[a1]\n\t"
                "ds_read_b64 v[192:193], %[kb] offset:1312\n\t"
                "ds_read_b64 v[194:195], %[kb] offset:1320\n\t"
                "ds_read_b128 v[196:199], %[ub] offset:3440\n\t"
                "s_waitcnt lgkmcnt(11)\n\t"
                "v_mfma_f32_32x32x16_bf16 %[a0], v[224:227], v[232:235], %[a0]\n\t"
                "ds_read_b128 v[200:203], %[ub] offset:880\n\t"
                "s_waitcnt lgkmcnt(10)\n\t"
                "v_mfma_f32_32x32x16_bf16 %[a1], v[236:239], v[232:235], %[a1]\n\t"
                "ds_read_b64 v[204:205], %[kb] offset:3360\n\t"
                "ds_read_b64 v[206:207], %[kb] offset:3368\n\t"
                "s_waitcnt lgkmcnt(9)\n\t"
                "v_mfma_f32_32x32x16_bf16 %[a1], v[240:243], v[244:247], %[a1]\n\t"
                "ds_read_b64 v[208:209], %[kb] offset:1280\n\t"
                "ds_read_b64 v[210:211], %[kb] offset:1288\n\t"
                "ds_read_b128 v[212:215], %[ub] offset:3472\n\t"
                "s_waitcnt lgkmcnt(11)\n\t"
                "v_mfma_f32_32x32x16_bf16 %[a0], v[240:243], v[248:251], %[a0]\n\t"
                "ds_read_b128 v[216:219], %[ub] offset:912\n\t"
                "s_waitcnt lgkmcnt(10)\n\t"
                "v_mfma_f32_32x32x16_bf16 %[a1], v[252:255], v[248:251], %[a1]\n\t"
                "ds_read_b64 v[220:221], %[kb] offset:3328\n\t"
                "ds_read_b64 v[222:223], %[kb] offset:3336\n\t"
                "s_waitcnt lgkmcnt(9)\n\t"
                "v_mfma_f32_32x32x16_bf16 %[a1], v[192:195], v[196:199], %[a1]\n\t"
                "ds_read_b64 v[224:225], %[kb] offset:1376\n\t"
                "ds_read_b64 v[226:227], %[kb] offset:1384\n\t"
                "ds_read_b128 v[228:231], %[ub] offset:3360\n\t"
                "s_waitcnt lgkmcnt(11)\n\t"
                "v_mfma_f32_32x32x16_bf16 %[a0], v[192:195], v[200:203], %[a0]\n\t"
                "ds_read_b128 v[232:235], %[ub] offset:800\n\t"
                "s_waitcnt lgkmcnt(10)\n\t"
                "v_mfma_f32_32x32x16_bf16 %[a1], v[204:207], v[200:203], %[a1]\n\t"
                "ds_read_b64 v[236:237], %[kb] offset:3424\n\t"
                "ds_read_b64 v[238:239], %[kb] offset:3432\n\t"
                "s_waitcnt lgkmcnt(9)\n\t"
                "v_mfma_f32_32x32x16_bf16 %[a1], v[208:211], v[212:215], %[a1]\n\t"
                "ds_read_b64 v[240:241], %[kb] offset:1344\n\t"
                "ds_read_b64 v[242:243], %[kb] offset:1352\n\t"
                "ds_read_b128 v[244:247], %[ub] offset:3392\n\t"
                "s_waitcnt lgkmcnt(11)\n\t"
                "v_mfma_f32_32x32x16_bf16 %[a0], v[208:211], v[216:219], %[a0]\n\t"
                "ds_read_b128 v[248:251], %[ub] offset:832\n\t"
                "s_waitcnt lgkmcnt(10)\n\t"
                "v_mfma_f32_32x32x16_bf16 %[a1], v[220:223], v[216:219], %[a1]\n\t"
                "ds_read_b64 v[252:253], %[kb] offset:3392\n\t"
                "ds_read_b64 v[254:255], %[kb] offset:3400\n\t"
                "s_waitcnt lgkmcnt(9)\n\t"
                "v_mfma_f32_32x32x16_bf16 %[a1], v[224:227], v[228:231], %[a1]\n\t"
                "ds_read_b64 v[192:193], %[kb] offset:1440\n\t"
                "ds_read_b64 v[194:195], %[kb] offset:1448\n\t"
                "ds_read_b128 v[196:199], %[ub] offset:3280\n\t"
                "s_waitcnt lgkmcnt(11)\n\t"
                "v_mfma_f32_32x32x16_bf16 %[a0], v[224:227], v[232:235], %[a0]\n\t"
                "ds_read_b128 v[200:203], %[ub] offset:720\n\t"
                "s_waitcnt lgkmcnt(10)\n\t"
                "v_mfma_f32_32x32x16_bf16 %[a1], v[236:239], v[232:235], %[a1]\n\t"
                "ds_read_b64 v[204:205], %[kb] offset:3488\n\t"
                "ds_read_b64 v[206:207], %[kb] offset:3496\n\t"
                "s_waitcnt lgkmcnt(9)\n\t"
                "v_mfma_f32_32x32x16_bf16 %[a1], v[240:243], v[244:247], %[a1]\n\t"
                "ds_read_b64 v[208:209], %[kb] offset:1408\n\t"
                "ds_read_b64 v[210:211], %[kb] offset:1416\n\t"
                "ds_read_b128 v[212:215], %[ub] offset:3312\n\t"
                "s_waitcnt lgkmcnt(11)\n\t"
                "v_mfma_f32_32x32x16_bf16 %[a0], v[240:243], v[248:251], %[a0]\n\t"
                "ds_read_b128 v[216:219], %[ub] offset:752\n\t"
                "s_waitcnt lgkmcnt(10)\n\t"
                "v_mfma_f32_32x32x16_bf16 %[a1], v[252:255], v[248:251], %[a1]\n\t"
                "ds_read_b64 v[220:221], %[kb] offset:3456\n\t"
                "ds_read_b64 v[222:223], %[kb] offset:3464\n\t"
                "s_waitcnt lgkmcnt(9)\n\t"
                "v_mfma_f32_32x32x16_bf16 %[a1], v[192:195], v[196:199], %[a1]\n\t"
                "ds_read_b64 v[224:225], %[kb] offset:1504\n\t"
                "ds_read_b64 v[226:227], %[kb] offset:1512\n\t"
                "ds_read_b128 v[228:231], %[ub] offset:3200\n\t"
                "s_waitcnt lgkmcnt(11)\n\t"
                "v_mfma_f32_32x32x16_bf16 %[a0], v[192:195], v[200:203], %[a0]\n\t"
                "ds_read_b128 v[232:235], %[ub] offset:640\n\t"
                "s_waitcnt lgkmcnt(10)\n\t"
                "v_mfma_f32_32x32x16_bf16 %[a1], v[204:207], v[200:203], %[a1]\n\t"
                "ds_read_b64 v[236:237], %[kb] offset:3552\n\t"
                "ds_read_b64 v[238:239], %[kb] offset:3560\n\t"
                "s_waitcnt lgkmcnt(9)\n\t"
                "v_mfma_f32_32x32x16_bf16 %[a1], v[208:211], v[212:215], %[a1]\n\t"
                "ds_read_b64 v[240:241], %[kb] offset:1472\n\t"
                "ds_read_b64 v[242:243], %[kb] offset:1480\n\t"
                "ds_read_b128 v[244:247], %[ub] offset:3232\n\t"
                "s_waitcnt lgkmcnt(11)\n\t"
                "v_mfma_f32_32x32x16_bf16 %[a0], v[208:211], v[216:219], %[a0]\n\t"
                "ds_read_b128 v[248:251], %[ub] offset:672\n\t"
                "s_waitcnt lgkmcnt(10)\n\t"
                "v_mfma_f32_32x32x16_bf16 %[a1], v[220:223], v[216:219], %[a1]\n\t"
                "ds_read_b64 v[252:253], %[kb] offset:3520\n\t"
                "ds_read_b64 v[254:255], %[kb] offset:3528\n\t"
                "s_waitcnt lgkmcnt(9)\n\t"
                "v_mfma_f32_32x32x16_bf16 %[a1], v[224:227], v[228:231], %[a1]\n\t"
                "ds_read_b64 v[192:193], %[kb] offset:1568\n\t"
                "ds_read_b64 v[194:195], %[kb] offset:1576\n\t"
                "ds_read_b128 v[196:199], %[ub] offset:3120\n\t"
                "s_waitcnt lgkmcnt(11)\n\t"
                "v_mfma_f32_32x32x16_bf16 %[a0], v[224:227], v[232:235], %[a0]\n\t"
                "ds_read_b128 v[200:203], %[ub] offset:560\n\t"
                "s_waitcnt lgkmcnt(10)\n\t"
                "v_mfma_f32_32x32x16_bf16 %[a1], v[236:239], v[232:235], %[a1]\n\t"
                "ds_read_b64 v[204:205], %[kb] offset:3616\n\t"
                "ds_read_b64 v[206:207], %[kb] offset:3624\n\t"
                "s_waitcnt lgkmcnt(9)\n\t"
                "v_mfma_f32_32x32x16_bf16 %[a1], v[240:243], v[244:247], %[a1]\n\t"
                "ds_read_b64 v[208:209], %[kb] offset:1536\n\t"
                "ds_read_b64 v[210:211], %[kb] offset:1544\n\t"
                "ds_read_b128 v[212:215], %[ub] offset:3152\n\t"
                "s_waitcnt lgkmcnt(11)\n\t"
                "v_mfma_f32_32x32x16_bf16 %[a0], v[240:243], v[248:251], %[a0]\n\t"
                "ds_read_b128 v[216:219], %[ub] offset:592\n\t"
                "s_waitcnt lgkmcnt(10)\n\t"
                "v_mfma_f32_32x32x16_bf16 %[a1], v[252:255], v[248:251], %[a1]\n\t"
                "ds_read_b64 v[220:221], %[kb] offset:3584\n\t"
                "ds_read_b64 v[222:223], %[kb] offset:3592\n\t"
                "s_waitcnt lgkmcnt(9)\n\t"
                "v_mfma_f32_32x32x16_bf16 %[a1], v[192:195], v[196:199], %[a1]\n\t"
                "ds_read_b64 v[224:225], %[kb] offset:1632\n\t"
                "ds_read_b64 v[226:227], %[kb] offset:1640\n\t"
                "ds_read_b128 v[228:231], %[ub] offset:3040\n\t"
                "s_waitcnt lgkmcnt(11)\n\t"
                "v_mfma_f32_32x32x16_bf16 %[a0], v[192:195], v[200:203], %[a0]\n\t"
                "ds_read_b128 v[232:235], %[ub] offset:480\n\t"
                "s_waitcnt lgkmcnt(10)\n\t"
                "v_mfma_f32_32x32x16_bf16 %[a1], v[204:207], v[200:203], %[a1]\n\t"
                "ds_read_b64 v[236:237], %[kb] offset:3680\n\t"
                "ds_read_b64 v[238:239], %[kb] offset:3688\n\t"
                "s_waitcnt lgkmcnt(9)\n\t"
                "v_mfma_f32_32x32x16_bf16 %[a1], v[208:211], v[212:215], %[a1]\n\t"
                "ds_read_b64 v[240:241], %[kb] offset:1600\n\t"
                "ds_read_b64 v[242:243], %[kb] offset:1608\n\t"
                "ds_read_b128 v[244:247], %[ub] offset:3072\n\t"
                "s_waitcnt lgkmcnt(11)\n\t"
                "v_mfma_f32_32x32x16_bf16 %[a0], v[208:211], v[216:219], %[a0]\n\t"
                "ds_read_b128 v[248:251], %[ub] offset:512\n\t"
                "s_waitcnt lgkmcnt(10)\n\t"
                "v_mfma_f32_32x32x16_bf16 %[a1], v[220:223], v[216:219], %[a1]\n\t"
                "ds_read_b64 v[252:253], %[kb] offset:3648\n\t"
                "ds_read_b64 v[254:255], %[kb] offset:3656\n\t"
                "s_waitcnt lgkmcnt(9)\n\t"
                "v_mfma_f32_32x32x16_bf16 %[a1], v[224:227], v[228:231], %[a1]\n\t"
                "ds_read_b64 v[192:193], %[kb] offset:1696\n\t"
                "ds_read_b64 v[194:195], %[kb] offset:1704\n\t"
                "ds_read_b128 v[196:199], %[ub] offset:2960\n\t"
                "s_waitcnt lgkmcnt(11)\n\t"
                "v_mfma_f32_32x32x16_bf16 %[a0], v[224:227], v[232:235], %[a0]\n\t"
                "ds_read_b128 v[200:203], %[ub] offset:400\n\t"
                "s_waitcnt lgkmcnt(10)\n\t"
                "v_mfma_f32_32x32x16_bf16 %[a1], v[236:239], v[232:235], %[a1]\n\t"
                "ds_read_b64 v[204:205], %[kb] offset:3744\n\t"
                "ds_read_b64 v[206:207], %[kb] offset:3752\n\t"
                "s_waitcnt lgkmcnt(9)\n\t"
                "v_mfma_f32_32x32x16_bf16 %[a1], v[240:243], v[244:247], %[a1]\n\t"
                "ds_read_b64 v[208:209], %[kb] offset:1664\n\t"
                "ds_read_b64 v[210:211], %[kb] offset:1672\n\t"
                "ds_read_b128 v[212:215], %[ub] offset:2992\n\t"
                "s_waitcnt lgkmcnt(11)\n\t"
                "v_mfma_f32_32x32x16_bf16 %[a0], v[240:243], v[248:251], %[a0]\n\t"
                "ds_read_b128 v[216:219], %[ub] offset:432\n\t"
                "s_waitcnt lgkmcnt(10)\n\t"
                "v_mfma_f32_32x32x16_bf16 %[a1], v[252:255], v[248:251], %[a1]\n\t"
                "ds_read_b64 v[220:221], %[kb] offset:3712\n\t"
                "ds_read_b64 v[222:223], %[kb] offset:3720\n\t"
                "s_waitcnt lgkmcnt(9)\n\t"
                "v_mfma_f32_32x32x16_bf16 %[a1], v[192:195], v[196:199], %[a1]\n\t"
                "ds_read_b64 v[224:225], %[kb] offset:1760\n\t"
                "ds_read_b64 v[226:227], %[kb] offset:1768\n\t"
                "ds_read_b128 v[228:231], %[ub] offset:2880\n\t"
                "s_waitcnt lgkmcnt(11)\n\t"
                "v_mfma_f32_32x32x16_bf16 %[a0], v[192:195], v[200:203], %[a0]\n\t"
                "ds_read_b128 v[232:235], %[ub] offset:320\n\t"
                "s_waitcnt lgkmcnt(10)\n\t"
                "v_mfma_f32_32x32x16_bf16 %[a1], v[204:207], v[200:203], %[a1]\n\t"
                "ds_read_b64 v[236:237], %[kb] offset:3808\n\t"
                "ds_read_b64 v[238:239], %[kb] offset:3816\n\t"
                "s_waitcnt lgkmcnt(9)\n\t"
                "v_mfma_f32_32x32x16_bf16 %[a1], v[208:211], v[212:215], %[a1]\n\t"
                "ds_read_b64 v[240:241], %[kb] offset:1728\n\t"
                "ds_read_b64 v[242:243], %[kb] offset:1736\n\t"
                "ds_read_b128 v[244:247], %[ub] offset:2912\n\t"
                "s_waitcnt lgkmcnt(11)\n\t"
                "v_mfma_f32_32x32x16_bf16 %[a0], v[208:211], v[216:219], %[a0]\n\t"
                "ds_read_b128 v[248:251], %[ub] offset:352\n\t"
                "s_waitcnt lgkmcnt(10)\n\t"
                "v_mfma_f32_32x32x16_bf16 %[a1], v[220:223], v[216:219], %[a1]\n\t"
                "ds_read_b64 v[252:253], %[kb] offset:3776\n\t"
                "ds_read_b64 v[254:255], %[kb] offset:3784\n\t"
                "s_waitcnt lgkmcnt(9)\n\t"
                "v_mfma_f32_32x32x16_bf16 %[a1], v[224:227], v[228:231], %[a1]\n\t"
                "ds_read_b64 v[192:193], %[kb] offset:1824\n\t"
                "ds_read_b64 v[194:195], %[kb] offset:1832\n\t"
                "ds_read_b128 v[196:199], %[ub] offset:2800\n\t"
                "s_waitcnt lgkmcnt(11)\n\t"
                "v_mfma_f32_32x32x16_bf16 %[a0], v[224:227], v[232:235], %[a0]\n\t"
                "ds_read_b128 v[200:203], %[ub] offset:240\n\t"
                "s_waitcnt lgkmcnt(10)\n\t"
                "v_mfma_f32_32x32x16_bf16 %[a1], v[236:239], v[232:235], %[a1]\n\t"
                "ds_read_b64 v[204:205], %[kb] offset:3872\n\t"
                "ds_read_b64 v[206:207], %[kb] offset:3880\n\t"
                "s_waitcnt lgkmcnt(9)\n\t"
                "v_mfma_f32_32x32x16_bf16 %[a1], v[240:243], v[244:247], %[a1]\n\t"
                "ds_read_b64 v[208:209], %[kb] offset:1792\n\t"
                "ds_read_b64 v[210:211], %[kb] offset:1800\n\t"
                "ds_read_b128 v[212:215], %[ub] offset:2832\n\t"
                "s_waitcnt lgkmcnt(11)\n\t"
                "v_mfma_f32_32x32x16_bf16 %[a0], v[240:243], v[248:251], %[a0]\n\t"
                "ds_read_b128 v[216:219], %[ub] offset:272\n\t"
                "s_waitcnt lgkmcnt(10)\n\t"
                "v_mfma_f32_32x32x16_bf16 %[a1], v[252:255], v[248:251], %[a1]\n\t"
                "ds_read_b64 v[220:221], %[kb] offset:3840\n\t"
                "ds_read_b64 v[222:223], %[kb] offset:3848\n\t"
                "s_waitcnt lgkmcnt(9)\n\t"
                "v_mfma_f32_32x32x16_bf16 %[a1], v[192:195], v[196:199], %[a1]\n\t"
                "ds_read_b64 v[224:225], %[kb] offset:1888\n\t"
                "ds_read_b64 v[226:227], %[kb] offset:1896\n\t"
                "ds_read_b128 v[228:231], %[ub] offset:2720\n\t"
                "s_waitcnt lgkmcnt(11)\n\t"
                "v_mfma_f32_32x32x16_bf16 %[a0], v[192:195], v[200:203], %[a0]\n\t"
                "ds_read_b128 v[232:235], %[ub] offset:160\n\t"
                "s_waitcnt lgkmcnt(10)\n\t"
                "v_mfma_f32_32x32x16_bf16 %[a1], v[204:207], v[200:203], %[a1]\n\t"
                "ds_read_b64 v[236:237], %[kb] offset:3936\n\t"
                "ds_read_b64 v[238:239], %[kb] offset:3944\n\t"
                "s_waitcnt lgkmcnt(9)\n\t"
                "v_mfma_f32_32x32x16_bf16 %[a1], v[208:211], v[212:215], %[a1]\n\t"
                "ds_read_b64 v[240:241], %[kb] offset:1856\n\t"
                "ds_read_b64 v[242:243], %[kb] offset:1864\n\t"
                "ds_read_b128 v[244:247], %[ub] offset:2752\n\t"
                "s_waitcnt lgkmcnt(11)\n\t"
                "v_mfma_f32_32x32x16_bf16 %[a0], v[208:211], v[216:219], %[a0]\n\t"
                "ds_read_b128 v[248:251], %[ub] offset:192\n\t"
                "s_waitcnt lgkmcnt(10)\n\t"
                "v_mfma_f32_32x32x16_bf16 %[a1], v[220:223], v[216:219], %[a1]\n\t"
                "ds_read_b64 v[252:253], %[kb] offset:3904\n\t"
                "ds_read_b64 v[254:255], %[kb] offset:3912\n\t"
                "s_waitcnt lgkmcnt(9)\n\t"
                "v_mfma_f32_32x32x16_bf16 %[a1], v[224:227], v[228:231], %[a1]\n\t"
                "ds_read_b64 v[192:193], %[kb] offset:1952\n\t"
                "ds_read_b64 v[194:195], %[kb] offset:1960\n\t"
                "ds_read_b128 v[196:199], %[ub] offset:2640\n\t"
                "s_waitcnt lgkmcnt(11)\n\t"
                "v_mfma_f32_32x32x16_bf16 %[a0], v[224:227], v[232:235], %[a0]\n\t"
                "ds_read_b128 v[200:203], %[ub] offset:80\n\t"
                "s_waitcnt lgkmcnt(10)\n\t"
                "v_mfma_f32_32x32x16_bf16 %[a1], v[236:239], v[232:235], %[a1]\n\t"
                "ds_read_b64 v[204:205], %[kb] offset:4000\n\t"
                "ds_read_b64 v[206:207], %[kb] offset:4008\n\t"
                "s_waitcnt lgkmcnt(9)\n\t"
                "v_mfma_f32_32x32x16_bf16 %[a1], v[240:243], v[244:247], %[a1]\n\t"
                "ds_read_b64 v[208:209], %[kb] offset:1920\n\t"
                "ds_read_b64 v[210:211], %[kb] offset:1928\n\t"
                "ds_read_b128 v[212:215], %[ub] offset:2672\n\t"
                "s_waitcnt lgkmcnt(11)\n\t"
                "v_mfma_f32_32x32x16_bf16 %[a0], v[240:243], v[248:251], %[a0]\n\t"
                "ds_read_b128 v[216:219], %[ub] offset:112\n\t"
                "s_waitcnt lgkmcnt(10)\n\t"
                "v_mfma_f32_32x32x16_bf16 %[a1], v[252:255], v[248:251], %[a1]\n\t"
                "ds_read_b64 v[220:221], %[kb] offset:3968\n\t"
                "ds_read_b64 v[222:223], %[kb] offset:3976\n\t"
                "s_waitcnt lgkmcnt(9)\n\t"
                "v_mfma_f32_32x32x16_bf16 %[a1], v[192:195], v[196:199], %[a1]\n\t"
                "ds_read_b64 v[224:225], %[kb] offset:2016\n\t"
                "ds_read_b64 v[226:227], %[kb] offset:2024\n\t"
                "ds_read_b128 v[228:231], %[ub] offset:2560\n\t"
                "s_waitcnt lgkmcnt(11)\n\t"
                "v_mfma_f32_32x32x16_bf16 %[a0], v[192:195], v[200:203], %[a0]\n\t"
                "ds_read_b128 v[232:235], %[ub] offset:0\n\t"
                "s_waitcnt lgkmcnt(10)\n\t"
                "v_mfma_f32_32x32x16_bf16 %[a1], v[204:207], v[200:203], %[a1]\n\t"
                "ds_read_b64 v[236:237], %[kb] offset:4064\n\t"
                "ds_read_b64 v[238:239], %[kb] offset:4072\n\t"
                "s_waitcnt lgkmcnt(9)\n\t"
                "v_mfma_f32_32x32x16_bf16 %[a1], v[208:211], v[212:215], %[a1]\n\t"
                "ds_read_b64 v[240:241], %[kb] offset:1984\n\t"
                "ds_read_b64 v[242:243], %[kb] offset:1992\n\t"
                "ds_read_b128 v[244:247], %[ub] offset:2592\n\t"
                "s_waitcnt lgkmcnt(11)\n\t"
                "v_mfma_f32_32x32x16_bf16 %[a0], v[208:211], v[216:219], %[a0]\n\t"
                "ds_read_b128 v[248:251], %[ub] offset:32\n\t"
                "s_waitcnt lgkmcnt(10)\n\t"
                "v_mfma_f32_32x32x16_bf16 %[a1], v[220:223], v[216:219], %[a1]\n\t"
                "ds_read_b64 v[252:253], %[kb] offset:4032\n\t"
                "ds_read_b64 v[254:255], %[kb] offset:4040\n\t"
                "s_waitcnt lgkmcnt(9)\n\t"
                "v_mfma_f32_32x32x16_bf16 %[a1], v[224:227], v[228:231], %[a1]\n\t"
                "s_waitcnt lgkmcnt(8)\n\t"
                "v_mfma_f32_32x32x16_bf16 %[a0], v[224:227], v[232:235], %[a0]\n\t"
                "s_waitcnt lgkmcnt(6)\n\t"
                "v_mfma_f32_32x32x16_bf16 %[a1], v[236:239], v[232:235], %[a1]\n\t"
                "s_waitcnt lgkmcnt(3)\n\t"
                "v_mfma_f32_32x32x16_bf16 %[a1], v[240:243], v[244:247], %[a1]\n\t"
                "s_waitcnt lgkmcnt(2)\n\t"
                "v_mfma_f32_32x32x16_bf16 %[a0], v[240:243], v[248:251], %[a0]\n\t"
                "s_waitcnt lgkmcnt(0)\n\t"
                "v_mfma_f32_32x32x16_bf16 %[a1], v[252:255], v[248:251], %[a1]\n\t"
                "s_nop 7\n\t"
                "s_nop 3\n\t"
                : [a0] "=&v"(acc0), [a1] "=&v"(acc1) : [kb] "v"(kb), [ub] "v"(ub0 + c8 * CV_U) : "memory", "v192", "v193", "v194", "v195", "v196", "v197", "v198", "v199", "v200", "v201", "v202", "v203", "v204", "v205", "v206", "v207", "v208", "v209", "v210", "v211", "v212", "v213", "v214", "v215", "v216", "v217", "v218", "v219", "v220", "v221", "v222", "v223", "v224", "v225", "v226", "v227", "v228", "v229", "v230", "v231", "v232", "v233", "v234", "v235", "v236", "v237", "v238", "v239", "v240", "v241", "v242", "v243", "v244", "v245", "v246", "v247", "v248", "v249", "v250", "v251", "v252", "v253", "v254", "v255");
            if (rd == 1) BSTAMP(ci == 0 ? 12 : 14);
            unsigned char* uc = lds + c8 * CV_U + (32 + b) * CV_RP + kg * 8;
#pragma unroll
            for (int q = 0; q < 4; ++q) {
                *(v2u*)(uc + q * 16) = (v2u){pack2(acc0[4 * q], acc0[4 * q + 1]), pack2(acc0[4 * q + 2], acc0[4 * q + 3])};
                *(v2u*)(uc + 32 * CV_RP + q * 16) = (v2u){pack2(acc1[4 * q], acc1[4 * q + 1]), pack2(acc1[4 * q + 2], acc1[4 * q + 3])}; }
            __builtin_amdgcn_wave_barrier();
        }
        __syncthreads();
        const float d0 = blo(dv[rd]), d1 = bhi(dv[rd]);
#pragma unroll
        for (int G = 0; G < 4; ++G) { const v4u lo = *(const v4u*)(urow + G * 16), hh = *(const v4u*)(urow + CV_U + G * 16);
#pragma unroll
            for (int q = 0; q < 8; ++q) { const unsigned x = u[8 * G + q][rd], ud = pack2(blo(x) * d0, bhi(x) * d1);
                u[8 * G + q][rd] = pack2(((q & 1) ? bhi(lo[q >> 1]) : blo(lo[q >> 1])) + blo(ud), ((q & 1) ? bhi(hh[q >> 1]) : blo(hh[q >> 1])) + bhi(ud)); } }
        if (rd == 0) BSTAMP(9);
        if (rd == 1) BSTAMP(15);
    }
    BSTAMP(11);
#pragma unroll
    for (int hf = 0; hf < 2; ++hf) { v4u qv[16];
#pragma unroll
        for (int i = 0; i < 16; ++i) qv[i] = *(const v4u*)(p.Aq + (size_t)(Q * 32 + hf * 16 + i) * D + ch0);
#pragma unroll
        for (int i = 0; i < 16; ++i) { v4u o;
#pragma unroll
            for (int q = 0; q < 4; ++q) o[q] = pack2(blo(qv[i][q]) * blo(u[hf * 16 + i][q]), bhi(qv[i][q]) * bhi(u[hf * 16 + i][q]));
            st16(r, W_A + hi * 131072 + (hf * 16 + i) * 4096 + tid * 16, o); } }
    DRAIN(); __syncthreads();
    if (tid == 0) __hip_atomic_fetch_add((unsigned*)(p.ws + F_CONV), 1u, __ATOMIC_RELAXED, __HIP_MEMORY_SCOPE_AGENT);
}

__device__ __forceinline__ void proj_block(const Params& p, unsigned char* lds, const int wv, const int h, const int c, const int j) {
    const int w = wv, l = otid() & 63, n = l & 15, g = l >> 4, tm = w >> 1, tn = w & 1;
    const unsigned lb = __builtin_amdgcn_readfirstlane((unsigned)(size_t)lds);
    const v4u sx = mk_srd(p.Aq, S * D * 2), sq = mk_srd(c < 2 ? p.Wq : p.Wk, 1024 * 4096 * 2), sv = mk_srd(p.Wv, 1024 * 4096 * 2), sw = mk_srd(p.ws, W_TOTAL);
    const unsigned lx = lb + (32 * tm + n) * 128 + ((g ^ (n & 7)) << 4), lr = lb + 131072, rr = 8 * g + (n >> 2);
    const int cv = ((c & 1) ^ 1) * 2 + (c >> 1);
    const unsigned aq = (c < 2 ? W_QX : W_KX) + (tm * 32 + h) * 8192 + (2 * (c & 1) + tn) * 1024 + l * 16;
    const int hq = h & 7, tk = ((hq - (hq > 1) - (hq > 5)) * 4 + c) * 4 + w, ts6 = tk % 6, tch = 15 - tk / 6, tx = tk - 6 * GTOUCH;
    const bf16_t* tp = (h & 3) == 1 ? p.gate_w : tx < 0 ? (ts6 % 3 == 0 ? p.Wq : ts6 % 3 == 1 ? p.Wk : p.Wv) + (size_t)(tch * 64 + l) * 4096 + ((h & 24) + 1 + (ts6 / 3) * 4) * 128 + 64
                     : tx < XTOUCH ? p.Aq + (tx * 64 + l) * 64 : p.gate_w;
    unsigned t0, xo, so, cn; unsigned long long ts, ta, tb, tc, td;
    asm volatile(
        "s_mov_b32 %[xo], 0\n\t"
        "s_mov_b32 %[so], 0\n\t"
        "s_mov_b32 %[cn], 1\n\t"
        "global_load_dword v88, v[114:115], off\n\t"
        "s_add_u32 m0, %[ld], 0\n\t"
        "s_add_u32 %[t0], %[xo], 0\n\t"
        "buffer_load_dwordx4 v100, %[sx], %[t0] offen lds\n\t"
        "s_add_u32 m0, %[ld], 1024\n\t"
        "s_add_u32 %[t0], %[xo], 16384\n\t"
        "buffer_load_dwordx4 v100, %[sx], %[t0] offen lds\n\t"
        "s_add_u32 m0, %[ld], 8192\n\t"
        "s_add_u32 %[t0], %[xo], 128\n\t"
        "buffer_load_dwordx4 v100, %[sx], %[t0] offen lds\n\t"
        "s_add_u32 m0, %[ld], 9216\n\t"
        "s_add_u32 %[t0], %[xo], 16512\n\t"
        "buffer_load_dwordx4 v100, %[sx], %[t0] offen lds\n\t"
        "s_add_u32 m0, %[ld], 16384\n\t"
        "s_add_u32 %[t0], %[xo], 256\n\t"
        "buffer_load_dwordx4 v100, %[sx], %[t0] offen lds\n\t"
        "s_add_u32 m0, %[ld], 17408\n\t"
        "s_add_u32 %[t0], %[xo], 16640\n\t"
        "buffer_load_dwordx4 v100, %[sx], %[t0] offen lds\n\t"
        "s_add_u32 m0, %[ld], 24576\n\t"
        "s_add_u32 %[t0], %[xo], 384\n\t"
        "buffer_load_dwordx4 v100, %[sx], %[t0] offen lds\n\t"
        "s_add_u32 m0, %[ld], 25600\n\t"
        "s_add_u32 %[t0], %[xo], 16768\n\t"
        "buffer_load_dwordx4 v100, %[sx], %[t0] offen lds\n\t"
        "s_mov_b32 %[t0], 0\n\t"
        "buffer_load_dwordx4 a[192:195], v101, %[sq], %[t0] offen\n\t"
        "buffer_load_dwordx4 a[200:203], v112, %[sv], %[t0] offen\n\t"
        "s_mov_b32 %[t0], 262144\n\t"
        "buffer_load_dwordx4 a[196:199], v101, %[sq], %[t0] offen\n\t"
        "s_mov_b32 %[t0], 524288\n\t"
        "buffer_load_dwordx4 a[204:207], v101, %[sq], %[t0] offen\n\t"
        "buffer_load_dwordx4 a[212:215], v112, %[sv], %[t0] offen\n\t"
        "s_mov_b32 %[t0], 786432\n\t"
        "buffer_load_dwordx4 a[208:211], v101, %[sq], %[t0] offen\n\t"
        "s_mov_b32 %[t0], 1048576\n\t"
        "buffer_load_dwordx4 a[216:219], v101, %[sq], %[t0] offen\n\t"
        "buffer_load_dwordx4 a[224:227], v112, %[sv], %[t0] offen\n\t"
        "s_mov_b32 %[t0], 1310720\n\t"
        "buffer_load_dwordx4 a[220:223], v101, %[sq], %[t0] offen\n\t"
        "s_mov_b32 %[t0], 1572864\n\t"
        "buffer_load_dwordx4 a[228:231], v101, %[sq], %[t0] offen\n\t"
        "buffer_load_dwordx4 a[236:239], v112, %[sv], %[t0] offen\n\t"
        "s_mov_b32 %[t0], 1835008\n\t"
        "buffer_load_dwordx4 a[232:235], v101, %[sq], %[t0] offen\n\t"
        "s_waitcnt vmcnt(9)\n\t"
        "ds_write_b128 v102, a[192:195] offset:0\n\t"
        "ds_write_b128 v102, a[196:199] offset:4096\n\t"
        "ds_write_b128 v116, a[200:203] offset:8192\n\t"
        "s_waitcnt lgkmcnt(0)\n\t"
        "s_barrier\n\t"
        "ds_read_b64_tr_b16 a[0:1], v103 offset:0\n\t"
        "ds_read_b64_tr_b16 a[2:3], v103 offset:512\n\t"
        "ds_read_b64_tr_b16 a[4:5], v103 offset:8\n\t"
        "ds_read_b64_tr_b16 a[6:7], v103 offset:520\n\t"
        "ds_read_b64_tr_b16 a[8:9], v104 offset:0\n\t"
        "ds_read_b64_tr_b16 a[10:11], v104 offset:256\n\t"
        "s_waitcnt lgkmcnt(5)\n\t"
        "ds_read_b64_tr_b16 a[12:13], v103 offset:4096\n\t"
        "ds_read_b64_tr_b16 a[14:15], v103 offset:4608\n\t"
        "ds_read_b64_tr_b16 a[16:17], v103 offset:4104\n\t"
        "ds_read_b64_tr_b16 a[18:19], v103 offset:4616\n\t"
        "ds_read_b64_tr_b16 a[20:21], v104 offset:2048\n\t"
        "ds_read_b64_tr_b16 a[22:23], v104 offset:2304\n\t"
        "ds_read_b128 v[56:59], v105 offset:0\n\t"
        "ds_read_b128 v[60:63], v105 offset:2048\n\t"
        "ds_read_b128 v[64:67], v106 offset:0\n\t"
        "ds_read_b128 v[68:71], v106 offset:2048\n\t"
        "s_add_u32 m0, %[ld], 32768\n\t"
        "s_add_u32 %[t0], %[xo], 512\n\t"
        "buffer_load_dwordx4 v100, %[sx], %[t0] offen lds\n\t"
        "s_add_u32 m0, %[ld], 33792\n\t"
        "s_add_u32 %[t0], %[xo], 16896\n\t"
        "buffer_load_dwordx4 v100, %[sx], %[t0] offen lds\n\t"
        "s_add_u32 m0, %[ld], 40960\n\t"
        "s_add_u32 %[t0], %[xo], 640\n\t"
        "buffer_load_dwordx4 v100, %[sx], %[t0] offen lds\n\t"
        "s_add_u32 m0, %[ld], 41984\n\t"
        "s_add_u32 %[t0], %[xo], 17024\n\t"
        "buffer_load_dwordx4 v100, %[sx], %[t0] offen lds\n\t"
        "s_add_u32 m0, %[ld], 49152\n\t"
        "s_add_u32 %[t0], %[xo], 768\n\t"
        "buffer_load_dwordx4 v100, %[sx], %[t0] offen lds\n\t"
        "s_add_u32 m0, %[ld], 50176\n\t"
        "s_add_u32 %[t0], %[xo], 17152\n\t"
        "buffer_load_dwordx4 v100, %[sx], %[t0] offen lds\n\t"
        "s_add_u32 m0, %[ld], 57344\n\t"
        "s_add_u32 %[t0], %[xo], 896\n\t"
        "buffer_load_dwordx4 v100, %[sx], %[t0] offen lds\n\t"
        "s_add_u32 m0, %[ld], 58368\n\t"
        "s_add_u32 %[t0], %[xo], 17280\n\t"
        "buffer_load_dwordx4 v100, %[sx], %[t0] offen lds\n\t"
        "s_mov_b32 %[t0], 2097152\n\t"
        "buffer_load_dwordx4 a[240:243], v101, %[sq], %[t0] offen\n\t"
        "buffer_load_dwordx4 a[248:251], v112, %[sv], %[t0] offen\n\t"
        "s_mov_b32 %[t0], 2359296\n\t"
        "buffer_load_dwordx4 a[244:247], v101, %[sq], %[t0] offen\n\t"
        "s_waitcnt vmcnt(17)\n\t"
        "ds_write_b128 v102, a[204:207] offset:16384\n\t"
        "ds_write_b128 v102, a[208:211] offset:20480\n\t"
        "ds_write_b128 v116, a[212:215] offset:24576\n\t"
        "s_waitcnt lgkmcnt(0)\n\t"
        "s_barrier\n\t"
        "v_mfma_f32_16x16x32_bf16 v[8:11], a[0:3], v[56:59], 0\n\t"
        "ds_read_b64_tr_b16 a[24:25], v103 offset:16384\n\t"
        "ds_read_b64_tr_b16 a[26:27], v103 offset:16896\n\t"
        "v_mfma_f32_16x16x32_bf16 v[12:15], a[4:7], v[56:59], 0\n\t"
        "ds_read_b64_tr_b16 a[28:29], v103 offset:16392\n\t"
        "ds_read_b64_tr_b16 a[30:31], v103 offset:16904\n\t"
        "v_mfma_f32_16x16x32_bf16 v[16:19], v[56:59], a[8:11], 0\n\t"
        "ds_read_b64_tr_b16 a[32:33], v104 offset:16384\n\t"
        "ds_read_b64_tr_b16 a[34:35], v104 offset:16640\n\t"
        "v_mfma_f32_16x16x32_bf16 v[20:23], a[0:3], v[60:63], 0\n\t"
        "s_waitcnt lgkmcnt(5)\n\t"
        "ds_read_b64_tr_b16 a[36:37], v103 offset:20480\n\t"
        "v_mfma_f32_16x16x32_bf16 v[24:27], a[4:7], v[60:63], 0\n\t"
        "ds_read_b64_tr_b16 a[38:39], v103 offset:20992\n\t"
        "ds_read_b64_tr_b16 a[40:41], v103 offset:20488\n\t"
        "v_mfma_f32_16x16x32_bf16 v[28:31], v[60:63], a[8:11], 0\n\t"
        "ds_read_b64_tr_b16 a[42:43], v103 offset:21000\n\t"
        "ds_read_b64_tr_b16 a[44:45], v104 offset:18432\n\t"
        "v_mfma_f32_16x16x32_bf16 v[8:11], a[12:15], v[64:67], v[8:11]\n\t"
        "ds_read_b64_tr_b16 a[46:47], v104 offset:18688\n\t"
        "ds_read_b128 v[72:75], v105 offset:8192\n\t"
        "v_mfma_f32_16x16x32_bf16 v[12:15], a[16:19], v[64:67], v[12:15]\n\t"
        "ds_read_b128 v[76:79], v105 offset:10240\n\t"
        "ds_read_b128 v[80:83], v106 offset:8192\n\t"
        "v_mfma_f32_16x16x32_bf16 v[16:19], v[64:67], a[20:23], v[16:19]\n\t"
        "ds_read_b128 v[84:87], v106 offset:10240\n\t"
        "v_mfma_f32_16x16x32_bf16 v[20:23], a[12:15], v[68:71], v[20:23]\n\t"
        "v_mfma_f32_16x16x32_bf16 v[24:27], a[16:19], v[68:71], v[24:27]\n\t"
        "v_mfma_f32_16x16x32_bf16 v[28:31], v[68:71], a[20:23], v[28:31]\n\t"
        "s_mov_b32 %[t0], 2621440\n\t"
        "buffer_load_dwordx4 a[252:255], v101, %[sq], %[t0] offen\n\t"
        "buffer_load_dwordx4 v[132:135], v112, %[sv], %[t0] offen\n\t"
        "s_mov_b32 %[t0], 2883584\n\t"
        "buffer_load_dwordx4 v[128:131], v101, %[sq], %[t0] offen\n\t"
        "s_waitcnt vmcnt(17)\n\t"
        "ds_write_b128 v102, a[216:219] offset:0\n\t"
        "ds_write_b128 v102, a[220:223] offset:4096\n\t"
        "ds_write_b128 v116, a[224:227] offset:8192\n\t"
        "s_waitcnt lgkmcnt(0)\n\t"
        "s_barrier\n\t"
        "v_mfma_f32_16x16x32_bf16 v[8:11], a[24:27], v[72:75], v[8:11]\n\t"
        "ds_read_b64_tr_b16 a[48:49], v103 offset:0\n\t"
        "ds_read_b64_tr_b16 a[50:51], v103 offset:512\n\t"
        "v_mfma_f32_16x16x32_bf16 v[12:15], a[28:31], v[72:75], v[12:15]\n\t"
        "ds_read_b64_tr_b16 a[52:53], v103 offset:8\n\t"
        "ds_read_b64_tr_b16 a[54:55], v103 offset:520\n\t"
        "v_mfma_f32_16x16x32_bf16 v[16:19], v[72:75], a[32:35], v[16:19]\n\t"
        "ds_read_b64_tr_b16 a[56:57], v104 offset:0\n\t"
        "ds_read_b64_tr_b16 a[58:59], v104 offset:256\n\t"
        "v_mfma_f32_16x16x32_bf16 v[20:23], a[24:27], v[76:79], v[20:23]\n\t"
        "s_waitcnt lgkmcnt(5)\n\t"
        "ds_read_b64_tr_b16 a[60:61], v103 offset:4096\n\t"
        "v_mfma_f32_16x16x32_bf16 v[24:27], a[28:31], v[76:79], v[24:27]\n\t"
        "ds_read_b64_tr_b16 a[62:63], v103 offset:4608\n\t"
        "ds_read_b64_tr_b16 a[64:65], v103 offset:4104\n\t"
        "v_mfma_f32_16x16x32_bf16 v[28:31], v[76:79], a[32:35], v[28:31]\n\t"
        "ds_read_b64_tr_b16 a[66:67], v103 offset:4616\n\t"
        "ds_read_b64_tr_b16 a[68:69], v104 offset:2048\n\t"
        "v_mfma_f32_16x16x32_bf16 v[8:11], a[36:39], v[80:83], v[8:11]\n\t"
        "ds_read_b64_tr_b16 a[70:71], v104 offset:2304\n\t"
        "ds_read_b128 v[56:59], v105 offset:16384\n\t"
        "v_mfma_f32_16x16x32_bf16 v[12:15], a[40:43], v[80:83], v[12:15]\n\t"
        "ds_read_b128 v[60:63], v105 offset:18432\n\t"
        "ds_read_b128 v[64:67], v106 offset:16384\n\t"
        "v_mfma_f32_16x16x32_bf16 v[16:19], v[80:83], a[44:47], v[16:19]\n\t"
        "ds_read_b128 v[68:71], v106 offset:18432\n\t"
        "v_mfma_f32_16x16x32_bf16 v[20:23], a[36:39], v[84:87], v[20:23]\n\t"
        "v_mfma_f32_16x16x32_bf16 v[24:27], a[40:43], v[84:87], v[24:27]\n\t"
        "v_mfma_f32_16x16x32_bf16 v[28:31], v[84:87], a[44:47], v[28:31]\n\t"
        "s_mov_b32 %[t0], 3145728\n\t"
        "buffer_load_dwordx4 v[136:139], v101, %[sq], %[t0] offen\n\t"
        "buffer_load_dwordx4 v[144:147], v112, %[sv], %[t0] offen\n\t"
        "s_mov_b32 %[t0], 3407872\n\t"
        "buffer_load_dwordx4 v[140:143], v101, %[sq], %[t0] offen\n\t"
        "s_waitcnt vmcnt(17)\n\t"
        "ds_write_b128 v102, a[228:231] offset:16384\n\t"
        "ds_write_b128 v102, a[232:235] offset:20480\n\t"
        "ds_write_b128 v116, a[236:239] offset:24576\n\t"
        "s_waitcnt lgkmcnt(0)\n\t"
        "s_barrier\n\t"
        "v_mfma_f32_16x16x32_bf16 v[8:11], a[48:51], v[56:59], v[8:11]\n\t"
        "ds_read_b64_tr_b16 a[72:73], v103 offset:16384\n\t"
        "ds_read_b64_tr_b16 a[74:75], v103 offset:16896\n\t"
        "v_mfma_f32_16x16x32_bf16 v[12:15], a[52:55], v[56:59], v[12:15]\n\t"
        "ds_read_b64_tr_b16 a[76:77], v103 offset:16392\n\t"
        "ds_read_b64_tr_b16 a[78:79], v103 offset:16904\n\t"
        "v_mfma_f32_16x16x32_bf16 v[16:19], v[56:59], a[56:59], v[16:19]\n\t"
        "ds_read_b64_tr_b16 a[80:81], v104 offset:16384\n\t"
        "ds_read_b64_tr_b16 a[82:83], v104 offset:16640\n\t"
        "v_mfma_f32_16x16x32_bf16 v[20:23], a[48:51], v[60:63], v[20:23]\n\t"
        "s_waitcnt lgkmcnt(5)\n\t"
        "ds_read_b64_tr_b16 a[84:85], v103 offset:20480\n\t"
        "v_mfma_f32_16x16x32_bf16 v[24:27], a[52:55], v[60:63], v[24:27]\n\t"
        "ds_read_b64_tr_b16 a[86:87], v103 offset:20992\n\t"
        "ds_read_b64_tr_b16 a[88:89], v103 offset:20488\n\t"
        "v_mfma_f32_16x16x32_bf16 v[28:31], v[60:63], a[56:59], v[28:31]\n\t"
        "ds_read_b64_tr_b16 a[90:91], v103 offset:21000\n\t"
        "ds_read_b64_tr_b16 a[92:93], v104 offset:18432\n\t"
        "v_mfma_f32_16x16x32_bf16 v[8:11], a[60:63], v[64:67], v[8:11]\n\t"
        "ds_read_b64_tr_b16 a[94:95], v104 offset:18688\n\t"
        "ds_read_b128 v[72:75], v105 offset:24576\n\t"
        "v_mfma_f32_16x16x32_bf16 v[12:15], a[64:67], v[64:67], v[12:15]\n\t"
        "ds_read_b128 v[76:79], v105 offset:26624\n\t"
        "ds_read_b128 v[80:83], v106 offset:24576\n\t"
        "v_mfma_f32_16x16x32_bf16 v[16:19], v[64:67], a[68:71], v[16:19]\n\t"
        "ds_read_b128 v[84:87], v106 offset:26624\n\t"
        "v_mfma_f32_16x16x32_bf16 v[20:23], a[60:63], v[68:71], v[20:23]\n\t"
        "v_mfma_f32_16x16x32_bf16 v[24:27], a[64:67], v[68:71], v[24:27]\n\t"
        "v_mfma_f32_16x16x32_bf16 v[28:31], v[68:71], a[68:71], v[28:31]\n\t"
        "s_mov_b32 %[t0], 3670016\n\t"
        "buffer_load_dwordx4 v[148:151], v101, %[sq], %[t0] offen\n\t"
        "buffer_load_dwordx4 v[156:159], v112, %[sv], %[t0] offen\n\t"
        "s_mov_b32 %[t0], 3932160\n\t"
        "buffer_load_dwordx4 v[152:155], v101, %[sq], %[t0] offen\n\t"
        "s_waitcnt vmcnt(9)\n\t"
        "ds_write_b128 v102, a[240:243] offset:0\n\t"
        "ds_write_b128 v102, a[244:247] offset:4096\n\t"
        "ds_write_b128 v116, a[248:251] offset:8192\n\t"
        "s_waitcnt lgkmcnt(0)\n\t"
        "s_barrier\n\t"
        "v_mfma_f32_16x16x32_bf16 v[8:11], a[72:75], v[72:75], v[8:11]\n\t"
        "ds_read_b64_tr_b16 a[96:97], v103 offset:0\n\t"
        "ds_read_b64_tr_b16 a[98:99], v103 offset:512\n\t"
        "v_mfma_f32_16x16x32_bf16 v[12:15], a[76:79], v[72:75], v[12:15]\n\t"
        "ds_read_b64_tr_b16 a[100:101], v103 offset:8\n\t"
        "ds_read_b64_tr_b16 a[102:103], v103 offset:520\n\t"
        "v_mfma_f32_16x16x32_bf16 v[16:19], v[72:75], a[80:83], v[16:19]\n\t"
        "ds_read_b64_tr_b16 a[104:105], v104 offset:0\n\t"
        "ds_read_b64_tr_b16 a[106:107], v104 offset:256\n\t"
        "v_mfma_f32_16x16x32_bf16 v[20:23], a[72:75], v[76:79], v[20:23]\n\t"
        "s_waitcnt lgkmcnt(5)\n\t"
        "ds_read_b64_tr_b16 a[108:109], v103 offset:4096\n\t"
        "v_mfma_f32_16x16x32_bf16 v[24:27], a[76:79], v[76:79], v[24:27]\n\t"
        "ds_read_b64_tr_b16 a[110:111], v103 offset:4608\n\t"
        "ds_read_b64_tr_b16 a[112:113], v103 offset:4104\n\t"
        "v_mfma_f32_16x16x32_bf16 v[28:31], v[76:79], a[80:83], v[28:31]\n\t"
        "ds_read_b64_tr_b16 a[114:115], v103 offset:4616\n\t"
        "ds_read_b64_tr_b16 a[116:117], v104 offset:2048\n\t"
        "v_mfma_f32_16x16x32_bf16 v[8:11], a[84:87], v[80:83], v[8:11]\n\t"
        "ds_read_b64_tr_b16 a[118:119], v104 offset:2304\n\t"
        "ds_read_b128 v[56:59], v105 offset:32768\n\t"
        "v_mfma_f32_16x16x32_bf16 v[12:15], a[88:91], v[80:83], v[12:15]\n\t"
        "ds_read_b128 v[60:63], v105 offset:34816\n\t"
        "ds_read_b128 v[64:67], v106 offset:32768\n\t"
        "v_mfma_f32_16x16x32_bf16 v[16:19], v[80:83], a[92:95], v[16:19]\n\t"
        "ds_read_b128 v[68:71], v106 offset:34816\n\t"
        "v_mfma_f32_16x16x32_bf16 v[20:23], a[84:87], v[84:87], v[20:23]\n\t"
        "v_mfma_f32_16x16x32_bf16 v[24:27], a[88:91], v[84:87], v[24:27]\n\t"
        "v_mfma_f32_16x16x32_bf16 v[28:31], v[84:87], a[92:95], v[28:31]\n\t"
        "s_add_u32 m0, %[ld], 65536\n\t"
        "s_add_u32 %[t0], %[xo], 1024\n\t"
        "buffer_load_dwordx4 v100, %[sx], %[t0] offen lds\n\t"
        "s_add_u32 m0, %[ld], 66560\n\t"
        "s_add_u32 %[t0], %[xo], 17408\n\t"
        "buffer_load_dwordx4 v100, %[sx], %[t0] offen lds\n\t"
        "s_add_u32 m0, %[ld], 73728\n\t"
        "s_add_u32 %[t0], %[xo], 1152\n\t"
        "buffer_load_dwordx4 v100, %[sx], %[t0] offen lds\n\t"
        "s_add_u32 m0, %[ld], 74752\n\t"
        "s_add_u32 %[t0], %[xo], 17536\n\t"
        "buffer_load_dwordx4 v100, %[sx], %[t0] offen lds\n\t"
        "s_add_u32 m0, %[ld], 81920\n\t"
        "s_add_u32 %[t0], %[xo], 1280\n\t"
        "buffer_load_dwordx4 v100, %[sx], %[t0] offen lds\n\t"
        "s_add_u32 m0, %[ld], 82944\n\t"
        "s_add_u32 %[t0], %[xo], 17664\n\t"
        "buffer_load_dwordx4 v100, %[sx], %[t0] offen lds\n\t"
        "s_add_u32 m0, %[ld], 90112\n\t"
        "s_add_u32 %[t0], %[xo], 1408\n\t"
        "buffer_load_dwordx4 v100, %[sx], %[t0] offen lds\n\t"
        "s_add_u32 m0, %[ld], 91136\n\t"
        "s_add_u32 %[t0], %[xo], 17792\n\t"
        "buffer_load_dwordx4 v100, %[sx], %[t0] offen lds\n\t"
        "s_mov_b32 %[t0], 4194304\n\t"
        "buffer_load_dwordx4 v[160:163], v101, %[sq], %[t0] offen\n\t"
        "buffer_load_dwordx4 v[168:171], v112, %[sv], %[t0] offen\n\t"
        "s_mov_b32 %[t0], 4456448\n\t"
        "buffer_load_dwordx4 v[164:167], v101, %[sq], %[t0] offen\n\t"
        "s_waitcnt vmcnt(17)\n\t"
        "ds_write_b128 v102, a[252:255] offset:16384\n\t"
        "ds_write_b128 v102, v[128:131] offset:20480\n\t"
        "ds_write_b128 v116, v[132:135] offset:24576\n\t"
        "s_waitcnt lgkmcnt(0)\n\t"
        "s_barrier\n\t"
        "v_mfma_f32_16x16x32_bf16 v[8:11], a[96:99], v[56:59], v[8:11]\n\t"
        "ds_read_b64_tr_b16 a[120:121], v103 offset:16384\n\t"
        "ds_read_b64_tr_b16 a[122:123], v103 offset:16896\n\t"
        "v_mfma_f32_16x16x32_bf16 v[12:15], a[100:103], v[56:59], v[12:15]\n\t"
        "ds_read_b64_tr_b16 a[124:125], v103 offset:16392\n\t"
        "ds_read_b64_tr_b16 a[126:127], v103 offset:16904\n\t"
        "v_mfma_f32_16x16x32_bf16 v[16:19], v[56:59], a[104:107], v[16:19]\n\t"
        "ds_read_b64_tr_b16 a[128:129], v104 offset:16384\n\t"
        "ds_read_b64_tr_b16 a[130:131], v104 offset:16640\n\t"
        "v_mfma_f32_16x16x32_bf16 v[20:23], a[96:99], v[60:63], v[20:23]\n\t"
        "s_waitcnt lgkmcnt(5)\n\t"
        "ds_read_b64_tr_b16 a[132:133], v103 offset:20480\n\t"
        "v_mfma_f32_16x16x32_bf16 v[24:27], a[100:103], v[60:63], v[24:27]\n\t"
        "ds_read_b64_tr_b16 a[134:135], v103 offset:20992\n\t"
        "ds_read_b64_tr_b16 a[136:137], v103 offset:20488\n\t"
        "v_mfma_f32_16x16x32_bf16 v[28:31], v[60:63], a[104:107], v[28:31]\n\t"
        "ds_read_b64_tr_b16 a[138:139], v103 offset:21000\n\t"
        "ds_read_b64_tr_b16 a[140:141], v104 offset:18432\n\t"
        "v_mfma_f32_16x16x32_bf16 v[8:11], a[108:111], v[64:67], v[8:11]\n\t"
        "ds_read_b64_tr_b16 a[142:143], v104 offset:18688\n\t"
        "ds_read_b128 v[72:75], v105 offset:40960\n\t"
        "v_mfma_f32_16x16x32_bf16 v[12:15], a[112:115], v[64:67], v[12:15]\n\t"
        "ds_read_b128 v[76:79], v105 offset:43008\n\t"
        "ds_read_b128 v[80:83], v106 offset:40960\n\t"
        "v_mfma_f32_16x16x32_bf16 v[16:19], v[64:67], a[116:119], v[16:19]\n\t"
        "ds_read_b128 v[84:87], v106 offset:43008\n\t"
        "v_mfma_f32_16x16x32_bf16 v[20:23], a[108:111], v[68:71], v[20:23]\n\t"
        "v_mfma_f32_16x16x32_bf16 v[24:27], a[112:115], v[68:71], v[24:27]\n\t"
        "v_mfma_f32_16x16x32_bf16 v[28:31], v[68:71], a[116:119], v[28:31]\n\t"
        "s_mov_b32 %[t0], 4718592\n\t"
        "buffer_load_dwordx4 v[172:175], v101, %[sq], %[t0] offen\n\t"
        "buffer_load_dwordx4 v[180:183], v112, %[sv], %[t0] offen\n\t"
        "s_mov_b32 %[t0], 4980736\n\t"
        "buffer_load_dwordx4 v[176:179], v101, %[sq], %[t0] offen\n\t"
        "s_waitcnt vmcnt(17)\n\t"
        "ds_write_b128 v102, v[136:139] offset:0\n\t"
        "ds_write_b128 v102, v[140:143] offset:4096\n\t"
        "ds_write_b128 v116, v[144:147] offset:8192\n\t"
        "s_waitcnt lgkmcnt(0)\n\t"
        "s_barrier\n\t"
        "v_mfma_f32_16x16x32_bf16 v[8:11], a[120:123], v[72:75], v[8:11]\n\t"
        "ds_read_b64_tr_b16 a[144:145], v103 offset:0\n\t"
        "ds_read_b64_tr_b16 a[146:147], v103 offset:512\n\t"
        "v_mfma_f32_16x16x32_bf16 v[12:15], a[124:127], v[72:75], v[12:15]\n\t"
        "ds_read_b64_tr_b16 a[148:149], v103 offset:8\n\t"
        "ds_read_b64_tr_b16 a[150:151], v103 offset:520\n\t"
        "v_mfma_f32_16x16x32_bf16 v[16:19], v[72:75], a[128:131], v[16:19]\n\t"
        "ds_read_b64_tr_b16 a[152:153], v104 offset:0\n\t"
        "ds_read_b64_tr_b16 a[154:155], v104 offset:256\n\t"
        "v_mfma_f32_16x16x32_bf16 v[20:23], a[120:123], v[76:79], v[20:23]\n\t"
        "s_waitcnt lgkmcnt(5)\n\t"
        "ds_read_b64_tr_b16 a[156:157], v103 offset:4096\n\t"
        "v_mfma_f32_16x16x32_bf16 v[24:27], a[124:127], v[76:79], v[24:27]\n\t"
        "ds_read_b64_tr_b16 a[158:159], v103 offset:4608\n\t"
        "ds_read_b64_tr_b16 a[160:161], v103 offset:4104\n\t"
        "v_mfma_f32_16x16x32_bf16 v[28:31], v[76:79], a[128:131], v[28:31]\n\t"
        "ds_read_b64_tr_b16 a[162:163], v103 offset:4616\n\t"
        "ds_read_b64_tr_b16 a[164:165], v104 offset:2048\n\t"
        "v_mfma_f32_16x16x32_bf16 v[8:11], a[132:135], v[80:83], v[8:11]\n\t"
        "ds_read_b64_tr_b16 a[166:167], v104 offset:2304\n\t"
        "ds_read_b128 v[56:59], v105 offset:49152\n\t"
        "v_mfma_f32_16x16x32_bf16 v[12:15], a[136:139], v[80:83], v[12:15]\n\t"
        "ds_read_b128 v[60:63], v105 offset:51200\n\t"
        "ds_read_b128 v[64:67], v106 offset:49152\n\t"
        "v_mfma_f32_16x16x32_bf16 v[16:19], v[80:83], a[140:143], v[16:19]\n\t"
        "ds_read_b128 v[68:71], v106 offset:51200\n\t"
        "v_mfma_f32_16x16x32_bf16 v[20:23], a[132:135], v[84:87], v[20:23]\n\t"
        "v_mfma_f32_16x16x32_bf16 v[24:27], a[136:139], v[84:87], v[24:27]\n\t"
        "v_mfma_f32_16x16x32_bf16 v[28:31], v[84:87], a[140:143], v[28:31]\n\t"
        "s_mov_b32 %[t0], 5242880\n\t"
        "buffer_load_dwordx4 v[184:187], v101, %[sq], %[t0] offen\n\t"
        "buffer_load_dwordx4 v[192:195], v112, %[sv], %[t0] offen\n\t"
        "s_mov_b32 %[t0], 5505024\n\t"
        "buffer_load_dwordx4 v[188:191], v101, %[sq], %[t0] offen\n\t"
        "s_waitcnt vmcnt(17)\n\t"
        "ds_write_b128 v102, v[148:151] offset:16384\n\t"
        "ds_write_b128 v102, v[152:155] offset:20480\n\t"
        "ds_write_b128 v116, v[156:159] offset:24576\n\t"
        "s_waitcnt lgkmcnt(0)\n\t"
        "s_barrier\n\t"
        "v_mfma_f32_16x16x32_bf16 v[8:11], a[144:147], v[56:59], v[8:11]\n\t"
        "ds_read_b64_tr_b16 a[168:169], v103 offset:16384\n\t"
        "ds_read_b64_tr_b16 a[170:171], v103 offset:16896\n\t"
        "v_mfma_f32_16x16x32_bf16 v[12:15], a[148:151], v[56:59], v[12:15]\n\t"
        "ds_read_b64_tr_b16 a[172:173], v103 offset:16392\n\t"
        "ds_read_b64_tr_b16 a[174:175], v103 offset:16904\n\t"
        "v_mfma_f32_16x16x32_bf16 v[16:19], v[56:59], a[152:155], v[16:19]\n\t"
        "ds_read_b64_tr_b16 a[176:177], v104 offset:16384\n\t"
        "ds_read_b64_tr_b16 a[178:179], v104 offset:16640\n\t"
        "v_mfma_f32_16x16x32_bf16 v[20:23], a[144:147], v[60:63], v[20:23]\n\t"
        "s_waitcnt lgkmcnt(5)\n\t"
        "ds_read_b64_tr_b16 a[180:181], v103 offset:20480\n\t"
        "v_mfma_f32_16x16x32_bf16 v[24:27], a[148:151], v[60:63], v[24:27]\n\t"
        "ds_read_b64_tr_b16 a[182:183], v103 offset:20992\n\t"
        "ds_read_b64_tr_b16 a[184:185], v103 offset:20488\n\t"
        "v_mfma_f32_16x16x32_bf16 v[28:31], v[60:63], a[152:155], v[28:31]\n\t"
        "ds_read_b64_tr_b16 a[186:187], v103 offset:21000\n\t"
        "ds_read_b64_tr_b16 a[188:189], v104 offset:18432\n\t"
        "v_mfma_f32_16x16x32_bf16 v[8:11], a[156:159], v[64:67], v[8:11]\n\t"
        "ds_read_b64_tr_b16 a[190:191], v104 offset:18688\n\t"
        "ds_read_b128 v[72:75], v105 offset:57344\n\t"
        "v_mfma_f32_16x16x32_bf16 v[12:15], a[160:163], v[64:67], v[12:15]\n\t"
        "ds_read_b128 v[76:79], v105 offset:59392\n\t"
        "ds_read_b128 v[80:83], v106 offset:57344\n\t"
        "v_mfma_f32_16x16x32_bf16 v[16:19], v[64:67], a[164:167], v[16:19]\n\t"
        "ds_read_b128 v[84:87], v106 offset:59392\n\t"
        "v_mfma_f32_16x16x32_bf16 v[20:23], a[156:159], v[68:71], v[20:23]\n\t"
        "v_mfma_f32_16x16x32_bf16 v[24:27], a[160:163], v[68:71], v[24:27]\n\t"
        "v_mfma_f32_16x16x32_bf16 v[28:31], v[68:71], a[164:167], v[28:31]\n\t"
        "s_mov_b32 %[t0], 5767168\n\t"
        "buffer_load_dwordx4 v[196:199], v101, %[sq], %[t0] offen\n\t"
        "buffer_load_dwordx4 v[204:207], v112, %[sv], %[t0] offen\n\t"
        "s_mov_b32 %[t0], 6029312\n\t"
        "buffer_load_dwordx4 v[200:203], v101, %[sq], %[t0] offen\n\t"
        "s_waitcnt vmcnt(9)\n\t"
        "ds_write_b128 v102, v[160:163] offset:0\n\t"
        "ds_write_b128 v102, v[164:167] offset:4096\n\t"
        "ds_write_b128 v116, v[168:171] offset:8192\n\t"
        "s_waitcnt lgkmcnt(0)\n\t"
        "s_barrier\n\t"
        "v_mfma_f32_16x16x32_bf16 v[8:11], a[168:171], v[72:75], v[8:11]\n\t"
        "ds_read_b64_tr_b16 a[192:193], v103 offset:0\n\t"
        "ds_read_b64_tr_b16 a[194:195], v103 offset:512\n\t"
        "v_mfma_f32_16x16x32_bf16 v[12:15], a[172:175], v[72:75], v[12:15]\n\t"
        "ds_read_b64_tr_b16 a[196:197], v103 offset:8\n\t"
        "ds_read_b64_tr_b16 a[198:199], v103 offset:520\n\t"
        "v_mfma_f32_16x16x32_bf16 v[16:19], v[72:75], a[176:179], v[16:19]\n\t"
        "ds_read_b64_tr_b16 a[200:201], v104 offset:0\n\t"
        "ds_read_b64_tr_b16 a[202:203], v104 offset:256\n\t"
        "v_mfma_f32_16x16x32_bf16 v[20:23], a[168:171], v[76:79], v[20:23]\n\t"
        "s_waitcnt lgkmcnt(5)\n\t"
        "ds_read_b64_tr_b16 a[204:205], v103 offset:4096\n\t"
        "v_mfma_f32_16x16x32_bf16 v[24:27], a[172:175], v[76:79], v[24:27]\n\t"
        "ds_read_b64_tr_b16 a[206:207], v103 offset:4608\n\t"
        "ds_read_b64_tr_b16 a[208:209], v103 offset:4104\n\t"
        "v_mfma_f32_16x16x32_bf16 v[28:31], v[76:79], a[176:179], v[28:31]\n\t"
        "ds_read_b64_tr_b16 a[210:211], v103 offset:4616\n\t"
        "ds_read_b64_tr_b16 a[212:213], v104 offset:2048\n\t"
        "v_mfma_f32_16x16x32_bf16 v[8:11], a[180:183], v[80:83], v[8:11]\n\t"
        "ds_read_b64_tr_b16 a[214:215], v104 offset:2304\n\t"
        "ds_read_b128 v[56:59], v107 offset:0\n\t"
        "v_mfma_f32_16x16x32_bf16 v[12:15], a[184:187], v[80:83], v[12:15]\n\t"
        "ds_read_b128 v[60:63], v107 offset:2048\n\t"
        "ds_read_b128 v[64:67], v108 offset:0\n\t"
        "v_mfma_f32_16x16x32_bf16 v[16:19], v[80:83], a[188:191], v[16:19]\n\t"
        "ds_read_b128 v[68:71], v108 offset:2048\n\t"
        "v_mfma_f32_16x16x32_bf16 v[20:23], a[180:183], v[84:87], v[20:23]\n\t"
        "v_mfma_f32_16x16x32_bf16 v[24:27], a[184:187], v[84:87], v[24:27]\n\t"
        "v_mfma_f32_16x16x32_bf16 v[28:31], v[84:87], a[188:191], v[28:31]\n\t"
        "s_add_u32 m0, %[ld], 98304\n\t"
        "s_add_u32 %[t0], %[xo], 1536\n\t"
        "buffer_load_dwordx4 v100, %[sx], %[t0] offen lds\n\t"
        "s_add_u32 m0, %[ld], 99328\n\t"
        "s_add_u32 %[t0], %[xo], 17920\n\t"
        "buffer_load_dwordx4 v100, %[sx], %[t0] offen lds\n\t"
        "s_add_u32 m0, %[ld], 106496\n\t"
        "s_add_u32 %[t0], %[xo], 1664\n\t"
        "buffer_load_dwordx4 v100, %[sx], %[t0] offen lds\n\t"
        "s_add_u32 m0, %[ld], 107520\n\t"
        "s_add_u32 %[t0], %[xo], 18048\n\t"
        "buffer_load_dwordx4 v100, %[sx], %[t0] offen lds\n\t"
        "s_add_u32 m0, %[ld], 114688\n\t"
        "s_add_u32 %[t0], %[xo], 1792\n\t"
        "buffer_load_dwordx4 v100, %[sx], %[t0] offen lds\n\t"
        "s_add_u32 m0, %[ld], 115712\n\t"
        "s_add_u32 %[t0], %[xo], 18176\n\t"
        "buffer_load_dwordx4 v100, %[sx], %[t0] offen lds\n\t"
        "s_add_u32 m0, %[ld], 122880\n\t"
        "s_add_u32 %[t0], %[xo], 1920\n\t"
        "buffer_load_dwordx4 v100, %[sx], %[t0] offen lds\n\t"
        "s_add_u32 m0, %[ld], 123904\n\t"
        "s_add_u32 %[t0], %[xo], 18304\n\t"
        "buffer_load_dwordx4 v100, %[sx], %[t0] offen lds\n\t"
        "s_mov_b32 %[t0], 6291456\n\t"
        "buffer_load_dwordx4 v[208:211], v101, %[sq], %[t0] offen\n\t"
        "buffer_load_dwordx4 v[216:219], v112, %[sv], %[t0] offen\n\t"
        "s_mov_b32 %[t0], 6553600\n\t"
        "buffer_load_dwordx4 v[212:215], v101, %[sq], %[t0] offen\n\t"
        "s_waitcnt vmcnt(17)\n\t"
        "ds_write_b128 v102, v[172:175] offset:16384\n\t"
        "ds_write_b128 v102, v[176:179] offset:20480\n\t"
        "ds_write_b128 v116, v[180:183] offset:24576\n\t"
        "s_waitcnt lgkmcnt(0)\n\t"
        "s_barrier\n\t"
        "v_mfma_f32_16x16x32_bf16 v[8:11], a[192:195], v[56:59], v[8:11]\n\t"
        "ds_read_b64_tr_b16 a[216:217], v103 offset:16384\n\t"
        "ds_read_b64_tr_b16 a[218:219], v103 offset:16896\n\t"
        "v_mfma_f32_16x16x32_bf16 v[12:15], a[196:199], v[56:59], v[12:15]\n\t"
        "ds_read_b64_tr_b16 a[220:221], v103 offset:16392\n\t"
        "ds_read_b64_tr_b16 a[222:223], v103 offset:16904\n\t"
        "v_mfma_f32_16x16x32_bf16 v[16:19], v[56:59], a[200:203], v[16:19]\n\t"
        "ds_read_b64_tr_b16 a[224:225], v104 offset:16384\n\t"
        "ds_read_b64_tr_b16 a[226:227], v104 offset:16640\n\t"
        "v_mfma_f32_16x16x32_bf16 v[20:23], a[192:195], v[60:63], v[20:23]\n\t"
        "s_waitcnt lgkmcnt(5)\n\t"
        "ds_read_b64_tr_b16 a[228:229], v103 offset:20480\n\t"
        "v_mfma_f32_16x16x32_bf16 v[24:27], a[196:199], v[60:63], v[24:27]\n\t"
        "ds_read_b64_tr_b16 a[230:231], v103 offset:20992\n\t"
        "ds_read_b64_tr_b16 a[232:233], v103 offset:20488\n\t"
        "v_mfma_f32_16x16x32_bf16 v[28:31], v[60:63], a[200:203], v[28:31]\n\t"
        "ds_read_b64_tr_b16 a[234:235], v103 offset:21000\n\t"
        "ds_read_b64_tr_b16 a[236:237], v104 offset:18432\n\t"
        "v_mfma_f32_16x16x32_bf16 v[8:11], a[204:207], v[64:67], v[8:11]\n\t"
        "ds_read_b64_tr_b16 a[238:239], v104 offset:18688\n\t"
        "ds_read_b128 v[72:75], v107 offset:8192\n\t"
        "v_mfma_f32_16x16x32_bf16 v[12:15], a[208:211], v[64:67], v[12:15]\n\t"
        "ds_read_b128 v[76:79], v107 offset:10240\n\t"
        "ds_read_b128 v[80:83], v108 offset:8192\n\t"
        "v_mfma_f32_16x16x32_bf16 v[16:19], v[64:67], a[212:215], v[16:19]\n\t"
        "ds_read_b128 v[84:87], v108 offset:10240\n\t"
        "v_mfma_f32_16x16x32_bf16 v[20:23], a[204:207], v[68:71], v[20:23]\n\t"
        "v_mfma_f32_16x16x32_bf16 v[24:27], a[208:211], v[68:71], v[24:27]\n\t"
        "v_mfma_f32_16x16x32_bf16 v[28:31], v[68:71], a[212:215], v[28:31]\n\t"
        "s_mov_b32 %[t0], 6815744\n\t"
        "buffer_load_dwordx4 v[220:223], v101, %[sq], %[t0] offen\n\t"
        "buffer_load_dwordx4 v[228:231], v112, %[sv], %[t0] offen\n\t"
        "s_mov_b32 %[t0], 7077888\n\t"
        "buffer_load_dwordx4 v[224:227], v101, %[sq], %[t0] offen\n\t"
        "s_waitcnt vmcnt(17)\n\t"
        "ds_write_b128 v102, v[184:187] offset:0\n\t"
        "ds_write_b128 v102, v[188:191] offset:4096\n\t"
        "ds_write_b128 v116, v[192:195] offset:8192\n\t"
        "s_waitcnt lgkmcnt(0)\n\t"
        "s_barrier\n\t"
        "v_mfma_f32_16x16x32_bf16 v[8:11], a[216:219], v[72:75], v[8:11]\n\t"
        "ds_read_b64_tr_b16 a[240:241], v103 offset:0\n\t"
        "ds_read_b64_tr_b16 a[242:243], v103 offset:512\n\t"
        "v_mfma_f32_16x16x32_bf16 v[12:15], a[220:223], v[72:75], v[12:15]\n\t"
        "ds_read_b64_tr_b16 a[244:245], v103 offset:8\n\t"
        "ds_read_b64_tr_b16 a[246:247], v103 offset:520\n\t"
        "v_mfma_f32_16x16x32_bf16 v[16:19], v[72:75], a[224:227], v[16:19]\n\t"
        "ds_read_b64_tr_b16 a[248:249], v104 offset:0\n\t"
        "ds_read_b64_tr_b16 a[250:251], v104 offset:256\n\t"
        "v_mfma_f32_16x16x32_bf16 v[20:23], a[216:219], v[76:79], v[20:23]\n\t"
        "s_waitcnt lgkmcnt(5)\n\t"
        "ds_read_b64_tr_b16 a[252:253], v103 offset:4096\n\t"
        "v_mfma_f32_16x16x32_bf16 v[24:27], a[220:223], v[76:79], v[24:27]\n\t"
        "ds_read_b64_tr_b16 a[254:255], v103 offset:4608\n\t"
        "ds_read_b64_tr_b16 v[128:129], v103 offset:4104\n\t"
        "v_mfma_f32_16x16x32_bf16 v[28:31], v[76:79], a[224:227], v[28:31]\n\t"
        "ds_read_b64_tr_b16 v[130:131], v103 offset:4616\n\t"
        "ds_read_b64_tr_b16 v[132:133], v104 offset:2048\n\t"
        "v_mfma_f32_16x16x32_bf16 v[8:11], a[228:231], v[80:83], v[8:11]\n\t"
        "ds_read_b64_tr_b16 v[134:135], v104 offset:2304\n\t"
        "ds_read_b128 v[56:59], v107 offset:16384\n\t"
        "v_mfma_f32_16x16x32_bf16 v[12:15], a[232:235], v[80:83], v[12:15]\n\t"
        "ds_read_b128 v[60:63], v107 offset:18432\n\t"
        "ds_read_b128 v[64:67], v108 offset:16384\n\t"
        "v_mfma_f32_16x16x32_bf16 v[16:19], v[80:83], a[236:239], v[16:19]\n\t"
        "ds_read_b128 v[68:71], v108 offset:18432\n\t"
        "v_mfma_f32_16x16x32_bf16 v[20:23], a[228:231], v[84:87], v[20:23]\n\t"
        "v_mfma_f32_16x16x32_bf16 v[24:27], a[232:235], v[84:87], v[24:27]\n\t"
        "v_mfma_f32_16x16x32_bf16 v[28:31], v[84:87], a[236:239], v[28:31]\n\t"
        "s_mov_b32 %[t0], 7340032\n\t"
        "buffer_load_dwordx4 v[232:235], v101, %[sq], %[t0] offen\n\t"
        "buffer_load_dwordx4 v[240:243], v112, %[sv], %[t0] offen\n\t"
        "s_mov_b32 %[t0], 7602176\n\t"
        "buffer_load_dwordx4 v[236:239], v101, %[sq], %[t0] offen\n\t"
        "s_waitcnt vmcnt(17)\n\t"
        "ds_write_b128 v102, v[196:199] offset:16384\n\t"
        "ds_write_b128 v102, v[200:203] offset:20480\n\t"
        "ds_write_b128 v116, v[204:207] offset:24576\n\t"
        "s_waitcnt lgkmcnt(0)\n\t"
        "s_barrier\n\t"
        "v_mfma_f32_16x16x32_bf16 v[8:11], a[240:243], v[56:59], v[8:11]\n\t"
        "ds_read_b64_tr_b16 v[136:137], v103 offset:16384\n\t"
        "ds_read_b64_tr_b16 v[138:139], v103 offset:16896\n\t"
        "v_mfma_f32_16x16x32_bf16 v[12:15], a[244:247], v[56:59], v[12:15]\n\t"
        "ds_read_b64_tr_b16 v[140:141], v103 offset:16392\n\t"
        "ds_read_b64_tr_b16 v[142:143], v103 offset:16904\n\t"
        "v_mfma_f32_16x16x32_bf16 v[16:19], v[56:59], a[248:251], v[16:19]\n\t"
        "ds_read_b64_tr_b16 v[144:145], v104 offset:16384\n\t"
        "ds_read_b64_tr_b16 v[146:147], v104 offset:16640\n\t"
        "v_mfma_f32_16x16x32_bf16 v[20:23], a[240:243], v[60:63], v[20:23]\n\t"
        "s_waitcnt lgkmcnt(5)\n\t"
        "ds_read_b64_tr_b16 v[148:149], v103 offset:20480\n\t"
        "v_mfma_f32_16x16x32_bf16 v[24:27], a[244:247], v[60:63], v[24:27]\n\t"
        "ds_read_b64_tr_b16 v[150:151], v103 offset:20992\n\t"
        "ds_read_b64_tr_b16 v[152:153], v103 offset:20488\n\t"
        "v_mfma_f32_16x16x32_bf16 v[28:31], v[60:63], a[248:251], v[28:31]\n\t"
        "ds_read_b64_tr_b16 v[154:155], v103 offset:21000\n\t"
        "ds_read_b64_tr_b16 v[156:157], v104 offset:18432\n\t"
        "v_mfma_f32_16x16x32_bf16 v[8:11], a[252:255], v[64:67], v[8:11]\n\t"
        "ds_read_b64_tr_b16 v[158:159], v104 offset:18688\n\t"
        "ds_read_b128 v[72:75], v107 offset:24576\n\t"
        "v_mfma_f32_16x16x32_bf16 v[12:15], v[128:131], v[64:67], v[12:15]\n\t"
        "ds_read_b128 v[76:79], v107 offset:26624\n\t"
        "ds_read_b128 v[80:83], v108 offset:24576\n\t"
        "v_mfma_f32_16x16x32_bf16 v[16:19], v[64:67], v[132:135], v[16:19]\n\t"
        "ds_read_b128 v[84:87], v108 offset:26624\n\t"
        "v_mfma_f32_16x16x32_bf16 v[20:23], a[252:255], v[68:71], v[20:23]\n\t"
        "v_mfma_f32_16x16x32_bf16 v[24:27], v[128:131], v[68:71], v[24:27]\n\t"
        "v_mfma_f32_16x16x32_bf16 v[28:31], v[68:71], v[132:135], v[28:31]\n\t"
        "s_mov_b32 %[t0], 7864320\n\t"
        "buffer_load_dwordx4 v[244:247], v101, %[sq], %[t0] offen\n\t"
        "buffer_load_dwordx4 v[252:255], v112, %[sv], %[t0] offen\n\t"
        "s_mov_b32 %[t0], 8126464\n\t"
        "buffer_load_dwordx4 v[248:251], v101, %[sq], %[t0] offen\n\t"
        "s_waitcnt vmcnt(9)\n\t"
        "ds_write_b128 v102, v[208:211] offset:0\n\t"
        "ds_write_b128 v102, v[212:215] offset:4096\n\t"
        "ds_write_b128 v116, v[216:219] offset:8192\n\t"
        "s_waitcnt lgkmcnt(0)\n\t"
        "s_barrier\n\t"
        "v_mfma_f32_16x16x32_bf16 v[8:11], v[136:139], v[72:75], v[8:11]\n\t"
        "ds_read_b64_tr_b16 v[160:161], v103 offset:0\n\t"
        "ds_read_b64_tr_b16 v[162:163], v103 offset:512\n\t"
        "v_mfma_f32_16x16x32_bf16 v[12:15], v[140:143], v[72:75], v[12:15]\n\t"
        "ds_read_b64_tr_b16 v[164:165], v103 offset:8\n\t"
        "ds_read_b64_tr_b16 v[166:167], v103 offset:520\n\t"
        "v_mfma_f32_16x16x32_bf16 v[16:19], v[72:75], v[144:147], v[16:19]\n\t"
        "ds_read_b64_tr_b16 v[168:169], v104 offset:0\n\t"
        "ds_read_b64_tr_b16 v[170:171], v104 offset:256\n\t"
        "v_mfma_f32_16x16x32_bf16 v[20:23], v[136:139], v[76:79], v[20:23]\n\t"
        "s_waitcnt lgkmcnt(5)\n\t"
        "ds_read_b64_tr_b16 v[172:173], v103 offset:4096\n\t"
        "v_mfma_f32_16x16x32_bf16 v[24:27], v[140:143], v[76:79], v[24:27]\n\t"
        "ds_read_b64_tr_b16 v[174:175], v103 offset:4608\n\t"
        "ds_read_b64_tr_b16 v[176:177], v103 offset:4104\n\t"
        "v_mfma_f32_16x16x32_bf16 v[28:31], v[76:79], v[144:147], v[28:31]\n\t"
        "ds_read_b64_tr_b16 v[178:179], v103 offset:4616\n\t"
        "ds_read_b64_tr_b16 v[180:181], v104 offset:2048\n\t"
        "v_mfma_f32_16x16x32_bf16 v[8:11], v[148:151], v[80:83], v[8:11]\n\t"
        "ds_read_b64_tr_b16 v[182:183], v104 offset:2304\n\t"
        "ds_read_b128 v[56:59], v107 offset:32768\n\t"
        "v_mfma_f32_16x16x32_bf16 v[12:15], v[152:155], v[80:83], v[12:15]\n\t"
        "ds_read_b128 v[60:63], v107 offset:34816\n\t"
        "ds_read_b128 v[64:67], v108 offset:32768\n\t"
        "v_mfma_f32_16x16x32_bf16 v[16:19], v[80:83], v[156:159], v[16:19]\n\t"
        "ds_read_b128 v[68:71], v108 offset:34816\n\t"
        "v_mfma_f32_16x16x32_bf16 v[20:23], v[148:151], v[84:87], v[20:23]\n\t"
        "v_mfma_f32_16x16x32_bf16 v[24:27], v[152:155], v[84:87], v[24:27]\n\t"
        "v_mfma_f32_16x16x32_bf16 v[28:31], v[84:87], v[156:159], v[28:31]\n\t"
        "s_mov_b32 %[xo], 0x20000\n\t"
        "s_add_u32 m0, %[ld], 0\n\t"
        "s_add_u32 %[t0], %[xo], 0\n\t"
        "buffer_load_dwordx4 v100, %[sx], %[t0] offen lds\n\t"
        "s_add_u32 m0, %[ld], 1024\n\t"
        "s_add_u32 %[t0], %[xo], 16384\n\t"
        "buffer_load_dwordx4 v100, %[sx], %[t0] offen lds\n\t"
        "s_add_u32 m0, %[ld], 8192\n\t"
        "s_add_u32 %[t0], %[xo], 128\n\t"
        "buffer_load_dwordx4 v100, %[sx], %[t0] offen lds\n\t"
        "s_add_u32 m0, %[ld], 9216\n\t"
        "s_add_u32 %[t0], %[xo], 16512\n\t"
        "buffer_load_dwordx4 v100, %[sx], %[t0] offen lds\n\t"
        "s_add_u32 m0, %[ld], 16384\n\t"
        "s_add_u32 %[t0], %[xo], 256\n\t"
        "buffer_load_dwordx4 v100, %[sx], %[t0] offen lds\n\t"
        "s_add_u32 m0, %[ld], 17408\n\t"
        "s_add_u32 %[t0], %[xo], 16640\n\t"
        "buffer_load_dwordx4 v100, %[sx], %[t0] offen lds\n\t"
        "s_add_u32 m0, %[ld], 24576\n\t"
        "s_add_u32 %[t0], %[xo], 384\n\t"
        "buffer_load_dwordx4 v100, %[sx], %[t0] offen lds\n\t"
        "s_add_u32 m0, %[ld], 25600\n\t"
        "s_add_u32 %[t0], %[xo], 16768\n\t"
        "buffer_load_dwordx4 v100, %[sx], %[t0] offen lds\n\t"
        "s_mov_b32 %[xo], 0\n\t"
        "s_waitcnt vmcnt(14)\n\t"
        "ds_write_b128 v102, v[220:223] offset:16384\n\t"
        "ds_write_b128 v102, v[224:227] offset:20480\n\t"
        "ds_write_b128 v116, v[228:231] offset:24576\n\t"
        "s_waitcnt lgkmcnt(0)\n\t"
        "s_barrier\n\t"
        "v_mfma_f32_16x16x32_bf16 v[8:11], v[160:163], v[56:59], v[8:11]\n\t"
        "ds_read_b64_tr_b16 v[184:185], v103 offset:16384\n\t"
        "ds_read_b64_tr_b16 v[186:187], v103 offset:16896\n\t"
        "v_mfma_f32_16x16x32_bf16 v[12:15], v[164:167], v[56:59], v[12:15]\n\t"
        "ds_read_b64_tr_b16 v[188:189], v103 offset:16392\n\t"
        "ds_read_b64_tr_b16 v[190:191], v103 offset:16904\n\t"
        "v_mfma_f32_16x16x32_bf16 v[16:19], v[56:59], v[168:171], v[16:19]\n\t"
        "ds_read_b64_tr_b16 v[192:193], v104 offset:16384\n\t"
        "ds_read_b64_tr_b16 v[194:195], v104 offset:16640\n\t"
        "v_mfma_f32_16x16x32_bf16 v[20:23], v[160:163], v[60:63], v[20:23]\n\t"
        "s_waitcnt lgkmcnt(5)\n\t"
        "ds_read_b64_tr_b16 v[196:197], v103 offset:20480\n\t"
        "v_mfma_f32_16x16x32_bf16 v[24:27], v[164:167], v[60:63], v[24:27]\n\t"
        "ds_read_b64_tr_b16 v[198:199], v103 offset:20992\n\t"
        "ds_read_b64_tr_b16 v[200:201], v103 offset:20488\n\t"
        "v_mfma_f32_16x16x32_bf16 v[28:31], v[60:63], v[168:171], v[28:31]\n\t"
        "ds_read_b64_tr_b16 v[202:203], v103 offset:21000\n\t"
        "ds_read_b64_tr_b16 v[204:205], v104 offset:18432\n\t"
        "v_mfma_f32_16x16x32_bf16 v[8:11], v[172:175], v[64:67], v[8:11]\n\t"
        "ds_read_b64_tr_b16 v[206:207], v104 offset:18688\n\t"
        "ds_read_b128 v[72:75], v107 offset:40960\n\t"
        "v_mfma_f32_16x16x32_bf16 v[12:15], v[176:179], v[64:67], v[12:15]\n\t"
        "ds_read_b128 v[76:79], v107 offset:43008\n\t"
        "ds_read_b128 v[80:83], v108 offset:40960\n\t"
        "v_mfma_f32_16x16x32_bf16 v[16:19], v[64:67], v[180:183], v[16:19]\n\t"
        "ds_read_b128 v[84:87], v108 offset:43008\n\t"
        "v_mfma_f32_16x16x32_bf16 v[20:23], v[172:175], v[68:71], v[20:23]\n\t"
        "v_mfma_f32_16x16x32_bf16 v[24:27], v[176:179], v[68:71], v[24:27]\n\t"
        "v_mfma_f32_16x16x32_bf16 v[28:31], v[68:71], v[180:183], v[28:31]\n\t"
        "s_waitcnt vmcnt(11)\n\t"
        "ds_write_b128 v102, v[232:235] offset:0\n\t"
        "ds_write_b128 v102, v[236:239] offset:4096\n\t"
        "ds_write_b128 v116, v[240:243] offset:8192\n\t"
        "s_waitcnt lgkmcnt(0)\n\t"
        "s_barrier\n\t"
        "v_mfma_f32_16x16x32_bf16 v[8:11], v[184:187], v[72:75], v[8:11]\n\t"
        "ds_read_b64_tr_b16 v[208:209], v103 offset:0\n\t"
        "ds_read_b64_tr_b16 v[210:211], v103 offset:512\n\t"
        "v_mfma_f32_16x16x32_bf16 v[12:15], v[188:191], v[72:75], v[12:15]\n\t"
        "ds_read_b64_tr_b16 v[212:213], v103 offset:8\n\t"
        "ds_read_b64_tr_b16 v[214:215], v103 offset:520\n\t"
        "v_mfma_f32_16x16x32_bf16 v[16:19], v[72:75], v[192:195], v[16:19]\n\t"
        "ds_read_b64_tr_b16 v[216:217], v104 offset:0\n\t"
        "ds_read_b64_tr_b16 v[218:219], v104 offset:256\n\t"
        "v_mfma_f32_16x16x32_bf16 v[20:23], v[184:187], v[76:79], v[20:23]\n\t"
        "s_waitcnt lgkmcnt(5)\n\t"
        "ds_read_b64_tr_b16 v[220:221], v103 offset:4096\n\t"
        "v_mfma_f32_16x16x32_bf16 v[24:27], v[188:191], v[76:79], v[24:27]\n\t"
        "ds_read_b64_tr_b16 v[222:223], v103 offset:4608\n\t"
        "ds_read_b64_tr_b16 v[224:225], v103 offset:4104\n\t"
        "v_mfma_f32_16x16x32_bf16 v[28:31], v[76:79], v[192:195], v[28:31]\n\t"
        "ds_read_b64_tr_b16 v[226:227], v103 offset:4616\n\t"
        "ds_read_b64_tr_b16 v[228:229], v104 offset:2048\n\t"
        "v_mfma_f32_16x16x32_bf16 v[8:11], v[196:199], v[80:83], v[8:11]\n\t"
        "ds_read_b64_tr_b16 v[230:231], v104 offset:2304\n\t"
        "ds_read_b128 v[56:59], v107 offset:49152\n\t"
        "v_mfma_f32_16x16x32_bf16 v[12:15], v[200:203], v[80:83], v[12:15]\n\t"
        "ds_read_b128 v[60:63], v107 offset:51200\n\t"
        "ds_read_b128 v[64:67], v108 offset:49152\n\t"
        "v_mfma_f32_16x16x32_bf16 v[16:19], v[80:83], v[204:207], v[16:19]\n\t"
        "ds_read_b128 v[68:71], v108 offset:51200\n\t"
        "v_mfma_f32_16x16x32_bf16 v[20:23], v[196:199], v[84:87], v[20:23]\n\t"
        "v_mfma_f32_16x16x32_bf16 v[24:27], v[200:203], v[84:87], v[24:27]\n\t"
        "v_mfma_f32_16x16x32_bf16 v[28:31], v[84:87], v[204:207], v[28:31]\n\t"
        "s_waitcnt vmcnt(8)\n\t"
        "ds_write_b128 v102, v[244:247] offset:16384\n\t"
        "ds_write_b128 v102, v[248:251] offset:20480\n\t"
        "ds_write_b128 v116, v[252:255] offset:24576\n\t"
        "s_waitcnt lgkmcnt(0)\n\t"
        "s_barrier\n\t"
        "v_mfma_f32_16x16x32_bf16 v[8:11], v[208:211], v[56:59], v[8:11]\n\t"
        "ds_read_b64_tr_b16 v[232:233], v103 offset:16384\n\t"
        "ds_read_b64_tr_b16 v[234:235], v103 offset:16896\n\t"
        "v_mfma_f32_16x16x32_bf16 v[12:15], v[212:215], v[56:59], v[12:15]\n\t"
        "ds_read_b64_tr_b16 v[236:237], v103 offset:16392\n\t"
        "ds_read_b64_tr_b16 v[238:239], v103 offset:16904\n\t"
        "v_mfma_f32_16x16x32_bf16 v[16:19], v[56:59], v[216:219], v[16:19]\n\t"
        "ds_read_b64_tr_b16 v[240:241], v104 offset:16384\n\t"
        "ds_read_b64_tr_b16 v[242:243], v104 offset:16640\n\t"
        "v_mfma_f32_16x16x32_bf16 v[20:23], v[208:211], v[60:63], v[20:23]\n\t"
        "s_waitcnt lgkmcnt(5)\n\t"
        "ds_read_b64_tr_b16 v[244:245], v103 offset:20480\n\t"
        "v_mfma_f32_16x16x32_bf16 v[24:27], v[212:215], v[60:63], v[24:27]\n\t"
        "ds_read_b64_tr_b16 v[246:247], v103 offset:20992\n\t"
        "ds_read_b64_tr_b16 v[248:249], v103 offset:20488\n\t"
        "v_mfma_f32_16x16x32_bf16 v[28:31], v[60:63], v[216:219], v[28:31]\n\t"
        "ds_read_b64_tr_b16 v[250:251], v103 offset:21000\n\t"
        "ds_read_b64_tr_b16 v[252:253], v104 offset:18432\n\t"
        "v_mfma_f32_16x16x32_bf16 v[8:11], v[220:223], v[64:67], v[8:11]\n\t"
        "ds_read_b64_tr_b16 v[254:255], v104 offset:18688\n\t"
        "ds_read_b128 v[72:75], v107 offset:57344\n\t"
        "v_mfma_f32_16x16x32_bf16 v[12:15], v[224:227], v[64:67], v[12:15]\n\t"
        "ds_read_b128 v[76:79], v107 offset:59392\n\t"
        "ds_read_b128 v[80:83], v108 offset:57344\n\t"
        "v_mfma_f32_16x16x32_bf16 v[16:19], v[64:67], v[228:231], v[16:19]\n\t"
        "ds_read_b128 v[84:87], v108 offset:59392\n\t"
        "v_mfma_f32_16x16x32_bf16 v[20:23], v[220:223], v[68:71], v[20:23]\n\t"
        "v_mfma_f32_16x16x32_bf16 v[24:27], v[224:227], v[68:71], v[24:27]\n\t"
        "v_mfma_f32_16x16x32_bf16 v[28:31], v[68:71], v[228:231], v[28:31]\n\t"
        "s_waitcnt vmcnt(0) lgkmcnt(0)\n\t"
        "s_barrier\n\t"
        "v_mfma_f32_16x16x32_bf16 v[8:11], v[232:235], v[72:75], v[8:11]\n\t"
        "v_mfma_f32_16x16x32_bf16 v[12:15], v[236:239], v[72:75], v[12:15]\n\t"
        "v_mfma_f32_16x16x32_bf16 v[16:19], v[72:75], v[240:243], v[16:19]\n\t"
        "v_mfma_f32_16x16x32_bf16 v[20:23], v[232:235], v[76:79], v[20:23]\n\t"
        "v_mfma_f32_16x16x32_bf16 v[24:27], v[236:239], v[76:79], v[24:27]\n\t"
        "v_mfma_f32_16x16x32_bf16 v[28:31], v[76:79], v[240:243], v[28:31]\n\t"
        "v_mfma_f32_16x16x32_bf16 v[8:11], v[244:247], v[80:83], v[8:11]\n\t"
        "v_mfma_f32_16x16x32_bf16 v[12:15], v[248:251], v[80:83], v[12:15]\n\t"
        "v_mfma_f32_16x16x32_bf16 v[16:19], v[80:83], v[252:255], v[16:19]\n\t"
        "v_mfma_f32_16x16x32_bf16 v[20:23], v[244:247], v[84:87], v[20:23]\n\t"
        "v_mfma_f32_16x16x32_bf16 v[24:27], v[248:251], v[84:87], v[24:27]\n\t"
        "v_mfma_f32_16x16x32_bf16 v[28:31], v[84:87], v[252:255], v[28:31]\n\t"
        "s_mov_b32 %[xo], 0x20000\n\t"
        "ds_read_b128 v[56:59], v105 offset:0\n\t"
        "ds_read_b128 v[60:63], v105 offset:2048\n\t"
        "ds_read_b128 v[64:67], v106 offset:0\n\t"
        "ds_read_b128 v[68:71], v106 offset:2048\n\t"
        "ds_read_b128 v[72:75], v105 offset:8192\n\t"
        "ds_read_b128 v[76:79], v105 offset:10240\n\t"
        "ds_read_b128 v[80:83], v106 offset:8192\n\t"
        "v_cvt_pk_bf16_f32 v88, v8, v9\n\t"
        "v_cvt_pk_bf16_f32 v89, v10, v11\n\t"
        "v_cvt_pk_bf16_f32 v90, v12, v13\n\t"
        "v_cvt_pk_bf16_f32 v91, v14, v15\n\t"
        "buffer_store_dwordx4 v[88:91], v109, %[sw], %[so] offen sc1\n\t"
        "v_cvt_pk_bf16_f32 v92, v20, v21\n\t"
        "v_cvt_pk_bf16_f32 v93, v22, v23\n\t"
        "v_cvt_pk_bf16_f32 v94, v24, v25\n\t"
        "v_cvt_pk_bf16_f32 v95, v26, v27\n\t"
        "buffer_store_dwordx4 v[92:95], v110, %[sw], %[so] offen sc1\n\t"
        "v_cvt_pk_bf16_f32 v96, v16, v17\n\t"
        "v_cvt_pk_bf16_f32 v97, v18, v19\n\t"
        "v_cvt_pk_bf16_f32 v98, v28, v29\n\t"
        "v_cvt_pk_bf16_f32 v99, v30, v31\n\t"
        "buffer_store_dwordx4 v[96:99], v111, %[sw], %[so] offen sc1\n\t"
        "s_mov_b32 %[so], 0xf0000000\n\t"
        "s_waitcnt vmcnt(0)\n\t"
        "v_mov_b32_e32 v117, %[cn]\n\t"
        "buffer_store_dword v117, v113, %[sw], 0 offen sc1\n\t"
        "s_add_u32 m0, %[ld], 32768\n\t"
        "s_add_u32 %[t0], %[xo], 512\n\t"
        "buffer_load_dwordx4 v100, %[sx], %[t0] offen lds\n\t"
        "s_add_u32 m0, %[ld], 33792\n\t"
        "s_add_u32 %[t0], %[xo], 16896\n\t"
        "buffer_load_dwordx4 v100, %[sx], %[t0] offen lds\n\t"
        "s_add_u32 m0, %[ld], 40960\n\t"
        "s_add_u32 %[t0], %[xo], 640\n\t"
        "buffer_load_dwordx4 v100, %[sx], %[t0] offen lds\n\t"
        "s_add_u32 m0, %[ld], 41984\n\t"
        "s_add_u32 %[t0], %[xo], 17024\n\t"
        "buffer_load_dwordx4 v100, %[sx], %[t0] offen lds\n\t"
        "s_add_u32 m0, %[ld], 49152\n\t"
        "s_add_u32 %[t0], %[xo], 768\n\t"
        "buffer_load_dwordx4 v100, %[sx], %[t0] offen lds\n\t"
        "s_add_u32 m0, %[ld], 50176\n\t"
        "s_add_u32 %[t0], %[xo], 17152\n\t"
        "buffer_load_dwordx4 v100, %[sx], %[t0] offen lds\n\t"
        "s_add_u32 m0, %[ld], 57344\n\t"
        "s_add_u32 %[t0], %[xo], 896\n\t"
        "buffer_load_dwordx4 v100, %[sx], %[t0] offen lds\n\t"
        "s_add_u32 m0, %[ld], 58368\n\t"
        "s_add_u32 %[t0], %[xo], 17280\n\t"
        "buffer_load_dwordx4 v100, %[sx], %[t0] offen lds\n\t"
        "s_add_u32 m0, %[ld], 65536\n\t"
        "s_add_u32 %[t0], %[xo], 1024\n\t"
        "buffer_load_dwordx4 v100, %[sx], %[t0] offen lds\n\t"
        "s_add_u32 m0, %[ld], 66560\n\t"
        "s_add_u32 %[t0], %[xo], 17408\n\t"
        "buffer_load_dwordx4 v100, %[sx], %[t0] offen lds\n\t"
        "s_add_u32 m0, %[ld], 73728\n\t"
        "s_add_u32 %[t0], %[xo], 1152\n\t"
        "buffer_load_dwordx4 v100, %[sx], %[t0] offen lds\n\t"
        "s_add_u32 m0, %[ld], 74752\n\t"
        "s_add_u32 %[t0], %[xo], 17536\n\t"
        "buffer_load_dwordx4 v100, %[sx], %[t0] offen lds\n\t"
        "s_add_u32 m0, %[ld], 81920\n\t"
        "s_add_u32 %[t0], %[xo], 1280\n\t"
        "buffer_load_dwordx4 v100, %[sx], %[t0] offen lds\n\t"
        "s_add_u32 m0, %[ld], 82944\n\t"
        "s_add_u32 %[t0], %[xo], 17664\n\t"
        "buffer_load_dwordx4 v100, %[sx], %[t0] offen lds\n\t"
        "s_add_u32 m0, %[ld], 90112\n\t"
        "s_add_u32 %[t0], %[xo], 1408\n\t"
        "buffer_load_dwordx4 v100, %[sx], %[t0] offen lds\n\t"
        "s_add_u32 m0, %[ld], 91136\n\t"
        "s_add_u32 %[t0], %[xo], 17792\n\t"
        "buffer_load_dwordx4 v100, %[sx], %[t0] offen lds\n\t"
        "s_add_u32 m0, %[ld], 98304\n\t"
        "s_add_u32 %[t0], %[xo], 1536\n\t"
        "buffer_load_dwordx4 v100, %[sx], %[t0] offen lds\n\t"
        "s_add_u32 m0, %[ld], 99328\n\t"
        "s_add_u32 %[t0], %[xo], 17920\n\t"
        "buffer_load_dwordx4 v100, %[sx], %[t0] offen lds\n\t"
        "s_add_u32 m0, %[ld], 106496\n\t"
        "s_add_u32 %[t0], %[xo], 1664\n\t"
        "buffer_load_dwordx4 v100, %[sx], %[t0] offen lds\n\t"
        "s_branch LG_mid%=\n\t"
        "LG_loop%=:\n\t"
        "s_waitcnt lgkmcnt(6)\n\t"
        "v_mfma_f32_16x16x32_bf16 v[8:11], a[0:3], v[56:59], 0\n\t"
        "v_mfma_f32_16x16x32_bf16 v[12:15], a[4:7], v[56:59], 0\n\t"
        "ds_read_b128 v[84:87], v106 offset:10240\n\t"
        "v_mfma_f32_16x16x32_bf16 v[16:19], v[56:59], a[8:11], 0\n\t"
        "s_add_u32 m0, %[ld], 107520\n\t"
        "s_add_u32 %[t0], %[xo], 18048\n\t"
        "s_waitcnt lgkmcnt(6)\n\t"
        "v_mfma_f32_16x16x32_bf16 v[20:23], a[0:3], v[60:63], 0\n\t"
        "buffer_load_dwordx4 v100, %[sx], %[t0] offen lds\n\t"
        "v_mfma_f32_16x16x32_bf16 v[24:27], a[4:7], v[60:63], 0\n\t"
        "ds_read_b128 v[56:59], v105 offset:16384\n\t"
        "v_mfma_f32_16x16x32_bf16 v[28:31], v[60:63], a[8:11], 0\n\t"
        "s_waitcnt lgkmcnt(6)\n\t"
        "v_mfma_f32_16x16x32_bf16 v[8:11], a[12:15], v[64:67], v[8:11]\n\t"
        "v_cvt_pk_bf16_f32 v88, v32, v33\n\t"
        "v_cvt_pk_bf16_f32 v89, v34, v35\n\t"
        "v_mfma_f32_16x16x32_bf16 v[12:15], a[16:19], v[64:67], v[12:15]\n\t"
        "ds_read_b128 v[60:63], v105 offset:18432\n\t"
        "v_cvt_pk_bf16_f32 v90, v36, v37\n\t"
        "v_mfma_f32_16x16x32_bf16 v[16:19], v[64:67], a[20:23], v[16:19]\n\t"
        "s_add_u32 m0, %[ld], 114688\n\t"
        "s_add_u32 %[t0], %[xo], 1792\n\t"
        "s_waitcnt lgkmcnt(6)\n\t"
        "v_mfma_f32_16x16x32_bf16 v[20:23], a[12:15], v[68:71], v[20:23]\n\t"
        "buffer_load_dwordx4 v100, %[sx], %[t0] offen lds\n\t"
        "v_mfma_f32_16x16x32_bf16 v[24:27], a[16:19], v[68:71], v[24:27]\n\t"
        "ds_read_b128 v[64:67], v106 offset:16384\n\t"
        "v_cvt_pk_bf16_f32 v91, v38, v39\n\t"
        "v_mfma_f32_16x16x32_bf16 v[28:31], v[68:71], a[20:23], v[28:31]\n\t"
        "buffer_store_dwordx4 v[88:91], v109, %[sw], %[so] offen sc1\n\t"
        "s_waitcnt lgkmcnt(6)\n\t"
        "v_mfma_f32_16x16x32_bf16 v[8:11], a[24:27], v[72:75], v[8:11]\n\t"
        "v_cvt_pk_bf16_f32 v92, v44, v45\n\t"
        "v_cvt_pk_bf16_f32 v93, v46, v47\n\t"
        "v_mfma_f32_16x16x32_bf16 v[12:15], a[28:31], v[72:75], v[12:15]\n\t"
        "ds_read_b128 v[68:71], v106 offset:18432\n\t"
        "v_cvt_pk_bf16_f32 v94, v48, v49\n\t"
        "v_mfma_f32_16x16x32_bf16 v[16:19], v[72:75], a[32:35], v[16:19]\n\t"
        "s_add_u32 m0, %[ld], 115712\n\t"
        "s_add_u32 %[t0], %[xo], 18176\n\t"
        "s_waitcnt lgkmcnt(6)\n\t"
        "v_mfma_f32_16x16x32_bf16 v[20:23], a[24:27], v[76:79], v[20:23]\n\t"
        "buffer_load_dwordx4 v100, %[sx], %[t0] offen lds\n\t"
        "v_mfma_f32_16x16x32_bf16 v[24:27], a[28:31], v[76:79], v[24:27]\n\t"
        "ds_read_b128 v[72:75], v105 offset:24576\n\t"
        "v_cvt_pk_bf16_f32 v95, v50, v51\n\t"
        "v_mfma_f32_16x16x32_bf16 v[28:31], v[76:79], a[32:35], v[28:31]\n\t"
        "buffer_store_dwordx4 v[92:95], v110, %[sw], %[so] offen sc1\n\t"
        "s_waitcnt lgkmcnt(6)\n\t"
        "v_mfma_f32_16x16x32_bf16 v[8:11], a[36:39], v[80:83], v[8:11]\n\t"
        "v_cvt_pk_bf16_f32 v96, v40, v41\n\t"
        "v_cvt_pk_bf16_f32 v97, v42, v43\n\t"
        "v_mfma_f32_16x16x32_bf16 v[12:15], a[40:43], v[80:83], v[12:15]\n\t"
        "ds_read_b128 v[76:79], v105 offset:26624\n\t"
        "v_cvt_pk_bf16_f32 v98, v52, v53\n\t"
        "v_mfma_f32_16x16x32_bf16 v[16:19], v[80:83], a[44:47], v[16:19]\n\t"
        "s_add_u32 m0, %[ld], 122880\n\t"
        "s_add_u32 %[t0], %[xo], 1920\n\t"
        "s_waitcnt lgkmcnt(6)\n\t"
        "v_mfma_f32_16x16x32_bf16 v[20:23], a[36:39], v[84:87], v[20:23]\n\t"
        "buffer_load_dwordx4 v100, %[sx], %[t0] offen lds\n\t"
        "v_mfma_f32_16x16x32_bf16 v[24:27], a[40:43], v[84:87], v[24:27]\n\t"
        "ds_read_b128 v[80:83], v106 offset:24576\n\t"
        "v_cvt_pk_bf16_f32 v99, v54, v55\n\t"
        "v_mfma_f32_16x16x32_bf16 v[28:31], v[84:87], a[44:47], v[28:31]\n\t"
        "buffer_store_dwordx4 v[96:99], v111, %[sw], %[so] offen sc1\n\t"
        "s_waitcnt lgkmcnt(6)\n\t"
        "v_mfma_f32_16x16x32_bf16 v[8:11], a[48:51], v[56:59], v[8:11]\n\t"
        "s_lshl_b32 %[so], %[cn], 19\n\t"
        "v_mfma_f32_16x16x32_bf16 v[12:15], a[52:55], v[56:59], v[12:15]\n\t"
        "ds_read_b128 v[84:87], v106 offset:26624\n\t"
        "v_mfma_f32_16x16x32_bf16 v[16:19], v[56:59], a[56:59], v[16:19]\n\t"
        "s_add_u32 m0, %[ld], 123904\n\t"
        "s_add_u32 %[t0], %[xo], 18304\n\t"
        "s_waitcnt lgkmcnt(6)\n\t"
        "v_mfma_f32_16x16x32_bf16 v[20:23], a[48:51], v[60:63], v[20:23]\n\t"
        "buffer_load_dwordx4 v100, %[sx], %[t0] offen lds\n\t"
        "v_mfma_f32_16x16x32_bf16 v[24:27], a[52:55], v[60:63], v[24:27]\n\t"
        "v_mfma_f32_16x16x32_bf16 v[28:31], v[60:63], a[56:59], v[28:31]\n\t"
        "s_waitcnt lgkmcnt(5)\n\t"
        "v_mfma_f32_16x16x32_bf16 v[8:11], a[60:63], v[64:67], v[8:11]\n\t"
        "s_waitcnt vmcnt(19) lgkmcnt(0)\n\t"
        "s_barrier\n\t"
        "s_add_u32 %[xo], %[xo], 0x20000\n\t"
        "v_mfma_f32_16x16x32_bf16 v[12:15], a[64:67], v[64:67], v[12:15]\n\t"
        "ds_read_b128 v[56:59], v105 offset:32768\n\t"
        "v_mfma_f32_16x16x32_bf16 v[16:19], v[64:67], a[68:71], v[16:19]\n\t"
        "ds_read_b128 v[60:63], v105 offset:34816\n\t"
        "s_add_u32 m0, %[ld], 0\n\t"
        "s_add_u32 %[t0], %[xo], 0\n\t"
        "v_mfma_f32_16x16x32_bf16 v[20:23], a[60:63], v[68:71], v[20:23]\n\t"
        "buffer_load_dwordx4 v100, %[sx], %[t0] offen lds\n\t"
        "v_mfma_f32_16x16x32_bf16 v[24:27], a[64:67], v[68:71], v[24:27]\n\t"
        "ds_read_b128 v[64:67], v106 offset:32768\n\t"
        "v_mfma_f32_16x16x32_bf16 v[28:31], v[68:71], a[68:71], v[28:31]\n\t"
        "v_mfma_f32_16x16x32_bf16 v[8:11], a[72:75], v[72:75], v[8:11]\n\t"
        "v_mfma_f32_16x16x32_bf16 v[12:15], a[76:79], v[72:75], v[12:15]\n\t"
        "ds_read_b128 v[68:71], v106 offset:34816\n\t"
        "v_mfma_f32_16x16x32_bf16 v[16:19], v[72:75], a[80:83], v[16:19]\n\t"
        "s_add_u32 m0, %[ld], 1024\n\t"
        "s_add_u32 %[t0], %[xo], 16384\n\t"
        "v_mfma_f32_16x16x32_bf16 v[20:23], a[72:75], v[76:79], v[20:23]\n\t"
        "buffer_load_dwordx4 v100, %[sx], %[t0] offen lds\n\t"
        "v_mfma_f32_16x16x32_bf16 v[24:27], a[76:79], v[76:79], v[24:27]\n\t"
        "ds_read_b128 v[72:75], v105 offset:40960\n\t"
        "v_mfma_f32_16x16x32_bf16 v[28:31], v[76:79], a[80:83], v[28:31]\n\t"
        "v_mfma_f32_16x16x32_bf16 v[8:11], a[84:87], v[80:83], v[8:11]\n\t"
        "v_mfma_f32_16x16x32_bf16 v[12:15], a[88:91], v[80:83], v[12:15]\n\t"
        "ds_read_b128 v[76:79], v105 offset:43008\n\t"
        "v_mfma_f32_16x16x32_bf16 v[16:19], v[80:83], a[92:95], v[16:19]\n\t"
        "s_add_u32 m0, %[ld], 8192\n\t"
        "s_add_u32 %[t0], %[xo], 128\n\t"
        "v_mfma_f32_16x16x32_bf16 v[20:23], a[84:87], v[84:87], v[20:23]\n\t"
        "buffer_load_dwordx4 v100, %[sx], %[t0] offen lds\n\t"
        "v_mfma_f32_16x16x32_bf16 v[24:27], a[88:91], v[84:87], v[24:27]\n\t"
        "ds_read_b128 v[80:83], v106 offset:40960\n\t"
        "v_mfma_f32_16x16x32_bf16 v[28:31], v[84:87], a[92:95], v[28:31]\n\t"
        "s_waitcnt lgkmcnt(6)\n\t"
        "v_mfma_f32_16x16x32_bf16 v[8:11], a[96:99], v[56:59], v[8:11]\n\t"
        "v_mfma_f32_16x16x32_bf16 v[12:15], a[100:103], v[56:59], v[12:15]\n\t"
        "ds_read_b128 v[84:87], v106 offset:43008\n\t"
        "v_mfma_f32_16x16x32_bf16 v[16:19], v[56:59], a[104:107], v[16:19]\n\t"
        "s_add_u32 m0, %[ld], 9216\n\t"
        "s_add_u32 %[t0], %[xo], 16512\n\t"
        "s_waitcnt lgkmcnt(6)\n\t"
        "v_mfma_f32_16x16x32_bf16 v[20:23], a[96:99], v[60:63], v[20:23]\n\t"
        "buffer_load_dwordx4 v100, %[sx], %[t0] offen lds\n\t"
        "v_mfma_f32_16x16x32_bf16 v[24:27], a[100:103], v[60:63], v[24:27]\n\t"
        "ds_read_b128 v[56:59], v105 offset:49152\n\t"
        "v_mfma_f32_16x16x32_bf16 v[28:31], v[60:63], a[104:107], v[28:31]\n\t"
        "s_waitcnt lgkmcnt(6)\n\t"
        "v_mfma_f32_16x16x32_bf16 v[8:11], a[108:111], v[64:67], v[8:11]\n\t"
        "v_mfma_f32_16x16x32_bf16 v[12:15], a[112:115], v[64:67], v[12:15]\n\t"
        "ds_read_b128 v[60:63], v105 offset:51200\n\t"
        "v_mfma_f32_16x16x32_bf16 v[16:19], v[64:67], a[116:119], v[16:19]\n\t"
        "s_add_u32 m0, %[ld], 16384\n\t"
        "s_add_u32 %[t0], %[xo], 256\n\t"
        "s_waitcnt lgkmcnt(6)\n\t"
        "v_mfma_f32_16x16x32_bf16 v[20:23], a[108:111], v[68:71], v[20:23]\n\t"
        "buffer_load_dwordx4 v100, %[sx], %[t0] offen lds\n\t"
        "v_mfma_f32_16x16x32_bf16 v[24:27], a[112:115], v[68:71], v[24:27]\n\t"
        "ds_read_b128 v[64:67], v106 offset:49152\n\t"
        "v_mfma_f32_16x16x32_bf16 v[28:31], v[68:71], a[116:119], v[28:31]\n\t"
        "s_waitcnt lgkmcnt(6)\n\t"
        "v_mfma_f32_16x16x32_bf16 v[8:11], a[120:123], v[72:75], v[8:11]\n\t"
        "v_mfma_f32_16x16x32_bf16 v[12:15], a[124:127], v[72:75], v[12:15]\n\t"
        "ds_read_b128 v[68:71], v106 offset:51200\n\t"
        "v_mfma_f32_16x16x32_bf16 v[16:19], v[72:75], a[128:131], v[16:19]\n\t"
        "s_add_u32 m0, %[ld], 17408\n\t"
        "s_add_u32 %[t0], %[xo], 16640\n\t"
        "s_waitcnt lgkmcnt(6)\n\t"
        "v_mfma_f32_16x16x32_bf16 v[20:23], a[120:123], v[76:79], v[20:23]\n\t"
        "buffer_load_dwordx4 v100, %[sx], %[t0] offen lds\n\t"
        "v_mfma_f32_16x16x32_bf16 v[24:27], a[124:127], v[76:79], v[24:27]\n\t"
        "ds_read_b128 v[72:75], v105 offset:57344\n\t"
        "v_mfma_f32_16x16x32_bf16 v[28:31], v[76:79], a[128:131], v[28:31]\n\t"
        "s_waitcnt lgkmcnt(6)\n\t"
        "v_mfma_f32_16x16x32_bf16 v[8:11], a[132:135], v[80:83], v[8:11]\n\t"
        "v_mfma_f32_16x16x32_bf16 v[12:15], a[136:139], v[80:83], v[12:15]\n\t"
        "ds_read_b128 v[76:79], v105 offset:59392\n\t"
        "v_mfma_f32_16x16x32_bf16 v[16:19], v[80:83], a[140:143], v[16:19]\n\t"
        "s_add_u32 m0, %[ld], 24576\n\t"
        "s_add_u32 %[t0], %[xo], 384\n\t"
        "s_waitcnt lgkmcnt(6)\n\t"
        "v_mfma_f32_16x16x32_bf16 v[20:23], a[132:135], v[84:87], v[20:23]\n\t"
        "buffer_load_dwordx4 v100, %[sx], %[t0] offen lds\n\t"
        "v_mfma_f32_16x16x32_bf16 v[24:27], a[136:139], v[84:87], v[24:27]\n\t"
        "ds_read_b128 v[80:83], v106 offset:57344\n\t"
        "v_mfma_f32_16x16x32_bf16 v[28:31], v[84:87], a[140:143], v[28:31]\n\t"
        "s_waitcnt lgkmcnt(6)\n\t"
        "v_mfma_f32_16x16x32_bf16 v[8:11], a[144:147], v[56:59], v[8:11]\n\t"
        "v_mfma_f32_16x16x32_bf16 v[12:15], a[148:151], v[56:59], v[12:15]\n\t"
        "ds_read_b128 v[84:87], v106 offset:59392\n\t"
        "v_mfma_f32_16x16x32_bf16 v[16:19], v[56:59], a[152:155], v[16:19]\n\t"
        "s_add_u32 m0, %[ld], 25600\n\t"
        "s_add_u32 %[t0], %[xo], 16768\n\t"
        "s_waitcnt lgkmcnt(6)\n\t"
        "v_mfma_f32_16x16x32_bf16 v[20:23], a[144:147], v[60:63], v[20:23]\n\t"
        "buffer_load_dwordx4 v100, %[sx], %[t0] offen lds\n\t"
        "v_mfma_f32_16x16x32_bf16 v[24:27], a[148:151], v[60:63], v[24:27]\n\t"
        "v_mfma_f32_16x16x32_bf16 v[28:31], v[60:63], a[152:155], v[28:31]\n\t"
        "s_waitcnt lgkmcnt(5)\n\t"
        "v_mfma_f32_16x16x32_bf16 v[8:11], a[156:159], v[64:67], v[8:11]\n\t"
        "s_waitcnt vmcnt(9) lgkmcnt(0)\n\t"
        "s_barrier\n\t"
        "v_mfma_f32_16x16x32_bf16 v[12:15], a[160:163], v[64:67], v[12:15]\n\t"
        "ds_read_b128 v[56:59], v107 offset:0\n\t"
        "v_mfma_f32_16x16x32_bf16 v[16:19], v[64:67], a[164:167], v[16:19]\n\t"
        "ds_read_b128 v[60:63], v107 offset:2048\n\t"
        "s_add_u32 m0, %[ld], 32768\n\t"
        "s_add_u32 %[t0], %[xo], 512\n\t"
        "v_mfma_f32_16x16x32_bf16 v[20:23], a[156:159], v[68:71], v[20:23]\n\t"
        "buffer_load_dwordx4 v100, %[sx], %[t0] offen lds\n\t"
        "v_mfma_f32_16x16x32_bf16 v[24:27], a[160:163], v[68:71], v[24:27]\n\t"
        "ds_read_b128 v[64:67], v108 offset:0\n\t"
        "v_mfma_f32_16x16x32_bf16 v[28:31], v[68:71], a[164:167], v[28:31]\n\t"
        "v_mfma_f32_16x16x32_bf16 v[8:11], a[168:171], v[72:75], v[8:11]\n\t"
        "v_mfma_f32_16x16x32_bf16 v[12:15], a[172:175], v[72:75], v[12:15]\n\t"
        "ds_read_b128 v[68:71], v108 offset:2048\n\t"
        "v_mov_b32_e32 v117, %[cn]\n\t"
        "buffer_store_dword v117, v113, %[sw], 0 offen sc1\n\t"
        "s_add_u32 %[cn], %[cn], 1\n\t"
        "v_mfma_f32_16x16x32_bf16 v[16:19], v[72:75], a[176:179], v[16:19]\n\t"
        "s_add_u32 m0, %[ld], 33792\n\t"
        "s_add_u32 %[t0], %[xo], 16896\n\t"
        "v_mfma_f32_16x16x32_bf16 v[20:23], a[168:171], v[76:79], v[20:23]\n\t"
        "buffer_load_dwordx4 v100, %[sx], %[t0] offen lds\n\t"
        "v_mfma_f32_16x16x32_bf16 v[24:27], a[172:175], v[76:79], v[24:27]\n\t"
        "ds_read_b128 v[72:75], v107 offset:8192\n\t"
        "v_mfma_f32_16x16x32_bf16 v[28:31], v[76:79], a[176:179], v[28:31]\n\t"
        "v_mfma_f32_16x16x32_bf16 v[8:11], a[180:183], v[80:83], v[8:11]\n\t"
        "v_mfma_f32_16x16x32_bf16 v[12:15], a[184:187], v[80:83], v[12:15]\n\t"
        "ds_read_b128 v[76:79], v107 offset:10240\n\t"
        "v_mfma_f32_16x16x32_bf16 v[16:19], v[80:83], a[188:191], v[16:19]\n\t"
        "s_add_u32 m0, %[ld], 40960\n\t"
        "s_add_u32 %[t0], %[xo], 640\n\t"
        "v_mfma_f32_16x16x32_bf16 v[20:23], a[180:183], v[84:87], v[20:23]\n\t"
        "buffer_load_dwordx4 v100, %[sx], %[t0] offen lds\n\t"
        "v_mfma_f32_16x16x32_bf16 v[24:27], a[184:187], v[84:87], v[24:27]\n\t"
        "ds_read_b128 v[80:83], v108 offset:8192\n\t"
        "v_mfma_f32_16x16x32_bf16 v[28:31], v[84:87], a[188:191], v[28:31]\n\t"
        "s_waitcnt lgkmcnt(6)\n\t"
        "v_mfma_f32_16x16x32_bf16 v[8:11], a[192:195], v[56:59], v[8:11]\n\t"
        "v_mfma_f32_16x16x32_bf16 v[12:15], a[196:199], v[56:59], v[12:15]\n\t"
        "ds_read_b128 v[84:87], v108 offset:10240\n\t"
        "v_mfma_f32_16x16x32_bf16 v[16:19], v[56:59], a[200:203], v[16:19]\n\t"
        "s_add_u32 m0, %[ld], 41984\n\t"
        "s_add_u32 %[t0], %[xo], 17024\n\t"
        "s_waitcnt lgkmcnt(6)\n\t"
        "v_mfma_f32_16x16x32_bf16 v[20:23], a[192:195], v[60:63], v[20:23]\n\t"
        "buffer_load_dwordx4 v100, %[sx], %[t0] offen lds\n\t"
        "v_mfma_f32_16x16x32_bf16 v[24:27], a[196:199], v[60:63], v[24:27]\n\t"
        "ds_read_b128 v[56:59], v107 offset:16384\n\t"
        "v_mfma_f32_16x16x32_bf16 v[28:31], v[60:63], a[200:203], v[28:31]\n\t"
        "s_waitcnt lgkmcnt(6)\n\t"
        "v_mfma_f32_16x16x32_bf16 v[8:11], a[204:207], v[64:67], v[8:11]\n\t"
        "v_mfma_f32_16x16x32_bf16 v[12:15], a[208:211], v[64:67], v[12:15]\n\t"
        "ds_read_b128 v[60:63], v107 offset:18432\n\t"
        "v_mfma_f32_16x16x32_bf16 v[16:19], v[64:67], a[212:215], v[16:19]\n\t"
        "s_add_u32 m0, %[ld], 49152\n\t"
        "s_add_u32 %[t0], %[xo], 768\n\t"
        "s_waitcnt lgkmcnt(6)\n\t"
        "v_mfma_f32_16x16x32_bf16 v[20:23], a[204:207], v[68:71], v[20:23]\n\t"
        "buffer_load_dwordx4 v100, %[sx], %[t0] offen lds\n\t"
        "v_mfma_f32_16x16x32_bf16 v[24:27], a[208:211], v[68:71], v[24:27]\n\t"
        "ds_read_b128 v[64:67], v108 offset:16384\n\t"
        "v_mfma_f32_16x16x32_bf16 v[28:31], v[68:71], a[212:215], v[28:31]\n\t"
        "s_waitcnt lgkmcnt(6)\n\t"
        "v_mfma_f32_16x16x32_bf16 v[8:11], a[216:219], v[72:75], v[8:11]\n\t"
        "v_mfma_f32_16x16x32_bf16 v[12:15], a[220:223], v[72:75], v[12:15]\n\t"
        "ds_read_b128 v[68:71], v108 offset:18432\n\t"
        "v_mfma_f32_16x16x32_bf16 v[16:19], v[72:75], a[224:227], v[16:19]\n\t"
        "s_add_u32 m0, %[ld], 50176\n\t"
        "s_add_u32 %[t0], %[xo], 17152\n\t"
        "s_waitcnt lgkmcnt(6)\n\t"
        "v_mfma_f32_16x16x32_bf16 v[20:23], a[216:219], v[76:79], v[20:23]\n\t"
        "buffer_load_dwordx4 v100, %[sx], %[t0] offen lds\n\t"
        "v_mfma_f32_16x16x32_bf16 v[24:27], a[220:223], v[76:79], v[24:27]\n\t"
        "ds_read_b128 v[72:75], v107 offset:24576\n\t"
        "v_mfma_f32_16x16x32_bf16 v[28:31], v[76:79], a[224:227], v[28:31]\n\t"
        "s_waitcnt lgkmcnt(6)\n\t"
        "v_mfma_f32_16x16x32_bf16 v[8:11], a[228:231], v[80:83], v[8:11]\n\t"
        "v_mfma_f32_16x16x32_bf16 v[12:15], a[232:235], v[80:83], v[12:15]\n\t"
        "ds_read_b128 v[76:79], v107 offset:26624\n\t"
        "v_mfma_f32_16x16x32_bf16 v[16:19], v[80:83], a[236:239], v[16:19]\n\t"
        "s_add_u32 m0, %[ld], 57344\n\t"
        "s_add_u32 %[t0], %[xo], 896\n\t"
        "s_waitcnt lgkmcnt(6)\n\t"
        "v_mfma_f32_16x16x32_bf16 v[20:23], a[228:231], v[84:87], v[20:23]\n\t"
        "buffer_load_dwordx4 v100, %[sx], %[t0] offen lds\n\t"
        "v_mfma_f32_16x16x32_bf16 v[24:27], a[232:235], v[84:87], v[24:27]\n\t"
        "ds_read_b128 v[80:83], v108 offset:24576\n\t"
        "v_mfma_f32_16x16x32_bf16 v[28:31], v[84:87], a[236:239], v[28:31]\n\t"
        "s_waitcnt lgkmcnt(6)\n\t"
        "v_mfma_f32_16x16x32_bf16 v[8:11], a[240:243], v[56:59], v[8:11]\n\t"
        "v_mfma_f32_16x16x32_bf16 v[12:15], a[244:247], v[56:59], v[12:15]\n\t"
        "ds_read_b128 v[84:87], v108 offset:26624\n\t"
        "v_mfma_f32_16x16x32_bf16 v[16:19], v[56:59], a[248:251], v[16:19]\n\t"
        "s_add_u32 m0, %[ld], 58368\n\t"
        "s_add_u32 %[t0], %[xo], 17280\n\t"
        "s_waitcnt lgkmcnt(6)\n\t"
        "v_mfma_f32_16x16x32_bf16 v[20:23], a[240:243], v[60:63], v[20:23]\n\t"
        "buffer_load_dwordx4 v100, %[sx], %[t0] offen lds\n\t"
        "v_mfma_f32_16x16x32_bf16 v[24:27], a[244:247], v[60:63], v[24:27]\n\t"
        "v_mfma_f32_16x16x32_bf16 v[28:31], v[60:63], a[248:251], v[28:31]\n\t"
        "s_waitcnt lgkmcnt(5)\n\t"
        "v_mfma_f32_16x16x32_bf16 v[8:11], a[252:255], v[64:67], v[8:11]\n\t"
        "s_waitcnt vmcnt(17) lgkmcnt(0)\n\t"
        "s_barrier\n\t"
        "v_mfma_f32_16x16x32_bf16 v[12:15], v[128:131], v[64:67], v[12:15]\n\t"
        "ds_read_b128 v[56:59], v107 offset:32768\n\t"
        "v_mfma_f32_16x16x32_bf16 v[16:19], v[64:67], v[132:135], v[16:19]\n\t"
        "ds_read_b128 v[60:63], v107 offset:34816\n\t"
        "s_add_u32 m0, %[ld], 65536\n\t"
        "s_add_u32 %[t0], %[xo], 1024\n\t"
        "v_mfma_f32_16x16x32_bf16 v[20:23], a[252:255], v[68:71], v[20:23]\n\t"
        "buffer_load_dwordx4 v100, %[sx], %[t0] offen lds\n\t"
        "v_mfma_f32_16x16x32_bf16 v[24:27], v[128:131], v[68:71], v[24:27]\n\t"
        "ds_read_b128 v[64:67], v108 offset:32768\n\t"
        "v_mfma_f32_16x16x32_bf16 v[28:31], v[68:71], v[132:135], v[28:31]\n\t"
        "v_mfma_f32_16x16x32_bf16 v[8:11], v[136:139], v[72:75], v[8:11]\n\t"
        "v_mfma_f32_16x16x32_bf16 v[12:15], v[140:143], v[72:75], v[12:15]\n\t"
        "ds_read_b128 v[68:71], v108 offset:34816\n\t"
        "v_mfma_f32_16x16x32_bf16 v[16:19], v[72:75], v[144:147], v[16:19]\n\t"
        "s_add_u32 m0, %[ld], 66560\n\t"
        "s_add_u32 %[t0], %[xo], 17408\n\t"
        "v_mfma_f32_16x16x32_bf16 v[20:23], v[136:139], v[76:79], v[20:23]\n\t"
        "buffer_load_dwordx4 v100, %[sx], %[t0] offen lds\n\t"
        "v_mfma_f32_16x16x32_bf16 v[24:27], v[140:143], v[76:79], v[24:27]\n\t"
        "ds_read_b128 v[72:75], v107 offset:40960\n\t"
        "v_mfma_f32_16x16x32_bf16 v[28:31], v[76:79], v[144:147], v[28:31]\n\t"
        "v_mfma_f32_16x16x32_bf16 v[8:11], v[148:151], v[80:83], v[8:11]\n\t"
        "v_mfma_f32_16x16x32_bf16 v[12:15], v[152:155], v[80:83], v[12:15]\n\t"
        "ds_read_b128 v[76:79], v107 offset:43008\n\t"
        "v_mfma_f32_16x16x32_bf16 v[16:19], v[80:83], v[156:159], v[16:19]\n\t"
        "s_add_u32 m0, %[ld], 73728\n\t"
        "s_add_u32 %[t0], %[xo], 1152\n\t"
        "v_mfma_f32_16x16x32_bf16 v[20:23], v[148:151], v[84:87], v[20:23]\n\t"
        "buffer_load_dwordx4 v100, %[sx], %[t0] offen lds\n\t"
        "v_mfma_f32_16x16x32_bf16 v[24:27], v[152:155], v[84:87], v[24:27]\n\t"
        "ds_read_b128 v[80:83], v108 offset:40960\n\t"
        "v_mfma_f32_16x16x32_bf16 v[28:31], v[84:87], v[156:159], v[28:31]\n\t"
        "s_waitcnt lgkmcnt(6)\n\t"
        "v_mfma_f32_16x16x32_bf16 v[8:11], v[160:163], v[56:59], v[8:11]\n\t"
        "v_mfma_f32_16x16x32_bf16 v[12:15], v[164:167], v[56:59], v[12:15]\n\t"
        "ds_read_b128 v[84:87], v108 offset:43008\n\t"
        "v_mfma_f32_16x16x32_bf16 v[16:19], v[56:59], v[168:171], v[16:19]\n\t"
        "s_add_u32 m0, %[ld], 74752\n\t"
        "s_add_u32 %[t0], %[xo], 17536\n\t"
        "s_waitcnt lgkmcnt(6)\n\t"
        "v_mfma_f32_16x16x32_bf16 v[20:23], v[160:163], v[60:63], v[20:23]\n\t"
        "buffer_load_dwordx4 v100, %[sx], %[t0] offen lds\n\t"
        "v_mfma_f32_16x16x32_bf16 v[24:27], v[164:167], v[60:63], v[24:27]\n\t"
        "ds_read_b128 v[56:59], v107 offset:49152\n\t"
        "v_mfma_f32_16x16x32_bf16 v[28:31], v[60:63], v[168:171], v[28:31]\n\t"
        "s_waitcnt lgkmcnt(6)\n\t"
        "v_mfma_f32_16x16x32_bf16 v[8:11], v[172:175], v[64:67], v[8:11]\n\t"
        "v_mfma_f32_16x16x32_bf16 v[12:15], v[176:179], v[64:67], v[12:15]\n\t"
        "ds_read_b128 v[60:63], v107 offset:51200\n\t"
        "v_mfma_f32_16x16x32_bf16 v[16:19], v[64:67], v[180:183], v[16:19]\n\t"
        "s_add_u32 m0, %[ld], 81920\n\t"
        "s_add_u32 %[t0], %[xo], 1280\n\t"
        "s_waitcnt lgkmcnt(6)\n\t"
        "v_mfma_f32_16x16x32_bf16 v[20:23], v[172:175], v[68:71], v[20:23]\n\t"
        "buffer_load_dwordx4 v100, %[sx], %[t0] offen lds\n\t"
        "v_mfma_f32_16x16x32_bf16 v[24:27], v[176:179], v[68:71], v[24:27]\n\t"
        "ds_read_b128 v[64:67], v108 offset:49152\n\t"
        "v_mfma_f32_16x16x32_bf16 v[28:31], v[68:71], v[180:183], v[28:31]\n\t"
        "s_waitcnt lgkmcnt(6)\n\t"
        "v_mfma_f32_16x16x32_bf16 v[8:11], v[184:187], v[72:75], v[8:11]\n\t"
        "v_mfma_f32_16x16x32_bf16 v[12:15], v[188:191], v[72:75], v[12:15]\n\t"
        "ds_read_b128 v[68:71], v108 offset:51200\n\t"
        "v_mfma_f32_16x16x32_bf16 v[16:19], v[72:75], v[192:195], v[16:19]\n\t"
        "s_add_u32 m0, %[ld], 82944\n\t"
        "s_add_u32 %[t0], %[xo], 17664\n\t"
        "s_waitcnt lgkmcnt(6)\n\t"
        "v_mfma_f32_16x16x32_bf16 v[20:23], v[184:187], v[76:79], v[20:23]\n\t"
        "buffer_load_dwordx4 v100, %[sx], %[t0] offen lds\n\t"
        "v_mfma_f32_16x16x32_bf16 v[24:27], v[188:191], v[76:79], v[24:27]\n\t"
        "ds_read_b128 v[72:75], v107 offset:57344\n\t"
        "v_mfma_f32_16x16x32_bf16 v[28:31], v[76:79], v[192:195], v[28:31]\n\t"
        "s_waitcnt lgkmcnt(6)\n\t"
        "v_mfma_f32_16x16x32_bf16 v[8:11], v[196:199], v[80:83], v[8:11]\n\t"
        "v_mfma_f32_16x16x32_bf16 v[12:15], v[200:203], v[80:83], v[12:15]\n\t"
        "ds_read_b128 v[76:79], v107 offset:59392\n\t"
        "v_mfma_f32_16x16x32_bf16 v[16:19], v[80:83], v[204:207], v[16:19]\n\t"
        "s_add_u32 m0, %[ld], 90112\n\t"
        "s_add_u32 %[t0], %[xo], 1408\n\t"
        "s_waitcnt lgkmcnt(6)\n\t"
        "v_mfma_f32_16x16x32_bf16 v[20:23], v[196:199], v[84:87], v[20:23]\n\t"
        "buffer_load_dwordx4 v100, %[sx], %[t0] offen lds\n\t"
        "v_mfma_f32_16x16x32_bf16 v[24:27], v[200:203], v[84:87], v[24:27]\n\t"
        "ds_read_b128 v[80:83], v108 offset:57344\n\t"
        "v_mfma_f32_16x16x32_bf16 v[28:31], v[84:87], v[204:207], v[28:31]\n\t"
        "s_waitcnt lgkmcnt(6)\n\t"
        "v_mfma_f32_16x16x32_bf16 v[8:11], v[208:211], v[56:59], v[8:11]\n\t"
        "v_mfma_f32_16x16x32_bf16 v[12:15], v[212:215], v[56:59], v[12:15]\n\t"
        "ds_read_b128 v[84:87], v108 offset:59392\n\t"
        "v_mfma_f32_16x16x32_bf16 v[16:19], v[56:59], v[216:219], v[16:19]\n\t"
        "s_add_u32 m0, %[ld], 91136\n\t"
        "s_add_u32 %[t0], %[xo], 17792\n\t"
        "s_waitcnt lgkmcnt(6)\n\t"
        "v_mfma_f32_16x16x32_bf16 v[20:23], v[208:211], v[60:63], v[20:23]\n\t"
        "buffer_load_dwordx4 v100, %[sx], %[t0] offen lds\n\t"
        "v_mfma_f32_16x16x32_bf16 v[24:27], v[212:215], v[60:63], v[24:27]\n\t"
        "v_mfma_f32_16x16x32_bf16 v[28:31], v[60:63], v[216:219], v[28:31]\n\t"
        "s_waitcnt lgkmcnt(5)\n\t"
        "v_mfma_f32_16x16x32_bf16 v[8:11], v[220:223], v[64:67], v[8:11]\n\t"
        "s_waitcnt vmcnt(17) lgkmcnt(0)\n\t"
        "s_barrier\n\t"
        "v_mfma_f32_16x16x32_bf16 v[12:15], v[224:227], v[64:67], v[12:15]\n\t"
        "ds_read_b128 v[56:59], v105 offset:0\n\t"
        "v_mfma_f32_16x16x32_bf16 v[16:19], v[64:67], v[228:231], v[16:19]\n\t"
        "ds_read_b128 v[60:63], v105 offset:2048\n\t"
        "s_add_u32 m0, %[ld], 98304\n\t"
        "s_add_u32 %[t0], %[xo], 1536\n\t"
        "v_mfma_f32_16x16x32_bf16 v[20:23], v[220:223], v[68:71], v[20:23]\n\t"
        "buffer_load_dwordx4 v100, %[sx], %[t0] offen lds\n\t"
        "v_mfma_f32_16x16x32_bf16 v[24:27], v[224:227], v[68:71], v[24:27]\n\t"
        "ds_read_b128 v[64:67], v106 offset:0\n\t"
        "v_mfma_f32_16x16x32_bf16 v[28:31], v[68:71], v[228:231], v[28:31]\n\t"
        "v_mfma_f32_16x16x32_bf16 v[8:11], v[232:235], v[72:75], v[8:11]\n\t"
        "v_mfma_f32_16x16x32_bf16 v[12:15], v[236:239], v[72:75], v[12:15]\n\t"
        "ds_read_b128 v[68:71], v106 offset:2048\n\t"
        "v_mfma_f32_16x16x32_bf16 v[16:19], v[72:75], v[240:243], v[16:19]\n\t"
        "s_add_u32 m0, %[ld], 99328\n\t"
        "s_add_u32 %[t0], %[xo], 17920\n\t"
        "v_mfma_f32_16x16x32_bf16 v[20:23], v[232:235], v[76:79], v[20:23]\n\t"
        "buffer_load_dwordx4 v100, %[sx], %[t0] offen lds\n\t"
        "v_mfma_f32_16x16x32_bf16 v[24:27], v[236:239], v[76:79], v[24:27]\n\t"
        "ds_read_b128 v[72:75], v105 offset:8192\n\t"
        "v_mfma_f32_16x16x32_bf16 v[28:31], v[76:79], v[240:243], v[28:31]\n\t"
        "v_mfma_f32_16x16x32_bf16 v[8:11], v[244:247], v[80:83], v[8:11]\n\t"
        "v_mfma_f32_16x16x32_bf16 v[12:15], v[248:251], v[80:83], v[12:15]\n\t"
        "ds_read_b128 v[76:79], v105 offset:10240\n\t"
        "v_mfma_f32_16x16x32_bf16 v[16:19], v[80:83], v[252:255], v[16:19]\n\t"
        "s_add_u32 m0, %[ld], 106496\n\t"
        "s_add_u32 %[t0], %[xo], 1664\n\t"
        "v_mfma_f32_16x16x32_bf16 v[20:23], v[244:247], v[84:87], v[20:23]\n\t"
        "buffer_load_dwordx4 v100, %[sx], %[t0] offen lds\n\t"
        "v_mfma_f32_16x16x32_bf16 v[24:27], v[248:251], v[84:87], v[24:27]\n\t"
        "ds_read_b128 v[80:83], v106 offset:8192\n\t"
        "v_mfma_f32_16x16x32_bf16 v[28:31], v[84:87], v[252:255], v[28:31]\n\t"
        "LG_mid%=:\n\t"
        "s_waitcnt lgkmcnt(6)\n\t"
        "v_mfma_f32_16x16x32_bf16 v[32:35], a[0:3], v[56:59], 0\n\t"
        "v_mfma_f32_16x16x32_bf16 v[36:39], a[4:7], v[56:59], 0\n\t"
        "ds_read_b128 v[84:87], v106 offset:10240\n\t"
        "v_mfma_f32_16x16x32_bf16 v[40:43], v[56:59], a[8:11], 0\n\t"
        "s_add_u32 m0, %[ld], 107520\n\t"
        "s_add_u32 %[t0], %[xo], 18048\n\t"
        "s_waitcnt lgkmcnt(6)\n\t"
        "v_mfma_f32_16x16x32_bf16 v[44:47], a[0:3], v[60:63], 0\n\t"
        "buffer_load_dwordx4 v100, %[sx], %[t0] offen lds\n\t"
        "v_mfma_f32_16x16x32_bf16 v[48:51], a[4:7], v[60:63], 0\n\t"
        "ds_read_b128 v[56:59], v105 offset:16384\n\t"
        "v_mfma_f32_16x16x32_bf16 v[52:55], v[60:63], a[8:11], 0\n\t"
        "s_waitcnt lgkmcnt(6)\n\t"
        "v_mfma_f32_16x16x32_bf16 v[32:35], a[12:15], v[64:67], v[32:35]\n\t"
        "v_cvt_pk_bf16_f32 v88, v8, v9\n\t"
        "v_cvt_pk_bf16_f32 v89, v10, v11\n\t"
        "v_mfma_f32_16x16x32_bf16 v[36:39], a[16:19], v[64:67], v[36:39]\n\t"
        "ds_read_b128 v[60:63], v105 offset:18432\n\t"
        "v_cvt_pk_bf16_f32 v90, v12, v13\n\t"
        "v_mfma_f32_16x16x32_bf16 v[40:43], v[64:67], a[20:23], v[40:43]\n\t"
        "s_add_u32 m0, %[ld], 114688\n\t"
        "s_add_u32 %[t0], %[xo], 1792\n\t"
        "s_waitcnt lgkmcnt(6)\n\t"
        "v_mfma_f32_16x16x32_bf16 v[44:47], a[12:15], v[68:71], v[44:47]\n\t"
        "buffer_load_dwordx4 v100, %[sx], %[t0] offen lds\n\t"
        "v_mfma_f32_16x16x32_bf16 v[48:51], a[16:19], v[68:71], v[48:51]\n\t"
        "ds_read_b128 v[64:67], v106 offset:16384\n\t"
        "v_cvt_pk_bf16_f32 v91, v14, v15\n\t"
        "v_mfma_f32_16x16x32_bf16 v[52:55], v[68:71], a[20:23], v[52:55]\n\t"
        "buffer_store_dwordx4 v[88:91], v109, %[sw], %[so] offen sc1\n\t"
        "s_waitcnt lgkmcnt(6)\n\t"
        "v_mfma_f32_16x16x32_bf16 v[32:35], a[24:27], v[72:75], v[32:35]\n\t"
        "v_cvt_pk_bf16_f32 v92, v20, v21\n\t"
        "v_cvt_pk_bf16_f32 v93, v22, v23\n\t"
        "v_mfma_f32_16x16x32_bf16 v[36:39], a[28:31], v[72:75], v[36:39]\n\t"
        "ds_read_b128 v[68:71], v106 offset:18432\n\t"
        "v_cvt_pk_bf16_f32 v94, v24, v25\n\t"
        "v_mfma_f32_16x16x32_bf16 v[40:43], v[72:75], a[32:35], v[40:43]\n\t"
        "s_add_u32 m0, %[ld], 115712\n\t"
        "s_add_u32 %[t0], %[xo], 18176\n\t"
        "s_waitcnt lgkmcnt(6)\n\t"
        "v_mfma_f32_16x16x32_bf16 v[44:47], a[24:27], v[76:79], v[44:47]\n\t"
        "buffer_load_dwordx4 v100, %[sx], %[t0] offen lds\n\t"
        "v_mfma_f32_16x16x32_bf16 v[48:51], a[28:31], v[76:79], v[48:51]\n\t"
        "ds_read_b128 v[72:75], v105 offset:24576\n\t"
        "v_cvt_pk_bf16_f32 v95, v26, v27\n\t"
        "v_mfma_f32_16x16x32_bf16 v[52:55], v[76:79], a[32:35], v[52:55]\n\t"
        "buffer_store_dwordx4 v[92:95], v110, %[sw], %[so] offen sc1\n\t"
        "s_waitcnt lgkmcnt(6)\n\t"
        "v_mfma_f32_16x16x32_bf16 v[32:35], a[36:39], v[80:83], v[32:35]\n\t"
        "v_cvt_pk_bf16_f32 v96, v16, v17\n\t"
        "v_cvt_pk_bf16_f32 v97, v18, v19\n\t"
        "v_mfma_f32_16x16x32_bf16 v[36:39], a[40:43], v[80:83], v[36:39]\n\t"
        "ds_read_b128 v[76:79], v105 offset:26624\n\t"
        "v_cvt_pk_bf16_f32 v98, v28, v29\n\t"
        "v_mfma_f32_16x16x32_bf16 v[40:43], v[80:83], a[44:47], v[40:43]\n\t"
        "s_add_u32 m0, %[ld], 122880\n\t"
        "s_add_u32 %[t0], %[xo], 1920\n\t"
        "s_waitcnt lgkmcnt(6)\n\t"
        "v_mfma_f32_16x16x32_bf16 v[44:47], a[36:39], v[84:87], v[44:47]\n\t"
        "buffer_load_dwordx4 v100, %[sx], %[t0] offen lds\n\t"
        "v_mfma_f32_16x16x32_bf16 v[48:51], a[40:43], v[84:87], v[48:51]\n\t"
        "ds_read_b128 v[80:83], v106 offset:24576\n\t"
        "v_cvt_pk_bf16_f32 v99, v30, v31\n\t"
        "v_mfma_f32_16x16x32_bf16 v[52:55], v[84:87], a[44:47], v[52:55]\n\t"
        "buffer_store_dwordx4 v[96:99], v111, %[sw], %[so] offen sc1\n\t"
        "s_waitcnt lgkmcnt(6)\n\t"
        "v_mfma_f32_16x16x32_bf16 v[32:35], a[48:51], v[56:59], v[32:35]\n\t"
        "s_lshl_b32 %[so], %[cn], 19\n\t"
        "v_mfma_f32_16x16x32_bf16 v[36:39], a[52:55], v[56:59], v[36:39]\n\t"
        "ds_read_b128 v[84:87], v106 offset:26624\n\t"
        "v_mfma_f32_16x16x32_bf16 v[40:43], v[56:59], a[56:59], v[40:43]\n\t"
        "s_add_u32 m0, %[ld], 123904\n\t"
        "s_add_u32 %[t0], %[xo], 18304\n\t"
        "s_waitcnt lgkmcnt(6)\n\t"
        "v_mfma_f32_16x16x32_bf16 v[44:47], a[48:51], v[60:63], v[44:47]\n\t"
        "buffer_load_dwordx4 v100, %[sx], %[t0] offen lds\n\t"
        "v_mfma_f32_16x16x32_bf16 v[48:51], a[52:55], v[60:63], v[48:51]\n\t"
        "v_mfma_f32_16x16x32_bf16 v[52:55], v[60:63], a[56:59], v[52:55]\n\t"
        "s_waitcnt lgkmcnt(5)\n\t"
        "v_mfma_f32_16x16x32_bf16 v[32:35], a[60:63], v[64:67], v[32:35]\n\t"
        "s_waitcnt vmcnt(19) lgkmcnt(0)\n\t"
        "s_barrier\n\t"
        "s_add_u32 %[xo], %[xo], 0x20000\n\t"
        "v_mfma_f32_16x16x32_bf16 v[36:39], a[64:67], v[64:67], v[36:39]\n\t"
        "ds_read_b128 v[56:59], v105 offset:32768\n\t"
        "v_mfma_f32_16x16x32_bf16 v[40:43], v[64:67], a[68:71], v[40:43]\n\t"
        "ds_read_b128 v[60:63], v105 offset:34816\n\t"
        "s_add_u32 m0, %[ld], 0\n\t"
        "s_add_u32 %[t0], %[xo], 0\n\t"
        "v_mfma_f32_16x16x32_bf16 v[44:47], a[60:63], v[68:71], v[44:47]\n\t"
        "buffer_load_dwordx4 v100, %[sx], %[t0] offen lds\n\t"
        "v_mfma_f32_16x16x32_bf16 v[48:51], a[64:67], v[68:71], v[48:51]\n\t"
        "ds_read_b128 v[64:67], v106 offset:32768\n\t"
        "v_mfma_f32_16x16x32_bf16 v[52:55], v[68:71], a[68:71], v[52:55]\n\t"
        "v_mfma_f32_16x16x32_bf16 v[32:35], a[72:75], v[72:75], v[32:35]\n\t"
        "v_mfma_f32_16x16x32_bf16 v[36:39], a[76:79], v[72:75], v[36:39]\n\t"
        "ds_read_b128 v[68:71], v106 offset:34816\n\t"
        "v_mfma_f32_16x16x32_bf16 v[40:43], v[72:75], a[80:83], v[40:43]\n\t"
        "s_add_u32 m0, %[ld], 1024\n\t"
        "s_add_u32 %[t0], %[xo], 16384\n\t"
        "v_mfma_f32_16x16x32_bf16 v[44:47], a[72:75], v[76:79], v[44:47]\n\t"
        "buffer_load_dwordx4 v100, %[sx], %[t0] offen lds\n\t"
        "v_mfma_f32_16x16x32_bf16 v[48:51], a[76:79], v[76:79], v[48:51]\n\t"
        "ds_read_b128 v[72:75], v105 offset:40960\n\t"
        "v_mfma_f32_16x16x32_bf16 v[52:55], v[76:79], a[80:83], v[52:55]\n\t"
        "v_mfma_f32_16x16x32_bf16 v[32:35], a[84:87], v[80:83], v[32:35]\n\t"
        "v_mfma_f32_16x16x32_bf16 v[36:39], a[88:91], v[80:83], v[36:39]\n\t"
        "ds_read_b128 v[76:79], v105 offset:43008\n\t"
        "v_mfma_f32_16x16x32_bf16 v[40:43], v[80:83], a[92:95], v[40:43]\n\t"
        "s_add_u32 m0, %[ld], 8192\n\t"
        "s_add_u32 %[t0], %[xo], 128\n\t"
        "v_mfma_f32_16x16x32_bf16 v[44:47], a[84:87], v[84:87], v[44:47]\n\t"
        "buffer_load_dwordx4 v100, %[sx], %[t0] offen lds\n\t"
        "v_mfma_f32_16x16x32_bf16 v[48:51], a[88:91], v[84:87], v[48:51]\n\t"
        "ds_read_b128 v[80:83], v106 offset:40960\n\t"
        "v_mfma_f32_16x16x32_bf16 v[52:55], v[84:87], a[92:95], v[52:55]\n\t"
        "s_waitcnt lgkmcnt(6)\n\t"
        "v_mfma_f32_16x16x32_bf16 v[32:35], a[96:99], v[56:59], v[32:35]\n\t"
        "v_mfma_f32_16x16x32_bf16 v[36:39], a[100:103], v[56:59], v[36:39]\n\t"
        "ds_read_b128 v[84:87], v106 offset:43008\n\t"
        "v_mfma_f32_16x16x32_bf16 v[40:43], v[56:59], a[104:107], v[40:43]\n\t"
        "s_add_u32 m0, %[ld], 9216\n\t"
        "s_add_u32 %[t0], %[xo], 16512\n\t"
        "s_waitcnt lgkmcnt(6)\n\t"
        "v_mfma_f32_16x16x32_bf16 v[44:47], a[96:99], v[60:63], v[44:47]\n\t"
        "buffer_load_dwordx4 v100, %[sx], %[t0] offen lds\n\t"
        "v_mfma_f32_16x16x32_bf16 v[48:51], a[100:103], v[60:63], v[48:51]\n\t"
        "ds_read_b128 v[56:59], v105 offset:49152\n\t"
        "v_mfma_f32_16x16x32_bf16 v[52:55], v[60:63], a[104:107], v[52:55]\n\t"
        "s_waitcnt lgkmcnt(6)\n\t"
        "v_mfma_f32_16x16x32_bf16 v[32:35], a[108:111], v[64:67], v[32:35]\n\t"
        "v_mfma_f32_16x16x32_bf16 v[36:39], a[112:115], v[64:67], v[36:39]\n\t"
        "ds_read_b128 v[60:63], v105 offset:51200\n\t"
        "v_mfma_f32_16x16x32_bf16 v[40:43], v[64:67], a[116:119], v[40:43]\n\t"
        "s_add_u32 m0, %[ld], 16384\n\t"
        "s_add_u32 %[t0], %[xo], 256\n\t"
        "s_waitcnt lgkmcnt(6)\n\t"
        "v_mfma_f32_16x16x32_bf16 v[44:47], a[108:111], v[68:71], v[44:47]\n\t"
        "buffer_load_dwordx4 v100, %[sx], %[t0] offen lds\n\t"
        "v_mfma_f32_16x16x32_bf16 v[48:51], a[112:115], v[68:71], v[48:51]\n\t"
        "ds_read_b128 v[64:67], v106 offset:49152\n\t"
        "v_mfma_f32_16x16x32_bf16 v[52:55], v[68:71], a[116:119], v[52:55]\n\t"
        "s_waitcnt lgkmcnt(6)\n\t"
        "v_mfma_f32_16x16x32_bf16 v[32:35], a[120:123], v[72:75], v[32:35]\n\t"
        "v_mfma_f32_16x16x32_bf16 v[36:39], a[124:127], v[72:75], v[36:39]\n\t"
        "ds_read_b128 v[68:71], v106 offset:51200\n\t"
        "v_mfma_f32_16x16x32_bf16 v[40:43], v[72:75], a[128:131], v[40:43]\n\t"
        "s_add_u32 m0, %[ld], 17408\n\t"
        "s_add_u32 %[t0], %[xo], 16640\n\t"
        "s_waitcnt lgkmcnt(6)\n\t"
        "v_mfma_f32_16x16x32_bf16 v[44:47], a[120:123], v[76:79], v[44:47]\n\t"
        "buffer_load_dwordx4 v100, %[sx], %[t0] offen lds\n\t"
        "v_mfma_f32_16x16x32_bf16 v[48:51], a[124:127], v[76:79], v[48:51]\n\t"
        "ds_read_b128 v[72:75], v105 offset:57344\n\t"
        "v_mfma_f32_16x16x32_bf16 v[52:55], v[76:79], a[128:131], v[52:55]\n\t"
        "s_waitcnt lgkmcnt(6)\n\t"
        "v_mfma_f32_16x16x32_bf16 v[32:35], a[132:135], v[80:83], v[32:35]\n\t"
        "v_mfma_f32_16x16x32_bf16 v[36:39], a[136:139], v[80:83], v[36:39]\n\t"
        "ds_read_b128 v[76:79], v105 offset:59392\n\t"
        "v_mfma_f32_16x16x32_bf16 v[40:43], v[80:83], a[140:143], v[40:43]\n\t"
        "s_add_u32 m0, %[ld], 24576\n\t"
        "s_add_u32 %[t0], %[xo], 384\n\t"
        "s_waitcnt lgkmcnt(6)\n\t"
        "v_mfma_f32_16x16x32_bf16 v[44:47], a[132:135], v[84:87], v[44:47]\n\t"
        "buffer_load_dwordx4 v100, %[sx], %[t0] offen lds\n\t"
        "v_mfma_f32_16x16x32_bf16 v[48:51], a[136:139], v[84:87], v[48:51]\n\t"
        "ds_read_b128 v[80:83], v106 offset:57344\n\t"
        "v_mfma_f32_16x16x32_bf16 v[52:55], v[84:87], a[140:143], v[52:55]\n\t"
        "s_waitcnt lgkmcnt(6)\n\t"
        "v_mfma_f32_16x16x32_bf16 v[32:35], a[144:147], v[56:59], v[32:35]\n\t"
        "v_mfma_f32_16x16x32_bf16 v[36:39], a[148:151], v[56:59], v[36:39]\n\t"
        "ds_read_b128 v[84:87], v106 offset:59392\n\t"
        "v_mfma_f32_16x16x32_bf16 v[40:43], v[56:59], a[152:155], v[40:43]\n\t"
        "s_add_u32 m0, %[ld], 25600\n\t"
        "s_add_u32 %[t0], %[xo], 16768\n\t"
        "s_waitcnt lgkmcnt(6)\n\t"
        "v_mfma_f32_16x16x32_bf16 v[44:47], a[144:147], v[60:63], v[44:47]\n\t"
        "buffer_load_dwordx4 v100, %[sx], %[t0] offen lds\n\t"
        "v_mfma_f32_16x16x32_bf16 v[48:51], a[148:151], v[60:63], v[48:51]\n\t"
        "v_mfma_f32_16x16x32_bf16 v[52:55], v[60:63], a[152:155], v[52:55]\n\t"
        "s_waitcnt lgkmcnt(5)\n\t"
        "v_mfma_f32_16x16x32_bf16 v[32:35], a[156:159], v[64:67], v[32:35]\n\t"
        "s_waitcnt vmcnt(9) lgkmcnt(0)\n\t"
        "s_barrier\n\t"
        "v_mfma_f32_16x16x32_bf16 v[36:39], a[160:163], v[64:67], v[36:39]\n\t"
        "ds_read_b128 v[56:59], v107 offset:0\n\t"
        "v_mfma_f32_16x16x32_bf16 v[40:43], v[64:67], a[164:167], v[40:43]\n\t"
        "ds_read_b128 v[60:63], v107 offset:2048\n\t"
        "s_add_u32 m0, %[ld], 32768\n\t"
        "s_add_u32 %[t0], %[xo], 512\n\t"
        "v_mfma_f32_16x16x32_bf16 v[44:47], a[156:159], v[68:71], v[44:47]\n\t"
        "buffer_load_dwordx4 v100, %[sx], %[t0] offen lds\n\t"
        "v_mfma_f32_16x16x32_bf16 v[48:51], a[160:163], v[68:71], v[48:51]\n\t"
        "ds_read_b128 v[64:67], v108 offset:0\n\t"
        "v_mfma_f32_16x16x32_bf16 v[52:55], v[68:71], a[164:167], v[52:55]\n\t"
        "v_mfma_f32_16x16x32_bf16 v[32:35], a[168:171], v[72:75], v[32:35]\n\t"
        "v_mfma_f32_16x16x32_bf16 v[36:39], a[172:175], v[72:75], v[36:39]\n\t"
        "ds_read_b128 v[68:71], v108 offset:2048\n\t"
        "v_mov_b32_e32 v117, %[cn]\n\t"
        "buffer_store_dword v117, v113, %[sw], 0 offen sc1\n\t"
        "s_add_u32 %[cn], %[cn], 1\n\t"
        "v_mfma_f32_16x16x32_bf16 v[40:43], v[72:75], a[176:179], v[40:43]\n\t"
        "s_add_u32 m0, %[ld], 33792\n\t"
        "s_add_u32 %[t0], %[xo], 16896\n\t"
        "v_mfma_f32_16x16x32_bf16 v[44:47], a[168:171], v[76:79], v[44:47]\n\t"
        "buffer_load_dwordx4 v100, %[sx], %[t0] offen lds\n\t"
        "v_mfma_f32_16x16x32_bf16 v[48:51], a[172:175], v[76:79], v[48:51]\n\t"
        "ds_read_b128 v[72:75], v107 offset:8192\n\t"
        "v_mfma_f32_16x16x32_bf16 v[52:55], v[76:79], a[176:179], v[52:55]\n\t"
        "v_mfma_f32_16x16x32_bf16 v[32:35], a[180:183], v[80:83], v[32:35]\n\t"
        "v_mfma_f32_16x16x32_bf16 v[36:39], a[184:187], v[80:83], v[36:39]\n\t"
        "ds_read_b128 v[76:79], v107 offset:10240\n\t"
        "v_mfma_f32_16x16x32_bf16 v[40:43], v[80:83], a[188:191], v[40:43]\n\t"
        "s_add_u32 m0, %[ld], 40960\n\t"
        "s_add_u32 %[t0], %[xo], 640\n\t"
        "v_mfma_f32_16x16x32_bf16 v[44:47], a[180:183], v[84:87], v[44:47]\n\t"
        "buffer_load_dwordx4 v100, %[sx], %[t0] offen lds\n\t"
        "v_mfma_f32_16x16x32_bf16 v[48:51], a[184:187], v[84:87], v[48:51]\n\t"
        "ds_read_b128 v[80:83], v108 offset:8192\n\t"
        "v_mfma_f32_16x16x32_bf16 v[52:55], v[84:87], a[188:191], v[52:55]\n\t"
        "s_waitcnt lgkmcnt(6)\n\t"
        "v_mfma_f32_16x16x32_bf16 v[32:35], a[192:195], v[56:59], v[32:35]\n\t"
        "v_mfma_f32_16x16x32_bf16 v[36:39], a[196:199], v[56:59], v[36:39]\n\t"
        "ds_read_b128 v[84:87], v108 offset:10240\n\t"
        "v_mfma_f32_16x16x32_bf16 v[40:43], v[56:59], a[200:203], v[40:43]\n\t"
        "s_add_u32 m0, %[ld], 41984\n\t"
        "s_add_u32 %[t0], %[xo], 17024\n\t"
        "s_waitcnt lgkmcnt(6)\n\t"
        "v_mfma_f32_16x16x32_bf16 v[44:47], a[192:195], v[60:63], v[44:47]\n\t"
        "buffer_load_dwordx4 v100, %[sx], %[t0] offen lds\n\t"
        "v_mfma_f32_16x16x32_bf16 v[48:51], a[196:199], v[60:63], v[48:51]\n\t"
        "ds_read_b128 v[56:59], v107 offset:16384\n\t"
        "v_mfma_f32_16x16x32_bf16 v[52:55], v[60:63], a[200:203], v[52:55]\n\t"
        "s_waitcnt lgkmcnt(6)\n\t"
        "v_mfma_f32_16x16x32_bf16 v[32:35], a[204:207], v[64:67], v[32:35]\n\t"
        "v_mfma_f32_16x16x32_bf16 v[36:39], a[208:211], v[64:67], v[36:39]\n\t"
        "ds_read_b128 v[60:63], v107 offset:18432\n\t"
        "v_mfma_f32_16x16x32_bf16 v[40:43], v[64:67], a[212:215], v[40:43]\n\t"
        "s_add_u32 m0, %[ld], 49152\n\t"
        "s_add_u32 %[t0], %[xo], 768\n\t"
        "s_waitcnt lgkmcnt(6)\n\t"
        "v_mfma_f32_16x16x32_bf16 v[44:47], a[204:207], v[68:71], v[44:47]\n\t"
        "buffer_load_dwordx4 v100, %[sx], %[t0] offen lds\n\t"
        "v_mfma_f32_16x16x32_bf16 v[48:51], a[208:211], v[68:71], v[48:51]\n\t"
        "ds_read_b128 v[64:67], v108 offset:16384\n\t"
        "v_mfma_f32_16x16x32_bf16 v[52:55], v[68:71], a[212:215], v[52:55]\n\t"
        "s_waitcnt lgkmcnt(6)\n\t"
        "v_mfma_f32_16x16x32_bf16 v[32:35], a[216:219], v[72:75], v[32:35]\n\t"
        "v_mfma_f32_16x16x32_bf16 v[36:39], a[220:223], v[72:75], v[36:39]\n\t"
        "ds_read_b128 v[68:71], v108 offset:18432\n\t"
        "v_mfma_f32_16x16x32_bf16 v[40:43], v[72:75], a[224:227], v[40:43]\n\t"
        "s_add_u32 m0, %[ld], 50176\n\t"
        "s_add_u32 %[t0], %[xo], 17152\n\t"
        "s_waitcnt lgkmcnt(6)\n\t"
        "v_mfma_f32_16x16x32_bf16 v[44:47], a[216:219], v[76:79], v[44:47]\n\t"
        "buffer_load_dwordx4 v100, %[sx], %[t0] offen lds\n\t"
        "v_mfma_f32_16x16x32_bf16 v[48:51], a[220:223], v[76:79], v[48:51]\n\t"
        "ds_read_b128 v[72:75], v107 offset:24576\n\t"
        "v_mfma_f32_16x16x32_bf16 v[52:55], v[76:79], a[224:227], v[52:55]\n\t"
        "s_waitcnt lgkmcnt(6)\n\t"
        "v_mfma_f32_16x16x32_bf16 v[32:35], a[228:231], v[80:83], v[32:35]\n\t"
        "v_mfma_f32_16x16x32_bf16 v[36:39], a[232:235], v[80:83], v[36:39]\n\t"
        "ds_read_b128 v[76:79], v107 offset:26624\n\t"
        "v_mfma_f32_16x16x32_bf16 v[40:43], v[80:83], a[236:239], v[40:43]\n\t"
        "s_add_u32 m0, %[ld], 57344\n\t"
        "s_add_u32 %[t0], %[xo], 896\n\t"
        "s_waitcnt lgkmcnt(6)\n\t"
        "v_mfma_f32_16x16x32_bf16 v[44:47], a[228:231], v[84:87], v[44:47]\n\t"
        "buffer_load_dwordx4 v100, %[sx], %[t0] offen lds\n\t"
        "v_mfma_f32_16x16x32_bf16 v[48:51], a[232:235], v[84:87], v[48:51]\n\t"
        "ds_read_b128 v[80:83], v108 offset:24576\n\t"
        "v_mfma_f32_16x16x32_bf16 v[52:55], v[84:87], a[236:239], v[52:55]\n\t"
        "s_waitcnt lgkmcnt(6)\n\t"
        "v_mfma_f32_16x16x32_bf16 v[32:35], a[240:243], v[56:59], v[32:35]\n\t"
        "v_mfma_f32_16x16x32_bf16 v[36:39], a[244:247], v[56:59], v[36:39]\n\t"
        "ds_read_b128 v[84:87], v108 offset:26624\n\t"
        "v_mfma_f32_16x16x32_bf16 v[40:43], v[56:59], a[248:251], v[40:43]\n\t"
        "s_add_u32 m0, %[ld], 58368\n\t"
        "s_add_u32 %[t0], %[xo], 17280\n\t"
        "s_waitcnt lgkmcnt(6)\n\t"
        "v_mfma_f32_16x16x32_bf16 v[44:47], a[240:243], v[60:63], v[44:47]\n\t"
        "buffer_load_dwordx4 v100, %[sx], %[t0] offen lds\n\t"
        "v_mfma_f32_16x16x32_bf16 v[48:51], a[244:247], v[60:63], v[48:51]\n\t"
        "v_mfma_f32_16x16x32_bf16 v[52:55], v[60:63], a[248:251], v[52:55]\n\t"
        "s_waitcnt lgkmcnt(5)\n\t"
        "v_mfma_f32_16x16x32_bf16 v[32:35], a[252:255], v[64:67], v[32:35]\n\t"
        "s_waitcnt vmcnt(17) lgkmcnt(0)\n\t"
        "s_barrier\n\t"
        "v_mfma_f32_16x16x32_bf16 v[36:39], v[128:131], v[64:67], v[36:39]\n\t"
        "ds_read_b128 v[56:59], v107 offset:32768\n\t"
        "v_mfma_f32_16x16x32_bf16 v[40:43], v[64:67], v[132:135], v[40:43]\n\t"
        "ds_read_b128 v[60:63], v107 offset:34816\n\t"
        "s_add_u32 m0, %[ld], 65536\n\t"
        "s_add_u32 %[t0], %[xo], 1024\n\t"
        "v_mfma_f32_16x16x32_bf16 v[44:47], a[252:255], v[68:71], v[44:47]\n\t"
        "buffer_load_dwordx4 v100, %[sx], %[t0] offen lds\n\t"
        "v_mfma_f32_16x16x32_bf16 v[48:51], v[128:131], v[68:71], v[48:51]\n\t"
        "ds_read_b128 v[64:67], v108 offset:32768\n\t"
        "v_mfma_f32_16x16x32_bf16 v[52:55], v[68:71], v[132:135], v[52:55]\n\t"
        "v_mfma_f32_16x16x32_bf16 v[32:35], v[136:139], v[72:75], v[32:35]\n\t"
        "v_mfma_f32_16x16x32_bf16 v[36:39], v[140:143], v[72:75], v[36:39]\n\t"
        "ds_read_b128 v[68:71], v108 offset:34816\n\t"
        "v_mfma_f32_16x16x32_bf16 v[40:43], v[72:75], v[144:147], v[40:43]\n\t"
        "s_add_u32 m0, %[ld], 66560\n\t"
        "s_add_u32 %[t0], %[xo], 17408\n\t"
        "v_mfma_f32_16x16x32_bf16 v[44:47], v[136:139], v[76:79], v[44:47]\n\t"
        "buffer_load_dwordx4 v100, %[sx], %[t0] offen lds\n\t"
        "v_mfma_f32_16x16x32_bf16 v[48:51], v[140:143], v[76:79], v[48:51]\n\t"
        "ds_read_b128 v[72:75], v107 offset:40960\n\t"
        "v_mfma_f32_16x16x32_bf16 v[52:55], v[76:79], v[144:147], v[52:55]\n\t"
        "v_mfma_f32_16x16x32_bf16 v[32:35], v[148:151], v[80:83], v[32:35]\n\t"
        "v_mfma_f32_16x16x32_bf16 v[36:39], v[152:155], v[80:83], v[36:39]\n\t"
        "ds_read_b128 v[76:79], v107 offset:43008\n\t"
        "v_mfma_f32_16x16x32_bf16 v[40:43], v[80:83], v[156:159], v[40:43]\n\t"
        "s_add_u32 m0, %[ld], 73728\n\t"
        "s_add_u32 %[t0], %[xo], 1152\n\t"
        "v_mfma_f32_16x16x32_bf16 v[44:47], v[148:151], v[84:87], v[44:47]\n\t"
        "buffer_load_dwordx4 v100, %[sx], %[t0] offen lds\n\t"
        "v_mfma_f32_16x16x32_bf16 v[48:51], v[152:155], v[84:87], v[48:51]\n\t"
        "ds_read_b128 v[80:83], v108 offset:40960\n\t"
        "v_mfma_f32_16x16x32_bf16 v[52:55], v[84:87], v[156:159], v[52:55]\n\t"
        "s_waitcnt lgkmcnt(6)\n\t"
        "v_mfma_f32_16x16x32_bf16 v[32:35], v[160:163], v[56:59], v[32:35]\n\t"
        "v_mfma_f32_16x16x32_bf16 v[36:39], v[164:167], v[56:59], v[36:39]\n\t"
        "ds_read_b128 v[84:87], v108 offset:43008\n\t"
        "v_mfma_f32_16x16x32_bf16 v[40:43], v[56:59], v[168:171], v[40:43]\n\t"
        "s_add_u32 m0, %[ld], 74752\n\t"
        "s_add_u32 %[t0], %[xo], 17536\n\t"
        "s_waitcnt lgkmcnt(6)\n\t"
        "v_mfma_f32_16x16x32_bf16 v[44:47], v[160:163], v[60:63], v[44:47]\n\t"
        "buffer_load_dwordx4 v100, %[sx], %[t0] offen lds\n\t"
        "v_mfma_f32_16x16x32_bf16 v[48:51], v[164:167], v[60:63], v[48:51]\n\t"
        "ds_read_b128 v[56:59], v107 offset:49152\n\t"
        "v_mfma_f32_16x16x32_bf16 v[52:55], v[60:63], v[168:171], v[52:55]\n\t"
        "s_waitcnt lgkmcnt(6)\n\t"
        "v_mfma_f32_16x16x32_bf16 v[32:35], v[172:175], v[64:67], v[32:35]\n\t"
        "v_mfma_f32_16x16x32_bf16 v[36:39], v[176:179], v[64:67], v[36:39]\n\t"
        "ds_read_b128 v[60:63], v107 offset:51200\n\t"
        "v_mfma_f32_16x16x32_bf16 v[40:43], v[64:67], v[180:183], v[40:43]\n\t"
        "s_add_u32 m0, %[ld], 81920\n\t"
        "s_add_u32 %[t0], %[xo], 1280\n\t"
        "s_waitcnt lgkmcnt(6)\n\t"
        "v_mfma_f32_16x16x32_bf16 v[44:47], v[172:175], v[68:71], v[44:47]\n\t"
        "buffer_load_dwordx4 v100, %[sx], %[t0] offen lds\n\t"
        "v_mfma_f32_16x16x32_bf16 v[48:51], v[176:179], v[68:71], v[48:51]\n\t"
        "ds_read_b128 v[64:67], v108 offset:49152\n\t"
        "v_mfma_f32_16x16x32_bf16 v[52:55], v[68:71], v[180:183], v[52:55]\n\t"
        "s_waitcnt lgkmcnt(6)\n\t"
        "v_mfma_f32_16x16x32_bf16 v[32:35], v[184:187], v[72:75], v[32:35]\n\t"
        "v_mfma_f32_16x16x32_bf16 v[36:39], v[188:191], v[72:75], v[36:39]\n\t"
        "ds_read_b128 v[68:71], v108 offset:51200\n\t"
        "v_mfma_f32_16x16x32_bf16 v[40:43], v[72:75], v[192:195], v[40:43]\n\t"
        "s_add_u32 m0, %[ld], 82944\n\t"
        "s_add_u32 %[t0], %[xo], 17664\n\t"
        "s_waitcnt lgkmcnt(6)\n\t"
        "v_mfma_f32_16x16x32_bf16 v[44:47], v[184:187], v[76:79], v[44:47]\n\t"
        "buffer_load_dwordx4 v100, %[sx], %[t0] offen lds\n\t"
        "v_mfma_f32_16x16x32_bf16 v[48:51], v[188:191], v[76:79], v[48:51]\n\t"
        "ds_read_b128 v[72:75], v107 offset:57344\n\t"
        "v_mfma_f32_16x16x32_bf16 v[52:55], v[76:79], v[192:195], v[52:55]\n\t"
        "s_waitcnt lgkmcnt(6)\n\t"
        "v_mfma_f32_16x16x32_bf16 v[32:35], v[196:199], v[80:83], v[32:35]\n\t"
        "v_mfma_f32_16x16x32_bf16 v[36:39], v[200:203], v[80:83], v[36:39]\n\t"
        "ds_read_b128 v[76:79], v107 offset:59392\n\t"
        "v_mfma_f32_16x16x32_bf16 v[40:43], v[80:83], v[204:207], v[40:43]\n\t"
        "s_add_u32 m0, %[ld], 90112\n\t"
        "s_add_u32 %[t0], %[xo], 1408\n\t"
        "s_waitcnt lgkmcnt(6)\n\t"
        "v_mfma_f32_16x16x32_bf16 v[44:47], v[196:199], v[84:87], v[44:47]\n\t"
        "buffer_load_dwordx4 v100, %[sx], %[t0] offen lds\n\t"
        "v_mfma_f32_16x16x32_bf16 v[48:51], v[200:203], v[84:87], v[48:51]\n\t"
        "ds_read_b128 v[80:83], v108 offset:57344\n\t"
        "v_mfma_f32_16x16x32_bf16 v[52:55], v[84:87], v[204:207], v[52:55]\n\t"
        "s_waitcnt lgkmcnt(6)\n\t"
        "v_mfma_f32_16x16x32_bf16 v[32:35], v[208:211], v[56:59], v[32:35]\n\t"
        "v_mfma_f32_16x16x32_bf16 v[36:39], v[212:215], v[56:59], v[36:39]\n\t"
        "ds_read_b128 v[84:87], v108 offset:59392\n\t"
        "v_mfma_f32_16x16x32_bf16 v[40:43], v[56:59], v[216:219], v[40:43]\n\t"
        "s_add_u32 m0, %[ld], 91136\n\t"
        "s_add_u32 %[t0], %[xo], 17792\n\t"
        "s_waitcnt lgkmcnt(6)\n\t"
        "v_mfma_f32_16x16x32_bf16 v[44:47], v[208:211], v[60:63], v[44:47]\n\t"
        "buffer_load_dwordx4 v100, %[sx], %[t0] offen lds\n\t"
        "v_mfma_f32_16x16x32_bf16 v[48:51], v[212:215], v[60:63], v[48:51]\n\t"
        "v_mfma_f32_16x16x32_bf16 v[52:55], v[60:63], v[216:219], v[52:55]\n\t"
        "s_waitcnt lgkmcnt(5)\n\t"
        "v_mfma_f32_16x16x32_bf16 v[32:35], v[220:223], v[64:67], v[32:35]\n\t"
        "s_waitcnt vmcnt(17) lgkmcnt(0)\n\t"
        "s_barrier\n\t"
        "v_mfma_f32_16x16x32_bf16 v[36:39], v[224:227], v[64:67], v[36:39]\n\t"
        "ds_read_b128 v[56:59], v105 offset:0\n\t"
        "v_mfma_f32_16x16x32_bf16 v[40:43], v[64:67], v[228:231], v[40:43]\n\t"
        "ds_read_b128 v[60:63], v105 offset:2048\n\t"
        "s_add_u32 m0, %[ld], 98304\n\t"
        "s_add_u32 %[t0], %[xo], 1536\n\t"
        "v_mfma_f32_16x16x32_bf16 v[44:47], v[220:223], v[68:71], v[44:47]\n\t"
        "buffer_load_dwordx4 v100, %[sx], %[t0] offen lds\n\t"
        "v_mfma_f32_16x16x32_bf16 v[48:51], v[224:227], v[68:71], v[48:51]\n\t"
        "ds_read_b128 v[64:67], v106 offset:0\n\t"
        "v_mfma_f32_16x16x32_bf16 v[52:55], v[68:71], v[228:231], v[52:55]\n\t"
        "v_mfma_f32_16x16x32_bf16 v[32:35], v[232:235], v[72:75], v[32:35]\n\t"
        "v_mfma_f32_16x16x32_bf16 v[36:39], v[236:239], v[72:75], v[36:39]\n\t"
        "ds_read_b128 v[68:71], v106 offset:2048\n\t"
        "v_mfma_f32_16x16x32_bf16 v[40:43], v[72:75], v[240:243], v[40:43]\n\t"
        "s_add_u32 m0, %[ld], 99328\n\t"
        "s_add_u32 %[t0], %[xo], 17920\n\t"
        "v_mfma_f32_16x16x32_bf16 v[44:47], v[232:235], v[76:79], v[44:47]\n\t"
        "buffer_load_dwordx4 v100, %[sx], %[t0] offen lds\n\t"
        "v_mfma_f32_16x16x32_bf16 v[48:51], v[236:239], v[76:79], v[48:51]\n\t"
        "ds_read_b128 v[72:75], v105 offset:8192\n\t"
        "v_mfma_f32_16x16x32_bf16 v[52:55], v[76:79], v[240:243], v[52:55]\n\t"
        "v_mfma_f32_16x16x32_bf16 v[32:35], v[244:247], v[80:83], v[32:35]\n\t"
        "v_mfma_f32_16x16x32_bf16 v[36:39], v[248:251], v[80:83], v[36:39]\n\t"
        "ds_read_b128 v[76:79], v105 offset:10240\n\t"
        "v_mfma_f32_16x16x32_bf16 v[40:43], v[80:83], v[252:255], v[40:43]\n\t"
        "s_add_u32 m0, %[ld], 106496\n\t"
        "s_add_u32 %[t0], %[xo], 1664\n\t"
        "v_mfma_f32_16x16x32_bf16 v[44:47], v[244:247], v[84:87], v[44:47]\n\t"
        "buffer_load_dwordx4 v100, %[sx], %[t0] offen lds\n\t"
        "v_mfma_f32_16x16x32_bf16 v[48:51], v[248:251], v[84:87], v[48:51]\n\t"
        "ds_read_b128 v[80:83], v106 offset:8192\n\t"
        "v_mfma_f32_16x16x32_bf16 v[52:55], v[84:87], v[252:255], v[52:55]\n\t"
        "s_cmp_lt_u32 %[cn], 32\n\t"
        "s_cbranch_scc1 LG_loop%=\n\t"
        "s_nop 7\n\t"
        "v_cvt_pk_bf16_f32 v88, v32, v33\n\t"
        "v_cvt_pk_bf16_f32 v89, v34, v35\n\t"
        "v_cvt_pk_bf16_f32 v90, v36, v37\n\t"
        "v_cvt_pk_bf16_f32 v91, v38, v39\n\t"
        "buffer_store_dwordx4 v[88:91], v109, %[sw], %[so] offen sc1\n\t"
        "v_cvt_pk_bf16_f32 v92, v44, v45\n\t"
        "v_cvt_pk_bf16_f32 v93, v46, v47\n\t"
        "v_cvt_pk_bf16_f32 v94, v48, v49\n\t"
        "v_cvt_pk_bf16_f32 v95, v50, v51\n\t"
        "buffer_store_dwordx4 v[92:95], v110, %[sw], %[so] offen sc1\n\t"
        "v_cvt_pk_bf16_f32 v96, v40, v41\n\t"
        "v_cvt_pk_bf16_f32 v97, v42, v43\n\t"
        "v_cvt_pk_bf16_f32 v98, v52, v53\n\t"
        "v_cvt_pk_bf16_f32 v99, v54, v55\n\t"
        "buffer_store_dwordx4 v[96:99], v111, %[sw], %[so] offen sc1\n\t"
        "s_waitcnt vmcnt(0) lgkmcnt(0)\n\t"
        "v_mov_b32_e32 v117, %[cn]\n\t"
        "buffer_store_dword v117, v113, %[sw], 0 offen sc1\n\t"
        "s_barrier\n\t"
        "LG_end%=:\n\t"
        ".Lk_end_G:\n\t"
        : [t0] "=&s"(t0), [xo] "=&s"(xo), [so] "=&s"(so), [cn] "=&s"(cn), [ts] "=&s"(ts), [ta] "=&s"(ta), [tb] "=&s"(tb), [tc] "=&s"(tc), [td] "=&s"(td)
        : [sx] "s"(sx), [sq] "s"(sq), [sv] "s"(sv), [sw] "s"(sw), [ld] "s"(lb + w * 2048),
          "{v100}"((16 * w + (l >> 3)) * 2048 + (((l & 7) ^ (l >> 3)) << 4)), "{v101}"((8 * w + (l >> 3)) * 8192 + h * 256 + (c & 1) * 128 + (l & 7) * 16),
          "{v112}"((16 * w + (l >> 2)) * 8192 + h * 256 + cv * 64 + (l & 3) * 16), "{v102}"(lb + 131072 + w * 1024 + ((l * 16) ^ ((l & 16) << 2))), "{v116}"(lb + 131072 + w * 1024 + ((l * 16) ^ (l & 32))),
          "{v103}"(lr + rr * 128 + ((tn ^ (n >> 3)) * 64) + (n & 3) * 16), "{v104}"(lr + 8192 + rr * 64 + ((tn ^ (g & 1)) * 32) + (n & 3) * 8), "{v105}"(lx), "{v106}"(lx ^ 64u), "{v107}"(lx + 65536), "{v108}"((lx ^ 64u) + 65536),
          "{v109}"(aq), "{v110}"(aq + 4096), "{v111}"(W_VF + (tm * 32 + h) * 8192 + (2 * cv + tn) * 1024 + l * 16), "{v113}"(l == 0 ? F_KVQ + h * 1024 + (c * 4 + w) * 64 : OOB), "{v[114:115]}"(tp)
        : "memory", "scc", "m0", "v8", "v9", "v10", "v11", "v12", "v13", "v14", "v15", "v16", "v17", "v18", "v19", "v20", "v21", "v22", "v23", "v24", "v25", "v26", "v27", "v28", "v29", "v30", "v31", "v32", "v33", "v34", "v35", "v36", "v37", "v38", "v39", "v40", "v41", "v42", "v43", "v44", "v45", "v46", "v47", "v48", "v49", "v50", "v51", "v52", "v53", "v54", "v55", "v56", "v57", "v58", "v59", "v60", "v61", "v62", "v63", "v64", "v65", "v66", "v67", "v68", "v69", "v70", "v71", "v72", "v73", "v74", "v75", "v76", "v77", "v78", "v79", "v80", "v81", "v82", "v83", "v84", "v85", "v86", "v87", "v88", "v89", "v90", "v91", "v92", "v93", "v94", "v95", "v96", "v97", "v98", "v99", "v117", "v118", "v119", "v120", "v121", "v122", "v123", "v124", "v125", "v126", "v127", "v128", "v129", "v130", "v131", "v132", "v133", "v134", "v135", "v136", "v137", "v138", "v139", "v140", "v141", "v142", "v143", "v144", "v145", "v146", "v147", "v148", "v149", "v150", "v151", "v152", "v153", "v154", "v155", "v156", "v157", "v158", "v159", "v160", "v161", "v162", "v163", "v164", "v165", "v166", "v167", "v168", "v169", "v170", "v171", "v172", "v173", "v174", "v175", "v176", "v177", "v178", "v179", "v180", "v181", "v182", "v183", "v184", "v185", "v186", "v187", "v188", "v189", "v190", "v191", "v192", "v193", "v194", "v195", "v196", "v197", "v198", "v199", "v200", "v201", "v202", "v203", "v204", "v205", "v206", "v207", "v208", "v209", "v210", "v211", "v212", "v213", "v214", "v215", "v216", "v217", "v218", "v219", "v220", "v221", "v222", "v223", "v224", "v225", "v226", "v227", "v228", "v229", "v230", "v231", "v232", "v233", "v234", "v235", "v236", "v237", "v238", "v239", "v240", "v241", "v242", "v243", "v244", "v245", "v246", "v247", "v248", "v249", "v250", "v251", "v252", "v253", "v254", "v255", "a0", "a1", "a2", "a3", "a4", "a5", "a6", "a7", "a8", "a9", "a10", "a11", "a12", "a13", "a14", "a15", "a16", "a17", "a18", "a19", "a20", "a21", "a22", "a23", "a24", "a25", "a26", "a27", "a28", "a29", "a30", "a31", "a32", "a33", "a34", "a35", "a36", "a37", "a38", "a39", "a40", "a41", "a42", "a43", "a44", "a45", "a46", "a47", "a48", "a49", "a50", "a51", "a52", "a53", "a54", "a55", "a56", "a57", "a58", "a59", "a60", "a61", "a62", "a63", "a64", "a65", "a66", "a67", "a68", "a69", "a70", "a71", "a72", "a73", "a74", "a75", "a76", "a77", "a78", "a79", "a80", "a81", "a82", "a83", "a84", "a85", "a86", "a87", "a88", "a89", "a90", "a91", "a92", "a93", "a94", "a95", "a96", "a97", "a98", "a99", "a100", "a101", "a102", "a103", "a104", "a105", "a106", "a107", "a108", "a109", "a110", "a111", "a112", "a113", "a114", "a115", "a116", "a117", "a118", "a119", "a120", "a121", "a122", "a123", "a124", "a125", "a126", "a127", "a128", "a129", "a130", "a131", "a132", "a133", "a134", "a135", "a136", "a137", "a138", "a139", "a140", "a141", "a142", "a143", "a144", "a145", "a146", "a147", "a148", "a149", "a150", "a151", "a152", "a153", "a154", "a155", "a156", "a157", "a158", "a159", "a160", "a161", "a162", "a163", "a164", "a165", "a166", "a167", "a168", "a169", "a170", "a171", "a172", "a173", "a174", "a175", "a176", "a177", "a178", "a179", "a180", "a181", "a182", "a183", "a184", "a185", "a186", "a187", "a188", "a189", "a190", "a191", "a192", "a193", "a194", "a195", "a196", "a197", "a198", "a199", "a200", "a201", "a202", "a203", "a204", "a205", "a206", "a207", "a208", "a209", "a210", "a211", "a212", "a213", "a214", "a215", "a216", "a217", "a218", "a219", "a220", "a221", "a222", "a223", "a224", "a225", "a226", "a227", "a228", "a229", "a230", "a231", "a232", "a233", "a234", "a235", "a236", "a237", "a238", "a239", "a240", "a241", "a242", "a243", "a244", "a245", "a246", "a247", "a248", "a249", "a250", "a251", "a252", "a253", "a254", "a255");
}

__device__ __forceinline__ unsigned min16(unsigned d) {
    d = min(d, (unsigned)__builtin_amdgcn_update_dpp((int)d, (int)d, 0xB1, 0xF, 0xF, false)); d = min(d, (unsigned)__builtin_amdgcn_update_dpp((int)d, (int)d, 0x4E, 0xF, 0xF, false));
    d = min(d, (unsigned)__builtin_amdgcn_update_dpp((int)d, (int)d, 0x124, 0xF, 0xF, false)); d = min(d, (unsigned)__builtin_amdgcn_update_dpp((int)d, (int)d, 0x128, 0xF, 0xF, false));
    return __builtin_amdgcn_readfirstlane(d); }
constexpr int OP_P = 72;
__device__ __forceinline__ void fin_loads(v4u (&fp)[8], v2u& fa0, v2u& fa1, unsigned& fg, unsigned po, unsigned ao, unsigned go, const v4u& rd) {
    asm volatile("buffer_load_dwordx4 %0, %11, %14, 0 offen sc1\n\tbuffer_load_dwordx4 %1, %11, %14, 0 offen offset:1024 sc1\n\t"
                 "buffer_load_dwordx4 %2, %11, %14, %15 offen sc1\n\tbuffer_load_dwordx4 %3, %11, %14, %15 offen offset:1024 sc1\n\t"
                 "buffer_load_dwordx4 %4, %11, %14, %16 offen sc1\n\tbuffer_load_dwordx4 %5, %11, %14, %16 offen offset:1024 sc1\n\t"
                 "buffer_load_dwordx4 %6, %11, %14, %17 offen sc1\n\tbuffer_load_dwordx4 %7, %11, %14, %17 offen offset:1024 sc1\n\t"
                 "buffer_load_dwordx2 %8, %12, %14, 0 offen sc1\n\tbuffer_load_dwordx2 %9, %12, %14, %18 offen sc1\n\tbuffer_load_dword %10, %13, %14, 0 offen sc1"
                 : "=&v"(fp[0]), "=&v"(fp[1]), "=&v"(fp[2]), "=&v"(fp[3]), "=&v"(fp[4]), "=&v"(fp[5]), "=&v"(fp[6]), "=&v"(fp[7]), "=&v"(fa0), "=&v"(fa1), "=&v"(fg)
                 : "v"(po), "v"(ao), "v"(go), "s"(rd), "s"(262144u), "s"(524288u), "s"(786432u), "s"(32u) : "memory"); }
__device__ __forceinline__ v2u mix(const v4u& p0, const v4u& p1, const v4u& p2, const v4u& p3, const v2u& av, unsigned fg) {
    const float gg = __uint_as_float(fg), og = rbfh(1.f - gg); float o[4];
#pragma unroll
    for (int q = 0; q < 4; ++q) { const unsigned aw = q < 2 ? av.x : av.y; const float aa = __uint_as_float((q & 1) ? (aw & 0xFFFF0000u) : (aw << 16));
        const float bsum = (__uint_as_float(p0[q]) + __uint_as_float(p1[q])) + (__uint_as_float(p2[q]) + __uint_as_float(p3[q]));
        o[q] = rbfh(gg * aa) + rbfh(og * rbfh(bsum)); }
    return (v2u){pack2(o[0], o[1]), pack2(o[2], o[3])}; }
__device__ __forceinline__ void bulk_block(const Params& p, rsrc_t r, unsigned char* lds, const int wv, const int j) {
    const int w = wv;
    BSTAMP(0);
    proj_block(p, lds, wv, (j & 6) * 4 + (j >> 4), ((j >> 2) & 2) | (j & 1), j);
    BSTAMP(2);
    {
        const rsrc_t ro = __builtin_amdgcn_make_buffer_rsrc(p.out, 0, S * D * 2, 0x00020000); const v4u z = {0u, 0u, 0u, 0u}; const int tid = otid();
#pragma unroll
        for (int i = 0; i < 8; ++i) st16(ro, (unsigned)(j * 32768 + i * 4096 + tid * 16), z);
        DRAIN(); __syncthreads();
        if (tid == 0) __hip_atomic_fetch_add((unsigned*)(p.ws + F_CONV), 1u, __ATOMIC_RELAXED, __HIP_MEMORY_SCOPE_AGENT);
    }
    const int tid4 = otid();
    const int kq = (j & 7) >> 1, nt = (j >> 3) * 2 + (j & 1), l = tid4 & 63, n16 = l & 15, g = l >> 4;
    __syncthreads();
    for (int i = tid4; i < 4096; i += 256) { const int row = i >> 2, q4 = i & 3;
        *(v4u*)(lds + row * OP_P + q4 * 16) = *(const v4u*)(p.Wo + (size_t)(kq * 1024 + row) * 1024 + nt * 32 + q4 * 8); }
    __syncthreads();
    BSTAMP(13);
    const unsigned ldsbase = (unsigned)(size_t)lds;
    auto finish = [&](int tt) {
        const int tok = tt * 64 + 16 * w + n16;
        const unsigned po = W_PART + tt * 1048576 + (w * 32 + nt) * 2048 + l * 16;
        v4u pp[4][2];
#pragma unroll
        for (int q = 0; q < 4; ++q)
#pragma unroll
            for (int jn = 0; jn < 2; ++jn) pp[q][jn] = ld16(r, po + q * 262144 + jn * 1024);
        const float gg = __uint_as_float(ld4(r, W_GATE + tok * 4)), og = rbfh(1.f - gg);
#pragma unroll
        for (int jn = 0; jn < 2; ++jn) {
            const int n = nt * 32 + 16 * jn + 4 * g;
            const v2u av = ld8(r, W_A + nt * 131072 + (tok & 31) * 4096 + (tok >> 5) * 64 + jn * 32 + g * 8);
            float o[4];
#pragma unroll
            for (int q = 0; q < 4; ++q) { const unsigned aw = q < 2 ? av.x : av.y; const float aa = __uint_as_float((q & 1) ? (aw & 0xFFFF0000u) : (aw << 16));
                const float bsum = (__uint_as_float(pp[0][jn][q]) + __uint_as_float(pp[1][jn][q])) + (__uint_as_float(pp[2][jn][q]) + __uint_as_float(pp[3][jn][q]));
                o[q] = rbfh(gg * aa) + rbfh(og * rbfh(bsum)); }
            v2u pk = {pack2(o[0], o[1]), pack2(o[2], o[3])}; *(v2u*)(p.out + (size_t)tok * 1024 + n) = pk; } };
    bf16x8 wof[32][2];
#pragma unroll
    for (int ks = 0; ks < 32; ++ks)
#pragma unroll
        for (int jn = 0; jn < 2; ++jn) {
            const unsigned a0 = ldsbase + (32 * ks + 4 * g + (n16 >> 2)) * OP_P + (16 * jn + 4 * (n16 & 3)) * 2;
            const v2u lo = trb(a0), hi = trb(a0 + 16 * OP_P);
            v4u v = {lo.x, lo.y, hi.x, hi.y}; asm volatile("" : "=a"(wof[ks][jn]) : "0"(as_frag(v))); }
    const v4u rd = mk_srd(p.ws, W_TOTAL), ro = mk_srd(p.out, S * D * 2);
    const int pw = w >> 1;
    int op = 0, nev = 0; unsigned idle = 0u; (void)nev;
    bool conv_ok = false; unsigned known = 0u;
    unsigned prog = 0u, tseen = rtnow() - 100000u;
    auto a_off = [&](int t) -> unsigned { return W_OX + ((t * 4 + w) * 32 + 8 * kq) * 4096 + l * 16; };
    const unsigned r_off = F_PROG + (kq * 8 + (l & 7)) * 128;
    auto units = [&](unsigned pg) -> unsigned { const unsigned c = (pg + 1u - pw) >> 1; return c >= 32u ? ~0u : (1u << c) - 1u; };
    while (op < 32) {
        unsigned rdy = known >> op;
        if (!(rdy & 1u)) {
            const unsigned now = rtnow(), est = prog + 1u + (now - tseen) / STEP_MIN;
            if (++idle > (1u << 22)) { st4(r, F_ERR, 128u); return; }
            if (est <= (unsigned)(2 * op + pw)) { __builtin_amdgcn_s_sleep(16); continue; }
            MEMBAR();
            prog = min16(ld4(r, r_off)); tseen = now; known = units(prog); rdy = known >> op;
            MEMBAR();
            if (!(rdy & 1u)) { __builtin_amdgcn_s_sleep(POLL_SLP); continue; }
        }
        idle = 0u;
        if (!conv_ok) { if (!wait_word(r, F_CONV, 160u, 256u)) return; conv_ok = true; }
        EV(128 + op);
        v4u af[32], wm, fp[8]; v2u fa0, fa1; unsigned aold = 0u, cfl = 0u, fg, d0;
        {
            const unsigned a0 = a_off(op);
            if (WARM1) asm volatile("buffer_load_dwordx4 %0, %1, %2, 0 offen sc1\n\ts_waitcnt vmcnt(0)" : "=&v"(wm) : "v"(a0 + nt * 1024), "s"(rd) : "memory");
#pragma unroll
            for (int q = 0; q < 8; ++q)
                asm volatile("buffer_load_dwordx4 %0, %4, %5, %6 offen sc1\n\tbuffer_load_dwordx4 %1, %4, %5, %6 offen offset:1024 sc1\n\t"
                             "buffer_load_dwordx4 %2, %4, %5, %6 offen offset:2048 sc1\n\tbuffer_load_dwordx4 %3, %4, %5, %6 offen offset:3072 sc1"
                             : "=&v"(af[4 * q]), "=&v"(af[4 * q + 1]), "=&v"(af[4 * q + 2]), "=&v"(af[4 * q + 3]) : "v"(a0), "s"(rd), "s"(q * 4096) : "memory");
            asm volatile("buffer_load_dwordx4 %3, %5, %6, 0 offen sc1\n\tbuffer_load_dword %1, %4, %6, 0 offen sc1\n\tbuffer_load_dword %2, %7, %6, 0 offen sc1\n\t"
                         "buffer_load_dword %0, %4, %6, 0 offen sc1\n\tbuffer_load_dword %0, %4, %6, 0 offen sc1\n\tbuffer_load_dword %0, %4, %6, 0 offen sc1\n\tbuffer_load_dword %0, %4, %6, 0 offen sc1"
                         : "=&v"(d0), "=&v"(aold), "=&v"(cfl), "=&v"(wm) : "v"(OOB), "v"((rdy & 2u) ? a_off(op + 1) + nt * 1024 : OOB), "s"(rd), "v"(r_off) : "memory");
            fin_loads(fp, fa0, fa1, fg, OOB, OOB, OOB, rd);
        }
        const int op0 = op; bool fin2 = false; int fpend = -1;
        for (;;) {
            const bool more = (rdy & 2u) != 0u;
            const unsigned an = more ? a_off(op + 1) : OOB;
            f32x4 acc0 = {0.f, 0.f, 0.f, 0.f}, acc1 = {0.f, 0.f, 0.f, 0.f};
            asm volatile("s_nop 1" : "+v"(acc0), "+v"(acc1));
#pragma unroll
            for (int q = 0; q < 8; ++q)
                asm volatile(
                    "s_waitcnt vmcnt(49)\n\tv_mfma_f32_16x16x32_bf16 %4, %6, %0, %4\n\tv_mfma_f32_16x16x32_bf16 %5, %7, %0, %5\n\tbuffer_load_dwordx4 %0, %14, %15, %16 offen sc1\n\t"
                    "s_waitcnt vmcnt(49)\n\tv_mfma_f32_16x16x32_bf16 %4, %8, %1, %4\n\tv_mfma_f32_16x16x32_bf16 %5, %9, %1, %5\n\tbuffer_load_dwordx4 %1, %14, %15, %16 offen offset:1024 sc1\n\t"
                    "s_waitcnt vmcnt(49)\n\tv_mfma_f32_16x16x32_bf16 %4, %10, %2, %4\n\tv_mfma_f32_16x16x32_bf16 %5, %11, %2, %5\n\tbuffer_load_dwordx4 %2, %14, %15, %16 offen offset:2048 sc1\n\t"
                    "s_waitcnt vmcnt(49)\n\tv_mfma_f32_16x16x32_bf16 %4, %12, %3, %4\n\tv_mfma_f32_16x16x32_bf16 %5, %13, %3, %5\n\tbuffer_load_dwordx4 %3, %14, %15, %16 offen offset:3072 sc1"
                    : "+v"(af[4 * q]), "+v"(af[4 * q + 1]), "+v"(af[4 * q + 2]), "+v"(af[4 * q + 3]), "+v"(acc0), "+v"(acc1)
                    : "a"(wof[4 * q][0]), "a"(wof[4 * q][1]), "a"(wof[4 * q + 1][0]), "a"(wof[4 * q + 1][1]), "a"(wof[4 * q + 2][0]), "a"(wof[4 * q + 2][1]), "a"(wof[4 * q + 3][0]), "a"(wof[4 * q + 3][1]),
                      "v"(an), "s"(rd), "s"(q * 4096) : "memory");
            const unsigned po = W_PART + (op * 4 + kq) * 262144 + (w * 32 + nt) * 2048 + l * 16;
            asm volatile("s_nop 7\n\tbuffer_store_dwordx4 %14, %16, %17, 0 offen sc1\n\tbuffer_store_dwordx4 %15, %16, %17, 0 offen offset:1024 sc1\n\ts_waitcnt vmcnt(34)"
                         : "+v"(aold), "+v"(cfl), "+v"(wm), "+v"(fp[0]), "+v"(fp[1]), "+v"(fp[2]), "+v"(fp[3]), "+v"(fp[4]), "+v"(fp[5]), "+v"(fp[6]), "+v"(fp[7]), "+v"(fa0), "+v"(fa1), "+v"(fg)
                         : "v"(acc0), "v"(acc1), "v"(po), "s"(rd) : "memory");
            const unsigned oldu = __builtin_amdgcn_readfirstlane(aold);
            rdy |= units(min16(cfl)) >> op;
            {
                const v2u o0 = mix(fp[0], fp[2], fp[4], fp[6], fa0, fg), o1 = mix(fp[1], fp[3], fp[5], fp[7], fa1, fg);
                const unsigned oo = fpend >= 0 ? (unsigned)(((fpend * 64 + 16 * w + n16) * 1024 + nt * 32 + 4 * g) * 2) : OOB;
                asm volatile("buffer_store_dwordx2 %0, %2, %3, 0 offen\n\tbuffer_store_dwordx2 %1, %2, %3, 0 offen offset:32" :: "v"(o0), "v"(o1), "v"(oo), "s"(ro) : "memory");
            }
            const bool fin = op >= op0 + 2 && oldu == 3u;
            if (!more) {
                if (!(rdy & 2u)) { fin2 = fin; break; }
                const unsigned a1 = a_off(op + 1);
#pragma unroll
                for (int q = 0; q < 8; ++q)
                    asm volatile("buffer_load_dwordx4 %0, %4, %5, %6 offen sc1\n\tbuffer_load_dwordx4 %1, %4, %5, %6 offen offset:1024 sc1\n\t"
                                 "buffer_load_dwordx4 %2, %4, %5, %6 offen offset:2048 sc1\n\tbuffer_load_dwordx4 %3, %4, %5, %6 offen offset:3072 sc1"
                                 : "+v"(af[4 * q]), "+v"(af[4 * q + 1]), "+v"(af[4 * q + 2]), "+v"(af[4 * q + 3]) : "v"(a1), "s"(rd), "s"(q * 4096) : "memory");
                asm volatile("buffer_load_dword %0, %1, %2, 0 offen sc1\n\tbuffer_load_dword %0, %1, %2, 0 offen sc1\n\tbuffer_load_dword %0, %1, %2, 0 offen sc1\n\tbuffer_load_dword %0, %1, %2, 0 offen sc1"
                             : "+v"(d0) : "v"(OOB), "s"(rd) : "memory");
            }
            {
                const unsigned tadr = op > op0 ? F_OP + ((op - 1) * 32 + nt) * 64 + w * 4 : F_DUM + (j * 4 + w) * 16;
                aold = 1u;
                asm volatile("s_mov_b64 exec, 1\n\tbuffer_atomic_add %0, %3, %6, 0 offen sc0\n\ts_mov_b64 exec, -1\n\t"
                             "buffer_load_dword %1, %4, %6, 0 offen sc1\n\tbuffer_load_dwordx4 %2, %5, %6, 0 offen sc1"
                             : "+v"(aold), "=&v"(cfl), "=&v"(wm) : "v"(tadr), "v"((rdy & 8u) ? OOB : r_off), "v"((rdy & 4u) ? a_off(op + 2) + nt * 1024 : OOB), "s"(rd) : "memory");
                fpend = fin ? op - 2 : -1;
                const int tok = (op - 2) * 64 + 16 * w + n16;
                fin_loads(fp, fa0, fa1, fg, fin ? W_PART + (op - 2) * 1048576 + (w * 32 + nt) * 2048 + l * 16 : OOB,
                          fin ? W_A + nt * 131072 + (tok & 31) * 4096 + (tok >> 5) * 64 + g * 8 : OOB, fin ? W_GATE + tok * 4 : OOB, rd);
            }
            if (op == 15) BSTAMP(5);
            rdy >>= 1; ++op;
        }
        asm volatile("s_waitcnt vmcnt(0)" : "+v"(af[0]), "+v"(af[1]), "+v"(af[2]), "+v"(af[3]), "+v"(af[4]), "+v"(af[5]), "+v"(af[6]), "+v"(af[7]), "+v"(af[8]), "+v"(af[9]), "+v"(af[10]), "+v"(af[11]),
                     "+v"(af[12]), "+v"(af[13]), "+v"(af[14]), "+v"(af[15]), "+v"(d0) :: "memory");
        asm volatile("" : "+v"(af[16]), "+v"(af[17]), "+v"(af[18]), "+v"(af[19]), "+v"(af[20]), "+v"(af[21]), "+v"(af[22]), "+v"(af[23]), "+v"(af[24]), "+v"(af[25]), "+v"(af[26]), "+v"(af[27]),
                     "+v"(af[28]), "+v"(af[29]), "+v"(af[30]), "+v"(af[31]) :: "memory");
        unsigned o1 = 0u, o2 = 0u;
        if (l == 0) { o2 = __hip_atomic_fetch_add((unsigned*)(p.ws + F_OP + (op * 32 + nt) * 64 + w * 4), 1u, __ATOMIC_RELAXED, __HIP_MEMORY_SCOPE_AGENT);
            if (op > op0) o1 = __hip_atomic_fetch_add((unsigned*)(p.ws + F_OP + ((op - 1) * 32 + nt) * 64 + w * 4), 1u, __ATOMIC_RELAXED, __HIP_MEMORY_SCOPE_AGENT); }
        {
            const unsigned now = rtnow();
            prog = min16(ld4(r, r_off)); tseen = now; known = units(prog);
        }
        if (fin2) finish(op - 2);
        if (__builtin_amdgcn_readfirstlane(o1) == 3u) finish(op - 1);
        if (__builtin_amdgcn_readfirstlane(o2) == 3u) finish(op);
        if (op == 15) BSTAMP(5);
        EV(192 + op);
        ++op;
    }
    BSTAMP(7);
}

__device__ __forceinline__ void rec_block(const Params& p, rsrc_t r, unsigned char* lds, const int wv, const int b) {
    const int xq = (b & 7) >> 1, t = (b >> 3) * 2 + (b & 1);
    const unsigned lb = __builtin_amdgcn_readfirstlane((unsigned)(size_t)lds);
    int kind, id, rg = 0, rot = 0, bq = 99;
    if (RPL == 0) { kind = t < 8 ? 0 : t < 24 ? 1 : 2; id = t < 8 ? xq * 8 + t : t < 24 ? (xq & 1) * 16 + t - 8 : xq * 8 + t - 24; rg = xq >> 1; }
    else if (RPL == 4) { kind = t < 8 ? 0 : t < 24 ? 1 : 2; id = t < 8 ? xq * 8 + t : t < 24 ? xq * 8 + ((t - 8) >> 1) : xq * 8 + t - 24; rg = t & 1; }
    else if (RPL == 1) { kind = xq == 0 ? 0 : xq < 3 ? 1 : 2; id = t; rg = xq - 1; }
    else if (RPL == 2) { const int na = xq == 2 ? 10 : 11, q = xq * 21 + t - na; kind = xq == 3 ? 2 : t < na ? 0 : 1; id = xq == 3 ? t : t < na ? xq * 11 + t : q >> 1; rg = q & 1; }
    else { const int na = xq == 2 ? 12 : 10, q = t - na; kind = xq == 3 ? 2 : t < na ? 0 : 1; rg = xq < 2 ? xq : q & 1; bq = q;
        const int c2 = q >> 1, cs = xq < 2 ? (q < 6 ? q : q < 20 ? q + 2 : q + 4) : (c2 < 2 ? 6 + c2 : c2 < 4 ? 20 + c2 : 22 + c2);
        id = xq == 3 ? t : t < na ? xq * 10 + t : cs; rot = !ROT ? 0 : xq < 2 ? (cs & 1) * 4 : ((cs & 1) * 2 + rg) * 2; }
    if (kind == 0) rec_a(p, lb, wv, id, xq);
    else if (kind == 1) rec_b(p, lb, wv, id, rg, xq, rot, bq);
    else {
        const int hi = id, j = hi - 32; (void)j;
        BSTAMP(0);
        rec_helper(p, r, lds, wv, hi);
        BSTAMP(6);
        if (XWAIT == 1) { if (!wait_word(r, F_GO + (hi >> 1) * 128, 1u, 1024u)) return; }
        gate_block(p, r, hi, wv);
        BSTAMP(2);
        if (XWAIT == 2) { if (!wait_word(r, F_GO + (hi >> 1) * 128, 1u, 1024u)) return; }
        conv_block(p, r, lds, wv, hi, j);
        BSTAMP(3);
    }
}

__global__ void __launch_bounds__(256, 1) k_main(Params p) {
    extern __shared__ __attribute__((aligned(16))) unsigned char lds[];
    rsrc_t r = __builtin_amdgcn_make_buffer_rsrc(p.ws, 0, W_TOTAL, 0x00020000);
    const int wv = __builtin_amdgcn_readfirstlane(threadIdx.x >> 6);
    int role = blockIdx.x;
    unsigned ctouch = 0u;
    if (CODE_T) {
        unsigned long long pc; int ea, eb, eg;
        asm volatile("s_getpc_b64 %0\n.Lk_begin:\n\ts_mov_b32 %1, .Lk_end_A-.Lk_begin\n\ts_mov_b32 %2, .Lk_end_B-.Lk_begin\n\ts_mov_b32 %3, .Lk_end_G-.Lk_begin" : "=s"(pc), "=s"(ea), "=s"(eb), "=s"(eg));
        const int o = ((blockIdx.x >> 3) * 64 + (threadIdx.x >> 2)) * 128;
        if ((threadIdx.x & 3) == 0 && o < max(ea, max(eb, eg)) - 4) asm volatile("global_load_dword %0, %1, off" : "+v"(ctouch) : "v"((const unsigned char*)pc + o) : "memory");
    }
    if (threadIdx.x == 0) {
        unsigned xcc; asm volatile("s_getreg_b32 %0, hwreg(HW_REG_XCC_ID)" : "=s"(xcc)); xcc &= 7u;
        unsigned* tk = (unsigned*)(p.ws + F_TKT);
        unsigned t = __hip_atomic_fetch_add(tk + xcc * 16, 1u, __ATOMIC_RELAXED, __HIP_MEMORY_SCOPE_AGENT);
        if (t >= 32u) {
            unsigned o = __hip_atomic_fetch_add(tk + 128, 1u, __ATOMIC_RELAXED, __HIP_MEMORY_SCOPE_AGENT);
            for (unsigned spin = 0; spin < (1u << 22); ++spin) { unsigned tot = 0u;
                for (int x = 0; x < 8; ++x) tot += __hip_atomic_load(tk + x * 16, __ATOMIC_RELAXED, __HIP_MEMORY_SCOPE_AGENT);
                if (tot >= 256u) break; }
            for (int x = 0; x < 8; ++x) { const unsigned cnt = __hip_atomic_load(tk + x * 16, __ATOMIC_RELAXED, __HIP_MEMORY_SCOPE_AGENT), fr = cnt < 32u ? 32u - cnt : 0u;
                if (o < fr) { xcc = (unsigned)x; t = cnt + o; break; }
                o -= fr; }
        }
        *(volatile int*)lds = (int)((xcc >> 2) * 128u + (t >> 1) * 8u + (xcc & 3u) * 2u + (t & 1u));
    }
    asm volatile("s_waitcnt vmcnt(0)" : "+v"(ctouch) :: "memory");
    __syncthreads();
    role = __builtin_amdgcn_readfirstlane(*(volatile int*)lds) & 255;
    __syncthreads();
    if (role >= 128) bulk_block(p, r, lds, wv, role - 128); else rec_block(p, r, lds, wv, role);
}

extern "C" void kernel_launch(void* const* d_in, const int* in_sizes, int n_in, void* d_out, int out_size, void* d_ws, size_t ws_size, hipStream_t stream) {
    Params p;
    p.Aq = (const bf16_t*)d_in[0]; p.Ak = (const bf16_t*)d_in[1]; p.Av = (const bf16_t*)d_in[2];
    p.shift_k = (const bf16_t*)d_in[3]; p.ssm_k = (const bf16_t*)d_in[4]; p.ssm_D = (const bf16_t*)d_in[5];
    p.mem0 = (const bf16_t*)d_in[7]; p.Wq = (const bf16_t*)d_in[8]; p.Wk = (const bf16_t*)d_in[9];
    p.Wv = (const bf16_t*)d_in[10]; p.Wo = (const bf16_t*)d_in[11]; p.gate_w = (const bf16_t*)d_in[13];
    p.out = (bf16_t*)d_out; p.ws = (unsigned char*)d_ws;
    unsigned char* ws = (unsigned char*)d_ws;
    (void)hipMemsetAsync(ws, 0, FLG_BYTES, stream);
    (void)hipFuncSetAttribute((const void*)k_main, hipFuncAttributeMaxDynamicSharedMemorySize, L_TOTAL);
    k_main<<<256, 256, L_TOTAL, stream>>>(p);
}
```

```cpp
#define PEN 44
#define LT_A 2
#include <hip/hip_runtime.h>
#include <stdint.h>
#include <type_traits>
typedef unsigned short bf16_t;
using bf16x8 = __attribute__((ext_vector_type(8))) short;
using f32x4 = __attribute__((ext_vector_type(4))) float;
typedef unsigned v4u __attribute__((ext_vector_type(4)));
typedef unsigned v2u __attribute__((ext_vector_type(2)));
typedef __amdgpu_buffer_rsrc_t rsrc_t;

__device__ __forceinline__ float bf2f(bf16_t b) { return __uint_as_float(((unsigned)b) << 16); }
typedef __bf16 hbf2 __attribute__((ext_vector_type(2)));
typedef float f32x2 __attribute__((ext_vector_type(2)));
__device__ __forceinline__ unsigned pack2(float a, float b) { f32x2 v = {a, b}; return __builtin_bit_cast(unsigned, __builtin_convertvector(v, hbf2)); }
__device__ __forceinline__ float rbfh(float f) { return __uint_as_float(pack2(f, 0.f) << 16); }
__device__ __forceinline__ unsigned nosent(unsigned x) {
    typedef unsigned short us2 __attribute__((ext_vector_type(2)));
    const us2 a = __builtin_bit_cast(us2, x), c = {(unsigned short)0xFFFE, (unsigned short)0xFFFE}; return __builtin_bit_cast(unsigned, __builtin_elementwise_min(a, c)); }

constexpr int S = 2048, D = 1024, H = 32, DH = 128, M = 8, SEG = 32, NSEG = 64;

constexpr unsigned W_FLG = 0;
constexpr unsigned F_KVQ = W_FLG;
constexpr unsigned F_CONV = W_FLG + 32768;
constexpr unsigned F_ERR = W_FLG + 32768 + 256;
constexpr unsigned F_PREP = W_FLG + 32768 + 512;
constexpr unsigned F_CAL = W_FLG + 33792;
constexpr unsigned F_TKT = W_FLG + 34816;
constexpr unsigned F_PROG = W_FLG + 49152;
constexpr unsigned F_CVU = W_FLG + 53248, F_CVY = W_FLG + 57344;
constexpr unsigned F_OP = W_FLG + 65536;
constexpr unsigned F_ATT = W_FLG + 131072;
constexpr unsigned F_RDY = W_FLG + 147456;
constexpr unsigned FLG_BYTES = 180224;
constexpr unsigned HO_KVM = W_FLG + FLG_BYTES;
constexpr unsigned W_A = HO_KVM + 64 * 32 * 4096;
constexpr unsigned W_GATE = W_A + S * D * 2;
constexpr unsigned W_QX = W_GATE + 8192;
constexpr unsigned W_KX = W_QX + S * 4096 * 2;
constexpr unsigned W_VF = W_KX + S * 4096 * 2;
constexpr unsigned W_OX = W_VF + 64 * 32 * 8192;
constexpr unsigned W_PART = W_OX + S * 4096 * 2;
constexpr unsigned W_Y = W_PART;
constexpr unsigned W_DBG = W_PART + 32 * 4 * 64 * 1024 * 4;
constexpr unsigned W_U = W_DBG + 256 * 64 * 16 * 8;
constexpr unsigned W_TOTAL = W_U + S * D * 2;

__device__ __forceinline__ v4u ld16(rsrc_t r, unsigned off) { return __builtin_amdgcn_raw_buffer_load_b128(r, off, 0, 16); }
__device__ __forceinline__ v2u ld8(rsrc_t r, unsigned off) { return __builtin_amdgcn_raw_buffer_load_b64(r, off, 0, 16); }
__device__ __forceinline__ unsigned ld4(rsrc_t r, unsigned off) { return __builtin_amdgcn_raw_buffer_load_b32(r, off, 0, 16); }
__device__ __forceinline__ void st16(rsrc_t r, unsigned off, v4u v) { __builtin_amdgcn_raw_buffer_store_b128(v, r, off, 0, 16); }
__device__ __forceinline__ void st8(rsrc_t r, unsigned off, v2u v) { __builtin_amdgcn_raw_buffer_store_b64(v, r, off, 0, 16); }
__device__ __forceinline__ void st4(rsrc_t r, unsigned off, unsigned v) { __builtin_amdgcn_raw_buffer_store_b32(v, r, off, 0, 16); }
__device__ __forceinline__ bool okdw(unsigned x) { return ((x & 0xFFFFu) != 0xFFFFu) & ((x >> 16) != 0xFFFFu); }
typedef unsigned short us2_t __attribute__((ext_vector_type(2)));
__device__ __forceinline__ unsigned pkmax(unsigned a, unsigned b) { return __builtin_bit_cast(unsigned, __builtin_elementwise_max(__builtin_bit_cast(us2_t, a), __builtin_bit_cast(us2_t, b))); }
__device__ __forceinline__ unsigned pkmax4(v4u v) { return pkmax(pkmax(v.x, v.y), pkmax(v.z, v.w)); }
__device__ __forceinline__ bool ok16(v4u v) { return okdw(pkmax4(v)); }
__device__ __forceinline__ bool ok8(v2u v) { return okdw(v.x) & okdw(v.y); }
__device__ __forceinline__ bf16x8 as_frag(v4u v) { return __builtin_bit_cast(bf16x8, v); }
#define MFMA(a, b, c) __builtin_amdgcn_mfma_f32_16x16x32_bf16((a), (b), (c), 0, 0, 0)
#define D1 136
#define D2 85
#define D3 76
#define LB0 12
#define LC0 20
#define LA0 24
#define TR_UP 4
#define TR_DN 2
#define L_DEC 3
#define WARM1 0
#define KROT 0
#define STAG 1
#define UP_SH 2
#define EPI_SLP 0
#define SITA 1
#define PAIRX 4
#define DUE_P 1
#define LATE 1
#define EARLY_FROM 56
#define SKEW 0
#define OUT_WARM 1
#define ADAPT 1
#define RPL 0
#define CV_POL 2
#define POLL_SLP 4
#define TOUCH 1
#define ROT 0
#define CODE_T 1
#define CAL_T 450u
#define GTOUCH 0
#define XTOUCH 32
#define ST_P 32
#define CV_NAP 4000u
#define XWAIT 1
#define MEMBAR() asm volatile("" ::: "memory")
#define FT(i) do { } while (0)
#define ENDPGM() asm volatile("s_endpgm" ::: "memory")
#define DRAIN() asm volatile("s_waitcnt vmcnt(0)" ::: "memory")

__device__ __forceinline__ float xor16_max(float x) { auto r = __builtin_amdgcn_permlane16_swap(__float_as_uint(x), __float_as_uint(x), false, false); return fmaxf(__uint_as_float(r[0]), __uint_as_float(r[1])); }
__device__ __forceinline__ float xor32_max(float x) { auto r = __builtin_amdgcn_permlane32_swap(__float_as_uint(x), __float_as_uint(x), false, false); return fmaxf(__uint_as_float(r[0]), __uint_as_float(r[1])); }
__device__ __forceinline__ float xor16_add(float x) { auto r = __builtin_amdgcn_permlane16_swap(__float_as_uint(x), __float_as_uint(x), false, false); return __uint_as_float(r[0]) + __uint_as_float(r[1]); }
__device__ __forceinline__ float xor32_add(float x) { auto r = __builtin_amdgcn_permlane32_swap(__float_as_uint(x), __float_as_uint(x), false, false); return __uint_as_float(r[0]) + __uint_as_float(r[1]); }
__device__ __forceinline__ v2u tr_read(unsigned lds_addr) { v2u r; asm volatile("ds_read_b64_tr_b16 %0, %1\n\ts_waitcnt lgkmcnt(0)" : "=&v"(r) : "v"(lds_addr) : "memory"); return r; }

typedef short v4i16_t __attribute__((ext_vector_type(4)));
__device__ __forceinline__ v2u trb(unsigned lds_addr) { return __builtin_bit_cast(v2u, __builtin_amdgcn_ds_read_tr16_b64_v4i16((__attribute__((address_space(3))) v4i16_t*)(size_t)lds_addr)); }
__device__ __forceinline__ int otid_(int wv) { int t; asm volatile("v_mbcnt_lo_u32_b32 %0, -1, 0\n\tv_mbcnt_hi_u32_b32 %0, -1, %0" : "=v"(t)); return wv * 64 + t; }
#define otid() otid_(wv)
__device__ __forceinline__ bool wait_word(rsrc_t r, unsigned off, unsigned want, unsigned code) {
    for (unsigned spin = 0;; ++spin) { MEMBAR();
        const unsigned v = __builtin_amdgcn_readfirstlane(ld4(r, off));
        if (v >= want) return true;
        if (spin > (1u << 20)) { st4(r, F_ERR, code); return false; }
        __builtin_amdgcn_s_sleep(8); }
}

__device__ __forceinline__ unsigned rtnow() { return (unsigned)__builtin_amdgcn_s_memrealtime(); }
__device__ __forceinline__ void wait_until(unsigned target) { for (int i = 0; i < 4096; ++i) { if ((int)(rtnow() - target) >= 0) break; } }
__device__ __forceinline__ void wait_long(unsigned target) { for (int i = 0; i < 4096; ++i) { if ((int)(rtnow() - target) >= 0) break; __builtin_amdgcn_s_sleep(8); } }
constexpr int RED_G = 320;
constexpr int RED_T = 4 * RED_G;
constexpr int L_RED = 0;
constexpr int L_KX = 65536;
constexpr int KXP = 272;
constexpr int L_VF = L_KX + 2 * 32 * KXP;
constexpr int L_QKV = L_VF + 16384;
constexpr int L_OFF = L_QKV + 6144;
constexpr int L_STMP = L_OFF + 256;
constexpr int L_TOTAL = 163840;
constexpr unsigned U_A = 0, U_S = 32, U_B = 36, U_INIT = 113, U_C = 128, U_END = 512;
#define PLACE 1
#define CAL_N 5
#define STEP_MIN 190u

struct Params {
    const bf16_t *Aq, *Ak, *Av, *shift_k, *ssm_k, *ssm_D, *mem0, *Wq, *Wk, *Wv, *Wo, *gate_w;
    bf16_t* out; unsigned char* ws;
};
__host__ __device__ __forceinline__ unsigned hx_base(const void* ws) { const size_t a = (size_t)ws; return (unsigned)((((a + W_TOTAL + 0x7FFFFFu) >> 23) << 23) - a); }

#define STAMP(i) do { } while (0)
#define BSTAMP(i) do { } while (0)
#define EV(code) do { } while (0)
#define SPINS(i) do { } while (0)
typedef short v4s __attribute__((ext_vector_type(4)));
typedef unsigned v32u __attribute__((ext_vector_type(32)));
typedef unsigned v16u __attribute__((ext_vector_type(16)));
static_assert(L_VF - L_KX == 16384 + 1024, "fragment read offsets");
#define MFMA16(a, b, c) __builtin_amdgcn_mfma_f32_16x16x16bf16_1k(__builtin_bit_cast(v4s, (a)), __builtin_bit_cast(v4s, (b)), (c), 0, 0, 0)
__device__ __forceinline__ unsigned ror8(unsigned x) { return (unsigned)__builtin_amdgcn_update_dpp(0, (int)x, 0x128, 0xF, 0xF, false); }
__device__ __forceinline__ unsigned qx1(unsigned x) { return (unsigned)__builtin_amdgcn_update_dpp(0, (int)x, 0xB1, 0xF, 0xF, false); }
__device__ __forceinline__ unsigned wave_pkmax(unsigned d) {
    d = pkmax(d, (unsigned)__builtin_amdgcn_update_dpp((int)d, (int)d, 0xB1, 0xF, 0xF, false)); d = pkmax(d, (unsigned)__builtin_amdgcn_update_dpp((int)d, (int)d, 0x4E, 0xF, 0xF, false));
    d = pkmax(d, (unsigned)__builtin_amdgcn_update_dpp((int)d, (int)d, 0x124, 0xF, 0xF, false)); d = pkmax(d, (unsigned)__builtin_amdgcn_update_dpp((int)d, (int)d, 0x128, 0xF, 0xF, false));
    { auto q = __builtin_amdgcn_permlane16_swap(d, d, false, false); d = pkmax(q[0], q[1]); }
    { auto q = __builtin_amdgcn_permlane32_swap(d, d, false, false); d = pkmax(q[0], q[1]); }
    return d; }
__device__ __forceinline__ int wave_max(int d) {
    d = max(d, __builtin_amdgcn_update_dpp(d, d, 0xB1, 0xF, 0xF, false)); d = max(d, __builtin_amdgcn_update_dpp(d, d, 0x4E, 0xF, 0xF, false));
    d = max(d, __builtin_amdgcn_update_dpp(d, d, 0x124, 0xF, 0xF, false)); d = max(d, __builtin_amdgcn_update_dpp(d, d, 0x128, 0xF, 0xF, false));
    { auto q = __builtin_amdgcn_permlane16_swap((unsigned)d, (unsigned)d, false, false); d = max((int)q[0], (int)q[1]); }
    { auto q = __builtin_amdgcn_permlane32_swap((unsigned)d, (unsigned)d, false, false); d = max((int)q[0], (int)q[1]); }
    return d; }

__device__ __forceinline__ v4u mk_srd(const void* p, unsigned bytes) { const size_t a = (size_t)p; v4u d;
    d.x = __builtin_amdgcn_readfirstlane((unsigned)a); d.y = __builtin_amdgcn_readfirstlane((unsigned)(a >> 32) & 0xFFFFu); d.z = __builtin_amdgcn_readfirstlane(bytes); d.w = 0x00020000u; return d; }
__device__ __forceinline__ bf16x8 ldsa(unsigned addr) { return *(const __attribute__((address_space(3))) bf16x8*)(size_t)addr; }
constexpr unsigned HX_BYTES = 2097152, OOB = 0xF0000000u;
constexpr unsigned F_RDY0 = W_FLG + 34304;
constexpr unsigned F_GO = W_FLG + 36864;
constexpr int L_KV = 90112, L_MS = 139264, L_IMG = 155648, IMG_PS = 1056, IMG_MS = 2112, LB_R = 8192, LB_STG = 16384, LB_STGP = 8704;
static_assert(F_KVQ == 0 && F_ERR == 33024 && F_CONV == 32768 && F_CAL == 33792 && F_RDY0 == 34304 && F_GO == 36864 && F_PROG == 49152 && W_QX == 12771328 && W_KX == 29548544 && W_OX == 63102976 && W_VF == 46325760 && W_DBG == 113434624 && L_KV == 90112 && L_MS == 139264 && L_IMG == 155648 && IMG_PS == 1056 && IMG_MS == 2112 && LB_R == 8192 && LB_STG == 16384 && LB_STGP == 8704, "constants of the generated code");
#define REC_S_IN "{s[8:11]}"(sh), "{s[12:15]}"(sw), "{s[16:19]}"(s1), "{s[20:23]}"(s2), "{s29}"(D1), "{s30}"(D2), "{s43}"(w), "{s44}"(id), "{s49}"(rb), "{s50}"(PEN), "{s51}"(LT_A), "{s45}"(F_GO + (cp * 4 + w) * 128)
__device__ __forceinline__ void rec_a(const Params& p, unsigned lb, const int wv, const int id, const int cp) {
    const int w = wv, l = otid() & 63, n = l & 15, g = l >> 4, h = id, rb = id;
    const v4u sh = mk_srd(p.ws + hx_base(p.ws), HX_BYTES - 8192), sw = mk_srd(p.ws, W_TOTAL), s1 = mk_srd(w < 2 ? p.Wq : p.Wk, 1024 * 4096 * 2), s2 = s1;
    const int r8 = l >> 3, pc = l & 7, pp = n >> 3, bb = (n >> 2) & 1, f = (g & 1) | (pp << 1);
    const unsigned stg = lb + 131072 + w * 8192;
    const unsigned g_w = r8 * 8192 + h * 256 + (w & 1) * 128 + pc * 16;
    const unsigned wb0 = stg + r8 * 128 + (((pc >> 1) ^ (r8 >> 2)) << 5) + (pc & 1) * 16, wb1 = wb0 ^ 64u;
    const unsigned rb0 = stg + (16 * (g >> 1) + 8 * pp + 4 * (g & 1) + bb) * 128 + (f << 5) + (n & 3) * 8;
    const unsigned a_mem = cp * 32768 + (w >> 1) * 16384 + (w & 1) * 4096 + (g >> 1) * 512 + ((n & 7) >> 2) * 256 + ((g & 1) * 2 + pp) * 64 + (n & 3) * 16;
    const unsigned l_ms = lb + L_MS + l * 16;
    const unsigned l_iw = lb + L_IMG + (w >> 1) * IMG_MS + (g & 1) * IMG_PS + (64 * (w & 1) + 16 * (g >> 1) + n) * 8;
    const unsigned l_iq = lb + L_IMG + (n & 1) * IMG_PS + (8 * g + (n >> 2)) * 8;
    const unsigned a_vm = (16 + (h >> 2)) * 16384 + (h & 3) * 2048 + w * 512 + n * 32 + (g & 1) * 16;
    const unsigned a_o = n < 8 ? (8 + (n >> 2) * 4 + (h >> 3)) * 16384 + (h & 7) * 1024 + w * 256 + g * 64 + (n & 3) * 16 : OOB;
    const unsigned a_ox = (((w & 1) * 32 + h) * 4 + w) * 1024 + l * 16;
    asm volatile(
        "s_lshl_b32 s34, s43, 2\n\t"
        "s_add_u32 s34, s34, 0\n\t"
        "s_and_b32 s34, s34, 15\n\t"
        "s_lshl_b32 s34, s34, 19\n\t"
        "buffer_load_dwordx4 v[224:227], v96, s[16:19], s34 offen nt\n\t"
        "s_add_u32 s34, s34, 0x10000\n\t"
        "buffer_load_dwordx4 v[228:231], v96, s[16:19], s34 offen nt\n\t"
        "s_add_u32 s34, s34, 0x10000\n\t"
        "buffer_load_dwordx4 v[232:235], v96, s[16:19], s34 offen nt\n\t"
        "s_add_u32 s34, s34, 0x10000\n\t"
        "buffer_load_dwordx4 v[236:239], v96, s[16:19], s34 offen nt\n\t"
        "s_add_u32 s34, s34, 0x10000\n\t"
        "buffer_load_dwordx4 v[240:243], v96, s[16:19], s34 offen nt\n\t"
        "s_add_u32 s34, s34, 0x10000\n\t"
        "buffer_load_dwordx4 v[244:247], v96, s[16:19], s34 offen nt\n\t"
        "s_add_u32 s34, s34, 0x10000\n\t"
        "buffer_load_dwordx4 v[248:251], v96, s[16:19], s34 offen nt\n\t"
        "s_add_u32 s34, s34, 0x10000\n\t"
        "buffer_load_dwordx4 v[252:255], v96, s[16:19], s34 offen nt\n\t"
        "s_lshl_b32 s34, s43, 2\n\t"
        "s_add_u32 s34, s34, 1\n\t"
        "s_and_b32 s34, s34, 15\n\t"
        "s_lshl_b32 s34, s34, 19\n\t"
        "buffer_load_dwordx4 v[0:3], v96, s[16:19], s34 offen nt\n\t"
        "s_add_u32 s34, s34, 0x10000\n\t"
        "buffer_load_dwordx4 v[4:7], v96, s[16:19], s34 offen nt\n\t"
        "s_add_u32 s34, s34, 0x10000\n\t"
        "buffer_load_dwordx4 v[8:11], v96, s[16:19], s34 offen nt\n\t"
        "s_add_u32 s34, s34, 0x10000\n\t"
        "buffer_load_dwordx4 v[12:15], v96, s[16:19], s34 offen nt\n\t"
        "s_add_u32 s34, s34, 0x10000\n\t"
        "buffer_load_dwordx4 v[16:19], v96, s[16:19], s34 offen nt\n\t"
        "s_add_u32 s34, s34, 0x10000\n\t"
        "buffer_load_dwordx4 v[20:23], v96, s[16:19], s34 offen nt\n\t"
        "s_add_u32 s34, s34, 0x10000\n\t"
        "buffer_load_dwordx4 v[24:27], v96, s[16:19], s34 offen nt\n\t"
        "s_add_u32 s34, s34, 0x10000\n\t"
        "buffer_load_dwordx4 v[28:31], v96, s[16:19], s34 offen nt\n\t"
        "s_lshl_b32 s34, s43, 2\n\t"
        "s_add_u32 s34, s34, 2\n\t"
        "s_and_b32 s34, s34, 15\n\t"
        "s_lshl_b32 s34, s34, 19\n\t"
        "buffer_load_dwordx4 v[32:35], v96, s[16:19], s34 offen nt\n\t"
        "s_add_u32 s34, s34, 0x10000\n\t"
        "buffer_load_dwordx4 v[36:39], v96, s[16:19], s34 offen nt\n\t"
        "s_add_u32 s34, s34, 0x10000\n\t"
        "buffer_load_dwordx4 v[40:43], v96, s[16:19], s34 offen nt\n\t"
        "s_add_u32 s34, s34, 0x10000\n\t"
        "buffer_load_dwordx4 v[44:47], v96, s[16:19], s34 offen nt\n\t"
        "s_add_u32 s34, s34, 0x10000\n\t"
        "buffer_load_dwordx4 v[48:51], v96, s[16:19], s34 offen nt\n\t"
        "s_add_u32 s34, s34, 0x10000\n\t"
        "buffer_load_dwordx4 v[52:55], v96, s[16:19], s34 offen nt\n\t"
        "s_add_u32 s34, s34, 0x10000\n\t"
        "buffer_load_dwordx4 v[56:59], v96, s[16:19], s34 offen nt\n\t"
        "s_add_u32 s34, s34, 0x10000\n\t"
        "buffer_load_dwordx4 v[60:63], v96, s[16:19], s34 offen nt\n\t"
        "v_cmp_le_i32_e64 s[60:61], 0, v126\n\t"
        "v_cmp_le_i32_e64 s[62:63], 1, v126\n\t"
        "v_cmp_le_i32_e64 s[64:65], 2, v126\n\t"
        "v_cmp_le_i32_e64 s[66:67], 3, v126\n\t"
        "v_cmp_le_i32_e64 s[68:69], 16, v126\n\t"
        "v_cmp_le_i32_e64 s[70:71], 17, v126\n\t"
        "v_cmp_le_i32_e64 s[72:73], 18, v126\n\t"
        "v_cmp_le_i32_e64 s[74:75], 19, v126\n\t"
        "s_mul_i32 s35, s43, 10240\n\t"
        "v_add_u32_e32 v126, s35, v124\n\t"
        "s_waitcnt vmcnt(16)\n\t"
        "ds_write_b128 v97, v[224:227] offset:0\n\t"
        "ds_write_b128 v98, v[228:231] offset:1024\n\t"
        "ds_write_b128 v97, v[232:235] offset:2048\n\t"
        "ds_write_b128 v98, v[236:239] offset:3072\n\t"
        "ds_write_b128 v97, v[240:243] offset:4096\n\t"
        "ds_write_b128 v98, v[244:247] offset:5120\n\t"
        "ds_write_b128 v97, v[248:251] offset:6144\n\t"
        "ds_write_b128 v98, v[252:255] offset:7168\n\t"
        "ds_read_b64_tr_b16 a[0:1], v99 offset:0\n\t"
        "ds_read_b64_tr_b16 a[2:3], v99 offset:256\n\t"
        "ds_read_b64_tr_b16 a[4:5], v99 offset:4096\n\t"
        "ds_read_b64_tr_b16 a[6:7], v99 offset:4352\n\t"
        "s_waitcnt lgkmcnt(8)\n\t"
        "ds_read_b64_tr_b16 a[8:9], v100 offset:0\n\t"
        "ds_read_b64_tr_b16 a[10:11], v100 offset:256\n\t"
        "ds_read_b64_tr_b16 a[12:13], v100 offset:4096\n\t"
        "ds_read_b64_tr_b16 a[14:15], v100 offset:4352\n\t"
        "s_waitcnt lgkmcnt(8)\n\t"
        "ds_read_b64_tr_b16 a[16:17], v101 offset:0\n\t"
        "ds_read_b64_tr_b16 a[18:19], v101 offset:256\n\t"
        "ds_read_b64_tr_b16 a[20:21], v101 offset:4096\n\t"
        "ds_read_b64_tr_b16 a[22:23], v101 offset:4352\n\t"
        "s_waitcnt lgkmcnt(8)\n\t"
        "ds_read_b64_tr_b16 v[104:105], v102 offset:0\n\t"
        "ds_read_b64_tr_b16 v[106:107], v102 offset:256\n\t"
        "ds_read_b64_tr_b16 v[108:109], v102 offset:4096\n\t"
        "ds_read_b64_tr_b16 v[110:111], v102 offset:4352\n\t"
        "s_lshl_b32 s34, s43, 2\n\t"
        "s_add_u32 s34, s34, 3\n\t"
        "s_and_b32 s34, s34, 15\n\t"
        "s_lshl_b32 s34, s34, 19\n\t"
        "buffer_load_dwordx4 v[64:67], v96, s[16:19], s34 offen nt\n\t"
        "s_add_u32 s34, s34, 0x10000\n\t"
        "buffer_load_dwordx4 v[68:71], v96, s[16:19], s34 offen nt\n\t"
        "s_add_u32 s34, s34, 0x10000\n\t"
        "buffer_load_dwordx4 v[72:75], v96, s[16:19], s34 offen nt\n\t"
        "s_add_u32 s34, s34, 0x10000\n\t"
        "buffer_load_dwordx4 v[76:79], v96, s[16:19], s34 offen nt\n\t"
        "s_add_u32 s34, s34, 0x10000\n\t"
        "buffer_load_dwordx4 v[80:83], v96, s[16:19], s34 offen nt\n\t"
        "s_add_u32 s34, s34, 0x10000\n\t"
        "buffer_load_dwordx4 v[84:87], v96, s[16:19], s34 offen nt\n\t"
        "s_add_u32 s34, s34, 0x10000\n\t"
        "buffer_load_dwordx4 v[88:91], v96, s[16:19], s34 offen nt\n\t"
        "s_add_u32 s34, s34, 0x10000\n\t"
        "buffer_load_dwordx4 v[92:95], v96, s[16:19], s34 offen nt\n\t"
        "s_waitcnt lgkmcnt(0)\n\t"
        "ds_write_b128 v117, v[104:107] offset:0\n\t"
        "ds_write_b128 v117, v[108:111] offset:1024\n\t"
        "s_waitcnt vmcnt(16)\n\t"
        "ds_write_b128 v97, v[0:3] offset:0\n\t"
        "ds_write_b128 v98, v[4:7] offset:1024\n\t"
        "ds_write_b128 v97, v[8:11] offset:2048\n\t"
        "ds_write_b128 v98, v[12:15] offset:3072\n\t"
        "ds_write_b128 v97, v[16:19] offset:4096\n\t"
        "ds_write_b128 v98, v[20:23] offset:5120\n\t"
        "ds_write_b128 v97, v[24:27] offset:6144\n\t"
        "ds_write_b128 v98, v[28:31] offset:7168\n\t"
        "ds_read_b64_tr_b16 a[24:25], v99 offset:0\n\t"
        "ds_read_b64_tr_b16 a[26:27], v99 offset:256\n\t"
        "ds_read_b64_tr_b16 a[28:29], v99 offset:4096\n\t"
        "ds_read_b64_tr_b16 a[30:31], v99 offset:4352\n\t"
        "s_waitcnt lgkmcnt(8)\n\t"
        "ds_read_b64_tr_b16 a[32:33], v100 offset:0\n\t"
        "ds_read_b64_tr_b16 a[34:35], v100 offset:256\n\t"
        "ds_read_b64_tr_b16 a[36:37], v100 offset:4096\n\t"
        "ds_read_b64_tr_b16 a[38:39], v100 offset:4352\n\t"
        "s_waitcnt lgkmcnt(8)\n\t"
        "ds_read_b64_tr_b16 a[40:41], v101 offset:0\n\t"
        "ds_read_b64_tr_b16 a[42:43], v101 offset:256\n\t"
        "ds_read_b64_tr_b16 a[44:45], v101 offset:4096\n\t"
        "ds_read_b64_tr_b16 a[46:47], v101 offset:4352\n\t"
        "s_waitcnt lgkmcnt(8)\n\t"
        "ds_read_b64_tr_b16 v[104:105], v102 offset:0\n\t"
        "ds_read_b64_tr_b16 v[106:107], v102 offset:256\n\t"
        "ds_read_b64_tr_b16 v[108:109], v102 offset:4096\n\t"
        "ds_read_b64_tr_b16 v[110:111], v102 offset:4352\n\t"
        "s_lshl_b32 s34, s43, 2\n\t"
        "s_add_u32 s34, s34, 4\n\t"
        "s_and_b32 s34, s34, 15\n\t"
        "s_lshl_b32 s34, s34, 19\n\t"
        "buffer_load_dwordx4 a[96:99], v96, s[16:19], s34 offen nt\n\t"
        "s_add_u32 s34, s34, 0x10000\n\t"
        "buffer_load_dwordx4 a[100:103], v96, s[16:19], s34 offen nt\n\t"
        "s_add_u32 s34, s34, 0x10000\n\t"
        "buffer_load_dwordx4 a[104:107], v96, s[16:19], s34 offen nt\n\t"
        "s_add_u32 s34, s34, 0x10000\n\t"
        "buffer_load_dwordx4 a[108:111], v96, s[16:19], s34 offen nt\n\t"
        "s_add_u32 s34, s34, 0x10000\n\t"
        "buffer_load_dwordx4 a[112:115], v96, s[16:19], s34 offen nt\n\t"
        "s_add_u32 s34, s34, 0x10000\n\t"
        "buffer_load_dwordx4 a[116:119], v96, s[16:19], s34 offen nt\n\t"
        "s_add_u32 s34, s34, 0x10000\n\t"
        "buffer_load_dwordx4 a[120:123], v96, s[16:19], s34 offen nt\n\t"
        "s_add_u32 s34, s34, 0x10000\n\t"
        "buffer_load_dwordx4 a[124:127], v96, s[16:19], s34 offen nt\n\t"
        "s_waitcnt lgkmcnt(0)\n\t"
        "ds_write_b128 v117, v[104:107] offset:2048\n\t"
        "ds_write_b128 v117, v[108:111] offset:3072\n\t"
        "s_waitcnt vmcnt(16)\n\t"
        "ds_write_b128 v97, v[32:35] offset:0\n\t"
        "ds_write_b128 v98, v[36:39] offset:1024\n\t"
        "ds_write_b128 v97, v[40:43] offset:2048\n\t"
        "ds_write_b128 v98, v[44:47] offset:3072\n\t"
        "ds_write_b128 v97, v[48:51] offset:4096\n\t"
        "ds_write_b128 v98, v[52:55] offset:5120\n\t"
        "ds_write_b128 v97, v[56:59] offset:6144\n\t"
        "ds_write_b128 v98, v[60:63] offset:7168\n\t"
        "ds_read_b64_tr_b16 a[48:49], v99 offset:0\n\t"
        "ds_read_b64_tr_b16 a[50:51], v99 offset:256\n\t"
        "ds_read_b64_tr_b16 a[52:53], v99 offset:4096\n\t"
        "ds_read_b64_tr_b16 a[54:55], v99 offset:4352\n\t"
        "s_waitcnt lgkmcnt(8)\n\t"
        "ds_read_b64_tr_b16 a[56:57], v100 offset:0\n\t"
        "ds_read_b64_tr_b16 a[58:59], v100 offset:256\n\t"
        "ds_read_b64_tr_b16 a[60:61], v100 offset:4096\n\t"
        "ds_read_b64_tr_b16 a[62:63], v100 offset:4352\n\t"
        "s_waitcnt lgkmcnt(8)\n\t"
        "ds_read_b64_tr_b16 a[64:65], v101 offset:0\n\t"
        "ds_read_b64_tr_b16 a[66:67], v101 offset:256\n\t"
        "ds_read_b64_tr_b16 a[68:69], v101 offset:4096\n\t"
        "ds_read_b64_tr_b16 a[70:71], v101 offset:4352\n\t"
        "s_waitcnt lgkmcnt(8)\n\t"
        "ds_read_b64_tr_b16 v[104:105], v102 offset:0\n\t"
        "ds_read_b64_tr_b16 v[106:107], v102 offset:256\n\t"
        "ds_read_b64_tr_b16 v[108:109], v102 offset:4096\n\t"
        "ds_read_b64_tr_b16 v[110:111], v102 offset:4352\n\t"
        "s_lshl_b32 s34, s43, 2\n\t"
        "s_add_u32 s34, s34, 5\n\t"
        "s_and_b32 s34, s34, 15\n\t"
        "s_lshl_b32 s34, s34, 19\n\t"
        "buffer_load_dwordx4 a[128:131], v96, s[16:19], s34 offen nt\n\t"
        "s_add_u32 s34, s34, 0x10000\n\t"
        "buffer_load_dwordx4 a[132:135], v96, s[16:19], s34 offen nt\n\t"
        "s_add_u32 s34, s34, 0x10000\n\t"
        "buffer_load_dwordx4 a[136:139], v96, s[16:19], s34 offen nt\n\t"
        "s_add_u32 s34, s34, 0x10000\n\t"
        "buffer_load_dwordx4 a[140:143], v96, s[16:19], s34 offen nt\n\t"
        "s_add_u32 s34, s34, 0x10000\n\t"
        "buffer_load_dwordx4 a[144:147], v96, s[16:19], s34 offen nt\n\t"
        "s_add_u32 s34, s34, 0x10000\n\t"
        "buffer_load_dwordx4 a[148:151], v96, s[16:19], s34 offen nt\n\t"
        "s_add_u32 s34, s34, 0x10000\n\t"
        "buffer_load_dwordx4 a[152:155], v96, s[16:19], s34 offen nt\n\t"
        "s_add_u32 s34, s34, 0x10000\n\t"
        "buffer_load_dwordx4 a[156:159], v96, s[16:19], s34 offen nt\n\t"
        "s_waitcnt lgkmcnt(0)\n\t"
        "ds_write_b128 v117, v[104:107] offset:4096\n\t"
        "ds_write_b128 v117, v[108:111] offset:5120\n\t"
        "s_waitcnt vmcnt(16)\n\t"
        "ds_write_b128 v97, v[64:67] offset:0\n\t"
        "ds_write_b128 v98, v[68:71] offset:1024\n\t"
        "ds_write_b128 v97, v[72:75] offset:2048\n\t"
        "ds_write_b128 v98, v[76:79] offset:3072\n\t"
        "ds_write_b128 v97, v[80:83] offset:4096\n\t"
        "ds_write_b128 v98, v[84:87] offset:5120\n\t"
        "ds_write_b128 v97, v[88:91] offset:6144\n\t"
        "ds_write_b128 v98, v[92:95] offset:7168\n\t"
        "ds_read_b64_tr_b16 a[72:73], v99 offset:0\n\t"
        "ds_read_b64_tr_b16 a[74:75], v99 offset:256\n\t"
        "ds_read_b64_tr_b16 a[76:77], v99 offset:4096\n\t"
        "ds_read_b64_tr_b16 a[78:79], v99 offset:4352\n\t"
        "s_waitcnt lgkmcnt(8)\n\t"
        "ds_read_b64_tr_b16 a[80:81], v100 offset:0\n\t"
        "ds_read_b64_tr_b16 a[82:83], v100 offset:256\n\t"
        "ds_read_b64_tr_b16 a[84:85], v100 offset:4096\n\t"
        "ds_read_b64_tr_b16 a[86:87], v100 offset:4352\n\t"
        "s_waitcnt lgkmcnt(8)\n\t"
        "ds_read_b64_tr_b16 a[88:89], v101 offset:0\n\t"
        "ds_read_b64_tr_b16 a[90:91], v101 offset:256\n\t"
        "ds_read_b64_tr_b16 a[92:93], v101 offset:4096\n\t"
        "ds_read_b64_tr_b16 a[94:95], v101 offset:4352\n\t"
        "s_waitcnt lgkmcnt(8)\n\t"
        "ds_read_b64_tr_b16 v[104:105], v102 offset:0\n\t"
        "ds_read_b64_tr_b16 v[106:107], v102 offset:256\n\t"
        "ds_read_b64_tr_b16 v[108:109], v102 offset:4096\n\t"
        "ds_read_b64_tr_b16 v[110:111], v102 offset:4352\n\t"
        "s_lshl_b32 s34, s43, 2\n\t"
        "s_add_u32 s34, s34, 6\n\t"
        "s_and_b32 s34, s34, 15\n\t"
        "s_lshl_b32 s34, s34, 19\n\t"
        "buffer_load_dwordx4 a[160:163], v96, s[16:19], s34 offen nt\n\t"
        "s_add_u32 s34, s34, 0x10000\n\t"
        "buffer_load_dwordx4 a[164:167], v96, s[16:19], s34 offen nt\n\t"
        "s_add_u32 s34, s34, 0x10000\n\t"
        "buffer_load_dwordx4 a[168:171], v96, s[16:19], s34 offen nt\n\t"
        "s_add_u32 s34, s34, 0x10000\n\t"
        "buffer_load_dwordx4 a[172:175], v96, s[16:19], s34 offen nt\n\t"
        "s_add_u32 s34, s34, 0x10000\n\t"
        "buffer_load_dwordx4 a[176:179], v96, s[16:19], s34 offen nt\n\t"
        "s_add_u32 s34, s34, 0x10000\n\t"
        "buffer_load_dwordx4 a[180:183], v96, s[16:19], s34 offen nt\n\t"
        "s_add_u32 s34, s34, 0x10000\n\t"
        "buffer_load_dwordx4 a[184:187], v96, s[16:19], s34 offen nt\n\t"
        "s_add_u32 s34, s34, 0x10000\n\t"
        "buffer_load_dwordx4 a[188:191], v96, s[16:19], s34 offen nt\n\t"
        "s_waitcnt lgkmcnt(0)\n\t"
        "ds_write_b128 v117, v[104:107] offset:6144\n\t"
        "ds_write_b128 v117, v[108:111] offset:7168\n\t"
        "s_waitcnt vmcnt(16)\n\t"
        "ds_write_b128 v97, a[96:99] offset:0\n\t"
        "ds_write_b128 v98, a[100:103] offset:1024\n\t"
        "ds_write_b128 v97, a[104:107] offset:2048\n\t"
        "ds_write_b128 v98, a[108:111] offset:3072\n\t"
        "ds_write_b128 v97, a[112:115] offset:4096\n\t"
        "ds_write_b128 v98, a[116:119] offset:5120\n\t"
        "ds_write_b128 v97, a[120:123] offset:6144\n\t"
        "ds_write_b128 v98, a[124:127] offset:7168\n\t"
        "ds_read_b64_tr_b16 a[96:97], v99 offset:0\n\t"
        "ds_read_b64_tr_b16 a[98:99], v99 offset:256\n\t"
        "ds_read_b64_tr_b16 a[100:101], v99 offset:4096\n\t"
        "ds_read_b64_tr_b16 a[102:103], v99 offset:4352\n\t"
        "s_waitcnt lgkmcnt(8)\n\t"
        "ds_read_b64_tr_b16 a[104:105], v100 offset:0\n\t"
        "ds_read_b64_tr_b16 a[106:107], v100 offset:256\n\t"
        "ds_read_b64_tr_b16 a[108:109], v100 offset:4096\n\t"
        "ds_read_b64_tr_b16 a[110:111], v100 offset:4352\n\t"
        "s_waitcnt lgkmcnt(8)\n\t"
        "ds_read_b64_tr_b16 a[112:113], v101 offset:0\n\t"
        "ds_read_b64_tr_b16 a[114:115], v101 offset:256\n\t"
        "ds_read_b64_tr_b16 a[116:117], v101 offset:4096\n\t"
        "ds_read_b64_tr_b16 a[118:119], v101 offset:4352\n\t"
        "s_waitcnt lgkmcnt(8)\n\t"
        "ds_read_b64_tr_b16 v[104:105], v102 offset:0\n\t"
        "ds_read_b64_tr_b16 v[106:107], v102 offset:256\n\t"
        "ds_read_b64_tr_b16 v[108:109], v102 offset:4096\n\t"
        "ds_read_b64_tr_b16 v[110:111], v102 offset:4352\n\t"
        "s_lshl_b32 s34, s43, 2\n\t"
        "s_add_u32 s34, s34, 7\n\t"
        "s_and_b32 s34, s34, 15\n\t"
        "s_lshl_b32 s34, s34, 19\n\t"
        "buffer_load_dwordx4 a[192:195], v96, s[16:19], s34 offen nt\n\t"
        "s_add_u32 s34, s34, 0x10000\n\t"
        "buffer_load_dwordx4 a[196:199], v96, s[16:19], s34 offen nt\n\t"
        "s_add_u32 s34, s34, 0x10000\n\t"
        "buffer_load_dwordx4 a[200:203], v96, s[16:19], s34 offen nt\n\t"
        "s_add_u32 s34, s34, 0x10000\n\t"
        "buffer_load_dwordx4 a[204:207], v96, s[16:19], s34 offen nt\n\t"
        "s_add_u32 s34, s34, 0x10000\n\t"
        "buffer_load_dwordx4 a[208:211], v96, s[16:19], s34 offen nt\n\t"
        "s_add_u32 s34, s34, 0x10000\n\t"
        "buffer_load_dwordx4 a[212:215], v96, s[16:19], s34 offen nt\n\t"
        "s_add_u32 s34, s34, 0x10000\n\t"
        "buffer_load_dwordx4 a[216:219], v96, s[16:19], s34 offen nt\n\t"
        "s_add_u32 s34, s34, 0x10000\n\t"
        "buffer_load_dwordx4 a[220:223], v96, s[16:19], s34 offen nt\n\t"
        "s_waitcnt lgkmcnt(0)\n\t"
        "ds_write_b128 v126, v[104:107] offset:0\n\t"
        "ds_write_b128 v126, v[108:111] offset:1024\n\t"
        "s_waitcnt vmcnt(16)\n\t"
        "ds_write_b128 v97, a[128:131] offset:0\n\t"
        "ds_write_b128 v98, a[132:135] offset:1024\n\t"
        "ds_write_b128 v97, a[136:139] offset:2048\n\t"
        "ds_write_b128 v98, a[140:143] offset:3072\n\t"
        "ds_write_b128 v97, a[144:147] offset:4096\n\t"
        "ds_write_b128 v98, a[148:151] offset:5120\n\t"
        "ds_write_b128 v97, a[152:155] offset:6144\n\t"
        "ds_write_b128 v98, a[156:159] offset:7168\n\t"
        "ds_read_b64_tr_b16 a[120:121], v99 offset:0\n\t"
        "ds_read_b64_tr_b16 a[122:123], v99 offset:256\n\t"
        "ds_read_b64_tr_b16 a[124:125], v99 offset:4096\n\t"
        "ds_read_b64_tr_b16 a[126:127], v99 offset:4352\n\t"
        "s_waitcnt lgkmcnt(8)\n\t"
        "ds_read_b64_tr_b16 a[128:129], v100 offset:0\n\t"
        "ds_read_b64_tr_b16 a[130:131], v100 offset:256\n\t"
        "ds_read_b64_tr_b16 a[132:133], v100 offset:4096\n\t"
        "ds_read_b64_tr_b16 a[134:135], v100 offset:4352\n\t"
        "s_waitcnt lgkmcnt(8)\n\t"
        "ds_read_b64_tr_b16 a[136:137], v101 offset:0\n\t"
        "ds_read_b64_tr_b16 a[138:139], v101 offset:256\n\t"
        "ds_read_b64_tr_b16 a[140:141], v101 offset:4096\n\t"
        "ds_read_b64_tr_b16 a[142:143], v101 offset:4352\n\t"
        "s_waitcnt lgkmcnt(8)\n\t"
        "ds_read_b64_tr_b16 v[104:105], v102 offset:0\n\t"
        "ds_read_b64_tr_b16 v[106:107], v102 offset:256\n\t"
        "ds_read_b64_tr_b16 v[108:109], v102 offset:4096\n\t"
        "ds_read_b64_tr_b16 v[110:111], v102 offset:4352\n\t"
        "s_lshl_b32 s34, s43, 2\n\t"
        "s_add_u32 s34, s34, 8\n\t"
        "s_and_b32 s34, s34, 15\n\t"
        "s_lshl_b32 s34, s34, 19\n\t"
        "buffer_load_dwordx4 a[224:227], v96, s[16:19], s34 offen nt\n\t"
        "s_add_u32 s34, s34, 0x10000\n\t"
        "buffer_load_dwordx4 a[228:231], v96, s[16:19], s34 offen nt\n\t"
        "s_add_u32 s34, s34, 0x10000\n\t"
        "buffer_load_dwordx4 a[232:235], v96, s[16:19], s34 offen nt\n\t"
        "s_add_u32 s34, s34, 0x10000\n\t"
        "buffer_load_dwordx4 a[236:239], v96, s[16:19], s34 offen nt\n\t"
        "s_add_u32 s34, s34, 0x10000\n\t"
        "buffer_load_dwordx4 a[240:243], v96, s[16:19], s34 offen nt\n\t"
        "s_add_u32 s34, s34, 0x10000\n\t"
        "buffer_load_dwordx4 a[244:247], v96, s[16:19], s34 offen nt\n\t"
        "s_add_u32 s34, s34, 0x10000\n\t"
        "buffer_load_dwordx4 a[248:251], v96, s[16:19], s34 offen nt\n\t"
        "s_add_u32 s34, s34, 0x10000\n\t"
        "buffer_load_dwordx4 a[252:255], v96, s[16:19], s34 offen nt\n\t"
        "s_waitcnt lgkmcnt(0)\n\t"
        "ds_write_b128 v126, v[104:107] offset:2048\n\t"
        "ds_write_b128 v126, v[108:111] offset:3072\n\t"
        "s_waitcnt vmcnt(16)\n\t"
        "ds_write_b128 v97, a[160:163] offset:0\n\t"
        "ds_write_b128 v98, a[164:167] offset:1024\n\t"
        "ds_write_b128 v97, a[168:171] offset:2048\n\t"
        "ds_write_b128 v98, a[172:175] offset:3072\n\t"
        "ds_write_b128 v97, a[176:179] offset:4096\n\t"
        "ds_write_b128 v98, a[180:183] offset:5120\n\t"
        "ds_write_b128 v97, a[184:187] offset:6144\n\t"
        "ds_write_b128 v98, a[188:191] offset:7168\n\t"
        "ds_read_b64_tr_b16 a[144:145], v99 offset:0\n\t"
        "ds_read_b64_tr_b16 a[146:147], v99 offset:256\n\t"
        "ds_read_b64_tr_b16 a[148:149], v99 offset:4096\n\t"
        "ds_read_b64_tr_b16 a[150:151], v99 offset:4352\n\t"
        "s_waitcnt lgkmcnt(8)\n\t"
        "ds_read_b64_tr_b16 a[152:153], v100 offset:0\n\t"
        "ds_read_b64_tr_b16 a[154:155], v100 offset:256\n\t"
        "ds_read_b64_tr_b16 a[156:157], v100 offset:4096\n\t"
        "ds_read_b64_tr_b16 a[158:159], v100 offset:4352\n\t"
        "s_waitcnt lgkmcnt(8)\n\t"
        "ds_read_b64_tr_b16 a[160:161], v101 offset:0\n\t"
        "ds_read_b64_tr_b16 a[162:163], v101 offset:256\n\t"
        "ds_read_b64_tr_b16 a[164:165], v101 offset:4096\n\t"
        "ds_read_b64_tr_b16 a[166:167], v101 offset:4352\n\t"
        "s_waitcnt lgkmcnt(8)\n\t"
        "ds_read_b64_tr_b16 v[104:105], v102 offset:0\n\t"
        "ds_read_b64_tr_b16 v[106:107], v102 offset:256\n\t"
        "ds_read_b64_tr_b16 v[108:109], v102 offset:4096\n\t"
        "ds_read_b64_tr_b16 v[110:111], v102 offset:4352\n\t"
        "s_lshl_b32 s34, s43, 2\n\t"
        "s_add_u32 s34, s34, 9\n\t"
        "s_and_b32 s34, s34, 15\n\t"
        "s_lshl_b32 s34, s34, 19\n\t"
        "buffer_load_dwordx4 v[128:131], v96, s[16:19], s34 offen nt\n\t"
        "s_add_u32 s34, s34, 0x10000\n\t"
        "buffer_load_dwordx4 v[132:135], v96, s[16:19], s34 offen nt\n\t"
        "s_add_u32 s34, s34, 0x10000\n\t"
        "buffer_load_dwordx4 v[136:139], v96, s[16:19], s34 offen nt\n\t"
        "s_add_u32 s34, s34, 0x10000\n\t"
        "buffer_load_dwordx4 v[140:143], v96, s[16:19], s34 offen nt\n\t"
        "s_add_u32 s34, s34, 0x10000\n\t"
        "buffer_load_dwordx4 v[144:147], v96, s[16:19], s34 offen nt\n\t"
        "s_add_u32 s34, s34, 0x10000\n\t"
        "buffer_load_dwordx4 v[148:151], v96, s[16:19], s34 offen nt\n\t"
        "s_add_u32 s34, s34, 0x10000\n\t"
        "buffer_load_dwordx4 v[152:155], v96, s[16:19], s34 offen nt\n\t"
        "s_add_u32 s34, s34, 0x10000\n\t"
        "buffer_load_dwordx4 v[156:159], v96, s[16:19], s34 offen nt\n\t"
        "s_waitcnt lgkmcnt(0)\n\t"
        "ds_write_b128 v126, v[104:107] offset:4096\n\t"
        "ds_write_b128 v126, v[108:111] offset:5120\n\t"
        "s_waitcnt vmcnt(16)\n\t"
        "ds_write_b128 v97, a[192:195] offset:0\n\t"
        "ds_write_b128 v98, a[196:199] offset:1024\n\t"
        "ds_write_b128 v97, a[200:203] offset:2048\n\t"
        "ds_write_b128 v98, a[204:207] offset:3072\n\t"
        "ds_write_b128 v97, a[208:211] offset:4096\n\t"
        "ds_write_b128 v98, a[212:215] offset:5120\n\t"
        "ds_write_b128 v97, a[216:219] offset:6144\n\t"
        "ds_write_b128 v98, a[220:223] offset:7168\n\t"
        "ds_read_b64_tr_b16 a[168:169], v99 offset:0\n\t"
        "ds_read_b64_tr_b16 a[170:171], v99 offset:256\n\t"
        "ds_read_b64_tr_b16 a[172:173], v99 offset:4096\n\t"
        "ds_read_b64_tr_b16 a[174:175], v99 offset:4352\n\t"
        "s_waitcnt lgkmcnt(8)\n\t"
        "ds_read_b64_tr_b16 a[176:177], v100 offset:0\n\t"
        "ds_read_b64_tr_b16 a[178:179], v100 offset:256\n\t"
        "ds_read_b64_tr_b16 a[180:181], v100 offset:4096\n\t"
        "ds_read_b64_tr_b16 a[182:183], v100 offset:4352\n\t"
        "s_waitcnt lgkmcnt(8)\n\t"
        "ds_read_b64_tr_b16 a[184:185], v101 offset:0\n\t"
        "ds_read_b64_tr_b16 a[186:187], v101 offset:256\n\t"
        "ds_read_b64_tr_b16 a[188:189], v101 offset:4096\n\t"
        "ds_read_b64_tr_b16 a[190:191], v101 offset:4352\n\t"
        "s_waitcnt lgkmcnt(8)\n\t"
        "ds_read_b64_tr_b16 v[104:105], v102 offset:0\n\t"
        "ds_read_b64_tr_b16 v[106:107], v102 offset:256\n\t"
        "ds_read_b64_tr_b16 v[108:109], v102 offset:4096\n\t"
        "ds_read_b64_tr_b16 v[110:111], v102 offset:4352\n\t"
        "s_lshl_b32 s34, s43, 2\n\t"
        "s_add_u32 s34, s34, 10\n\t"
        "s_and_b32 s34, s34, 15\n\t"
        "s_lshl_b32 s34, s34, 19\n\t"
        "buffer_load_dwordx4 v[160:163], v96, s[16:19], s34 offen nt\n\t"
        "s_add_u32 s34, s34, 0x10000\n\t"
        "buffer_load_dwordx4 v[164:167], v96, s[16:19], s34 offen nt\n\t"
        "s_add_u32 s34, s34, 0x10000\n\t"
        "buffer_load_dwordx4 v[168:171], v96, s[16:19], s34 offen nt\n\t"
        "s_add_u32 s34, s34, 0x10000\n\t"
        "buffer_load_dwordx4 v[172:175], v96, s[16:19], s34 offen nt\n\t"
        "s_add_u32 s34, s34, 0x10000\n\t"
        "buffer_load_dwordx4 v[176:179], v96, s[16:19], s34 offen nt\n\t"
        "s_add_u32 s34, s34, 0x10000\n\t"
        "buffer_load_dwordx4 v[180:183], v96, s[16:19], s34 offen nt\n\t"
        "s_add_u32 s34, s34, 0x10000\n\t"
        "buffer_load_dwordx4 v[184:187], v96, s[16:19], s34 offen nt\n\t"
        "s_add_u32 s34, s34, 0x10000\n\t"
        "buffer_load_dwordx4 v[188:191], v96, s[16:19], s34 offen nt\n\t"
        "s_waitcnt lgkmcnt(0)\n\t"
        "ds_write_b128 v126, v[104:107] offset:6144\n\t"
        "ds_write_b128 v126, v[108:111] offset:7168\n\t"
        "s_waitcnt vmcnt(16)\n\t"
        "ds_write_b128 v97, a[224:227] offset:0\n\t"
        "ds_write_b128 v98, a[228:231] offset:1024\n\t"
        "ds_write_b128 v97, a[232:235] offset:2048\n\t"
        "ds_write_b128 v98, a[236:239] offset:3072\n\t"
        "ds_write_b128 v97, a[240:243] offset:4096\n\t"
        "ds_write_b128 v98, a[244:247] offset:5120\n\t"
        "ds_write_b128 v97, a[248:251] offset:6144\n\t"
        "ds_write_b128 v98, a[252:255] offset:7168\n\t"
        "ds_read_b64_tr_b16 a[192:193], v99 offset:0\n\t"
        "ds_read_b64_tr_b16 a[194:195], v99 offset:256\n\t"
        "ds_read_b64_tr_b16 a[196:197], v99 offset:4096\n\t"
        "ds_read_b64_tr_b16 a[198:199], v99 offset:4352\n\t"
        "s_waitcnt lgkmcnt(8)\n\t"
        "ds_read_b64_tr_b16 a[200:201], v100 offset:0\n\t"
        "ds_read_b64_tr_b16 a[202:203], v100 offset:256\n\t"
        "ds_read_b64_tr_b16 a[204:205], v100 offset:4096\n\t"
        "ds_read_b64_tr_b16 a[206:207], v100 offset:4352\n\t"
        "s_waitcnt lgkmcnt(8)\n\t"
        "ds_read_b64_tr_b16 a[208:209], v101 offset:0\n\t"
        "ds_read_b64_tr_b16 a[210:211], v101 offset:256\n\t"
        "ds_read_b64_tr_b16 a[212:213], v101 offset:4096\n\t"
        "ds_read_b64_tr_b16 a[214:215], v101 offset:4352\n\t"
        "s_waitcnt lgkmcnt(8)\n\t"
        "ds_read_b64_tr_b16 v[104:105], v102 offset:0\n\t"
        "ds_read_b64_tr_b16 v[106:107], v102 offset:256\n\t"
        "ds_read_b64_tr_b16 v[108:109], v102 offset:4096\n\t"
        "ds_read_b64_tr_b16 v[110:111], v102 offset:4352\n\t"
        "s_lshl_b32 s34, s43, 2\n\t"
        "s_add_u32 s34, s34, 11\n\t"
        "s_and_b32 s34, s34, 15\n\t"
        "s_lshl_b32 s34, s34, 19\n\t"
        "buffer_load_dwordx4 v[192:195], v96, s[16:19], s34 offen nt\n\t"
        "s_add_u32 s34, s34, 0x10000\n\t"
        "buffer_load_dwordx4 v[196:199], v96, s[16:19], s34 offen nt\n\t"
        "s_add_u32 s34, s34, 0x10000\n\t"
        "buffer_load_dwordx4 v[200:203], v96, s[16:19], s34 offen nt\n\t"
        "s_add_u32 s34, s34, 0x10000\n\t"
        "buffer_load_dwordx4 v[204:207], v96, s[16:19], s34 offen nt\n\t"
        "s_add_u32 s34, s34, 0x10000\n\t"
        "buffer_load_dwordx4 v[208:211], v96, s[16:19], s34 offen nt\n\t"
        "s_add_u32 s34, s34, 0x10000\n\t"
        "buffer_load_dwordx4 v[212:215], v96, s[16:19], s34 offen nt\n\t"
        "s_add_u32 s34, s34, 0x10000\n\t"
        "buffer_load_dwordx4 v[216:219], v96, s[16:19], s34 offen nt\n\t"
        "s_add_u32 s34, s34, 0x10000\n\t"
        "buffer_load_dwordx4 v[220:223], v96, s[16:19], s34 offen nt\n\t"
        "s_waitcnt lgkmcnt(0)\n\t"
        "ds_write_b128 v117, v[104:107] offset:8192\n\t"
        "ds_write_b128 v117, v[108:111] offset:9216\n\t"
        "s_waitcnt vmcnt(16)\n\t"
        "ds_write_b128 v97, v[128:131] offset:0\n\t"
        "ds_write_b128 v98, v[132:135] offset:1024\n\t"
        "ds_write_b128 v97, v[136:139] offset:2048\n\t"
        "ds_write_b128 v98, v[140:143] offset:3072\n\t"
        "ds_write_b128 v97, v[144:147] offset:4096\n\t"
        "ds_write_b128 v98, v[148:151] offset:5120\n\t"
        "ds_write_b128 v97, v[152:155] offset:6144\n\t"
        "ds_write_b128 v98, v[156:159] offset:7168\n\t"
        "ds_read_b64_tr_b16 a[216:217], v99 offset:0\n\t"
        "ds_read_b64_tr_b16 a[218:219], v99 offset:256\n\t"
        "ds_read_b64_tr_b16 a[220:221], v99 offset:4096\n\t"
        "ds_read_b64_tr_b16 a[222:223], v99 offset:4352\n\t"
        "s_waitcnt lgkmcnt(8)\n\t"
        "ds_read_b64_tr_b16 a[224:225], v100 offset:0\n\t"
        "ds_read_b64_tr_b16 a[226:227], v100 offset:256\n\t"
        "ds_read_b64_tr_b16 a[228:229], v100 offset:4096\n\t"
        "ds_read_b64_tr_b16 a[230:231], v100 offset:4352\n\t"
        "s_waitcnt lgkmcnt(8)\n\t"
        "ds_read_b64_tr_b16 a[232:233], v101 offset:0\n\t"
        "ds_read_b64_tr_b16 a[234:235], v101 offset:256\n\t"
        "ds_read_b64_tr_b16 a[236:237], v101 offset:4096\n\t"
        "ds_read_b64_tr_b16 a[238:239], v101 offset:4352\n\t"
        "s_waitcnt lgkmcnt(8)\n\t"
        "ds_read_b64_tr_b16 v[104:105], v102 offset:0\n\t"
        "ds_read_b64_tr_b16 v[106:107], v102 offset:256\n\t"
        "ds_read_b64_tr_b16 v[108:109], v102 offset:4096\n\t"
        "ds_read_b64_tr_b16 v[110:111], v102 offset:4352\n\t"
        "s_lshl_b32 s34, s43, 2\n\t"
        "s_add_u32 s34, s34, 12\n\t"
        "s_and_b32 s34, s34, 15\n\t"
        "s_lshl_b32 s34, s34, 19\n\t"
        "buffer_load_dwordx4 v[224:227], v96, s[16:19], s34 offen nt\n\t"
        "s_add_u32 s34, s34, 0x10000\n\t"
        "buffer_load_dwordx4 v[228:231], v96, s[16:19], s34 offen nt\n\t"
        "s_add_u32 s34, s34, 0x10000\n\t"
        "buffer_load_dwordx4 v[232:235], v96, s[16:19], s34 offen nt\n\t"
        "s_add_u32 s34, s34, 0x10000\n\t"
        "buffer_load_dwordx4 v[236:239], v96, s[16:19], s34 offen nt\n\t"
        "s_add_u32 s34, s34, 0x10000\n\t"
        "buffer_load_dwordx4 v[240:243], v96, s[16:19], s34 offen nt\n\t"
        "s_add_u32 s34, s34, 0x10000\n\t"
        "buffer_load_dwordx4 v[244:247], v96, s[16:19], s34 offen nt\n\t"
        "s_add_u32 s34, s34, 0x10000\n\t"
        "buffer_load_dwordx4 v[248:251], v96, s[16:19], s34 offen nt\n\t"
        "s_add_u32 s34, s34, 0x10000\n\t"
        "buffer_load_dwordx4 v[252:255], v96, s[16:19], s34 offen nt\n\t"
        "s_waitcnt lgkmcnt(0)\n\t"
        "ds_write_b128 v117, v[104:107] offset:10240\n\t"
        "ds_write_b128 v117, v[108:111] offset:11264\n\t"
        "s_waitcnt vmcnt(16)\n\t"
        "ds_write_b128 v97, v[160:163] offset:0\n\t"
        "ds_write_b128 v98, v[164:167] offset:1024\n\t"
        "ds_write_b128 v97, v[168:171] offset:2048\n\t"
        "ds_write_b128 v98, v[172:175] offset:3072\n\t"
        "ds_write_b128 v97, v[176:179] offset:4096\n\t"
        "ds_write_b128 v98, v[180:183] offset:5120\n\t"
        "ds_write_b128 v97, v[184:187] offset:6144\n\t"
        "ds_write_b128 v98, v[188:191] offset:7168\n\t"
        "ds_read_b64_tr_b16 a[240:241], v99 offset:0\n\t"
        "ds_read_b64_tr_b16 a[242:243], v99 offset:256\n\t"
        "ds_read_b64_tr_b16 a[244:245], v99 offset:4096\n\t"
        "ds_read_b64_tr_b16 a[246:247], v99 offset:4352\n\t"
        "s_waitcnt lgkmcnt(8)\n\t"
        "ds_read_b64_tr_b16 a[248:249], v100 offset:0\n\t"
        "ds_read_b64_tr_b16 a[250:251], v100 offset:256\n\t"
        "ds_read_b64_tr_b16 a[252:253], v100 offset:4096\n\t"
        "ds_read_b64_tr_b16 a[254:255], v100 offset:4352\n\t"
        "s_waitcnt lgkmcnt(8)\n\t"
        "ds_read_b64_tr_b16 v[128:129], v101 offset:0\n\t"
        "ds_read_b64_tr_b16 v[130:131], v101 offset:256\n\t"
        "ds_read_b64_tr_b16 v[132:133], v101 offset:4096\n\t"
        "ds_read_b64_tr_b16 v[134:135], v101 offset:4352\n\t"
        "s_waitcnt lgkmcnt(8)\n\t"
        "ds_read_b64_tr_b16 v[104:105], v102 offset:0\n\t"
        "ds_read_b64_tr_b16 v[106:107], v102 offset:256\n\t"
        "ds_read_b64_tr_b16 v[108:109], v102 offset:4096\n\t"
        "ds_read_b64_tr_b16 v[110:111], v102 offset:4352\n\t"
        "s_lshl_b32 s34, s43, 2\n\t"
        "s_add_u32 s34, s34, 13\n\t"
        "s_and_b32 s34, s34, 15\n\t"
        "s_lshl_b32 s34, s34, 19\n\t"
        "buffer_load_dwordx4 v[0:3], v96, s[16:19], s34 offen nt\n\t"
        "s_add_u32 s34, s34, 0x10000\n\t"
        "buffer_load_dwordx4 v[4:7], v96, s[16:19], s34 offen nt\n\t"
        "s_add_u32 s34, s34, 0x10000\n\t"
        "buffer_load_dwordx4 v[8:11], v96, s[16:19], s34 offen nt\n\t"
        "s_add_u32 s34, s34, 0x10000\n\t"
        "buffer_load_dwordx4 v[12:15], v96, s[16:19], s34 offen nt\n\t"
        "s_add_u32 s34, s34, 0x10000\n\t"
        "buffer_load_dwordx4 v[16:19], v96, s[16:19], s34 offen nt\n\t"
        "s_add_u32 s34, s34, 0x10000\n\t"
        "buffer_load_dwordx4 v[20:23], v96, s[16:19], s34 offen nt\n\t"
        "s_add_u32 s34, s34, 0x10000\n\t"
        "buffer_load_dwordx4 v[24:27], v96, s[16:19], s34 offen nt\n\t"
        "s_add_u32 s34, s34, 0x10000\n\t"
        "buffer_load_dwordx4 v[28:31], v96, s[16:19], s34 offen nt\n\t"
        "s_waitcnt lgkmcnt(0)\n\t"
        "ds_write_b128 v117, v[104:107] offset:12288\n\t"
        "ds_write_b128 v117, v[108:111] offset:13312\n\t"
        "s_waitcnt vmcnt(16)\n\t"
        "ds_write_b128 v97, v[192:195] offset:0\n\t"
        "ds_write_b128 v98, v[196:199] offset:1024\n\t"
        "ds_write_b128 v97, v[200:203] offset:2048\n\t"
        "ds_write_b128 v98, v[204:207] offset:3072\n\t"
        "ds_write_b128 v97, v[208:211] offset:4096\n\t"
        "ds_write_b128 v98, v[212:215] offset:5120\n\t"
        "ds_write_b128 v97, v[216:219] offset:6144\n\t"
        "ds_write_b128 v98, v[220:223] offset:7168\n\t"
        "ds_read_b64_tr_b16 v[136:137], v99 offset:0\n\t"
        "ds_read_b64_tr_b16 v[138:139], v99 offset:256\n\t"
        "ds_read_b64_tr_b16 v[140:141], v99 offset:4096\n\t"
        "ds_read_b64_tr_b16 v[142:143], v99 offset:4352\n\t"
        "s_waitcnt lgkmcnt(8)\n\t"
        "ds_read_b64_tr_b16 v[144:145], v100 offset:0\n\t"
        "ds_read_b64_tr_b16 v[146:147], v100 offset:256\n\t"
        "ds_read_b64_tr_b16 v[148:149], v100 offset:4096\n\t"
        "ds_read_b64_tr_b16 v[150:151], v100 offset:4352\n\t"
        "s_waitcnt lgkmcnt(8)\n\t"
        "ds_read_b64_tr_b16 v[152:153], v101 offset:0\n\t"
        "ds_read_b64_tr_b16 v[154:155], v101 offset:256\n\t"
        "ds_read_b64_tr_b16 v[156:157], v101 offset:4096\n\t"
        "ds_read_b64_tr_b16 v[158:159], v101 offset:4352\n\t"
        "s_waitcnt lgkmcnt(8)\n\t"
        "ds_read_b64_tr_b16 v[104:105], v102 offset:0\n\t"
        "ds_read_b64_tr_b16 v[106:107], v102 offset:256\n\t"
        "ds_read_b64_tr_b16 v[108:109], v102 offset:4096\n\t"
        "ds_read_b64_tr_b16 v[110:111], v102 offset:4352\n\t"
        "s_lshl_b32 s34, s43, 2\n\t"
        "s_add_u32 s34, s34, 14\n\t"
        "s_and_b32 s34, s34, 15\n\t"
        "s_lshl_b32 s34, s34, 19\n\t"
        "buffer_load_dwordx4 v[32:35], v96, s[16:19], s34 offen nt\n\t"
        "s_add_u32 s34, s34, 0x10000\n\t"
        "buffer_load_dwordx4 v[36:39], v96, s[16:19], s34 offen nt\n\t"
        "s_add_u32 s34, s34, 0x10000\n\t"
        "buffer_load_dwordx4 v[40:43], v96, s[16:19], s34 offen nt\n\t"
        "s_add_u32 s34, s34, 0x10000\n\t"
        "buffer_load_dwordx4 v[44:47], v96, s[16:19], s34 offen nt\n\t"
        "s_add_u32 s34, s34, 0x10000\n\t"
        "buffer_load_dwordx4 v[48:51], v96, s[16:19], s34 offen nt\n\t"
        "s_add_u32 s34, s34, 0x10000\n\t"
        "buffer_load_dwordx4 v[52:55], v96, s[16:19], s34 offen nt\n\t"
        "s_add_u32 s34, s34, 0x10000\n\t"
        "buffer_load_dwordx4 v[56:59], v96, s[16:19], s34 offen nt\n\t"
        "s_add_u32 s34, s34, 0x10000\n\t"
        "buffer_load_dwordx4 v[60:63], v96, s[16:19], s34 offen nt\n\t"
        "s_waitcnt lgkmcnt(0)\n\t"
        "ds_write_b128 v117, v[104:107] offset:14336\n\t"
        "ds_write_b128 v117, v[108:111] offset:15360\n\t"
        "s_waitcnt vmcnt(16)\n\t"
        "ds_write_b128 v97, v[224:227] offset:0\n\t"
        "ds_write_b128 v98, v[228:231] offset:1024\n\t"
        "ds_write_b128 v97, v[232:235] offset:2048\n\t"
        "ds_write_b128 v98, v[236:239] offset:3072\n\t"
        "ds_write_b128 v97, v[240:243] offset:4096\n\t"
        "ds_write_b128 v98, v[244:247] offset:5120\n\t"
        "ds_write_b128 v97, v[248:251] offset:6144\n\t"
        "ds_write_b128 v98, v[252:255] offset:7168\n\t"
        "ds_read_b64_tr_b16 v[160:161], v99 offset:0\n\t"
        "ds_read_b64_tr_b16 v[162:163], v99 offset:256\n\t"
        "ds_read_b64_tr_b16 v[164:165], v99 offset:4096\n\t"
        "ds_read_b64_tr_b16 v[166:167], v99 offset:4352\n\t"
        "s_waitcnt lgkmcnt(8)\n\t"
        "ds_read_b64_tr_b16 v[168:169], v100 offset:0\n\t"
        "ds_read_b64_tr_b16 v[170:171], v100 offset:256\n\t"
        "ds_read_b64_tr_b16 v[172:173], v100 offset:4096\n\t"
        "ds_read_b64_tr_b16 v[174:175], v100 offset:4352\n\t"
        "s_waitcnt lgkmcnt(8)\n\t"
        "ds_read_b64_tr_b16 v[176:177], v101 offset:0\n\t"
        "ds_read_b64_tr_b16 v[178:179], v101 offset:256\n\t"
        "ds_read_b64_tr_b16 v[180:181], v101 offset:4096\n\t"
        "ds_read_b64_tr_b16 v[182:183], v101 offset:4352\n\t"
        "s_waitcnt lgkmcnt(8)\n\t"
        "ds_read_b64_tr_b16 v[104:105], v102 offset:0\n\t"
        "ds_read_b64_tr_b16 v[106:107], v102 offset:256\n\t"
        "ds_read_b64_tr_b16 v[108:109], v102 offset:4096\n\t"
        "ds_read_b64_tr_b16 v[110:111], v102 offset:4352\n\t"
        "s_lshl_b32 s34, s43, 2\n\t"
        "s_add_u32 s34, s34, 15\n\t"
        "s_and_b32 s34, s34, 15\n\t"
        "s_lshl_b32 s34, s34, 19\n\t"
        "buffer_load_dwordx4 v[64:67], v96, s[16:19], s34 offen nt\n\t"
        "s_add_u32 s34, s34, 0x10000\n\t"
        "buffer_load_dwordx4 v[68:71], v96, s[16:19], s34 offen nt\n\t"
        "s_add_u32 s34, s34, 0x10000\n\t"
        "buffer_load_dwordx4 v[72:75], v96, s[16:19], s34 offen nt\n\t"
        "s_add_u32 s34, s34, 0x10000\n\t"
        "buffer_load_dwordx4 v[76:79], v96, s[16:19], s34 offen nt\n\t"
        "s_add_u32 s34, s34, 0x10000\n\t"
        "buffer_load_dwordx4 v[80:83], v96, s[16:19], s34 offen nt\n\t"
        "s_add_u32 s34, s34, 0x10000\n\t"
        "buffer_load_dwordx4 v[84:87], v96, s[16:19], s34 offen nt\n\t"
        "s_add_u32 s34, s34, 0x10000\n\t"
        "buffer_load_dwordx4 v[88:91], v96, s[16:19], s34 offen nt\n\t"
        "s_add_u32 s34, s34, 0x10000\n\t"
        "buffer_load_dwordx4 v[92:95], v96, s[16:19], s34 offen nt\n\t"
        "s_waitcnt lgkmcnt(0)\n\t"
        "ds_write_b128 v117, v[104:107] offset:16384\n\t"
        "ds_write_b128 v117, v[108:111] offset:17408\n\t"
        "s_waitcnt vmcnt(16)\n\t"
        "ds_write_b128 v97, v[0:3] offset:0\n\t"
        "ds_write_b128 v98, v[4:7] offset:1024\n\t"
        "ds_write_b128 v97, v[8:11] offset:2048\n\t"
        "ds_write_b128 v98, v[12:15] offset:3072\n\t"
        "ds_write_b128 v97, v[16:19] offset:4096\n\t"
        "ds_write_b128 v98, v[20:23] offset:5120\n\t"
        "ds_write_b128 v97, v[24:27] offset:6144\n\t"
        "ds_write_b128 v98, v[28:31] offset:7168\n\t"
        "ds_read_b64_tr_b16 v[184:185], v99 offset:0\n\t"
        "ds_read_b64_tr_b16 v[186:187], v99 offset:256\n\t"
        "ds_read_b64_tr_b16 v[188:189], v99 offset:4096\n\t"
        "ds_read_b64_tr_b16 v[190:191], v99 offset:4352\n\t"
        "s_waitcnt lgkmcnt(8)\n\t"
        "ds_read_b64_tr_b16 v[192:193], v100 offset:0\n\t"
        "ds_read_b64_tr_b16 v[194:195], v100 offset:256\n\t"
        "ds_read_b64_tr_b16 v[196:197], v100 offset:4096\n\t"
        "ds_read_b64_tr_b16 v[198:199], v100 offset:4352\n\t"
        "s_waitcnt lgkmcnt(8)\n\t"
        "ds_read_b64_tr_b16 v[200:201], v101 offset:0\n\t"
        "ds_read_b64_tr_b16 v[202:203], v101 offset:256\n\t"
        "ds_read_b64_tr_b16 v[204:205], v101 offset:4096\n\t"
        "ds_read_b64_tr_b16 v[206:207], v101 offset:4352\n\t"
        "s_waitcnt lgkmcnt(8)\n\t"
        "ds_read_b64_tr_b16 v[104:105], v102 offset:0\n\t"
        "ds_read_b64_tr_b16 v[106:107], v102 offset:256\n\t"
        "ds_read_b64_tr_b16 v[108:109], v102 offset:4096\n\t"
        "ds_read_b64_tr_b16 v[110:111], v102 offset:4352\n\t"
        "s_waitcnt lgkmcnt(0)\n\t"
        "ds_write_b128 v117, v[104:107] offset:18432\n\t"
        "ds_write_b128 v117, v[108:111] offset:19456\n\t"
        "s_waitcnt vmcnt(8)\n\t"
        "ds_write_b128 v97, v[32:35] offset:0\n\t"
        "ds_write_b128 v98, v[36:39] offset:1024\n\t"
        "ds_write_b128 v97, v[40:43] offset:2048\n\t"
        "ds_write_b128 v98, v[44:47] offset:3072\n\t"
        "ds_write_b128 v97, v[48:51] offset:4096\n\t"
        "ds_write_b128 v98, v[52:55] offset:5120\n\t"
        "ds_write_b128 v97, v[56:59] offset:6144\n\t"
        "ds_write_b128 v98, v[60:63] offset:7168\n\t"
        "ds_read_b64_tr_b16 v[208:209], v99 offset:0\n\t"
        "ds_read_b64_tr_b16 v[210:211], v99 offset:256\n\t"
        "ds_read_b64_tr_b16 v[212:213], v99 offset:4096\n\t"
        "ds_read_b64_tr_b16 v[214:215], v99 offset:4352\n\t"
        "s_waitcnt lgkmcnt(8)\n\t"
        "ds_read_b64_tr_b16 v[216:217], v100 offset:0\n\t"
        "ds_read_b64_tr_b16 v[218:219], v100 offset:256\n\t"
        "ds_read_b64_tr_b16 v[220:221], v100 offset:4096\n\t"
        "ds_read_b64_tr_b16 v[222:223], v100 offset:4352\n\t"
        "s_waitcnt lgkmcnt(8)\n\t"
        "ds_read_b64_tr_b16 v[224:225], v101 offset:0\n\t"
        "ds_read_b64_tr_b16 v[226:227], v101 offset:256\n\t"
        "ds_read_b64_tr_b16 v[228:229], v101 offset:4096\n\t"
        "ds_read_b64_tr_b16 v[230:231], v101 offset:4352\n\t"
        "s_waitcnt lgkmcnt(8)\n\t"
        "ds_read_b64_tr_b16 v[104:105], v102 offset:0\n\t"
        "ds_read_b64_tr_b16 v[106:107], v102 offset:256\n\t"
        "ds_read_b64_tr_b16 v[108:109], v102 offset:4096\n\t"
        "ds_read_b64_tr_b16 v[110:111], v102 offset:4352\n\t"
        "s_waitcnt lgkmcnt(0)\n\t"
        "ds_write_b128 v117, v[104:107] offset:20480\n\t"
        "ds_write_b128 v117, v[108:111] offset:21504\n\t"
        "s_waitcnt vmcnt(0)\n\t"
        "ds_write_b128 v97, v[64:67] offset:0\n\t"
        "ds_write_b128 v98, v[68:71] offset:1024\n\t"
        "ds_write_b128 v97, v[72:75] offset:2048\n\t"
        "ds_write_b128 v98, v[76:79] offset:3072\n\t"
        "ds_write_b128 v97, v[80:83] offset:4096\n\t"
        "ds_write_b128 v98, v[84:87] offset:5120\n\t"
        "ds_write_b128 v97, v[88:91] offset:6144\n\t"
        "ds_write_b128 v98, v[92:95] offset:7168\n\t"
        "ds_read_b64_tr_b16 v[232:233], v99 offset:0\n\t"
        "ds_read_b64_tr_b16 v[234:235], v99 offset:256\n\t"
        "ds_read_b64_tr_b16 v[236:237], v99 offset:4096\n\t"
        "ds_read_b64_tr_b16 v[238:239], v99 offset:4352\n\t"
        "s_waitcnt lgkmcnt(8)\n\t"
        "ds_read_b64_tr_b16 v[240:241], v100 offset:0\n\t"
        "ds_read_b64_tr_b16 v[242:243], v100 offset:256\n\t"
        "ds_read_b64_tr_b16 v[244:245], v100 offset:4096\n\t"
        "ds_read_b64_tr_b16 v[246:247], v100 offset:4352\n\t"
        "s_waitcnt lgkmcnt(8)\n\t"
        "ds_read_b64_tr_b16 v[248:249], v101 offset:0\n\t"
        "ds_read_b64_tr_b16 v[250:251], v101 offset:256\n\t"
        "ds_read_b64_tr_b16 v[252:253], v101 offset:4096\n\t"
        "ds_read_b64_tr_b16 v[254:255], v101 offset:4352\n\t"
        "s_waitcnt lgkmcnt(8)\n\t"
        "ds_read_b64_tr_b16 v[104:105], v102 offset:0\n\t"
        "ds_read_b64_tr_b16 v[106:107], v102 offset:256\n\t"
        "ds_read_b64_tr_b16 v[108:109], v102 offset:4096\n\t"
        "ds_read_b64_tr_b16 v[110:111], v102 offset:4352\n\t"
        "s_mov_b32 s35, 33792\n\t"
        "buffer_load_dword v0, off, s[12:15], s35 sc1\n\t"
        "s_lshl_b32 s35, s44, 10\n\t"
        "v_lshlrev_b32_e32 v1, 2, v122\n\t"
        "v_and_b32_e32 v1, 0x3c0, v1\n\t"
        "buffer_load_dword v1, v1, s[12:15], s35 offen offset:0 sc1\n\t"
        "s_waitcnt lgkmcnt(0)\n\t"
        "ds_write_b128 v126, v[104:107] offset:8192\n\t"
        "ds_write_b128 v126, v[108:111] offset:9216\n\t"
        "s_waitcnt lgkmcnt(0)\n\t"
        "v_mov_b32_e32 v110, v103\n\t"
        "ds_read_b128 v[64:67], v126 offset:0\n\t"
        "ds_read_b128 v[68:71], v126 offset:1024\n\t"
        "ds_read_b128 v[72:75], v126 offset:2048\n\t"
        "ds_read_b128 v[76:79], v126 offset:3072\n\t"
        "ds_read_b128 v[80:83], v126 offset:4096\n\t"
        "ds_read_b128 v[84:87], v126 offset:5120\n\t"
        "ds_read_b128 v[88:91], v126 offset:6144\n\t"
        "ds_read_b128 v[92:95], v126 offset:7168\n\t"
        "ds_read_b128 v[96:99], v126 offset:8192\n\t"
        "ds_read_b128 v[100:103], v126 offset:9216\n\t"
        "s_waitcnt lgkmcnt(0)\n\t"
        "v_mov_b32_e32 v126, 0\n\t"
        "s_barrier\n\t"
        "s_mov_b32 s48, 0\n\t"
        "s_mov_b32 s54, 0\n\t"
        "s_movk_i32 s56, 24576\n\t"
        "s_and_b32 s58, s43, 1\n\t"
        "s_lshl_b32 s58, s58, 12\n\t"
        "s_lshl_b32 s57, s44, 5\n\t"
        "s_add_u32 s57, s57, s43\n\t"
        "s_lshl_b32 s57, s57, 2\n\t"
        "s_add_u32 s57, s57, 458752\n\t"
        "s_waitcnt vmcnt(0)\n\t"
        "v_mov_b32_e32 v111, v1\n\t"
        "s_lshr_b32 s36, s48, 1\n\t"
        "s_cmp_gt_u32 s54, s36\n\t"
        "s_cbranch_scc1 LA_kv0ok%=\n\t"
        "s_mov_b32 s42, 0\n\t"
        "LA_kv0%=:\n\t"
        "s_waitcnt vmcnt(0)\n\t"
        "v_min_u32_dpp v111, v111, v111 quad_perm:[1,0,3,2] row_mask:0xf bank_mask:0xf\n\t"
        "s_nop 1\n\t"
        "v_min_u32_dpp v111, v111, v111 quad_perm:[2,3,0,1] row_mask:0xf bank_mask:0xf\n\t"
        "s_nop 1\n\t"
        "v_min_u32_dpp v111, v111, v111 row_ror:4 row_mask:0xf bank_mask:0xf\n\t"
        "s_nop 1\n\t"
        "v_min_u32_dpp v111, v111, v111 row_ror:8 row_mask:0xf bank_mask:0xf\n\t"
        "s_nop 0\n\t"
        "v_readfirstlane_b32 s54, v111\n\t"
        "s_nop 0\n\t"
        "s_cmp_gt_u32 s54, s36\n\t"
        "s_cbranch_scc1 LA_kv0ok%=\n\t"
        "s_sleep 4\n\t"
        "s_lshl_b32 s35, s44, 10\n\t"
        "v_lshlrev_b32_e32 v111, 2, v122\n\t"
        "v_and_b32_e32 v111, 0x3c0, v111\n\t"
        "buffer_load_dword v111, v111, s[12:15], s35 offen offset:0 sc1\n\t"
        "s_add_u32 s42, s42, 1\n\t"
        "s_cmp_lt_u32 s42, 0x100000\n\t"
        "s_cbranch_scc1 LA_kv0%=\n\t"
        "s_movk_i32 s34, 16\n\t"
        "s_branch LA_die%=\n\t"
        "LA_kv0ok%=:\n\t"
        "s_and_b32 s36, s48, 1\n\t"
        "s_mul_i32 s36, s36, 24576\n\t"
        "s_add_u32 s36, s36, s55\n\t"
        "s_lshl_b32 s37, s48, 18\n\t"
        "s_lshl_b32 s34, s43, 11\n\t"
        "s_add_u32 s37, s37, s34\n\t"
        "s_add_u32 m0, s36, 0\n\t"
        "s_add_u32 s34, s37, 29548544\n\t"
        "buffer_load_dwordx4 v122, s[12:15], s34 offen sc1 lds\n\t"
        "s_add_u32 m0, s36, 1024\n\t"
        "s_add_u32 s34, s37, 29549568\n\t"
        "buffer_load_dwordx4 v122, s[12:15], s34 offen sc1 lds\n\t"
        "s_add_u32 m0, s36, 8192\n\t"
        "s_add_u32 s34, s37, 12771328\n\t"
        "buffer_load_dwordx4 v122, s[12:15], s34 offen sc1 lds\n\t"
        "s_add_u32 m0, s36, 9216\n\t"
        "s_add_u32 s34, s37, 12772352\n\t"
        "buffer_load_dwordx4 v122, s[12:15], s34 offen sc1 lds\n\t"
        "s_add_u32 m0, s36, 16384\n\t"
        "s_add_u32 s34, s37, 46325760\n\t"
        "buffer_load_dwordx4 v122, s[12:15], s34 offen sc1 lds\n\t"
        "s_add_u32 m0, s36, 17408\n\t"
        "s_add_u32 s34, s37, 46326784\n\t"
        "buffer_load_dwordx4 v122, s[12:15], s34 offen sc1 lds\n\t"
        "s_mov_b32 s42, 0\n\t"
        "LA_cal%=:\n\t"
        "v_readfirstlane_b32 s34, v0\n\t"
        "s_nop 0\n\t"
        "s_cmp_lg_u32 s34, 0\n\t"
        "s_cbranch_scc1 LA_calok%=\n\t"
        "s_sleep 4\n\t"
        "s_mov_b32 s35, 33792\n\t"
        "buffer_load_dword v0, off, s[12:15], s35 sc1\n\t"
        "s_waitcnt vmcnt(0)\n\t"
        "s_add_u32 s42, s42, 1\n\t"
        "s_cmp_lt_u32 s42, 0x100000\n\t"
        "s_cbranch_scc1 LA_cal%=\n\t"
        "s_movk_i32 s34, 8\n\t"
        "s_branch LA_die%=\n\t"
        "LA_calok%=:\n\t"
        "s_and_b32 s34, s34, 8192\n\t"
        "s_add_u32 s8, s8, s34\n\t"
        "s_addc_u32 s9, s9, 0\n\t"
        "s_barrier\n\t"
        "s_cmp_lg_u32 s43, 0\n\t"
        "s_cbranch_scc1 LA_st%=\n\t"
        "v_mov_b32_e32 v2, 1\n\t"
        "s_mov_b32 s35, 34304\n\t"
        "s_mov_b64 exec, 1\n\t"
        "buffer_atomic_add v2, off, s[12:15], s35 sc0\n\t"
        "s_mov_b64 exec, -1\n\t"
        "s_waitcnt vmcnt(0)\n\t"
        "v_readfirstlane_b32 s34, v2\n\t"
        "s_nop 0\n\t"
        "s_cmp_lg_u32 s34, 95\n\t"
        "s_cbranch_scc1 LA_st%=\n\t"
        "s_memrealtime s[32:33]\n\t"
        "v_mbcnt_lo_u32_b32 v3, -1, 0\n\t"
        "v_lshlrev_b32_e32 v3, 7, v3\n\t"
        "s_waitcnt lgkmcnt(0)\n\t"
        "s_add_u32 s34, s32, 50\n\t"
        "s_or_b32 s34, s34, 1\n\t"
        "v_mov_b32_e32 v2, s34\n\t"
        "s_mov_b32 s35, 36864\n\t"
        "s_mov_b64 exec, 0xffff\n\t"
        "buffer_store_dword v2, v3, s[12:15], s35 offen sc1\n\t"
        "s_mov_b64 exec, -1\n\t"
        "LA_st%=:\n\t"
        "s_mov_b32 s24, 0\n\t"
        "s_mov_b32 s26, 0\n\t"
        "s_mov_b32 s27, 0x80000\n\t"
        "s_mov_b32 s28, 0x180000\n\t"
        "s_mov_b32 s31, 0\n\t"
        "s_mov_b32 s47, 0\n\t"
        "s_mov_b32 s38, 0xff00ff00\n\t"
        "s_mov_b32 s39, 0xff00ff00\n\t"
        "s_mov_b32 s40, 0\n\t"
        "s_mov_b32 s41, -1\n\t"
        "ds_read_b128 v[32:35], v117 offset:0\n\t"
        "ds_read_b128 v[36:39], v117 offset:1024\n\t"
        "ds_read_b128 v[40:43], v117 offset:2048\n\t"
        "ds_read_b128 v[44:47], v117 offset:3072\n\t"
        "ds_read_b128 v[48:51], v117 offset:4096\n\t"
        "ds_read_b128 v[52:55], v117 offset:5120\n\t"
        "ds_read_b128 v[56:59], v117 offset:6144\n\t"
        "ds_read_b128 v[60:63], v117 offset:7168\n\t"
        "v_mov_b64_e32 v[0:1], 0\n\t"
        "v_mov_b64_e32 v[2:3], 0\n\t"
        "v_mov_b64_e32 v[4:5], 0\n\t"
        "v_mov_b64_e32 v[6:7], 0\n\t"
        "v_mov_b64_e32 v[8:9], 0\n\t"
        "v_mov_b64_e32 v[10:11], 0\n\t"
        "v_mov_b64_e32 v[12:13], 0\n\t"
        "v_mov_b64_e32 v[14:15], 0\n\t"
        "s_mov_b32 s42, 0\n\t"
        "LA_gw%=:\n\t"
        "buffer_load_dword v16, off, s[12:15], s45 sc1\n\t"
        "s_waitcnt vmcnt(0)\n\t"
        "v_readfirstlane_b32 s25, v16\n\t"
        "s_nop 0\n\t"
        "s_cmp_lg_u32 s25, 0\n\t"
        "s_cbranch_scc1 LA_loop%=\n\t"
        "s_sleep 2\n\t"
        "s_add_u32 s42, s42, 1\n\t"
        "s_cmp_lt_u32 s42, 0x100000\n\t"
        "s_cbranch_scc1 LA_gw%=\n\t"
        "s_movk_i32 s34, 9\n\t"
        "s_branch LA_die%=\n\t"
        "LA_loop%=:\n\t"
        "s_mov_b32 s42, 0\n\t"
        "s_sub_u32 s35, s25, 8\n\t"
        "s_waitcnt lgkmcnt(0)\n\t"
        "s_memrealtime s[0:1]\n\t"
        "s_memrealtime s[2:3]\n\t"
        "s_memrealtime s[4:5]\n\t"
        "LA_wa%=:\n\t"
        "s_memrealtime s[6:7]\n\t"
        "s_waitcnt lgkmcnt(6)\n\t"
        "s_sub_u32 s34, s35, s0\n\t"
        "s_cmpk_lt_u32 s34, 0x2000\n\t"
        "s_cbranch_scc0 LA_wa%=x\n\t"
        "s_memrealtime s[0:1]\n\t"
        "s_waitcnt lgkmcnt(6)\n\t"
        "s_sub_u32 s34, s35, s2\n\t"
        "s_cmpk_lt_u32 s34, 0x2000\n\t"
        "s_cbranch_scc0 LA_wa%=x\n\t"
        "s_memrealtime s[2:3]\n\t"
        "s_waitcnt lgkmcnt(6)\n\t"
        "s_sub_u32 s34, s35, s4\n\t"
        "s_cmpk_lt_u32 s34, 0x2000\n\t"
        "s_cbranch_scc0 LA_wa%=x\n\t"
        "s_memrealtime s[4:5]\n\t"
        "s_waitcnt lgkmcnt(6)\n\t"
        "s_sub_u32 s34, s35, s6\n\t"
        "s_cmpk_lt_u32 s34, 0x2000\n\t"
        "s_cbranch_scc1 LA_wa%=\n\t"
        "s_nop 3\n\t"
        "LA_wa%=x:\n\t"
        "s_add_i32 s34, s34, 4\n\t"
        "s_max_i32 s34, s34, 0\n\t"
        "s_bitcmp1_b32 s34, 1\n\t"
        "s_cbranch_scc0 LA_wa%=y\n\t"
        "s_nop 15\n\t"
        "LA_wa%=y:\n\t"
        "s_bitcmp1_b32 s34, 0\n\t"
        "s_cbranch_scc0 LA_wa%=z\n\t"
        "s_nop 9\n\t"
        "LA_wa%=z:\n\t"
        "LA_rd%=:\n\t"
        "buffer_load_dwordx4 v[16:19], v112, s[8:11], s26 offen offset:0 sc1\n\t"
        "s_nop 12\n\t"
        "buffer_load_dwordx4 v[20:23], v112, s[8:11], s26 offen offset:1024 sc1\n\t"
        "s_nop 12\n\t"
        "buffer_load_dwordx4 v[24:27], v112, s[8:11], s26 offen offset:2048 sc1\n\t"
        "s_nop 12\n\t"
        "buffer_load_dwordx4 v[28:31], v112, s[8:11], s26 offen offset:3072 sc1\n\t"
        "LA_go%=:\n\t"
        "s_waitcnt vmcnt(3)\n\t"
        "v_smfmac_f32_16x16x64_bf16 v[0:3], v[16:19], a[0:7], v110\n\t"
        "ds_write_b128 v113, v[16:19] offset:0\n\t"
        "v_smfmac_f32_16x16x64_bf16 v[4:7], v[16:19], a[8:15], v110\n\t"
        "v_max3_u32 v111, v16, v17, v18\n\t"
        "v_max_u32_e32 v111, v19, v111\n\t"
        "v_smfmac_f32_16x16x64_bf16 v[8:11], v[16:19], a[16:23], v110\n\t"
        "v_smfmac_f32_16x16x64_bf16 v[12:15], v[16:19], v[32:39], v110\n\t"
        "s_waitcnt vmcnt(2)\n\t"
        "v_smfmac_f32_16x16x64_bf16 v[0:3], v[20:23], a[24:31], v110\n\t"
        "ds_write_b128 v113, v[20:23] offset:1024\n\t"
        "v_smfmac_f32_16x16x64_bf16 v[4:7], v[20:23], a[32:39], v110\n\t"
        "v_max3_u32 v111, v20, v21, v111\n\t"
        "v_max3_u32 v111, v22, v23, v111\n\t"
        "v_smfmac_f32_16x16x64_bf16 v[8:11], v[20:23], a[40:47], v110\n\t"
        "s_waitcnt vmcnt(1)\n\t"
        "ds_write_b128 v113, v[24:27] offset:2048\n\t"
        "v_smfmac_f32_16x16x64_bf16 v[12:15], v[20:23], v[40:47], v110\n\t"
        "v_max3_u32 v111, v24, v25, v111\n\t"
        "v_max3_u32 v111, v26, v27, v111\n\t"
        "v_smfmac_f32_16x16x64_bf16 v[0:3], v[24:27], a[48:55], v110\n\t"
        "v_smfmac_f32_16x16x64_bf16 v[4:7], v[24:27], a[56:63], v110\n\t"
        "s_waitcnt vmcnt(0)\n\t"
        "ds_write_b128 v113, v[28:31] offset:3072\n\t"
        "v_max3_u32 v111, v28, v29, v111\n\t"
        "v_max3_u32 v111, v30, v31, v111\n\t"
        "v_smfmac_f32_16x16x64_bf16 v[8:11], v[24:27], a[64:71], v110\n\t"
        "v_cmp_eq_u32_e32 vcc, -1, v111\n\t"
        "v_smfmac_f32_16x16x64_bf16 v[12:15], v[24:27], v[48:55], v110\n\t"
        "s_waitcnt lgkmcnt(0)\n\t"
        "s_cbranch_vccnz LA_rdfail%=\n\t"
        "s_barrier\n\t"
        "ds_read_b128 v[16:19], v114 offset:0\n\t"
        "ds_read_b128 v[20:23], v114 offset:1024\n\t"
        "ds_read_b128 v[24:27], v114 offset:2048\n\t"
        "v_smfmac_f32_16x16x64_bf16 v[0:3], v[28:31], a[72:79], v110\n\t"
        "v_smfmac_f32_16x16x64_bf16 v[4:7], v[28:31], a[80:87], v110\n\t"
        "v_smfmac_f32_16x16x64_bf16 v[8:11], v[28:31], a[88:95], v110\n\t"
        "v_smfmac_f32_16x16x64_bf16 v[12:15], v[28:31], v[56:63], v110\n\t"
        "ds_read_b128 v[28:31], v114 offset:3072\n\t"
        "s_waitcnt lgkmcnt(3)\n\t"
        "v_smfmac_f32_16x16x64_bf16 v[12:15], v[16:19], v[64:71], v110\n\t"
        "ds_read_b128 v[32:35], v117 offset:8192\n\t"
        "v_smfmac_f32_16x16x64_bf16 v[0:3], v[16:19], a[96:103], v110\n\t"
        "ds_read_b128 v[36:39], v117 offset:9216\n\t"
        "v_smfmac_f32_16x16x64_bf16 v[4:7], v[16:19], a[104:111], v110\n\t"
        "v_smfmac_f32_16x16x64_bf16 v[8:11], v[16:19], a[112:119], v110\n\t"
        "ds_read_b128 v[16:19], v115 offset:0\n\t"
        "s_waitcnt lgkmcnt(5)\n\t"
        "v_smfmac_f32_16x16x64_bf16 v[12:15], v[20:23], v[72:79], v110\n\t"
        "ds_read_b128 v[40:43], v117 offset:10240\n\t"
        "v_smfmac_f32_16x16x64_bf16 v[0:3], v[20:23], a[120:127], v110\n\t"
        "ds_read_b128 v[44:47], v117 offset:11264\n\t"
        "v_smfmac_f32_16x16x64_bf16 v[4:7], v[20:23], a[128:135], v110\n\t"
        "v_smfmac_f32_16x16x64_bf16 v[8:11], v[20:23], a[136:143], v110\n\t"
        "ds_read_b128 v[20:23], v115 offset:1024\n\t"
        "s_waitcnt lgkmcnt(7)\n\t"
        "v_smfmac_f32_16x16x64_bf16 v[12:15], v[24:27], v[80:87], v110\n\t"
        "ds_read_b128 v[48:51], v117 offset:12288\n\t"
        "v_smfmac_f32_16x16x64_bf16 v[0:3], v[24:27], a[144:151], v110\n\t"
        "ds_read_b128 v[52:55], v117 offset:13312\n\t"
        "v_smfmac_f32_16x16x64_bf16 v[4:7], v[24:27], a[152:159], v110\n\t"
        "v_smfmac_f32_16x16x64_bf16 v[8:11], v[24:27], a[160:167], v110\n\t"
        "ds_read_b128 v[24:27], v115 offset:2048\n\t"
        "s_waitcnt lgkmcnt(9)\n\t"
        "v_smfmac_f32_16x16x64_bf16 v[12:15], v[28:31], v[88:95], v110\n\t"
        "ds_read_b128 v[56:59], v117 offset:14336\n\t"
        "v_smfmac_f32_16x16x64_bf16 v[0:3], v[28:31], a[168:175], v110\n\t"
        "ds_read_b128 v[60:63], v117 offset:15360\n\t"
        "v_smfmac_f32_16x16x64_bf16 v[4:7], v[28:31], a[176:183], v110\n\t"
        "v_smfmac_f32_16x16x64_bf16 v[8:11], v[28:31], a[184:191], v110\n\t"
        "ds_read_b128 v[28:31], v115 offset:3072\n\t"
        "s_waitcnt lgkmcnt(9)\n\t"
        "v_smfmac_f32_16x16x64_bf16 v[12:15], v[16:19], v[32:39], v110\n\t"
        "ds_read_b128 v[32:35], v117 offset:16384\n\t"
        "v_smfmac_f32_16x16x64_bf16 v[0:3], v[16:19], a[192:199], v110\n\t"
        "ds_read_b128 v[36:39], v117 offset:17408\n\t"
        "v_smfmac_f32_16x16x64_bf16 v[4:7], v[16:19], a[200:207], v110\n\t"
        "v_smfmac_f32_16x16x64_bf16 v[8:11], v[16:19], a[208:215], v110\n\t"
        "ds_read_b128 v[16:19], v116 offset:0\n\t"
        "s_waitcnt lgkmcnt(9)\n\t"
        "v_smfmac_f32_16x16x64_bf16 v[12:15], v[20:23], v[40:47], v110\n\t"
        "ds_read_b128 v[40:43], v117 offset:18432\n\t"
        "v_smfmac_f32_16x16x64_bf16 v[0:3], v[20:23], a[216:223], v110\n\t"
        "ds_read_b128 v[44:47], v117 offset:19456\n\t"
        "v_smfmac_f32_16x16x64_bf16 v[4:7], v[20:23], a[224:231], v110\n\t"
        "v_smfmac_f32_16x16x64_bf16 v[8:11], v[20:23], a[232:239], v110\n\t"
        "ds_read_b128 v[20:23], v116 offset:1024\n\t"
        "s_waitcnt lgkmcnt(9)\n\t"
        "v_smfmac_f32_16x16x64_bf16 v[12:15], v[24:27], v[48:55], v110\n\t"
        "ds_read_b128 v[48:51], v117 offset:20480\n\t"
        "v_smfmac_f32_16x16x64_bf16 v[0:3], v[24:27], a[240:247], v110\n\t"
        "ds_read_b128 v[52:55], v117 offset:21504\n\t"
        "v_smfmac_f32_16x16x64_bf16 v[4:7], v[24:27], a[248:255], v110\n\t"
        "v_smfmac_f32_16x16x64_bf16 v[8:11], v[24:27], v[128:135], v110\n\t"
        "ds_read_b128 v[24:27], v116 offset:2048\n\t"
        "s_waitcnt lgkmcnt(9)\n\t"
        "v_smfmac_f32_16x16x64_bf16 v[12:15], v[28:31], v[56:63], v110\n\t"
        "ds_read_b128 v[56:59], v124 offset:3072\n\t"
        "v_smfmac_f32_16x16x64_bf16 v[0:3], v[28:31], v[136:143], v110\n\t"
        "ds_read_b128 v[60:63], v124 offset:7168\n\t"
        "v_smfmac_f32_16x16x64_bf16 v[4:7], v[28:31], v[144:151], v110\n\t"
        "v_smfmac_f32_16x16x64_bf16 v[8:11], v[28:31], v[152:159], v110\n\t"
        "ds_read_b128 v[28:31], v116 offset:3072\n\t"
        "s_waitcnt lgkmcnt(9)\n\t"
        "v_smfmac_f32_16x16x64_bf16 v[12:15], v[16:19], v[32:39], v110\n\t"
        "ds_read_b128 v[32:35], v124 offset:0\n\t"
        "v_smfmac_f32_16x16x64_bf16 v[0:3], v[16:19], v[160:167], v110\n\t"
        "ds_read_b128 v[36:39], v124 offset:4096\n\t"
        "v_smfmac_f32_16x16x64_bf16 v[4:7], v[16:19], v[168:175], v110\n\t"
        "v_smfmac_f32_16x16x64_bf16 v[8:11], v[16:19], v[176:183], v110\n\t"
        "s_waitcnt lgkmcnt(8)\n\t"
        "v_smfmac_f32_16x16x64_bf16 v[12:15], v[20:23], v[40:47], v110\n\t"
        "ds_read_b128 v[40:43], v124 offset:1024\n\t"
        "v_smfmac_f32_16x16x64_bf16 v[8:11], v[20:23], v[200:207], v110\n\t"
        "ds_read_b128 v[44:47], v124 offset:5120\n\t"
        "s_waitcnt lgkmcnt(7)\n\t"
        "v_smfmac_f32_16x16x64_bf16 v[12:15], v[24:27], v[48:55], v110\n\t"
        "ds_read_b128 v[48:51], v124 offset:2048\n\t"
        "v_smfmac_f32_16x16x64_bf16 v[8:11], v[24:27], v[224:231], v110\n\t"
        "ds_read_b128 v[52:55], v124 offset:6144\n\t"
        "s_waitcnt lgkmcnt(6)\n\t"
        "v_smfmac_f32_16x16x64_bf16 v[12:15], v[28:31], v[96:103], v110\n\t"
        "v_smfmac_f32_16x16x64_bf16 v[8:11], v[28:31], v[248:255], v110\n\t"
        "v_smfmac_f32_16x16x64_bf16 v[0:3], v[20:23], v[184:191], v110\n\t"
        "buffer_load_dwordx4 v[104:107], v120, s[8:11], s26 offen sc1\n\t"
        "s_lshl_b32 s34, s43, 11\n\t"
        "v_add_u32_e32 v108, s34, v124\n\t"
        "v_smfmac_f32_16x16x64_bf16 v[0:3], v[24:27], v[208:215], v110\n\t"
        "v_add_u32_e32 v109, s58, v124\n\t"
        "v_smfmac_f32_16x16x64_bf16 v[4:7], v[20:23], v[192:199], v110\n\t"
        "v_smfmac_f32_16x16x64_bf16 v[0:3], v[28:31], v[232:239], v110\n\t"
        "v_smfmac_f32_16x16x64_bf16 v[4:7], v[24:27], v[216:223], v110\n\t"
        "v_smfmac_f32_16x16x64_bf16 v[4:7], v[28:31], v[240:247], v110\n\t"
        "v_permlane32_swap_b32_e32 v8, v12\n\t"
        "v_permlane32_swap_b32_e32 v9, v13\n\t"
        "v_permlane32_swap_b32_e32 v10, v14\n\t"
        "v_permlane32_swap_b32_e32 v11, v15\n\t"
        "v_add_f32_e32 v8, v8, v12\n\t"
        "v_add_f32_e32 v9, v9, v13\n\t"
        "v_add_f32_e32 v10, v10, v14\n\t"
        "v_add_f32_e32 v11, v11, v15\n\t"
        "v_cvt_pk_bf16_f32 v12, v8, v9\n\t"
        "v_cvt_pk_bf16_f32 v13, v10, v11\n\t"
        "ds_write_b64 v118, v[12:13] offset:256\n\t"
        "v_permlane32_swap_b32_e32 v0, v4\n\t"
        "v_permlane32_swap_b32_e32 v1, v5\n\t"
        "v_permlane32_swap_b32_e32 v2, v6\n\t"
        "v_permlane32_swap_b32_e32 v3, v7\n\t"
        "v_pk_add_f32 v[0:1], v[0:1], v[4:5]\n\t"
        "v_pk_add_f32 v[2:3], v[2:3], v[6:7]\n\t"
        "v_cvt_pk_bf16_f32 v4, v0, v1\n\t"
        "v_cvt_pk_bf16_f32 v5, v2, v3\n\t"
        "ds_write_b64 v118, v[4:5]\n\t"
        "s_waitcnt lgkmcnt(0)\n\t"
        "s_barrier\n\t"
        "ds_read_b64_tr_b16 v[16:17], v119 offset:0\n\t"
        "ds_read_b64_tr_b16 v[18:19], v119 offset:32\n\t"
        "ds_read_b64_tr_b16 v[20:21], v119 offset:256\n\t"
        "ds_read_b64_tr_b16 v[22:23], v119 offset:288\n\t"
        "ds_read_b64_tr_b16 v[24:25], v119 offset:512\n\t"
        "ds_read_b64_tr_b16 v[26:27], v119 offset:544\n\t"
        "ds_read_b64_tr_b16 v[28:29], v119 offset:768\n\t"
        "ds_read_b64_tr_b16 v[30:31], v119 offset:800\n\t"
        "buffer_store_dword v126, v127, s[12:15], 0 offen sc1\n\t"
        "s_waitcnt lgkmcnt(6)\n\t"
        "v_mfma_f32_16x16x32_bf16 v[0:3], v[32:35], v[16:19], 0\n\t"
        "ds_read_b64_tr_b16 v[32:33], v119 offset:2112\n\t"
        "ds_read_b64_tr_b16 v[34:35], v119 offset:2144\n\t"
        "v_mfma_f32_16x16x32_bf16 v[4:7], v[36:39], v[16:19], 0\n\t"
        "ds_read_b64_tr_b16 v[36:37], v119 offset:2368\n\t"
        "ds_read_b64_tr_b16 v[38:39], v119 offset:2400\n\t"
        "s_waitcnt lgkmcnt(8)\n\t"
        "v_mfma_f32_16x16x32_bf16 v[0:3], v[40:43], v[20:23], v[0:3]\n\t"
        "ds_read_b64_tr_b16 v[40:41], v119 offset:2624\n\t"
        "ds_read_b64_tr_b16 v[42:43], v119 offset:2656\n\t"
        "v_mfma_f32_16x16x32_bf16 v[4:7], v[44:47], v[20:23], v[4:7]\n\t"
        "ds_read_b64_tr_b16 v[44:45], v119 offset:2880\n\t"
        "ds_read_b64_tr_b16 v[46:47], v119 offset:2912\n\t"
        "s_waitcnt lgkmcnt(10)\n\t"
        "v_mfma_f32_16x16x32_bf16 v[0:3], v[48:51], v[24:27], v[0:3]\n\t"
        "ds_read_b128 v[48:51], v108 offset:16384\n\t"
        "v_mfma_f32_16x16x32_bf16 v[4:7], v[52:55], v[24:27], v[4:7]\n\t"
        "ds_read_b128 v[52:55], v108 offset:17408\n\t"
        "s_waitcnt lgkmcnt(10)\n\t"
        "v_mfma_f32_16x16x32_bf16 v[0:3], v[56:59], v[28:31], v[0:3]\n\t"
        "v_mfma_f32_16x16x32_bf16 v[4:7], v[60:63], v[28:31], v[4:7]\n\t"
        "s_waitcnt lgkmcnt(2)\n\t"
        "v_mfma_f32_16x16x32_bf16 v[8:11], v[32:35], v[16:19], 0\n\t"
        "s_nop 1\n\t"
        "v_mfma_f32_16x16x32_bf16 v[8:11], v[36:39], v[20:23], v[8:11]\n\t"
        "s_nop 1\n\t"
        "v_mfma_f32_16x16x32_bf16 v[8:11], v[40:43], v[24:27], v[8:11]\n\t"
        "v_max3_f32 v59, v0, v1, v2\n\t"
        "v_max3_f32 v59, v59, v3, v4\n\t"
        "v_mfma_f32_16x16x32_bf16 v[8:11], v[44:47], v[28:31], v[8:11]\n\t"
        "v_max3_f32 v59, v59, v5, v6\n\t"
        "v_max_f32_e32 v60, v59, v7\n\t"
        "v_max_f32_e32 v59, v59, v7\n\t"
        "v_cndmask_b32_e64 v0, v0, v4, s[38:39]\n\t"
        "v_cndmask_b32_e64 v1, v1, v5, s[38:39]\n\t"
        "v_permlane32_swap_b32_e32 v59, v60\n\t"
        "v_cndmask_b32_e64 v2, v2, v6, s[38:39]\n\t"
        "v_cndmask_b32_e64 v3, v3, v7, s[38:39]\n\t"
        "v_max3_f32 v59, v59, v60, v8\n\t"
        "v_max3_f32 v59, v59, v9, v10\n\t"
        "v_max_f32_e32 v60, v59, v11\n\t"
        "v_max_f32_e32 v59, v59, v11\n\t"
        "v_cndmask_b32_e64 v8, v8, v9, s[38:39]\n\t"
        "v_cndmask_b32_e64 v10, v10, v11, s[38:39]\n\t"
        "v_permlane16_swap_b32_e32 v59, v60\n\t"
        "v_max_f32_e32 v59, v59, v60\n\t"
        "v_mul_f32_e32 v59, 0xbe02940c, v59\n\t"
        "v_cndmask_b32_e64 v58, v8, v10, s[40:41]\n\t"
        "v_fmamk_f32 v0, v0, 0x3e02940c, v59\n\t"
        "v_exp_f32_e32 v0, v0\n\t"
        "v_fmamk_f32 v1, v1, 0x3e02940c, v59\n\t"
        "v_exp_f32_e32 v1, v1\n\t"
        "v_fmamk_f32 v2, v2, 0x3e02940c, v59\n\t"
        "v_exp_f32_e32 v2, v2\n\t"
        "v_fmamk_f32 v3, v3, 0x3e02940c, v59\n\t"
        "v_exp_f32_e32 v3, v3\n\t"
        "v_fmamk_f32 v58, v58, 0x3e02940c, v59\n\t"
        "v_cvt_pk_bf16_f32 v12, v0, v1\n\t"
        "v_cvt_pk_bf16_f32 v13, v2, v3\n\t"
        "v_exp_f32_e32 v58, v58\n\t"
        "v_add_f32_e32 v60, v0, v1\n\t"
        "v_mov_b32_dpp v14, v12 row_ror:8 row_mask:0xf bank_mask:0xf\n\t"
        "v_mov_b32_dpp v15, v13 row_ror:8 row_mask:0xf bank_mask:0xf\n\t"
        "v_mov_b32_dpp v59, v58 row_ror:8 row_mask:0xf bank_mask:0xf\n\t"
        "v_add_f32_e32 v61, v2, v3\n\t"
        "s_waitcnt lgkmcnt(0)\n\t"
        "v_mfma_f32_16x16x32_bf16 v[4:7], v[48:51], v[12:15], 0\n\t"
        "v_cvt_pk_bf16_f32 v59, v58, v59\n\t"
        "v_add_f32_e32 v60, v60, v58\n\t"
        "v_mfma_f32_16x16x32_bf16 v[8:11], v[52:55], v[12:15], 0\n\t"
        "v_cndmask_b32_e64 v56, v59, 0, s[40:41]\n\t"
        "v_cndmask_b32_e64 v57, 0, v59, s[40:41]\n\t"
        "v_add_f32_e32 v59, v60, v61\n\t"
        "s_waitcnt vmcnt(1)\n\t"
        "v_max3_u32 v111, v104, v105, v106\n\t"
        "v_mfma_f32_16x16x16_bf16 v[4:7], v[104:105], v[56:57], v[4:7]\n\t"
        "v_add_f32_dpp v60, v59, v59 row_ror:8 row_mask:0xf bank_mask:0xf\n\t"
        "v_add_f32_dpp v61, v59, v59 row_ror:8 row_mask:0xf bank_mask:0xf\n\t"
        "v_mfma_f32_16x16x16_bf16 v[8:11], v[106:107], v[56:57], v[8:11]\n\t"
        "v_max_u32_e32 v111, v111, v107\n\t"
        "v_permlane16_swap_b32_e32 v60, v61\n\t"
        "v_add_f32_e32 v59, v60, v61\n\t"
        "v_add_f32_e32 v60, v60, v61\n\t"
        "v_cmp_eq_u32_e32 vcc, -1, v111\n\t"
        "s_nop 0\n\t"
        "v_permlane32_swap_b32_e32 v59, v60\n\t"
        "v_add_f32_e32 v59, v59, v60\n\t"
        "v_rcp_f32_e32 v59, v59\n\t"
        "s_cbranch_vccnz LA_vmfail%=\n\t"
        "LA_vmok%=:\n\t"
        "v_mul_f32_e32 v4, v4, v59\n\t"
        "v_mul_f32_e32 v5, v5, v59\n\t"
        "v_cvt_pk_bf16_f32 v0, v4, v5\n\t"
        "v_mul_f32_e32 v6, v6, v59\n\t"
        "v_mul_f32_e32 v7, v7, v59\n\t"
        "v_cvt_pk_bf16_f32 v1, v6, v7\n\t"
        "v_mul_f32_e32 v8, v8, v59\n\t"
        "v_mul_f32_e32 v9, v9, v59\n\t"
        "v_cvt_pk_bf16_f32 v2, v8, v9\n\t"
        "v_mul_f32_e32 v10, v10, v59\n\t"
        "v_mul_f32_e32 v11, v11, v59\n\t"
        "v_cvt_pk_bf16_f32 v3, v10, v11\n\t"
        "buffer_store_dwordx4 v[0:3], v121, s[8:11], s26 offen sc1\n\t"
        "s_memrealtime s[52:53]\n\t"
        "ds_read_b128 v[16:19], v109 offset:8192\n\t"
        "ds_read_b128 v[20:23], v109 offset:9216\n\t"
        "ds_read_b128 v[24:27], v109 offset:10240\n\t"
        "ds_read_b128 v[28:31], v109 offset:11264\n\t"
        "ds_read_b128 v[56:59], v124 offset:0\n\t"
        "ds_read_b128 v[60:63], v124 offset:4096\n\t"
        "ds_read_b128 v[12:15], v124 offset:1024\n\t"
        "v_xor_b32_e32 v126, 0x800, v108\n\t"
        "s_add_u32 s48, s24, 1\n\t"
        "s_min_u32 s48, s48, 63\n\t"
        "buffer_load_dwordx2 v[108:109], v125, s[8:11], s26 offen sc1\n\t"
        "s_lshl_b32 s35, s44, 10\n\t"
        "v_lshlrev_b32_e32 v111, 2, v122\n\t"
        "v_and_b32_e32 v111, 0x3c0, v111\n\t"
        "buffer_load_dword v111, v111, s[12:15], s35 offen offset:0 sc1\n\t"
        "s_lshr_b32 s36, s48, 1\n\t"
        "s_cmp_gt_u32 s54, s36\n\t"
        "s_cbranch_scc1 LA_kvok%=\n\t"
        "s_mov_b32 s42, 0\n\t"
        "LA_kv%=:\n\t"
        "s_waitcnt vmcnt(0)\n\t"
        "v_min_u32_dpp v111, v111, v111 quad_perm:[1,0,3,2] row_mask:0xf bank_mask:0xf\n\t"
        "s_nop 1\n\t"
        "v_min_u32_dpp v111, v111, v111 quad_perm:[2,3,0,1] row_mask:0xf bank_mask:0xf\n\t"
        "s_nop 1\n\t"
        "v_min_u32_dpp v111, v111, v111 row_ror:4 row_mask:0xf bank_mask:0xf\n\t"
        "s_nop 1\n\t"
        "v_min_u32_dpp v111, v111, v111 row_ror:8 row_mask:0xf bank_mask:0xf\n\t"
        "s_nop 0\n\t"
        "v_readfirstlane_b32 s54, v111\n\t"
        "s_nop 0\n\t"
        "s_cmp_gt_u32 s54, s36\n\t"
        "s_cbranch_scc1 LA_kvok%=\n\t"
        "s_sleep 4\n\t"
        "s_lshl_b32 s35, s44, 10\n\t"
        "v_lshlrev_b32_e32 v111, 2, v122\n\t"
        "v_and_b32_e32 v111, 0x3c0, v111\n\t"
        "buffer_load_dword v111, v111, s[12:15], s35 offen offset:0 sc1\n\t"
        "s_add_u32 s42, s42, 1\n\t"
        "s_cmp_lt_u32 s42, 0x100000\n\t"
        "s_cbranch_scc1 LA_kv%=\n\t"
        "s_movk_i32 s34, 16\n\t"
        "s_branch LA_die%=\n\t"
        "LA_kvok%=:\n\t"
        "s_waitcnt lgkmcnt(6)\n\t"
        "v_mfma_f32_16x16x32_bf16 v[8:11], v[32:35], v[16:19], 0\n\t"
        "ds_read_b128 v[32:35], v124 offset:5120\n\t"
        "s_waitcnt lgkmcnt(6)\n\t"
        "v_mfma_f32_16x16x32_bf16 v[8:11], v[36:39], v[20:23], v[8:11]\n\t"
        "ds_read_b128 v[36:39], v124 offset:2048\n\t"
        "s_waitcnt lgkmcnt(6)\n\t"
        "v_mfma_f32_16x16x32_bf16 v[8:11], v[40:43], v[24:27], v[8:11]\n\t"
        "ds_read_b128 v[40:43], v124 offset:6144\n\t"
        "s_waitcnt lgkmcnt(6)\n\t"
        "v_mfma_f32_16x16x32_bf16 v[8:11], v[44:47], v[28:31], v[8:11]\n\t"
        "ds_read_b128 v[44:47], v124 offset:3072\n\t"
        "s_add_u32 s35, s24, 1\n\t"
        "s_waitcnt lgkmcnt(6)\n\t"
        "v_mfma_f32_16x16x32_bf16 v[0:3], v[56:59], v[16:19], 0\n\t"
        "ds_read_b128 v[56:59], v124 offset:7168\n\t"
        "s_waitcnt lgkmcnt(6)\n\t"
        "v_mfma_f32_16x16x32_bf16 v[4:7], v[60:63], v[16:19], 0\n\t"
        "s_and_b32 s36, s35, 1\n\t"
        "s_mul_i32 s36, s36, 24576\n\t"
        "s_waitcnt lgkmcnt(5)\n\t"
        "v_mfma_f32_16x16x32_bf16 v[0:3], v[12:15], v[20:23], v[0:3]\n\t"
        "s_add_u32 s36, s36, s55\n\t"
        "s_lshl_b32 s37, s48, 18\n\t"
        "s_waitcnt lgkmcnt(4)\n\t"
        "v_mfma_f32_16x16x32_bf16 v[4:7], v[32:35], v[20:23], v[4:7]\n\t"
        "v_xor_b32_e32 v32, 0x200, v120\n\t"
        "buffer_load_dwordx4 v[12:15], v32, s[8:11], s26 offen sc1\n\t"
        "s_waitcnt lgkmcnt(3)\n\t"
        "v_mfma_f32_16x16x32_bf16 v[0:3], v[36:39], v[24:27], v[0:3]\n\t"
        "s_lshl_b32 s34, s43, 11\n\t"
        "s_add_u32 s37, s37, s34\n\t"
        "s_waitcnt lgkmcnt(2)\n\t"
        "v_mfma_f32_16x16x32_bf16 v[4:7], v[40:43], v[24:27], v[4:7]\n\t"
        "v_cvt_pk_bf16_f32 v8, 0, v8\n\t"
        "v_cvt_pk_bf16_f32 v9, 0, v9\n\t"
        "v_cvt_pk_bf16_f32 v10, 0, v10\n\t"
        "v_cvt_pk_bf16_f32 v11, 0, v11\n\t"
        "s_waitcnt lgkmcnt(1)\n\t"
        "v_mfma_f32_16x16x32_bf16 v[0:3], v[44:47], v[28:31], v[0:3]\n\t"
        "v_mul_f32_e32 v8, 0x3db504f3, v8\n\t"
        "v_mul_f32_e32 v9, 0x3db504f3, v9\n\t"
        "v_mul_f32_e32 v10, 0x3db504f3, v10\n\t"
        "v_mul_f32_e32 v11, 0x3db504f3, v11\n\t"
        "s_waitcnt lgkmcnt(0)\n\t"
        "v_mfma_f32_16x16x32_bf16 v[4:7], v[56:59], v[28:31], v[4:7]\n\t"
        "v_cvt_pk_bf16_f32 v8, 0, v8\n\t"
        "v_cvt_pk_bf16_f32 v9, 0, v9\n\t"
        "v_cvt_pk_bf16_f32 v10, 0, v10\n\t"
        "v_cvt_pk_bf16_f32 v11, 0, v11\n\t"
        "ds_read_b128 v[56:59], v126 offset:16384\n\t"
        "ds_read_b128 v[60:63], v126 offset:17408\n\t"
        "v_mov_b64_e32 v[36:37], -1\n\t"
        "v_mov_b64_e32 v[38:39], -1\n\t"
        "v_mov_b32_e32 v40, 0xce6e6b28\n\t"
        "v_mov_b32_e32 v46, 0x3db504f3\n\t"
        "v_mov_b32_e32 v47, 0x3fb8aa3b\n\t"
        "v_cvt_pk_bf16_f32 v0, 0, v0\n\t"
        "v_cvt_pk_bf16_f32 v1, 0, v1\n\t"
        "v_cvt_pk_bf16_f32 v2, 0, v2\n\t"
        "v_cvt_pk_bf16_f32 v3, 0, v3\n\t"
        "s_add_u32 m0, s36, 0\n\t"
        "s_add_u32 s34, s37, 29548544\n\t"
        "buffer_load_dwordx4 v122, s[12:15], s34 offen sc1 lds\n\t"
        "v_cvt_pk_bf16_f32 v4, 0, v4\n\t"
        "v_cvt_pk_bf16_f32 v5, 0, v5\n\t"
        "v_cvt_pk_bf16_f32 v6, 0, v6\n\t"
        "v_cvt_pk_bf16_f32 v7, 0, v7\n\t"
        "v_pk_mul_f32 v[0:1], v[0:1], v[46:47] op_sel_hi:[1,0]\n\t"
        "v_pk_mul_f32 v[2:3], v[2:3], v[46:47] op_sel_hi:[1,0]\n\t"
        "v_pk_mul_f32 v[4:5], v[4:5], v[46:47] op_sel_hi:[1,0]\n\t"
        "v_pk_mul_f32 v[6:7], v[6:7], v[46:47] op_sel_hi:[1,0]\n\t"
        "v_cvt_pk_bf16_f32 v0, 0, v0\n\t"
        "v_cvt_pk_bf16_f32 v1, 0, v1\n\t"
        "v_cvt_pk_bf16_f32 v2, 0, v2\n\t"
        "s_add_u32 m0, s36, 1024\n\t"
        "s_add_u32 s34, s37, 29549568\n\t"
        "buffer_load_dwordx4 v122, s[12:15], s34 offen sc1 lds\n\t"
        "v_cvt_pk_bf16_f32 v3, 0, v3\n\t"
        "v_cvt_pk_bf16_f32 v4, 0, v4\n\t"
        "v_cvt_pk_bf16_f32 v5, 0, v5\n\t"
        "v_cvt_pk_bf16_f32 v6, 0, v6\n\t"
        "v_cvt_pk_bf16_f32 v7, 0, v7\n\t"
        "v_cndmask_b32_e64 v0, v40, v0, s[60:61]\n\t"
        "v_cndmask_b32_e64 v1, v40, v1, s[62:63]\n\t"
        "v_cndmask_b32_e64 v2, v40, v2, s[64:65]\n\t"
        "v_cndmask_b32_e64 v3, v40, v3, s[66:67]\n\t"
        "v_cndmask_b32_e64 v4, v40, v4, s[68:69]\n\t"
        "v_cndmask_b32_e64 v5, v40, v5, s[70:71]\n\t"
        "s_add_u32 m0, s36, 8192\n\t"
        "s_add_u32 s34, s37, 12771328\n\t"
        "buffer_load_dwordx4 v122, s[12:15], s34 offen sc1 lds\n\t"
        "v_cndmask_b32_e64 v6, v40, v6, s[72:73]\n\t"
        "v_cndmask_b32_e64 v7, v40, v7, s[74:75]\n\t"
        "v_max3_f32 v42, v0, v1, v2\n\t"
        "v_max3_f32 v42, v42, v3, v4\n\t"
        "v_max3_f32 v42, v42, v5, v6\n\t"
        "v_max3_f32 v42, v42, v7, v8\n\t"
        "v_max3_f32 v42, v42, v9, v10\n\t"
        "v_mov_b32_e32 v43, v11\n\t"
        "v_max_f32_e32 v44, v42, v43\n\t"
        "v_max_f32_e32 v42, v42, v43\n\t"
        "s_pack_ll_b32_b16 s34, s52, s24\n\t"
        "v_mov_b32_e32 v40, s34\n\t"
        "s_add_u32 s35, s26, s57\n\t"
        "v_permlane16_swap_b32_e32 v42, v44\n\t"
        "v_max_f32_e32 v45, v42, v44\n\t"
        "v_max_f32_e32 v42, v42, v44\n\t"
        "s_mov_b64 exec, 1\n\t"
        "buffer_store_dword v40, off, s[8:11], s35 sc1\n\t"
        "s_mov_b64 exec, -1\n\t"
        "v_permlane32_swap_b32_e32 v42, v45\n\t"
        "v_max_f32_e32 v42, v42, v45\n\t"
        "v_mul_f32_e32 v42, 0xbfb8aa3b, v42\n\t"
        "v_pk_fma_f32 v[0:1], v[0:1], v[46:47], v[42:43] op_sel:[0,1,0] op_sel_hi:[1,1,0]\n\t"
        "s_add_u32 m0, s36, 9216\n\t"
        "s_add_u32 s34, s37, 12772352\n\t"
        "buffer_load_dwordx4 v122, s[12:15], s34 offen sc1 lds\n\t"
        "v_exp_f32_e32 v0, v0\n\t"
        "v_exp_f32_e32 v1, v1\n\t"
        "v_pk_fma_f32 v[2:3], v[2:3], v[46:47], v[42:43] op_sel:[0,1,0] op_sel_hi:[1,1,0]\n\t"
        "v_exp_f32_e32 v2, v2\n\t"
        "v_exp_f32_e32 v3, v3\n\t"
        "v_pk_fma_f32 v[4:5], v[4:5], v[46:47], v[42:43] op_sel:[0,1,0] op_sel_hi:[1,1,0]\n\t"
        "v_exp_f32_e32 v4, v4\n\t"
        "s_add_u32 m0, s36, 16384\n\t"
        "s_add_u32 s34, s37, 46325760\n\t"
        "buffer_load_dwordx4 v122, s[12:15], s34 offen sc1 lds\n\t"
        "v_exp_f32_e32 v5, v5\n\t"
        "v_pk_fma_f32 v[6:7], v[6:7], v[46:47], v[42:43] op_sel:[0,1,0] op_sel_hi:[1,1,0]\n\t"
        "v_exp_f32_e32 v6, v6\n\t"
        "v_exp_f32_e32 v7, v7\n\t"
        "v_pk_fma_f32 v[8:9], v[8:9], v[46:47], v[42:43] op_sel:[0,1,0] op_sel_hi:[1,1,0]\n\t"
        "v_exp_f32_e32 v8, v8\n\t"
        "v_exp_f32_e32 v9, v9\n\t"
        "s_add_u32 m0, s36, 17408\n\t"
        "s_add_u32 s34, s37, 46326784\n\t"
        "buffer_load_dwordx4 v122, s[12:15], s34 offen sc1 lds\n\t"
        "v_pk_fma_f32 v[10:11], v[10:11], v[46:47], v[42:43] op_sel:[0,1,0] op_sel_hi:[1,1,0]\n\t"
        "v_exp_f32_e32 v10, v10\n\t"
        "v_exp_f32_e32 v11, v11\n\t"
        "v_pk_add_f32 v[42:43], v[0:1], v[2:3]\n\t"
        "v_cndmask_b32_e64 v8, v8, 0, s[40:41]\n\t"
        "v_cndmask_b32_e64 v9, v9, 0, s[40:41]\n\t"
        "v_cndmask_b32_e64 v10, v10, 0, s[40:41]\n\t"
        "buffer_store_dwordx4 v[36:39], v121, s[8:11], s28 offen sc1\n\t"
        "v_cndmask_b32_e64 v11, v11, 0, s[40:41]\n\t"
        "v_pk_add_f32 v[42:43], v[42:43], v[4:5]\n\t"
        "v_pk_add_f32 v[42:43], v[42:43], v[6:7]\n\t"
        "v_pk_add_f32 v[42:43], v[42:43], v[8:9]\n\t"
        "v_pk_add_f32 v[42:43], v[42:43], v[10:11]\n\t"
        "v_add_f32_e32 v44, v42, v43\n\t"
        "v_add_f32_e32 v42, v42, v43\n\t"
        "s_lshl_b32 s34, s24, 18\n\t"
        "s_add_u32 s34, s34, 63102976\n\t"
        "v_permlane16_swap_b32_e32 v42, v44\n\t"
        "v_add_f32_e32 v45, v42, v44\n\t"
        "v_add_f32_e32 v42, v42, v44\n\t"
        "s_add_u32 s35, s24, 1\n\t"
        "v_mov_b32_e32 v126, s35\n\t"
        "v_permlane32_swap_b32_e32 v42, v45\n\t"
        "v_add_f32_e32 v42, v42, v45\n\t"
        "v_rcp_f32_e32 v42, v42\n\t"
        "s_waitcnt vmcnt(8) lgkmcnt(0)\n\t"
        "v_max3_u32 v45, v12, v13, v14\n\t"
        "v_max_u32_e32 v45, v45, v15\n\t"
        "v_cmp_eq_u32_e32 vcc, -1, v45\n\t"
        "s_nop 0\n\t"
        "s_cbranch_vccnz LA_v2fail%=\n\t"
        "LA_v2ok%=:\n\t"
        "v_and_b32_e32 v45, 0xff, v109\n\t"
        "v_cmp_ne_u32_e32 vcc, s24, v45\n\t"
        "s_nop 0\n\t"
        "s_cbranch_vccnz LA_stre%=\n\t"
        "LA_stok%=:\n\t"
        "v_pk_mul_f32 v[0:1], v[0:1], v[42:43] op_sel_hi:[1,0]\n\t"
        "v_subrev_u32_e32 v108, s25, v108\n\t"
        "v_min_u32_dpp v111, v111, v111 quad_perm:[1,0,3,2] row_mask:0xf bank_mask:0xf\n\t"
        "v_cvt_pk_bf16_f32 v32, v0, v1\n\t"
        "v_pk_mul_f32 v[2:3], v[2:3], v[42:43] op_sel_hi:[1,0]\n\t"
        "v_cvt_pk_bf16_f32 v33, v2, v3\n\t"
        "v_max_i32_dpp v108, v108, v108 quad_perm:[1,0,3,2] row_mask:0xf bank_mask:0xf\n\t"
        "v_min_u32_dpp v111, v111, v111 quad_perm:[2,3,0,1] row_mask:0xf bank_mask:0xf\n\t"
        "v_pk_mul_f32 v[4:5], v[4:5], v[42:43] op_sel_hi:[1,0]\n\t"
        "v_cvt_pk_bf16_f32 v34, v4, v5\n\t"
        "v_pk_mul_f32 v[6:7], v[6:7], v[42:43] op_sel_hi:[1,0]\n\t"
        "v_max_i32_dpp v108, v108, v108 quad_perm:[2,3,0,1] row_mask:0xf bank_mask:0xf\n\t"
        "v_min_u32_dpp v111, v111, v111 row_ror:4 row_mask:0xf bank_mask:0xf\n\t"
        "v_cvt_pk_bf16_f32 v35, v6, v7\n\t"
        "v_pk_mul_f32 v[8:9], v[8:9], v[42:43] op_sel_hi:[1,0]\n\t"
        "v_cvt_pk_bf16_f32 v36, v8, v9\n\t"
        "v_max_i32_dpp v108, v108, v108 row_ror:4 row_mask:0xf bank_mask:0xf\n\t"
        "v_min_u32_dpp v111, v111, v111 row_ror:8 row_mask:0xf bank_mask:0xf\n\t"
        "v_pk_mul_f32 v[10:11], v[10:11], v[42:43] op_sel_hi:[1,0]\n\t"
        "v_cvt_pk_bf16_f32 v37, v10, v11\n\t"
        "v_mfma_f32_16x16x32_bf16 v[16:19], v[48:51], v[32:35], 0\n\t"
        "v_max_i32_dpp v108, v108, v108 row_ror:8 row_mask:0xf bank_mask:0xf\n\t"
        "v_mfma_f32_16x16x32_bf16 v[20:23], v[52:55], v[32:35], 0\n\t"
        "v_mfma_f32_16x16x32_bf16 v[24:27], v[56:59], v[32:35], 0\n\t"
        "v_mfma_f32_16x16x32_bf16 v[28:31], v[60:63], v[32:35], 0\n\t"
        "v_max_i32_dpp v108, v108, v108 row_bcast:15 row_mask:0xa bank_mask:0xf\n\t"
        "v_xor_b32_e32 v45, 0x400, v123\n\t"
        "v_add_u32_e32 v124, s56, v124\n\t"
        "v_mfma_f32_16x16x16_bf16 v[16:19], v[104:105], v[36:37], v[16:19]\n\t"
        "v_max_i32_dpp v108, v108, v108 row_bcast:31 row_mask:0xc bank_mask:0xf\n\t"
        "v_mfma_f32_16x16x16_bf16 v[20:23], v[106:107], v[36:37], v[20:23]\n\t"
        "v_mfma_f32_16x16x16_bf16 v[24:27], v[12:13], v[36:37], v[24:27]\n\t"
        "v_mfma_f32_16x16x16_bf16 v[28:31], v[14:15], v[36:37], v[28:31]\n\t"
        "ds_read_b128 v[48:51], v117 offset:4096\n\t"
        "ds_read_b128 v[52:55], v117 offset:5120\n\t"
        "ds_read_b128 v[56:59], v117 offset:6144\n\t"
        "ds_read_b128 v[60:63], v117 offset:7168\n\t"
        "s_sub_u32 s56, 0, s56\n\t"
        "v_cvt_pk_bf16_f32 v0, v16, v17\n\t"
        "v_cvt_pk_bf16_f32 v1, v18, v19\n\t"
        "v_cvt_pk_bf16_f32 v2, v20, v21\n\t"
        "v_cvt_pk_bf16_f32 v3, v22, v23\n\t"
        "buffer_store_dwordx4 v[0:3], v123, s[12:15], s34 offen sc1\n\t"
        "v_cvt_pk_bf16_f32 v4, v24, v25\n\t"
        "v_cvt_pk_bf16_f32 v5, v26, v27\n\t"
        "v_cvt_pk_bf16_f32 v6, v28, v29\n\t"
        "v_cvt_pk_bf16_f32 v7, v30, v31\n\t"
        "buffer_store_dwordx4 v[4:7], v45, s[12:15], s34 offen sc1\n\t"
        "s_cmp_eq_u32 s24, 63\n\t"
        "s_cbranch_scc1 LA_last%=\n\t"
        "ds_read_b128 v[32:35], v117 offset:0\n\t"
        "ds_read_b128 v[36:39], v117 offset:1024\n\t"
        "ds_read_b128 v[40:43], v117 offset:2048\n\t"
        "ds_read_b128 v[44:47], v117 offset:3072\n\t"
        "v_mov_b64_e32 v[14:15], 0\n\t"
        "v_mov_b64_e32 v[12:13], 0\n\t"
        "v_mov_b64_e32 v[10:11], 0\n\t"
        "v_mov_b64_e32 v[8:9], 0\n\t"
        "v_mov_b64_e32 v[6:7], 0\n\t"
        "v_mov_b64_e32 v[4:5], 0\n\t"
        "v_mov_b64_e32 v[2:3], 0\n\t"
        "v_mov_b64_e32 v[0:1], 0\n\t"
        "v_readfirstlane_b32 s54, v111\n\t"
        "v_readfirstlane_b32 s35, v109\n\t"
        "v_readlane_b32 s36, v108, 63\n\t"
        "s_bfe_u32 s29, s35, 0xc0008\n\t"
        "s_lshr_b32 s30, s35, 20\n\t"
        "s_add_u32 s35, s31, s50\n\t"
        "s_mul_i32 s37, s50, 3\n\t"
        "s_min_u32 s35, s35, s37\n\t"
        "s_add_i32 s34, s36, s51\n\t"
        "s_cmp_gt_i32 s34, 0\n\t"
        "s_cselect_b32 s31, s35, 0\n\t"
        "s_cselect_b32 s34, s34, 0\n\t"
        "s_add_u32 s34, s34, s31\n\t"
        "s_add_u32 s37, s25, s34\n\t"
        "s_add_u32 s45, s37, s29\n\t"
        "s_add_u32 s25, s45, s30\n\t"
        "s_add_u32 s24, s24, 1\n\t"
        "s_mov_b32 s28, s26\n\t"
        "s_mov_b32 s26, s27\n\t"
        "s_add_u32 s27, s27, 0x80000\n\t"
        "s_and_b32 s27, s27, 0x1fffff\n\t"
        "s_branch LA_loop%=\n\t"
        "LA_last%=:\n\t"
        "s_waitcnt vmcnt(0)\n\t"
        "s_barrier\n\t"
        "buffer_store_dword v126, v127, s[12:15], 0 offen sc1\n\t"
        "s_endpgm\n\t"
        "LA_stre%=:\n\t"
        "s_mov_b32 s42, 0\n\t"
        "LA_str2%=:\n\t"
        "buffer_load_dwordx2 v[108:109], v125, s[8:11], s26 offen sc1\n\t"
        "s_waitcnt vmcnt(0)\n\t"
        "v_and_b32_e32 v45, 0xff, v109\n\t"
        "v_cmp_ne_u32_e32 vcc, s24, v45\n\t"
        "s_nop 0\n\t"
        "s_cbranch_vccz LA_stok%=\n\t"
        "s_add_u32 s42, s42, 1\n\t"
        "s_cmp_lt_u32 s42, 0x100000\n\t"
        "s_cbranch_scc1 LA_str2%=\n\t"
        "s_movk_i32 s34, 5\n\t"
        "s_branch LA_die%=\n\t"
        "LA_v2fail%=:\n\t"
        "s_mov_b32 s42, 0\n\t"
        "LA_v2re%=:\n\t"
        "v_xor_b32_e32 v45, 0x200, v120\n\t"
        "buffer_load_dwordx4 v[12:15], v45, s[8:11], s26 offen sc1\n\t"
        "s_waitcnt vmcnt(0)\n\t"
        "v_max3_u32 v45, v12, v13, v14\n\t"
        "v_max_u32_e32 v45, v45, v15\n\t"
        "v_cmp_eq_u32_e32 vcc, -1, v45\n\t"
        "s_nop 0\n\t"
        "s_cbranch_vccz LA_v2ok%=\n\t"
        "s_add_u32 s42, s42, 1\n\t"
        "s_cmp_lt_u32 s42, 0x100000\n\t"
        "s_cbranch_scc1 LA_v2re%=\n\t"
        "s_movk_i32 s34, 6\n\t"
        "s_branch LA_die%=\n\t"
        "LA_rdfail%=:\n\t"
        "s_or_b32 s47, s47, 1\n\t"
        "buffer_load_dwordx4 v[16:19], v112, s[8:11], s26 offen offset:0 sc1\n\t"
        "buffer_load_dwordx4 v[20:23], v112, s[8:11], s26 offen offset:1024 sc1\n\t"
        "buffer_load_dwordx4 v[24:27], v112, s[8:11], s26 offen offset:2048 sc1\n\t"
        "buffer_load_dwordx4 v[28:31], v112, s[8:11], s26 offen offset:3072 sc1\n\t"
        "s_nop 7\n\t"
        "v_mov_b64_e32 v[0:1], 0\n\t"
        "v_mov_b64_e32 v[2:3], 0\n\t"
        "v_mov_b64_e32 v[4:5], 0\n\t"
        "v_mov_b64_e32 v[6:7], 0\n\t"
        "v_mov_b64_e32 v[8:9], 0\n\t"
        "v_mov_b64_e32 v[10:11], 0\n\t"
        "v_mov_b64_e32 v[12:13], 0\n\t"
        "v_mov_b64_e32 v[14:15], 0\n\t"
        "s_branch LA_rdre%=w\n\t"
        "LA_rdre%=:\n\t"
        "buffer_load_dwordx4 v[16:19], v112, s[8:11], s26 offen offset:0 sc1\n\t"
        "buffer_load_dwordx4 v[20:23], v112, s[8:11], s26 offen offset:1024 sc1\n\t"
        "buffer_load_dwordx4 v[24:27], v112, s[8:11], s26 offen offset:2048 sc1\n\t"
        "buffer_load_dwordx4 v[28:31], v112, s[8:11], s26 offen offset:3072 sc1\n\t"
        "LA_rdre%=w:\n\t"
        "s_waitcnt vmcnt(0) lgkmcnt(0)\n\t"
        "v_max3_u32 v111, v16, v17, v18\n\t"
        "v_max3_u32 v111, v111, v19, v20\n\t"
        "v_max3_u32 v111, v111, v21, v22\n\t"
        "v_max3_u32 v111, v111, v23, v24\n\t"
        "v_max3_u32 v111, v111, v25, v26\n\t"
        "v_max3_u32 v111, v111, v27, v28\n\t"
        "v_max3_u32 v111, v111, v29, v30\n\t"
        "v_max_u32_e32 v111, v111, v31\n\t"
        "v_cmp_eq_u32_e32 vcc, -1, v111\n\t"
        "s_nop 0\n\t"
        "s_cbranch_vccz LA_go%=\n\t"
        "s_add_u32 s42, s42, 1\n\t"
        "s_cmp_lt_u32 s42, 0x100000\n\t"
        "s_cbranch_scc1 LA_rdre%=\n\t"
        "s_movk_i32 s34, 1\n\t"
        "s_branch LA_die%=\n\t"
        "LA_f2%=:\n\t"
        "s_or_b32 s47, s47, 1\n\t"
        "buffer_load_dwordx4 v[24:27], v112, s[8:11], s26 offen offset:2048 sc1\n\t"
        "s_nop 7\n\t"
        "s_branch LA_r2%=w\n\t"
        "LA_r2%=:\n\t"
        "buffer_load_dwordx4 v[24:27], v112, s[8:11], s26 offen offset:2048 sc1\n\t"
        "LA_r2%=w:\n\t"
        "s_waitcnt vmcnt(0) lgkmcnt(0)\n\t"
        "v_max3_u32 v111, v24, v25, v26\n\t"
        "v_max_u32_e32 v111, v111, v27\n\t"
        "v_cmp_eq_u32_e32 vcc, -1, v111\n\t"
        "s_nop 0\n\t"
        "s_cbranch_vccz LA_go%=\n\t"
        "s_add_u32 s42, s42, 1\n\t"
        "s_cmp_lt_u32 s42, 0x100000\n\t"
        "s_cbranch_scc1 LA_r2%=\n\t"
        "s_movk_i32 s34, 1\n\t"
        "s_branch LA_die%=\n\t"
        "LA_f3%=:\n\t"
        "s_or_b32 s47, s47, 1\n\t"
        "buffer_load_dwordx4 v[28:31], v112, s[8:11], s26 offen offset:3072 sc1\n\t"
        "s_nop 7\n\t"
        "s_branch LA_r3%=w\n\t"
        "LA_r3%=:\n\t"
        "buffer_load_dwordx4 v[28:31], v112, s[8:11], s26 offen offset:3072 sc1\n\t"
        "LA_r3%=w:\n\t"
        "s_waitcnt vmcnt(0) lgkmcnt(0)\n\t"
        "v_max3_u32 v111, v28, v29, v30\n\t"
        "v_max_u32_e32 v111, v111, v31\n\t"
        "v_cmp_eq_u32_e32 vcc, -1, v111\n\t"
        "s_nop 0\n\t"
        "s_cbranch_vccz LA_go%=\n\t"
        "s_add_u32 s42, s42, 1\n\t"
        "s_cmp_lt_u32 s42, 0x100000\n\t"
        "s_cbranch_scc1 LA_r3%=\n\t"
        "s_movk_i32 s34, 1\n\t"
        "s_branch LA_die%=\n\t"
        "LA_vmfail%=:\n\t"
        "s_or_b32 s47, s47, 2\n\t"
        "s_mov_b32 s42, 0\n\t"
        "LA_vmre%=:\n\t"
        "buffer_load_dwordx4 v[104:107], v120, s[8:11], s26 offen sc1\n\t"
        "v_mfma_f32_16x16x32_bf16 v[4:7], v[48:51], v[12:15], 0\n\t"
        "v_mfma_f32_16x16x32_bf16 v[8:11], v[52:55], v[12:15], 0\n\t"
        "s_nop 7\n\t"
        "s_waitcnt vmcnt(0)\n\t"
        "v_mfma_f32_16x16x16_bf16 v[4:7], v[104:105], v[56:57], v[4:7]\n\t"
        "v_mfma_f32_16x16x16_bf16 v[8:11], v[106:107], v[56:57], v[8:11]\n\t"
        "v_max3_u32 v111, v104, v105, v106\n\t"
        "v_max_u32_e32 v111, v111, v107\n\t"
        "v_cmp_eq_u32_e32 vcc, -1, v111\n\t"
        "s_nop 7\n\t"
        "s_cbranch_vccz LA_vmok%=\n\t"
        "s_add_u32 s42, s42, 1\n\t"
        "s_cmp_lt_u32 s42, 0x100000\n\t"
        "s_cbranch_scc1 LA_vmre%=\n\t"
        "s_movk_i32 s34, 2\n\t"
        "s_branch LA_die%=\n\t"
        "LA_die%=:\n\t"
        "v_mov_b32_e32 v30, s34\n\t"
        "s_mov_b32 s35, 33024\n\t"
        "s_mov_b64 exec, 1\n\t"
        "buffer_store_dword v30, off, s[12:15], s35 sc1\n\t"
        "s_endpgm\n\t"
        ".Lk_end_A:\n\t"
        :: REC_S_IN, "{v96}"(g_w), "{v97}"(wb0), "{v98}"(wb1), "{v99}"(rb0), "{v100}"(rb0 ^ 32u), "{v101}"(rb0 ^ 64u), "{v102}"(rb0 ^ 96u),
           "{v112}"(a_mem), "{v113}"(l_ms + w * 4096), "{v114}"(l_ms + ((w + 1) & 3) * 4096), "{v115}"(l_ms + ((w + 2) & 3) * 4096), "{v116}"(l_ms + ((w + 3) & 3) * 4096),
           "{v117}"(lb + w * 22528 + l * 16), "{v118}"(l_iw), "{v119}"(l_iq), "{v120}"(a_vm), "{v121}"(a_o), "{v122}"(h * 8192 + l * 16), "{v123}"(a_ox), "{v124}"(lb + L_KV + l * 16), "{s55}"(lb + L_KV + w * 2048),
           "{v125}"((24 + cp) * 16384 + l * ST_P), "{v126}"(16 * (w & 1) + n - 4 * g), "{v103}"(pp ? 0xEEEEEEEEu : 0x44444444u),
           "{v127}"((l == 0 && w == 0) ? F_PROG + h * 128 : OOB)
        : "memory", "vcc", "scc", "v0", "v255", "a0", "a255", "s101");
    __builtin_unreachable();
}
__device__ __forceinline__ void rec_b(const Params& p, unsigned lb, const int wv, const int id, const int rg, const int cp, const int rot, const int bq) {
    const int w = wv, l = otid() & 63, n = l & 15, g = l >> 4, cs = id, rb = 32 + id * 2 + rg;
    const v4u sh = mk_srd(p.ws + hx_base(p.ws), HX_BYTES - 8192), sw = mk_srd(p.ws, W_TOTAL), s1 = mk_srd(p.Wv, 1024 * 4096 * 2), s2 = mk_srd(p.Wo, 1024 * 4096 * 2);
    const int r8 = l >> 3, pc = l & 7, pp = n >> 3, bb = (n >> 2) & 1, f = (g & 1) | (pp << 1);
    const unsigned stg = lb + LB_STG + w * LB_STGP;
    const unsigned g_v = r8 * 8192 + cs * 256 + rg * 128 + pc * 16, g_o = (l >> 2) * 2048 + cs * 64 + (l & 3) * 16;
    const unsigned wb0 = stg + r8 * 128 + (((pc >> 1) ^ (r8 >> 2)) << 5) + (pc & 1) * 16, wb1 = wb0 ^ 64u;
    const unsigned rb0 = stg + (16 * (g >> 1) + 8 * pp + 4 * (g & 1) + bb) * 128 + (f << 5) + (n & 3) * 8;
    const unsigned o_r = stg + pp * (2048 + 128) + (g & 1) * (1024 + 32) + (bb + 4 * (g >> 1)) * 64 + (n & 3) * 8;
    const unsigned a_mem = cp * 32768 + (w >> 1) * 16384 + (w & 1) * 4096 + (g >> 1) * 512 + ((n & 7) >> 2) * 256 + ((g & 1) * 2 + pp) * 64 + (n & 3) * 16;
    const unsigned a_vmw = w < 2 ? (16 + (cs >> 2)) * 16384 + (cs & 3) * 2048 + (2 * rg + w) * 512 + n * 32 + (g & 1) * 16 + (g >> 1) * 8 : OOB;
    const unsigned a_or = (8 + rg * 4 + w) * 16384 + (n >> 2) * 256 + g * 64 + (n & 3) * 16;
    const unsigned a_mw = (RPL != 0 && RPL != 4 && w == 3) ? OOB : w * 32768 + (cs >> 4) * 16384 + ((cs >> 1) & 7) * 1024 + (cs & 1) * 512 + rg * 256 + l * 4;
    const unsigned a_sw = (w == 0 && l < 4) ? (24 + l) * 16384 + (id * 2 + rg) * ST_P : OOB;
    const bf16_t* m0p = p.mem0 + (4 * rg + ((l >> 2) & 3)) * 1024 + cs * 32 + g * 8 + (l & 3) * 2;
    asm volatile(
        "global_load_dword v109, v[108:109], off\n\t"
        "s_add_u32 s35, s68, 0\n\t"
        "s_and_b32 s35, s35, 7\n\t"
        "s_lshl_b32 s69, s35, 10\n\t"
        "s_add_u32 s35, s68, 2\n\t"
        "s_and_b32 s35, s35, 7\n\t"
        "s_lshl_b32 s70, s35, 10\n\t"
        "s_add_u32 s35, s68, 4\n\t"
        "s_and_b32 s35, s35, 7\n\t"
        "s_lshl_b32 s71, s35, 10\n\t"
        "s_add_u32 s35, s68, 6\n\t"
        "s_and_b32 s35, s35, 7\n\t"
        "s_lshl_b32 s72, s35, 10\n\t"
        "s_lshl_b32 s34, s43, 21\n\t"
        "s_add_u32 s34, s34, 0\n\t"
        "buffer_load_dwordx4 v[128:131], v96, s[16:19], s34 offen nt\n\t"
        "s_add_u32 s34, s34, 0x10000\n\t"
        "buffer_load_dwordx4 v[132:135], v96, s[16:19], s34 offen nt\n\t"
        "s_add_u32 s34, s34, 0x10000\n\t"
        "buffer_load_dwordx4 v[136:139], v96, s[16:19], s34 offen nt\n\t"
        "s_add_u32 s34, s34, 0x10000\n\t"
        "buffer_load_dwordx4 v[140:143], v96, s[16:19], s34 offen nt\n\t"
        "s_add_u32 s34, s34, 0x10000\n\t"
        "buffer_load_dwordx4 v[144:147], v96, s[16:19], s34 offen nt\n\t"
        "s_add_u32 s34, s34, 0x10000\n\t"
        "buffer_load_dwordx4 v[148:151], v96, s[16:19], s34 offen nt\n\t"
        "s_add_u32 s34, s34, 0x10000\n\t"
        "buffer_load_dwordx4 v[152:155], v96, s[16:19], s34 offen nt\n\t"
        "s_add_u32 s34, s34, 0x10000\n\t"
        "buffer_load_dwordx4 v[156:159], v96, s[16:19], s34 offen nt\n\t"
        "s_lshl_b32 s34, s43, 21\n\t"
        "s_add_u32 s34, s34, 524288\n\t"
        "buffer_load_dwordx4 v[160:163], v96, s[16:19], s34 offen nt\n\t"
        "s_add_u32 s34, s34, 0x10000\n\t"
        "buffer_load_dwordx4 v[164:167], v96, s[16:19], s34 offen nt\n\t"
        "s_add_u32 s34, s34, 0x10000\n\t"
        "buffer_load_dwordx4 v[168:171], v96, s[16:19], s34 offen nt\n\t"
        "s_add_u32 s34, s34, 0x10000\n\t"
        "buffer_load_dwordx4 v[172:175], v96, s[16:19], s34 offen nt\n\t"
        "s_add_u32 s34, s34, 0x10000\n\t"
        "buffer_load_dwordx4 v[176:179], v96, s[16:19], s34 offen nt\n\t"
        "s_add_u32 s34, s34, 0x10000\n\t"
        "buffer_load_dwordx4 v[180:183], v96, s[16:19], s34 offen nt\n\t"
        "s_add_u32 s34, s34, 0x10000\n\t"
        "buffer_load_dwordx4 v[184:187], v96, s[16:19], s34 offen nt\n\t"
        "s_add_u32 s34, s34, 0x10000\n\t"
        "buffer_load_dwordx4 v[188:191], v96, s[16:19], s34 offen nt\n\t"
        "s_lshl_b32 s34, s43, 21\n\t"
        "s_add_u32 s34, s34, 1048576\n\t"
        "buffer_load_dwordx4 v[192:195], v96, s[16:19], s34 offen nt\n\t"
        "s_add_u32 s34, s34, 0x10000\n\t"
        "buffer_load_dwordx4 v[196:199], v96, s[16:19], s34 offen nt\n\t"
        "s_add_u32 s34, s34, 0x10000\n\t"
        "buffer_load_dwordx4 v[200:203], v96, s[16:19], s34 offen nt\n\t"
        "s_add_u32 s34, s34, 0x10000\n\t"
        "buffer_load_dwordx4 v[204:207], v96, s[16:19], s34 offen nt\n\t"
        "s_add_u32 s34, s34, 0x10000\n\t"
        "buffer_load_dwordx4 v[208:211], v96, s[16:19], s34 offen nt\n\t"
        "s_add_u32 s34, s34, 0x10000\n\t"
        "buffer_load_dwordx4 v[212:215], v96, s[16:19], s34 offen nt\n\t"
        "s_add_u32 s34, s34, 0x10000\n\t"
        "buffer_load_dwordx4 v[216:219], v96, s[16:19], s34 offen nt\n\t"
        "s_add_u32 s34, s34, 0x10000\n\t"
        "buffer_load_dwordx4 v[220:223], v96, s[16:19], s34 offen nt\n\t"
        "s_waitcnt vmcnt(16)\n\t"
        "ds_write_b128 v98, v[128:131] offset:0\n\t"
        "ds_write_b128 v99, v[132:135] offset:1024\n\t"
        "ds_write_b128 v98, v[136:139] offset:2048\n\t"
        "ds_write_b128 v99, v[140:143] offset:3072\n\t"
        "ds_write_b128 v98, v[144:147] offset:4096\n\t"
        "ds_write_b128 v99, v[148:151] offset:5120\n\t"
        "ds_write_b128 v98, v[152:155] offset:6144\n\t"
        "ds_write_b128 v99, v[156:159] offset:7168\n\t"
        "ds_read_b64_tr_b16 v[128:129], v100 offset:0\n\t"
        "ds_read_b64_tr_b16 v[130:131], v100 offset:256\n\t"
        "ds_read_b64_tr_b16 v[132:133], v100 offset:4096\n\t"
        "ds_read_b64_tr_b16 v[134:135], v100 offset:4352\n\t"
        "s_waitcnt lgkmcnt(8)\n\t"
        "ds_read_b64_tr_b16 v[136:137], v101 offset:0\n\t"
        "ds_read_b64_tr_b16 v[138:139], v101 offset:256\n\t"
        "ds_read_b64_tr_b16 v[140:141], v101 offset:4096\n\t"
        "ds_read_b64_tr_b16 v[142:143], v101 offset:4352\n\t"
        "s_waitcnt lgkmcnt(8)\n\t"
        "ds_read_b64_tr_b16 v[144:145], v102 offset:0\n\t"
        "ds_read_b64_tr_b16 v[146:147], v102 offset:256\n\t"
        "ds_read_b64_tr_b16 v[148:149], v102 offset:4096\n\t"
        "ds_read_b64_tr_b16 v[150:151], v102 offset:4352\n\t"
        "s_waitcnt lgkmcnt(8)\n\t"
        "ds_read_b64_tr_b16 v[152:153], v103 offset:0\n\t"
        "ds_read_b64_tr_b16 v[154:155], v103 offset:256\n\t"
        "ds_read_b64_tr_b16 v[156:157], v103 offset:4096\n\t"
        "ds_read_b64_tr_b16 v[158:159], v103 offset:4352\n\t"
        "s_lshl_b32 s34, s43, 21\n\t"
        "s_add_u32 s34, s34, 1572864\n\t"
        "buffer_load_dwordx4 v[224:227], v96, s[16:19], s34 offen nt\n\t"
        "s_add_u32 s34, s34, 0x10000\n\t"
        "buffer_load_dwordx4 v[228:231], v96, s[16:19], s34 offen nt\n\t"
        "s_add_u32 s34, s34, 0x10000\n\t"
        "buffer_load_dwordx4 v[232:235], v96, s[16:19], s34 offen nt\n\t"
        "s_add_u32 s34, s34, 0x10000\n\t"
        "buffer_load_dwordx4 v[236:239], v96, s[16:19], s34 offen nt\n\t"
        "s_add_u32 s34, s34, 0x10000\n\t"
        "buffer_load_dwordx4 v[240:243], v96, s[16:19], s34 offen nt\n\t"
        "s_add_u32 s34, s34, 0x10000\n\t"
        "buffer_load_dwordx4 v[244:247], v96, s[16:19], s34 offen nt\n\t"
        "s_add_u32 s34, s34, 0x10000\n\t"
        "buffer_load_dwordx4 v[248:251], v96, s[16:19], s34 offen nt\n\t"
        "s_add_u32 s34, s34, 0x10000\n\t"
        "buffer_load_dwordx4 v[252:255], v96, s[16:19], s34 offen nt\n\t"
        "s_waitcnt lgkmcnt(0)\n\t"
        "s_waitcnt vmcnt(16)\n\t"
        "ds_write_b128 v98, v[160:163] offset:0\n\t"
        "ds_write_b128 v99, v[164:167] offset:1024\n\t"
        "ds_write_b128 v98, v[168:171] offset:2048\n\t"
        "ds_write_b128 v99, v[172:175] offset:3072\n\t"
        "ds_write_b128 v98, v[176:179] offset:4096\n\t"
        "ds_write_b128 v99, v[180:183] offset:5120\n\t"
        "ds_write_b128 v98, v[184:187] offset:6144\n\t"
        "ds_write_b128 v99, v[188:191] offset:7168\n\t"
        "ds_read_b64_tr_b16 v[160:161], v100 offset:0\n\t"
        "ds_read_b64_tr_b16 v[162:163], v100 offset:256\n\t"
        "ds_read_b64_tr_b16 v[164:165], v100 offset:4096\n\t"
        "ds_read_b64_tr_b16 v[166:167], v100 offset:4352\n\t"
        "s_waitcnt lgkmcnt(8)\n\t"
        "ds_read_b64_tr_b16 v[168:169], v101 offset:0\n\t"
        "ds_read_b64_tr_b16 v[170:171], v101 offset:256\n\t"
        "ds_read_b64_tr_b16 v[172:173], v101 offset:4096\n\t"
        "ds_read_b64_tr_b16 v[174:175], v101 offset:4352\n\t"
        "s_waitcnt lgkmcnt(8)\n\t"
        "ds_read_b64_tr_b16 v[176:177], v102 offset:0\n\t"
        "ds_read_b64_tr_b16 v[178:179], v102 offset:256\n\t"
        "ds_read_b64_tr_b16 v[180:181], v102 offset:4096\n\t"
        "ds_read_b64_tr_b16 v[182:183], v102 offset:4352\n\t"
        "s_waitcnt lgkmcnt(8)\n\t"
        "ds_read_b64_tr_b16 v[184:185], v103 offset:0\n\t"
        "ds_read_b64_tr_b16 v[186:187], v103 offset:256\n\t"
        "ds_read_b64_tr_b16 v[188:189], v103 offset:4096\n\t"
        "ds_read_b64_tr_b16 v[190:191], v103 offset:4352\n\t"
        "s_add_u32 s35, s68, 0\n\t"
        "s_and_b32 s35, s35, 7\n\t"
        "s_lshl_b32 s35, s35, 18\n\t"
        "s_lshl_b32 s34, s43, 21\n\t"
        "s_add_u32 s34, s34, s35\n\t"
        "buffer_load_dwordx4 a[0:3], v97, s[20:23], s34 offen\n\t"
        "s_add_u32 s34, s34, 0x8000\n\t"
        "buffer_load_dwordx4 a[4:7], v97, s[20:23], s34 offen\n\t"
        "s_add_u32 s34, s34, 0x8000\n\t"
        "buffer_load_dwordx4 a[8:11], v97, s[20:23], s34 offen\n\t"
        "s_add_u32 s34, s34, 0x8000\n\t"
        "buffer_load_dwordx4 a[12:15], v97, s[20:23], s34 offen\n\t"
        "s_add_u32 s34, s34, 0x8000\n\t"
        "buffer_load_dwordx4 a[16:19], v97, s[20:23], s34 offen\n\t"
        "s_add_u32 s34, s34, 0x8000\n\t"
        "buffer_load_dwordx4 a[20:23], v97, s[20:23], s34 offen\n\t"
        "s_add_u32 s34, s34, 0x8000\n\t"
        "buffer_load_dwordx4 a[24:27], v97, s[20:23], s34 offen\n\t"
        "s_add_u32 s34, s34, 0x8000\n\t"
        "buffer_load_dwordx4 a[28:31], v97, s[20:23], s34 offen\n\t"
        "s_waitcnt lgkmcnt(0)\n\t"
        "s_waitcnt vmcnt(16)\n\t"
        "ds_write_b128 v98, v[192:195] offset:0\n\t"
        "ds_write_b128 v99, v[196:199] offset:1024\n\t"
        "ds_write_b128 v98, v[200:203] offset:2048\n\t"
        "ds_write_b128 v99, v[204:207] offset:3072\n\t"
        "ds_write_b128 v98, v[208:211] offset:4096\n\t"
        "ds_write_b128 v99, v[212:215] offset:5120\n\t"
        "ds_write_b128 v98, v[216:219] offset:6144\n\t"
        "ds_write_b128 v99, v[220:223] offset:7168\n\t"
        "ds_read_b64_tr_b16 v[192:193], v100 offset:0\n\t"
        "ds_read_b64_tr_b16 v[194:195], v100 offset:256\n\t"
        "ds_read_b64_tr_b16 v[196:197], v100 offset:4096\n\t"
        "ds_read_b64_tr_b16 v[198:199], v100 offset:4352\n\t"
        "s_waitcnt lgkmcnt(8)\n\t"
        "ds_read_b64_tr_b16 v[200:201], v101 offset:0\n\t"
        "ds_read_b64_tr_b16 v[202:203], v101 offset:256\n\t"
        "ds_read_b64_tr_b16 v[204:205], v101 offset:4096\n\t"
        "ds_read_b64_tr_b16 v[206:207], v101 offset:4352\n\t"
        "s_waitcnt lgkmcnt(8)\n\t"
        "ds_read_b64_tr_b16 v[208:209], v102 offset:0\n\t"
        "ds_read_b64_tr_b16 v[210:211], v102 offset:256\n\t"
        "ds_read_b64_tr_b16 v[212:213], v102 offset:4096\n\t"
        "ds_read_b64_tr_b16 v[214:215], v102 offset:4352\n\t"
        "s_waitcnt lgkmcnt(8)\n\t"
        "ds_read_b64_tr_b16 v[216:217], v103 offset:0\n\t"
        "ds_read_b64_tr_b16 v[218:219], v103 offset:256\n\t"
        "ds_read_b64_tr_b16 v[220:221], v103 offset:4096\n\t"
        "ds_read_b64_tr_b16 v[222:223], v103 offset:4352\n\t"
        "s_add_u32 s35, s68, 1\n\t"
        "s_and_b32 s35, s35, 7\n\t"
        "s_lshl_b32 s35, s35, 18\n\t"
        "s_lshl_b32 s34, s43, 21\n\t"
        "s_add_u32 s34, s34, s35\n\t"
        "buffer_load_dwordx4 a[32:35], v97, s[20:23], s34 offen\n\t"
        "s_add_u32 s34, s34, 0x8000\n\t"
        "buffer_load_dwordx4 a[36:39], v97, s[20:23], s34 offen\n\t"
        "s_add_u32 s34, s34, 0x8000\n\t"
        "buffer_load_dwordx4 a[40:43], v97, s[20:23], s34 offen\n\t"
        "s_add_u32 s34, s34, 0x8000\n\t"
        "buffer_load_dwordx4 a[44:47], v97, s[20:23], s34 offen\n\t"
        "s_add_u32 s34, s34, 0x8000\n\t"
        "buffer_load_dwordx4 a[48:51], v97, s[20:23], s34 offen\n\t"
        "s_add_u32 s34, s34, 0x8000\n\t"
        "buffer_load_dwordx4 a[52:55], v97, s[20:23], s34 offen\n\t"
        "s_add_u32 s34, s34, 0x8000\n\t"
        "buffer_load_dwordx4 a[56:59], v97, s[20:23], s34 offen\n\t"
        "s_add_u32 s34, s34, 0x8000\n\t"
        "buffer_load_dwordx4 a[60:63], v97, s[20:23], s34 offen\n\t"
        "s_waitcnt lgkmcnt(0)\n\t"
        "s_waitcnt vmcnt(16)\n\t"
        "ds_write_b128 v98, v[224:227] offset:0\n\t"
        "ds_write_b128 v99, v[228:231] offset:1024\n\t"
        "ds_write_b128 v98, v[232:235] offset:2048\n\t"
        "ds_write_b128 v99, v[236:239] offset:3072\n\t"
        "ds_write_b128 v98, v[240:243] offset:4096\n\t"
        "ds_write_b128 v99, v[244:247] offset:5120\n\t"
        "ds_write_b128 v98, v[248:251] offset:6144\n\t"
        "ds_write_b128 v99, v[252:255] offset:7168\n\t"
        "ds_read_b64_tr_b16 v[224:225], v100 offset:0\n\t"
        "ds_read_b64_tr_b16 v[226:227], v100 offset:256\n\t"
        "ds_read_b64_tr_b16 v[228:229], v100 offset:4096\n\t"
        "ds_read_b64_tr_b16 v[230:231], v100 offset:4352\n\t"
        "s_waitcnt lgkmcnt(8)\n\t"
        "ds_read_b64_tr_b16 v[232:233], v101 offset:0\n\t"
        "ds_read_b64_tr_b16 v[234:235], v101 offset:256\n\t"
        "ds_read_b64_tr_b16 v[236:237], v101 offset:4096\n\t"
        "ds_read_b64_tr_b16 v[238:239], v101 offset:4352\n\t"
        "s_waitcnt lgkmcnt(8)\n\t"
        "ds_read_b64_tr_b16 v[240:241], v102 offset:0\n\t"
        "ds_read_b64_tr_b16 v[242:243], v102 offset:256\n\t"
        "ds_read_b64_tr_b16 v[244:245], v102 offset:4096\n\t"
        "ds_read_b64_tr_b16 v[246:247], v102 offset:4352\n\t"
        "s_waitcnt lgkmcnt(8)\n\t"
        "ds_read_b64_tr_b16 v[248:249], v103 offset:0\n\t"
        "ds_read_b64_tr_b16 v[250:251], v103 offset:256\n\t"
        "ds_read_b64_tr_b16 v[252:253], v103 offset:4096\n\t"
        "ds_read_b64_tr_b16 v[254:255], v103 offset:4352\n\t"
        "s_add_u32 s35, s68, 2\n\t"
        "s_and_b32 s35, s35, 7\n\t"
        "s_lshl_b32 s35, s35, 18\n\t"
        "s_lshl_b32 s34, s43, 21\n\t"
        "s_add_u32 s34, s34, s35\n\t"
        "buffer_load_dwordx4 a[64:67], v97, s[20:23], s34 offen\n\t"
        "s_add_u32 s34, s34, 0x8000\n\t"
        "buffer_load_dwordx4 a[68:71], v97, s[20:23], s34 offen\n\t"
        "s_add_u32 s34, s34, 0x8000\n\t"
        "buffer_load_dwordx4 a[72:75], v97, s[20:23], s34 offen\n\t"
        "s_add_u32 s34, s34, 0x8000\n\t"
        "buffer_load_dwordx4 a[76:79], v97, s[20:23], s34 offen\n\t"
        "s_add_u32 s34, s34, 0x8000\n\t"
        "buffer_load_dwordx4 a[80:83], v97, s[20:23], s34 offen\n\t"
        "s_add_u32 s34, s34, 0x8000\n\t"
        "buffer_load_dwordx4 a[84:87], v97, s[20:23], s34 offen\n\t"
        "s_add_u32 s34, s34, 0x8000\n\t"
        "buffer_load_dwordx4 a[88:91], v97, s[20:23], s34 offen\n\t"
        "s_add_u32 s34, s34, 0x8000\n\t"
        "buffer_load_dwordx4 a[92:95], v97, s[20:23], s34 offen\n\t"
        "s_waitcnt lgkmcnt(0)\n\t"
        "s_waitcnt vmcnt(16)\n\t"
        "ds_write_b128 v122, a[0:3] offset:0\n\t"
        "ds_write_b128 v122, a[4:7] offset:1056\n\t"
        "ds_write_b128 v122, a[8:11] offset:2176\n\t"
        "ds_write_b128 v122, a[12:15] offset:3232\n\t"
        "ds_write_b128 v122, a[16:19] offset:4352\n\t"
        "ds_write_b128 v122, a[20:23] offset:5408\n\t"
        "ds_write_b128 v122, a[24:27] offset:6528\n\t"
        "ds_write_b128 v122, a[28:31] offset:7584\n\t"
        "ds_read_b64_tr_b16 a[0:1], v123 offset:0\n\t"
        "ds_read_b64_tr_b16 a[2:3], v123 offset:128\n\t"
        "ds_read_b64_tr_b16 a[4:5], v123 offset:512\n\t"
        "ds_read_b64_tr_b16 a[6:7], v123 offset:640\n\t"
        "s_waitcnt lgkmcnt(8)\n\t"
        "ds_read_b64_tr_b16 a[8:9], v123 offset:32\n\t"
        "ds_read_b64_tr_b16 a[10:11], v123 offset:160\n\t"
        "ds_read_b64_tr_b16 a[12:13], v123 offset:544\n\t"
        "ds_read_b64_tr_b16 a[14:15], v123 offset:672\n\t"
        "s_waitcnt lgkmcnt(8)\n\t"
        "ds_read_b64_tr_b16 a[16:17], v123 offset:4352\n\t"
        "ds_read_b64_tr_b16 a[18:19], v123 offset:4480\n\t"
        "ds_read_b64_tr_b16 a[20:21], v123 offset:4864\n\t"
        "ds_read_b64_tr_b16 a[22:23], v123 offset:4992\n\t"
        "s_waitcnt lgkmcnt(8)\n\t"
        "ds_read_b64_tr_b16 a[24:25], v123 offset:4384\n\t"
        "ds_read_b64_tr_b16 a[26:27], v123 offset:4512\n\t"
        "ds_read_b64_tr_b16 a[28:29], v123 offset:4896\n\t"
        "ds_read_b64_tr_b16 a[30:31], v123 offset:5024\n\t"
        "s_waitcnt lgkmcnt(8)\n\t"
        "s_add_u32 s35, s68, 3\n\t"
        "s_and_b32 s35, s35, 7\n\t"
        "s_lshl_b32 s35, s35, 18\n\t"
        "s_lshl_b32 s34, s43, 21\n\t"
        "s_add_u32 s34, s34, s35\n\t"
        "buffer_load_dwordx4 a[96:99], v97, s[20:23], s34 offen\n\t"
        "s_add_u32 s34, s34, 0x8000\n\t"
        "buffer_load_dwordx4 a[100:103], v97, s[20:23], s34 offen\n\t"
        "s_add_u32 s34, s34, 0x8000\n\t"
        "buffer_load_dwordx4 a[104:107], v97, s[20:23], s34 offen\n\t"
        "s_add_u32 s34, s34, 0x8000\n\t"
        "buffer_load_dwordx4 a[108:111], v97, s[20:23], s34 offen\n\t"
        "s_add_u32 s34, s34, 0x8000\n\t"
        "buffer_load_dwordx4 a[112:115], v97, s[20:23], s34 offen\n\t"
        "s_add_u32 s34, s34, 0x8000\n\t"
        "buffer_load_dwordx4 a[116:119], v97, s[20:23], s34 offen\n\t"
        "s_add_u32 s34, s34, 0x8000\n\t"
        "buffer_load_dwordx4 a[120:123], v97, s[20:23], s34 offen\n\t"
        "s_add_u32 s34, s34, 0x8000\n\t"
        "buffer_load_dwordx4 a[124:127], v97, s[20:23], s34 offen\n\t"
        "s_waitcnt lgkmcnt(0)\n\t"
        "s_waitcnt vmcnt(16)\n\t"
        "ds_write_b128 v122, a[32:35] offset:0\n\t"
        "ds_write_b128 v122, a[36:39] offset:1056\n\t"
        "ds_write_b128 v122, a[40:43] offset:2176\n\t"
        "ds_write_b128 v122, a[44:47] offset:3232\n\t"
        "ds_write_b128 v122, a[48:51] offset:4352\n\t"
        "ds_write_b128 v122, a[52:55] offset:5408\n\t"
        "ds_write_b128 v122, a[56:59] offset:6528\n\t"
        "ds_write_b128 v122, a[60:63] offset:7584\n\t"
        "ds_read_b64_tr_b16 a[32:33], v123 offset:0\n\t"
        "ds_read_b64_tr_b16 a[34:35], v123 offset:128\n\t"
        "ds_read_b64_tr_b16 a[36:37], v123 offset:512\n\t"
        "ds_read_b64_tr_b16 a[38:39], v123 offset:640\n\t"
        "s_waitcnt lgkmcnt(8)\n\t"
        "ds_read_b64_tr_b16 a[40:41], v123 offset:32\n\t"
        "ds_read_b64_tr_b16 a[42:43], v123 offset:160\n\t"
        "ds_read_b64_tr_b16 a[44:45], v123 offset:544\n\t"
        "ds_read_b64_tr_b16 a[46:47], v123 offset:672\n\t"
        "s_waitcnt lgkmcnt(8)\n\t"
        "ds_read_b64_tr_b16 a[48:49], v123 offset:4352\n\t"
        "ds_read_b64_tr_b16 a[50:51], v123 offset:4480\n\t"
        "ds_read_b64_tr_b16 a[52:53], v123 offset:4864\n\t"
        "ds_read_b64_tr_b16 a[54:55], v123 offset:4992\n\t"
        "s_waitcnt lgkmcnt(8)\n\t"
        "ds_read_b64_tr_b16 a[56:57], v123 offset:4384\n\t"
        "ds_read_b64_tr_b16 a[58:59], v123 offset:4512\n\t"
        "ds_read_b64_tr_b16 a[60:61], v123 offset:4896\n\t"
        "ds_read_b64_tr_b16 a[62:63], v123 offset:5024\n\t"
        "s_waitcnt lgkmcnt(8)\n\t"
        "s_add_u32 s35, s68, 4\n\t"
        "s_and_b32 s35, s35, 7\n\t"
        "s_lshl_b32 s35, s35, 18\n\t"
        "s_lshl_b32 s34, s43, 21\n\t"
        "s_add_u32 s34, s34, s35\n\t"
        "buffer_load_dwordx4 a[128:131], v97, s[20:23], s34 offen\n\t"
        "s_add_u32 s34, s34, 0x8000\n\t"
        "buffer_load_dwordx4 a[132:135], v97, s[20:23], s34 offen\n\t"
        "s_add_u32 s34, s34, 0x8000\n\t"
        "buffer_load_dwordx4 a[136:139], v97, s[20:23], s34 offen\n\t"
        "s_add_u32 s34, s34, 0x8000\n\t"
        "buffer_load_dwordx4 a[140:143], v97, s[20:23], s34 offen\n\t"
        "s_add_u32 s34, s34, 0x8000\n\t"
        "buffer_load_dwordx4 a[144:147], v97, s[20:23], s34 offen\n\t"
        "s_add_u32 s34, s34, 0x8000\n\t"
        "buffer_load_dwordx4 a[148:151], v97, s[20:23], s34 offen\n\t"
        "s_add_u32 s34, s34, 0x8000\n\t"
        "buffer_load_dwordx4 a[152:155], v97, s[20:23], s34 offen\n\t"
        "s_add_u32 s34, s34, 0x8000\n\t"
        "buffer_load_dwordx4 a[156:159], v97, s[20:23], s34 offen\n\t"
        "s_waitcnt lgkmcnt(0)\n\t"
        "s_waitcnt vmcnt(16)\n\t"
        "ds_write_b128 v122, a[64:67] offset:0\n\t"
        "ds_write_b128 v122, a[68:71] offset:1056\n\t"
        "ds_write_b128 v122, a[72:75] offset:2176\n\t"
        "ds_write_b128 v122, a[76:79] offset:3232\n\t"
        "ds_write_b128 v122, a[80:83] offset:4352\n\t"
        "ds_write_b128 v122, a[84:87] offset:5408\n\t"
        "ds_write_b128 v122, a[88:91] offset:6528\n\t"
        "ds_write_b128 v122, a[92:95] offset:7584\n\t"
        "ds_read_b64_tr_b16 a[64:65], v123 offset:0\n\t"
        "ds_read_b64_tr_b16 a[66:67], v123 offset:128\n\t"
        "ds_read_b64_tr_b16 a[68:69], v123 offset:512\n\t"
        "ds_read_b64_tr_b16 a[70:71], v123 offset:640\n\t"
        "s_waitcnt lgkmcnt(8)\n\t"
        "ds_read_b64_tr_b16 a[72:73], v123 offset:32\n\t"
        "ds_read_b64_tr_b16 a[74:75], v123 offset:160\n\t"
        "ds_read_b64_tr_b16 a[76:77], v123 offset:544\n\t"
        "ds_read_b64_tr_b16 a[78:79], v123 offset:672\n\t"
        "s_waitcnt lgkmcnt(8)\n\t"
        "ds_read_b64_tr_b16 a[80:81], v123 offset:4352\n\t"
        "ds_read_b64_tr_b16 a[82:83], v123 offset:4480\n\t"
        "ds_read_b64_tr_b16 a[84:85], v123 offset:4864\n\t"
        "ds_read_b64_tr_b16 a[86:87], v123 offset:4992\n\t"
        "s_waitcnt lgkmcnt(8)\n\t"
        "ds_read_b64_tr_b16 a[88:89], v123 offset:4384\n\t"
        "ds_read_b64_tr_b16 a[90:91], v123 offset:4512\n\t"
        "ds_read_b64_tr_b16 a[92:93], v123 offset:4896\n\t"
        "ds_read_b64_tr_b16 a[94:95], v123 offset:5024\n\t"
        "s_waitcnt lgkmcnt(8)\n\t"
        "s_add_u32 s35, s68, 5\n\t"
        "s_and_b32 s35, s35, 7\n\t"
        "s_lshl_b32 s35, s35, 18\n\t"
        "s_lshl_b32 s34, s43, 21\n\t"
        "s_add_u32 s34, s34, s35\n\t"
        "buffer_load_dwordx4 a[160:163], v97, s[20:23], s34 offen\n\t"
        "s_add_u32 s34, s34, 0x8000\n\t"
        "buffer_load_dwordx4 a[164:167], v97, s[20:23], s34 offen\n\t"
        "s_add_u32 s34, s34, 0x8000\n\t"
        "buffer_load_dwordx4 a[168:171], v97, s[20:23], s34 offen\n\t"
        "s_add_u32 s34, s34, 0x8000\n\t"
        "buffer_load_dwordx4 a[172:175], v97, s[20:23], s34 offen\n\t"
        "s_add_u32 s34, s34, 0x8000\n\t"
        "buffer_load_dwordx4 a[176:179], v97, s[20:23], s34 offen\n\t"
        "s_add_u32 s34, s34, 0x8000\n\t"
        "buffer_load_dwordx4 a[180:183], v97, s[20:23], s34 offen\n\t"
        "s_add_u32 s34, s34, 0x8000\n\t"
        "buffer_load_dwordx4 a[184:187], v97, s[20:23], s34 offen\n\t"
        "s_add_u32 s34, s34, 0x8000\n\t"
        "buffer_load_dwordx4 a[188:191], v97, s[20:23], s34 offen\n\t"
        "s_waitcnt lgkmcnt(0)\n\t"
        "s_waitcnt vmcnt(16)\n\t"
        "ds_write_b128 v122, a[96:99] offset:0\n\t"
        "ds_write_b128 v122, a[100:103] offset:1056\n\t"
        "ds_write_b128 v122, a[104:107] offset:2176\n\t"
        "ds_write_b128 v122, a[108:111] offset:3232\n\t"
        "ds_write_b128 v122, a[112:115] offset:4352\n\t"
        "ds_write_b128 v122, a[116:119] offset:5408\n\t"
        "ds_write_b128 v122, a[120:123] offset:6528\n\t"
        "ds_write_b128 v122, a[124:127] offset:7584\n\t"
        "ds_read_b64_tr_b16 a[96:97], v123 offset:0\n\t"
        "ds_read_b64_tr_b16 a[98:99], v123 offset:128\n\t"
        "ds_read_b64_tr_b16 a[100:101], v123 offset:512\n\t"
        "ds_read_b64_tr_b16 a[102:103], v123 offset:640\n\t"
        "s_waitcnt lgkmcnt(8)\n\t"
        "ds_read_b64_tr_b16 a[104:105], v123 offset:32\n\t"
        "ds_read_b64_tr_b16 a[106:107], v123 offset:160\n\t"
        "ds_read_b64_tr_b16 a[108:109], v123 offset:544\n\t"
        "ds_read_b64_tr_b16 a[110:111], v123 offset:672\n\t"
        "s_waitcnt lgkmcnt(8)\n\t"
        "ds_read_b64_tr_b16 a[112:113], v123 offset:4352\n\t"
        "ds_read_b64_tr_b16 a[114:115], v123 offset:4480\n\t"
        "ds_read_b64_tr_b16 a[116:117], v123 offset:4864\n\t"
        "ds_read_b64_tr_b16 a[118:119], v123 offset:4992\n\t"
        "s_waitcnt lgkmcnt(8)\n\t"
        "ds_read_b64_tr_b16 a[120:121], v123 offset:4384\n\t"
        "ds_read_b64_tr_b16 a[122:123], v123 offset:4512\n\t"
        "ds_read_b64_tr_b16 a[124:125], v123 offset:4896\n\t"
        "ds_read_b64_tr_b16 a[126:127], v123 offset:5024\n\t"
        "s_waitcnt lgkmcnt(8)\n\t"
        "s_add_u32 s35, s68, 6\n\t"
        "s_and_b32 s35, s35, 7\n\t"
        "s_lshl_b32 s35, s35, 18\n\t"
        "s_lshl_b32 s34, s43, 21\n\t"
        "s_add_u32 s34, s34, s35\n\t"
        "buffer_load_dwordx4 a[192:195], v97, s[20:23], s34 offen\n\t"
        "s_add_u32 s34, s34, 0x8000\n\t"
        "buffer_load_dwordx4 a[196:199], v97, s[20:23], s34 offen\n\t"
        "s_add_u32 s34, s34, 0x8000\n\t"
        "buffer_load_dwordx4 a[200:203], v97, s[20:23], s34 offen\n\t"
        "s_add_u32 s34, s34, 0x8000\n\t"
        "buffer_load_dwordx4 a[204:207], v97, s[20:23], s34 offen\n\t"
        "s_add_u32 s34, s34, 0x8000\n\t"
        "buffer_load_dwordx4 a[208:211], v97, s[20:23], s34 offen\n\t"
        "s_add_u32 s34, s34, 0x8000\n\t"
        "buffer_load_dwordx4 a[212:215], v97, s[20:23], s34 offen\n\t"
        "s_add_u32 s34, s34, 0x8000\n\t"
        "buffer_load_dwordx4 a[216:219], v97, s[20:23], s34 offen\n\t"
        "s_add_u32 s34, s34, 0x8000\n\t"
        "buffer_load_dwordx4 a[220:223], v97, s[20:23], s34 offen\n\t"
        "s_waitcnt lgkmcnt(0)\n\t"
        "s_waitcnt vmcnt(16)\n\t"
        "ds_write_b128 v122, a[128:131] offset:0\n\t"
        "ds_write_b128 v122, a[132:135] offset:1056\n\t"
        "ds_write_b128 v122, a[136:139] offset:2176\n\t"
        "ds_write_b128 v122, a[140:143] offset:3232\n\t"
        "ds_write_b128 v122, a[144:147] offset:4352\n\t"
        "ds_write_b128 v122, a[148:151] offset:5408\n\t"
        "ds_write_b128 v122, a[152:155] offset:6528\n\t"
        "ds_write_b128 v122, a[156:159] offset:7584\n\t"
        "ds_read_b64_tr_b16 a[128:129], v123 offset:0\n\t"
        "ds_read_b64_tr_b16 a[130:131], v123 offset:128\n\t"
        "ds_read_b64_tr_b16 a[132:133], v123 offset:512\n\t"
        "ds_read_b64_tr_b16 a[134:135], v123 offset:640\n\t"
        "s_waitcnt lgkmcnt(8)\n\t"
        "ds_read_b64_tr_b16 a[136:137], v123 offset:32\n\t"
        "ds_read_b64_tr_b16 a[138:139], v123 offset:160\n\t"
        "ds_read_b64_tr_b16 a[140:141], v123 offset:544\n\t"
        "ds_read_b64_tr_b16 a[142:143], v123 offset:672\n\t"
        "s_waitcnt lgkmcnt(8)\n\t"
        "ds_read_b64_tr_b16 a[144:145], v123 offset:4352\n\t"
        "ds_read_b64_tr_b16 a[146:147], v123 offset:4480\n\t"
        "ds_read_b64_tr_b16 a[148:149], v123 offset:4864\n\t"
        "ds_read_b64_tr_b16 a[150:151], v123 offset:4992\n\t"
        "s_waitcnt lgkmcnt(8)\n\t"
        "ds_read_b64_tr_b16 a[152:153], v123 offset:4384\n\t"
        "ds_read_b64_tr_b16 a[154:155], v123 offset:4512\n\t"
        "ds_read_b64_tr_b16 a[156:157], v123 offset:4896\n\t"
        "ds_read_b64_tr_b16 a[158:159], v123 offset:5024\n\t"
        "s_waitcnt lgkmcnt(8)\n\t"
        "s_add_u32 s35, s68, 7\n\t"
        "s_and_b32 s35, s35, 7\n\t"
        "s_lshl_b32 s35, s35, 18\n\t"
        "s_lshl_b32 s34, s43, 21\n\t"
        "s_add_u32 s34, s34, s35\n\t"
        "buffer_load_dwordx4 a[224:227], v97, s[20:23], s34 offen\n\t"
        "s_add_u32 s34, s34, 0x8000\n\t"
        "buffer_load_dwordx4 a[228:231], v97, s[20:23], s34 offen\n\t"
        "s_add_u32 s34, s34, 0x8000\n\t"
        "buffer_load_dwordx4 a[232:235], v97, s[20:23], s34 offen\n\t"
        "s_add_u32 s34, s34, 0x8000\n\t"
        "buffer_load_dwordx4 a[236:239], v97, s[20:23], s34 offen\n\t"
        "s_add_u32 s34, s34, 0x8000\n\t"
        "buffer_load_dwordx4 a[240:243], v97, s[20:23], s34 offen\n\t"
        "s_add_u32 s34, s34, 0x8000\n\t"
        "buffer_load_dwordx4 a[244:247], v97, s[20:23], s34 offen\n\t"
        "s_add_u32 s34, s34, 0x8000\n\t"
        "buffer_load_dwordx4 a[248:251], v97, s[20:23], s34 offen\n\t"
        "s_add_u32 s34, s34, 0x8000\n\t"
        "buffer_load_dwordx4 a[252:255], v97, s[20:23], s34 offen\n\t"
        "s_waitcnt lgkmcnt(0)\n\t"
        "s_waitcnt vmcnt(16)\n\t"
        "ds_write_b128 v122, a[160:163] offset:0\n\t"
        "ds_write_b128 v122, a[164:167] offset:1056\n\t"
        "ds_write_b128 v122, a[168:171] offset:2176\n\t"
        "ds_write_b128 v122, a[172:175] offset:3232\n\t"
        "ds_write_b128 v122, a[176:179] offset:4352\n\t"
        "ds_write_b128 v122, a[180:183] offset:5408\n\t"
        "ds_write_b128 v122, a[184:187] offset:6528\n\t"
        "ds_write_b128 v122, a[188:191] offset:7584\n\t"
        "ds_read_b64_tr_b16 a[160:161], v123 offset:0\n\t"
        "ds_read_b64_tr_b16 a[162:163], v123 offset:128\n\t"
        "ds_read_b64_tr_b16 a[164:165], v123 offset:512\n\t"
        "ds_read_b64_tr_b16 a[166:167], v123 offset:640\n\t"
        "s_waitcnt lgkmcnt(8)\n\t"
        "ds_read_b64_tr_b16 a[168:169], v123 offset:32\n\t"
        "ds_read_b64_tr_b16 a[170:171], v123 offset:160\n\t"
        "ds_read_b64_tr_b16 a[172:173], v123 offset:544\n\t"
        "ds_read_b64_tr_b16 a[174:175], v123 offset:672\n\t"
        "s_waitcnt lgkmcnt(8)\n\t"
        "ds_read_b64_tr_b16 a[176:177], v123 offset:4352\n\t"
        "ds_read_b64_tr_b16 a[178:179], v123 offset:4480\n\t"
        "ds_read_b64_tr_b16 a[180:181], v123 offset:4864\n\t"
        "ds_read_b64_tr_b16 a[182:183], v123 offset:4992\n\t"
        "s_waitcnt lgkmcnt(8)\n\t"
        "ds_read_b64_tr_b16 a[184:185], v123 offset:4384\n\t"
        "ds_read_b64_tr_b16 a[186:187], v123 offset:4512\n\t"
        "ds_read_b64_tr_b16 a[188:189], v123 offset:4896\n\t"
        "ds_read_b64_tr_b16 a[190:191], v123 offset:5024\n\t"
        "s_waitcnt lgkmcnt(8)\n\t"
        "s_waitcnt lgkmcnt(0)\n\t"
        "s_waitcnt vmcnt(8)\n\t"
        "ds_write_b128 v122, a[192:195] offset:0\n\t"
        "ds_write_b128 v122, a[196:199] offset:1056\n\t"
        "ds_write_b128 v122, a[200:203] offset:2176\n\t"
        "ds_write_b128 v122, a[204:207] offset:3232\n\t"
        "ds_write_b128 v122, a[208:211] offset:4352\n\t"
        "ds_write_b128 v122, a[212:215] offset:5408\n\t"
        "ds_write_b128 v122, a[216:219] offset:6528\n\t"
        "ds_write_b128 v122, a[220:223] offset:7584\n\t"
        "ds_read_b64_tr_b16 a[192:193], v123 offset:0\n\t"
        "ds_read_b64_tr_b16 a[194:195], v123 offset:128\n\t"
        "ds_read_b64_tr_b16 a[196:197], v123 offset:512\n\t"
        "ds_read_b64_tr_b16 a[198:199], v123 offset:640\n\t"
        "s_waitcnt lgkmcnt(8)\n\t"
        "ds_read_b64_tr_b16 a[200:201], v123 offset:32\n\t"
        "ds_read_b64_tr_b16 a[202:203], v123 offset:160\n\t"
        "ds_read_b64_tr_b16 a[204:205], v123 offset:544\n\t"
        "ds_read_b64_tr_b16 a[206:207], v123 offset:672\n\t"
        "s_waitcnt lgkmcnt(8)\n\t"
        "ds_read_b64_tr_b16 a[208:209], v123 offset:4352\n\t"
        "ds_read_b64_tr_b16 a[210:211], v123 offset:4480\n\t"
        "ds_read_b64_tr_b16 a[212:213], v123 offset:4864\n\t"
        "ds_read_b64_tr_b16 a[214:215], v123 offset:4992\n\t"
        "s_waitcnt lgkmcnt(8)\n\t"
        "ds_read_b64_tr_b16 a[216:217], v123 offset:4384\n\t"
        "ds_read_b64_tr_b16 a[218:219], v123 offset:4512\n\t"
        "ds_read_b64_tr_b16 a[220:221], v123 offset:4896\n\t"
        "ds_read_b64_tr_b16 a[222:223], v123 offset:5024\n\t"
        "s_waitcnt lgkmcnt(8)\n\t"
        "s_waitcnt lgkmcnt(0)\n\t"
        "s_waitcnt vmcnt(0)\n\t"
        "ds_write_b128 v122, a[224:227] offset:0\n\t"
        "ds_write_b128 v122, a[228:231] offset:1056\n\t"
        "ds_write_b128 v122, a[232:235] offset:2176\n\t"
        "ds_write_b128 v122, a[236:239] offset:3232\n\t"
        "ds_write_b128 v122, a[240:243] offset:4352\n\t"
        "ds_write_b128 v122, a[244:247] offset:5408\n\t"
        "ds_write_b128 v122, a[248:251] offset:6528\n\t"
        "ds_write_b128 v122, a[252:255] offset:7584\n\t"
        "ds_read_b64_tr_b16 a[224:225], v123 offset:0\n\t"
        "ds_read_b64_tr_b16 a[226:227], v123 offset:128\n\t"
        "ds_read_b64_tr_b16 a[228:229], v123 offset:512\n\t"
        "ds_read_b64_tr_b16 a[230:231], v123 offset:640\n\t"
        "s_waitcnt lgkmcnt(8)\n\t"
        "ds_read_b64_tr_b16 a[232:233], v123 offset:32\n\t"
        "ds_read_b64_tr_b16 a[234:235], v123 offset:160\n\t"
        "ds_read_b64_tr_b16 a[236:237], v123 offset:544\n\t"
        "ds_read_b64_tr_b16 a[238:239], v123 offset:672\n\t"
        "s_waitcnt lgkmcnt(8)\n\t"
        "ds_read_b64_tr_b16 a[240:241], v123 offset:4352\n\t"
        "ds_read_b64_tr_b16 a[242:243], v123 offset:4480\n\t"
        "ds_read_b64_tr_b16 a[244:245], v123 offset:4864\n\t"
        "ds_read_b64_tr_b16 a[246:247], v123 offset:4992\n\t"
        "s_waitcnt lgkmcnt(8)\n\t"
        "ds_read_b64_tr_b16 a[248:249], v123 offset:4384\n\t"
        "ds_read_b64_tr_b16 a[250:251], v123 offset:4512\n\t"
        "ds_read_b64_tr_b16 a[252:253], v123 offset:4896\n\t"
        "ds_read_b64_tr_b16 a[254:255], v123 offset:5024\n\t"
        "s_waitcnt lgkmcnt(8)\n\t"
        "s_mov_b32 s35, 33792\n\t"
        "buffer_load_dword v0, off, s[12:15], s35 sc1\n\t"
        "s_waitcnt lgkmcnt(0)\n\t"
        "s_waitcnt vmcnt(0)\n\t"
        "s_mov_b32 s42, 0\n\t"
        "LB_cal%=:\n\t"
        "v_readfirstlane_b32 s34, v0\n\t"
        "s_nop 0\n\t"
        "s_cmp_lg_u32 s34, 0\n\t"
        "s_cbranch_scc1 LB_calok%=\n\t"
        "s_sleep 4\n\t"
        "s_mov_b32 s35, 33792\n\t"
        "buffer_load_dword v0, off, s[12:15], s35 sc1\n\t"
        "s_waitcnt vmcnt(0)\n\t"
        "s_add_u32 s42, s42, 1\n\t"
        "s_cmp_lt_u32 s42, 0x100000\n\t"
        "s_cbranch_scc1 LB_cal%=\n\t"
        "s_movk_i32 s34, 8\n\t"
        "s_branch LB_die%=\n\t"
        "LB_calok%=:\n\t"
        "s_and_b32 s34, s34, 8192\n\t"
        "s_add_u32 s8, s8, s34\n\t"
        "s_addc_u32 s9, s9, 0\n\t"
        "v_min_u32_e32 v109, -2, v109\n\t"
        "buffer_store_dword v109, v116, s[8:11], 0 offen sc1\n\t"
        "s_memrealtime s[32:33]\n\t"
        "s_waitcnt lgkmcnt(0)\n\t"
        "v_mov_b32_e32 v110, s32\n\t"
        "s_lshl_b32 s34, s29, 8\n\t"
        "s_lshl_b32 s35, s30, 20\n\t"
        "s_or_b32 s54, s34, s35\n\t"
        "s_add_u32 s34, s54, 12589056\n\t"
        "v_mov_b32_e32 v111, s34\n\t"
        "s_barrier\n\t"
        "s_cmp_lg_u32 s43, 0\n\t"
        "s_cbranch_scc1 LB_st%=\n\t"
        "v_mov_b32_e32 v2, 1\n\t"
        "s_mov_b32 s35, 34304\n\t"
        "s_mov_b64 exec, 1\n\t"
        "buffer_atomic_add v2, off, s[12:15], s35 sc0\n\t"
        "s_mov_b64 exec, -1\n\t"
        "s_waitcnt vmcnt(0)\n\t"
        "v_readfirstlane_b32 s34, v2\n\t"
        "s_nop 0\n\t"
        "s_cmp_lg_u32 s34, 95\n\t"
        "s_cbranch_scc1 LB_st%=\n\t"
        "s_memrealtime s[32:33]\n\t"
        "v_mbcnt_lo_u32_b32 v3, -1, 0\n\t"
        "v_lshlrev_b32_e32 v3, 7, v3\n\t"
        "s_waitcnt lgkmcnt(0)\n\t"
        "s_add_u32 s34, s32, 50\n\t"
        "s_or_b32 s34, s34, 1\n\t"
        "v_mov_b32_e32 v2, s34\n\t"
        "s_mov_b32 s35, 36864\n\t"
        "s_mov_b64 exec, 0xffff\n\t"
        "buffer_store_dword v2, v3, s[12:15], s35 offen sc1\n\t"
        "s_mov_b64 exec, -1\n\t"
        "LB_st%=:\n\t"
        "s_mov_b32 s24, 0\n\t"
        "s_mov_b32 s26, 0\n\t"
        "s_mov_b32 s27, 0x80000\n\t"
        "s_mov_b32 s28, 0x180000\n\t"
        "s_mov_b32 s31, 0\n\t"
        "s_mov_b32 s47, 0\n\t"
        "s_mov_b32 s38, 0xff00ff00\n\t"
        "s_mov_b32 s39, 0xff00ff00\n\t"
        "s_mov_b32 s40, 0\n\t"
        "s_mov_b32 s41, -1\n\t"
        "v_mov_b64_e32 v[0:1], 0\n\t"
        "v_mov_b64_e32 v[2:3], 0\n\t"
        "v_mov_b64_e32 v[4:5], 0\n\t"
        "v_mov_b64_e32 v[6:7], 0\n\t"
        "v_mov_b64_e32 v[8:9], 0\n\t"
        "v_mov_b64_e32 v[10:11], 0\n\t"
        "v_mov_b64_e32 v[12:13], 0\n\t"
        "v_mov_b64_e32 v[14:15], 0\n\t"
        "s_mov_b32 s42, 0\n\t"
        "LB_gw%=:\n\t"
        "buffer_load_dword v16, off, s[12:15], s45 sc1\n\t"
        "s_waitcnt vmcnt(0)\n\t"
        "v_readfirstlane_b32 s25, v16\n\t"
        "s_nop 0\n\t"
        "s_cmp_lg_u32 s25, 0\n\t"
        "s_cbranch_scc1 LB_loop%=\n\t"
        "s_sleep 2\n\t"
        "s_add_u32 s42, s42, 1\n\t"
        "s_cmp_lt_u32 s42, 0x100000\n\t"
        "s_cbranch_scc1 LB_gw%=\n\t"
        "s_movk_i32 s34, 9\n\t"
        "s_branch LB_die%=\n\t"
        "LB_loop%=:\n\t"
        "s_mov_b32 s42, 0\n\t"
        "s_sub_u32 s35, s25, 8\n\t"
        "s_waitcnt lgkmcnt(0)\n\t"
        "s_memrealtime s[0:1]\n\t"
        "s_memrealtime s[2:3]\n\t"
        "s_memrealtime s[4:5]\n\t"
        "LB_wa%=:\n\t"
        "s_memrealtime s[6:7]\n\t"
        "s_waitcnt lgkmcnt(6)\n\t"
        "s_sub_u32 s34, s35, s0\n\t"
        "s_cmpk_lt_u32 s34, 0x2000\n\t"
        "s_cbranch_scc0 LB_wa%=x\n\t"
        "s_memrealtime s[0:1]\n\t"
        "s_waitcnt lgkmcnt(6)\n\t"
        "s_sub_u32 s34, s35, s2\n\t"
        "s_cmpk_lt_u32 s34, 0x2000\n\t"
        "s_cbranch_scc0 LB_wa%=x\n\t"
        "s_memrealtime s[2:3]\n\t"
        "s_waitcnt lgkmcnt(6)\n\t"
        "s_sub_u32 s34, s35, s4\n\t"
        "s_cmpk_lt_u32 s34, 0x2000\n\t"
        "s_cbranch_scc0 LB_wa%=x\n\t"
        "s_memrealtime s[4:5]\n\t"
        "s_waitcnt lgkmcnt(6)\n\t"
        "s_sub_u32 s34, s35, s6\n\t"
        "s_cmpk_lt_u32 s34, 0x2000\n\t"
        "s_cbranch_scc1 LB_wa%=\n\t"
        "s_nop 3\n\t"
        "LB_wa%=x:\n\t"
        "s_add_i32 s34, s34, 4\n\t"
        "s_max_i32 s34, s34, 0\n\t"
        "s_bitcmp1_b32 s34, 1\n\t"
        "s_cbranch_scc0 LB_wa%=y\n\t"
        "s_nop 15\n\t"
        "LB_wa%=y:\n\t"
        "s_bitcmp1_b32 s34, 0\n\t"
        "s_cbranch_scc0 LB_wa%=z\n\t"
        "s_nop 9\n\t"
        "LB_wa%=z:\n\t"
        "LB_rd%=:\n\t"
        "buffer_load_dwordx4 v[16:19], v112, s[8:11], s26 offen offset:0 sc1\n\t"
        "s_nop 9\n\t"
        "buffer_load_dwordx4 v[20:23], v112, s[8:11], s26 offen offset:1024 sc1\n\t"
        "s_nop 9\n\t"
        "buffer_load_dwordx4 v[24:27], v112, s[8:11], s26 offen offset:2048 sc1\n\t"
        "s_nop 9\n\t"
        "buffer_load_dwordx4 v[28:31], v112, s[8:11], s26 offen offset:3072 sc1\n\t"
        "LB_go%=:\n\t"
        "s_waitcnt vmcnt(3)\n\t"
        "v_smfmac_f32_16x16x64_bf16 v[0:3], v[16:19], v[128:135], v106\n\t"
        "v_max3_u32 v108, v16, v17, v18\n\t"
        "v_max_u32_e32 v108, v19, v108\n\t"
        "v_smfmac_f32_16x16x64_bf16 v[4:7], v[16:19], v[136:143], v106\n\t"
        "v_smfmac_f32_16x16x64_bf16 v[8:11], v[16:19], v[144:151], v106\n\t"
        "v_smfmac_f32_16x16x64_bf16 v[12:15], v[16:19], v[152:159], v106\n\t"
        "s_waitcnt vmcnt(2)\n\t"
        "v_smfmac_f32_16x16x64_bf16 v[0:3], v[20:23], v[160:167], v106\n\t"
        "v_max3_u32 v108, v20, v21, v108\n\t"
        "v_max3_u32 v108, v22, v23, v108\n\t"
        "v_smfmac_f32_16x16x64_bf16 v[4:7], v[20:23], v[168:175], v106\n\t"
        "v_smfmac_f32_16x16x64_bf16 v[8:11], v[20:23], v[176:183], v106\n\t"
        "v_smfmac_f32_16x16x64_bf16 v[12:15], v[20:23], v[184:191], v106\n\t"
        "s_waitcnt vmcnt(1)\n\t"
        "v_smfmac_f32_16x16x64_bf16 v[0:3], v[24:27], v[192:199], v106\n\t"
        "v_max3_u32 v108, v24, v25, v108\n\t"
        "v_max3_u32 v108, v26, v27, v108\n\t"
        "v_smfmac_f32_16x16x64_bf16 v[4:7], v[24:27], v[200:207], v106\n\t"
        "v_smfmac_f32_16x16x64_bf16 v[8:11], v[24:27], v[208:215], v106\n\t"
        "v_smfmac_f32_16x16x64_bf16 v[12:15], v[24:27], v[216:223], v106\n\t"
        "s_waitcnt vmcnt(0)\n\t"
        "v_smfmac_f32_16x16x64_bf16 v[0:3], v[28:31], v[224:231], v106\n\t"
        "v_max3_u32 v108, v28, v29, v108\n\t"
        "v_max3_u32 v108, v30, v31, v108\n\t"
        "v_smfmac_f32_16x16x64_bf16 v[4:7], v[28:31], v[232:239], v106\n\t"
        "v_cmp_eq_u32_e32 vcc, -1, v108\n\t"
        "v_smfmac_f32_16x16x64_bf16 v[8:11], v[28:31], v[240:247], v106\n\t"
        "v_smfmac_f32_16x16x64_bf16 v[12:15], v[28:31], v[248:255], v106\n\t"
        "s_cbranch_vccnz LB_rdfail%=\n\t"
        "s_nop 3\n\t"
        "v_permlane32_swap_b32_e32 v0, v4\n\t"
        "v_permlane32_swap_b32_e32 v1, v5\n\t"
        "v_permlane32_swap_b32_e32 v2, v6\n\t"
        "v_permlane32_swap_b32_e32 v3, v7\n\t"
        "v_pk_add_f32 v[0:1], v[0:1], v[4:5]\n\t"
        "v_pk_add_f32 v[2:3], v[2:3], v[6:7]\n\t"
        "ds_write_b128 v118, v[0:3] offset:0\n\t"
        "v_permlane32_swap_b32_e32 v8, v12\n\t"
        "v_permlane32_swap_b32_e32 v9, v13\n\t"
        "v_permlane32_swap_b32_e32 v10, v14\n\t"
        "v_permlane32_swap_b32_e32 v11, v15\n\t"
        "v_pk_add_f32 v[8:9], v[8:9], v[12:13]\n\t"
        "v_pk_add_f32 v[10:11], v[10:11], v[14:15]\n\t"
        "ds_write_b128 v118, v[8:11] offset:1024\n\t"
        "buffer_store_dwordx2 v[110:111], v117, s[8:11], s26 offen sc1\n\t"
        "s_waitcnt lgkmcnt(0)\n\t"
        "s_barrier\n\t"
        "ds_read_b128 v[16:19], v119 offset:0\n\t"
        "ds_read_b128 v[20:23], v119 offset:2048\n\t"
        "ds_read_b128 v[24:27], v119 offset:4096\n\t"
        "ds_read_b128 v[28:31], v119 offset:6144\n\t"
        "buffer_load_dwordx4 v[64:67], v124, s[8:11], s28 offen sc1\n\t"
        "s_waitcnt lgkmcnt(2)\n\t"
        "v_pk_add_f32 v[16:17], v[16:17], v[20:21]\n\t"
        "v_pk_add_f32 v[18:19], v[18:19], v[22:23]\n\t"
        "s_waitcnt lgkmcnt(0)\n\t"
        "v_pk_add_f32 v[24:25], v[24:25], v[28:29]\n\t"
        "v_pk_add_f32 v[26:27], v[26:27], v[30:31]\n\t"
        "v_pk_add_f32 v[16:17], v[16:17], v[24:25]\n\t"
        "v_pk_add_f32 v[18:19], v[18:19], v[26:27]\n\t"
        "v_cvt_pk_bf16_f32 v20, v16, v17\n\t"
        "v_cvt_pk_bf16_f32 v21, v18, v19\n\t"
        "buffer_store_dwordx2 v[20:21], v114, s[8:11], s26 offen sc1\n\t"
        "s_cmp_eq_u32 s24, 63\n\t"
        "s_cbranch_scc1 LB_end%=\n\t"
        "s_memrealtime s[52:53]\n\t"
        "v_mov_b64_e32 v[24:25], -1\n\t"
        "v_mov_b64_e32 v[32:33], 0\n\t"
        "v_mov_b64_e32 v[34:35], 0\n\t"
        "v_mov_b64_e32 v[36:37], 0\n\t"
        "v_mov_b64_e32 v[38:39], 0\n\t"
        "v_mov_b64_e32 v[40:41], 0\n\t"
        "v_mov_b64_e32 v[42:43], 0\n\t"
        "v_mov_b64_e32 v[44:45], 0\n\t"
        "v_mov_b64_e32 v[46:47], 0\n\t"
        "v_mov_b64_e32 v[0:1], 0\n\t"
        "v_mov_b64_e32 v[2:3], 0\n\t"
        "v_mov_b64_e32 v[4:5], 0\n\t"
        "v_mov_b64_e32 v[6:7], 0\n\t"
        "v_mov_b64_e32 v[8:9], 0\n\t"
        "v_mov_b64_e32 v[10:11], 0\n\t"
        "v_mov_b64_e32 v[12:13], 0\n\t"
        "v_mov_b64_e32 v[14:15], 0\n\t"
        "s_add_u32 s46, s26, s69\n\t"
        "s_add_u32 s48, s26, s70\n\t"
        "s_add_u32 s73, s26, s71\n\t"
        "s_add_u32 s74, s26, s72\n\t"
        "s_sleep 1\n\t"
        "buffer_load_dwordx2 v[104:105], v113, s[8:11], s26 offen sc1\n\t"
        "buffer_store_dword v24, v116, s[8:11], s28 offen sc1\n\t"
        "s_xor_b32 s34, s26, 0x100000\n\t"
        "buffer_store_dwordx2 v[24:25], v114, s[8:11], s34 offen sc1\n\t"
        "s_add_u32 s66, s49, s43\n\t"
        "s_cmp_lg_u32 s66, 32\n\t"
        "s_cbranch_scc1 LB_norep%=\n\t"
        "s_waitcnt vmcnt(4)\n\t"
        "s_sub_u32 s66, s24, 1\n\t"
        "v_lshrrev_b32_e32 v68, 16, v64\n\t"
        "v_cmp_eq_u32_e32 vcc, s66, v68\n\t"
        "v_subrev_u32_e32 v64, s59, v64\n\t"
        "v_bfe_i32 v64, v64, 0, 16\n\t"
        "v_cndmask_b32_e32 v64, 0, v64, vcc\n\t"
        "v_lshrrev_b32_e32 v68, 16, v65\n\t"
        "v_cmp_eq_u32_e32 vcc, s66, v68\n\t"
        "v_subrev_u32_e32 v65, s59, v65\n\t"
        "v_bfe_i32 v65, v65, 0, 16\n\t"
        "v_cndmask_b32_e32 v65, 0, v65, vcc\n\t"
        "v_lshrrev_b32_e32 v68, 16, v66\n\t"
        "v_cmp_eq_u32_e32 vcc, s66, v68\n\t"
        "v_subrev_u32_e32 v66, s59, v66\n\t"
        "v_bfe_i32 v66, v66, 0, 16\n\t"
        "v_cndmask_b32_e32 v66, 0, v66, vcc\n\t"
        "v_lshrrev_b32_e32 v68, 16, v67\n\t"
        "v_cmp_eq_u32_e32 vcc, s66, v68\n\t"
        "v_subrev_u32_e32 v67, s59, v67\n\t"
        "v_bfe_i32 v67, v67, 0, 16\n\t"
        "v_cndmask_b32_e32 v67, 0, v67, vcc\n\t"
        "v_max3_i32 v64, v64, v65, v66\n\t"
        "v_max_i32_e32 v64, v64, v67\n\t"
        "s_nop 1\n\t"
        "v_max_i32_dpp v64, v64, v64 quad_perm:[1,0,3,2] row_mask:0xf bank_mask:0xf\n\t"
        "s_nop 1\n\t"
        "v_max_i32_dpp v64, v64, v64 quad_perm:[2,3,0,1] row_mask:0xf bank_mask:0xf\n\t"
        "s_nop 1\n\t"
        "v_max_i32_dpp v64, v64, v64 row_ror:4 row_mask:0xf bank_mask:0xf\n\t"
        "s_nop 1\n\t"
        "v_max_i32_dpp v64, v64, v64 row_ror:8 row_mask:0xf bank_mask:0xf\n\t"
        "v_mov_b32_e32 v65, v64\n\t"
        "s_nop 1\n\t"
        "v_permlane16_swap_b32_e32 v64, v65\n\t"
        "v_max_i32_e32 v64, v64, v65\n\t"
        "v_mov_b32_e32 v65, v64\n\t"
        "s_nop 1\n\t"
        "v_permlane32_swap_b32_e32 v64, v65\n\t"
        "v_max_i32_e32 v64, v64, v65\n\t"
        "s_nop 0\n\t"
        "v_readfirstlane_b32 s64, v64\n\t"
        "LB_norep%=:\n\t"
        "s_waitcnt vmcnt(2) lgkmcnt(0)\n\t"
        "s_mov_b32 s61, s29\n\t"
        "s_mov_b32 s62, s30\n\t"
        "s_mov_b32 s63, s31\n\t"
        "v_and_b32_e32 v30, 0xff, v105\n\t"
        "v_cmp_ne_u32_e32 vcc, s24, v30\n\t"
        "s_nop 0\n\t"
        "s_cbranch_vccnz LB_stre%=\n\t"
        "LB_stok%=:\n\t"
        "v_subrev_u32_e32 v104, s25, v104\n\t"
        "s_nop 1\n\t"
        "v_max_i32_dpp v104, v104, v104 quad_perm:[1,0,3,2] row_mask:0xf bank_mask:0xf\n\t"
        "s_nop 1\n\t"
        "v_max_i32_dpp v104, v104, v104 quad_perm:[2,3,0,1] row_mask:0xf bank_mask:0xf\n\t"
        "s_nop 1\n\t"
        "v_max_i32_dpp v104, v104, v104 row_ror:4 row_mask:0xf bank_mask:0xf\n\t"
        "s_nop 1\n\t"
        "v_max_i32_dpp v104, v104, v104 row_ror:8 row_mask:0xf bank_mask:0xf\n\t"
        "s_nop 1\n\t"
        "v_max_i32_dpp v104, v104, v104 row_bcast:15 row_mask:0xa bank_mask:0xf\n\t"
        "s_nop 1\n\t"
        "v_max_i32_dpp v104, v104, v104 row_bcast:31 row_mask:0xc bank_mask:0xf\n\t"
        "v_readfirstlane_b32 s35, v105\n\t"
        "v_readlane_b32 s36, v104, 63\n\t"
        "s_bfe_u32 s29, s35, 0xc0008\n\t"
        "s_lshr_b32 s30, s35, 20\n\t"
        "s_add_u32 s35, s31, s50\n\t"
        "s_mul_i32 s37, s50, 3\n\t"
        "s_min_u32 s35, s35, s37\n\t"
        "s_add_i32 s34, s36, s51\n\t"
        "s_cmp_gt_i32 s34, 0\n\t"
        "s_cselect_b32 s31, s35, 0\n\t"
        "s_cselect_b32 s34, s34, 0\n\t"
        "s_add_u32 s34, s34, s31\n\t"
        "s_add_u32 s37, s25, s34\n\t"
        "s_add_u32 s45, s37, s29\n\t"
        "s_add_u32 s25, s45, s30\n\t"
        "s_add_u32 s66, s49, s43\n\t"
        "s_cmp_lg_u32 s66, 32\n\t"
        "s_cbranch_scc1 LB_noctl%=\n\t"
        "s_cmp_eq_u32 s24, 0\n\t"
        "s_cbranch_scc1 LB_ctl0%=\n\t"
        "s_cmp_eq_u32 s24, 1\n\t"
        "s_cbranch_scc1 LB_ctl1%=\n\t"
        "s_add_i32 s65, s36, s62\n\t"
        "s_lshl_b32 s66, s65, 1\n\t"
        "s_sub_i32 s66, s66, s56\n\t"
        "s_cmp_gt_i32 s66, 56\n\t"
        "s_cbranch_scc1 LB_c1%=\n\t"
        "s_lshl_b32 s66, s65, 1\n\t"
        "s_add_u32 s34, s56, 16\n\t"
        "s_min_i32 s34, s34, s66\n\t"
        "s_sub_u32 s35, s56, s66\n\t"
        "s_ashr_i32 s35, s35, 2\n\t"
        "s_max_i32 s35, s35, 1\n\t"
        "s_sub_u32 s35, s56, s35\n\t"
        "s_max_i32 s56, s34, s35\n\t"
        "s_branch LB_c2%=\n\t"
        "LB_c1%=:\n\t"
        "s_add_u32 s56, s56, 2\n\t"
        "s_add_i32 s66, s64, s57\n\t"
        "s_cmp_gt_i32 s66, s61\n\t"
        "s_cbranch_scc1 LB_c2%=\n\t"
        "s_add_u32 s57, s57, 4\n\t"
        "s_min_u32 s57, s57, 25\n\t"
        "LB_c2%=:\n\t"
        "s_cmp_lg_u32 s63, 0\n\t"
        "s_cbranch_scc1 LB_c4%=\n\t"
        "s_lshl_b32 s66, s64, 1\n\t"
        "s_sub_i32 s66, s66, s55\n\t"
        "s_cmp_gt_i32 s66, 56\n\t"
        "s_cbranch_scc1 LB_c3%=\n\t"
        "s_lshl_b32 s66, s64, 1\n\t"
        "s_add_u32 s34, s55, 16\n\t"
        "s_min_i32 s34, s34, s66\n\t"
        "s_sub_u32 s35, s55, s66\n\t"
        "s_ashr_i32 s35, s35, 2\n\t"
        "s_max_i32 s35, s35, 1\n\t"
        "s_sub_u32 s35, s55, s35\n\t"
        "s_max_i32 s55, s34, s35\n\t"
        "s_branch LB_c4%=\n\t"
        "LB_c3%=:\n\t"
        "s_add_u32 s55, s55, 2\n\t"
        "s_cmp_lt_i32 s60, s58\n\t"
        "s_cbranch_scc1 LB_c4%=\n\t"
        "s_add_u32 s66, s58, 8\n\t"
        "s_cmp_gt_i32 s60, s66\n\t"
        "s_cbranch_scc1 LB_c4%=\n\t"
        "s_add_u32 s58, s58, 4\n\t"
        "s_min_u32 s58, s58, 23\n\t"
        "LB_c4%=:\n\t"
        "s_and_b32 s66, s24, 3\n\t"
        "s_cmp_eq_u32 s66, 0\n\t"
        "s_cselect_b32 s66, 1, 0\n\t"
        "s_sub_u32 s57, s57, s66\n\t"
        "s_max_u32 s57, s57, 9\n\t"
        "s_sub_u32 s58, s58, s66\n\t"
        "s_max_u32 s58, s58, 7\n\t"
        "s_add_u32 s34, s55, 1\n\t"
        "s_lshr_b32 s34, s34, 1\n\t"
        "s_add_u32 s34, s34, s57\n\t"
        "s_min_u32 s34, s34, 1000\n\t"
        "s_lshl_b32 s34, s34, 8\n\t"
        "s_add_u32 s35, s56, 1\n\t"
        "s_lshr_b32 s35, s35, 1\n\t"
        "s_add_u32 s35, s35, s58\n\t"
        "s_min_u32 s35, s35, 1000\n\t"
        "s_lshl_b32 s35, s35, 20\n\t"
        "s_or_b32 s54, s34, s35\n\t"
        "s_branch LB_ctl1%=\n\t"
        "LB_ctl0%=:\n\t"
        "s_movk_i32 s57, 9\n\t"
        "s_movk_i32 s58, 7\n\t"
        "s_sub_u32 s55, s29, 33\n\t"
        "s_lshl_b32 s55, s55, 1\n\t"
        "s_sub_u32 s56, s30, 19\n\t"
        "s_lshl_b32 s56, s56, 1\n\t"
        "LB_ctl1%=:\n\t"
        "s_mov_b32 s59, s37\n\t"
        "s_sub_u32 s60, 0, s36\n\t"
        "LB_noctl%=:\n\t"
        "s_add_u32 s34, s24, 1\n\t"
        "s_or_b32 s34, s34, s54\n\t"
        "v_mov_b32_e32 v111, s34\n\t"
        "s_mov_b32 s42, 0\n\t"
        "s_sub_u32 s35, s45, 8\n\t"
        "s_waitcnt lgkmcnt(0)\n\t"
        "s_memrealtime s[0:1]\n\t"
        "s_memrealtime s[2:3]\n\t"
        "s_memrealtime s[4:5]\n\t"
        "LB_wo%=:\n\t"
        "s_memrealtime s[6:7]\n\t"
        "s_waitcnt lgkmcnt(6)\n\t"
        "s_sub_u32 s34, s35, s0\n\t"
        "s_cmpk_lt_u32 s34, 0x2000\n\t"
        "s_cbranch_scc0 LB_wo%=x\n\t"
        "s_memrealtime s[0:1]\n\t"
        "s_waitcnt lgkmcnt(6)\n\t"
        "s_sub_u32 s34, s35, s2\n\t"
        "s_cmpk_lt_u32 s34, 0x2000\n\t"
        "s_cbranch_scc0 LB_wo%=x\n\t"
        "s_memrealtime s[2:3]\n\t"
        "s_waitcnt lgkmcnt(6)\n\t"
        "s_sub_u32 s34, s35, s4\n\t"
        "s_cmpk_lt_u32 s34, 0x2000\n\t"
        "s_cbranch_scc0 LB_wo%=x\n\t"
        "s_memrealtime s[4:5]\n\t"
        "s_waitcnt lgkmcnt(6)\n\t"
        "s_sub_u32 s34, s35, s6\n\t"
        "s_cmpk_lt_u32 s34, 0x2000\n\t"
        "s_cbranch_scc1 LB_wo%=\n\t"
        "s_nop 3\n\t"
        "LB_wo%=x:\n\t"
        "s_add_i32 s34, s34, 4\n\t"
        "s_max_i32 s34, s34, 0\n\t"
        "s_bitcmp1_b32 s34, 1\n\t"
        "s_cbranch_scc0 LB_wo%=y\n\t"
        "s_nop 15\n\t"
        "LB_wo%=y:\n\t"
        "s_bitcmp1_b32 s34, 0\n\t"
        "s_cbranch_scc0 LB_wo%=z\n\t"
        "s_nop 9\n\t"
        "LB_wo%=z:\n\t"
        "LB_ro%=:\n\t"
        "buffer_load_dwordx4 v[64:67], v115, s[8:11], s46 offen offset:0 sc1\n\t"
        "s_nop 11\n\t"
        "buffer_load_dwordx4 v[68:71], v115, s[8:11], s46 offen offset:1024 sc1\n\t"
        "s_nop 11\n\t"
        "buffer_load_dwordx4 v[72:75], v115, s[8:11], s48 offen offset:0 sc1\n\t"
        "s_nop 11\n\t"
        "buffer_load_dwordx4 v[76:79], v115, s[8:11], s48 offen offset:1024 sc1\n\t"
        "s_nop 11\n\t"
        "buffer_load_dwordx4 v[80:83], v115, s[8:11], s73 offen offset:0 sc1\n\t"
        "s_nop 11\n\t"
        "buffer_load_dwordx4 v[84:87], v115, s[8:11], s73 offen offset:1024 sc1\n\t"
        "s_nop 11\n\t"
        "buffer_load_dwordx4 v[88:91], v115, s[8:11], s74 offen offset:0 sc1\n\t"
        "s_nop 11\n\t"
        "buffer_load_dwordx4 v[92:95], v115, s[8:11], s74 offen offset:1024 sc1\n\t"
        "LB_go2%=:\n\t"
        "s_waitcnt vmcnt(7)\n\t"
        "v_smfmac_f32_16x16x64_bf16 v[40:43], v[64:67], a[16:23], v107\n\t"
        "v_max3_u32 v108, v64, v65, v66\n\t"
        "v_max_u32_e32 v108, v67, v108\n\t"
        "v_smfmac_f32_16x16x64_bf16 v[36:39], v[64:67], a[8:15], v107\n\t"
        "v_smfmac_f32_16x16x64_bf16 v[32:35], v[64:67], a[0:7], v107\n\t"
        "v_smfmac_f32_16x16x64_bf16 v[44:47], v[64:67], a[24:31], v107\n\t"
        "s_waitcnt vmcnt(6)\n\t"
        "v_smfmac_f32_16x16x64_bf16 v[40:43], v[68:71], a[48:55], v107\n\t"
        "v_max3_u32 v108, v68, v69, v108\n\t"
        "v_max3_u32 v108, v70, v71, v108\n\t"
        "v_smfmac_f32_16x16x64_bf16 v[36:39], v[68:71], a[40:47], v107\n\t"
        "v_smfmac_f32_16x16x64_bf16 v[32:35], v[68:71], a[32:39], v107\n\t"
        "v_smfmac_f32_16x16x64_bf16 v[44:47], v[68:71], a[56:63], v107\n\t"
        "s_waitcnt vmcnt(5)\n\t"
        "v_smfmac_f32_16x16x64_bf16 v[40:43], v[72:75], a[80:87], v107\n\t"
        "v_max3_u32 v108, v72, v73, v108\n\t"
        "v_max3_u32 v108, v74, v75, v108\n\t"
        "v_smfmac_f32_16x16x64_bf16 v[36:39], v[72:75], a[72:79], v107\n\t"
        "v_smfmac_f32_16x16x64_bf16 v[32:35], v[72:75], a[64:71], v107\n\t"
        "v_smfmac_f32_16x16x64_bf16 v[44:47], v[72:75], a[88:95], v107\n\t"
        "s_waitcnt vmcnt(4)\n\t"
        "v_smfmac_f32_16x16x64_bf16 v[40:43], v[76:79], a[112:119], v107\n\t"
        "v_max3_u32 v108, v76, v77, v108\n\t"
        "v_max3_u32 v108, v78, v79, v108\n\t"
        "v_smfmac_f32_16x16x64_bf16 v[36:39], v[76:79], a[104:111], v107\n\t"
        "v_smfmac_f32_16x16x64_bf16 v[32:35], v[76:79], a[96:103], v107\n\t"
        "v_smfmac_f32_16x16x64_bf16 v[44:47], v[76:79], a[120:127], v107\n\t"
        "s_waitcnt vmcnt(3)\n\t"
        "v_smfmac_f32_16x16x64_bf16 v[40:43], v[80:83], a[144:151], v107\n\t"
        "v_max3_u32 v108, v80, v81, v108\n\t"
        "v_max3_u32 v108, v82, v83, v108\n\t"
        "v_smfmac_f32_16x16x64_bf16 v[36:39], v[80:83], a[136:143], v107\n\t"
        "v_smfmac_f32_16x16x64_bf16 v[32:35], v[80:83], a[128:135], v107\n\t"
        "v_smfmac_f32_16x16x64_bf16 v[44:47], v[80:83], a[152:159], v107\n\t"
        "s_waitcnt vmcnt(2)\n\t"
        "v_smfmac_f32_16x16x64_bf16 v[40:43], v[84:87], a[176:183], v107\n\t"
        "v_max3_u32 v108, v84, v85, v108\n\t"
        "v_max3_u32 v108, v86, v87, v108\n\t"
        "v_smfmac_f32_16x16x64_bf16 v[36:39], v[84:87], a[168:175], v107\n\t"
        "v_smfmac_f32_16x16x64_bf16 v[32:35], v[84:87], a[160:167], v107\n\t"
        "v_smfmac_f32_16x16x64_bf16 v[44:47], v[84:87], a[184:191], v107\n\t"
        "s_waitcnt vmcnt(1)\n\t"
        "v_smfmac_f32_16x16x64_bf16 v[40:43], v[88:91], a[208:215], v107\n\t"
        "v_max3_u32 v108, v88, v89, v108\n\t"
        "v_max3_u32 v108, v90, v91, v108\n\t"
        "v_smfmac_f32_16x16x64_bf16 v[36:39], v[88:91], a[200:207], v107\n\t"
        "v_smfmac_f32_16x16x64_bf16 v[32:35], v[88:91], a[192:199], v107\n\t"
        "v_smfmac_f32_16x16x64_bf16 v[44:47], v[88:91], a[216:223], v107\n\t"
        "s_waitcnt vmcnt(0)\n\t"
        "v_smfmac_f32_16x16x64_bf16 v[40:43], v[92:95], a[240:247], v107\n\t"
        "v_max3_u32 v108, v92, v93, v108\n\t"
        "v_max3_u32 v108, v94, v95, v108\n\t"
        "v_smfmac_f32_16x16x64_bf16 v[36:39], v[92:95], a[232:239], v107\n\t"
        "v_cmp_eq_u32_e32 vcc, -1, v108\n\t"
        "v_smfmac_f32_16x16x64_bf16 v[32:35], v[92:95], a[224:231], v107\n\t"
        "v_smfmac_f32_16x16x64_bf16 v[44:47], v[92:95], a[248:255], v107\n\t"
        "s_cbranch_vccnz LB_rofail%=\n\t"
        "s_nop 3\n\t"
        "v_permlane32_swap_b32_e32 v40, v36\n\t"
        "v_permlane32_swap_b32_e32 v41, v37\n\t"
        "v_permlane32_swap_b32_e32 v42, v38\n\t"
        "v_permlane32_swap_b32_e32 v43, v39\n\t"
        "v_pk_add_f32 v[32:33], v[32:33], v[36:37]\n\t"
        "v_pk_add_f32 v[34:35], v[34:35], v[38:39]\n\t"
        "s_nop 0\n\t"
        "v_add_f32_dpp v32, v40, v44 quad_perm:[0,1,2,3] row_mask:0xc bank_mask:0xf\n\t"
        "v_add_f32_dpp v33, v41, v45 quad_perm:[0,1,2,3] row_mask:0xc bank_mask:0xf\n\t"
        "v_add_f32_dpp v34, v42, v46 quad_perm:[0,1,2,3] row_mask:0xc bank_mask:0xf\n\t"
        "v_add_f32_dpp v35, v43, v47 quad_perm:[0,1,2,3] row_mask:0xc bank_mask:0xf\n\t"
        "v_permlane16_swap_b32_e32 v32, v33\n\t"
        "s_nop 0\n\t"
        "v_permlane16_swap_b32_e32 v34, v35\n\t"
        "v_add_f32_e32 v32, v32, v33\n\t"
        "v_add_f32_e32 v34, v34, v35\n\t"
        "ds_write2_b32 v120, v32, v34 offset0:0 offset1:16\n\t"
        "s_waitcnt lgkmcnt(0)\n\t"
        "s_barrier\n\t"
        "ds_read_b64 v[64:65], v121 offset:0\n\t"
        "ds_read_b64 v[66:67], v121 offset:512\n\t"
        "ds_read_b64 v[68:69], v121 offset:1024\n\t"
        "ds_read_b64 v[70:71], v121 offset:1536\n\t"
        "s_waitcnt lgkmcnt(2)\n\t"
        "v_pk_add_f32 v[64:65], v[64:65], v[66:67]\n\t"
        "s_waitcnt lgkmcnt(0)\n\t"
        "v_pk_add_f32 v[68:69], v[68:69], v[70:71]\n\t"
        "v_pk_add_f32 v[64:65], v[64:65], v[68:69]\n\t"
        "v_cvt_pk_bf16_f32 v64, v64, v65\n\t"
        "buffer_store_dword v64, v116, s[8:11], s27 offen sc1\n\t"
        "s_memrealtime s[32:33]\n\t"
        "s_waitcnt lgkmcnt(0)\n\t"
        "v_mov_b32_e32 v110, s32\n\t"
        "s_add_u32 s24, s24, 1\n\t"
        "s_mov_b32 s28, s26\n\t"
        "s_mov_b32 s26, s27\n\t"
        "s_add_u32 s27, s27, 0x80000\n\t"
        "s_and_b32 s27, s27, 0x1fffff\n\t"
        "s_branch LB_loop%=\n\t"
        "LB_end%=:\n\t"
        "s_endpgm\n\t"
        "LB_stre%=:\n\t"
        "s_mov_b32 s42, 0\n\t"
        "LB_str2%=:\n\t"
        "buffer_load_dwordx2 v[104:105], v113, s[8:11], s26 offen sc1\n\t"
        "s_waitcnt vmcnt(0)\n\t"
        "v_and_b32_e32 v30, 0xff, v105\n\t"
        "v_cmp_ne_u32_e32 vcc, s24, v30\n\t"
        "s_nop 0\n\t"
        "s_cbranch_vccz LB_stok%=\n\t"
        "s_add_u32 s42, s42, 1\n\t"
        "s_cmp_lt_u32 s42, 0x100000\n\t"
        "s_cbranch_scc1 LB_str2%=\n\t"
        "s_movk_i32 s34, 5\n\t"
        "s_branch LB_die%=\n\t"
        "LB_rdfail%=:\n\t"
        "s_or_b32 s47, s47, 1\n\t"
        "buffer_load_dwordx4 v[16:19], v112, s[8:11], s26 offen offset:0 sc1\n\t"
        "buffer_load_dwordx4 v[20:23], v112, s[8:11], s26 offen offset:1024 sc1\n\t"
        "buffer_load_dwordx4 v[24:27], v112, s[8:11], s26 offen offset:2048 sc1\n\t"
        "buffer_load_dwordx4 v[28:31], v112, s[8:11], s26 offen offset:3072 sc1\n\t"
        "s_nop 7\n\t"
        "v_mov_b64_e32 v[0:1], 0\n\t"
        "v_mov_b64_e32 v[2:3], 0\n\t"
        "v_mov_b64_e32 v[4:5], 0\n\t"
        "v_mov_b64_e32 v[6:7], 0\n\t"
        "v_mov_b64_e32 v[8:9], 0\n\t"
        "v_mov_b64_e32 v[10:11], 0\n\t"
        "v_mov_b64_e32 v[12:13], 0\n\t"
        "v_mov_b64_e32 v[14:15], 0\n\t"
        "s_branch LB_rdre%=w\n\t"
        "LB_rdre%=:\n\t"
        "buffer_load_dwordx4 v[16:19], v112, s[8:11], s26 offen offset:0 sc1\n\t"
        "buffer_load_dwordx4 v[20:23], v112, s[8:11], s26 offen offset:1024 sc1\n\t"
        "buffer_load_dwordx4 v[24:27], v112, s[8:11], s26 offen offset:2048 sc1\n\t"
        "buffer_load_dwordx4 v[28:31], v112, s[8:11], s26 offen offset:3072 sc1\n\t"
        "LB_rdre%=w:\n\t"
        "s_waitcnt vmcnt(0) lgkmcnt(0)\n\t"
        "v_max3_u32 v108, v16, v17, v18\n\t"
        "v_max3_u32 v108, v108, v19, v20\n\t"
        "v_max3_u32 v108, v108, v21, v22\n\t"
        "v_max3_u32 v108, v108, v23, v24\n\t"
        "v_max3_u32 v108, v108, v25, v26\n\t"
        "v_max3_u32 v108, v108, v27, v28\n\t"
        "v_max3_u32 v108, v108, v29, v30\n\t"
        "v_max_u32_e32 v108, v108, v31\n\t"
        "v_cmp_eq_u32_e32 vcc, -1, v108\n\t"
        "s_nop 0\n\t"
        "s_cbranch_vccz LB_go%=\n\t"
        "s_add_u32 s42, s42, 1\n\t"
        "s_cmp_lt_u32 s42, 0x100000\n\t"
        "s_cbranch_scc1 LB_rdre%=\n\t"
        "s_movk_i32 s34, 3\n\t"
        "s_branch LB_die%=\n\t"
        "LB_rofail%=:\n\t"
        "s_or_b32 s47, s47, 4\n\t"
        "buffer_load_dwordx4 v[64:67], v115, s[8:11], s46 offen offset:0 sc1\n\t"
        "buffer_load_dwordx4 v[68:71], v115, s[8:11], s46 offen offset:1024 sc1\n\t"
        "buffer_load_dwordx4 v[72:75], v115, s[8:11], s48 offen offset:0 sc1\n\t"
        "buffer_load_dwordx4 v[76:79], v115, s[8:11], s48 offen offset:1024 sc1\n\t"
        "buffer_load_dwordx4 v[80:83], v115, s[8:11], s73 offen offset:0 sc1\n\t"
        "buffer_load_dwordx4 v[84:87], v115, s[8:11], s73 offen offset:1024 sc1\n\t"
        "buffer_load_dwordx4 v[88:91], v115, s[8:11], s74 offen offset:0 sc1\n\t"
        "buffer_load_dwordx4 v[92:95], v115, s[8:11], s74 offen offset:1024 sc1\n\t"
        "s_nop 7\n\t"
        "v_mov_b64_e32 v[32:33], 0\n\t"
        "v_mov_b64_e32 v[34:35], 0\n\t"
        "v_mov_b64_e32 v[36:37], 0\n\t"
        "v_mov_b64_e32 v[38:39], 0\n\t"
        "v_mov_b64_e32 v[40:41], 0\n\t"
        "v_mov_b64_e32 v[42:43], 0\n\t"
        "v_mov_b64_e32 v[44:45], 0\n\t"
        "v_mov_b64_e32 v[46:47], 0\n\t"
        "s_branch LB_rore%=w\n\t"
        "LB_rore%=:\n\t"
        "buffer_load_dwordx4 v[64:67], v115, s[8:11], s46 offen offset:0 sc1\n\t"
        "buffer_load_dwordx4 v[68:71], v115, s[8:11], s46 offen offset:1024 sc1\n\t"
        "buffer_load_dwordx4 v[72:75], v115, s[8:11], s48 offen offset:0 sc1\n\t"
        "buffer_load_dwordx4 v[76:79], v115, s[8:11], s48 offen offset:1024 sc1\n\t"
        "buffer_load_dwordx4 v[80:83], v115, s[8:11], s73 offen offset:0 sc1\n\t"
        "buffer_load_dwordx4 v[84:87], v115, s[8:11], s73 offen offset:1024 sc1\n\t"
        "buffer_load_dwordx4 v[88:91], v115, s[8:11], s74 offen offset:0 sc1\n\t"
        "buffer_load_dwordx4 v[92:95], v115, s[8:11], s74 offen offset:1024 sc1\n\t"
        "LB_rore%=w:\n\t"
        "s_waitcnt vmcnt(0) lgkmcnt(0)\n\t"
        "v_max3_u32 v108, v64, v65, v66\n\t"
        "v_max3_u32 v108, v108, v67, v68\n\t"
        "v_max3_u32 v108, v108, v69, v70\n\t"
        "v_max3_u32 v108, v108, v71, v72\n\t"
        "v_max3_u32 v108, v108, v73, v74\n\t"
        "v_max3_u32 v108, v108, v75, v76\n\t"
        "v_max3_u32 v108, v108, v77, v78\n\t"
        "v_max3_u32 v108, v108, v79, v80\n\t"
        "v_max3_u32 v108, v108, v81, v82\n\t"
        "v_max3_u32 v108, v108, v83, v84\n\t"
        "v_max3_u32 v108, v108, v85, v86\n\t"
        "v_max3_u32 v108, v108, v87, v88\n\t"
        "v_max3_u32 v108, v108, v89, v90\n\t"
        "v_max3_u32 v108, v108, v91, v92\n\t"
        "v_max3_u32 v108, v108, v93, v94\n\t"
        "v_max_u32_e32 v108, v108, v95\n\t"
        "v_cmp_eq_u32_e32 vcc, -1, v108\n\t"
        "s_nop 0\n\t"
        "s_cbranch_vccz LB_go2%=\n\t"
        "s_add_u32 s42, s42, 1\n\t"
        "s_cmp_lt_u32 s42, 0x100000\n\t"
        "s_cbranch_scc1 LB_rore%=\n\t"
        "s_movk_i32 s34, 4\n\t"
        "s_branch LB_die%=\n\t"
        "LB_die%=:\n\t"
        "v_mov_b32_e32 v30, s34\n\t"
        "s_mov_b32 s35, 33024\n\t"
        "s_mov_b64 exec, 1\n\t"
        "buffer_store_dword v30, off, s[12:15], s35 sc1\n\t"
        "s_endpgm\n\t"
        ".Lk_end_B:\n\t"
        :: REC_S_IN, "{v96}"(g_v), "{v97}"(g_o), "{v98}"(wb0), "{v99}"(wb1), "{v100}"(rb0), "{v101}"(rb0 ^ 32u), "{v102}"(rb0 ^ 64u), "{v103}"(rb0 ^ 96u),
           "{v106}"(pp ? 0xEEEEEEEEu : 0x44444444u), "{v107}"(bb ? 0xEEEEEEEEu : 0x44444444u), "{v[108:109]}"(m0p), "{s68}"(rot),
           "{v112}"(a_mem), "{v113}"((24 + cp) * 16384 + l * ST_P), "{v114}"(a_vmw), "{v115}"(a_or), "{v116}"(a_mw), "{v117}"(a_sw),
           "{v118}"(lb + w * 2048 + l * 16), "{v119}"(lb + (w & 1) * 1024 + l * 16), "{v120}"(lb + LB_R + w * 512 + ((g >> 1) * 2 + (n >> 3)) * 128 + (g & 1) * 32 + (n & 7) * 4),
           "{v121}"(lb + LB_R + l * 8), "{v122}"(stg + l * 16), "{v123}"(o_r), "{v124}"(rb + w == 32 ? 28 * 16384 + (l & 31) * 128 : OOB)
        : "memory", "vcc", "scc", "v0", "v255", "a0", "a255", "s101");
    __builtin_unreachable();
}
__device__ __forceinline__ void rec_helper(const Params& p, rsrc_t r, unsigned char* lds, const int wv, const int hi) {
    const int tid = otid(), l = tid & 63;
    const rsrc_t rh = __builtin_amdgcn_make_buffer_rsrc(p.ws + hx_base(p.ws), 0, HX_BYTES, 0x00020000);
    const v4u ones = {0xFFFFFFFFu, 0xFFFFFFFFu, 0xFFFFFFFFu, 0xFFFFFFFFu};
    unsigned best0 = ~0u, best1 = ~0u, d[5] = {0u, 0u, 0u, 0u, 0u};
    const bool meas = (hi & 7) < 2;
    if (meas) {
        const unsigned ca = l >= 8 ? OOB : (31 + wv * 32) * 16384 + hi * 256 + l * 16;
        const unsigned tend = rtnow() + CAL_T;
#pragma unroll 1
        for (int i = 0; i < 64 && (i < 3 || (int)(rtnow() - tend) < 0); ++i) {
            const unsigned t0 = (unsigned)__builtin_amdgcn_s_memtime(); __builtin_amdgcn_s_waitcnt(0);
            st16(rh, ca, ones); DRAIN();
            const unsigned t1 = (unsigned)__builtin_amdgcn_s_memtime(); __builtin_amdgcn_s_waitcnt(0);
            st16(rh, ca + 8192, ones); DRAIN();
            const unsigned t2 = (unsigned)__builtin_amdgcn_s_memtime(); __builtin_amdgcn_s_waitcnt(0);
            best0 = min(best0, t1 - t0); best1 = min(best1, t2 - t1); }
        if (l < 2) __hip_atomic_fetch_max((unsigned*)(p.ws + F_CAL + 128) + l, 65535u - min(l ? best1 : best0, 65535u), __ATOMIC_RELAXED, __HIP_MEMORY_SCOPE_AGENT);
    } else if (TOUCH) {
        const int xq = hi >> 3, k = ((hi & 7) - 2) * 4 + wv;
#pragma unroll
        for (int i = 0; i < 5; ++i) { const bf16_t* q;
            if (i & 1) { const int z = (i >> 1) * 24 + k, st = z & 3, c = 4 + (z >> 2);
                q = ((st & 1) ? p.Wk : p.Wq) + (size_t)(((((st & 1) * 2 + 1) * 4 + c) & 15) * 64 + l) * 4096 + (xq * 8 + 1 + (st >> 1) * 4) * 128 + 64; }
            else { const int z = min((i >> 1) * 24 + k, 63);
                q = p.Wo + (size_t)(((z & 3) * 8 + (z >> 3)) * 128 + ((z >> 2) & 1) * 64 + l) * 1024 + (RPL == 4 ? (xq >> 1) : (xq & 1)) * 512 + 192; if (RPL == 4 && (xq & 1)) q = p.gate_w; }
            asm volatile("global_load_dword %0, %1, off" : "+v"(d[i]) : "v"(q) : "memory"); }
    }
#pragma unroll
    for (int i = 0; i < 16; ++i) st16(rh, hi * 65536 + i * 4096 + tid * 16, ones);
    asm volatile("s_waitcnt vmcnt(0)" : "+v"(d[0]), "+v"(d[1]), "+v"(d[2]), "+v"(d[3]), "+v"(d[4]) :: "memory");
    __syncthreads();
    if (tid == 0 && __hip_atomic_fetch_add((unsigned*)(p.ws + F_CAL + 256), 1u, __ATOMIC_RELAXED, __HIP_MEMORY_SCOPE_AGENT) == 31u) {
        const v2u c = ld8(r, F_CAL + 128); st4(r, F_CAL, c.x < c.y ? 8193u : 1u); }
}
using f32x16 = __attribute__((ext_vector_type(16))) float;
constexpr int CV_RP = 80;
constexpr int CV_U = 96 * CV_RP;
constexpr int CV_KP = 4416;
constexpr int CV_K0 = 8 * CV_U;
__device__ __forceinline__ float blo(unsigned x) { return __uint_as_float(x << 16); }
__device__ __forceinline__ float bhi(unsigned x) { return __uint_as_float(x & 0xFFFF0000u); }
__device__ __forceinline__ void gate_block(const Params& p, rsrc_t r, const int hi, const int wv) {
    const int l = otid() & 63, t0 = hi * 64 + wv * 16;
    v4u xg[16][2], gw[2];
#pragma unroll
    for (int q = 0; q < 2; ++q) gw[q] = *(const v4u*)(p.gate_w + q * 512 + l * 8);
#pragma unroll
    for (int i = 0; i < 16; ++i)
#pragma unroll
        for (int q = 0; q < 2; ++q) xg[i][q] = *(const v4u*)(p.Aq + (size_t)(t0 + i) * D + q * 512 + l * 8);
    float mine = 0.f;
#pragma unroll
    for (int i = 0; i < 16; ++i) { float acc = 0.f;
#pragma unroll
        for (int q = 0; q < 2; ++q)
#pragma unroll
            for (int e = 0; e < 4; ++e) { const unsigned pr = pack2(blo(xg[i][q][e]) * blo(gw[q][e]), bhi(xg[i][q][e]) * bhi(gw[q][e])); acc += blo(pr); acc += bhi(pr); }
        for (int o = 32; o > 0; o >>= 1) acc += __shfl_xor(acc, o);
        if (l == i) mine = acc; }
    if (l < 16) { const float z = rbfh(mine); st4(r, W_GATE + (t0 + l) * 4, __float_as_uint(rbfh(1.f / (1.f + expf(-z))))); }
}
__device__ __forceinline__ bool conv_ends(const Params& p, rsrc_t r, const int wv, const int hi, const int j) {
    (void)j;
    const int tid = otid(), Q = tid >> 2, cg = tid & 3, ch0 = hi * 32 + cg * 8;
    v4u u[32];
    {
        v4u sk[4]; float sv[8][4];
#pragma unroll
        for (int q = 0; q < 4; ++q) sk[q] = *(const v4u*)(p.shift_k + ch0 * 4 + q * 8);
        const rsrc_t rk = __builtin_amdgcn_make_buffer_rsrc((void*)p.Ak, 0, S * D * 2, 0x00020000), rv = __builtin_amdgcn_make_buffer_rsrc((void*)p.Av, 0, S * D * 2, 0x00020000);
        const unsigned ro = (unsigned)(Q * 32 * D + ch0) * 2u;
        v4u kr[19], kn[16];
#pragma unroll
        for (int i = 0; i < 19; ++i) kr[i] = __builtin_amdgcn_raw_buffer_load_b128(rk, (i < 3 && Q == 0) ? OOB : ro + (i - 3) * 2048, 0, CV_POL);
#pragma unroll
        for (int i = 0; i < 16; ++i) u[i] = __builtin_amdgcn_raw_buffer_load_b128(rv, ro + i * 2048, 0, CV_POL);
#pragma unroll
        for (int i = 0; i < 16; ++i) kn[i] = __builtin_amdgcn_raw_buffer_load_b128(rk, ro + (16 + i) * 2048, 0, CV_POL);
#pragma unroll
        for (int c8 = 0; c8 < 8; ++c8)
#pragma unroll
            for (int tp = 0; tp < 4; ++tp) { const unsigned sw = sk[c8 >> 1][(c8 & 1) * 2 + (tp >> 1)]; sv[c8][tp] = (tp & 1) ? bhi(sw) : blo(sw); }
        auto row = [&](int i, const v4u& k0, const v4u& k1, const v4u& k2, const v4u& k3) {
#pragma unroll
            for (int q = 0; q < 4; ++q) {
                unsigned ks = pack2(blo(k0[q]) * sv[2 * q][3], bhi(k0[q]) * sv[2 * q + 1][3]), pr;
                pr = pack2(blo(k1[q]) * sv[2 * q][2], bhi(k1[q]) * sv[2 * q + 1][2]); ks = pack2(blo(ks) + blo(pr), bhi(ks) + bhi(pr));
                pr = pack2(blo(k2[q]) * sv[2 * q][1], bhi(k2[q]) * sv[2 * q + 1][1]); ks = pack2(blo(ks) + blo(pr), bhi(ks) + bhi(pr));
                pr = pack2(blo(k3[q]) * sv[2 * q][0], bhi(k3[q]) * sv[2 * q + 1][0]); ks = pack2(blo(ks) + blo(pr), bhi(ks) + bhi(pr));
                u[i][q] = pack2(blo(ks) * blo(u[i][q]), bhi(ks) * bhi(u[i][q])); } };
        __builtin_amdgcn_sched_barrier(0);
#pragma unroll
        for (int i = 0; i < 16; ++i) { row(i, kr[i], kr[i + 1], kr[i + 2], kr[i + 3]); u[16 + i] = __builtin_amdgcn_raw_buffer_load_b128(rv, ro + (16 + i) * 2048, 0, CV_POL); __builtin_amdgcn_sched_barrier(0); }
#pragma unroll
        for (int i = 0; i < 16; ++i) row(16 + i, i < 3 ? kr[16 + i] : kn[i - 3], i < 2 ? kr[17 + i] : kn[i - 2], i < 1 ? kr[18] : kn[i - 1], kn[i]);
    }
#pragma unroll
    for (int rd = 0; rd < 4; ++rd)
#pragma unroll
        for (int G = 0; G < 4; ++G) { v4u lo, hh;
#pragma unroll
            for (int q = 0; q < 4; ++q) { const unsigned x = u[8 * G + 7 - 2 * q][rd], y = u[8 * G + 6 - 2 * q][rd];
                lo[q] = __builtin_amdgcn_perm(y, x, 0x05040100u); hh[q] = __builtin_amdgcn_perm(y, x, 0x07060302u); }
            st16(r, W_U + (hi * 4 + rd) * 32768 + G * 4096 + tid * 16, lo); st16(r, W_U + (hi * 4 + rd) * 32768 + 16384 + G * 4096 + tid * 16, hh); }
    DRAIN(); __syncthreads();
    if (tid == 0) st4(r, F_CVU + hi * 128, 1u);
    BSTAMP(8);
    const v4u dv = *(const v4u*)(p.ssm_D + ch0);
    wait_long(rtnow() + CV_NAP);
    if (!wait_word(r, F_CVY + hi * 128, 4u, 512u)) return false;
    BSTAMP(9);
#pragma unroll
    for (int rd = 0; rd < 4; ++rd) { v4u lo[4], hh[4];
#pragma unroll
        for (int G = 0; G < 4; ++G) { lo[G] = ld16(r, W_Y + (hi * 4 + rd) * 32768 + G * 4096 + tid * 16); hh[G] = ld16(r, W_Y + (hi * 4 + rd) * 32768 + 16384 + G * 4096 + tid * 16); }
        const float d0 = blo(dv[rd]), d1 = bhi(dv[rd]);
#pragma unroll
        for (int G = 0; G < 4; ++G)
#pragma unroll
            for (int q = 0; q < 8; ++q) { const unsigned x = u[8 * G + q][rd], ud = pack2(blo(x) * d0, bhi(x) * d1);
                u[8 * G + q][rd] = pack2(((q & 1) ? bhi(lo[G][q >> 1]) : blo(lo[G][q >> 1])) + blo(ud), ((q & 1) ? bhi(hh[G][q >> 1]) : blo(hh[G][q >> 1])) + bhi(ud)); } }
    BSTAMP(11);
#pragma unroll
    for (int hf = 0; hf < 2; ++hf) { v4u qv[16];
#pragma unroll
        for (int i = 0; i < 16; ++i) qv[i] = *(const v4u*)(p.Aq + (size_t)(Q * 32 + hf * 16 + i) * D + ch0);
#pragma unroll
        for (int i = 0; i < 16; ++i) { v4u o;
#pragma unroll
            for (int q = 0; q < 4; ++q) o[q] = pack2(blo(qv[i][q]) * blo(u[hf * 16 + i][q]), bhi(qv[i][q]) * bhi(u[hf * 16 + i][q]));
            st16(r, W_A + hi * 131072 + (hf * 16 + i) * 4096 + tid * 16, o); } }
    DRAIN(); __syncthreads();
    if (tid == 0) __hip_atomic_fetch_add((unsigned*)(p.ws + F_CONV), 1u, __ATOMIC_RELAXED, __HIP_MEMORY_SCOPE_AGENT);
    return true;
}

__device__ __forceinline__ bool conv_core(const Params& p, rsrc_t r, unsigned char* lds, const int wv, const int un) {
    const int tid = otid(), w = wv, l = tid & 63, Q = tid >> 2, cg = tid & 3;
    const rsrc_t rs = __builtin_amdgcn_make_buffer_rsrc((void*)p.ssm_k, 0, S * D * 2, 0x00020000);
    v4u kraw[4]; v2u kext[4];
    auto kload = [&](int n) { const unsigned o = (unsigned)(((un >> 2) * 32 + w * 8 + (un & 3) * 2 + n) * S + l * 8) * 2u;
#pragma unroll
        for (int i = 0; i < 4; ++i) { kraw[i] = __builtin_amdgcn_raw_buffer_load_b128(rs, o + i * 1024, 0, CV_POL);
            kext[i] = __builtin_amdgcn_raw_buffer_load_b64(rs, (i == 3 && l == 63) ? OOB : o + i * 1024 + 16, 0, 0); } };
    kload(0);
    if (!wait_word(r, F_CVU + (un >> 2) * 128, 1u, 2048u)) return false;
    v4u ur[8];
#pragma unroll
    for (int i = 0; i < 8; ++i) ur[i] = ld16(r, W_U + un * 32768 + i * 4096 + tid * 16);
    for (int i = tid; i < 8 * 160; i += 256) *(v4u*)(lds + (i / 160) * CV_U + (i % 160) * 16) = (v4u){0u, 0u, 0u, 0u};
    unsigned char* kc = lds + CV_K0 + w * 4 * CV_KP;
    if (l < 32) {
#pragma unroll
        for (int q = 0; q < 4; ++q) *(unsigned*)(kc + q * CV_KP + l * 4) = 0u; }
    const int b = l & 31, kg = l >> 5;
    const unsigned kb = (unsigned)(size_t)kc + (b & 3) * CV_KP + ((b & 28) - 8 * kg + 56 - 16) * 2, ub0 = (unsigned)(size_t)lds + (1 + b) * CV_RP + kg * 16;
    unsigned char* const urow = lds + 2 * cg * CV_U + (32 + Q) * CV_RP;
#pragma unroll
    for (int i = 0; i < 8; ++i) *(v4u*)(urow + (i >> 2) * CV_U + (i & 3) * 16) = ur[i];
    __syncthreads();
#pragma unroll 1
        for (int ci = 0; ci < 2; ++ci) {
            const int c8 = 2 * w + ci;
#pragma unroll
            for (int i = 0; i < 4; ++i) { const v4u e = kraw[i]; const v2u f = kext[i]; unsigned char* q = kc + 128 + (i * 64 + l) * 16;
                const unsigned s0 = __builtin_amdgcn_alignbit(e.y, e.x, 16), s1 = __builtin_amdgcn_alignbit(e.z, e.y, 16), s2 = __builtin_amdgcn_alignbit(e.w, e.z, 16),
                               s3 = __builtin_amdgcn_alignbit(f.x, e.w, 16), s4 = __builtin_amdgcn_alignbit(f.y, f.x, 16);
                *(v4u*)q = (v4u){s0, s1, s2, s3}; *(v4u*)(q + CV_KP) = (v4u){e.y, e.z, e.w, f.x}; *(v4u*)(q + 2 * CV_KP) = (v4u){s1, s2, s3, s4}; *(v4u*)(q + 3 * CV_KP) = (v4u){e.z, e.w, f.x, f.y};
                if (i == 0 && l == 0) {
                    *(v2u*)(q - 8) = (v2u){0u, e.x << 16}; *(v2u*)(q + CV_KP - 8) = (v2u){0u, e.x}; *(v2u*)(q + 2 * CV_KP - 8) = (v2u){e.x << 16, s0}; *(v2u*)(q + 3 * CV_KP - 8) = (v2u){e.x, e.y}; } }
            if (ci == 0) kload(1);
            __builtin_amdgcn_wave_barrier();
            f32x16 acc0, acc1;
            asm volatile(
                "ds_read_b64 v[192:193], %[kb] offset:32\n\t"
                "ds_read_b64 v[194:195], %[kb] offset:40\n\t"
                "ds_read_b128 v[196:199], %[ub] offset:5040\n\t"
                "ds_read_b128 v[200:203], %[ub] offset:2480\n\t"
                "ds_read_b64 v[204:205], %[kb] offset:2080\n\t"
                "ds_read_b64 v[206:207], %[kb] offset:2088\n\t"
                "ds_read_b64 v[208:209], %[kb] offset:0\n\t"
                "ds_read_b64 v[210:211], %[kb] offset:8\n\t"
                "ds_read_b128 v[212:215], %[ub] offset:5072\n\t"
                "ds_read_b128 v[216:219], %[ub] offset:2512\n\t"
                "ds_read_b64 v[220:221], %[kb] offset:2048\n\t"
                "ds_read_b64 v[222:223], %[kb] offset:2056\n\t"
                "s_waitcnt lgkmcnt(9)\n\t"
                "v_mfma_f32_32x32x16_bf16 %[a1], v[192:195], v[196:199], 0\n\t"
                "ds_read_b64 v[224:225], %[kb] offset:96\n\t"
                "ds_read_b64 v[226:227], %[kb] offset:104\n\t"
                "ds_read_b128 v[228:231], %[ub] offset:4960\n\t"
                "s_waitcnt lgkmcnt(11)\n\t"
                "v_mfma_f32_32x32x16_bf16 %[a0], v[192:195], v[200:203], 0\n\t"
                "ds_read_b128 v[232:235], %[ub] offset:2400\n\t"
                "s_waitcnt lgkmcnt(10)\n\t"
                "v_mfma_f32_32x32x16_bf16 %[a1], v[204:207], v[200:203], %[a1]\n\t"
                "ds_read_b64 v[236:237], %[kb] offset:2144\n\t"
                "ds_read_b64 v[238:239], %[kb] offset:2152\n\t"
                "s_waitcnt lgkmcnt(9)\n\t"
                "v_mfma_f32_32x32x16_bf16 %[a1], v[208:211], v[212:215], %[a1]\n\t"
                "ds_read_b64 v[240:241], %[kb] offset:64\n\t"
                "ds_read_b64 v[242:243], %[kb] offset:72\n\t"
                "ds_read_b128 v[244:247], %[ub] offset:4992\n\t"
                "s_waitcnt lgkmcnt(11)\n\t"
                "v_mfma_f32_32x32x16_bf16 %[a0], v[208:211], v[216:219], %[a0]\n\t"
                "ds_read_b128 v[248:251], %[ub] offset:2432\n\t"
                "s_waitcnt lgkmcnt(10)\n\t"
                "v_mfma_f32_32x32x16_bf16 %[a1], v[220:223], v[216:219], %[a1]\n\t"
                "ds_read_b64 v[252:253], %[kb] offset:2112\n\t"
                "ds_read_b64 v[254:255], %[kb] offset:2120\n\t"
                "s_waitcnt lgkmcnt(9)\n\t"
                "v_mfma_f32_32x32x16_bf16 %[a1], v[224:227], v[228:231], %[a1]\n\t"
                "ds_read_b64 v[192:193], %[kb] offset:160\n\t"
                "ds_read_b64 v[194:195], %[kb] offset:168\n\t"
                "ds_read_b128 v[196:199], %[ub] offset:4880\n\t"
                "s_waitcnt lgkmcnt(11)\n\t"
                "v_mfma_f32_32x32x16_bf16 %[a0], v[224:227], v[232:235], %[a0]\n\t"
                "ds_read_b128 v[200:203], %[ub] offset:2320\n\t"
                "s_waitcnt lgkmcnt(10)\n\t"
                "v_mfma_f32_32x32x16_bf16 %[a1], v[236:239], v[232:235], %[a1]\n\t"
                "ds_read_b64 v[204:205], %[kb] offset:2208\n\t"
                "ds_read_b64 v[206:207], %[kb] offset:2216\n\t"
                "s_waitcnt lgkmcnt(9)\n\t"
                "v_mfma_f32_32x32x16_bf16 %[a1], v[240:243], v[244:247], %[a1]\n\t"
                "ds_read_b64 v[208:209], %[kb] offset:128\n\t"
                "ds_read_b64 v[210:211], %[kb] offset:136\n\t"
                "ds_read_b128 v[212:215], %[ub] offset:4912\n\t"
                "s_waitcnt lgkmcnt(11)\n\t"
                "v_mfma_f32_32x32x16_bf16 %[a0], v[240:243], v[248:251], %[a0]\n\t"
                "ds_read_b128 v[216:219], %[ub] offset:2352\n\t"
                "s_waitcnt lgkmcnt(10)\n\t"
                "v_mfma_f32_32x32x16_bf16 %[a1], v[252:255], v[248:251], %[a1]\n\t"
                "ds_read_b64 v[220:221], %[kb] offset:2176\n\t"
                "ds_read_b64 v[222:223], %[kb] offset:2184\n\t"
                "s_waitcnt lgkmcnt(9)\n\t"
                "v_mfma_f32_32x32x16_bf16 %[a1], v[192:195], v[196:199], %[a1]\n\t"
                "ds_read_b64 v[224:225], %[kb] offset:224\n\t"
                "ds_read_b64 v[226:227], %[kb] offset:232\n\t"
                "ds_read_b128 v[228:231], %[ub] offset:4800\n\t"
                "s_waitcnt lgkmcnt(11)\n\t"
                "v_mfma_f32_32x32x16_bf16 %[a0], v[192:195], v[200:203], %[a0]\n\t"
                "ds_read_b128 v[232:235], %[ub] offset:2240\n\t"
                "s_waitcnt lgkmcnt(10)\n\t"
                "v_mfma_f32_32x32x16_bf16 %[a1], v[204:207], v[200:203], %[a1]\n\t"
                "ds_read_b64 v[236:237], %[kb] offset:2272\n\t"
                "ds_read_b64 v[238:239], %[kb] offset:2280\n\t"
                "s_waitcnt lgkmcnt(9)\n\t"
                "v_mfma_f32_32x32x16_bf16 %[a1], v[208:211], v[212:215], %[a1]\n\t"
                "ds_read_b64 v[240:241], %[kb] offset:192\n\t"
                "ds_read_b64 v[242:243], %[kb] offset:200\n\t"
                "ds_read_b128 v[244:247], %[ub] offset:4832\n\t"
                "s_waitcnt lgkmcnt(11)\n\t"
                "v_mfma_f32_32x32x16_bf16 %[a0], v[208:211], v[216:219], %[a0]\n\t"
                "ds_read_b128 v[248:251], %[ub] offset:2272\n\t"
                "s_waitcnt lgkmcnt(10)\n\t"
                "v_mfma_f32_32x32x16_bf16 %[a1], v[220:223], v[216:219], %[a1]\n\t"
                "ds_read_b64 v[252:253], %[kb] offset:2240\n\t"
                "ds_read_b64 v[254:255], %[kb] offset:2248\n\t"
                "s_waitcnt lgkmcnt(9)\n\t"
                "v_mfma_f32_32x32x16_bf16 %[a1], v[224:227], v[228:231], %[a1]\n\t"
                "ds_read_b64 v[192:193], %[kb] offset:288\n\t"
                "ds_read_b64 v[194:195], %[kb] offset:296\n\t"
                "ds_read_b128 v[196:199], %[ub] offset:4720\n\t"
                "s_waitcnt lgkmcnt(11)\n\t"
                "v_mfma_f32_32x32x16_bf16 %[a0], v[224:227], v[232:235], %[a0]\n\t"
                "ds_read_b128 v[200:203], %[ub] offset:2160\n\t"
                "s_waitcnt lgkmcnt(10)\n\t"
                "v_mfma_f32_32x32x16_bf16 %[a1], v[236:239], v[232:235], %[a1]\n\t"
                "ds_read_b64 v[204:205], %[kb] offset:2336\n\t"
                "ds_read_b64 v[206:207], %[kb] offset:2344\n\t"
                "s_waitcnt lgkmcnt(9)\n\t"
                "v_mfma_f32_32x32x16_bf16 %[a1], v[240:243], v[244:247], %[a1]\n\t"
                "ds_read_b64 v[208:209], %[kb] offset:256\n\t"
                "ds_read_b64 v[210:211], %[kb] offset:264\n\t"
                "ds_read_b128 v[212:215], %[ub] offset:4752\n\t"
                "s_waitcnt lgkmcnt(11)\n\t"
                "v_mfma_f32_32x32x16_bf16 %[a0], v[240:243], v[248:251], %[a0]\n\t"
                "ds_read_b128 v[216:219], %[ub] offset:2192\n\t"
                "s_waitcnt lgkmcnt(10)\n\t"
                "v_mfma_f32_32x32x16_bf16 %[a1], v[252:255], v[248:251], %[a1]\n\t"
                "ds_read_b64 v[220:221], %[kb] offset:2304\n\t"
                "ds_read_b64 v[222:223], %[kb] offset:2312\n\t"
                "s_waitcnt lgkmcnt(9)\n\t"
                "v_mfma_f32_32x32x16_bf16 %[a1], v[192:195], v[196:199], %[a1]\n\t"
                "ds_read_b64 v[224:225], %[kb] offset:352\n\t"
                "ds_read_b64 v[226:227], %[kb] offset:360\n\t"
                "ds_read_b128 v[228:231], %[ub] offset:4640\n\t"
                "s_waitcnt lgkmcnt(11)\n\t"
                "v_mfma_f32_32x32x16_bf16 %[a0], v[192:195], v[200:203], %[a0]\n\t"
                "ds_read_b128 v[232:235], %[ub] offset:2080\n\t"
                "s_waitcnt lgkmcnt(10)\n\t"
                "v_mfma_f32_32x32x16_bf16 %[a1], v[204:207], v[200:203], %[a1]\n\t"
                "ds_read_b64 v[236:237], %[kb] offset:2400\n\t"
                "ds_read_b64 v[238:239], %[kb] offset:2408\n\t"
                "s_waitcnt lgkmcnt(9)\n\t"
                "v_mfma_f32_32x32x16_bf16 %[a1], v[208:211], v[212:215], %[a1]\n\t"
                "ds_read_b64 v[240:241], %[kb] offset:320\n\t"
                "ds_read_b64 v[242:243], %[kb] offset:328\n\t"
                "ds_read_b128 v[244:247], %[ub] offset:4672\n\t"
                "s_waitcnt lgkmcnt(11)\n\t"
                "v_mfma_f32_32x32x16_bf16 %[a0], v[208:211], v[216:219], %[a0]\n\t"
                "ds_read_b128 v[248:251], %[ub] offset:2112\n\t"
                "s_waitcnt lgkmcnt(10)\n\t"
                "v_mfma_f32_32x32x16_bf16 %[a1], v[220:223], v[216:219], %[a1]\n\t"
                "ds_read_b64 v[252:253], %[kb] offset:2368\n\t"
                "ds_read_b64 v[254:255], %[kb] offset:2376\n\t"
                "s_waitcnt lgkmcnt(9)\n\t"
                "v_mfma_f32_32x32x16_bf16 %[a1], v[224:227], v[228:231], %[a1]\n\t"
                "ds_read_b64 v[192:193], %[kb] offset:416\n\t"
                "ds_read_b64 v[194:195], %[kb] offset:424\n\t"
                "ds_read_b128 v[196:199], %[ub] offset:4560\n\t"
                "s_waitcnt lgkmcnt(11)\n\t"
                "v_mfma_f32_32x32x16_bf16 %[a0], v[224:227], v[232:235], %[a0]\n\t"
                "ds_read_b128 v[200:203], %[ub] offset:2000\n\t"
                "s_waitcnt lgkmcnt(10)\n\t"
                "v_mfma_f32_32x32x16_bf16 %[a1], v[236:239], v[232:235], %[a1]\n\t"
                "ds_read_b64 v[204:205], %[kb] offset:2464\n\t"
                "ds_read_b64 v[206:207], %[kb] offset:2472\n\t"
                "s_waitcnt lgkmcnt(9)\n\t"
                "v_mfma_f32_32x32x16_bf16 %[a1], v[240:243], v[244:247], %[a1]\n\t"
                "ds_read_b64 v[208:209], %[kb] offset:384\n\t"
                "ds_read_b64 v[210:211], %[kb] offset:392\n\t"
                "ds_read_b128 v[212:215], %[ub] offset:4592\n\t"
                "s_waitcnt lgkmcnt(11)\n\t"
                "v_mfma_f32_32x32x16_bf16 %[a0], v[240:243], v[248:251], %[a0]\n\t"
                "ds_read_b128 v[216:219], %[ub] offset:2032\n\t"
                "s_waitcnt lgkmcnt(10)\n\t"
                "v_mfma_f32_32x32x16_bf16 %[a1], v[252:255], v[248:251], %[a1]\n\t"
                "ds_read_b64 v[220:221], %[kb] offset:2432\n\t"
                "ds_read_b64 v[222:223], %[kb] offset:2440\n\t"
                "s_waitcnt lgkmcnt(9)\n\t"
                "v_mfma_f32_32x32x16_bf16 %[a1], v[192:195], v[196:199], %[a1]\n\t"
                "ds_read_b64 v[224:225], %[kb] offset:480\n\t"
                "ds_read_b64 v[226:227], %[kb] offset:488\n\t"
                "ds_read_b128 v[228:231], %[ub] offset:4480\n\t"
                "s_waitcnt lgkmcnt(11)\n\t"
                "v_mfma_f32_32x32x16_bf16 %[a0], v[192:195], v[200:203], %[a0]\n\t"
                "ds_read_b128 v[232:235], %[ub] offset:1920\n\t"
                "s_waitcnt lgkmcnt(10)\n\t"
                "v_mfma_f32_32x32x16_bf16 %[a1], v[204:207], v[200:203], %[a1]\n\t"
                "ds_read_b64 v[236:237], %[kb] offset:2528\n\t"
                "ds_read_b64 v[238:239], %[kb] offset:2536\n\t"
                "s_waitcnt lgkmcnt(9)\n\t"
                "v_mfma_f32_32x32x16_bf16 %[a1], v[208:211], v[212:215], %[a1]\n\t"
                "ds_read_b64 v[240:241], %[kb] offset:448\n\t"
                "ds_read_b64 v[242:243], %[kb] offset:456\n\t"
                "ds_read_b128 v[244:247], %[ub] offset:4512\n\t"
                "s_waitcnt lgkmcnt(11)\n\t"
                "v_mfma_f32_32x32x16_bf16 %[a0], v[208:211], v[216:219], %[a0]\n\t"
                "ds_read_b128 v[248:251], %[ub] offset:1952\n\t"
                "s_waitcnt lgkmcnt(10)\n\t"
                "v_mfma_f32_32x32x16_bf16 %[a1], v[220:223], v[216:219], %[a1]\n\t"
                "ds_read_b64 v[252:253], %[kb] offset:2496\n\t"
                "ds_read_b64 v[254:255], %[kb] offset:2504\n\t"
                "s_waitcnt lgkmcnt(9)\n\t"
                "v_mfma_f32_32x32x16_bf16 %[a1], v[224:227], v[228:231], %[a1]\n\t"
                "ds_read_b64 v[192:193], %[kb] offset:544\n\t"
                "ds_read_b64 v[194:195], %[kb] offset:552\n\t"
                "ds_read_b128 v[196:199], %[ub] offset:4400\n\t"
                "s_waitcnt lgkmcnt(11)\n\t"
                "v_mfma_f32_32x32x16_bf16 %[a0], v[224:227], v[232:235], %[a0]\n\t"
                "ds_read_b128 v[200:203], %[ub] offset:1840\n\t"
                "s_waitcnt lgkmcnt(10)\n\t"
                "v_mfma_f32_32x32x16_bf16 %[a1], v[236:239], v[232:235], %[a1]\n\t"
                "ds_read_b64 v[204:205], %[kb] offset:2592\n\t"
                "ds_read_b64 v[206:207], %[kb] offset:2600\n\t"
                "s_waitcnt lgkmcnt(9)\n\t"
                "v_mfma_f32_32x32x16_bf16 %[a1], v[240:243], v[244:247], %[a1]\n\t"
                "ds_read_b64 v[208:209], %[kb] offset:512\n\t"
                "ds_read_b64 v[210:211], %[kb] offset:520\n\t"
                "ds_read_b128 v[212:215], %[ub] offset:4432\n\t"
                "s_waitcnt lgkmcnt(11)\n\t"
                "v_mfma_f32_32x32x16_bf16 %[a0], v[240:243], v[248:251], %[a0]\n\t"
                "ds_read_b128 v[216:219], %[ub] offset:1872\n\t"
                "s_waitcnt lgkmcnt(10)\n\t"
                "v_mfma_f32_32x32x16_bf16 %[a1], v[252:255], v[248:251], %[a1]\n\t"
                "ds_read_b64 v[220:221], %[kb] offset:2560\n\t"
                "ds_read_b64 v[222:223], %[kb] offset:2568\n\t"
                "s_waitcnt lgkmcnt(9)\n\t"
                "v_mfma_f32_32x32x16_bf16 %[a1], v[192:195], v[196:199], %[a1]\n\t"
                "ds_read_b64 v[224:225], %[kb] offset:608\n\t"
                "ds_read_b64 v[226:227], %[kb] offset:616\n\t"
                "ds_read_b128 v[228:231], %[ub] offset:4320\n\t"
                "s_waitcnt lgkmcnt(11)\n\t"
                "v_mfma_f32_32x32x16_bf16 %[a0], v[192:195], v[200:203], %[a0]\n\t"
                "ds_read_b128 v[232:235], %[ub] offset:1760\n\t"
                "s_waitcnt lgkmcnt(10)\n\t"
                "v_mfma_f32_32x32x16_bf16 %[a1], v[204:207], v[200:203], %[a1]\n\t"
                "ds_read_b64 v[236:237], %[kb] offset:2656\n\t"
                "ds_read_b64 v[238:239], %[kb] offset:2664\n\t"
                "s_waitcnt lgkmcnt(9)\n\t"
                "v_mfma_f32_32x32x16_bf16 %[a1], v[208:211], v[212:215], %[a1]\n\t"
                "ds_read_b64 v[240:241], %[kb] offset:576\n\t"
                "ds_read_b64 v[242:243], %[kb] offset:584\n\t"
                "ds_read_b128 v[244:247], %[ub] offset:4352\n\t"
                "s_waitcnt lgkmcnt(11)\n\t"
                "v_mfma_f32_32x32x16_bf16 %[a0], v[208:211], v[216:219], %[a0]\n\t"
                "ds_read_b128 v[248:251], %[ub] offset:1792\n\t"
                "s_waitcnt lgkmcnt(10)\n\t"
                "v_mfma_f32_32x32x16_bf16 %[a1], v[220:223], v[216:219], %[a1]\n\t"
                "ds_read_b64 v[252:253], %[kb] offset:2624\n\t"
                "ds_read_b64 v[254:255], %[kb] offset:2632\n\t"
                "s_waitcnt lgkmcnt(9)\n\t"
                "v_mfma_f32_32x32x16_bf16 %[a1], v[224:227], v[228:231], %[a1]\n\t"
                "ds_read_b64 v[192:193], %[kb] offset:672\n\t"
                "ds_read_b64 v[194:195], %[kb] offset:680\n\t"
                "ds_read_b128 v[196:199], %[ub] offset:4240\n\t"
                "s_waitcnt lgkmcnt(11)\n\t"
                "v_mfma_f32_32x32x16_bf16 %[a0], v[224:227], v[232:235], %[a0]\n\t"
                "ds_read_b128 v[200:203], %[ub] offset:1680\n\t"
                "s_waitcnt lgkmcnt(10)\n\t"
                "v_mfma_f32_32x32x16_bf16 %[a1], v[236:239], v[232:235], %[a1]\n\t"
                "ds_read_b64 v[204:205], %[kb] offset:2720\n\t"
                "ds_read_b64 v[206:207], %[kb] offset:2728\n\t"
                "s_waitcnt lgkmcnt(9)\n\t"
                "v_mfma_f32_32x32x16_bf16 %[a1], v[240:243], v[244:247], %[a1]\n\t"
                "ds_read_b64 v[208:209], %[kb] offset:640\n\t"
                "ds_read_b64 v[210:211], %[kb] offset:648\n\t"
                "ds_read_b128 v[212:215], %[ub] offset:4272\n\t"
                "s_waitcnt lgkmcnt(11)\n\t"
                "v_mfma_f32_32x32x16_bf16 %[a0], v[240:243], v[248:251], %[a0]\n\t"
                "ds_read_b128 v[216:219], %[ub] offset:1712\n\t"
                "s_waitcnt lgkmcnt(10)\n\t"
                "v_mfma_f32_32x32x16_bf16 %[a1], v[252:255], v[248:251], %[a1]\n\t"
                "ds_read_b64 v[220:221], %[kb] offset:2688\n\t"
                "ds_read_b64 v[222:223], %[kb] offset:2696\n\t"
                "s_waitcnt lgkmcnt(9)\n\t"
                "v_mfma_f32_32x32x16_bf16 %[a1], v[192:195], v[196:199], %[a1]\n\t"
                "ds_read_b64 v[224:225], %[kb] offset:736\n\t"
                "ds_read_b64 v[226:227], %[kb] offset:744\n\t"
                "ds_read_b128 v[228:231], %[ub] offset:4160\n\t"
                "s_waitcnt lgkmcnt(11)\n\t"
                "v_mfma_f32_32x32x16_bf16 %[a0], v[192:195], v[200:203], %[a0]\n\t"
                "ds_read_b128 v[232:235], %[ub] offset:1600\n\t"
                "s_waitcnt lgkmcnt(10)\n\t"
                "v_mfma_f32_32x32x16_bf16 %[a1], v[204:207], v[200:203], %[a1]\n\t"
                "ds_read_b64 v[236:237], %[kb] offset:2784\n\t"
                "ds_read_b64 v[238:239], %[kb] offset:2792\n\t"
                "s_waitcnt lgkmcnt(9)\n\t"
                "v_mfma_f32_32x32x16_bf16 %[a1], v[208:211], v[212:215], %[a1]\n\t"
                "ds_read_b64 v[240:241], %[kb] offset:704\n\t"
                "ds_read_b64 v[242:243], %[kb] offset:712\n\t"
                "ds_read_b128 v[244:247], %[ub] offset:4192\n\t"
                "s_waitcnt lgkmcnt(11)\n\t"
                "v_mfma_f32_32x32x16_bf16 %[a0], v[208:211], v[216:219], %[a0]\n\t"
                "ds_read_b128 v[248:251], %[ub] offset:1632\n\t"
                "s_waitcnt lgkmcnt(10)\n\t"
                "v_mfma_f32_32x32x16_bf16 %[a1], v[220:223], v[216:219], %[a1]\n\t"
                "ds_read_b64 v[252:253], %[kb] offset:2752\n\t"
                "ds_read_b64 v[254:255], %[kb] offset:2760\n\t"
                "s_waitcnt lgkmcnt(9)\n\t"
                "v_mfma_f32_32x32x16_bf16 %[a1], v[224:227], v[228:231], %[a1]\n\t"
                "ds_read_b64 v[192:193], %[kb] offset:800\n\t"
                "ds_read_b64 v[194:195], %[kb] offset:808\n\t"
                "ds_read_b128 v[196:199], %[ub] offset:4080\n\t"
                "s_waitcnt lgkmcnt(11)\n\t"
                "v_mfma_f32_32x32x16_bf16 %[a0], v[224:227], v[232:235], %[a0]\n\t"
                "ds_read_b128 v[200:203], %[ub] offset:1520\n\t"
                "s_waitcnt lgkmcnt(10)\n\t"
                "v_mfma_f32_32x32x16_bf16 %[a1], v[236:239], v[232:235], %[a1]\n\t"
                "ds_read_b64 v[204:205], %[kb] offset:2848\n\t"
                "ds_read_b64 v[206:207], %[kb] offset:2856\n\t"
                "s_waitcnt lgkmcnt(9)\n\t"
                "v_mfma_f32_32x32x16_bf16 %[a1], v[240:243], v[244:247], %[a1]\n\t"
                "ds_read_b64 v[208:209], %[kb] offset:768\n\t"
                "ds_read_b64 v[210:211], %[kb] offset:776\n\t"
                "ds_read_b128 v[212:215], %[ub] offset:4112\n\t"
                "s_waitcnt lgkmcnt(11)\n\t"
                "v_mfma_f32_32x32x16_bf16 %[a0], v[240:243], v[248:251], %[a0]\n\t"
                "ds_read_b128 v[216:219], %[ub] offset:1552\n\t"
                "s_waitcnt lgkmcnt(10)\n\t"
                "v_mfma_f32_32x32x16_bf16 %[a1], v[252:255], v[248:251], %[a1]\n\t"
                "ds_read_b64 v[220:221], %[kb] offset:2816\n\t"
                "ds_read_b64 v[222:223], %[kb] offset:2824\n\t"
                "s_waitcnt lgkmcnt(9)\n\t"
                "v_mfma_f32_32x32x16_bf16 %[a1], v[192:195], v[196:199], %[a1]\n\t"
                "ds_read_b64 v[224:225], %[kb] offset:864\n\t"
                "ds_read_b64 v[226:227], %[kb] offset:872\n\t"
                "ds_read_b128 v[228:231], %[ub] offset:4000\n\t"
                "s_waitcnt lgkmcnt(11)\n\t"
                "v_mfma_f32_32x32x16_bf16 %[a0], v[192:195], v[200:203], %[a0]\n\t"
                "ds_read_b128 v[232:235], %[ub] offset:1440\n\t"
                "s_waitcnt lgkmcnt(10)\n\t"
                "v_mfma_f32_32x32x16_bf16 %[a1], v[204:207], v[200:203], %[a1]\n\t"
                "ds_read_b64 v[236:237], %[kb] offset:2912\n\t"
                "ds_read_b64 v[238:239], %[kb] offset:2920\n\t"
                "s_waitcnt lgkmcnt(9)\n\t"
                "v_mfma_f32_32x32x16_bf16 %[a1], v[208:211], v[212:215], %[a1]\n\t"
                "ds_read_b64 v[240:241], %[kb] offset:832\n\t"
                "ds_read_b64 v[242:243], %[kb] offset:840\n\t"
                "ds_read_b128 v[244:247], %[ub] offset:4032\n\t"
                "s_waitcnt lgkmcnt(11)\n\t"
                "v_mfma_f32_32x32x16_bf16 %[a0], v[208:211], v[216:219], %[a0]\n\t"
                "ds_read_b128 v[248:251], %[ub] offset:1472\n\t"
                "s_waitcnt lgkmcnt(10)\n\t"
                "v_mfma_f32_32x32x16_bf16 %[a1], v[220:223], v[216:219], %[a1]\n\t"
                "ds_read_b64 v[252:253], %[kb] offset:2880\n\t"
                "ds_read_b64 v[254:255], %[kb] offset:2888\n\t"
                "s_waitcnt lgkmcnt(9)\n\t"
                "v_mfma_f32_32x32x16_bf16 %[a1], v[224:227], v[228:231], %[a1]\n\t"
                "ds_read_b64 v[192:193], %[kb] offset:928\n\t"
                "ds_read_b64 v[194:195], %[kb] offset:936\n\t"
                "ds_read_b128 v[196:199], %[ub] offset:3920\n\t"
                "s_waitcnt lgkmcnt(11)\n\t"
                "v_mfma_f32_32x32x16_bf16 %[a0], v[224:227], v[232:235], %[a0]\n\t"
                "ds_read_b128 v[200:203], %[ub] offset:1360\n\t"
                "s_waitcnt lgkmcnt(10)\n\t"
                "v_mfma_f32_32x32x16_bf16 %[a1], v[236:239], v[232:235], %[a1]\n\t"
                "ds_read_b64 v[204:205], %[kb] offset:2976\n\t"
                "ds_read_b64 v[206:207], %[kb] offset:2984\n\t"
                "s_waitcnt lgkmcnt(9)\n\t"
                "v_mfma_f32_32x32x16_bf16 %[a1], v[240:243], v[244:247], %[a1]\n\t"
                "ds_read_b64 v[208:209], %[kb] offset:896\n\t"
                "ds_read_b64 v[210:211], %[kb] offset:904\n\t"
                "ds_read_b128 v[212:215], %[ub] offset:3952\n\t"
                "s_waitcnt lgkmcnt(11)\n\t"
                "v_mfma_f32_32x32x16_bf16 %[a0], v[240:243], v[248:251], %[a0]\n\t"
                "ds_read_b128 v[216:219], %[ub] offset:1392\n\t"
                "s_waitcnt lgkmcnt(10)\n\t"
                "v_mfma_f32_32x32x16_bf16 %[a1], v[252:255], v[248:251], %[a1]\n\t"
                "ds_read_b64 v[220:221], %[kb] offset:2944\n\t"
                "ds_read_b64 v[222:223], %[kb] offset:2952\n\t"
                "s_waitcnt lgkmcnt(9)\n\t"
                "v_mfma_f32_32x32x16_bf16 %[a1], v[192:195], v[196:199], %[a1]\n\t"
                "ds_read_b64 v[224:225], %[kb] offset:992\n\t"
                "ds_read_b64 v[226:227], %[kb] offset:1000\n\t"
                "ds_read_b128 v[228:231], %[ub] offset:3840\n\t"
                "s_waitcnt lgkmcnt(11)\n\t"
                "v_mfma_f32_32x32x16_bf16 %[a0], v[192:195], v[200:203], %[a0]\n\t"
                "ds_read_b128 v[232:235], %[ub] offset:1280\n\t"
                "s_waitcnt lgkmcnt(10)\n\t"
                "v_mfma_f32_32x32x16_bf16 %[a1], v[204:207], v[200:203], %[a1]\n\t"
                "ds_read_b64 v[236:237], %[kb] offset:3040\n\t"
                "ds_read_b64 v[238:239], %[kb] offset:3048\n\t"
                "s_waitcnt lgkmcnt(9)\n\t"
                "v_mfma_f32_32x32x16_bf16 %[a1], v[208:211], v[212:215], %[a1]\n\t"
                "ds_read_b64 v[240:241], %[kb] offset:960\n\t"
                "ds_read_b64 v[242:243], %[kb] offset:968\n\t"
                "ds_read_b128 v[244:247], %[ub] offset:3872\n\t"
                "s_waitcnt lgkmcnt(11)\n\t"
                "v_mfma_f32_32x32x16_bf16 %[a0], v[208:211], v[216:219], %[a0]\n\t"
                "ds_read_b128 v[248:251], %[ub] offset:1312\n\t"
                "s_waitcnt lgkmcnt(10)\n\t"
                "v_mfma_f32_32x32x16_bf16 %[a1], v[220:223], v[216:219], %[a1]\n\t"
                "ds_read_b64 v[252:253], %[kb] offset:3008\n\t"
                "ds_read_b64 v[254:255], %[kb] offset:3016\n\t"
                "s_waitcnt lgkmcnt(9)\n\t"
                "v_mfma_f32_32x32x16_bf16 %[a1], v[224:227], v[228:231], %[a1]\n\t"
                "ds_read_b64 v[192:193], %[kb] offset:1056\n\t"
                "ds_read_b64 v[194:195], %[kb] offset:1064\n\t"
                "ds_read_b128 v[196:199], %[ub] offset:3760\n\t"
                "s_waitcnt lgkmcnt(11)\n\t"
                "v_mfma_f32_32x32x16_bf16 %[a0], v[224:227], v[232:235], %[a0]\n\t"
                "ds_read_b128 v[200:203], %[ub] offset:1200\n\t"
                "s_waitcnt lgkmcnt(10)\n\t"
                "v_mfma_f32_32x32x16_bf16 %[a1], v[236:239], v[232:235], %[a1]\n\t"
                "ds_read_b64 v[204:205], %[kb] offset:3104\n\t"
                "ds_read_b64 v[206:207], %[kb] offset:3112\n\t"
                "s_waitcnt lgkmcnt(9)\n\t"
                "v_mfma_f32_32x32x16_bf16 %[a1], v[240:243], v[244:247], %[a1]\n\t"
                "ds_read_b64 v[208:209], %[kb] offset:1024\n\t"
                "ds_read_b64 v[210:211], %[kb] offset:1032\n\t"
                "ds_read_b128 v[212:215], %[ub] offset:3792\n\t"
                "s_waitcnt lgkmcnt(11)\n\t"
                "v_mfma_f32_32x32x16_bf16 %[a0], v[240:243], v[248:251], %[a0]\n\t"
                "ds_read_b128 v[216:219], %[ub] offset:1232\n\t"
                "s_waitcnt lgkmcnt(10)\n\t"
                "v_mfma_f32_32x32x16_bf16 %[a1], v[252:255], v[248:251], %[a1]\n\t"
                "ds_read_b64 v[220:221], %[kb] offset:3072\n\t"
                "ds_read_b64 v[222:223], %[kb] offset:3080\n\t"
                "s_waitcnt lgkmcnt(9)\n\t"
                "v_mfma_f32_32x32x16_bf16 %[a1], v[192:195], v[196:199], %[a1]\n\t"
                "ds_read_b64 v[224:225], %[kb] offset:1120\n\t"
                "ds_read_b64 v[226:227], %[kb] offset:1128\n\t"
                "ds_read_b128 v[228:231], %[ub] offset:3680\n\t"
                "s_waitcnt lgkmcnt(11)\n\t"
                "v_mfma_f32_32x32x16_bf16 %[a0], v[192:195], v[200:203], %[a0]\n\t"
                "ds_read_b128 v[232:235], %[ub] offset:1120\n\t"
                "s_waitcnt lgkmcnt(10)\n\t"
                "v_mfma_f32_32x32x16_bf16 %[a1], v[204:207], v[200:203], %[a1]\n\t"
                "ds_read_b64 v[236:237], %[kb] offset:3168\n\t"
                "ds_read_b64 v[238:239], %[kb] offset:3176\n\t"
                "s_waitcnt lgkmcnt(9)\n\t"
                "v_mfma_f32_32x32x16_bf16 %[a1], v[208:211], v[212:215], %[a1]\n\t"
                "ds_read_b64 v[240:241], %[kb] offset:1088\n\t"
                "ds_read_b64 v[242:243], %[kb] offset:1096\n\t"
                "ds_read_b128 v[244:247], %[ub] offset:3712\n\t"
                "s_waitcnt lgkmcnt(11)\n\t"
                "v_mfma_f32_32x32x16_bf16 %[a0], v[208:211], v[216:219], %[a0]\n\t"
                "ds_read_b128 v[248:251], %[ub] offset:1152\n\t"
                "s_waitcnt lgkmcnt(10)\n\t"
                "v_mfma_f32_32x32x16_bf16 %[a1], v[220:223], v[216:219], %[a1]\n\t"
                "ds_read_b64 v[252:253], %[kb] offset:3136\n\t"
                "ds_read_b64 v[254:255], %[kb] offset:3144\n\t"
                "s_waitcnt lgkmcnt(9)\n\t"
                "v_mfma_f32_32x32x16_bf16 %[a1], v[224:227], v[228:231], %[a1]\n\t"
                "ds_read_b64 v[192:193], %[kb] offset:1184\n\t"
                "ds_read_b64 v[194:195], %[kb] offset:1192\n\t"
                "ds_read_b128 v[196:199], %[ub] offset:3600\n\t"
                "s_waitcnt lgkmcnt(11)\n\t"
                "v_mfma_f32_32x32x16_bf16 %[a0], v[224:227], v[232:235], %[a0]\n\t"
                "ds_read_b128 v[200:203], %[ub] offset:1040\n\t"
                "s_waitcnt lgkmcnt(10)\n\t"
                "v_mfma_f32_32x32x16_bf16 %[a1], v[236:239], v[232:235], %[a1]\n\t"
                "ds_read_b64 v[204:205], %[kb] offset:3232\n\t"
                "ds_read_b64 v[206:207], %[kb] offset:3240\n\t"
                "s_waitcnt lgkmcnt(9)\n\t"
                "v_mfma_f32_32x32x16_bf16 %[a1], v[240:243], v[244:247], %[a1]\n\t"
                "ds_read_b64 v[208:209], %[kb] offset:1152\n\t"
                "ds_read_b64 v[210:211], %[kb] offset:1160\n\t"
                "ds_read_b128 v[212:215], %[ub] offset:3632\n\t"
                "s_waitcnt lgkmcnt(11)\n\t"
                "v_mfma_f32_32x32x16_bf16 %[a0], v[240:243], v[248:251], %[a0]\n\t"
                "ds_read_b128 v[216:219], %[ub] offset:1072\n\t"
                "s_waitcnt lgkmcnt(10)\n\t"
                "v_mfma_f32_32x32x16_bf16 %[a1], v[252:255], v[248:251], %[a1]\n\t"
                "ds_read_b64 v[220:221], %[kb] offset:3200\n\t"
                "ds_read_b64 v[222:223], %[kb] offset:3208\n\t"
                "s_waitcnt lgkmcnt(9)\n\t"
                "v_mfma_f32_32x32x16_bf16 %[a1], v[192:195], v[196:199], %[a1]\n\t"
                "ds_read_b64 v[224:225], %[kb] offset:1248\n\t"
                "ds_read_b64 v[226:227], %[kb] offset:1256\n\t"
                "ds_read_b128 v[228:231], %[ub] offset:3520\n\t"
                "s_waitcnt lgkmcnt(11)\n\t"
                "v_mfma_f32_32x32x16_bf16 %[a0], v[192:195], v[200:203], %[a0]\n\t"
                "ds_read_b128 v[232:235], %[ub] offset:960\n\t"
                "s_waitcnt lgkmcnt(10)\n\t"
                "v_mfma_f32_32x32x16_bf16 %[a1], v[204:207], v[200:203], %[a1]\n\t"
                "ds_read_b64 v[236:237], %[kb] offset:3296\n\t"
                "ds_read_b64 v[238:239], %[kb] offset:3304\n\t"
                "s_waitcnt lgkmcnt(9)\n\t"
                "v_mfma_f32_32x32x16_bf16 %[a1], v[208:211], v[212:215], %[a1]\n\t"
                "ds_read_b64 v[240:241], %[kb] offset:1216\n\t"
                "ds_read_b64 v[242:243], %[kb] offset:1224\n\t"
                "ds_read_b128 v[244:247], %[ub] offset:3552\n\t"
                "s_waitcnt lgkmcnt(11)\n\t"
                "v_mfma_f32_32x32x16_bf16 %[a0], v[208:211], v[216:219], %[a0]\n\t"
                "ds_read_b128 v[248:251], %[ub] offset:992\n\t"
                "s_waitcnt lgkmcnt(10)\n\t"
                "v_mfma_f32_32x32x16_bf16 %[a1], v[220:223], v[216:219], %[a1]\n\t"
                "ds_read_b64 v[252:253], %[kb] offset:3264\n\t"
                "ds_read_b64 v[254:255], %[kb] offset:3272\n\t"
                "s_waitcnt lgkmcnt(9)\n\t"
                "v_mfma_f32_32x32x16_bf16 %[a1], v[224:227], v[228:231], %[a1]\n\t"
                "ds_read_b64 v[192:193], %[kb] offset:1312\n\t"
                "ds_read_b64 v[194:195], %[kb] offset:1320\n\t"
                "ds_read_b128 v[196:199], %[ub] offset:3440\n\t"
                "s_waitcnt lgkmcnt(11)\n\t"
                "v_mfma_f32_32x32x16_bf16 %[a0], v[224:227], v[232:235], %[a0]\n\t"
                "ds_read_b128 v[200:203], %[ub] offset:880\n\t"
                "s_waitcnt lgkmcnt(10)\n\t"
                "v_mfma_f32_32x32x16_bf16 %[a1], v[236:239], v[232:235], %[a1]\n\t"
                "ds_read_b64 v[204:205], %[kb] offset:3360\n\t"
                "ds_read_b64 v[206:207], %[kb] offset:3368\n\t"
                "s_waitcnt lgkmcnt(9)\n\t"
                "v_mfma_f32_32x32x16_bf16 %[a1], v[240:243], v[244:247], %[a1]\n\t"
                "ds_read_b64 v[208:209], %[kb] offset:1280\n\t"
                "ds_read_b64 v[210:211], %[kb] offset:1288\n\t"
                "ds_read_b128 v[212:215], %[ub] offset:3472\n\t"
                "s_waitcnt lgkmcnt(11)\n\t"
                "v_mfma_f32_32x32x16_bf16 %[a0], v[240:243], v[248:251], %[a0]\n\t"
                "ds_read_b128 v[216:219], %[ub] offset:912\n\t"
                "s_waitcnt lgkmcnt(10)\n\t"
                "v_mfma_f32_32x32x16_bf16 %[a1], v[252:255], v[248:251], %[a1]\n\t"
                "ds_read_b64 v[220:221], %[kb] offset:3328\n\t"
                "ds_read_b64 v[222:223], %[kb] offset:3336\n\t"
                "s_waitcnt lgkmcnt(9)\n\t"
                "v_mfma_f32_32x32x16_bf16 %[a1], v[192:195], v[196:199], %[a1]\n\t"
                "ds_read_b64 v[224:225], %[kb] offset:1376\n\t"
                "ds_read_b64 v[226:227], %[kb] offset:1384\n\t"
                "ds_read_b128 v[228:231], %[ub] offset:3360\n\t"
                "s_waitcnt lgkmcnt(11)\n\t"
                "v_mfma_f32_32x32x16_bf16 %[a0], v[192:195], v[200:203], %[a0]\n\t"
                "ds_read_b128 v[232:235], %[ub] offset:800\n\t"
                "s_waitcnt lgkmcnt(10)\n\t"
                "v_mfma_f32_32x32x16_bf16 %[a1], v[204:207], v[200:203], %[a1]\n\t"
                "ds_read_b64 v[236:237], %[kb] offset:3424\n\t"
                "ds_read_b64 v[238:239], %[kb] offset:3432\n\t"
                "s_waitcnt lgkmcnt(9)\n\t"
                "v_mfma_f32_32x32x16_bf16 %[a1], v[208:211], v[212:215], %[a1]\n\t"
                "ds_read_b64 v[240:241], %[kb] offset:1344\n\t"
                "ds_read_b64 v[242:243], %[kb] offset:1352\n\t"
                "ds_read_b128 v[244:247], %[ub] offset:3392\n\t"
                "s_waitcnt lgkmcnt(11)\n\t"
                "v_mfma_f32_32x32x16_bf16 %[a0], v[208:211], v[216:219], %[a0]\n\t"
                "ds_read_b128 v[248:251], %[ub] offset:832\n\t"
                "s_waitcnt lgkmcnt(10)\n\t"
                "v_mfma_f32_32x32x16_bf16 %[a1], v[220:223], v[216:219], %[a1]\n\t"
                "ds_read_b64 v[252:253], %[kb] offset:3392\n\t"
                "ds_read_b64 v[254:255], %[kb] offset:3400\n\t"
                "s_waitcnt lgkmcnt(9)\n\t"
                "v_mfma_f32_32x32x16_bf16 %[a1], v[224:227], v[228:231], %[a1]\n\t"
                "ds_read_b64 v[192:193], %[kb] offset:1440\n\t"
                "ds_read_b64 v[194:195], %[kb] offset:1448\n\t"
                "ds_read_b128 v[196:199], %[ub] offset:3280\n\t"
                "s_waitcnt lgkmcnt(11)\n\t"
                "v_mfma_f32_32x32x16_bf16 %[a0], v[224:227], v[232:235], %[a0]\n\t"
                "ds_read_b128 v[200:203], %[ub] offset:720\n\t"
                "s_waitcnt lgkmcnt(10)\n\t"
                "v_mfma_f32_32x32x16_bf16 %[a1], v[236:239], v[232:235], %[a1]\n\t"
                "ds_read_b64 v[204:205], %[kb] offset:3488\n\t"
                "ds_read_b64 v[206:207], %[kb] offset:3496\n\t"
                "s_waitcnt lgkmcnt(9)\n\t"
                "v_mfma_f32_32x32x16_bf16 %[a1], v[240:243], v[244:247], %[a1]\n\t"
                "ds_read_b64 v[208:209], %[kb] offset:1408\n\t"
                "ds_read_b64 v[210:211], %[kb] offset:1416\n\t"
                "ds_read_b128 v[212:215], %[ub] offset:3312\n\t"
                "s_waitcnt lgkmcnt(11)\n\t"
                "v_mfma_f32_32x32x16_bf16 %[a0], v[240:243], v[248:251], %[a0]\n\t"
                "ds_read_b128 v[216:219], %[ub] offset:752\n\t"
                "s_waitcnt lgkmcnt(10)\n\t"
                "v_mfma_f32_32x32x16_bf16 %[a1], v[252:255], v[248:251], %[a1]\n\t"
                "ds_read_b64 v[220:221], %[kb] offset:3456\n\t"
                "ds_read_b64 v[222:223], %[kb] offset:3464\n\t"
                "s_waitcnt lgkmcnt(9)\n\t"
                "v_mfma_f32_32x32x16_bf16 %[a1], v[192:195], v[196:199], %[a1]\n\t"
                "ds_read_b64 v[224:225], %[kb] offset:1504\n\t"
                "ds_read_b64 v[226:227], %[kb] offset:1512\n\t"
                "ds_read_b128 v[228:231], %[ub] offset:3200\n\t"
                "s_waitcnt lgkmcnt(11)\n\t"
                "v_mfma_f32_32x32x16_bf16 %[a0], v[192:195], v[200:203], %[a0]\n\t"
                "ds_read_b128 v[232:235], %[ub] offset:640\n\t"
                "s_waitcnt lgkmcnt(10)\n\t"
                "v_mfma_f32_32x32x16_bf16 %[a1], v[204:207], v[200:203], %[a1]\n\t"
                "ds_read_b64 v[236:237], %[kb] offset:3552\n\t"
                "ds_read_b64 v[238:239], %[kb] offset:3560\n\t"
                "s_waitcnt lgkmcnt(9)\n\t"
                "v_mfma_f32_32x32x16_bf16 %[a1], v[208:211], v[212:215], %[a1]\n\t"
                "ds_read_b64 v[240:241], %[kb] offset:1472\n\t"
                "ds_read_b64 v[242:243], %[kb] offset:1480\n\t"
                "ds_read_b128 v[244:247], %[ub] offset:3232\n\t"
                "s_waitcnt lgkmcnt(11)\n\t"
                "v_mfma_f32_32x32x16_bf16 %[a0], v[208:211], v[216:219], %[a0]\n\t"
                "ds_read_b128 v[248:251], %[ub] offset:672\n\t"
                "s_waitcnt lgkmcnt(10)\n\t"
                "v_mfma_f32_32x32x16_bf16 %[a1], v[220:223], v[216:219], %[a1]\n\t"
                "ds_read_b64 v[252:253], %[kb] offset:3520\n\t"
                "ds_read_b64 v[254:255], %[kb] offset:3528\n\t"
                "s_waitcnt lgkmcnt(9)\n\t"
                "v_mfma_f32_32x32x16_bf16 %[a1], v[224:227], v[228:231], %[a1]\n\t"
                "ds_read_b64 v[192:193], %[kb] offset:1568\n\t"
                "ds_read_b64 v[194:195], %[kb] offset:1576\n\t"
                "ds_read_b128 v[196:199], %[ub] offset:3120\n\t"
                "s_waitcnt lgkmcnt(11)\n\t"
                "v_mfma_f32_32x32x16_bf16 %[a0], v[224:227], v[232:235], %[a0]\n\t"
                "ds_read_b128 v[200:203], %[ub] offset:560\n\t"
                "s_waitcnt lgkmcnt(10)\n\t"
                "v_mfma_f32_32x32x16_bf16 %[a1], v[236:239], v[232:235], %[a1]\n\t"
                "ds_read_b64 v[204:205], %[kb] offset:3616\n\t"
                "ds_read_b64 v[206:207], %[kb] offset:3624\n\t"
                "s_waitcnt lgkmcnt(9)\n\t"
                "v_mfma_f32_32x32x16_bf16 %[a1], v[240:243], v[244:247], %[a1]\n\t"
                "ds_read_b64 v[208:209], %[kb] offset:1536\n\t"
                "ds_read_b64 v[210:211], %[kb] offset:1544\n\t"
                "ds_read_b128 v[212:215], %[ub] offset:3152\n\t"
                "s_waitcnt lgkmcnt(11)\n\t"
                "v_mfma_f32_32x32x16_bf16 %[a0], v[240:243], v[248:251], %[a0]\n\t"
                "ds_read_b128 v[216:219], %[ub] offset:592\n\t"
                "s_waitcnt lgkmcnt(10)\n\t"
                "v_mfma_f32_32x32x16_bf16 %[a1], v[252:255], v[248:251], %[a1]\n\t"
                "ds_read_b64 v[220:221], %[kb] offset:3584\n\t"
                "ds_read_b64 v[222:223], %[kb] offset:3592\n\t"
                "s_waitcnt lgkmcnt(9)\n\t"
                "v_mfma_f32_32x32x16_bf16 %[a1], v[192:195], v[196:199], %[a1]\n\t"
                "ds_read_b64 v[224:225], %[kb] offset:1632\n\t"
                "ds_read_b64 v[226:227], %[kb] offset:1640\n\t"
                "ds_read_b128 v[228:231], %[ub] offset:3040\n\t"
                "s_waitcnt lgkmcnt(11)\n\t"
                "v_mfma_f32_32x32x16_bf16 %[a0], v[192:195], v[200:203], %[a0]\n\t"
                "ds_read_b128 v[232:235], %[ub] offset:480\n\t"
                "s_waitcnt lgkmcnt(10)\n\t"
                "v_mfma_f32_32x32x16_bf16 %[a1], v[204:207], v[200:203], %[a1]\n\t"
                "ds_read_b64 v[236:237], %[kb] offset:3680\n\t"
                "ds_read_b64 v[238:239], %[kb] offset:3688\n\t"
                "s_waitcnt lgkmcnt(9)\n\t"
                "v_mfma_f32_32x32x16_bf16 %[a1], v[208:211], v[212:215], %[a1]\n\t"
                "ds_read_b64 v[240:241], %[kb] offset:1600\n\t"
                "ds_read_b64 v[242:243], %[kb] offset:1608\n\t"
                "ds_read_b128 v[244:247], %[ub] offset:3072\n\t"
                "s_waitcnt lgkmcnt(11)\n\t"
                "v_mfma_f32_32x32x16_bf16 %[a0], v[208:211], v[216:219], %[a0]\n\t"
                "ds_read_b128 v[248:251], %[ub] offset:512\n\t"
                "s_waitcnt lgkmcnt(10)\n\t"
                "v_mfma_f32_32x32x16_bf16 %[a1], v[220:223], v[216:219], %[a1]\n\t"
                "ds_read_b64 v[252:253], %[kb] offset:3648\n\t"
                "ds_read_b64 v[254:255], %[kb] offset:3656\n\t"
                "s_waitcnt lgkmcnt(9)\n\t"
                "v_mfma_f32_32x32x16_bf16 %[a1], v[224:227], v[228:231], %[a1]\n\t"
                "ds_read_b64 v[192:193], %[kb] offset:1696\n\t"
                "ds_read_b64 v[194:195], %[kb] offset:1704\n\t"
                "ds_read_b128 v[196:199], %[ub] offset:2960\n\t"
                "s_waitcnt lgkmcnt(11)\n\t"
                "v_mfma_f32_32x32x16_bf16 %[a0], v[224:227], v[232:235], %[a0]\n\t"
                "ds_read_b128 v[200:203], %[ub] offset:400\n\t"
                "s_waitcnt lgkmcnt(10)\n\t"
                "v_mfma_f32_32x32x16_bf16 %[a1], v[236:239], v[232:235], %[a1]\n\t"
                "ds_read_b64 v[204:205], %[kb] offset:3744\n\t"
                "ds_read_b64 v[206:207], %[kb] offset:3752\n\t"
                "s_waitcnt lgkmcnt(9)\n\t"
                "v_mfma_f32_32x32x16_bf16 %[a1], v[240:243], v[244:247], %[a1]\n\t"
                "ds_read_b64 v[208:209], %[kb] offset:1664\n\t"
                "ds_read_b64 v[210:211], %[kb] offset:1672\n\t"
                "ds_read_b128 v[212:215], %[ub] offset:2992\n\t"
                "s_waitcnt lgkmcnt(11)\n\t"
                "v_mfma_f32_32x32x16_bf16 %[a0], v[240:243], v[248:251], %[a0]\n\t"
                "ds_read_b128 v[216:219], %[ub] offset:432\n\t"
                "s_waitcnt lgkmcnt(10)\n\t"
                "v_mfma_f32_32x32x16_bf16 %[a1], v[252:255], v[248:251], %[a1]\n\t"
                "ds_read_b64 v[220:221], %[kb] offset:3712\n\t"
                "ds_read_b64 v[222:223], %[kb] offset:3720\n\t"
                "s_waitcnt lgkmcnt(9)\n\t"
                "v_mfma_f32_32x32x16_bf16 %[a1], v[192:195], v[196:199], %[a1]\n\t"
                "ds_read_b64 v[224:225], %[kb] offset:1760\n\t"
                "ds_read_b64 v[226:227], %[kb] offset:1768\n\t"
                "ds_read_b128 v[228:231], %[ub] offset:2880\n\t"
                "s_waitcnt lgkmcnt(11)\n\t"
                "v_mfma_f32_32x32x16_bf16 %[a0], v[192:195], v[200:203], %[a0]\n\t"
                "ds_read_b128 v[232:235], %[ub] offset:320\n\t"
                "s_waitcnt lgkmcnt(10)\n\t"
                "v_mfma_f32_32x32x16_bf16 %[a1], v[204:207], v[200:203], %[a1]\n\t"
                "ds_read_b64 v[236:237], %[kb] offset:3808\n\t"
                "ds_read_b64 v[238:239], %[kb] offset:3816\n\t"
                "s_waitcnt lgkmcnt(9)\n\t"
                "v_mfma_f32_32x32x16_bf16 %[a1], v[208:211], v[212:215], %[a1]\n\t"
                "ds_read_b64 v[240:241], %[kb] offset:1728\n\t"
                "ds_read_b64 v[242:243], %[kb] offset:1736\n\t"
                "ds_read_b128 v[244:247], %[ub] offset:2912\n\t"
                "s_waitcnt lgkmcnt(11)\n\t"
                "v_mfma_f32_32x32x16_bf16 %[a0], v[208:211], v[216:219], %[a0]\n\t"
                "ds_read_b128 v[248:251], %[ub] offset:352\n\t"
                "s_waitcnt lgkmcnt(10)\n\t"
                "v_mfma_f32_32x32x16_bf16 %[a1], v[220:223], v[216:219], %[a1]\n\t"
                "ds_read_b64 v[252:253], %[kb] offset:3776\n\t"
                "ds_read_b64 v[254:255], %[kb] offset:3784\n\t"
                "s_waitcnt lgkmcnt(9)\n\t"
                "v_mfma_f32_32x32x16_bf16 %[a1], v[224:227], v[228:231], %[a1]\n\t"
                "ds_read_b64 v[192:193], %[kb] offset:1824\n\t"
                "ds_read_b64 v[194:195], %[kb] offset:1832\n\t"
                "ds_read_b128 v[196:199], %[ub] offset:2800\n\t"
                "s_waitcnt lgkmcnt(11)\n\t"
                "v_mfma_f32_32x32x16_bf16 %[a0], v[224:227], v[232:235], %[a0]\n\t"
                "ds_read_b128 v[200:203], %[ub] offset:240\n\t"
                "s_waitcnt lgkmcnt(10)\n\t"
                "v_mfma_f32_32x32x16_bf16 %[a1], v[236:239], v[232:235], %[a1]\n\t"
                "ds_read_b64 v[204:205], %[kb] offset:3872\n\t"
                "ds_read_b64 v[206:207], %[kb] offset:3880\n\t"
                "s_waitcnt lgkmcnt(9)\n\t"
                "v_mfma_f32_32x32x16_bf16 %[a1], v[240:243], v[244:247], %[a1]\n\t"
                "ds_read_b64 v[208:209], %[kb] offset:1792\n\t"
                "ds_read_b64 v[210:211], %[kb] offset:1800\n\t"
                "ds_read_b128 v[212:215], %[ub] offset:2832\n\t"
                "s_waitcnt lgkmcnt(11)\n\t"
                "v_mfma_f32_32x32x16_bf16 %[a0], v[240:243], v[248:251], %[a0]\n\t"
                "ds_read_b128 v[216:219], %[ub] offset:272\n\t"
                "s_waitcnt lgkmcnt(10)\n\t"
                "v_mfma_f32_32x32x16_bf16 %[a1], v[252:255], v[248:251], %[a1]\n\t"
                "ds_read_b64 v[220:221], %[kb] offset:3840\n\t"
                "ds_read_b64 v[222:223], %[kb] offset:3848\n\t"
                "s_waitcnt lgkmcnt(9)\n\t"
                "v_mfma_f32_32x32x16_bf16 %[a1], v[192:195], v[196:199], %[a1]\n\t"
                "ds_read_b64 v[224:225], %[kb] offset:1888\n\t"
                "ds_read_b64 v[226:227], %[kb] offset:1896\n\t"
                "ds_read_b128 v[228:231], %[ub] offset:2720\n\t"
                "s_waitcnt lgkmcnt(11)\n\t"
                "v_mfma_f32_32x32x16_bf16 %[a0], v[192:195], v[200:203], %[a0]\n\t"
                "ds_read_b128 v[232:235], %[ub] offset:160\n\t"
                "s_waitcnt lgkmcnt(10)\n\t"
                "v_mfma_f32_32x32x16_bf16 %[a1], v[204:207], v[200:203], %[a1]\n\t"
                "ds_read_b64 v[236:237], %[kb] offset:3936\n\t"
                "ds_read_b64 v[238:239], %[kb] offset:3944\n\t"
                "s_waitcnt lgkmcnt(9)\n\t"
                "v_mfma_f32_32x32x16_bf16 %[a1], v[208:211], v[212:215], %[a1]\n\t"
                "ds_read_b64 v[240:241], %[kb] offset:1856\n\t"
                "ds_read_b64 v[242:243], %[kb] offset:1864\n\t"
                "ds_read_b128 v[244:247], %[ub] offset:2752\n\t"
                "s_waitcnt lgkmcnt(11)\n\t"
                "v_mfma_f32_32x32x16_bf16 %[a0], v[208:211], v[216:219], %[a0]\n\t"
                "ds_read_b128 v[248:251], %[ub] offset:192\n\t"
                "s_waitcnt lgkmcnt(10)\n\t"
                "v_mfma_f32_32x32x16_bf16 %[a1], v[220:223], v[216:219], %[a1]\n\t"
                "ds_read_b64 v[252:253], %[kb] offset:3904\n\t"
                "ds_read_b64 v[254:255], %[kb] offset:3912\n\t"
                "s_waitcnt lgkmcnt(9)\n\t"
                "v_mfma_f32_32x32x16_bf16 %[a1], v[224:227], v[228:231], %[a1]\n\t"
                "ds_read_b64 v[192:193], %[kb] offset:1952\n\t"
                "ds_read_b64 v[194:195], %[kb] offset:1960\n\t"
                "ds_read_b128 v[196:199], %[ub] offset:2640\n\t"
                "s_waitcnt lgkmcnt(11)\n\t"
                "v_mfma_f32_32x32x16_bf16 %[a0], v[224:227], v[232:235], %[a0]\n\t"
                "ds_read_b128 v[200:203], %[ub] offset:80\n\t"
                "s_waitcnt lgkmcnt(10)\n\t"
                "v_mfma_f32_32x32x16_bf16 %[a1], v[236:239], v[232:235], %[a1]\n\t"
                "ds_read_b64 v[204:205], %[kb] offset:4000\n\t"
                "ds_read_b64 v[206:207], %[kb] offset:4008\n\t"
                "s_waitcnt lgkmcnt(9)\n\t"
                "v_mfma_f32_32x32x16_bf16 %[a1], v[240:243], v[244:247], %[a1]\n\t"
                "ds_read_b64 v[208:209], %[kb] offset:1920\n\t"
                "ds_read_b64 v[210:211], %[kb] offset:1928\n\t"
                "ds_read_b128 v[212:215], %[ub] offset:2672\n\t"
                "s_waitcnt lgkmcnt(11)\n\t"
                "v_mfma_f32_32x32x16_bf16 %[a0], v[240:243], v[248:251], %[a0]\n\t"
                "ds_read_b128 v[216:219], %[ub] offset:112\n\t"
                "s_waitcnt lgkmcnt(10)\n\t"
                "v_mfma_f32_32x32x16_bf16 %[a1], v[252:255], v[248:251], %[a1]\n\t"
                "ds_read_b64 v[220:221], %[kb] offset:3968\n\t"
                "ds_read_b64 v[222:223], %[kb] offset:3976\n\t"
                "s_waitcnt lgkmcnt(9)\n\t"
                "v_mfma_f32_32x32x16_bf16 %[a1], v[192:195], v[196:199], %[a1]\n\t"
                "ds_read_b64 v[224:225], %[kb] offset:2016\n\t"
                "ds_read_b64 v[226:227], %[kb] offset:2024\n\t"
                "ds_read_b128 v[228:231], %[ub] offset:2560\n\t"
                "s_waitcnt lgkmcnt(11)\n\t"
                "v_mfma_f32_32x32x16_bf16 %[a0], v[192:195], v[200:203], %[a0]\n\t"
                "ds_read_b128 v[232:235], %[ub] offset:0\n\t"
                "s_waitcnt lgkmcnt(10)\n\t"
                "v_mfma_f32_32x32x16_bf16 %[a1], v[204:207], v[200:203], %[a1]\n\t"
                "ds_read_b64 v[236:237], %[kb] offset:4064\n\t"
                "ds_read_b64 v[238:239], %[kb] offset:4072\n\t"
                "s_waitcnt lgkmcnt(9)\n\t"
                "v_mfma_f32_32x32x16_bf16 %[a1], v[208:211], v[212:215], %[a1]\n\t"
                "ds_read_b64 v[240:241], %[kb] offset:1984\n\t"
                "ds_read_b64 v[242:243], %[kb] offset:1992\n\t"
                "ds_read_b128 v[244:247], %[ub] offset:2592\n\t"
                "s_waitcnt lgkmcnt(11)\n\t"
                "v_mfma_f32_32x32x16_bf16 %[a0], v[208:211], v[216:219], %[a0]\n\t"
                "ds_read_b128 v[248:251], %[ub] offset:32\n\t"
                "s_waitcnt lgkmcnt(10)\n\t"
                "v_mfma_f32_32x32x16_bf16 %[a1], v[220:223], v[216:219], %[a1]\n\t"
                "ds_read_b64 v[252:253], %[kb] offset:4032\n\t"
                "ds_read_b64 v[254:255], %[kb] offset:4040\n\t"
                "s_waitcnt lgkmcnt(9)\n\t"
                "v_mfma_f32_32x32x16_bf16 %[a1], v[224:227], v[228:231], %[a1]\n\t"
                "s_waitcnt lgkmcnt(8)\n\t"
                "v_mfma_f32_32x32x16_bf16 %[a0], v[224:227], v[232:235], %[a0]\n\t"
                "s_waitcnt lgkmcnt(6)\n\t"
                "v_mfma_f32_32x32x16_bf16 %[a1], v[236:239], v[232:235], %[a1]\n\t"
                "s_waitcnt lgkmcnt(3)\n\t"
                "v_mfma_f32_32x32x16_bf16 %[a1], v[240:243], v[244:247], %[a1]\n\t"
                "s_waitcnt lgkmcnt(2)\n\t"
                "v_mfma_f32_32x32x16_bf16 %[a0], v[240:243], v[248:251], %[a0]\n\t"
                "s_waitcnt lgkmcnt(0)\n\t"
                "v_mfma_f32_32x32x16_bf16 %[a1], v[252:255], v[248:251], %[a1]\n\t"
                "s_nop 7\n\t"
                "s_nop 3\n\t"
                : [a0] "=&v"(acc0), [a1] "=&v"(acc1) : [kb] "v"(kb), [ub] "v"(ub0 + c8 * CV_U) : "memory", "v192", "v193", "v194", "v195", "v196", "v197", "v198", "v199", "v200", "v201", "v202", "v203", "v204", "v205", "v206", "v207", "v208", "v209", "v210", "v211", "v212", "v213", "v214", "v215", "v216", "v217", "v218", "v219", "v220", "v221", "v222", "v223", "v224", "v225", "v226", "v227", "v228", "v229", "v230", "v231", "v232", "v233", "v234", "v235", "v236", "v237", "v238", "v239", "v240", "v241", "v242", "v243", "v244", "v245", "v246", "v247", "v248", "v249", "v250", "v251", "v252", "v253", "v254", "v255");
            unsigned char* uc = lds + c8 * CV_U + (32 + b) * CV_RP + kg * 8;
#pragma unroll
            for (int q = 0; q < 4; ++q) {
                *(v2u*)(uc + q * 16) = (v2u){pack2(acc0[4 * q], acc0[4 * q + 1]), pack2(acc0[4 * q + 2], acc0[4 * q + 3])};
                *(v2u*)(uc + 32 * CV_RP + q * 16) = (v2u){pack2(acc1[4 * q], acc1[4 * q + 1]), pack2(acc1[4 * q + 2], acc1[4 * q + 3])}; }
            __builtin_amdgcn_wave_barrier();
        }
    __syncthreads();
#pragma unroll
    for (int i = 0; i < 8; ++i) st16(r, W_Y + un * 32768 + i * 4096 + tid * 16, *(const v4u*)(urow + (i >> 2) * CV_U + (i & 3) * 16));
    DRAIN(); __syncthreads();
    if (tid == 0) __hip_atomic_fetch_add((unsigned*)(p.ws + F_CVY + (un >> 2) * 128), 1u, __ATOMIC_RELAXED, __HIP_MEMORY_SCOPE_AGENT);
    return true;
}

__device__ __forceinline__ void proj_block(const Params& p, unsigned char* lds, const int wv, const int h, const int c, const int j) {
    const int w = wv, l = otid() & 63, n = l & 15, g = l >> 4, tm = w >> 1, tn = w & 1;
    const unsigned lb = __builtin_amdgcn_readfirstlane((unsigned)(size_t)lds);
    const v4u sx = mk_srd(p.Aq, S * D * 2), sq = mk_srd(c < 2 ? p.Wq : p.Wk, 1024 * 4096 * 2), sv = mk_srd(p.Wv, 1024 * 4096 * 2), sw = mk_srd(p.ws, W_TOTAL);
    const unsigned lx = lb + (32 * tm + n) * 128 + ((g ^ (n & 7)) << 4), lr = lb + 131072, rr = 8 * g + (n >> 2);
    const int cv = ((c & 1) ^ 1) * 2 + (c >> 1);
    const unsigned aq = (c < 2 ? W_QX : W_KX) + (tm * 32 + h) * 8192 + (2 * (c & 1) + tn) * 1024 + l * 16;
    const int hq = h & 7, tk = ((hq - (hq > 1) - (hq > 5)) * 4 + c) * 4 + w, ts6 = tk % 6, tch = 15 - tk / 6, tx = tk - 6 * GTOUCH;
    const bf16_t* tp = (h & 3) == 1 ? p.gate_w : tx < 0 ? (ts6 % 3 == 0 ? p.Wq : ts6 % 3 == 1 ? p.Wk : p.Wv) + (size_t)(tch * 64 + l) * 4096 + ((h & 24) + 1 + (ts6 / 3) * 4) * 128 + 64
                     : tx < XTOUCH ? p.Aq + (tx * 64 + l) * 64 : p.gate_w;
    unsigned t0, xo, so, cn; unsigned long long ts, ta, tb, tc, td;
    asm volatile(
        "s_mov_b32 %[xo], 0\n\t"
        "s_mov_b32 %[so], 0\n\t"
        "s_mov_b32 %[cn], 1\n\t"
        "global_load_dword v88, v[114:115], off\n\t"
        "s_add_u32 m0, %[ld], 0\n\t"
        "s_add_u32 %[t0], %[xo], 0\n\t"
        "buffer_load_dwordx4 v100, %[sx], %[t0] offen lds\n\t"
        "s_add_u32 m0, %[ld], 1024\n\t"
        "s_add_u32 %[t0], %[xo], 16384\n\t"
        "buffer_load_dwordx4 v100, %[sx], %[t0] offen lds\n\t"
        "s_add_u32 m0, %[ld], 8192\n\t"
        "s_add_u32 %[t0], %[xo], 128\n\t"
        "buffer_load_dwordx4 v100, %[sx], %[t0] offen lds\n\t"
        "s_add_u32 m0, %[ld], 9216\n\t"
        "s_add_u32 %[t0], %[xo], 16512\n\t"
        "buffer_load_dwordx4 v100, %[sx], %[t0] offen lds\n\t"
        "s_add_u32 m0, %[ld], 16384\n\t"
        "s_add_u32 %[t0], %[xo], 256\n\t"
        "buffer_load_dwordx4 v100, %[sx], %[t0] offen lds\n\t"
        "s_add_u32 m0, %[ld], 17408\n\t"
        "s_add_u32 %[t0], %[xo], 16640\n\t"
        "buffer_load_dwordx4 v100, %[sx], %[t0] offen lds\n\t"
        "s_add_u32 m0, %[ld], 24576\n\t"
        "s_add_u32 %[t0], %[xo], 384\n\t"
        "buffer_load_dwordx4 v100, %[sx], %[t0] offen lds\n\t"
        "s_add_u32 m0, %[ld], 25600\n\t"
        "s_add_u32 %[t0], %[xo], 16768\n\t"
        "buffer_load_dwordx4 v100, %[sx], %[t0] offen lds\n\t"
        "s_mov_b32 %[t0], 0\n\t"
        "buffer_load_dwordx4 a[192:195], v101, %[sq], %[t0] offen\n\t"
        "buffer_load_dwordx4 a[200:203], v112, %[sv], %[t0] offen\n\t"
        "s_mov_b32 %[t0], 262144\n\t"
        "buffer_load_dwordx4 a[196:199], v101, %[sq], %[t0] offen\n\t"
        "s_mov_b32 %[t0], 524288\n\t"
        "buffer_load_dwordx4 a[204:207], v101, %[sq], %[t0] offen\n\t"
        "buffer_load_dwordx4 a[212:215], v112, %[sv], %[t0] offen\n\t"
        "s_mov_b32 %[t0], 786432\n\t"
        "buffer_load_dwordx4 a[208:211], v101, %[sq], %[t0] offen\n\t"
        "s_mov_b32 %[t0], 1048576\n\t"
        "buffer_load_dwordx4 a[216:219], v101, %[sq], %[t0] offen\n\t"
        "buffer_load_dwordx4 a[224:227], v112, %[sv], %[t0] offen\n\t"
        "s_mov_b32 %[t0], 1310720\n\t"
        "buffer_load_dwordx4 a[220:223], v101, %[sq], %[t0] offen\n\t"
        "s_mov_b32 %[t0], 1572864\n\t"
        "buffer_load_dwordx4 a[228:231], v101, %[sq], %[t0] offen\n\t"
        "buffer_load_dwordx4 a[236:239], v112, %[sv], %[t0] offen\n\t"
        "s_mov_b32 %[t0], 1835008\n\t"
        "buffer_load_dwordx4 a[232:235], v101, %[sq], %[t0] offen\n\t"
        "s_waitcnt vmcnt(9)\n\t"
        "ds_write_b128 v102, a[192:195] offset:0\n\t"
        "ds_write_b128 v102, a[196:199] offset:4096\n\t"
        "ds_write_b128 v116, a[200:203] offset:8192\n\t"
        "s_waitcnt lgkmcnt(0)\n\t"
        "s_barrier\n\t"
        "ds_read_b64_tr_b16 a[0:1], v103 offset:0\n\t"
        "ds_read_b64_tr_b16 a[2:3], v103 offset:512\n\t"
        "ds_read_b64_tr_b16 a[4:5], v103 offset:8\n\t"
        "ds_read_b64_tr_b16 a[6:7], v103 offset:520\n\t"
        "ds_read_b64_tr_b16 a[8:9], v104 offset:0\n\t"
        "ds_read_b64_tr_b16 a[10:11], v104 offset:256\n\t"
        "s_waitcnt lgkmcnt(5)\n\t"
        "ds_read_b64_tr_b16 a[12:13], v103 offset:4096\n\t"
        "ds_read_b64_tr_b16 a[14:15], v103 offset:4608\n\t"
        "ds_read_b64_tr_b16 a[16:17], v103 offset:4104\n\t"
        "ds_read_b64_tr_b16 a[18:19], v103 offset:4616\n\t"
        "ds_read_b64_tr_b16 a[20:21], v104 offset:2048\n\t"
        "ds_read_b64_tr_b16 a[22:23], v104 offset:2304\n\t"
        "ds_read_b128 v[56:59], v105 offset:0\n\t"
        "ds_read_b128 v[60:63], v105 offset:2048\n\t"
        "ds_read_b128 v[64:67], v106 offset:0\n\t"
        "ds_read_b128 v[68:71], v106 offset:2048\n\t"
        "s_add_u32 m0, %[ld], 32768\n\t"
        "s_add_u32 %[t0], %[xo], 512\n\t"
        "buffer_load_dwordx4 v100, %[sx], %[t0] offen lds\n\t"
        "s_add_u32 m0, %[ld], 33792\n\t"
        "s_add_u32 %[t0], %[xo], 16896\n\t"
        "buffer_load_dwordx4 v100, %[sx], %[t0] offen lds\n\t"
        "s_add_u32 m0, %[ld], 40960\n\t"
        "s_add_u32 %[t0], %[xo], 640\n\t"
        "buffer_load_dwordx4 v100, %[sx], %[t0] offen lds\n\t"
        "s_add_u32 m0, %[ld], 41984\n\t"
        "s_add_u32 %[t0], %[xo], 17024\n\t"
        "buffer_load_dwordx4 v100, %[sx], %[t0] offen lds\n\t"
        "s_add_u32 m0, %[ld], 49152\n\t"
        "s_add_u32 %[t0], %[xo], 768\n\t"
        "buffer_load_dwordx4 v100, %[sx], %[t0] offen lds\n\t"
        "s_add_u32 m0, %[ld], 50176\n\t"
        "s_add_u32 %[t0], %[xo], 17152\n\t"
        "buffer_load_dwordx4 v100, %[sx], %[t0] offen lds\n\t"
        "s_add_u32 m0, %[ld], 57344\n\t"
        "s_add_u32 %[t0], %[xo], 896\n\t"
        "buffer_load_dwordx4 v100, %[sx], %[t0] offen lds\n\t"
        "s_add_u32 m0, %[ld], 58368\n\t"
        "s_add_u32 %[t0], %[xo], 17280\n\t"
        "buffer_load_dwordx4 v100, %[sx], %[t0] offen lds\n\t"
        "s_mov_b32 %[t0], 2097152\n\t"
        "buffer_load_dwordx4 a[240:243], v101, %[sq], %[t0] offen\n\t"
        "buffer_load_dwordx4 a[248:251], v112, %[sv], %[t0] offen\n\t"
        "s_mov_b32 %[t0], 2359296\n\t"
        "buffer_load_dwordx4 a[244:247], v101, %[sq], %[t0] offen\n\t"
        "s_waitcnt vmcnt(17)\n\t"
        "ds_write_b128 v102, a[204:207] offset:16384\n\t"
        "ds_write_b128 v102, a[208:211] offset:20480\n\t"
        "ds_write_b128 v116, a[212:215] offset:24576\n\t"
        "s_waitcnt lgkmcnt(0)\n\t"
        "s_barrier\n\t"
        "v_mfma_f32_16x16x32_bf16 v[8:11], a[0:3], v[56:59], 0\n\t"
        "ds_read_b64_tr_b16 a[24:25], v103 offset:16384\n\t"
        "ds_read_b64_tr_b16 a[26:27], v103 offset:16896\n\t"
        "v_mfma_f32_16x16x32_bf16 v[12:15], a[4:7], v[56:59], 0\n\t"
        "ds_read_b64_tr_b16 a[28:29], v103 offset:16392\n\t"
        "ds_read_b64_tr_b16 a[30:31], v103 offset:16904\n\t"
        "v_mfma_f32_16x16x32_bf16 v[16:19], v[56:59], a[8:11], 0\n\t"
        "ds_read_b64_tr_b16 a[32:33], v104 offset:16384\n\t"
        "ds_read_b64_tr_b16 a[34:35], v104 offset:16640\n\t"
        "v_mfma_f32_16x16x32_bf16 v[20:23], a[0:3], v[60:63], 0\n\t"
        "s_waitcnt lgkmcnt(5)\n\t"
        "ds_read_b64_tr_b16 a[36:37], v103 offset:20480\n\t"
        "v_mfma_f32_16x16x32_bf16 v[24:27], a[4:7], v[60:63], 0\n\t"
        "ds_read_b64_tr_b16 a[38:39], v103 offset:20992\n\t"
        "ds_read_b64_tr_b16 a[40:41], v103 offset:20488\n\t"
        "v_mfma_f32_16x16x32_bf16 v[28:31], v[60:63], a[8:11], 0\n\t"
        "ds_read_b64_tr_b16 a[42:43], v103 offset:21000\n\t"
        "ds_read_b64_tr_b16 a[44:45], v104 offset:18432\n\t"
        "v_mfma_f32_16x16x32_bf16 v[8:11], a[12:15], v[64:67], v[8:11]\n\t"
        "ds_read_b64_tr_b16 a[46:47], v104 offset:18688\n\t"
        "ds_read_b128 v[72:75], v105 offset:8192\n\t"
        "v_mfma_f32_16x16x32_bf16 v[12:15], a[16:19], v[64:67], v[12:15]\n\t"
        "ds_read_b128 v[76:79], v105 offset:10240\n\t"
        "ds_read_b128 v[80:83], v106 offset:8192\n\t"
        "v_mfma_f32_16x16x32_bf16 v[16:19], v[64:67], a[20:23], v[16:19]\n\t"
        "ds_read_b128 v[84:87], v106 offset:10240\n\t"
        "v_mfma_f32_16x16x32_bf16 v[20:23], a[12:15], v[68:71], v[20:23]\n\t"
        "v_mfma_f32_16x16x32_bf16 v[24:27], a[16:19], v[68:71], v[24:27]\n\t"
        "v_mfma_f32_16x16x32_bf16 v[28:31], v[68:71], a[20:23], v[28:31]\n\t"
        "s_mov_b32 %[t0], 2621440\n\t"
        "buffer_load_dwordx4 a[252:255], v101, %[sq], %[t0] offen\n\t"
        "buffer_load_dwordx4 v[132:135], v112, %[sv], %[t0] offen\n\t"
        "s_mov_b32 %[t0], 2883584\n\t"
        "buffer_load_dwordx4 v[128:131], v101, %[sq], %[t0] offen\n\t"
        "s_waitcnt vmcnt(17)\n\t"
        "ds_write_b128 v102, a[216:219] offset:0\n\t"
        "ds_write_b128 v102, a[220:223] offset:4096\n\t"
        "ds_write_b128 v116, a[224:227] offset:8192\n\t"
        "s_waitcnt lgkmcnt(0)\n\t"
        "s_barrier\n\t"
        "v_mfma_f32_16x16x32_bf16 v[8:11], a[24:27], v[72:75], v[8:11]\n\t"
        "ds_read_b64_tr_b16 a[48:49], v103 offset:0\n\t"
        "ds_read_b64_tr_b16 a[50:51], v103 offset:512\n\t"
        "v_mfma_f32_16x16x32_bf16 v[12:15], a[28:31], v[72:75], v[12:15]\n\t"
        "ds_read_b64_tr_b16 a[52:53], v103 offset:8\n\t"
        "ds_read_b64_tr_b16 a[54:55], v103 offset:520\n\t"
        "v_mfma_f32_16x16x32_bf16 v[16:19], v[72:75], a[32:35], v[16:19]\n\t"
        "ds_read_b64_tr_b16 a[56:57], v104 offset:0\n\t"
        "ds_read_b64_tr_b16 a[58:59], v104 offset:256\n\t"
        "v_mfma_f32_16x16x32_bf16 v[20:23], a[24:27], v[76:79], v[20:23]\n\t"
        "s_waitcnt lgkmcnt(5)\n\t"
        "ds_read_b64_tr_b16 a[60:61], v103 offset:4096\n\t"
        "v_mfma_f32_16x16x32_bf16 v[24:27], a[28:31], v[76:79], v[24:27]\n\t"
        "ds_read_b64_tr_b16 a[62:63], v103 offset:4608\n\t"
        "ds_read_b64_tr_b16 a[64:65], v103 offset:4104\n\t"
        "v_mfma_f32_16x16x32_bf16 v[28:31], v[76:79], a[32:35], v[28:31]\n\t"
        "ds_read_b64_tr_b16 a[66:67], v103 offset:4616\n\t"
        "ds_read_b64_tr_b16 a[68:69], v104 offset:2048\n\t"
        "v_mfma_f32_16x16x32_bf16 v[8:11], a[36:39], v[80:83], v[8:11]\n\t"
        "ds_read_b64_tr_b16 a[70:71], v104 offset:2304\n\t"
        "ds_read_b128 v[56:59], v105 offset:16384\n\t"
        "v_mfma_f32_16x16x32_bf16 v[12:15], a[40:43], v[80:83], v[12:15]\n\t"
        "ds_read_b128 v[60:63], v105 offset:18432\n\t"
        "ds_read_b128 v[64:67], v106 offset:16384\n\t"
        "v_mfma_f32_16x16x32_bf16 v[16:19], v[80:83], a[44:47], v[16:19]\n\t"
        "ds_read_b128 v[68:71], v106 offset:18432\n\t"
        "v_mfma_f32_16x16x32_bf16 v[20:23], a[36:39], v[84:87], v[20:23]\n\t"
        "v_mfma_f32_16x16x32_bf16 v[24:27], a[40:43], v[84:87], v[24:27]\n\t"
        "v_mfma_f32_16x16x32_bf16 v[28:31], v[84:87], a[44:47], v[28:31]\n\t"
        "s_mov_b32 %[t0], 3145728\n\t"
        "buffer_load_dwordx4 v[136:139], v101, %[sq], %[t0] offen\n\t"
        "buffer_load_dwordx4 v[144:147], v112, %[sv], %[t0] offen\n\t"
        "s_mov_b32 %[t0], 3407872\n\t"
        "buffer_load_dwordx4 v[140:143], v101, %[sq], %[t0] offen\n\t"
        "s_waitcnt vmcnt(17)\n\t"
        "ds_write_b128 v102, a[228:231] offset:16384\n\t"
        "ds_write_b128 v102, a[232:235] offset:20480\n\t"
        "ds_write_b128 v116, a[236:239] offset:24576\n\t"
        "s_waitcnt lgkmcnt(0)\n\t"
        "s_barrier\n\t"
        "v_mfma_f32_16x16x32_bf16 v[8:11], a[48:51], v[56:59], v[8:11]\n\t"
        "ds_read_b64_tr_b16 a[72:73], v103 offset:16384\n\t"
        "ds_read_b64_tr_b16 a[74:75], v103 offset:16896\n\t"
        "v_mfma_f32_16x16x32_bf16 v[12:15], a[52:55], v[56:59], v[12:15]\n\t"
        "ds_read_b64_tr_b16 a[76:77], v103 offset:16392\n\t"
        "ds_read_b64_tr_b16 a[78:79], v103 offset:16904\n\t"
        "v_mfma_f32_16x16x32_bf16 v[16:19], v[56:59], a[56:59], v[16:19]\n\t"
        "ds_read_b64_tr_b16 a[80:81], v104 offset:16384\n\t"
        "ds_read_b64_tr_b16 a[82:83], v104 offset:16640\n\t"
        "v_mfma_f32_16x16x32_bf16 v[20:23], a[48:51], v[60:63], v[20:23]\n\t"
        "s_waitcnt lgkmcnt(5)\n\t"
        "ds_read_b64_tr_b16 a[84:85], v103 offset:20480\n\t"
        "v_mfma_f32_16x16x32_bf16 v[24:27], a[52:55], v[60:63], v[24:27]\n\t"
        "ds_read_b64_tr_b16 a[86:87], v103 offset:20992\n\t"
        "ds_read_b64_tr_b16 a[88:89], v103 offset:20488\n\t"
        "v_mfma_f32_16x16x32_bf16 v[28:31], v[60:63], a[56:59], v[28:31]\n\t"
        "ds_read_b64_tr_b16 a[90:91], v103 offset:21000\n\t"
        "ds_read_b64_tr_b16 a[92:93], v104 offset:18432\n\t"
        "v_mfma_f32_16x16x32_bf16 v[8:11], a[60:63], v[64:67], v[8:11]\n\t"
        "ds_read_b64_tr_b16 a[94:95], v104 offset:18688\n\t"
        "ds_read_b128 v[72:75], v105 offset:24576\n\t"
        "v_mfma_f32_16x16x32_bf16 v[12:15], a[64:67], v[64:67], v[12:15]\n\t"
        "ds_read_b128 v[76:79], v105 offset:26624\n\t"
        "ds_read_b128 v[80:83], v106 offset:24576\n\t"
        "v_mfma_f32_16x16x32_bf16 v[16:19], v[64:67], a[68:71], v[16:19]\n\t"
        "ds_read_b128 v[84:87], v106 offset:26624\n\t"
        "v_mfma_f32_16x16x32_bf16 v[20:23], a[60:63], v[68:71], v[20:23]\n\t"
        "v_mfma_f32_16x16x32_bf16 v[24:27], a[64:67], v[68:71], v[24:27]\n\t"
        "v_mfma_f32_16x16x32_bf16 v[28:31], v[68:71], a[68:71], v[28:31]\n\t"
        "s_mov_b32 %[t0], 3670016\n\t"
        "buffer_load_dwordx4 v[148:151], v101, %[sq], %[t0] offen\n\t"
        "buffer_load_dwordx4 v[156:159], v112, %[sv], %[t0] offen\n\t"
        "s_mov_b32 %[t0], 3932160\n\t"
        "buffer_load_dwordx4 v[152:155], v101, %[sq], %[t0] offen\n\t"
        "s_waitcnt vmcnt(9)\n\t"
        "ds_write_b128 v102, a[240:243] offset:0\n\t"
        "ds_write_b128 v102, a[244:247] offset:4096\n\t"
        "ds_write_b128 v116, a[248:251] offset:8192\n\t"
        "s_waitcnt lgkmcnt(0)\n\t"
        "s_barrier\n\t"
        "v_mfma_f32_16x16x32_bf16 v[8:11], a[72:75], v[72:75], v[8:11]\n\t"
        "ds_read_b64_tr_b16 a[96:97], v103 offset:0\n\t"
        "ds_read_b64_tr_b16 a[98:99], v103 offset:512\n\t"
        "v_mfma_f32_16x16x32_bf16 v[12:15], a[76:79], v[72:75], v[12:15]\n\t"
        "ds_read_b64_tr_b16 a[100:101], v103 offset:8\n\t"
        "ds_read_b64_tr_b16 a[102:103], v103 offset:520\n\t"
        "v_mfma_f32_16x16x32_bf16 v[16:19], v[72:75], a[80:83], v[16:19]\n\t"
        "ds_read_b64_tr_b16 a[104:105], v104 offset:0\n\t"
        "ds_read_b64_tr_b16 a[106:107], v104 offset:256\n\t"
        "v_mfma_f32_16x16x32_bf16 v[20:23], a[72:75], v[76:79], v[20:23]\n\t"
        "s_waitcnt lgkmcnt(5)\n\t"
        "ds_read_b64_tr_b16 a[108:109], v103 offset:4096\n\t"
        "v_mfma_f32_16x16x32_bf16 v[24:27], a[76:79], v[76:79], v[24:27]\n\t"
        "ds_read_b64_tr_b16 a[110:111], v103 offset:4608\n\t"
        "ds_read_b64_tr_b16 a[112:113], v103 offset:4104\n\t"
        "v_mfma_f32_16x16x32_bf16 v[28:31], v[76:79], a[80:83], v[28:31]\n\t"
        "ds_read_b64_tr_b16 a[114:115], v103 offset:4616\n\t"
        "ds_read_b64_tr_b16 a[116:117], v104 offset:2048\n\t"
        "v_mfma_f32_16x16x32_bf16 v[8:11], a[84:87], v[80:83], v[8:11]\n\t"
        "ds_read_b64_tr_b16 a[118:119], v104 offset:2304\n\t"
        "ds_read_b128 v[56:59], v105 offset:32768\n\t"
        "v_mfma_f32_16x16x32_bf16 v[12:15], a[88:91], v[80:83], v[12:15]\n\t"
        "ds_read_b128 v[60:63], v105 offset:34816\n\t"
        "ds_read_b128 v[64:67], v106 offset:32768\n\t"
        "v_mfma_f32_16x16x32_bf16 v[16:19], v[80:83], a[92:95], v[16:19]\n\t"
        "ds_read_b128 v[68:71], v106 offset:34816\n\t"
        "v_mfma_f32_16x16x32_bf16 v[20:23], a[84:87], v[84:87], v[20:23]\n\t"
        "v_mfma_f32_16x16x32_bf16 v[24:27], a[88:91], v[84:87], v[24:27]\n\t"
        "v_mfma_f32_16x16x32_bf16 v[28:31], v[84:87], a[92:95], v[28:31]\n\t"
        "s_add_u32 m0, %[ld], 65536\n\t"
        "s_add_u32 %[t0], %[xo], 1024\n\t"
        "buffer_load_dwordx4 v100, %[sx], %[t0] offen lds\n\t"
        "s_add_u32 m0, %[ld], 66560\n\t"
        "s_add_u32 %[t0], %[xo], 17408\n\t"
        "buffer_load_dwordx4 v100, %[sx], %[t0] offen lds\n\t"
        "s_add_u32 m0, %[ld], 73728\n\t"
        "s_add_u32 %[t0], %[xo], 1152\n\t"
        "buffer_load_dwordx4 v100, %[sx], %[t0] offen lds\n\t"
        "s_add_u32 m0, %[ld], 74752\n\t"
        "s_add_u32 %[t0], %[xo], 17536\n\t"
        "buffer_load_dwordx4 v100, %[sx], %[t0] offen lds\n\t"
        "s_add_u32 m0, %[ld], 81920\n\t"
        "s_add_u32 %[t0], %[xo], 1280\n\t"
        "buffer_load_dwordx4 v100, %[sx], %[t0] offen lds\n\t"
        "s_add_u32 m0, %[ld], 82944\n\t"
        "s_add_u32 %[t0], %[xo], 17664\n\t"
        "buffer_load_dwordx4 v100, %[sx], %[t0] offen lds\n\t"
        "s_add_u32 m0, %[ld], 90112\n\t"
        "s_add_u32 %[t0], %[xo], 1408\n\t"
        "buffer_load_dwordx4 v100, %[sx], %[t0] offen lds\n\t"
        "s_add_u32 m0, %[ld], 91136\n\t"
        "s_add_u32 %[t0], %[xo], 17792\n\t"
        "buffer_load_dwordx4 v100, %[sx], %[t0] offen lds\n\t"
        "s_mov_b32 %[t0], 4194304\n\t"
        "buffer_load_dwordx4 v[160:163], v101, %[sq], %[t0] offen\n\t"
        "buffer_load_dwordx4 v[168:171], v112, %[sv], %[t0] offen\n\t"
        "s_mov_b32 %[t0], 4456448\n\t"
        "buffer_load_dwordx4 v[164:167], v101, %[sq], %[t0] offen\n\t"
        "s_waitcnt vmcnt(17)\n\t"
        "ds_write_b128 v102, a[252:255] offset:16384\n\t"
        "ds_write_b128 v102, v[128:131] offset:20480\n\t"
        "ds_write_b128 v116, v[132:135] offset:24576\n\t"
        "s_waitcnt lgkmcnt(0)\n\t"
        "s_barrier\n\t"
        "v_mfma_f32_16x16x32_bf16 v[8:11], a[96:99], v[56:59], v[8:11]\n\t"
        "ds_read_b64_tr_b16 a[120:121], v103 offset:16384\n\t"
        "ds_read_b64_tr_b16 a[122:123], v103 offset:16896\n\t"
        "v_mfma_f32_16x16x32_bf16 v[12:15], a[100:103], v[56:59], v[12:15]\n\t"
        "ds_read_b64_tr_b16 a[124:125], v103 offset:16392\n\t"
        "ds_read_b64_tr_b16 a[126:127], v103 offset:16904\n\t"
        "v_mfma_f32_16x16x32_bf16 v[16:19], v[56:59], a[104:107], v[16:19]\n\t"
        "ds_read_b64_tr_b16 a[128:129], v104 offset:16384\n\t"
        "ds_read_b64_tr_b16 a[130:131], v104 offset:16640\n\t"
        "v_mfma_f32_16x16x32_bf16 v[20:23], a[96:99], v[60:63], v[20:23]\n\t"
        "s_waitcnt lgkmcnt(5)\n\t"
        "ds_read_b64_tr_b16 a[132:133], v103 offset:20480\n\t"
        "v_mfma_f32_16x16x32_bf16 v[24:27], a[100:103], v[60:63], v[24:27]\n\t"
        "ds_read_b64_tr_b16 a[134:135], v103 offset:20992\n\t"
        "ds_read_b64_tr_b16 a[136:137], v103 offset:20488\n\t"
        "v_mfma_f32_16x16x32_bf16 v[28:31], v[60:63], a[104:107], v[28:31]\n\t"
        "ds_read_b64_tr_b16 a[138:139], v103 offset:21000\n\t"
        "ds_read_b64_tr_b16 a[140:141], v104 offset:18432\n\t"
        "v_mfma_f32_16x16x32_bf16 v[8:11], a[108:111], v[64:67], v[8:11]\n\t"
        "ds_read_b64_tr_b16 a[142:143], v104 offset:18688\n\t"
        "ds_read_b128 v[72:75], v105 offset:40960\n\t"
        "v_mfma_f32_16x16x32_bf16 v[12:15], a[112:115], v[64:67], v[12:15]\n\t"
        "ds_read_b128 v[76:79], v105 offset:43008\n\t"
        "ds_read_b128 v[80:83], v106 offset:40960\n\t"
        "v_mfma_f32_16x16x32_bf16 v[16:19], v[64:67], a[116:119], v[16:19]\n\t"
        "ds_read_b128 v[84:87], v106 offset:43008\n\t"
        "v_mfma_f32_16x16x32_bf16 v[20:23], a[108:111], v[68:71], v[20:23]\n\t"
        "v_mfma_f32_16x16x32_bf16 v[24:27], a[112:115], v[68:71], v[24:27]\n\t"
        "v_mfma_f32_16x16x32_bf16 v[28:31], v[68:71], a[116:119], v[28:31]\n\t"
        "s_mov_b32 %[t0], 4718592\n\t"
        "buffer_load_dwordx4 v[172:175], v101, %[sq], %[t0] offen\n\t"
        "buffer_load_dwordx4 v[180:183], v112, %[sv], %[t0] offen\n\t"
        "s_mov_b32 %[t0], 4980736\n\t"
        "buffer_load_dwordx4 v[176:179], v101, %[sq], %[t0] offen\n\t"
        "s_waitcnt vmcnt(17)\n\t"
        "ds_write_b128 v102, v[136:139] offset:0\n\t"
        "ds_write_b128 v102, v[140:143] offset:4096\n\t"
        "ds_write_b128 v116, v[144:147] offset:8192\n\t"
        "s_waitcnt lgkmcnt(0)\n\t"
        "s_barrier\n\t"
        "v_mfma_f32_16x16x32_bf16 v[8:11], a[120:123], v[72:75], v[8:11]\n\t"
        "ds_read_b64_tr_b16 a[144:145], v103 offset:0\n\t"
        "ds_read_b64_tr_b16 a[146:147], v103 offset:512\n\t"
        "v_mfma_f32_16x16x32_bf16 v[12:15], a[124:127], v[72:75], v[12:15]\n\t"
        "ds_read_b64_tr_b16 a[148:149], v103 offset:8\n\t"
        "ds_read_b64_tr_b16 a[150:151], v103 offset:520\n\t"
        "v_mfma_f32_16x16x32_bf16 v[16:19], v[72:75], a[128:131], v[16:19]\n\t"
        "ds_read_b64_tr_b16 a[152:153], v104 offset:0\n\t"
        "ds_read_b64_tr_b16 a[154:155], v104 offset:256\n\t"
        "v_mfma_f32_16x16x32_bf16 v[20:23], a[120:123], v[76:79], v[20:23]\n\t"
        "s_waitcnt lgkmcnt(5)\n\t"
        "ds_read_b64_tr_b16 a[156:157], v103 offset:4096\n\t"
        "v_mfma_f32_16x16x32_bf16 v[24:27], a[124:127], v[76:79], v[24:27]\n\t"
        "ds_read_b64_tr_b16 a[158:159], v103 offset:4608\n\t"
        "ds_read_b64_tr_b16 a[160:161], v103 offset:4104\n\t"
        "v_mfma_f32_16x16x32_bf16 v[28:31], v[76:79], a[128:131], v[28:31]\n\t"
        "ds_read_b64_tr_b16 a[162:163], v103 offset:4616\n\t"
        "ds_read_b64_tr_b16 a[164:165], v104 offset:2048\n\t"
        "v_mfma_f32_16x16x32_bf16 v[8:11], a[132:135], v[80:83], v[8:11]\n\t"
        "ds_read_b64_tr_b16 a[166:167], v104 offset:2304\n\t"
        "ds_read_b128 v[56:59], v105 offset:49152\n\t"
        "v_mfma_f32_16x16x32_bf16 v[12:15], a[136:139], v[80:83], v[12:15]\n\t"
        "ds_read_b128 v[60:63], v105 offset:51200\n\t"
        "ds_read_b128 v[64:67], v106 offset:49152\n\t"
        "v_mfma_f32_16x16x32_bf16 v[16:19], v[80:83], a[140:143], v[16:19]\n\t"
        "ds_read_b128 v[68:71], v106 offset:51200\n\t"
        "v_mfma_f32_16x16x32_bf16 v[20:23], a[132:135], v[84:87], v[20:23]\n\t"
        "v_mfma_f32_16x16x32_bf16 v[24:27], a[136:139], v[84:87], v[24:27]\n\t"
        "v_mfma_f32_16x16x32_bf16 v[28:31], v[84:87], a[140:143], v[28:31]\n\t"
        "s_mov_b32 %[t0], 5242880\n\t"
        "buffer_load_dwordx4 v[184:187], v101, %[sq], %[t0] offen\n\t"
        "buffer_load_dwordx4 v[192:195], v112, %[sv], %[t0] offen\n\t"
        "s_mov_b32 %[t0], 5505024\n\t"
        "buffer_load_dwordx4 v[188:191], v101, %[sq], %[t0] offen\n\t"
        "s_waitcnt vmcnt(17)\n\t"
        "ds_write_b128 v102, v[148:151] offset:16384\n\t"
        "ds_write_b128 v102, v[152:155] offset:20480\n\t"
        "ds_write_b128 v116, v[156:159] offset:24576\n\t"
        "s_waitcnt lgkmcnt(0)\n\t"
        "s_barrier\n\t"
        "v_mfma_f32_16x16x32_bf16 v[8:11], a[144:147], v[56:59], v[8:11]\n\t"
        "ds_read_b64_tr_b16 a[168:169], v103 offset:16384\n\t"
        "ds_read_b64_tr_b16 a[170:171], v103 offset:16896\n\t"
        "v_mfma_f32_16x16x32_bf16 v[12:15], a[148:151], v[56:59], v[12:15]\n\t"
        "ds_read_b64_tr_b16 a[172:173], v103 offset:16392\n\t"
        "ds_read_b64_tr_b16 a[174:175], v103 offset:16904\n\t"
        "v_mfma_f32_16x16x32_bf16 v[16:19], v[56:59], a[152:155], v[16:19]\n\t"
        "ds_read_b64_tr_b16 a[176:177], v104 offset:16384\n\t"
        "ds_read_b64_tr_b16 a[178:179], v104 offset:16640\n\t"
        "v_mfma_f32_16x16x32_bf16 v[20:23], a[144:147], v[60:63], v[20:23]\n\t"
        "s_waitcnt lgkmcnt(5)\n\t"
        "ds_read_b64_tr_b16 a[180:181], v103 offset:20480\n\t"
        "v_mfma_f32_16x16x32_bf16 v[24:27], a[148:151], v[60:63], v[24:27]\n\t"
        "ds_read_b64_tr_b16 a[182:183], v103 offset:20992\n\t"
        "ds_read_b64_tr_b16 a[184:185], v103 offset:20488\n\t"
        "v_mfma_f32_16x16x32_bf16 v[28:31], v[60:63], a[152:155], v[28:31]\n\t"
        "ds_read_b64_tr_b16 a[186:187], v103 offset:21000\n\t"
        "ds_read_b64_tr_b16 a[188:189], v104 offset:18432\n\t"
        "v_mfma_f32_16x16x32_bf16 v[8:11], a[156:159], v[64:67], v[8:11]\n\t"
        "ds_read_b64_tr_b16 a[190:191], v104 offset:18688\n\t"
        "ds_read_b128 v[72:75], v105 offset:57344\n\t"
        "v_mfma_f32_16x16x32_bf16 v[12:15], a[160:163], v[64:67], v[12:15]\n\t"
        "ds_read_b128 v[76:79], v105 offset:59392\n\t"
        "ds_read_b128 v[80:83], v106 offset:57344\n\t"
        "v_mfma_f32_16x16x32_bf16 v[16:19], v[64:67], a[164:167], v[16:19]\n\t"
        "ds_read_b128 v[84:87], v106 offset:59392\n\t"
        "v_mfma_f32_16x16x32_bf16 v[20:23], a[156:159], v[68:71], v[20:23]\n\t"
        "v_mfma_f32_16x16x32_bf16 v[24:27], a[160:163], v[68:71], v[24:27]\n\t"
        "v_mfma_f32_16x16x32_bf16 v[28:31], v[68:71], a[164:167], v[28:31]\n\t"
        "s_mov_b32 %[t0], 5767168\n\t"
        "buffer_load_dwordx4 v[196:199], v101, %[sq], %[t0] offen\n\t"
        "buffer_load_dwordx4 v[204:207], v112, %[sv], %[t0] offen\n\t"
        "s_mov_b32 %[t0], 6029312\n\t"
        "buffer_load_dwordx4 v[200:203], v101, %[sq], %[t0] offen\n\t"
        "s_waitcnt vmcnt(9)\n\t"
        "ds_write_b128 v102, v[160:163] offset:0\n\t"
        "ds_write_b128 v102, v[164:167] offset:4096\n\t"
        "ds_write_b128 v116, v[168:171] offset:8192\n\t"
        "s_waitcnt lgkmcnt(0)\n\t"
        "s_barrier\n\t"
        "v_mfma_f32_16x16x32_bf16 v[8:11], a[168:171], v[72:75], v[8:11]\n\t"
        "ds_read_b64_tr_b16 a[192:193], v103 offset:0\n\t"
        "ds_read_b64_tr_b16 a[194:195], v103 offset:512\n\t"
        "v_mfma_f32_16x16x32_bf16 v[12:15], a[172:175], v[72:75], v[12:15]\n\t"
        "ds_read_b64_tr_b16 a[196:197], v103 offset:8\n\t"
        "ds_read_b64_tr_b16 a[198:199], v103 offset:520\n\t"
        "v_mfma_f32_16x16x32_bf16 v[16:19], v[72:75], a[176:179], v[16:19]\n\t"
        "ds_read_b64_tr_b16 a[200:201], v104 offset:0\n\t"
        "ds_read_b64_tr_b16 a[202:203], v104 offset:256\n\t"
        "v_mfma_f32_16x16x32_bf16 v[20:23], a[168:171], v[76:79], v[20:23]\n\t"
        "s_waitcnt lgkmcnt(5)\n\t"
        "ds_read_b64_tr_b16 a[204:205], v103 offset:4096\n\t"
        "v_mfma_f32_16x16x32_bf16 v[24:27], a[172:175], v[76:79], v[24:27]\n\t"
        "ds_read_b64_tr_b16 a[206:207], v103 offset:4608\n\t"
        "ds_read_b64_tr_b16 a[208:209], v103 offset:4104\n\t"
        "v_mfma_f32_16x16x32_bf16 v[28:31], v[76:79], a[176:179], v[28:31]\n\t"
        "ds_read_b64_tr_b16 a[210:211], v103 offset:4616\n\t"
        "ds_read_b64_tr_b16 a[212:213], v104 offset:2048\n\t"
        "v_mfma_f32_16x16x32_bf16 v[8:11], a[180:183], v[80:83], v[8:11]\n\t"
        "ds_read_b64_tr_b16 a[214:215], v104 offset:2304\n\t"
        "ds_read_b128 v[56:59], v107 offset:0\n\t"
        "v_mfma_f32_16x16x32_bf16 v[12:15], a[184:187], v[80:83], v[12:15]\n\t"
        "ds_read_b128 v[60:63], v107 offset:2048\n\t"
        "ds_read_b128 v[64:67], v108 offset:0\n\t"
        "v_mfma_f32_16x16x32_bf16 v[16:19], v[80:83], a[188:191], v[16:19]\n\t"
        "ds_read_b128 v[68:71], v108 offset:2048\n\t"
        "v_mfma_f32_16x16x32_bf16 v[20:23], a[180:183], v[84:87], v[20:23]\n\t"
        "v_mfma_f32_16x16x32_bf16 v[24:27], a[184:187], v[84:87], v[24:27]\n\t"
        "v_mfma_f32_16x16x32_bf16 v[28:31], v[84:87], a[188:191], v[28:31]\n\t"
        "s_add_u32 m0, %[ld], 98304\n\t"
        "s_add_u32 %[t0], %[xo], 1536\n\t"
        "buffer_load_dwordx4 v100, %[sx], %[t0] offen lds\n\t"
        "s_add_u32 m0, %[ld], 99328\n\t"
        "s_add_u32 %[t0], %[xo], 17920\n\t"
        "buffer_load_dwordx4 v100, %[sx], %[t0] offen lds\n\t"
        "s_add_u32 m0, %[ld], 106496\n\t"
        "s_add_u32 %[t0], %[xo], 1664\n\t"
        "buffer_load_dwordx4 v100, %[sx], %[t0] offen lds\n\t"
        "s_add_u32 m0, %[ld], 107520\n\t"
        "s_add_u32 %[t0], %[xo], 18048\n\t"
        "buffer_load_dwordx4 v100, %[sx], %[t0] offen lds\n\t"
        "s_add_u32 m0, %[ld], 114688\n\t"
        "s_add_u32 %[t0], %[xo], 1792\n\t"
        "buffer_load_dwordx4 v100, %[sx], %[t0] offen lds\n\t"
        "s_add_u32 m0, %[ld], 115712\n\t"
        "s_add_u32 %[t0], %[xo], 18176\n\t"
        "buffer_load_dwordx4 v100, %[sx], %[t0] offen lds\n\t"
        "s_add_u32 m0, %[ld], 122880\n\t"
        "s_add_u32 %[t0], %[xo], 1920\n\t"
        "buffer_load_dwordx4 v100, %[sx], %[t0] offen lds\n\t"
        "s_add_u32 m0, %[ld], 123904\n\t"
        "s_add_u32 %[t0], %[xo], 18304\n\t"
        "buffer_load_dwordx4 v100, %[sx], %[t0] offen lds\n\t"
        "s_mov_b32 %[t0], 6291456\n\t"
        "buffer_load_dwordx4 v[208:211], v101, %[sq], %[t0] offen\n\t"
        "buffer_load_dwordx4 v[216:219], v112, %[sv], %[t0] offen\n\t"
        "s_mov_b32 %[t0], 6553600\n\t"
        "buffer_load_dwordx4 v[212:215], v101, %[sq], %[t0] offen\n\t"
        "s_waitcnt vmcnt(17)\n\t"
        "ds_write_b128 v102, v[172:175] offset:16384\n\t"
        "ds_write_b128 v102, v[176:179] offset:20480\n\t"
        "ds_write_b128 v116, v[180:183] offset:24576\n\t"
        "s_waitcnt lgkmcnt(0)\n\t"
        "s_barrier\n\t"
        "v_mfma_f32_16x16x32_bf16 v[8:11], a[192:195], v[56:59], v[8:11]\n\t"
        "ds_read_b64_tr_b16 a[216:217], v103 offset:16384\n\t"
        "ds_read_b64_tr_b16 a[218:219], v103 offset:16896\n\t"
        "v_mfma_f32_16x16x32_bf16 v[12:15], a[196:199], v[56:59], v[12:15]\n\t"
        "ds_read_b64_tr_b16 a[220:221], v103 offset:16392\n\t"
        "ds_read_b64_tr_b16 a[222:223], v103 offset:16904\n\t"
        "v_mfma_f32_16x16x32_bf16 v[16:19], v[56:59], a[200:203], v[16:19]\n\t"
        "ds_read_b64_tr_b16 a[224:225], v104 offset:16384\n\t"
        "ds_read_b64_tr_b16 a[226:227], v104 offset:16640\n\t"
        "v_mfma_f32_16x16x32_bf16 v[20:23], a[192:195], v[60:63], v[20:23]\n\t"
        "s_waitcnt lgkmcnt(5)\n\t"
        "ds_read_b64_tr_b16 a[228:229], v103 offset:20480\n\t"
        "v_mfma_f32_16x16x32_bf16 v[24:27], a[196:199], v[60:63], v[24:27]\n\t"
        "ds_read_b64_tr_b16 a[230:231], v103 offset:20992\n\t"
        "ds_read_b64_tr_b16 a[232:233], v103 offset:20488\n\t"
        "v_mfma_f32_16x16x32_bf16 v[28:31], v[60:63], a[200:203], v[28:31]\n\t"
        "ds_read_b64_tr_b16 a[234:235], v103 offset:21000\n\t"
        "ds_read_b64_tr_b16 a[236:237], v104 offset:18432\n\t"
        "v_mfma_f32_16x16x32_bf16 v[8:11], a[204:207], v[64:67], v[8:11]\n\t"
        "ds_read_b64_tr_b16 a[238:239], v104 offset:18688\n\t"
        "ds_read_b128 v[72:75], v107 offset:8192\n\t"
        "v_mfma_f32_16x16x32_bf16 v[12:15], a[208:211], v[64:67], v[12:15]\n\t"
        "ds_read_b128 v[76:79], v107 offset:10240\n\t"
        "ds_read_b128 v[80:83], v108 offset:8192\n\t"
        "v_mfma_f32_16x16x32_bf16 v[16:19], v[64:67], a[212:215], v[16:19]\n\t"
        "ds_read_b128 v[84:87], v108 offset:10240\n\t"
        "v_mfma_f32_16x16x32_bf16 v[20:23], a[204:207], v[68:71], v[20:23]\n\t"
        "v_mfma_f32_16x16x32_bf16 v[24:27], a[208:211], v[68:71], v[24:27]\n\t"
        "v_mfma_f32_16x16x32_bf16 v[28:31], v[68:71], a[212:215], v[28:31]\n\t"
        "s_mov_b32 %[t0], 6815744\n\t"
        "buffer_load_dwordx4 v[220:223], v101, %[sq], %[t0] offen\n\t"
        "buffer_load_dwordx4 v[228:231], v112, %[sv], %[t0] offen\n\t"
        "s_mov_b32 %[t0], 7077888\n\t"
        "buffer_load_dwordx4 v[224:227], v101, %[sq], %[t0] offen\n\t"
        "s_waitcnt vmcnt(17)\n\t"
        "ds_write_b128 v102, v[184:187] offset:0\n\t"
        "ds_write_b128 v102, v[188:191] offset:4096\n\t"
        "ds_write_b128 v116, v[192:195] offset:8192\n\t"
        "s_waitcnt lgkmcnt(0)\n\t"
        "s_barrier\n\t"
        "v_mfma_f32_16x16x32_bf16 v[8:11], a[216:219], v[72:75], v[8:11]\n\t"
        "ds_read_b64_tr_b16 a[240:241], v103 offset:0\n\t"
        "ds_read_b64_tr_b16 a[242:243], v103 offset:512\n\t"
        "v_mfma_f32_16x16x32_bf16 v[12:15], a[220:223], v[72:75], v[12:15]\n\t"
        "ds_read_b64_tr_b16 a[244:245], v103 offset:8\n\t"
        "ds_read_b64_tr_b16 a[246:247], v103 offset:520\n\t"
        "v_mfma_f32_16x16x32_bf16 v[16:19], v[72:75], a[224:227], v[16:19]\n\t"
        "ds_read_b64_tr_b16 a[248:249], v104 offset:0\n\t"
        "ds_read_b64_tr_b16 a[250:251], v104 offset:256\n\t"
        "v_mfma_f32_16x16x32_bf16 v[20:23], a[216:219], v[76:79], v[20:23]\n\t"
        "s_waitcnt lgkmcnt(5)\n\t"
        "ds_read_b64_tr_b16 a[252:253], v103 offset:4096\n\t"
        "v_mfma_f32_16x16x32_bf16 v[24:27], a[220:223], v[76:79], v[24:27]\n\t"
        "ds_read_b64_tr_b16 a[254:255], v103 offset:4608\n\t"
        "ds_read_b64_tr_b16 v[128:129], v103 offset:4104\n\t"
        "v_mfma_f32_16x16x32_bf16 v[28:31], v[76:79], a[224:227], v[28:31]\n\t"
        "ds_read_b64_tr_b16 v[130:131], v103 offset:4616\n\t"
        "ds_read_b64_tr_b16 v[132:133], v104 offset:2048\n\t"
        "v_mfma_f32_16x16x32_bf16 v[8:11], a[228:231], v[80:83], v[8:11]\n\t"
        "ds_read_b64_tr_b16 v[134:135], v104 offset:2304\n\t"
        "ds_read_b128 v[56:59], v107 offset:16384\n\t"
        "v_mfma_f32_16x16x32_bf16 v[12:15], a[232:235], v[80:83], v[12:15]\n\t"
        "ds_read_b128 v[60:63], v107 offset:18432\n\t"
        "ds_read_b128 v[64:67], v108 offset:16384\n\t"
        "v_mfma_f32_16x16x32_bf16 v[16:19], v[80:83], a[236:239], v[16:19]\n\t"
        "ds_read_b128 v[68:71], v108 offset:18432\n\t"
        "v_mfma_f32_16x16x32_bf16 v[20:23], a[228:231], v[84:87], v[20:23]\n\t"
        "v_mfma_f32_16x16x32_bf16 v[24:27], a[232:235], v[84:87], v[24:27]\n\t"
        "v_mfma_f32_16x16x32_bf16 v[28:31], v[84:87], a[236:239], v[28:31]\n\t"
        "s_mov_b32 %[t0], 7340032\n\t"
        "buffer_load_dwordx4 v[232:235], v101, %[sq], %[t0] offen\n\t"
        "buffer_load_dwordx4 v[240:243], v112, %[sv], %[t0] offen\n\t"
        "s_mov_b32 %[t0], 7602176\n\t"
        "buffer_load_dwordx4 v[236:239], v101, %[sq], %[t0] offen\n\t"
        "s_waitcnt vmcnt(17)\n\t"
        "ds_write_b128 v102, v[196:199] offset:16384\n\t"
        "ds_write_b128 v102, v[200:203] offset:20480\n\t"
        "ds_write_b128 v116, v[204:207] offset:24576\n\t"
        "s_waitcnt lgkmcnt(0)\n\t"
        "s_barrier\n\t"
        "v_mfma_f32_16x16x32_bf16 v[8:11], a[240:243], v[56:59], v[8:11]\n\t"
        "ds_read_b64_tr_b16 v[136:137], v103 offset:16384\n\t"
        "ds_read_b64_tr_b16 v[138:139], v103 offset:16896\n\t"
        "v_mfma_f32_16x16x32_bf16 v[12:15], a[244:247], v[56:59], v[12:15]\n\t"
        "ds_read_b64_tr_b16 v[140:141], v103 offset:16392\n\t"
        "ds_read_b64_tr_b16 v[142:143], v103 offset:16904\n\t"
        "v_mfma_f32_16x16x32_bf16 v[16:19], v[56:59], a[248:251], v[16:19]\n\t"
        "ds_read_b64_tr_b16 v[144:145], v104 offset:16384\n\t"
        "ds_read_b64_tr_b16 v[146:147], v104 offset:16640\n\t"
        "v_mfma_f32_16x16x32_bf16 v[20:23], a[240:243], v[60:63], v[20:23]\n\t"
        "s_waitcnt lgkmcnt(5)\n\t"
        "ds_read_b64_tr_b16 v[148:149], v103 offset:20480\n\t"
        "v_mfma_f32_16x16x32_bf16 v[24:27], a[244:247], v[60:63], v[24:27]\n\t"
        "ds_read_b64_tr_b16 v[150:151], v103 offset:20992\n\t"
        "ds_read_b64_tr_b16 v[152:153], v103 offset:20488\n\t"
        "v_mfma_f32_16x16x32_bf16 v[28:31], v[60:63], a[248:251], v[28:31]\n\t"
        "ds_read_b64_tr_b16 v[154:155], v103 offset:21000\n\t"
        "ds_read_b64_tr_b16 v[156:157], v104 offset:18432\n\t"
        "v_mfma_f32_16x16x32_bf16 v[8:11], a[252:255], v[64:67], v[8:11]\n\t"
        "ds_read_b64_tr_b16 v[158:159], v104 offset:18688\n\t"
        "ds_read_b128 v[72:75], v107 offset:24576\n\t"
        "v_mfma_f32_16x16x32_bf16 v[12:15], v[128:131], v[64:67], v[12:15]\n\t"
        "ds_read_b128 v[76:79], v107 offset:26624\n\t"
        "ds_read_b128 v[80:83], v108 offset:24576\n\t"
        "v_mfma_f32_16x16x32_bf16 v[16:19], v[64:67], v[132:135], v[16:19]\n\t"
        "ds_read_b128 v[84:87], v108 offset:26624\n\t"
        "v_mfma_f32_16x16x32_bf16 v[20:23], a[252:255], v[68:71], v[20:23]\n\t"
        "v_mfma_f32_16x16x32_bf16 v[24:27], v[128:131], v[68:71], v[24:27]\n\t"
        "v_mfma_f32_16x16x32_bf16 v[28:31], v[68:71], v[132:135], v[28:31]\n\t"
        "s_mov_b32 %[t0], 7864320\n\t"
        "buffer_load_dwordx4 v[244:247], v101, %[sq], %[t0] offen\n\t"
        "buffer_load_dwordx4 v[252:255], v112, %[sv], %[t0] offen\n\t"
        "s_mov_b32 %[t0], 8126464\n\t"
        "buffer_load_dwordx4 v[248:251], v101, %[sq], %[t0] offen\n\t"
        "s_waitcnt vmcnt(9)\n\t"
        "ds_write_b128 v102, v[208:211] offset:0\n\t"
        "ds_write_b128 v102, v[212:215] offset:4096\n\t"
        "ds_write_b128 v116, v[216:219] offset:8192\n\t"
        "s_waitcnt lgkmcnt(0)\n\t"
        "s_barrier\n\t"
        "v_mfma_f32_16x16x32_bf16 v[8:11], v[136:139], v[72:75], v[8:11]\n\t"
        "ds_read_b64_tr_b16 v[160:161], v103 offset:0\n\t"
        "ds_read_b64_tr_b16 v[162:163], v103 offset:512\n\t"
        "v_mfma_f32_16x16x32_bf16 v[12:15], v[140:143], v[72:75], v[12:15]\n\t"
        "ds_read_b64_tr_b16 v[164:165], v103 offset:8\n\t"
        "ds_read_b64_tr_b16 v[166:167], v103 offset:520\n\t"
        "v_mfma_f32_16x16x32_bf16 v[16:19], v[72:75], v[144:147], v[16:19]\n\t"
        "ds_read_b64_tr_b16 v[168:169], v104 offset:0\n\t"
        "ds_read_b64_tr_b16 v[170:171], v104 offset:256\n\t"
        "v_mfma_f32_16x16x32_bf16 v[20:23], v[136:139], v[76:79], v[20:23]\n\t"
        "s_waitcnt lgkmcnt(5)\n\t"
        "ds_read_b64_tr_b16 v[172:173], v103 offset:4096\n\t"
        "v_mfma_f32_16x16x32_bf16 v[24:27], v[140:143], v[76:79], v[24:27]\n\t"
        "ds_read_b64_tr_b16 v[174:175], v103 offset:4608\n\t"
        "ds_read_b64_tr_b16 v[176:177], v103 offset:4104\n\t"
        "v_mfma_f32_16x16x32_bf16 v[28:31], v[76:79], v[144:147], v[28:31]\n\t"
        "ds_read_b64_tr_b16 v[178:179], v103 offset:4616\n\t"
        "ds_read_b64_tr_b16 v[180:181], v104 offset:2048\n\t"
        "v_mfma_f32_16x16x32_bf16 v[8:11], v[148:151], v[80:83], v[8:11]\n\t"
        "ds_read_b64_tr_b16 v[182:183], v104 offset:2304\n\t"
        "ds_read_b128 v[56:59], v107 offset:32768\n\t"
        "v_mfma_f32_16x16x32_bf16 v[12:15], v[152:155], v[80:83], v[12:15]\n\t"
        "ds_read_b128 v[60:63], v107 offset:34816\n\t"
        "ds_read_b128 v[64:67], v108 offset:32768\n\t"
        "v_mfma_f32_16x16x32_bf16 v[16:19], v[80:83], v[156:159], v[16:19]\n\t"
        "ds_read_b128 v[68:71], v108 offset:34816\n\t"
        "v_mfma_f32_16x16x32_bf16 v[20:23], v[148:151], v[84:87], v[20:23]\n\t"
        "v_mfma_f32_16x16x32_bf16 v[24:27], v[152:155], v[84:87], v[24:27]\n\t"
        "v_mfma_f32_16x16x32_bf16 v[28:31], v[84:87], v[156:159], v[28:31]\n\t"
        "s_mov_b32 %[xo], 0x20000\n\t"
        "s_add_u32 m0, %[ld], 0\n\t"
        "s_add_u32 %[t0], %[xo], 0\n\t"
        "buffer_load_dwordx4 v100, %[sx], %[t0] offen lds\n\t"
        "s_add_u32 m0, %[ld], 1024\n\t"
        "s_add_u32 %[t0], %[xo], 16384\n\t"
        "buffer_load_dwordx4 v100, %[sx], %[t0] offen lds\n\t"
        "s_add_u32 m0, %[ld], 8192\n\t"
        "s_add_u32 %[t0], %[xo], 128\n\t"
        "buffer_load_dwordx4 v100, %[sx], %[t0] offen lds\n\t"
        "s_add_u32 m0, %[ld], 9216\n\t"
        "s_add_u32 %[t0], %[xo], 16512\n\t"
        "buffer_load_dwordx4 v100, %[sx], %[t0] offen lds\n\t"
        "s_add_u32 m0, %[ld], 16384\n\t"
        "s_add_u32 %[t0], %[xo], 256\n\t"
        "buffer_load_dwordx4 v100, %[sx], %[t0] offen lds\n\t"
        "s_add_u32 m0, %[ld], 17408\n\t"
        "s_add_u32 %[t0], %[xo], 16640\n\t"
        "buffer_load_dwordx4 v100, %[sx], %[t0] offen lds\n\t"
        "s_add_u32 m0, %[ld], 24576\n\t"
        "s_add_u32 %[t0], %[xo], 384\n\t"
        "buffer_load_dwordx4 v100, %[sx], %[t0] offen lds\n\t"
        "s_add_u32 m0, %[ld], 25600\n\t"
        "s_add_u32 %[t0], %[xo], 16768\n\t"
        "buffer_load_dwordx4 v100, %[sx], %[t0] offen lds\n\t"
        "s_mov_b32 %[xo], 0\n\t"
        "s_waitcnt vmcnt(14)\n\t"
        "ds_write_b128 v102, v[220:223] offset:16384\n\t"
        "ds_write_b128 v102, v[224:227] offset:20480\n\t"
        "ds_write_b128 v116, v[228:231] offset:24576\n\t"
        "s_waitcnt lgkmcnt(0)\n\t"
        "s_barrier\n\t"
        "v_mfma_f32_16x16x32_bf16 v[8:11], v[160:163], v[56:59], v[8:11]\n\t"
        "ds_read_b64_tr_b16 v[184:185], v103 offset:16384\n\t"
        "ds_read_b64_tr_b16 v[186:187], v103 offset:16896\n\t"
        "v_mfma_f32_16x16x32_bf16 v[12:15], v[164:167], v[56:59], v[12:15]\n\t"
        "ds_read_b64_tr_b16 v[188:189], v103 offset:16392\n\t"
        "ds_read_b64_tr_b16 v[190:191], v103 offset:16904\n\t"
        "v_mfma_f32_16x16x32_bf16 v[16:19], v[56:59], v[168:171], v[16:19]\n\t"
        "ds_read_b64_tr_b16 v[192:193], v104 offset:16384\n\t"
        "ds_read_b64_tr_b16 v[194:195], v104 offset:16640\n\t"
        "v_mfma_f32_16x16x32_bf16 v[20:23], v[160:163], v[60:63], v[20:23]\n\t"
        "s_waitcnt lgkmcnt(5)\n\t"
        "ds_read_b64_tr_b16 v[196:197], v103 offset:20480\n\t"
        "v_mfma_f32_16x16x32_bf16 v[24:27], v[164:167], v[60:63], v[24:27]\n\t"
        "ds_read_b64_tr_b16 v[198:199], v103 offset:20992\n\t"
        "ds_read_b64_tr_b16 v[200:201], v103 offset:20488\n\t"
        "v_mfma_f32_16x16x32_bf16 v[28:31], v[60:63], v[168:171], v[28:31]\n\t"
        "ds_read_b64_tr_b16 v[202:203], v103 offset:21000\n\t"
        "ds_read_b64_tr_b16 v[204:205], v104 offset:18432\n\t"
        "v_mfma_f32_16x16x32_bf16 v[8:11], v[172:175], v[64:67], v[8:11]\n\t"
        "ds_read_b64_tr_b16 v[206:207], v104 offset:18688\n\t"
        "ds_read_b128 v[72:75], v107 offset:40960\n\t"
        "v_mfma_f32_16x16x32_bf16 v[12:15], v[176:179], v[64:67], v[12:15]\n\t"
        "ds_read_b128 v[76:79], v107 offset:43008\n\t"
        "ds_read_b128 v[80:83], v108 offset:40960\n\t"
        "v_mfma_f32_16x16x32_bf16 v[16:19], v[64:67], v[180:183], v[16:19]\n\t"
        "ds_read_b128 v[84:87], v108 offset:43008\n\t"
        "v_mfma_f32_16x16x32_bf16 v[20:23], v[172:175], v[68:71], v[20:23]\n\t"
        "v_mfma_f32_16x16x32_bf16 v[24:27], v[176:179], v[68:71], v[24:27]\n\t"
        "v_mfma_f32_16x16x32_bf16 v[28:31], v[68:71], v[180:183], v[28:31]\n\t"
        "s_waitcnt vmcnt(11)\n\t"
        "ds_write_b128 v102, v[232:235] offset:0\n\t"
        "ds_write_b128 v102, v[236:239] offset:4096\n\t"
        "ds_write_b128 v116, v[240:243] offset:8192\n\t"
        "s_waitcnt lgkmcnt(0)\n\t"
        "s_barrier\n\t"
        "v_mfma_f32_16x16x32_bf16 v[8:11], v[184:187], v[72:75], v[8:11]\n\t"
        "ds_read_b64_tr_b16 v[208:209], v103 offset:0\n\t"
        "ds_read_b64_tr_b16 v[210:211], v103 offset:512\n\t"
        "v_mfma_f32_16x16x32_bf16 v[12:15], v[188:191], v[72:75], v[12:15]\n\t"
        "ds_read_b64_tr_b16 v[212:213], v103 offset:8\n\t"
        "ds_read_b64_tr_b16 v[214:215], v103 offset:520\n\t"
        "v_mfma_f32_16x16x32_bf16 v[16:19], v[72:75], v[192:195], v[16:19]\n\t"
        "ds_read_b64_tr_b16 v[216:217], v104 offset:0\n\t"
        "ds_read_b64_tr_b16 v[218:219], v104 offset:256\n\t"
        "v_mfma_f32_16x16x32_bf16 v[20:23], v[184:187], v[76:79], v[20:23]\n\t"
        "s_waitcnt lgkmcnt(5)\n\t"
        "ds_read_b64_tr_b16 v[220:221], v103 offset:4096\n\t"
        "v_mfma_f32_16x16x32_bf16 v[24:27], v[188:191], v[76:79], v[24:27]\n\t"
        "ds_read_b64_tr_b16 v[222:223], v103 offset:4608\n\t"
        "ds_read_b64_tr_b16 v[224:225], v103 offset:4104\n\t"
        "v_mfma_f32_16x16x32_bf16 v[28:31], v[76:79], v[192:195], v[28:31]\n\t"
        "ds_read_b64_tr_b16 v[226:227], v103 offset:4616\n\t"
        "ds_read_b64_tr_b16 v[228:229], v104 offset:2048\n\t"
        "v_mfma_f32_16x16x32_bf16 v[8:11], v[196:199], v[80:83], v[8:11]\n\t"
        "ds_read_b64_tr_b16 v[230:231], v104 offset:2304\n\t"
        "ds_read_b128 v[56:59], v107 offset:49152\n\t"
        "v_mfma_f32_16x16x32_bf16 v[12:15], v[200:203], v[80:83], v[12:15]\n\t"
        "ds_read_b128 v[60:63], v107 offset:51200\n\t"
        "ds_read_b128 v[64:67], v108 offset:49152\n\t"
        "v_mfma_f32_16x16x32_bf16 v[16:19], v[80:83], v[204:207], v[16:19]\n\t"
        "ds_read_b128 v[68:71], v108 offset:51200\n\t"
        "v_mfma_f32_16x16x32_bf16 v[20:23], v[196:199], v[84:87], v[20:23]\n\t"
        "v_mfma_f32_16x16x32_bf16 v[24:27], v[200:203], v[84:87], v[24:27]\n\t"
        "v_mfma_f32_16x16x32_bf16 v[28:31], v[84:87], v[204:207], v[28:31]\n\t"
        "s_waitcnt vmcnt(8)\n\t"
        "ds_write_b128 v102, v[244:247] offset:16384\n\t"
        "ds_write_b128 v102, v[248:251] offset:20480\n\t"
        "ds_write_b128 v116, v[252:255] offset:24576\n\t"
        "s_waitcnt lgkmcnt(0)\n\t"
        "s_barrier\n\t"
        "v_mfma_f32_16x16x32_bf16 v[8:11], v[208:211], v[56:59], v[8:11]\n\t"
        "ds_read_b64_tr_b16 v[232:233], v103 offset:16384\n\t"
        "ds_read_b64_tr_b16 v[234:235], v103 offset:16896\n\t"
        "v_mfma_f32_16x16x32_bf16 v[12:15], v[212:215], v[56:59], v[12:15]\n\t"
        "ds_read_b64_tr_b16 v[236:237], v103 offset:16392\n\t"
        "ds_read_b64_tr_b16 v[238:239], v103 offset:16904\n\t"
        "v_mfma_f32_16x16x32_bf16 v[16:19], v[56:59], v[216:219], v[16:19]\n\t"
        "ds_read_b64_tr_b16 v[240:241], v104 offset:16384\n\t"
        "ds_read_b64_tr_b16 v[242:243], v104 offset:16640\n\t"
        "v_mfma_f32_16x16x32_bf16 v[20:23], v[208:211], v[60:63], v[20:23]\n\t"
        "s_waitcnt lgkmcnt(5)\n\t"
        "ds_read_b64_tr_b16 v[244:245], v103 offset:20480\n\t"
        "v_mfma_f32_16x16x32_bf16 v[24:27], v[212:215], v[60:63], v[24:27]\n\t"
        "ds_read_b64_tr_b16 v[246:247], v103 offset:20992\n\t"
        "ds_read_b64_tr_b16 v[248:249], v103 offset:20488\n\t"
        "v_mfma_f32_16x16x32_bf16 v[28:31], v[60:63], v[216:219], v[28:31]\n\t"
        "ds_read_b64_tr_b16 v[250:251], v103 offset:21000\n\t"
        "ds_read_b64_tr_b16 v[252:253], v104 offset:18432\n\t"
        "v_mfma_f32_16x16x32_bf16 v[8:11], v[220:223], v[64:67], v[8:11]\n\t"
        "ds_read_b64_tr_b16 v[254:255], v104 offset:18688\n\t"
        "ds_read_b128 v[72:75], v107 offset:57344\n\t"
        "v_mfma_f32_16x16x32_bf16 v[12:15], v[224:227], v[64:67], v[12:15]\n\t"
        "ds_read_b128 v[76:79], v107 offset:59392\n\t"
        "ds_read_b128 v[80:83], v108 offset:57344\n\t"
        "v_mfma_f32_16x16x32_bf16 v[16:19], v[64:67], v[228:231], v[16:19]\n\t"
        "ds_read_b128 v[84:87], v108 offset:59392\n\t"
        "v_mfma_f32_16x16x32_bf16 v[20:23], v[220:223], v[68:71], v[20:23]\n\t"
        "v_mfma_f32_16x16x32_bf16 v[24:27], v[224:227], v[68:71], v[24:27]\n\t"
        "v_mfma_f32_16x16x32_bf16 v[28:31], v[68:71], v[228:231], v[28:31]\n\t"
        "s_waitcnt vmcnt(0) lgkmcnt(0)\n\t"
        "s_barrier\n\t"
        "v_mfma_f32_16x16x32_bf16 v[8:11], v[232:235], v[72:75], v[8:11]\n\t"
        "v_mfma_f32_16x16x32_bf16 v[12:15], v[236:239], v[72:75], v[12:15]\n\t"
        "v_mfma_f32_16x16x32_bf16 v[16:19], v[72:75], v[240:243], v[16:19]\n\t"
        "v_mfma_f32_16x16x32_bf16 v[20:23], v[232:235], v[76:79], v[20:23]\n\t"
        "v_mfma_f32_16x16x32_bf16 v[24:27], v[236:239], v[76:79], v[24:27]\n\t"
        "v_mfma_f32_16x16x32_bf16 v[28:31], v[76:79], v[240:243], v[28:31]\n\t"
        "v_mfma_f32_16x16x32_bf16 v[8:11], v[244:247], v[80:83], v[8:11]\n\t"
        "v_mfma_f32_16x16x32_bf16 v[12:15], v[248:251], v[80:83], v[12:15]\n\t"
        "v_mfma_f32_16x16x32_bf16 v[16:19], v[80:83], v[252:255], v[16:19]\n\t"
        "v_mfma_f32_16x16x32_bf16 v[20:23], v[244:247], v[84:87], v[20:23]\n\t"
        "v_mfma_f32_16x16x32_bf16 v[24:27], v[248:251], v[84:87], v[24:27]\n\t"
        "v_mfma_f32_16x16x32_bf16 v[28:31], v[84:87], v[252:255], v[28:31]\n\t"
        "s_mov_b32 %[xo], 0x20000\n\t"
        "ds_read_b128 v[56:59], v105 offset:0\n\t"
        "ds_read_b128 v[60:63], v105 offset:2048\n\t"
        "ds_read_b128 v[64:67], v106 offset:0\n\t"
        "ds_read_b128 v[68:71], v106 offset:2048\n\t"
        "ds_read_b128 v[72:75], v105 offset:8192\n\t"
        "ds_read_b128 v[76:79], v105 offset:10240\n\t"
        "ds_read_b128 v[80:83], v106 offset:8192\n\t"
        "v_cvt_pk_bf16_f32 v88, v8, v9\n\t"
        "v_cvt_pk_bf16_f32 v89, v10, v11\n\t"
        "v_cvt_pk_bf16_f32 v90, v12, v13\n\t"
        "v_cvt_pk_bf16_f32 v91, v14, v15\n\t"
        "buffer_store_dwordx4 v[88:91], v109, %[sw], %[so] offen sc1\n\t"
        "v_cvt_pk_bf16_f32 v92, v20, v21\n\t"
        "v_cvt_pk_bf16_f32 v93, v22, v23\n\t"
        "v_cvt_pk_bf16_f32 v94, v24, v25\n\t"
        "v_cvt_pk_bf16_f32 v95, v26, v27\n\t"
        "buffer_store_dwordx4 v[92:95], v110, %[sw], %[so] offen sc1\n\t"
        "v_cvt_pk_bf16_f32 v96, v16, v17\n\t"
        "v_cvt_pk_bf16_f32 v97, v18, v19\n\t"
        "v_cvt_pk_bf16_f32 v98, v28, v29\n\t"
        "v_cvt_pk_bf16_f32 v99, v30, v31\n\t"
        "buffer_store_dwordx4 v[96:99], v111, %[sw], %[so] offen sc1\n\t"
        "s_mov_b32 %[so], 0xf0000000\n\t"
        "s_waitcnt vmcnt(0)\n\t"
        "v_mov_b32_e32 v117, %[cn]\n\t"
        "buffer_store_dword v117, v113, %[sw], 0 offen sc1\n\t"
        "s_add_u32 m0, %[ld], 32768\n\t"
        "s_add_u32 %[t0], %[xo], 512\n\t"
        "buffer_load_dwordx4 v100, %[sx], %[t0] offen lds\n\t"
        "s_add_u32 m0, %[ld], 33792\n\t"
        "s_add_u32 %[t0], %[xo], 16896\n\t"
        "buffer_load_dwordx4 v100, %[sx], %[t0] offen lds\n\t"
        "s_add_u32 m0, %[ld], 40960\n\t"
        "s_add_u32 %[t0], %[xo], 640\n\t"
        "buffer_load_dwordx4 v100, %[sx], %[t0] offen lds\n\t"
        "s_add_u32 m0, %[ld], 41984\n\t"
        "s_add_u32 %[t0], %[xo], 17024\n\t"
        "buffer_load_dwordx4 v100, %[sx], %[t0] offen lds\n\t"
        "s_add_u32 m0, %[ld], 49152\n\t"
        "s_add_u32 %[t0], %[xo], 768\n\t"
        "buffer_load_dwordx4 v100, %[sx], %[t0] offen lds\n\t"
        "s_add_u32 m0, %[ld], 50176\n\t"
        "s_add_u32 %[t0], %[xo], 17152\n\t"
        "buffer_load_dwordx4 v100, %[sx], %[t0] offen lds\n\t"
        "s_add_u32 m0, %[ld], 57344\n\t"
        "s_add_u32 %[t0], %[xo], 896\n\t"
        "buffer_load_dwordx4 v100, %[sx], %[t0] offen lds\n\t"
        "s_add_u32 m0, %[ld], 58368\n\t"
        "s_add_u32 %[t0], %[xo], 17280\n\t"
        "buffer_load_dwordx4 v100, %[sx], %[t0] offen lds\n\t"
        "s_add_u32 m0, %[ld], 65536\n\t"
        "s_add_u32 %[t0], %[xo], 1024\n\t"
        "buffer_load_dwordx4 v100, %[sx], %[t0] offen lds\n\t"
        "s_add_u32 m0, %[ld], 66560\n\t"
        "s_add_u32 %[t0], %[xo], 17408\n\t"
        "buffer_load_dwordx4 v100, %[sx], %[t0] offen lds\n\t"
        "s_add_u32 m0, %[ld], 73728\n\t"
        "s_add_u32 %[t0], %[xo], 1152\n\t"
        "buffer_load_dwordx4 v100, %[sx], %[t0] offen lds\n\t"
        "s_add_u32 m0, %[ld], 74752\n\t"
        "s_add_u32 %[t0], %[xo], 17536\n\t"
        "buffer_load_dwordx4 v100, %[sx], %[t0] offen lds\n\t"
        "s_add_u32 m0, %[ld], 81920\n\t"
        "s_add_u32 %[t0], %[xo], 1280\n\t"
        "buffer_load_dwordx4 v100, %[sx], %[t0] offen lds\n\t"
        "s_add_u32 m0, %[ld], 82944\n\t"
        "s_add_u32 %[t0], %[xo], 17664\n\t"
        "buffer_load_dwordx4 v100, %[sx], %[t0] offen lds\n\t"
        "s_add_u32 m0, %[ld], 90112\n\t"
        "s_add_u32 %[t0], %[xo], 1408\n\t"
        "buffer_load_dwordx4 v100, %[sx], %[t0] offen lds\n\t"
        "s_add_u32 m0, %[ld], 91136\n\t"
        "s_add_u32 %[t0], %[xo], 17792\n\t"
        "buffer_load_dwordx4 v100, %[sx], %[t0] offen lds\n\t"
        "s_add_u32 m0, %[ld], 98304\n\t"
        "s_add_u32 %[t0], %[xo], 1536\n\t"
        "buffer_load_dwordx4 v100, %[sx], %[t0] offen lds\n\t"
        "s_add_u32 m0, %[ld], 99328\n\t"
        "s_add_u32 %[t0], %[xo], 17920\n\t"
        "buffer_load_dwordx4 v100, %[sx], %[t0] offen lds\n\t"
        "s_add_u32 m0, %[ld], 106496\n\t"
        "s_add_u32 %[t0], %[xo], 1664\n\t"
        "buffer_load_dwordx4 v100, %[sx], %[t0] offen lds\n\t"
        "s_branch LG_mid%=\n\t"
        "LG_loop%=:\n\t"
        "s_waitcnt lgkmcnt(6)\n\t"
        "v_mfma_f32_16x16x32_bf16 v[8:11], a[0:3], v[56:59], 0\n\t"
        "v_mfma_f32_16x16x32_bf16 v[12:15], a[4:7], v[56:59], 0\n\t"
        "ds_read_b128 v[84:87], v106 offset:10240\n\t"
        "v_mfma_f32_16x16x32_bf16 v[16:19], v[56:59], a[8:11], 0\n\t"
        "s_add_u32 m0, %[ld], 107520\n\t"
        "s_add_u32 %[t0], %[xo], 18048\n\t"
        "s_waitcnt lgkmcnt(6)\n\t"
        "v_mfma_f32_16x16x32_bf16 v[20:23], a[0:3], v[60:63], 0\n\t"
        "buffer_load_dwordx4 v100, %[sx], %[t0] offen lds\n\t"
        "v_mfma_f32_16x16x32_bf16 v[24:27], a[4:7], v[60:63], 0\n\t"
        "ds_read_b128 v[56:59], v105 offset:16384\n\t"
        "v_mfma_f32_16x16x32_bf16 v[28:31], v[60:63], a[8:11], 0\n\t"
        "s_waitcnt lgkmcnt(6)\n\t"
        "v_mfma_f32_16x16x32_bf16 v[8:11], a[12:15], v[64:67], v[8:11]\n\t"
        "v_cvt_pk_bf16_f32 v88, v32, v33\n\t"
        "v_cvt_pk_bf16_f32 v89, v34, v35\n\t"
        "v_mfma_f32_16x16x32_bf16 v[12:15], a[16:19], v[64:67], v[12:15]\n\t"
        "ds_read_b128 v[60:63], v105 offset:18432\n\t"
        "v_cvt_pk_bf16_f32 v90, v36, v37\n\t"
        "v_mfma_f32_16x16x32_bf16 v[16:19], v[64:67], a[20:23], v[16:19]\n\t"
        "s_add_u32 m0, %[ld], 114688\n\t"
        "s_add_u32 %[t0], %[xo], 1792\n\t"
        "s_waitcnt lgkmcnt(6)\n\t"
        "v_mfma_f32_16x16x32_bf16 v[20:23], a[12:15], v[68:71], v[20:23]\n\t"
        "buffer_load_dwordx4 v100, %[sx], %[t0] offen lds\n\t"
        "v_mfma_f32_16x16x32_bf16 v[24:27], a[16:19], v[68:71], v[24:27]\n\t"
        "ds_read_b128 v[64:67], v106 offset:16384\n\t"
        "v_cvt_pk_bf16_f32 v91, v38, v39\n\t"
        "v_mfma_f32_16x16x32_bf16 v[28:31], v[68:71], a[20:23], v[28:31]\n\t"
        "buffer_store_dwordx4 v[88:91], v109, %[sw], %[so] offen sc1\n\t"
        "s_waitcnt lgkmcnt(6)\n\t"
        "v_mfma_f32_16x16x32_bf16 v[8:11], a[24:27], v[72:75], v[8:11]\n\t"
        "v_cvt_pk_bf16_f32 v92, v44, v45\n\t"
        "v_cvt_pk_bf16_f32 v93, v46, v47\n\t"
        "v_mfma_f32_16x16x32_bf16 v[12:15], a[28:31], v[72:75], v[12:15]\n\t"
        "ds_read_b128 v[68:71], v106 offset:18432\n\t"
        "v_cvt_pk_bf16_f32 v94, v48, v49\n\t"
        "v_mfma_f32_16x16x32_bf16 v[16:19], v[72:75], a[32:35], v[16:19]\n\t"
        "s_add_u32 m0, %[ld], 115712\n\t"
        "s_add_u32 %[t0], %[xo], 18176\n\t"
        "s_waitcnt lgkmcnt(6)\n\t"
        "v_mfma_f32_16x16x32_bf16 v[20:23], a[24:27], v[76:79], v[20:23]\n\t"
        "buffer_load_dwordx4 v100, %[sx], %[t0] offen lds\n\t"
        "v_mfma_f32_16x16x32_bf16 v[24:27], a[28:31], v[76:79], v[24:27]\n\t"
        "ds_read_b128 v[72:75], v105 offset:24576\n\t"
        "v_cvt_pk_bf16_f32 v95, v50, v51\n\t"
        "v_mfma_f32_16x16x32_bf16 v[28:31], v[76:79], a[32:35], v[28:31]\n\t"
        "buffer_store_dwordx4 v[92:95], v110, %[sw], %[so] offen sc1\n\t"
        "s_waitcnt lgkmcnt(6)\n\t"
        "v_mfma_f32_16x16x32_bf16 v[8:11], a[36:39], v[80:83], v[8:11]\n\t"
        "v_cvt_pk_bf16_f32 v96, v40, v41\n\t"
        "v_cvt_pk_bf16_f32 v97, v42, v43\n\t"
        "v_mfma_f32_16x16x32_bf16 v[12:15], a[40:43], v[80:83], v[12:15]\n\t"
        "ds_read_b128 v[76:79], v105 offset:26624\n\t"
        "v_cvt_pk_bf16_f32 v98, v52, v53\n\t"
        "v_mfma_f32_16x16x32_bf16 v[16:19], v[80:83], a[44:47], v[16:19]\n\t"
        "s_add_u32 m0, %[ld], 122880\n\t"
        "s_add_u32 %[t0], %[xo], 1920\n\t"
        "s_waitcnt lgkmcnt(6)\n\t"
        "v_mfma_f32_16x16x32_bf16 v[20:23], a[36:39], v[84:87], v[20:23]\n\t"
        "buffer_load_dwordx4 v100, %[sx], %[t0] offen lds\n\t"
        "v_mfma_f32_16x16x32_bf16 v[24:27], a[40:43], v[84:87], v[24:27]\n\t"
        "ds_read_b128 v[80:83], v106 offset:24576\n\t"
        "v_cvt_pk_bf16_f32 v99, v54, v55\n\t"
        "v_mfma_f32_16x16x32_bf16 v[28:31], v[84:87], a[44:47], v[28:31]\n\t"
        "buffer_store_dwordx4 v[96:99], v111, %[sw], %[so] offen sc1\n\t"
        "s_waitcnt lgkmcnt(6)\n\t"
        "v_mfma_f32_16x16x32_bf16 v[8:11], a[48:51], v[56:59], v[8:11]\n\t"
        "s_lshl_b32 %[so], %[cn], 19\n\t"
        "v_mfma_f32_16x16x32_bf16 v[12:15], a[52:55], v[56:59], v[12:15]\n\t"
        "ds_read_b128 v[84:87], v106 offset:26624\n\t"
        "v_mfma_f32_16x16x32_bf16 v[16:19], v[56:59], a[56:59], v[16:19]\n\t"
        "s_add_u32 m0, %[ld], 123904\n\t"
        "s_add_u32 %[t0], %[xo], 18304\n\t"
        "s_waitcnt lgkmcnt(6)\n\t"
        "v_mfma_f32_16x16x32_bf16 v[20:23], a[48:51], v[60:63], v[20:23]\n\t"
        "buffer_load_dwordx4 v100, %[sx], %[t0] offen lds\n\t"
        "v_mfma_f32_16x16x32_bf16 v[24:27], a[52:55], v[60:63], v[24:27]\n\t"
        "v_mfma_f32_16x16x32_bf16 v[28:31], v[60:63], a[56:59], v[28:31]\n\t"
        "s_waitcnt lgkmcnt(5)\n\t"
        "v_mfma_f32_16x16x32_bf16 v[8:11], a[60:63], v[64:67], v[8:11]\n\t"
        "s_waitcnt vmcnt(19) lgkmcnt(0)\n\t"
        "s_barrier\n\t"
        "s_add_u32 %[xo], %[xo], 0x20000\n\t"
        "v_mfma_f32_16x16x32_bf16 v[12:15], a[64:67], v[64:67], v[12:15]\n\t"
        "ds_read_b128 v[56:59], v105 offset:32768\n\t"
        "v_mfma_f32_16x16x32_bf16 v[16:19], v[64:67], a[68:71], v[16:19]\n\t"
        "ds_read_b128 v[60:63], v105 offset:34816\n\t"
        "s_add_u32 m0, %[ld], 0\n\t"
        "s_add_u32 %[t0], %[xo], 0\n\t"
        "v_mfma_f32_16x16x32_bf16 v[20:23], a[60:63], v[68:71], v[20:23]\n\t"
        "buffer_load_dwordx4 v100, %[sx], %[t0] offen lds\n\t"
        "v_mfma_f32_16x16x32_bf16 v[24:27], a[64:67], v[68:71], v[24:27]\n\t"
        "ds_read_b128 v[64:67], v106 offset:32768\n\t"
        "v_mfma_f32_16x16x32_bf16 v[28:31], v[68:71], a[68:71], v[28:31]\n\t"
        "v_mfma_f32_16x16x32_bf16 v[8:11], a[72:75], v[72:75], v[8:11]\n\t"
        "v_mfma_f32_16x16x32_bf16 v[12:15], a[76:79], v[72:75], v[12:15]\n\t"
        "ds_read_b128 v[68:71], v106 offset:34816\n\t"
        "v_mfma_f32_16x16x32_bf16 v[16:19], v[72:75], a[80:83], v[16:19]\n\t"
        "s_add_u32 m0, %[ld], 1024\n\t"
        "s_add_u32 %[t0], %[xo], 16384\n\t"
        "v_mfma_f32_16x16x32_bf16 v[20:23], a[72:75], v[76:79], v[20:23]\n\t"
        "buffer_load_dwordx4 v100, %[sx], %[t0] offen lds\n\t"
        "v_mfma_f32_16x16x32_bf16 v[24:27], a[76:79], v[76:79], v[24:27]\n\t"
        "ds_read_b128 v[72:75], v105 offset:40960\n\t"
        "v_mfma_f32_16x16x32_bf16 v[28:31], v[76:79], a[80:83], v[28:31]\n\t"
        "v_mfma_f32_16x16x32_bf16 v[8:11], a[84:87], v[80:83], v[8:11]\n\t"
        "v_mfma_f32_16x16x32_bf16 v[12:15], a[88:91], v[80:83], v[12:15]\n\t"
        "ds_read_b128 v[76:79], v105 offset:43008\n\t"
        "v_mfma_f32_16x16x32_bf16 v[16:19], v[80:83], a[92:95], v[16:19]\n\t"
        "s_add_u32 m0, %[ld], 8192\n\t"
        "s_add_u32 %[t0], %[xo], 128\n\t"
        "v_mfma_f32_16x16x32_bf16 v[20:23], a[84:87], v[84:87], v[20:23]\n\t"
        "buffer_load_dwordx4 v100, %[sx], %[t0] offen lds\n\t"
        "v_mfma_f32_16x16x32_bf16 v[24:27], a[88:91], v[84:87], v[24:27]\n\t"
        "ds_read_b128 v[80:83], v106 offset:40960\n\t"
        "v_mfma_f32_16x16x32_bf16 v[28:31], v[84:87], a[92:95], v[28:31]\n\t"
        "s_waitcnt lgkmcnt(6)\n\t"
        "v_mfma_f32_16x16x32_bf16 v[8:11], a[96:99], v[56:59], v[8:11]\n\t"
        "v_mfma_f32_16x16x32_bf16 v[12:15], a[100:103], v[56:59], v[12:15]\n\t"
        "ds_read_b128 v[84:87], v106 offset:43008\n\t"
        "v_mfma_f32_16x16x32_bf16 v[16:19], v[56:59], a[104:107], v[16:19]\n\t"
        "s_add_u32 m0, %[ld], 9216\n\t"
        "s_add_u32 %[t0], %[xo], 16512\n\t"
        "s_waitcnt lgkmcnt(6)\n\t"
        "v_mfma_f32_16x16x32_bf16 v[20:23], a[96:99], v[60:63], v[20:23]\n\t"
        "buffer_load_dwordx4 v100, %[sx], %[t0] offen lds\n\t"
        "v_mfma_f32_16x16x32_bf16 v[24:27], a[100:103], v[60:63], v[24:27]\n\t"
        "ds_read_b128 v[56:59], v105 offset:49152\n\t"
        "v_mfma_f32_16x16x32_bf16 v[28:31], v[60:63], a[104:107], v[28:31]\n\t"
        "s_waitcnt lgkmcnt(6)\n\t"
        "v_mfma_f32_16x16x32_bf16 v[8:11], a[108:111], v[64:67], v[8:11]\n\t"
        "v_mfma_f32_16x16x32_bf16 v[12:15], a[112:115], v[64:67], v[12:15]\n\t"
        "ds_read_b128 v[60:63], v105 offset:51200\n\t"
        "v_mfma_f32_16x16x32_bf16 v[16:19], v[64:67], a[116:119], v[16:19]\n\t"
        "s_add_u32 m0, %[ld], 16384\n\t"
        "s_add_u32 %[t0], %[xo], 256\n\t"
        "s_waitcnt lgkmcnt(6)\n\t"
        "v_mfma_f32_16x16x32_bf16 v[20:23], a[108:111], v[68:71], v[20:23]\n\t"
        "buffer_load_dwordx4 v100, %[sx], %[t0] offen lds\n\t"
        "v_mfma_f32_16x16x32_bf16 v[24:27], a[112:115], v[68:71], v[24:27]\n\t"
        "ds_read_b128 v[64:67], v106 offset:49152\n\t"
        "v_mfma_f32_16x16x32_bf16 v[28:31], v[68:71], a[116:119], v[28:31]\n\t"
        "s_waitcnt lgkmcnt(6)\n\t"
        "v_mfma_f32_16x16x32_bf16 v[8:11], a[120:123], v[72:75], v[8:11]\n\t"
        "v_mfma_f32_16x16x32_bf16 v[12:15], a[124:127], v[72:75], v[12:15]\n\t"
        "ds_read_b128 v[68:71], v106 offset:51200\n\t"
        "v_mfma_f32_16x16x32_bf16 v[16:19], v[72:75], a[128:131], v[16:19]\n\t"
        "s_add_u32 m0, %[ld], 17408\n\t"
        "s_add_u32 %[t0], %[xo], 16640\n\t"
        "s_waitcnt lgkmcnt(6)\n\t"
        "v_mfma_f32_16x16x32_bf16 v[20:23], a[120:123], v[76:79], v[20:23]\n\t"
        "buffer_load_dwordx4 v100, %[sx], %[t0] offen lds\n\t"
        "v_mfma_f32_16x16x32_bf16 v[24:27], a[124:127], v[76:79], v[24:27]\n\t"
        "ds_read_b128 v[72:75], v105 offset:57344\n\t"
        "v_mfma_f32_16x16x32_bf16 v[28:31], v[76:79], a[128:131], v[28:31]\n\t"
        "s_waitcnt lgkmcnt(6)\n\t"
        "v_mfma_f32_16x16x32_bf16 v[8:11], a[132:135], v[80:83], v[8:11]\n\t"
        "v_mfma_f32_16x16x32_bf16 v[12:15], a[136:139], v[80:83], v[12:15]\n\t"
        "ds_read_b128 v[76:79], v105 offset:59392\n\t"
        "v_mfma_f32_16x16x32_bf16 v[16:19], v[80:83], a[140:143], v[16:19]\n\t"
        "s_add_u32 m0, %[ld], 24576\n\t"
        "s_add_u32 %[t0], %[xo], 384\n\t"
        "s_waitcnt lgkmcnt(6)\n\t"
        "v_mfma_f32_16x16x32_bf16 v[20:23], a[132:135], v[84:87], v[20:23]\n\t"
        "buffer_load_dwordx4 v100, %[sx], %[t0] offen lds\n\t"
        "v_mfma_f32_16x16x32_bf16 v[24:27], a[136:139], v[84:87], v[24:27]\n\t"
        "ds_read_b128 v[80:83], v106 offset:57344\n\t"
        "v_mfma_f32_16x16x32_bf16 v[28:31], v[84:87], a[140:143], v[28:31]\n\t"
        "s_waitcnt lgkmcnt(6)\n\t"
        "v_mfma_f32_16x16x32_bf16 v[8:11], a[144:147], v[56:59], v[8:11]\n\t"
        "v_mfma_f32_16x16x32_bf16 v[12:15], a[148:151], v[56:59], v[12:15]\n\t"
        "ds_read_b128 v[84:87], v106 offset:59392\n\t"
        "v_mfma_f32_16x16x32_bf16 v[16:19], v[56:59], a[152:155], v[16:19]\n\t"
        "s_add_u32 m0, %[ld], 25600\n\t"
        "s_add_u32 %[t0], %[xo], 16768\n\t"
        "s_waitcnt lgkmcnt(6)\n\t"
        "v_mfma_f32_16x16x32_bf16 v[20:23], a[144:147], v[60:63], v[20:23]\n\t"
        "buffer_load_dwordx4 v100, %[sx], %[t0] offen lds\n\t"
        "v_mfma_f32_16x16x32_bf16 v[24:27], a[148:151], v[60:63], v[24:27]\n\t"
        "v_mfma_f32_16x16x32_bf16 v[28:31], v[60:63], a[152:155], v[28:31]\n\t"
        "s_waitcnt lgkmcnt(5)\n\t"
        "v_mfma_f32_16x16x32_bf16 v[8:11], a[156:159], v[64:67], v[8:11]\n\t"
        "s_waitcnt vmcnt(9) lgkmcnt(0)\n\t"
        "s_barrier\n\t"
        "v_mfma_f32_16x16x32_bf16 v[12:15], a[160:163], v[64:67], v[12:15]\n\t"
        "ds_read_b128 v[56:59], v107 offset:0\n\t"
        "v_mfma_f32_16x16x32_bf16 v[16:19], v[64:67], a[164:167], v[16:19]\n\t"
        "ds_read_b128 v[60:63], v107 offset:2048\n\t"
        "s_add_u32 m0, %[ld], 32768\n\t"
        "s_add_u32 %[t0], %[xo], 512\n\t"
        "v_mfma_f32_16x16x32_bf16 v[20:23], a[156:159], v[68:71], v[20:23]\n\t"
        "buffer_load_dwordx4 v100, %[sx], %[t0] offen lds\n\t"
        "v_mfma_f32_16x16x32_bf16 v[24:27], a[160:163], v[68:71], v[24:27]\n\t"
        "ds_read_b128 v[64:67], v108 offset:0\n\t"
        "v_mfma_f32_16x16x32_bf16 v[28:31], v[68:71], a[164:167], v[28:31]\n\t"
        "v_mfma_f32_16x16x32_bf16 v[8:11], a[168:171], v[72:75], v[8:11]\n\t"
        "v_mfma_f32_16x16x32_bf16 v[12:15], a[172:175], v[72:75], v[12:15]\n\t"
        "ds_read_b128 v[68:71], v108 offset:2048\n\t"
        "v_mov_b32_e32 v117, %[cn]\n\t"
        "buffer_store_dword v117, v113, %[sw], 0 offen sc1\n\t"
        "s_add_u32 %[cn], %[cn], 1\n\t"
        "v_mfma_f32_16x16x32_bf16 v[16:19], v[72:75], a[176:179], v[16:19]\n\t"
        "s_add_u32 m0, %[ld], 33792\n\t"
        "s_add_u32 %[t0], %[xo], 16896\n\t"
        "v_mfma_f32_16x16x32_bf16 v[20:23], a[168:171], v[76:79], v[20:23]\n\t"
        "buffer_load_dwordx4 v100, %[sx], %[t0] offen lds\n\t"
        "v_mfma_f32_16x16x32_bf16 v[24:27], a[172:175], v[76:79], v[24:27]\n\t"
        "ds_read_b128 v[72:75], v107 offset:8192\n\t"
        "v_mfma_f32_16x16x32_bf16 v[28:31], v[76:79], a[176:179], v[28:31]\n\t"
        "v_mfma_f32_16x16x32_bf16 v[8:11], a[180:183], v[80:83], v[8:11]\n\t"
        "v_mfma_f32_16x16x32_bf16 v[12:15], a[184:187], v[80:83], v[12:15]\n\t"
        "ds_read_b128 v[76:79], v107 offset:10240\n\t"
        "v_mfma_f32_16x16x32_bf16 v[16:19], v[80:83], a[188:191], v[16:19]\n\t"
        "s_add_u32 m0, %[ld], 40960\n\t"
        "s_add_u32 %[t0], %[xo], 640\n\t"
        "v_mfma_f32_16x16x32_bf16 v[20:23], a[180:183], v[84:87], v[20:23]\n\t"
        "buffer_load_dwordx4 v100, %[sx], %[t0] offen lds\n\t"
        "v_mfma_f32_16x16x32_bf16 v[24:27], a[184:187], v[84:87], v[24:27]\n\t"
        "ds_read_b128 v[80:83], v108 offset:8192\n\t"
        "v_mfma_f32_16x16x32_bf16 v[28:31], v[84:87], a[188:191], v[28:31]\n\t"
        "s_waitcnt lgkmcnt(6)\n\t"
        "v_mfma_f32_16x16x32_bf16 v[8:11], a[192:195], v[56:59], v[8:11]\n\t"
        "v_mfma_f32_16x16x32_bf16 v[12:15], a[196:199], v[56:59], v[12:15]\n\t"
        "ds_read_b128 v[84:87], v108 offset:10240\n\t"
        "v_mfma_f32_16x16x32_bf16 v[16:19], v[56:59], a[200:203], v[16:19]\n\t"
        "s_add_u32 m0, %[ld], 41984\n\t"
        "s_add_u32 %[t0], %[xo], 17024\n\t"
        "s_waitcnt lgkmcnt(6)\n\t"
        "v_mfma_f32_16x16x32_bf16 v[20:23], a[192:195], v[60:63], v[20:23]\n\t"
        "buffer_load_dwordx4 v100, %[sx], %[t0] offen lds\n\t"
        "v_mfma_f32_16x16x32_bf16 v[24:27], a[196:199], v[60:63], v[24:27]\n\t"
        "ds_read_b128 v[56:59], v107 offset:16384\n\t"
        "v_mfma_f32_16x16x32_bf16 v[28:31], v[60:63], a[200:203], v[28:31]\n\t"
        "s_waitcnt lgkmcnt(6)\n\t"
        "v_mfma_f32_16x16x32_bf16 v[8:11], a[204:207], v[64:67], v[8:11]\n\t"
        "v_mfma_f32_16x16x32_bf16 v[12:15], a[208:211], v[64:67], v[12:15]\n\t"
        "ds_read_b128 v[60:63], v107 offset:18432\n\t"
        "v_mfma_f32_16x16x32_bf16 v[16:19], v[64:67], a[212:215], v[16:19]\n\t"
        "s_add_u32 m0, %[ld], 49152\n\t"
        "s_add_u32 %[t0], %[xo], 768\n\t"
        "s_waitcnt lgkmcnt(6)\n\t"
        "v_mfma_f32_16x16x32_bf16 v[20:23], a[204:207], v[68:71], v[20:23]\n\t"
        "buffer_load_dwordx4 v100, %[sx], %[t0] offen lds\n\t"
        "v_mfma_f32_16x16x32_bf16 v[24:27], a[208:211], v[68:71], v[24:27]\n\t"
        "ds_read_b128 v[64:67], v108 offset:16384\n\t"
        "v_mfma_f32_16x16x32_bf16 v[28:31], v[68:71], a[212:215], v[28:31]\n\t"
        "s_waitcnt lgkmcnt(6)\n\t"
        "v_mfma_f32_16x16x32_bf16 v[8:11], a[216:219], v[72:75], v[8:11]\n\t"
        "v_mfma_f32_16x16x32_bf16 v[12:15], a[220:223], v[72:75], v[12:15]\n\t"
        "ds_read_b128 v[68:71], v108 offset:18432\n\t"
        "v_mfma_f32_16x16x32_bf16 v[16:19], v[72:75], a[224:227], v[16:19]\n\t"
        "s_add_u32 m0, %[ld], 50176\n\t"
        "s_add_u32 %[t0], %[xo], 17152\n\t"
        "s_waitcnt lgkmcnt(6)\n\t"
        "v_mfma_f32_16x16x32_bf16 v[20:23], a[216:219], v[76:79], v[20:23]\n\t"
        "buffer_load_dwordx4 v100, %[sx], %[t0] offen lds\n\t"
        "v_mfma_f32_16x16x32_bf16 v[24:27], a[220:223], v[76:79], v[24:27]\n\t"
        "ds_read_b128 v[72:75], v107 offset:24576\n\t"
        "v_mfma_f32_16x16x32_bf16 v[28:31], v[76:79], a[224:227], v[28:31]\n\t"
        "s_waitcnt lgkmcnt(6)\n\t"
        "v_mfma_f32_16x16x32_bf16 v[8:11], a[228:231], v[80:83], v[8:11]\n\t"
        "v_mfma_f32_16x16x32_bf16 v[12:15], a[232:235], v[80:83], v[12:15]\n\t"
        "ds_read_b128 v[76:79], v107 offset:26624\n\t"
        "v_mfma_f32_16x16x32_bf16 v[16:19], v[80:83], a[236:239], v[16:19]\n\t"
        "s_add_u32 m0, %[ld], 57344\n\t"
        "s_add_u32 %[t0], %[xo], 896\n\t"
        "s_waitcnt lgkmcnt(6)\n\t"
        "v_mfma_f32_16x16x32_bf16 v[20:23], a[228:231], v[84:87], v[20:23]\n\t"
        "buffer_load_dwordx4 v100, %[sx], %[t0] offen lds\n\t"
        "v_mfma_f32_16x16x32_bf16 v[24:27], a[232:235], v[84:87], v[24:27]\n\t"
        "ds_read_b128 v[80:83], v108 offset:24576\n\t"
        "v_mfma_f32_16x16x32_bf16 v[28:31], v[84:87], a[236:239], v[28:31]\n\t"
        "s_waitcnt lgkmcnt(6)\n\t"
        "v_mfma_f32_16x16x32_bf16 v[8:11], a[240:243], v[56:59], v[8:11]\n\t"
        "v_mfma_f32_16x16x32_bf16 v[12:15], a[244:247], v[56:59], v[12:15]\n\t"
        "ds_read_b128 v[84:87], v108 offset:26624\n\t"
        "v_mfma_f32_16x16x32_bf16 v[16:19], v[56:59], a[248:251], v[16:19]\n\t"
        "s_add_u32 m0, %[ld], 58368\n\t"
        "s_add_u32 %[t0], %[xo], 17280\n\t"
        "s_waitcnt lgkmcnt(6)\n\t"
        "v_mfma_f32_16x16x32_bf16 v[20:23], a[240:243], v[60:63], v[20:23]\n\t"
        "buffer_load_dwordx4 v100, %[sx], %[t0] offen lds\n\t"
        "v_mfma_f32_16x16x32_bf16 v[24:27], a[244:247], v[60:63], v[24:27]\n\t"
        "v_mfma_f32_16x16x32_bf16 v[28:31], v[60:63], a[248:251], v[28:31]\n\t"
        "s_waitcnt lgkmcnt(5)\n\t"
        "v_mfma_f32_16x16x32_bf16 v[8:11], a[252:255], v[64:67], v[8:11]\n\t"
        "s_waitcnt vmcnt(17) lgkmcnt(0)\n\t"
        "s_barrier\n\t"
        "v_mfma_f32_16x16x32_bf16 v[12:15], v[128:131], v[64:67], v[12:15]\n\t"
        "ds_read_b128 v[56:59], v107 offset:32768\n\t"
        "v_mfma_f32_16x16x32_bf16 v[16:19], v[64:67], v[132:135], v[16:19]\n\t"
        "ds_read_b128 v[60:63], v107 offset:34816\n\t"
        "s_add_u32 m0, %[ld], 65536\n\t"
        "s_add_u32 %[t0], %[xo], 1024\n\t"
        "v_mfma_f32_16x16x32_bf16 v[20:23], a[252:255], v[68:71], v[20:23]\n\t"
        "buffer_load_dwordx4 v100, %[sx], %[t0] offen lds\n\t"
        "v_mfma_f32_16x16x32_bf16 v[24:27], v[128:131], v[68:71], v[24:27]\n\t"
        "ds_read_b128 v[64:67], v108 offset:32768\n\t"
        "v_mfma_f32_16x16x32_bf16 v[28:31], v[68:71], v[132:135], v[28:31]\n\t"
        "v_mfma_f32_16x16x32_bf16 v[8:11], v[136:139], v[72:75], v[8:11]\n\t"
        "v_mfma_f32_16x16x32_bf16 v[12:15], v[140:143], v[72:75], v[12:15]\n\t"
        "ds_read_b128 v[68:71], v108 offset:34816\n\t"
        "v_mfma_f32_16x16x32_bf16 v[16:19], v[72:75], v[144:147], v[16:19]\n\t"
        "s_add_u32 m0, %[ld], 66560\n\t"
        "s_add_u32 %[t0], %[xo], 17408\n\t"
        "v_mfma_f32_16x16x32_bf16 v[20:23], v[136:139], v[76:79], v[20:23]\n\t"
        "buffer_load_dwordx4 v100, %[sx], %[t0] offen lds\n\t"
        "v_mfma_f32_16x16x32_bf16 v[24:27], v[140:143], v[76:79], v[24:27]\n\t"
        "ds_read_b128 v[72:75], v107 offset:40960\n\t"
        "v_mfma_f32_16x16x32_bf16 v[28:31], v[76:79], v[144:147], v[28:31]\n\t"
        "v_mfma_f32_16x16x32_bf16 v[8:11], v[148:151], v[80:83], v[8:11]\n\t"
        "v_mfma_f32_16x16x32_bf16 v[12:15], v[152:155], v[80:83], v[12:15]\n\t"
        "ds_read_b128 v[76:79], v107 offset:43008\n\t"
        "v_mfma_f32_16x16x32_bf16 v[16:19], v[80:83], v[156:159], v[16:19]\n\t"
        "s_add_u32 m0, %[ld], 73728\n\t"
        "s_add_u32 %[t0], %[xo], 1152\n\t"
        "v_mfma_f32_16x16x32_bf16 v[20:23], v[148:151], v[84:87], v[20:23]\n\t"
        "buffer_load_dwordx4 v100, %[sx], %[t0] offen lds\n\t"
        "v_mfma_f32_16x16x32_bf16 v[24:27], v[152:155], v[84:87], v[24:27]\n\t"
        "ds_read_b128 v[80:83], v108 offset:40960\n\t"
        "v_mfma_f32_16x16x32_bf16 v[28:31], v[84:87], v[156:159], v[28:31]\n\t"
        "s_waitcnt lgkmcnt(6)\n\t"
        "v_mfma_f32_16x16x32_bf16 v[8:11], v[160:163], v[56:59], v[8:11]\n\t"
        "v_mfma_f32_16x16x32_bf16 v[12:15], v[164:167], v[56:59], v[12:15]\n\t"
        "ds_read_b128 v[84:87], v108 offset:43008\n\t"
        "v_mfma_f32_16x16x32_bf16 v[16:19], v[56:59], v[168:171], v[16:19]\n\t"
        "s_add_u32 m0, %[ld], 74752\n\t"
        "s_add_u32 %[t0], %[xo], 17536\n\t"
        "s_waitcnt lgkmcnt(6)\n\t"
        "v_mfma_f32_16x16x32_bf16 v[20:23], v[160:163], v[60:63], v[20:23]\n\t"
        "buffer_load_dwordx4 v100, %[sx], %[t0] offen lds\n\t"
        "v_mfma_f32_16x16x32_bf16 v[24:27], v[164:167], v[60:63], v[24:27]\n\t"
        "ds_read_b128 v[56:59], v107 offset:49152\n\t"
        "v_mfma_f32_16x16x32_bf16 v[28:31], v[60:63], v[168:171], v[28:31]\n\t"
        "s_waitcnt lgkmcnt(6)\n\t"
        "v_mfma_f32_16x16x32_bf16 v[8:11], v[172:175], v[64:67], v[8:11]\n\t"
        "v_mfma_f32_16x16x32_bf16 v[12:15], v[176:179], v[64:67], v[12:15]\n\t"
        "ds_read_b128 v[60:63], v107 offset:51200\n\t"
        "v_mfma_f32_16x16x32_bf16 v[16:19], v[64:67], v[180:183], v[16:19]\n\t"
        "s_add_u32 m0, %[ld], 81920\n\t"
        "s_add_u32 %[t0], %[xo], 1280\n\t"
        "s_waitcnt lgkmcnt(6)\n\t"
        "v_mfma_f32_16x16x32_bf16 v[20:23], v[172:175], v[68:71], v[20:23]\n\t"
        "buffer_load_dwordx4 v100, %[sx], %[t0] offen lds\n\t"
        "v_mfma_f32_16x16x32_bf16 v[24:27], v[176:179], v[68:71], v[24:27]\n\t"
        "ds_read_b128 v[64:67], v108 offset:49152\n\t"
        "v_mfma_f32_16x16x32_bf16 v[28:31], v[68:71], v[180:183], v[28:31]\n\t"
        "s_waitcnt lgkmcnt(6)\n\t"
        "v_mfma_f32_16x16x32_bf16 v[8:11], v[184:187], v[72:75], v[8:11]\n\t"
        "v_mfma_f32_16x16x32_bf16 v[12:15], v[188:191], v[72:75], v[12:15]\n\t"
        "ds_read_b128 v[68:71], v108 offset:51200\n\t"
        "v_mfma_f32_16x16x32_bf16 v[16:19], v[72:75], v[192:195], v[16:19]\n\t"
        "s_add_u32 m0, %[ld], 82944\n\t"
        "s_add_u32 %[t0], %[xo], 17664\n\t"
        "s_waitcnt lgkmcnt(6)\n\t"
        "v_mfma_f32_16x16x32_bf16 v[20:23], v[184:187], v[76:79], v[20:23]\n\t"
        "buffer_load_dwordx4 v100, %[sx], %[t0] offen lds\n\t"
        "v_mfma_f32_16x16x32_bf16 v[24:27], v[188:191], v[76:79], v[24:27]\n\t"
        "ds_read_b128 v[72:75], v107 offset:57344\n\t"
        "v_mfma_f32_16x16x32_bf16 v[28:31], v[76:79], v[192:195], v[28:31]\n\t"
        "s_waitcnt lgkmcnt(6)\n\t"
        "v_mfma_f32_16x16x32_bf16 v[8:11], v[196:199], v[80:83], v[8:11]\n\t"
        "v_mfma_f32_16x16x32_bf16 v[12:15], v[200:203], v[80:83], v[12:15]\n\t"
        "ds_read_b128 v[76:79], v107 offset:59392\n\t"
        "v_mfma_f32_16x16x32_bf16 v[16:19], v[80:83], v[204:207], v[16:19]\n\t"
        "s_add_u32 m0, %[ld], 90112\n\t"
        "s_add_u32 %[t0], %[xo], 1408\n\t"
        "s_waitcnt lgkmcnt(6)\n\t"
        "v_mfma_f32_16x16x32_bf16 v[20:23], v[196:199], v[84:87], v[20:23]\n\t"
        "buffer_load_dwordx4 v100, %[sx], %[t0] offen lds\n\t"
        "v_mfma_f32_16x16x32_bf16 v[24:27], v[200:203], v[84:87], v[24:27]\n\t"
        "ds_read_b128 v[80:83], v108 offset:57344\n\t"
        "v_mfma_f32_16x16x32_bf16 v[28:31], v[84:87], v[204:207], v[28:31]\n\t"
        "s_waitcnt lgkmcnt(6)\n\t"
        "v_mfma_f32_16x16x32_bf16 v[8:11], v[208:211], v[56:59], v[8:11]\n\t"
        "v_mfma_f32_16x16x32_bf16 v[12:15], v[212:215], v[56:59], v[12:15]\n\t"
        "ds_read_b128 v[84:87], v108 offset:59392\n\t"
        "v_mfma_f32_16x16x32_bf16 v[16:19], v[56:59], v[216:219], v[16:19]\n\t"
        "s_add_u32 m0, %[ld], 91136\n\t"
        "s_add_u32 %[t0], %[xo], 17792\n\t"
        "s_waitcnt lgkmcnt(6)\n\t"
        "v_mfma_f32_16x16x32_bf16 v[20:23], v[208:211], v[60:63], v[20:23]\n\t"
        "buffer_load_dwordx4 v100, %[sx], %[t0] offen lds\n\t"
        "v_mfma_f32_16x16x32_bf16 v[24:27], v[212:215], v[60:63], v[24:27]\n\t"
        "v_mfma_f32_16x16x32_bf16 v[28:31], v[60:63], v[216:219], v[28:31]\n\t"
        "s_waitcnt lgkmcnt(5)\n\t"
        "v_mfma_f32_16x16x32_bf16 v[8:11], v[220:223], v[64:67], v[8:11]\n\t"
        "s_waitcnt vmcnt(17) lgkmcnt(0)\n\t"
        "s_barrier\n\t"
        "v_mfma_f32_16x16x32_bf16 v[12:15], v[224:227], v[64:67], v[12:15]\n\t"
        "ds_read_b128 v[56:59], v105 offset:0\n\t"
        "v_mfma_f32_16x16x32_bf16 v[16:19], v[64:67], v[228:231], v[16:19]\n\t"
        "ds_read_b128 v[60:63], v105 offset:2048\n\t"
        "s_add_u32 m0, %[ld], 98304\n\t"
        "s_add_u32 %[t0], %[xo], 1536\n\t"
        "v_mfma_f32_16x16x32_bf16 v[20:23], v[220:223], v[68:71], v[20:23]\n\t"
        "buffer_load_dwordx4 v100, %[sx], %[t0] offen lds\n\t"
        "v_mfma_f32_16x16x32_bf16 v[24:27], v[224:227], v[68:71], v[24:27]\n\t"
        "ds_read_b128 v[64:67], v106 offset:0\n\t"
        "v_mfma_f32_16x16x32_bf16 v[28:31], v[68:71], v[228:231], v[28:31]\n\t"
        "v_mfma_f32_16x16x32_bf16 v[8:11], v[232:235], v[72:75], v[8:11]\n\t"
        "v_mfma_f32_16x16x32_bf16 v[12:15], v[236:239], v[72:75], v[12:15]\n\t"
        "ds_read_b128 v[68:71], v106 offset:2048\n\t"
        "v_mfma_f32_16x16x32_bf16 v[16:19], v[72:75], v[240:243], v[16:19]\n\t"
        "s_add_u32 m0, %[ld], 99328\n\t"
        "s_add_u32 %[t0], %[xo], 17920\n\t"
        "v_mfma_f32_16x16x32_bf16 v[20:23], v[232:235], v[76:79], v[20:23]\n\t"
        "buffer_load_dwordx4 v100, %[sx], %[t0] offen lds\n\t"
        "v_mfma_f32_16x16x32_bf16 v[24:27], v[236:239], v[76:79], v[24:27]\n\t"
        "ds_read_b128 v[72:75], v105 offset:8192\n\t"
        "v_mfma_f32_16x16x32_bf16 v[28:31], v[76:79], v[240:243], v[28:31]\n\t"
        "v_mfma_f32_16x16x32_bf16 v[8:11], v[244:247], v[80:83], v[8:11]\n\t"
        "v_mfma_f32_16x16x32_bf16 v[12:15], v[248:251], v[80:83], v[12:15]\n\t"
        "ds_read_b128 v[76:79], v105 offset:10240\n\t"
        "v_mfma_f32_16x16x32_bf16 v[16:19], v[80:83], v[252:255], v[16:19]\n\t"
        "s_add_u32 m0, %[ld], 106496\n\t"
        "s_add_u32 %[t0], %[xo], 1664\n\t"
        "v_mfma_f32_16x16x32_bf16 v[20:23], v[244:247], v[84:87], v[20:23]\n\t"
        "buffer_load_dwordx4 v100, %[sx], %[t0] offen lds\n\t"
        "v_mfma_f32_16x16x32_bf16 v[24:27], v[248:251], v[84:87], v[24:27]\n\t"
        "ds_read_b128 v[80:83], v106 offset:8192\n\t"
        "v_mfma_f32_16x16x32_bf16 v[28:31], v[84:87], v[252:255], v[28:31]\n\t"
        "LG_mid%=:\n\t"
        "s_waitcnt lgkmcnt(6)\n\t"
        "v_mfma_f32_16x16x32_bf16 v[32:35], a[0:3], v[56:59], 0\n\t"
        "v_mfma_f32_16x16x32_bf16 v[36:39], a[4:7], v[56:59], 0\n\t"
        "ds_read_b128 v[84:87], v106 offset:10240\n\t"
        "v_mfma_f32_16x16x32_bf16 v[40:43], v[56:59], a[8:11], 0\n\t"
        "s_add_u32 m0, %[ld], 107520\n\t"
        "s_add_u32 %[t0], %[xo], 18048\n\t"
        "s_waitcnt lgkmcnt(6)\n\t"
        "v_mfma_f32_16x16x32_bf16 v[44:47], a[0:3], v[60:63], 0\n\t"
        "buffer_load_dwordx4 v100, %[sx], %[t0] offen lds\n\t"
        "v_mfma_f32_16x16x32_bf16 v[48:51], a[4:7], v[60:63], 0\n\t"
        "ds_read_b128 v[56:59], v105 offset:16384\n\t"
        "v_mfma_f32_16x16x32_bf16 v[52:55], v[60:63], a[8:11], 0\n\t"
        "s_waitcnt lgkmcnt(6)\n\t"
        "v_mfma_f32_16x16x32_bf16 v[32:35], a[12:15], v[64:67], v[32:35]\n\t"
        "v_cvt_pk_bf16_f32 v88, v8, v9\n\t"
        "v_cvt_pk_bf16_f32 v89, v10, v11\n\t"
        "v_mfma_f32_16x16x32_bf16 v[36:39], a[16:19], v[64:67], v[36:39]\n\t"
        "ds_read_b128 v[60:63], v105 offset:18432\n\t"
        "v_cvt_pk_bf16_f32 v90, v12, v13\n\t"
        "v_mfma_f32_16x16x32_bf16 v[40:43], v[64:67], a[20:23], v[40:43]\n\t"
        "s_add_u32 m0, %[ld], 114688\n\t"
        "s_add_u32 %[t0], %[xo], 1792\n\t"
        "s_waitcnt lgkmcnt(6)\n\t"
        "v_mfma_f32_16x16x32_bf16 v[44:47], a[12:15], v[68:71], v[44:47]\n\t"
        "buffer_load_dwordx4 v100, %[sx], %[t0] offen lds\n\t"
        "v_mfma_f32_16x16x32_bf16 v[48:51], a[16:19], v[68:71], v[48:51]\n\t"
        "ds_read_b128 v[64:67], v106 offset:16384\n\t"
        "v_cvt_pk_bf16_f32 v91, v14, v15\n\t"
        "v_mfma_f32_16x16x32_bf16 v[52:55], v[68:71], a[20:23], v[52:55]\n\t"
        "buffer_store_dwordx4 v[88:91], v109, %[sw], %[so] offen sc1\n\t"
        "s_waitcnt lgkmcnt(6)\n\t"
        "v_mfma_f32_16x16x32_bf16 v[32:35], a[24:27], v[72:75], v[32:35]\n\t"
        "v_cvt_pk_bf16_f32 v92, v20, v21\n\t"
        "v_cvt_pk_bf16_f32 v93, v22, v23\n\t"
        "v_mfma_f32_16x16x32_bf16 v[36:39], a[28:31], v[72:75], v[36:39]\n\t"
        "ds_read_b128 v[68:71], v106 offset:18432\n\t"
        "v_cvt_pk_bf16_f32 v94, v24, v25\n\t"
        "v_mfma_f32_16x16x32_bf16 v[40:43], v[72:75], a[32:35], v[40:43]\n\t"
        "s_add_u32 m0, %[ld], 115712\n\t"
        "s_add_u32 %[t0], %[xo], 18176\n\t"
        "s_waitcnt lgkmcnt(6)\n\t"
        "v_mfma_f32_16x16x32_bf16 v[44:47], a[24:27], v[76:79], v[44:47]\n\t"
        "buffer_load_dwordx4 v100, %[sx], %[t0] offen lds\n\t"
        "v_mfma_f32_16x16x32_bf16 v[48:51], a[28:31], v[76:79], v[48:51]\n\t"
        "ds_read_b128 v[72:75], v105 offset:24576\n\t"
        "v_cvt_pk_bf16_f32 v95, v26, v27\n\t"
        "v_mfma_f32_16x16x32_bf16 v[52:55], v[76:79], a[32:35], v[52:55]\n\t"
        "buffer_store_dwordx4 v[92:95], v110, %[sw], %[so] offen sc1\n\t"
        "s_waitcnt lgkmcnt(6)\n\t"
        "v_mfma_f32_16x16x32_bf16 v[32:35], a[36:39], v[80:83], v[32:35]\n\t"
        "v_cvt_pk_bf16_f32 v96, v16, v17\n\t"
        "v_cvt_pk_bf16_f32 v97, v18, v19\n\t"
        "v_mfma_f32_16x16x32_bf16 v[36:39], a[40:43], v[80:83], v[36:39]\n\t"
        "ds_read_b128 v[76:79], v105 offset:26624\n\t"
        "v_cvt_pk_bf16_f32 v98, v28, v29\n\t"
        "v_mfma_f32_16x16x32_bf16 v[40:43], v[80:83], a[44:47], v[40:43]\n\t"
        "s_add_u32 m0, %[ld], 122880\n\t"
        "s_add_u32 %[t0], %[xo], 1920\n\t"
        "s_waitcnt lgkmcnt(6)\n\t"
        "v_mfma_f32_16x16x32_bf16 v[44:47], a[36:39], v[84:87], v[44:47]\n\t"
        "buffer_load_dwordx4 v100, %[sx], %[t0] offen lds\n\t"
        "v_mfma_f32_16x16x32_bf16 v[48:51], a[40:43], v[84:87], v[48:51]\n\t"
        "ds_read_b128 v[80:83], v106 offset:24576\n\t"
        "v_cvt_pk_bf16_f32 v99, v30, v31\n\t"
        "v_mfma_f32_16x16x32_bf16 v[52:55], v[84:87], a[44:47], v[52:55]\n\t"
        "buffer_store_dwordx4 v[96:99], v111, %[sw], %[so] offen sc1\n\t"
        "s_waitcnt lgkmcnt(6)\n\t"
        "v_mfma_f32_16x16x32_bf16 v[32:35], a[48:51], v[56:59], v[32:35]\n\t"
        "s_lshl_b32 %[so], %[cn], 19\n\t"
        "v_mfma_f32_16x16x32_bf16 v[36:39], a[52:55], v[56:59], v[36:39]\n\t"
        "ds_read_b128 v[84:87], v106 offset:26624\n\t"
        "v_mfma_f32_16x16x32_bf16 v[40:43], v[56:59], a[56:59], v[40:43]\n\t"
        "s_add_u32 m0, %[ld], 123904\n\t"
        "s_add_u32 %[t0], %[xo], 18304\n\t"
        "s_waitcnt lgkmcnt(6)\n\t"
        "v_mfma_f32_16x16x32_bf16 v[44:47], a[48:51], v[60:63], v[44:47]\n\t"
        "buffer_load_dwordx4 v100, %[sx], %[t0] offen lds\n\t"
        "v_mfma_f32_16x16x32_bf16 v[48:51], a[52:55], v[60:63], v[48:51]\n\t"
        "v_mfma_f32_16x16x32_bf16 v[52:55], v[60:63], a[56:59], v[52:55]\n\t"
        "s_waitcnt lgkmcnt(5)\n\t"
        "v_mfma_f32_16x16x32_bf16 v[32:35], a[60:63], v[64:67], v[32:35]\n\t"
        "s_waitcnt vmcnt(19) lgkmcnt(0)\n\t"
        "s_barrier\n\t"
        "s_add_u32 %[xo], %[xo], 0x20000\n\t"
        "v_mfma_f32_16x16x32_bf16 v[36:39], a[64:67], v[64:67], v[36:39]\n\t"
        "ds_read_b128 v[56:59], v105 offset:32768\n\t"
        "v_mfma_f32_16x16x32_bf16 v[40:43], v[64:67], a[68:71], v[40:43]\n\t"
        "ds_read_b128 v[60:63], v105 offset:34816\n\t"
        "s_add_u32 m0, %[ld], 0\n\t"
        "s_add_u32 %[t0], %[xo], 0\n\t"
        "v_mfma_f32_16x16x32_bf16 v[44:47], a[60:63], v[68:71], v[44:47]\n\t"
        "buffer_load_dwordx4 v100, %[sx], %[t0] offen lds\n\t"
        "v_mfma_f32_16x16x32_bf16 v[48:51], a[64:67], v[68:71], v[48:51]\n\t"
        "ds_read_b128 v[64:67], v106 offset:32768\n\t"
        "v_mfma_f32_16x16x32_bf16 v[52:55], v[68:71], a[68:71], v[52:55]\n\t"
        "v_mfma_f32_16x16x32_bf16 v[32:35], a[72:75], v[72:75], v[32:35]\n\t"
        "v_mfma_f32_16x16x32_bf16 v[36:39], a[76:79], v[72:75], v[36:39]\n\t"
        "ds_read_b128 v[68:71], v106 offset:34816\n\t"
        "v_mfma_f32_16x16x32_bf16 v[40:43], v[72:75], a[80:83], v[40:43]\n\t"
        "s_add_u32 m0, %[ld], 1024\n\t"
        "s_add_u32 %[t0], %[xo], 16384\n\t"
        "v_mfma_f32_16x16x32_bf16 v[44:47], a[72:75], v[76:79], v[44:47]\n\t"
        "buffer_load_dwordx4 v100, %[sx], %[t0] offen lds\n\t"
        "v_mfma_f32_16x16x32_bf16 v[48:51], a[76:79], v[76:79], v[48:51]\n\t"
        "ds_read_b128 v[72:75], v105 offset:40960\n\t"
        "v_mfma_f32_16x16x32_bf16 v[52:55], v[76:79], a[80:83], v[52:55]\n\t"
        "v_mfma_f32_16x16x32_bf16 v[32:35], a[84:87], v[80:83], v[32:35]\n\t"
        "v_mfma_f32_16x16x32_bf16 v[36:39], a[88:91], v[80:83], v[36:39]\n\t"
        "ds_read_b128 v[76:79], v105 offset:43008\n\t"
        "v_mfma_f32_16x16x32_bf16 v[40:43], v[80:83], a[92:95], v[40:43]\n\t"
        "s_add_u32 m0, %[ld], 8192\n\t"
        "s_add_u32 %[t0], %[xo], 128\n\t"
        "v_mfma_f32_16x16x32_bf16 v[44:47], a[84:87], v[84:87], v[44:47]\n\t"
        "buffer_load_dwordx4 v100, %[sx], %[t0] offen lds\n\t"
        "v_mfma_f32_16x16x32_bf16 v[48:51], a[88:91], v[84:87], v[48:51]\n\t"
        "ds_read_b128 v[80:83], v106 offset:40960\n\t"
        "v_mfma_f32_16x16x32_bf16 v[52:55], v[84:87], a[92:95], v[52:55]\n\t"
        "s_waitcnt lgkmcnt(6)\n\t"
        "v_mfma_f32_16x16x32_bf16 v[32:35], a[96:99], v[56:59], v[32:35]\n\t"
        "v_mfma_f32_16x16x32_bf16 v[36:39], a[100:103], v[56:59], v[36:39]\n\t"
        "ds_read_b128 v[84:87], v106 offset:43008\n\t"
        "v_mfma_f32_16x16x32_bf16 v[40:43], v[56:59], a[104:107], v[40:43]\n\t"
        "s_add_u32 m0, %[ld], 9216\n\t"
        "s_add_u32 %[t0], %[xo], 16512\n\t"
        "s_waitcnt lgkmcnt(6)\n\t"
        "v_mfma_f32_16x16x32_bf16 v[44:47], a[96:99], v[60:63], v[44:47]\n\t"
        "buffer_load_dwordx4 v100, %[sx], %[t0] offen lds\n\t"
        "v_mfma_f32_16x16x32_bf16 v[48:51], a[100:103], v[60:63], v[48:51]\n\t"
        "ds_read_b128 v[56:59], v105 offset:49152\n\t"
        "v_mfma_f32_16x16x32_bf16 v[52:55], v[60:63], a[104:107], v[52:55]\n\t"
        "s_waitcnt lgkmcnt(6)\n\t"
        "v_mfma_f32_16x16x32_bf16 v[32:35], a[108:111], v[64:67], v[32:35]\n\t"
        "v_mfma_f32_16x16x32_bf16 v[36:39], a[112:115], v[64:67], v[36:39]\n\t"
        "ds_read_b128 v[60:63], v105 offset:51200\n\t"
        "v_mfma_f32_16x16x32_bf16 v[40:43], v[64:67], a[116:119], v[40:43]\n\t"
        "s_add_u32 m0, %[ld], 16384\n\t"
        "s_add_u32 %[t0], %[xo], 256\n\t"
        "s_waitcnt lgkmcnt(6)\n\t"
        "v_mfma_f32_16x16x32_bf16 v[44:47], a[108:111], v[68:71], v[44:47]\n\t"
        "buffer_load_dwordx4 v100, %[sx], %[t0] offen lds\n\t"
        "v_mfma_f32_16x16x32_bf16 v[48:51], a[112:115], v[68:71], v[48:51]\n\t"
        "ds_read_b128 v[64:67], v106 offset:49152\n\t"
        "v_mfma_f32_16x16x32_bf16 v[52:55], v[68:71], a[116:119], v[52:55]\n\t"
        "s_waitcnt lgkmcnt(6)\n\t"
        "v_mfma_f32_16x16x32_bf16 v[32:35], a[120:123], v[72:75], v[32:35]\n\t"
        "v_mfma_f32_16x16x32_bf16 v[36:39], a[124:127], v[72:75], v[36:39]\n\t"
        "ds_read_b128 v[68:71], v106 offset:51200\n\t"
        "v_mfma_f32_16x16x32_bf16 v[40:43], v[72:75], a[128:131], v[40:43]\n\t"
        "s_add_u32 m0, %[ld], 17408\n\t"
        "s_add_u32 %[t0], %[xo], 16640\n\t"
        "s_waitcnt lgkmcnt(6)\n\t"
        "v_mfma_f32_16x16x32_bf16 v[44:47], a[120:123], v[76:79], v[44:47]\n\t"
        "buffer_load_dwordx4 v100, %[sx], %[t0] offen lds\n\t"
        "v_mfma_f32_16x16x32_bf16 v[48:51], a[124:127], v[76:79], v[48:51]\n\t"
        "ds_read_b128 v[72:75], v105 offset:57344\n\t"
        "v_mfma_f32_16x16x32_bf16 v[52:55], v[76:79], a[128:131], v[52:55]\n\t"
        "s_waitcnt lgkmcnt(6)\n\t"
        "v_mfma_f32_16x16x32_bf16 v[32:35], a[132:135], v[80:83], v[32:35]\n\t"
        "v_mfma_f32_16x16x32_bf16 v[36:39], a[136:139], v[80:83], v[36:39]\n\t"
        "ds_read_b128 v[76:79], v105 offset:59392\n\t"
        "v_mfma_f32_16x16x32_bf16 v[40:43], v[80:83], a[140:143], v[40:43]\n\t"
        "s_add_u32 m0, %[ld], 24576\n\t"
        "s_add_u32 %[t0], %[xo], 384\n\t"
        "s_waitcnt lgkmcnt(6)\n\t"
        "v_mfma_f32_16x16x32_bf16 v[44:47], a[132:135], v[84:87], v[44:47]\n\t"
        "buffer_load_dwordx4 v100, %[sx], %[t0] offen lds\n\t"
        "v_mfma_f32_16x16x32_bf16 v[48:51], a[136:139], v[84:87], v[48:51]\n\t"
        "ds_read_b128 v[80:83], v106 offset:57344\n\t"
        "v_mfma_f32_16x16x32_bf16 v[52:55], v[84:87], a[140:143], v[52:55]\n\t"
        "s_waitcnt lgkmcnt(6)\n\t"
        "v_mfma_f32_16x16x32_bf16 v[32:35], a[144:147], v[56:59], v[32:35]\n\t"
        "v_mfma_f32_16x16x32_bf16 v[36:39], a[148:151], v[56:59], v[36:39]\n\t"
        "ds_read_b128 v[84:87], v106 offset:59392\n\t"
        "v_mfma_f32_16x16x32_bf16 v[40:43], v[56:59], a[152:155], v[40:43]\n\t"
        "s_add_u32 m0, %[ld], 25600\n\t"
        "s_add_u32 %[t0], %[xo], 16768\n\t"
        "s_waitcnt lgkmcnt(6)\n\t"
        "v_mfma_f32_16x16x32_bf16 v[44:47], a[144:147], v[60:63], v[44:47]\n\t"
        "buffer_load_dwordx4 v100, %[sx], %[t0] offen lds\n\t"
        "v_mfma_f32_16x16x32_bf16 v[48:51], a[148:151], v[60:63], v[48:51]\n\t"
        "v_mfma_f32_16x16x32_bf16 v[52:55], v[60:63], a[152:155], v[52:55]\n\t"
        "s_waitcnt lgkmcnt(5)\n\t"
        "v_mfma_f32_16x16x32_bf16 v[32:35], a[156:159], v[64:67], v[32:35]\n\t"
        "s_waitcnt vmcnt(9) lgkmcnt(0)\n\t"
        "s_barrier\n\t"
        "v_mfma_f32_16x16x32_bf16 v[36:39], a[160:163], v[64:67], v[36:39]\n\t"
        "ds_read_b128 v[56:59], v107 offset:0\n\t"
        "v_mfma_f32_16x16x32_bf16 v[40:43], v[64:67], a[164:167], v[40:43]\n\t"
        "ds_read_b128 v[60:63], v107 offset:2048\n\t"
        "s_add_u32 m0, %[ld], 32768\n\t"
        "s_add_u32 %[t0], %[xo], 512\n\t"
        "v_mfma_f32_16x16x32_bf16 v[44:47], a[156:159], v[68:71], v[44:47]\n\t"
        "buffer_load_dwordx4 v100, %[sx], %[t0] offen lds\n\t"
        "v_mfma_f32_16x16x32_bf16 v[48:51], a[160:163], v[68:71], v[48:51]\n\t"
        "ds_read_b128 v[64:67], v108 offset:0\n\t"
        "v_mfma_f32_16x16x32_bf16 v[52:55], v[68:71], a[164:167], v[52:55]\n\t"
        "v_mfma_f32_16x16x32_bf16 v[32:35], a[168:171], v[72:75], v[32:35]\n\t"
        "v_mfma_f32_16x16x32_bf16 v[36:39], a[172:175], v[72:75], v[36:39]\n\t"
        "ds_read_b128 v[68:71], v108 offset:2048\n\t"
        "v_mov_b32_e32 v117, %[cn]\n\t"
        "buffer_store_dword v117, v113, %[sw], 0 offen sc1\n\t"
        "s_add_u32 %[cn], %[cn], 1\n\t"
        "v_mfma_f32_16x16x32_bf16 v[40:43], v[72:75], a[176:179], v[40:43]\n\t"
        "s_add_u32 m0, %[ld], 33792\n\t"
        "s_add_u32 %[t0], %[xo], 16896\n\t"
        "v_mfma_f32_16x16x32_bf16 v[44:47], a[168:171], v[76:79], v[44:47]\n\t"
        "buffer_load_dwordx4 v100, %[sx], %[t0] offen lds\n\t"
        "v_mfma_f32_16x16x32_bf16 v[48:51], a[172:175], v[76:79], v[48:51]\n\t"
        "ds_read_b128 v[72:75], v107 offset:8192\n\t"
        "v_mfma_f32_16x16x32_bf16 v[52:55], v[76:79], a[176:179], v[52:55]\n\t"
        "v_mfma_f32_16x16x32_bf16 v[32:35], a[180:183], v[80:83], v[32:35]\n\t"
        "v_mfma_f32_16x16x32_bf16 v[36:39], a[184:187], v[80:83], v[36:39]\n\t"
        "ds_read_b128 v[76:79], v107 offset:10240\n\t"
        "v_mfma_f32_16x16x32_bf16 v[40:43], v[80:83], a[188:191], v[40:43]\n\t"
        "s_add_u32 m0, %[ld], 40960\n\t"
        "s_add_u32 %[t0], %[xo], 640\n\t"
        "v_mfma_f32_16x16x32_bf16 v[44:47], a[180:183], v[84:87], v[44:47]\n\t"
        "buffer_load_dwordx4 v100, %[sx], %[t0] offen lds\n\t"
        "v_mfma_f32_16x16x32_bf16 v[48:51], a[184:187], v[84:87], v[48:51]\n\t"
        "ds_read_b128 v[80:83], v108 offset:8192\n\t"
        "v_mfma_f32_16x16x32_bf16 v[52:55], v[84:87], a[188:191], v[52:55]\n\t"
        "s_waitcnt lgkmcnt(6)\n\t"
        "v_mfma_f32_16x16x32_bf16 v[32:35], a[192:195], v[56:59], v[32:35]\n\t"
        "v_mfma_f32_16x16x32_bf16 v[36:39], a[196:199], v[56:59], v[36:39]\n\t"
        "ds_read_b128 v[84:87], v108 offset:10240\n\t"
        "v_mfma_f32_16x16x32_bf16 v[40:43], v[56:59], a[200:203], v[40:43]\n\t"
        "s_add_u32 m0, %[ld], 41984\n\t"
        "s_add_u32 %[t0], %[xo], 17024\n\t"
        "s_waitcnt lgkmcnt(6)\n\t"
        "v_mfma_f32_16x16x32_bf16 v[44:47], a[192:195], v[60:63], v[44:47]\n\t"
        "buffer_load_dwordx4 v100, %[sx], %[t0] offen lds\n\t"
        "v_mfma_f32_16x16x32_bf16 v[48:51], a[196:199], v[60:63], v[48:51]\n\t"
        "ds_read_b128 v[56:59], v107 offset:16384\n\t"
        "v_mfma_f32_16x16x32_bf16 v[52:55], v[60:63], a[200:203], v[52:55]\n\t"
        "s_waitcnt lgkmcnt(6)\n\t"
        "v_mfma_f32_16x16x32_bf16 v[32:35], a[204:207], v[64:67], v[32:35]\n\t"
        "v_mfma_f32_16x16x32_bf16 v[36:39], a[208:211], v[64:67], v[36:39]\n\t"
        "ds_read_b128 v[60:63], v107 offset:18432\n\t"
        "v_mfma_f32_16x16x32_bf16 v[40:43], v[64:67], a[212:215], v[40:43]\n\t"
        "s_add_u32 m0, %[ld], 49152\n\t"
        "s_add_u32 %[t0], %[xo], 768\n\t"
        "s_waitcnt lgkmcnt(6)\n\t"
        "v_mfma_f32_16x16x32_bf16 v[44:47], a[204:207], v[68:71], v[44:47]\n\t"
        "buffer_load_dwordx4 v100, %[sx], %[t0] offen lds\n\t"
        "v_mfma_f32_16x16x32_bf16 v[48:51], a[208:211], v[68:71], v[48:51]\n\t"
        "ds_read_b128 v[64:67], v108 offset:16384\n\t"
        "v_mfma_f32_16x16x32_bf16 v[52:55], v[68:71], a[212:215], v[52:55]\n\t"
        "s_waitcnt lgkmcnt(6)\n\t"
        "v_mfma_f32_16x16x32_bf16 v[32:35], a[216:219], v[72:75], v[32:35]\n\t"
        "v_mfma_f32_16x16x32_bf16 v[36:39], a[220:223], v[72:75], v[36:39]\n\t"
        "ds_read_b128 v[68:71], v108 offset:18432\n\t"
        "v_mfma_f32_16x16x32_bf16 v[40:43], v[72:75], a[224:227], v[40:43]\n\t"
        "s_add_u32 m0, %[ld], 50176\n\t"
        "s_add_u32 %[t0], %[xo], 17152\n\t"
        "s_waitcnt lgkmcnt(6)\n\t"
        "v_mfma_f32_16x16x32_bf16 v[44:47], a[216:219], v[76:79], v[44:47]\n\t"
        "buffer_load_dwordx4 v100, %[sx], %[t0] offen lds\n\t"
        "v_mfma_f32_16x16x32_bf16 v[48:51], a[220:223], v[76:79], v[48:51]\n\t"
        "ds_read_b128 v[72:75], v107 offset:24576\n\t"
        "v_mfma_f32_16x16x32_bf16 v[52:55], v[76:79], a[224:227], v[52:55]\n\t"
        "s_waitcnt lgkmcnt(6)\n\t"
        "v_mfma_f32_16x16x32_bf16 v[32:35], a[228:231], v[80:83], v[32:35]\n\t"
        "v_mfma_f32_16x16x32_bf16 v[36:39], a[232:235], v[80:83], v[36:39]\n\t"
        "ds_read_b128 v[76:79], v107 offset:26624\n\t"
        "v_mfma_f32_16x16x32_bf16 v[40:43], v[80:83], a[236:239], v[40:43]\n\t"
        "s_add_u32 m0, %[ld], 57344\n\t"
        "s_add_u32 %[t0], %[xo], 896\n\t"
        "s_waitcnt lgkmcnt(6)\n\t"
        "v_mfma_f32_16x16x32_bf16 v[44:47], a[228:231], v[84:87], v[44:47]\n\t"
        "buffer_load_dwordx4 v100, %[sx], %[t0] offen lds\n\t"
        "v_mfma_f32_16x16x32_bf16 v[48:51], a[232:235], v[84:87], v[48:51]\n\t"
        "ds_read_b128 v[80:83], v108 offset:24576\n\t"
        "v_mfma_f32_16x16x32_bf16 v[52:55], v[84:87], a[236:239], v[52:55]\n\t"
        "s_waitcnt lgkmcnt(6)\n\t"
        "v_mfma_f32_16x16x32_bf16 v[32:35], a[240:243], v[56:59], v[32:35]\n\t"
        "v_mfma_f32_16x16x32_bf16 v[36:39], a[244:247], v[56:59], v[36:39]\n\t"
        "ds_read_b128 v[84:87], v108 offset:26624\n\t"
        "v_mfma_f32_16x16x32_bf16 v[40:43], v[56:59], a[248:251], v[40:43]\n\t"
        "s_add_u32 m0, %[ld], 58368\n\t"
        "s_add_u32 %[t0], %[xo], 17280\n\t"
        "s_waitcnt lgkmcnt(6)\n\t"
        "v_mfma_f32_16x16x32_bf16 v[44:47], a[240:243], v[60:63], v[44:47]\n\t"
        "buffer_load_dwordx4 v100, %[sx], %[t0] offen lds\n\t"
        "v_mfma_f32_16x16x32_bf16 v[48:51], a[244:247], v[60:63], v[48:51]\n\t"
        "v_mfma_f32_16x16x32_bf16 v[52:55], v[60:63], a[248:251], v[52:55]\n\t"
        "s_waitcnt lgkmcnt(5)\n\t"
        "v_mfma_f32_16x16x32_bf16 v[32:35], a[252:255], v[64:67], v[32:35]\n\t"
        "s_waitcnt vmcnt(17) lgkmcnt(0)\n\t"
        "s_barrier\n\t"
        "v_mfma_f32_16x16x32_bf16 v[36:39], v[128:131], v[64:67], v[36:39]\n\t"
        "ds_read_b128 v[56:59], v107 offset:32768\n\t"
        "v_mfma_f32_16x16x32_bf16 v[40:43], v[64:67], v[132:135], v[40:43]\n\t"
        "ds_read_b128 v[60:63], v107 offset:34816\n\t"
        "s_add_u32 m0, %[ld], 65536\n\t"
        "s_add_u32 %[t0], %[xo], 1024\n\t"
        "v_mfma_f32_16x16x32_bf16 v[44:47], a[252:255], v[68:71], v[44:47]\n\t"
        "buffer_load_dwordx4 v100, %[sx], %[t0] offen lds\n\t"
        "v_mfma_f32_16x16x32_bf16 v[48:51], v[128:131], v[68:71], v[48:51]\n\t"
        "ds_read_b128 v[64:67], v108 offset:32768\n\t"
        "v_mfma_f32_16x16x32_bf16 v[52:55], v[68:71], v[132:135], v[52:55]\n\t"
        "v_mfma_f32_16x16x32_bf16 v[32:35], v[136:139], v[72:75], v[32:35]\n\t"
        "v_mfma_f32_16x16x32_bf16 v[36:39], v[140:143], v[72:75], v[36:39]\n\t"
        "ds_read_b128 v[68:71], v108 offset:34816\n\t"
        "v_mfma_f32_16x16x32_bf16 v[40:43], v[72:75], v[144:147], v[40:43]\n\t"
        "s_add_u32 m0, %[ld], 66560\n\t"
        "s_add_u32 %[t0], %[xo], 17408\n\t"
        "v_mfma_f32_16x16x32_bf16 v[44:47], v[136:139], v[76:79], v[44:47]\n\t"
        "buffer_load_dwordx4 v100, %[sx], %[t0] offen lds\n\t"
        "v_mfma_f32_16x16x32_bf16 v[48:51], v[140:143], v[76:79], v[48:51]\n\t"
        "ds_read_b128 v[72:75], v107 offset:40960\n\t"
        "v_mfma_f32_16x16x32_bf16 v[52:55], v[76:79], v[144:147], v[52:55]\n\t"
        "v_mfma_f32_16x16x32_bf16 v[32:35], v[148:151], v[80:83], v[32:35]\n\t"
        "v_mfma_f32_16x16x32_bf16 v[36:39], v[152:155], v[80:83], v[36:39]\n\t"
        "ds_read_b128 v[76:79], v107 offset:43008\n\t"
        "v_mfma_f32_16x16x32_bf16 v[40:43], v[80:83], v[156:159], v[40:43]\n\t"
        "s_add_u32 m0, %[ld], 73728\n\t"
        "s_add_u32 %[t0], %[xo], 1152\n\t"
        "v_mfma_f32_16x16x32_bf16 v[44:47], v[148:151], v[84:87], v[44:47]\n\t"
        "buffer_load_dwordx4 v100, %[sx], %[t0] offen lds\n\t"
        "v_mfma_f32_16x16x32_bf16 v[48:51], v[152:155], v[84:87], v[48:51]\n\t"
        "ds_read_b128 v[80:83], v108 offset:40960\n\t"
        "v_mfma_f32_16x16x32_bf16 v[52:55], v[84:87], v[156:159], v[52:55]\n\t"
        "s_waitcnt lgkmcnt(6)\n\t"
        "v_mfma_f32_16x16x32_bf16 v[32:35], v[160:163], v[56:59], v[32:35]\n\t"
        "v_mfma_f32_16x16x32_bf16 v[36:39], v[164:167], v[56:59], v[36:39]\n\t"
        "ds_read_b128 v[84:87], v108 offset:43008\n\t"
        "v_mfma_f32_16x16x32_bf16 v[40:43], v[56:59], v[168:171], v[40:43]\n\t"
        "s_add_u32 m0, %[ld], 74752\n\t"
        "s_add_u32 %[t0], %[xo], 17536\n\t"
        "s_waitcnt lgkmcnt(6)\n\t"
        "v_mfma_f32_16x16x32_bf16 v[44:47], v[160:163], v[60:63], v[44:47]\n\t"
        "buffer_load_dwordx4 v100, %[sx], %[t0] offen lds\n\t"
        "v_mfma_f32_16x16x32_bf16 v[48:51], v[164:167], v[60:63], v[48:51]\n\t"
        "ds_read_b128 v[56:59], v107 offset:49152\n\t"
        "v_mfma_f32_16x16x32_bf16 v[52:55], v[60:63], v[168:171], v[52:55]\n\t"
        "s_waitcnt lgkmcnt(6)\n\t"
        "v_mfma_f32_16x16x32_bf16 v[32:35], v[172:175], v[64:67], v[32:35]\n\t"
        "v_mfma_f32_16x16x32_bf16 v[36:39], v[176:179], v[64:67], v[36:39]\n\t"
        "ds_read_b128 v[60:63], v107 offset:51200\n\t"
        "v_mfma_f32_16x16x32_bf16 v[40:43], v[64:67], v[180:183], v[40:43]\n\t"
        "s_add_u32 m0, %[ld], 81920\n\t"
        "s_add_u32 %[t0], %[xo], 1280\n\t"
        "s_waitcnt lgkmcnt(6)\n\t"
        "v_mfma_f32_16x16x32_bf16 v[44:47], v[172:175], v[68:71], v[44:47]\n\t"
        "buffer_load_dwordx4 v100, %[sx], %[t0] offen lds\n\t"
        "v_mfma_f32_16x16x32_bf16 v[48:51], v[176:179], v[68:71], v[48:51]\n\t"
        "ds_read_b128 v[64:67], v108 offset:49152\n\t"
        "v_mfma_f32_16x16x32_bf16 v[52:55], v[68:71], v[180:183], v[52:55]\n\t"
        "s_waitcnt lgkmcnt(6)\n\t"
        "v_mfma_f32_16x16x32_bf16 v[32:35], v[184:187], v[72:75], v[32:35]\n\t"
        "v_mfma_f32_16x16x32_bf16 v[36:39], v[188:191], v[72:75], v[36:39]\n\t"
        "ds_read_b128 v[68:71], v108 offset:51200\n\t"
        "v_mfma_f32_16x16x32_bf16 v[40:43], v[72:75], v[192:195], v[40:43]\n\t"
        "s_add_u32 m0, %[ld], 82944\n\t"
        "s_add_u32 %[t0], %[xo], 17664\n\t"
        "s_waitcnt lgkmcnt(6)\n\t"
        "v_mfma_f32_16x16x32_bf16 v[44:47], v[184:187], v[76:79], v[44:47]\n\t"
        "buffer_load_dwordx4 v100, %[sx], %[t0] offen lds\n\t"
        "v_mfma_f32_16x16x32_bf16 v[48:51], v[188:191], v[76:79], v[48:51]\n\t"
        "ds_read_b128 v[72:75], v107 offset:57344\n\t"
        "v_mfma_f32_16x16x32_bf16 v[52:55], v[76:79], v[192:195], v[52:55]\n\t"
        "s_waitcnt lgkmcnt(6)\n\t"
        "v_mfma_f32_16x16x32_bf16 v[32:35], v[196:199], v[80:83], v[32:35]\n\t"
        "v_mfma_f32_16x16x32_bf16 v[36:39], v[200:203], v[80:83], v[36:39]\n\t"
        "ds_read_b128 v[76:79], v107 offset:59392\n\t"
        "v_mfma_f32_16x16x32_bf16 v[40:43], v[80:83], v[204:207], v[40:43]\n\t"
        "s_add_u32 m0, %[ld], 90112\n\t"
        "s_add_u32 %[t0], %[xo], 1408\n\t"
        "s_waitcnt lgkmcnt(6)\n\t"
        "v_mfma_f32_16x16x32_bf16 v[44:47], v[196:199], v[84:87], v[44:47]\n\t"
        "buffer_load_dwordx4 v100, %[sx], %[t0] offen lds\n\t"
        "v_mfma_f32_16x16x32_bf16 v[48:51], v[200:203], v[84:87], v[48:51]\n\t"
        "ds_read_b128 v[80:83], v108 offset:57344\n\t"
        "v_mfma_f32_16x16x32_bf16 v[52:55], v[84:87], v[204:207], v[52:55]\n\t"
        "s_waitcnt lgkmcnt(6)\n\t"
        "v_mfma_f32_16x16x32_bf16 v[32:35], v[208:211], v[56:59], v[32:35]\n\t"
        "v_mfma_f32_16x16x32_bf16 v[36:39], v[212:215], v[56:59], v[36:39]\n\t"
        "ds_read_b128 v[84:87], v108 offset:59392\n\t"
        "v_mfma_f32_16x16x32_bf16 v[40:43], v[56:59], v[216:219], v[40:43]\n\t"
        "s_add_u32 m0, %[ld], 91136\n\t"
        "s_add_u32 %[t0], %[xo], 17792\n\t"
        "s_waitcnt lgkmcnt(6)\n\t"
        "v_mfma_f32_16x16x32_bf16 v[44:47], v[208:211], v[60:63], v[44:47]\n\t"
        "buffer_load_dwordx4 v100, %[sx], %[t0] offen lds\n\t"
        "v_mfma_f32_16x16x32_bf16 v[48:51], v[212:215], v[60:63], v[48:51]\n\t"
        "v_mfma_f32_16x16x32_bf16 v[52:55], v[60:63], v[216:219], v[52:55]\n\t"
        "s_waitcnt lgkmcnt(5)\n\t"
        "v_mfma_f32_16x16x32_bf16 v[32:35], v[220:223], v[64:67], v[32:35]\n\t"
        "s_waitcnt vmcnt(17) lgkmcnt(0)\n\t"
        "s_barrier\n\t"
        "v_mfma_f32_16x16x32_bf16 v[36:39], v[224:227], v[64:67], v[36:39]\n\t"
        "ds_read_b128 v[56:59], v105 offset:0\n\t"
        "v_mfma_f32_16x16x32_bf16 v[40:43], v[64:67], v[228:231], v[40:43]\n\t"
        "ds_read_b128 v[60:63], v105 offset:2048\n\t"
        "s_add_u32 m0, %[ld], 98304\n\t"
        "s_add_u32 %[t0], %[xo], 1536\n\t"
        "v_mfma_f32_16x16x32_bf16 v[44:47], v[220:223], v[68:71], v[44:47]\n\t"
        "buffer_load_dwordx4 v100, %[sx], %[t0] offen lds\n\t"
        "v_mfma_f32_16x16x32_bf16 v[48:51], v[224:227], v[68:71], v[48:51]\n\t"
        "ds_read_b128 v[64:67], v106 offset:0\n\t"
        "v_mfma_f32_16x16x32_bf16 v[52:55], v[68:71], v[228:231], v[52:55]\n\t"
        "v_mfma_f32_16x16x32_bf16 v[32:35], v[232:235], v[72:75], v[32:35]\n\t"
        "v_mfma_f32_16x16x32_bf16 v[36:39], v[236:239], v[72:75], v[36:39]\n\t"
        "ds_read_b128 v[68:71], v106 offset:2048\n\t"
        "v_mfma_f32_16x16x32_bf16 v[40:43], v[72:75], v[240:243], v[40:43]\n\t"
        "s_add_u32 m0, %[ld], 99328\n\t"
        "s_add_u32 %[t0], %[xo], 17920\n\t"
        "v_mfma_f32_16x16x32_bf16 v[44:47], v[232:235], v[76:79], v[44:47]\n\t"
        "buffer_load_dwordx4 v100, %[sx], %[t0] offen lds\n\t"
        "v_mfma_f32_16x16x32_bf16 v[48:51], v[236:239], v[76:79], v[48:51]\n\t"
        "ds_read_b128 v[72:75], v105 offset:8192\n\t"
        "v_mfma_f32_16x16x32_bf16 v[52:55], v[76:79], v[240:243], v[52:55]\n\t"
        "v_mfma_f32_16x16x32_bf16 v[32:35], v[244:247], v[80:83], v[32:35]\n\t"
        "v_mfma_f32_16x16x32_bf16 v[36:39], v[248:251], v[80:83], v[36:39]\n\t"
        "ds_read_b128 v[76:79], v105 offset:10240\n\t"
        "v_mfma_f32_16x16x32_bf16 v[40:43], v[80:83], v[252:255], v[40:43]\n\t"
        "s_add_u32 m0, %[ld], 106496\n\t"
        "s_add_u32 %[t0], %[xo], 1664\n\t"
        "v_mfma_f32_16x16x32_bf16 v[44:47], v[244:247], v[84:87], v[44:47]\n\t"
        "buffer_load_dwordx4 v100, %[sx], %[t0] offen lds\n\t"
        "v_mfma_f32_16x16x32_bf16 v[48:51], v[248:251], v[84:87], v[48:51]\n\t"
        "ds_read_b128 v[80:83], v106 offset:8192\n\t"
        "v_mfma_f32_16x16x32_bf16 v[52:55], v[84:87], v[252:255], v[52:55]\n\t"
        "s_cmp_lt_u32 %[cn], 32\n\t"
        "s_cbranch_scc1 LG_loop%=\n\t"
        "s_nop 7\n\t"
        "v_cvt_pk_bf16_f32 v88, v32, v33\n\t"
        "v_cvt_pk_bf16_f32 v89, v34, v35\n\t"
        "v_cvt_pk_bf16_f32 v90, v36, v37\n\t"
        "v_cvt_pk_bf16_f32 v91, v38, v39\n\t"
        "buffer_store_dwordx4 v[88:91], v109, %[sw], %[so] offen sc1\n\t"
        "v_cvt_pk_bf16_f32 v92, v44, v45\n\t"
        "v_cvt_pk_bf16_f32 v93, v46, v47\n\t"
        "v_cvt_pk_bf16_f32 v94, v48, v49\n\t"
        "v_cvt_pk_bf16_f32 v95, v50, v51\n\t"
        "buffer_store_dwordx4 v[92:95], v110, %[sw], %[so] offen sc1\n\t"
        "v_cvt_pk_bf16_f32 v96, v40, v41\n\t"
        "v_cvt_pk_bf16_f32 v97, v42, v43\n\t"
        "v_cvt_pk_bf16_f32 v98, v52, v53\n\t"
        "v_cvt_pk_bf16_f32 v99, v54, v55\n\t"
        "buffer_store_dwordx4 v[96:99], v111, %[sw], %[so] offen sc1\n\t"
        "s_waitcnt vmcnt(0) lgkmcnt(0)\n\t"
        "v_mov_b32_e32 v117, %[cn]\n\t"
        "buffer_store_dword v117, v113, %[sw], 0 offen sc1\n\t"
        "s_barrier\n\t"
        "LG_end%=:\n\t"
        ".Lk_end_G:\n\t"
        : [t0] "=&s"(t0), [xo] "=&s"(xo), [so] "=&s"(so), [cn] "=&s"(cn), [ts] "=&s"(ts), [ta] "=&s"(ta), [tb] "=&s"(tb), [tc] "=&s"(tc), [td] "=&s"(td)
        : [sx] "s"(sx), [sq] "s"(sq), [sv] "s"(sv), [sw] "s"(sw), [ld] "s"(lb + w * 2048),
          "{v100}"((16 * w + (l >> 3)) * 2048 + (((l & 7) ^ (l >> 3)) << 4)), "{v101}"((8 * w + (l >> 3)) * 8192 + h * 256 + (c & 1) * 128 + (l & 7) * 16),
          "{v112}"((16 * w + (l >> 2)) * 8192 + h * 256 + cv * 64 + (l & 3) * 16), "{v102}"(lb + 131072 + w * 1024 + ((l * 16) ^ ((l & 16) << 2))), "{v116}"(lb + 131072 + w * 1024 + ((l * 16) ^ (l & 32))),
          "{v103}"(lr + rr * 128 + ((tn ^ (n >> 3)) * 64) + (n & 3) * 16), "{v104}"(lr + 8192 + rr * 64 + ((tn ^ (g & 1)) * 32) + (n & 3) * 8), "{v105}"(lx), "{v106}"(lx ^ 64u), "{v107}"(lx + 65536), "{v108}"((lx ^ 64u) + 65536),
          "{v109}"(aq), "{v110}"(aq + 4096), "{v111}"(W_VF + (tm * 32 + h) * 8192 + (2 * cv + tn) * 1024 + l * 16), "{v113}"(l == 0 ? F_KVQ + h * 1024 + (c * 4 + w) * 64 : OOB), "{v[114:115]}"(tp)
        : "memory", "scc", "m0", "v8", "v9", "v10", "v11", "v12", "v13", "v14", "v15", "v16", "v17", "v18", "v19", "v20", "v21", "v22", "v23", "v24", "v25", "v26", "v27", "v28", "v29", "v30", "v31", "v32", "v33", "v34", "v35", "v36", "v37", "v38", "v39", "v40", "v41", "v42", "v43", "v44", "v45", "v46", "v47", "v48", "v49", "v50", "v51", "v52", "v53", "v54", "v55", "v56", "v57", "v58", "v59", "v60", "v61", "v62", "v63", "v64", "v65", "v66", "v67", "v68", "v69", "v70", "v71", "v72", "v73", "v74", "v75", "v76", "v77", "v78", "v79", "v80", "v81", "v82", "v83", "v84", "v85", "v86", "v87", "v88", "v89", "v90", "v91", "v92", "v93", "v94", "v95", "v96", "v97", "v98", "v99", "v117", "v118", "v119", "v120", "v121", "v122", "v123", "v124", "v125", "v126", "v127", "v128", "v129", "v130", "v131", "v132", "v133", "v134", "v135", "v136", "v137", "v138", "v139", "v140", "v141", "v142", "v143", "v144", "v145", "v146", "v147", "v148", "v149", "v150", "v151", "v152", "v153", "v154", "v155", "v156", "v157", "v158", "v159", "v160", "v161", "v162", "v163", "v164", "v165", "v166", "v167", "v168", "v169", "v170", "v171", "v172", "v173", "v174", "v175", "v176", "v177", "v178", "v179", "v180", "v181", "v182", "v183", "v184", "v185", "v186", "v187", "v188", "v189", "v190", "v191", "v192", "v193", "v194", "v195", "v196", "v197", "v198", "v199", "v200", "v201", "v202", "v203", "v204", "v205", "v206", "v207", "v208", "v209", "v210", "v211", "v212", "v213", "v214", "v215", "v216", "v217", "v218", "v219", "v220", "v221", "v222", "v223", "v224", "v225", "v226", "v227", "v228", "v229", "v230", "v231", "v232", "v233", "v234", "v235", "v236", "v237", "v238", "v239", "v240", "v241", "v242", "v243", "v244", "v245", "v246", "v247", "v248", "v249", "v250", "v251", "v252", "v253", "v254", "v255", "a0", "a1", "a2", "a3", "a4", "a5", "a6", "a7", "a8", "a9", "a10", "a11", "a12", "a13", "a14", "a15", "a16", "a17", "a18", "a19", "a20", "a21", "a22", "a23", "a24", "a25", "a26", "a27", "a28", "a29", "a30", "a31", "a32", "a33", "a34", "a35", "a36", "a37", "a38", "a39", "a40", "a41", "a42", "a43", "a44", "a45", "a46", "a47", "a48", "a49", "a50", "a51", "a52", "a53", "a54", "a55", "a56", "a57", "a58", "a59", "a60", "a61", "a62", "a63", "a64", "a65", "a66", "a67", "a68", "a69", "a70", "a71", "a72", "a73", "a74", "a75", "a76", "a77", "a78", "a79", "a80", "a81", "a82", "a83", "a84", "a85", "a86", "a87", "a88", "a89", "a90", "a91", "a92", "a93", "a94", "a95", "a96", "a97", "a98", "a99", "a100", "a101", "a102", "a103", "a104", "a105", "a106", "a107", "a108", "a109", "a110", "a111", "a112", "a113", "a114", "a115", "a116", "a117", "a118", "a119", "a120", "a121", "a122", "a123", "a124", "a125", "a126", "a127", "a128", "a129", "a130", "a131", "a132", "a133", "a134", "a135", "a136", "a137", "a138", "a139", "a140", "a141", "a142", "a143", "a144", "a145", "a146", "a147", "a148", "a149", "a150", "a151", "a152", "a153", "a154", "a155", "a156", "a157", "a158", "a159", "a160", "a161", "a162", "a163", "a164", "a165", "a166", "a167", "a168", "a169", "a170", "a171", "a172", "a173", "a174", "a175", "a176", "a177", "a178", "a179", "a180", "a181", "a182", "a183", "a184", "a185", "a186", "a187", "a188", "a189", "a190", "a191", "a192", "a193", "a194", "a195", "a196", "a197", "a198", "a199", "a200", "a201", "a202", "a203", "a204", "a205", "a206", "a207", "a208", "a209", "a210", "a211", "a212", "a213", "a214", "a215", "a216", "a217", "a218", "a219", "a220", "a221", "a222", "a223", "a224", "a225", "a226", "a227", "a228", "a229", "a230", "a231", "a232", "a233", "a234", "a235", "a236", "a237", "a238", "a239", "a240", "a241", "a242", "a243", "a244", "a245", "a246", "a247", "a248", "a249", "a250", "a251", "a252", "a253", "a254", "a255");
}

__device__ __forceinline__ void out_block(const Params& p, unsigned char* lds, const int wv, const int x, const int nt, const int j) {
    const int w = wv, l = otid() & 63, n = l & 15, g = l >> 4;
    const unsigned lb = __builtin_amdgcn_readfirstlane((unsigned)(size_t)lds), stg = lb + w * 8192;
    const v4u so = mk_srd(p.Wo, 1024 * 4096 * 2), sw = mk_srd(p.ws, W_TOTAL), su = mk_srd(p.out, S * D * 2);
    unsigned t0, t1, tl = x, pg, nx, sp, law = lb + w * 2304 + n * 144 + g * 16, lar = lb + (4 * w + g) * 144 + n * 8; unsigned long long ts;
    asm volatile(
        "s_mov_b32 %[t0], 0\n\t"
        "buffer_load_dwordx4 a[0:3], v100, %[so], %[t0] offen\n\t"
        "s_mov_b32 %[t0], 32768\n\t"
        "buffer_load_dwordx4 a[4:7], v100, %[so], %[t0] offen\n\t"
        "s_mov_b32 %[t0], 65536\n\t"
        "buffer_load_dwordx4 a[8:11], v100, %[so], %[t0] offen\n\t"
        "s_mov_b32 %[t0], 98304\n\t"
        "buffer_load_dwordx4 a[12:15], v100, %[so], %[t0] offen\n\t"
        "s_mov_b32 %[t0], 131072\n\t"
        "buffer_load_dwordx4 a[16:19], v100, %[so], %[t0] offen\n\t"
        "s_mov_b32 %[t0], 163840\n\t"
        "buffer_load_dwordx4 a[20:23], v100, %[so], %[t0] offen\n\t"
        "s_mov_b32 %[t0], 196608\n\t"
        "buffer_load_dwordx4 a[24:27], v100, %[so], %[t0] offen\n\t"
        "s_mov_b32 %[t0], 229376\n\t"
        "buffer_load_dwordx4 a[28:31], v100, %[so], %[t0] offen\n\t"
        "s_mov_b32 %[t0], 262144\n\t"
        "buffer_load_dwordx4 a[32:35], v100, %[so], %[t0] offen\n\t"
        "s_mov_b32 %[t0], 294912\n\t"
        "buffer_load_dwordx4 a[36:39], v100, %[so], %[t0] offen\n\t"
        "s_mov_b32 %[t0], 327680\n\t"
        "buffer_load_dwordx4 a[40:43], v100, %[so], %[t0] offen\n\t"
        "s_mov_b32 %[t0], 360448\n\t"
        "buffer_load_dwordx4 a[44:47], v100, %[so], %[t0] offen\n\t"
        "s_mov_b32 %[t0], 393216\n\t"
        "buffer_load_dwordx4 a[48:51], v100, %[so], %[t0] offen\n\t"
        "s_mov_b32 %[t0], 425984\n\t"
        "buffer_load_dwordx4 a[52:55], v100, %[so], %[t0] offen\n\t"
        "s_mov_b32 %[t0], 458752\n\t"
        "buffer_load_dwordx4 a[56:59], v100, %[so], %[t0] offen\n\t"
        "s_mov_b32 %[t0], 491520\n\t"
        "buffer_load_dwordx4 a[60:63], v100, %[so], %[t0] offen\n\t"
        "s_mov_b32 %[t0], 524288\n\t"
        "buffer_load_dwordx4 a[64:67], v100, %[so], %[t0] offen\n\t"
        "s_mov_b32 %[t0], 557056\n\t"
        "buffer_load_dwordx4 a[68:71], v100, %[so], %[t0] offen\n\t"
        "s_mov_b32 %[t0], 589824\n\t"
        "buffer_load_dwordx4 a[72:75], v100, %[so], %[t0] offen\n\t"
        "s_mov_b32 %[t0], 622592\n\t"
        "buffer_load_dwordx4 a[76:79], v100, %[so], %[t0] offen\n\t"
        "s_mov_b32 %[t0], 655360\n\t"
        "buffer_load_dwordx4 a[80:83], v100, %[so], %[t0] offen\n\t"
        "s_mov_b32 %[t0], 688128\n\t"
        "buffer_load_dwordx4 a[84:87], v100, %[so], %[t0] offen\n\t"
        "s_mov_b32 %[t0], 720896\n\t"
        "buffer_load_dwordx4 a[88:91], v100, %[so], %[t0] offen\n\t"
        "s_mov_b32 %[t0], 753664\n\t"
        "buffer_load_dwordx4 a[92:95], v100, %[so], %[t0] offen\n\t"
        "s_waitcnt vmcnt(16)\n\t"
        "ds_write_b128 v101, a[0:3] offset:0\n\t"
        "ds_write_b128 v101, a[4:7] offset:1024\n\t"
        "ds_write_b128 v101, a[8:11] offset:2048\n\t"
        "ds_write_b128 v101, a[12:15] offset:3072\n\t"
        "ds_write_b128 v101, a[16:19] offset:4096\n\t"
        "ds_write_b128 v101, a[20:23] offset:5120\n\t"
        "ds_write_b128 v101, a[24:27] offset:6144\n\t"
        "ds_write_b128 v101, a[28:31] offset:7168\n\t"
        "ds_read_b64_tr_b16 a[0:1], v102 offset:0\n\t"
        "ds_read_b64_tr_b16 a[2:3], v102 offset:1024\n\t"
        "ds_read_b64_tr_b16 a[4:5], v102 offset:32\n\t"
        "ds_read_b64_tr_b16 a[6:7], v102 offset:1056\n\t"
        "s_waitcnt lgkmcnt(8)\n\t"
        "ds_read_b64_tr_b16 a[8:9], v102 offset:2048\n\t"
        "ds_read_b64_tr_b16 a[10:11], v102 offset:3072\n\t"
        "ds_read_b64_tr_b16 a[12:13], v102 offset:2080\n\t"
        "ds_read_b64_tr_b16 a[14:15], v102 offset:3104\n\t"
        "s_waitcnt lgkmcnt(8)\n\t"
        "ds_read_b64_tr_b16 a[16:17], v102 offset:4096\n\t"
        "ds_read_b64_tr_b16 a[18:19], v102 offset:5120\n\t"
        "ds_read_b64_tr_b16 a[20:21], v102 offset:4128\n\t"
        "ds_read_b64_tr_b16 a[22:23], v102 offset:5152\n\t"
        "s_waitcnt lgkmcnt(8)\n\t"
        "ds_read_b64_tr_b16 a[24:25], v102 offset:6144\n\t"
        "ds_read_b64_tr_b16 a[26:27], v102 offset:7168\n\t"
        "ds_read_b64_tr_b16 a[28:29], v102 offset:6176\n\t"
        "ds_read_b64_tr_b16 a[30:31], v102 offset:7200\n\t"
        "s_waitcnt lgkmcnt(8)\n\t"
        "s_mov_b32 %[t0], 786432\n\t"
        "buffer_load_dwordx4 a[96:99], v100, %[so], %[t0] offen\n\t"
        "s_mov_b32 %[t0], 819200\n\t"
        "buffer_load_dwordx4 a[100:103], v100, %[so], %[t0] offen\n\t"
        "s_mov_b32 %[t0], 851968\n\t"
        "buffer_load_dwordx4 a[104:107], v100, %[so], %[t0] offen\n\t"
        "s_mov_b32 %[t0], 884736\n\t"
        "buffer_load_dwordx4 a[108:111], v100, %[so], %[t0] offen\n\t"
        "s_mov_b32 %[t0], 917504\n\t"
        "buffer_load_dwordx4 a[112:115], v100, %[so], %[t0] offen\n\t"
        "s_mov_b32 %[t0], 950272\n\t"
        "buffer_load_dwordx4 a[116:119], v100, %[so], %[t0] offen\n\t"
        "s_mov_b32 %[t0], 983040\n\t"
        "buffer_load_dwordx4 a[120:123], v100, %[so], %[t0] offen\n\t"
        "s_mov_b32 %[t0], 1015808\n\t"
        "buffer_load_dwordx4 a[124:127], v100, %[so], %[t0] offen\n\t"
        "s_waitcnt vmcnt(16)\n\t"
        "ds_write_b128 v101, a[32:35] offset:0\n\t"
        "ds_write_b128 v101, a[36:39] offset:1024\n\t"
        "ds_write_b128 v101, a[40:43] offset:2048\n\t"
        "ds_write_b128 v101, a[44:47] offset:3072\n\t"
        "ds_write_b128 v101, a[48:51] offset:4096\n\t"
        "ds_write_b128 v101, a[52:55] offset:5120\n\t"
        "ds_write_b128 v101, a[56:59] offset:6144\n\t"
        "ds_write_b128 v101, a[60:63] offset:7168\n\t"
        "ds_read_b64_tr_b16 a[32:33], v102 offset:0\n\t"
        "ds_read_b64_tr_b16 a[34:35], v102 offset:1024\n\t"
        "ds_read_b64_tr_b16 a[36:37], v102 offset:32\n\t"
        "ds_read_b64_tr_b16 a[38:39], v102 offset:1056\n\t"
        "s_waitcnt lgkmcnt(8)\n\t"
        "ds_read_b64_tr_b16 a[40:41], v102 offset:2048\n\t"
        "ds_read_b64_tr_b16 a[42:43], v102 offset:3072\n\t"
        "ds_read_b64_tr_b16 a[44:45], v102 offset:2080\n\t"
        "ds_read_b64_tr_b16 a[46:47], v102 offset:3104\n\t"
        "s_waitcnt lgkmcnt(8)\n\t"
        "ds_read_b64_tr_b16 a[48:49], v102 offset:4096\n\t"
        "ds_read_b64_tr_b16 a[50:51], v102 offset:5120\n\t"
        "ds_read_b64_tr_b16 a[52:53], v102 offset:4128\n\t"
        "ds_read_b64_tr_b16 a[54:55], v102 offset:5152\n\t"
        "s_waitcnt lgkmcnt(8)\n\t"
        "ds_read_b64_tr_b16 a[56:57], v102 offset:6144\n\t"
        "ds_read_b64_tr_b16 a[58:59], v102 offset:7168\n\t"
        "ds_read_b64_tr_b16 a[60:61], v102 offset:6176\n\t"
        "ds_read_b64_tr_b16 a[62:63], v102 offset:7200\n\t"
        "s_waitcnt lgkmcnt(8)\n\t"
        "s_mov_b32 %[t0], 1048576\n\t"
        "buffer_load_dwordx4 a[128:131], v100, %[so], %[t0] offen\n\t"
        "s_mov_b32 %[t0], 1081344\n\t"
        "buffer_load_dwordx4 a[132:135], v100, %[so], %[t0] offen\n\t"
        "s_mov_b32 %[t0], 1114112\n\t"
        "buffer_load_dwordx4 a[136:139], v100, %[so], %[t0] offen\n\t"
        "s_mov_b32 %[t0], 1146880\n\t"
        "buffer_load_dwordx4 a[140:143], v100, %[so], %[t0] offen\n\t"
        "s_mov_b32 %[t0], 1179648\n\t"
        "buffer_load_dwordx4 a[144:147], v100, %[so], %[t0] offen\n\t"
        "s_mov_b32 %[t0], 1212416\n\t"
        "buffer_load_dwordx4 a[148:151], v100, %[so], %[t0] offen\n\t"
        "s_mov_b32 %[t0], 1245184\n\t"
        "buffer_load_dwordx4 a[152:155], v100, %[so], %[t0] offen\n\t"
        "s_mov_b32 %[t0], 1277952\n\t"
        "buffer_load_dwordx4 a[156:159], v100, %[so], %[t0] offen\n\t"
        "s_waitcnt vmcnt(16)\n\t"
        "ds_write_b128 v101, a[64:67] offset:0\n\t"
        "ds_write_b128 v101, a[68:71] offset:1024\n\t"
        "ds_write_b128 v101, a[72:75] offset:2048\n\t"
        "ds_write_b128 v101, a[76:79] offset:3072\n\t"
        "ds_write_b128 v101, a[80:83] offset:4096\n\t"
        "ds_write_b128 v101, a[84:87] offset:5120\n\t"
        "ds_write_b128 v101, a[88:91] offset:6144\n\t"
        "ds_write_b128 v101, a[92:95] offset:7168\n\t"
        "ds_read_b64_tr_b16 a[64:65], v102 offset:0\n\t"
        "ds_read_b64_tr_b16 a[66:67], v102 offset:1024\n\t"
        "ds_read_b64_tr_b16 a[68:69], v102 offset:32\n\t"
        "ds_read_b64_tr_b16 a[70:71], v102 offset:1056\n\t"
        "s_waitcnt lgkmcnt(8)\n\t"
        "ds_read_b64_tr_b16 a[72:73], v102 offset:2048\n\t"
        "ds_read_b64_tr_b16 a[74:75], v102 offset:3072\n\t"
        "ds_read_b64_tr_b16 a[76:77], v102 offset:2080\n\t"
        "ds_read_b64_tr_b16 a[78:79], v102 offset:3104\n\t"
        "s_waitcnt lgkmcnt(8)\n\t"
        "ds_read_b64_tr_b16 a[80:81], v102 offset:4096\n\t"
        "ds_read_b64_tr_b16 a[82:83], v102 offset:5120\n\t"
        "ds_read_b64_tr_b16 a[84:85], v102 offset:4128\n\t"
        "ds_read_b64_tr_b16 a[86:87], v102 offset:5152\n\t"
        "s_waitcnt lgkmcnt(8)\n\t"
        "ds_read_b64_tr_b16 a[88:89], v102 offset:6144\n\t"
        "ds_read_b64_tr_b16 a[90:91], v102 offset:7168\n\t"
        "ds_read_b64_tr_b16 a[92:93], v102 offset:6176\n\t"
        "ds_read_b64_tr_b16 a[94:95], v102 offset:7200\n\t"
        "s_waitcnt lgkmcnt(8)\n\t"
        "s_mov_b32 %[t0], 1310720\n\t"
        "buffer_load_dwordx4 a[160:163], v100, %[so], %[t0] offen\n\t"
        "s_mov_b32 %[t0], 1343488\n\t"
        "buffer_load_dwordx4 a[164:167], v100, %[so], %[t0] offen\n\t"
        "s_mov_b32 %[t0], 1376256\n\t"
        "buffer_load_dwordx4 a[168:171], v100, %[so], %[t0] offen\n\t"
        "s_mov_b32 %[t0], 1409024\n\t"
        "buffer_load_dwordx4 a[172:175], v100, %[so], %[t0] offen\n\t"
        "s_mov_b32 %[t0], 1441792\n\t"
        "buffer_load_dwordx4 a[176:179], v100, %[so], %[t0] offen\n\t"
        "s_mov_b32 %[t0], 1474560\n\t"
        "buffer_load_dwordx4 a[180:183], v100, %[so], %[t0] offen\n\t"
        "s_mov_b32 %[t0], 1507328\n\t"
        "buffer_load_dwordx4 a[184:187], v100, %[so], %[t0] offen\n\t"
        "s_mov_b32 %[t0], 1540096\n\t"
        "buffer_load_dwordx4 a[188:191], v100, %[so], %[t0] offen\n\t"
        "s_waitcnt vmcnt(16)\n\t"
        "ds_write_b128 v101, a[96:99] offset:0\n\t"
        "ds_write_b128 v101, a[100:103] offset:1024\n\t"
        "ds_write_b128 v101, a[104:107] offset:2048\n\t"
        "ds_write_b128 v101, a[108:111] offset:3072\n\t"
        "ds_write_b128 v101, a[112:115] offset:4096\n\t"
        "ds_write_b128 v101, a[116:119] offset:5120\n\t"
        "ds_write_b128 v101, a[120:123] offset:6144\n\t"
        "ds_write_b128 v101, a[124:127] offset:7168\n\t"
        "ds_read_b64_tr_b16 a[96:97], v102 offset:0\n\t"
        "ds_read_b64_tr_b16 a[98:99], v102 offset:1024\n\t"
        "ds_read_b64_tr_b16 a[100:101], v102 offset:32\n\t"
        "ds_read_b64_tr_b16 a[102:103], v102 offset:1056\n\t"
        "s_waitcnt lgkmcnt(8)\n\t"
        "ds_read_b64_tr_b16 a[104:105], v102 offset:2048\n\t"
        "ds_read_b64_tr_b16 a[106:107], v102 offset:3072\n\t"
        "ds_read_b64_tr_b16 a[108:109], v102 offset:2080\n\t"
        "ds_read_b64_tr_b16 a[110:111], v102 offset:3104\n\t"
        "s_waitcnt lgkmcnt(8)\n\t"
        "ds_read_b64_tr_b16 a[112:113], v102 offset:4096\n\t"
        "ds_read_b64_tr_b16 a[114:115], v102 offset:5120\n\t"
        "ds_read_b64_tr_b16 a[116:117], v102 offset:4128\n\t"
        "ds_read_b64_tr_b16 a[118:119], v102 offset:5152\n\t"
        "s_waitcnt lgkmcnt(8)\n\t"
        "ds_read_b64_tr_b16 a[120:121], v102 offset:6144\n\t"
        "ds_read_b64_tr_b16 a[122:123], v102 offset:7168\n\t"
        "ds_read_b64_tr_b16 a[124:125], v102 offset:6176\n\t"
        "ds_read_b64_tr_b16 a[126:127], v102 offset:7200\n\t"
        "s_waitcnt lgkmcnt(8)\n\t"
        "s_mov_b32 %[t0], 1572864\n\t"
        "buffer_load_dwordx4 a[192:195], v100, %[so], %[t0] offen\n\t"
        "s_mov_b32 %[t0], 1605632\n\t"
        "buffer_load_dwordx4 a[196:199], v100, %[so], %[t0] offen\n\t"
        "s_mov_b32 %[t0], 1638400\n\t"
        "buffer_load_dwordx4 a[200:203], v100, %[so], %[t0] offen\n\t"
        "s_mov_b32 %[t0], 1671168\n\t"
        "buffer_load_dwordx4 a[204:207], v100, %[so], %[t0] offen\n\t"
        "s_mov_b32 %[t0], 1703936\n\t"
        "buffer_load_dwordx4 a[208:211], v100, %[so], %[t0] offen\n\t"
        "s_mov_b32 %[t0], 1736704\n\t"
        "buffer_load_dwordx4 a[212:215], v100, %[so], %[t0] offen\n\t"
        "s_mov_b32 %[t0], 1769472\n\t"
        "buffer_load_dwordx4 a[216:219], v100, %[so], %[t0] offen\n\t"
        "s_mov_b32 %[t0], 1802240\n\t"
        "buffer_load_dwordx4 a[220:223], v100, %[so], %[t0] offen\n\t"
        "s_waitcnt vmcnt(16)\n\t"
        "ds_write_b128 v101, a[128:131] offset:0\n\t"
        "ds_write_b128 v101, a[132:135] offset:1024\n\t"
        "ds_write_b128 v101, a[136:139] offset:2048\n\t"
        "ds_write_b128 v101, a[140:143] offset:3072\n\t"
        "ds_write_b128 v101, a[144:147] offset:4096\n\t"
        "ds_write_b128 v101, a[148:151] offset:5120\n\t"
        "ds_write_b128 v101, a[152:155] offset:6144\n\t"
        "ds_write_b128 v101, a[156:159] offset:7168\n\t"
        "ds_read_b64_tr_b16 a[128:129], v102 offset:0\n\t"
        "ds_read_b64_tr_b16 a[130:131], v102 offset:1024\n\t"
        "ds_read_b64_tr_b16 a[132:133], v102 offset:32\n\t"
        "ds_read_b64_tr_b16 a[134:135], v102 offset:1056\n\t"
        "s_waitcnt lgkmcnt(8)\n\t"
        "ds_read_b64_tr_b16 a[136:137], v102 offset:2048\n\t"
        "ds_read_b64_tr_b16 a[138:139], v102 offset:3072\n\t"
        "ds_read_b64_tr_b16 a[140:141], v102 offset:2080\n\t"
        "ds_read_b64_tr_b16 a[142:143], v102 offset:3104\n\t"
        "s_waitcnt lgkmcnt(8)\n\t"
        "ds_read_b64_tr_b16 a[144:145], v102 offset:4096\n\t"
        "ds_read_b64_tr_b16 a[146:147], v102 offset:5120\n\t"
        "ds_read_b64_tr_b16 a[148:149], v102 offset:4128\n\t"
        "ds_read_b64_tr_b16 a[150:151], v102 offset:5152\n\t"
        "s_waitcnt lgkmcnt(8)\n\t"
        "ds_read_b64_tr_b16 a[152:153], v102 offset:6144\n\t"
        "ds_read_b64_tr_b16 a[154:155], v102 offset:7168\n\t"
        "ds_read_b64_tr_b16 a[156:157], v102 offset:6176\n\t"
        "ds_read_b64_tr_b16 a[158:159], v102 offset:7200\n\t"
        "s_waitcnt lgkmcnt(8)\n\t"
        "s_mov_b32 %[t0], 1835008\n\t"
        "buffer_load_dwordx4 a[224:227], v100, %[so], %[t0] offen\n\t"
        "s_mov_b32 %[t0], 1867776\n\t"
        "buffer_load_dwordx4 a[228:231], v100, %[so], %[t0] offen\n\t"
        "s_mov_b32 %[t0], 1900544\n\t"
        "buffer_load_dwordx4 a[232:235], v100, %[so], %[t0] offen\n\t"
        "s_mov_b32 %[t0], 1933312\n\t"
        "buffer_load_dwordx4 a[236:239], v100, %[so], %[t0] offen\n\t"
        "s_mov_b32 %[t0], 1966080\n\t"
        "buffer_load_dwordx4 a[240:243], v100, %[so], %[t0] offen\n\t"
        "s_mov_b32 %[t0], 1998848\n\t"
        "buffer_load_dwordx4 a[244:247], v100, %[so], %[t0] offen\n\t"
        "s_mov_b32 %[t0], 2031616\n\t"
        "buffer_load_dwordx4 a[248:251], v100, %[so], %[t0] offen\n\t"
        "s_mov_b32 %[t0], 2064384\n\t"
        "buffer_load_dwordx4 a[252:255], v100, %[so], %[t0] offen\n\t"
        "s_waitcnt vmcnt(16)\n\t"
        "ds_write_b128 v101, a[160:163] offset:0\n\t"
        "ds_write_b128 v101, a[164:167] offset:1024\n\t"
        "ds_write_b128 v101, a[168:171] offset:2048\n\t"
        "ds_write_b128 v101, a[172:175] offset:3072\n\t"
        "ds_write_b128 v101, a[176:179] offset:4096\n\t"
        "ds_write_b128 v101, a[180:183] offset:5120\n\t"
        "ds_write_b128 v101, a[184:187] offset:6144\n\t"
        "ds_write_b128 v101, a[188:191] offset:7168\n\t"
        "ds_read_b64_tr_b16 a[160:161], v102 offset:0\n\t"
        "ds_read_b64_tr_b16 a[162:163], v102 offset:1024\n\t"
        "ds_read_b64_tr_b16 a[164:165], v102 offset:32\n\t"
        "ds_read_b64_tr_b16 a[166:167], v102 offset:1056\n\t"
        "s_waitcnt lgkmcnt(8)\n\t"
        "ds_read_b64_tr_b16 a[168:169], v102 offset:2048\n\t"
        "ds_read_b64_tr_b16 a[170:171], v102 offset:3072\n\t"
        "ds_read_b64_tr_b16 a[172:173], v102 offset:2080\n\t"
        "ds_read_b64_tr_b16 a[174:175], v102 offset:3104\n\t"
        "s_waitcnt lgkmcnt(8)\n\t"
        "ds_read_b64_tr_b16 a[176:177], v102 offset:4096\n\t"
        "ds_read_b64_tr_b16 a[178:179], v102 offset:5120\n\t"
        "ds_read_b64_tr_b16 a[180:181], v102 offset:4128\n\t"
        "ds_read_b64_tr_b16 a[182:183], v102 offset:5152\n\t"
        "s_waitcnt lgkmcnt(8)\n\t"
        "ds_read_b64_tr_b16 a[184:185], v102 offset:6144\n\t"
        "ds_read_b64_tr_b16 a[186:187], v102 offset:7168\n\t"
        "ds_read_b64_tr_b16 a[188:189], v102 offset:6176\n\t"
        "ds_read_b64_tr_b16 a[190:191], v102 offset:7200\n\t"
        "s_waitcnt lgkmcnt(8)\n\t"
        "s_waitcnt vmcnt(8)\n\t"
        "ds_write_b128 v101, a[192:195] offset:0\n\t"
        "ds_write_b128 v101, a[196:199] offset:1024\n\t"
        "ds_write_b128 v101, a[200:203] offset:2048\n\t"
        "ds_write_b128 v101, a[204:207] offset:3072\n\t"
        "ds_write_b128 v101, a[208:211] offset:4096\n\t"
        "ds_write_b128 v101, a[212:215] offset:5120\n\t"
        "ds_write_b128 v101, a[216:219] offset:6144\n\t"
        "ds_write_b128 v101, a[220:223] offset:7168\n\t"
        "ds_read_b64_tr_b16 a[192:193], v102 offset:0\n\t"
        "ds_read_b64_tr_b16 a[194:195], v102 offset:1024\n\t"
        "ds_read_b64_tr_b16 a[196:197], v102 offset:32\n\t"
        "ds_read_b64_tr_b16 a[198:199], v102 offset:1056\n\t"
        "s_waitcnt lgkmcnt(8)\n\t"
        "ds_read_b64_tr_b16 a[200:201], v102 offset:2048\n\t"
        "ds_read_b64_tr_b16 a[202:203], v102 offset:3072\n\t"
        "ds_read_b64_tr_b16 a[204:205], v102 offset:2080\n\t"
        "ds_read_b64_tr_b16 a[206:207], v102 offset:3104\n\t"
        "s_waitcnt lgkmcnt(8)\n\t"
        "ds_read_b64_tr_b16 a[208:209], v102 offset:4096\n\t"
        "ds_read_b64_tr_b16 a[210:211], v102 offset:5120\n\t"
        "ds_read_b64_tr_b16 a[212:213], v102 offset:4128\n\t"
        "ds_read_b64_tr_b16 a[214:215], v102 offset:5152\n\t"
        "s_waitcnt lgkmcnt(8)\n\t"
        "ds_read_b64_tr_b16 a[216:217], v102 offset:6144\n\t"
        "ds_read_b64_tr_b16 a[218:219], v102 offset:7168\n\t"
        "ds_read_b64_tr_b16 a[220:221], v102 offset:6176\n\t"
        "ds_read_b64_tr_b16 a[222:223], v102 offset:7200\n\t"
        "s_waitcnt lgkmcnt(8)\n\t"
        "s_waitcnt vmcnt(0)\n\t"
        "ds_write_b128 v101, a[224:227] offset:0\n\t"
        "ds_write_b128 v101, a[228:231] offset:1024\n\t"
        "ds_write_b128 v101, a[232:235] offset:2048\n\t"
        "ds_write_b128 v101, a[236:239] offset:3072\n\t"
        "ds_write_b128 v101, a[240:243] offset:4096\n\t"
        "ds_write_b128 v101, a[244:247] offset:5120\n\t"
        "ds_write_b128 v101, a[248:251] offset:6144\n\t"
        "ds_write_b128 v101, a[252:255] offset:7168\n\t"
        "ds_read_b64_tr_b16 a[224:225], v102 offset:0\n\t"
        "ds_read_b64_tr_b16 a[226:227], v102 offset:1024\n\t"
        "ds_read_b64_tr_b16 a[228:229], v102 offset:32\n\t"
        "ds_read_b64_tr_b16 a[230:231], v102 offset:1056\n\t"
        "s_waitcnt lgkmcnt(8)\n\t"
        "ds_read_b64_tr_b16 a[232:233], v102 offset:2048\n\t"
        "ds_read_b64_tr_b16 a[234:235], v102 offset:3072\n\t"
        "ds_read_b64_tr_b16 a[236:237], v102 offset:2080\n\t"
        "ds_read_b64_tr_b16 a[238:239], v102 offset:3104\n\t"
        "s_waitcnt lgkmcnt(8)\n\t"
        "ds_read_b64_tr_b16 a[240:241], v102 offset:4096\n\t"
        "ds_read_b64_tr_b16 a[242:243], v102 offset:5120\n\t"
        "ds_read_b64_tr_b16 a[244:245], v102 offset:4128\n\t"
        "ds_read_b64_tr_b16 a[246:247], v102 offset:5152\n\t"
        "s_waitcnt lgkmcnt(8)\n\t"
        "ds_read_b64_tr_b16 a[248:249], v102 offset:6144\n\t"
        "ds_read_b64_tr_b16 a[250:251], v102 offset:7168\n\t"
        "ds_read_b64_tr_b16 a[252:253], v102 offset:6176\n\t"
        "ds_read_b64_tr_b16 a[254:255], v102 offset:7200\n\t"
        "s_waitcnt lgkmcnt(8)\n\t"
        "s_waitcnt lgkmcnt(0)\n\t"
        "s_mov_b32 %[sp], 0\n\t"
        "s_mov_b32 %[t1], 32768\n\t"
        "LP_cv%=:\n\t"
        "buffer_load_dword v28, off, %[sw], %[t1] sc1\n\t"
        "s_waitcnt vmcnt(0)\n\t"
        "v_readfirstlane_b32 %[t0], v28\n\t"
        "s_cmp_ge_u32 %[t0], 32\n\t"
        "s_cbranch_scc1 LP_cvok%=\n\t"
        "s_sleep 8\n\t"
        "s_add_u32 %[sp], %[sp], 1\n\t"
        "s_cmp_lt_u32 %[sp], 0x100000\n\t"
        "s_cbranch_scc1 LP_cv%=\n\t"
        "v_mov_b32_e32 v28, 256\n\t"
        "s_mov_b32 %[t0], 33024\n\t"
        "buffer_store_dword v28, off, %[sw], %[t0] sc1\n\t"
        "s_endpgm\n\t"
        "LP_cvok%=:\n\t"
        "s_barrier\n\t"
        "LP_wait%=:\n\t"
        "s_mov_b32 %[sp], 0\n\t"
        "LP_poll%=:\n\t"
        "buffer_load_dword v26, v104, %[sw], 0 offen sc1\n\t"
        "s_lshr_b32 %[t1], %[tl], 1\n\t"
        "s_waitcnt vmcnt(0)\n\t"
        "v_min_u32_dpp v26, v26, v26 quad_perm:[1,0,3,2] row_mask:0xf bank_mask:0xf\n\t"
        "s_nop 1\n\t"
        "v_min_u32_dpp v26, v26, v26 quad_perm:[2,3,0,1] row_mask:0xf bank_mask:0xf\n\t"
        "s_nop 1\n\t"
        "v_min_u32_dpp v26, v26, v26 row_ror:4 row_mask:0xf bank_mask:0xf\n\t"
        "s_nop 0\n\t"
        "v_readfirstlane_b32 %[pg], v26\n\t"
        "s_cmp_gt_u32 %[pg], %[t1]\n\t"
        "s_cbranch_scc1 LP_have%=\n\t"
        "s_cmp_eq_u32 %[pg], %[t1]\n\t"
        "s_cbranch_scc1 LP_near%=\n\t"
        "s_sleep 24\n\t"
        "LP_near%=:\n\t"
        "s_sleep 2\n\t"
        "s_add_u32 %[sp], %[sp], 1\n\t"
        "s_cmp_lt_u32 %[sp], 0x100000\n\t"
        "s_cbranch_scc1 LP_poll%=\n\t"
        "v_mov_b32_e32 v28, 128\n\t"
        "s_mov_b32 %[t0], 33024\n\t"
        "buffer_store_dword v28, off, %[sw], %[t0] sc1\n\t"
        "s_endpgm\n\t"
        "LP_have%=:\n\t"
        "s_lshl_b32 %[nx], %[tl], 17\n\t"
        "s_add_u32 %[t0], %[nx], 0\n\t"
        "buffer_load_dwordx4 v[128:131], v103, %[sw], %[t0] offen offset:0 sc1\n\t"
        "buffer_load_dwordx4 v[132:135], v103, %[sw], %[t0] offen offset:1024 sc1\n\t"
        "buffer_load_dwordx4 v[136:139], v103, %[sw], %[t0] offen offset:2048 sc1\n\t"
        "buffer_load_dwordx4 v[140:143], v103, %[sw], %[t0] offen offset:3072 sc1\n\t"
        "s_add_u32 %[t0], %[nx], 4096\n\t"
        "buffer_load_dwordx4 v[144:147], v103, %[sw], %[t0] offen offset:0 sc1\n\t"
        "buffer_load_dwordx4 v[148:151], v103, %[sw], %[t0] offen offset:1024 sc1\n\t"
        "buffer_load_dwordx4 v[152:155], v103, %[sw], %[t0] offen offset:2048 sc1\n\t"
        "buffer_load_dwordx4 v[156:159], v103, %[sw], %[t0] offen offset:3072 sc1\n\t"
        "s_add_u32 %[t0], %[nx], 8192\n\t"
        "buffer_load_dwordx4 v[160:163], v103, %[sw], %[t0] offen offset:0 sc1\n\t"
        "buffer_load_dwordx4 v[164:167], v103, %[sw], %[t0] offen offset:1024 sc1\n\t"
        "buffer_load_dwordx4 v[168:171], v103, %[sw], %[t0] offen offset:2048 sc1\n\t"
        "buffer_load_dwordx4 v[172:175], v103, %[sw], %[t0] offen offset:3072 sc1\n\t"
        "s_add_u32 %[t0], %[nx], 12288\n\t"
        "buffer_load_dwordx4 v[176:179], v103, %[sw], %[t0] offen offset:0 sc1\n\t"
        "buffer_load_dwordx4 v[180:183], v103, %[sw], %[t0] offen offset:1024 sc1\n\t"
        "buffer_load_dwordx4 v[184:187], v103, %[sw], %[t0] offen offset:2048 sc1\n\t"
        "buffer_load_dwordx4 v[188:191], v103, %[sw], %[t0] offen offset:3072 sc1\n\t"
        "s_add_u32 %[t0], %[nx], 16384\n\t"
        "buffer_load_dwordx4 v[192:195], v103, %[sw], %[t0] offen offset:0 sc1\n\t"
        "buffer_load_dwordx4 v[196:199], v103, %[sw], %[t0] offen offset:1024 sc1\n\t"
        "buffer_load_dwordx4 v[200:203], v103, %[sw], %[t0] offen offset:2048 sc1\n\t"
        "buffer_load_dwordx4 v[204:207], v103, %[sw], %[t0] offen offset:3072 sc1\n\t"
        "s_add_u32 %[t0], %[nx], 20480\n\t"
        "buffer_load_dwordx4 v[208:211], v103, %[sw], %[t0] offen offset:0 sc1\n\t"
        "buffer_load_dwordx4 v[212:215], v103, %[sw], %[t0] offen offset:1024 sc1\n\t"
        "buffer_load_dwordx4 v[216:219], v103, %[sw], %[t0] offen offset:2048 sc1\n\t"
        "buffer_load_dwordx4 v[220:223], v103, %[sw], %[t0] offen offset:3072 sc1\n\t"
        "s_add_u32 %[t0], %[nx], 24576\n\t"
        "buffer_load_dwordx4 v[224:227], v103, %[sw], %[t0] offen offset:0 sc1\n\t"
        "buffer_load_dwordx4 v[228:231], v103, %[sw], %[t0] offen offset:1024 sc1\n\t"
        "buffer_load_dwordx4 v[232:235], v103, %[sw], %[t0] offen offset:2048 sc1\n\t"
        "buffer_load_dwordx4 v[236:239], v103, %[sw], %[t0] offen offset:3072 sc1\n\t"
        "s_add_u32 %[t0], %[nx], 28672\n\t"
        "buffer_load_dwordx4 v[240:243], v103, %[sw], %[t0] offen offset:0 sc1\n\t"
        "buffer_load_dwordx4 v[244:247], v103, %[sw], %[t0] offen offset:1024 sc1\n\t"
        "buffer_load_dwordx4 v[248:251], v103, %[sw], %[t0] offen offset:2048 sc1\n\t"
        "buffer_load_dwordx4 v[252:255], v103, %[sw], %[t0] offen offset:3072 sc1\n\t"
        "buffer_load_dword v26, v104, %[sw], 0 offen sc1\n\t"
        "LP_tile%=:\n\t"
        "s_and_b32 %[t0], %[tl], 1\n\t"
        "s_lshl_b32 %[t0], %[t0], 16\n\t"
        "s_lshr_b32 %[t1], %[tl], 1\n\t"
        "s_lshl_b32 %[t1], %[t1], 6\n\t"
        "s_add_u32 %[t0], %[t0], %[t1]\n\t"
        "buffer_load_dword v24, v105, %[sw], %[t0] offen sc1\n\t"
        "s_lshl_b32 %[t0], %[tl], 6\n\t"
        "buffer_load_dword v25, v106, %[sw], %[t0] offen sc1\n\t"
        "buffer_load_dword v26, v104, %[sw], 0 offen sc1\n\t"
        "s_add_u32 %[t1], %[tl], 4\n\t"
        "s_lshl_b32 %[nx], %[t1], 17\n\t"
        "s_lshr_b32 %[t1], %[t1], 1\n\t"
        "s_cmp_gt_u32 %[pg], %[t1]\n\t"
        "s_cselect_b32 %[nx], %[nx], 0xf0000000\n\t"
        "s_waitcnt vmcnt(35)\n\t"
        "v_mfma_f32_16x16x32_bf16 v[8:11], a[0:3], v[128:131], 0\n\t"
        "v_mfma_f32_16x16x32_bf16 v[12:15], a[4:7], v[128:131], 0\n\t"
        "s_add_u32 %[t0], %[nx], 0\n\t"
        "buffer_load_dwordx4 v[128:131], v103, %[sw], %[t0] offen offset:0 sc1\n\t"
        "s_waitcnt vmcnt(35)\n\t"
        "v_mfma_f32_16x16x32_bf16 v[8:11], a[8:11], v[132:135], v[8:11]\n\t"
        "v_mfma_f32_16x16x32_bf16 v[12:15], a[12:15], v[132:135], v[12:15]\n\t"
        "buffer_load_dwordx4 v[132:135], v103, %[sw], %[t0] offen offset:1024 sc1\n\t"
        "s_waitcnt vmcnt(35)\n\t"
        "v_mfma_f32_16x16x32_bf16 v[8:11], a[16:19], v[136:139], v[8:11]\n\t"
        "v_mfma_f32_16x16x32_bf16 v[12:15], a[20:23], v[136:139], v[12:15]\n\t"
        "buffer_load_dwordx4 v[136:139], v103, %[sw], %[t0] offen offset:2048 sc1\n\t"
        "s_waitcnt vmcnt(35)\n\t"
        "v_mfma_f32_16x16x32_bf16 v[8:11], a[24:27], v[140:143], v[8:11]\n\t"
        "v_mfma_f32_16x16x32_bf16 v[12:15], a[28:31], v[140:143], v[12:15]\n\t"
        "buffer_load_dwordx4 v[140:143], v103, %[sw], %[t0] offen offset:3072 sc1\n\t"
        "s_waitcnt vmcnt(35)\n\t"
        "v_mfma_f32_16x16x32_bf16 v[8:11], a[32:35], v[144:147], v[8:11]\n\t"
        "v_mfma_f32_16x16x32_bf16 v[12:15], a[36:39], v[144:147], v[12:15]\n\t"
        "s_add_u32 %[t0], %[nx], 4096\n\t"
        "buffer_load_dwordx4 v[144:147], v103, %[sw], %[t0] offen offset:0 sc1\n\t"
        "s_waitcnt vmcnt(35)\n\t"
        "v_mfma_f32_16x16x32_bf16 v[8:11], a[40:43], v[148:151], v[8:11]\n\t"
        "v_mfma_f32_16x16x32_bf16 v[12:15], a[44:47], v[148:151], v[12:15]\n\t"
        "buffer_load_dwordx4 v[148:151], v103, %[sw], %[t0] offen offset:1024 sc1\n\t"
        "s_waitcnt vmcnt(35)\n\t"
        "v_mfma_f32_16x16x32_bf16 v[8:11], a[48:51], v[152:155], v[8:11]\n\t"
        "v_mfma_f32_16x16x32_bf16 v[12:15], a[52:55], v[152:155], v[12:15]\n\t"
        "buffer_load_dwordx4 v[152:155], v103, %[sw], %[t0] offen offset:2048 sc1\n\t"
        "s_waitcnt vmcnt(35)\n\t"
        "v_mfma_f32_16x16x32_bf16 v[8:11], a[56:59], v[156:159], v[8:11]\n\t"
        "v_mfma_f32_16x16x32_bf16 v[12:15], a[60:63], v[156:159], v[12:15]\n\t"
        "buffer_load_dwordx4 v[156:159], v103, %[sw], %[t0] offen offset:3072 sc1\n\t"
        "s_waitcnt vmcnt(35)\n\t"
        "v_mfma_f32_16x16x32_bf16 v[8:11], a[64:67], v[160:163], v[8:11]\n\t"
        "v_mfma_f32_16x16x32_bf16 v[12:15], a[68:71], v[160:163], v[12:15]\n\t"
        "s_add_u32 %[t0], %[nx], 8192\n\t"
        "buffer_load_dwordx4 v[160:163], v103, %[sw], %[t0] offen offset:0 sc1\n\t"
        "s_waitcnt vmcnt(35)\n\t"
        "v_mfma_f32_16x16x32_bf16 v[8:11], a[72:75], v[164:167], v[8:11]\n\t"
        "v_mfma_f32_16x16x32_bf16 v[12:15], a[76:79], v[164:167], v[12:15]\n\t"
        "buffer_load_dwordx4 v[164:167], v103, %[sw], %[t0] offen offset:1024 sc1\n\t"
        "s_waitcnt vmcnt(35)\n\t"
        "v_mfma_f32_16x16x32_bf16 v[8:11], a[80:83], v[168:171], v[8:11]\n\t"
        "v_mfma_f32_16x16x32_bf16 v[12:15], a[84:87], v[168:171], v[12:15]\n\t"
        "buffer_load_dwordx4 v[168:171], v103, %[sw], %[t0] offen offset:2048 sc1\n\t"
        "s_waitcnt vmcnt(35)\n\t"
        "v_mfma_f32_16x16x32_bf16 v[8:11], a[88:91], v[172:175], v[8:11]\n\t"
        "v_mfma_f32_16x16x32_bf16 v[12:15], a[92:95], v[172:175], v[12:15]\n\t"
        "buffer_load_dwordx4 v[172:175], v103, %[sw], %[t0] offen offset:3072 sc1\n\t"
        "s_waitcnt vmcnt(35)\n\t"
        "v_mfma_f32_16x16x32_bf16 v[8:11], a[96:99], v[176:179], v[8:11]\n\t"
        "v_mfma_f32_16x16x32_bf16 v[12:15], a[100:103], v[176:179], v[12:15]\n\t"
        "s_add_u32 %[t0], %[nx], 12288\n\t"
        "buffer_load_dwordx4 v[176:179], v103, %[sw], %[t0] offen offset:0 sc1\n\t"
        "s_waitcnt vmcnt(35)\n\t"
        "v_mfma_f32_16x16x32_bf16 v[8:11], a[104:107], v[180:183], v[8:11]\n\t"
        "v_mfma_f32_16x16x32_bf16 v[12:15], a[108:111], v[180:183], v[12:15]\n\t"
        "buffer_load_dwordx4 v[180:183], v103, %[sw], %[t0] offen offset:1024 sc1\n\t"
        "s_waitcnt vmcnt(35)\n\t"
        "v_mfma_f32_16x16x32_bf16 v[8:11], a[112:115], v[184:187], v[8:11]\n\t"
        "v_mfma_f32_16x16x32_bf16 v[12:15], a[116:119], v[184:187], v[12:15]\n\t"
        "buffer_load_dwordx4 v[184:187], v103, %[sw], %[t0] offen offset:2048 sc1\n\t"
        "s_waitcnt vmcnt(35)\n\t"
        "v_mfma_f32_16x16x32_bf16 v[8:11], a[120:123], v[188:191], v[8:11]\n\t"
        "v_mfma_f32_16x16x32_bf16 v[12:15], a[124:127], v[188:191], v[12:15]\n\t"
        "buffer_load_dwordx4 v[188:191], v103, %[sw], %[t0] offen offset:3072 sc1\n\t"
        "s_waitcnt vmcnt(35)\n\t"
        "v_mfma_f32_16x16x32_bf16 v[8:11], a[128:131], v[192:195], v[8:11]\n\t"
        "v_mfma_f32_16x16x32_bf16 v[12:15], a[132:135], v[192:195], v[12:15]\n\t"
        "s_add_u32 %[t0], %[nx], 16384\n\t"
        "buffer_load_dwordx4 v[192:195], v103, %[sw], %[t0] offen offset:0 sc1\n\t"
        "s_waitcnt vmcnt(35)\n\t"
        "v_mfma_f32_16x16x32_bf16 v[8:11], a[136:139], v[196:199], v[8:11]\n\t"
        "v_mfma_f32_16x16x32_bf16 v[12:15], a[140:143], v[196:199], v[12:15]\n\t"
        "buffer_load_dwordx4 v[196:199], v103, %[sw], %[t0] offen offset:1024 sc1\n\t"
        "s_waitcnt vmcnt(35)\n\t"
        "v_mfma_f32_16x16x32_bf16 v[8:11], a[144:147], v[200:203], v[8:11]\n\t"
        "v_mfma_f32_16x16x32_bf16 v[12:15], a[148:151], v[200:203], v[12:15]\n\t"
        "buffer_load_dwordx4 v[200:203], v103, %[sw], %[t0] offen offset:2048 sc1\n\t"
        "s_waitcnt vmcnt(35)\n\t"
        "v_mfma_f32_16x16x32_bf16 v[8:11], a[152:155], v[204:207], v[8:11]\n\t"
        "v_mfma_f32_16x16x32_bf16 v[12:15], a[156:159], v[204:207], v[12:15]\n\t"
        "buffer_load_dwordx4 v[204:207], v103, %[sw], %[t0] offen offset:3072 sc1\n\t"
        "s_waitcnt vmcnt(35)\n\t"
        "v_mfma_f32_16x16x32_bf16 v[8:11], a[160:163], v[208:211], v[8:11]\n\t"
        "v_mfma_f32_16x16x32_bf16 v[12:15], a[164:167], v[208:211], v[12:15]\n\t"
        "s_add_u32 %[t0], %[nx], 20480\n\t"
        "buffer_load_dwordx4 v[208:211], v103, %[sw], %[t0] offen offset:0 sc1\n\t"
        "s_waitcnt vmcnt(35)\n\t"
        "v_mfma_f32_16x16x32_bf16 v[8:11], a[168:171], v[212:215], v[8:11]\n\t"
        "v_mfma_f32_16x16x32_bf16 v[12:15], a[172:175], v[212:215], v[12:15]\n\t"
        "buffer_load_dwordx4 v[212:215], v103, %[sw], %[t0] offen offset:1024 sc1\n\t"
        "s_waitcnt vmcnt(35)\n\t"
        "v_mfma_f32_16x16x32_bf16 v[8:11], a[176:179], v[216:219], v[8:11]\n\t"
        "v_mfma_f32_16x16x32_bf16 v[12:15], a[180:183], v[216:219], v[12:15]\n\t"
        "buffer_load_dwordx4 v[216:219], v103, %[sw], %[t0] offen offset:2048 sc1\n\t"
        "s_waitcnt vmcnt(35)\n\t"
        "v_mfma_f32_16x16x32_bf16 v[8:11], a[184:187], v[220:223], v[8:11]\n\t"
        "v_mfma_f32_16x16x32_bf16 v[12:15], a[188:191], v[220:223], v[12:15]\n\t"
        "buffer_load_dwordx4 v[220:223], v103, %[sw], %[t0] offen offset:3072 sc1\n\t"
        "s_waitcnt vmcnt(35)\n\t"
        "v_mfma_f32_16x16x32_bf16 v[8:11], a[192:195], v[224:227], v[8:11]\n\t"
        "v_mfma_f32_16x16x32_bf16 v[12:15], a[196:199], v[224:227], v[12:15]\n\t"
        "s_add_u32 %[t0], %[nx], 24576\n\t"
        "buffer_load_dwordx4 v[224:227], v103, %[sw], %[t0] offen offset:0 sc1\n\t"
        "s_waitcnt vmcnt(35)\n\t"
        "v_mfma_f32_16x16x32_bf16 v[8:11], a[200:203], v[228:231], v[8:11]\n\t"
        "v_mfma_f32_16x16x32_bf16 v[12:15], a[204:207], v[228:231], v[12:15]\n\t"
        "buffer_load_dwordx4 v[228:231], v103, %[sw], %[t0] offen offset:1024 sc1\n\t"
        "s_waitcnt vmcnt(35)\n\t"
        "v_mfma_f32_16x16x32_bf16 v[8:11], a[208:211], v[232:235], v[8:11]\n\t"
        "v_mfma_f32_16x16x32_bf16 v[12:15], a[212:215], v[232:235], v[12:15]\n\t"
        "buffer_load_dwordx4 v[232:235], v103, %[sw], %[t0] offen offset:2048 sc1\n\t"
        "s_waitcnt vmcnt(35)\n\t"
        "v_mfma_f32_16x16x32_bf16 v[8:11], a[216:219], v[236:239], v[8:11]\n\t"
        "v_mfma_f32_16x16x32_bf16 v[12:15], a[220:223], v[236:239], v[12:15]\n\t"
        "buffer_load_dwordx4 v[236:239], v103, %[sw], %[t0] offen offset:3072 sc1\n\t"
        "s_waitcnt vmcnt(35)\n\t"
        "v_mfma_f32_16x16x32_bf16 v[8:11], a[224:227], v[240:243], v[8:11]\n\t"
        "v_mfma_f32_16x16x32_bf16 v[12:15], a[228:231], v[240:243], v[12:15]\n\t"
        "s_add_u32 %[t0], %[nx], 28672\n\t"
        "buffer_load_dwordx4 v[240:243], v103, %[sw], %[t0] offen offset:0 sc1\n\t"
        "s_waitcnt vmcnt(35)\n\t"
        "v_mfma_f32_16x16x32_bf16 v[8:11], a[232:235], v[244:247], v[8:11]\n\t"
        "v_mfma_f32_16x16x32_bf16 v[12:15], a[236:239], v[244:247], v[12:15]\n\t"
        "buffer_load_dwordx4 v[244:247], v103, %[sw], %[t0] offen offset:1024 sc1\n\t"
        "s_waitcnt vmcnt(35)\n\t"
        "v_mfma_f32_16x16x32_bf16 v[8:11], a[240:243], v[248:251], v[8:11]\n\t"
        "v_mfma_f32_16x16x32_bf16 v[12:15], a[244:247], v[248:251], v[12:15]\n\t"
        "buffer_load_dwordx4 v[248:251], v103, %[sw], %[t0] offen offset:2048 sc1\n\t"
        "s_waitcnt vmcnt(35)\n\t"
        "v_mfma_f32_16x16x32_bf16 v[8:11], a[248:251], v[252:255], v[8:11]\n\t"
        "v_mfma_f32_16x16x32_bf16 v[12:15], a[252:255], v[252:255], v[12:15]\n\t"
        "buffer_load_dwordx4 v[252:255], v103, %[sw], %[t0] offen offset:3072 sc1\n\t"
        "s_waitcnt vmcnt(32)\n\t"
        "v_min_u32_dpp v26, v26, v26 quad_perm:[1,0,3,2] row_mask:0xf bank_mask:0xf\n\t"
        "v_sub_f32_e32 v28, 1.0, v25\n\t"
        "v_lshlrev_b32_e32 v30, 16, v24\n\t"
        "v_min_u32_dpp v26, v26, v26 quad_perm:[2,3,0,1] row_mask:0xf bank_mask:0xf\n\t"
        "v_and_b32_e32 v31, 0xffff0000, v24\n\t"
        "v_cvt_pk_bf16_f32 v28, 0, v28\n\t"
        "ds_write_b128 v108, v[8:11] offset:0\n\t"
        "ds_write_b128 v108, v[12:15] offset:64\n\t"
        "v_min_u32_dpp v26, v26, v26 row_ror:4 row_mask:0xf bank_mask:0xf\n\t"
        "v_mul_f32_e32 v30, v25, v30\n\t"
        "v_cvt_pk_bf16_f32 v30, 0, v30\n\t"
        "v_mul_f32_e32 v31, v25, v31\n\t"
        "v_cvt_pk_bf16_f32 v31, 0, v31\n\t"
        "v_readfirstlane_b32 %[pg], v26\n\t"
        "s_waitcnt lgkmcnt(0)\n\t"
        "s_barrier\n\t"
        "ds_read_b64 v[16:17], v109 offset:0\n\t"
        "ds_read_b64 v[18:19], v109 offset:2304\n\t"
        "ds_read_b64 v[20:21], v109 offset:4608\n\t"
        "ds_read_b64 v[22:23], v109 offset:6912\n\t"
        "v_xor_b32_e32 v108, 0x4000, v108\n\t"
        "v_xor_b32_e32 v109, 0x4000, v109\n\t"
        "s_lshl_b32 %[t0], %[tl], 15\n\t"
        "s_waitcnt lgkmcnt(0)\n\t"
        "v_pk_add_f32 v[16:17], v[16:17], v[18:19]\n\t"
        "v_pk_add_f32 v[20:21], v[20:21], v[22:23]\n\t"
        "v_pk_add_f32 v[16:17], v[16:17], v[20:21]\n\t"
        "v_cvt_pk_bf16_f32 v16, 0, v16\n\t"
        "v_cvt_pk_bf16_f32 v17, 0, v17\n\t"
        "v_mul_f32_e32 v16, v28, v16\n\t"
        "v_mul_f32_e32 v17, v28, v17\n\t"
        "v_cvt_pk_bf16_f32 v16, 0, v16\n\t"
        "v_cvt_pk_bf16_f32 v17, 0, v17\n\t"
        "v_add_f32_e32 v16, v30, v16\n\t"
        "v_add_f32_e32 v17, v31, v17\n\t"
        "v_cvt_pk_bf16_f32 v16, v16, v17\n\t"
        "buffer_store_dword v16, v107, %[su], %[t0] offen\n\t"
        "s_add_u32 %[tl], %[tl], 4\n\t"
        "s_cmp_ge_u32 %[tl], 128\n\t"
        "s_cbranch_scc1 LP_end%=\n\t"
        "s_cmp_lg_u32 %[nx], 0xf0000000\n\t"
        "s_cbranch_scc1 LP_tile%=\n\t"
        "s_lshr_b32 %[t1], %[tl], 1\n\t"
        "s_cmp_gt_u32 %[pg], %[t1]\n\t"
        "s_cbranch_scc1 LP_have%=\n\t"
        "s_branch LP_wait%=\n\t"
        "LP_end%=:\n\t"
        "s_waitcnt vmcnt(0)\n\t"
        ".Lk_end_P:\n\t"
        : [t0] "=&s"(t0), [t1] "=&s"(t1), [tl] "+s"(tl), [pg] "=&s"(pg), [nx] "=&s"(nx), [sp] "=&s"(sp), [ts] "=&s"(ts), "+{v108}"(law), "+{v109}"(lar)
        : [so] "s"(so), [sw] "s"(sw), [su] "s"(su),
          "{v100}"((w * 1024 + (l >> 2)) * 2048 + nt * 64 + (l & 3) * 16), "{v101}"(stg + l * 16), "{v102}"(stg + (4 * g + (n >> 2)) * 64 + (n & 3) * 8),
          "{v103}"(W_OX + w * 32768 + l * 16), "{v104}"(F_PROG + (w * 8 + (l & 7)) * 128), "{v105}"(W_A + nt * 131072 + (4 * w + g) * 4096 + n * 4),
          "{v106}"(W_GATE + (4 * w + g) * 4), "{v107}"((4 * w + g) * 2048 + nt * 64 + n * 4)
        : "memory", "scc", "v8", "v9", "v10", "v11", "v12", "v13", "v14", "v15", "v16", "v17", "v18", "v19", "v20", "v21", "v22", "v23", "v24", "v25", "v26", "v27", "v28", "v29", "v30", "v31", "v32", "v33", "v34", "v35", "v36", "v37", "v38", "v39", "v128", "v129", "v130", "v131", "v132", "v133", "v134", "v135", "v136", "v137", "v138", "v139", "v140", "v141", "v142", "v143", "v144", "v145", "v146", "v147", "v148", "v149", "v150", "v151", "v152", "v153", "v154", "v155", "v156", "v157", "v158", "v159", "v160", "v161", "v162", "v163", "v164", "v165", "v166", "v167", "v168", "v169", "v170", "v171", "v172", "v173", "v174", "v175", "v176", "v177", "v178", "v179", "v180", "v181", "v182", "v183", "v184", "v185", "v186", "v187", "v188", "v189", "v190", "v191", "v192", "v193", "v194", "v195", "v196", "v197", "v198", "v199", "v200", "v201", "v202", "v203", "v204", "v205", "v206", "v207", "v208", "v209", "v210", "v211", "v212", "v213", "v214", "v215", "v216", "v217", "v218", "v219", "v220", "v221", "v222", "v223", "v224", "v225", "v226", "v227", "v228", "v229", "v230", "v231", "v232", "v233", "v234", "v235", "v236", "v237", "v238", "v239", "v240", "v241", "v242", "v243", "v244", "v245", "v246", "v247", "v248", "v249", "v250", "v251", "v252", "v253", "v254", "v255", "a0", "a1", "a2", "a3", "a4", "a5", "a6", "a7", "a8", "a9", "a10", "a11", "a12", "a13", "a14", "a15", "a16", "a17", "a18", "a19", "a20", "a21", "a22", "a23", "a24", "a25", "a26", "a27", "a28", "a29", "a30", "a31", "a32", "a33", "a34", "a35", "a36", "a37", "a38", "a39", "a40", "a41", "a42", "a43", "a44", "a45", "a46", "a47", "a48", "a49", "a50", "a51", "a52", "a53", "a54", "a55", "a56", "a57", "a58", "a59", "a60", "a61", "a62", "a63", "a64", "a65", "a66", "a67", "a68", "a69", "a70", "a71", "a72", "a73", "a74", "a75", "a76", "a77", "a78", "a79", "a80", "a81", "a82", "a83", "a84", "a85", "a86", "a87", "a88", "a89", "a90", "a91", "a92", "a93", "a94", "a95", "a96", "a97", "a98", "a99", "a100", "a101", "a102", "a103", "a104", "a105", "a106", "a107", "a108", "a109", "a110", "a111", "a112", "a113", "a114", "a115", "a116", "a117", "a118", "a119", "a120", "a121", "a122", "a123", "a124", "a125", "a126", "a127", "a128", "a129", "a130", "a131", "a132", "a133", "a134", "a135", "a136", "a137", "a138", "a139", "a140", "a141", "a142", "a143", "a144", "a145", "a146", "a147", "a148", "a149", "a150", "a151", "a152", "a153", "a154", "a155", "a156", "a157", "a158", "a159", "a160", "a161", "a162", "a163", "a164", "a165", "a166", "a167", "a168", "a169", "a170", "a171", "a172", "a173", "a174", "a175", "a176", "a177", "a178", "a179", "a180", "a181", "a182", "a183", "a184", "a185", "a186", "a187", "a188", "a189", "a190", "a191", "a192", "a193", "a194", "a195", "a196", "a197", "a198", "a199", "a200", "a201", "a202", "a203", "a204", "a205", "a206", "a207", "a208", "a209", "a210", "a211", "a212", "a213", "a214", "a215", "a216", "a217", "a218", "a219", "a220", "a221", "a222", "a223", "a224", "a225", "a226", "a227", "a228", "a229", "a230", "a231", "a232", "a233", "a234", "a235", "a236", "a237", "a238", "a239", "a240", "a241", "a242", "a243", "a244", "a245", "a246", "a247", "a248", "a249", "a250", "a251", "a252", "a253", "a254", "a255");
}
__device__ __forceinline__ void bulk_block(const Params& p, rsrc_t r, unsigned char* lds, const int wv, const int j) {
    BSTAMP(0);
    proj_block(p, lds, wv, (j & 6) * 4 + (j >> 4), ((j >> 2) & 2) | (j & 1), j);
    BSTAMP(2);
    if (!conv_core(p, r, lds, wv, j)) return;
    BSTAMP(3);
    out_block(p, lds, wv, (j & 7) >> 1, (j >> 3) * 2 + (j & 1), j);
    BSTAMP(7);
}

__device__ __forceinline__ void rec_block(const Params& p, rsrc_t r, unsigned char* lds, const int wv, const int b) {
    const int xq = (b & 7) >> 1, t = (b >> 3) * 2 + (b & 1);
    const unsigned lb = __builtin_amdgcn_readfirstlane((unsigned)(size_t)lds);
    int kind, id, rg = 0, rot = 0, bq = 99;
    if (RPL == 0) { kind = t < 8 ? 0 : t < 24 ? 1 : 2; id = t < 8 ? xq * 8 + t : t < 24 ? (xq & 1) * 16 + t - 8 : xq * 8 + t - 24; rg = xq >> 1; }
    else if (RPL == 4) { kind = t < 8 ? 0 : t < 24 ? 1 : 2; id = t < 8 ? xq * 8 + t : t < 24 ? xq * 8 + ((t - 8) >> 1) : xq * 8 + t - 24; rg = t & 1; }
    else if (RPL == 1) { kind = xq == 0 ? 0 : xq < 3 ? 1 : 2; id = t; rg = xq - 1; }
    else if (RPL == 2) { const int na = xq == 2 ? 10 : 11, q = xq * 21 + t - na; kind = xq == 3 ? 2 : t < na ? 0 : 1; id = xq == 3 ? t : t < na ? xq * 11 + t : q >> 1; rg = q & 1; }
    else { const int na = xq == 2 ? 12 : 10, q = t - na; kind = xq == 3 ? 2 : t < na ? 0 : 1; rg = xq < 2 ? xq : q & 1; bq = q;
        const int c2 = q >> 1, cs = xq < 2 ? (q < 6 ? q : q < 20 ? q + 2 : q + 4) : (c2 < 2 ? 6 + c2 : c2 < 4 ? 20 + c2 : 22 + c2);
        id = xq == 3 ? t : t < na ? xq * 10 + t : cs; rot = !ROT ? 0 : xq < 2 ? (cs & 1) * 4 : ((cs & 1) * 2 + rg) * 2; }
    if (kind == 0) rec_a(p, lb, wv, id, xq);
    else if (kind == 1) rec_b(p, lb, wv, id, rg, xq, rot, bq);
    else {
        const int hi = id, j = hi - 32; (void)j;
        BSTAMP(0);
        rec_helper(p, r, lds, wv, hi);
        BSTAMP(6);
        if (XWAIT == 1) { if (!wait_word(r, F_GO + (hi >> 1) * 128, 1u, 1024u)) return; }
        gate_block(p, r, hi, wv);
        BSTAMP(2);
        if (XWAIT == 2) { if (!wait_word(r, F_GO + (hi >> 1) * 128, 1u, 1024u)) return; }
        if (!conv_ends(p, r, wv, hi, j)) return;
        BSTAMP(3);
    }
}

__global__ void __launch_bounds__(256, 1) k_main(Params p) {
    extern __shared__ __attribute__((aligned(16))) unsigned char lds[];
    rsrc_t r = __builtin_amdgcn_make_buffer_rsrc(p.ws, 0, W_TOTAL, 0x00020000);
    const int wv = __builtin_amdgcn_readfirstlane(threadIdx.x >> 6);
    int role = blockIdx.x;
    unsigned ctouch = 0u;
    if (CODE_T) {
        unsigned long long pc; int ea, eb, eg;
        asm volatile("s_getpc_b64 %0\n.Lk_begin:\n\ts_mov_b32 %1, .Lk_end_A-.Lk_begin\n\ts_mov_b32 %2, .Lk_end_B-.Lk_begin\n\ts_mov_b32 %3, .Lk_end_P-.Lk_begin" : "=s"(pc), "=s"(ea), "=s"(eb), "=s"(eg));
        const int o = ((blockIdx.x >> 3) * 64 + (threadIdx.x >> 2)) * 128;
        if ((threadIdx.x & 3) == 0 && o < max(ea, max(eb, eg)) - 4) asm volatile("global_load_dword %0, %1, off" : "+v"(ctouch) : "v"((const unsigned char*)pc + o) : "memory");
    }
    if (threadIdx.x == 0) {
        unsigned xcc; asm volatile("s_getreg_b32 %0, hwreg(HW_REG_XCC_ID)" : "=s"(xcc)); xcc &= 7u;
        unsigned* tk = (unsigned*)(p.ws + F_TKT);
        unsigned t = __hip_atomic_fetch_add(tk + xcc * 16, 1u, __ATOMIC_RELAXED, __HIP_MEMORY_SCOPE_AGENT);
        if (t >= 32u) {
            unsigned o = __hip_atomic_fetch_add(tk + 128, 1u, __ATOMIC_RELAXED, __HIP_MEMORY_SCOPE_AGENT);
            for (unsigned spin = 0; spin < (1u << 22); ++spin) { unsigned tot = 0u;
                for (int x = 0; x < 8; ++x) tot += __hip_atomic_load(tk + x * 16, __ATOMIC_RELAXED, __HIP_MEMORY_SCOPE_AGENT);
                if (tot >= 256u) break; }
            for (int x = 0; x < 8; ++x) { const unsigned cnt = __hip_atomic_load(tk + x * 16, __ATOMIC_RELAXED, __HIP_MEMORY_SCOPE_AGENT), fr = cnt < 32u ? 32u - cnt : 0u;
                if (o < fr) { xcc = (unsigned)x; t = cnt + o; break; }
                o -= fr; }
        }
        *(volatile int*)lds = (int)((xcc >> 2) * 128u + (t >> 1) * 8u + (xcc & 3u) * 2u + (t & 1u));
    }
    asm volatile("s_waitcnt vmcnt(0)" : "+v"(ctouch) :: "memory");
    __syncthreads();
    role = __builtin_amdgcn_readfirstlane(*(volatile int*)lds) & 255;
    __syncthreads();
    if (role >= 128) bulk_block(p, r, lds, wv, role - 128); else rec_block(p, r, lds, wv, role);
}

extern "C" void kernel_launch(void* const* d_in, const int* in_sizes, int n_in, void* d_out, int out_size, void* d_ws, size_t ws_size, hipStream_t stream) {
    Params p;
    p.Aq = (const bf16_t*)d_in[0]; p.Ak = (const bf16_t*)d_in[1]; p.Av = (const bf16_t*)d_in[2];
    p.shift_k = (const bf16_t*)d_in[3]; p.ssm_k = (const bf16_t*)d_in[4]; p.ssm_D = (const bf16_t*)d_in[5];
    p.mem0 = (const bf16_t*)d_in[7]; p.Wq = (const bf16_t*)d_in[8]; p.Wk = (const bf16_t*)d_in[9];
    p.Wv = (const bf16_t*)d_in[10]; p.Wo = (const bf16_t*)d_in[11]; p.gate_w = (const bf16_t*)d_in[13];
    p.out = (bf16_t*)d_out; p.ws = (unsigned char*)d_ws;
    unsigned char* ws = (unsigned char*)d_ws;
    (void)hipMemsetAsync(ws, 0, FLG_BYTES, stream);
    (void)hipFuncSetAttribute((const void*)k_main, hipFuncAttributeMaxDynamicSharedMemorySize, L_TOTAL);
    k_main<<<256, 256, L_TOTAL, stream>>>(p);
}
```

```cpp
#include <hip/hip_runtime.h>
#include <stdint.h>
#include <type_traits>
typedef unsigned short bf16_t;
using bf16x8 = __attribute__((ext_vector_type(8))) short;
using f32x4 = __attribute__((ext_vector_type(4))) float;
typedef unsigned v4u __attribute__((ext_vector_type(4)));
typedef unsigned v2u __attribute__((ext_vector_type(2)));
typedef __amdgpu_buffer_rsrc_t rsrc_t;

__device__ __forceinline__ float bf2f(bf16_t b) { return __uint_as_float(((unsigned)b) << 16); }
typedef __bf16 hbf2 __attribute__((ext_vector_type(2)));
typedef float f32x2 __attribute__((ext_vector_type(2)));
__device__ __forceinline__ unsigned pack2(float a, float b) { f32x2 v = {a, b}; return __builtin_bit_cast(unsigned, __builtin_convertvector(v, hbf2)); }
__device__ __forceinline__ float rbfh(float f) { return __uint_as_float(pack2(f, 0.f) << 16); }
__device__ __forceinline__ unsigned nosent(unsigned x) {
    typedef unsigned short us2 __attribute__((ext_vector_type(2)));
    const us2 a = __builtin_bit_cast(us2, x), c = {(unsigned short)0xFFFE, (unsigned short)0xFFFE}; return __builtin_bit_cast(unsigned, __builtin_elementwise_min(a, c)); }

constexpr int S = 2048, D = 1024, H = 32, DH = 128, M = 8, SEG = 32, NSEG = 64;

constexpr unsigned W_FLG = 0;
constexpr unsigned F_KVQ = W_FLG;
constexpr unsigned F_CONV = W_FLG + 32768;
constexpr unsigned F_ERR = W_FLG + 32768 + 256;
constexpr unsigned F_PREP = W_FLG + 32768 + 512;
constexpr unsigned F_CAL = W_FLG + 33792;
constexpr unsigned F_TKT = W_FLG + 34816;
constexpr unsigned F_PROG = W_FLG + 49152;
constexpr unsigned F_CVU = W_FLG + 53248, F_CVY = W_FLG + 57344;
constexpr unsigned F_OP = W_FLG + 65536;
constexpr unsigned F_ATT = W_FLG + 131072;
constexpr unsigned F_RDY = W_FLG + 147456;
constexpr unsigned FLG_BYTES = 180224;
constexpr unsigned HO_KVM = W_FLG + FLG_BYTES;
constexpr unsigned W_A = HO_KVM + 64 * 32 * 4096;
constexpr unsigned W_GATE = W_A + S * D * 2;
constexpr unsigned W_QX = W_GATE + 8192;
constexpr unsigned W_KX = W_QX + S * 4096 * 2;
constexpr unsigned W_VF = W_KX + S * 4096 * 2;
constexpr unsigned W_OX = W_VF + 64 * 32 * 8192;
constexpr unsigned W_PART = W_OX + S * 4096 * 2;
constexpr unsigned W_Y = W_PART;
constexpr unsigned W_DBG = W_PART + 32 * 4 * 64 * 1024 * 4;
constexpr unsigned W_U = W_DBG + 256 * 64 * 16 * 8;
constexpr unsigned W_TOTAL = W_U + S * D * 2;

__device__ __forceinline__ v4u ld16(rsrc_t r, unsigned off) { return __builtin_amdgcn_raw_buffer_load_b128(r, off, 0, 16); }
__device__ __forceinline__ v2u ld8(rsrc_t r, unsigned off) { return __builtin_amdgcn_raw_buffer_load_b64(r, off, 0, 16); }
__device__ __forceinline__ unsigned ld4(rsrc_t r, unsigned off) { return __builtin_amdgcn_raw_buffer_load_b32(r, off, 0, 16); }
__device__ __forceinline__ void st16(rsrc_t r, unsigned off, v4u v) { __builtin_amdgcn_raw_buffer_store_b128(v, r, off, 0, 16); }
__device__ __forceinline__ void st8(rsrc_t r, unsigned off, v2u v) { __builtin_amdgcn_raw_buffer_store_b64(v, r, off, 0, 16); }
__device__ __forceinline__ void st4(rsrc_t r, unsigned off, unsigned v) { __builtin_amdgcn_raw_buffer_store_b32(v, r, off, 0, 16); }
__device__ __forceinline__ bool okdw(unsigned x) { return ((x & 0xFFFFu) != 0xFFFFu) & ((x >> 16) != 0xFFFFu); }
typedef unsigned short us2_t __attribute__((ext_vector_type(2)));
__device__ __forceinline__ unsigned pkmax(unsigned a, unsigned b) { return __builtin_bit_cast(unsigned, __builtin_elementwise_max(__builtin_bit_cast(us2_t, a), __builtin_bit_cast(us2_t, b))); }
__device__ __forceinline__ unsigned pkmax4(v4u v) { return pkmax(pkmax(v.x, v.y), pkmax(v.z, v.w)); }
__device__ __forceinline__ bool ok16(v4u v) { return okdw(pkmax4(v)); }
__device__ __forceinline__ bool ok8(v2u v) { return okdw(v.x) & okdw(v.y); }
__device__ __forceinline__ bf16x8 as_frag(v4u v) { return __builtin_bit_cast(bf16x8, v); }
#define MFMA(a, b, c) __builtin_amdgcn_mfma_f32_16x16x32_bf16((a), (b), (c), 0, 0, 0)
#define D1 136
#define D2 85
#define D3 76
#define LT_A 1
#define LB0 12
#define LC0 20
#define LA0 24
#define TR_UP 4
#define TR_DN 2
#define L_DEC 3
#define WARM1 0
#define KROT 0
#define STAG 1
#define UP_SH 2
#define EPI_SLP 0
#define SITA 1
#define PAIRX 4
#define DUE_P 1
#define LATE 1
#define EARLY_FROM 56
#define SKEW 0
#define OUT_WARM 1
#define ADAPT 1
#define RPL 0
#define CV_POL 2
#define POLL_SLP 4
#define TOUCH 1
#define ROT 0
#define CODE_T 1
#define CAL_T 450u
#define GTOUCH 0
#define XTOUCH 32
#define ST_P 32
#define CV_NAP 4000u
#define XSLP 2
#define XDLY 0
#define GDLY 0
#define YDLY 0
#define PROT 3
#define XSTG 0
#define XWAIT 1
#define PEN 48
#define MEMBAR() asm volatile("" ::: "memory")
#define FT(i) do { } while (0)
#define ENDPGM() asm volatile("s_endpgm" ::: "memory")
#define DRAIN() asm volatile("s_waitcnt vmcnt(0)" ::: "memory")

__device__ __forceinline__ float xor16_max(float x) { auto r = __builtin_amdgcn_permlane16_swap(__float_as_uint(x), __float_as_uint(x), false, false); return fmaxf(__uint_as_float(r[0]), __uint_as_float(r[1])); }
__device__ __forceinline__ float xor32_max(float x) { auto r = __builtin_amdgcn_permlane32_swap(__float_as_uint(x), __float_as_uint(x), false, false); return fmaxf(__uint_as_float(r[0]), __uint_as_float(r[1])); }
__device__ __forceinline__ float xor16_add(float x) { auto r = __builtin_amdgcn_permlane16_swap(__float_as_uint(x), __float_as_uint(x), false, false); return __uint_as_float(r[0]) + __uint_as_float(r[1]); }
__device__ __forceinline__ float xor32_add(float x) { auto r = __builtin_amdgcn_permlane32_swap(__float_as_uint(x), __float_as_uint(x), false, false); return __uint_as_float(r[0]) + __uint_as_float(r[1]); }
__device__ __forceinline__ v2u tr_read(unsigned lds_addr) { v2u r; asm volatile("ds_read_b64_tr_b16 %0, %1\n\ts_waitcnt lgkmcnt(0)" : "=&v"(r) : "v"(lds_addr) : "memory"); return r; }

typedef short v4i16_t __attribute__((ext_vector_type(4)));
__device__ __forceinline__ v2u trb(unsigned lds_addr) { return __builtin_bit_cast(v2u, __builtin_amdgcn_ds_read_tr16_b64_v4i16((__attribute__((address_space(3))) v4i16_t*)(size_t)lds_addr)); }
__device__ __forceinline__ int otid_(int wv) { int t; asm volatile("v_mbcnt_lo_u32_b32 %0, -1, 0\n\tv_mbcnt_hi_u32_b32 %0, -1, %0" : "=v"(t)); return wv * 64 + t; }
#define otid() otid_(wv)
__device__ __forceinline__ bool wait_word(rsrc_t r, unsigned off, unsigned want, unsigned code) {
    for (unsigned spin = 0;; ++spin) { MEMBAR();
        const unsigned v = __builtin_amdgcn_readfirstlane(ld4(r, off));
        if (v >= want) return true;
        if (spin > (1u << 20)) { st4(r, F_ERR, code); return false; }
        __builtin_amdgcn_s_sleep(8); }
}

__device__ __forceinline__ unsigned rtnow() { return (unsigned)__builtin_amdgcn_s_memrealtime(); }
__device__ __forceinline__ void wait_until(unsigned target) { for (int i = 0; i < 4096; ++i) { if ((int)(rtnow() - target) >= 0) break; } }
__device__ __forceinline__ void wait_long(unsigned target) { for (int i = 0; i < 4096; ++i) { if ((int)(rtnow() - target) >= 0) break; __builtin_amdgcn_s_sleep(8); } }
constexpr int RED_G = 320;
constexpr int RED_T = 4 * RED_G;
constexpr int L_RED = 0;
constexpr int L_KX = 65536;
constexpr int KXP = 272;
constexpr int L_VF = L_KX + 2 * 32 * KXP;
constexpr int L_QKV = L_VF + 16384;
constexpr int L_OFF = L_QKV + 6144;
constexpr int L_STMP = L_OFF + 256;
constexpr int L_TOTAL = 163840;
constexpr unsigned U_A = 0, U_S = 32, U_B = 36, U_INIT = 113, U_C = 128, U_END = 512;
#define PLACE 1
#define CAL_N 5
#define STEP_MIN 190u

struct Params {
    const bf16_t *Aq, *Ak, *Av, *shift_k, *ssm_k, *ssm_D, *mem0, *Wq, *Wk, *Wv, *Wo, *gate_w;
    bf16_t* out; unsigned char* ws;
};
__host__ __device__ __forceinline__ unsigned hx_base(const void* ws) { const size_t a = (size_t)ws; return (unsigned)((((a + W_TOTAL + 0x7FFFFFu) >> 23) << 23) - a); }

#define STAMP(i) do { } while (0)
#define BSTAMP(i) do { } while (0)
#define EV(code) do { } while (0)
#define SPINS(i) do { } while (0)
typedef short v4s __attribute__((ext_vector_type(4)));
typedef unsigned v32u __attribute__((ext_vector_type(32)));
typedef unsigned v16u __attribute__((ext_vector_type(16)));
static_assert(L_VF - L_KX == 16384 + 1024, "fragment read offsets");
#define MFMA16(a, b, c) __builtin_amdgcn_mfma_f32_16x16x16bf16_1k(__builtin_bit_cast(v4s, (a)), __builtin_bit_cast(v4s, (b)), (c), 0, 0, 0)
__device__ __forceinline__ unsigned ror8(unsigned x) { return (unsigned)__builtin_amdgcn_update_dpp(0, (int)x, 0x128, 0xF, 0xF, false); }
__device__ __forceinline__ unsigned qx1(unsigned x) { return (unsigned)__builtin_amdgcn_update_dpp(0, (int)x, 0xB1, 0xF, 0xF, false); }
__device__ __forceinline__ unsigned wave_pkmax(unsigned d) {
    d = pkmax(d, (unsigned)__builtin_amdgcn_update_dpp((int)d, (int)d, 0xB1, 0xF, 0xF, false)); d = pkmax(d, (unsigned)__builtin_amdgcn_update_dpp((int)d, (int)d, 0x4E, 0xF, 0xF, false));
    d = pkmax(d, (unsigned)__builtin_amdgcn_update_dpp((int)d, (int)d, 0x124, 0xF, 0xF, false)); d = pkmax(d, (unsigned)__builtin_amdgcn_update_dpp((int)d, (int)d, 0x128, 0xF, 0xF, false));
    { auto q = __builtin_amdgcn_permlane16_swap(d, d, false, false); d = pkmax(q[0], q[1]); }
    { auto q = __builtin_amdgcn_permlane32_swap(d, d, false, false); d = pkmax(q[0], q[1]); }
    return d; }
__device__ __forceinline__ int wave_max(int d) {
    d = max(d, __builtin_amdgcn_update_dpp(d, d, 0xB1, 0xF, 0xF, false)); d = max(d, __builtin_amdgcn_update_dpp(d, d, 0x4E, 0xF, 0xF, false));
    d = max(d, __builtin_amdgcn_update_dpp(d, d, 0x124, 0xF, 0xF, false)); d = max(d, __builtin_amdgcn_update_dpp(d, d, 0x128, 0xF, 0xF, false));
    { auto q = __builtin_amdgcn_permlane16_swap((unsigned)d, (unsigned)d, false, false); d = max((int)q[0], (int)q[1]); }
    { auto q = __builtin_amdgcn_permlane32_swap((unsigned)d, (unsigned)d, false, false); d = max((int)q[0], (int)q[1]); }
    return d; }

__device__ __forceinline__ v4u mk_srd(const void* p, unsigned bytes) { const size_t a = (size_t)p; v4u d;
    d.x = __builtin_amdgcn_readfirstlane((unsigned)a); d.y = __builtin_amdgcn_readfirstlane((unsigned)(a >> 32) & 0xFFFFu); d.z = __builtin_amdgcn_readfirstlane(bytes); d.w = 0x00020000u; return d; }
__device__ __forceinline__ bf16x8 ldsa(unsigned addr) { return *(const __attribute__((address_space(3))) bf16x8*)(size_t)addr; }
constexpr unsigned HX_BYTES = 2097152, OOB = 0xF0000000u;
constexpr unsigned F_RDY0 = W_FLG + 34304;
constexpr unsigned F_GO = W_FLG + 36864;
constexpr int L_KV = 90112, L_MS = 139264, L_IMG = 155648, IMG_PS = 1056, IMG_MS = 2112, LB_R = 8192, LB_STG = 16384, LB_STGP = 8704;
static_assert(F_KVQ == 0 && F_ERR == 33024 && F_CONV == 32768 && F_CAL == 33792 && F_RDY0 == 34304 && F_GO == 36864 && F_PROG == 49152 && W_QX == 12771328 && W_KX == 29548544 && W_OX == 63102976 && W_VF == 46325760 && W_DBG == 113434624 && L_KV == 90112 && L_MS == 139264 && L_IMG == 155648 && IMG_PS == 1056 && IMG_MS == 2112 && LB_R == 8192 && LB_STG == 16384 && LB_STGP == 8704, "constants of the generated code");
#define REC_S_IN "{s[8:11]}"(sh), "{s[12:15]}"(sw), "{s[16:19]}"(s1), "{s[20:23]}"(s2), "{s29}"(D1), "{s30}"(D2), "{s43}"(w), "{s44}"(id), "{s49}"(rb), "{s50}"(PEN), "{s51}"(LT_A), "{s45}"(F_GO + (cp * 4 + w) * 128)
__device__ __forceinline__ void rec_a(const Params& p, unsigned lb, const int wv, const int id, const int cp) {
    const int w = wv, l = otid() & 63, n = l & 15, g = l >> 4, h = id, rb = id;
    const v4u sh = mk_srd(p.ws + hx_base(p.ws), HX_BYTES - 8192), sw = mk_srd(p.ws, W_TOTAL), s1 = mk_srd(w < 2 ? p.Wq : p.Wk, 1024 * 4096 * 2), s2 = s1;
    const int r8 = l >> 3, pc = l & 7, pp = n >> 3, bb = (n >> 2) & 1, f = (g & 1) | (pp << 1);
    const unsigned stg = lb + 131072 + w * 8192;
    const unsigned g_w = r8 * 8192 + h * 256 + (w & 1) * 128 + pc * 16;
    const unsigned wb0 = stg + r8 * 128 + (((pc >> 1) ^ (r8 >> 2)) << 5) + (pc & 1) * 16, wb1 = wb0 ^ 64u;
    const unsigned rb0 = stg + (16 * (g >> 1) + 8 * pp + 4 * (g & 1) + bb) * 128 + (f << 5) + (n & 3) * 8;
    const unsigned a_mem = cp * 32768 + (w >> 1) * 16384 + (w & 1) * 4096 + (g >> 1) * 512 + ((n & 7) >> 2) * 256 + ((g & 1) * 2 + pp) * 64 + (n & 3) * 16;
    const unsigned l_ms = lb + L_MS + l * 16;
    const unsigned l_iw = lb + L_IMG + (w >> 1) * IMG_MS + (g & 1) * IMG_PS + (64 * (w & 1) + 16 * (g >> 1) + n) * 8;
    const unsigned l_iq = lb + L_IMG + (n & 1) * IMG_PS + (8 * g + (n >> 2)) * 8;
    const unsigned a_vm = (16 + (h >> 2)) * 16384 + (h & 3) * 2048 + w * 512 + n * 32 + (g & 1) * 16;
    const unsigned a_o = n < 8 ? (8 + (n >> 2) * 4 + (h >> 3)) * 16384 + (h & 7) * 1024 + w * 256 + g * 64 + (n & 3) * 16 : OOB;
    const unsigned a_ox = (((w & 1) * 32 + h) * 4 + w) * 1024 + l * 16;
    asm volatile(
        "s_lshl_b32 s34, s43, 2\n\t"
        "s_add_u32 s34, s34, 0\n\t"
        "s_and_b32 s34, s34, 15\n\t"
        "s_lshl_b32 s34, s34, 19\n\t"
        "buffer_load_dwordx4 v[224:227], v96, s[16:19], s34 offen nt\n\t"
        "s_add_u32 s34, s34, 0x10000\n\t"
        "buffer_load_dwordx4 v[228:231], v96, s[16:19], s34 offen nt\n\t"
        "s_add_u32 s34, s34, 0x10000\n\t"
        "buffer_load_dwordx4 v[232:235], v96, s[16:19], s34 offen nt\n\t"
        "s_add_u32 s34, s34, 0x10000\n\t"
        "buffer_load_dwordx4 v[236:239], v96, s[16:19], s34 offen nt\n\t"
        "s_add_u32 s34, s34, 0x10000\n\t"
        "buffer_load_dwordx4 v[240:243], v96, s[16:19], s34 offen nt\n\t"
        "s_add_u32 s34, s34, 0x10000\n\t"
        "buffer_load_dwordx4 v[244:247], v96, s[16:19], s34 offen nt\n\t"
        "s_add_u32 s34, s34, 0x10000\n\t"
        "buffer_load_dwordx4 v[248:251], v96, s[16:19], s34 offen nt\n\t"
        "s_add_u32 s34, s34, 0x10000\n\t"
        "buffer_load_dwordx4 v[252:255], v96, s[16:19], s34 offen nt\n\t"
        "s_lshl_b32 s34, s43, 2\n\t"
        "s_add_u32 s34, s34, 1\n\t"
        "s_and_b32 s34, s34, 15\n\t"
        "s_lshl_b32 s34, s34, 19\n\t"
        "buffer_load_dwordx4 v[0:3], v96, s[16:19], s34 offen nt\n\t"
        "s_add_u32 s34, s34, 0x10000\n\t"
        "buffer_load_dwordx4 v[4:7], v96, s[16:19], s34 offen nt\n\t"
        "s_add_u32 s34, s34, 0x10000\n\t"
        "buffer_load_dwordx4 v[8:11], v96, s[16:19], s34 offen nt\n\t"
        "s_add_u32 s34, s34, 0x10000\n\t"
        "buffer_load_dwordx4 v[12:15], v96, s[16:19], s34 offen nt\n\t"
        "s_add_u32 s34, s34, 0x10000\n\t"
        "buffer_load_dwordx4 v[16:19], v96, s[16:19], s34 offen nt\n\t"
        "s_add_u32 s34, s34, 0x10000\n\t"
        "buffer_load_dwordx4 v[20:23], v96, s[16:19], s34 offen nt\n\t"
        "s_add_u32 s34, s34, 0x10000\n\t"
        "buffer_load_dwordx4 v[24:27], v96, s[16:19], s34 offen nt\n\t"
        "s_add_u32 s34, s34, 0x10000\n\t"
        "buffer_load_dwordx4 v[28:31], v96, s[16:19], s34 offen nt\n\t"
        "s_lshl_b32 s34, s43, 2\n\t"
        "s_add_u32 s34, s34, 2\n\t"
        "s_and_b32 s34, s34, 15\n\t"
        "s_lshl_b32 s34, s34, 19\n\t"
        "buffer_load_dwordx4 v[32:35], v96, s[16:19], s34 offen nt\n\t"
        "s_add_u32 s34, s34, 0x10000\n\t"
        "buffer_load_dwordx4 v[36:39], v96, s[16:19], s34 offen nt\n\t"
        "s_add_u32 s34, s34, 0x10000\n\t"
        "buffer_load_dwordx4 v[40:43], v96, s[16:19], s34 offen nt\n\t"
        "s_add_u32 s34, s34, 0x10000\n\t"
        "buffer_load_dwordx4 v[44:47], v96, s[16:19], s34 offen nt\n\t"
        "s_add_u32 s34, s34, 0x10000\n\t"
        "buffer_load_dwordx4 v[48:51], v96, s[16:19], s34 offen nt\n\t"
        "s_add_u32 s34, s34, 0x10000\n\t"
        "buffer_load_dwordx4 v[52:55], v96, s[16:19], s34 offen nt\n\t"
        "s_add_u32 s34, s34, 0x10000\n\t"
        "buffer_load_dwordx4 v[56:59], v96, s[16:19], s34 offen nt\n\t"
        "s_add_u32 s34, s34, 0x10000\n\t"
        "buffer_load_dwordx4 v[60:63], v96, s[16:19], s34 offen nt\n\t"
        "v_cmp_le_i32_e64 s[60:61], 0, v126\n\t"
        "v_cmp_le_i32_e64 s[62:63], 1, v126\n\t"
        "v_cmp_le_i32_e64 s[64:65], 2, v126\n\t"
        "v_cmp_le_i32_e64 s[66:67], 3, v126\n\t"
        "v_cmp_le_i32_e64 s[68:69], 16, v126\n\t"
        "v_cmp_le_i32_e64 s[70:71], 17, v126\n\t"
        "v_cmp_le_i32_e64 s[72:73], 18, v126\n\t"
        "v_cmp_le_i32_e64 s[74:75], 19, v126\n\t"
        "s_mul_i32 s35, s43, 10240\n\t"
        "v_add_u32_e32 v126, s35, v124\n\t"
        "s_waitcnt vmcnt(16)\n\t"
        "ds_write_b128 v97, v[224:227] offset:0\n\t"
        "ds_write_b128 v98, v[228:231] offset:1024\n\t"
        "ds_write_b128 v97, v[232:235] offset:2048\n\t"
        "ds_write_b128 v98, v[236:239] offset:3072\n\t"
        "ds_write_b128 v97, v[240:243] offset:4096\n\t"
        "ds_write_b128 v98, v[244:247] offset:5120\n\t"
        "ds_write_b128 v97, v[248:251] offset:6144\n\t"
        "ds_write_b128 v98, v[252:255] offset:7168\n\t"
        "ds_read_b64_tr_b16 a[0:1], v99 offset:0\n\t"
        "ds_read_b64_tr_b16 a[2:3], v99 offset:256\n\t"
        "ds_read_b64_tr_b16 a[4:5], v99 offset:4096\n\t"
        "ds_read_b64_tr_b16 a[6:7], v99 offset:4352\n\t"
        "s_waitcnt lgkmcnt(8)\n\t"
        "ds_read_b64_tr_b16 a[8:9], v100 offset:0\n\t"
        "ds_read_b64_tr_b16 a[10:11], v100 offset:256\n\t"
        "ds_read_b64_tr_b16 a[12:13], v100 offset:4096\n\t"
        "ds_read_b64_tr_b16 a[14:15], v100 offset:4352\n\t"
        "s_waitcnt lgkmcnt(8)\n\t"
        "ds_read_b64_tr_b16 a[16:17], v101 offset:0\n\t"
        "ds_read_b64_tr_b16 a[18:19], v101 offset:256\n\t"
        "ds_read_b64_tr_b16 a[20:21], v101 offset:4096\n\t"
        "ds_read_b64_tr_b16 a[22:23], v101 offset:4352\n\t"
        "s_waitcnt lgkmcnt(8)\n\t"
        "ds_read_b64_tr_b16 v[104:105], v102 offset:0\n\t"
        "ds_read_b64_tr_b16 v[106:107], v102 offset:256\n\t"
        "ds_read_b64_tr_b16 v[108:109], v102 offset:4096\n\t"
        "ds_read_b64_tr_b16 v[110:111], v102 offset:4352\n\t"
        "s_lshl_b32 s34, s43, 2\n\t"
        "s_add_u32 s34, s34, 3\n\t"
        "s_and_b32 s34, s34, 15\n\t"
        "s_lshl_b32 s34, s34, 19\n\t"
        "buffer_load_dwordx4 v[64:67], v96, s[16:19], s34 offen nt\n\t"
        "s_add_u32 s34, s34, 0x10000\n\t"
        "buffer_load_dwordx4 v[68:71], v96, s[16:19], s34 offen nt\n\t"
        "s_add_u32 s34, s34, 0x10000\n\t"
        "buffer_load_dwordx4 v[72:75], v96, s[16:19], s34 offen nt\n\t"
        "s_add_u32 s34, s34, 0x10000\n\t"
        "buffer_load_dwordx4 v[76:79], v96, s[16:19], s34 offen nt\n\t"
        "s_add_u32 s34, s34, 0x10000\n\t"
        "buffer_load_dwordx4 v[80:83], v96, s[16:19], s34 offen nt\n\t"
        "s_add_u32 s34, s34, 0x10000\n\t"
        "buffer_load_dwordx4 v[84:87], v96, s[16:19], s34 offen nt\n\t"
        "s_add_u32 s34, s34, 0x10000\n\t"
        "buffer_load_dwordx4 v[88:91], v96, s[16:19], s34 offen nt\n\t"
        "s_add_u32 s34, s34, 0x10000\n\t"
        "buffer_load_dwordx4 v[92:95], v96, s[16:19], s34 offen nt\n\t"
        "s_waitcnt lgkmcnt(0)\n\t"
        "ds_write_b128 v117, v[104:107] offset:0\n\t"
        "ds_write_b128 v117, v[108:111] offset:1024\n\t"
        "s_waitcnt vmcnt(16)\n\t"
        "ds_write_b128 v97, v[0:3] offset:0\n\t"
        "ds_write_b128 v98, v[4:7] offset:1024\n\t"
        "ds_write_b128 v97, v[8:11] offset:2048\n\t"
        "ds_write_b128 v98, v[12:15] offset:3072\n\t"
        "ds_write_b128 v97, v[16:19] offset:4096\n\t"
        "ds_write_b128 v98, v[20:23] offset:5120\n\t"
        "ds_write_b128 v97, v[24:27] offset:6144\n\t"
        "ds_write_b128 v98, v[28:31] offset:7168\n\t"
        "ds_read_b64_tr_b16 a[24:25], v99 offset:0\n\t"
        "ds_read_b64_tr_b16 a[26:27], v99 offset:256\n\t"
        "ds_read_b64_tr_b16 a[28:29], v99 offset:4096\n\t"
        "ds_read_b64_tr_b16 a[30:31], v99 offset:4352\n\t"
        "s_waitcnt lgkmcnt(8)\n\t"
        "ds_read_b64_tr_b16 a[32:33], v100 offset:0\n\t"
        "ds_read_b64_tr_b16 a[34:35], v100 offset:256\n\t"
        "ds_read_b64_tr_b16 a[36:37], v100 offset:4096\n\t"
        "ds_read_b64_tr_b16 a[38:39], v100 offset:4352\n\t"
        "s_waitcnt lgkmcnt(8)\n\t"
        "ds_read_b64_tr_b16 a[40:41], v101 offset:0\n\t"
        "ds_read_b64_tr_b16 a[42:43], v101 offset:256\n\t"
        "ds_read_b64_tr_b16 a[44:45], v101 offset:4096\n\t"
        "ds_read_b64_tr_b16 a[46:47], v101 offset:4352\n\t"
        "s_waitcnt lgkmcnt(8)\n\t"
        "ds_read_b64_tr_b16 v[104:105], v102 offset:0\n\t"
        "ds_read_b64_tr_b16 v[106:107], v102 offset:256\n\t"
        "ds_read_b64_tr_b16 v[108:109], v102 offset:4096\n\t"
        "ds_read_b64_tr_b16 v[110:111], v102 offset:4352\n\t"
        "s_lshl_b32 s34, s43, 2\n\t"
        "s_add_u32 s34, s34, 4\n\t"
        "s_and_b32 s34, s34, 15\n\t"
        "s_lshl_b32 s34, s34, 19\n\t"
        "buffer_load_dwordx4 a[96:99], v96, s[16:19], s34 offen nt\n\t"
        "s_add_u32 s34, s34, 0x10000\n\t"
        "buffer_load_dwordx4 a[100:103], v96, s[16:19], s34 offen nt\n\t"
        "s_add_u32 s34, s34, 0x10000\n\t"
        "buffer_load_dwordx4 a[104:107], v96, s[16:19], s34 offen nt\n\t"
        "s_add_u32 s34, s34, 0x10000\n\t"
        "buffer_load_dwordx4 a[108:111], v96, s[16:19], s34 offen nt\n\t"
        "s_add_u32 s34, s34, 0x10000\n\t"
        "buffer_load_dwordx4 a[112:115], v96, s[16:19], s34 offen nt\n\t"
        "s_add_u32 s34, s34, 0x10000\n\t"
        "buffer_load_dwordx4 a[116:119], v96, s[16:19], s34 offen nt\n\t"
        "s_add_u32 s34, s34, 0x10000\n\t"
        "buffer_load_dwordx4 a[120:123], v96, s[16:19], s34 offen nt\n\t"
        "s_add_u32 s34, s34, 0x10000\n\t"
        "buffer_load_dwordx4 a[124:127], v96, s[16:19], s34 offen nt\n\t"
        "s_waitcnt lgkmcnt(0)\n\t"
        "ds_write_b128 v117, v[104:107] offset:2048\n\t"
        "ds_write_b128 v117, v[108:111] offset:3072\n\t"
        "s_waitcnt vmcnt(16)\n\t"
        "ds_write_b128 v97, v[32:35] offset:0\n\t"
        "ds_write_b128 v98, v[36:39] offset:1024\n\t"
        "ds_write_b128 v97, v[40:43] offset:2048\n\t"
        "ds_write_b128 v98, v[44:47] offset:3072\n\t"
        "ds_write_b128 v97, v[48:51] offset:4096\n\t"
        "ds_write_b128 v98, v[52:55] offset:5120\n\t"
        "ds_write_b128 v97, v[56:59] offset:6144\n\t"
        "ds_write_b128 v98, v[60:63] offset:7168\n\t"
        "ds_read_b64_tr_b16 a[48:49], v99 offset:0\n\t"
        "ds_read_b64_tr_b16 a[50:51], v99 offset:256\n\t"
        "ds_read_b64_tr_b16 a[52:53], v99 offset:4096\n\t"
        "ds_read_b64_tr_b16 a[54:55], v99 offset:4352\n\t"
        "s_waitcnt lgkmcnt(8)\n\t"
        "ds_read_b64_tr_b16 a[56:57], v100 offset:0\n\t"
        "ds_read_b64_tr_b16 a[58:59], v100 offset:256\n\t"
        "ds_read_b64_tr_b16 a[60:61], v100 offset:4096\n\t"
        "ds_read_b64_tr_b16 a[62:63], v100 offset:4352\n\t"
        "s_waitcnt lgkmcnt(8)\n\t"
        "ds_read_b64_tr_b16 a[64:65], v101 offset:0\n\t"
        "ds_read_b64_tr_b16 a[66:67], v101 offset:256\n\t"
        "ds_read_b64_tr_b16 a[68:69], v101 offset:4096\n\t"
        "ds_read_b64_tr_b16 a[70:71], v101 offset:4352\n\t"
        "s_waitcnt lgkmcnt(8)\n\t"
        "ds_read_b64_tr_b16 v[104:105], v102 offset:0\n\t"
        "ds_read_b64_tr_b16 v[106:107], v102 offset:256\n\t"
        "ds_read_b64_tr_b16 v[108:109], v102 offset:4096\n\t"
        "ds_read_b64_tr_b16 v[110:111], v102 offset:4352\n\t"
        "s_lshl_b32 s34, s43, 2\n\t"
        "s_add_u32 s34, s34, 5\n\t"
        "s_and_b32 s34, s34, 15\n\t"
        "s_lshl_b32 s34, s34, 19\n\t"
        "buffer_load_dwordx4 a[128:131], v96, s[16:19], s34 offen nt\n\t"
        "s_add_u32 s34, s34, 0x10000\n\t"
        "buffer_load_dwordx4 a[132:135], v96, s[16:19], s34 offen nt\n\t"
        "s_add_u32 s34, s34, 0x10000\n\t"
        "buffer_load_dwordx4 a[136:139], v96, s[16:19], s34 offen nt\n\t"
        "s_add_u32 s34, s34, 0x10000\n\t"
        "buffer_load_dwordx4 a[140:143], v96, s[16:19], s34 offen nt\n\t"
        "s_add_u32 s34, s34, 0x10000\n\t"
        "buffer_load_dwordx4 a[144:147], v96, s[16:19], s34 offen nt\n\t"
        "s_add_u32 s34, s34, 0x10000\n\t"
        "buffer_load_dwordx4 a[148:151], v96, s[16:19], s34 offen nt\n\t"
        "s_add_u32 s34, s34, 0x10000\n\t"
        "buffer_load_dwordx4 a[152:155], v96, s[16:19], s34 offen nt\n\t"
        "s_add_u32 s34, s34, 0x10000\n\t"
        "buffer_load_dwordx4 a[156:159], v96, s[16:19], s34 offen nt\n\t"
        "s_waitcnt lgkmcnt(0)\n\t"
        "ds_write_b128 v117, v[104:107] offset:4096\n\t"
        "ds_write_b128 v117, v[108:111] offset:5120\n\t"
        "s_waitcnt vmcnt(16)\n\t"
        "ds_write_b128 v97, v[64:67] offset:0\n\t"
        "ds_write_b128 v98, v[68:71] offset:1024\n\t"
        "ds_write_b128 v97, v[72:75] offset:2048\n\t"
        "ds_write_b128 v98, v[76:79] offset:3072\n\t"
        "ds_write_b128 v97, v[80:83] offset:4096\n\t"
        "ds_write_b128 v98, v[84:87] offset:5120\n\t"
        "ds_write_b128 v97, v[88:91] offset:6144\n\t"
        "ds_write_b128 v98, v[92:95] offset:7168\n\t"
        "ds_read_b64_tr_b16 a[72:73], v99 offset:0\n\t"
        "ds_read_b64_tr_b16 a[74:75], v99 offset:256\n\t"
        "ds_read_b64_tr_b16 a[76:77], v99 offset:4096\n\t"
        "ds_read_b64_tr_b16 a[78:79], v99 offset:4352\n\t"
        "s_waitcnt lgkmcnt(8)\n\t"
        "ds_read_b64_tr_b16 a[80:81], v100 offset:0\n\t"
        "ds_read_b64_tr_b16 a[82:83], v100 offset:256\n\t"
        "ds_read_b64_tr_b16 a[84:85], v100 offset:4096\n\t"
        "ds_read_b64_tr_b16 a[86:87], v100 offset:4352\n\t"
        "s_waitcnt lgkmcnt(8)\n\t"
        "ds_read_b64_tr_b16 a[88:89], v101 offset:0\n\t"
        "ds_read_b64_tr_b16 a[90:91], v101 offset:256\n\t"
        "ds_read_b64_tr_b16 a[92:93], v101 offset:4096\n\t"
        "ds_read_b64_tr_b16 a[94:95], v101 offset:4352\n\t"
        "s_waitcnt lgkmcnt(8)\n\t"
        "ds_read_b64_tr_b16 v[104:105], v102 offset:0\n\t"
        "ds_read_b64_tr_b16 v[106:107], v102 offset:256\n\t"
        "ds_read_b64_tr_b16 v[108:109], v102 offset:4096\n\t"
        "ds_read_b64_tr_b16 v[110:111], v102 offset:4352\n\t"
        "s_lshl_b32 s34, s43, 2\n\t"
        "s_add_u32 s34, s34, 6\n\t"
        "s_and_b32 s34, s34, 15\n\t"
        "s_lshl_b32 s34, s34, 19\n\t"
        "buffer_load_dwordx4 a[160:163], v96, s[16:19], s34 offen nt\n\t"
        "s_add_u32 s34, s34, 0x10000\n\t"
        "buffer_load_dwordx4 a[164:167], v96, s[16:19], s34 offen nt\n\t"
        "s_add_u32 s34, s34, 0x10000\n\t"
        "buffer_load_dwordx4 a[168:171], v96, s[16:19], s34 offen nt\n\t"
        "s_add_u32 s34, s34, 0x10000\n\t"
        "buffer_load_dwordx4 a[172:175], v96, s[16:19], s34 offen nt\n\t"
        "s_add_u32 s34, s34, 0x10000\n\t"
        "buffer_load_dwordx4 a[176:179], v96, s[16:19], s34 offen nt\n\t"
        "s_add_u32 s34, s34, 0x10000\n\t"
        "buffer_load_dwordx4 a[180:183], v96, s[16:19], s34 offen nt\n\t"
        "s_add_u32 s34, s34, 0x10000\n\t"
        "buffer_load_dwordx4 a[184:187], v96, s[16:19], s34 offen nt\n\t"
        "s_add_u32 s34, s34, 0x10000\n\t"
        "buffer_load_dwordx4 a[188:191], v96, s[16:19], s34 offen nt\n\t"
        "s_waitcnt lgkmcnt(0)\n\t"
        "ds_write_b128 v117, v[104:107] offset:6144\n\t"
        "ds_write_b128 v117, v[108:111] offset:7168\n\t"
        "s_waitcnt vmcnt(16)\n\t"
        "ds_write_b128 v97, a[96:99] offset:0\n\t"
        "ds_write_b128 v98, a[100:103] offset:1024\n\t"
        "ds_write_b128 v97, a[104:107] offset:2048\n\t"
        "ds_write_b128 v98, a[108:111] offset:3072\n\t"
        "ds_write_b128 v97, a[112:115] offset:4096\n\t"
        "ds_write_b128 v98, a[116:119] offset:5120\n\t"
        "ds_write_b128 v97, a[120:123] offset:6144\n\t"
        "ds_write_b128 v98, a[124:127] offset:7168\n\t"
        "ds_read_b64_tr_b16 a[96:97], v99 offset:0\n\t"
        "ds_read_b64_tr_b16 a[98:99], v99 offset:256\n\t"
        "ds_read_b64_tr_b16 a[100:101], v99 offset:4096\n\t"
        "ds_read_b64_tr_b16 a[102:103], v99 offset:4352\n\t"
        "s_waitcnt lgkmcnt(8)\n\t"
        "ds_read_b64_tr_b16 a[104:105], v100 offset:0\n\t"
        "ds_read_b64_tr_b16 a[106:107], v100 offset:256\n\t"
        "ds_read_b64_tr_b16 a[108:109], v100 offset:4096\n\t"
        "ds_read_b64_tr_b16 a[110:111], v100 offset:4352\n\t"
        "s_waitcnt lgkmcnt(8)\n\t"
        "ds_read_b64_tr_b16 a[112:113], v101 offset:0\n\t"
        "ds_read_b64_tr_b16 a[114:115], v101 offset:256\n\t"
        "ds_read_b64_tr_b16 a[116:117], v101 offset:4096\n\t"
        "ds_read_b64_tr_b16 a[118:119], v101 offset:4352\n\t"
        "s_waitcnt lgkmcnt(8)\n\t"
        "ds_read_b64_tr_b16 v[104:105], v102 offset:0\n\t"
        "ds_read_b64_tr_b16 v[106:107], v102 offset:256\n\t"
        "ds_read_b64_tr_b16 v[108:109], v102 offset:4096\n\t"
        "ds_read_b64_tr_b16 v[110:111], v102 offset:4352\n\t"
        "s_lshl_b32 s34, s43, 2\n\t"
        "s_add_u32 s34, s34, 7\n\t"
        "s_and_b32 s34, s34, 15\n\t"
        "s_lshl_b32 s34, s34, 19\n\t"
        "buffer_load_dwordx4 a[192:195], v96, s[16:19], s34 offen nt\n\t"
        "s_add_u32 s34, s34, 0x10000\n\t"
        "buffer_load_dwordx4 a[196:199], v96, s[16:19], s34 offen nt\n\t"
        "s_add_u32 s34, s34, 0x10000\n\t"
        "buffer_load_dwordx4 a[200:203], v96, s[16:19], s34 offen nt\n\t"
        "s_add_u32 s34, s34, 0x10000\n\t"
        "buffer_load_dwordx4 a[204:207], v96, s[16:19], s34 offen nt\n\t"
        "s_add_u32 s34, s34, 0x10000\n\t"
        "buffer_load_dwordx4 a[208:211], v96, s[16:19], s34 offen nt\n\t"
        "s_add_u32 s34, s34, 0x10000\n\t"
        "buffer_load_dwordx4 a[212:215], v96, s[16:19], s34 offen nt\n\t"
        "s_add_u32 s34, s34, 0x10000\n\t"
        "buffer_load_dwordx4 a[216:219], v96, s[16:19], s34 offen nt\n\t"
        "s_add_u32 s34, s34, 0x10000\n\t"
        "buffer_load_dwordx4 a[220:223], v96, s[16:19], s34 offen nt\n\t"
        "s_waitcnt lgkmcnt(0)\n\t"
        "ds_write_b128 v126, v[104:107] offset:0\n\t"
        "ds_write_b128 v126, v[108:111] offset:1024\n\t"
        "s_waitcnt vmcnt(16)\n\t"
        "ds_write_b128 v97, a[128:131] offset:0\n\t"
        "ds_write_b128 v98, a[132:135] offset:1024\n\t"
        "ds_write_b128 v97, a[136:139] offset:2048\n\t"
        "ds_write_b128 v98, a[140:143] offset:3072\n\t"
        "ds_write_b128 v97, a[144:147] offset:4096\n\t"
        "ds_write_b128 v98, a[148:151] offset:5120\n\t"
        "ds_write_b128 v97, a[152:155] offset:6144\n\t"
        "ds_write_b128 v98, a[156:159] offset:7168\n\t"
        "ds_read_b64_tr_b16 a[120:121], v99 offset:0\n\t"
        "ds_read_b64_tr_b16 a[122:123], v99 offset:256\n\t"
        "ds_read_b64_tr_b16 a[124:125], v99 offset:4096\n\t"
        "ds_read_b64_tr_b16 a[126:127], v99 offset:4352\n\t"
        "s_waitcnt lgkmcnt(8)\n\t"
        "ds_read_b64_tr_b16 a[128:129], v100 offset:0\n\t"
        "ds_read_b64_tr_b16 a[130:131], v100 offset:256\n\t"
        "ds_read_b64_tr_b16 a[132:133], v100 offset:4096\n\t"
        "ds_read_b64_tr_b16 a[134:135], v100 offset:4352\n\t"
        "s_waitcnt lgkmcnt(8)\n\t"
        "ds_read_b64_tr_b16 a[136:137], v101 offset:0\n\t"
        "ds_read_b64_tr_b16 a[138:139], v101 offset:256\n\t"
        "ds_read_b64_tr_b16 a[140:141], v101 offset:4096\n\t"
        "ds_read_b64_tr_b16 a[142:143], v101 offset:4352\n\t"
        "s_waitcnt lgkmcnt(8)\n\t"
        "ds_read_b64_tr_b16 v[104:105], v102 offset:0\n\t"
        "ds_read_b64_tr_b16 v[106:107], v102 offset:256\n\t"
        "ds_read_b64_tr_b16 v[108:109], v102 offset:4096\n\t"
        "ds_read_b64_tr_b16 v[110:111], v102 offset:4352\n\t"
        "s_lshl_b32 s34, s43, 2\n\t"
        "s_add_u32 s34, s34, 8\n\t"
        "s_and_b32 s34, s34, 15\n\t"
        "s_lshl_b32 s34, s34, 19\n\t"
        "buffer_load_dwordx4 a[224:227], v96, s[16:19], s34 offen nt\n\t"
        "s_add_u32 s34, s34, 0x10000\n\t"
        "buffer_load_dwordx4 a[228:231], v96, s[16:19], s34 offen nt\n\t"
        "s_add_u32 s34, s34, 0x10000\n\t"
        "buffer_load_dwordx4 a[232:235], v96, s[16:19], s34 offen nt\n\t"
        "s_add_u32 s34, s34, 0x10000\n\t"
        "buffer_load_dwordx4 a[236:239], v96, s[16:19], s34 offen nt\n\t"
        "s_add_u32 s34, s34, 0x10000\n\t"
        "buffer_load_dwordx4 a[240:243], v96, s[16:19], s34 offen nt\n\t"
        "s_add_u32 s34, s34, 0x10000\n\t"
        "buffer_load_dwordx4 a[244:247], v96, s[16:19], s34 offen nt\n\t"
        "s_add_u32 s34, s34, 0x10000\n\t"
        "buffer_load_dwordx4 a[248:251], v96, s[16:19], s34 offen nt\n\t"
        "s_add_u32 s34, s34, 0x10000\n\t"
        "buffer_load_dwordx4 a[252:255], v96, s[16:19], s34 offen nt\n\t"
        "s_waitcnt lgkmcnt(0)\n\t"
        "ds_write_b128 v126, v[104:107] offset:2048\n\t"
        "ds_write_b128 v126, v[108:111] offset:3072\n\t"
        "s_waitcnt vmcnt(16)\n\t"
        "ds_write_b128 v97, a[160:163] offset:0\n\t"
        "ds_write_b128 v98, a[164:167] offset:1024\n\t"
        "ds_write_b128 v97, a[168:171] offset:2048\n\t"
        "ds_write_b128 v98, a[172:175] offset:3072\n\t"
        "ds_write_b128 v97, a[176:179] offset:4096\n\t"
        "ds_write_b128 v98, a[180:183] offset:5120\n\t"
        "ds_write_b128 v97, a[184:187] offset:6144\n\t"
        "ds_write_b128 v98, a[188:191] offset:7168\n\t"
        "ds_read_b64_tr_b16 a[144:145], v99 offset:0\n\t"
        "ds_read_b64_tr_b16 a[146:147], v99 offset:256\n\t"
        "ds_read_b64_tr_b16 a[148:149], v99 offset:4096\n\t"
        "ds_read_b64_tr_b16 a[150:151], v99 offset:4352\n\t"
        "s_waitcnt lgkmcnt(8)\n\t"
        "ds_read_b64_tr_b16 a[152:153], v100 offset:0\n\t"
        "ds_read_b64_tr_b16 a[154:155], v100 offset:256\n\t"
        "ds_read_b64_tr_b16 a[156:157], v100 offset:4096\n\t"
        "ds_read_b64_tr_b16 a[158:159], v100 offset:4352\n\t"
        "s_waitcnt lgkmcnt(8)\n\t"
        "ds_read_b64_tr_b16 a[160:161], v101 offset:0\n\t"
        "ds_read_b64_tr_b16 a[162:163], v101 offset:256\n\t"
        "ds_read_b64_tr_b16 a[164:165], v101 offset:4096\n\t"
        "ds_read_b64_tr_b16 a[166:167], v101 offset:4352\n\t"
        "s_waitcnt lgkmcnt(8)\n\t"
        "ds_read_b64_tr_b16 v[104:105], v102 offset:0\n\t"
        "ds_read_b64_tr_b16 v[106:107], v102 offset:256\n\t"
        "ds_read_b64_tr_b16 v[108:109], v102 offset:4096\n\t"
        "ds_read_b64_tr_b16 v[110:111], v102 offset:4352\n\t"
        "s_lshl_b32 s34, s43, 2\n\t"
        "s_add_u32 s34, s34, 9\n\t"
        "s_and_b32 s34, s34, 15\n\t"
        "s_lshl_b32 s34, s34, 19\n\t"
        "buffer_load_dwordx4 v[128:131], v96, s[16:19], s34 offen nt\n\t"
        "s_add_u32 s34, s34, 0x10000\n\t"
        "buffer_load_dwordx4 v[132:135], v96, s[16:19], s34 offen nt\n\t"
        "s_add_u32 s34, s34, 0x10000\n\t"
        "buffer_load_dwordx4 v[136:139], v96, s[16:19], s34 offen nt\n\t"
        "s_add_u32 s34, s34, 0x10000\n\t"
        "buffer_load_dwordx4 v[140:143], v96, s[16:19], s34 offen nt\n\t"
        "s_add_u32 s34, s34, 0x10000\n\t"
        "buffer_load_dwordx4 v[144:147], v96, s[16:19], s34 offen nt\n\t"
        "s_add_u32 s34, s34, 0x10000\n\t"
        "buffer_load_dwordx4 v[148:151], v96, s[16:19], s34 offen nt\n\t"
        "s_add_u32 s34, s34, 0x10000\n\t"
        "buffer_load_dwordx4 v[152:155], v96, s[16:19], s34 offen nt\n\t"
        "s_add_u32 s34, s34, 0x10000\n\t"
        "buffer_load_dwordx4 v[156:159], v96, s[16:19], s34 offen nt\n\t"
        "s_waitcnt lgkmcnt(0)\n\t"
        "ds_write_b128 v126, v[104:107] offset:4096\n\t"
        "ds_write_b128 v126, v[108:111] offset:5120\n\t"
        "s_waitcnt vmcnt(16)\n\t"
        "ds_write_b128 v97, a[192:195] offset:0\n\t"
        "ds_write_b128 v98, a[196:199] offset:1024\n\t"
        "ds_write_b128 v97, a[200:203] offset:2048\n\t"
        "ds_write_b128 v98, a[204:207] offset:3072\n\t"
        "ds_write_b128 v97, a[208:211] offset:4096\n\t"
        "ds_write_b128 v98, a[212:215] offset:5120\n\t"
        "ds_write_b128 v97, a[216:219] offset:6144\n\t"
        "ds_write_b128 v98, a[220:223] offset:7168\n\t"
        "ds_read_b64_tr_b16 a[168:169], v99 offset:0\n\t"
        "ds_read_b64_tr_b16 a[170:171], v99 offset:256\n\t"
        "ds_read_b64_tr_b16 a[172:173], v99 offset:4096\n\t"
        "ds_read_b64_tr_b16 a[174:175], v99 offset:4352\n\t"
        "s_waitcnt lgkmcnt(8)\n\t"
        "ds_read_b64_tr_b16 a[176:177], v100 offset:0\n\t"
        "ds_read_b64_tr_b16 a[178:179], v100 offset:256\n\t"
        "ds_read_b64_tr_b16 a[180:181], v100 offset:4096\n\t"
        "ds_read_b64_tr_b16 a[182:183], v100 offset:4352\n\t"
        "s_waitcnt lgkmcnt(8)\n\t"
        "ds_read_b64_tr_b16 a[184:185], v101 offset:0\n\t"
        "ds_read_b64_tr_b16 a[186:187], v101 offset:256\n\t"
        "ds_read_b64_tr_b16 a[188:189], v101 offset:4096\n\t"
        "ds_read_b64_tr_b16 a[190:191], v101 offset:4352\n\t"
        "s_waitcnt lgkmcnt(8)\n\t"
        "ds_read_b64_tr_b16 v[104:105], v102 offset:0\n\t"
        "ds_read_b64_tr_b16 v[106:107], v102 offset:256\n\t"
        "ds_read_b64_tr_b16 v[108:109], v102 offset:4096\n\t"
        "ds_read_b64_tr_b16 v[110:111], v102 offset:4352\n\t"
        "s_lshl_b32 s34, s43, 2\n\t"
        "s_add_u32 s34, s34, 10\n\t"
        "s_and_b32 s34, s34, 15\n\t"
        "s_lshl_b32 s34, s34, 19\n\t"
        "buffer_load_dwordx4 v[160:163], v96, s[16:19], s34 offen nt\n\t"
        "s_add_u32 s34, s34, 0x10000\n\t"
        "buffer_load_dwordx4 v[164:167], v96, s[16:19], s34 offen nt\n\t"
        "s_add_u32 s34, s34, 0x10000\n\t"
        "buffer_load_dwordx4 v[168:171], v96, s[16:19], s34 offen nt\n\t"
        "s_add_u32 s34, s34, 0x10000\n\t"
        "buffer_load_dwordx4 v[172:175], v96, s[16:19], s34 offen nt\n\t"
        "s_add_u32 s34, s34, 0x10000\n\t"
        "buffer_load_dwordx4 v[176:179], v96, s[16:19], s34 offen nt\n\t"
        "s_add_u32 s34, s34, 0x10000\n\t"
        "buffer_load_dwordx4 v[180:183], v96, s[16:19], s34 offen nt\n\t"
        "s_add_u32 s34, s34, 0x10000\n\t"
        "buffer_load_dwordx4 v[184:187], v96, s[16:19], s34 offen nt\n\t"
        "s_add_u32 s34, s34, 0x10000\n\t"
        "buffer_load_dwordx4 v[188:191], v96, s[16:19], s34 offen nt\n\t"
        "s_waitcnt lgkmcnt(0)\n\t"
        "ds_write_b128 v126, v[104:107] offset:6144\n\t"
        "ds_write_b128 v126, v[108:111] offset:7168\n\t"
        "s_waitcnt vmcnt(16)\n\t"
        "ds_write_b128 v97, a[224:227] offset:0\n\t"
        "ds_write_b128 v98, a[228:231] offset:1024\n\t"
        "ds_write_b128 v97, a[232:235] offset:2048\n\t"
        "ds_write_b128 v98, a[236:239] offset:3072\n\t"
        "ds_write_b128 v97, a[240:243] offset:4096\n\t"
        "ds_write_b128 v98, a[244:247] offset:5120\n\t"
        "ds_write_b128 v97, a[248:251] offset:6144\n\t"
        "ds_write_b128 v98, a[252:255] offset:7168\n\t"
        "ds_read_b64_tr_b16 a[192:193], v99 offset:0\n\t"
        "ds_read_b64_tr_b16 a[194:195], v99 offset:256\n\t"
        "ds_read_b64_tr_b16 a[196:197], v99 offset:4096\n\t"
        "ds_read_b64_tr_b16 a[198:199], v99 offset:4352\n\t"
        "s_waitcnt lgkmcnt(8)\n\t"
        "ds_read_b64_tr_b16 a[200:201], v100 offset:0\n\t"
        "ds_read_b64_tr_b16 a[202:203], v100 offset:256\n\t"
        "ds_read_b64_tr_b16 a[204:205], v100 offset:4096\n\t"
        "ds_read_b64_tr_b16 a[206:207], v100 offset:4352\n\t"
        "s_waitcnt lgkmcnt(8)\n\t"
        "ds_read_b64_tr_b16 a[208:209], v101 offset:0\n\t"
        "ds_read_b64_tr_b16 a[210:211], v101 offset:256\n\t"
        "ds_read_b64_tr_b16 a[212:213], v101 offset:4096\n\t"
        "ds_read_b64_tr_b16 a[214:215], v101 offset:4352\n\t"
        "s_waitcnt lgkmcnt(8)\n\t"
        "ds_read_b64_tr_b16 v[104:105], v102 offset:0\n\t"
        "ds_read_b64_tr_b16 v[106:107], v102 offset:256\n\t"
        "ds_read_b64_tr_b16 v[108:109], v102 offset:4096\n\t"
        "ds_read_b64_tr_b16 v[110:111], v102 offset:4352\n\t"
        "s_lshl_b32 s34, s43, 2\n\t"
        "s_add_u32 s34, s34, 11\n\t"
        "s_and_b32 s34, s34, 15\n\t"
        "s_lshl_b32 s34, s34, 19\n\t"
        "buffer_load_dwordx4 v[192:195], v96, s[16:19], s34 offen nt\n\t"
        "s_add_u32 s34, s34, 0x10000\n\t"
        "buffer_load_dwordx4 v[196:199], v96, s[16:19], s34 offen nt\n\t"
        "s_add_u32 s34, s34, 0x10000\n\t"
        "buffer_load_dwordx4 v[200:203], v96, s[16:19], s34 offen nt\n\t"
        "s_add_u32 s34, s34, 0x10000\n\t"
        "buffer_load_dwordx4 v[204:207], v96, s[16:19], s34 offen nt\n\t"
        "s_add_u32 s34, s34, 0x10000\n\t"
        "buffer_load_dwordx4 v[208:211], v96, s[16:19], s34 offen nt\n\t"
        "s_add_u32 s34, s34, 0x10000\n\t"
        "buffer_load_dwordx4 v[212:215], v96, s[16:19], s34 offen nt\n\t"
        "s_add_u32 s34, s34, 0x10000\n\t"
        "buffer_load_dwordx4 v[216:219], v96, s[16:19], s34 offen nt\n\t"
        "s_add_u32 s34, s34, 0x10000\n\t"
        "buffer_load_dwordx4 v[220:223], v96, s[16:19], s34 offen nt\n\t"
        "s_waitcnt lgkmcnt(0)\n\t"
        "ds_write_b128 v117, v[104:107] offset:8192\n\t"
        "ds_write_b128 v117, v[108:111] offset:9216\n\t"
        "s_waitcnt vmcnt(16)\n\t"
        "ds_write_b128 v97, v[128:131] offset:0\n\t"
        "ds_write_b128 v98, v[132:135] offset:1024\n\t"
        "ds_write_b128 v97, v[136:139] offset:2048\n\t"
        "ds_write_b128 v98, v[140:143] offset:3072\n\t"
        "ds_write_b128 v97, v[144:147] offset:4096\n\t"
        "ds_write_b128 v98, v[148:151] offset:5120\n\t"
        "ds_write_b128 v97, v[152:155] offset:6144\n\t"
        "ds_write_b128 v98, v[156:159] offset:7168\n\t"
        "ds_read_b64_tr_b16 a[216:217], v99 offset:0\n\t"
        "ds_read_b64_tr_b16 a[218:219], v99 offset:256\n\t"
        "ds_read_b64_tr_b16 a[220:221], v99 offset:4096\n\t"
        "ds_read_b64_tr_b16 a[222:223], v99 offset:4352\n\t"
        "s_waitcnt lgkmcnt(8)\n\t"
        "ds_read_b64_tr_b16 a[224:225], v100 offset:0\n\t"
        "ds_read_b64_tr_b16 a[226:227], v100 offset:256\n\t"
        "ds_read_b64_tr_b16 a[228:229], v100 offset:4096\n\t"
        "ds_read_b64_tr_b16 a[230:231], v100 offset:4352\n\t"
        "s_waitcnt lgkmcnt(8)\n\t"
        "ds_read_b64_tr_b16 a[232:233], v101 offset:0\n\t"
        "ds_read_b64_tr_b16 a[234:235], v101 offset:256\n\t"
        "ds_read_b64_tr_b16 a[236:237], v101 offset:4096\n\t"
        "ds_read_b64_tr_b16 a[238:239], v101 offset:4352\n\t"
        "s_waitcnt lgkmcnt(8)\n\t"
        "ds_read_b64_tr_b16 v[104:105], v102 offset:0\n\t"
        "ds_read_b64_tr_b16 v[106:107], v102 offset:256\n\t"
        "ds_read_b64_tr_b16 v[108:109], v102 offset:4096\n\t"
        "ds_read_b64_tr_b16 v[110:111], v102 offset:4352\n\t"
        "s_lshl_b32 s34, s43, 2\n\t"
        "s_add_u32 s34, s34, 12\n\t"
        "s_and_b32 s34, s34, 15\n\t"
        "s_lshl_b32 s34, s34, 19\n\t"
        "buffer_load_dwordx4 v[224:227], v96, s[16:19], s34 offen nt\n\t"
        "s_add_u32 s34, s34, 0x10000\n\t"
        "buffer_load_dwordx4 v[228:231], v96, s[16:19], s34 offen nt\n\t"
        "s_add_u32 s34, s34, 0x10000\n\t"
        "buffer_load_dwordx4 v[232:235], v96, s[16:19], s34 offen nt\n\t"
        "s_add_u32 s34, s34, 0x10000\n\t"
        "buffer_load_dwordx4 v[236:239], v96, s[16:19], s34 offen nt\n\t"
        "s_add_u32 s34, s34, 0x10000\n\t"
        "buffer_load_dwordx4 v[240:243], v96, s[16:19], s34 offen nt\n\t"
        "s_add_u32 s34, s34, 0x10000\n\t"
        "buffer_load_dwordx4 v[244:247], v96, s[16:19], s34 offen nt\n\t"
        "s_add_u32 s34, s34, 0x10000\n\t"
        "buffer_load_dwordx4 v[248:251], v96, s[16:19], s34 offen nt\n\t"
        "s_add_u32 s34, s34, 0x10000\n\t"
        "buffer_load_dwordx4 v[252:255], v96, s[16:19], s34 offen nt\n\t"
        "s_waitcnt lgkmcnt(0)\n\t"
        "ds_write_b128 v117, v[104:107] offset:10240\n\t"
        "ds_write_b128 v117, v[108:111] offset:11264\n\t"
        "s_waitcnt vmcnt(16)\n\t"
        "ds_write_b128 v97, v[160:163] offset:0\n\t"
        "ds_write_b128 v98, v[164:167] offset:1024\n\t"
        "ds_write_b128 v97, v[168:171] offset:2048\n\t"
        "ds_write_b128 v98, v[172:175] offset:3072\n\t"
        "ds_write_b128 v97, v[176:179] offset:4096\n\t"
        "ds_write_b128 v98, v[180:183] offset:5120\n\t"
        "ds_write_b128 v97, v[184:187] offset:6144\n\t"
        "ds_write_b128 v98, v[188:191] offset:7168\n\t"
        "ds_read_b64_tr_b16 a[240:241], v99 offset:0\n\t"
        "ds_read_b64_tr_b16 a[242:243], v99 offset:256\n\t"
        "ds_read_b64_tr_b16 a[244:245], v99 offset:4096\n\t"
        "ds_read_b64_tr_b16 a[246:247], v99 offset:4352\n\t"
        "s_waitcnt lgkmcnt(8)\n\t"
        "ds_read_b64_tr_b16 a[248:249], v100 offset:0\n\t"
        "ds_read_b64_tr_b16 a[250:251], v100 offset:256\n\t"
        "ds_read_b64_tr_b16 a[252:253], v100 offset:4096\n\t"
        "ds_read_b64_tr_b16 a[254:255], v100 offset:4352\n\t"
        "s_waitcnt lgkmcnt(8)\n\t"
        "ds_read_b64_tr_b16 v[128:129], v101 offset:0\n\t"
        "ds_read_b64_tr_b16 v[130:131], v101 offset:256\n\t"
        "ds_read_b64_tr_b16 v[132:133], v101 offset:4096\n\t"
        "ds_read_b64_tr_b16 v[134:135], v101 offset:4352\n\t"
        "s_waitcnt lgkmcnt(8)\n\t"
        "ds_read_b64_tr_b16 v[104:105], v102 offset:0\n\t"
        "ds_read_b64_tr_b16 v[106:107], v102 offset:256\n\t"
        "ds_read_b64_tr_b16 v[108:109], v102 offset:4096\n\t"
        "ds_read_b64_tr_b16 v[110:111], v102 offset:4352\n\t"
        "s_lshl_b32 s34, s43, 2\n\t"
        "s_add_u32 s34, s34, 13\n\t"
        "s_and_b32 s34, s34, 15\n\t"
        "s_lshl_b32 s34, s34, 19\n\t"
        "buffer_load_dwordx4 v[0:3], v96, s[16:19], s34 offen nt\n\t"
        "s_add_u32 s34, s34, 0x10000\n\t"
        "buffer_load_dwordx4 v[4:7], v96, s[16:19], s34 offen nt\n\t"
        "s_add_u32 s34, s34, 0x10000\n\t"
        "buffer_load_dwordx4 v[8:11], v96, s[16:19], s34 offen nt\n\t"
        "s_add_u32 s34, s34, 0x10000\n\t"
        "buffer_load_dwordx4 v[12:15], v96, s[16:19], s34 offen nt\n\t"
        "s_add_u32 s34, s34, 0x10000\n\t"
        "buffer_load_dwordx4 v[16:19], v96, s[16:19], s34 offen nt\n\t"
        "s_add_u32 s34, s34, 0x10000\n\t"
        "buffer_load_dwordx4 v[20:23], v96, s[16:19], s34 offen nt\n\t"
        "s_add_u32 s34, s34, 0x10000\n\t"
        "buffer_load_dwordx4 v[24:27], v96, s[16:19], s34 offen nt\n\t"
        "s_add_u32 s34, s34, 0x10000\n\t"
        "buffer_load_dwordx4 v[28:31], v96, s[16:19], s34 offen nt\n\t"
        "s_waitcnt lgkmcnt(0)\n\t"
        "ds_write_b128 v117, v[104:107] offset:12288\n\t"
        "ds_write_b128 v117, v[108:111] offset:13312\n\t"
        "s_waitcnt vmcnt(16)\n\t"
        "ds_write_b128 v97, v[192:195] offset:0\n\t"
        "ds_write_b128 v98, v[196:199] offset:1024\n\t"
        "ds_write_b128 v97, v[200:203] offset:2048\n\t"
        "ds_write_b128 v98, v[204:207] offset:3072\n\t"
        "ds_write_b128 v97, v[208:211] offset:4096\n\t"
        "ds_write_b128 v98, v[212:215] offset:5120\n\t"
        "ds_write_b128 v97, v[216:219] offset:6144\n\t"
        "ds_write_b128 v98, v[220:223] offset:7168\n\t"
        "ds_read_b64_tr_b16 v[136:137], v99 offset:0\n\t"
        "ds_read_b64_tr_b16 v[138:139], v99 offset:256\n\t"
        "ds_read_b64_tr_b16 v[140:141], v99 offset:4096\n\t"
        "ds_read_b64_tr_b16 v[142:143], v99 offset:4352\n\t"
        "s_waitcnt lgkmcnt(8)\n\t"
        "ds_read_b64_tr_b16 v[144:145], v100 offset:0\n\t"
        "ds_read_b64_tr_b16 v[146:147], v100 offset:256\n\t"
        "ds_read_b64_tr_b16 v[148:149], v100 offset:4096\n\t"
        "ds_read_b64_tr_b16 v[150:151], v100 offset:4352\n\t"
        "s_waitcnt lgkmcnt(8)\n\t"
        "ds_read_b64_tr_b16 v[152:153], v101 offset:0\n\t"
        "ds_read_b64_tr_b16 v[154:155], v101 offset:256\n\t"
        "ds_read_b64_tr_b16 v[156:157], v101 offset:4096\n\t"
        "ds_read_b64_tr_b16 v[158:159], v101 offset:4352\n\t"
        "s_waitcnt lgkmcnt(8)\n\t"
        "ds_read_b64_tr_b16 v[104:105], v102 offset:0\n\t"
        "ds_read_b64_tr_b16 v[106:107], v102 offset:256\n\t"
        "ds_read_b64_tr_b16 v[108:109], v102 offset:4096\n\t"
        "ds_read_b64_tr_b16 v[110:111], v102 offset:4352\n\t"
        "s_lshl_b32 s34, s43, 2\n\t"
        "s_add_u32 s34, s34, 14\n\t"
        "s_and_b32 s34, s34, 15\n\t"
        "s_lshl_b32 s34, s34, 19\n\t"
        "buffer_load_dwordx4 v[32:35], v96, s[16:19], s34 offen nt\n\t"
        "s_add_u32 s34, s34, 0x10000\n\t"
        "buffer_load_dwordx4 v[36:39], v96, s[16:19], s34 offen nt\n\t"
        "s_add_u32 s34, s34, 0x10000\n\t"
        "buffer_load_dwordx4 v[40:43], v96, s[16:19], s34 offen nt\n\t"
        "s_add_u32 s34, s34, 0x10000\n\t"
        "buffer_load_dwordx4 v[44:47], v96, s[16:19], s34 offen nt\n\t"
        "s_add_u32 s34, s34, 0x10000\n\t"
        "buffer_load_dwordx4 v[48:51], v96, s[16:19], s34 offen nt\n\t"
        "s_add_u32 s34, s34, 0x10000\n\t"
        "buffer_load_dwordx4 v[52:55], v96, s[16:19], s34 offen nt\n\t"
        "s_add_u32 s34, s34, 0x10000\n\t"
        "buffer_load_dwordx4 v[56:59], v96, s[16:19], s34 offen nt\n\t"
        "s_add_u32 s34, s34, 0x10000\n\t"
        "buffer_load_dwordx4 v[60:63], v96, s[16:19], s34 offen nt\n\t"
        "s_waitcnt lgkmcnt(0)\n\t"
        "ds_write_b128 v117, v[104:107] offset:14336\n\t"
        "ds_write_b128 v117, v[108:111] offset:15360\n\t"
        "s_waitcnt vmcnt(16)\n\t"
        "ds_write_b128 v97, v[224:227] offset:0\n\t"
        "ds_write_b128 v98, v[228:231] offset:1024\n\t"
        "ds_write_b128 v97, v[232:235] offset:2048\n\t"
        "ds_write_b128 v98, v[236:239] offset:3072\n\t"
        "ds_write_b128 v97, v[240:243] offset:4096\n\t"
        "ds_write_b128 v98, v[244:247] offset:5120\n\t"
        "ds_write_b128 v97, v[248:251] offset:6144\n\t"
        "ds_write_b128 v98, v[252:255] offset:7168\n\t"
        "ds_read_b64_tr_b16 v[160:161], v99 offset:0\n\t"
        "ds_read_b64_tr_b16 v[162:163], v99 offset:256\n\t"
        "ds_read_b64_tr_b16 v[164:165], v99 offset:4096\n\t"
        "ds_read_b64_tr_b16 v[166:167], v99 offset:4352\n\t"
        "s_waitcnt lgkmcnt(8)\n\t"
        "ds_read_b64_tr_b16 v[168:169], v100 offset:0\n\t"
        "ds_read_b64_tr_b16 v[170:171], v100 offset:256\n\t"
        "ds_read_b64_tr_b16 v[172:173], v100 offset:4096\n\t"
        "ds_read_b64_tr_b16 v[174:175], v100 offset:4352\n\t"
        "s_waitcnt lgkmcnt(8)\n\t"
        "ds_read_b64_tr_b16 v[176:177], v101 offset:0\n\t"
        "ds_read_b64_tr_b16 v[178:179], v101 offset:256\n\t"
        "ds_read_b64_tr_b16 v[180:181], v101 offset:4096\n\t"
        "ds_read_b64_tr_b16 v[182:183], v101 offset:4352\n\t"
        "s_waitcnt lgkmcnt(8)\n\t"
        "ds_read_b64_tr_b16 v[104:105], v102 offset:0\n\t"
        "ds_read_b64_tr_b16 v[106:107], v102 offset:256\n\t"
        "ds_read_b64_tr_b16 v[108:109], v102 offset:4096\n\t"
        "ds_read_b64_tr_b16 v[110:111], v102 offset:4352\n\t"
        "s_lshl_b32 s34, s43, 2\n\t"
        "s_add_u32 s34, s34, 15\n\t"
        "s_and_b32 s34, s34, 15\n\t"
        "s_lshl_b32 s34, s34, 19\n\t"
        "buffer_load_dwordx4 v[64:67], v96, s[16:19], s34 offen nt\n\t"
        "s_add_u32 s34, s34, 0x10000\n\t"
        "buffer_load_dwordx4 v[68:71], v96, s[16:19], s34 offen nt\n\t"
        "s_add_u32 s34, s34, 0x10000\n\t"
        "buffer_load_dwordx4 v[72:75], v96, s[16:19], s34 offen nt\n\t"
        "s_add_u32 s34, s34, 0x10000\n\t"
        "buffer_load_dwordx4 v[76:79], v96, s[16:19], s34 offen nt\n\t"
        "s_add_u32 s34, s34, 0x10000\n\t"
        "buffer_load_dwordx4 v[80:83], v96, s[16:19], s34 offen nt\n\t"
        "s_add_u32 s34, s34, 0x10000\n\t"
        "buffer_load_dwordx4 v[84:87], v96, s[16:19], s34 offen nt\n\t"
        "s_add_u32 s34, s34, 0x10000\n\t"
        "buffer_load_dwordx4 v[88:91], v96, s[16:19], s34 offen nt\n\t"
        "s_add_u32 s34, s34, 0x10000\n\t"
        "buffer_load_dwordx4 v[92:95], v96, s[16:19], s34 offen nt\n\t"
        "s_waitcnt lgkmcnt(0)\n\t"
        "ds_write_b128 v117, v[104:107] offset:16384\n\t"
        "ds_write_b128 v117, v[108:111] offset:17408\n\t"
        "s_waitcnt vmcnt(16)\n\t"
        "ds_write_b128 v97, v[0:3] offset:0\n\t"
        "ds_write_b128 v98, v[4:7] offset:1024\n\t"
        "ds_write_b128 v97, v[8:11] offset:2048\n\t"
        "ds_write_b128 v98, v[12:15] offset:3072\n\t"
        "ds_write_b128 v97, v[16:19] offset:4096\n\t"
        "ds_write_b128 v98, v[20:23] offset:5120\n\t"
        "ds_write_b128 v97, v[24:27] offset:6144\n\t"
        "ds_write_b128 v98, v[28:31] offset:7168\n\t"
        "ds_read_b64_tr_b16 v[184:185], v99 offset:0\n\t"
        "ds_read_b64_tr_b16 v[186:187], v99 offset:256\n\t"
        "ds_read_b64_tr_b16 v[188:189], v99 offset:4096\n\t"
        "ds_read_b64_tr_b16 v[190:191], v99 offset:4352\n\t"
        "s_waitcnt lgkmcnt(8)\n\t"
        "ds_read_b64_tr_b16 v[192:193], v100 offset:0\n\t"
        "ds_read_b64_tr_b16 v[194:195], v100 offset:256\n\t"
        "ds_read_b64_tr_b16 v[196:197], v100 offset:4096\n\t"
        "ds_read_b64_tr_b16 v[198:199], v100 offset:4352\n\t"
        "s_waitcnt lgkmcnt(8)\n\t"
        "ds_read_b64_tr_b16 v[200:201], v101 offset:0\n\t"
        "ds_read_b64_tr_b16 v[202:203], v101 offset:256\n\t"
        "ds_read_b64_tr_b16 v[204:205], v101 offset:4096\n\t"
        "ds_read_b64_tr_b16 v[206:207], v101 offset:4352\n\t"
        "s_waitcnt lgkmcnt(8)\n\t"
        "ds_read_b64_tr_b16 v[104:105], v102 offset:0\n\t"
        "ds_read_b64_tr_b16 v[106:107], v102 offset:256\n\t"
        "ds_read_b64_tr_b16 v[108:109], v102 offset:4096\n\t"
        "ds_read_b64_tr_b16 v[110:111], v102 offset:4352\n\t"
        "s_waitcnt lgkmcnt(0)\n\t"
        "ds_write_b128 v117, v[104:107] offset:18432\n\t"
        "ds_write_b128 v117, v[108:111] offset:19456\n\t"
        "s_waitcnt vmcnt(8)\n\t"
        "ds_write_b128 v97, v[32:35] offset:0\n\t"
        "ds_write_b128 v98, v[36:39] offset:1024\n\t"
        "ds_write_b128 v97, v[40:43] offset:2048\n\t"
        "ds_write_b128 v98, v[44:47] offset:3072\n\t"
        "ds_write_b128 v97, v[48:51] offset:4096\n\t"
        "ds_write_b128 v98, v[52:55] offset:5120\n\t"
        "ds_write_b128 v97, v[56:59] offset:6144\n\t"
        "ds_write_b128 v98, v[60:63] offset:7168\n\t"
        "ds_read_b64_tr_b16 v[208:209], v99 offset:0\n\t"
        "ds_read_b64_tr_b16 v[210:211], v99 offset:256\n\t"
        "ds_read_b64_tr_b16 v[212:213], v99 offset:4096\n\t"
        "ds_read_b64_tr_b16 v[214:215], v99 offset:4352\n\t"
        "s_waitcnt lgkmcnt(8)\n\t"
        "ds_read_b64_tr_b16 v[216:217], v100 offset:0\n\t"
        "ds_read_b64_tr_b16 v[218:219], v100 offset:256\n\t"
        "ds_read_b64_tr_b16 v[220:221], v100 offset:4096\n\t"
        "ds_read_b64_tr_b16 v[222:223], v100 offset:4352\n\t"
        "s_waitcnt lgkmcnt(8)\n\t"
        "ds_read_b64_tr_b16 v[224:225], v101 offset:0\n\t"
        "ds_read_b64_tr_b16 v[226:227], v101 offset:256\n\t"
        "ds_read_b64_tr_b16 v[228:229], v101 offset:4096\n\t"
        "ds_read_b64_tr_b16 v[230:231], v101 offset:4352\n\t"
        "s_waitcnt lgkmcnt(8)\n\t"
        "ds_read_b64_tr_b16 v[104:105], v102 offset:0\n\t"
        "ds_read_b64_tr_b16 v[106:107], v102 offset:256\n\t"
        "ds_read_b64_tr_b16 v[108:109], v102 offset:4096\n\t"
        "ds_read_b64_tr_b16 v[110:111], v102 offset:4352\n\t"
        "s_waitcnt lgkmcnt(0)\n\t"
        "ds_write_b128 v117, v[104:107] offset:20480\n\t"
        "ds_write_b128 v117, v[108:111] offset:21504\n\t"
        "s_waitcnt vmcnt(0)\n\t"
        "ds_write_b128 v97, v[64:67] offset:0\n\t"
        "ds_write_b128 v98, v[68:71] offset:1024\n\t"
        "ds_write_b128 v97, v[72:75] offset:2048\n\t"
        "ds_write_b128 v98, v[76:79] offset:3072\n\t"
        "ds_write_b128 v97, v[80:83] offset:4096\n\t"
        "ds_write_b128 v98, v[84:87] offset:5120\n\t"
        "ds_write_b128 v97, v[88:91] offset:6144\n\t"
        "ds_write_b128 v98, v[92:95] offset:7168\n\t"
        "ds_read_b64_tr_b16 v[232:233], v99 offset:0\n\t"
        "ds_read_b64_tr_b16 v[234:235], v99 offset:256\n\t"
        "ds_read_b64_tr_b16 v[236:237], v99 offset:4096\n\t"
        "ds_read_b64_tr_b16 v[238:239], v99 offset:4352\n\t"
        "s_waitcnt lgkmcnt(8)\n\t"
        "ds_read_b64_tr_b16 v[240:241], v100 offset:0\n\t"
        "ds_read_b64_tr_b16 v[242:243], v100 offset:256\n\t"
        "ds_read_b64_tr_b16 v[244:245], v100 offset:4096\n\t"
        "ds_read_b64_tr_b16 v[246:247], v100 offset:4352\n\t"
        "s_waitcnt lgkmcnt(8)\n\t"
        "ds_read_b64_tr_b16 v[248:249], v101 offset:0\n\t"
        "ds_read_b64_tr_b16 v[250:251], v101 offset:256\n\t"
        "ds_read_b64_tr_b16 v[252:253], v101 offset:4096\n\t"
        "ds_read_b64_tr_b16 v[254:255], v101 offset:4352\n\t"
        "s_waitcnt lgkmcnt(8)\n\t"
        "ds_read_b64_tr_b16 v[104:105], v102 offset:0\n\t"
        "ds_read_b64_tr_b16 v[106:107], v102 offset:256\n\t"
        "ds_read_b64_tr_b16 v[108:109], v102 offset:4096\n\t"
        "ds_read_b64_tr_b16 v[110:111], v102 offset:4352\n\t"
        "s_mov_b32 s35, 33792\n\t"
        "buffer_load_dword v0, off, s[12:15], s35 sc1\n\t"
        "s_lshl_b32 s35, s44, 10\n\t"
        "v_lshlrev_b32_e32 v1, 2, v122\n\t"
        "v_and_b32_e32 v1, 0x3c0, v1\n\t"
        "buffer_load_dword v1, v1, s[12:15], s35 offen offset:0 sc1\n\t"
        "s_waitcnt lgkmcnt(0)\n\t"
        "ds_write_b128 v126, v[104:107] offset:8192\n\t"
        "ds_write_b128 v126, v[108:111] offset:9216\n\t"
        "s_waitcnt lgkmcnt(0)\n\t"
        "v_mov_b32_e32 v110, v103\n\t"
        "ds_read_b128 v[64:67], v126 offset:0\n\t"
        "ds_read_b128 v[68:71], v126 offset:1024\n\t"
        "ds_read_b128 v[72:75], v126 offset:2048\n\t"
        "ds_read_b128 v[76:79], v126 offset:3072\n\t"
        "ds_read_b128 v[80:83], v126 offset:4096\n\t"
        "ds_read_b128 v[84:87], v126 offset:5120\n\t"
        "ds_read_b128 v[88:91], v126 offset:6144\n\t"
        "ds_read_b128 v[92:95], v126 offset:7168\n\t"
        "ds_read_b128 v[96:99], v126 offset:8192\n\t"
        "ds_read_b128 v[100:103], v126 offset:9216\n\t"
        "s_waitcnt lgkmcnt(0)\n\t"
        "v_mov_b32_e32 v126, 0\n\t"
        "s_barrier\n\t"
        "s_mov_b32 s48, 0\n\t"
        "s_mov_b32 s54, 0\n\t"
        "s_movk_i32 s56, 24576\n\t"
        "s_and_b32 s58, s43, 1\n\t"
        "s_lshl_b32 s58, s58, 12\n\t"
        "s_lshl_b32 s57, s44, 5\n\t"
        "s_add_u32 s57, s57, s43\n\t"
        "s_lshl_b32 s57, s57, 2\n\t"
        "s_add_u32 s57, s57, 458752\n\t"
        "s_waitcnt vmcnt(0)\n\t"
        "v_mov_b32_e32 v111, v1\n\t"
        "s_lshr_b32 s59, s48, 1\n\t"
        "s_cmp_gt_u32 s54, s59\n\t"
        "s_cbranch_scc1 LA_kv0ok%=\n\t"
        "s_mov_b32 s42, 0\n\t"
        "s_waitcnt vmcnt(0)\n\t"
        "s_branch LA_kv0w%=\n\t"
        "LA_kv0%=:\n\t"
        "s_waitcnt vmcnt(0)\n\t"
        "LA_kv0w%=:\n\t"
        "v_min_u32_dpp v111, v111, v111 quad_perm:[1,0,3,2] row_mask:0xf bank_mask:0xf\n\t"
        "s_nop 1\n\t"
        "v_min_u32_dpp v111, v111, v111 quad_perm:[2,3,0,1] row_mask:0xf bank_mask:0xf\n\t"
        "s_nop 1\n\t"
        "v_min_u32_dpp v111, v111, v111 row_ror:4 row_mask:0xf bank_mask:0xf\n\t"
        "s_nop 1\n\t"
        "v_min_u32_dpp v111, v111, v111 row_ror:8 row_mask:0xf bank_mask:0xf\n\t"
        "s_nop 0\n\t"
        "v_readfirstlane_b32 s54, v111\n\t"
        "s_nop 0\n\t"
        "s_cmp_gt_u32 s54, s59\n\t"
        "s_cbranch_scc1 LA_kv0ok%=\n\t"
        "s_sleep 4\n\t"
        "s_lshl_b32 s35, s44, 10\n\t"
        "v_lshlrev_b32_e32 v111, 2, v122\n\t"
        "v_and_b32_e32 v111, 0x3c0, v111\n\t"
        "buffer_load_dword v111, v111, s[12:15], s35 offen offset:0 sc1\n\t"
        "s_add_u32 s42, s42, 1\n\t"
        "s_cmp_lt_u32 s42, 0x100000\n\t"
        "s_cbranch_scc1 LA_kv0%=\n\t"
        "s_movk_i32 s34, 16\n\t"
        "s_branch LA_die%=\n\t"
        "LA_kv0ok%=:\n\t"
        "s_and_b32 s36, s48, 1\n\t"
        "s_mul_i32 s36, s36, 24576\n\t"
        "s_add_u32 s36, s36, s55\n\t"
        "s_lshl_b32 s37, s48, 18\n\t"
        "s_lshl_b32 s34, s43, 11\n\t"
        "s_add_u32 s37, s37, s34\n\t"
        "s_add_u32 m0, s36, 0\n\t"
        "s_add_u32 s34, s37, 29548544\n\t"
        "buffer_load_dwordx4 v122, s[12:15], s34 offen sc1 lds\n\t"
        "s_add_u32 m0, s36, 1024\n\t"
        "s_add_u32 s34, s37, 29549568\n\t"
        "buffer_load_dwordx4 v122, s[12:15], s34 offen sc1 lds\n\t"
        "s_add_u32 m0, s36, 8192\n\t"
        "s_add_u32 s34, s37, 12771328\n\t"
        "buffer_load_dwordx4 v122, s[12:15], s34 offen sc1 lds\n\t"
        "s_add_u32 m0, s36, 9216\n\t"
        "s_add_u32 s34, s37, 12772352\n\t"
        "buffer_load_dwordx4 v122, s[12:15], s34 offen sc1 lds\n\t"
        "s_add_u32 m0, s36, 16384\n\t"
        "s_add_u32 s34, s37, 46325760\n\t"
        "buffer_load_dwordx4 v122, s[12:15], s34 offen sc1 lds\n\t"
        "s_add_u32 m0, s36, 17408\n\t"
        "s_add_u32 s34, s37, 46326784\n\t"
        "buffer_load_dwordx4 v122, s[12:15], s34 offen sc1 lds\n\t"
        "s_mov_b32 s42, 0\n\t"
        "LA_cal%=:\n\t"
        "v_readfirstlane_b32 s34, v0\n\t"
        "s_nop 0\n\t"
        "s_cmp_lg_u32 s34, 0\n\t"
        "s_cbranch_scc1 LA_calok%=\n\t"
        "s_sleep 4\n\t"
        "s_mov_b32 s35, 33792\n\t"
        "buffer_load_dword v0, off, s[12:15], s35 sc1\n\t"
        "s_waitcnt vmcnt(0)\n\t"
        "s_add_u32 s42, s42, 1\n\t"
        "s_cmp_lt_u32 s42, 0x100000\n\t"
        "s_cbranch_scc1 LA_cal%=\n\t"
        "s_movk_i32 s34, 8\n\t"
        "s_branch LA_die%=\n\t"
        "LA_calok%=:\n\t"
        "s_and_b32 s34, s34, 8192\n\t"
        "s_add_u32 s8, s8, s34\n\t"
        "s_addc_u32 s9, s9, 0\n\t"
        "s_barrier\n\t"
        "s_cmp_lg_u32 s43, 0\n\t"
        "s_cbranch_scc1 LA_st%=\n\t"
        "v_mov_b32_e32 v2, 1\n\t"
        "s_mov_b32 s35, 34304\n\t"
        "s_mov_b64 exec, 1\n\t"
        "buffer_atomic_add v2, off, s[12:15], s35 sc0\n\t"
        "s_mov_b64 exec, -1\n\t"
        "s_waitcnt vmcnt(0)\n\t"
        "v_readfirstlane_b32 s34, v2\n\t"
        "s_nop 0\n\t"
        "s_cmp_lg_u32 s34, 95\n\t"
        "s_cbranch_scc1 LA_st%=\n\t"
        "s_memrealtime s[32:33]\n\t"
        "v_mbcnt_lo_u32_b32 v3, -1, 0\n\t"
        "v_lshlrev_b32_e32 v3, 7, v3\n\t"
        "s_waitcnt lgkmcnt(0)\n\t"
        "s_add_u32 s34, s32, 50\n\t"
        "s_or_b32 s34, s34, 1\n\t"
        "v_mov_b32_e32 v2, s34\n\t"
        "s_mov_b32 s35, 36864\n\t"
        "s_mov_b64 exec, 0xffff\n\t"
        "buffer_store_dword v2, v3, s[12:15], s35 offen sc1\n\t"
        "s_mov_b64 exec, -1\n\t"
        "LA_st%=:\n\t"
        "s_mov_b32 s24, 0\n\t"
        "s_mov_b32 s26, 0\n\t"
        "s_mov_b32 s27, 0x80000\n\t"
        "s_mov_b32 s28, 0x180000\n\t"
        "s_mov_b32 s31, 0\n\t"
        "s_mov_b32 s47, 0\n\t"
        "s_mov_b32 s38, 0xff00ff00\n\t"
        "s_mov_b32 s39, 0xff00ff00\n\t"
        "s_mov_b32 s40, 0\n\t"
        "s_mov_b32 s41, -1\n\t"
        "ds_read_b128 v[32:35], v117 offset:0\n\t"
        "ds_read_b128 v[36:39], v117 offset:1024\n\t"
        "ds_read_b128 v[40:43], v117 offset:2048\n\t"
        "ds_read_b128 v[44:47], v117 offset:3072\n\t"
        "ds_read_b128 v[48:51], v117 offset:4096\n\t"
        "ds_read_b128 v[52:55], v117 offset:5120\n\t"
        "ds_read_b128 v[56:59], v117 offset:6144\n\t"
        "ds_read_b128 v[60:63], v117 offset:7168\n\t"
        "v_mov_b64_e32 v[0:1], 0\n\t"
        "v_mov_b64_e32 v[2:3], 0\n\t"
        "v_mov_b64_e32 v[4:5], 0\n\t"
        "v_mov_b64_e32 v[6:7], 0\n\t"
        "v_mov_b64_e32 v[8:9], 0\n\t"
        "v_mov_b64_e32 v[10:11], 0\n\t"
        "v_mov_b64_e32 v[12:13], 0\n\t"
        "v_mov_b64_e32 v[14:15], 0\n\t"
        "s_mov_b32 s42, 0\n\t"
        "LA_gw%=:\n\t"
        "buffer_load_dword v16, off, s[12:15], s45 sc1\n\t"
        "s_waitcnt vmcnt(0)\n\t"
        "v_readfirstlane_b32 s25, v16\n\t"
        "s_nop 0\n\t"
        "s_cmp_lg_u32 s25, 0\n\t"
        "s_cbranch_scc1 LA_loop%=\n\t"
        "s_sleep 2\n\t"
        "s_add_u32 s42, s42, 1\n\t"
        "s_cmp_lt_u32 s42, 0x100000\n\t"
        "s_cbranch_scc1 LA_gw%=\n\t"
        "s_movk_i32 s34, 9\n\t"
        "s_branch LA_die%=\n\t"
        "LA_loop%=:\n\t"
        "s_mov_b32 s42, 0\n\t"
        "s_sub_u32 s35, s25, 1\n\t"
        "LA_wa%=:\n\t"
        "s_memrealtime s[32:33]\n\t"
        "s_waitcnt lgkmcnt(0)\n\t"
        "s_sub_u32 s34, s35, s32\n\t"
        "s_cmpk_lt_u32 s34, 0x2000\n\t"
        "s_cbranch_scc1 LA_wa%=\n\t"
        "LA_rd%=:\n\t"
        "buffer_load_dwordx4 v[16:19], v112, s[8:11], s26 offen offset:0 sc1\n\t"
        "s_nop 12\n\t"
        "buffer_load_dwordx4 v[20:23], v112, s[8:11], s26 offen offset:1024 sc1\n\t"
        "s_nop 12\n\t"
        "buffer_load_dwordx4 v[24:27], v112, s[8:11], s26 offen offset:2048 sc1\n\t"
        "s_nop 12\n\t"
        "buffer_load_dwordx4 v[28:31], v112, s[8:11], s26 offen offset:3072 sc1\n\t"
        "LA_go%=:\n\t"
        "s_waitcnt vmcnt(3)\n\t"
        "v_smfmac_f32_16x16x64_bf16 v[0:3], v[16:19], a[0:7], v110\n\t"
        "ds_write_b128 v113, v[16:19] offset:0\n\t"
        "v_smfmac_f32_16x16x64_bf16 v[4:7], v[16:19], a[8:15], v110\n\t"
        "v_max3_u32 v111, v16, v17, v18\n\t"
        "v_max_u32_e32 v111, v19, v111\n\t"
        "v_smfmac_f32_16x16x64_bf16 v[8:11], v[16:19], a[16:23], v110\n\t"
        "v_smfmac_f32_16x16x64_bf16 v[12:15], v[16:19], v[32:39], v110\n\t"
        "s_waitcnt vmcnt(2)\n\t"
        "v_smfmac_f32_16x16x64_bf16 v[0:3], v[20:23], a[24:31], v110\n\t"
        "ds_write_b128 v113, v[20:23] offset:1024\n\t"
        "v_smfmac_f32_16x16x64_bf16 v[4:7], v[20:23], a[32:39], v110\n\t"
        "v_max3_u32 v111, v20, v21, v111\n\t"
        "v_max3_u32 v111, v22, v23, v111\n\t"
        "v_smfmac_f32_16x16x64_bf16 v[8:11], v[20:23], a[40:47], v110\n\t"
        "s_waitcnt vmcnt(1)\n\t"
        "ds_write_b128 v113, v[24:27] offset:2048\n\t"
        "v_smfmac_f32_16x16x64_bf16 v[12:15], v[20:23], v[40:47], v110\n\t"
        "v_max3_u32 v111, v24, v25, v111\n\t"
        "v_max3_u32 v111, v26, v27, v111\n\t"
        "v_smfmac_f32_16x16x64_bf16 v[0:3], v[24:27], a[48:55], v110\n\t"
        "v_smfmac_f32_16x16x64_bf16 v[4:7], v[24:27], a[56:63], v110\n\t"
        "s_waitcnt vmcnt(0)\n\t"
        "ds_write_b128 v113, v[28:31] offset:3072\n\t"
        "v_max3_u32 v111, v28, v29, v111\n\t"
        "v_max3_u32 v111, v30, v31, v111\n\t"
        "v_smfmac_f32_16x16x64_bf16 v[8:11], v[24:27], a[64:71], v110\n\t"
        "v_cmp_eq_u32_e32 vcc, -1, v111\n\t"
        "v_smfmac_f32_16x16x64_bf16 v[12:15], v[24:27], v[48:55], v110\n\t"
        "s_waitcnt lgkmcnt(0)\n\t"
        "s_cbranch_vccnz LA_rdfail%=\n\t"
        "s_barrier\n\t"
        "ds_read_b128 v[16:19], v114 offset:0\n\t"
        "ds_read_b128 v[20:23], v114 offset:1024\n\t"
        "ds_read_b128 v[24:27], v114 offset:2048\n\t"
        "v_smfmac_f32_16x16x64_bf16 v[0:3], v[28:31], a[72:79], v110\n\t"
        "v_smfmac_f32_16x16x64_bf16 v[4:7], v[28:31], a[80:87], v110\n\t"
        "v_smfmac_f32_16x16x64_bf16 v[8:11], v[28:31], a[88:95], v110\n\t"
        "v_smfmac_f32_16x16x64_bf16 v[12:15], v[28:31], v[56:63], v110\n\t"
        "ds_read_b128 v[28:31], v114 offset:3072\n\t"
        "s_waitcnt lgkmcnt(3)\n\t"
        "v_smfmac_f32_16x16x64_bf16 v[12:15], v[16:19], v[64:71], v110\n\t"
        "ds_read_b128 v[32:35], v117 offset:8192\n\t"
        "v_smfmac_f32_16x16x64_bf16 v[0:3], v[16:19], a[96:103], v110\n\t"
        "ds_read_b128 v[36:39], v117 offset:9216\n\t"
        "v_smfmac_f32_16x16x64_bf16 v[4:7], v[16:19], a[104:111], v110\n\t"
        "v_smfmac_f32_16x16x64_bf16 v[8:11], v[16:19], a[112:119], v110\n\t"
        "ds_read_b128 v[16:19], v115 offset:0\n\t"
        "s_waitcnt lgkmcnt(5)\n\t"
        "v_smfmac_f32_16x16x64_bf16 v[12:15], v[20:23], v[72:79], v110\n\t"
        "ds_read_b128 v[40:43], v117 offset:10240\n\t"
        "v_smfmac_f32_16x16x64_bf16 v[0:3], v[20:23], a[120:127], v110\n\t"
        "ds_read_b128 v[44:47], v117 offset:11264\n\t"
        "v_smfmac_f32_16x16x64_bf16 v[4:7], v[20:23], a[128:135], v110\n\t"
        "v_smfmac_f32_16x16x64_bf16 v[8:11], v[20:23], a[136:143], v110\n\t"
        "ds_read_b128 v[20:23], v115 offset:1024\n\t"
        "s_waitcnt lgkmcnt(7)\n\t"
        "v_smfmac_f32_16x16x64_bf16 v[12:15], v[24:27], v[80:87], v110\n\t"
        "ds_read_b128 v[48:51], v117 offset:12288\n\t"
        "v_smfmac_f32_16x16x64_bf16 v[0:3], v[24:27], a[144:151], v110\n\t"
        "ds_read_b128 v[52:55], v117 offset:13312\n\t"
        "v_smfmac_f32_16x16x64_bf16 v[4:7], v[24:27], a[152:159], v110\n\t"
        "v_smfmac_f32_16x16x64_bf16 v[8:11], v[24:27], a[160:167], v110\n\t"
        "ds_read_b128 v[24:27], v115 offset:2048\n\t"
        "s_waitcnt lgkmcnt(9)\n\t"
        "v_smfmac_f32_16x16x64_bf16 v[12:15], v[28:31], v[88:95], v110\n\t"
        "ds_read_b128 v[56:59], v117 offset:14336\n\t"
        "v_smfmac_f32_16x16x64_bf16 v[0:3], v[28:31], a[168:175], v110\n\t"
        "ds_read_b128 v[60:63], v117 offset:15360\n\t"
        "v_smfmac_f32_16x16x64_bf16 v[4:7], v[28:31], a[176:183], v110\n\t"
        "v_smfmac_f32_16x16x64_bf16 v[8:11], v[28:31], a[184:191], v110\n\t"
        "ds_read_b128 v[28:31], v115 offset:3072\n\t"
        "s_waitcnt lgkmcnt(9)\n\t"
        "v_smfmac_f32_16x16x64_bf16 v[12:15], v[16:19], v[32:39], v110\n\t"
        "ds_read_b128 v[32:35], v117 offset:16384\n\t"
        "v_smfmac_f32_16x16x64_bf16 v[0:3], v[16:19], a[192:199], v110\n\t"
        "ds_read_b128 v[36:39], v117 offset:17408\n\t"
        "v_smfmac_f32_16x16x64_bf16 v[4:7], v[16:19], a[200:207], v110\n\t"
        "v_smfmac_f32_16x16x64_bf16 v[8:11], v[16:19], a[208:215], v110\n\t"
        "ds_read_b128 v[16:19], v116 offset:0\n\t"
        "s_waitcnt lgkmcnt(9)\n\t"
        "v_smfmac_f32_16x16x64_bf16 v[12:15], v[20:23], v[40:47], v110\n\t"
        "ds_read_b128 v[40:43], v117 offset:18432\n\t"
        "v_smfmac_f32_16x16x64_bf16 v[0:3], v[20:23], a[216:223], v110\n\t"
        "ds_read_b128 v[44:47], v117 offset:19456\n\t"
        "v_smfmac_f32_16x16x64_bf16 v[4:7], v[20:23], a[224:231], v110\n\t"
        "v_smfmac_f32_16x16x64_bf16 v[8:11], v[20:23], a[232:239], v110\n\t"
        "ds_read_b128 v[20:23], v116 offset:1024\n\t"
        "s_waitcnt lgkmcnt(9)\n\t"
        "v_smfmac_f32_16x16x64_bf16 v[12:15], v[24:27], v[48:55], v110\n\t"
        "ds_read_b128 v[48:51], v117 offset:20480\n\t"
        "v_smfmac_f32_16x16x64_bf16 v[0:3], v[24:27], a[240:247], v110\n\t"
        "ds_read_b128 v[52:55], v117 offset:21504\n\t"
        "v_smfmac_f32_16x16x64_bf16 v[4:7], v[24:27], a[248:255], v110\n\t"
        "v_smfmac_f32_16x16x64_bf16 v[8:11], v[24:27], v[128:135], v110\n\t"
        "ds_read_b128 v[24:27], v116 offset:2048\n\t"
        "s_waitcnt lgkmcnt(9)\n\t"
        "v_smfmac_f32_16x16x64_bf16 v[12:15], v[28:31], v[56:63], v110\n\t"
        "ds_read_b128 v[56:59], v124 offset:3072\n\t"
        "v_smfmac_f32_16x16x64_bf16 v[0:3], v[28:31], v[136:143], v110\n\t"
        "ds_read_b128 v[60:63], v124 offset:7168\n\t"
        "v_smfmac_f32_16x16x64_bf16 v[4:7], v[28:31], v[144:151], v110\n\t"
        "v_smfmac_f32_16x16x64_bf16 v[8:11], v[28:31], v[152:159], v110\n\t"
        "ds_read_b128 v[28:31], v116 offset:3072\n\t"
        "s_waitcnt lgkmcnt(9)\n\t"
        "v_smfmac_f32_16x16x64_bf16 v[12:15], v[16:19], v[32:39], v110\n\t"
        "ds_read_b128 v[32:35], v124 offset:0\n\t"
        "v_smfmac_f32_16x16x64_bf16 v[0:3], v[16:19], v[160:167], v110\n\t"
        "ds_read_b128 v[36:39], v124 offset:4096\n\t"
        "v_smfmac_f32_16x16x64_bf16 v[4:7], v[16:19], v[168:175], v110\n\t"
        "v_smfmac_f32_16x16x64_bf16 v[8:11], v[16:19], v[176:183], v110\n\t"
        "s_waitcnt lgkmcnt(8)\n\t"
        "v_smfmac_f32_16x16x64_bf16 v[12:15], v[20:23], v[40:47], v110\n\t"
        "ds_read_b128 v[40:43], v124 offset:1024\n\t"
        "v_smfmac_f32_16x16x64_bf16 v[8:11], v[20:23], v[200:207], v110\n\t"
        "ds_read_b128 v[44:47], v124 offset:5120\n\t"
        "s_waitcnt lgkmcnt(7)\n\t"
        "v_smfmac_f32_16x16x64_bf16 v[12:15], v[24:27], v[48:55], v110\n\t"
        "ds_read_b128 v[48:51], v124 offset:2048\n\t"
        "v_smfmac_f32_16x16x64_bf16 v[8:11], v[24:27], v[224:231], v110\n\t"
        "ds_read_b128 v[52:55], v124 offset:6144\n\t"
        "s_waitcnt lgkmcnt(6)\n\t"
        "v_smfmac_f32_16x16x64_bf16 v[12:15], v[28:31], v[96:103], v110\n\t"
        "v_smfmac_f32_16x16x64_bf16 v[8:11], v[28:31], v[248:255], v110\n\t"
        "v_smfmac_f32_16x16x64_bf16 v[0:3], v[20:23], v[184:191], v110\n\t"
        "buffer_load_dwordx4 v[104:107], v120, s[8:11], s26 offen sc1\n\t"
        "s_lshl_b32 s34, s43, 11\n\t"
        "v_add_u32_e32 v108, s34, v124\n\t"
        "v_smfmac_f32_16x16x64_bf16 v[0:3], v[24:27], v[208:215], v110\n\t"
        "v_add_u32_e32 v109, s58, v124\n\t"
        "v_smfmac_f32_16x16x64_bf16 v[4:7], v[20:23], v[192:199], v110\n\t"
        "v_smfmac_f32_16x16x64_bf16 v[0:3], v[28:31], v[232:239], v110\n\t"
        "v_smfmac_f32_16x16x64_bf16 v[4:7], v[24:27], v[216:223], v110\n\t"
        "v_smfmac_f32_16x16x64_bf16 v[4:7], v[28:31], v[240:247], v110\n\t"
        "v_permlane32_swap_b32_e32 v8, v12\n\t"
        "v_permlane32_swap_b32_e32 v9, v13\n\t"
        "v_permlane32_swap_b32_e32 v10, v14\n\t"
        "v_permlane32_swap_b32_e32 v11, v15\n\t"
        "v_add_f32_e32 v8, v8, v12\n\t"
        "v_add_f32_e32 v9, v9, v13\n\t"
        "v_add_f32_e32 v10, v10, v14\n\t"
        "v_add_f32_e32 v11, v11, v15\n\t"
        "v_cvt_pk_bf16_f32 v12, v8, v9\n\t"
        "v_cvt_pk_bf16_f32 v13, v10, v11\n\t"
        "ds_write_b64 v118, v[12:13] offset:256\n\t"
        "v_permlane32_swap_b32_e32 v0, v4\n\t"
        "v_permlane32_swap_b32_e32 v1, v5\n\t"
        "v_permlane32_swap_b32_e32 v2, v6\n\t"
        "v_permlane32_swap_b32_e32 v3, v7\n\t"
        "v_pk_add_f32 v[0:1], v[0:1], v[4:5]\n\t"
        "v_pk_add_f32 v[2:3], v[2:3], v[6:7]\n\t"
        "v_cvt_pk_bf16_f32 v4, v0, v1\n\t"
        "v_cvt_pk_bf16_f32 v5, v2, v3\n\t"
        "ds_write_b64 v118, v[4:5]\n\t"
        "s_waitcnt lgkmcnt(0)\n\t"
        "s_barrier\n\t"
        "ds_read_b64_tr_b16 v[16:17], v119 offset:0\n\t"
        "ds_read_b64_tr_b16 v[18:19], v119 offset:32\n\t"
        "ds_read_b64_tr_b16 v[20:21], v119 offset:256\n\t"
        "ds_read_b64_tr_b16 v[22:23], v119 offset:288\n\t"
        "ds_read_b64_tr_b16 v[24:25], v119 offset:512\n\t"
        "ds_read_b64_tr_b16 v[26:27], v119 offset:544\n\t"
        "ds_read_b64_tr_b16 v[28:29], v119 offset:768\n\t"
        "ds_read_b64_tr_b16 v[30:31], v119 offset:800\n\t"
        "buffer_store_dword v126, v127, s[12:15], 0 offen sc1\n\t"
        "s_waitcnt lgkmcnt(6)\n\t"
        "v_mfma_f32_16x16x32_bf16 v[0:3], v[32:35], v[16:19], 0\n\t"
        "ds_read_b64_tr_b16 v[32:33], v119 offset:2112\n\t"
        "ds_read_b64_tr_b16 v[34:35], v119 offset:2144\n\t"
        "v_mfma_f32_16x16x32_bf16 v[4:7], v[36:39], v[16:19], 0\n\t"
        "ds_read_b64_tr_b16 v[36:37], v119 offset:2368\n\t"
        "ds_read_b64_tr_b16 v[38:39], v119 offset:2400\n\t"
        "s_waitcnt lgkmcnt(8)\n\t"
        "v_mfma_f32_16x16x32_bf16 v[0:3], v[40:43], v[20:23], v[0:3]\n\t"
        "ds_read_b64_tr_b16 v[40:41], v119 offset:2624\n\t"
        "ds_read_b64_tr_b16 v[42:43], v119 offset:2656\n\t"
        "v_mfma_f32_16x16x32_bf16 v[4:7], v[44:47], v[20:23], v[4:7]\n\t"
        "ds_read_b64_tr_b16 v[44:45], v119 offset:2880\n\t"
        "ds_read_b64_tr_b16 v[46:47], v119 offset:2912\n\t"
        "s_waitcnt lgkmcnt(10)\n\t"
        "v_mfma_f32_16x16x32_bf16 v[0:3], v[48:51], v[24:27], v[0:3]\n\t"
        "ds_read_b128 v[48:51], v108 offset:16384\n\t"
        "v_mfma_f32_16x16x32_bf16 v[4:7], v[52:55], v[24:27], v[4:7]\n\t"
        "ds_read_b128 v[52:55], v108 offset:17408\n\t"
        "s_waitcnt lgkmcnt(10)\n\t"
        "v_mfma_f32_16x16x32_bf16 v[0:3], v[56:59], v[28:31], v[0:3]\n\t"
        "v_mfma_f32_16x16x32_bf16 v[4:7], v[60:63], v[28:31], v[4:7]\n\t"
        "s_waitcnt lgkmcnt(2)\n\t"
        "v_mfma_f32_16x16x32_bf16 v[8:11], v[32:35], v[16:19], 0\n\t"
        "s_nop 1\n\t"
        "v_mfma_f32_16x16x32_bf16 v[8:11], v[36:39], v[20:23], v[8:11]\n\t"
        "s_nop 1\n\t"
        "v_mfma_f32_16x16x32_bf16 v[8:11], v[40:43], v[24:27], v[8:11]\n\t"
        "v_max3_f32 v59, v0, v1, v2\n\t"
        "v_max3_f32 v59, v59, v3, v4\n\t"
        "v_mfma_f32_16x16x32_bf16 v[8:11], v[44:47], v[28:31], v[8:11]\n\t"
        "v_max3_f32 v59, v59, v5, v6\n\t"
        "v_max_f32_e32 v60, v59, v7\n\t"
        "v_max_f32_e32 v59, v59, v7\n\t"
        "v_cndmask_b32_e64 v0, v0, v4, s[38:39]\n\t"
        "v_cndmask_b32_e64 v1, v1, v5, s[38:39]\n\t"
        "v_permlane32_swap_b32_e32 v59, v60\n\t"
        "v_cndmask_b32_e64 v2, v2, v6, s[38:39]\n\t"
        "v_cndmask_b32_e64 v3, v3, v7, s[38:39]\n\t"
        "v_max3_f32 v59, v59, v60, v8\n\t"
        "v_max3_f32 v59, v59, v9, v10\n\t"
        "v_max_f32_e32 v60, v59, v11\n\t"
        "v_max_f32_e32 v59, v59, v11\n\t"
        "v_cndmask_b32_e64 v8, v8, v9, s[38:39]\n\t"
        "v_cndmask_b32_e64 v10, v10, v11, s[38:39]\n\t"
        "v_permlane16_swap_b32_e32 v59, v60\n\t"
        "v_max_f32_e32 v59, v59, v60\n\t"
        "v_mul_f32_e32 v59, 0xbe02940c, v59\n\t"
        "v_cndmask_b32_e64 v58, v8, v10, s[40:41]\n\t"
        "v_fmamk_f32 v0, v0, 0x3e02940c, v59\n\t"
        "v_exp_f32_e32 v0, v0\n\t"
        "v_fmamk_f32 v1, v1, 0x3e02940c, v59\n\t"
        "v_exp_f32_e32 v1, v1\n\t"
        "v_fmamk_f32 v2, v2, 0x3e02940c, v59\n\t"
        "v_exp_f32_e32 v2, v2\n\t"
        "v_fmamk_f32 v3, v3, 0x3e02940c, v59\n\t"
        "v_exp_f32_e32 v3, v3\n\t"
        "v_fmamk_f32 v58, v58, 0x3e02940c, v59\n\t"
        "v_cvt_pk_bf16_f32 v12, v0, v1\n\t"
        "v_cvt_pk_bf16_f32 v13, v2, v3\n\t"
        "v_exp_f32_e32 v58, v58\n\t"
        "v_add_f32_e32 v60, v0, v1\n\t"
        "v_mov_b32_dpp v14, v12 row_ror:8 row_mask:0xf bank_mask:0xf\n\t"
        "v_mov_b32_dpp v15, v13 row_ror:8 row_mask:0xf bank_mask:0xf\n\t"
        "v_mov_b32_dpp v59, v58 row_ror:8 row_mask:0xf bank_mask:0xf\n\t"
        "v_add_f32_e32 v61, v2, v3\n\t"
        "s_waitcnt lgkmcnt(0)\n\t"
        "v_mfma_f32_16x16x32_bf16 v[4:7], v[48:51], v[12:15], 0\n\t"
        "v_cvt_pk_bf16_f32 v59, v58, v59\n\t"
        "v_add_f32_e32 v60, v60, v58\n\t"
        "v_mfma_f32_16x16x32_bf16 v[8:11], v[52:55], v[12:15], 0\n\t"
        "v_cndmask_b32_e64 v56, v59, 0, s[40:41]\n\t"
        "v_cndmask_b32_e64 v57, 0, v59, s[40:41]\n\t"
        "v_add_f32_e32 v59, v60, v61\n\t"
        "s_waitcnt vmcnt(1)\n\t"
        "v_max3_u32 v111, v104, v105, v106\n\t"
        "v_mfma_f32_16x16x16_bf16 v[4:7], v[104:105], v[56:57], v[4:7]\n\t"
        "v_add_f32_dpp v60, v59, v59 row_ror:8 row_mask:0xf bank_mask:0xf\n\t"
        "v_add_f32_dpp v61, v59, v59 row_ror:8 row_mask:0xf bank_mask:0xf\n\t"
        "v_mfma_f32_16x16x16_bf16 v[8:11], v[106:107], v[56:57], v[8:11]\n\t"
        "v_max_u32_e32 v111, v111, v107\n\t"
        "v_permlane16_swap_b32_e32 v60, v61\n\t"
        "v_add_f32_e32 v59, v60, v61\n\t"
        "v_add_f32_e32 v60, v60, v61\n\t"
        "v_cmp_eq_u32_e32 vcc, -1, v111\n\t"
        "s_nop 0\n\t"
        "v_permlane32_swap_b32_e32 v59, v60\n\t"
        "v_add_f32_e32 v59, v59, v60\n\t"
        "v_rcp_f32_e32 v59, v59\n\t"
        "s_cbranch_vccnz LA_vmfail%=\n\t"
        "LA_vmok%=:\n\t"
        "v_mul_f32_e32 v4, v4, v59\n\t"
        "v_mul_f32_e32 v5, v5, v59\n\t"
        "v_cvt_pk_bf16_f32 v0, v4, v5\n\t"
        "v_mul_f32_e32 v6, v6, v59\n\t"
        "v_mul_f32_e32 v7, v7, v59\n\t"
        "v_cvt_pk_bf16_f32 v1, v6, v7\n\t"
        "v_mul_f32_e32 v8, v8, v59\n\t"
        "v_mul_f32_e32 v9, v9, v59\n\t"
        "v_cvt_pk_bf16_f32 v2, v8, v9\n\t"
        "v_mul_f32_e32 v10, v10, v59\n\t"
        "v_mul_f32_e32 v11, v11, v59\n\t"
        "v_cvt_pk_bf16_f32 v3, v10, v11\n\t"
        "buffer_store_dwordx4 v[0:3], v121, s[8:11], s26 offen sc1\n\t"
        "s_memrealtime s[52:53]\n\t"
        "ds_read_b128 v[16:19], v109 offset:8192\n\t"
        "ds_read_b128 v[20:23], v109 offset:9216\n\t"
        "ds_read_b128 v[24:27], v109 offset:10240\n\t"
        "ds_read_b128 v[28:31], v109 offset:11264\n\t"
        "ds_read_b128 v[56:59], v124 offset:0\n\t"
        "ds_read_b128 v[60:63], v124 offset:4096\n\t"
        "ds_read_b128 v[12:15], v124 offset:1024\n\t"
        "v_xor_b32_e32 v126, 0x800, v108\n\t"
        "s_add_u32 s48, s24, 1\n\t"
        "s_min_u32 s48, s48, 63\n\t"
        "buffer_load_dwordx2 v[108:109], v125, s[8:11], s26 offen sc1\n\t"
        "s_lshl_b32 s35, s44, 10\n\t"
        "v_lshlrev_b32_e32 v111, 2, v122\n\t"
        "v_and_b32_e32 v111, 0x3c0, v111\n\t"
        "buffer_load_dword v111, v111, s[12:15], s35 offen offset:0 sc1\n\t"
        "s_waitcnt lgkmcnt(6)\n\t"
        "v_mfma_f32_16x16x32_bf16 v[8:11], v[32:35], v[16:19], 0\n\t"
        "ds_read_b128 v[32:35], v124 offset:5120\n\t"
        "s_waitcnt lgkmcnt(6)\n\t"
        "v_mfma_f32_16x16x32_bf16 v[8:11], v[36:39], v[20:23], v[8:11]\n\t"
        "ds_read_b128 v[36:39], v124 offset:2048\n\t"
        "s_waitcnt lgkmcnt(6)\n\t"
        "v_mfma_f32_16x16x32_bf16 v[8:11], v[40:43], v[24:27], v[8:11]\n\t"
        "ds_read_b128 v[40:43], v124 offset:6144\n\t"
        "s_waitcnt lgkmcnt(6)\n\t"
        "v_mfma_f32_16x16x32_bf16 v[8:11], v[44:47], v[28:31], v[8:11]\n\t"
        "ds_read_b128 v[44:47], v124 offset:3072\n\t"
        "s_add_u32 s35, s24, 1\n\t"
        "s_waitcnt lgkmcnt(6)\n\t"
        "v_mfma_f32_16x16x32_bf16 v[0:3], v[56:59], v[16:19], 0\n\t"
        "ds_read_b128 v[56:59], v124 offset:7168\n\t"
        "s_waitcnt lgkmcnt(6)\n\t"
        "v_mfma_f32_16x16x32_bf16 v[4:7], v[60:63], v[16:19], 0\n\t"
        "s_and_b32 s36, s35, 1\n\t"
        "s_mul_i32 s36, s36, 24576\n\t"
        "s_waitcnt lgkmcnt(5)\n\t"
        "v_mfma_f32_16x16x32_bf16 v[0:3], v[12:15], v[20:23], v[0:3]\n\t"
        "s_add_u32 s36, s36, s55\n\t"
        "s_lshl_b32 s37, s48, 18\n\t"
        "s_waitcnt lgkmcnt(4)\n\t"
        "v_mfma_f32_16x16x32_bf16 v[4:7], v[32:35], v[20:23], v[4:7]\n\t"
        "v_xor_b32_e32 v32, 0x200, v120\n\t"
        "buffer_load_dwordx4 v[12:15], v32, s[8:11], s26 offen sc1\n\t"
        "s_waitcnt lgkmcnt(3)\n\t"
        "v_mfma_f32_16x16x32_bf16 v[0:3], v[36:39], v[24:27], v[0:3]\n\t"
        "s_lshl_b32 s34, s43, 11\n\t"
        "s_add_u32 s37, s37, s34\n\t"
        "s_waitcnt lgkmcnt(2)\n\t"
        "v_mfma_f32_16x16x32_bf16 v[4:7], v[40:43], v[24:27], v[4:7]\n\t"
        "v_cvt_pk_bf16_f32 v8, 0, v8\n\t"
        "v_cvt_pk_bf16_f32 v9, 0, v9\n\t"
        "v_cvt_pk_bf16_f32 v10, 0, v10\n\t"
        "v_cvt_pk_bf16_f32 v11, 0, v11\n\t"
        "s_waitcnt lgkmcnt(1)\n\t"
        "v_mfma_f32_16x16x32_bf16 v[0:3], v[44:47], v[28:31], v[0:3]\n\t"
        "v_mul_f32_e32 v8, 0x3db504f3, v8\n\t"
        "v_mul_f32_e32 v9, 0x3db504f3, v9\n\t"
        "v_mul_f32_e32 v10, 0x3db504f3, v10\n\t"
        "v_mul_f32_e32 v11, 0x3db504f3, v11\n\t"
        "s_waitcnt lgkmcnt(0)\n\t"
        "v_mfma_f32_16x16x32_bf16 v[4:7], v[56:59], v[28:31], v[4:7]\n\t"
        "v_cvt_pk_bf16_f32 v8, 0, v8\n\t"
        "v_cvt_pk_bf16_f32 v9, 0, v9\n\t"
        "v_cvt_pk_bf16_f32 v10, 0, v10\n\t"
        "v_cvt_pk_bf16_f32 v11, 0, v11\n\t"
        "ds_read_b128 v[56:59], v126 offset:16384\n\t"
        "ds_read_b128 v[60:63], v126 offset:17408\n\t"
        "v_mov_b64_e32 v[36:37], -1\n\t"
        "v_mov_b64_e32 v[38:39], -1\n\t"
        "v_mov_b32_e32 v40, 0xce6e6b28\n\t"
        "v_mov_b32_e32 v46, 0x3db504f3\n\t"
        "v_mov_b32_e32 v47, 0x3fb8aa3b\n\t"
        "v_cvt_pk_bf16_f32 v0, 0, v0\n\t"
        "v_cvt_pk_bf16_f32 v1, 0, v1\n\t"
        "v_cvt_pk_bf16_f32 v2, 0, v2\n\t"
        "v_cvt_pk_bf16_f32 v3, 0, v3\n\t"
        "v_cvt_pk_bf16_f32 v4, 0, v4\n\t"
        "v_cvt_pk_bf16_f32 v5, 0, v5\n\t"
        "v_cvt_pk_bf16_f32 v6, 0, v6\n\t"
        "v_cvt_pk_bf16_f32 v7, 0, v7\n\t"
        "v_pk_mul_f32 v[0:1], v[0:1], v[46:47] op_sel_hi:[1,0]\n\t"
        "v_pk_mul_f32 v[2:3], v[2:3], v[46:47] op_sel_hi:[1,0]\n\t"
        "v_pk_mul_f32 v[4:5], v[4:5], v[46:47] op_sel_hi:[1,0]\n\t"
        "v_pk_mul_f32 v[6:7], v[6:7], v[46:47] op_sel_hi:[1,0]\n\t"
        "s_lshr_b32 s59, s48, 1\n\t"
        "s_cmp_gt_u32 s54, s59\n\t"
        "s_cbranch_scc1 LA_kvok%=\n\t"
        "s_mov_b32 s42, 0\n\t"
        "s_waitcnt vmcnt(1)\n\t"
        "s_branch LA_kvw%=\n\t"
        "LA_kv%=:\n\t"
        "s_waitcnt vmcnt(0)\n\t"
        "LA_kvw%=:\n\t"
        "v_min_u32_dpp v111, v111, v111 quad_perm:[1,0,3,2] row_mask:0xf bank_mask:0xf\n\t"
        "s_nop 1\n\t"
        "v_min_u32_dpp v111, v111, v111 quad_perm:[2,3,0,1] row_mask:0xf bank_mask:0xf\n\t"
        "s_nop 1\n\t"
        "v_min_u32_dpp v111, v111, v111 row_ror:4 row_mask:0xf bank_mask:0xf\n\t"
        "s_nop 1\n\t"
        "v_min_u32_dpp v111, v111, v111 row_ror:8 row_mask:0xf bank_mask:0xf\n\t"
        "s_nop 0\n\t"
        "v_readfirstlane_b32 s54, v111\n\t"
        "s_nop 0\n\t"
        "s_cmp_gt_u32 s54, s59\n\t"
        "s_cbranch_scc1 LA_kvok%=\n\t"
        "s_sleep 4\n\t"
        "s_lshl_b32 s35, s44, 10\n\t"
        "v_lshlrev_b32_e32 v111, 2, v122\n\t"
        "v_and_b32_e32 v111, 0x3c0, v111\n\t"
        "buffer_load_dword v111, v111, s[12:15], s35 offen offset:0 sc1\n\t"
        "s_add_u32 s42, s42, 1\n\t"
        "s_cmp_lt_u32 s42, 0x100000\n\t"
        "s_cbranch_scc1 LA_kv%=\n\t"
        "s_movk_i32 s34, 16\n\t"
        "s_branch LA_die%=\n\t"
        "LA_kvok%=:\n\t"
        "s_add_u32 m0, s36, 0\n\t"
        "s_add_u32 s34, s37, 29548544\n\t"
        "buffer_load_dwordx4 v122, s[12:15], s34 offen sc1 lds\n\t"
        "v_cvt_pk_bf16_f32 v0, 0, v0\n\t"
        "v_cvt_pk_bf16_f32 v1, 0, v1\n\t"
        "v_cvt_pk_bf16_f32 v2, 0, v2\n\t"
        "v_cvt_pk_bf16_f32 v3, 0, v3\n\t"
        "v_cvt_pk_bf16_f32 v4, 0, v4\n\t"
        "v_cvt_pk_bf16_f32 v5, 0, v5\n\t"
        "v_cvt_pk_bf16_f32 v6, 0, v6\n\t"
        "v_cvt_pk_bf16_f32 v7, 0, v7\n\t"
        "s_add_u32 m0, s36, 1024\n\t"
        "s_add_u32 s34, s37, 29549568\n\t"
        "buffer_load_dwordx4 v122, s[12:15], s34 offen sc1 lds\n\t"
        "v_cndmask_b32_e64 v0, v40, v0, s[60:61]\n\t"
        "v_cndmask_b32_e64 v1, v40, v1, s[62:63]\n\t"
        "v_cndmask_b32_e64 v2, v40, v2, s[64:65]\n\t"
        "v_cndmask_b32_e64 v3, v40, v3, s[66:67]\n\t"
        "v_cndmask_b32_e64 v4, v40, v4, s[68:69]\n\t"
        "v_cndmask_b32_e64 v5, v40, v5, s[70:71]\n\t"
        "v_cndmask_b32_e64 v6, v40, v6, s[72:73]\n\t"
        "v_cndmask_b32_e64 v7, v40, v7, s[74:75]\n\t"
        "s_add_u32 m0, s36, 8192\n\t"
        "s_add_u32 s34, s37, 12771328\n\t"
        "buffer_load_dwordx4 v122, s[12:15], s34 offen sc1 lds\n\t"
        "v_max3_f32 v42, v0, v1, v2\n\t"
        "v_max3_f32 v42, v42, v3, v4\n\t"
        "v_max3_f32 v42, v42, v5, v6\n\t"
        "v_max3_f32 v42, v42, v7, v8\n\t"
        "v_max3_f32 v42, v42, v9, v10\n\t"
        "v_mov_b32_e32 v43, v11\n\t"
        "v_max_f32_e32 v44, v42, v43\n\t"
        "v_max_f32_e32 v42, v42, v43\n\t"
        "s_pack_ll_b32_b16 s34, s52, s24\n\t"
        "v_mov_b32_e32 v40, s34\n\t"
        "s_add_u32 s35, s26, s57\n\t"
        "v_permlane16_swap_b32_e32 v42, v44\n\t"
        "v_max_f32_e32 v45, v42, v44\n\t"
        "v_max_f32_e32 v42, v42, v44\n\t"
        "s_mov_b64 exec, 1\n\t"
        "buffer_store_dword v40, off, s[8:11], s35 sc1\n\t"
        "s_mov_b64 exec, -1\n\t"
        "v_permlane32_swap_b32_e32 v42, v45\n\t"
        "v_max_f32_e32 v42, v42, v45\n\t"
        "v_mul_f32_e32 v42, 0xbfb8aa3b, v42\n\t"
        "v_pk_fma_f32 v[0:1], v[0:1], v[46:47], v[42:43] op_sel:[0,1,0] op_sel_hi:[1,1,0]\n\t"
        "s_add_u32 m0, s36, 9216\n\t"
        "s_add_u32 s34, s37, 12772352\n\t"
        "buffer_load_dwordx4 v122, s[12:15], s34 offen sc1 lds\n\t"
        "v_exp_f32_e32 v0, v0\n\t"
        "v_exp_f32_e32 v1, v1\n\t"
        "v_pk_fma_f32 v[2:3], v[2:3], v[46:47], v[42:43] op_sel:[0,1,0] op_sel_hi:[1,1,0]\n\t"
        "v_exp_f32_e32 v2, v2\n\t"
        "v_exp_f32_e32 v3, v3\n\t"
        "v_pk_fma_f32 v[4:5], v[4:5], v[46:47], v[42:43] op_sel:[0,1,0] op_sel_hi:[1,1,0]\n\t"
        "v_exp_f32_e32 v4, v4\n\t"
        "s_add_u32 m0, s36, 16384\n\t"
        "s_add_u32 s34, s37, 46325760\n\t"
        "buffer_load_dwordx4 v122, s[12:15], s34 offen sc1 lds\n\t"
        "v_exp_f32_e32 v5, v5\n\t"
        "v_pk_fma_f32 v[6:7], v[6:7], v[46:47], v[42:43] op_sel:[0,1,0] op_sel_hi:[1,1,0]\n\t"
        "v_exp_f32_e32 v6, v6\n\t"
        "v_exp_f32_e32 v7, v7\n\t"
        "v_pk_fma_f32 v[8:9], v[8:9], v[46:47], v[42:43] op_sel:[0,1,0] op_sel_hi:[1,1,0]\n\t"
        "v_exp_f32_e32 v8, v8\n\t"
        "v_exp_f32_e32 v9, v9\n\t"
        "s_add_u32 m0, s36, 17408\n\t"
        "s_add_u32 s34, s37, 46326784\n\t"
        "buffer_load_dwordx4 v122, s[12:15], s34 offen sc1 lds\n\t"
        "v_pk_fma_f32 v[10:11], v[10:11], v[46:47], v[42:43] op_sel:[0,1,0] op_sel_hi:[1,1,0]\n\t"
        "v_exp_f32_e32 v10, v10\n\t"
        "v_exp_f32_e32 v11, v11\n\t"
        "v_pk_add_f32 v[42:43], v[0:1], v[2:3]\n\t"
        "v_cndmask_b32_e64 v8, v8, 0, s[40:41]\n\t"
        "v_cndmask_b32_e64 v9, v9, 0, s[40:41]\n\t"
        "v_cndmask_b32_e64 v10, v10, 0, s[40:41]\n\t"
        "buffer_store_dwordx4 v[36:39], v121, s[8:11], s28 offen sc1\n\t"
        "v_cndmask_b32_e64 v11, v11, 0, s[40:41]\n\t"
        "v_pk_add_f32 v[42:43], v[42:43], v[4:5]\n\t"
        "v_pk_add_f32 v[42:43], v[42:43], v[6:7]\n\t"
        "v_pk_add_f32 v[42:43], v[42:43], v[8:9]\n\t"
        "v_pk_add_f32 v[42:43], v[42:43], v[10:11]\n\t"
        "v_add_f32_e32 v44, v42, v43\n\t"
        "v_add_f32_e32 v42, v42, v43\n\t"
        "s_lshl_b32 s34, s24, 18\n\t"
        "s_add_u32 s34, s34, 63102976\n\t"
        "v_permlane16_swap_b32_e32 v42, v44\n\t"
        "v_add_f32_e32 v45, v42, v44\n\t"
        "v_add_f32_e32 v42, v42, v44\n\t"
        "s_add_u32 s35, s24, 1\n\t"
        "v_mov_b32_e32 v126, s35\n\t"
        "v_permlane32_swap_b32_e32 v42, v45\n\t"
        "v_add_f32_e32 v42, v42, v45\n\t"
        "v_rcp_f32_e32 v42, v42\n\t"
        "s_waitcnt vmcnt(8) lgkmcnt(0)\n\t"
        "v_max3_u32 v45, v12, v13, v14\n\t"
        "v_max_u32_e32 v45, v45, v15\n\t"
        "v_cmp_eq_u32_e32 vcc, -1, v45\n\t"
        "s_nop 0\n\t"
        "s_cbranch_vccnz LA_v2fail%=\n\t"
        "LA_v2ok%=:\n\t"
        "v_and_b32_e32 v45, 0xff, v109\n\t"
        "v_cmp_ne_u32_e32 vcc, s24, v45\n\t"
        "s_nop 0\n\t"
        "s_cbranch_vccnz LA_stre%=\n\t"
        "LA_stok%=:\n\t"
        "v_pk_mul_f32 v[0:1], v[0:1], v[42:43] op_sel_hi:[1,0]\n\t"
        "v_subrev_u32_e32 v108, s25, v108\n\t"
        "v_min_u32_dpp v111, v111, v111 quad_perm:[1,0,3,2] row_mask:0xf bank_mask:0xf\n\t"
        "v_cvt_pk_bf16_f32 v32, v0, v1\n\t"
        "v_pk_mul_f32 v[2:3], v[2:3], v[42:43] op_sel_hi:[1,0]\n\t"
        "v_cvt_pk_bf16_f32 v33, v2, v3\n\t"
        "v_max_i32_dpp v108, v108, v108 quad_perm:[1,0,3,2] row_mask:0xf bank_mask:0xf\n\t"
        "v_min_u32_dpp v111, v111, v111 quad_perm:[2,3,0,1] row_mask:0xf bank_mask:0xf\n\t"
        "v_pk_mul_f32 v[4:5], v[4:5], v[42:43] op_sel_hi:[1,0]\n\t"
        "v_cvt_pk_bf16_f32 v34, v4, v5\n\t"
        "v_pk_mul_f32 v[6:7], v[6:7], v[42:43] op_sel_hi:[1,0]\n\t"
        "v_max_i32_dpp v108, v108, v108 quad_perm:[2,3,0,1] row_mask:0xf bank_mask:0xf\n\t"
        "v_min_u32_dpp v111, v111, v111 row_ror:4 row_mask:0xf bank_mask:0xf\n\t"
        "v_cvt_pk_bf16_f32 v35, v6, v7\n\t"
        "v_pk_mul_f32 v[8:9], v[8:9], v[42:43] op_sel_hi:[1,0]\n\t"
        "v_cvt_pk_bf16_f32 v36, v8, v9\n\t"
        "v_max_i32_dpp v108, v108, v108 row_ror:4 row_mask:0xf bank_mask:0xf\n\t"
        "v_min_u32_dpp v111, v111, v111 row_ror:8 row_mask:0xf bank_mask:0xf\n\t"
        "v_pk_mul_f32 v[10:11], v[10:11], v[42:43] op_sel_hi:[1,0]\n\t"
        "v_cvt_pk_bf16_f32 v37, v10, v11\n\t"
        "v_mfma_f32_16x16x32_bf16 v[16:19], v[48:51], v[32:35], 0\n\t"
        "v_max_i32_dpp v108, v108, v108 row_ror:8 row_mask:0xf bank_mask:0xf\n\t"
        "v_mfma_f32_16x16x32_bf16 v[20:23], v[52:55], v[32:35], 0\n\t"
        "v_mfma_f32_16x16x32_bf16 v[24:27], v[56:59], v[32:35], 0\n\t"
        "v_mfma_f32_16x16x32_bf16 v[28:31], v[60:63], v[32:35], 0\n\t"
        "v_max_i32_dpp v108, v108, v108 row_bcast:15 row_mask:0xa bank_mask:0xf\n\t"
        "v_xor_b32_e32 v45, 0x400, v123\n\t"
        "v_add_u32_e32 v124, s56, v124\n\t"
        "v_mfma_f32_16x16x16_bf16 v[16:19], v[104:105], v[36:37], v[16:19]\n\t"
        "v_max_i32_dpp v108, v108, v108 row_bcast:31 row_mask:0xc bank_mask:0xf\n\t"
        "v_mfma_f32_16x16x16_bf16 v[20:23], v[106:107], v[36:37], v[20:23]\n\t"
        "v_mfma_f32_16x16x16_bf16 v[24:27], v[12:13], v[36:37], v[24:27]\n\t"
        "v_mfma_f32_16x16x16_bf16 v[28:31], v[14:15], v[36:37], v[28:31]\n\t"
        "ds_read_b128 v[48:51], v117 offset:4096\n\t"
        "ds_read_b128 v[52:55], v117 offset:5120\n\t"
        "ds_read_b128 v[56:59], v117 offset:6144\n\t"
        "ds_read_b128 v[60:63], v117 offset:7168\n\t"
        "s_sub_u32 s56, 0, s56\n\t"
        "v_cvt_pk_bf16_f32 v0, v16, v17\n\t"
        "v_cvt_pk_bf16_f32 v1, v18, v19\n\t"
        "v_cvt_pk_bf16_f32 v2, v20, v21\n\t"
        "v_cvt_pk_bf16_f32 v3, v22, v23\n\t"
        "buffer_store_dwordx4 v[0:3], v123, s[12:15], s34 offen sc1\n\t"
        "v_cvt_pk_bf16_f32 v4, v24, v25\n\t"
        "v_cvt_pk_bf16_f32 v5, v26, v27\n\t"
        "v_cvt_pk_bf16_f32 v6, v28, v29\n\t"
        "v_cvt_pk_bf16_f32 v7, v30, v31\n\t"
        "buffer_store_dwordx4 v[4:7], v45, s[12:15], s34 offen sc1\n\t"
        "s_cmp_eq_u32 s24, 63\n\t"
        "s_cbranch_scc1 LA_last%=\n\t"
        "ds_read_b128 v[32:35], v117 offset:0\n\t"
        "ds_read_b128 v[36:39], v117 offset:1024\n\t"
        "ds_read_b128 v[40:43], v117 offset:2048\n\t"
        "ds_read_b128 v[44:47], v117 offset:3072\n\t"
        "v_mov_b64_e32 v[14:15], 0\n\t"
        "v_mov_b64_e32 v[12:13], 0\n\t"
        "v_mov_b64_e32 v[10:11], 0\n\t"
        "v_mov_b64_e32 v[8:9], 0\n\t"
        "v_mov_b64_e32 v[6:7], 0\n\t"
        "v_mov_b64_e32 v[4:5], 0\n\t"
        "v_mov_b64_e32 v[2:3], 0\n\t"
        "v_mov_b64_e32 v[0:1], 0\n\t"
        "v_readfirstlane_b32 s54, v111\n\t"
        "v_readfirstlane_b32 s35, v109\n\t"
        "v_readlane_b32 s36, v108, 63\n\t"
        "s_bfe_u32 s29, s35, 0xc0008\n\t"
        "s_lshr_b32 s30, s35, 20\n\t"
        "s_add_u32 s35, s31, s50\n\t"
        "s_mul_i32 s37, s50, 3\n\t"
        "s_min_u32 s35, s35, s37\n\t"
        "s_add_i32 s34, s36, s51\n\t"
        "s_cmp_gt_i32 s34, 0\n\t"
        "s_cselect_b32 s31, s35, 0\n\t"
        "s_cselect_b32 s34, s34, 0\n\t"
        "s_add_u32 s34, s34, s31\n\t"
        "s_add_u32 s37, s25, s34\n\t"
        "s_add_u32 s45, s37, s29\n\t"
        "s_add_u32 s25, s45, s30\n\t"
        "s_add_u32 s24, s24, 1\n\t"
        "s_mov_b32 s28, s26\n\t"
        "s_mov_b32 s26, s27\n\t"
        "s_add_u32 s27, s27, 0x80000\n\t"
        "s_and_b32 s27, s27, 0x1fffff\n\t"
        "s_branch LA_loop%=\n\t"
        "LA_last%=:\n\t"
        "s_waitcnt vmcnt(0)\n\t"
        "s_barrier\n\t"
        "buffer_store_dword v126, v127, s[12:15], 0 offen sc1\n\t"
        "s_endpgm\n\t"
        "LA_stre%=:\n\t"
        "s_mov_b32 s42, 0\n\t"
        "LA_str2%=:\n\t"
        "buffer_load_dwordx2 v[108:109], v125, s[8:11], s26 offen sc1\n\t"
        "s_waitcnt vmcnt(0)\n\t"
        "v_and_b32_e32 v45, 0xff, v109\n\t"
        "v_cmp_ne_u32_e32 vcc, s24, v45\n\t"
        "s_nop 0\n\t"
        "s_cbranch_vccz LA_stok%=\n\t"
        "s_add_u32 s42, s42, 1\n\t"
        "s_cmp_lt_u32 s42, 0x100000\n\t"
        "s_cbranch_scc1 LA_str2%=\n\t"
        "s_movk_i32 s34, 5\n\t"
        "s_branch LA_die%=\n\t"
        "LA_v2fail%=:\n\t"
        "s_mov_b32 s42, 0\n\t"
        "LA_v2re%=:\n\t"
        "v_xor_b32_e32 v45, 0x200, v120\n\t"
        "buffer_load_dwordx4 v[12:15], v45, s[8:11], s26 offen sc1\n\t"
        "s_waitcnt vmcnt(0)\n\t"
        "v_max3_u32 v45, v12, v13, v14\n\t"
        "v_max_u32_e32 v45, v45, v15\n\t"
        "v_cmp_eq_u32_e32 vcc, -1, v45\n\t"
        "s_nop 0\n\t"
        "s_cbranch_vccz LA_v2ok%=\n\t"
        "s_add_u32 s42, s42, 1\n\t"
        "s_cmp_lt_u32 s42, 0x100000\n\t"
        "s_cbranch_scc1 LA_v2re%=\n\t"
        "s_movk_i32 s34, 6\n\t"
        "s_branch LA_die%=\n\t"
        "LA_rdfail%=:\n\t"
        "s_or_b32 s47, s47, 1\n\t"
        "buffer_load_dwordx4 v[16:19], v112, s[8:11], s26 offen offset:0 sc1\n\t"
        "buffer_load_dwordx4 v[20:23], v112, s[8:11], s26 offen offset:1024 sc1\n\t"
        "buffer_load_dwordx4 v[24:27], v112, s[8:11], s26 offen offset:2048 sc1\n\t"
        "buffer_load_dwordx4 v[28:31], v112, s[8:11], s26 offen offset:3072 sc1\n\t"
        "s_nop 7\n\t"
        "v_mov_b64_e32 v[0:1], 0\n\t"
        "v_mov_b64_e32 v[2:3], 0\n\t"
        "v_mov_b64_e32 v[4:5], 0\n\t"
        "v_mov_b64_e32 v[6:7], 0\n\t"
        "v_mov_b64_e32 v[8:9], 0\n\t"
        "v_mov_b64_e32 v[10:11], 0\n\t"
        "v_mov_b64_e32 v[12:13], 0\n\t"
        "v_mov_b64_e32 v[14:15], 0\n\t"
        "s_branch LA_rdre%=w\n\t"
        "LA_rdre%=:\n\t"
        "buffer_load_dwordx4 v[16:19], v112, s[8:11], s26 offen offset:0 sc1\n\t"
        "buffer_load_dwordx4 v[20:23], v112, s[8:11], s26 offen offset:1024 sc1\n\t"
        "buffer_load_dwordx4 v[24:27], v112, s[8:11], s26 offen offset:2048 sc1\n\t"
        "buffer_load_dwordx4 v[28:31], v112, s[8:11], s26 offen offset:3072 sc1\n\t"
        "LA_rdre%=w:\n\t"
        "s_waitcnt vmcnt(0) lgkmcnt(0)\n\t"
        "v_max3_u32 v111, v16, v17, v18\n\t"
        "v_max3_u32 v111, v111, v19, v20\n\t"
        "v_max3_u32 v111, v111, v21, v22\n\t"
        "v_max3_u32 v111, v111, v23, v24\n\t"
        "v_max3_u32 v111, v111, v25, v26\n\t"
        "v_max3_u32 v111, v111, v27, v28\n\t"
        "v_max3_u32 v111, v111, v29, v30\n\t"
        "v_max_u32_e32 v111, v111, v31\n\t"
        "v_cmp_eq_u32_e32 vcc, -1, v111\n\t"
        "s_nop 0\n\t"
        "s_cbranch_vccz LA_go%=\n\t"
        "s_add_u32 s42, s42, 1\n\t"
        "s_cmp_lt_u32 s42, 0x100000\n\t"
        "s_cbranch_scc1 LA_rdre%=\n\t"
        "s_movk_i32 s34, 1\n\t"
        "s_branch LA_die%=\n\t"
        "LA_f2%=:\n\t"
        "s_or_b32 s47, s47, 1\n\t"
        "buffer_load_dwordx4 v[24:27], v112, s[8:11], s26 offen offset:2048 sc1\n\t"
        "s_nop 7\n\t"
        "s_branch LA_r2%=w\n\t"
        "LA_r2%=:\n\t"
        "buffer_load_dwordx4 v[24:27], v112, s[8:11], s26 offen offset:2048 sc1\n\t"
        "LA_r2%=w:\n\t"
        "s_waitcnt vmcnt(0) lgkmcnt(0)\n\t"
        "v_max3_u32 v111, v24, v25, v26\n\t"
        "v_max_u32_e32 v111, v111, v27\n\t"
        "v_cmp_eq_u32_e32 vcc, -1, v111\n\t"
        "s_nop 0\n\t"
        "s_cbranch_vccz LA_go%=\n\t"
        "s_add_u32 s42, s42, 1\n\t"
        "s_cmp_lt_u32 s42, 0x100000\n\t"
        "s_cbranch_scc1 LA_r2%=\n\t"
        "s_movk_i32 s34, 1\n\t"
        "s_branch LA_die%=\n\t"
        "LA_f3%=:\n\t"
        "s_or_b32 s47, s47, 1\n\t"
        "buffer_load_dwordx4 v[28:31], v112, s[8:11], s26 offen offset:3072 sc1\n\t"
        "s_nop 7\n\t"
        "s_branch LA_r3%=w\n\t"
        "LA_r3%=:\n\t"
        "buffer_load_dwordx4 v[28:31], v112, s[8:11], s26 offen offset:3072 sc1\n\t"
        "LA_r3%=w:\n\t"
        "s_waitcnt vmcnt(0) lgkmcnt(0)\n\t"
        "v_max3_u32 v111, v28, v29, v30\n\t"
        "v_max_u32_e32 v111, v111, v31\n\t"
        "v_cmp_eq_u32_e32 vcc, -1, v111\n\t"
        "s_nop 0\n\t"
        "s_cbranch_vccz LA_go%=\n\t"
        "s_add_u32 s42, s42, 1\n\t"
        "s_cmp_lt_u32 s42, 0x100000\n\t"
        "s_cbranch_scc1 LA_r3%=\n\t"
        "s_movk_i32 s34, 1\n\t"
        "s_branch LA_die%=\n\t"
        "LA_vmfail%=:\n\t"
        "s_or_b32 s47, s47, 2\n\t"
        "s_mov_b32 s42, 0\n\t"
        "LA_vmre%=:\n\t"
        "buffer_load_dwordx4 v[104:107], v120, s[8:11], s26 offen sc1\n\t"
        "v_mfma_f32_16x16x32_bf16 v[4:7], v[48:51], v[12:15], 0\n\t"
        "v_mfma_f32_16x16x32_bf16 v[8:11], v[52:55], v[12:15], 0\n\t"
        "s_nop 7\n\t"
        "s_waitcnt vmcnt(0)\n\t"
        "v_mfma_f32_16x16x16_bf16 v[4:7], v[104:105], v[56:57], v[4:7]\n\t"
        "v_mfma_f32_16x16x16_bf16 v[8:11], v[106:107], v[56:57], v[8:11]\n\t"
        "v_max3_u32 v111, v104, v105, v106\n\t"
        "v_max_u32_e32 v111, v111, v107\n\t"
        "v_cmp_eq_u32_e32 vcc, -1, v111\n\t"
        "s_nop 7\n\t"
        "s_cbranch_vccz LA_vmok%=\n\t"
        "s_add_u32 s42, s42, 1\n\t"
        "s_cmp_lt_u32 s42, 0x100000\n\t"
        "s_cbranch_scc1 LA_vmre%=\n\t"
        "s_movk_i32 s34, 2\n\t"
        "s_branch LA_die%=\n\t"
        "LA_die%=:\n\t"
        "v_mov_b32_e32 v30, s34\n\t"
        "s_mov_b32 s35, 33024\n\t"
        "s_mov_b64 exec, 1\n\t"
        "buffer_store_dword v30, off, s[12:15], s35 sc1\n\t"
        "s_endpgm\n\t"
        ".Lk_end_A:\n\t"
        :: REC_S_IN, "{v96}"(g_w), "{v97}"(wb0), "{v98}"(wb1), "{v99}"(rb0), "{v100}"(rb0 ^ 32u), "{v101}"(rb0 ^ 64u), "{v102}"(rb0 ^ 96u),
           "{v112}"(a_mem), "{v113}"(l_ms + w * 4096), "{v114}"(l_ms + ((w + 1) & 3) * 4096), "{v115}"(l_ms + ((w + 2) & 3) * 4096), "{v116}"(l_ms + ((w + 3) & 3) * 4096),
           "{v117}"(lb + w * 22528 + l * 16), "{v118}"(l_iw), "{v119}"(l_iq), "{v120}"(a_vm), "{v121}"(a_o), "{v122}"(h * 8192 + l * 16), "{v123}"(a_ox), "{v124}"(lb + L_KV + l * 16), "{s55}"(lb + L_KV + w * 2048),
           "{v125}"((24 + cp) * 16384 + l * ST_P), "{v126}"(16 * (w & 1) + n - 4 * g), "{v103}"(pp ? 0xEEEEEEEEu : 0x44444444u),
           "{v127}"((l == 0 && w == 0) ? F_PROG + h * 128 : OOB)
        : "memory", "vcc", "scc", "v0", "v255", "a0", "a255", "s101");
    __builtin_unreachable();
}
__device__ __forceinline__ void rec_b(const Params& p, unsigned lb, const int wv, const int id, const int rg, const int cp, const int rot, const int bq) {
    const int w = wv, l = otid() & 63, n = l & 15, g = l >> 4, cs = id, rb = 32 + id * 2 + rg;
    const v4u sh = mk_srd(p.ws + hx_base(p.ws), HX_BYTES - 8192), sw = mk_srd(p.ws, W_TOTAL), s1 = mk_srd(p.Wv, 1024 * 4096 * 2), s2 = mk_srd(p.Wo, 1024 * 4096 * 2);
    const int r8 = l >> 3, pc = l & 7, pp = n >> 3, bb = (n >> 2) & 1, f = (g & 1) | (pp << 1);
    const unsigned stg = lb + LB_STG + w * LB_STGP;
    const unsigned g_v = r8 * 8192 + cs * 256 + rg * 128 + pc * 16, g_o = (l >> 2) * 2048 + cs * 64 + (l & 3) * 16;
    const unsigned wb0 = stg + r8 * 128 + (((pc >> 1) ^ (r8 >> 2)) << 5) + (pc & 1) * 16, wb1 = wb0 ^ 64u;
    const unsigned rb0 = stg + (16 * (g >> 1) + 8 * pp + 4 * (g & 1) + bb) * 128 + (f << 5) + (n & 3) * 8;
    const unsigned o_r = stg + pp * (2048 + 128) + (g & 1) * (1024 + 32) + (bb + 4 * (g >> 1)) * 64 + (n & 3) * 8;
    const unsigned a_mem = cp * 32768 + (w >> 1) * 16384 + (w & 1) * 4096 + (g >> 1) * 512 + ((n & 7) >> 2) * 256 + ((g & 1) * 2 + pp) * 64 + (n & 3) * 16;
    const unsigned a_vmw = w < 2 ? (16 + (cs >> 2)) * 16384 + (cs & 3) * 2048 + (2 * rg + w) * 512 + n * 32 + (g & 1) * 16 + (g >> 1) * 8 : OOB;
    const unsigned a_or = (8 + rg * 4 + w) * 16384 + (n >> 2) * 256 + g * 64 + (n & 3) * 16;
    const unsigned a_mw = (RPL != 0 && RPL != 4 && w == 3) ? OOB : w * 32768 + (cs >> 4) * 16384 + ((cs >> 1) & 7) * 1024 + (cs & 1) * 512 + rg * 256 + l * 4;
    const unsigned a_sw = (w == 0 && l < 4) ? (24 + l) * 16384 + (id * 2 + rg) * ST_P : OOB;
    const bf16_t* m0p = p.mem0 + (4 * rg + ((l >> 2) & 3)) * 1024 + cs * 32 + g * 8 + (l & 3) * 2;
    asm volatile(
        "global_load_dword v109, v[108:109], off\n\t"
        "s_add_u32 s35, s68, 0\n\t"
        "s_and_b32 s35, s35, 7\n\t"
        "s_lshl_b32 s69, s35, 10\n\t"
        "s_add_u32 s35, s68, 2\n\t"
        "s_and_b32 s35, s35, 7\n\t"
        "s_lshl_b32 s70, s35, 10\n\t"
        "s_add_u32 s35, s68, 4\n\t"
        "s_and_b32 s35, s35, 7\n\t"
        "s_lshl_b32 s71, s35, 10\n\t"
        "s_add_u32 s35, s68, 6\n\t"
        "s_and_b32 s35, s35, 7\n\t"
        "s_lshl_b32 s72, s35, 10\n\t"
        "s_lshl_b32 s34, s43, 21\n\t"
        "s_add_u32 s34, s34, 0\n\t"
        "buffer_load_dwordx4 v[128:131], v96, s[16:19], s34 offen nt\n\t"
        "s_add_u32 s34, s34, 0x10000\n\t"
        "buffer_load_dwordx4 v[132:135], v96, s[16:19], s34 offen nt\n\t"
        "s_add_u32 s34, s34, 0x10000\n\t"
        "buffer_load_dwordx4 v[136:139], v96, s[16:19], s34 offen nt\n\t"
        "s_add_u32 s34, s34, 0x10000\n\t"
        "buffer_load_dwordx4 v[140:143], v96, s[16:19], s34 offen nt\n\t"
        "s_add_u32 s34, s34, 0x10000\n\t"
        "buffer_load_dwordx4 v[144:147], v96, s[16:19], s34 offen nt\n\t"
        "s_add_u32 s34, s34, 0x10000\n\t"
        "buffer_load_dwordx4 v[148:151], v96, s[16:19], s34 offen nt\n\t"
        "s_add_u32 s34, s34, 0x10000\n\t"
        "buffer_load_dwordx4 v[152:155], v96, s[16:19], s34 offen nt\n\t"
        "s_add_u32 s34, s34, 0x10000\n\t"
        "buffer_load_dwordx4 v[156:159], v96, s[16:19], s34 offen nt\n\t"
        "s_lshl_b32 s34, s43, 21\n\t"
        "s_add_u32 s34, s34, 524288\n\t"
        "buffer_load_dwordx4 v[160:163], v96, s[16:19], s34 offen nt\n\t"
        "s_add_u32 s34, s34, 0x10000\n\t"
        "buffer_load_dwordx4 v[164:167], v96, s[16:19], s34 offen nt\n\t"
        "s_add_u32 s34, s34, 0x10000\n\t"
        "buffer_load_dwordx4 v[168:171], v96, s[16:19], s34 offen nt\n\t"
        "s_add_u32 s34, s34, 0x10000\n\t"
        "buffer_load_dwordx4 v[172:175], v96, s[16:19], s34 offen nt\n\t"
        "s_add_u32 s34, s34, 0x10000\n\t"
        "buffer_load_dwordx4 v[176:179], v96, s[16:19], s34 offen nt\n\t"
        "s_add_u32 s34, s34, 0x10000\n\t"
        "buffer_load_dwordx4 v[180:183], v96, s[16:19], s34 offen nt\n\t"
        "s_add_u32 s34, s34, 0x10000\n\t"
        "buffer_load_dwordx4 v[184:187], v96, s[16:19], s34 offen nt\n\t"
        "s_add_u32 s34, s34, 0x10000\n\t"
        "buffer_load_dwordx4 v[188:191], v96, s[16:19], s34 offen nt\n\t"
        "s_lshl_b32 s34, s43, 21\n\t"
        "s_add_u32 s34, s34, 1048576\n\t"
        "buffer_load_dwordx4 v[192:195], v96, s[16:19], s34 offen nt\n\t"
        "s_add_u32 s34, s34, 0x10000\n\t"
        "buffer_load_dwordx4 v[196:199], v96, s[16:19], s34 offen nt\n\t"
        "s_add_u32 s34, s34, 0x10000\n\t"
        "buffer_load_dwordx4 v[200:203], v96, s[16:19], s34 offen nt\n\t"
        "s_add_u32 s34, s34, 0x10000\n\t"
        "buffer_load_dwordx4 v[204:207], v96, s[16:19], s34 offen nt\n\t"
        "s_add_u32 s34, s34, 0x10000\n\t"
        "buffer_load_dwordx4 v[208:211], v96, s[16:19], s34 offen nt\n\t"
        "s_add_u32 s34, s34, 0x10000\n\t"
        "buffer_load_dwordx4 v[212:215], v96, s[16:19], s34 offen nt\n\t"
        "s_add_u32 s34, s34, 0x10000\n\t"
        "buffer_load_dwordx4 v[216:219], v96, s[16:19], s34 offen nt\n\t"
        "s_add_u32 s34, s34, 0x10000\n\t"
        "buffer_load_dwordx4 v[220:223], v96, s[16:19], s34 offen nt\n\t"
        "s_waitcnt vmcnt(16)\n\t"
        "ds_write_b128 v98, v[128:131] offset:0\n\t"
        "ds_write_b128 v99, v[132:135] offset:1024\n\t"
        "ds_write_b128 v98, v[136:139] offset:2048\n\t"
        "ds_write_b128 v99, v[140:143] offset:3072\n\t"
        "ds_write_b128 v98, v[144:147] offset:4096\n\t"
        "ds_write_b128 v99, v[148:151] offset:5120\n\t"
        "ds_write_b128 v98, v[152:155] offset:6144\n\t"
        "ds_write_b128 v99, v[156:159] offset:7168\n\t"
        "ds_read_b64_tr_b16 v[128:129], v100 offset:0\n\t"
        "ds_read_b64_tr_b16 v[130:131], v100 offset:256\n\t"
        "ds_read_b64_tr_b16 v[132:133], v100 offset:4096\n\t"
        "ds_read_b64_tr_b16 v[134:135], v100 offset:4352\n\t"
        "s_waitcnt lgkmcnt(8)\n\t"
        "ds_read_b64_tr_b16 v[136:137], v101 offset:0\n\t"
        "ds_read_b64_tr_b16 v[138:139], v101 offset:256\n\t"
        "ds_read_b64_tr_b16 v[140:141], v101 offset:4096\n\t"
        "ds_read_b64_tr_b16 v[142:143], v101 offset:4352\n\t"
        "s_waitcnt lgkmcnt(8)\n\t"
        "ds_read_b64_tr_b16 v[144:145], v102 offset:0\n\t"
        "ds_read_b64_tr_b16 v[146:147], v102 offset:256\n\t"
        "ds_read_b64_tr_b16 v[148:149], v102 offset:4096\n\t"
        "ds_read_b64_tr_b16 v[150:151], v102 offset:4352\n\t"
        "s_waitcnt lgkmcnt(8)\n\t"
        "ds_read_b64_tr_b16 v[152:153], v103 offset:0\n\t"
        "ds_read_b64_tr_b16 v[154:155], v103 offset:256\n\t"
        "ds_read_b64_tr_b16 v[156:157], v103 offset:4096\n\t"
        "ds_read_b64_tr_b16 v[158:159], v103 offset:4352\n\t"
        "s_lshl_b32 s34, s43, 21\n\t"
        "s_add_u32 s34, s34, 1572864\n\t"
        "buffer_load_dwordx4 v[224:227], v96, s[16:19], s34 offen nt\n\t"
        "s_add_u32 s34, s34, 0x10000\n\t"
        "buffer_load_dwordx4 v[228:231], v96, s[16:19], s34 offen nt\n\t"
        "s_add_u32 s34, s34, 0x10000\n\t"
        "buffer_load_dwordx4 v[232:235], v96, s[16:19], s34 offen nt\n\t"
        "s_add_u32 s34, s34, 0x10000\n\t"
        "buffer_load_dwordx4 v[236:239], v96, s[16:19], s34 offen nt\n\t"
        "s_add_u32 s34, s34, 0x10000\n\t"
        "buffer_load_dwordx4 v[240:243], v96, s[16:19], s34 offen nt\n\t"
        "s_add_u32 s34, s34, 0x10000\n\t"
        "buffer_load_dwordx4 v[244:247], v96, s[16:19], s34 offen nt\n\t"
        "s_add_u32 s34, s34, 0x10000\n\t"
        "buffer_load_dwordx4 v[248:251], v96, s[16:19], s34 offen nt\n\t"
        "s_add_u32 s34, s34, 0x10000\n\t"
        "buffer_load_dwordx4 v[252:255], v96, s[16:19], s34 offen nt\n\t"
        "s_waitcnt lgkmcnt(0)\n\t"
        "s_waitcnt vmcnt(16)\n\t"
        "ds_write_b128 v98, v[160:163] offset:0\n\t"
        "ds_write_b128 v99, v[164:167] offset:1024\n\t"
        "ds_write_b128 v98, v[168:171] offset:2048\n\t"
        "ds_write_b128 v99, v[172:175] offset:3072\n\t"
        "ds_write_b128 v98, v[176:179] offset:4096\n\t"
        "ds_write_b128 v99, v[180:183] offset:5120\n\t"
        "ds_write_b128 v98, v[184:187] offset:6144\n\t"
        "ds_write_b128 v99, v[188:191] offset:7168\n\t"
        "ds_read_b64_tr_b16 v[160:161], v100 offset:0\n\t"
        "ds_read_b64_tr_b16 v[162:163], v100 offset:256\n\t"
        "ds_read_b64_tr_b16 v[164:165], v100 offset:4096\n\t"
        "ds_read_b64_tr_b16 v[166:167], v100 offset:4352\n\t"
        "s_waitcnt lgkmcnt(8)\n\t"
        "ds_read_b64_tr_b16 v[168:169], v101 offset:0\n\t"
        "ds_read_b64_tr_b16 v[170:171], v101 offset:256\n\t"
        "ds_read_b64_tr_b16 v[172:173], v101 offset:4096\n\t"
        "ds_read_b64_tr_b16 v[174:175], v101 offset:4352\n\t"
        "s_waitcnt lgkmcnt(8)\n\t"
        "ds_read_b64_tr_b16 v[176:177], v102 offset:0\n\t"
        "ds_read_b64_tr_b16 v[178:179], v102 offset:256\n\t"
        "ds_read_b64_tr_b16 v[180:181], v102 offset:4096\n\t"
        "ds_read_b64_tr_b16 v[182:183], v102 offset:4352\n\t"
        "s_waitcnt lgkmcnt(8)\n\t"
        "ds_read_b64_tr_b16 v[184:185], v103 offset:0\n\t"
        "ds_read_b64_tr_b16 v[186:187], v103 offset:256\n\t"
        "ds_read_b64_tr_b16 v[188:189], v103 offset:4096\n\t"
        "ds_read_b64_tr_b16 v[190:191], v103 offset:4352\n\t"
        "s_add_u32 s35, s68, 0\n\t"
        "s_and_b32 s35, s35, 7\n\t"
        "s_lshl_b32 s35, s35, 18\n\t"
        "s_lshl_b32 s34, s43, 21\n\t"
        "s_add_u32 s34, s34, s35\n\t"
        "buffer_load_dwordx4 a[0:3], v97, s[20:23], s34 offen\n\t"
        "s_add_u32 s34, s34, 0x8000\n\t"
        "buffer_load_dwordx4 a[4:7], v97, s[20:23], s34 offen\n\t"
        "s_add_u32 s34, s34, 0x8000\n\t"
        "buffer_load_dwordx4 a[8:11], v97, s[20:23], s34 offen\n\t"
        "s_add_u32 s34, s34, 0x8000\n\t"
        "buffer_load_dwordx4 a[12:15], v97, s[20:23], s34 offen\n\t"
        "s_add_u32 s34, s34, 0x8000\n\t"
        "buffer_load_dwordx4 a[16:19], v97, s[20:23], s34 offen\n\t"
        "s_add_u32 s34, s34, 0x8000\n\t"
        "buffer_load_dwordx4 a[20:23], v97, s[20:23], s34 offen\n\t"
        "s_add_u32 s34, s34, 0x8000\n\t"
        "buffer_load_dwordx4 a[24:27], v97, s[20:23], s34 offen\n\t"
        "s_add_u32 s34, s34, 0x8000\n\t"
        "buffer_load_dwordx4 a[28:31], v97, s[20:23], s34 offen\n\t"
        "s_waitcnt lgkmcnt(0)\n\t"
        "s_waitcnt vmcnt(16)\n\t"
        "ds_write_b128 v98, v[192:195] offset:0\n\t"
        "ds_write_b128 v99, v[196:199] offset:1024\n\t"
        "ds_write_b128 v98, v[200:203] offset:2048\n\t"
        "ds_write_b128 v99, v[204:207] offset:3072\n\t"
        "ds_write_b128 v98, v[208:211] offset:4096\n\t"
        "ds_write_b128 v99, v[212:215] offset:5120\n\t"
        "ds_write_b128 v98, v[216:219] offset:6144\n\t"
        "ds_write_b128 v99, v[220:223] offset:7168\n\t"
        "ds_read_b64_tr_b16 v[192:193], v100 offset:0\n\t"
        "ds_read_b64_tr_b16 v[194:195], v100 offset:256\n\t"
        "ds_read_b64_tr_b16 v[196:197], v100 offset:4096\n\t"
        "ds_read_b64_tr_b16 v[198:199], v100 offset:4352\n\t"
        "s_waitcnt lgkmcnt(8)\n\t"
        "ds_read_b64_tr_b16 v[200:201], v101 offset:0\n\t"
        "ds_read_b64_tr_b16 v[202:203], v101 offset:256\n\t"
        "ds_read_b64_tr_b16 v[204:205], v101 offset:4096\n\t"
        "ds_read_b64_tr_b16 v[206:207], v101 offset:4352\n\t"
        "s_waitcnt lgkmcnt(8)\n\t"
        "ds_read_b64_tr_b16 v[208:209], v102 offset:0\n\t"
        "ds_read_b64_tr_b16 v[210:211], v102 offset:256\n\t"
        "ds_read_b64_tr_b16 v[212:213], v102 offset:4096\n\t"
        "ds_read_b64_tr_b16 v[214:215], v102 offset:4352\n\t"
        "s_waitcnt lgkmcnt(8)\n\t"
        "ds_read_b64_tr_b16 v[216:217], v103 offset:0\n\t"
        "ds_read_b64_tr_b16 v[218:219], v103 offset:256\n\t"
        "ds_read_b64_tr_b16 v[220:221], v103 offset:4096\n\t"
        "ds_read_b64_tr_b16 v[222:223], v103 offset:4352\n\t"
        "s_add_u32 s35, s68, 1\n\t"
        "s_and_b32 s35, s35, 7\n\t"
        "s_lshl_b32 s35, s35, 18\n\t"
        "s_lshl_b32 s34, s43, 21\n\t"
        "s_add_u32 s34, s34, s35\n\t"
        "buffer_load_dwordx4 a[32:35], v97, s[20:23], s34 offen\n\t"
        "s_add_u32 s34, s34, 0x8000\n\t"
        "buffer_load_dwordx4 a[36:39], v97, s[20:23], s34 offen\n\t"
        "s_add_u32 s34, s34, 0x8000\n\t"
        "buffer_load_dwordx4 a[40:43], v97, s[20:23], s34 offen\n\t"
        "s_add_u32 s34, s34, 0x8000\n\t"
        "buffer_load_dwordx4 a[44:47], v97, s[20:23], s34 offen\n\t"
        "s_add_u32 s34, s34, 0x8000\n\t"
        "buffer_load_dwordx4 a[48:51], v97, s[20:23], s34 offen\n\t"
        "s_add_u32 s34, s34, 0x8000\n\t"
        "buffer_load_dwordx4 a[52:55], v97, s[20:23], s34 offen\n\t"
        "s_add_u32 s34, s34, 0x8000\n\t"
        "buffer_load_dwordx4 a[56:59], v97, s[20:23], s34 offen\n\t"
        "s_add_u32 s34, s34, 0x8000\n\t"
        "buffer_load_dwordx4 a[60:63], v97, s[20:23], s34 offen\n\t"
        "s_waitcnt lgkmcnt(0)\n\t"
        "s_waitcnt vmcnt(16)\n\t"
        "ds_write_b128 v98, v[224:227] offset:0\n\t"
        "ds_write_b128 v99, v[228:231] offset:1024\n\t"
        "ds_write_b128 v98, v[232:235] offset:2048\n\t"
        "ds_write_b128 v99, v[236:239] offset:3072\n\t"
        "ds_write_b128 v98, v[240:243] offset:4096\n\t"
        "ds_write_b128 v99, v[244:247] offset:5120\n\t"
        "ds_write_b128 v98, v[248:251] offset:6144\n\t"
        "ds_write_b128 v99, v[252:255] offset:7168\n\t"
        "ds_read_b64_tr_b16 v[224:225], v100 offset:0\n\t"
        "ds_read_b64_tr_b16 v[226:227], v100 offset:256\n\t"
        "ds_read_b64_tr_b16 v[228:229], v100 offset:4096\n\t"
        "ds_read_b64_tr_b16 v[230:231], v100 offset:4352\n\t"
        "s_waitcnt lgkmcnt(8)\n\t"
        "ds_read_b64_tr_b16 v[232:233], v101 offset:0\n\t"
        "ds_read_b64_tr_b16 v[234:235], v101 offset:256\n\t"
        "ds_read_b64_tr_b16 v[236:237], v101 offset:4096\n\t"
        "ds_read_b64_tr_b16 v[238:239], v101 offset:4352\n\t"
        "s_waitcnt lgkmcnt(8)\n\t"
        "ds_read_b64_tr_b16 v[240:241], v102 offset:0\n\t"
        "ds_read_b64_tr_b16 v[242:243], v102 offset:256\n\t"
        "ds_read_b64_tr_b16 v[244:245], v102 offset:4096\n\t"
        "ds_read_b64_tr_b16 v[246:247], v102 offset:4352\n\t"
        "s_waitcnt lgkmcnt(8)\n\t"
        "ds_read_b64_tr_b16 v[248:249], v103 offset:0\n\t"
        "ds_read_b64_tr_b16 v[250:251], v103 offset:256\n\t"
        "ds_read_b64_tr_b16 v[252:253], v103 offset:4096\n\t"
        "ds_read_b64_tr_b16 v[254:255], v103 offset:4352\n\t"
        "s_add_u32 s35, s68, 2\n\t"
        "s_and_b32 s35, s35, 7\n\t"
        "s_lshl_b32 s35, s35, 18\n\t"
        "s_lshl_b32 s34, s43, 21\n\t"
        "s_add_u32 s34, s34, s35\n\t"
        "buffer_load_dwordx4 a[64:67], v97, s[20:23], s34 offen\n\t"
        "s_add_u32 s34, s34, 0x8000\n\t"
        "buffer_load_dwordx4 a[68:71], v97, s[20:23], s34 offen\n\t"
        "s_add_u32 s34, s34, 0x8000\n\t"
        "buffer_load_dwordx4 a[72:75], v97, s[20:23], s34 offen\n\t"
        "s_add_u32 s34, s34, 0x8000\n\t"
        "buffer_load_dwordx4 a[76:79], v97, s[20:23], s34 offen\n\t"
        "s_add_u32 s34, s34, 0x8000\n\t"
        "buffer_load_dwordx4 a[80:83], v97, s[20:23], s34 offen\n\t"
        "s_add_u32 s34, s34, 0x8000\n\t"
        "buffer_load_dwordx4 a[84:87], v97, s[20:23], s34 offen\n\t"
        "s_add_u32 s34, s34, 0x8000\n\t"
        "buffer_load_dwordx4 a[88:91], v97, s[20:23], s34 offen\n\t"
        "s_add_u32 s34, s34, 0x8000\n\t"
        "buffer_load_dwordx4 a[92:95], v97, s[20:23], s34 offen\n\t"
        "s_waitcnt lgkmcnt(0)\n\t"
        "s_waitcnt vmcnt(16)\n\t"
        "ds_write_b128 v122, a[0:3] offset:0\n\t"
        "ds_write_b128 v122, a[4:7] offset:1056\n\t"
        "ds_write_b128 v122, a[8:11] offset:2176\n\t"
        "ds_write_b128 v122, a[12:15] offset:3232\n\t"
        "ds_write_b128 v122, a[16:19] offset:4352\n\t"
        "ds_write_b128 v122, a[20:23] offset:5408\n\t"
        "ds_write_b128 v122, a[24:27] offset:6528\n\t"
        "ds_write_b128 v122, a[28:31] offset:7584\n\t"
        "ds_read_b64_tr_b16 a[0:1], v123 offset:0\n\t"
        "ds_read_b64_tr_b16 a[2:3], v123 offset:128\n\t"
        "ds_read_b64_tr_b16 a[4:5], v123 offset:512\n\t"
        "ds_read_b64_tr_b16 a[6:7], v123 offset:640\n\t"
        "s_waitcnt lgkmcnt(8)\n\t"
        "ds_read_b64_tr_b16 a[8:9], v123 offset:32\n\t"
        "ds_read_b64_tr_b16 a[10:11], v123 offset:160\n\t"
        "ds_read_b64_tr_b16 a[12:13], v123 offset:544\n\t"
        "ds_read_b64_tr_b16 a[14:15], v123 offset:672\n\t"
        "s_waitcnt lgkmcnt(8)\n\t"
        "ds_read_b64_tr_b16 a[16:17], v123 offset:4352\n\t"
        "ds_read_b64_tr_b16 a[18:19], v123 offset:4480\n\t"
        "ds_read_b64_tr_b16 a[20:21], v123 offset:4864\n\t"
        "ds_read_b64_tr_b16 a[22:23], v123 offset:4992\n\t"
        "s_waitcnt lgkmcnt(8)\n\t"
        "ds_read_b64_tr_b16 a[24:25], v123 offset:4384\n\t"
        "ds_read_b64_tr_b16 a[26:27], v123 offset:4512\n\t"
        "ds_read_b64_tr_b16 a[28:29], v123 offset:4896\n\t"
        "ds_read_b64_tr_b16 a[30:31], v123 offset:5024\n\t"
        "s_waitcnt lgkmcnt(8)\n\t"
        "s_add_u32 s35, s68, 3\n\t"
        "s_and_b32 s35, s35, 7\n\t"
        "s_lshl_b32 s35, s35, 18\n\t"
        "s_lshl_b32 s34, s43, 21\n\t"
        "s_add_u32 s34, s34, s35\n\t"
        "buffer_load_dwordx4 a[96:99], v97, s[20:23], s34 offen\n\t"
        "s_add_u32 s34, s34, 0x8000\n\t"
        "buffer_load_dwordx4 a[100:103], v97, s[20:23], s34 offen\n\t"
        "s_add_u32 s34, s34, 0x8000\n\t"
        "buffer_load_dwordx4 a[104:107], v97, s[20:23], s34 offen\n\t"
        "s_add_u32 s34, s34, 0x8000\n\t"
        "buffer_load_dwordx4 a[108:111], v97, s[20:23], s34 offen\n\t"
        "s_add_u32 s34, s34, 0x8000\n\t"
        "buffer_load_dwordx4 a[112:115], v97, s[20:23], s34 offen\n\t"
        "s_add_u32 s34, s34, 0x8000\n\t"
        "buffer_load_dwordx4 a[116:119], v97, s[20:23], s34 offen\n\t"
        "s_add_u32 s34, s34, 0x8000\n\t"
        "buffer_load_dwordx4 a[120:123], v97, s[20:23], s34 offen\n\t"
        "s_add_u32 s34, s34, 0x8000\n\t"
        "buffer_load_dwordx4 a[124:127], v97, s[20:23], s34 offen\n\t"
        "s_waitcnt lgkmcnt(0)\n\t"
        "s_waitcnt vmcnt(16)\n\t"
        "ds_write_b128 v122, a[32:35] offset:0\n\t"
        "ds_write_b128 v122, a[36:39] offset:1056\n\t"
        "ds_write_b128 v122, a[40:43] offset:2176\n\t"
        "ds_write_b128 v122, a[44:47] offset:3232\n\t"
        "ds_write_b128 v122, a[48:51] offset:4352\n\t"
        "ds_write_b128 v122, a[52:55] offset:5408\n\t"
        "ds_write_b128 v122, a[56:59] offset:6528\n\t"
        "ds_write_b128 v122, a[60:63] offset:7584\n\t"
        "ds_read_b64_tr_b16 a[32:33], v123 offset:0\n\t"
        "ds_read_b64_tr_b16 a[34:35], v123 offset:128\n\t"
        "ds_read_b64_tr_b16 a[36:37], v123 offset:512\n\t"
        "ds_read_b64_tr_b16 a[38:39], v123 offset:640\n\t"
        "s_waitcnt lgkmcnt(8)\n\t"
        "ds_read_b64_tr_b16 a[40:41], v123 offset:32\n\t"
        "ds_read_b64_tr_b16 a[42:43], v123 offset:160\n\t"
        "ds_read_b64_tr_b16 a[44:45], v123 offset:544\n\t"
        "ds_read_b64_tr_b16 a[46:47], v123 offset:672\n\t"
        "s_waitcnt lgkmcnt(8)\n\t"
        "ds_read_b64_tr_b16 a[48:49], v123 offset:4352\n\t"
        "ds_read_b64_tr_b16 a[50:51], v123 offset:4480\n\t"
        "ds_read_b64_tr_b16 a[52:53], v123 offset:4864\n\t"
        "ds_read_b64_tr_b16 a[54:55], v123 offset:4992\n\t"
        "s_waitcnt lgkmcnt(8)\n\t"
        "ds_read_b64_tr_b16 a[56:57], v123 offset:4384\n\t"
        "ds_read_b64_tr_b16 a[58:59], v123 offset:4512\n\t"
        "ds_read_b64_tr_b16 a[60:61], v123 offset:4896\n\t"
        "ds_read_b64_tr_b16 a[62:63], v123 offset:5024\n\t"
        "s_waitcnt lgkmcnt(8)\n\t"
        "s_add_u32 s35, s68, 4\n\t"
        "s_and_b32 s35, s35, 7\n\t"
        "s_lshl_b32 s35, s35, 18\n\t"
        "s_lshl_b32 s34, s43, 21\n\t"
        "s_add_u32 s34, s34, s35\n\t"
        "buffer_load_dwordx4 a[128:131], v97, s[20:23], s34 offen\n\t"
        "s_add_u32 s34, s34, 0x8000\n\t"
        "buffer_load_dwordx4 a[132:135], v97, s[20:23], s34 offen\n\t"
        "s_add_u32 s34, s34, 0x8000\n\t"
        "buffer_load_dwordx4 a[136:139], v97, s[20:23], s34 offen\n\t"
        "s_add_u32 s34, s34, 0x8000\n\t"
        "buffer_load_dwordx4 a[140:143], v97, s[20:23], s34 offen\n\t"
        "s_add_u32 s34, s34, 0x8000\n\t"
        "buffer_load_dwordx4 a[144:147], v97, s[20:23], s34 offen\n\t"
        "s_add_u32 s34, s34, 0x8000\n\t"
        "buffer_load_dwordx4 a[148:151], v97, s[20:23], s34 offen\n\t"
        "s_add_u32 s34, s34, 0x8000\n\t"
        "buffer_load_dwordx4 a[152:155], v97, s[20:23], s34 offen\n\t"
        "s_add_u32 s34, s34, 0x8000\n\t"
        "buffer_load_dwordx4 a[156:159], v97, s[20:23], s34 offen\n\t"
        "s_waitcnt lgkmcnt(0)\n\t"
        "s_waitcnt vmcnt(16)\n\t"
        "ds_write_b128 v122, a[64:67] offset:0\n\t"
        "ds_write_b128 v122, a[68:71] offset:1056\n\t"
        "ds_write_b128 v122, a[72:75] offset:2176\n\t"
        "ds_write_b128 v122, a[76:79] offset:3232\n\t"
        "ds_write_b128 v122, a[80:83] offset:4352\n\t"
        "ds_write_b128 v122, a[84:87] offset:5408\n\t"
        "ds_write_b128 v122, a[88:91] offset:6528\n\t"
        "ds_write_b128 v122, a[92:95] offset:7584\n\t"
        "ds_read_b64_tr_b16 a[64:65], v123 offset:0\n\t"
        "ds_read_b64_tr_b16 a[66:67], v123 offset:128\n\t"
        "ds_read_b64_tr_b16 a[68:69], v123 offset:512\n\t"
        "ds_read_b64_tr_b16 a[70:71], v123 offset:640\n\t"
        "s_waitcnt lgkmcnt(8)\n\t"
        "ds_read_b64_tr_b16 a[72:73], v123 offset:32\n\t"
        "ds_read_b64_tr_b16 a[74:75], v123 offset:160\n\t"
        "ds_read_b64_tr_b16 a[76:77], v123 offset:544\n\t"
        "ds_read_b64_tr_b16 a[78:79], v123 offset:672\n\t"
        "s_waitcnt lgkmcnt(8)\n\t"
        "ds_read_b64_tr_b16 a[80:81], v123 offset:4352\n\t"
        "ds_read_b64_tr_b16 a[82:83], v123 offset:4480\n\t"
        "ds_read_b64_tr_b16 a[84:85], v123 offset:4864\n\t"
        "ds_read_b64_tr_b16 a[86:87], v123 offset:4992\n\t"
        "s_waitcnt lgkmcnt(8)\n\t"
        "ds_read_b64_tr_b16 a[88:89], v123 offset:4384\n\t"
        "ds_read_b64_tr_b16 a[90:91], v123 offset:4512\n\t"
        "ds_read_b64_tr_b16 a[92:93], v123 offset:4896\n\t"
        "ds_read_b64_tr_b16 a[94:95], v123 offset:5024\n\t"
        "s_waitcnt lgkmcnt(8)\n\t"
        "s_add_u32 s35, s68, 5\n\t"
        "s_and_b32 s35, s35, 7\n\t"
        "s_lshl_b32 s35, s35, 18\n\t"
        "s_lshl_b32 s34, s43, 21\n\t"
        "s_add_u32 s34, s34, s35\n\t"
        "buffer_load_dwordx4 a[160:163], v97, s[20:23], s34 offen\n\t"
        "s_add_u32 s34, s34, 0x8000\n\t"
        "buffer_load_dwordx4 a[164:167], v97, s[20:23], s34 offen\n\t"
        "s_add_u32 s34, s34, 0x8000\n\t"
        "buffer_load_dwordx4 a[168:171], v97, s[20:23], s34 offen\n\t"
        "s_add_u32 s34, s34, 0x8000\n\t"
        "buffer_load_dwordx4 a[172:175], v97, s[20:23], s34 offen\n\t"
        "s_add_u32 s34, s34, 0x8000\n\t"
        "buffer_load_dwordx4 a[176:179], v97, s[20:23], s34 offen\n\t"
        "s_add_u32 s34, s34, 0x8000\n\t"
        "buffer_load_dwordx4 a[180:183], v97, s[20:23], s34 offen\n\t"
        "s_add_u32 s34, s34, 0x8000\n\t"
        "buffer_load_dwordx4 a[184:187], v97, s[20:23], s34 offen\n\t"
        "s_add_u32 s34, s34, 0x8000\n\t"
        "buffer_load_dwordx4 a[188:191], v97, s[20:23], s34 offen\n\t"
        "s_waitcnt lgkmcnt(0)\n\t"
        "s_waitcnt vmcnt(16)\n\t"
        "ds_write_b128 v122, a[96:99] offset:0\n\t"
        "ds_write_b128 v122, a[100:103] offset:1056\n\t"
        "ds_write_b128 v122, a[104:107] offset:2176\n\t"
        "ds_write_b128 v122, a[108:111] offset:3232\n\t"
        "ds_write_b128 v122, a[112:115] offset:4352\n\t"
        "ds_write_b128 v122, a[116:119] offset:5408\n\t"
        "ds_write_b128 v122, a[120:123] offset:6528\n\t"
        "ds_write_b128 v122, a[124:127] offset:7584\n\t"
        "ds_read_b64_tr_b16 a[96:97], v123 offset:0\n\t"
        "ds_read_b64_tr_b16 a[98:99], v123 offset:128\n\t"
        "ds_read_b64_tr_b16 a[100:101], v123 offset:512\n\t"
        "ds_read_b64_tr_b16 a[102:103], v123 offset:640\n\t"
        "s_waitcnt lgkmcnt(8)\n\t"
        "ds_read_b64_tr_b16 a[104:105], v123 offset:32\n\t"
        "ds_read_b64_tr_b16 a[106:107], v123 offset:160\n\t"
        "ds_read_b64_tr_b16 a[108:109], v123 offset:544\n\t"
        "ds_read_b64_tr_b16 a[110:111], v123 offset:672\n\t"
        "s_waitcnt lgkmcnt(8)\n\t"
        "ds_read_b64_tr_b16 a[112:113], v123 offset:4352\n\t"
        "ds_read_b64_tr_b16 a[114:115], v123 offset:4480\n\t"
        "ds_read_b64_tr_b16 a[116:117], v123 offset:4864\n\t"
        "ds_read_b64_tr_b16 a[118:119], v123 offset:4992\n\t"
        "s_waitcnt lgkmcnt(8)\n\t"
        "ds_read_b64_tr_b16 a[120:121], v123 offset:4384\n\t"
        "ds_read_b64_tr_b16 a[122:123], v123 offset:4512\n\t"
        "ds_read_b64_tr_b16 a[124:125], v123 offset:4896\n\t"
        "ds_read_b64_tr_b16 a[126:127], v123 offset:5024\n\t"
        "s_waitcnt lgkmcnt(8)\n\t"
        "s_add_u32 s35, s68, 6\n\t"
        "s_and_b32 s35, s35, 7\n\t"
        "s_lshl_b32 s35, s35, 18\n\t"
        "s_lshl_b32 s34, s43, 21\n\t"
        "s_add_u32 s34, s34, s35\n\t"
        "buffer_load_dwordx4 a[192:195], v97, s[20:23], s34 offen\n\t"
        "s_add_u32 s34, s34, 0x8000\n\t"
        "buffer_load_dwordx4 a[196:199], v97, s[20:23], s34 offen\n\t"
        "s_add_u32 s34, s34, 0x8000\n\t"
        "buffer_load_dwordx4 a[200:203], v97, s[20:23], s34 offen\n\t"
        "s_add_u32 s34, s34, 0x8000\n\t"
        "buffer_load_dwordx4 a[204:207], v97, s[20:23], s34 offen\n\t"
        "s_add_u32 s34, s34, 0x8000\n\t"
        "buffer_load_dwordx4 a[208:211], v97, s[20:23], s34 offen\n\t"
        "s_add_u32 s34, s34, 0x8000\n\t"
        "buffer_load_dwordx4 a[212:215], v97, s[20:23], s34 offen\n\t"
        "s_add_u32 s34, s34, 0x8000\n\t"
        "buffer_load_dwordx4 a[216:219], v97, s[20:23], s34 offen\n\t"
        "s_add_u32 s34, s34, 0x8000\n\t"
        "buffer_load_dwordx4 a[220:223], v97, s[20:23], s34 offen\n\t"
        "s_waitcnt lgkmcnt(0)\n\t"
        "s_waitcnt vmcnt(16)\n\t"
        "ds_write_b128 v122, a[128:131] offset:0\n\t"
        "ds_write_b128 v122, a[132:135] offset:1056\n\t"
        "ds_write_b128 v122, a[136:139] offset:2176\n\t"
        "ds_write_b128 v122, a[140:143] offset:3232\n\t"
        "ds_write_b128 v122, a[144:147] offset:4352\n\t"
        "ds_write_b128 v122, a[148:151] offset:5408\n\t"
        "ds_write_b128 v122, a[152:155] offset:6528\n\t"
        "ds_write_b128 v122, a[156:159] offset:7584\n\t"
        "ds_read_b64_tr_b16 a[128:129], v123 offset:0\n\t"
        "ds_read_b64_tr_b16 a[130:131], v123 offset:128\n\t"
        "ds_read_b64_tr_b16 a[132:133], v123 offset:512\n\t"
        "ds_read_b64_tr_b16 a[134:135], v123 offset:640\n\t"
        "s_waitcnt lgkmcnt(8)\n\t"
        "ds_read_b64_tr_b16 a[136:137], v123 offset:32\n\t"
        "ds_read_b64_tr_b16 a[138:139], v123 offset:160\n\t"
        "ds_read_b64_tr_b16 a[140:141], v123 offset:544\n\t"
        "ds_read_b64_tr_b16 a[142:143], v123 offset:672\n\t"
        "s_waitcnt lgkmcnt(8)\n\t"
        "ds_read_b64_tr_b16 a[144:145], v123 offset:4352\n\t"
        "ds_read_b64_tr_b16 a[146:147], v123 offset:4480\n\t"
        "ds_read_b64_tr_b16 a[148:149], v123 offset:4864\n\t"
        "ds_read_b64_tr_b16 a[150:151], v123 offset:4992\n\t"
        "s_waitcnt lgkmcnt(8)\n\t"
        "ds_read_b64_tr_b16 a[152:153], v123 offset:4384\n\t"
        "ds_read_b64_tr_b16 a[154:155], v123 offset:4512\n\t"
        "ds_read_b64_tr_b16 a[156:157], v123 offset:4896\n\t"
        "ds_read_b64_tr_b16 a[158:159], v123 offset:5024\n\t"
        "s_waitcnt lgkmcnt(8)\n\t"
        "s_add_u32 s35, s68, 7\n\t"
        "s_and_b32 s35, s35, 7\n\t"
        "s_lshl_b32 s35, s35, 18\n\t"
        "s_lshl_b32 s34, s43, 21\n\t"
        "s_add_u32 s34, s34, s35\n\t"
        "buffer_load_dwordx4 a[224:227], v97, s[20:23], s34 offen\n\t"
        "s_add_u32 s34, s34, 0x8000\n\t"
        "buffer_load_dwordx4 a[228:231], v97, s[20:23], s34 offen\n\t"
        "s_add_u32 s34, s34, 0x8000\n\t"
        "buffer_load_dwordx4 a[232:235], v97, s[20:23], s34 offen\n\t"
        "s_add_u32 s34, s34, 0x8000\n\t"
        "buffer_load_dwordx4 a[236:239], v97, s[20:23], s34 offen\n\t"
        "s_add_u32 s34, s34, 0x8000\n\t"
        "buffer_load_dwordx4 a[240:243], v97, s[20:23], s34 offen\n\t"
        "s_add_u32 s34, s34, 0x8000\n\t"
        "buffer_load_dwordx4 a[244:247], v97, s[20:23], s34 offen\n\t"
        "s_add_u32 s34, s34, 0x8000\n\t"
        "buffer_load_dwordx4 a[248:251], v97, s[20:23], s34 offen\n\t"
        "s_add_u32 s34, s34, 0x8000\n\t"
        "buffer_load_dwordx4 a[252:255], v97, s[20:23], s34 offen\n\t"
        "s_waitcnt lgkmcnt(0)\n\t"
        "s_waitcnt vmcnt(16)\n\t"
        "ds_write_b128 v122, a[160:163] offset:0\n\t"
        "ds_write_b128 v122, a[164:167] offset:1056\n\t"
        "ds_write_b128 v122, a[168:171] offset:2176\n\t"
        "ds_write_b128 v122, a[172:175] offset:3232\n\t"
        "ds_write_b128 v122, a[176:179] offset:4352\n\t"
        "ds_write_b128 v122, a[180:183] offset:5408\n\t"
        "ds_write_b128 v122, a[184:187] offset:6528\n\t"
        "ds_write_b128 v122, a[188:191] offset:7584\n\t"
        "ds_read_b64_tr_b16 a[160:161], v123 offset:0\n\t"
        "ds_read_b64_tr_b16 a[162:163], v123 offset:128\n\t"
        "ds_read_b64_tr_b16 a[164:165], v123 offset:512\n\t"
        "ds_read_b64_tr_b16 a[166:167], v123 offset:640\n\t"
        "s_waitcnt lgkmcnt(8)\n\t"
        "ds_read_b64_tr_b16 a[168:169], v123 offset:32\n\t"
        "ds_read_b64_tr_b16 a[170:171], v123 offset:160\n\t"
        "ds_read_b64_tr_b16 a[172:173], v123 offset:544\n\t"
        "ds_read_b64_tr_b16 a[174:175], v123 offset:672\n\t"
        "s_waitcnt lgkmcnt(8)\n\t"
        "ds_read_b64_tr_b16 a[176:177], v123 offset:4352\n\t"
        "ds_read_b64_tr_b16 a[178:179], v123 offset:4480\n\t"
        "ds_read_b64_tr_b16 a[180:181], v123 offset:4864\n\t"
        "ds_read_b64_tr_b16 a[182:183], v123 offset:4992\n\t"
        "s_waitcnt lgkmcnt(8)\n\t"
        "ds_read_b64_tr_b16 a[184:185], v123 offset:4384\n\t"
        "ds_read_b64_tr_b16 a[186:187], v123 offset:4512\n\t"
        "ds_read_b64_tr_b16 a[188:189], v123 offset:4896\n\t"
        "ds_read_b64_tr_b16 a[190:191], v123 offset:5024\n\t"
        "s_waitcnt lgkmcnt(8)\n\t"
        "s_waitcnt lgkmcnt(0)\n\t"
        "s_waitcnt vmcnt(8)\n\t"
        "ds_write_b128 v122, a[192:195] offset:0\n\t"
        "ds_write_b128 v122, a[196:199] offset:1056\n\t"
        "ds_write_b128 v122, a[200:203] offset:2176\n\t"
        "ds_write_b128 v122, a[204:207] offset:3232\n\t"
        "ds_write_b128 v122, a[208:211] offset:4352\n\t"
        "ds_write_b128 v122, a[212:215] offset:5408\n\t"
        "ds_write_b128 v122, a[216:219] offset:6528\n\t"
        "ds_write_b128 v122, a[220:223] offset:7584\n\t"
        "ds_read_b64_tr_b16 a[192:193], v123 offset:0\n\t"
        "ds_read_b64_tr_b16 a[194:195], v123 offset:128\n\t"
        "ds_read_b64_tr_b16 a[196:197], v123 offset:512\n\t"
        "ds_read_b64_tr_b16 a[198:199], v123 offset:640\n\t"
        "s_waitcnt lgkmcnt(8)\n\t"
        "ds_read_b64_tr_b16 a[200:201], v123 offset:32\n\t"
        "ds_read_b64_tr_b16 a[202:203], v123 offset:160\n\t"
        "ds_read_b64_tr_b16 a[204:205], v123 offset:544\n\t"
        "ds_read_b64_tr_b16 a[206:207], v123 offset:672\n\t"
        "s_waitcnt lgkmcnt(8)\n\t"
        "ds_read_b64_tr_b16 a[208:209], v123 offset:4352\n\t"
        "ds_read_b64_tr_b16 a[210:211], v123 offset:4480\n\t"
        "ds_read_b64_tr_b16 a[212:213], v123 offset:4864\n\t"
        "ds_read_b64_tr_b16 a[214:215], v123 offset:4992\n\t"
        "s_waitcnt lgkmcnt(8)\n\t"
        "ds_read_b64_tr_b16 a[216:217], v123 offset:4384\n\t"
        "ds_read_b64_tr_b16 a[218:219], v123 offset:4512\n\t"
        "ds_read_b64_tr_b16 a[220:221], v123 offset:4896\n\t"
        "ds_read_b64_tr_b16 a[222:223], v123 offset:5024\n\t"
        "s_waitcnt lgkmcnt(8)\n\t"
        "s_waitcnt lgkmcnt(0)\n\t"
        "s_waitcnt vmcnt(0)\n\t"
        "ds_write_b128 v122, a[224:227] offset:0\n\t"
        "ds_write_b128 v122, a[228:231] offset:1056\n\t"
        "ds_write_b128 v122, a[232:235] offset:2176\n\t"
        "ds_write_b128 v122, a[236:239] offset:3232\n\t"
        "ds_write_b128 v122, a[240:243] offset:4352\n\t"
        "ds_write_b128 v122, a[244:247] offset:5408\n\t"
        "ds_write_b128 v122, a[248:251] offset:6528\n\t"
        "ds_write_b128 v122, a[252:255] offset:7584\n\t"
        "ds_read_b64_tr_b16 a[224:225], v123 offset:0\n\t"
        "ds_read_b64_tr_b16 a[226:227], v123 offset:128\n\t"
        "ds_read_b64_tr_b16 a[228:229], v123 offset:512\n\t"
        "ds_read_b64_tr_b16 a[230:231], v123 offset:640\n\t"
        "s_waitcnt lgkmcnt(8)\n\t"
        "ds_read_b64_tr_b16 a[232:233], v123 offset:32\n\t"
        "ds_read_b64_tr_b16 a[234:235], v123 offset:160\n\t"
        "ds_read_b64_tr_b16 a[236:237], v123 offset:544\n\t"
        "ds_read_b64_tr_b16 a[238:239], v123 offset:672\n\t"
        "s_waitcnt lgkmcnt(8)\n\t"
        "ds_read_b64_tr_b16 a[240:241], v123 offset:4352\n\t"
        "ds_read_b64_tr_b16 a[242:243], v123 offset:4480\n\t"
        "ds_read_b64_tr_b16 a[244:245], v123 offset:4864\n\t"
        "ds_read_b64_tr_b16 a[246:247], v123 offset:4992\n\t"
        "s_waitcnt lgkmcnt(8)\n\t"
        "ds_read_b64_tr_b16 a[248:249], v123 offset:4384\n\t"
        "ds_read_b64_tr_b16 a[250:251], v123 offset:4512\n\t"
        "ds_read_b64_tr_b16 a[252:253], v123 offset:4896\n\t"
        "ds_read_b64_tr_b16 a[254:255], v123 offset:5024\n\t"
        "s_waitcnt lgkmcnt(8)\n\t"
        "s_mov_b32 s35, 33792\n\t"
        "buffer_load_dword v0, off, s[12:15], s35 sc1\n\t"
        "s_waitcnt lgkmcnt(0)\n\t"
        "s_waitcnt vmcnt(0)\n\t"
        "s_mov_b32 s42, 0\n\t"
        "LB_cal%=:\n\t"
        "v_readfirstlane_b32 s34, v0\n\t"
        "s_nop 0\n\t"
        "s_cmp_lg_u32 s34, 0\n\t"
        "s_cbranch_scc1 LB_calok%=\n\t"
        "s_sleep 4\n\t"
        "s_mov_b32 s35, 33792\n\t"
        "buffer_load_dword v0, off, s[12:15], s35 sc1\n\t"
        "s_waitcnt vmcnt(0)\n\t"
        "s_add_u32 s42, s42, 1\n\t"
        "s_cmp_lt_u32 s42, 0x100000\n\t"
        "s_cbranch_scc1 LB_cal%=\n\t"
        "s_movk_i32 s34, 8\n\t"
        "s_branch LB_die%=\n\t"
        "LB_calok%=:\n\t"
        "s_and_b32 s34, s34, 8192\n\t"
        "s_add_u32 s8, s8, s34\n\t"
        "s_addc_u32 s9, s9, 0\n\t"
        "v_min_u32_e32 v109, -2, v109\n\t"
        "buffer_store_dword v109, v116, s[8:11], 0 offen sc1\n\t"
        "s_memrealtime s[32:33]\n\t"
        "s_waitcnt lgkmcnt(0)\n\t"
        "v_mov_b32_e32 v110, s32\n\t"
        "s_lshl_b32 s34, s29, 8\n\t"
        "s_lshl_b32 s35, s30, 20\n\t"
        "s_or_b32 s54, s34, s35\n\t"
        "s_add_u32 s34, s54, 12589056\n\t"
        "v_mov_b32_e32 v111, s34\n\t"
        "s_barrier\n\t"
        "s_cmp_lg_u32 s43, 0\n\t"
        "s_cbranch_scc1 LB_st%=\n\t"
        "v_mov_b32_e32 v2, 1\n\t"
        "s_mov_b32 s35, 34304\n\t"
        "s_mov_b64 exec, 1\n\t"
        "buffer_atomic_add v2, off, s[12:15], s35 sc0\n\t"
        "s_mov_b64 exec, -1\n\t"
        "s_waitcnt vmcnt(0)\n\t"
        "v_readfirstlane_b32 s34, v2\n\t"
        "s_nop 0\n\t"
        "s_cmp_lg_u32 s34, 95\n\t"
        "s_cbranch_scc1 LB_st%=\n\t"
        "s_memrealtime s[32:33]\n\t"
        "v_mbcnt_lo_u32_b32 v3, -1, 0\n\t"
        "v_lshlrev_b32_e32 v3, 7, v3\n\t"
        "s_waitcnt lgkmcnt(0)\n\t"
        "s_add_u32 s34, s32, 50\n\t"
        "s_or_b32 s34, s34, 1\n\t"
        "v_mov_b32_e32 v2, s34\n\t"
        "s_mov_b32 s35, 36864\n\t"
        "s_mov_b64 exec, 0xffff\n\t"
        "buffer_store_dword v2, v3, s[12:15], s35 offen sc1\n\t"
        "s_mov_b64 exec, -1\n\t"
        "LB_st%=:\n\t"
        "s_mov_b32 s24, 0\n\t"
        "s_mov_b32 s26, 0\n\t"
        "s_mov_b32 s27, 0x80000\n\t"
        "s_mov_b32 s28, 0x180000\n\t"
        "s_mov_b32 s31, 0\n\t"
        "s_mov_b32 s47, 0\n\t"
        "s_mov_b32 s38, 0xff00ff00\n\t"
        "s_mov_b32 s39, 0xff00ff00\n\t"
        "s_mov_b32 s40, 0\n\t"
        "s_mov_b32 s41, -1\n\t"
        "v_mov_b64_e32 v[0:1], 0\n\t"
        "v_mov_b64_e32 v[2:3], 0\n\t"
        "v_mov_b64_e32 v[4:5], 0\n\t"
        "v_mov_b64_e32 v[6:7], 0\n\t"
        "v_mov_b64_e32 v[8:9], 0\n\t"
        "v_mov_b64_e32 v[10:11], 0\n\t"
        "v_mov_b64_e32 v[12:13], 0\n\t"
        "v_mov_b64_e32 v[14:15], 0\n\t"
        "s_mov_b32 s42, 0\n\t"
        "LB_gw%=:\n\t"
        "buffer_load_dword v16, off, s[12:15], s45 sc1\n\t"
        "s_waitcnt vmcnt(0)\n\t"
        "v_readfirstlane_b32 s25, v16\n\t"
        "s_nop 0\n\t"
        "s_cmp_lg_u32 s25, 0\n\t"
        "s_cbranch_scc1 LB_loop%=\n\t"
        "s_sleep 2\n\t"
        "s_add_u32 s42, s42, 1\n\t"
        "s_cmp_lt_u32 s42, 0x100000\n\t"
        "s_cbranch_scc1 LB_gw%=\n\t"
        "s_movk_i32 s34, 9\n\t"
        "s_branch LB_die%=\n\t"
        "LB_loop%=:\n\t"
        "s_mov_b32 s42, 0\n\t"
        "s_sub_u32 s35, s25, 1\n\t"
        "LB_wa%=:\n\t"
        "s_memrealtime s[32:33]\n\t"
        "s_waitcnt lgkmcnt(0)\n\t"
        "s_sub_u32 s34, s35, s32\n\t"
        "s_cmpk_lt_u32 s34, 0x2000\n\t"
        "s_cbranch_scc1 LB_wa%=\n\t"
        "LB_rd%=:\n\t"
        "buffer_load_dwordx4 v[16:19], v112, s[8:11], s26 offen offset:0 sc1\n\t"
        "s_nop 9\n\t"
        "buffer_load_dwordx4 v[20:23], v112, s[8:11], s26 offen offset:1024 sc1\n\t"
        "s_nop 9\n\t"
        "buffer_load_dwordx4 v[24:27], v112, s[8:11], s26 offen offset:2048 sc1\n\t"
        "s_nop 9\n\t"
        "buffer_load_dwordx4 v[28:31], v112, s[8:11], s26 offen offset:3072 sc1\n\t"
        "LB_go%=:\n\t"
        "s_waitcnt vmcnt(3)\n\t"
        "v_smfmac_f32_16x16x64_bf16 v[0:3], v[16:19], v[128:135], v106\n\t"
        "v_max3_u32 v108, v16, v17, v18\n\t"
        "v_max_u32_e32 v108, v19, v108\n\t"
        "v_smfmac_f32_16x16x64_bf16 v[4:7], v[16:19], v[136:143], v106\n\t"
        "v_smfmac_f32_16x16x64_bf16 v[8:11], v[16:19], v[144:151], v106\n\t"
        "v_smfmac_f32_16x16x64_bf16 v[12:15], v[16:19], v[152:159], v106\n\t"
        "s_waitcnt vmcnt(2)\n\t"
        "v_smfmac_f32_16x16x64_bf16 v[0:3], v[20:23], v[160:167], v106\n\t"
        "v_max3_u32 v108, v20, v21, v108\n\t"
        "v_max3_u32 v108, v22, v23, v108\n\t"
        "v_smfmac_f32_16x16x64_bf16 v[4:7], v[20:23], v[168:175], v106\n\t"
        "v_smfmac_f32_16x16x64_bf16 v[8:11], v[20:23], v[176:183], v106\n\t"
        "v_smfmac_f32_16x16x64_bf16 v[12:15], v[20:23], v[184:191], v106\n\t"
        "s_waitcnt vmcnt(1)\n\t"
        "v_smfmac_f32_16x16x64_bf16 v[0:3], v[24:27], v[192:199], v106\n\t"
        "v_max3_u32 v108, v24, v25, v108\n\t"
        "v_max3_u32 v108, v26, v27, v108\n\t"
        "v_smfmac_f32_16x16x64_bf16 v[4:7], v[24:27], v[200:207], v106\n\t"
        "v_smfmac_f32_16x16x64_bf16 v[8:11], v[24:27], v[208:215], v106\n\t"
        "v_smfmac_f32_16x16x64_bf16 v[12:15], v[24:27], v[216:223], v106\n\t"
        "s_waitcnt vmcnt(0)\n\t"
        "v_smfmac_f32_16x16x64_bf16 v[0:3], v[28:31], v[224:231], v106\n\t"
        "v_max3_u32 v108, v28, v29, v108\n\t"
        "v_max3_u32 v108, v30, v31, v108\n\t"
        "v_smfmac_f32_16x16x64_bf16 v[4:7], v[28:31], v[232:239], v106\n\t"
        "v_cmp_eq_u32_e32 vcc, -1, v108\n\t"
        "v_smfmac_f32_16x16x64_bf16 v[8:11], v[28:31], v[240:247], v106\n\t"
        "v_smfmac_f32_16x16x64_bf16 v[12:15], v[28:31], v[248:255], v106\n\t"
        "s_cbranch_vccnz LB_rdfail%=\n\t"
        "s_nop 3\n\t"
        "v_permlane32_swap_b32_e32 v0, v4\n\t"
        "v_permlane32_swap_b32_e32 v1, v5\n\t"
        "v_permlane32_swap_b32_e32 v2, v6\n\t"
        "v_permlane32_swap_b32_e32 v3, v7\n\t"
        "v_pk_add_f32 v[0:1], v[0:1], v[4:5]\n\t"
        "v_pk_add_f32 v[2:3], v[2:3], v[6:7]\n\t"
        "ds_write_b128 v118, v[0:3] offset:0\n\t"
        "v_permlane32_swap_b32_e32 v8, v12\n\t"
        "v_permlane32_swap_b32_e32 v9, v13\n\t"
        "v_permlane32_swap_b32_e32 v10, v14\n\t"
        "v_permlane32_swap_b32_e32 v11, v15\n\t"
        "v_pk_add_f32 v[8:9], v[8:9], v[12:13]\n\t"
        "v_pk_add_f32 v[10:11], v[10:11], v[14:15]\n\t"
        "ds_write_b128 v118, v[8:11] offset:1024\n\t"
        "buffer_store_dwordx2 v[110:111], v117, s[8:11], s26 offen sc1\n\t"
        "s_waitcnt lgkmcnt(0)\n\t"
        "s_barrier\n\t"
        "ds_read_b128 v[16:19], v119 offset:0\n\t"
        "ds_read_b128 v[20:23], v119 offset:2048\n\t"
        "ds_read_b128 v[24:27], v119 offset:4096\n\t"
        "ds_read_b128 v[28:31], v119 offset:6144\n\t"
        "buffer_load_dwordx4 v[64:67], v124, s[8:11], s28 offen sc1\n\t"
        "s_waitcnt lgkmcnt(2)\n\t"
        "v_pk_add_f32 v[16:17], v[16:17], v[20:21]\n\t"
        "v_pk_add_f32 v[18:19], v[18:19], v[22:23]\n\t"
        "s_waitcnt lgkmcnt(0)\n\t"
        "v_pk_add_f32 v[24:25], v[24:25], v[28:29]\n\t"
        "v_pk_add_f32 v[26:27], v[26:27], v[30:31]\n\t"
        "v_pk_add_f32 v[16:17], v[16:17], v[24:25]\n\t"
        "v_pk_add_f32 v[18:19], v[18:19], v[26:27]\n\t"
        "v_cvt_pk_bf16_f32 v20, v16, v17\n\t"
        "v_cvt_pk_bf16_f32 v21, v18, v19\n\t"
        "buffer_store_dwordx2 v[20:21], v114, s[8:11], s26 offen sc1\n\t"
        "s_cmp_eq_u32 s24, 63\n\t"
        "s_cbranch_scc1 LB_end%=\n\t"
        "s_memrealtime s[52:53]\n\t"
        "v_mov_b64_e32 v[24:25], -1\n\t"
        "v_mov_b64_e32 v[32:33], 0\n\t"
        "v_mov_b64_e32 v[34:35], 0\n\t"
        "v_mov_b64_e32 v[36:37], 0\n\t"
        "v_mov_b64_e32 v[38:39], 0\n\t"
        "v_mov_b64_e32 v[40:41], 0\n\t"
        "v_mov_b64_e32 v[42:43], 0\n\t"
        "v_mov_b64_e32 v[44:45], 0\n\t"
        "v_mov_b64_e32 v[46:47], 0\n\t"
        "v_mov_b64_e32 v[0:1], 0\n\t"
        "v_mov_b64_e32 v[2:3], 0\n\t"
        "v_mov_b64_e32 v[4:5], 0\n\t"
        "v_mov_b64_e32 v[6:7], 0\n\t"
        "v_mov_b64_e32 v[8:9], 0\n\t"
        "v_mov_b64_e32 v[10:11], 0\n\t"
        "v_mov_b64_e32 v[12:13], 0\n\t"
        "v_mov_b64_e32 v[14:15], 0\n\t"
        "s_add_u32 s46, s26, s69\n\t"
        "s_add_u32 s48, s26, s70\n\t"
        "s_add_u32 s73, s26, s71\n\t"
        "s_add_u32 s74, s26, s72\n\t"
        "s_sleep 1\n\t"
        "buffer_load_dwordx2 v[104:105], v113, s[8:11], s26 offen sc1\n\t"
        "buffer_store_dword v24, v116, s[8:11], s28 offen sc1\n\t"
        "s_xor_b32 s34, s26, 0x100000\n\t"
        "buffer_store_dwordx2 v[24:25], v114, s[8:11], s34 offen sc1\n\t"
        "s_add_u32 s66, s49, s43\n\t"
        "s_cmp_lg_u32 s66, 32\n\t"
        "s_cbranch_scc1 LB_norep%=\n\t"
        "s_waitcnt vmcnt(4)\n\t"
        "s_sub_u32 s66, s24, 1\n\t"
        "v_lshrrev_b32_e32 v68, 16, v64\n\t"
        "v_cmp_eq_u32_e32 vcc, s66, v68\n\t"
        "v_subrev_u32_e32 v64, s59, v64\n\t"
        "v_bfe_i32 v64, v64, 0, 16\n\t"
        "v_cndmask_b32_e32 v64, 0, v64, vcc\n\t"
        "v_lshrrev_b32_e32 v68, 16, v65\n\t"
        "v_cmp_eq_u32_e32 vcc, s66, v68\n\t"
        "v_subrev_u32_e32 v65, s59, v65\n\t"
        "v_bfe_i32 v65, v65, 0, 16\n\t"
        "v_cndmask_b32_e32 v65, 0, v65, vcc\n\t"
        "v_lshrrev_b32_e32 v68, 16, v66\n\t"
        "v_cmp_eq_u32_e32 vcc, s66, v68\n\t"
        "v_subrev_u32_e32 v66, s59, v66\n\t"
        "v_bfe_i32 v66, v66, 0, 16\n\t"
        "v_cndmask_b32_e32 v66, 0, v66, vcc\n\t"
        "v_lshrrev_b32_e32 v68, 16, v67\n\t"
        "v_cmp_eq_u32_e32 vcc, s66, v68\n\t"
        "v_subrev_u32_e32 v67, s59, v67\n\t"
        "v_bfe_i32 v67, v67, 0, 16\n\t"
        "v_cndmask_b32_e32 v67, 0, v67, vcc\n\t"
        "v_max3_i32 v64, v64, v65, v66\n\t"
        "v_max_i32_e32 v64, v64, v67\n\t"
        "s_nop 1\n\t"
        "v_max_i32_dpp v64, v64, v64 quad_perm:[1,0,3,2] row_mask:0xf bank_mask:0xf\n\t"
        "s_nop 1\n\t"
        "v_max_i32_dpp v64, v64, v64 quad_perm:[2,3,0,1] row_mask:0xf bank_mask:0xf\n\t"
        "s_nop 1\n\t"
        "v_max_i32_dpp v64, v64, v64 row_ror:4 row_mask:0xf bank_mask:0xf\n\t"
        "s_nop 1\n\t"
        "v_max_i32_dpp v64, v64, v64 row_ror:8 row_mask:0xf bank_mask:0xf\n\t"
        "v_mov_b32_e32 v65, v64\n\t"
        "s_nop 1\n\t"
        "v_permlane16_swap_b32_e32 v64, v65\n\t"
        "v_max_i32_e32 v64, v64, v65\n\t"
        "v_mov_b32_e32 v65, v64\n\t"
        "s_nop 1\n\t"
        "v_permlane32_swap_b32_e32 v64, v65\n\t"
        "v_max_i32_e32 v64, v64, v65\n\t"
        "s_nop 0\n\t"
        "v_readfirstlane_b32 s64, v64\n\t"
        "LB_norep%=:\n\t"
        "s_waitcnt vmcnt(2) lgkmcnt(0)\n\t"
        "s_mov_b32 s61, s29\n\t"
        "s_mov_b32 s62, s30\n\t"
        "s_mov_b32 s63, s31\n\t"
        "v_and_b32_e32 v30, 0xff, v105\n\t"
        "v_cmp_ne_u32_e32 vcc, s24, v30\n\t"
        "s_nop 0\n\t"
        "s_cbranch_vccnz LB_stre%=\n\t"
        "LB_stok%=:\n\t"
        "v_subrev_u32_e32 v104, s25, v104\n\t"
        "s_nop 1\n\t"
        "v_max_i32_dpp v104, v104, v104 quad_perm:[1,0,3,2] row_mask:0xf bank_mask:0xf\n\t"
        "s_nop 1\n\t"
        "v_max_i32_dpp v104, v104, v104 quad_perm:[2,3,0,1] row_mask:0xf bank_mask:0xf\n\t"
        "s_nop 1\n\t"
        "v_max_i32_dpp v104, v104, v104 row_ror:4 row_mask:0xf bank_mask:0xf\n\t"
        "s_nop 1\n\t"
        "v_max_i32_dpp v104, v104, v104 row_ror:8 row_mask:0xf bank_mask:0xf\n\t"
        "s_nop 1\n\t"
        "v_max_i32_dpp v104, v104, v104 row_bcast:15 row_mask:0xa bank_mask:0xf\n\t"
        "s_nop 1\n\t"
        "v_max_i32_dpp v104, v104, v104 row_bcast:31 row_mask:0xc bank_mask:0xf\n\t"
        "v_readfirstlane_b32 s35, v105\n\t"
        "v_readlane_b32 s36, v104, 63\n\t"
        "s_bfe_u32 s29, s35, 0xc0008\n\t"
        "s_lshr_b32 s30, s35, 20\n\t"
        "s_add_u32 s35, s31, s50\n\t"
        "s_mul_i32 s37, s50, 3\n\t"
        "s_min_u32 s35, s35, s37\n\t"
        "s_add_i32 s34, s36, s51\n\t"
        "s_cmp_gt_i32 s34, 0\n\t"
        "s_cselect_b32 s31, s35, 0\n\t"
        "s_cselect_b32 s34, s34, 0\n\t"
        "s_add_u32 s34, s34, s31\n\t"
        "s_add_u32 s37, s25, s34\n\t"
        "s_add_u32 s45, s37, s29\n\t"
        "s_add_u32 s25, s45, s30\n\t"
        "s_add_u32 s66, s49, s43\n\t"
        "s_cmp_lg_u32 s66, 32\n\t"
        "s_cbranch_scc1 LB_noctl%=\n\t"
        "s_cmp_eq_u32 s24, 0\n\t"
        "s_cbranch_scc1 LB_ctl0%=\n\t"
        "s_cmp_eq_u32 s24, 1\n\t"
        "s_cbranch_scc1 LB_ctl1%=\n\t"
        "s_add_i32 s65, s36, s62\n\t"
        "s_lshl_b32 s66, s65, 1\n\t"
        "s_sub_i32 s66, s66, s56\n\t"
        "s_cmp_gt_i32 s66, 56\n\t"
        "s_cbranch_scc1 LB_c1%=\n\t"
        "s_lshl_b32 s66, s65, 1\n\t"
        "s_add_u32 s34, s56, 8\n\t"
        "s_min_i32 s34, s34, s66\n\t"
        "s_sub_u32 s35, s56, s66\n\t"
        "s_ashr_i32 s35, s35, 2\n\t"
        "s_max_i32 s35, s35, 1\n\t"
        "s_sub_u32 s35, s56, s35\n\t"
        "s_max_i32 s56, s34, s35\n\t"
        "s_branch LB_c2%=\n\t"
        "LB_c1%=:\n\t"
        "s_add_u32 s56, s56, 2\n\t"
        "s_add_i32 s66, s64, s57\n\t"
        "s_cmp_gt_i32 s66, s61\n\t"
        "s_cbranch_scc1 LB_c2%=\n\t"
        "s_add_u32 s57, s57, 4\n\t"
        "s_min_u32 s57, s57, 23\n\t"
        "LB_c2%=:\n\t"
        "s_cmp_lg_u32 s63, 0\n\t"
        "s_cbranch_scc1 LB_c4%=\n\t"
        "s_lshl_b32 s66, s64, 1\n\t"
        "s_sub_i32 s66, s66, s55\n\t"
        "s_cmp_gt_i32 s66, 56\n\t"
        "s_cbranch_scc1 LB_c3%=\n\t"
        "s_lshl_b32 s66, s64, 1\n\t"
        "s_add_u32 s34, s55, 8\n\t"
        "s_min_i32 s34, s34, s66\n\t"
        "s_sub_u32 s35, s55, s66\n\t"
        "s_ashr_i32 s35, s35, 2\n\t"
        "s_max_i32 s35, s35, 1\n\t"
        "s_sub_u32 s35, s55, s35\n\t"
        "s_max_i32 s55, s34, s35\n\t"
        "s_branch LB_c4%=\n\t"
        "LB_c3%=:\n\t"
        "s_add_u32 s55, s55, 2\n\t"
        "s_cmp_lt_i32 s60, s58\n\t"
        "s_cbranch_scc1 LB_c4%=\n\t"
        "s_add_u32 s66, s58, 8\n\t"
        "s_cmp_gt_i32 s60, s66\n\t"
        "s_cbranch_scc1 LB_c4%=\n\t"
        "s_add_u32 s58, s58, 4\n\t"
        "s_min_u32 s58, s58, 20\n\t"
        "LB_c4%=:\n\t"
        "s_and_b32 s66, s24, 3\n\t"
        "s_cmp_eq_u32 s66, 0\n\t"
        "s_cselect_b32 s66, 1, 0\n\t"
        "s_sub_u32 s57, s57, s66\n\t"
        "s_max_u32 s57, s57, 7\n\t"
        "s_sub_u32 s58, s58, s66\n\t"
        "s_max_u32 s58, s58, 4\n\t"
        "s_add_u32 s34, s55, 1\n\t"
        "s_lshr_b32 s34, s34, 1\n\t"
        "s_add_u32 s34, s34, s57\n\t"
        "s_min_u32 s34, s34, 1000\n\t"
        "s_lshl_b32 s34, s34, 8\n\t"
        "s_add_u32 s35, s56, 1\n\t"
        "s_lshr_b32 s35, s35, 1\n\t"
        "s_add_u32 s35, s35, s58\n\t"
        "s_min_u32 s35, s35, 1000\n\t"
        "s_lshl_b32 s35, s35, 20\n\t"
        "s_or_b32 s54, s34, s35\n\t"
        "s_branch LB_ctl1%=\n\t"
        "LB_ctl0%=:\n\t"
        "s_movk_i32 s57, 7\n\t"
        "s_movk_i32 s58, 4\n\t"
        "s_sub_u32 s55, s29, 31\n\t"
        "s_lshl_b32 s55, s55, 1\n\t"
        "s_sub_u32 s56, s30, 16\n\t"
        "s_lshl_b32 s56, s56, 1\n\t"
        "LB_ctl1%=:\n\t"
        "s_mov_b32 s59, s37\n\t"
        "s_sub_u32 s60, 0, s36\n\t"
        "LB_noctl%=:\n\t"
        "s_add_u32 s34, s24, 1\n\t"
        "s_or_b32 s34, s34, s54\n\t"
        "v_mov_b32_e32 v111, s34\n\t"
        "s_mov_b32 s42, 0\n\t"
        "s_sub_u32 s35, s45, 1\n\t"
        "LB_wo%=:\n\t"
        "s_memrealtime s[32:33]\n\t"
        "s_waitcnt lgkmcnt(0)\n\t"
        "s_sub_u32 s34, s35, s32\n\t"
        "s_cmpk_lt_u32 s34, 0x2000\n\t"
        "s_cbranch_scc1 LB_wo%=\n\t"
        "LB_ro%=:\n\t"
        "buffer_load_dwordx4 v[64:67], v115, s[8:11], s46 offen offset:0 sc1\n\t"
        "s_nop 11\n\t"
        "buffer_load_dwordx4 v[68:71], v115, s[8:11], s46 offen offset:1024 sc1\n\t"
        "s_nop 11\n\t"
        "buffer_load_dwordx4 v[72:75], v115, s[8:11], s48 offen offset:0 sc1\n\t"
        "s_nop 11\n\t"
        "buffer_load_dwordx4 v[76:79], v115, s[8:11], s48 offen offset:1024 sc1\n\t"
        "s_nop 11\n\t"
        "buffer_load_dwordx4 v[80:83], v115, s[8:11], s73 offen offset:0 sc1\n\t"
        "s_nop 11\n\t"
        "buffer_load_dwordx4 v[84:87], v115, s[8:11], s73 offen offset:1024 sc1\n\t"
        "s_nop 11\n\t"
        "buffer_load_dwordx4 v[88:91], v115, s[8:11], s74 offen offset:0 sc1\n\t"
        "s_nop 11\n\t"
        "buffer_load_dwordx4 v[92:95], v115, s[8:11], s74 offen offset:1024 sc1\n\t"
        "LB_go2%=:\n\t"
        "s_waitcnt vmcnt(7)\n\t"
        "v_smfmac_f32_16x16x64_bf16 v[40:43], v[64:67], a[16:23], v107\n\t"
        "v_max3_u32 v108, v64, v65, v66\n\t"
        "v_max_u32_e32 v108, v67, v108\n\t"
        "v_smfmac_f32_16x16x64_bf16 v[36:39], v[64:67], a[8:15], v107\n\t"
        "v_smfmac_f32_16x16x64_bf16 v[32:35], v[64:67], a[0:7], v107\n\t"
        "v_smfmac_f32_16x16x64_bf16 v[44:47], v[64:67], a[24:31], v107\n\t"
        "s_waitcnt vmcnt(6)\n\t"
        "v_smfmac_f32_16x16x64_bf16 v[40:43], v[68:71], a[48:55], v107\n\t"
        "v_max3_u32 v108, v68, v69, v108\n\t"
        "v_max3_u32 v108, v70, v71, v108\n\t"
        "v_smfmac_f32_16x16x64_bf16 v[36:39], v[68:71], a[40:47], v107\n\t"
        "v_smfmac_f32_16x16x64_bf16 v[32:35], v[68:71], a[32:39], v107\n\t"
        "v_smfmac_f32_16x16x64_bf16 v[44:47], v[68:71], a[56:63], v107\n\t"
        "s_waitcnt vmcnt(5)\n\t"
        "v_smfmac_f32_16x16x64_bf16 v[40:43], v[72:75], a[80:87], v107\n\t"
        "v_max3_u32 v108, v72, v73, v108\n\t"
        "v_max3_u32 v108, v74, v75, v108\n\t"
        "v_smfmac_f32_16x16x64_bf16 v[36:39], v[72:75], a[72:79], v107\n\t"
        "v_smfmac_f32_16x16x64_bf16 v[32:35], v[72:75], a[64:71], v107\n\t"
        "v_smfmac_f32_16x16x64_bf16 v[44:47], v[72:75], a[88:95], v107\n\t"
        "s_waitcnt vmcnt(4)\n\t"
        "v_smfmac_f32_16x16x64_bf16 v[40:43], v[76:79], a[112:119], v107\n\t"
        "v_max3_u32 v108, v76, v77, v108\n\t"
        "v_max3_u32 v108, v78, v79, v108\n\t"
        "v_smfmac_f32_16x16x64_bf16 v[36:39], v[76:79], a[104:111], v107\n\t"
        "v_smfmac_f32_16x16x64_bf16 v[32:35], v[76:79], a[96:103], v107\n\t"
        "v_smfmac_f32_16x16x64_bf16 v[44:47], v[76:79], a[120:127], v107\n\t"
        "s_waitcnt vmcnt(3)\n\t"
        "v_smfmac_f32_16x16x64_bf16 v[40:43], v[80:83], a[144:151], v107\n\t"
        "v_max3_u32 v108, v80, v81, v108\n\t"
        "v_max3_u32 v108, v82, v83, v108\n\t"
        "v_smfmac_f32_16x16x64_bf16 v[36:39], v[80:83], a[136:143], v107\n\t"
        "v_smfmac_f32_16x16x64_bf16 v[32:35], v[80:83], a[128:135], v107\n\t"
        "v_smfmac_f32_16x16x64_bf16 v[44:47], v[80:83], a[152:159], v107\n\t"
        "s_waitcnt vmcnt(2)\n\t"
        "v_smfmac_f32_16x16x64_bf16 v[40:43], v[84:87], a[176:183], v107\n\t"
        "v_max3_u32 v108, v84, v85, v108\n\t"
        "v_max3_u32 v108, v86, v87, v108\n\t"
        "v_smfmac_f32_16x16x64_bf16 v[36:39], v[84:87], a[168:175], v107\n\t"
        "v_smfmac_f32_16x16x64_bf16 v[32:35], v[84:87], a[160:167], v107\n\t"
        "v_smfmac_f32_16x16x64_bf16 v[44:47], v[84:87], a[184:191], v107\n\t"
        "s_waitcnt vmcnt(1)\n\t"
        "v_smfmac_f32_16x16x64_bf16 v[40:43], v[88:91], a[208:215], v107\n\t"
        "v_max3_u32 v108, v88, v89, v108\n\t"
        "v_max3_u32 v108, v90, v91, v108\n\t"
        "v_smfmac_f32_16x16x64_bf16 v[36:39], v[88:91], a[200:207], v107\n\t"
        "v_smfmac_f32_16x16x64_bf16 v[32:35], v[88:91], a[192:199], v107\n\t"
        "v_smfmac_f32_16x16x64_bf16 v[44:47], v[88:91], a[216:223], v107\n\t"
        "s_waitcnt vmcnt(0)\n\t"
        "v_smfmac_f32_16x16x64_bf16 v[40:43], v[92:95], a[240:247], v107\n\t"
        "v_max3_u32 v108, v92, v93, v108\n\t"
        "v_max3_u32 v108, v94, v95, v108\n\t"
        "v_smfmac_f32_16x16x64_bf16 v[36:39], v[92:95], a[232:239], v107\n\t"
        "v_cmp_eq_u32_e32 vcc, -1, v108\n\t"
        "v_smfmac_f32_16x16x64_bf16 v[32:35], v[92:95], a[224:231], v107\n\t"
        "v_smfmac_f32_16x16x64_bf16 v[44:47], v[92:95], a[248:255], v107\n\t"
        "s_cbranch_vccnz LB_rofail%=\n\t"
        "s_nop 3\n\t"
        "v_permlane32_swap_b32_e32 v40, v36\n\t"
        "v_permlane32_swap_b32_e32 v41, v37\n\t"
        "v_permlane32_swap_b32_e32 v42, v38\n\t"
        "v_permlane32_swap_b32_e32 v43, v39\n\t"
        "v_pk_add_f32 v[32:33], v[32:33], v[36:37]\n\t"
        "v_pk_add_f32 v[34:35], v[34:35], v[38:39]\n\t"
        "s_nop 0\n\t"
        "v_add_f32_dpp v32, v40, v44 quad_perm:[0,1,2,3] row_mask:0xc bank_mask:0xf\n\t"
        "v_add_f32_dpp v33, v41, v45 quad_perm:[0,1,2,3] row_mask:0xc bank_mask:0xf\n\t"
        "v_add_f32_dpp v34, v42, v46 quad_perm:[0,1,2,3] row_mask:0xc bank_mask:0xf\n\t"
        "v_add_f32_dpp v35, v43, v47 quad_perm:[0,1,2,3] row_mask:0xc bank_mask:0xf\n\t"
        "v_permlane16_swap_b32_e32 v32, v33\n\t"
        "s_nop 0\n\t"
        "v_permlane16_swap_b32_e32 v34, v35\n\t"
        "v_add_f32_e32 v32, v32, v33\n\t"
        "v_add_f32_e32 v34, v34, v35\n\t"
        "ds_write2_b32 v120, v32, v34 offset0:0 offset1:16\n\t"
        "s_waitcnt lgkmcnt(0)\n\t"
        "s_barrier\n\t"
        "ds_read_b64 v[64:65], v121 offset:0\n\t"
        "ds_read_b64 v[66:67], v121 offset:512\n\t"
        "ds_read_b64 v[68:69], v121 offset:1024\n\t"
        "ds_read_b64 v[70:71], v121 offset:1536\n\t"
        "s_waitcnt lgkmcnt(2)\n\t"
        "v_pk_add_f32 v[64:65], v[64:65], v[66:67]\n\t"
        "s_waitcnt lgkmcnt(0)\n\t"
        "v_pk_add_f32 v[68:69], v[68:69], v[70:71]\n\t"
        "v_pk_add_f32 v[64:65], v[64:65], v[68:69]\n\t"
        "v_cvt_pk_bf16_f32 v64, v64, v65\n\t"
        "buffer_store_dword v64, v116, s[8:11], s27 offen sc1\n\t"
        "s_memrealtime s[32:33]\n\t"
        "s_waitcnt lgkmcnt(0)\n\t"
        "v_mov_b32_e32 v110, s32\n\t"
        "s_add_u32 s24, s24, 1\n\t"
        "s_mov_b32 s28, s26\n\t"
        "s_mov_b32 s26, s27\n\t"
        "s_add_u32 s27, s27, 0x80000\n\t"
        "s_and_b32 s27, s27, 0x1fffff\n\t"
        "s_branch LB_loop%=\n\t"
        "LB_end%=:\n\t"
        "s_endpgm\n\t"
        "LB_stre%=:\n\t"
        "s_mov_b32 s42, 0\n\t"
        "LB_str2%=:\n\t"
        "buffer_load_dwordx2 v[104:105], v113, s[8:11], s26 offen sc1\n\t"
        "s_waitcnt vmcnt(0)\n\t"
        "v_and_b32_e32 v30, 0xff, v105\n\t"
        "v_cmp_ne_u32_e32 vcc, s24, v30\n\t"
        "s_nop 0\n\t"
        "s_cbranch_vccz LB_stok%=\n\t"
        "s_add_u32 s42, s42, 1\n\t"
        "s_cmp_lt_u32 s42, 0x100000\n\t"
        "s_cbranch_scc1 LB_str2%=\n\t"
        "s_movk_i32 s34, 5\n\t"
        "s_branch LB_die%=\n\t"
        "LB_rdfail%=:\n\t"
        "s_or_b32 s47, s47, 1\n\t"
        "buffer_load_dwordx4 v[16:19], v112, s[8:11], s26 offen offset:0 sc1\n\t"
        "buffer_load_dwordx4 v[20:23], v112, s[8:11], s26 offen offset:1024 sc1\n\t"
        "buffer_load_dwordx4 v[24:27], v112, s[8:11], s26 offen offset:2048 sc1\n\t"
        "buffer_load_dwordx4 v[28:31], v112, s[8:11], s26 offen offset:3072 sc1\n\t"
        "s_nop 7\n\t"
        "v_mov_b64_e32 v[0:1], 0\n\t"
        "v_mov_b64_e32 v[2:3], 0\n\t"
        "v_mov_b64_e32 v[4:5], 0\n\t"
        "v_mov_b64_e32 v[6:7], 0\n\t"
        "v_mov_b64_e32 v[8:9], 0\n\t"
        "v_mov_b64_e32 v[10:11], 0\n\t"
        "v_mov_b64_e32 v[12:13], 0\n\t"
        "v_mov_b64_e32 v[14:15], 0\n\t"
        "s_branch LB_rdre%=w\n\t"
        "LB_rdre%=:\n\t"
        "buffer_load_dwordx4 v[16:19], v112, s[8:11], s26 offen offset:0 sc1\n\t"
        "buffer_load_dwordx4 v[20:23], v112, s[8:11], s26 offen offset:1024 sc1\n\t"
        "buffer_load_dwordx4 v[24:27], v112, s[8:11], s26 offen offset:2048 sc1\n\t"
        "buffer_load_dwordx4 v[28:31], v112, s[8:11], s26 offen offset:3072 sc1\n\t"
        "LB_rdre%=w:\n\t"
        "s_waitcnt vmcnt(0) lgkmcnt(0)\n\t"
        "v_max3_u32 v108, v16, v17, v18\n\t"
        "v_max3_u32 v108, v108, v19, v20\n\t"
        "v_max3_u32 v108, v108, v21, v22\n\t"
        "v_max3_u32 v108, v108, v23, v24\n\t"
        "v_max3_u32 v108, v108, v25, v26\n\t"
        "v_max3_u32 v108, v108, v27, v28\n\t"
        "v_max3_u32 v108, v108, v29, v30\n\t"
        "v_max_u32_e32 v108, v108, v31\n\t"
        "v_cmp_eq_u32_e32 vcc, -1, v108\n\t"
        "s_nop 0\n\t"
        "s_cbranch_vccz LB_go%=\n\t"
        "s_add_u32 s42, s42, 1\n\t"
        "s_cmp_lt_u32 s42, 0x100000\n\t"
        "s_cbranch_scc1 LB_rdre%=\n\t"
        "s_movk_i32 s34, 3\n\t"
        "s_branch LB_die%=\n\t"
        "LB_rofail%=:\n\t"
        "s_or_b32 s47, s47, 4\n\t"
        "buffer_load_dwordx4 v[64:67], v115, s[8:11], s46 offen offset:0 sc1\n\t"
        "buffer_load_dwordx4 v[68:71], v115, s[8:11], s46 offen offset:1024 sc1\n\t"
        "buffer_load_dwordx4 v[72:75], v115, s[8:11], s48 offen offset:0 sc1\n\t"
        "buffer_load_dwordx4 v[76:79], v115, s[8:11], s48 offen offset:1024 sc1\n\t"
        "buffer_load_dwordx4 v[80:83], v115, s[8:11], s73 offen offset:0 sc1\n\t"
        "buffer_load_dwordx4 v[84:87], v115, s[8:11], s73 offen offset:1024 sc1\n\t"
        "buffer_load_dwordx4 v[88:91], v115, s[8:11], s74 offen offset:0 sc1\n\t"
        "buffer_load_dwordx4 v[92:95], v115, s[8:11], s74 offen offset:1024 sc1\n\t"
        "s_nop 7\n\t"
        "v_mov_b64_e32 v[32:33], 0\n\t"
        "v_mov_b64_e32 v[34:35], 0\n\t"
        "v_mov_b64_e32 v[36:37], 0\n\t"
        "v_mov_b64_e32 v[38:39], 0\n\t"
        "v_mov_b64_e32 v[40:41], 0\n\t"
        "v_mov_b64_e32 v[42:43], 0\n\t"
        "v_mov_b64_e32 v[44:45], 0\n\t"
        "v_mov_b64_e32 v[46:47], 0\n\t"
        "s_branch LB_rore%=w\n\t"
        "LB_rore%=:\n\t"
        "buffer_load_dwordx4 v[64:67], v115, s[8:11], s46 offen offset:0 sc1\n\t"
        "buffer_load_dwordx4 v[68:71], v115, s[8:11], s46 offen offset:1024 sc1\n\t"
        "buffer_load_dwordx4 v[72:75], v115, s[8:11], s48 offen offset:0 sc1\n\t"
        "buffer_load_dwordx4 v[76:79], v115, s[8:11], s48 offen offset:1024 sc1\n\t"
        "buffer_load_dwordx4 v[80:83], v115, s[8:11], s73 offen offset:0 sc1\n\t"
        "buffer_load_dwordx4 v[84:87], v115, s[8:11], s73 offen offset:1024 sc1\n\t"
        "buffer_load_dwordx4 v[88:91], v115, s[8:11], s74 offen offset:0 sc1\n\t"
        "buffer_load_dwordx4 v[92:95], v115, s[8:11], s74 offen offset:1024 sc1\n\t"
        "LB_rore%=w:\n\t"
        "s_waitcnt vmcnt(0) lgkmcnt(0)\n\t"
        "v_max3_u32 v108, v64, v65, v66\n\t"
        "v_max3_u32 v108, v108, v67, v68\n\t"
        "v_max3_u32 v108, v108, v69, v70\n\t"
        "v_max3_u32 v108, v108, v71, v72\n\t"
        "v_max3_u32 v108, v108, v73, v74\n\t"
        "v_max3_u32 v108, v108, v75, v76\n\t"
        "v_max3_u32 v108, v108, v77, v78\n\t"
        "v_max3_u32 v108, v108, v79, v80\n\t"
        "v_max3_u32 v108, v108, v81, v82\n\t"
        "v_max3_u32 v108, v108, v83, v84\n\t"
        "v_max3_u32 v108, v108, v85, v86\n\t"
        "v_max3_u32 v108, v108, v87, v88\n\t"
        "v_max3_u32 v108, v108, v89, v90\n\t"
        "v_max3_u32 v108, v108, v91, v92\n\t"
        "v_max3_u32 v108, v108, v93, v94\n\t"
        "v_max_u32_e32 v108, v108, v95\n\t"
        "v_cmp_eq_u32_e32 vcc, -1, v108\n\t"
        "s_nop 0\n\t"
        "s_cbranch_vccz LB_go2%=\n\t"
        "s_add_u32 s42, s42, 1\n\t"
        "s_cmp_lt_u32 s42, 0x100000\n\t"
        "s_cbranch_scc1 LB_rore%=\n\t"
        "s_movk_i32 s34, 4\n\t"
        "s_branch LB_die%=\n\t"
        "LB_die%=:\n\t"
        "v_mov_b32_e32 v30, s34\n\t"
        "s_mov_b32 s35, 33024\n\t"
        "s_mov_b64 exec, 1\n\t"
        "buffer_store_dword v30, off, s[12:15], s35 sc1\n\t"
        "s_endpgm\n\t"
        ".Lk_end_B:\n\t"
        :: REC_S_IN, "{v96}"(g_v), "{v97}"(g_o), "{v98}"(wb0), "{v99}"(wb1), "{v100}"(rb0), "{v101}"(rb0 ^ 32u), "{v102}"(rb0 ^ 64u), "{v103}"(rb0 ^ 96u),
           "{v106}"(pp ? 0xEEEEEEEEu : 0x44444444u), "{v107}"(bb ? 0xEEEEEEEEu : 0x44444444u), "{v[108:109]}"(m0p), "{s68}"(rot),
           "{v112}"(a_mem), "{v113}"((24 + cp) * 16384 + l * ST_P), "{v114}"(a_vmw), "{v115}"(a_or), "{v116}"(a_mw), "{v117}"(a_sw),
           "{v118}"(lb + w * 2048 + l * 16), "{v119}"(lb + (w & 1) * 1024 + l * 16), "{v120}"(lb + LB_R + w * 512 + ((g >> 1) * 2 + (n >> 3)) * 128 + (g & 1) * 32 + (n & 7) * 4),
           "{v121}"(lb + LB_R + l * 8), "{v122}"(stg + l * 16), "{v123}"(o_r), "{v124}"(rb + w == 32 ? 28 * 16384 + (l & 31) * 128 : OOB)
        : "memory", "vcc", "scc", "v0", "v255", "a0", "a255", "s101");
    __builtin_unreachable();
}
__device__ __forceinline__ void rec_helper(const Params& p, rsrc_t r, unsigned char* lds, const int wv, const int hi) {
    const int tid = otid(), l = tid & 63;
    const rsrc_t rh = __builtin_amdgcn_make_buffer_rsrc(p.ws + hx_base(p.ws), 0, HX_BYTES, 0x00020000);
    const v4u ones = {0xFFFFFFFFu, 0xFFFFFFFFu, 0xFFFFFFFFu, 0xFFFFFFFFu};
    unsigned best0 = ~0u, best1 = ~0u, d[5] = {0u, 0u, 0u, 0u, 0u};
    const bool meas = (hi & 7) < 2;
    if (meas) {
        const unsigned ca = l >= 8 ? OOB : (31 + wv * 32) * 16384 + hi * 256 + l * 16;
        const unsigned tend = rtnow() + CAL_T;
#pragma unroll 1
        for (int i = 0; i < 64 && (i < 3 || (int)(rtnow() - tend) < 0); ++i) {
            const unsigned t0 = (unsigned)__builtin_amdgcn_s_memtime(); __builtin_amdgcn_s_waitcnt(0);
            st16(rh, ca, ones); DRAIN();
            const unsigned t1 = (unsigned)__builtin_amdgcn_s_memtime(); __builtin_amdgcn_s_waitcnt(0);
            st16(rh, ca + 8192, ones); DRAIN();
            const unsigned t2 = (unsigned)__builtin_amdgcn_s_memtime(); __builtin_amdgcn_s_waitcnt(0);
            best0 = min(best0, t1 - t0); best1 = min(best1, t2 - t1); }
        if (l < 2) __hip_atomic_fetch_max((unsigned*)(p.ws + F_CAL + 128) + l, 65535u - min(l ? best1 : best0, 65535u), __ATOMIC_RELAXED, __HIP_MEMORY_SCOPE_AGENT);
    } else if (TOUCH) {
        const int xq = hi >> 3, k = ((hi & 7) - 2) * 4 + wv;
#pragma unroll
        for (int i = 0; i < 5; ++i) { const bf16_t* q;
            if (i & 1) { const int z = (i >> 1) * 24 + k, st = z & 3, c = 4 + (z >> 2);
                q = ((st & 1) ? p.Wk : p.Wq) + (size_t)(((((st & 1) * 2 + 1) * 4 + c) & 15) * 64 + l) * 4096 + (xq * 8 + 1 + (st >> 1) * 4) * 128 + 64; }
            else { const int z = min((i >> 1) * 24 + k, 63);
                q = p.Wo + (size_t)(((z & 3) * 8 + (z >> 3)) * 128 + ((z >> 2) & 1) * 64 + l) * 1024 + (RPL == 4 ? (xq >> 1) : (xq & 1)) * 512 + 192; if (RPL == 4 && (xq & 1)) q = p.gate_w; }
            asm volatile("global_load_dword %0, %1, off" : "+v"(d[i]) : "v"(q) : "memory"); }
    }
#pragma unroll
    for (int i = 0; i < 16; ++i) st16(rh, hi * 65536 + i * 4096 + tid * 16, ones);
    asm volatile("s_waitcnt vmcnt(0)" : "+v"(d[0]), "+v"(d[1]), "+v"(d[2]), "+v"(d[3]), "+v"(d[4]) :: "memory");
    __syncthreads();
    if (tid == 0 && __hip_atomic_fetch_add((unsigned*)(p.ws + F_CAL + 256), 1u, __ATOMIC_RELAXED, __HIP_MEMORY_SCOPE_AGENT) == 31u) {
        const v2u c = ld8(r, F_CAL + 128); st4(r, F_CAL, c.x < c.y ? 8193u : 1u); }
}
using f32x16 = __attribute__((ext_vector_type(16))) float;
constexpr int CV_RP = 80;
constexpr int CV_U = 96 * CV_RP;
constexpr int CV_KP = 4416;
constexpr int CV_K0 = 8 * CV_U;
__device__ __forceinline__ float blo(unsigned x) { return __uint_as_float(x << 16); }
__device__ __forceinline__ float bhi(unsigned x) { return __uint_as_float(x & 0xFFFF0000u); }
#define NAP() do { __builtin_amdgcn_sched_barrier(0); __builtin_amdgcn_s_sleep(XSLP); __builtin_amdgcn_sched_barrier(0); } while (0)
__device__ __forceinline__ void gate_block(const Params& p, rsrc_t r, const int hi, const int wv) {
    const int l = otid() & 63, t0 = hi * 64 + wv * 16;
    v4u xg[16][2], gw[2];
#pragma unroll
    for (int q = 0; q < 2; ++q) gw[q] = *(const v4u*)(p.gate_w + q * 512 + l * 8);
#pragma unroll
    for (int i = 0; i < 16; ++i)
#pragma unroll
        for (int q = 0; q < 2; ++q) { xg[i][q] = *(const v4u*)(p.Aq + (size_t)(t0 + i) * D + q * 512 + l * 8); NAP(); }
    float mine = 0.f;
#pragma unroll
    for (int i = 0; i < 16; ++i) { float acc = 0.f;
#pragma unroll
        for (int q = 0; q < 2; ++q)
#pragma unroll
            for (int e = 0; e < 4; ++e) { const unsigned pr = pack2(blo(xg[i][q][e]) * blo(gw[q][e]), bhi(xg[i][q][e]) * bhi(gw[q][e])); acc += blo(pr); acc += bhi(pr); }
        for (int o = 32; o > 0; o >>= 1) acc += __shfl_xor(acc, o);
        if (l == i) mine = acc; }
    if (l < 16) { const float z = rbfh(mine); st4(r, W_GATE + (t0 + l) * 4, __float_as_uint(rbfh(1.f / (1.f + expf(-z))))); }
}
__device__ __forceinline__ void conv_prep(const Params& p, rsrc_t r, const int wv, const int hi) {
    const int tid = otid(), Q = tid >> 2, cg = tid & 3, ch0 = hi * 32 + cg * 8;
    v4u sk[4]; float sv[8][4];
#pragma unroll
    for (int q = 0; q < 4; ++q) sk[q] = *(const v4u*)(p.shift_k + ch0 * 4 + q * 8);
#pragma unroll
    for (int c8 = 0; c8 < 8; ++c8)
#pragma unroll
        for (int tp = 0; tp < 4; ++tp) { const unsigned sw = sk[c8 >> 1][(c8 & 1) * 2 + (tp >> 1)]; sv[c8][tp] = (tp & 1) ? bhi(sw) : blo(sw); }
    const rsrc_t rk = __builtin_amdgcn_make_buffer_rsrc((void*)p.Ak, 0, S * D * 2, 0x00020000), rv = __builtin_amdgcn_make_buffer_rsrc((void*)p.Av, 0, S * D * 2, 0x00020000),
                 rq = __builtin_amdgcn_make_buffer_rsrc((void*)p.Aq, 0, S * D * 2, 0x00020000);
#pragma unroll 1
    for (int G = 0; G < 4; ++G) {
        const unsigned ro = (unsigned)((Q * 32 + G * 8) * D + ch0) * 2u, uo = hi * 131072 + tid * 16;
        v4u kr[11], u[8], qv[8];
#pragma unroll
        for (int i = 0; i < 11; ++i) { kr[i] = __builtin_amdgcn_raw_buffer_load_b128(rk, (i < 3 && Q + G == 0) ? OOB : ro + (i - 3) * 2048, 0, 0); NAP(); }
#pragma unroll
        for (int i = 0; i < 8; ++i) { u[i] = __builtin_amdgcn_raw_buffer_load_b128(rv, ro + i * 2048, 0, 0); NAP(); }
#pragma unroll
        for (int i = 0; i < 8; ++i) { qv[i] = __builtin_amdgcn_raw_buffer_load_b128(rq, ro + i * 2048, 0, 0); NAP(); }
#pragma unroll
        for (int q = 0; q < 4; ++q) {
#pragma unroll
            for (int i = 0; i < 8; ++i) {
                unsigned ks = pack2(blo(kr[i][q]) * sv[2 * q][3], bhi(kr[i][q]) * sv[2 * q + 1][3]), pr;
#pragma unroll
                for (int d = 1; d < 4; ++d) { pr = pack2(blo(kr[i + d][q]) * sv[2 * q][3 - d], bhi(kr[i + d][q]) * sv[2 * q + 1][3 - d]); ks = pack2(blo(ks) + blo(pr), bhi(ks) + bhi(pr)); }
                u[i][q] = pack2(blo(ks) * blo(u[i][q]), bhi(ks) * bhi(u[i][q])); }
            v4u lo, hh;
#pragma unroll
            for (int z = 0; z < 4; ++z) { const unsigned x = u[7 - 2 * z][q], y = u[6 - 2 * z][q]; lo[z] = __builtin_amdgcn_perm(y, x, 0x05040100u); hh[z] = __builtin_amdgcn_perm(y, x, 0x07060302u); }
            st16(r, W_U + uo + q * 32768 + G * 4096, lo); NAP(); st16(r, W_U + uo + q * 32768 + 16384 + G * 4096, hh); NAP();
#pragma unroll
            for (int z = 0; z < 2; ++z) { st16(r, W_Y + uo + q * 32768 + (2 * G + z) * 4096, (v4u){qv[4 * z][q], qv[4 * z + 1][q], qv[4 * z + 2][q], qv[4 * z + 3][q]}); NAP(); }
        }
    }
    DRAIN(); __syncthreads();
    if (tid == 0) __hip_atomic_fetch_add((unsigned*)(p.ws + F_CONV), 1u, __ATOMIC_RELAXED, __HIP_MEMORY_SCOPE_AGENT);
}
__device__ __forceinline__ bool conv_core(const Params& p, rsrc_t r, unsigned char* lds, const int wv, const int un) {
    const int tid = otid(), w = wv, l = tid & 63, Q = tid >> 2, cg = tid & 3;
    const rsrc_t rs = __builtin_amdgcn_make_buffer_rsrc((void*)p.ssm_k, 0, S * D * 2, 0x00020000);
    v4u kraw[4]; v2u kext[4];
    auto kload = [&](int n) { const unsigned o = (unsigned)(((un >> 2) * 32 + w * 8 + (un & 3) * 2 + n) * S + l * 8) * 2u;
#pragma unroll
        for (int i = 0; i < 4; ++i) { kraw[i] = __builtin_amdgcn_raw_buffer_load_b128(rs, o + i * 1024, 0, CV_POL);
            kext[i] = __builtin_amdgcn_raw_buffer_load_b64(rs, (i == 3 && l == 63) ? OOB : o + i * 1024 + 16, 0, 0); } };
    kload(0);
    if (!wait_word(r, F_CONV, 32u, 2048u)) return false;
    v4u ur[8], qr[8];
#pragma unroll
    for (int i = 0; i < 8; ++i) ur[i] = ld16(r, W_U + un * 32768 + i * 4096 + tid * 16);
#pragma unroll
    for (int i = 0; i < 8; ++i) qr[i] = ld16(r, W_Y + un * 32768 + i * 4096 + tid * 16);
    const unsigned dv = *(const unsigned*)(p.ssm_D + (un >> 2) * 32 + cg * 8 + (un & 3) * 2);
    for (int i = tid; i < 8 * 160; i += 256) *(v4u*)(lds + (i / 160) * CV_U + (i % 160) * 16) = (v4u){0u, 0u, 0u, 0u};
    unsigned char* kc = lds + CV_K0 + w * 4 * CV_KP;
    if (l < 32) {
#pragma unroll
        for (int q = 0; q < 4; ++q) *(unsigned*)(kc + q * CV_KP + l * 4) = 0u; }
    const int b = l & 31, kg = l >> 5;
    const unsigned kb = (unsigned)(size_t)kc + (b & 3) * CV_KP + ((b & 28) - 8 * kg + 56 - 16) * 2, ub0 = (unsigned)(size_t)lds + (1 + b) * CV_RP + kg * 16;
    unsigned char* const urow = lds + 2 * cg * CV_U + (32 + Q) * CV_RP;
#pragma unroll
    for (int i = 0; i < 8; ++i) *(v4u*)(urow + (i >> 2) * CV_U + (i & 3) * 16) = ur[i];
    __syncthreads();
#pragma unroll 1
        for (int ci = 0; ci < 2; ++ci) {
            const int c8 = 2 * w + ci;
#pragma unroll
            for (int i = 0; i < 4; ++i) { const v4u e = kraw[i]; const v2u f = kext[i]; unsigned char* q = kc + 128 + (i * 64 + l) * 16;
                const unsigned s0 = __builtin_amdgcn_alignbit(e.y, e.x, 16), s1 = __builtin_amdgcn_alignbit(e.z, e.y, 16), s2 = __builtin_amdgcn_alignbit(e.w, e.z, 16),
                               s3 = __builtin_amdgcn_alignbit(f.x, e.w, 16), s4 = __builtin_amdgcn_alignbit(f.y, f.x, 16);
                *(v4u*)q = (v4u){s0, s1, s2, s3}; *(v4u*)(q + CV_KP) = (v4u){e.y, e.z, e.w, f.x}; *(v4u*)(q + 2 * CV_KP) = (v4u){s1, s2, s3, s4}; *(v4u*)(q + 3 * CV_KP) = (v4u){e.z, e.w, f.x, f.y};
                if (i == 0 && l == 0) {
                    *(v2u*)(q - 8) = (v2u){0u, e.x << 16}; *(v2u*)(q + CV_KP - 8) = (v2u){0u, e.x}; *(v2u*)(q + 2 * CV_KP - 8) = (v2u){e.x << 16, s0}; *(v2u*)(q + 3 * CV_KP - 8) = (v2u){e.x, e.y}; } }
            if (ci == 0) kload(1);
            __builtin_amdgcn_wave_barrier();
            f32x16 acc0, acc1;
            asm volatile(
                "ds_read_b64 v[192:193], %[kb] offset:32\n\t"
                "ds_read_b64 v[194:195], %[kb] offset:40\n\t"
                "ds_read_b128 v[196:199], %[ub] offset:5040\n\t"
                "ds_read_b128 v[200:203], %[ub] offset:2480\n\t"
                "ds_read_b64 v[204:205], %[kb] offset:2080\n\t"
                "ds_read_b64 v[206:207], %[kb] offset:2088\n\t"
                "ds_read_b64 v[208:209], %[kb] offset:0\n\t"
                "ds_read_b64 v[210:211], %[kb] offset:8\n\t"
                "ds_read_b128 v[212:215], %[ub] offset:5072\n\t"
                "ds_read_b128 v[216:219], %[ub] offset:2512\n\t"
                "ds_read_b64 v[220:221], %[kb] offset:2048\n\t"
                "ds_read_b64 v[222:223], %[kb] offset:2056\n\t"
                "s_waitcnt lgkmcnt(9)\n\t"
                "v_mfma_f32_32x32x16_bf16 %[a1], v[192:195], v[196:199], 0\n\t"
                "ds_read_b64 v[224:225], %[kb] offset:96\n\t"
                "ds_read_b64 v[226:227], %[kb] offset:104\n\t"
                "ds_read_b128 v[228:231], %[ub] offset:4960\n\t"
                "s_waitcnt lgkmcnt(11)\n\t"
                "v_mfma_f32_32x32x16_bf16 %[a0], v[192:195], v[200:203], 0\n\t"
                "ds_read_b128 v[232:235], %[ub] offset:2400\n\t"
                "s_waitcnt lgkmcnt(10)\n\t"
                "v_mfma_f32_32x32x16_bf16 %[a1], v[204:207], v[200:203], %[a1]\n\t"
                "ds_read_b64 v[236:237], %[kb] offset:2144\n\t"
                "ds_read_b64 v[238:239], %[kb] offset:2152\n\t"
                "s_waitcnt lgkmcnt(9)\n\t"
                "v_mfma_f32_32x32x16_bf16 %[a1], v[208:211], v[212:215], %[a1]\n\t"
                "ds_read_b64 v[240:241], %[kb] offset:64\n\t"
                "ds_read_b64 v[242:243], %[kb] offset:72\n\t"
                "ds_read_b128 v[244:247], %[ub] offset:4992\n\t"
                "s_waitcnt lgkmcnt(11)\n\t"
                "v_mfma_f32_32x32x16_bf16 %[a0], v[208:211], v[216:219], %[a0]\n\t"
                "ds_read_b128 v[248:251], %[ub] offset:2432\n\t"
                "s_waitcnt lgkmcnt(10)\n\t"
                "v_mfma_f32_32x32x16_bf16 %[a1], v[220:223], v[216:219], %[a1]\n\t"
                "ds_read_b64 v[252:253], %[kb] offset:2112\n\t"
                "ds_read_b64 v[254:255], %[kb] offset:2120\n\t"
                "s_waitcnt lgkmcnt(9)\n\t"
                "v_mfma_f32_32x32x16_bf16 %[a1], v[224:227], v[228:231], %[a1]\n\t"
                "ds_read_b64 v[192:193], %[kb] offset:160\n\t"
                "ds_read_b64 v[194:195], %[kb] offset:168\n\t"
                "ds_read_b128 v[196:199], %[ub] offset:4880\n\t"
                "s_waitcnt lgkmcnt(11)\n\t"
                "v_mfma_f32_32x32x16_bf16 %[a0], v[224:227], v[232:235], %[a0]\n\t"
                "ds_read_b128 v[200:203], %[ub] offset:2320\n\t"
                "s_waitcnt lgkmcnt(10)\n\t"
                "v_mfma_f32_32x32x16_bf16 %[a1], v[236:239], v[232:235], %[a1]\n\t"
                "ds_read_b64 v[204:205], %[kb] offset:2208\n\t"
                "ds_read_b64 v[206:207], %[kb] offset:2216\n\t"
                "s_waitcnt lgkmcnt(9)\n\t"
                "v_mfma_f32_32x32x16_bf16 %[a1], v[240:243], v[244:247], %[a1]\n\t"
                "ds_read_b64 v[208:209], %[kb] offset:128\n\t"
                "ds_read_b64 v[210:211], %[kb] offset:136\n\t"
                "ds_read_b128 v[212:215], %[ub] offset:4912\n\t"
                "s_waitcnt lgkmcnt(11)\n\t"
                "v_mfma_f32_32x32x16_bf16 %[a0], v[240:243], v[248:251], %[a0]\n\t"
                "ds_read_b128 v[216:219], %[ub] offset:2352\n\t"
                "s_waitcnt lgkmcnt(10)\n\t"
                "v_mfma_f32_32x32x16_bf16 %[a1], v[252:255], v[248:251], %[a1]\n\t"
                "ds_read_b64 v[220:221], %[kb] offset:2176\n\t"
                "ds_read_b64 v[222:223], %[kb] offset:2184\n\t"
                "s_waitcnt lgkmcnt(9)\n\t"
                "v_mfma_f32_32x32x16_bf16 %[a1], v[192:195], v[196:199], %[a1]\n\t"
                "ds_read_b64 v[224:225], %[kb] offset:224\n\t"
                "ds_read_b64 v[226:227], %[kb] offset:232\n\t"
                "ds_read_b128 v[228:231], %[ub] offset:4800\n\t"
                "s_waitcnt lgkmcnt(11)\n\t"
                "v_mfma_f32_32x32x16_bf16 %[a0], v[192:195], v[200:203], %[a0]\n\t"
                "ds_read_b128 v[232:235], %[ub] offset:2240\n\t"
                "s_waitcnt lgkmcnt(10)\n\t"
                "v_mfma_f32_32x32x16_bf16 %[a1], v[204:207], v[200:203], %[a1]\n\t"
                "ds_read_b64 v[236:237], %[kb] offset:2272\n\t"
                "ds_read_b64 v[238:239], %[kb] offset:2280\n\t"
                "s_waitcnt lgkmcnt(9)\n\t"
                "v_mfma_f32_32x32x16_bf16 %[a1], v[208:211], v[212:215], %[a1]\n\t"
                "ds_read_b64 v[240:241], %[kb] offset:192\n\t"
                "ds_read_b64 v[242:243], %[kb] offset:200\n\t"
                "ds_read_b128 v[244:247], %[ub] offset:4832\n\t"
                "s_waitcnt lgkmcnt(11)\n\t"
                "v_mfma_f32_32x32x16_bf16 %[a0], v[208:211], v[216:219], %[a0]\n\t"
                "ds_read_b128 v[248:251], %[ub] offset:2272\n\t"
                "s_waitcnt lgkmcnt(10)\n\t"
                "v_mfma_f32_32x32x16_bf16 %[a1], v[220:223], v[216:219], %[a1]\n\t"
                "ds_read_b64 v[252:253], %[kb] offset:2240\n\t"
                "ds_read_b64 v[254:255], %[kb] offset:2248\n\t"
                "s_waitcnt lgkmcnt(9)\n\t"
                "v_mfma_f32_32x32x16_bf16 %[a1], v[224:227], v[228:231], %[a1]\n\t"
                "ds_read_b64 v[192:193], %[kb] offset:288\n\t"
                "ds_read_b64 v[194:195], %[kb] offset:296\n\t"
                "ds_read_b128 v[196:199], %[ub] offset:4720\n\t"
                "s_waitcnt lgkmcnt(11)\n\t"
                "v_mfma_f32_32x32x16_bf16 %[a0], v[224:227], v[232:235], %[a0]\n\t"
                "ds_read_b128 v[200:203], %[ub] offset:2160\n\t"
                "s_waitcnt lgkmcnt(10)\n\t"
                "v_mfma_f32_32x32x16_bf16 %[a1], v[236:239], v[232:235], %[a1]\n\t"
                "ds_read_b64 v[204:205], %[kb] offset:2336\n\t"
                "ds_read_b64 v[206:207], %[kb] offset:2344\n\t"
                "s_waitcnt lgkmcnt(9)\n\t"
                "v_mfma_f32_32x32x16_bf16 %[a1], v[240:243], v[244:247], %[a1]\n\t"
                "ds_read_b64 v[208:209], %[kb] offset:256\n\t"
                "ds_read_b64 v[210:211], %[kb] offset:264\n\t"
                "ds_read_b128 v[212:215], %[ub] offset:4752\n\t"
                "s_waitcnt lgkmcnt(11)\n\t"
                "v_mfma_f32_32x32x16_bf16 %[a0], v[240:243], v[248:251], %[a0]\n\t"
                "ds_read_b128 v[216:219], %[ub] offset:2192\n\t"
                "s_waitcnt lgkmcnt(10)\n\t"
                "v_mfma_f32_32x32x16_bf16 %[a1], v[252:255], v[248:251], %[a1]\n\t"
                "ds_read_b64 v[220:221], %[kb] offset:2304\n\t"
                "ds_read_b64 v[222:223], %[kb] offset:2312\n\t"
                "s_waitcnt lgkmcnt(9)\n\t"
                "v_mfma_f32_32x32x16_bf16 %[a1], v[192:195], v[196:199], %[a1]\n\t"
                "ds_read_b64 v[224:225], %[kb] offset:352\n\t"
                "ds_read_b64 v[226:227], %[kb] offset:360\n\t"
                "ds_read_b128 v[228:231], %[ub] offset:4640\n\t"
                "s_waitcnt lgkmcnt(11)\n\t"
                "v_mfma_f32_32x32x16_bf16 %[a0], v[192:195], v[200:203], %[a0]\n\t"
                "ds_read_b128 v[232:235], %[ub] offset:2080\n\t"
                "s_waitcnt lgkmcnt(10)\n\t"
                "v_mfma_f32_32x32x16_bf16 %[a1], v[204:207], v[200:203], %[a1]\n\t"
                "ds_read_b64 v[236:237], %[kb] offset:2400\n\t"
                "ds_read_b64 v[238:239], %[kb] offset:2408\n\t"
                "s_waitcnt lgkmcnt(9)\n\t"
                "v_mfma_f32_32x32x16_bf16 %[a1], v[208:211], v[212:215], %[a1]\n\t"
                "ds_read_b64 v[240:241], %[kb] offset:320\n\t"
                "ds_read_b64 v[242:243], %[kb] offset:328\n\t"
                "ds_read_b128 v[244:247], %[ub] offset:4672\n\t"
                "s_waitcnt lgkmcnt(11)\n\t"
                "v_mfma_f32_32x32x16_bf16 %[a0], v[208:211], v[216:219], %[a0]\n\t"
                "ds_read_b128 v[248:251], %[ub] offset:2112\n\t"
                "s_waitcnt lgkmcnt(10)\n\t"
                "v_mfma_f32_32x32x16_bf16 %[a1], v[220:223], v[216:219], %[a1]\n\t"
                "ds_read_b64 v[252:253], %[kb] offset:2368\n\t"
                "ds_read_b64 v[254:255], %[kb] offset:2376\n\t"
                "s_waitcnt lgkmcnt(9)\n\t"
                "v_mfma_f32_32x32x16_bf16 %[a1], v[224:227], v[228:231], %[a1]\n\t"
                "ds_read_b64 v[192:193], %[kb] offset:416\n\t"
                "ds_read_b64 v[194:195], %[kb] offset:424\n\t"
                "ds_read_b128 v[196:199], %[ub] offset:4560\n\t"
                "s_waitcnt lgkmcnt(11)\n\t"
                "v_mfma_f32_32x32x16_bf16 %[a0], v[224:227], v[232:235], %[a0]\n\t"
                "ds_read_b128 v[200:203], %[ub] offset:2000\n\t"
                "s_waitcnt lgkmcnt(10)\n\t"
                "v_mfma_f32_32x32x16_bf16 %[a1], v[236:239], v[232:235], %[a1]\n\t"
                "ds_read_b64 v[204:205], %[kb] offset:2464\n\t"
                "ds_read_b64 v[206:207], %[kb] offset:2472\n\t"
                "s_waitcnt lgkmcnt(9)\n\t"
                "v_mfma_f32_32x32x16_bf16 %[a1], v[240:243], v[244:247], %[a1]\n\t"
                "ds_read_b64 v[208:209], %[kb] offset:384\n\t"
                "ds_read_b64 v[210:211], %[kb] offset:392\n\t"
                "ds_read_b128 v[212:215], %[ub] offset:4592\n\t"
                "s_waitcnt lgkmcnt(11)\n\t"
                "v_mfma_f32_32x32x16_bf16 %[a0], v[240:243], v[248:251], %[a0]\n\t"
                "ds_read_b128 v[216:219], %[ub] offset:2032\n\t"
                "s_waitcnt lgkmcnt(10)\n\t"
                "v_mfma_f32_32x32x16_bf16 %[a1], v[252:255], v[248:251], %[a1]\n\t"
                "ds_read_b64 v[220:221], %[kb] offset:2432\n\t"
                "ds_read_b64 v[222:223], %[kb] offset:2440\n\t"
                "s_waitcnt lgkmcnt(9)\n\t"
                "v_mfma_f32_32x32x16_bf16 %[a1], v[192:195], v[196:199], %[a1]\n\t"
                "ds_read_b64 v[224:225], %[kb] offset:480\n\t"
                "ds_read_b64 v[226:227], %[kb] offset:488\n\t"
                "ds_read_b128 v[228:231], %[ub] offset:4480\n\t"
                "s_waitcnt lgkmcnt(11)\n\t"
                "v_mfma_f32_32x32x16_bf16 %[a0], v[192:195], v[200:203], %[a0]\n\t"
                "ds_read_b128 v[232:235], %[ub] offset:1920\n\t"
                "s_waitcnt lgkmcnt(10)\n\t"
                "v_mfma_f32_32x32x16_bf16 %[a1], v[204:207], v[200:203], %[a1]\n\t"
                "ds_read_b64 v[236:237], %[kb] offset:2528\n\t"
                "ds_read_b64 v[238:239], %[kb] offset:2536\n\t"
                "s_waitcnt lgkmcnt(9)\n\t"
                "v_mfma_f32_32x32x16_bf16 %[a1], v[208:211], v[212:215], %[a1]\n\t"
                "ds_read_b64 v[240:241], %[kb] offset:448\n\t"
                "ds_read_b64 v[242:243], %[kb] offset:456\n\t"
                "ds_read_b128 v[244:247], %[ub] offset:4512\n\t"
                "s_waitcnt lgkmcnt(11)\n\t"
                "v_mfma_f32_32x32x16_bf16 %[a0], v[208:211], v[216:219], %[a0]\n\t"
                "ds_read_b128 v[248:251], %[ub] offset:1952\n\t"
                "s_waitcnt lgkmcnt(10)\n\t"
                "v_mfma_f32_32x32x16_bf16 %[a1], v[220:223], v[216:219], %[a1]\n\t"
                "ds_read_b64 v[252:253], %[kb] offset:2496\n\t"
                "ds_read_b64 v[254:255], %[kb] offset:2504\n\t"
                "s_waitcnt lgkmcnt(9)\n\t"
                "v_mfma_f32_32x32x16_bf16 %[a1], v[224:227], v[228:231], %[a1]\n\t"
                "ds_read_b64 v[192:193], %[kb] offset:544\n\t"
                "ds_read_b64 v[194:195], %[kb] offset:552\n\t"
                "ds_read_b128 v[196:199], %[ub] offset:4400\n\t"
                "s_waitcnt lgkmcnt(11)\n\t"
                "v_mfma_f32_32x32x16_bf16 %[a0], v[224:227], v[232:235], %[a0]\n\t"
                "ds_read_b128 v[200:203], %[ub] offset:1840\n\t"
                "s_waitcnt lgkmcnt(10)\n\t"
                "v_mfma_f32_32x32x16_bf16 %[a1], v[236:239], v[232:235], %[a1]\n\t"
                "ds_read_b64 v[204:205], %[kb] offset:2592\n\t"
                "ds_read_b64 v[206:207], %[kb] offset:2600\n\t"
                "s_waitcnt lgkmcnt(9)\n\t"
                "v_mfma_f32_32x32x16_bf16 %[a1], v[240:243], v[244:247], %[a1]\n\t"
                "ds_read_b64 v[208:209], %[kb] offset:512\n\t"
                "ds_read_b64 v[210:211], %[kb] offset:520\n\t"
                "ds_read_b128 v[212:215], %[ub] offset:4432\n\t"
                "s_waitcnt lgkmcnt(11)\n\t"
                "v_mfma_f32_32x32x16_bf16 %[a0], v[240:243], v[248:251], %[a0]\n\t"
                "ds_read_b128 v[216:219], %[ub] offset:1872\n\t"
                "s_waitcnt lgkmcnt(10)\n\t"
                "v_mfma_f32_32x32x16_bf16 %[a1], v[252:255], v[248:251], %[a1]\n\t"
                "ds_read_b64 v[220:221], %[kb] offset:2560\n\t"
                "ds_read_b64 v[222:223], %[kb] offset:2568\n\t"
                "s_waitcnt lgkmcnt(9)\n\t"
                "v_mfma_f32_32x32x16_bf16 %[a1], v[192:195], v[196:199], %[a1]\n\t"
                "ds_read_b64 v[224:225], %[kb] offset:608\n\t"
                "ds_read_b64 v[226:227], %[kb] offset:616\n\t"
                "ds_read_b128 v[228:231], %[ub] offset:4320\n\t"
                "s_waitcnt lgkmcnt(11)\n\t"
                "v_mfma_f32_32x32x16_bf16 %[a0], v[192:195], v[200:203], %[a0]\n\t"
                "ds_read_b128 v[232:235], %[ub] offset:1760\n\t"
                "s_waitcnt lgkmcnt(10)\n\t"
                "v_mfma_f32_32x32x16_bf16 %[a1], v[204:207], v[200:203], %[a1]\n\t"
                "ds_read_b64 v[236:237], %[kb] offset:2656\n\t"
                "ds_read_b64 v[238:239], %[kb] offset:2664\n\t"
                "s_waitcnt lgkmcnt(9)\n\t"
                "v_mfma_f32_32x32x16_bf16 %[a1], v[208:211], v[212:215], %[a1]\n\t"
                "ds_read_b64 v[240:241], %[kb] offset:576\n\t"
                "ds_read_b64 v[242:243], %[kb] offset:584\n\t"
                "ds_read_b128 v[244:247], %[ub] offset:4352\n\t"
                "s_waitcnt lgkmcnt(11)\n\t"
                "v_mfma_f32_32x32x16_bf16 %[a0], v[208:211], v[216:219], %[a0]\n\t"
                "ds_read_b128 v[248:251], %[ub] offset:1792\n\t"
                "s_waitcnt lgkmcnt(10)\n\t"
                "v_mfma_f32_32x32x16_bf16 %[a1], v[220:223], v[216:219], %[a1]\n\t"
                "ds_read_b64 v[252:253], %[kb] offset:2624\n\t"
                "ds_read_b64 v[254:255], %[kb] offset:2632\n\t"
                "s_waitcnt lgkmcnt(9)\n\t"
                "v_mfma_f32_32x32x16_bf16 %[a1], v[224:227], v[228:231], %[a1]\n\t"
                "ds_read_b64 v[192:193], %[kb] offset:672\n\t"
                "ds_read_b64 v[194:195], %[kb] offset:680\n\t"
                "ds_read_b128 v[196:199], %[ub] offset:4240\n\t"
                "s_waitcnt lgkmcnt(11)\n\t"
                "v_mfma_f32_32x32x16_bf16 %[a0], v[224:227], v[232:235], %[a0]\n\t"
                "ds_read_b128 v[200:203], %[ub] offset:1680\n\t"
                "s_waitcnt lgkmcnt(10)\n\t"
                "v_mfma_f32_32x32x16_bf16 %[a1], v[236:239], v[232:235], %[a1]\n\t"
                "ds_read_b64 v[204:205], %[kb] offset:2720\n\t"
                "ds_read_b64 v[206:207], %[kb] offset:2728\n\t"
                "s_waitcnt lgkmcnt(9)\n\t"
                "v_mfma_f32_32x32x16_bf16 %[a1], v[240:243], v[244:247], %[a1]\n\t"
                "ds_read_b64 v[208:209], %[kb] offset:640\n\t"
                "ds_read_b64 v[210:211], %[kb] offset:648\n\t"
                "ds_read_b128 v[212:215], %[ub] offset:4272\n\t"
                "s_waitcnt lgkmcnt(11)\n\t"
                "v_mfma_f32_32x32x16_bf16 %[a0], v[240:243], v[248:251], %[a0]\n\t"
                "ds_read_b128 v[216:219], %[ub] offset:1712\n\t"
                "s_waitcnt lgkmcnt(10)\n\t"
                "v_mfma_f32_32x32x16_bf16 %[a1], v[252:255], v[248:251], %[a1]\n\t"
                "ds_read_b64 v[220:221], %[kb] offset:2688\n\t"
                "ds_read_b64 v[222:223], %[kb] offset:2696\n\t"
                "s_waitcnt lgkmcnt(9)\n\t"
                "v_mfma_f32_32x32x16_bf16 %[a1], v[192:195], v[196:199], %[a1]\n\t"
                "ds_read_b64 v[224:225], %[kb] offset:736\n\t"
                "ds_read_b64 v[226:227], %[kb] offset:744\n\t"
                "ds_read_b128 v[228:231], %[ub] offset:4160\n\t"
                "s_waitcnt lgkmcnt(11)\n\t"
                "v_mfma_f32_32x32x16_bf16 %[a0], v[192:195], v[200:203], %[a0]\n\t"
                "ds_read_b128 v[232:235], %[ub] offset:1600\n\t"
                "s_waitcnt lgkmcnt(10)\n\t"
                "v_mfma_f32_32x32x16_bf16 %[a1], v[204:207], v[200:203], %[a1]\n\t"
                "ds_read_b64 v[236:237], %[kb] offset:2784\n\t"
                "ds_read_b64 v[238:239], %[kb] offset:2792\n\t"
                "s_waitcnt lgkmcnt(9)\n\t"
                "v_mfma_f32_32x32x16_bf16 %[a1], v[208:211], v[212:215], %[a1]\n\t"
                "ds_read_b64 v[240:241], %[kb] offset:704\n\t"
                "ds_read_b64 v[242:243], %[kb] offset:712\n\t"
                "ds_read_b128 v[244:247], %[ub] offset:4192\n\t"
                "s_waitcnt lgkmcnt(11)\n\t"
                "v_mfma_f32_32x32x16_bf16 %[a0], v[208:211], v[216:219], %[a0]\n\t"
                "ds_read_b128 v[248:251], %[ub] offset:1632\n\t"
                "s_waitcnt lgkmcnt(10)\n\t"
                "v_mfma_f32_32x32x16_bf16 %[a1], v[220:223], v[216:219], %[a1]\n\t"
                "ds_read_b64 v[252:253], %[kb] offset:2752\n\t"
                "ds_read_b64 v[254:255], %[kb] offset:2760\n\t"
                "s_waitcnt lgkmcnt(9)\n\t"
                "v_mfma_f32_32x32x16_bf16 %[a1], v[224:227], v[228:231], %[a1]\n\t"
                "ds_read_b64 v[192:193], %[kb] offset:800\n\t"
                "ds_read_b64 v[194:195], %[kb] offset:808\n\t"
                "ds_read_b128 v[196:199], %[ub] offset:4080\n\t"
                "s_waitcnt lgkmcnt(11)\n\t"
                "v_mfma_f32_32x32x16_bf16 %[a0], v[224:227], v[232:235], %[a0]\n\t"
                "ds_read_b128 v[200:203], %[ub] offset:1520\n\t"
                "s_waitcnt lgkmcnt(10)\n\t"
                "v_mfma_f32_32x32x16_bf16 %[a1], v[236:239], v[232:235], %[a1]\n\t"
                "ds_read_b64 v[204:205], %[kb] offset:2848\n\t"
                "ds_read_b64 v[206:207], %[kb] offset:2856\n\t"
                "s_waitcnt lgkmcnt(9)\n\t"
                "v_mfma_f32_32x32x16_bf16 %[a1], v[240:243], v[244:247], %[a1]\n\t"
                "ds_read_b64 v[208:209], %[kb] offset:768\n\t"
                "ds_read_b64 v[210:211], %[kb] offset:776\n\t"
                "ds_read_b128 v[212:215], %[ub] offset:4112\n\t"
                "s_waitcnt lgkmcnt(11)\n\t"
                "v_mfma_f32_32x32x16_bf16 %[a0], v[240:243], v[248:251], %[a0]\n\t"
                "ds_read_b128 v[216:219], %[ub] offset:1552\n\t"
                "s_waitcnt lgkmcnt(10)\n\t"
                "v_mfma_f32_32x32x16_bf16 %[a1], v[252:255], v[248:251], %[a1]\n\t"
                "ds_read_b64 v[220:221], %[kb] offset:2816\n\t"
                "ds_read_b64 v[222:223], %[kb] offset:2824\n\t"
                "s_waitcnt lgkmcnt(9)\n\t"
                "v_mfma_f32_32x32x16_bf16 %[a1], v[192:195], v[196:199], %[a1]\n\t"
                "ds_read_b64 v[224:225], %[kb] offset:864\n\t"
                "ds_read_b64 v[226:227], %[kb] offset:872\n\t"
                "ds_read_b128 v[228:231], %[ub] offset:4000\n\t"
                "s_waitcnt lgkmcnt(11)\n\t"
                "v_mfma_f32_32x32x16_bf16 %[a0], v[192:195], v[200:203], %[a0]\n\t"
                "ds_read_b128 v[232:235], %[ub] offset:1440\n\t"
                "s_waitcnt lgkmcnt(10)\n\t"
                "v_mfma_f32_32x32x16_bf16 %[a1], v[204:207], v[200:203], %[a1]\n\t"
                "ds_read_b64 v[236:237], %[kb] offset:2912\n\t"
                "ds_read_b64 v[238:239], %[kb] offset:2920\n\t"
                "s_waitcnt lgkmcnt(9)\n\t"
                "v_mfma_f32_32x32x16_bf16 %[a1], v[208:211], v[212:215], %[a1]\n\t"
                "ds_read_b64 v[240:241], %[kb] offset:832\n\t"
                "ds_read_b64 v[242:243], %[kb] offset:840\n\t"
                "ds_read_b128 v[244:247], %[ub] offset:4032\n\t"
                "s_waitcnt lgkmcnt(11)\n\t"
                "v_mfma_f32_32x32x16_bf16 %[a0], v[208:211], v[216:219], %[a0]\n\t"
                "ds_read_b128 v[248:251], %[ub] offset:1472\n\t"
                "s_waitcnt lgkmcnt(10)\n\t"
                "v_mfma_f32_32x32x16_bf16 %[a1], v[220:223], v[216:219], %[a1]\n\t"
                "ds_read_b64 v[252:253], %[kb] offset:2880\n\t"
                "ds_read_b64 v[254:255], %[kb] offset:2888\n\t"
                "s_waitcnt lgkmcnt(9)\n\t"
                "v_mfma_f32_32x32x16_bf16 %[a1], v[224:227], v[228:231], %[a1]\n\t"
                "ds_read_b64 v[192:193], %[kb] offset:928\n\t"
                "ds_read_b64 v[194:195], %[kb] offset:936\n\t"
                "ds_read_b128 v[196:199], %[ub] offset:3920\n\t"
                "s_waitcnt lgkmcnt(11)\n\t"
                "v_mfma_f32_32x32x16_bf16 %[a0], v[224:227], v[232:235], %[a0]\n\t"
                "ds_read_b128 v[200:203], %[ub] offset:1360\n\t"
                "s_waitcnt lgkmcnt(10)\n\t"
                "v_mfma_f32_32x32x16_bf16 %[a1], v[236:239], v[232:235], %[a1]\n\t"
                "ds_read_b64 v[204:205], %[kb] offset:2976\n\t"
                "ds_read_b64 v[206:207], %[kb] offset:2984\n\t"
                "s_waitcnt lgkmcnt(9)\n\t"
                "v_mfma_f32_32x32x16_bf16 %[a1], v[240:243], v[244:247], %[a1]\n\t"
                "ds_read_b64 v[208:209], %[kb] offset:896\n\t"
                "ds_read_b64 v[210:211], %[kb] offset:904\n\t"
                "ds_read_b128 v[212:215], %[ub] offset:3952\n\t"
                "s_waitcnt lgkmcnt(11)\n\t"
                "v_mfma_f32_32x32x16_bf16 %[a0], v[240:243], v[248:251], %[a0]\n\t"
                "ds_read_b128 v[216:219], %[ub] offset:1392\n\t"
                "s_waitcnt lgkmcnt(10)\n\t"
                "v_mfma_f32_32x32x16_bf16 %[a1], v[252:255], v[248:251], %[a1]\n\t"
                "ds_read_b64 v[220:221], %[kb] offset:2944\n\t"
                "ds_read_b64 v[222:223], %[kb] offset:2952\n\t"
                "s_waitcnt lgkmcnt(9)\n\t"
                "v_mfma_f32_32x32x16_bf16 %[a1], v[192:195], v[196:199], %[a1]\n\t"
                "ds_read_b64 v[224:225], %[kb] offset:992\n\t"
                "ds_read_b64 v[226:227], %[kb] offset:1000\n\t"
                "ds_read_b128 v[228:231], %[ub] offset:3840\n\t"
                "s_waitcnt lgkmcnt(11)\n\t"
                "v_mfma_f32_32x32x16_bf16 %[a0], v[192:195], v[200:203], %[a0]\n\t"
                "ds_read_b128 v[232:235], %[ub] offset:1280\n\t"
                "s_waitcnt lgkmcnt(10)\n\t"
                "v_mfma_f32_32x32x16_bf16 %[a1], v[204:207], v[200:203], %[a1]\n\t"
                "ds_read_b64 v[236:237], %[kb] offset:3040\n\t"
                "ds_read_b64 v[238:239], %[kb] offset:3048\n\t"
                "s_waitcnt lgkmcnt(9)\n\t"
                "v_mfma_f32_32x32x16_bf16 %[a1], v[208:211], v[212:215], %[a1]\n\t"
                "ds_read_b64 v[240:241], %[kb] offset:960\n\t"
                "ds_read_b64 v[242:243], %[kb] offset:968\n\t"
                "ds_read_b128 v[244:247], %[ub] offset:3872\n\t"
                "s_waitcnt lgkmcnt(11)\n\t"
                "v_mfma_f32_32x32x16_bf16 %[a0], v[208:211], v[216:219], %[a0]\n\t"
                "ds_read_b128 v[248:251], %[ub] offset:1312\n\t"
                "s_waitcnt lgkmcnt(10)\n\t"
                "v_mfma_f32_32x32x16_bf16 %[a1], v[220:223], v[216:219], %[a1]\n\t"
                "ds_read_b64 v[252:253], %[kb] offset:3008\n\t"
                "ds_read_b64 v[254:255], %[kb] offset:3016\n\t"
                "s_waitcnt lgkmcnt(9)\n\t"
                "v_mfma_f32_32x32x16_bf16 %[a1], v[224:227], v[228:231], %[a1]\n\t"
                "ds_read_b64 v[192:193], %[kb] offset:1056\n\t"
                "ds_read_b64 v[194:195], %[kb] offset:1064\n\t"
                "ds_read_b128 v[196:199], %[ub] offset:3760\n\t"
                "s_waitcnt lgkmcnt(11)\n\t"
                "v_mfma_f32_32x32x16_bf16 %[a0], v[224:227], v[232:235], %[a0]\n\t"
                "ds_read_b128 v[200:203], %[ub] offset:1200\n\t"
                "s_waitcnt lgkmcnt(10)\n\t"
                "v_mfma_f32_32x32x16_bf16 %[a1], v[236:239], v[232:235], %[a1]\n\t"
                "ds_read_b64 v[204:205], %[kb] offset:3104\n\t"
                "ds_read_b64 v[206:207], %[kb] offset:3112\n\t"
                "s_waitcnt lgkmcnt(9)\n\t"
                "v_mfma_f32_32x32x16_bf16 %[a1], v[240:243], v[244:247], %[a1]\n\t"
                "ds_read_b64 v[208:209], %[kb] offset:1024\n\t"
                "ds_read_b64 v[210:211], %[kb] offset:1032\n\t"
                "ds_read_b128 v[212:215], %[ub] offset:3792\n\t"
                "s_waitcnt lgkmcnt(11)\n\t"
                "v_mfma_f32_32x32x16_bf16 %[a0], v[240:243], v[248:251], %[a0]\n\t"
                "ds_read_b128 v[216:219], %[ub] offset:1232\n\t"
                "s_waitcnt lgkmcnt(10)\n\t"
                "v_mfma_f32_32x32x16_bf16 %[a1], v[252:255], v[248:251], %[a1]\n\t"
                "ds_read_b64 v[220:221], %[kb] offset:3072\n\t"
                "ds_read_b64 v[222:223], %[kb] offset:3080\n\t"
                "s_waitcnt lgkmcnt(9)\n\t"
                "v_mfma_f32_32x32x16_bf16 %[a1], v[192:195], v[196:199], %[a1]\n\t"
                "ds_read_b64 v[224:225], %[kb] offset:1120\n\t"
                "ds_read_b64 v[226:227], %[kb] offset:1128\n\t"
                "ds_read_b128 v[228:231], %[ub] offset:3680\n\t"
                "s_waitcnt lgkmcnt(11)\n\t"
                "v_mfma_f32_32x32x16_bf16 %[a0], v[192:195], v[200:203], %[a0]\n\t"
                "ds_read_b128 v[232:235], %[ub] offset:1120\n\t"
                "s_waitcnt lgkmcnt(10)\n\t"
                "v_mfma_f32_32x32x16_bf16 %[a1], v[204:207], v[200:203], %[a1]\n\t"
                "ds_read_b64 v[236:237], %[kb] offset:3168\n\t"
                "ds_read_b64 v[238:239], %[kb] offset:3176\n\t"
                "s_waitcnt lgkmcnt(9)\n\t"
                "v_mfma_f32_32x32x16_bf16 %[a1], v[208:211], v[212:215], %[a1]\n\t"
                "ds_read_b64 v[240:241], %[kb] offset:1088\n\t"
                "ds_read_b64 v[242:243], %[kb] offset:1096\n\t"
                "ds_read_b128 v[244:247], %[ub] offset:3712\n\t"
                "s_waitcnt lgkmcnt(11)\n\t"
                "v_mfma_f32_32x32x16_bf16 %[a0], v[208:211], v[216:219], %[a0]\n\t"
                "ds_read_b128 v[248:251], %[ub] offset:1152\n\t"
                "s_waitcnt lgkmcnt(10)\n\t"
                "v_mfma_f32_32x32x16_bf16 %[a1], v[220:223], v[216:219], %[a1]\n\t"
                "ds_read_b64 v[252:253], %[kb] offset:3136\n\t"
                "ds_read_b64 v[254:255], %[kb] offset:3144\n\t"
                "s_waitcnt lgkmcnt(9)\n\t"
                "v_mfma_f32_32x32x16_bf16 %[a1], v[224:227], v[228:231], %[a1]\n\t"
                "ds_read_b64 v[192:193], %[kb] offset:1184\n\t"
                "ds_read_b64 v[194:195], %[kb] offset:1192\n\t"
                "ds_read_b128 v[196:199], %[ub] offset:3600\n\t"
                "s_waitcnt lgkmcnt(11)\n\t"
                "v_mfma_f32_32x32x16_bf16 %[a0], v[224:227], v[232:235], %[a0]\n\t"
                "ds_read_b128 v[200:203], %[ub] offset:1040\n\t"
                "s_waitcnt lgkmcnt(10)\n\t"
                "v_mfma_f32_32x32x16_bf16 %[a1], v[236:239], v[232:235], %[a1]\n\t"
                "ds_read_b64 v[204:205], %[kb] offset:3232\n\t"
                "ds_read_b64 v[206:207], %[kb] offset:3240\n\t"
                "s_waitcnt lgkmcnt(9)\n\t"
                "v_mfma_f32_32x32x16_bf16 %[a1], v[240:243], v[244:247], %[a1]\n\t"
                "ds_read_b64 v[208:209], %[kb] offset:1152\n\t"
                "ds_read_b64 v[210:211], %[kb] offset:1160\n\t"
                "ds_read_b128 v[212:215], %[ub] offset:3632\n\t"
                "s_waitcnt lgkmcnt(11)\n\t"
                "v_mfma_f32_32x32x16_bf16 %[a0], v[240:243], v[248:251], %[a0]\n\t"
                "ds_read_b128 v[216:219], %[ub] offset:1072\n\t"
                "s_waitcnt lgkmcnt(10)\n\t"
                "v_mfma_f32_32x32x16_bf16 %[a1], v[252:255], v[248:251], %[a1]\n\t"
                "ds_read_b64 v[220:221], %[kb] offset:3200\n\t"
                "ds_read_b64 v[222:223], %[kb] offset:3208\n\t"
                "s_waitcnt lgkmcnt(9)\n\t"
                "v_mfma_f32_32x32x16_bf16 %[a1], v[192:195], v[196:199], %[a1]\n\t"
                "ds_read_b64 v[224:225], %[kb] offset:1248\n\t"
                "ds_read_b64 v[226:227], %[kb] offset:1256\n\t"
                "ds_read_b128 v[228:231], %[ub] offset:3520\n\t"
                "s_waitcnt lgkmcnt(11)\n\t"
                "v_mfma_f32_32x32x16_bf16 %[a0], v[192:195], v[200:203], %[a0]\n\t"
                "ds_read_b128 v[232:235], %[ub] offset:960\n\t"
                "s_waitcnt lgkmcnt(10)\n\t"
                "v_mfma_f32_32x32x16_bf16 %[a1], v[204:207], v[200:203], %[a1]\n\t"
                "ds_read_b64 v[236:237], %[kb] offset:3296\n\t"
                "ds_read_b64 v[238:239], %[kb] offset:3304\n\t"
                "s_waitcnt lgkmcnt(9)\n\t"
                "v_mfma_f32_32x32x16_bf16 %[a1], v[208:211], v[212:215], %[a1]\n\t"
                "ds_read_b64 v[240:241], %[kb] offset:1216\n\t"
                "ds_read_b64 v[242:243], %[kb] offset:1224\n\t"
                "ds_read_b128 v[244:247], %[ub] offset:3552\n\t"
                "s_waitcnt lgkmcnt(11)\n\t"
                "v_mfma_f32_32x32x16_bf16 %[a0], v[208:211], v[216:219], %[a0]\n\t"
                "ds_read_b128 v[248:251], %[ub] offset:992\n\t"
                "s_waitcnt lgkmcnt(10)\n\t"
                "v_mfma_f32_32x32x16_bf16 %[a1], v[220:223], v[216:219], %[a1]\n\t"
                "ds_read_b64 v[252:253], %[kb] offset:3264\n\t"
                "ds_read_b64 v[254:255], %[kb] offset:3272\n\t"
                "s_waitcnt lgkmcnt(9)\n\t"
                "v_mfma_f32_32x32x16_bf16 %[a1], v[224:227], v[228:231], %[a1]\n\t"
                "ds_read_b64 v[192:193], %[kb] offset:1312\n\t"
                "ds_read_b64 v[194:195], %[kb] offset:1320\n\t"
                "ds_read_b128 v[196:199], %[ub] offset:3440\n\t"
                "s_waitcnt lgkmcnt(11)\n\t"
                "v_mfma_f32_32x32x16_bf16 %[a0], v[224:227], v[232:235], %[a0]\n\t"
                "ds_read_b128 v[200:203], %[ub] offset:880\n\t"
                "s_waitcnt lgkmcnt(10)\n\t"
                "v_mfma_f32_32x32x16_bf16 %[a1], v[236:239], v[232:235], %[a1]\n\t"
                "ds_read_b64 v[204:205], %[kb] offset:3360\n\t"
                "ds_read_b64 v[206:207], %[kb] offset:3368\n\t"
                "s_waitcnt lgkmcnt(9)\n\t"
                "v_mfma_f32_32x32x16_bf16 %[a1], v[240:243], v[244:247], %[a1]\n\t"
                "ds_read_b64 v[208:209], %[kb] offset:1280\n\t"
                "ds_read_b64 v[210:211], %[kb] offset:1288\n\t"
                "ds_read_b128 v[212:215], %[ub] offset:3472\n\t"
                "s_waitcnt lgkmcnt(11)\n\t"
                "v_mfma_f32_32x32x16_bf16 %[a0], v[240:243], v[248:251], %[a0]\n\t"
                "ds_read_b128 v[216:219], %[ub] offset:912\n\t"
                "s_waitcnt lgkmcnt(10)\n\t"
                "v_mfma_f32_32x32x16_bf16 %[a1], v[252:255], v[248:251], %[a1]\n\t"
                "ds_read_b64 v[220:221], %[kb] offset:3328\n\t"
                "ds_read_b64 v[222:223], %[kb] offset:3336\n\t"
                "s_waitcnt lgkmcnt(9)\n\t"
                "v_mfma_f32_32x32x16_bf16 %[a1], v[192:195], v[196:199], %[a1]\n\t"
                "ds_read_b64 v[224:225], %[kb] offset:1376\n\t"
                "ds_read_b64 v[226:227], %[kb] offset:1384\n\t"
                "ds_read_b128 v[228:231], %[ub] offset:3360\n\t"
                "s_waitcnt lgkmcnt(11)\n\t"
                "v_mfma_f32_32x32x16_bf16 %[a0], v[192:195], v[200:203], %[a0]\n\t"
                "ds_read_b128 v[232:235], %[ub] offset:800\n\t"
                "s_waitcnt lgkmcnt(10)\n\t"
                "v_mfma_f32_32x32x16_bf16 %[a1], v[204:207], v[200:203], %[a1]\n\t"
                "ds_read_b64 v[236:237], %[kb] offset:3424\n\t"
                "ds_read_b64 v[238:239], %[kb] offset:3432\n\t"
                "s_waitcnt lgkmcnt(9)\n\t"
                "v_mfma_f32_32x32x16_bf16 %[a1], v[208:211], v[212:215], %[a1]\n\t"
                "ds_read_b64 v[240:241], %[kb] offset:1344\n\t"
                "ds_read_b64 v[242:243], %[kb] offset:1352\n\t"
                "ds_read_b128 v[244:247], %[ub] offset:3392\n\t"
                "s_waitcnt lgkmcnt(11)\n\t"
                "v_mfma_f32_32x32x16_bf16 %[a0], v[208:211], v[216:219], %[a0]\n\t"
                "ds_read_b128 v[248:251], %[ub] offset:832\n\t"
                "s_waitcnt lgkmcnt(10)\n\t"
                "v_mfma_f32_32x32x16_bf16 %[a1], v[220:223], v[216:219], %[a1]\n\t"
                "ds_read_b64 v[252:253], %[kb] offset:3392\n\t"
                "ds_read_b64 v[254:255], %[kb] offset:3400\n\t"
                "s_waitcnt lgkmcnt(9)\n\t"
                "v_mfma_f32_32x32x16_bf16 %[a1], v[224:227], v[228:231], %[a1]\n\t"
                "ds_read_b64 v[192:193], %[kb] offset:1440\n\t"
                "ds_read_b64 v[194:195], %[kb] offset:1448\n\t"
                "ds_read_b128 v[196:199], %[ub] offset:3280\n\t"
                "s_waitcnt lgkmcnt(11)\n\t"
                "v_mfma_f32_32x32x16_bf16 %[a0], v[224:227], v[232:235], %[a0]\n\t"
                "ds_read_b128 v[200:203], %[ub] offset:720\n\t"
                "s_waitcnt lgkmcnt(10)\n\t"
                "v_mfma_f32_32x32x16_bf16 %[a1], v[236:239], v[232:235], %[a1]\n\t"
                "ds_read_b64 v[204:205], %[kb] offset:3488\n\t"
                "ds_read_b64 v[206:207], %[kb] offset:3496\n\t"
                "s_waitcnt lgkmcnt(9)\n\t"
                "v_mfma_f32_32x32x16_bf16 %[a1], v[240:243], v[244:247], %[a1]\n\t"
                "ds_read_b64 v[208:209], %[kb] offset:1408\n\t"
                "ds_read_b64 v[210:211], %[kb] offset:1416\n\t"
                "ds_read_b128 v[212:215], %[ub] offset:3312\n\t"
                "s_waitcnt lgkmcnt(11)\n\t"
                "v_mfma_f32_32x32x16_bf16 %[a0], v[240:243], v[248:251], %[a0]\n\t"
                "ds_read_b128 v[216:219], %[ub] offset:752\n\t"
                "s_waitcnt lgkmcnt(10)\n\t"
                "v_mfma_f32_32x32x16_bf16 %[a1], v[252:255], v[248:251], %[a1]\n\t"
                "ds_read_b64 v[220:221], %[kb] offset:3456\n\t"
                "ds_read_b64 v[222:223], %[kb] offset:3464\n\t"
                "s_waitcnt lgkmcnt(9)\n\t"
                "v_mfma_f32_32x32x16_bf16 %[a1], v[192:195], v[196:199], %[a1]\n\t"
                "ds_read_b64 v[224:225], %[kb] offset:1504\n\t"
                "ds_read_b64 v[226:227], %[kb] offset:1512\n\t"
                "ds_read_b128 v[228:231], %[ub] offset:3200\n\t"
                "s_waitcnt lgkmcnt(11)\n\t"
                "v_mfma_f32_32x32x16_bf16 %[a0], v[192:195], v[200:203], %[a0]\n\t"
                "ds_read_b128 v[232:235], %[ub] offset:640\n\t"
                "s_waitcnt lgkmcnt(10)\n\t"
                "v_mfma_f32_32x32x16_bf16 %[a1], v[204:207], v[200:203], %[a1]\n\t"
                "ds_read_b64 v[236:237], %[kb] offset:3552\n\t"
                "ds_read_b64 v[238:239], %[kb] offset:3560\n\t"
                "s_waitcnt lgkmcnt(9)\n\t"
                "v_mfma_f32_32x32x16_bf16 %[a1], v[208:211], v[212:215], %[a1]\n\t"
                "ds_read_b64 v[240:241], %[kb] offset:1472\n\t"
                "ds_read_b64 v[242:243], %[kb] offset:1480\n\t"
                "ds_read_b128 v[244:247], %[ub] offset:3232\n\t"
                "s_waitcnt lgkmcnt(11)\n\t"
                "v_mfma_f32_32x32x16_bf16 %[a0], v[208:211], v[216:219], %[a0]\n\t"
                "ds_read_b128 v[248:251], %[ub] offset:672\n\t"
                "s_waitcnt lgkmcnt(10)\n\t"
                "v_mfma_f32_32x32x16_bf16 %[a1], v[220:223], v[216:219], %[a1]\n\t"
                "ds_read_b64 v[252:253], %[kb] offset:3520\n\t"
                "ds_read_b64 v[254:255], %[kb] offset:3528\n\t"
                "s_waitcnt lgkmcnt(9)\n\t"
                "v_mfma_f32_32x32x16_bf16 %[a1], v[224:227], v[228:231], %[a1]\n\t"
                "ds_read_b64 v[192:193], %[kb] offset:1568\n\t"
                "ds_read_b64 v[194:195], %[kb] offset:1576\n\t"
                "ds_read_b128 v[196:199], %[ub] offset:3120\n\t"
                "s_waitcnt lgkmcnt(11)\n\t"
                "v_mfma_f32_32x32x16_bf16 %[a0], v[224:227], v[232:235], %[a0]\n\t"
                "ds_read_b128 v[200:203], %[ub] offset:560\n\t"
                "s_waitcnt lgkmcnt(10)\n\t"
                "v_mfma_f32_32x32x16_bf16 %[a1], v[236:239], v[232:235], %[a1]\n\t"
                "ds_read_b64 v[204:205], %[kb] offset:3616\n\t"
                "ds_read_b64 v[206:207], %[kb] offset:3624\n\t"
                "s_waitcnt lgkmcnt(9)\n\t"
                "v_mfma_f32_32x32x16_bf16 %[a1], v[240:243], v[244:247], %[a1]\n\t"
                "ds_read_b64 v[208:209], %[kb] offset:1536\n\t"
                "ds_read_b64 v[210:211], %[kb] offset:1544\n\t"
                "ds_read_b128 v[212:215], %[ub] offset:3152\n\t"
                "s_waitcnt lgkmcnt(11)\n\t"
                "v_mfma_f32_32x32x16_bf16 %[a0], v[240:243], v[248:251], %[a0]\n\t"
                "ds_read_b128 v[216:219], %[ub] offset:592\n\t"
                "s_waitcnt lgkmcnt(10)\n\t"
                "v_mfma_f32_32x32x16_bf16 %[a1], v[252:255], v[248:251], %[a1]\n\t"
                "ds_read_b64 v[220:221], %[kb] offset:3584\n\t"
                "ds_read_b64 v[222:223], %[kb] offset:3592\n\t"
                "s_waitcnt lgkmcnt(9)\n\t"
                "v_mfma_f32_32x32x16_bf16 %[a1], v[192:195], v[196:199], %[a1]\n\t"
                "ds_read_b64 v[224:225], %[kb] offset:1632\n\t"
                "ds_read_b64 v[226:227], %[kb] offset:1640\n\t"
                "ds_read_b128 v[228:231], %[ub] offset:3040\n\t"
                "s_waitcnt lgkmcnt(11)\n\t"
                "v_mfma_f32_32x32x16_bf16 %[a0], v[192:195], v[200:203], %[a0]\n\t"
                "ds_read_b128 v[232:235], %[ub] offset:480\n\t"
                "s_waitcnt lgkmcnt(10)\n\t"
                "v_mfma_f32_32x32x16_bf16 %[a1], v[204:207], v[200:203], %[a1]\n\t"
                "ds_read_b64 v[236:237], %[kb] offset:3680\n\t"
                "ds_read_b64 v[238:239], %[kb] offset:3688\n\t"
                "s_waitcnt lgkmcnt(9)\n\t"
                "v_mfma_f32_32x32x16_bf16 %[a1], v[208:211], v[212:215], %[a1]\n\t"
                "ds_read_b64 v[240:241], %[kb] offset:1600\n\t"
                "ds_read_b64 v[242:243], %[kb] offset:1608\n\t"
                "ds_read_b128 v[244:247], %[ub] offset:3072\n\t"
                "s_waitcnt lgkmcnt(11)\n\t"
                "v_mfma_f32_32x32x16_bf16 %[a0], v[208:211], v[216:219], %[a0]\n\t"
                "ds_read_b128 v[248:251], %[ub] offset:512\n\t"
                "s_waitcnt lgkmcnt(10)\n\t"
                "v_mfma_f32_32x32x16_bf16 %[a1], v[220:223], v[216:219], %[a1]\n\t"
                "ds_read_b64 v[252:253], %[kb] offset:3648\n\t"
                "ds_read_b64 v[254:255], %[kb] offset:3656\n\t"
                "s_waitcnt lgkmcnt(9)\n\t"
                "v_mfma_f32_32x32x16_bf16 %[a1], v[224:227], v[228:231], %[a1]\n\t"
                "ds_read_b64 v[192:193], %[kb] offset:1696\n\t"
                "ds_read_b64 v[194:195], %[kb] offset:1704\n\t"
                "ds_read_b128 v[196:199], %[ub] offset:2960\n\t"
                "s_waitcnt lgkmcnt(11)\n\t"
                "v_mfma_f32_32x32x16_bf16 %[a0], v[224:227], v[232:235], %[a0]\n\t"
                "ds_read_b128 v[200:203], %[ub] offset:400\n\t"
                "s_waitcnt lgkmcnt(10)\n\t"
                "v_mfma_f32_32x32x16_bf16 %[a1], v[236:239], v[232:235], %[a1]\n\t"
                "ds_read_b64 v[204:205], %[kb] offset:3744\n\t"
                "ds_read_b64 v[206:207], %[kb] offset:3752\n\t"
                "s_waitcnt lgkmcnt(9)\n\t"
                "v_mfma_f32_32x32x16_bf16 %[a1], v[240:243], v[244:247], %[a1]\n\t"
                "ds_read_b64 v[208:209], %[kb] offset:1664\n\t"
                "ds_read_b64 v[210:211], %[kb] offset:1672\n\t"
                "ds_read_b128 v[212:215], %[ub] offset:2992\n\t"
                "s_waitcnt lgkmcnt(11)\n\t"
                "v_mfma_f32_32x32x16_bf16 %[a0], v[240:243], v[248:251], %[a0]\n\t"
                "ds_read_b128 v[216:219], %[ub] offset:432\n\t"
                "s_waitcnt lgkmcnt(10)\n\t"
                "v_mfma_f32_32x32x16_bf16 %[a1], v[252:255], v[248:251], %[a1]\n\t"
                "ds_read_b64 v[220:221], %[kb] offset:3712\n\t"
                "ds_read_b64 v[222:223], %[kb] offset:3720\n\t"
                "s_waitcnt lgkmcnt(9)\n\t"
                "v_mfma_f32_32x32x16_bf16 %[a1], v[192:195], v[196:199], %[a1]\n\t"
                "ds_read_b64 v[224:225], %[kb] offset:1760\n\t"
                "ds_read_b64 v[226:227], %[kb] offset:1768\n\t"
                "ds_read_b128 v[228:231], %[ub] offset:2880\n\t"
                "s_waitcnt lgkmcnt(11)\n\t"
                "v_mfma_f32_32x32x16_bf16 %[a0], v[192:195], v[200:203], %[a0]\n\t"
                "ds_read_b128 v[232:235], %[ub] offset:320\n\t"
                "s_waitcnt lgkmcnt(10)\n\t"
                "v_mfma_f32_32x32x16_bf16 %[a1], v[204:207], v[200:203], %[a1]\n\t"
                "ds_read_b64 v[236:237], %[kb] offset:3808\n\t"
                "ds_read_b64 v[238:239], %[kb] offset:3816\n\t"
                "s_waitcnt lgkmcnt(9)\n\t"
                "v_mfma_f32_32x32x16_bf16 %[a1], v[208:211], v[212:215], %[a1]\n\t"
                "ds_read_b64 v[240:241], %[kb] offset:1728\n\t"
                "ds_read_b64 v[242:243], %[kb] offset:1736\n\t"
                "ds_read_b128 v[244:247], %[ub] offset:2912\n\t"
                "s_waitcnt lgkmcnt(11)\n\t"
                "v_mfma_f32_32x32x16_bf16 %[a0], v[208:211], v[216:219], %[a0]\n\t"
                "ds_read_b128 v[248:251], %[ub] offset:352\n\t"
                "s_waitcnt lgkmcnt(10)\n\t"
                "v_mfma_f32_32x32x16_bf16 %[a1], v[220:223], v[216:219], %[a1]\n\t"
                "ds_read_b64 v[252:253], %[kb] offset:3776\n\t"
                "ds_read_b64 v[254:255], %[kb] offset:3784\n\t"
                "s_waitcnt lgkmcnt(9)\n\t"
                "v_mfma_f32_32x32x16_bf16 %[a1], v[224:227], v[228:231], %[a1]\n\t"
                "ds_read_b64 v[192:193], %[kb] offset:1824\n\t"
                "ds_read_b64 v[194:195], %[kb] offset:1832\n\t"
                "ds_read_b128 v[196:199], %[ub] offset:2800\n\t"
                "s_waitcnt lgkmcnt(11)\n\t"
                "v_mfma_f32_32x32x16_bf16 %[a0], v[224:227], v[232:235], %[a0]\n\t"
                "ds_read_b128 v[200:203], %[ub] offset:240\n\t"
                "s_waitcnt lgkmcnt(10)\n\t"
                "v_mfma_f32_32x32x16_bf16 %[a1], v[236:239], v[232:235], %[a1]\n\t"
                "ds_read_b64 v[204:205], %[kb] offset:3872\n\t"
                "ds_read_b64 v[206:207], %[kb] offset:3880\n\t"
                "s_waitcnt lgkmcnt(9)\n\t"
                "v_mfma_f32_32x32x16_bf16 %[a1], v[240:243], v[244:247], %[a1]\n\t"
                "ds_read_b64 v[208:209], %[kb] offset:1792\n\t"
                "ds_read_b64 v[210:211], %[kb] offset:1800\n\t"
                "ds_read_b128 v[212:215], %[ub] offset:2832\n\t"
                "s_waitcnt lgkmcnt(11)\n\t"
                "v_mfma_f32_32x32x16_bf16 %[a0], v[240:243], v[248:251], %[a0]\n\t"
                "ds_read_b128 v[216:219], %[ub] offset:272\n\t"
                "s_waitcnt lgkmcnt(10)\n\t"
                "v_mfma_f32_32x32x16_bf16 %[a1], v[252:255], v[248:251], %[a1]\n\t"
                "ds_read_b64 v[220:221], %[kb] offset:3840\n\t"
                "ds_read_b64 v[222:223], %[kb] offset:3848\n\t"
                "s_waitcnt lgkmcnt(9)\n\t"
                "v_mfma_f32_32x32x16_bf16 %[a1], v[192:195], v[196:199], %[a1]\n\t"
                "ds_read_b64 v[224:225], %[kb] offset:1888\n\t"
                "ds_read_b64 v[226:227], %[kb] offset:1896\n\t"
                "ds_read_b128 v[228:231], %[ub] offset:2720\n\t"
                "s_waitcnt lgkmcnt(11)\n\t"
                "v_mfma_f32_32x32x16_bf16 %[a0], v[192:195], v[200:203], %[a0]\n\t"
                "ds_read_b128 v[232:235], %[ub] offset:160\n\t"
                "s_waitcnt lgkmcnt(10)\n\t"
                "v_mfma_f32_32x32x16_bf16 %[a1], v[204:207], v[200:203], %[a1]\n\t"
                "ds_read_b64 v[236:237], %[kb] offset:3936\n\t"
                "ds_read_b64 v[238:239], %[kb] offset:3944\n\t"
                "s_waitcnt lgkmcnt(9)\n\t"
                "v_mfma_f32_32x32x16_bf16 %[a1], v[208:211], v[212:215], %[a1]\n\t"
                "ds_read_b64 v[240:241], %[kb] offset:1856\n\t"
                "ds_read_b64 v[242:243], %[kb] offset:1864\n\t"
                "ds_read_b128 v[244:247], %[ub] offset:2752\n\t"
                "s_waitcnt lgkmcnt(11)\n\t"
                "v_mfma_f32_32x32x16_bf16 %[a0], v[208:211], v[216:219], %[a0]\n\t"
                "ds_read_b128 v[248:251], %[ub] offset:192\n\t"
                "s_waitcnt lgkmcnt(10)\n\t"
                "v_mfma_f32_32x32x16_bf16 %[a1], v[220:223], v[216:219], %[a1]\n\t"
                "ds_read_b64 v[252:253], %[kb] offset:3904\n\t"
                "ds_read_b64 v[254:255], %[kb] offset:3912\n\t"
                "s_waitcnt lgkmcnt(9)\n\t"
                "v_mfma_f32_32x32x16_bf16 %[a1], v[224:227], v[228:231], %[a1]\n\t"
                "ds_read_b64 v[192:193], %[kb] offset:1952\n\t"
                "ds_read_b64 v[194:195], %[kb] offset:1960\n\t"
                "ds_read_b128 v[196:199], %[ub] offset:2640\n\t"
                "s_waitcnt lgkmcnt(11)\n\t"
                "v_mfma_f32_32x32x16_bf16 %[a0], v[224:227], v[232:235], %[a0]\n\t"
                "ds_read_b128 v[200:203], %[ub] offset:80\n\t"
                "s_waitcnt lgkmcnt(10)\n\t"
                "v_mfma_f32_32x32x16_bf16 %[a1], v[236:239], v[232:235], %[a1]\n\t"
                "ds_read_b64 v[204:205], %[kb] offset:4000\n\t"
                "ds_read_b64 v[206:207], %[kb] offset:4008\n\t"
                "s_waitcnt lgkmcnt(9)\n\t"
                "v_mfma_f32_32x32x16_bf16 %[a1], v[240:243], v[244:247], %[a1]\n\t"
                "ds_read_b64 v[208:209], %[kb] offset:1920\n\t"
                "ds_read_b64 v[210:211], %[kb] offset:1928\n\t"
                "ds_read_b128 v[212:215], %[ub] offset:2672\n\t"
                "s_waitcnt lgkmcnt(11)\n\t"
                "v_mfma_f32_32x32x16_bf16 %[a0], v[240:243], v[248:251], %[a0]\n\t"
                "ds_read_b128 v[216:219], %[ub] offset:112\n\t"
                "s_waitcnt lgkmcnt(10)\n\t"
                "v_mfma_f32_32x32x16_bf16 %[a1], v[252:255], v[248:251], %[a1]\n\t"
                "ds_read_b64 v[220:221], %[kb] offset:3968\n\t"
                "ds_read_b64 v[222:223], %[kb] offset:3976\n\t"
                "s_waitcnt lgkmcnt(9)\n\t"
                "v_mfma_f32_32x32x16_bf16 %[a1], v[192:195], v[196:199], %[a1]\n\t"
                "ds_read_b64 v[224:225], %[kb] offset:2016\n\t"
                "ds_read_b64 v[226:227], %[kb] offset:2024\n\t"
                "ds_read_b128 v[228:231], %[ub] offset:2560\n\t"
                "s_waitcnt lgkmcnt(11)\n\t"
                "v_mfma_f32_32x32x16_bf16 %[a0], v[192:195], v[200:203], %[a0]\n\t"
                "ds_read_b128 v[232:235], %[ub] offset:0\n\t"
                "s_waitcnt lgkmcnt(10)\n\t"
                "v_mfma_f32_32x32x16_bf16 %[a1], v[204:207], v[200:203], %[a1]\n\t"
                "ds_read_b64 v[236:237], %[kb] offset:4064\n\t"
                "ds_read_b64 v[238:239], %[kb] offset:4072\n\t"
                "s_waitcnt lgkmcnt(9)\n\t"
                "v_mfma_f32_32x32x16_bf16 %[a1], v[208:211], v[212:215], %[a1]\n\t"
                "ds_read_b64 v[240:241], %[kb] offset:1984\n\t"
                "ds_read_b64 v[242:243], %[kb] offset:1992\n\t"
                "ds_read_b128 v[244:247], %[ub] offset:2592\n\t"
                "s_waitcnt lgkmcnt(11)\n\t"
                "v_mfma_f32_32x32x16_bf16 %[a0], v[208:211], v[216:219], %[a0]\n\t"
                "ds_read_b128 v[248:251], %[ub] offset:32\n\t"
                "s_waitcnt lgkmcnt(10)\n\t"
                "v_mfma_f32_32x32x16_bf16 %[a1], v[220:223], v[216:219], %[a1]\n\t"
                "ds_read_b64 v[252:253], %[kb] offset:4032\n\t"
                "ds_read_b64 v[254:255], %[kb] offset:4040\n\t"
                "s_waitcnt lgkmcnt(9)\n\t"
                "v_mfma_f32_32x32x16_bf16 %[a1], v[224:227], v[228:231], %[a1]\n\t"
                "s_waitcnt lgkmcnt(8)\n\t"
                "v_mfma_f32_32x32x16_bf16 %[a0], v[224:227], v[232:235], %[a0]\n\t"
                "s_waitcnt lgkmcnt(6)\n\t"
                "v_mfma_f32_32x32x16_bf16 %[a1], v[236:239], v[232:235], %[a1]\n\t"
                "s_waitcnt lgkmcnt(3)\n\t"
                "v_mfma_f32_32x32x16_bf16 %[a1], v[240:243], v[244:247], %[a1]\n\t"
                "s_waitcnt lgkmcnt(2)\n\t"
                "v_mfma_f32_32x32x16_bf16 %[a0], v[240:243], v[248:251], %[a0]\n\t"
                "s_waitcnt lgkmcnt(0)\n\t"
                "v_mfma_f32_32x32x16_bf16 %[a1], v[252:255], v[248:251], %[a1]\n\t"
                "s_nop 7\n\t"
                "s_nop 3\n\t"
                : [a0] "=&v"(acc0), [a1] "=&v"(acc1) : [kb] "v"(kb), [ub] "v"(ub0 + c8 * CV_U) : "memory", "v192", "v193", "v194", "v195", "v196", "v197", "v198", "v199", "v200", "v201", "v202", "v203", "v204", "v205", "v206", "v207", "v208", "v209", "v210", "v211", "v212", "v213", "v214", "v215", "v216", "v217", "v218", "v219", "v220", "v221", "v222", "v223", "v224", "v225", "v226", "v227", "v228", "v229", "v230", "v231", "v232", "v233", "v234", "v235", "v236", "v237", "v238", "v239", "v240", "v241", "v242", "v243", "v244", "v245", "v246", "v247", "v248", "v249", "v250", "v251", "v252", "v253", "v254", "v255");
            unsigned char* uc = lds + c8 * CV_U + (32 + b) * CV_RP + kg * 8;
#pragma unroll
            for (int q = 0; q < 4; ++q) {
                *(v2u*)(uc + q * 16) = (v2u){pack2(acc0[4 * q], acc0[4 * q + 1]), pack2(acc0[4 * q + 2], acc0[4 * q + 3])};
                *(v2u*)(uc + 32 * CV_RP + q * 16) = (v2u){pack2(acc1[4 * q], acc1[4 * q + 1]), pack2(acc1[4 * q + 2], acc1[4 * q + 3])}; }
            __builtin_amdgcn_wave_barrier();
        }
    __syncthreads();
#pragma unroll
    for (int G = 0; G < 4; ++G) { const v4u lo = *(const v4u*)(urow + G * 16), hh = *(const v4u*)(urow + CV_U + G * 16); v4u o[2];
#pragma unroll
        for (int i = 0; i < 8; ++i) { const unsigned u0 = ur[G][(7 - i) >> 1], u1 = ur[4 + G][(7 - i) >> 1], qq = qr[2 * G + (i >> 2)][i & 3];
            const unsigned ud = pack2(((i & 1) ? blo(u0) : bhi(u0)) * blo(dv), ((i & 1) ? blo(u1) : bhi(u1)) * bhi(dv));
            const unsigned y = pack2(((i & 1) ? bhi(lo[i >> 1]) : blo(lo[i >> 1])) + blo(ud), ((i & 1) ? bhi(hh[i >> 1]) : blo(hh[i >> 1])) + bhi(ud));
            o[i >> 2][i & 3] = pack2(blo(qq) * blo(y), bhi(qq) * bhi(y)); }
        st16(r, W_A + un * 32768 + 2 * G * 4096 + tid * 16, o[0]); st16(r, W_A + un * 32768 + (2 * G + 1) * 4096 + tid * 16, o[1]); }
    DRAIN(); __syncthreads();
    if (tid == 0) __hip_atomic_fetch_add((unsigned*)(p.ws + F_CVY + (un >> 2) * 128), 1u, __ATOMIC_RELAXED, __HIP_MEMORY_SCOPE_AGENT);
    return true;
}

__device__ __forceinline__ void proj_block(const Params& p, unsigned char* lds, const int wv, const int h, const int c, const int j) {
    const int w = wv, l = otid() & 63, n = l & 15, g = l >> 4, tm = w >> 1, tn = w & 1;
    const unsigned lb = __builtin_amdgcn_readfirstlane((unsigned)(size_t)lds);
    const v4u sx = mk_srd(p.Aq, S * D * 2), sq = mk_srd(c < 2 ? p.Wq : p.Wk, 1024 * 4096 * 2), sv = mk_srd(p.Wv, 1024 * 4096 * 2), sw = mk_srd(p.ws, W_TOTAL);
    const unsigned lx = lb + (32 * tm + n) * 128 + ((g ^ (n & 7)) << 4), lr = lb + 131072, rr = 8 * g + (n >> 2);
    const int cv = ((c & 1) ^ 1) * 2 + (c >> 1);
    const unsigned aq = (c < 2 ? W_QX : W_KX) + (tm * 32 + h) * 8192 + (2 * (c & 1) + tn) * 1024 + l * 16;
    const int hq = h & 7, tk = ((hq - (hq > 1) - (hq > 5)) * 4 + c) * 4 + w, ts6 = tk % 6, tch = 15 - tk / 6, tx = tk - 6 * GTOUCH;
    const bf16_t* tp = (h & 3) == 1 ? p.gate_w : tx < 0 ? (ts6 % 3 == 0 ? p.Wq : ts6 % 3 == 1 ? p.Wk : p.Wv) + (size_t)(tch * 64 + l) * 4096 + ((h & 24) + 1 + (ts6 / 3) * 4) * 128 + 64
                     : tx < XTOUCH ? p.Aq + (tx * 64 + l) * 64 : p.gate_w;
    unsigned t0, xo, so, cn; unsigned long long ts, ta, tb, tc, td;
    asm volatile(
        "s_mov_b32 %[xo], 0\n\t"
        "s_mov_b32 %[so], 0\n\t"
        "s_mov_b32 %[cn], 1\n\t"
        "global_load_dword v88, v[114:115], off\n\t"
        "s_add_u32 m0, %[ld], 0\n\t"
        "s_add_u32 %[t0], %[xo], 0\n\t"
        "buffer_load_dwordx4 v100, %[sx], %[t0] offen lds\n\t"
        "s_add_u32 m0, %[ld], 1024\n\t"
        "s_add_u32 %[t0], %[xo], 16384\n\t"
        "buffer_load_dwordx4 v100, %[sx], %[t0] offen lds\n\t"
        "s_add_u32 m0, %[ld], 8192\n\t"
        "s_add_u32 %[t0], %[xo], 128\n\t"
        "buffer_load_dwordx4 v100, %[sx], %[t0] offen lds\n\t"
        "s_add_u32 m0, %[ld], 9216\n\t"
        "s_add_u32 %[t0], %[xo], 16512\n\t"
        "buffer_load_dwordx4 v100, %[sx], %[t0] offen lds\n\t"
        "s_add_u32 m0, %[ld], 16384\n\t"
        "s_add_u32 %[t0], %[xo], 256\n\t"
        "buffer_load_dwordx4 v100, %[sx], %[t0] offen lds\n\t"
        "s_add_u32 m0, %[ld], 17408\n\t"
        "s_add_u32 %[t0], %[xo], 16640\n\t"
        "buffer_load_dwordx4 v100, %[sx], %[t0] offen lds\n\t"
        "s_add_u32 m0, %[ld], 24576\n\t"
        "s_add_u32 %[t0], %[xo], 384\n\t"
        "buffer_load_dwordx4 v100, %[sx], %[t0] offen lds\n\t"
        "s_add_u32 m0, %[ld], 25600\n\t"
        "s_add_u32 %[t0], %[xo], 16768\n\t"
        "buffer_load_dwordx4 v100, %[sx], %[t0] offen lds\n\t"
        "s_mov_b32 %[t0], 0\n\t"
        "buffer_load_dwordx4 a[192:195], v101, %[sq], %[t0] offen\n\t"
        "buffer_load_dwordx4 a[200:203], v112, %[sv], %[t0] offen\n\t"
        "s_mov_b32 %[t0], 262144\n\t"
        "buffer_load_dwordx4 a[196:199], v101, %[sq], %[t0] offen\n\t"
        "s_mov_b32 %[t0], 524288\n\t"
        "buffer_load_dwordx4 a[204:207], v101, %[sq], %[t0] offen\n\t"
        "buffer_load_dwordx4 a[212:215], v112, %[sv], %[t0] offen\n\t"
        "s_mov_b32 %[t0], 786432\n\t"
        "buffer_load_dwordx4 a[208:211], v101, %[sq], %[t0] offen\n\t"
        "s_mov_b32 %[t0], 1048576\n\t"
        "buffer_load_dwordx4 a[216:219], v101, %[sq], %[t0] offen\n\t"
        "buffer_load_dwordx4 a[224:227], v112, %[sv], %[t0] offen\n\t"
        "s_mov_b32 %[t0], 1310720\n\t"
        "buffer_load_dwordx4 a[220:223], v101, %[sq], %[t0] offen\n\t"
        "s_mov_b32 %[t0], 1572864\n\t"
        "buffer_load_dwordx4 a[228:231], v101, %[sq], %[t0] offen\n\t"
        "buffer_load_dwordx4 a[236:239], v112, %[sv], %[t0] offen\n\t"
        "s_mov_b32 %[t0], 1835008\n\t"
        "buffer_load_dwordx4 a[232:235], v101, %[sq], %[t0] offen\n\t"
        "s_waitcnt vmcnt(9)\n\t"
        "ds_write_b128 v102, a[192:195] offset:0\n\t"
        "ds_write_b128 v102, a[196:199] offset:4096\n\t"
        "ds_write_b128 v116, a[200:203] offset:8192\n\t"
        "s_waitcnt lgkmcnt(0)\n\t"
        "s_barrier\n\t"
        "ds_read_b64_tr_b16 a[0:1], v103 offset:0\n\t"
        "ds_read_b64_tr_b16 a[2:3], v103 offset:512\n\t"
        "ds_read_b64_tr_b16 a[4:5], v103 offset:8\n\t"
        "ds_read_b64_tr_b16 a[6:7], v103 offset:520\n\t"
        "ds_read_b64_tr_b16 a[8:9], v104 offset:0\n\t"
        "ds_read_b64_tr_b16 a[10:11], v104 offset:256\n\t"
        "s_waitcnt lgkmcnt(5)\n\t"
        "ds_read_b64_tr_b16 a[12:13], v103 offset:4096\n\t"
        "ds_read_b64_tr_b16 a[14:15], v103 offset:4608\n\t"
        "ds_read_b64_tr_b16 a[16:17], v103 offset:4104\n\t"
        "ds_read_b64_tr_b16 a[18:19], v103 offset:4616\n\t"
        "ds_read_b64_tr_b16 a[20:21], v104 offset:2048\n\t"
        "ds_read_b64_tr_b16 a[22:23], v104 offset:2304\n\t"
        "ds_read_b128 v[56:59], v105 offset:0\n\t"
        "ds_read_b128 v[60:63], v105 offset:2048\n\t"
        "ds_read_b128 v[64:67], v106 offset:0\n\t"
        "ds_read_b128 v[68:71], v106 offset:2048\n\t"
        "s_add_u32 m0, %[ld], 32768\n\t"
        "s_add_u32 %[t0], %[xo], 512\n\t"
        "buffer_load_dwordx4 v100, %[sx], %[t0] offen lds\n\t"
        "s_add_u32 m0, %[ld], 33792\n\t"
        "s_add_u32 %[t0], %[xo], 16896\n\t"
        "buffer_load_dwordx4 v100, %[sx], %[t0] offen lds\n\t"
        "s_add_u32 m0, %[ld], 40960\n\t"
        "s_add_u32 %[t0], %[xo], 640\n\t"
        "buffer_load_dwordx4 v100, %[sx], %[t0] offen lds\n\t"
        "s_add_u32 m0, %[ld], 41984\n\t"
        "s_add_u32 %[t0], %[xo], 17024\n\t"
        "buffer_load_dwordx4 v100, %[sx], %[t0] offen lds\n\t"
        "s_add_u32 m0, %[ld], 49152\n\t"
        "s_add_u32 %[t0], %[xo], 768\n\t"
        "buffer_load_dwordx4 v100, %[sx], %[t0] offen lds\n\t"
        "s_add_u32 m0, %[ld], 50176\n\t"
        "s_add_u32 %[t0], %[xo], 17152\n\t"
        "buffer_load_dwordx4 v100, %[sx], %[t0] offen lds\n\t"
        "s_add_u32 m0, %[ld], 57344\n\t"
        "s_add_u32 %[t0], %[xo], 896\n\t"
        "buffer_load_dwordx4 v100, %[sx], %[t0] offen lds\n\t"
        "s_add_u32 m0, %[ld], 58368\n\t"
        "s_add_u32 %[t0], %[xo], 17280\n\t"
        "buffer_load_dwordx4 v100, %[sx], %[t0] offen lds\n\t"
        "s_mov_b32 %[t0], 2097152\n\t"
        "buffer_load_dwordx4 a[240:243], v101, %[sq], %[t0] offen\n\t"
        "buffer_load_dwordx4 a[248:251], v112, %[sv], %[t0] offen\n\t"
        "s_mov_b32 %[t0], 2359296\n\t"
        "buffer_load_dwordx4 a[244:247], v101, %[sq], %[t0] offen\n\t"
        "s_waitcnt vmcnt(17)\n\t"
        "ds_write_b128 v102, a[204:207] offset:16384\n\t"
        "ds_write_b128 v102, a[208:211] offset:20480\n\t"
        "ds_write_b128 v116, a[212:215] offset:24576\n\t"
        "s_waitcnt lgkmcnt(0)\n\t"
        "s_barrier\n\t"
        "v_mfma_f32_16x16x32_bf16 v[8:11], a[0:3], v[56:59], 0\n\t"
        "ds_read_b64_tr_b16 a[24:25], v103 offset:16384\n\t"
        "ds_read_b64_tr_b16 a[26:27], v103 offset:16896\n\t"
        "v_mfma_f32_16x16x32_bf16 v[12:15], a[4:7], v[56:59], 0\n\t"
        "ds_read_b64_tr_b16 a[28:29], v103 offset:16392\n\t"
        "ds_read_b64_tr_b16 a[30:31], v103 offset:16904\n\t"
        "v_mfma_f32_16x16x32_bf16 v[16:19], v[56:59], a[8:11], 0\n\t"
        "ds_read_b64_tr_b16 a[32:33], v104 offset:16384\n\t"
        "ds_read_b64_tr_b16 a[34:35], v104 offset:16640\n\t"
        "v_mfma_f32_16x16x32_bf16 v[20:23], a[0:3], v[60:63], 0\n\t"
        "s_waitcnt lgkmcnt(5)\n\t"
        "ds_read_b64_tr_b16 a[36:37], v103 offset:20480\n\t"
        "v_mfma_f32_16x16x32_bf16 v[24:27], a[4:7], v[60:63], 0\n\t"
        "ds_read_b64_tr_b16 a[38:39], v103 offset:20992\n\t"
        "ds_read_b64_tr_b16 a[40:41], v103 offset:20488\n\t"
        "v_mfma_f32_16x16x32_bf16 v[28:31], v[60:63], a[8:11], 0\n\t"
        "ds_read_b64_tr_b16 a[42:43], v103 offset:21000\n\t"
        "ds_read_b64_tr_b16 a[44:45], v104 offset:18432\n\t"
        "v_mfma_f32_16x16x32_bf16 v[8:11], a[12:15], v[64:67], v[8:11]\n\t"
        "ds_read_b64_tr_b16 a[46:47], v104 offset:18688\n\t"
        "ds_read_b128 v[72:75], v105 offset:8192\n\t"
        "v_mfma_f32_16x16x32_bf16 v[12:15], a[16:19], v[64:67], v[12:15]\n\t"
        "ds_read_b128 v[76:79], v105 offset:10240\n\t"
        "ds_read_b128 v[80:83], v106 offset:8192\n\t"
        "v_mfma_f32_16x16x32_bf16 v[16:19], v[64:67], a[20:23], v[16:19]\n\t"
        "ds_read_b128 v[84:87], v106 offset:10240\n\t"
        "v_mfma_f32_16x16x32_bf16 v[20:23], a[12:15], v[68:71], v[20:23]\n\t"
        "v_mfma_f32_16x16x32_bf16 v[24:27], a[16:19], v[68:71], v[24:27]\n\t"
        "v_mfma_f32_16x16x32_bf16 v[28:31], v[68:71], a[20:23], v[28:31]\n\t"
        "s_mov_b32 %[t0], 2621440\n\t"
        "buffer_load_dwordx4 a[252:255], v101, %[sq], %[t0] offen\n\t"
        "buffer_load_dwordx4 v[132:135], v112, %[sv], %[t0] offen\n\t"
        "s_mov_b32 %[t0], 2883584\n\t"
        "buffer_load_dwordx4 v[128:131], v101, %[sq], %[t0] offen\n\t"
        "s_waitcnt vmcnt(17)\n\t"
        "ds_write_b128 v102, a[216:219] offset:0\n\t"
        "ds_write_b128 v102, a[220:223] offset:4096\n\t"
        "ds_write_b128 v116, a[224:227] offset:8192\n\t"
        "s_waitcnt lgkmcnt(0)\n\t"
        "s_barrier\n\t"
        "v_mfma_f32_16x16x32_bf16 v[8:11], a[24:27], v[72:75], v[8:11]\n\t"
        "ds_read_b64_tr_b16 a[48:49], v103 offset:0\n\t"
        "ds_read_b64_tr_b16 a[50:51], v103 offset:512\n\t"
        "v_mfma_f32_16x16x32_bf16 v[12:15], a[28:31], v[72:75], v[12:15]\n\t"
        "ds_read_b64_tr_b16 a[52:53], v103 offset:8\n\t"
        "ds_read_b64_tr_b16 a[54:55], v103 offset:520\n\t"
        "v_mfma_f32_16x16x32_bf16 v[16:19], v[72:75], a[32:35], v[16:19]\n\t"
        "ds_read_b64_tr_b16 a[56:57], v104 offset:0\n\t"
        "ds_read_b64_tr_b16 a[58:59], v104 offset:256\n\t"
        "v_mfma_f32_16x16x32_bf16 v[20:23], a[24:27], v[76:79], v[20:23]\n\t"
        "s_waitcnt lgkmcnt(5)\n\t"
        "ds_read_b64_tr_b16 a[60:61], v103 offset:4096\n\t"
        "v_mfma_f32_16x16x32_bf16 v[24:27], a[28:31], v[76:79], v[24:27]\n\t"
        "ds_read_b64_tr_b16 a[62:63], v103 offset:4608\n\t"
        "ds_read_b64_tr_b16 a[64:65], v103 offset:4104\n\t"
        "v_mfma_f32_16x16x32_bf16 v[28:31], v[76:79], a[32:35], v[28:31]\n\t"
        "ds_read_b64_tr_b16 a[66:67], v103 offset:4616\n\t"
        "ds_read_b64_tr_b16 a[68:69], v104 offset:2048\n\t"
        "v_mfma_f32_16x16x32_bf16 v[8:11], a[36:39], v[80:83], v[8:11]\n\t"
        "ds_read_b64_tr_b16 a[70:71], v104 offset:2304\n\t"
        "ds_read_b128 v[56:59], v105 offset:16384\n\t"
        "v_mfma_f32_16x16x32_bf16 v[12:15], a[40:43], v[80:83], v[12:15]\n\t"
        "ds_read_b128 v[60:63], v105 offset:18432\n\t"
        "ds_read_b128 v[64:67], v106 offset:16384\n\t"
        "v_mfma_f32_16x16x32_bf16 v[16:19], v[80:83], a[44:47], v[16:19]\n\t"
        "ds_read_b128 v[68:71], v106 offset:18432\n\t"
        "v_mfma_f32_16x16x32_bf16 v[20:23], a[36:39], v[84:87], v[20:23]\n\t"
        "v_mfma_f32_16x16x32_bf16 v[24:27], a[40:43], v[84:87], v[24:27]\n\t"
        "v_mfma_f32_16x16x32_bf16 v[28:31], v[84:87], a[44:47], v[28:31]\n\t"
        "s_mov_b32 %[t0], 3145728\n\t"
        "buffer_load_dwordx4 v[136:139], v101, %[sq], %[t0] offen\n\t"
        "buffer_load_dwordx4 v[144:147], v112, %[sv], %[t0] offen\n\t"
        "s_mov_b32 %[t0], 3407872\n\t"
        "buffer_load_dwordx4 v[140:143], v101, %[sq], %[t0] offen\n\t"
        "s_waitcnt vmcnt(17)\n\t"
        "ds_write_b128 v102, a[228:231] offset:16384\n\t"
        "ds_write_b128 v102, a[232:235] offset:20480\n\t"
        "ds_write_b128 v116, a[236:239] offset:24576\n\t"
        "s_waitcnt lgkmcnt(0)\n\t"
        "s_barrier\n\t"
        "v_mfma_f32_16x16x32_bf16 v[8:11], a[48:51], v[56:59], v[8:11]\n\t"
        "ds_read_b64_tr_b16 a[72:73], v103 offset:16384\n\t"
        "ds_read_b64_tr_b16 a[74:75], v103 offset:16896\n\t"
        "v_mfma_f32_16x16x32_bf16 v[12:15], a[52:55], v[56:59], v[12:15]\n\t"
        "ds_read_b64_tr_b16 a[76:77], v103 offset:16392\n\t"
        "ds_read_b64_tr_b16 a[78:79], v103 offset:16904\n\t"
        "v_mfma_f32_16x16x32_bf16 v[16:19], v[56:59], a[56:59], v[16:19]\n\t"
        "ds_read_b64_tr_b16 a[80:81], v104 offset:16384\n\t"
        "ds_read_b64_tr_b16 a[82:83], v104 offset:16640\n\t"
        "v_mfma_f32_16x16x32_bf16 v[20:23], a[48:51], v[60:63], v[20:23]\n\t"
        "s_waitcnt lgkmcnt(5)\n\t"
        "ds_read_b64_tr_b16 a[84:85], v103 offset:20480\n\t"
        "v_mfma_f32_16x16x32_bf16 v[24:27], a[52:55], v[60:63], v[24:27]\n\t"
        "ds_read_b64_tr_b16 a[86:87], v103 offset:20992\n\t"
        "ds_read_b64_tr_b16 a[88:89], v103 offset:20488\n\t"
        "v_mfma_f32_16x16x32_bf16 v[28:31], v[60:63], a[56:59], v[28:31]\n\t"
        "ds_read_b64_tr_b16 a[90:91], v103 offset:21000\n\t"
        "ds_read_b64_tr_b16 a[92:93], v104 offset:18432\n\t"
        "v_mfma_f32_16x16x32_bf16 v[8:11], a[60:63], v[64:67], v[8:11]\n\t"
        "ds_read_b64_tr_b16 a[94:95], v104 offset:18688\n\t"
        "ds_read_b128 v[72:75], v105 offset:24576\n\t"
        "v_mfma_f32_16x16x32_bf16 v[12:15], a[64:67], v[64:67], v[12:15]\n\t"
        "ds_read_b128 v[76:79], v105 offset:26624\n\t"
        "ds_read_b128 v[80:83], v106 offset:24576\n\t"
        "v_mfma_f32_16x16x32_bf16 v[16:19], v[64:67], a[68:71], v[16:19]\n\t"
        "ds_read_b128 v[84:87], v106 offset:26624\n\t"
        "v_mfma_f32_16x16x32_bf16 v[20:23], a[60:63], v[68:71], v[20:23]\n\t"
        "v_mfma_f32_16x16x32_bf16 v[24:27], a[64:67], v[68:71], v[24:27]\n\t"
        "v_mfma_f32_16x16x32_bf16 v[28:31], v[68:71], a[68:71], v[28:31]\n\t"
        "s_mov_b32 %[t0], 3670016\n\t"
        "buffer_load_dwordx4 v[148:151], v101, %[sq], %[t0] offen\n\t"
        "buffer_load_dwordx4 v[156:159], v112, %[sv], %[t0] offen\n\t"
        "s_mov_b32 %[t0], 3932160\n\t"
        "buffer_load_dwordx4 v[152:155], v101, %[sq], %[t0] offen\n\t"
        "s_waitcnt vmcnt(9)\n\t"
        "ds_write_b128 v102, a[240:243] offset:0\n\t"
        "ds_write_b128 v102, a[244:247] offset:4096\n\t"
        "ds_write_b128 v116, a[248:251] offset:8192\n\t"
        "s_waitcnt lgkmcnt(0)\n\t"
        "s_barrier\n\t"
        "v_mfma_f32_16x16x32_bf16 v[8:11], a[72:75], v[72:75], v[8:11]\n\t"
        "ds_read_b64_tr_b16 a[96:97], v103 offset:0\n\t"
        "ds_read_b64_tr_b16 a[98:99], v103 offset:512\n\t"
        "v_mfma_f32_16x16x32_bf16 v[12:15], a[76:79], v[72:75], v[12:15]\n\t"
        "ds_read_b64_tr_b16 a[100:101], v103 offset:8\n\t"
        "ds_read_b64_tr_b16 a[102:103], v103 offset:520\n\t"
        "v_mfma_f32_16x16x32_bf16 v[16:19], v[72:75], a[80:83], v[16:19]\n\t"
        "ds_read_b64_tr_b16 a[104:105], v104 offset:0\n\t"
        "ds_read_b64_tr_b16 a[106:107], v104 offset:256\n\t"
        "v_mfma_f32_16x16x32_bf16 v[20:23], a[72:75], v[76:79], v[20:23]\n\t"
        "s_waitcnt lgkmcnt(5)\n\t"
        "ds_read_b64_tr_b16 a[108:109], v103 offset:4096\n\t"
        "v_mfma_f32_16x16x32_bf16 v[24:27], a[76:79], v[76:79], v[24:27]\n\t"
        "ds_read_b64_tr_b16 a[110:111], v103 offset:4608\n\t"
        "ds_read_b64_tr_b16 a[112:113], v103 offset:4104\n\t"
        "v_mfma_f32_16x16x32_bf16 v[28:31], v[76:79], a[80:83], v[28:31]\n\t"
        "ds_read_b64_tr_b16 a[114:115], v103 offset:4616\n\t"
        "ds_read_b64_tr_b16 a[116:117], v104 offset:2048\n\t"
        "v_mfma_f32_16x16x32_bf16 v[8:11], a[84:87], v[80:83], v[8:11]\n\t"
        "ds_read_b64_tr_b16 a[118:119], v104 offset:2304\n\t"
        "ds_read_b128 v[56:59], v105 offset:32768\n\t"
        "v_mfma_f32_16x16x32_bf16 v[12:15], a[88:91], v[80:83], v[12:15]\n\t"
        "ds_read_b128 v[60:63], v105 offset:34816\n\t"
        "ds_read_b128 v[64:67], v106 offset:32768\n\t"
        "v_mfma_f32_16x16x32_bf16 v[16:19], v[80:83], a[92:95], v[16:19]\n\t"
        "ds_read_b128 v[68:71], v106 offset:34816\n\t"
        "v_mfma_f32_16x16x32_bf16 v[20:23], a[84:87], v[84:87], v[20:23]\n\t"
        "v_mfma_f32_16x16x32_bf16 v[24:27], a[88:91], v[84:87], v[24:27]\n\t"
        "v_mfma_f32_16x16x32_bf16 v[28:31], v[84:87], a[92:95], v[28:31]\n\t"
        "s_add_u32 m0, %[ld], 65536\n\t"
        "s_add_u32 %[t0], %[xo], 1024\n\t"
        "buffer_load_dwordx4 v100, %[sx], %[t0] offen lds\n\t"
        "s_add_u32 m0, %[ld], 66560\n\t"
        "s_add_u32 %[t0], %[xo], 17408\n\t"
        "buffer_load_dwordx4 v100, %[sx], %[t0] offen lds\n\t"
        "s_add_u32 m0, %[ld], 73728\n\t"
        "s_add_u32 %[t0], %[xo], 1152\n\t"
        "buffer_load_dwordx4 v100, %[sx], %[t0] offen lds\n\t"
        "s_add_u32 m0, %[ld], 74752\n\t"
        "s_add_u32 %[t0], %[xo], 17536\n\t"
        "buffer_load_dwordx4 v100, %[sx], %[t0] offen lds\n\t"
        "s_add_u32 m0, %[ld], 81920\n\t"
        "s_add_u32 %[t0], %[xo], 1280\n\t"
        "buffer_load_dwordx4 v100, %[sx], %[t0] offen lds\n\t"
        "s_add_u32 m0, %[ld], 82944\n\t"
        "s_add_u32 %[t0], %[xo], 17664\n\t"
        "buffer_load_dwordx4 v100, %[sx], %[t0] offen lds\n\t"
        "s_add_u32 m0, %[ld], 90112\n\t"
        "s_add_u32 %[t0], %[xo], 1408\n\t"
        "buffer_load_dwordx4 v100, %[sx], %[t0] offen lds\n\t"
        "s_add_u32 m0, %[ld], 91136\n\t"
        "s_add_u32 %[t0], %[xo], 17792\n\t"
        "buffer_load_dwordx4 v100, %[sx], %[t0] offen lds\n\t"
        "s_mov_b32 %[t0], 4194304\n\t"
        "buffer_load_dwordx4 v[160:163], v101, %[sq], %[t0] offen\n\t"
        "buffer_load_dwordx4 v[168:171], v112, %[sv], %[t0] offen\n\t"
        "s_mov_b32 %[t0], 4456448\n\t"
        "buffer_load_dwordx4 v[164:167], v101, %[sq], %[t0] offen\n\t"
        "s_waitcnt vmcnt(17)\n\t"
        "ds_write_b128 v102, a[252:255] offset:16384\n\t"
        "ds_write_b128 v102, v[128:131] offset:20480\n\t"
        "ds_write_b128 v116, v[132:135] offset:24576\n\t"
        "s_waitcnt lgkmcnt(0)\n\t"
        "s_barrier\n\t"
        "v_mfma_f32_16x16x32_bf16 v[8:11], a[96:99], v[56:59], v[8:11]\n\t"
        "ds_read_b64_tr_b16 a[120:121], v103 offset:16384\n\t"
        "ds_read_b64_tr_b16 a[122:123], v103 offset:16896\n\t"
        "v_mfma_f32_16x16x32_bf16 v[12:15], a[100:103], v[56:59], v[12:15]\n\t"
        "ds_read_b64_tr_b16 a[124:125], v103 offset:16392\n\t"
        "ds_read_b64_tr_b16 a[126:127], v103 offset:16904\n\t"
        "v_mfma_f32_16x16x32_bf16 v[16:19], v[56:59], a[104:107], v[16:19]\n\t"
        "ds_read_b64_tr_b16 a[128:129], v104 offset:16384\n\t"
        "ds_read_b64_tr_b16 a[130:131], v104 offset:16640\n\t"
        "v_mfma_f32_16x16x32_bf16 v[20:23], a[96:99], v[60:63], v[20:23]\n\t"
        "s_waitcnt lgkmcnt(5)\n\t"
        "ds_read_b64_tr_b16 a[132:133], v103 offset:20480\n\t"
        "v_mfma_f32_16x16x32_bf16 v[24:27], a[100:103], v[60:63], v[24:27]\n\t"
        "ds_read_b64_tr_b16 a[134:135], v103 offset:20992\n\t"
        "ds_read_b64_tr_b16 a[136:137], v103 offset:20488\n\t"
        "v_mfma_f32_16x16x32_bf16 v[28:31], v[60:63], a[104:107], v[28:31]\n\t"
        "ds_read_b64_tr_b16 a[138:139], v103 offset:21000\n\t"
        "ds_read_b64_tr_b16 a[140:141], v104 offset:18432\n\t"
        "v_mfma_f32_16x16x32_bf16 v[8:11], a[108:111], v[64:67], v[8:11]\n\t"
        "ds_read_b64_tr_b16 a[142:143], v104 offset:18688\n\t"
        "ds_read_b128 v[72:75], v105 offset:40960\n\t"
        "v_mfma_f32_16x16x32_bf16 v[12:15], a[112:115], v[64:67], v[12:15]\n\t"
        "ds_read_b128 v[76:79], v105 offset:43008\n\t"
        "ds_read_b128 v[80:83], v106 offset:40960\n\t"
        "v_mfma_f32_16x16x32_bf16 v[16:19], v[64:67], a[116:119], v[16:19]\n\t"
        "ds_read_b128 v[84:87], v106 offset:43008\n\t"
        "v_mfma_f32_16x16x32_bf16 v[20:23], a[108:111], v[68:71], v[20:23]\n\t"
        "v_mfma_f32_16x16x32_bf16 v[24:27], a[112:115], v[68:71], v[24:27]\n\t"
        "v_mfma_f32_16x16x32_bf16 v[28:31], v[68:71], a[116:119], v[28:31]\n\t"
        "s_mov_b32 %[t0], 4718592\n\t"
        "buffer_load_dwordx4 v[172:175], v101, %[sq], %[t0] offen\n\t"
        "buffer_load_dwordx4 v[180:183], v112, %[sv], %[t0] offen\n\t"
        "s_mov_b32 %[t0], 4980736\n\t"
        "buffer_load_dwordx4 v[176:179], v101, %[sq], %[t0] offen\n\t"
        "s_waitcnt vmcnt(17)\n\t"
        "ds_write_b128 v102, v[136:139] offset:0\n\t"
        "ds_write_b128 v102, v[140:143] offset:4096\n\t"
        "ds_write_b128 v116, v[144:147] offset:8192\n\t"
        "s_waitcnt lgkmcnt(0)\n\t"
        "s_barrier\n\t"
        "v_mfma_f32_16x16x32_bf16 v[8:11], a[120:123], v[72:75], v[8:11]\n\t"
        "ds_read_b64_tr_b16 a[144:145], v103 offset:0\n\t"
        "ds_read_b64_tr_b16 a[146:147], v103 offset:512\n\t"
        "v_mfma_f32_16x16x32_bf16 v[12:15], a[124:127], v[72:75], v[12:15]\n\t"
        "ds_read_b64_tr_b16 a[148:149], v103 offset:8\n\t"
        "ds_read_b64_tr_b16 a[150:151], v103 offset:520\n\t"
        "v_mfma_f32_16x16x32_bf16 v[16:19], v[72:75], a[128:131], v[16:19]\n\t"
        "ds_read_b64_tr_b16 a[152:153], v104 offset:0\n\t"
        "ds_read_b64_tr_b16 a[154:155], v104 offset:256\n\t"
        "v_mfma_f32_16x16x32_bf16 v[20:23], a[120:123], v[76:79], v[20:23]\n\t"
        "s_waitcnt lgkmcnt(5)\n\t"
        "ds_read_b64_tr_b16 a[156:157], v103 offset:4096\n\t"
        "v_mfma_f32_16x16x32_bf16 v[24:27], a[124:127], v[76:79], v[24:27]\n\t"
        "ds_read_b64_tr_b16 a[158:159], v103 offset:4608\n\t"
        "ds_read_b64_tr_b16 a[160:161], v103 offset:4104\n\t"
        "v_mfma_f32_16x16x32_bf16 v[28:31], v[76:79], a[128:131], v[28:31]\n\t"
        "ds_read_b64_tr_b16 a[162:163], v103 offset:4616\n\t"
        "ds_read_b64_tr_b16 a[164:165], v104 offset:2048\n\t"
        "v_mfma_f32_16x16x32_bf16 v[8:11], a[132:135], v[80:83], v[8:11]\n\t"
        "ds_read_b64_tr_b16 a[166:167], v104 offset:2304\n\t"
        "ds_read_b128 v[56:59], v105 offset:49152\n\t"
        "v_mfma_f32_16x16x32_bf16 v[12:15], a[136:139], v[80:83], v[12:15]\n\t"
        "ds_read_b128 v[60:63], v105 offset:51200\n\t"
        "ds_read_b128 v[64:67], v106 offset:49152\n\t"
        "v_mfma_f32_16x16x32_bf16 v[16:19], v[80:83], a[140:143], v[16:19]\n\t"
        "ds_read_b128 v[68:71], v106 offset:51200\n\t"
        "v_mfma_f32_16x16x32_bf16 v[20:23], a[132:135], v[84:87], v[20:23]\n\t"
        "v_mfma_f32_16x16x32_bf16 v[24:27], a[136:139], v[84:87], v[24:27]\n\t"
        "v_mfma_f32_16x16x32_bf16 v[28:31], v[84:87], a[140:143], v[28:31]\n\t"
        "s_mov_b32 %[t0], 5242880\n\t"
        "buffer_load_dwordx4 v[184:187], v101, %[sq], %[t0] offen\n\t"
        "buffer_load_dwordx4 v[192:195], v112, %[sv], %[t0] offen\n\t"
        "s_mov_b32 %[t0], 5505024\n\t"
        "buffer_load_dwordx4 v[188:191], v101, %[sq], %[t0] offen\n\t"
        "s_waitcnt vmcnt(17)\n\t"
        "ds_write_b128 v102, v[148:151] offset:16384\n\t"
        "ds_write_b128 v102, v[152:155] offset:20480\n\t"
        "ds_write_b128 v116, v[156:159] offset:24576\n\t"
        "s_waitcnt lgkmcnt(0)\n\t"
        "s_barrier\n\t"
        "v_mfma_f32_16x16x32_bf16 v[8:11], a[144:147], v[56:59], v[8:11]\n\t"
        "ds_read_b64_tr_b16 a[168:169], v103 offset:16384\n\t"
        "ds_read_b64_tr_b16 a[170:171], v103 offset:16896\n\t"
        "v_mfma_f32_16x16x32_bf16 v[12:15], a[148:151], v[56:59], v[12:15]\n\t"
        "ds_read_b64_tr_b16 a[172:173], v103 offset:16392\n\t"
        "ds_read_b64_tr_b16 a[174:175], v103 offset:16904\n\t"
        "v_mfma_f32_16x16x32_bf16 v[16:19], v[56:59], a[152:155], v[16:19]\n\t"
        "ds_read_b64_tr_b16 a[176:177], v104 offset:16384\n\t"
        "ds_read_b64_tr_b16 a[178:179], v104 offset:16640\n\t"
        "v_mfma_f32_16x16x32_bf16 v[20:23], a[144:147], v[60:63], v[20:23]\n\t"
        "s_waitcnt lgkmcnt(5)\n\t"
        "ds_read_b64_tr_b16 a[180:181], v103 offset:20480\n\t"
        "v_mfma_f32_16x16x32_bf16 v[24:27], a[148:151], v[60:63], v[24:27]\n\t"
        "ds_read_b64_tr_b16 a[182:183], v103 offset:20992\n\t"
        "ds_read_b64_tr_b16 a[184:185], v103 offset:20488\n\t"
        "v_mfma_f32_16x16x32_bf16 v[28:31], v[60:63], a[152:155], v[28:31]\n\t"
        "ds_read_b64_tr_b16 a[186:187], v103 offset:21000\n\t"
        "ds_read_b64_tr_b16 a[188:189], v104 offset:18432\n\t"
        "v_mfma_f32_16x16x32_bf16 v[8:11], a[156:159], v[64:67], v[8:11]\n\t"
        "ds_read_b64_tr_b16 a[190:191], v104 offset:18688\n\t"
        "ds_read_b128 v[72:75], v105 offset:57344\n\t"
        "v_mfma_f32_16x16x32_bf16 v[12:15], a[160:163], v[64:67], v[12:15]\n\t"
        "ds_read_b128 v[76:79], v105 offset:59392\n\t"
        "ds_read_b128 v[80:83], v106 offset:57344\n\t"
        "v_mfma_f32_16x16x32_bf16 v[16:19], v[64:67], a[164:167], v[16:19]\n\t"
        "ds_read_b128 v[84:87], v106 offset:59392\n\t"
        "v_mfma_f32_16x16x32_bf16 v[20:23], a[156:159], v[68:71], v[20:23]\n\t"
        "v_mfma_f32_16x16x32_bf16 v[24:27], a[160:163], v[68:71], v[24:27]\n\t"
        "v_mfma_f32_16x16x32_bf16 v[28:31], v[68:71], a[164:167], v[28:31]\n\t"
        "s_mov_b32 %[t0], 5767168\n\t"
        "buffer_load_dwordx4 v[196:199], v101, %[sq], %[t0] offen\n\t"
        "buffer_load_dwordx4 v[204:207], v112, %[sv], %[t0] offen\n\t"
        "s_mov_b32 %[t0], 6029312\n\t"
        "buffer_load_dwordx4 v[200:203], v101, %[sq], %[t0] offen\n\t"
        "s_waitcnt vmcnt(9)\n\t"
        "ds_write_b128 v102, v[160:163] offset:0\n\t"
        "ds_write_b128 v102, v[164:167] offset:4096\n\t"
        "ds_write_b128 v116, v[168:171] offset:8192\n\t"
        "s_waitcnt lgkmcnt(0)\n\t"
        "s_barrier\n\t"
        "v_mfma_f32_16x16x32_bf16 v[8:11], a[168:171], v[72:75], v[8:11]\n\t"
        "ds_read_b64_tr_b16 a[192:193], v103 offset:0\n\t"
        "ds_read_b64_tr_b16 a[194:195], v103 offset:512\n\t"
        "v_mfma_f32_16x16x32_bf16 v[12:15], a[172:175], v[72:75], v[12:15]\n\t"
        "ds_read_b64_tr_b16 a[196:197], v103 offset:8\n\t"
        "ds_read_b64_tr_b16 a[198:199], v103 offset:520\n\t"
        "v_mfma_f32_16x16x32_bf16 v[16:19], v[72:75], a[176:179], v[16:19]\n\t"
        "ds_read_b64_tr_b16 a[200:201], v104 offset:0\n\t"
        "ds_read_b64_tr_b16 a[202:203], v104 offset:256\n\t"
        "v_mfma_f32_16x16x32_bf16 v[20:23], a[168:171], v[76:79], v[20:23]\n\t"
        "s_waitcnt lgkmcnt(5)\n\t"
        "ds_read_b64_tr_b16 a[204:205], v103 offset:4096\n\t"
        "v_mfma_f32_16x16x32_bf16 v[24:27], a[172:175], v[76:79], v[24:27]\n\t"
        "ds_read_b64_tr_b16 a[206:207], v103 offset:4608\n\t"
        "ds_read_b64_tr_b16 a[208:209], v103 offset:4104\n\t"
        "v_mfma_f32_16x16x32_bf16 v[28:31], v[76:79], a[176:179], v[28:31]\n\t"
        "ds_read_b64_tr_b16 a[210:211], v103 offset:4616\n\t"
        "ds_read_b64_tr_b16 a[212:213], v104 offset:2048\n\t"
        "v_mfma_f32_16x16x32_bf16 v[8:11], a[180:183], v[80:83], v[8:11]\n\t"
        "ds_read_b64_tr_b16 a[214:215], v104 offset:2304\n\t"
        "ds_read_b128 v[56:59], v107 offset:0\n\t"
        "v_mfma_f32_16x16x32_bf16 v[12:15], a[184:187], v[80:83], v[12:15]\n\t"
        "ds_read_b128 v[60:63], v107 offset:2048\n\t"
        "ds_read_b128 v[64:67], v108 offset:0\n\t"
        "v_mfma_f32_16x16x32_bf16 v[16:19], v[80:83], a[188:191], v[16:19]\n\t"
        "ds_read_b128 v[68:71], v108 offset:2048\n\t"
        "v_mfma_f32_16x16x32_bf16 v[20:23], a[180:183], v[84:87], v[20:23]\n\t"
        "v_mfma_f32_16x16x32_bf16 v[24:27], a[184:187], v[84:87], v[24:27]\n\t"
        "v_mfma_f32_16x16x32_bf16 v[28:31], v[84:87], a[188:191], v[28:31]\n\t"
        "s_add_u32 m0, %[ld], 98304\n\t"
        "s_add_u32 %[t0], %[xo], 1536\n\t"
        "buffer_load_dwordx4 v100, %[sx], %[t0] offen lds\n\t"
        "s_add_u32 m0, %[ld], 99328\n\t"
        "s_add_u32 %[t0], %[xo], 17920\n\t"
        "buffer_load_dwordx4 v100, %[sx], %[t0] offen lds\n\t"
        "s_add_u32 m0, %[ld], 106496\n\t"
        "s_add_u32 %[t0], %[xo], 1664\n\t"
        "buffer_load_dwordx4 v100, %[sx], %[t0] offen lds\n\t"
        "s_add_u32 m0, %[ld], 107520\n\t"
        "s_add_u32 %[t0], %[xo], 18048\n\t"
        "buffer_load_dwordx4 v100, %[sx], %[t0] offen lds\n\t"
        "s_add_u32 m0, %[ld], 114688\n\t"
        "s_add_u32 %[t0], %[xo], 1792\n\t"
        "buffer_load_dwordx4 v100, %[sx], %[t0] offen lds\n\t"
        "s_add_u32 m0, %[ld], 115712\n\t"
        "s_add_u32 %[t0], %[xo], 18176\n\t"
        "buffer_load_dwordx4 v100, %[sx], %[t0] offen lds\n\t"
        "s_add_u32 m0, %[ld], 122880\n\t"
        "s_add_u32 %[t0], %[xo], 1920\n\t"
        "buffer_load_dwordx4 v100, %[sx], %[t0] offen lds\n\t"
        "s_add_u32 m0, %[ld], 123904\n\t"
        "s_add_u32 %[t0], %[xo], 18304\n\t"
        "buffer_load_dwordx4 v100, %[sx], %[t0] offen lds\n\t"
        "s_mov_b32 %[t0], 6291456\n\t"
        "buffer_load_dwordx4 v[208:211], v101, %[sq], %[t0] offen\n\t"
        "buffer_load_dwordx4 v[216:219], v112, %[sv], %[t0] offen\n\t"
        "s_mov_b32 %[t0], 6553600\n\t"
        "buffer_load_dwordx4 v[212:215], v101, %[sq], %[t0] offen\n\t"
        "s_waitcnt vmcnt(17)\n\t"
        "ds_write_b128 v102, v[172:175] offset:16384\n\t"
        "ds_write_b128 v102, v[176:179] offset:20480\n\t"
        "ds_write_b128 v116, v[180:183] offset:24576\n\t"
        "s_waitcnt lgkmcnt(0)\n\t"
        "s_barrier\n\t"
        "v_mfma_f32_16x16x32_bf16 v[8:11], a[192:195], v[56:59], v[8:11]\n\t"
        "ds_read_b64_tr_b16 a[216:217], v103 offset:16384\n\t"
        "ds_read_b64_tr_b16 a[218:219], v103 offset:16896\n\t"
        "v_mfma_f32_16x16x32_bf16 v[12:15], a[196:199], v[56:59], v[12:15]\n\t"
        "ds_read_b64_tr_b16 a[220:221], v103 offset:16392\n\t"
        "ds_read_b64_tr_b16 a[222:223], v103 offset:16904\n\t"
        "v_mfma_f32_16x16x32_bf16 v[16:19], v[56:59], a[200:203], v[16:19]\n\t"
        "ds_read_b64_tr_b16 a[224:225], v104 offset:16384\n\t"
        "ds_read_b64_tr_b16 a[226:227], v104 offset:16640\n\t"
        "v_mfma_f32_16x16x32_bf16 v[20:23], a[192:195], v[60:63], v[20:23]\n\t"
        "s_waitcnt lgkmcnt(5)\n\t"
        "ds_read_b64_tr_b16 a[228:229], v103 offset:20480\n\t"
        "v_mfma_f32_16x16x32_bf16 v[24:27], a[196:199], v[60:63], v[24:27]\n\t"
        "ds_read_b64_tr_b16 a[230:231], v103 offset:20992\n\t"
        "ds_read_b64_tr_b16 a[232:233], v103 offset:20488\n\t"
        "v_mfma_f32_16x16x32_bf16 v[28:31], v[60:63], a[200:203], v[28:31]\n\t"
        "ds_read_b64_tr_b16 a[234:235], v103 offset:21000\n\t"
        "ds_read_b64_tr_b16 a[236:237], v104 offset:18432\n\t"
        "v_mfma_f32_16x16x32_bf16 v[8:11], a[204:207], v[64:67], v[8:11]\n\t"
        "ds_read_b64_tr_b16 a[238:239], v104 offset:18688\n\t"
        "ds_read_b128 v[72:75], v107 offset:8192\n\t"
        "v_mfma_f32_16x16x32_bf16 v[12:15], a[208:211], v[64:67], v[12:15]\n\t"
        "ds_read_b128 v[76:79], v107 offset:10240\n\t"
        "ds_read_b128 v[80:83], v108 offset:8192\n\t"
        "v_mfma_f32_16x16x32_bf16 v[16:19], v[64:67], a[212:215], v[16:19]\n\t"
        "ds_read_b128 v[84:87], v108 offset:10240\n\t"
        "v_mfma_f32_16x16x32_bf16 v[20:23], a[204:207], v[68:71], v[20:23]\n\t"
        "v_mfma_f32_16x16x32_bf16 v[24:27], a[208:211], v[68:71], v[24:27]\n\t"
        "v_mfma_f32_16x16x32_bf16 v[28:31], v[68:71], a[212:215], v[28:31]\n\t"
        "s_mov_b32 %[t0], 6815744\n\t"
        "buffer_load_dwordx4 v[220:223], v101, %[sq], %[t0] offen\n\t"
        "buffer_load_dwordx4 v[228:231], v112, %[sv], %[t0] offen\n\t"
        "s_mov_b32 %[t0], 7077888\n\t"
        "buffer_load_dwordx4 v[224:227], v101, %[sq], %[t0] offen\n\t"
        "s_waitcnt vmcnt(17)\n\t"
        "ds_write_b128 v102, v[184:187] offset:0\n\t"
        "ds_write_b128 v102, v[188:191] offset:4096\n\t"
        "ds_write_b128 v116, v[192:195] offset:8192\n\t"
        "s_waitcnt lgkmcnt(0)\n\t"
        "s_barrier\n\t"
        "v_mfma_f32_16x16x32_bf16 v[8:11], a[216:219], v[72:75], v[8:11]\n\t"
        "ds_read_b64_tr_b16 a[240:241], v103 offset:0\n\t"
        "ds_read_b64_tr_b16 a[242:243], v103 offset:512\n\t"
        "v_mfma_f32_16x16x32_bf16 v[12:15], a[220:223], v[72:75], v[12:15]\n\t"
        "ds_read_b64_tr_b16 a[244:245], v103 offset:8\n\t"
        "ds_read_b64_tr_b16 a[246:247], v103 offset:520\n\t"
        "v_mfma_f32_16x16x32_bf16 v[16:19], v[72:75], a[224:227], v[16:19]\n\t"
        "ds_read_b64_tr_b16 a[248:249], v104 offset:0\n\t"
        "ds_read_b64_tr_b16 a[250:251], v104 offset:256\n\t"
        "v_mfma_f32_16x16x32_bf16 v[20:23], a[216:219], v[76:79], v[20:23]\n\t"
        "s_waitcnt lgkmcnt(5)\n\t"
        "ds_read_b64_tr_b16 a[252:253], v103 offset:4096\n\t"
        "v_mfma_f32_16x16x32_bf16 v[24:27], a[220:223], v[76:79], v[24:27]\n\t"
        "ds_read_b64_tr_b16 a[254:255], v103 offset:4608\n\t"
        "ds_read_b64_tr_b16 v[128:129], v103 offset:4104\n\t"
        "v_mfma_f32_16x16x32_bf16 v[28:31], v[76:79], a[224:227], v[28:31]\n\t"
        "ds_read_b64_tr_b16 v[130:131], v103 offset:4616\n\t"
        "ds_read_b64_tr_b16 v[132:133], v104 offset:2048\n\t"
        "v_mfma_f32_16x16x32_bf16 v[8:11], a[228:231], v[80:83], v[8:11]\n\t"
        "ds_read_b64_tr_b16 v[134:135], v104 offset:2304\n\t"
        "ds_read_b128 v[56:59], v107 offset:16384\n\t"
        "v_mfma_f32_16x16x32_bf16 v[12:15], a[232:235], v[80:83], v[12:15]\n\t"
        "ds_read_b128 v[60:63], v107 offset:18432\n\t"
        "ds_read_b128 v[64:67], v108 offset:16384\n\t"
        "v_mfma_f32_16x16x32_bf16 v[16:19], v[80:83], a[236:239], v[16:19]\n\t"
        "ds_read_b128 v[68:71], v108 offset:18432\n\t"
        "v_mfma_f32_16x16x32_bf16 v[20:23], a[228:231], v[84:87], v[20:23]\n\t"
        "v_mfma_f32_16x16x32_bf16 v[24:27], a[232:235], v[84:87], v[24:27]\n\t"
        "v_mfma_f32_16x16x32_bf16 v[28:31], v[84:87], a[236:239], v[28:31]\n\t"
        "s_mov_b32 %[t0], 7340032\n\t"
        "buffer_load_dwordx4 v[232:235], v101, %[sq], %[t0] offen\n\t"
        "buffer_load_dwordx4 v[240:243], v112, %[sv], %[t0] offen\n\t"
        "s_mov_b32 %[t0], 7602176\n\t"
        "buffer_load_dwordx4 v[236:239], v101, %[sq], %[t0] offen\n\t"
        "s_waitcnt vmcnt(17)\n\t"
        "ds_write_b128 v102, v[196:199] offset:16384\n\t"
        "ds_write_b128 v102, v[200:203] offset:20480\n\t"
        "ds_write_b128 v116, v[204:207] offset:24576\n\t"
        "s_waitcnt lgkmcnt(0)\n\t"
        "s_barrier\n\t"
        "v_mfma_f32_16x16x32_bf16 v[8:11], a[240:243], v[56:59], v[8:11]\n\t"
        "ds_read_b64_tr_b16 v[136:137], v103 offset:16384\n\t"
        "ds_read_b64_tr_b16 v[138:139], v103 offset:16896\n\t"
        "v_mfma_f32_16x16x32_bf16 v[12:15], a[244:247], v[56:59], v[12:15]\n\t"
        "ds_read_b64_tr_b16 v[140:141], v103 offset:16392\n\t"
        "ds_read_b64_tr_b16 v[142:143], v103 offset:16904\n\t"
        "v_mfma_f32_16x16x32_bf16 v[16:19], v[56:59], a[248:251], v[16:19]\n\t"
        "ds_read_b64_tr_b16 v[144:145], v104 offset:16384\n\t"
        "ds_read_b64_tr_b16 v[146:147], v104 offset:16640\n\t"
        "v_mfma_f32_16x16x32_bf16 v[20:23], a[240:243], v[60:63], v[20:23]\n\t"
        "s_waitcnt lgkmcnt(5)\n\t"
        "ds_read_b64_tr_b16 v[148:149], v103 offset:20480\n\t"
        "v_mfma_f32_16x16x32_bf16 v[24:27], a[244:247], v[60:63], v[24:27]\n\t"
        "ds_read_b64_tr_b16 v[150:151], v103 offset:20992\n\t"
        "ds_read_b64_tr_b16 v[152:153], v103 offset:20488\n\t"
        "v_mfma_f32_16x16x32_bf16 v[28:31], v[60:63], a[248:251], v[28:31]\n\t"
        "ds_read_b64_tr_b16 v[154:155], v103 offset:21000\n\t"
        "ds_read_b64_tr_b16 v[156:157], v104 offset:18432\n\t"
        "v_mfma_f32_16x16x32_bf16 v[8:11], a[252:255], v[64:67], v[8:11]\n\t"
        "ds_read_b64_tr_b16 v[158:159], v104 offset:18688\n\t"
        "ds_read_b128 v[72:75], v107 offset:24576\n\t"
        "v_mfma_f32_16x16x32_bf16 v[12:15], v[128:131], v[64:67], v[12:15]\n\t"
        "ds_read_b128 v[76:79], v107 offset:26624\n\t"
        "ds_read_b128 v[80:83], v108 offset:24576\n\t"
        "v_mfma_f32_16x16x32_bf16 v[16:19], v[64:67], v[132:135], v[16:19]\n\t"
        "ds_read_b128 v[84:87], v108 offset:26624\n\t"
        "v_mfma_f32_16x16x32_bf16 v[20:23], a[252:255], v[68:71], v[20:23]\n\t"
        "v_mfma_f32_16x16x32_bf16 v[24:27], v[128:131], v[68:71], v[24:27]\n\t"
        "v_mfma_f32_16x16x32_bf16 v[28:31], v[68:71], v[132:135], v[28:31]\n\t"
        "s_mov_b32 %[t0], 7864320\n\t"
        "buffer_load_dwordx4 v[244:247], v101, %[sq], %[t0] offen\n\t"
        "buffer_load_dwordx4 v[252:255], v112, %[sv], %[t0] offen\n\t"
        "s_mov_b32 %[t0], 8126464\n\t"
        "buffer_load_dwordx4 v[248:251], v101, %[sq], %[t0] offen\n\t"
        "s_waitcnt vmcnt(9)\n\t"
        "ds_write_b128 v102, v[208:211] offset:0\n\t"
        "ds_write_b128 v102, v[212:215] offset:4096\n\t"
        "ds_write_b128 v116, v[216:219] offset:8192\n\t"
        "s_waitcnt lgkmcnt(0)\n\t"
        "s_barrier\n\t"
        "v_mfma_f32_16x16x32_bf16 v[8:11], v[136:139], v[72:75], v[8:11]\n\t"
        "ds_read_b64_tr_b16 v[160:161], v103 offset:0\n\t"
        "ds_read_b64_tr_b16 v[162:163], v103 offset:512\n\t"
        "v_mfma_f32_16x16x32_bf16 v[12:15], v[140:143], v[72:75], v[12:15]\n\t"
        "ds_read_b64_tr_b16 v[164:165], v103 offset:8\n\t"
        "ds_read_b64_tr_b16 v[166:167], v103 offset:520\n\t"
        "v_mfma_f32_16x16x32_bf16 v[16:19], v[72:75], v[144:147], v[16:19]\n\t"
        "ds_read_b64_tr_b16 v[168:169], v104 offset:0\n\t"
        "ds_read_b64_tr_b16 v[170:171], v104 offset:256\n\t"
        "v_mfma_f32_16x16x32_bf16 v[20:23], v[136:139], v[76:79], v[20:23]\n\t"
        "s_waitcnt lgkmcnt(5)\n\t"
        "ds_read_b64_tr_b16 v[172:173], v103 offset:4096\n\t"
        "v_mfma_f32_16x16x32_bf16 v[24:27], v[140:143], v[76:79], v[24:27]\n\t"
        "ds_read_b64_tr_b16 v[174:175], v103 offset:4608\n\t"
        "ds_read_b64_tr_b16 v[176:177], v103 offset:4104\n\t"
        "v_mfma_f32_16x16x32_bf16 v[28:31], v[76:79], v[144:147], v[28:31]\n\t"
        "ds_read_b64_tr_b16 v[178:179], v103 offset:4616\n\t"
        "ds_read_b64_tr_b16 v[180:181], v104 offset:2048\n\t"
        "v_mfma_f32_16x16x32_bf16 v[8:11], v[148:151], v[80:83], v[8:11]\n\t"
        "ds_read_b64_tr_b16 v[182:183], v104 offset:2304\n\t"
        "ds_read_b128 v[56:59], v107 offset:32768\n\t"
        "v_mfma_f32_16x16x32_bf16 v[12:15], v[152:155], v[80:83], v[12:15]\n\t"
        "ds_read_b128 v[60:63], v107 offset:34816\n\t"
        "ds_read_b128 v[64:67], v108 offset:32768\n\t"
        "v_mfma_f32_16x16x32_bf16 v[16:19], v[80:83], v[156:159], v[16:19]\n\t"
        "ds_read_b128 v[68:71], v108 offset:34816\n\t"
        "v_mfma_f32_16x16x32_bf16 v[20:23], v[148:151], v[84:87], v[20:23]\n\t"
        "v_mfma_f32_16x16x32_bf16 v[24:27], v[152:155], v[84:87], v[24:27]\n\t"
        "v_mfma_f32_16x16x32_bf16 v[28:31], v[84:87], v[156:159], v[28:31]\n\t"
        "s_mov_b32 %[xo], 0x20000\n\t"
        "s_add_u32 m0, %[ld], 0\n\t"
        "s_add_u32 %[t0], %[xo], 0\n\t"
        "buffer_load_dwordx4 v100, %[sx], %[t0] offen lds\n\t"
        "s_add_u32 m0, %[ld], 1024\n\t"
        "s_add_u32 %[t0], %[xo], 16384\n\t"
        "buffer_load_dwordx4 v100, %[sx], %[t0] offen lds\n\t"
        "s_add_u32 m0, %[ld], 8192\n\t"
        "s_add_u32 %[t0], %[xo], 128\n\t"
        "buffer_load_dwordx4 v100, %[sx], %[t0] offen lds\n\t"
        "s_add_u32 m0, %[ld], 9216\n\t"
        "s_add_u32 %[t0], %[xo], 16512\n\t"
        "buffer_load_dwordx4 v100, %[sx], %[t0] offen lds\n\t"
        "s_add_u32 m0, %[ld], 16384\n\t"
        "s_add_u32 %[t0], %[xo], 256\n\t"
        "buffer_load_dwordx4 v100, %[sx], %[t0] offen lds\n\t"
        "s_add_u32 m0, %[ld], 17408\n\t"
        "s_add_u32 %[t0], %[xo], 16640\n\t"
        "buffer_load_dwordx4 v100, %[sx], %[t0] offen lds\n\t"
        "s_add_u32 m0, %[ld], 24576\n\t"
        "s_add_u32 %[t0], %[xo], 384\n\t"
        "buffer_load_dwordx4 v100, %[sx], %[t0] offen lds\n\t"
        "s_add_u32 m0, %[ld], 25600\n\t"
        "s_add_u32 %[t0], %[xo], 16768\n\t"
        "buffer_load_dwordx4 v100, %[sx], %[t0] offen lds\n\t"
        "s_mov_b32 %[xo], 0\n\t"
        "s_waitcnt vmcnt(14)\n\t"
        "ds_write_b128 v102, v[220:223] offset:16384\n\t"
        "ds_write_b128 v102, v[224:227] offset:20480\n\t"
        "ds_write_b128 v116, v[228:231] offset:24576\n\t"
        "s_waitcnt lgkmcnt(0)\n\t"
        "s_barrier\n\t"
        "v_mfma_f32_16x16x32_bf16 v[8:11], v[160:163], v[56:59], v[8:11]\n\t"
        "ds_read_b64_tr_b16 v[184:185], v103 offset:16384\n\t"
        "ds_read_b64_tr_b16 v[186:187], v103 offset:16896\n\t"
        "v_mfma_f32_16x16x32_bf16 v[12:15], v[164:167], v[56:59], v[12:15]\n\t"
        "ds_read_b64_tr_b16 v[188:189], v103 offset:16392\n\t"
        "ds_read_b64_tr_b16 v[190:191], v103 offset:16904\n\t"
        "v_mfma_f32_16x16x32_bf16 v[16:19], v[56:59], v[168:171], v[16:19]\n\t"
        "ds_read_b64_tr_b16 v[192:193], v104 offset:16384\n\t"
        "ds_read_b64_tr_b16 v[194:195], v104 offset:16640\n\t"
        "v_mfma_f32_16x16x32_bf16 v[20:23], v[160:163], v[60:63], v[20:23]\n\t"
        "s_waitcnt lgkmcnt(5)\n\t"
        "ds_read_b64_tr_b16 v[196:197], v103 offset:20480\n\t"
        "v_mfma_f32_16x16x32_bf16 v[24:27], v[164:167], v[60:63], v[24:27]\n\t"
        "ds_read_b64_tr_b16 v[198:199], v103 offset:20992\n\t"
        "ds_read_b64_tr_b16 v[200:201], v103 offset:20488\n\t"
        "v_mfma_f32_16x16x32_bf16 v[28:31], v[60:63], v[168:171], v[28:31]\n\t"
        "ds_read_b64_tr_b16 v[202:203], v103 offset:21000\n\t"
        "ds_read_b64_tr_b16 v[204:205], v104 offset:18432\n\t"
        "v_mfma_f32_16x16x32_bf16 v[8:11], v[172:175], v[64:67], v[8:11]\n\t"
        "ds_read_b64_tr_b16 v[206:207], v104 offset:18688\n\t"
        "ds_read_b128 v[72:75], v107 offset:40960\n\t"
        "v_mfma_f32_16x16x32_bf16 v[12:15], v[176:179], v[64:67], v[12:15]\n\t"
        "ds_read_b128 v[76:79], v107 offset:43008\n\t"
        "ds_read_b128 v[80:83], v108 offset:40960\n\t"
        "v_mfma_f32_16x16x32_bf16 v[16:19], v[64:67], v[180:183], v[16:19]\n\t"
        "ds_read_b128 v[84:87], v108 offset:43008\n\t"
        "v_mfma_f32_16x16x32_bf16 v[20:23], v[172:175], v[68:71], v[20:23]\n\t"
        "v_mfma_f32_16x16x32_bf16 v[24:27], v[176:179], v[68:71], v[24:27]\n\t"
        "v_mfma_f32_16x16x32_bf16 v[28:31], v[68:71], v[180:183], v[28:31]\n\t"
        "s_waitcnt vmcnt(11)\n\t"
        "ds_write_b128 v102, v[232:235] offset:0\n\t"
        "ds_write_b128 v102, v[236:239] offset:4096\n\t"
        "ds_write_b128 v116, v[240:243] offset:8192\n\t"
        "s_waitcnt lgkmcnt(0)\n\t"
        "s_barrier\n\t"
        "v_mfma_f32_16x16x32_bf16 v[8:11], v[184:187], v[72:75], v[8:11]\n\t"
        "ds_read_b64_tr_b16 v[208:209], v103 offset:0\n\t"
        "ds_read_b64_tr_b16 v[210:211], v103 offset:512\n\t"
        "v_mfma_f32_16x16x32_bf16 v[12:15], v[188:191], v[72:75], v[12:15]\n\t"
        "ds_read_b64_tr_b16 v[212:213], v103 offset:8\n\t"
        "ds_read_b64_tr_b16 v[214:215], v103 offset:520\n\t"
        "v_mfma_f32_16x16x32_bf16 v[16:19], v[72:75], v[192:195], v[16:19]\n\t"
        "ds_read_b64_tr_b16 v[216:217], v104 offset:0\n\t"
        "ds_read_b64_tr_b16 v[218:219], v104 offset:256\n\t"
        "v_mfma_f32_16x16x32_bf16 v[20:23], v[184:187], v[76:79], v[20:23]\n\t"
        "s_waitcnt lgkmcnt(5)\n\t"
        "ds_read_b64_tr_b16 v[220:221], v103 offset:4096\n\t"
        "v_mfma_f32_16x16x32_bf16 v[24:27], v[188:191], v[76:79], v[24:27]\n\t"
        "ds_read_b64_tr_b16 v[222:223], v103 offset:4608\n\t"
        "ds_read_b64_tr_b16 v[224:225], v103 offset:4104\n\t"
        "v_mfma_f32_16x16x32_bf16 v[28:31], v[76:79], v[192:195], v[28:31]\n\t"
        "ds_read_b64_tr_b16 v[226:227], v103 offset:4616\n\t"
        "ds_read_b64_tr_b16 v[228:229], v104 offset:2048\n\t"
        "v_mfma_f32_16x16x32_bf16 v[8:11], v[196:199], v[80:83], v[8:11]\n\t"
        "ds_read_b64_tr_b16 v[230:231], v104 offset:2304\n\t"
        "ds_read_b128 v[56:59], v107 offset:49152\n\t"
        "v_mfma_f32_16x16x32_bf16 v[12:15], v[200:203], v[80:83], v[12:15]\n\t"
        "ds_read_b128 v[60:63], v107 offset:51200\n\t"
        "ds_read_b128 v[64:67], v108 offset:49152\n\t"
        "v_mfma_f32_16x16x32_bf16 v[16:19], v[80:83], v[204:207], v[16:19]\n\t"
        "ds_read_b128 v[68:71], v108 offset:51200\n\t"
        "v_mfma_f32_16x16x32_bf16 v[20:23], v[196:199], v[84:87], v[20:23]\n\t"
        "v_mfma_f32_16x16x32_bf16 v[24:27], v[200:203], v[84:87], v[24:27]\n\t"
        "v_mfma_f32_16x16x32_bf16 v[28:31], v[84:87], v[204:207], v[28:31]\n\t"
        "s_waitcnt vmcnt(8)\n\t"
        "ds_write_b128 v102, v[244:247] offset:16384\n\t"
        "ds_write_b128 v102, v[248:251] offset:20480\n\t"
        "ds_write_b128 v116, v[252:255] offset:24576\n\t"
        "s_waitcnt lgkmcnt(0)\n\t"
        "s_barrier\n\t"
        "v_mfma_f32_16x16x32_bf16 v[8:11], v[208:211], v[56:59], v[8:11]\n\t"
        "ds_read_b64_tr_b16 v[232:233], v103 offset:16384\n\t"
        "ds_read_b64_tr_b16 v[234:235], v103 offset:16896\n\t"
        "v_mfma_f32_16x16x32_bf16 v[12:15], v[212:215], v[56:59], v[12:15]\n\t"
        "ds_read_b64_tr_b16 v[236:237], v103 offset:16392\n\t"
        "ds_read_b64_tr_b16 v[238:239], v103 offset:16904\n\t"
        "v_mfma_f32_16x16x32_bf16 v[16:19], v[56:59], v[216:219], v[16:19]\n\t"
        "ds_read_b64_tr_b16 v[240:241], v104 offset:16384\n\t"
        "ds_read_b64_tr_b16 v[242:243], v104 offset:16640\n\t"
        "v_mfma_f32_16x16x32_bf16 v[20:23], v[208:211], v[60:63], v[20:23]\n\t"
        "s_waitcnt lgkmcnt(5)\n\t"
        "ds_read_b64_tr_b16 v[244:245], v103 offset:20480\n\t"
        "v_mfma_f32_16x16x32_bf16 v[24:27], v[212:215], v[60:63], v[24:27]\n\t"
        "ds_read_b64_tr_b16 v[246:247], v103 offset:20992\n\t"
        "ds_read_b64_tr_b16 v[248:249], v103 offset:20488\n\t"
        "v_mfma_f32_16x16x32_bf16 v[28:31], v[60:63], v[216:219], v[28:31]\n\t"
        "ds_read_b64_tr_b16 v[250:251], v103 offset:21000\n\t"
        "ds_read_b64_tr_b16 v[252:253], v104 offset:18432\n\t"
        "v_mfma_f32_16x16x32_bf16 v[8:11], v[220:223], v[64:67], v[8:11]\n\t"
        "ds_read_b64_tr_b16 v[254:255], v104 offset:18688\n\t"
        "ds_read_b128 v[72:75], v107 offset:57344\n\t"
        "v_mfma_f32_16x16x32_bf16 v[12:15], v[224:227], v[64:67], v[12:15]\n\t"
        "ds_read_b128 v[76:79], v107 offset:59392\n\t"
        "ds_read_b128 v[80:83], v108 offset:57344\n\t"
        "v_mfma_f32_16x16x32_bf16 v[16:19], v[64:67], v[228:231], v[16:19]\n\t"
        "ds_read_b128 v[84:87], v108 offset:59392\n\t"
        "v_mfma_f32_16x16x32_bf16 v[20:23], v[220:223], v[68:71], v[20:23]\n\t"
        "v_mfma_f32_16x16x32_bf16 v[24:27], v[224:227], v[68:71], v[24:27]\n\t"
        "v_mfma_f32_16x16x32_bf16 v[28:31], v[68:71], v[228:231], v[28:31]\n\t"
        "s_waitcnt vmcnt(0) lgkmcnt(0)\n\t"
        "s_barrier\n\t"
        "v_mfma_f32_16x16x32_bf16 v[8:11], v[232:235], v[72:75], v[8:11]\n\t"
        "v_mfma_f32_16x16x32_bf16 v[12:15], v[236:239], v[72:75], v[12:15]\n\t"
        "v_mfma_f32_16x16x32_bf16 v[16:19], v[72:75], v[240:243], v[16:19]\n\t"
        "v_mfma_f32_16x16x32_bf16 v[20:23], v[232:235], v[76:79], v[20:23]\n\t"
        "v_mfma_f32_16x16x32_bf16 v[24:27], v[236:239], v[76:79], v[24:27]\n\t"
        "v_mfma_f32_16x16x32_bf16 v[28:31], v[76:79], v[240:243], v[28:31]\n\t"
        "v_mfma_f32_16x16x32_bf16 v[8:11], v[244:247], v[80:83], v[8:11]\n\t"
        "v_mfma_f32_16x16x32_bf16 v[12:15], v[248:251], v[80:83], v[12:15]\n\t"
        "v_mfma_f32_16x16x32_bf16 v[16:19], v[80:83], v[252:255], v[16:19]\n\t"
        "v_mfma_f32_16x16x32_bf16 v[20:23], v[244:247], v[84:87], v[20:23]\n\t"
        "v_mfma_f32_16x16x32_bf16 v[24:27], v[248:251], v[84:87], v[24:27]\n\t"
        "v_mfma_f32_16x16x32_bf16 v[28:31], v[84:87], v[252:255], v[28:31]\n\t"
        "s_mov_b32 %[xo], 0x20000\n\t"
        "ds_read_b128 v[56:59], v105 offset:0\n\t"
        "ds_read_b128 v[60:63], v105 offset:2048\n\t"
        "ds_read_b128 v[64:67], v106 offset:0\n\t"
        "ds_read_b128 v[68:71], v106 offset:2048\n\t"
        "ds_read_b128 v[72:75], v105 offset:8192\n\t"
        "ds_read_b128 v[76:79], v105 offset:10240\n\t"
        "ds_read_b128 v[80:83], v106 offset:8192\n\t"
        "v_cvt_pk_bf16_f32 v88, v8, v9\n\t"
        "v_cvt_pk_bf16_f32 v89, v10, v11\n\t"
        "v_cvt_pk_bf16_f32 v90, v12, v13\n\t"
        "v_cvt_pk_bf16_f32 v91, v14, v15\n\t"
        "buffer_store_dwordx4 v[88:91], v109, %[sw], %[so] offen sc1\n\t"
        "v_cvt_pk_bf16_f32 v92, v20, v21\n\t"
        "v_cvt_pk_bf16_f32 v93, v22, v23\n\t"
        "v_cvt_pk_bf16_f32 v94, v24, v25\n\t"
        "v_cvt_pk_bf16_f32 v95, v26, v27\n\t"
        "buffer_store_dwordx4 v[92:95], v110, %[sw], %[so] offen sc1\n\t"
        "v_cvt_pk_bf16_f32 v96, v16, v17\n\t"
        "v_cvt_pk_bf16_f32 v97, v18, v19\n\t"
        "v_cvt_pk_bf16_f32 v98, v28, v29\n\t"
        "v_cvt_pk_bf16_f32 v99, v30, v31\n\t"
        "buffer_store_dwordx4 v[96:99], v111, %[sw], %[so] offen sc1\n\t"
        "s_mov_b32 %[so], 0xf0000000\n\t"
        "s_waitcnt vmcnt(0)\n\t"
        "v_mov_b32_e32 v117, %[cn]\n\t"
        "buffer_store_dword v117, v113, %[sw], 0 offen sc1\n\t"
        "s_add_u32 m0, %[ld], 32768\n\t"
        "s_add_u32 %[t0], %[xo], 512\n\t"
        "buffer_load_dwordx4 v100, %[sx], %[t0] offen lds\n\t"
        "s_add_u32 m0, %[ld], 33792\n\t"
        "s_add_u32 %[t0], %[xo], 16896\n\t"
        "buffer_load_dwordx4 v100, %[sx], %[t0] offen lds\n\t"
        "s_add_u32 m0, %[ld], 40960\n\t"
        "s_add_u32 %[t0], %[xo], 640\n\t"
        "buffer_load_dwordx4 v100, %[sx], %[t0] offen lds\n\t"
        "s_add_u32 m0, %[ld], 41984\n\t"
        "s_add_u32 %[t0], %[xo], 17024\n\t"
        "buffer_load_dwordx4 v100, %[sx], %[t0] offen lds\n\t"
        "s_add_u32 m0, %[ld], 49152\n\t"
        "s_add_u32 %[t0], %[xo], 768\n\t"
        "buffer_load_dwordx4 v100, %[sx], %[t0] offen lds\n\t"
        "s_add_u32 m0, %[ld], 50176\n\t"
        "s_add_u32 %[t0], %[xo], 17152\n\t"
        "buffer_load_dwordx4 v100, %[sx], %[t0] offen lds\n\t"
        "s_add_u32 m0, %[ld], 57344\n\t"
        "s_add_u32 %[t0], %[xo], 896\n\t"
        "buffer_load_dwordx4 v100, %[sx], %[t0] offen lds\n\t"
        "s_add_u32 m0, %[ld], 58368\n\t"
        "s_add_u32 %[t0], %[xo], 17280\n\t"
        "buffer_load_dwordx4 v100, %[sx], %[t0] offen lds\n\t"
        "s_add_u32 m0, %[ld], 65536\n\t"
        "s_add_u32 %[t0], %[xo], 1024\n\t"
        "buffer_load_dwordx4 v100, %[sx], %[t0] offen lds\n\t"
        "s_add_u32 m0, %[ld], 66560\n\t"
        "s_add_u32 %[t0], %[xo], 17408\n\t"
        "buffer_load_dwordx4 v100, %[sx], %[t0] offen lds\n\t"
        "s_add_u32 m0, %[ld], 73728\n\t"
        "s_add_u32 %[t0], %[xo], 1152\n\t"
        "buffer_load_dwordx4 v100, %[sx], %[t0] offen lds\n\t"
        "s_add_u32 m0, %[ld], 74752\n\t"
        "s_add_u32 %[t0], %[xo], 17536\n\t"
        "buffer_load_dwordx4 v100, %[sx], %[t0] offen lds\n\t"
        "s_add_u32 m0, %[ld], 81920\n\t"
        "s_add_u32 %[t0], %[xo], 1280\n\t"
        "buffer_load_dwordx4 v100, %[sx], %[t0] offen lds\n\t"
        "s_add_u32 m0, %[ld], 82944\n\t"
        "s_add_u32 %[t0], %[xo], 17664\n\t"
        "buffer_load_dwordx4 v100, %[sx], %[t0] offen lds\n\t"
        "s_add_u32 m0, %[ld], 90112\n\t"
        "s_add_u32 %[t0], %[xo], 1408\n\t"
        "buffer_load_dwordx4 v100, %[sx], %[t0] offen lds\n\t"
        "s_add_u32 m0, %[ld], 91136\n\t"
        "s_add_u32 %[t0], %[xo], 17792\n\t"
        "buffer_load_dwordx4 v100, %[sx], %[t0] offen lds\n\t"
        "s_add_u32 m0, %[ld], 98304\n\t"
        "s_add_u32 %[t0], %[xo], 1536\n\t"
        "buffer_load_dwordx4 v100, %[sx], %[t0] offen lds\n\t"
        "s_add_u32 m0, %[ld], 99328\n\t"
        "s_add_u32 %[t0], %[xo], 17920\n\t"
        "buffer_load_dwordx4 v100, %[sx], %[t0] offen lds\n\t"
        "s_add_u32 m0, %[ld], 106496\n\t"
        "s_add_u32 %[t0], %[xo], 1664\n\t"
        "buffer_load_dwordx4 v100, %[sx], %[t0] offen lds\n\t"
        "s_branch LG_mid%=\n\t"
        "LG_loop%=:\n\t"
        "s_waitcnt lgkmcnt(6)\n\t"
        "v_mfma_f32_16x16x32_bf16 v[8:11], a[0:3], v[56:59], 0\n\t"
        "v_mfma_f32_16x16x32_bf16 v[12:15], a[4:7], v[56:59], 0\n\t"
        "ds_read_b128 v[84:87], v106 offset:10240\n\t"
        "v_mfma_f32_16x16x32_bf16 v[16:19], v[56:59], a[8:11], 0\n\t"
        "s_add_u32 m0, %[ld], 107520\n\t"
        "s_add_u32 %[t0], %[xo], 18048\n\t"
        "s_waitcnt lgkmcnt(6)\n\t"
        "v_mfma_f32_16x16x32_bf16 v[20:23], a[0:3], v[60:63], 0\n\t"
        "buffer_load_dwordx4 v100, %[sx], %[t0] offen lds\n\t"
        "v_mfma_f32_16x16x32_bf16 v[24:27], a[4:7], v[60:63], 0\n\t"
        "ds_read_b128 v[56:59], v105 offset:16384\n\t"
        "v_mfma_f32_16x16x32_bf16 v[28:31], v[60:63], a[8:11], 0\n\t"
        "s_waitcnt lgkmcnt(6)\n\t"
        "v_mfma_f32_16x16x32_bf16 v[8:11], a[12:15], v[64:67], v[8:11]\n\t"
        "v_cvt_pk_bf16_f32 v88, v32, v33\n\t"
        "v_cvt_pk_bf16_f32 v89, v34, v35\n\t"
        "v_mfma_f32_16x16x32_bf16 v[12:15], a[16:19], v[64:67], v[12:15]\n\t"
        "ds_read_b128 v[60:63], v105 offset:18432\n\t"
        "v_cvt_pk_bf16_f32 v90, v36, v37\n\t"
        "v_mfma_f32_16x16x32_bf16 v[16:19], v[64:67], a[20:23], v[16:19]\n\t"
        "s_add_u32 m0, %[ld], 114688\n\t"
        "s_add_u32 %[t0], %[xo], 1792\n\t"
        "s_waitcnt lgkmcnt(6)\n\t"
        "v_mfma_f32_16x16x32_bf16 v[20:23], a[12:15], v[68:71], v[20:23]\n\t"
        "buffer_load_dwordx4 v100, %[sx], %[t0] offen lds\n\t"
        "v_mfma_f32_16x16x32_bf16 v[24:27], a[16:19], v[68:71], v[24:27]\n\t"
        "ds_read_b128 v[64:67], v106 offset:16384\n\t"
        "v_cvt_pk_bf16_f32 v91, v38, v39\n\t"
        "v_mfma_f32_16x16x32_bf16 v[28:31], v[68:71], a[20:23], v[28:31]\n\t"
        "buffer_store_dwordx4 v[88:91], v109, %[sw], %[so] offen sc1\n\t"
        "s_waitcnt lgkmcnt(6)\n\t"
        "v_mfma_f32_16x16x32_bf16 v[8:11], a[24:27], v[72:75], v[8:11]\n\t"
        "v_cvt_pk_bf16_f32 v92, v44, v45\n\t"
        "v_cvt_pk_bf16_f32 v93, v46, v47\n\t"
        "v_mfma_f32_16x16x32_bf16 v[12:15], a[28:31], v[72:75], v[12:15]\n\t"
        "ds_read_b128 v[68:71], v106 offset:18432\n\t"
        "v_cvt_pk_bf16_f32 v94, v48, v49\n\t"
        "v_mfma_f32_16x16x32_bf16 v[16:19], v[72:75], a[32:35], v[16:19]\n\t"
        "s_add_u32 m0, %[ld], 115712\n\t"
        "s_add_u32 %[t0], %[xo], 18176\n\t"
        "s_waitcnt lgkmcnt(6)\n\t"
        "v_mfma_f32_16x16x32_bf16 v[20:23], a[24:27], v[76:79], v[20:23]\n\t"
        "buffer_load_dwordx4 v100, %[sx], %[t0] offen lds\n\t"
        "v_mfma_f32_16x16x32_bf16 v[24:27], a[28:31], v[76:79], v[24:27]\n\t"
        "ds_read_b128 v[72:75], v105 offset:24576\n\t"
        "v_cvt_pk_bf16_f32 v95, v50, v51\n\t"
        "v_mfma_f32_16x16x32_bf16 v[28:31], v[76:79], a[32:35], v[28:31]\n\t"
        "buffer_store_dwordx4 v[92:95], v110, %[sw], %[so] offen sc1\n\t"
        "s_waitcnt lgkmcnt(6)\n\t"
        "v_mfma_f32_16x16x32_bf16 v[8:11], a[36:39], v[80:83], v[8:11]\n\t"
        "v_cvt_pk_bf16_f32 v96, v40, v41\n\t"
        "v_cvt_pk_bf16_f32 v97, v42, v43\n\t"
        "v_mfma_f32_16x16x32_bf16 v[12:15], a[40:43], v[80:83], v[12:15]\n\t"
        "ds_read_b128 v[76:79], v105 offset:26624\n\t"
        "v_cvt_pk_bf16_f32 v98, v52, v53\n\t"
        "v_mfma_f32_16x16x32_bf16 v[16:19], v[80:83], a[44:47], v[16:19]\n\t"
        "s_add_u32 m0, %[ld], 122880\n\t"
        "s_add_u32 %[t0], %[xo], 1920\n\t"
        "s_waitcnt lgkmcnt(6)\n\t"
        "v_mfma_f32_16x16x32_bf16 v[20:23], a[36:39], v[84:87], v[20:23]\n\t"
        "buffer_load_dwordx4 v100, %[sx], %[t0] offen lds\n\t"
        "v_mfma_f32_16x16x32_bf16 v[24:27], a[40:43], v[84:87], v[24:27]\n\t"
        "ds_read_b128 v[80:83], v106 offset:24576\n\t"
        "v_cvt_pk_bf16_f32 v99, v54, v55\n\t"
        "v_mfma_f32_16x16x32_bf16 v[28:31], v[84:87], a[44:47], v[28:31]\n\t"
        "buffer_store_dwordx4 v[96:99], v111, %[sw], %[so] offen sc1\n\t"
        "s_waitcnt lgkmcnt(6)\n\t"
        "v_mfma_f32_16x16x32_bf16 v[8:11], a[48:51], v[56:59], v[8:11]\n\t"
        "s_lshl_b32 %[so], %[cn], 19\n\t"
        "v_mfma_f32_16x16x32_bf16 v[12:15], a[52:55], v[56:59], v[12:15]\n\t"
        "ds_read_b128 v[84:87], v106 offset:26624\n\t"
        "v_mfma_f32_16x16x32_bf16 v[16:19], v[56:59], a[56:59], v[16:19]\n\t"
        "s_add_u32 m0, %[ld], 123904\n\t"
        "s_add_u32 %[t0], %[xo], 18304\n\t"
        "s_waitcnt lgkmcnt(6)\n\t"
        "v_mfma_f32_16x16x32_bf16 v[20:23], a[48:51], v[60:63], v[20:23]\n\t"
        "buffer_load_dwordx4 v100, %[sx], %[t0] offen lds\n\t"
        "v_mfma_f32_16x16x32_bf16 v[24:27], a[52:55], v[60:63], v[24:27]\n\t"
        "v_mfma_f32_16x16x32_bf16 v[28:31], v[60:63], a[56:59], v[28:31]\n\t"
        "s_waitcnt lgkmcnt(5)\n\t"
        "v_mfma_f32_16x16x32_bf16 v[8:11], a[60:63], v[64:67], v[8:11]\n\t"
        "s_waitcnt vmcnt(19) lgkmcnt(0)\n\t"
        "s_barrier\n\t"
        "s_add_u32 %[xo], %[xo], 0x20000\n\t"
        "v_mfma_f32_16x16x32_bf16 v[12:15], a[64:67], v[64:67], v[12:15]\n\t"
        "ds_read_b128 v[56:59], v105 offset:32768\n\t"
        "v_mfma_f32_16x16x32_bf16 v[16:19], v[64:67], a[68:71], v[16:19]\n\t"
        "ds_read_b128 v[60:63], v105 offset:34816\n\t"
        "s_add_u32 m0, %[ld], 0\n\t"
        "s_add_u32 %[t0], %[xo], 0\n\t"
        "v_mfma_f32_16x16x32_bf16 v[20:23], a[60:63], v[68:71], v[20:23]\n\t"
        "buffer_load_dwordx4 v100, %[sx], %[t0] offen lds\n\t"
        "v_mfma_f32_16x16x32_bf16 v[24:27], a[64:67], v[68:71], v[24:27]\n\t"
        "ds_read_b128 v[64:67], v106 offset:32768\n\t"
        "v_mfma_f32_16x16x32_bf16 v[28:31], v[68:71], a[68:71], v[28:31]\n\t"
        "v_mfma_f32_16x16x32_bf16 v[8:11], a[72:75], v[72:75], v[8:11]\n\t"
        "v_mfma_f32_16x16x32_bf16 v[12:15], a[76:79], v[72:75], v[12:15]\n\t"
        "ds_read_b128 v[68:71], v106 offset:34816\n\t"
        "v_mfma_f32_16x16x32_bf16 v[16:19], v[72:75], a[80:83], v[16:19]\n\t"
        "s_add_u32 m0, %[ld], 1024\n\t"
        "s_add_u32 %[t0], %[xo], 16384\n\t"
        "v_mfma_f32_16x16x32_bf16 v[20:23], a[72:75], v[76:79], v[20:23]\n\t"
        "buffer_load_dwordx4 v100, %[sx], %[t0] offen lds\n\t"
        "v_mfma_f32_16x16x32_bf16 v[24:27], a[76:79], v[76:79], v[24:27]\n\t"
        "ds_read_b128 v[72:75], v105 offset:40960\n\t"
        "v_mfma_f32_16x16x32_bf16 v[28:31], v[76:79], a[80:83], v[28:31]\n\t"
        "v_mfma_f32_16x16x32_bf16 v[8:11], a[84:87], v[80:83], v[8:11]\n\t"
        "v_mfma_f32_16x16x32_bf16 v[12:15], a[88:91], v[80:83], v[12:15]\n\t"
        "ds_read_b128 v[76:79], v105 offset:43008\n\t"
        "v_mfma_f32_16x16x32_bf16 v[16:19], v[80:83], a[92:95], v[16:19]\n\t"
        "s_add_u32 m0, %[ld], 8192\n\t"
        "s_add_u32 %[t0], %[xo], 128\n\t"
        "v_mfma_f32_16x16x32_bf16 v[20:23], a[84:87], v[84:87], v[20:23]\n\t"
        "buffer_load_dwordx4 v100, %[sx], %[t0] offen lds\n\t"
        "v_mfma_f32_16x16x32_bf16 v[24:27], a[88:91], v[84:87], v[24:27]\n\t"
        "ds_read_b128 v[80:83], v106 offset:40960\n\t"
        "v_mfma_f32_16x16x32_bf16 v[28:31], v[84:87], a[92:95], v[28:31]\n\t"
        "s_waitcnt lgkmcnt(6)\n\t"
        "v_mfma_f32_16x16x32_bf16 v[8:11], a[96:99], v[56:59], v[8:11]\n\t"
        "v_mfma_f32_16x16x32_bf16 v[12:15], a[100:103], v[56:59], v[12:15]\n\t"
        "ds_read_b128 v[84:87], v106 offset:43008\n\t"
        "v_mfma_f32_16x16x32_bf16 v[16:19], v[56:59], a[104:107], v[16:19]\n\t"
        "s_add_u32 m0, %[ld], 9216\n\t"
        "s_add_u32 %[t0], %[xo], 16512\n\t"
        "s_waitcnt lgkmcnt(6)\n\t"
        "v_mfma_f32_16x16x32_bf16 v[20:23], a[96:99], v[60:63], v[20:23]\n\t"
        "buffer_load_dwordx4 v100, %[sx], %[t0] offen lds\n\t"
        "v_mfma_f32_16x16x32_bf16 v[24:27], a[100:103], v[60:63], v[24:27]\n\t"
        "ds_read_b128 v[56:59], v105 offset:49152\n\t"
        "v_mfma_f32_16x16x32_bf16 v[28:31], v[60:63], a[104:107], v[28:31]\n\t"
        "s_waitcnt lgkmcnt(6)\n\t"
        "v_mfma_f32_16x16x32_bf16 v[8:11], a[108:111], v[64:67], v[8:11]\n\t"
        "v_mfma_f32_16x16x32_bf16 v[12:15], a[112:115], v[64:67], v[12:15]\n\t"
        "ds_read_b128 v[60:63], v105 offset:51200\n\t"
        "v_mfma_f32_16x16x32_bf16 v[16:19], v[64:67], a[116:119], v[16:19]\n\t"
        "s_add_u32 m0, %[ld], 16384\n\t"
        "s_add_u32 %[t0], %[xo], 256\n\t"
        "s_waitcnt lgkmcnt(6)\n\t"
        "v_mfma_f32_16x16x32_bf16 v[20:23], a[108:111], v[68:71], v[20:23]\n\t"
        "buffer_load_dwordx4 v100, %[sx], %[t0] offen lds\n\t"
        "v_mfma_f32_16x16x32_bf16 v[24:27], a[112:115], v[68:71], v[24:27]\n\t"
        "ds_read_b128 v[64:67], v106 offset:49152\n\t"
        "v_mfma_f32_16x16x32_bf16 v[28:31], v[68:71], a[116:119], v[28:31]\n\t"
        "s_waitcnt lgkmcnt(6)\n\t"
        "v_mfma_f32_16x16x32_bf16 v[8:11], a[120:123], v[72:75], v[8:11]\n\t"
        "v_mfma_f32_16x16x32_bf16 v[12:15], a[124:127], v[72:75], v[12:15]\n\t"
        "ds_read_b128 v[68:71], v106 offset:51200\n\t"
        "v_mfma_f32_16x16x32_bf16 v[16:19], v[72:75], a[128:131], v[16:19]\n\t"
        "s_add_u32 m0, %[ld], 17408\n\t"
        "s_add_u32 %[t0], %[xo], 16640\n\t"
        "s_waitcnt lgkmcnt(6)\n\t"
        "v_mfma_f32_16x16x32_bf16 v[20:23], a[120:123], v[76:79], v[20:23]\n\t"
        "buffer_load_dwordx4 v100, %[sx], %[t0] offen lds\n\t"
        "v_mfma_f32_16x16x32_bf16 v[24:27], a[124:127], v[76:79], v[24:27]\n\t"
        "ds_read_b128 v[72:75], v105 offset:57344\n\t"
        "v_mfma_f32_16x16x32_bf16 v[28:31], v[76:79], a[128:131], v[28:31]\n\t"
        "s_waitcnt lgkmcnt(6)\n\t"
        "v_mfma_f32_16x16x32_bf16 v[8:11], a[132:135], v[80:83], v[8:11]\n\t"
        "v_mfma_f32_16x16x32_bf16 v[12:15], a[136:139], v[80:83], v[12:15]\n\t"
        "ds_read_b128 v[76:79], v105 offset:59392\n\t"
        "v_mfma_f32_16x16x32_bf16 v[16:19], v[80:83], a[140:143], v[16:19]\n\t"
        "s_add_u32 m0, %[ld], 24576\n\t"
        "s_add_u32 %[t0], %[xo], 384\n\t"
        "s_waitcnt lgkmcnt(6)\n\t"
        "v_mfma_f32_16x16x32_bf16 v[20:23], a[132:135], v[84:87], v[20:23]\n\t"
        "buffer_load_dwordx4 v100, %[sx], %[t0] offen lds\n\t"
        "v_mfma_f32_16x16x32_bf16 v[24:27], a[136:139], v[84:87], v[24:27]\n\t"
        "ds_read_b128 v[80:83], v106 offset:57344\n\t"
        "v_mfma_f32_16x16x32_bf16 v[28:31], v[84:87], a[140:143], v[28:31]\n\t"
        "s_waitcnt lgkmcnt(6)\n\t"
        "v_mfma_f32_16x16x32_bf16 v[8:11], a[144:147], v[56:59], v[8:11]\n\t"
        "v_mfma_f32_16x16x32_bf16 v[12:15], a[148:151], v[56:59], v[12:15]\n\t"
        "ds_read_b128 v[84:87], v106 offset:59392\n\t"
        "v_mfma_f32_16x16x32_bf16 v[16:19], v[56:59], a[152:155], v[16:19]\n\t"
        "s_add_u32 m0, %[ld], 25600\n\t"
        "s_add_u32 %[t0], %[xo], 16768\n\t"
        "s_waitcnt lgkmcnt(6)\n\t"
        "v_mfma_f32_16x16x32_bf16 v[20:23], a[144:147], v[60:63], v[20:23]\n\t"
        "buffer_load_dwordx4 v100, %[sx], %[t0] offen lds\n\t"
        "v_mfma_f32_16x16x32_bf16 v[24:27], a[148:151], v[60:63], v[24:27]\n\t"
        "v_mfma_f32_16x16x32_bf16 v[28:31], v[60:63], a[152:155], v[28:31]\n\t"
        "s_waitcnt lgkmcnt(5)\n\t"
        "v_mfma_f32_16x16x32_bf16 v[8:11], a[156:159], v[64:67], v[8:11]\n\t"
        "s_waitcnt vmcnt(9) lgkmcnt(0)\n\t"
        "s_barrier\n\t"
        "v_mfma_f32_16x16x32_bf16 v[12:15], a[160:163], v[64:67], v[12:15]\n\t"
        "ds_read_b128 v[56:59], v107 offset:0\n\t"
        "v_mfma_f32_16x16x32_bf16 v[16:19], v[64:67], a[164:167], v[16:19]\n\t"
        "ds_read_b128 v[60:63], v107 offset:2048\n\t"
        "s_add_u32 m0, %[ld], 32768\n\t"
        "s_add_u32 %[t0], %[xo], 512\n\t"
        "v_mfma_f32_16x16x32_bf16 v[20:23], a[156:159], v[68:71], v[20:23]\n\t"
        "buffer_load_dwordx4 v100, %[sx], %[t0] offen lds\n\t"
        "v_mfma_f32_16x16x32_bf16 v[24:27], a[160:163], v[68:71], v[24:27]\n\t"
        "ds_read_b128 v[64:67], v108 offset:0\n\t"
        "v_mfma_f32_16x16x32_bf16 v[28:31], v[68:71], a[164:167], v[28:31]\n\t"
        "v_mfma_f32_16x16x32_bf16 v[8:11], a[168:171], v[72:75], v[8:11]\n\t"
        "v_mfma_f32_16x16x32_bf16 v[12:15], a[172:175], v[72:75], v[12:15]\n\t"
        "ds_read_b128 v[68:71], v108 offset:2048\n\t"
        "v_mov_b32_e32 v117, %[cn]\n\t"
        "buffer_store_dword v117, v113, %[sw], 0 offen sc1\n\t"
        "s_add_u32 %[cn], %[cn], 1\n\t"
        "v_mfma_f32_16x16x32_bf16 v[16:19], v[72:75], a[176:179], v[16:19]\n\t"
        "s_add_u32 m0, %[ld], 33792\n\t"
        "s_add_u32 %[t0], %[xo], 16896\n\t"
        "v_mfma_f32_16x16x32_bf16 v[20:23], a[168:171], v[76:79], v[20:23]\n\t"
        "buffer_load_dwordx4 v100, %[sx], %[t0] offen lds\n\t"
        "v_mfma_f32_16x16x32_bf16 v[24:27], a[172:175], v[76:79], v[24:27]\n\t"
        "ds_read_b128 v[72:75], v107 offset:8192\n\t"
        "v_mfma_f32_16x16x32_bf16 v[28:31], v[76:79], a[176:179], v[28:31]\n\t"
        "v_mfma_f32_16x16x32_bf16 v[8:11], a[180:183], v[80:83], v[8:11]\n\t"
        "v_mfma_f32_16x16x32_bf16 v[12:15], a[184:187], v[80:83], v[12:15]\n\t"
        "ds_read_b128 v[76:79], v107 offset:10240\n\t"
        "v_mfma_f32_16x16x32_bf16 v[16:19], v[80:83], a[188:191], v[16:19]\n\t"
        "s_add_u32 m0, %[ld], 40960\n\t"
        "s_add_u32 %[t0], %[xo], 640\n\t"
        "v_mfma_f32_16x16x32_bf16 v[20:23], a[180:183], v[84:87], v[20:23]\n\t"
        "buffer_load_dwordx4 v100, %[sx], %[t0] offen lds\n\t"
        "v_mfma_f32_16x16x32_bf16 v[24:27], a[184:187], v[84:87], v[24:27]\n\t"
        "ds_read_b128 v[80:83], v108 offset:8192\n\t"
        "v_mfma_f32_16x16x32_bf16 v[28:31], v[84:87], a[188:191], v[28:31]\n\t"
        "s_waitcnt lgkmcnt(6)\n\t"
        "v_mfma_f32_16x16x32_bf16 v[8:11], a[192:195], v[56:59], v[8:11]\n\t"
        "v_mfma_f32_16x16x32_bf16 v[12:15], a[196:199], v[56:59], v[12:15]\n\t"
        "ds_read_b128 v[84:87], v108 offset:10240\n\t"
        "v_mfma_f32_16x16x32_bf16 v[16:19], v[56:59], a[200:203], v[16:19]\n\t"
        "s_add_u32 m0, %[ld], 41984\n\t"
        "s_add_u32 %[t0], %[xo], 17024\n\t"
        "s_waitcnt lgkmcnt(6)\n\t"
        "v_mfma_f32_16x16x32_bf16 v[20:23], a[192:195], v[60:63], v[20:23]\n\t"
        "buffer_load_dwordx4 v100, %[sx], %[t0] offen lds\n\t"
        "v_mfma_f32_16x16x32_bf16 v[24:27], a[196:199], v[60:63], v[24:27]\n\t"
        "ds_read_b128 v[56:59], v107 offset:16384\n\t"
        "v_mfma_f32_16x16x32_bf16 v[28:31], v[60:63], a[200:203], v[28:31]\n\t"
        "s_waitcnt lgkmcnt(6)\n\t"
        "v_mfma_f32_16x16x32_bf16 v[8:11], a[204:207], v[64:67], v[8:11]\n\t"
        "v_mfma_f32_16x16x32_bf16 v[12:15], a[208:211], v[64:67], v[12:15]\n\t"
        "ds_read_b128 v[60:63], v107 offset:18432\n\t"
        "v_mfma_f32_16x16x32_bf16 v[16:19], v[64:67], a[212:215], v[16:19]\n\t"
        "s_add_u32 m0, %[ld], 49152\n\t"
        "s_add_u32 %[t0], %[xo], 768\n\t"
        "s_waitcnt lgkmcnt(6)\n\t"
        "v_mfma_f32_16x16x32_bf16 v[20:23], a[204:207], v[68:71], v[20:23]\n\t"
        "buffer_load_dwordx4 v100, %[sx], %[t0] offen lds\n\t"
        "v_mfma_f32_16x16x32_bf16 v[24:27], a[208:211], v[68:71], v[24:27]\n\t"
        "ds_read_b128 v[64:67], v108 offset:16384\n\t"
        "v_mfma_f32_16x16x32_bf16 v[28:31], v[68:71], a[212:215], v[28:31]\n\t"
        "s_waitcnt lgkmcnt(6)\n\t"
        "v_mfma_f32_16x16x32_bf16 v[8:11], a[216:219], v[72:75], v[8:11]\n\t"
        "v_mfma_f32_16x16x32_bf16 v[12:15], a[220:223], v[72:75], v[12:15]\n\t"
        "ds_read_b128 v[68:71], v108 offset:18432\n\t"
        "v_mfma_f32_16x16x32_bf16 v[16:19], v[72:75], a[224:227], v[16:19]\n\t"
        "s_add_u32 m0, %[ld], 50176\n\t"
        "s_add_u32 %[t0], %[xo], 17152\n\t"
        "s_waitcnt lgkmcnt(6)\n\t"
        "v_mfma_f32_16x16x32_bf16 v[20:23], a[216:219], v[76:79], v[20:23]\n\t"
        "buffer_load_dwordx4 v100, %[sx], %[t0] offen lds\n\t"
        "v_mfma_f32_16x16x32_bf16 v[24:27], a[220:223], v[76:79], v[24:27]\n\t"
        "ds_read_b128 v[72:75], v107 offset:24576\n\t"
        "v_mfma_f32_16x16x32_bf16 v[28:31], v[76:79], a[224:227], v[28:31]\n\t"
        "s_waitcnt lgkmcnt(6)\n\t"
        "v_mfma_f32_16x16x32_bf16 v[8:11], a[228:231], v[80:83], v[8:11]\n\t"
        "v_mfma_f32_16x16x32_bf16 v[12:15], a[232:235], v[80:83], v[12:15]\n\t"
        "ds_read_b128 v[76:79], v107 offset:26624\n\t"
        "v_mfma_f32_16x16x32_bf16 v[16:19], v[80:83], a[236:239], v[16:19]\n\t"
        "s_add_u32 m0, %[ld], 57344\n\t"
        "s_add_u32 %[t0], %[xo], 896\n\t"
        "s_waitcnt lgkmcnt(6)\n\t"
        "v_mfma_f32_16x16x32_bf16 v[20:23], a[228:231], v[84:87], v[20:23]\n\t"
        "buffer_load_dwordx4 v100, %[sx], %[t0] offen lds\n\t"
        "v_mfma_f32_16x16x32_bf16 v[24:27], a[232:235], v[84:87], v[24:27]\n\t"
        "ds_read_b128 v[80:83], v108 offset:24576\n\t"
        "v_mfma_f32_16x16x32_bf16 v[28:31], v[84:87], a[236:239], v[28:31]\n\t"
        "s_waitcnt lgkmcnt(6)\n\t"
        "v_mfma_f32_16x16x32_bf16 v[8:11], a[240:243], v[56:59], v[8:11]\n\t"
        "v_mfma_f32_16x16x32_bf16 v[12:15], a[244:247], v[56:59], v[12:15]\n\t"
        "ds_read_b128 v[84:87], v108 offset:26624\n\t"
        "v_mfma_f32_16x16x32_bf16 v[16:19], v[56:59], a[248:251], v[16:19]\n\t"
        "s_add_u32 m0, %[ld], 58368\n\t"
        "s_add_u32 %[t0], %[xo], 17280\n\t"
        "s_waitcnt lgkmcnt(6)\n\t"
        "v_mfma_f32_16x16x32_bf16 v[20:23], a[240:243], v[60:63], v[20:23]\n\t"
        "buffer_load_dwordx4 v100, %[sx], %[t0] offen lds\n\t"
        "v_mfma_f32_16x16x32_bf16 v[24:27], a[244:247], v[60:63], v[24:27]\n\t"
        "v_mfma_f32_16x16x32_bf16 v[28:31], v[60:63], a[248:251], v[28:31]\n\t"
        "s_waitcnt lgkmcnt(5)\n\t"
        "v_mfma_f32_16x16x32_bf16 v[8:11], a[252:255], v[64:67], v[8:11]\n\t"
        "s_waitcnt vmcnt(17) lgkmcnt(0)\n\t"
        "s_barrier\n\t"
        "v_mfma_f32_16x16x32_bf16 v[12:15], v[128:131], v[64:67], v[12:15]\n\t"
        "ds_read_b128 v[56:59], v107 offset:32768\n\t"
        "v_mfma_f32_16x16x32_bf16 v[16:19], v[64:67], v[132:135], v[16:19]\n\t"
        "ds_read_b128 v[60:63], v107 offset:34816\n\t"
        "s_add_u32 m0, %[ld], 65536\n\t"
        "s_add_u32 %[t0], %[xo], 1024\n\t"
        "v_mfma_f32_16x16x32_bf16 v[20:23], a[252:255], v[68:71], v[20:23]\n\t"
        "buffer_load_dwordx4 v100, %[sx], %[t0] offen lds\n\t"
        "v_mfma_f32_16x16x32_bf16 v[24:27], v[128:131], v[68:71], v[24:27]\n\t"
        "ds_read_b128 v[64:67], v108 offset:32768\n\t"
        "v_mfma_f32_16x16x32_bf16 v[28:31], v[68:71], v[132:135], v[28:31]\n\t"
        "v_mfma_f32_16x16x32_bf16 v[8:11], v[136:139], v[72:75], v[8:11]\n\t"
        "v_mfma_f32_16x16x32_bf16 v[12:15], v[140:143], v[72:75], v[12:15]\n\t"
        "ds_read_b128 v[68:71], v108 offset:34816\n\t"
        "v_mfma_f32_16x16x32_bf16 v[16:19], v[72:75], v[144:147], v[16:19]\n\t"
        "s_add_u32 m0, %[ld], 66560\n\t"
        "s_add_u32 %[t0], %[xo], 17408\n\t"
        "v_mfma_f32_16x16x32_bf16 v[20:23], v[136:139], v[76:79], v[20:23]\n\t"
        "buffer_load_dwordx4 v100, %[sx], %[t0] offen lds\n\t"
        "v_mfma_f32_16x16x32_bf16 v[24:27], v[140:143], v[76:79], v[24:27]\n\t"
        "ds_read_b128 v[72:75], v107 offset:40960\n\t"
        "v_mfma_f32_16x16x32_bf16 v[28:31], v[76:79], v[144:147], v[28:31]\n\t"
        "v_mfma_f32_16x16x32_bf16 v[8:11], v[148:151], v[80:83], v[8:11]\n\t"
        "v_mfma_f32_16x16x32_bf16 v[12:15], v[152:155], v[80:83], v[12:15]\n\t"
        "ds_read_b128 v[76:79], v107 offset:43008\n\t"
        "v_mfma_f32_16x16x32_bf16 v[16:19], v[80:83], v[156:159], v[16:19]\n\t"
        "s_add_u32 m0, %[ld], 73728\n\t"
        "s_add_u32 %[t0], %[xo], 1152\n\t"
        "v_mfma_f32_16x16x32_bf16 v[20:23], v[148:151], v[84:87], v[20:23]\n\t"
        "buffer_load_dwordx4 v100, %[sx], %[t0] offen lds\n\t"
        "v_mfma_f32_16x16x32_bf16 v[24:27], v[152:155], v[84:87], v[24:27]\n\t"
        "ds_read_b128 v[80:83], v108 offset:40960\n\t"
        "v_mfma_f32_16x16x32_bf16 v[28:31], v[84:87], v[156:159], v[28:31]\n\t"
        "s_waitcnt lgkmcnt(6)\n\t"
        "v_mfma_f32_16x16x32_bf16 v[8:11], v[160:163], v[56:59], v[8:11]\n\t"
        "v_mfma_f32_16x16x32_bf16 v[12:15], v[164:167], v[56:59], v[12:15]\n\t"
        "ds_read_b128 v[84:87], v108 offset:43008\n\t"
        "v_mfma_f32_16x16x32_bf16 v[16:19], v[56:59], v[168:171], v[16:19]\n\t"
        "s_add_u32 m0, %[ld], 74752\n\t"
        "s_add_u32 %[t0], %[xo], 17536\n\t"
        "s_waitcnt lgkmcnt(6)\n\t"
        "v_mfma_f32_16x16x32_bf16 v[20:23], v[160:163], v[60:63], v[20:23]\n\t"
        "buffer_load_dwordx4 v100, %[sx], %[t0] offen lds\n\t"
        "v_mfma_f32_16x16x32_bf16 v[24:27], v[164:167], v[60:63], v[24:27]\n\t"
        "ds_read_b128 v[56:59], v107 offset:49152\n\t"
        "v_mfma_f32_16x16x32_bf16 v[28:31], v[60:63], v[168:171], v[28:31]\n\t"
        "s_waitcnt lgkmcnt(6)\n\t"
        "v_mfma_f32_16x16x32_bf16 v[8:11], v[172:175], v[64:67], v[8:11]\n\t"
        "v_mfma_f32_16x16x32_bf16 v[12:15], v[176:179], v[64:67], v[12:15]\n\t"
        "ds_read_b128 v[60:63], v107 offset:51200\n\t"
        "v_mfma_f32_16x16x32_bf16 v[16:19], v[64:67], v[180:183], v[16:19]\n\t"
        "s_add_u32 m0, %[ld], 81920\n\t"
        "s_add_u32 %[t0], %[xo], 1280\n\t"
        "s_waitcnt lgkmcnt(6)\n\t"
        "v_mfma_f32_16x16x32_bf16 v[20:23], v[172:175], v[68:71], v[20:23]\n\t"
        "buffer_load_dwordx4 v100, %[sx], %[t0] offen lds\n\t"
        "v_mfma_f32_16x16x32_bf16 v[24:27], v[176:179], v[68:71], v[24:27]\n\t"
        "ds_read_b128 v[64:67], v108 offset:49152\n\t"
        "v_mfma_f32_16x16x32_bf16 v[28:31], v[68:71], v[180:183], v[28:31]\n\t"
        "s_waitcnt lgkmcnt(6)\n\t"
        "v_mfma_f32_16x16x32_bf16 v[8:11], v[184:187], v[72:75], v[8:11]\n\t"
        "v_mfma_f32_16x16x32_bf16 v[12:15], v[188:191], v[72:75], v[12:15]\n\t"
        "ds_read_b128 v[68:71], v108 offset:51200\n\t"
        "v_mfma_f32_16x16x32_bf16 v[16:19], v[72:75], v[192:195], v[16:19]\n\t"
        "s_add_u32 m0, %[ld], 82944\n\t"
        "s_add_u32 %[t0], %[xo], 17664\n\t"
        "s_waitcnt lgkmcnt(6)\n\t"
        "v_mfma_f32_16x16x32_bf16 v[20:23], v[184:187], v[76:79], v[20:23]\n\t"
        "buffer_load_dwordx4 v100, %[sx], %[t0] offen lds\n\t"
        "v_mfma_f32_16x16x32_bf16 v[24:27], v[188:191], v[76:79], v[24:27]\n\t"
        "ds_read_b128 v[72:75], v107 offset:57344\n\t"
        "v_mfma_f32_16x16x32_bf16 v[28:31], v[76:79], v[192:195], v[28:31]\n\t"
        "s_waitcnt lgkmcnt(6)\n\t"
        "v_mfma_f32_16x16x32_bf16 v[8:11], v[196:199], v[80:83], v[8:11]\n\t"
        "v_mfma_f32_16x16x32_bf16 v[12:15], v[200:203], v[80:83], v[12:15]\n\t"
        "ds_read_b128 v[76:79], v107 offset:59392\n\t"
        "v_mfma_f32_16x16x32_bf16 v[16:19], v[80:83], v[204:207], v[16:19]\n\t"
        "s_add_u32 m0, %[ld], 90112\n\t"
        "s_add_u32 %[t0], %[xo], 1408\n\t"
        "s_waitcnt lgkmcnt(6)\n\t"
        "v_mfma_f32_16x16x32_bf16 v[20:23], v[196:199], v[84:87], v[20:23]\n\t"
        "buffer_load_dwordx4 v100, %[sx], %[t0] offen lds\n\t"
        "v_mfma_f32_16x16x32_bf16 v[24:27], v[200:203], v[84:87], v[24:27]\n\t"
        "ds_read_b128 v[80:83], v108 offset:57344\n\t"
        "v_mfma_f32_16x16x32_bf16 v[28:31], v[84:87], v[204:207], v[28:31]\n\t"
        "s_waitcnt lgkmcnt(6)\n\t"
        "v_mfma_f32_16x16x32_bf16 v[8:11], v[208:211], v[56:59], v[8:11]\n\t"
        "v_mfma_f32_16x16x32_bf16 v[12:15], v[212:215], v[56:59], v[12:15]\n\t"
        "ds_read_b128 v[84:87], v108 offset:59392\n\t"
        "v_mfma_f32_16x16x32_bf16 v[16:19], v[56:59], v[216:219], v[16:19]\n\t"
        "s_add_u32 m0, %[ld], 91136\n\t"
        "s_add_u32 %[t0], %[xo], 17792\n\t"
        "s_waitcnt lgkmcnt(6)\n\t"
        "v_mfma_f32_16x16x32_bf16 v[20:23], v[208:211], v[60:63], v[20:23]\n\t"
        "buffer_load_dwordx4 v100, %[sx], %[t0] offen lds\n\t"
        "v_mfma_f32_16x16x32_bf16 v[24:27], v[212:215], v[60:63], v[24:27]\n\t"
        "v_mfma_f32_16x16x32_bf16 v[28:31], v[60:63], v[216:219], v[28:31]\n\t"
        "s_waitcnt lgkmcnt(5)\n\t"
        "v_mfma_f32_16x16x32_bf16 v[8:11], v[220:223], v[64:67], v[8:11]\n\t"
        "s_waitcnt vmcnt(17) lgkmcnt(0)\n\t"
        "s_barrier\n\t"
        "v_mfma_f32_16x16x32_bf16 v[12:15], v[224:227], v[64:67], v[12:15]\n\t"
        "ds_read_b128 v[56:59], v105 offset:0\n\t"
        "v_mfma_f32_16x16x32_bf16 v[16:19], v[64:67], v[228:231], v[16:19]\n\t"
        "ds_read_b128 v[60:63], v105 offset:2048\n\t"
        "s_add_u32 m0, %[ld], 98304\n\t"
        "s_add_u32 %[t0], %[xo], 1536\n\t"
        "v_mfma_f32_16x16x32_bf16 v[20:23], v[220:223], v[68:71], v[20:23]\n\t"
        "buffer_load_dwordx4 v100, %[sx], %[t0] offen lds\n\t"
        "v_mfma_f32_16x16x32_bf16 v[24:27], v[224:227], v[68:71], v[24:27]\n\t"
        "ds_read_b128 v[64:67], v106 offset:0\n\t"
        "v_mfma_f32_16x16x32_bf16 v[28:31], v[68:71], v[228:231], v[28:31]\n\t"
        "v_mfma_f32_16x16x32_bf16 v[8:11], v[232:235], v[72:75], v[8:11]\n\t"
        "v_mfma_f32_16x16x32_bf16 v[12:15], v[236:239], v[72:75], v[12:15]\n\t"
        "ds_read_b128 v[68:71], v106 offset:2048\n\t"
        "v_mfma_f32_16x16x32_bf16 v[16:19], v[72:75], v[240:243], v[16:19]\n\t"
        "s_add_u32 m0, %[ld], 99328\n\t"
        "s_add_u32 %[t0], %[xo], 17920\n\t"
        "v_mfma_f32_16x16x32_bf16 v[20:23], v[232:235], v[76:79], v[20:23]\n\t"
        "buffer_load_dwordx4 v100, %[sx], %[t0] offen lds\n\t"
        "v_mfma_f32_16x16x32_bf16 v[24:27], v[236:239], v[76:79], v[24:27]\n\t"
        "ds_read_b128 v[72:75], v105 offset:8192\n\t"
        "v_mfma_f32_16x16x32_bf16 v[28:31], v[76:79], v[240:243], v[28:31]\n\t"
        "v_mfma_f32_16x16x32_bf16 v[8:11], v[244:247], v[80:83], v[8:11]\n\t"
        "v_mfma_f32_16x16x32_bf16 v[12:15], v[248:251], v[80:83], v[12:15]\n\t"
        "ds_read_b128 v[76:79], v105 offset:10240\n\t"
        "v_mfma_f32_16x16x32_bf16 v[16:19], v[80:83], v[252:255], v[16:19]\n\t"
        "s_add_u32 m0, %[ld], 106496\n\t"
        "s_add_u32 %[t0], %[xo], 1664\n\t"
        "v_mfma_f32_16x16x32_bf16 v[20:23], v[244:247], v[84:87], v[20:23]\n\t"
        "buffer_load_dwordx4 v100, %[sx], %[t0] offen lds\n\t"
        "v_mfma_f32_16x16x32_bf16 v[24:27], v[248:251], v[84:87], v[24:27]\n\t"
        "ds_read_b128 v[80:83], v106 offset:8192\n\t"
        "v_mfma_f32_16x16x32_bf16 v[28:31], v[84:87], v[252:255], v[28:31]\n\t"
        "LG_mid%=:\n\t"
        "s_waitcnt lgkmcnt(6)\n\t"
        "v_mfma_f32_16x16x32_bf16 v[32:35], a[0:3], v[56:59], 0\n\t"
        "v_mfma_f32_16x16x32_bf16 v[36:39], a[4:7], v[56:59], 0\n\t"
        "ds_read_b128 v[84:87], v106 offset:10240\n\t"
        "v_mfma_f32_16x16x32_bf16 v[40:43], v[56:59], a[8:11], 0\n\t"
        "s_add_u32 m0, %[ld], 107520\n\t"
        "s_add_u32 %[t0], %[xo], 18048\n\t"
        "s_waitcnt lgkmcnt(6)\n\t"
        "v_mfma_f32_16x16x32_bf16 v[44:47], a[0:3], v[60:63], 0\n\t"
        "buffer_load_dwordx4 v100, %[sx], %[t0] offen lds\n\t"
        "v_mfma_f32_16x16x32_bf16 v[48:51], a[4:7], v[60:63], 0\n\t"
        "ds_read_b128 v[56:59], v105 offset:16384\n\t"
        "v_mfma_f32_16x16x32_bf16 v[52:55], v[60:63], a[8:11], 0\n\t"
        "s_waitcnt lgkmcnt(6)\n\t"
        "v_mfma_f32_16x16x32_bf16 v[32:35], a[12:15], v[64:67], v[32:35]\n\t"
        "v_cvt_pk_bf16_f32 v88, v8, v9\n\t"
        "v_cvt_pk_bf16_f32 v89, v10, v11\n\t"
        "v_mfma_f32_16x16x32_bf16 v[36:39], a[16:19], v[64:67], v[36:39]\n\t"
        "ds_read_b128 v[60:63], v105 offset:18432\n\t"
        "v_cvt_pk_bf16_f32 v90, v12, v13\n\t"
        "v_mfma_f32_16x16x32_bf16 v[40:43], v[64:67], a[20:23], v[40:43]\n\t"
        "s_add_u32 m0, %[ld], 114688\n\t"
        "s_add_u32 %[t0], %[xo], 1792\n\t"
        "s_waitcnt lgkmcnt(6)\n\t"
        "v_mfma_f32_16x16x32_bf16 v[44:47], a[12:15], v[68:71], v[44:47]\n\t"
        "buffer_load_dwordx4 v100, %[sx], %[t0] offen lds\n\t"
        "v_mfma_f32_16x16x32_bf16 v[48:51], a[16:19], v[68:71], v[48:51]\n\t"
        "ds_read_b128 v[64:67], v106 offset:16384\n\t"
        "v_cvt_pk_bf16_f32 v91, v14, v15\n\t"
        "v_mfma_f32_16x16x32_bf16 v[52:55], v[68:71], a[20:23], v[52:55]\n\t"
        "buffer_store_dwordx4 v[88:91], v109, %[sw], %[so] offen sc1\n\t"
        "s_waitcnt lgkmcnt(6)\n\t"
        "v_mfma_f32_16x16x32_bf16 v[32:35], a[24:27], v[72:75], v[32:35]\n\t"
        "v_cvt_pk_bf16_f32 v92, v20, v21\n\t"
        "v_cvt_pk_bf16_f32 v93, v22, v23\n\t"
        "v_mfma_f32_16x16x32_bf16 v[36:39], a[28:31], v[72:75], v[36:39]\n\t"
        "ds_read_b128 v[68:71], v106 offset:18432\n\t"
        "v_cvt_pk_bf16_f32 v94, v24, v25\n\t"
        "v_mfma_f32_16x16x32_bf16 v[40:43], v[72:75], a[32:35], v[40:43]\n\t"
        "s_add_u32 m0, %[ld], 115712\n\t"
        "s_add_u32 %[t0], %[xo], 18176\n\t"
        "s_waitcnt lgkmcnt(6)\n\t"
        "v_mfma_f32_16x16x32_bf16 v[44:47], a[24:27], v[76:79], v[44:47]\n\t"
        "buffer_load_dwordx4 v100, %[sx], %[t0] offen lds\n\t"
        "v_mfma_f32_16x16x32_bf16 v[48:51], a[28:31], v[76:79], v[48:51]\n\t"
        "ds_read_b128 v[72:75], v105 offset:24576\n\t"
        "v_cvt_pk_bf16_f32 v95, v26, v27\n\t"
        "v_mfma_f32_16x16x32_bf16 v[52:55], v[76:79], a[32:35], v[52:55]\n\t"
        "buffer_store_dwordx4 v[92:95], v110, %[sw], %[so] offen sc1\n\t"
        "s_waitcnt lgkmcnt(6)\n\t"
        "v_mfma_f32_16x16x32_bf16 v[32:35], a[36:39], v[80:83], v[32:35]\n\t"
        "v_cvt_pk_bf16_f32 v96, v16, v17\n\t"
        "v_cvt_pk_bf16_f32 v97, v18, v19\n\t"
        "v_mfma_f32_16x16x32_bf16 v[36:39], a[40:43], v[80:83], v[36:39]\n\t"
        "ds_read_b128 v[76:79], v105 offset:26624\n\t"
        "v_cvt_pk_bf16_f32 v98, v28, v29\n\t"
        "v_mfma_f32_16x16x32_bf16 v[40:43], v[80:83], a[44:47], v[40:43]\n\t"
        "s_add_u32 m0, %[ld], 122880\n\t"
        "s_add_u32 %[t0], %[xo], 1920\n\t"
        "s_waitcnt lgkmcnt(6)\n\t"
        "v_mfma_f32_16x16x32_bf16 v[44:47], a[36:39], v[84:87], v[44:47]\n\t"
        "buffer_load_dwordx4 v100, %[sx], %[t0] offen lds\n\t"
        "v_mfma_f32_16x16x32_bf16 v[48:51], a[40:43], v[84:87], v[48:51]\n\t"
        "ds_read_b128 v[80:83], v106 offset:24576\n\t"
        "v_cvt_pk_bf16_f32 v99, v30, v31\n\t"
        "v_mfma_f32_16x16x32_bf16 v[52:55], v[84:87], a[44:47], v[52:55]\n\t"
        "buffer_store_dwordx4 v[96:99], v111, %[sw], %[so] offen sc1\n\t"
        "s_waitcnt lgkmcnt(6)\n\t"
        "v_mfma_f32_16x16x32_bf16 v[32:35], a[48:51], v[56:59], v[32:35]\n\t"
        "s_lshl_b32 %[so], %[cn], 19\n\t"
        "v_mfma_f32_16x16x32_bf16 v[36:39], a[52:55], v[56:59], v[36:39]\n\t"
        "ds_read_b128 v[84:87], v106 offset:26624\n\t"
        "v_mfma_f32_16x16x32_bf16 v[40:43], v[56:59], a[56:59], v[40:43]\n\t"
        "s_add_u32 m0, %[ld], 123904\n\t"
        "s_add_u32 %[t0], %[xo], 18304\n\t"
        "s_waitcnt lgkmcnt(6)\n\t"
        "v_mfma_f32_16x16x32_bf16 v[44:47], a[48:51], v[60:63], v[44:47]\n\t"
        "buffer_load_dwordx4 v100, %[sx], %[t0] offen lds\n\t"
        "v_mfma_f32_16x16x32_bf16 v[48:51], a[52:55], v[60:63], v[48:51]\n\t"
        "v_mfma_f32_16x16x32_bf16 v[52:55], v[60:63], a[56:59], v[52:55]\n\t"
        "s_waitcnt lgkmcnt(5)\n\t"
        "v_mfma_f32_16x16x32_bf16 v[32:35], a[60:63], v[64:67], v[32:35]\n\t"
        "s_waitcnt vmcnt(19) lgkmcnt(0)\n\t"
        "s_barrier\n\t"
        "s_add_u32 %[xo], %[xo], 0x20000\n\t"
        "v_mfma_f32_16x16x32_bf16 v[36:39], a[64:67], v[64:67], v[36:39]\n\t"
        "ds_read_b128 v[56:59], v105 offset:32768\n\t"
        "v_mfma_f32_16x16x32_bf16 v[40:43], v[64:67], a[68:71], v[40:43]\n\t"
        "ds_read_b128 v[60:63], v105 offset:34816\n\t"
        "s_add_u32 m0, %[ld], 0\n\t"
        "s_add_u32 %[t0], %[xo], 0\n\t"
        "v_mfma_f32_16x16x32_bf16 v[44:47], a[60:63], v[68:71], v[44:47]\n\t"
        "buffer_load_dwordx4 v100, %[sx], %[t0] offen lds\n\t"
        "v_mfma_f32_16x16x32_bf16 v[48:51], a[64:67], v[68:71], v[48:51]\n\t"
        "ds_read_b128 v[64:67], v106 offset:32768\n\t"
        "v_mfma_f32_16x16x32_bf16 v[52:55], v[68:71], a[68:71], v[52:55]\n\t"
        "v_mfma_f32_16x16x32_bf16 v[32:35], a[72:75], v[72:75], v[32:35]\n\t"
        "v_mfma_f32_16x16x32_bf16 v[36:39], a[76:79], v[72:75], v[36:39]\n\t"
        "ds_read_b128 v[68:71], v106 offset:34816\n\t"
        "v_mfma_f32_16x16x32_bf16 v[40:43], v[72:75], a[80:83], v[40:43]\n\t"
        "s_add_u32 m0, %[ld], 1024\n\t"
        "s_add_u32 %[t0], %[xo], 16384\n\t"
        "v_mfma_f32_16x16x32_bf16 v[44:47], a[72:75], v[76:79], v[44:47]\n\t"
        "buffer_load_dwordx4 v100, %[sx], %[t0] offen lds\n\t"
        "v_mfma_f32_16x16x32_bf16 v[48:51], a[76:79], v[76:79], v[48:51]\n\t"
        "ds_read_b128 v[72:75], v105 offset:40960\n\t"
        "v_mfma_f32_16x16x32_bf16 v[52:55], v[76:79], a[80:83], v[52:55]\n\t"
        "v_mfma_f32_16x16x32_bf16 v[32:35], a[84:87], v[80:83], v[32:35]\n\t"
        "v_mfma_f32_16x16x32_bf16 v[36:39], a[88:91], v[80:83], v[36:39]\n\t"
        "ds_read_b128 v[76:79], v105 offset:43008\n\t"
        "v_mfma_f32_16x16x32_bf16 v[40:43], v[80:83], a[92:95], v[40:43]\n\t"
        "s_add_u32 m0, %[ld], 8192\n\t"
        "s_add_u32 %[t0], %[xo], 128\n\t"
        "v_mfma_f32_16x16x32_bf16 v[44:47], a[84:87], v[84:87], v[44:47]\n\t"
        "buffer_load_dwordx4 v100, %[sx], %[t0] offen lds\n\t"
        "v_mfma_f32_16x16x32_bf16 v[48:51], a[88:91], v[84:87], v[48:51]\n\t"
        "ds_read_b128 v[80:83], v106 offset:40960\n\t"
        "v_mfma_f32_16x16x32_bf16 v[52:55], v[84:87], a[92:95], v[52:55]\n\t"
        "s_waitcnt lgkmcnt(6)\n\t"
        "v_mfma_f32_16x16x32_bf16 v[32:35], a[96:99], v[56:59], v[32:35]\n\t"
        "v_mfma_f32_16x16x32_bf16 v[36:39], a[100:103], v[56:59], v[36:39]\n\t"
        "ds_read_b128 v[84:87], v106 offset:43008\n\t"
        "v_mfma_f32_16x16x32_bf16 v[40:43], v[56:59], a[104:107], v[40:43]\n\t"
        "s_add_u32 m0, %[ld], 9216\n\t"
        "s_add_u32 %[t0], %[xo], 16512\n\t"
        "s_waitcnt lgkmcnt(6)\n\t"
        "v_mfma_f32_16x16x32_bf16 v[44:47], a[96:99], v[60:63], v[44:47]\n\t"
        "buffer_load_dwordx4 v100, %[sx], %[t0] offen lds\n\t"
        "v_mfma_f32_16x16x32_bf16 v[48:51], a[100:103], v[60:63], v[48:51]\n\t"
        "ds_read_b128 v[56:59], v105 offset:49152\n\t"
        "v_mfma_f32_16x16x32_bf16 v[52:55], v[60:63], a[104:107], v[52:55]\n\t"
        "s_waitcnt lgkmcnt(6)\n\t"
        "v_mfma_f32_16x16x32_bf16 v[32:35], a[108:111], v[64:67], v[32:35]\n\t"
        "v_mfma_f32_16x16x32_bf16 v[36:39], a[112:115], v[64:67], v[36:39]\n\t"
        "ds_read_b128 v[60:63], v105 offset:51200\n\t"
        "v_mfma_f32_16x16x32_bf16 v[40:43], v[64:67], a[116:119], v[40:43]\n\t"
        "s_add_u32 m0, %[ld], 16384\n\t"
        "s_add_u32 %[t0], %[xo], 256\n\t"
        "s_waitcnt lgkmcnt(6)\n\t"
        "v_mfma_f32_16x16x32_bf16 v[44:47], a[108:111], v[68:71], v[44:47]\n\t"
        "buffer_load_dwordx4 v100, %[sx], %[t0] offen lds\n\t"
        "v_mfma_f32_16x16x32_bf16 v[48:51], a[112:115], v[68:71], v[48:51]\n\t"
        "ds_read_b128 v[64:67], v106 offset:49152\n\t"
        "v_mfma_f32_16x16x32_bf16 v[52:55], v[68:71], a[116:119], v[52:55]\n\t"
        "s_waitcnt lgkmcnt(6)\n\t"
        "v_mfma_f32_16x16x32_bf16 v[32:35], a[120:123], v[72:75], v[32:35]\n\t"
        "v_mfma_f32_16x16x32_bf16 v[36:39], a[124:127], v[72:75], v[36:39]\n\t"
        "ds_read_b128 v[68:71], v106 offset:51200\n\t"
        "v_mfma_f32_16x16x32_bf16 v[40:43], v[72:75], a[128:131], v[40:43]\n\t"
        "s_add_u32 m0, %[ld], 17408\n\t"
        "s_add_u32 %[t0], %[xo], 16640\n\t"
        "s_waitcnt lgkmcnt(6)\n\t"
        "v_mfma_f32_16x16x32_bf16 v[44:47], a[120:123], v[76:79], v[44:47]\n\t"
        "buffer_load_dwordx4 v100, %[sx], %[t0] offen lds\n\t"
        "v_mfma_f32_16x16x32_bf16 v[48:51], a[124:127], v[76:79], v[48:51]\n\t"
        "ds_read_b128 v[72:75], v105 offset:57344\n\t"
        "v_mfma_f32_16x16x32_bf16 v[52:55], v[76:79], a[128:131], v[52:55]\n\t"
        "s_waitcnt lgkmcnt(6)\n\t"
        "v_mfma_f32_16x16x32_bf16 v[32:35], a[132:135], v[80:83], v[32:35]\n\t"
        "v_mfma_f32_16x16x32_bf16 v[36:39], a[136:139], v[80:83], v[36:39]\n\t"
        "ds_read_b128 v[76:79], v105 offset:59392\n\t"
        "v_mfma_f32_16x16x32_bf16 v[40:43], v[80:83], a[140:143], v[40:43]\n\t"
        "s_add_u32 m0, %[ld], 24576\n\t"
        "s_add_u32 %[t0], %[xo], 384\n\t"
        "s_waitcnt lgkmcnt(6)\n\t"
        "v_mfma_f32_16x16x32_bf16 v[44:47], a[132:135], v[84:87], v[44:47]\n\t"
        "buffer_load_dwordx4 v100, %[sx], %[t0] offen lds\n\t"
        "v_mfma_f32_16x16x32_bf16 v[48:51], a[136:139], v[84:87], v[48:51]\n\t"
        "ds_read_b128 v[80:83], v106 offset:57344\n\t"
        "v_mfma_f32_16x16x32_bf16 v[52:55], v[84:87], a[140:143], v[52:55]\n\t"
        "s_waitcnt lgkmcnt(6)\n\t"
        "v_mfma_f32_16x16x32_bf16 v[32:35], a[144:147], v[56:59], v[32:35]\n\t"
        "v_mfma_f32_16x16x32_bf16 v[36:39], a[148:151], v[56:59], v[36:39]\n\t"
        "ds_read_b128 v[84:87], v106 offset:59392\n\t"
        "v_mfma_f32_16x16x32_bf16 v[40:43], v[56:59], a[152:155], v[40:43]\n\t"
        "s_add_u32 m0, %[ld], 25600\n\t"
        "s_add_u32 %[t0], %[xo], 16768\n\t"
        "s_waitcnt lgkmcnt(6)\n\t"
        "v_mfma_f32_16x16x32_bf16 v[44:47], a[144:147], v[60:63], v[44:47]\n\t"
        "buffer_load_dwordx4 v100, %[sx], %[t0] offen lds\n\t"
        "v_mfma_f32_16x16x32_bf16 v[48:51], a[148:151], v[60:63], v[48:51]\n\t"
        "v_mfma_f32_16x16x32_bf16 v[52:55], v[60:63], a[152:155], v[52:55]\n\t"
        "s_waitcnt lgkmcnt(5)\n\t"
        "v_mfma_f32_16x16x32_bf16 v[32:35], a[156:159], v[64:67], v[32:35]\n\t"
        "s_waitcnt vmcnt(9) lgkmcnt(0)\n\t"
        "s_barrier\n\t"
        "v_mfma_f32_16x16x32_bf16 v[36:39], a[160:163], v[64:67], v[36:39]\n\t"
        "ds_read_b128 v[56:59], v107 offset:0\n\t"
        "v_mfma_f32_16x16x32_bf16 v[40:43], v[64:67], a[164:167], v[40:43]\n\t"
        "ds_read_b128 v[60:63], v107 offset:2048\n\t"
        "s_add_u32 m0, %[ld], 32768\n\t"
        "s_add_u32 %[t0], %[xo], 512\n\t"
        "v_mfma_f32_16x16x32_bf16 v[44:47], a[156:159], v[68:71], v[44:47]\n\t"
        "buffer_load_dwordx4 v100, %[sx], %[t0] offen lds\n\t"
        "v_mfma_f32_16x16x32_bf16 v[48:51], a[160:163], v[68:71], v[48:51]\n\t"
        "ds_read_b128 v[64:67], v108 offset:0\n\t"
        "v_mfma_f32_16x16x32_bf16 v[52:55], v[68:71], a[164:167], v[52:55]\n\t"
        "v_mfma_f32_16x16x32_bf16 v[32:35], a[168:171], v[72:75], v[32:35]\n\t"
        "v_mfma_f32_16x16x32_bf16 v[36:39], a[172:175], v[72:75], v[36:39]\n\t"
        "ds_read_b128 v[68:71], v108 offset:2048\n\t"
        "v_mov_b32_e32 v117, %[cn]\n\t"
        "buffer_store_dword v117, v113, %[sw], 0 offen sc1\n\t"
        "s_add_u32 %[cn], %[cn], 1\n\t"
        "v_mfma_f32_16x16x32_bf16 v[40:43], v[72:75], a[176:179], v[40:43]\n\t"
        "s_add_u32 m0, %[ld], 33792\n\t"
        "s_add_u32 %[t0], %[xo], 16896\n\t"
        "v_mfma_f32_16x16x32_bf16 v[44:47], a[168:171], v[76:79], v[44:47]\n\t"
        "buffer_load_dwordx4 v100, %[sx], %[t0] offen lds\n\t"
        "v_mfma_f32_16x16x32_bf16 v[48:51], a[172:175], v[76:79], v[48:51]\n\t"
        "ds_read_b128 v[72:75], v107 offset:8192\n\t"
        "v_mfma_f32_16x16x32_bf16 v[52:55], v[76:79], a[176:179], v[52:55]\n\t"
        "v_mfma_f32_16x16x32_bf16 v[32:35], a[180:183], v[80:83], v[32:35]\n\t"
        "v_mfma_f32_16x16x32_bf16 v[36:39], a[184:187], v[80:83], v[36:39]\n\t"
        "ds_read_b128 v[76:79], v107 offset:10240\n\t"
        "v_mfma_f32_16x16x32_bf16 v[40:43], v[80:83], a[188:191], v[40:43]\n\t"
        "s_add_u32 m0, %[ld], 40960\n\t"
        "s_add_u32 %[t0], %[xo], 640\n\t"
        "v_mfma_f32_16x16x32_bf16 v[44:47], a[180:183], v[84:87], v[44:47]\n\t"
        "buffer_load_dwordx4 v100, %[sx], %[t0] offen lds\n\t"
        "v_mfma_f32_16x16x32_bf16 v[48:51], a[184:187], v[84:87], v[48:51]\n\t"
        "ds_read_b128 v[80:83], v108 offset:8192\n\t"
        "v_mfma_f32_16x16x32_bf16 v[52:55], v[84:87], a[188:191], v[52:55]\n\t"
        "s_waitcnt lgkmcnt(6)\n\t"
        "v_mfma_f32_16x16x32_bf16 v[32:35], a[192:195], v[56:59], v[32:35]\n\t"
        "v_mfma_f32_16x16x32_bf16 v[36:39], a[196:199], v[56:59], v[36:39]\n\t"
        "ds_read_b128 v[84:87], v108 offset:10240\n\t"
        "v_mfma_f32_16x16x32_bf16 v[40:43], v[56:59], a[200:203], v[40:43]\n\t"
        "s_add_u32 m0, %[ld], 41984\n\t"
        "s_add_u32 %[t0], %[xo], 17024\n\t"
        "s_waitcnt lgkmcnt(6)\n\t"
        "v_mfma_f32_16x16x32_bf16 v[44:47], a[192:195], v[60:63], v[44:47]\n\t"
        "buffer_load_dwordx4 v100, %[sx], %[t0] offen lds\n\t"
        "v_mfma_f32_16x16x32_bf16 v[48:51], a[196:199], v[60:63], v[48:51]\n\t"
        "ds_read_b128 v[56:59], v107 offset:16384\n\t"
        "v_mfma_f32_16x16x32_bf16 v[52:55], v[60:63], a[200:203], v[52:55]\n\t"
        "s_waitcnt lgkmcnt(6)\n\t"
        "v_mfma_f32_16x16x32_bf16 v[32:35], a[204:207], v[64:67], v[32:35]\n\t"
        "v_mfma_f32_16x16x32_bf16 v[36:39], a[208:211], v[64:67], v[36:39]\n\t"
        "ds_read_b128 v[60:63], v107 offset:18432\n\t"
        "v_mfma_f32_16x16x32_bf16 v[40:43], v[64:67], a[212:215], v[40:43]\n\t"
        "s_add_u32 m0, %[ld], 49152\n\t"
        "s_add_u32 %[t0], %[xo], 768\n\t"
        "s_waitcnt lgkmcnt(6)\n\t"
        "v_mfma_f32_16x16x32_bf16 v[44:47], a[204:207], v[68:71], v[44:47]\n\t"
        "buffer_load_dwordx4 v100, %[sx], %[t0] offen lds\n\t"
        "v_mfma_f32_16x16x32_bf16 v[48:51], a[208:211], v[68:71], v[48:51]\n\t"
        "ds_read_b128 v[64:67], v108 offset:16384\n\t"
        "v_mfma_f32_16x16x32_bf16 v[52:55], v[68:71], a[212:215], v[52:55]\n\t"
        "s_waitcnt lgkmcnt(6)\n\t"
        "v_mfma_f32_16x16x32_bf16 v[32:35], a[216:219], v[72:75], v[32:35]\n\t"
        "v_mfma_f32_16x16x32_bf16 v[36:39], a[220:223], v[72:75], v[36:39]\n\t"
        "ds_read_b128 v[68:71], v108 offset:18432\n\t"
        "v_mfma_f32_16x16x32_bf16 v[40:43], v[72:75], a[224:227], v[40:43]\n\t"
        "s_add_u32 m0, %[ld], 50176\n\t"
        "s_add_u32 %[t0], %[xo], 17152\n\t"
        "s_waitcnt lgkmcnt(6)\n\t"
        "v_mfma_f32_16x16x32_bf16 v[44:47], a[216:219], v[76:79], v[44:47]\n\t"
        "buffer_load_dwordx4 v100, %[sx], %[t0] offen lds\n\t"
        "v_mfma_f32_16x16x32_bf16 v[48:51], a[220:223], v[76:79], v[48:51]\n\t"
        "ds_read_b128 v[72:75], v107 offset:24576\n\t"
        "v_mfma_f32_16x16x32_bf16 v[52:55], v[76:79], a[224:227], v[52:55]\n\t"
        "s_waitcnt lgkmcnt(6)\n\t"
        "v_mfma_f32_16x16x32_bf16 v[32:35], a[228:231], v[80:83], v[32:35]\n\t"
        "v_mfma_f32_16x16x32_bf16 v[36:39], a[232:235], v[80:83], v[36:39]\n\t"
        "ds_read_b128 v[76:79], v107 offset:26624\n\t"
        "v_mfma_f32_16x16x32_bf16 v[40:43], v[80:83], a[236:239], v[40:43]\n\t"
        "s_add_u32 m0, %[ld], 57344\n\t"
        "s_add_u32 %[t0], %[xo], 896\n\t"
        "s_waitcnt lgkmcnt(6)\n\t"
        "v_mfma_f32_16x16x32_bf16 v[44:47], a[228:231], v[84:87], v[44:47]\n\t"
        "buffer_load_dwordx4 v100, %[sx], %[t0] offen lds\n\t"
        "v_mfma_f32_16x16x32_bf16 v[48:51], a[232:235], v[84:87], v[48:51]\n\t"
        "ds_read_b128 v[80:83], v108 offset:24576\n\t"
        "v_mfma_f32_16x16x32_bf16 v[52:55], v[84:87], a[236:239], v[52:55]\n\t"
        "s_waitcnt lgkmcnt(6)\n\t"
        "v_mfma_f32_16x16x32_bf16 v[32:35], a[240:243], v[56:59], v[32:35]\n\t"
        "v_mfma_f32_16x16x32_bf16 v[36:39], a[244:247], v[56:59], v[36:39]\n\t"
        "ds_read_b128 v[84:87], v108 offset:26624\n\t"
        "v_mfma_f32_16x16x32_bf16 v[40:43], v[56:59], a[248:251], v[40:43]\n\t"
        "s_add_u32 m0, %[ld], 58368\n\t"
        "s_add_u32 %[t0], %[xo], 17280\n\t"
        "s_waitcnt lgkmcnt(6)\n\t"
        "v_mfma_f32_16x16x32_bf16 v[44:47], a[240:243], v[60:63], v[44:47]\n\t"
        "buffer_load_dwordx4 v100, %[sx], %[t0] offen lds\n\t"
        "v_mfma_f32_16x16x32_bf16 v[48:51], a[244:247], v[60:63], v[48:51]\n\t"
        "v_mfma_f32_16x16x32_bf16 v[52:55], v[60:63], a[248:251], v[52:55]\n\t"
        "s_waitcnt lgkmcnt(5)\n\t"
        "v_mfma_f32_16x16x32_bf16 v[32:35], a[252:255], v[64:67], v[32:35]\n\t"
        "s_waitcnt vmcnt(17) lgkmcnt(0)\n\t"
        "s_barrier\n\t"
        "v_mfma_f32_16x16x32_bf16 v[36:39], v[128:131], v[64:67], v[36:39]\n\t"
        "ds_read_b128 v[56:59], v107 offset:32768\n\t"
        "v_mfma_f32_16x16x32_bf16 v[40:43], v[64:67], v[132:135], v[40:43]\n\t"
        "ds_read_b128 v[60:63], v107 offset:34816\n\t"
        "s_add_u32 m0, %[ld], 65536\n\t"
        "s_add_u32 %[t0], %[xo], 1024\n\t"
        "v_mfma_f32_16x16x32_bf16 v[44:47], a[252:255], v[68:71], v[44:47]\n\t"
        "buffer_load_dwordx4 v100, %[sx], %[t0] offen lds\n\t"
        "v_mfma_f32_16x16x32_bf16 v[48:51], v[128:131], v[68:71], v[48:51]\n\t"
        "ds_read_b128 v[64:67], v108 offset:32768\n\t"
        "v_mfma_f32_16x16x32_bf16 v[52:55], v[68:71], v[132:135], v[52:55]\n\t"
        "v_mfma_f32_16x16x32_bf16 v[32:35], v[136:139], v[72:75], v[32:35]\n\t"
        "v_mfma_f32_16x16x32_bf16 v[36:39], v[140:143], v[72:75], v[36:39]\n\t"
        "ds_read_b128 v[68:71], v108 offset:34816\n\t"
        "v_mfma_f32_16x16x32_bf16 v[40:43], v[72:75], v[144:147], v[40:43]\n\t"
        "s_add_u32 m0, %[ld], 66560\n\t"
        "s_add_u32 %[t0], %[xo], 17408\n\t"
        "v_mfma_f32_16x16x32_bf16 v[44:47], v[136:139], v[76:79], v[44:47]\n\t"
        "buffer_load_dwordx4 v100, %[sx], %[t0] offen lds\n\t"
        "v_mfma_f32_16x16x32_bf16 v[48:51], v[140:143], v[76:79], v[48:51]\n\t"
        "ds_read_b128 v[72:75], v107 offset:40960\n\t"
        "v_mfma_f32_16x16x32_bf16 v[52:55], v[76:79], v[144:147], v[52:55]\n\t"
        "v_mfma_f32_16x16x32_bf16 v[32:35], v[148:151], v[80:83], v[32:35]\n\t"
        "v_mfma_f32_16x16x32_bf16 v[36:39], v[152:155], v[80:83], v[36:39]\n\t"
        "ds_read_b128 v[76:79], v107 offset:43008\n\t"
        "v_mfma_f32_16x16x32_bf16 v[40:43], v[80:83], v[156:159], v[40:43]\n\t"
        "s_add_u32 m0, %[ld], 73728\n\t"
        "s_add_u32 %[t0], %[xo], 1152\n\t"
        "v_mfma_f32_16x16x32_bf16 v[44:47], v[148:151], v[84:87], v[44:47]\n\t"
        "buffer_load_dwordx4 v100, %[sx], %[t0] offen lds\n\t"
        "v_mfma_f32_16x16x32_bf16 v[48:51], v[152:155], v[84:87], v[48:51]\n\t"
        "ds_read_b128 v[80:83], v108 offset:40960\n\t"
        "v_mfma_f32_16x16x32_bf16 v[52:55], v[84:87], v[156:159], v[52:55]\n\t"
        "s_waitcnt lgkmcnt(6)\n\t"
        "v_mfma_f32_16x16x32_bf16 v[32:35], v[160:163], v[56:59], v[32:35]\n\t"
        "v_mfma_f32_16x16x32_bf16 v[36:39], v[164:167], v[56:59], v[36:39]\n\t"
        "ds_read_b128 v[84:87], v108 offset:43008\n\t"
        "v_mfma_f32_16x16x32_bf16 v[40:43], v[56:59], v[168:171], v[40:43]\n\t"
        "s_add_u32 m0, %[ld], 74752\n\t"
        "s_add_u32 %[t0], %[xo], 17536\n\t"
        "s_waitcnt lgkmcnt(6)\n\t"
        "v_mfma_f32_16x16x32_bf16 v[44:47], v[160:163], v[60:63], v[44:47]\n\t"
        "buffer_load_dwordx4 v100, %[sx], %[t0] offen lds\n\t"
        "v_mfma_f32_16x16x32_bf16 v[48:51], v[164:167], v[60:63], v[48:51]\n\t"
        "ds_read_b128 v[56:59], v107 offset:49152\n\t"
        "v_mfma_f32_16x16x32_bf16 v[52:55], v[60:63], v[168:171], v[52:55]\n\t"
        "s_waitcnt lgkmcnt(6)\n\t"
        "v_mfma_f32_16x16x32_bf16 v[32:35], v[172:175], v[64:67], v[32:35]\n\t"
        "v_mfma_f32_16x16x32_bf16 v[36:39], v[176:179], v[64:67], v[36:39]\n\t"
        "ds_read_b128 v[60:63], v107 offset:51200\n\t"
        "v_mfma_f32_16x16x32_bf16 v[40:43], v[64:67], v[180:183], v[40:43]\n\t"
        "s_add_u32 m0, %[ld], 81920\n\t"
        "s_add_u32 %[t0], %[xo], 1280\n\t"
        "s_waitcnt lgkmcnt(6)\n\t"
        "v_mfma_f32_16x16x32_bf16 v[44:47], v[172:175], v[68:71], v[44:47]\n\t"
        "buffer_load_dwordx4 v100, %[sx], %[t0] offen lds\n\t"
        "v_mfma_f32_16x16x32_bf16 v[48:51], v[176:179], v[68:71], v[48:51]\n\t"
        "ds_read_b128 v[64:67], v108 offset:49152\n\t"
        "v_mfma_f32_16x16x32_bf16 v[52:55], v[68:71], v[180:183], v[52:55]\n\t"
        "s_waitcnt lgkmcnt(6)\n\t"
        "v_mfma_f32_16x16x32_bf16 v[32:35], v[184:187], v[72:75], v[32:35]\n\t"
        "v_mfma_f32_16x16x32_bf16 v[36:39], v[188:191], v[72:75], v[36:39]\n\t"
        "ds_read_b128 v[68:71], v108 offset:51200\n\t"
        "v_mfma_f32_16x16x32_bf16 v[40:43], v[72:75], v[192:195], v[40:43]\n\t"
        "s_add_u32 m0, %[ld], 82944\n\t"
        "s_add_u32 %[t0], %[xo], 17664\n\t"
        "s_waitcnt lgkmcnt(6)\n\t"
        "v_mfma_f32_16x16x32_bf16 v[44:47], v[184:187], v[76:79], v[44:47]\n\t"
        "buffer_load_dwordx4 v100, %[sx], %[t0] offen lds\n\t"
        "v_mfma_f32_16x16x32_bf16 v[48:51], v[188:191], v[76:79], v[48:51]\n\t"
        "ds_read_b128 v[72:75], v107 offset:57344\n\t"
        "v_mfma_f32_16x16x32_bf16 v[52:55], v[76:79], v[192:195], v[52:55]\n\t"
        "s_waitcnt lgkmcnt(6)\n\t"
        "v_mfma_f32_16x16x32_bf16 v[32:35], v[196:199], v[80:83], v[32:35]\n\t"
        "v_mfma_f32_16x16x32_bf16 v[36:39], v[200:203], v[80:83], v[36:39]\n\t"
        "ds_read_b128 v[76:79], v107 offset:59392\n\t"
        "v_mfma_f32_16x16x32_bf16 v[40:43], v[80:83], v[204:207], v[40:43]\n\t"
        "s_add_u32 m0, %[ld], 90112\n\t"
        "s_add_u32 %[t0], %[xo], 1408\n\t"
        "s_waitcnt lgkmcnt(6)\n\t"
        "v_mfma_f32_16x16x32_bf16 v[44:47], v[196:199], v[84:87], v[44:47]\n\t"
        "buffer_load_dwordx4 v100, %[sx], %[t0] offen lds\n\t"
        "v_mfma_f32_16x16x32_bf16 v[48:51], v[200:203], v[84:87], v[48:51]\n\t"
        "ds_read_b128 v[80:83], v108 offset:57344\n\t"
        "v_mfma_f32_16x16x32_bf16 v[52:55], v[84:87], v[204:207], v[52:55]\n\t"
        "s_waitcnt lgkmcnt(6)\n\t"
        "v_mfma_f32_16x16x32_bf16 v[32:35], v[208:211], v[56:59], v[32:35]\n\t"
        "v_mfma_f32_16x16x32_bf16 v[36:39], v[212:215], v[56:59], v[36:39]\n\t"
        "ds_read_b128 v[84:87], v108 offset:59392\n\t"
        "v_mfma_f32_16x16x32_bf16 v[40:43], v[56:59], v[216:219], v[40:43]\n\t"
        "s_add_u32 m0, %[ld], 91136\n\t"
        "s_add_u32 %[t0], %[xo], 17792\n\t"
        "s_waitcnt lgkmcnt(6)\n\t"
        "v_mfma_f32_16x16x32_bf16 v[44:47], v[208:211], v[60:63], v[44:47]\n\t"
        "buffer_load_dwordx4 v100, %[sx], %[t0] offen lds\n\t"
        "v_mfma_f32_16x16x32_bf16 v[48:51], v[212:215], v[60:63], v[48:51]\n\t"
        "v_mfma_f32_16x16x32_bf16 v[52:55], v[60:63], v[216:219], v[52:55]\n\t"
        "s_waitcnt lgkmcnt(5)\n\t"
        "v_mfma_f32_16x16x32_bf16 v[32:35], v[220:223], v[64:67], v[32:35]\n\t"
        "s_waitcnt vmcnt(17) lgkmcnt(0)\n\t"
        "s_barrier\n\t"
        "v_mfma_f32_16x16x32_bf16 v[36:39], v[224:227], v[64:67], v[36:39]\n\t"
        "ds_read_b128 v[56:59], v105 offset:0\n\t"
        "v_mfma_f32_16x16x32_bf16 v[40:43], v[64:67], v[228:231], v[40:43]\n\t"
        "ds_read_b128 v[60:63], v105 offset:2048\n\t"
        "s_add_u32 m0, %[ld], 98304\n\t"
        "s_add_u32 %[t0], %[xo], 1536\n\t"
        "v_mfma_f32_16x16x32_bf16 v[44:47], v[220:223], v[68:71], v[44:47]\n\t"
        "buffer_load_dwordx4 v100, %[sx], %[t0] offen lds\n\t"
        "v_mfma_f32_16x16x32_bf16 v[48:51], v[224:227], v[68:71], v[48:51]\n\t"
        "ds_read_b128 v[64:67], v106 offset:0\n\t"
        "v_mfma_f32_16x16x32_bf16 v[52:55], v[68:71], v[228:231], v[52:55]\n\t"
        "v_mfma_f32_16x16x32_bf16 v[32:35], v[232:235], v[72:75], v[32:35]\n\t"
        "v_mfma_f32_16x16x32_bf16 v[36:39], v[236:239], v[72:75], v[36:39]\n\t"
        "ds_read_b128 v[68:71], v106 offset:2048\n\t"
        "v_mfma_f32_16x16x32_bf16 v[40:43], v[72:75], v[240:243], v[40:43]\n\t"
        "s_add_u32 m0, %[ld], 99328\n\t"
        "s_add_u32 %[t0], %[xo], 17920\n\t"
        "v_mfma_f32_16x16x32_bf16 v[44:47], v[232:235], v[76:79], v[44:47]\n\t"
        "buffer_load_dwordx4 v100, %[sx], %[t0] offen lds\n\t"
        "v_mfma_f32_16x16x32_bf16 v[48:51], v[236:239], v[76:79], v[48:51]\n\t"
        "ds_read_b128 v[72:75], v105 offset:8192\n\t"
        "v_mfma_f32_16x16x32_bf16 v[52:55], v[76:79], v[240:243], v[52:55]\n\t"
        "v_mfma_f32_16x16x32_bf16 v[32:35], v[244:247], v[80:83], v[32:35]\n\t"
        "v_mfma_f32_16x16x32_bf16 v[36:39], v[248:251], v[80:83], v[36:39]\n\t"
        "ds_read_b128 v[76:79], v105 offset:10240\n\t"
        "v_mfma_f32_16x16x32_bf16 v[40:43], v[80:83], v[252:255], v[40:43]\n\t"
        "s_add_u32 m0, %[ld], 106496\n\t"
        "s_add_u32 %[t0], %[xo], 1664\n\t"
        "v_mfma_f32_16x16x32_bf16 v[44:47], v[244:247], v[84:87], v[44:47]\n\t"
        "buffer_load_dwordx4 v100, %[sx], %[t0] offen lds\n\t"
        "v_mfma_f32_16x16x32_bf16 v[48:51], v[248:251], v[84:87], v[48:51]\n\t"
        "ds_read_b128 v[80:83], v106 offset:8192\n\t"
        "v_mfma_f32_16x16x32_bf16 v[52:55], v[84:87], v[252:255], v[52:55]\n\t"
        "s_cmp_lt_u32 %[cn], 32\n\t"
        "s_cbranch_scc1 LG_loop%=\n\t"
        "s_nop 7\n\t"
        "v_cvt_pk_bf16_f32 v88, v32, v33\n\t"
        "v_cvt_pk_bf16_f32 v89, v34, v35\n\t"
        "v_cvt_pk_bf16_f32 v90, v36, v37\n\t"
        "v_cvt_pk_bf16_f32 v91, v38, v39\n\t"
        "buffer_store_dwordx4 v[88:91], v109, %[sw], %[so] offen sc1\n\t"
        "v_cvt_pk_bf16_f32 v92, v44, v45\n\t"
        "v_cvt_pk_bf16_f32 v93, v46, v47\n\t"
        "v_cvt_pk_bf16_f32 v94, v48, v49\n\t"
        "v_cvt_pk_bf16_f32 v95, v50, v51\n\t"
        "buffer_store_dwordx4 v[92:95], v110, %[sw], %[so] offen sc1\n\t"
        "v_cvt_pk_bf16_f32 v96, v40, v41\n\t"
        "v_cvt_pk_bf16_f32 v97, v42, v43\n\t"
        "v_cvt_pk_bf16_f32 v98, v52, v53\n\t"
        "v_cvt_pk_bf16_f32 v99, v54, v55\n\t"
        "buffer_store_dwordx4 v[96:99], v111, %[sw], %[so] offen sc1\n\t"
        "s_waitcnt vmcnt(0) lgkmcnt(0)\n\t"
        "v_mov_b32_e32 v117, %[cn]\n\t"
        "buffer_store_dword v117, v113, %[sw], 0 offen sc1\n\t"
        "s_barrier\n\t"
        "LG_end%=:\n\t"
        ".Lk_end_G:\n\t"
        : [t0] "=&s"(t0), [xo] "=&s"(xo), [so] "=&s"(so), [cn] "=&s"(cn), [ts] "=&s"(ts), [ta] "=&s"(ta), [tb] "=&s"(tb), [tc] "=&s"(tc), [td] "=&s"(td)
        : [sx] "s"(sx), [sq] "s"(sq), [sv] "s"(sv), [sw] "s"(sw), [ld] "s"(lb + w * 2048),
          "{v100}"((16 * w + (l >> 3)) * 2048 + (((l & 7) ^ (l >> 3)) << 4)), "{v101}"((8 * w + (l >> 3)) * 8192 + h * 256 + (c & 1) * 128 + (l & 7) * 16),
          "{v112}"((16 * w + (l >> 2)) * 8192 + h * 256 + cv * 64 + (l & 3) * 16), "{v102}"(lb + 131072 + w * 1024 + ((l * 16) ^ ((l & 16) << 2))), "{v116}"(lb + 131072 + w * 1024 + ((l * 16) ^ (l & 32))),
          "{v103}"(lr + rr * 128 + ((tn ^ (n >> 3)) * 64) + (n & 3) * 16), "{v104}"(lr + 8192 + rr * 64 + ((tn ^ (g & 1)) * 32) + (n & 3) * 8), "{v105}"(lx), "{v106}"(lx ^ 64u), "{v107}"(lx + 65536), "{v108}"((lx ^ 64u) + 65536),
          "{v109}"(aq), "{v110}"(aq + 4096), "{v111}"(W_VF + (tm * 32 + h) * 8192 + (2 * cv + tn) * 1024 + l * 16), "{v113}"(l == 0 ? F_KVQ + h * 1024 + (c * 4 + w) * 64 : OOB), "{v[114:115]}"(tp)
        : "memory", "scc", "m0", "v8", "v9", "v10", "v11", "v12", "v13", "v14", "v15", "v16", "v17", "v18", "v19", "v20", "v21", "v22", "v23", "v24", "v25", "v26", "v27", "v28", "v29", "v30", "v31", "v32", "v33", "v34", "v35", "v36", "v37", "v38", "v39", "v40", "v41", "v42", "v43", "v44", "v45", "v46", "v47", "v48", "v49", "v50", "v51", "v52", "v53", "v54", "v55", "v56", "v57", "v58", "v59", "v60", "v61", "v62", "v63", "v64", "v65", "v66", "v67", "v68", "v69", "v70", "v71", "v72", "v73", "v74", "v75", "v76", "v77", "v78", "v79", "v80", "v81", "v82", "v83", "v84", "v85", "v86", "v87", "v88", "v89", "v90", "v91", "v92", "v93", "v94", "v95", "v96", "v97", "v98", "v99", "v117", "v118", "v119", "v120", "v121", "v122", "v123", "v124", "v125", "v126", "v127", "v128", "v129", "v130", "v131", "v132", "v133", "v134", "v135", "v136", "v137", "v138", "v139", "v140", "v141", "v142", "v143", "v144", "v145", "v146", "v147", "v148", "v149", "v150", "v151", "v152", "v153", "v154", "v155", "v156", "v157", "v158", "v159", "v160", "v161", "v162", "v163", "v164", "v165", "v166", "v167", "v168", "v169", "v170", "v171", "v172", "v173", "v174", "v175", "v176", "v177", "v178", "v179", "v180", "v181", "v182", "v183", "v184", "v185", "v186", "v187", "v188", "v189", "v190", "v191", "v192", "v193", "v194", "v195", "v196", "v197", "v198", "v199", "v200", "v201", "v202", "v203", "v204", "v205", "v206", "v207", "v208", "v209", "v210", "v211", "v212", "v213", "v214", "v215", "v216", "v217", "v218", "v219", "v220", "v221", "v222", "v223", "v224", "v225", "v226", "v227", "v228", "v229", "v230", "v231", "v232", "v233", "v234", "v235", "v236", "v237", "v238", "v239", "v240", "v241", "v242", "v243", "v244", "v245", "v246", "v247", "v248", "v249", "v250", "v251", "v252", "v253", "v254", "v255", "a0", "a1", "a2", "a3", "a4", "a5", "a6", "a7", "a8", "a9", "a10", "a11", "a12", "a13", "a14", "a15", "a16", "a17", "a18", "a19", "a20", "a21", "a22", "a23", "a24", "a25", "a26", "a27", "a28", "a29", "a30", "a31", "a32", "a33", "a34", "a35", "a36", "a37", "a38", "a39", "a40", "a41", "a42", "a43", "a44", "a45", "a46", "a47", "a48", "a49", "a50", "a51", "a52", "a53", "a54", "a55", "a56", "a57", "a58", "a59", "a60", "a61", "a62", "a63", "a64", "a65", "a66", "a67", "a68", "a69", "a70", "a71", "a72", "a73", "a74", "a75", "a76", "a77", "a78", "a79", "a80", "a81", "a82", "a83", "a84", "a85", "a86", "a87", "a88", "a89", "a90", "a91", "a92", "a93", "a94", "a95", "a96", "a97", "a98", "a99", "a100", "a101", "a102", "a103", "a104", "a105", "a106", "a107", "a108", "a109", "a110", "a111", "a112", "a113", "a114", "a115", "a116", "a117", "a118", "a119", "a120", "a121", "a122", "a123", "a124", "a125", "a126", "a127", "a128", "a129", "a130", "a131", "a132", "a133", "a134", "a135", "a136", "a137", "a138", "a139", "a140", "a141", "a142", "a143", "a144", "a145", "a146", "a147", "a148", "a149", "a150", "a151", "a152", "a153", "a154", "a155", "a156", "a157", "a158", "a159", "a160", "a161", "a162", "a163", "a164", "a165", "a166", "a167", "a168", "a169", "a170", "a171", "a172", "a173", "a174", "a175", "a176", "a177", "a178", "a179", "a180", "a181", "a182", "a183", "a184", "a185", "a186", "a187", "a188", "a189", "a190", "a191", "a192", "a193", "a194", "a195", "a196", "a197", "a198", "a199", "a200", "a201", "a202", "a203", "a204", "a205", "a206", "a207", "a208", "a209", "a210", "a211", "a212", "a213", "a214", "a215", "a216", "a217", "a218", "a219", "a220", "a221", "a222", "a223", "a224", "a225", "a226", "a227", "a228", "a229", "a230", "a231", "a232", "a233", "a234", "a235", "a236", "a237", "a238", "a239", "a240", "a241", "a242", "a243", "a244", "a245", "a246", "a247", "a248", "a249", "a250", "a251", "a252", "a253", "a254", "a255");
}

__device__ __forceinline__ void out_block(const Params& p, unsigned char* lds, const int wv, const int x, const int nt, const int j) {
    const int w = wv, l = otid() & 63, n = l & 15, g = l >> 4;
    const unsigned lb = __builtin_amdgcn_readfirstlane((unsigned)(size_t)lds), stg = lb + w * 8192;
    const v4u so = mk_srd(p.Wo, 1024 * 4096 * 2), sw = mk_srd(p.ws, W_TOTAL), su = mk_srd(p.out, S * D * 2);
    unsigned t0, t1, tl = x, pg, nx, sp, law = lb + w * 2304 + n * 144 + g * 16, lar = lb + (4 * w + g) * 144 + n * 8; unsigned long long ts, tu;
    asm volatile(
        "s_add_u32 %[t1], %[ro], 0\n\t"
        "s_and_b32 %[t1], %[t1], 7\n\t"
        "s_lshl_b32 %[t1], %[t1], 18\n\t"
        "s_add_u32 %[t0], %[t1], 0\n\t"
        "buffer_load_dwordx4 a[0:3], v100, %[so], %[t0] offen\n\t"
        "s_sleep 4\n\t"
        "s_add_u32 %[t0], %[t1], 32768\n\t"
        "buffer_load_dwordx4 a[4:7], v100, %[so], %[t0] offen\n\t"
        "s_sleep 4\n\t"
        "s_add_u32 %[t0], %[t1], 65536\n\t"
        "buffer_load_dwordx4 a[8:11], v100, %[so], %[t0] offen\n\t"
        "s_sleep 4\n\t"
        "s_add_u32 %[t0], %[t1], 98304\n\t"
        "buffer_load_dwordx4 a[12:15], v100, %[so], %[t0] offen\n\t"
        "s_sleep 4\n\t"
        "s_add_u32 %[t0], %[t1], 131072\n\t"
        "buffer_load_dwordx4 a[16:19], v100, %[so], %[t0] offen\n\t"
        "s_sleep 4\n\t"
        "s_add_u32 %[t0], %[t1], 163840\n\t"
        "buffer_load_dwordx4 a[20:23], v100, %[so], %[t0] offen\n\t"
        "s_sleep 4\n\t"
        "s_add_u32 %[t0], %[t1], 196608\n\t"
        "buffer_load_dwordx4 a[24:27], v100, %[so], %[t0] offen\n\t"
        "s_sleep 4\n\t"
        "s_add_u32 %[t0], %[t1], 229376\n\t"
        "buffer_load_dwordx4 a[28:31], v100, %[so], %[t0] offen\n\t"
        "s_sleep 4\n\t"
        "s_add_u32 %[t1], %[ro], 1\n\t"
        "s_and_b32 %[t1], %[t1], 7\n\t"
        "s_lshl_b32 %[t1], %[t1], 18\n\t"
        "s_add_u32 %[t0], %[t1], 0\n\t"
        "buffer_load_dwordx4 a[32:35], v100, %[so], %[t0] offen\n\t"
        "s_sleep 4\n\t"
        "s_add_u32 %[t0], %[t1], 32768\n\t"
        "buffer_load_dwordx4 a[36:39], v100, %[so], %[t0] offen\n\t"
        "s_sleep 4\n\t"
        "s_add_u32 %[t0], %[t1], 65536\n\t"
        "buffer_load_dwordx4 a[40:43], v100, %[so], %[t0] offen\n\t"
        "s_sleep 4\n\t"
        "s_add_u32 %[t0], %[t1], 98304\n\t"
        "buffer_load_dwordx4 a[44:47], v100, %[so], %[t0] offen\n\t"
        "s_sleep 4\n\t"
        "s_add_u32 %[t0], %[t1], 131072\n\t"
        "buffer_load_dwordx4 a[48:51], v100, %[so], %[t0] offen\n\t"
        "s_sleep 4\n\t"
        "s_add_u32 %[t0], %[t1], 163840\n\t"
        "buffer_load_dwordx4 a[52:55], v100, %[so], %[t0] offen\n\t"
        "s_sleep 4\n\t"
        "s_add_u32 %[t0], %[t1], 196608\n\t"
        "buffer_load_dwordx4 a[56:59], v100, %[so], %[t0] offen\n\t"
        "s_sleep 4\n\t"
        "s_add_u32 %[t0], %[t1], 229376\n\t"
        "buffer_load_dwordx4 a[60:63], v100, %[so], %[t0] offen\n\t"
        "s_sleep 4\n\t"
        "s_waitcnt vmcnt(8)\n\t"
        "ds_write_b128 v101, a[0:3] offset:0\n\t"
        "ds_write_b128 v101, a[4:7] offset:1024\n\t"
        "ds_write_b128 v101, a[8:11] offset:2048\n\t"
        "ds_write_b128 v101, a[12:15] offset:3072\n\t"
        "ds_write_b128 v101, a[16:19] offset:4096\n\t"
        "ds_write_b128 v101, a[20:23] offset:5120\n\t"
        "ds_write_b128 v101, a[24:27] offset:6144\n\t"
        "ds_write_b128 v101, a[28:31] offset:7168\n\t"
        "ds_read_b64_tr_b16 a[0:1], v102 offset:0\n\t"
        "ds_read_b64_tr_b16 a[2:3], v102 offset:1024\n\t"
        "ds_read_b64_tr_b16 a[4:5], v102 offset:32\n\t"
        "ds_read_b64_tr_b16 a[6:7], v102 offset:1056\n\t"
        "s_waitcnt lgkmcnt(8)\n\t"
        "ds_read_b64_tr_b16 a[8:9], v102 offset:2048\n\t"
        "ds_read_b64_tr_b16 a[10:11], v102 offset:3072\n\t"
        "ds_read_b64_tr_b16 a[12:13], v102 offset:2080\n\t"
        "ds_read_b64_tr_b16 a[14:15], v102 offset:3104\n\t"
        "s_waitcnt lgkmcnt(8)\n\t"
        "ds_read_b64_tr_b16 a[16:17], v102 offset:4096\n\t"
        "ds_read_b64_tr_b16 a[18:19], v102 offset:5120\n\t"
        "ds_read_b64_tr_b16 a[20:21], v102 offset:4128\n\t"
        "ds_read_b64_tr_b16 a[22:23], v102 offset:5152\n\t"
        "s_waitcnt lgkmcnt(8)\n\t"
        "ds_read_b64_tr_b16 a[24:25], v102 offset:6144\n\t"
        "ds_read_b64_tr_b16 a[26:27], v102 offset:7168\n\t"
        "ds_read_b64_tr_b16 a[28:29], v102 offset:6176\n\t"
        "ds_read_b64_tr_b16 a[30:31], v102 offset:7200\n\t"
        "s_waitcnt lgkmcnt(8)\n\t"
        "s_add_u32 %[t1], %[ro], 2\n\t"
        "s_and_b32 %[t1], %[t1], 7\n\t"
        "s_lshl_b32 %[t1], %[t1], 18\n\t"
        "s_add_u32 %[t0], %[t1], 0\n\t"
        "buffer_load_dwordx4 a[64:67], v100, %[so], %[t0] offen\n\t"
        "s_sleep 4\n\t"
        "s_add_u32 %[t0], %[t1], 32768\n\t"
        "buffer_load_dwordx4 a[68:71], v100, %[so], %[t0] offen\n\t"
        "s_sleep 4\n\t"
        "s_add_u32 %[t0], %[t1], 65536\n\t"
        "buffer_load_dwordx4 a[72:75], v100, %[so], %[t0] offen\n\t"
        "s_sleep 4\n\t"
        "s_add_u32 %[t0], %[t1], 98304\n\t"
        "buffer_load_dwordx4 a[76:79], v100, %[so], %[t0] offen\n\t"
        "s_sleep 4\n\t"
        "s_add_u32 %[t0], %[t1], 131072\n\t"
        "buffer_load_dwordx4 a[80:83], v100, %[so], %[t0] offen\n\t"
        "s_sleep 4\n\t"
        "s_add_u32 %[t0], %[t1], 163840\n\t"
        "buffer_load_dwordx4 a[84:87], v100, %[so], %[t0] offen\n\t"
        "s_sleep 4\n\t"
        "s_add_u32 %[t0], %[t1], 196608\n\t"
        "buffer_load_dwordx4 a[88:91], v100, %[so], %[t0] offen\n\t"
        "s_sleep 4\n\t"
        "s_add_u32 %[t0], %[t1], 229376\n\t"
        "buffer_load_dwordx4 a[92:95], v100, %[so], %[t0] offen\n\t"
        "s_sleep 4\n\t"
        "s_waitcnt vmcnt(8)\n\t"
        "ds_write_b128 v101, a[32:35] offset:0\n\t"
        "ds_write_b128 v101, a[36:39] offset:1024\n\t"
        "ds_write_b128 v101, a[40:43] offset:2048\n\t"
        "ds_write_b128 v101, a[44:47] offset:3072\n\t"
        "ds_write_b128 v101, a[48:51] offset:4096\n\t"
        "ds_write_b128 v101, a[52:55] offset:5120\n\t"
        "ds_write_b128 v101, a[56:59] offset:6144\n\t"
        "ds_write_b128 v101, a[60:63] offset:7168\n\t"
        "ds_read_b64_tr_b16 a[32:33], v102 offset:0\n\t"
        "ds_read_b64_tr_b16 a[34:35], v102 offset:1024\n\t"
        "ds_read_b64_tr_b16 a[36:37], v102 offset:32\n\t"
        "ds_read_b64_tr_b16 a[38:39], v102 offset:1056\n\t"
        "s_waitcnt lgkmcnt(8)\n\t"
        "ds_read_b64_tr_b16 a[40:41], v102 offset:2048\n\t"
        "ds_read_b64_tr_b16 a[42:43], v102 offset:3072\n\t"
        "ds_read_b64_tr_b16 a[44:45], v102 offset:2080\n\t"
        "ds_read_b64_tr_b16 a[46:47], v102 offset:3104\n\t"
        "s_waitcnt lgkmcnt(8)\n\t"
        "ds_read_b64_tr_b16 a[48:49], v102 offset:4096\n\t"
        "ds_read_b64_tr_b16 a[50:51], v102 offset:5120\n\t"
        "ds_read_b64_tr_b16 a[52:53], v102 offset:4128\n\t"
        "ds_read_b64_tr_b16 a[54:55], v102 offset:5152\n\t"
        "s_waitcnt lgkmcnt(8)\n\t"
        "ds_read_b64_tr_b16 a[56:57], v102 offset:6144\n\t"
        "ds_read_b64_tr_b16 a[58:59], v102 offset:7168\n\t"
        "ds_read_b64_tr_b16 a[60:61], v102 offset:6176\n\t"
        "ds_read_b64_tr_b16 a[62:63], v102 offset:7200\n\t"
        "s_waitcnt lgkmcnt(8)\n\t"
        "s_add_u32 %[t1], %[ro], 3\n\t"
        "s_and_b32 %[t1], %[t1], 7\n\t"
        "s_lshl_b32 %[t1], %[t1], 18\n\t"
        "s_add_u32 %[t0], %[t1], 0\n\t"
        "buffer_load_dwordx4 a[96:99], v100, %[so], %[t0] offen\n\t"
        "s_sleep 4\n\t"
        "s_add_u32 %[t0], %[t1], 32768\n\t"
        "buffer_load_dwordx4 a[100:103], v100, %[so], %[t0] offen\n\t"
        "s_sleep 4\n\t"
        "s_add_u32 %[t0], %[t1], 65536\n\t"
        "buffer_load_dwordx4 a[104:107], v100, %[so], %[t0] offen\n\t"
        "s_sleep 4\n\t"
        "s_add_u32 %[t0], %[t1], 98304\n\t"
        "buffer_load_dwordx4 a[108:111], v100, %[so], %[t0] offen\n\t"
        "s_sleep 4\n\t"
        "s_add_u32 %[t0], %[t1], 131072\n\t"
        "buffer_load_dwordx4 a[112:115], v100, %[so], %[t0] offen\n\t"
        "s_sleep 4\n\t"
        "s_add_u32 %[t0], %[t1], 163840\n\t"
        "buffer_load_dwordx4 a[116:119], v100, %[so], %[t0] offen\n\t"
        "s_sleep 4\n\t"
        "s_add_u32 %[t0], %[t1], 196608\n\t"
        "buffer_load_dwordx4 a[120:123], v100, %[so], %[t0] offen\n\t"
        "s_sleep 4\n\t"
        "s_add_u32 %[t0], %[t1], 229376\n\t"
        "buffer_load_dwordx4 a[124:127], v100, %[so], %[t0] offen\n\t"
        "s_sleep 4\n\t"
        "s_waitcnt vmcnt(8)\n\t"
        "ds_write_b128 v101, a[64:67] offset:0\n\t"
        "ds_write_b128 v101, a[68:71] offset:1024\n\t"
        "ds_write_b128 v101, a[72:75] offset:2048\n\t"
        "ds_write_b128 v101, a[76:79] offset:3072\n\t"
        "ds_write_b128 v101, a[80:83] offset:4096\n\t"
        "ds_write_b128 v101, a[84:87] offset:5120\n\t"
        "ds_write_b128 v101, a[88:91] offset:6144\n\t"
        "ds_write_b128 v101, a[92:95] offset:7168\n\t"
        "ds_read_b64_tr_b16 a[64:65], v102 offset:0\n\t"
        "ds_read_b64_tr_b16 a[66:67], v102 offset:1024\n\t"
        "ds_read_b64_tr_b16 a[68:69], v102 offset:32\n\t"
        "ds_read_b64_tr_b16 a[70:71], v102 offset:1056\n\t"
        "s_waitcnt lgkmcnt(8)\n\t"
        "ds_read_b64_tr_b16 a[72:73], v102 offset:2048\n\t"
        "ds_read_b64_tr_b16 a[74:75], v102 offset:3072\n\t"
        "ds_read_b64_tr_b16 a[76:77], v102 offset:2080\n\t"
        "ds_read_b64_tr_b16 a[78:79], v102 offset:3104\n\t"
        "s_waitcnt lgkmcnt(8)\n\t"
        "ds_read_b64_tr_b16 a[80:81], v102 offset:4096\n\t"
        "ds_read_b64_tr_b16 a[82:83], v102 offset:5120\n\t"
        "ds_read_b64_tr_b16 a[84:85], v102 offset:4128\n\t"
        "ds_read_b64_tr_b16 a[86:87], v102 offset:5152\n\t"
        "s_waitcnt lgkmcnt(8)\n\t"
        "ds_read_b64_tr_b16 a[88:89], v102 offset:6144\n\t"
        "ds_read_b64_tr_b16 a[90:91], v102 offset:7168\n\t"
        "ds_read_b64_tr_b16 a[92:93], v102 offset:6176\n\t"
        "ds_read_b64_tr_b16 a[94:95], v102 offset:7200\n\t"
        "s_waitcnt lgkmcnt(8)\n\t"
        "s_add_u32 %[t1], %[ro], 4\n\t"
        "s_and_b32 %[t1], %[t1], 7\n\t"
        "s_lshl_b32 %[t1], %[t1], 18\n\t"
        "s_add_u32 %[t0], %[t1], 0\n\t"
        "buffer_load_dwordx4 a[128:131], v100, %[so], %[t0] offen\n\t"
        "s_sleep 4\n\t"
        "s_add_u32 %[t0], %[t1], 32768\n\t"
        "buffer_load_dwordx4 a[132:135], v100, %[so], %[t0] offen\n\t"
        "s_sleep 4\n\t"
        "s_add_u32 %[t0], %[t1], 65536\n\t"
        "buffer_load_dwordx4 a[136:139], v100, %[so], %[t0] offen\n\t"
        "s_sleep 4\n\t"
        "s_add_u32 %[t0], %[t1], 98304\n\t"
        "buffer_load_dwordx4 a[140:143], v100, %[so], %[t0] offen\n\t"
        "s_sleep 4\n\t"
        "s_add_u32 %[t0], %[t1], 131072\n\t"
        "buffer_load_dwordx4 a[144:147], v100, %[so], %[t0] offen\n\t"
        "s_sleep 4\n\t"
        "s_add_u32 %[t0], %[t1], 163840\n\t"
        "buffer_load_dwordx4 a[148:151], v100, %[so], %[t0] offen\n\t"
        "s_sleep 4\n\t"
        "s_add_u32 %[t0], %[t1], 196608\n\t"
        "buffer_load_dwordx4 a[152:155], v100, %[so], %[t0] offen\n\t"
        "s_sleep 4\n\t"
        "s_add_u32 %[t0], %[t1], 229376\n\t"
        "buffer_load_dwordx4 a[156:159], v100, %[so], %[t0] offen\n\t"
        "s_sleep 4\n\t"
        "s_waitcnt vmcnt(8)\n\t"
        "ds_write_b128 v101, a[96:99] offset:0\n\t"
        "ds_write_b128 v101, a[100:103] offset:1024\n\t"
        "ds_write_b128 v101, a[104:107] offset:2048\n\t"
        "ds_write_b128 v101, a[108:111] offset:3072\n\t"
        "ds_write_b128 v101, a[112:115] offset:4096\n\t"
        "ds_write_b128 v101, a[116:119] offset:5120\n\t"
        "ds_write_b128 v101, a[120:123] offset:6144\n\t"
        "ds_write_b128 v101, a[124:127] offset:7168\n\t"
        "ds_read_b64_tr_b16 a[96:97], v102 offset:0\n\t"
        "ds_read_b64_tr_b16 a[98:99], v102 offset:1024\n\t"
        "ds_read_b64_tr_b16 a[100:101], v102 offset:32\n\t"
        "ds_read_b64_tr_b16 a[102:103], v102 offset:1056\n\t"
        "s_waitcnt lgkmcnt(8)\n\t"
        "ds_read_b64_tr_b16 a[104:105], v102 offset:2048\n\t"
        "ds_read_b64_tr_b16 a[106:107], v102 offset:3072\n\t"
        "ds_read_b64_tr_b16 a[108:109], v102 offset:2080\n\t"
        "ds_read_b64_tr_b16 a[110:111], v102 offset:3104\n\t"
        "s_waitcnt lgkmcnt(8)\n\t"
        "ds_read_b64_tr_b16 a[112:113], v102 offset:4096\n\t"
        "ds_read_b64_tr_b16 a[114:115], v102 offset:5120\n\t"
        "ds_read_b64_tr_b16 a[116:117], v102 offset:4128\n\t"
        "ds_read_b64_tr_b16 a[118:119], v102 offset:5152\n\t"
        "s_waitcnt lgkmcnt(8)\n\t"
        "ds_read_b64_tr_b16 a[120:121], v102 offset:6144\n\t"
        "ds_read_b64_tr_b16 a[122:123], v102 offset:7168\n\t"
        "ds_read_b64_tr_b16 a[124:125], v102 offset:6176\n\t"
        "ds_read_b64_tr_b16 a[126:127], v102 offset:7200\n\t"
        "s_waitcnt lgkmcnt(8)\n\t"
        "s_add_u32 %[t1], %[ro], 5\n\t"
        "s_and_b32 %[t1], %[t1], 7\n\t"
        "s_lshl_b32 %[t1], %[t1], 18\n\t"
        "s_add_u32 %[t0], %[t1], 0\n\t"
        "buffer_load_dwordx4 a[160:163], v100, %[so], %[t0] offen\n\t"
        "s_sleep 4\n\t"
        "s_add_u32 %[t0], %[t1], 32768\n\t"
        "buffer_load_dwordx4 a[164:167], v100, %[so], %[t0] offen\n\t"
        "s_sleep 4\n\t"
        "s_add_u32 %[t0], %[t1], 65536\n\t"
        "buffer_load_dwordx4 a[168:171], v100, %[so], %[t0] offen\n\t"
        "s_sleep 4\n\t"
        "s_add_u32 %[t0], %[t1], 98304\n\t"
        "buffer_load_dwordx4 a[172:175], v100, %[so], %[t0] offen\n\t"
        "s_sleep 4\n\t"
        "s_add_u32 %[t0], %[t1], 131072\n\t"
        "buffer_load_dwordx4 a[176:179], v100, %[so], %[t0] offen\n\t"
        "s_sleep 4\n\t"
        "s_add_u32 %[t0], %[t1], 163840\n\t"
        "buffer_load_dwordx4 a[180:183], v100, %[so], %[t0] offen\n\t"
        "s_sleep 4\n\t"
        "s_add_u32 %[t0], %[t1], 196608\n\t"
        "buffer_load_dwordx4 a[184:187], v100, %[so], %[t0] offen\n\t"
        "s_sleep 4\n\t"
        "s_add_u32 %[t0], %[t1], 229376\n\t"
        "buffer_load_dwordx4 a[188:191], v100, %[so], %[t0] offen\n\t"
        "s_sleep 4\n\t"
        "s_waitcnt vmcnt(8)\n\t"
        "ds_write_b128 v101, a[128:131] offset:0\n\t"
        "ds_write_b128 v101, a[132:135] offset:1024\n\t"
        "ds_write_b128 v101, a[136:139] offset:2048\n\t"
        "ds_write_b128 v101, a[140:143] offset:3072\n\t"
        "ds_write_b128 v101, a[144:147] offset:4096\n\t"
        "ds_write_b128 v101, a[148:151] offset:5120\n\t"
        "ds_write_b128 v101, a[152:155] offset:6144\n\t"
        "ds_write_b128 v101, a[156:159] offset:7168\n\t"
        "ds_read_b64_tr_b16 a[128:129], v102 offset:0\n\t"
        "ds_read_b64_tr_b16 a[130:131], v102 offset:1024\n\t"
        "ds_read_b64_tr_b16 a[132:133], v102 offset:32\n\t"
        "ds_read_b64_tr_b16 a[134:135], v102 offset:1056\n\t"
        "s_waitcnt lgkmcnt(8)\n\t"
        "ds_read_b64_tr_b16 a[136:137], v102 offset:2048\n\t"
        "ds_read_b64_tr_b16 a[138:139], v102 offset:3072\n\t"
        "ds_read_b64_tr_b16 a[140:141], v102 offset:2080\n\t"
        "ds_read_b64_tr_b16 a[142:143], v102 offset:3104\n\t"
        "s_waitcnt lgkmcnt(8)\n\t"
        "ds_read_b64_tr_b16 a[144:145], v102 offset:4096\n\t"
        "ds_read_b64_tr_b16 a[146:147], v102 offset:5120\n\t"
        "ds_read_b64_tr_b16 a[148:149], v102 offset:4128\n\t"
        "ds_read_b64_tr_b16 a[150:151], v102 offset:5152\n\t"
        "s_waitcnt lgkmcnt(8)\n\t"
        "ds_read_b64_tr_b16 a[152:153], v102 offset:6144\n\t"
        "ds_read_b64_tr_b16 a[154:155], v102 offset:7168\n\t"
        "ds_read_b64_tr_b16 a[156:157], v102 offset:6176\n\t"
        "ds_read_b64_tr_b16 a[158:159], v102 offset:7200\n\t"
        "s_waitcnt lgkmcnt(8)\n\t"
        "s_add_u32 %[t1], %[ro], 6\n\t"
        "s_and_b32 %[t1], %[t1], 7\n\t"
        "s_lshl_b32 %[t1], %[t1], 18\n\t"
        "s_add_u32 %[t0], %[t1], 0\n\t"
        "buffer_load_dwordx4 a[192:195], v100, %[so], %[t0] offen\n\t"
        "s_sleep 4\n\t"
        "s_add_u32 %[t0], %[t1], 32768\n\t"
        "buffer_load_dwordx4 a[196:199], v100, %[so], %[t0] offen\n\t"
        "s_sleep 4\n\t"
        "s_add_u32 %[t0], %[t1], 65536\n\t"
        "buffer_load_dwordx4 a[200:203], v100, %[so], %[t0] offen\n\t"
        "s_sleep 4\n\t"
        "s_add_u32 %[t0], %[t1], 98304\n\t"
        "buffer_load_dwordx4 a[204:207], v100, %[so], %[t0] offen\n\t"
        "s_sleep 4\n\t"
        "s_add_u32 %[t0], %[t1], 131072\n\t"
        "buffer_load_dwordx4 a[208:211], v100, %[so], %[t0] offen\n\t"
        "s_sleep 4\n\t"
        "s_add_u32 %[t0], %[t1], 163840\n\t"
        "buffer_load_dwordx4 a[212:215], v100, %[so], %[t0] offen\n\t"
        "s_sleep 4\n\t"
        "s_add_u32 %[t0], %[t1], 196608\n\t"
        "buffer_load_dwordx4 a[216:219], v100, %[so], %[t0] offen\n\t"
        "s_sleep 4\n\t"
        "s_add_u32 %[t0], %[t1], 229376\n\t"
        "buffer_load_dwordx4 a[220:223], v100, %[so], %[t0] offen\n\t"
        "s_sleep 4\n\t"
        "s_waitcnt vmcnt(8)\n\t"
        "ds_write_b128 v101, a[160:163] offset:0\n\t"
        "ds_write_b128 v101, a[164:167] offset:1024\n\t"
        "ds_write_b128 v101, a[168:171] offset:2048\n\t"
        "ds_write_b128 v101, a[172:175] offset:3072\n\t"
        "ds_write_b128 v101, a[176:179] offset:4096\n\t"
        "ds_write_b128 v101, a[180:183] offset:5120\n\t"
        "ds_write_b128 v101, a[184:187] offset:6144\n\t"
        "ds_write_b128 v101, a[188:191] offset:7168\n\t"
        "ds_read_b64_tr_b16 a[160:161], v102 offset:0\n\t"
        "ds_read_b64_tr_b16 a[162:163], v102 offset:1024\n\t"
        "ds_read_b64_tr_b16 a[164:165], v102 offset:32\n\t"
        "ds_read_b64_tr_b16 a[166:167], v102 offset:1056\n\t"
        "s_waitcnt lgkmcnt(8)\n\t"
        "ds_read_b64_tr_b16 a[168:169], v102 offset:2048\n\t"
        "ds_read_b64_tr_b16 a[170:171], v102 offset:3072\n\t"
        "ds_read_b64_tr_b16 a[172:173], v102 offset:2080\n\t"
        "ds_read_b64_tr_b16 a[174:175], v102 offset:3104\n\t"
        "s_waitcnt lgkmcnt(8)\n\t"
        "ds_read_b64_tr_b16 a[176:177], v102 offset:4096\n\t"
        "ds_read_b64_tr_b16 a[178:179], v102 offset:5120\n\t"
        "ds_read_b64_tr_b16 a[180:181], v102 offset:4128\n\t"
        "ds_read_b64_tr_b16 a[182:183], v102 offset:5152\n\t"
        "s_waitcnt lgkmcnt(8)\n\t"
        "ds_read_b64_tr_b16 a[184:185], v102 offset:6144\n\t"
        "ds_read_b64_tr_b16 a[186:187], v102 offset:7168\n\t"
        "ds_read_b64_tr_b16 a[188:189], v102 offset:6176\n\t"
        "ds_read_b64_tr_b16 a[190:191], v102 offset:7200\n\t"
        "s_waitcnt lgkmcnt(8)\n\t"
        "s_add_u32 %[t1], %[ro], 7\n\t"
        "s_and_b32 %[t1], %[t1], 7\n\t"
        "s_lshl_b32 %[t1], %[t1], 18\n\t"
        "s_add_u32 %[t0], %[t1], 0\n\t"
        "buffer_load_dwordx4 a[224:227], v100, %[so], %[t0] offen\n\t"
        "s_sleep 4\n\t"
        "s_add_u32 %[t0], %[t1], 32768\n\t"
        "buffer_load_dwordx4 a[228:231], v100, %[so], %[t0] offen\n\t"
        "s_sleep 4\n\t"
        "s_add_u32 %[t0], %[t1], 65536\n\t"
        "buffer_load_dwordx4 a[232:235], v100, %[so], %[t0] offen\n\t"
        "s_sleep 4\n\t"
        "s_add_u32 %[t0], %[t1], 98304\n\t"
        "buffer_load_dwordx4 a[236:239], v100, %[so], %[t0] offen\n\t"
        "s_sleep 4\n\t"
        "s_add_u32 %[t0], %[t1], 131072\n\t"
        "buffer_load_dwordx4 a[240:243], v100, %[so], %[t0] offen\n\t"
        "s_sleep 4\n\t"
        "s_add_u32 %[t0], %[t1], 163840\n\t"
        "buffer_load_dwordx4 a[244:247], v100, %[so], %[t0] offen\n\t"
        "s_sleep 4\n\t"
        "s_add_u32 %[t0], %[t1], 196608\n\t"
        "buffer_load_dwordx4 a[248:251], v100, %[so], %[t0] offen\n\t"
        "s_sleep 4\n\t"
        "s_add_u32 %[t0], %[t1], 229376\n\t"
        "buffer_load_dwordx4 a[252:255], v100, %[so], %[t0] offen\n\t"
        "s_sleep 4\n\t"
        "s_waitcnt vmcnt(8)\n\t"
        "ds_write_b128 v101, a[192:195] offset:0\n\t"
        "ds_write_b128 v101, a[196:199] offset:1024\n\t"
        "ds_write_b128 v101, a[200:203] offset:2048\n\t"
        "ds_write_b128 v101, a[204:207] offset:3072\n\t"
        "ds_write_b128 v101, a[208:211] offset:4096\n\t"
        "ds_write_b128 v101, a[212:215] offset:5120\n\t"
        "ds_write_b128 v101, a[216:219] offset:6144\n\t"
        "ds_write_b128 v101, a[220:223] offset:7168\n\t"
        "ds_read_b64_tr_b16 a[192:193], v102 offset:0\n\t"
        "ds_read_b64_tr_b16 a[194:195], v102 offset:1024\n\t"
        "ds_read_b64_tr_b16 a[196:197], v102 offset:32\n\t"
        "ds_read_b64_tr_b16 a[198:199], v102 offset:1056\n\t"
        "s_waitcnt lgkmcnt(8)\n\t"
        "ds_read_b64_tr_b16 a[200:201], v102 offset:2048\n\t"
        "ds_read_b64_tr_b16 a[202:203], v102 offset:3072\n\t"
        "ds_read_b64_tr_b16 a[204:205], v102 offset:2080\n\t"
        "ds_read_b64_tr_b16 a[206:207], v102 offset:3104\n\t"
        "s_waitcnt lgkmcnt(8)\n\t"
        "ds_read_b64_tr_b16 a[208:209], v102 offset:4096\n\t"
        "ds_read_b64_tr_b16 a[210:211], v102 offset:5120\n\t"
        "ds_read_b64_tr_b16 a[212:213], v102 offset:4128\n\t"
        "ds_read_b64_tr_b16 a[214:215], v102 offset:5152\n\t"
        "s_waitcnt lgkmcnt(8)\n\t"
        "ds_read_b64_tr_b16 a[216:217], v102 offset:6144\n\t"
        "ds_read_b64_tr_b16 a[218:219], v102 offset:7168\n\t"
        "ds_read_b64_tr_b16 a[220:221], v102 offset:6176\n\t"
        "ds_read_b64_tr_b16 a[222:223], v102 offset:7200\n\t"
        "s_waitcnt lgkmcnt(8)\n\t"
        "s_waitcnt vmcnt(0)\n\t"
        "ds_write_b128 v101, a[224:227] offset:0\n\t"
        "ds_write_b128 v101, a[228:231] offset:1024\n\t"
        "ds_write_b128 v101, a[232:235] offset:2048\n\t"
        "ds_write_b128 v101, a[236:239] offset:3072\n\t"
        "ds_write_b128 v101, a[240:243] offset:4096\n\t"
        "ds_write_b128 v101, a[244:247] offset:5120\n\t"
        "ds_write_b128 v101, a[248:251] offset:6144\n\t"
        "ds_write_b128 v101, a[252:255] offset:7168\n\t"
        "ds_read_b64_tr_b16 a[224:225], v102 offset:0\n\t"
        "ds_read_b64_tr_b16 a[226:227], v102 offset:1024\n\t"
        "ds_read_b64_tr_b16 a[228:229], v102 offset:32\n\t"
        "ds_read_b64_tr_b16 a[230:231], v102 offset:1056\n\t"
        "s_waitcnt lgkmcnt(8)\n\t"
        "ds_read_b64_tr_b16 a[232:233], v102 offset:2048\n\t"
        "ds_read_b64_tr_b16 a[234:235], v102 offset:3072\n\t"
        "ds_read_b64_tr_b16 a[236:237], v102 offset:2080\n\t"
        "ds_read_b64_tr_b16 a[238:239], v102 offset:3104\n\t"
        "s_waitcnt lgkmcnt(8)\n\t"
        "ds_read_b64_tr_b16 a[240:241], v102 offset:4096\n\t"
        "ds_read_b64_tr_b16 a[242:243], v102 offset:5120\n\t"
        "ds_read_b64_tr_b16 a[244:245], v102 offset:4128\n\t"
        "ds_read_b64_tr_b16 a[246:247], v102 offset:5152\n\t"
        "s_waitcnt lgkmcnt(8)\n\t"
        "ds_read_b64_tr_b16 a[248:249], v102 offset:6144\n\t"
        "ds_read_b64_tr_b16 a[250:251], v102 offset:7168\n\t"
        "ds_read_b64_tr_b16 a[252:253], v102 offset:6176\n\t"
        "ds_read_b64_tr_b16 a[254:255], v102 offset:7200\n\t"
        "s_waitcnt lgkmcnt(8)\n\t"
        "s_waitcnt lgkmcnt(0)\n\t"
        "s_mov_b32 %[sp], 0\n\t"
        "LP_cv%=:\n\t"
        "buffer_load_dword v28, off, %[sw], %[cv] sc1\n\t"
        "s_waitcnt vmcnt(0)\n\t"
        "v_readfirstlane_b32 %[t0], v28\n\t"
        "s_cmp_ge_u32 %[t0], 4\n\t"
        "s_cbranch_scc1 LP_cvok%=\n\t"
        "s_sleep 8\n\t"
        "s_add_u32 %[sp], %[sp], 1\n\t"
        "s_cmp_lt_u32 %[sp], 0x100000\n\t"
        "s_cbranch_scc1 LP_cv%=\n\t"
        "v_mov_b32_e32 v28, 256\n\t"
        "s_mov_b32 %[t0], 33024\n\t"
        "buffer_store_dword v28, off, %[sw], %[t0] sc1\n\t"
        "s_endpgm\n\t"
        "LP_cvok%=:\n\t"
        "s_barrier\n\t"
        "LP_wait%=:\n\t"
        "s_mov_b32 %[sp], 0\n\t"
        "LP_poll%=:\n\t"
        "buffer_load_dword v26, v104, %[sw], 0 offen sc1\n\t"
        "s_lshr_b32 %[t1], %[tl], 1\n\t"
        "s_waitcnt vmcnt(0)\n\t"
        "v_min_u32_dpp v26, v26, v26 quad_perm:[1,0,3,2] row_mask:0xf bank_mask:0xf\n\t"
        "s_nop 1\n\t"
        "v_min_u32_dpp v26, v26, v26 quad_perm:[2,3,0,1] row_mask:0xf bank_mask:0xf\n\t"
        "s_nop 1\n\t"
        "v_min_u32_dpp v26, v26, v26 row_ror:4 row_mask:0xf bank_mask:0xf\n\t"
        "s_nop 0\n\t"
        "v_readfirstlane_b32 %[pg], v26\n\t"
        "s_cmp_gt_u32 %[pg], %[t1]\n\t"
        "s_cbranch_scc1 LP_have%=\n\t"
        "s_cmp_eq_u32 %[pg], %[t1]\n\t"
        "s_cbranch_scc1 LP_near%=\n\t"
        "s_sleep 24\n\t"
        "LP_near%=:\n\t"
        "s_sleep 2\n\t"
        "s_add_u32 %[sp], %[sp], 1\n\t"
        "s_cmp_lt_u32 %[sp], 0x100000\n\t"
        "s_cbranch_scc1 LP_poll%=\n\t"
        "v_mov_b32_e32 v28, 128\n\t"
        "s_mov_b32 %[t0], 33024\n\t"
        "buffer_store_dword v28, off, %[sw], %[t0] sc1\n\t"
        "s_endpgm\n\t"
        "LP_have%=:\n\t"
        "s_lshl_b32 %[nx], %[tl], 17\n\t"
        "s_add_u32 %[t0], %[ro], 0\n\t"
        "s_and_b32 %[t0], %[t0], 7\n\t"
        "s_lshl_b32 %[t0], %[t0], 12\n\t"
        "s_add_u32 %[t0], %[t0], %[nx]\n\t"
        "buffer_load_dwordx4 v[128:131], v103, %[sw], %[t0] offen offset:0 sc1\n\t"
        "buffer_load_dwordx4 v[132:135], v103, %[sw], %[t0] offen offset:1024 sc1\n\t"
        "buffer_load_dwordx4 v[136:139], v103, %[sw], %[t0] offen offset:2048 sc1\n\t"
        "buffer_load_dwordx4 v[140:143], v103, %[sw], %[t0] offen offset:3072 sc1\n\t"
        "s_add_u32 %[t0], %[ro], 1\n\t"
        "s_and_b32 %[t0], %[t0], 7\n\t"
        "s_lshl_b32 %[t0], %[t0], 12\n\t"
        "s_add_u32 %[t0], %[t0], %[nx]\n\t"
        "buffer_load_dwordx4 v[144:147], v103, %[sw], %[t0] offen offset:0 sc1\n\t"
        "buffer_load_dwordx4 v[148:151], v103, %[sw], %[t0] offen offset:1024 sc1\n\t"
        "buffer_load_dwordx4 v[152:155], v103, %[sw], %[t0] offen offset:2048 sc1\n\t"
        "buffer_load_dwordx4 v[156:159], v103, %[sw], %[t0] offen offset:3072 sc1\n\t"
        "s_add_u32 %[t0], %[ro], 2\n\t"
        "s_and_b32 %[t0], %[t0], 7\n\t"
        "s_lshl_b32 %[t0], %[t0], 12\n\t"
        "s_add_u32 %[t0], %[t0], %[nx]\n\t"
        "buffer_load_dwordx4 v[160:163], v103, %[sw], %[t0] offen offset:0 sc1\n\t"
        "buffer_load_dwordx4 v[164:167], v103, %[sw], %[t0] offen offset:1024 sc1\n\t"
        "buffer_load_dwordx4 v[168:171], v103, %[sw], %[t0] offen offset:2048 sc1\n\t"
        "buffer_load_dwordx4 v[172:175], v103, %[sw], %[t0] offen offset:3072 sc1\n\t"
        "s_add_u32 %[t0], %[ro], 3\n\t"
        "s_and_b32 %[t0], %[t0], 7\n\t"
        "s_lshl_b32 %[t0], %[t0], 12\n\t"
        "s_add_u32 %[t0], %[t0], %[nx]\n\t"
        "buffer_load_dwordx4 v[176:179], v103, %[sw], %[t0] offen offset:0 sc1\n\t"
        "buffer_load_dwordx4 v[180:183], v103, %[sw], %[t0] offen offset:1024 sc1\n\t"
        "buffer_load_dwordx4 v[184:187], v103, %[sw], %[t0] offen offset:2048 sc1\n\t"
        "buffer_load_dwordx4 v[188:191], v103, %[sw], %[t0] offen offset:3072 sc1\n\t"
        "s_add_u32 %[t0], %[ro], 4\n\t"
        "s_and_b32 %[t0], %[t0], 7\n\t"
        "s_lshl_b32 %[t0], %[t0], 12\n\t"
        "s_add_u32 %[t0], %[t0], %[nx]\n\t"
        "buffer_load_dwordx4 v[192:195], v103, %[sw], %[t0] offen offset:0 sc1\n\t"
        "buffer_load_dwordx4 v[196:199], v103, %[sw], %[t0] offen offset:1024 sc1\n\t"
        "buffer_load_dwordx4 v[200:203], v103, %[sw], %[t0] offen offset:2048 sc1\n\t"
        "buffer_load_dwordx4 v[204:207], v103, %[sw], %[t0] offen offset:3072 sc1\n\t"
        "s_add_u32 %[t0], %[ro], 5\n\t"
        "s_and_b32 %[t0], %[t0], 7\n\t"
        "s_lshl_b32 %[t0], %[t0], 12\n\t"
        "s_add_u32 %[t0], %[t0], %[nx]\n\t"
        "buffer_load_dwordx4 v[208:211], v103, %[sw], %[t0] offen offset:0 sc1\n\t"
        "buffer_load_dwordx4 v[212:215], v103, %[sw], %[t0] offen offset:1024 sc1\n\t"
        "buffer_load_dwordx4 v[216:219], v103, %[sw], %[t0] offen offset:2048 sc1\n\t"
        "buffer_load_dwordx4 v[220:223], v103, %[sw], %[t0] offen offset:3072 sc1\n\t"
        "s_add_u32 %[t0], %[ro], 6\n\t"
        "s_and_b32 %[t0], %[t0], 7\n\t"
        "s_lshl_b32 %[t0], %[t0], 12\n\t"
        "s_add_u32 %[t0], %[t0], %[nx]\n\t"
        "buffer_load_dwordx4 v[224:227], v103, %[sw], %[t0] offen offset:0 sc1\n\t"
        "buffer_load_dwordx4 v[228:231], v103, %[sw], %[t0] offen offset:1024 sc1\n\t"
        "buffer_load_dwordx4 v[232:235], v103, %[sw], %[t0] offen offset:2048 sc1\n\t"
        "buffer_load_dwordx4 v[236:239], v103, %[sw], %[t0] offen offset:3072 sc1\n\t"
        "s_add_u32 %[t0], %[ro], 7\n\t"
        "s_and_b32 %[t0], %[t0], 7\n\t"
        "s_lshl_b32 %[t0], %[t0], 12\n\t"
        "s_add_u32 %[t0], %[t0], %[nx]\n\t"
        "buffer_load_dwordx4 v[240:243], v103, %[sw], %[t0] offen offset:0 sc1\n\t"
        "buffer_load_dwordx4 v[244:247], v103, %[sw], %[t0] offen offset:1024 sc1\n\t"
        "buffer_load_dwordx4 v[248:251], v103, %[sw], %[t0] offen offset:2048 sc1\n\t"
        "buffer_load_dwordx4 v[252:255], v103, %[sw], %[t0] offen offset:3072 sc1\n\t"
        "buffer_load_dword v26, v104, %[sw], 0 offen sc1\n\t"
        "LP_tile%=:\n\t"
        "s_and_b32 %[t0], %[tl], 1\n\t"
        "s_lshl_b32 %[t0], %[t0], 14\n\t"
        "s_lshr_b32 %[t1], %[tl], 1\n\t"
        "s_lshl_b32 %[t1], %[t1], 6\n\t"
        "s_add_u32 %[t0], %[t0], %[t1]\n\t"
        "buffer_load_dword v24, v105, %[sw], %[t0] offen sc1\n\t"
        "s_lshl_b32 %[t0], %[tl], 6\n\t"
        "buffer_load_dword v25, v106, %[sw], %[t0] offen sc1\n\t"
        "buffer_load_dword v26, v104, %[sw], 0 offen sc1\n\t"
        "s_add_u32 %[t1], %[tl], 4\n\t"
        "s_lshl_b32 %[nx], %[t1], 17\n\t"
        "s_lshr_b32 %[t1], %[t1], 1\n\t"
        "s_cmp_gt_u32 %[pg], %[t1]\n\t"
        "s_cselect_b32 %[nx], %[nx], 0xf0000000\n\t"
        "s_waitcnt vmcnt(35)\n\t"
        "v_mfma_f32_16x16x32_bf16 v[8:11], a[0:3], v[128:131], 0\n\t"
        "v_mfma_f32_16x16x32_bf16 v[12:15], a[4:7], v[128:131], 0\n\t"
        "s_add_u32 %[t0], %[ro], 0\n\t"
        "s_and_b32 %[t0], %[t0], 7\n\t"
        "s_lshl_b32 %[t0], %[t0], 12\n\t"
        "s_add_u32 %[t0], %[t0], %[nx]\n\t"
        "buffer_load_dwordx4 v[128:131], v103, %[sw], %[t0] offen offset:0 sc1\n\t"
        "s_waitcnt vmcnt(35)\n\t"
        "v_mfma_f32_16x16x32_bf16 v[8:11], a[8:11], v[132:135], v[8:11]\n\t"
        "v_mfma_f32_16x16x32_bf16 v[12:15], a[12:15], v[132:135], v[12:15]\n\t"
        "buffer_load_dwordx4 v[132:135], v103, %[sw], %[t0] offen offset:1024 sc1\n\t"
        "s_waitcnt vmcnt(35)\n\t"
        "v_mfma_f32_16x16x32_bf16 v[8:11], a[16:19], v[136:139], v[8:11]\n\t"
        "v_mfma_f32_16x16x32_bf16 v[12:15], a[20:23], v[136:139], v[12:15]\n\t"
        "buffer_load_dwordx4 v[136:139], v103, %[sw], %[t0] offen offset:2048 sc1\n\t"
        "s_waitcnt vmcnt(35)\n\t"
        "v_mfma_f32_16x16x32_bf16 v[8:11], a[24:27], v[140:143], v[8:11]\n\t"
        "v_mfma_f32_16x16x32_bf16 v[12:15], a[28:31], v[140:143], v[12:15]\n\t"
        "buffer_load_dwordx4 v[140:143], v103, %[sw], %[t0] offen offset:3072 sc1\n\t"
        "s_waitcnt vmcnt(35)\n\t"
        "v_mfma_f32_16x16x32_bf16 v[8:11], a[32:35], v[144:147], v[8:11]\n\t"
        "v_mfma_f32_16x16x32_bf16 v[12:15], a[36:39], v[144:147], v[12:15]\n\t"
        "s_add_u32 %[t0], %[ro], 1\n\t"
        "s_and_b32 %[t0], %[t0], 7\n\t"
        "s_lshl_b32 %[t0], %[t0], 12\n\t"
        "s_add_u32 %[t0], %[t0], %[nx]\n\t"
        "buffer_load_dwordx4 v[144:147], v103, %[sw], %[t0] offen offset:0 sc1\n\t"
        "s_waitcnt vmcnt(35)\n\t"
        "v_mfma_f32_16x16x32_bf16 v[8:11], a[40:43], v[148:151], v[8:11]\n\t"
        "v_mfma_f32_16x16x32_bf16 v[12:15], a[44:47], v[148:151], v[12:15]\n\t"
        "buffer_load_dwordx4 v[148:151], v103, %[sw], %[t0] offen offset:1024 sc1\n\t"
        "s_waitcnt vmcnt(35)\n\t"
        "v_mfma_f32_16x16x32_bf16 v[8:11], a[48:51], v[152:155], v[8:11]\n\t"
        "v_mfma_f32_16x16x32_bf16 v[12:15], a[52:55], v[152:155], v[12:15]\n\t"
        "buffer_load_dwordx4 v[152:155], v103, %[sw], %[t0] offen offset:2048 sc1\n\t"
        "s_waitcnt vmcnt(35)\n\t"
        "v_mfma_f32_16x16x32_bf16 v[8:11], a[56:59], v[156:159], v[8:11]\n\t"
        "v_mfma_f32_16x16x32_bf16 v[12:15], a[60:63], v[156:159], v[12:15]\n\t"
        "buffer_load_dwordx4 v[156:159], v103, %[sw], %[t0] offen offset:3072 sc1\n\t"
        "s_waitcnt vmcnt(35)\n\t"
        "v_mfma_f32_16x16x32_bf16 v[8:11], a[64:67], v[160:163], v[8:11]\n\t"
        "v_mfma_f32_16x16x32_bf16 v[12:15], a[68:71], v[160:163], v[12:15]\n\t"
        "s_add_u32 %[t0], %[ro], 2\n\t"
        "s_and_b32 %[t0], %[t0], 7\n\t"
        "s_lshl_b32 %[t0], %[t0], 12\n\t"
        "s_add_u32 %[t0], %[t0], %[nx]\n\t"
        "buffer_load_dwordx4 v[160:163], v103, %[sw], %[t0] offen offset:0 sc1\n\t"
        "s_waitcnt vmcnt(35)\n\t"
        "v_mfma_f32_16x16x32_bf16 v[8:11], a[72:75], v[164:167], v[8:11]\n\t"
        "v_mfma_f32_16x16x32_bf16 v[12:15], a[76:79], v[164:167], v[12:15]\n\t"
        "buffer_load_dwordx4 v[164:167], v103, %[sw], %[t0] offen offset:1024 sc1\n\t"
        "s_waitcnt vmcnt(35)\n\t"
        "v_mfma_f32_16x16x32_bf16 v[8:11], a[80:83], v[168:171], v[8:11]\n\t"
        "v_mfma_f32_16x16x32_bf16 v[12:15], a[84:87], v[168:171], v[12:15]\n\t"
        "buffer_load_dwordx4 v[168:171], v103, %[sw], %[t0] offen offset:2048 sc1\n\t"
        "s_waitcnt vmcnt(35)\n\t"
        "v_mfma_f32_16x16x32_bf16 v[8:11], a[88:91], v[172:175], v[8:11]\n\t"
        "v_mfma_f32_16x16x32_bf16 v[12:15], a[92:95], v[172:175], v[12:15]\n\t"
        "buffer_load_dwordx4 v[172:175], v103, %[sw], %[t0] offen offset:3072 sc1\n\t"
        "s_waitcnt vmcnt(35)\n\t"
        "v_mfma_f32_16x16x32_bf16 v[8:11], a[96:99], v[176:179], v[8:11]\n\t"
        "v_mfma_f32_16x16x32_bf16 v[12:15], a[100:103], v[176:179], v[12:15]\n\t"
        "s_add_u32 %[t0], %[ro], 3\n\t"
        "s_and_b32 %[t0], %[t0], 7\n\t"
        "s_lshl_b32 %[t0], %[t0], 12\n\t"
        "s_add_u32 %[t0], %[t0], %[nx]\n\t"
        "buffer_load_dwordx4 v[176:179], v103, %[sw], %[t0] offen offset:0 sc1\n\t"
        "s_waitcnt vmcnt(35)\n\t"
        "v_mfma_f32_16x16x32_bf16 v[8:11], a[104:107], v[180:183], v[8:11]\n\t"
        "v_mfma_f32_16x16x32_bf16 v[12:15], a[108:111], v[180:183], v[12:15]\n\t"
        "buffer_load_dwordx4 v[180:183], v103, %[sw], %[t0] offen offset:1024 sc1\n\t"
        "s_waitcnt vmcnt(35)\n\t"
        "v_mfma_f32_16x16x32_bf16 v[8:11], a[112:115], v[184:187], v[8:11]\n\t"
        "v_mfma_f32_16x16x32_bf16 v[12:15], a[116:119], v[184:187], v[12:15]\n\t"
        "buffer_load_dwordx4 v[184:187], v103, %[sw], %[t0] offen offset:2048 sc1\n\t"
        "s_waitcnt vmcnt(35)\n\t"
        "v_mfma_f32_16x16x32_bf16 v[8:11], a[120:123], v[188:191], v[8:11]\n\t"
        "v_mfma_f32_16x16x32_bf16 v[12:15], a[124:127], v[188:191], v[12:15]\n\t"
        "buffer_load_dwordx4 v[188:191], v103, %[sw], %[t0] offen offset:3072 sc1\n\t"
        "s_waitcnt vmcnt(35)\n\t"
        "v_mfma_f32_16x16x32_bf16 v[8:11], a[128:131], v[192:195], v[8:11]\n\t"
        "v_mfma_f32_16x16x32_bf16 v[12:15], a[132:135], v[192:195], v[12:15]\n\t"
        "s_add_u32 %[t0], %[ro], 4\n\t"
        "s_and_b32 %[t0], %[t0], 7\n\t"
        "s_lshl_b32 %[t0], %[t0], 12\n\t"
        "s_add_u32 %[t0], %[t0], %[nx]\n\t"
        "buffer_load_dwordx4 v[192:195], v103, %[sw], %[t0] offen offset:0 sc1\n\t"
        "s_waitcnt vmcnt(35)\n\t"
        "v_mfma_f32_16x16x32_bf16 v[8:11], a[136:139], v[196:199], v[8:11]\n\t"
        "v_mfma_f32_16x16x32_bf16 v[12:15], a[140:143], v[196:199], v[12:15]\n\t"
        "buffer_load_dwordx4 v[196:199], v103, %[sw], %[t0] offen offset:1024 sc1\n\t"
        "s_waitcnt vmcnt(35)\n\t"
        "v_mfma_f32_16x16x32_bf16 v[8:11], a[144:147], v[200:203], v[8:11]\n\t"
        "v_mfma_f32_16x16x32_bf16 v[12:15], a[148:151], v[200:203], v[12:15]\n\t"
        "buffer_load_dwordx4 v[200:203], v103, %[sw], %[t0] offen offset:2048 sc1\n\t"
        "s_waitcnt vmcnt(35)\n\t"
        "v_mfma_f32_16x16x32_bf16 v[8:11], a[152:155], v[204:207], v[8:11]\n\t"
        "v_mfma_f32_16x16x32_bf16 v[12:15], a[156:159], v[204:207], v[12:15]\n\t"
        "buffer_load_dwordx4 v[204:207], v103, %[sw], %[t0] offen offset:3072 sc1\n\t"
        "s_waitcnt vmcnt(35)\n\t"
        "v_mfma_f32_16x16x32_bf16 v[8:11], a[160:163], v[208:211], v[8:11]\n\t"
        "v_mfma_f32_16x16x32_bf16 v[12:15], a[164:167], v[208:211], v[12:15]\n\t"
        "s_add_u32 %[t0], %[ro], 5\n\t"
        "s_and_b32 %[t0], %[t0], 7\n\t"
        "s_lshl_b32 %[t0], %[t0], 12\n\t"
        "s_add_u32 %[t0], %[t0], %[nx]\n\t"
        "buffer_load_dwordx4 v[208:211], v103, %[sw], %[t0] offen offset:0 sc1\n\t"
        "s_waitcnt vmcnt(35)\n\t"
        "v_mfma_f32_16x16x32_bf16 v[8:11], a[168:171], v[212:215], v[8:11]\n\t"
        "v_mfma_f32_16x16x32_bf16 v[12:15], a[172:175], v[212:215], v[12:15]\n\t"
        "buffer_load_dwordx4 v[212:215], v103, %[sw], %[t0] offen offset:1024 sc1\n\t"
        "s_waitcnt vmcnt(35)\n\t"
        "v_mfma_f32_16x16x32_bf16 v[8:11], a[176:179], v[216:219], v[8:11]\n\t"
        "v_mfma_f32_16x16x32_bf16 v[12:15], a[180:183], v[216:219], v[12:15]\n\t"
        "buffer_load_dwordx4 v[216:219], v103, %[sw], %[t0] offen offset:2048 sc1\n\t"
        "s_waitcnt vmcnt(35)\n\t"
        "v_mfma_f32_16x16x32_bf16 v[8:11], a[184:187], v[220:223], v[8:11]\n\t"
        "v_mfma_f32_16x16x32_bf16 v[12:15], a[188:191], v[220:223], v[12:15]\n\t"
        "buffer_load_dwordx4 v[220:223], v103, %[sw], %[t0] offen offset:3072 sc1\n\t"
        "s_waitcnt vmcnt(35)\n\t"
        "v_mfma_f32_16x16x32_bf16 v[8:11], a[192:195], v[224:227], v[8:11]\n\t"
        "v_mfma_f32_16x16x32_bf16 v[12:15], a[196:199], v[224:227], v[12:15]\n\t"
        "s_add_u32 %[t0], %[ro], 6\n\t"
        "s_and_b32 %[t0], %[t0], 7\n\t"
        "s_lshl_b32 %[t0], %[t0], 12\n\t"
        "s_add_u32 %[t0], %[t0], %[nx]\n\t"
        "buffer_load_dwordx4 v[224:227], v103, %[sw], %[t0] offen offset:0 sc1\n\t"
        "s_waitcnt vmcnt(35)\n\t"
        "v_mfma_f32_16x16x32_bf16 v[8:11], a[200:203], v[228:231], v[8:11]\n\t"
        "v_mfma_f32_16x16x32_bf16 v[12:15], a[204:207], v[228:231], v[12:15]\n\t"
        "buffer_load_dwordx4 v[228:231], v103, %[sw], %[t0] offen offset:1024 sc1\n\t"
        "s_waitcnt vmcnt(35)\n\t"
        "v_mfma_f32_16x16x32_bf16 v[8:11], a[208:211], v[232:235], v[8:11]\n\t"
        "v_mfma_f32_16x16x32_bf16 v[12:15], a[212:215], v[232:235], v[12:15]\n\t"
        "buffer_load_dwordx4 v[232:235], v103, %[sw], %[t0] offen offset:2048 sc1\n\t"
        "s_waitcnt vmcnt(35)\n\t"
        "v_mfma_f32_16x16x32_bf16 v[8:11], a[216:219], v[236:239], v[8:11]\n\t"
        "v_mfma_f32_16x16x32_bf16 v[12:15], a[220:223], v[236:239], v[12:15]\n\t"
        "buffer_load_dwordx4 v[236:239], v103, %[sw], %[t0] offen offset:3072 sc1\n\t"
        "s_waitcnt vmcnt(35)\n\t"
        "v_mfma_f32_16x16x32_bf16 v[8:11], a[224:227], v[240:243], v[8:11]\n\t"
        "v_mfma_f32_16x16x32_bf16 v[12:15], a[228:231], v[240:243], v[12:15]\n\t"
        "s_add_u32 %[t0], %[ro], 7\n\t"
        "s_and_b32 %[t0], %[t0], 7\n\t"
        "s_lshl_b32 %[t0], %[t0], 12\n\t"
        "s_add_u32 %[t0], %[t0], %[nx]\n\t"
        "buffer_load_dwordx4 v[240:243], v103, %[sw], %[t0] offen offset:0 sc1\n\t"
        "s_waitcnt vmcnt(35)\n\t"
        "v_mfma_f32_16x16x32_bf16 v[8:11], a[232:235], v[244:247], v[8:11]\n\t"
        "v_mfma_f32_16x16x32_bf16 v[12:15], a[236:239], v[244:247], v[12:15]\n\t"
        "buffer_load_dwordx4 v[244:247], v103, %[sw], %[t0] offen offset:1024 sc1\n\t"
        "s_waitcnt vmcnt(35)\n\t"
        "v_mfma_f32_16x16x32_bf16 v[8:11], a[240:243], v[248:251], v[8:11]\n\t"
        "v_mfma_f32_16x16x32_bf16 v[12:15], a[244:247], v[248:251], v[12:15]\n\t"
        "buffer_load_dwordx4 v[248:251], v103, %[sw], %[t0] offen offset:2048 sc1\n\t"
        "s_waitcnt vmcnt(35)\n\t"
        "v_mfma_f32_16x16x32_bf16 v[8:11], a[248:251], v[252:255], v[8:11]\n\t"
        "v_mfma_f32_16x16x32_bf16 v[12:15], a[252:255], v[252:255], v[12:15]\n\t"
        "buffer_load_dwordx4 v[252:255], v103, %[sw], %[t0] offen offset:3072 sc1\n\t"
        "s_waitcnt vmcnt(32)\n\t"
        "v_min_u32_dpp v26, v26, v26 quad_perm:[1,0,3,2] row_mask:0xf bank_mask:0xf\n\t"
        "v_sub_f32_e32 v28, 1.0, v25\n\t"
        "v_lshlrev_b32_e32 v30, 16, v24\n\t"
        "v_min_u32_dpp v26, v26, v26 quad_perm:[2,3,0,1] row_mask:0xf bank_mask:0xf\n\t"
        "v_and_b32_e32 v31, 0xffff0000, v24\n\t"
        "v_cvt_pk_bf16_f32 v28, 0, v28\n\t"
        "ds_write_b128 v108, v[8:11] offset:0\n\t"
        "ds_write_b128 v108, v[12:15] offset:64\n\t"
        "v_min_u32_dpp v26, v26, v26 row_ror:4 row_mask:0xf bank_mask:0xf\n\t"
        "v_mul_f32_e32 v30, v25, v30\n\t"
        "v_cvt_pk_bf16_f32 v30, 0, v30\n\t"
        "v_mul_f32_e32 v31, v25, v31\n\t"
        "v_cvt_pk_bf16_f32 v31, 0, v31\n\t"
        "v_readfirstlane_b32 %[pg], v26\n\t"
        "s_waitcnt lgkmcnt(0)\n\t"
        "s_barrier\n\t"
        "ds_read_b64 v[16:17], v109 offset:0\n\t"
        "ds_read_b64 v[18:19], v109 offset:2304\n\t"
        "ds_read_b64 v[20:21], v109 offset:4608\n\t"
        "ds_read_b64 v[22:23], v109 offset:6912\n\t"
        "v_xor_b32_e32 v108, 0x4000, v108\n\t"
        "v_xor_b32_e32 v109, 0x4000, v109\n\t"
        "s_lshl_b32 %[t0], %[tl], 15\n\t"
        "s_waitcnt lgkmcnt(0)\n\t"
        "v_pk_add_f32 v[16:17], v[16:17], v[18:19]\n\t"
        "v_pk_add_f32 v[20:21], v[20:21], v[22:23]\n\t"
        "v_pk_add_f32 v[16:17], v[16:17], v[20:21]\n\t"
        "v_cvt_pk_bf16_f32 v16, 0, v16\n\t"
        "v_cvt_pk_bf16_f32 v17, 0, v17\n\t"
        "v_mul_f32_e32 v16, v28, v16\n\t"
        "v_mul_f32_e32 v17, v28, v17\n\t"
        "v_cvt_pk_bf16_f32 v16, 0, v16\n\t"
        "v_cvt_pk_bf16_f32 v17, 0, v17\n\t"
        "v_add_f32_e32 v16, v30, v16\n\t"
        "v_add_f32_e32 v17, v31, v17\n\t"
        "v_cvt_pk_bf16_f32 v16, v16, v17\n\t"
        "buffer_store_dword v16, v107, %[su], %[t0] offen\n\t"
        "s_add_u32 %[tl], %[tl], 4\n\t"
        "s_cmp_ge_u32 %[tl], 128\n\t"
        "s_cbranch_scc1 LP_end%=\n\t"
        "s_cmp_lg_u32 %[nx], 0xf0000000\n\t"
        "s_cbranch_scc1 LP_tile%=\n\t"
        "s_lshr_b32 %[t1], %[tl], 1\n\t"
        "s_cmp_gt_u32 %[pg], %[t1]\n\t"
        "s_cbranch_scc1 LP_have%=\n\t"
        "s_branch LP_wait%=\n\t"
        "LP_end%=:\n\t"
        "s_waitcnt vmcnt(0)\n\t"
        ".Lk_end_P:\n\t"
        : [t0] "=&s"(t0), [t1] "=&s"(t1), [tl] "+s"(tl), [pg] "=&s"(pg), [nx] "=&s"(nx), [sp] "=&s"(sp), [ts] "=&s"(ts), [tu] "=&s"(tu), "+{v108}"(law), "+{v109}"(lar)
        : [so] "s"(so), [sw] "s"(sw), [su] "s"(su), [cv] "s"(F_CVY + nt * 128), [ro] "s"(PROT == 1 ? (nt >> 1) & 7 : PROT == 2 ? 2 * x : PROT == 3 ? ((nt >> 1) + 2 * x) & 7 : 0), [db] "s"(W_DBG + (128 + j) * 8192 + 512),
          "{v100}"((w * 1024 + (l >> 2)) * 2048 + nt * 64 + (l & 3) * 16), "{v101}"(stg + l * 16), "{v102}"(stg + (4 * g + (n >> 2)) * 64 + (n & 3) * 8),
          "{v103}"(W_OX + w * 32768 + l * 16), "{v104}"(F_PROG + (w * 8 + (l & 7)) * 128), "{v105}"(W_A + (nt * 4 + (n & 3)) * 32768 + w * 4096 + (n >> 2) * 16 + g * 4),
          "{v106}"(W_GATE + (4 * w + g) * 4), "{v107}"((4 * w + g) * 2048 + nt * 64 + n * 4)
        : "memory", "scc", "vcc", "v8", "v9", "v10", "v11", "v12", "v13", "v14", "v15", "v16", "v17", "v18", "v19", "v20", "v21", "v22", "v23", "v24", "v25", "v26", "v27", "v28", "v29", "v30", "v31", "v32", "v33", "v34", "v35", "v36", "v37", "v38", "v39", "v128", "v129", "v130", "v131", "v132", "v133", "v134", "v135", "v136", "v137", "v138", "v139", "v140", "v141", "v142", "v143", "v144", "v145", "v146", "v147", "v148", "v149", "v150", "v151", "v152", "v153", "v154", "v155", "v156", "v157", "v158", "v159", "v160", "v161", "v162", "v163", "v164", "v165", "v166", "v167", "v168", "v169", "v170", "v171", "v172", "v173", "v174", "v175", "v176", "v177", "v178", "v179", "v180", "v181", "v182", "v183", "v184", "v185", "v186", "v187", "v188", "v189", "v190", "v191", "v192", "v193", "v194", "v195", "v196", "v197", "v198", "v199", "v200", "v201", "v202", "v203", "v204", "v205", "v206", "v207", "v208", "v209", "v210", "v211", "v212", "v213", "v214", "v215", "v216", "v217", "v218", "v219", "v220", "v221", "v222", "v223", "v224", "v225", "v226", "v227", "v228", "v229", "v230", "v231", "v232", "v233", "v234", "v235", "v236", "v237", "v238", "v239", "v240", "v241", "v242", "v243", "v244", "v245", "v246", "v247", "v248", "v249", "v250", "v251", "v252", "v253", "v254", "v255", "a0", "a1", "a2", "a3", "a4", "a5", "a6", "a7", "a8", "a9", "a10", "a11", "a12", "a13", "a14", "a15", "a16", "a17", "a18", "a19", "a20", "a21", "a22", "a23", "a24", "a25", "a26", "a27", "a28", "a29", "a30", "a31", "a32", "a33", "a34", "a35", "a36", "a37", "a38", "a39", "a40", "a41", "a42", "a43", "a44", "a45", "a46", "a47", "a48", "a49", "a50", "a51", "a52", "a53", "a54", "a55", "a56", "a57", "a58", "a59", "a60", "a61", "a62", "a63", "a64", "a65", "a66", "a67", "a68", "a69", "a70", "a71", "a72", "a73", "a74", "a75", "a76", "a77", "a78", "a79", "a80", "a81", "a82", "a83", "a84", "a85", "a86", "a87", "a88", "a89", "a90", "a91", "a92", "a93", "a94", "a95", "a96", "a97", "a98", "a99", "a100", "a101", "a102", "a103", "a104", "a105", "a106", "a107", "a108", "a109", "a110", "a111", "a112", "a113", "a114", "a115", "a116", "a117", "a118", "a119", "a120", "a121", "a122", "a123", "a124", "a125", "a126", "a127", "a128", "a129", "a130", "a131", "a132", "a133", "a134", "a135", "a136", "a137", "a138", "a139", "a140", "a141", "a142", "a143", "a144", "a145", "a146", "a147", "a148", "a149", "a150", "a151", "a152", "a153", "a154", "a155", "a156", "a157", "a158", "a159", "a160", "a161", "a162", "a163", "a164", "a165", "a166", "a167", "a168", "a169", "a170", "a171", "a172", "a173", "a174", "a175", "a176", "a177", "a178", "a179", "a180", "a181", "a182", "a183", "a184", "a185", "a186", "a187", "a188", "a189", "a190", "a191", "a192", "a193", "a194", "a195", "a196", "a197", "a198", "a199", "a200", "a201", "a202", "a203", "a204", "a205", "a206", "a207", "a208", "a209", "a210", "a211", "a212", "a213", "a214", "a215", "a216", "a217", "a218", "a219", "a220", "a221", "a222", "a223", "a224", "a225", "a226", "a227", "a228", "a229", "a230", "a231", "a232", "a233", "a234", "a235", "a236", "a237", "a238", "a239", "a240", "a241", "a242", "a243", "a244", "a245", "a246", "a247", "a248", "a249", "a250", "a251", "a252", "a253", "a254", "a255");
}
__device__ __forceinline__ void bulk_block(const Params& p, rsrc_t r, unsigned char* lds, const int wv, const int j) {
    BSTAMP(0);
    proj_block(p, lds, wv, (j & 6) * 4 + (j >> 4), ((j >> 2) & 2) | (j & 1), j);
    BSTAMP(2);
    if (!conv_core(p, r, lds, wv, j)) return;
    BSTAMP(3);
    if (GDLY) wait_long(rtnow() + GDLY);
    out_block(p, lds, wv, (j & 7) >> 1, (j >> 3) * 2 + (j & 1), j);
    BSTAMP(7);
}

__device__ __forceinline__ void rec_block(const Params& p, rsrc_t r, unsigned char* lds, const int wv, const int b) {
    const int xq = (b & 7) >> 1, t = (b >> 3) * 2 + (b & 1);
    const unsigned lb = __builtin_amdgcn_readfirstlane((unsigned)(size_t)lds);
    int kind, id, rg = 0, rot = 0, bq = 99;
    if (RPL == 0) { kind = t < 8 ? 0 : t < 24 ? 1 : 2; id = t < 8 ? xq * 8 + t : t < 24 ? (xq & 1) * 16 + t - 8 : xq * 8 + t - 24; rg = xq >> 1; }
    else if (RPL == 4) { kind = t < 8 ? 0 : t < 24 ? 1 : 2; id = t < 8 ? xq * 8 + t : t < 24 ? xq * 8 + ((t - 8) >> 1) : xq * 8 + t - 24; rg = t & 1; }
    else if (RPL == 1) { kind = xq == 0 ? 0 : xq < 3 ? 1 : 2; id = t; rg = xq - 1; }
    else if (RPL == 2) { const int na = xq == 2 ? 10 : 11, q = xq * 21 + t - na; kind = xq == 3 ? 2 : t < na ? 0 : 1; id = xq == 3 ? t : t < na ? xq * 11 + t : q >> 1; rg = q & 1; }
    else { const int na = xq == 2 ? 12 : 10, q = t - na; kind = xq == 3 ? 2 : t < na ? 0 : 1; rg = xq < 2 ? xq : q & 1; bq = q;
        const int c2 = q >> 1, cs = xq < 2 ? (q < 6 ? q : q < 20 ? q + 2 : q + 4) : (c2 < 2 ? 6 + c2 : c2 < 4 ? 20 + c2 : 22 + c2);
        id = xq == 3 ? t : t < na ? xq * 10 + t : cs; rot = !ROT ? 0 : xq < 2 ? (cs & 1) * 4 : ((cs & 1) * 2 + rg) * 2; }
    if (kind == 0) rec_a(p, lb, wv, id, xq);
    else if (kind == 1) rec_b(p, lb, wv, id, rg, xq, rot, bq);
    else {
        const int hi = id, j = hi - 32; (void)j;
        BSTAMP(0);
        rec_helper(p, r, lds, wv, hi);
        BSTAMP(6);
        if (XWAIT == 1) { if (!wait_word(r, F_GO + (hi >> 1) * 128, 1u, 1024u)) return; }
        if (XSTG) wait_long(rtnow() + (hi & 7) * XSTG);
        gate_block(p, r, hi, wv);
        BSTAMP(2);
        if (XWAIT == 2) { if (!wait_word(r, F_GO + (hi >> 1) * 128, 1u, 1024u)) return; }
        conv_prep(p, r, wv, hi);
        BSTAMP(3);
    }
}

__global__ void __launch_bounds__(256, 1) k_main(Params p) {
    extern __shared__ __attribute__((aligned(16))) unsigned char lds[];
    rsrc_t r = __builtin_amdgcn_make_buffer_rsrc(p.ws, 0, W_TOTAL, 0x00020000);
    const int wv = __builtin_amdgcn_readfirstlane(threadIdx.x >> 6);
    int role = blockIdx.x;
    unsigned ctouch = 0u;
    if (CODE_T) {
        unsigned long long pc; int ea, eb, eg;
        asm volatile("s_getpc_b64 %0\n.Lk_begin:\n\ts_mov_b32 %1, .Lk_end_A-.Lk_begin\n\ts_mov_b32 %2, .Lk_end_B-.Lk_begin\n\ts_mov_b32 %3, .Lk_end_P-.Lk_begin" : "=s"(pc), "=s"(ea), "=s"(eb), "=s"(eg));
        const int o = ((blockIdx.x >> 3) * 64 + (threadIdx.x >> 2)) * 128;
        if ((threadIdx.x & 3) == 0 && o < max(ea, max(eb, eg)) - 4) asm volatile("global_load_dword %0, %1, off" : "+v"(ctouch) : "v"((const unsigned char*)pc + o) : "memory");
    }
    if (threadIdx.x == 0) {
        unsigned xcc; asm volatile("s_getreg_b32 %0, hwreg(HW_REG_XCC_ID)" : "=s"(xcc)); xcc &= 7u;
        unsigned* tk = (unsigned*)(p.ws + F_TKT);
        unsigned t = __hip_atomic_fetch_add(tk + xcc * 16, 1u, __ATOMIC_RELAXED, __HIP_MEMORY_SCOPE_AGENT);
        if (t >= 32u) {
            unsigned o = __hip_atomic_fetch_add(tk + 128, 1u, __ATOMIC_RELAXED, __HIP_MEMORY_SCOPE_AGENT);
            for (unsigned spin = 0; spin < (1u << 22); ++spin) { unsigned tot = 0u;
                for (int x = 0; x < 8; ++x) tot += __hip_atomic_load(tk + x * 16, __ATOMIC_RELAXED, __HIP_MEMORY_SCOPE_AGENT);
                if (tot >= 256u) break; }
            for (int x = 0; x < 8; ++x) { const unsigned cnt = __hip_atomic_load(tk + x * 16, __ATOMIC_RELAXED, __HIP_MEMORY_SCOPE_AGENT), fr = cnt < 32u ? 32u - cnt : 0u;
                if (o < fr) { xcc = (unsigned)x; t = cnt + o; break; }
                o -= fr; }
        }
        *(volatile int*)lds = (int)((xcc >> 2) * 128u + (t >> 1) * 8u + (xcc & 3u) * 2u + (t & 1u));
    }
    asm volatile("s_waitcnt vmcnt(0)" : "+v"(ctouch) :: "memory");
    __syncthreads();
    role = __builtin_amdgcn_readfirstlane(*(volatile int*)lds) & 255;
    __syncthreads();
    if (role >= 128) bulk_block(p, r, lds, wv, role - 128); else rec_block(p, r, lds, wv, role);
}

extern "C" void kernel_launch(void* const* d_in, const int* in_sizes, int n_in, void* d_out, int out_size, void* d_ws, size_t ws_size, hipStream_t stream) {
    Params p;
    p.Aq = (const bf16_t*)d_in[0]; p.Ak = (const bf16_t*)d_in[1]; p.Av = (const bf16_t*)d_in[2];
    p.shift_k = (const bf16_t*)d_in[3]; p.ssm_k = (const bf16_t*)d_in[4]; p.ssm_D = (const bf16_t*)d_in[5];
    p.mem0 = (const bf16_t*)d_in[7]; p.Wq = (const bf16_t*)d_in[8]; p.Wk = (const bf16_t*)d_in[9];
    p.Wv = (const bf16_t*)d_in[10]; p.Wo = (const bf16_t*)d_in[11]; p.gate_w = (const bf16_t*)d_in[13];
    p.out = (bf16_t*)d_out; p.ws = (unsigned char*)d_ws;
    unsigned char* ws = (unsigned char*)d_ws;
    (void)hipMemsetAsync(ws, 0, FLG_BYTES, stream);
    (void)hipFuncSetAttribute((const void*)k_main, hipFuncAttributeMaxDynamicSharedMemorySize, L_TOTAL);
    k_main<<<256, 256, L_TOTAL, stream>>>(p);
}
```

```cpp
#include <hip/hip_runtime.h>
#include <stdint.h>
#include <type_traits>
typedef unsigned short bf16_t;
using bf16x8 = __attribute__((ext_vector_type(8))) short;
using f32x4 = __attribute__((ext_vector_type(4))) float;
typedef unsigned v4u __attribute__((ext_vector_type(4)));
typedef unsigned v2u __attribute__((ext_vector_type(2)));
typedef __amdgpu_buffer_rsrc_t rsrc_t;

__device__ __forceinline__ float bf2f(bf16_t b) { return __uint_as_float(((unsigned)b) << 16); }
typedef __bf16 hbf2 __attribute__((ext_vector_type(2)));
typedef float f32x2 __attribute__((ext_vector_type(2)));
__device__ __forceinline__ unsigned pack2(float a, float b) { f32x2 v = {a, b}; return __builtin_bit_cast(unsigned, __builtin_convertvector(v, hbf2)); }
__device__ __forceinline__ float rbfh(float f) { return __uint_as_float(pack2(f, 0.f) << 16); }
__device__ __forceinline__ unsigned nosent(unsigned x) {
    typedef unsigned short us2 __attribute__((ext_vector_type(2)));
    const us2 a = __builtin_bit_cast(us2, x), c = {(unsigned short)0xFFFE, (unsigned short)0xFFFE}; return __builtin_bit_cast(unsigned, __builtin_elementwise_min(a, c)); }

constexpr int S = 2048, D = 1024, H = 32, DH = 128, M = 8, SEG = 32, NSEG = 64;

constexpr unsigned W_FLG = 0;
constexpr unsigned F_KVQ = W_FLG;
constexpr unsigned F_CONV = W_FLG + 32768;
constexpr unsigned F_ERR = W_FLG + 32768 + 256;
constexpr unsigned F_PREP = W_FLG + 32768 + 512;
constexpr unsigned F_CAL = W_FLG + 33792;
constexpr unsigned F_TKT = W_FLG + 34816;
constexpr unsigned F_PROG = W_FLG + 49152;
constexpr unsigned F_CVU = W_FLG + 53248, F_CVY = W_FLG + 57344;
constexpr unsigned F_OP = W_FLG + 65536;
constexpr unsigned F_ATT = W_FLG + 131072;
constexpr unsigned F_RDY = W_FLG + 147456;
constexpr unsigned FLG_BYTES = 180224;
constexpr unsigned HO_KVM = W_FLG + FLG_BYTES;
constexpr unsigned W_A = HO_KVM + 64 * 32 * 4096;
constexpr unsigned W_GATE = W_A + S * D * 2;
constexpr unsigned W_QX = W_GATE + 8192;
constexpr unsigned W_KX = W_QX + S * 4096 * 2;
constexpr unsigned W_VF = W_KX + S * 4096 * 2;
constexpr unsigned W_OX = W_VF + 64 * 32 * 8192;
constexpr unsigned W_PART = W_OX + S * 4096 * 2;
constexpr unsigned W_Y = W_PART;
constexpr unsigned W_DBG = W_PART + 32 * 4 * 64 * 1024 * 4;
constexpr unsigned W_U = W_DBG + 256 * 64 * 16 * 8;
constexpr unsigned W_TOTAL = W_U + S * D * 2;

__device__ __forceinline__ v4u ld16(rsrc_t r, unsigned off) { return __builtin_amdgcn_raw_buffer_load_b128(r, off, 0, 16); }
__device__ __forceinline__ v2u ld8(rsrc_t r, unsigned off) { return __builtin_amdgcn_raw_buffer_load_b64(r, off, 0, 16); }
__device__ __forceinline__ unsigned ld4(rsrc_t r, unsigned off) { return __builtin_amdgcn_raw_buffer_load_b32(r, off, 0, 16); }
__device__ __forceinline__ void st16(rsrc_t r, unsigned off, v4u v) { __builtin_amdgcn_raw_buffer_store_b128(v, r, off, 0, 16); }
__device__ __forceinline__ void st8(rsrc_t r, unsigned off, v2u v) { __builtin_amdgcn_raw_buffer_store_b64(v, r, off, 0, 16); }
__device__ __forceinline__ void st4(rsrc_t r, unsigned off, unsigned v) { __builtin_amdgcn_raw_buffer_store_b32(v, r, off, 0, 16); }
__device__ __forceinline__ bool okdw(unsigned x) { return ((x & 0xFFFFu) != 0xFFFFu) & ((x >> 16) != 0xFFFFu); }
typedef unsigned short us2_t __attribute__((ext_vector_type(2)));
__device__ __forceinline__ unsigned pkmax(unsigned a, unsigned b) { return __builtin_bit_cast(unsigned, __builtin_elementwise_max(__builtin_bit_cast(us2_t, a), __builtin_bit_cast(us2_t, b))); }
__device__ __forceinline__ unsigned pkmax4(v4u v) { return pkmax(pkmax(v.x, v.y), pkmax(v.z, v.w)); }
__device__ __forceinline__ bool ok16(v4u v) { return okdw(pkmax4(v)); }
__device__ __forceinline__ bool ok8(v2u v) { return okdw(v.x) & okdw(v.y); }
__device__ __forceinline__ bf16x8 as_frag(v4u v) { return __builtin_bit_cast(bf16x8, v); }
#define MFMA(a, b, c) __builtin_amdgcn_mfma_f32_16x16x32_bf16((a), (b), (c), 0, 0, 0)
#define D1 136
#define D2 85
#define D3 76
#define LT_A 1
#define LB0 12
#define LC0 20
#define LA0 24
#define TR_UP 4
#define TR_DN 2
#define L_DEC 3
#define WARM1 0
#define KROT 0
#define STAG 1
#define UP_SH 2
#define EPI_SLP 0
#define SITA 1
#define PAIRX 4
#define DUE_P 1
#define LATE 1
#define EARLY_FROM 56
#define SKEW 0
#define OUT_WARM 1
#define ADAPT 1
#define RPL 0
#define CV_POL 2
#define POLL_SLP 4
#define TOUCH 1
#define ROT 0
#define CODE_T 1
#define CAL_T 450u
#define GTOUCH 0
#define XTOUCH 32
#define ST_P 32
#define CV_NAP 4000u
#define XSLP 2
#define XDLY 0
#define GDLY 0
#define YDLY 0
#define PROT 3
#define XSTG 0
#define XWAIT 1
#define PEN 48
#define MEMBAR() asm volatile("" ::: "memory")
#define FT(i) do { } while (0)
#define ENDPGM() asm volatile("s_endpgm" ::: "memory")
#define DRAIN() asm volatile("s_waitcnt vmcnt(0)" ::: "memory")

__device__ __forceinline__ float xor16_max(float x) { auto r = __builtin_amdgcn_permlane16_swap(__float_as_uint(x), __float_as_uint(x), false, false); return fmaxf(__uint_as_float(r[0]), __uint_as_float(r[1])); }
__device__ __forceinline__ float xor32_max(float x) { auto r = __builtin_amdgcn_permlane32_swap(__float_as_uint(x), __float_as_uint(x), false, false); return fmaxf(__uint_as_float(r[0]), __uint_as_float(r[1])); }
__device__ __forceinline__ float xor16_add(float x) { auto r = __builtin_amdgcn_permlane16_swap(__float_as_uint(x), __float_as_uint(x), false, false); return __uint_as_float(r[0]) + __uint_as_float(r[1]); }
__device__ __forceinline__ float xor32_add(float x) { auto r = __builtin_amdgcn_permlane32_swap(__float_as_uint(x), __float_as_uint(x), false, false); return __uint_as_float(r[0]) + __uint_as_float(r[1]); }
__device__ __forceinline__ v2u tr_read(unsigned lds_addr) { v2u r; asm volatile("ds_read_b64_tr_b16 %0, %1\n\ts_waitcnt lgkmcnt(0)" : "=&v"(r) : "v"(lds_addr) : "memory"); return r; }

typedef short v4i16_t __attribute__((ext_vector_type(4)));
__device__ __forceinline__ v2u trb(unsigned lds_addr) { return __builtin_bit_cast(v2u, __builtin_amdgcn_ds_read_tr16_b64_v4i16((__attribute__((address_space(3))) v4i16_t*)(size_t)lds_addr)); }
__device__ __forceinline__ int otid_(int wv) { int t; asm volatile("v_mbcnt_lo_u32_b32 %0, -1, 0\n\tv_mbcnt_hi_u32_b32 %0, -1, %0" : "=v"(t)); return wv * 64 + t; }
#define otid() otid_(wv)
__device__ __forceinline__ bool wait_word(rsrc_t r, unsigned off, unsigned want, unsigned code) {
    for (unsigned spin = 0;; ++spin) { MEMBAR();
        const unsigned v = __builtin_amdgcn_readfirstlane(ld4(r, off));
        if (v >= want) return true;
        if (spin > (1u << 20)) { st4(r, F_ERR, code); return false; }
        __builtin_amdgcn_s_sleep(8); }
}

__device__ __forceinline__ unsigned rtnow() { return (unsigned)__builtin_amdgcn_s_memrealtime(); }
__device__ __forceinline__ void wait_until(unsigned target) { for (int i = 0; i < 4096; ++i) { if ((int)(rtnow() - target) >= 0) break; } }
__device__ __forceinline__ void wait_long(unsigned target) { for (int i = 0; i < 4096; ++i) { if ((int)(rtnow() - target) >= 0) break; __builtin_amdgcn_s_sleep(8); } }
constexpr int RED_G = 320;
constexpr int RED_T = 4 * RED_G;
constexpr int L_RED = 0;
constexpr int L_KX = 65536;
constexpr int KXP = 272;
constexpr int L_VF = L_KX + 2 * 32 * KXP;
constexpr int L_QKV = L_VF + 16384;
constexpr int L_OFF = L_QKV + 6144;
constexpr int L_STMP = L_OFF + 256;
constexpr int L_TOTAL = 163840;
constexpr unsigned U_A = 0, U_S = 32, U_B = 36, U_INIT = 113, U_C = 128, U_END = 512;
#define PLACE 1
#define CAL_N 5
#define STEP_MIN 190u

struct Params {
    const bf16_t *Aq, *Ak, *Av, *shift_k, *ssm_k, *ssm_D, *mem0, *Wq, *Wk, *Wv, *Wo, *gate_w;
    bf16_t* out; unsigned char* ws;
};
__host__ __device__ __forceinline__ unsigned hx_base(const void* ws) { const size_t a = (size_t)ws; return (unsigned)((((a + W_TOTAL + 0x7FFFFFu) >> 23) << 23) - a); }

#define STAMP(i) do { } while (0)
#define BSTAMP(i) do { } while (0)
#define EV(code) do { } while (0)
#define SPINS(i) do { } while (0)
typedef short v4s __attribute__((ext_vector_type(4)));
typedef unsigned v32u __attribute__((ext_vector_type(32)));
typedef unsigned v16u __attribute__((ext_vector_type(16)));
static_assert(L_VF - L_KX == 16384 + 1024, "fragment read offsets");
#define MFMA16(a, b, c) __builtin_amdgcn_mfma_f32_16x16x16bf16_1k(__builtin_bit_cast(v4s, (a)), __builtin_bit_cast(v4s, (b)), (c), 0, 0, 0)
__device__ __forceinline__ unsigned ror8(unsigned x) { return (unsigned)__builtin_amdgcn_update_dpp(0, (int)x, 0x128, 0xF, 0xF, false); }
__device__ __forceinline__ unsigned qx1(unsigned x) { return (unsigned)__builtin_amdgcn_update_dpp(0, (int)x, 0xB1, 0xF, 0xF, false); }
__device__ __forceinline__ unsigned wave_pkmax(unsigned d) {
    d = pkmax(d, (unsigned)__builtin_amdgcn_update_dpp((int)d, (int)d, 0xB1, 0xF, 0xF, false)); d = pkmax(d, (unsigned)__builtin_amdgcn_update_dpp((int)d, (int)d, 0x4E, 0xF, 0xF, false));
    d = pkmax(d, (unsigned)__builtin_amdgcn_update_dpp((int)d, (int)d, 0x124, 0xF, 0xF, false)); d = pkmax(d, (unsigned)__builtin_amdgcn_update_dpp((int)d, (int)d, 0x128, 0xF, 0xF, false));
    { auto q = __builtin_amdgcn_permlane16_swap(d, d, false, false); d = pkmax(q[0], q[1]); }
    { auto q = __builtin_amdgcn_permlane32_swap(d, d, false, false); d = pkmax(q[0], q[1]); }
    return d; }
__device__ __forceinline__ int wave_max(int d) {
    d = max(d, __builtin_amdgcn_update_dpp(d, d, 0xB1, 0xF, 0xF, false)); d = max(d, __builtin_amdgcn_update_dpp(d, d, 0x4E, 0xF, 0xF, false));
    d = max(d, __builtin_amdgcn_update_dpp(d, d, 0x124, 0xF, 0xF, false)); d = max(d, __builtin_amdgcn_update_dpp(d, d, 0x128, 0xF, 0xF, false));
    { auto q = __builtin_amdgcn_permlane16_swap((unsigned)d, (unsigned)d, false, false); d = max((int)q[0], (int)q[1]); }
    { auto q = __builtin_amdgcn_permlane32_swap((unsigned)d, (unsigned)d, false, false); d = max((int)q[0], (int)q[1]); }
    return d; }

__device__ __forceinline__ v4u mk_srd(const void* p, unsigned bytes) { const size_t a = (size_t)p; v4u d;
    d.x = __builtin_amdgcn_readfirstlane((unsigned)a); d.y = __builtin_amdgcn_readfirstlane((unsigned)(a >> 32) & 0xFFFFu); d.z = __builtin_amdgcn_readfirstlane(bytes); d.w = 0x00020000u; return d; }
__device__ __forceinline__ bf16x8 ldsa(unsigned addr) { return *(const __attribute__((address_space(3))) bf16x8*)(size_t)addr; }
constexpr unsigned HX_BYTES = 2097152, OOB = 0xF0000000u;
constexpr unsigned F_RDY0 = W_FLG + 34304;
constexpr unsigned F_GO = W_FLG + 36864;
constexpr int L_KV = 90112, L_MS = 139264, L_IMG = 155648, IMG_PS = 1056, IMG_MS = 2112, LB_R = 8192, LB_STG = 16384, LB_STGP = 8704;
static_assert(F_KVQ == 0 && F_ERR == 33024 && F_CONV == 32768 && F_CAL == 33792 && F_RDY0 == 34304 && F_GO == 36864 && F_PROG == 49152 && W_QX == 12771328 && W_KX == 29548544 && W_OX == 63102976 && W_VF == 46325760 && W_DBG == 113434624 && L_KV == 90112 && L_MS == 139264 && L_IMG == 155648 && IMG_PS == 1056 && IMG_MS == 2112 && LB_R == 8192 && LB_STG == 16384 && LB_STGP == 8704, "constants of the generated code");
#define REC_S_IN "{s[8:11]}"(sh), "{s[12:15]}"(sw), "{s[16:19]}"(s1), "{s[20:23]}"(s2), "{s29}"(D1), "{s30}"(D2), "{s43}"(w), "{s44}"(id), "{s49}"(rb), "{s50}"(PEN), "{s51}"(LT_A), "{s45}"(F_GO + (cp * 4 + w) * 128)
__device__ __forceinline__ void rec_a(const Params& p, unsigned lb, const int wv, const int id, const int cp) {
    const int w = wv, l = otid() & 63, n = l & 15, g = l >> 4, h = id, rb = id;
    const v4u sh = mk_srd(p.ws + hx_base(p.ws), HX_BYTES - 8192), sw = mk_srd(p.ws, W_TOTAL), s1 = mk_srd(w < 2 ? p.Wq : p.Wk, 1024 * 4096 * 2), s2 = s1;
    const int r8 = l >> 3, pc = l & 7, pp = n >> 3, bb = (n >> 2) & 1, f = (g & 1) | (pp << 1);
    const unsigned stg = lb + 131072 + w * 8192;
    const unsigned g_w = r8 * 8192 + h * 256 + (w & 1) * 128 + pc * 16;
    const unsigned wb0 = stg + r8 * 128 + (((pc >> 1) ^ (r8 >> 2)) << 5) + (pc & 1) * 16, wb1 = wb0 ^ 64u;
    const unsigned rb0 = stg + (16 * (g >> 1) + 8 * pp + 4 * (g & 1) + bb) * 128 + (f << 5) + (n & 3) * 8;
    const unsigned a_mem = cp * 32768 + (w >> 1) * 16384 + (w & 1) * 4096 + (g >> 1) * 512 + ((n & 7) >> 2) * 256 + ((g & 1) * 2 + pp) * 64 + (n & 3) * 16;
    const unsigned l_ms = lb + L_MS + l * 16;
    const unsigned l_iw = lb + L_IMG + (w >> 1) * IMG_MS + (g & 1) * IMG_PS + (64 * (w & 1) + 16 * (g >> 1) + n) * 8;
    const unsigned l_iq = lb + L_IMG + (n & 1) * IMG_PS + (8 * g + (n >> 2)) * 8;
    const unsigned a_vm = (16 + (h >> 2)) * 16384 + (h & 3) * 2048 + w * 512 + n * 32 + (g & 1) * 16;
    const unsigned a_o = n < 8 ? (8 + (n >> 2) * 4 + (h >> 3)) * 16384 + (h & 7) * 1024 + w * 256 + g * 64 + (n & 3) * 16 : OOB;
    const unsigned a_ox = (((w & 1) * 32 + h) * 4 + w) * 1024 + l * 16;
    asm volatile(
        "s_lshl_b32 s34, s43, 2\n\t"
        "s_add_u32 s34, s34, 0\n\t"
        "s_and_b32 s34, s34, 15\n\t"
        "s_lshl_b32 s34, s34, 19\n\t"
        "buffer_load_dwordx4 v[224:227], v96, s[16:19], s34 offen nt\n\t"
        "s_add_u32 s34, s34, 0x10000\n\t"
        "buffer_load_dwordx4 v[228:231], v96, s[16:19], s34 offen nt\n\t"
        "s_add_u32 s34, s34, 0x10000\n\t"
        "buffer_load_dwordx4 v[232:235], v96, s[16:19], s34 offen nt\n\t"
        "s_add_u32 s34, s34, 0x10000\n\t"
        "buffer_load_dwordx4 v[236:239], v96, s[16:19], s34 offen nt\n\t"
        "s_add_u32 s34, s34, 0x10000\n\t"
        "buffer_load_dwordx4 v[240:243], v96, s[16:19], s34 offen nt\n\t"
        "s_add_u32 s34, s34, 0x10000\n\t"
        "buffer_load_dwordx4 v[244:247], v96, s[16:19], s34 offen nt\n\t"
        "s_add_u32 s34, s34, 0x10000\n\t"
        "buffer_load_dwordx4 v[248:251], v96, s[16:19], s34 offen nt\n\t"
        "s_add_u32 s34, s34, 0x10000\n\t"
        "buffer_load_dwordx4 v[252:255], v96, s[16:19], s34 offen nt\n\t"
        "s_lshl_b32 s34, s43, 2\n\t"
        "s_add_u32 s34, s34, 1\n\t"
        "s_and_b32 s34, s34, 15\n\t"
        "s_lshl_b32 s34, s34, 19\n\t"
        "buffer_load_dwordx4 v[0:3], v96, s[16:19], s34 offen nt\n\t"
        "s_add_u32 s34, s34, 0x10000\n\t"
        "buffer_load_dwordx4 v[4:7], v96, s[16:19], s34 offen nt\n\t"
        "s_add_u32 s34, s34, 0x10000\n\t"
        "buffer_load_dwordx4 v[8:11], v96, s[16:19], s34 offen nt\n\t"
        "s_add_u32 s34, s34, 0x10000\n\t"
        "buffer_load_dwordx4 v[12:15], v96, s[16:19], s34 offen nt\n\t"
        "s_add_u32 s34, s34, 0x10000\n\t"
        "buffer_load_dwordx4 v[16:19], v96, s[16:19], s34 offen nt\n\t"
        "s_add_u32 s34, s34, 0x10000\n\t"
        "buffer_load_dwordx4 v[20:23], v96, s[16:19], s34 offen nt\n\t"
        "s_add_u32 s34, s34, 0x10000\n\t"
        "buffer_load_dwordx4 v[24:27], v96, s[16:19], s34 offen nt\n\t"
        "s_add_u32 s34, s34, 0x10000\n\t"
        "buffer_load_dwordx4 v[28:31], v96, s[16:19], s34 offen nt\n\t"
        "s_lshl_b32 s34, s43, 2\n\t"
        "s_add_u32 s34, s34, 2\n\t"
        "s_and_b32 s34, s34, 15\n\t"
        "s_lshl_b32 s34, s34, 19\n\t"
        "buffer_load_dwordx4 v[32:35], v96, s[16:19], s34 offen nt\n\t"
        "s_add_u32 s34, s34, 0x10000\n\t"
        "buffer_load_dwordx4 v[36:39], v96, s[16:19], s34 offen nt\n\t"
        "s_add_u32 s34, s34, 0x10000\n\t"
        "buffer_load_dwordx4 v[40:43], v96, s[16:19], s34 offen nt\n\t"
        "s_add_u32 s34, s34, 0x10000\n\t"
        "buffer_load_dwordx4 v[44:47], v96, s[16:19], s34 offen nt\n\t"
        "s_add_u32 s34, s34, 0x10000\n\t"
        "buffer_load_dwordx4 v[48:51], v96, s[16:19], s34 offen nt\n\t"
        "s_add_u32 s34, s34, 0x10000\n\t"
        "buffer_load_dwordx4 v[52:55], v96, s[16:19], s34 offen nt\n\t"
        "s_add_u32 s34, s34, 0x10000\n\t"
        "buffer_load_dwordx4 v[56:59], v96, s[16:19], s34 offen nt\n\t"
        "s_add_u32 s34, s34, 0x10000\n\t"
        "buffer_load_dwordx4 v[60:63], v96, s[16:19], s34 offen nt\n\t"
        "v_cmp_le_i32_e64 s[60:61], 0, v126\n\t"
        "v_cmp_le_i32_e64 s[62:63], 1, v126\n\t"
        "v_cmp_le_i32_e64 s[64:65], 2, v126\n\t"
        "v_cmp_le_i32_e64 s[66:67], 3, v126\n\t"
        "v_cmp_le_i32_e64 s[68:69], 16, v126\n\t"
        "v_cmp_le_i32_e64 s[70:71], 17, v126\n\t"
        "v_cmp_le_i32_e64 s[72:73], 18, v126\n\t"
        "v_cmp_le_i32_e64 s[74:75], 19, v126\n\t"
        "s_mul_i32 s35, s43, 10240\n\t"
        "v_add_u32_e32 v126, s35, v124\n\t"
        "s_waitcnt vmcnt(16)\n\t"
        "ds_write_b128 v97, v[224:227] offset:0\n\t"
        "ds_write_b128 v98, v[228:231] offset:1024\n\t"
        "ds_write_b128 v97, v[232:235] offset:2048\n\t"
        "ds_write_b128 v98, v[236:239] offset:3072\n\t"
        "ds_write_b128 v97, v[240:243] offset:4096\n\t"
        "ds_write_b128 v98, v[244:247] offset:5120\n\t"
        "ds_write_b128 v97, v[248:251] offset:6144\n\t"
        "ds_write_b128 v98, v[252:255] offset:7168\n\t"
        "ds_read_b64_tr_b16 a[0:1], v99 offset:0\n\t"
        "ds_read_b64_tr_b16 a[2:3], v99 offset:256\n\t"
        "ds_read_b64_tr_b16 a[4:5], v99 offset:4096\n\t"
        "ds_read_b64_tr_b16 a[6:7], v99 offset:4352\n\t"
        "s_waitcnt lgkmcnt(8)\n\t"
        "ds_read_b64_tr_b16 a[8:9], v100 offset:0\n\t"
        "ds_read_b64_tr_b16 a[10:11], v100 offset:256\n\t"
        "ds_read_b64_tr_b16 a[12:13], v100 offset:4096\n\t"
        "ds_read_b64_tr_b16 a[14:15], v100 offset:4352\n\t"
        "s_waitcnt lgkmcnt(8)\n\t"
        "ds_read_b64_tr_b16 a[16:17], v101 offset:0\n\t"
        "ds_read_b64_tr_b16 a[18:19], v101 offset:256\n\t"
        "ds_read_b64_tr_b16 a[20:21], v101 offset:4096\n\t"
        "ds_read_b64_tr_b16 a[22:23], v101 offset:4352\n\t"
        "s_waitcnt lgkmcnt(8)\n\t"
        "ds_read_b64_tr_b16 v[104:105], v102 offset:0\n\t"
        "ds_read_b64_tr_b16 v[106:107], v102 offset:256\n\t"
        "ds_read_b64_tr_b16 v[108:109], v102 offset:4096\n\t"
        "ds_read_b64_tr_b16 v[110:111], v102 offset:4352\n\t"
        "s_lshl_b32 s34, s43, 2\n\t"
        "s_add_u32 s34, s34, 3\n\t"
        "s_and_b32 s34, s34, 15\n\t"
        "s_lshl_b32 s34, s34, 19\n\t"
        "buffer_load_dwordx4 v[64:67], v96, s[16:19], s34 offen nt\n\t"
        "s_add_u32 s34, s34, 0x10000\n\t"
        "buffer_load_dwordx4 v[68:71], v96, s[16:19], s34 offen nt\n\t"
        "s_add_u32 s34, s34, 0x10000\n\t"
        "buffer_load_dwordx4 v[72:75], v96, s[16:19], s34 offen nt\n\t"
        "s_add_u32 s34, s34, 0x10000\n\t"
        "buffer_load_dwordx4 v[76:79], v96, s[16:19], s34 offen nt\n\t"
        "s_add_u32 s34, s34, 0x10000\n\t"
        "buffer_load_dwordx4 v[80:83], v96, s[16:19], s34 offen nt\n\t"
        "s_add_u32 s34, s34, 0x10000\n\t"
        "buffer_load_dwordx4 v[84:87], v96, s[16:19], s34 offen nt\n\t"
        "s_add_u32 s34, s34, 0x10000\n\t"
        "buffer_load_dwordx4 v[88:91], v96, s[16:19], s34 offen nt\n\t"
        "s_add_u32 s34, s34, 0x10000\n\t"
        "buffer_load_dwordx4 v[92:95], v96, s[16:19], s34 offen nt\n\t"
        "s_waitcnt lgkmcnt(0)\n\t"
        "ds_write_b128 v117, v[104:107] offset:0\n\t"
        "ds_write_b128 v117, v[108:111] offset:1024\n\t"
        "s_waitcnt vmcnt(16)\n\t"
        "ds_write_b128 v97, v[0:3] offset:0\n\t"
        "ds_write_b128 v98, v[4:7] offset:1024\n\t"
        "ds_write_b128 v97, v[8:11] offset:2048\n\t"
        "ds_write_b128 v98, v[12:15] offset:3072\n\t"
        "ds_write_b128 v97, v[16:19] offset:4096\n\t"
        "ds_write_b128 v98, v[20:23] offset:5120\n\t"
        "ds_write_b128 v97, v[24:27] offset:6144\n\t"
        "ds_write_b128 v98, v[28:31] offset:7168\n\t"
        "ds_read_b64_tr_b16 a[24:25], v99 offset:0\n\t"
        "ds_read_b64_tr_b16 a[26:27], v99 offset:256\n\t"
        "ds_read_b64_tr_b16 a[28:29], v99 offset:4096\n\t"
        "ds_read_b64_tr_b16 a[30:31], v99 offset:4352\n\t"
        "s_waitcnt lgkmcnt(8)\n\t"
        "ds_read_b64_tr_b16 a[32:33], v100 offset:0\n\t"
        "ds_read_b64_tr_b16 a[34:35], v100 offset:256\n\t"
        "ds_read_b64_tr_b16 a[36:37], v100 offset:4096\n\t"
        "ds_read_b64_tr_b16 a[38:39], v100 offset:4352\n\t"
        "s_waitcnt lgkmcnt(8)\n\t"
        "ds_read_b64_tr_b16 a[40:41], v101 offset:0\n\t"
        "ds_read_b64_tr_b16 a[42:43], v101 offset:256\n\t"
        "ds_read_b64_tr_b16 a[44:45], v101 offset:4096\n\t"
        "ds_read_b64_tr_b16 a[46:47], v101 offset:4352\n\t"
        "s_waitcnt lgkmcnt(8)\n\t"
        "ds_read_b64_tr_b16 v[104:105], v102 offset:0\n\t"
        "ds_read_b64_tr_b16 v[106:107], v102 offset:256\n\t"
        "ds_read_b64_tr_b16 v[108:109], v102 offset:4096\n\t"
        "ds_read_b64_tr_b16 v[110:111], v102 offset:4352\n\t"
        "s_lshl_b32 s34, s43, 2\n\t"
        "s_add_u32 s34, s34, 4\n\t"
        "s_and_b32 s34, s34, 15\n\t"
        "s_lshl_b32 s34, s34, 19\n\t"
        "buffer_load_dwordx4 a[96:99], v96, s[16:19], s34 offen nt\n\t"
        "s_add_u32 s34, s34, 0x10000\n\t"
        "buffer_load_dwordx4 a[100:103], v96, s[16:19], s34 offen nt\n\t"
        "s_add_u32 s34, s34, 0x10000\n\t"
        "buffer_load_dwordx4 a[104:107], v96, s[16:19], s34 offen nt\n\t"
        "s_add_u32 s34, s34, 0x10000\n\t"
        "buffer_load_dwordx4 a[108:111], v96, s[16:19], s34 offen nt\n\t"
        "s_add_u32 s34, s34, 0x10000\n\t"
        "buffer_load_dwordx4 a[112:115], v96, s[16:19], s34 offen nt\n\t"
        "s_add_u32 s34, s34, 0x10000\n\t"
        "buffer_load_dwordx4 a[116:119], v96, s[16:19], s34 offen nt\n\t"
        "s_add_u32 s34, s34, 0x10000\n\t"
        "buffer_load_dwordx4 a[120:123], v96, s[16:19], s34 offen nt\n\t"
        "s_add_u32 s34, s34, 0x10000\n\t"
        "buffer_load_dwordx4 a[124:127], v96, s[16:19], s34 offen nt\n\t"
        "s_waitcnt lgkmcnt(0)\n\t"
        "ds_write_b128 v117, v[104:107] offset:2048\n\t"
        "ds_write_b128 v117, v[108:111] offset:3072\n\t"
        "s_waitcnt vmcnt(16)\n\t"
        "ds_write_b128 v97, v[32:35] offset:0\n\t"
        "ds_write_b128 v98, v[36:39] offset:1024\n\t"
        "ds_write_b128 v97, v[40:43] offset:2048\n\t"
        "ds_write_b128 v98, v[44:47] offset:3072\n\t"
        "ds_write_b128 v97, v[48:51] offset:4096\n\t"
        "ds_write_b128 v98, v[52:55] offset:5120\n\t"
        "ds_write_b128 v97, v[56:59] offset:6144\n\t"
        "ds_write_b128 v98, v[60:63] offset:7168\n\t"
        "ds_read_b64_tr_b16 a[48:49], v99 offset:0\n\t"
        "ds_read_b64_tr_b16 a[50:51], v99 offset:256\n\t"
        "ds_read_b64_tr_b16 a[52:53], v99 offset:4096\n\t"
        "ds_read_b64_tr_b16 a[54:55], v99 offset:4352\n\t"
        "s_waitcnt lgkmcnt(8)\n\t"
        "ds_read_b64_tr_b16 a[56:57], v100 offset:0\n\t"
        "ds_read_b64_tr_b16 a[58:59], v100 offset:256\n\t"
        "ds_read_b64_tr_b16 a[60:61], v100 offset:4096\n\t"
        "ds_read_b64_tr_b16 a[62:63], v100 offset:4352\n\t"
        "s_waitcnt lgkmcnt(8)\n\t"
        "ds_read_b64_tr_b16 a[64:65], v101 offset:0\n\t"
        "ds_read_b64_tr_b16 a[66:67], v101 offset:256\n\t"
        "ds_read_b64_tr_b16 a[68:69], v101 offset:4096\n\t"
        "ds_read_b64_tr_b16 a[70:71], v101 offset:4352\n\t"
        "s_waitcnt lgkmcnt(8)\n\t"
        "ds_read_b64_tr_b16 v[104:105], v102 offset:0\n\t"
        "ds_read_b64_tr_b16 v[106:107], v102 offset:256\n\t"
        "ds_read_b64_tr_b16 v[108:109], v102 offset:4096\n\t"
        "ds_read_b64_tr_b16 v[110:111], v102 offset:4352\n\t"
        "s_lshl_b32 s34, s43, 2\n\t"
        "s_add_u32 s34, s34, 5\n\t"
        "s_and_b32 s34, s34, 15\n\t"
        "s_lshl_b32 s34, s34, 19\n\t"
        "buffer_load_dwordx4 a[128:131], v96, s[16:19], s34 offen nt\n\t"
        "s_add_u32 s34, s34, 0x10000\n\t"
        "buffer_load_dwordx4 a[132:135], v96, s[16:19], s34 offen nt\n\t"
        "s_add_u32 s34, s34, 0x10000\n\t"
        "buffer_load_dwordx4 a[136:139], v96, s[16:19], s34 offen nt\n\t"
        "s_add_u32 s34, s34, 0x10000\n\t"
        "buffer_load_dwordx4 a[140:143], v96, s[16:19], s34 offen nt\n\t"
        "s_add_u32 s34, s34, 0x10000\n\t"
        "buffer_load_dwordx4 a[144:147], v96, s[16:19], s34 offen nt\n\t"
        "s_add_u32 s34, s34, 0x10000\n\t"
        "buffer_load_dwordx4 a[148:151], v96, s[16:19], s34 offen nt\n\t"
        "s_add_u32 s34, s34, 0x10000\n\t"
        "buffer_load_dwordx4 a[152:155], v96, s[16:19], s34 offen nt\n\t"
        "s_add_u32 s34, s34, 0x10000\n\t"
        "buffer_load_dwordx4 a[156:159], v96, s[16:19], s34 offen nt\n\t"
        "s_waitcnt lgkmcnt(0)\n\t"
        "ds_write_b128 v117, v[104:107] offset:4096\n\t"
        "ds_write_b128 v117, v[108:111] offset:5120\n\t"
        "s_waitcnt vmcnt(16)\n\t"
        "ds_write_b128 v97, v[64:67] offset:0\n\t"
        "ds_write_b128 v98, v[68:71] offset:1024\n\t"
        "ds_write_b128 v97, v[72:75] offset:2048\n\t"
        "ds_write_b128 v98, v[76:79] offset:3072\n\t"
        "ds_write_b128 v97, v[80:83] offset:4096\n\t"
        "ds_write_b128 v98, v[84:87] offset:5120\n\t"
        "ds_write_b128 v97, v[88:91] offset:6144\n\t"
        "ds_write_b128 v98, v[92:95] offset:7168\n\t"
        "ds_read_b64_tr_b16 a[72:73], v99 offset:0\n\t"
        "ds_read_b64_tr_b16 a[74:75], v99 offset:256\n\t"
        "ds_read_b64_tr_b16 a[76:77], v99 offset:4096\n\t"
        "ds_read_b64_tr_b16 a[78:79], v99 offset:4352\n\t"
        "s_waitcnt lgkmcnt(8)\n\t"
        "ds_read_b64_tr_b16 a[80:81], v100 offset:0\n\t"
        "ds_read_b64_tr_b16 a[82:83], v100 offset:256\n\t"
        "ds_read_b64_tr_b16 a[84:85], v100 offset:4096\n\t"
        "ds_read_b64_tr_b16 a[86:87], v100 offset:4352\n\t"
        "s_waitcnt lgkmcnt(8)\n\t"
        "ds_read_b64_tr_b16 a[88:89], v101 offset:0\n\t"
        "ds_read_b64_tr_b16 a[90:91], v101 offset:256\n\t"
        "ds_read_b64_tr_b16 a[92:93], v101 offset:4096\n\t"
        "ds_read_b64_tr_b16 a[94:95], v101 offset:4352\n\t"
        "s_waitcnt lgkmcnt(8)\n\t"
        "ds_read_b64_tr_b16 v[104:105], v102 offset:0\n\t"
        "ds_read_b64_tr_b16 v[106:107], v102 offset:256\n\t"
        "ds_read_b64_tr_b16 v[108:109], v102 offset:4096\n\t"
        "ds_read_b64_tr_b16 v[110:111], v102 offset:4352\n\t"
        "s_lshl_b32 s34, s43, 2\n\t"
        "s_add_u32 s34, s34, 6\n\t"
        "s_and_b32 s34, s34, 15\n\t"
        "s_lshl_b32 s34, s34, 19\n\t"
        "buffer_load_dwordx4 a[160:163], v96, s[16:19], s34 offen nt\n\t"
        "s_add_u32 s34, s34, 0x10000\n\t"
        "buffer_load_dwordx4 a[164:167], v96, s[16:19], s34 offen nt\n\t"
        "s_add_u32 s34, s34, 0x10000\n\t"
        "buffer_load_dwordx4 a[168:171], v96, s[16:19], s34 offen nt\n\t"
        "s_add_u32 s34, s34, 0x10000\n\t"
        "buffer_load_dwordx4 a[172:175], v96, s[16:19], s34 offen nt\n\t"
        "s_add_u32 s34, s34, 0x10000\n\t"
        "buffer_load_dwordx4 a[176:179], v96, s[16:19], s34 offen nt\n\t"
        "s_add_u32 s34, s34, 0x10000\n\t"
        "buffer_load_dwordx4 a[180:183], v96, s[16:19], s34 offen nt\n\t"
        "s_add_u32 s34, s34, 0x10000\n\t"
        "buffer_load_dwordx4 a[184:187], v96, s[16:19], s34 offen nt\n\t"
        "s_add_u32 s34, s34, 0x10000\n\t"
        "buffer_load_dwordx4 a[188:191], v96, s[16:19], s34 offen nt\n\t"
        "s_waitcnt lgkmcnt(0)\n\t"
        "ds_write_b128 v117, v[104:107] offset:6144\n\t"
        "ds_write_b128 v117, v[108:111] offset:7168\n\t"
        "s_waitcnt vmcnt(16)\n\t"
        "ds_write_b128 v97, a[96:99] offset:0\n\t"
        "ds_write_b128 v98, a[100:103] offset:1024\n\t"
        "ds_write_b128 v97, a[104:107] offset:2048\n\t"
        "ds_write_b128 v98, a[108:111] offset:3072\n\t"
        "ds_write_b128 v97, a[112:115] offset:4096\n\t"
        "ds_write_b128 v98, a[116:119] offset:5120\n\t"
        "ds_write_b128 v97, a[120:123] offset:6144\n\t"
        "ds_write_b128 v98, a[124:127] offset:7168\n\t"
        "ds_read_b64_tr_b16 a[96:97], v99 offset:0\n\t"
        "ds_read_b64_tr_b16 a[98:99], v99 offset:256\n\t"
        "ds_read_b64_tr_b16 a[100:101], v99 offset:4096\n\t"
        "ds_read_b64_tr_b16 a[102:103], v99 offset:4352\n\t"
        "s_waitcnt lgkmcnt(8)\n\t"
        "ds_read_b64_tr_b16 a[104:105], v100 offset:0\n\t"
        "ds_read_b64_tr_b16 a[106:107], v100 offset:256\n\t"
        "ds_read_b64_tr_b16 a[108:109], v100 offset:4096\n\t"
        "ds_read_b64_tr_b16 a[110:111], v100 offset:4352\n\t"
        "s_waitcnt lgkmcnt(8)\n\t"
        "ds_read_b64_tr_b16 a[112:113], v101 offset:0\n\t"
        "ds_read_b64_tr_b16 a[114:115], v101 offset:256\n\t"
        "ds_read_b64_tr_b16 a[116:117], v101 offset:4096\n\t"
        "ds_read_b64_tr_b16 a[118:119], v101 offset:4352\n\t"
        "s_waitcnt lgkmcnt(8)\n\t"
        "ds_read_b64_tr_b16 v[104:105], v102 offset:0\n\t"
        "ds_read_b64_tr_b16 v[106:107], v102 offset:256\n\t"
        "ds_read_b64_tr_b16 v[108:109], v102 offset:4096\n\t"
        "ds_read_b64_tr_b16 v[110:111], v102 offset:4352\n\t"
        "s_lshl_b32 s34, s43, 2\n\t"
        "s_add_u32 s34, s34, 7\n\t"
        "s_and_b32 s34, s34, 15\n\t"
        "s_lshl_b32 s34, s34, 19\n\t"
        "buffer_load_dwordx4 a[192:195], v96, s[16:19], s34 offen nt\n\t"
        "s_add_u32 s34, s34, 0x10000\n\t"
        "buffer_load_dwordx4 a[196:199], v96, s[16:19], s34 offen nt\n\t"
        "s_add_u32 s34, s34, 0x10000\n\t"
        "buffer_load_dwordx4 a[200:203], v96, s[16:19], s34 offen nt\n\t"
        "s_add_u32 s34, s34, 0x10000\n\t"
        "buffer_load_dwordx4 a[204:207], v96, s[16:19], s34 offen nt\n\t"
        "s_add_u32 s34, s34, 0x10000\n\t"
        "buffer_load_dwordx4 a[208:211], v96, s[16:19], s34 offen nt\n\t"
        "s_add_u32 s34, s34, 0x10000\n\t"
        "buffer_load_dwordx4 a[212:215], v96, s[16:19], s34 offen nt\n\t"
        "s_add_u32 s34, s34, 0x10000\n\t"
        "buffer_load_dwordx4 a[216:219], v96, s[16:19], s34 offen nt\n\t"
        "s_add_u32 s34, s34, 0x10000\n\t"
        "buffer_load_dwordx4 a[220:223], v96, s[16:19], s34 offen nt\n\t"
        "s_waitcnt lgkmcnt(0)\n\t"
        "ds_write_b128 v126, v[104:107] offset:0\n\t"
        "ds_write_b128 v126, v[108:111] offset:1024\n\t"
        "s_waitcnt vmcnt(16)\n\t"
        "ds_write_b128 v97, a[128:131] offset:0\n\t"
        "ds_write_b128 v98, a[132:135] offset:1024\n\t"
        "ds_write_b128 v97, a[136:139] offset:2048\n\t"
        "ds_write_b128 v98, a[140:143] offset:3072\n\t"
        "ds_write_b128 v97, a[144:147] offset:4096\n\t"
        "ds_write_b128 v98, a[148:151] offset:5120\n\t"
        "ds_write_b128 v97, a[152:155] offset:6144\n\t"
        "ds_write_b128 v98, a[156:159] offset:7168\n\t"
        "ds_read_b64_tr_b16 a[120:121], v99 offset:0\n\t"
        "ds_read_b64_tr_b16 a[122:123], v99 offset:256\n\t"
        "ds_read_b64_tr_b16 a[124:125], v99 offset:4096\n\t"
        "ds_read_b64_tr_b16 a[126:127], v99 offset:4352\n\t"
        "s_waitcnt lgkmcnt(8)\n\t"
        "ds_read_b64_tr_b16 a[128:129], v100 offset:0\n\t"
        "ds_read_b64_tr_b16 a[130:131], v100 offset:256\n\t"
        "ds_read_b64_tr_b16 a[132:133], v100 offset:4096\n\t"
        "ds_read_b64_tr_b16 a[134:135], v100 offset:4352\n\t"
        "s_waitcnt lgkmcnt(8)\n\t"
        "ds_read_b64_tr_b16 a[136:137], v101 offset:0\n\t"
        "ds_read_b64_tr_b16 a[138:139], v101 offset:256\n\t"
        "ds_read_b64_tr_b16 a[140:141], v101 offset:4096\n\t"
        "ds_read_b64_tr_b16 a[142:143], v101 offset:4352\n\t"
        "s_waitcnt lgkmcnt(8)\n\t"
        "ds_read_b64_tr_b16 v[104:105], v102 offset:0\n\t"
        "ds_read_b64_tr_b16 v[106:107], v102 offset:256\n\t"
        "ds_read_b64_tr_b16 v[108:109], v102 offset:4096\n\t"
        "ds_read_b64_tr_b16 v[110:111], v102 offset:4352\n\t"
        "s_lshl_b32 s34, s43, 2\n\t"
        "s_add_u32 s34, s34, 8\n\t"
        "s_and_b32 s34, s34, 15\n\t"
        "s_lshl_b32 s34, s34, 19\n\t"
        "buffer_load_dwordx4 a[224:227], v96, s[16:19], s34 offen nt\n\t"
        "s_add_u32 s34, s34, 0x10000\n\t"
        "buffer_load_dwordx4 a[228:231], v96, s[16:19], s34 offen nt\n\t"
        "s_add_u32 s34, s34, 0x10000\n\t"
        "buffer_load_dwordx4 a[232:235], v96, s[16:19], s34 offen nt\n\t"
        "s_add_u32 s34, s34, 0x10000\n\t"
        "buffer_load_dwordx4 a[236:239], v96, s[16:19], s34 offen nt\n\t"
        "s_add_u32 s34, s34, 0x10000\n\t"
        "buffer_load_dwordx4 a[240:243], v96, s[16:19], s34 offen nt\n\t"
        "s_add_u32 s34, s34, 0x10000\n\t"
        "buffer_load_dwordx4 a[244:247], v96, s[16:19], s34 offen nt\n\t"
        "s_add_u32 s34, s34, 0x10000\n\t"
        "buffer_load_dwordx4 a[248:251], v96, s[16:19], s34 offen nt\n\t"
        "s_add_u32 s34, s34, 0x10000\n\t"
        "buffer_load_dwordx4 a[252:255], v96, s[16:19], s34 offen nt\n\t"
        "s_waitcnt lgkmcnt(0)\n\t"
        "ds_write_b128 v126, v[104:107] offset:2048\n\t"
        "ds_write_b128 v126, v[108:111] offset:3072\n\t"
        "s_waitcnt vmcnt(16)\n\t"
        "ds_write_b128 v97, a[160:163] offset:0\n\t"
        "ds_write_b128 v98, a[164:167] offset:1024\n\t"
        "ds_write_b128 v97, a[168:171] offset:2048\n\t"
        "ds_write_b128 v98, a[172:175] offset:3072\n\t"
        "ds_write_b128 v97, a[176:179] offset:4096\n\t"
        "ds_write_b128 v98, a[180:183] offset:5120\n\t"
        "ds_write_b128 v97, a[184:187] offset:6144\n\t"
        "ds_write_b128 v98, a[188:191] offset:7168\n\t"
        "ds_read_b64_tr_b16 a[144:145], v99 offset:0\n\t"
        "ds_read_b64_tr_b16 a[146:147], v99 offset:256\n\t"
        "ds_read_b64_tr_b16 a[148:149], v99 offset:4096\n\t"
        "ds_read_b64_tr_b16 a[150:151], v99 offset:4352\n\t"
        "s_waitcnt lgkmcnt(8)\n\t"
        "ds_read_b64_tr_b16 a[152:153], v100 offset:0\n\t"
        "ds_read_b64_tr_b16 a[154:155], v100 offset:256\n\t"
        "ds_read_b64_tr_b16 a[156:157], v100 offset:4096\n\t"
        "ds_read_b64_tr_b16 a[158:159], v100 offset:4352\n\t"
        "s_waitcnt lgkmcnt(8)\n\t"
        "ds_read_b64_tr_b16 a[160:161], v101 offset:0\n\t"
        "ds_read_b64_tr_b16 a[162:163], v101 offset:256\n\t"
        "ds_read_b64_tr_b16 a[164:165], v101 offset:4096\n\t"
        "ds_read_b64_tr_b16 a[166:167], v101 offset:4352\n\t"
        "s_waitcnt lgkmcnt(8)\n\t"
        "ds_read_b64_tr_b16 v[104:105], v102 offset:0\n\t"
        "ds_read_b64_tr_b16 v[106:107], v102 offset:256\n\t"
        "ds_read_b64_tr_b16 v[108:109], v102 offset:4096\n\t"
        "ds_read_b64_tr_b16 v[110:111], v102 offset:4352\n\t"
        "s_lshl_b32 s34, s43, 2\n\t"
        "s_add_u32 s34, s34, 9\n\t"
        "s_and_b32 s34, s34, 15\n\t"
        "s_lshl_b32 s34, s34, 19\n\t"
        "buffer_load_dwordx4 v[128:131], v96, s[16:19], s34 offen nt\n\t"
        "s_add_u32 s34, s34, 0x10000\n\t"
        "buffer_load_dwordx4 v[132:135], v96, s[16:19], s34 offen nt\n\t"
        "s_add_u32 s34, s34, 0x10000\n\t"
        "buffer_load_dwordx4 v[136:139], v96, s[16:19], s34 offen nt\n\t"
        "s_add_u32 s34, s34, 0x10000\n\t"
        "buffer_load_dwordx4 v[140:143], v96, s[16:19], s34 offen nt\n\t"
        "s_add_u32 s34, s34, 0x10000\n\t"
        "buffer_load_dwordx4 v[144:147], v96, s[16:19], s34 offen nt\n\t"
        "s_add_u32 s34, s34, 0x10000\n\t"
        "buffer_load_dwordx4 v[148:151], v96, s[16:19], s34 offen nt\n\t"
        "s_add_u32 s34, s34, 0x10000\n\t"
        "buffer_load_dwordx4 v[152:155], v96, s[16:19], s34 offen nt\n\t"
        "s_add_u32 s34, s34, 0x10000\n\t"
        "buffer_load_dwordx4 v[156:159], v96, s[16:19], s34 offen nt\n\t"
        "s_waitcnt lgkmcnt(0)\n\t"
        "ds_write_b128 v126, v[104:107] offset:4096\n\t"
        "ds_write_b128 v126, v[108:111] offset:5120\n\t"
        "s_waitcnt vmcnt(16)\n\t"
        "ds_write_b128 v97, a[192:195] offset:0\n\t"
        "ds_write_b128 v98, a[196:199] offset:1024\n\t"
        "ds_write_b128 v97, a[200:203] offset:2048\n\t"
        "ds_write_b128 v98, a[204:207] offset:3072\n\t"
        "ds_write_b128 v97, a[208:211] offset:4096\n\t"
        "ds_write_b128 v98, a[212:215] offset:5120\n\t"
        "ds_write_b128 v97, a[216:219] offset:6144\n\t"
        "ds_write_b128 v98, a[220:223] offset:7168\n\t"
        "ds_read_b64_tr_b16 a[168:169], v99 offset:0\n\t"
        "ds_read_b64_tr_b16 a[170:171], v99 offset:256\n\t"
        "ds_read_b64_tr_b16 a[172:173], v99 offset:4096\n\t"
        "ds_read_b64_tr_b16 a[174:175], v99 offset:4352\n\t"
        "s_waitcnt lgkmcnt(8)\n\t"
        "ds_read_b64_tr_b16 a[176:177], v100 offset:0\n\t"
        "ds_read_b64_tr_b16 a[178:179], v100 offset:256\n\t"
        "ds_read_b64_tr_b16 a[180:181], v100 offset:4096\n\t"
        "ds_read_b64_tr_b16 a[182:183], v100 offset:4352\n\t"
        "s_waitcnt lgkmcnt(8)\n\t"
        "ds_read_b64_tr_b16 a[184:185], v101 offset:0\n\t"
        "ds_read_b64_tr_b16 a[186:187], v101 offset:256\n\t"
        "ds_read_b64_tr_b16 a[188:189], v101 offset:4096\n\t"
        "ds_read_b64_tr_b16 a[190:191], v101 offset:4352\n\t"
        "s_waitcnt lgkmcnt(8)\n\t"
        "ds_read_b64_tr_b16 v[104:105], v102 offset:0\n\t"
        "ds_read_b64_tr_b16 v[106:107], v102 offset:256\n\t"
        "ds_read_b64_tr_b16 v[108:109], v102 offset:4096\n\t"
        "ds_read_b64_tr_b16 v[110:111], v102 offset:4352\n\t"
        "s_lshl_b32 s34, s43, 2\n\t"
        "s_add_u32 s34, s34, 10\n\t"
        "s_and_b32 s34, s34, 15\n\t"
        "s_lshl_b32 s34, s34, 19\n\t"
        "buffer_load_dwordx4 v[160:163], v96, s[16:19], s34 offen nt\n\t"
        "s_add_u32 s34, s34, 0x10000\n\t"
        "buffer_load_dwordx4 v[164:167], v96, s[16:19], s34 offen nt\n\t"
        "s_add_u32 s34, s34, 0x10000\n\t"
        "buffer_load_dwordx4 v[168:171], v96, s[16:19], s34 offen nt\n\t"
        "s_add_u32 s34, s34, 0x10000\n\t"
        "buffer_load_dwordx4 v[172:175], v96, s[16:19], s34 offen nt\n\t"
        "s_add_u32 s34, s34, 0x10000\n\t"
        "buffer_load_dwordx4 v[176:179], v96, s[16:19], s34 offen nt\n\t"
        "s_add_u32 s34, s34, 0x10000\n\t"
        "buffer_load_dwordx4 v[180:183], v96, s[16:19], s34 offen nt\n\t"
        "s_add_u32 s34, s34, 0x10000\n\t"
        "buffer_load_dwordx4 v[184:187], v96, s[16:19], s34 offen nt\n\t"
        "s_add_u32 s34, s34, 0x10000\n\t"
        "buffer_load_dwordx4 v[188:191], v96, s[16:19], s34 offen nt\n\t"
        "s_waitcnt lgkmcnt(0)\n\t"
        "ds_write_b128 v126, v[104:107] offset:6144\n\t"
        "ds_write_b128 v126, v[108:111] offset:7168\n\t"
        "s_waitcnt vmcnt(16)\n\t"
        "ds_write_b128 v97, a[224:227] offset:0\n\t"
        "ds_write_b128 v98, a[228:231] offset:1024\n\t"
        "ds_write_b128 v97, a[232:235] offset:2048\n\t"
        "ds_write_b128 v98, a[236:239] offset:3072\n\t"
        "ds_write_b128 v97, a[240:243] offset:4096\n\t"
        "ds_write_b128 v98, a[244:247] offset:5120\n\t"
        "ds_write_b128 v97, a[248:251] offset:6144\n\t"
        "ds_write_b128 v98, a[252:255] offset:7168\n\t"
        "ds_read_b64_tr_b16 a[192:193], v99 offset:0\n\t"
        "ds_read_b64_tr_b16 a[194:195], v99 offset:256\n\t"
        "ds_read_b64_tr_b16 a[196:197], v99 offset:4096\n\t"
        "ds_read_b64_tr_b16 a[198:199], v99 offset:4352\n\t"
        "s_waitcnt lgkmcnt(8)\n\t"
        "ds_read_b64_tr_b16 a[200:201], v100 offset:0\n\t"
        "ds_read_b64_tr_b16 a[202:203], v100 offset:256\n\t"
        "ds_read_b64_tr_b16 a[204:205], v100 offset:4096\n\t"
        "ds_read_b64_tr_b16 a[206:207], v100 offset:4352\n\t"
        "s_waitcnt lgkmcnt(8)\n\t"
        "ds_read_b64_tr_b16 a[208:209], v101 offset:0\n\t"
        "ds_read_b64_tr_b16 a[210:211], v101 offset:256\n\t"
        "ds_read_b64_tr_b16 a[212:213], v101 offset:4096\n\t"
        "ds_read_b64_tr_b16 a[214:215], v101 offset:4352\n\t"
        "s_waitcnt lgkmcnt(8)\n\t"
        "ds_read_b64_tr_b16 v[104:105], v102 offset:0\n\t"
        "ds_read_b64_tr_b16 v[106:107], v102 offset:256\n\t"
        "ds_read_b64_tr_b16 v[108:109], v102 offset:4096\n\t"
        "ds_read_b64_tr_b16 v[110:111], v102 offset:4352\n\t"
        "s_lshl_b32 s34, s43, 2\n\t"
        "s_add_u32 s34, s34, 11\n\t"
        "s_and_b32 s34, s34, 15\n\t"
        "s_lshl_b32 s34, s34, 19\n\t"
        "buffer_load_dwordx4 v[192:195], v96, s[16:19], s34 offen nt\n\t"
        "s_add_u32 s34, s34, 0x10000\n\t"
        "buffer_load_dwordx4 v[196:199], v96, s[16:19], s34 offen nt\n\t"
        "s_add_u32 s34, s34, 0x10000\n\t"
        "buffer_load_dwordx4 v[200:203], v96, s[16:19], s34 offen nt\n\t"
        "s_add_u32 s34, s34, 0x10000\n\t"
        "buffer_load_dwordx4 v[204:207], v96, s[16:19], s34 offen nt\n\t"
        "s_add_u32 s34, s34, 0x10000\n\t"
        "buffer_load_dwordx4 v[208:211], v96, s[16:19], s34 offen nt\n\t"
        "s_add_u32 s34, s34, 0x10000\n\t"
        "buffer_load_dwordx4 v[212:215], v96, s[16:19], s34 offen nt\n\t"
        "s_add_u32 s34, s34, 0x10000\n\t"
        "buffer_load_dwordx4 v[216:219], v96, s[16:19], s34 offen nt\n\t"
        "s_add_u32 s34, s34, 0x10000\n\t"
        "buffer_load_dwordx4 v[220:223], v96, s[16:19], s34 offen nt\n\t"
        "s_waitcnt lgkmcnt(0)\n\t"
        "ds_write_b128 v117, v[104:107] offset:8192\n\t"
        "ds_write_b128 v117, v[108:111] offset:9216\n\t"
        "s_waitcnt vmcnt(16)\n\t"
        "ds_write_b128 v97, v[128:131] offset:0\n\t"
        "ds_write_b128 v98, v[132:135] offset:1024\n\t"
        "ds_write_b128 v97, v[136:139] offset:2048\n\t"
        "ds_write_b128 v98, v[140:143] offset:3072\n\t"
        "ds_write_b128 v97, v[144:147] offset:4096\n\t"
        "ds_write_b128 v98, v[148:151] offset:5120\n\t"
        "ds_write_b128 v97, v[152:155] offset:6144\n\t"
        "ds_write_b128 v98, v[156:159] offset:7168\n\t"
        "ds_read_b64_tr_b16 a[216:217], v99 offset:0\n\t"
        "ds_read_b64_tr_b16 a[218:219], v99 offset:256\n\t"
        "ds_read_b64_tr_b16 a[220:221], v99 offset:4096\n\t"
        "ds_read_b64_tr_b16 a[222:223], v99 offset:4352\n\t"
        "s_waitcnt lgkmcnt(8)\n\t"
        "ds_read_b64_tr_b16 a[224:225], v100 offset:0\n\t"
        "ds_read_b64_tr_b16 a[226:227], v100 offset:256\n\t"
        "ds_read_b64_tr_b16 a[228:229], v100 offset:4096\n\t"
        "ds_read_b64_tr_b16 a[230:231], v100 offset:4352\n\t"
        "s_waitcnt lgkmcnt(8)\n\t"
        "ds_read_b64_tr_b16 a[232:233], v101 offset:0\n\t"
        "ds_read_b64_tr_b16 a[234:235], v101 offset:256\n\t"
        "ds_read_b64_tr_b16 a[236:237], v101 offset:4096\n\t"
        "ds_read_b64_tr_b16 a[238:239], v101 offset:4352\n\t"
        "s_waitcnt lgkmcnt(8)\n\t"
        "ds_read_b64_tr_b16 v[104:105], v102 offset:0\n\t"
        "ds_read_b64_tr_b16 v[106:107], v102 offset:256\n\t"
        "ds_read_b64_tr_b16 v[108:109], v102 offset:4096\n\t"
        "ds_read_b64_tr_b16 v[110:111], v102 offset:4352\n\t"
        "s_lshl_b32 s34, s43, 2\n\t"
        "s_add_u32 s34, s34, 12\n\t"
        "s_and_b32 s34, s34, 15\n\t"
        "s_lshl_b32 s34, s34, 19\n\t"
        "buffer_load_dwordx4 v[224:227], v96, s[16:19], s34 offen nt\n\t"
        "s_add_u32 s34, s34, 0x10000\n\t"
        "buffer_load_dwordx4 v[228:231], v96, s[16:19], s34 offen nt\n\t"
        "s_add_u32 s34, s34, 0x10000\n\t"
        "buffer_load_dwordx4 v[232:235], v96, s[16:19], s34 offen nt\n\t"
        "s_add_u32 s34, s34, 0x10000\n\t"
        "buffer_load_dwordx4 v[236:239], v96, s[16:19], s34 offen nt\n\t"
        "s_add_u32 s34, s34, 0x10000\n\t"
        "buffer_load_dwordx4 v[240:243], v96, s[16:19], s34 offen nt\n\t"
        "s_add_u32 s34, s34, 0x10000\n\t"
        "buffer_load_dwordx4 v[244:247], v96, s[16:19], s34 offen nt\n\t"
        "s_add_u32 s34, s34, 0x10000\n\t"
        "buffer_load_dwordx4 v[248:251], v96, s[16:19], s34 offen nt\n\t"
        "s_add_u32 s34, s34, 0x10000\n\t"
        "buffer_load_dwordx4 v[252:255], v96, s[16:19], s34 offen nt\n\t"
        "s_waitcnt lgkmcnt(0)\n\t"
        "ds_write_b128 v117, v[104:107] offset:10240\n\t"
        "ds_write_b128 v117, v[108:111] offset:11264\n\t"
        "s_waitcnt vmcnt(16)\n\t"
        "ds_write_b128 v97, v[160:163] offset:0\n\t"
        "ds_write_b128 v98, v[164:167] offset:1024\n\t"
        "ds_write_b128 v97, v[168:171] offset:2048\n\t"
        "ds_write_b128 v98, v[172:175] offset:3072\n\t"
        "ds_write_b128 v97, v[176:179] offset:4096\n\t"
        "ds_write_b128 v98, v[180:183] offset:5120\n\t"
        "ds_write_b128 v97, v[184:187] offset:6144\n\t"
        "ds_write_b128 v98, v[188:191] offset:7168\n\t"
        "ds_read_b64_tr_b16 a[240:241], v99 offset:0\n\t"
        "ds_read_b64_tr_b16 a[242:243], v99 offset:256\n\t"
        "ds_read_b64_tr_b16 a[244:245], v99 offset:4096\n\t"
        "ds_read_b64_tr_b16 a[246:247], v99 offset:4352\n\t"
        "s_waitcnt lgkmcnt(8)\n\t"
        "ds_read_b64_tr_b16 a[248:249], v100 offset:0\n\t"
        "ds_read_b64_tr_b16 a[250:251], v100 offset:256\n\t"
        "ds_read_b64_tr_b16 a[252:253], v100 offset:4096\n\t"
        "ds_read_b64_tr_b16 a[254:255], v100 offset:4352\n\t"
        "s_waitcnt lgkmcnt(8)\n\t"
        "ds_read_b64_tr_b16 v[128:129], v101 offset:0\n\t"
        "ds_read_b64_tr_b16 v[130:131], v101 offset:256\n\t"
        "ds_read_b64_tr_b16 v[132:133], v101 offset:4096\n\t"
        "ds_read_b64_tr_b16 v[134:135], v101 offset:4352\n\t"
        "s_waitcnt lgkmcnt(8)\n\t"
        "ds_read_b64_tr_b16 v[104:105], v102 offset:0\n\t"
        "ds_read_b64_tr_b16 v[106:107], v102 offset:256\n\t"
        "ds_read_b64_tr_b16 v[108:109], v102 offset:4096\n\t"
        "ds_read_b64_tr_b16 v[110:111], v102 offset:4352\n\t"
        "s_lshl_b32 s34, s43, 2\n\t"
        "s_add_u32 s34, s34, 13\n\t"
        "s_and_b32 s34, s34, 15\n\t"
        "s_lshl_b32 s34, s34, 19\n\t"
        "buffer_load_dwordx4 v[0:3], v96, s[16:19], s34 offen nt\n\t"
        "s_add_u32 s34, s34, 0x10000\n\t"
        "buffer_load_dwordx4 v[4:7], v96, s[16:19], s34 offen nt\n\t"
        "s_add_u32 s34, s34, 0x10000\n\t"
        "buffer_load_dwordx4 v[8:11], v96, s[16:19], s34 offen nt\n\t"
        "s_add_u32 s34, s34, 0x10000\n\t"
        "buffer_load_dwordx4 v[12:15], v96, s[16:19], s34 offen nt\n\t"
        "s_add_u32 s34, s34, 0x10000\n\t"
        "buffer_load_dwordx4 v[16:19], v96, s[16:19], s34 offen nt\n\t"
        "s_add_u32 s34, s34, 0x10000\n\t"
        "buffer_load_dwordx4 v[20:23], v96, s[16:19], s34 offen nt\n\t"
        "s_add_u32 s34, s34, 0x10000\n\t"
        "buffer_load_dwordx4 v[24:27], v96, s[16:19], s34 offen nt\n\t"
        "s_add_u32 s34, s34, 0x10000\n\t"
        "buffer_load_dwordx4 v[28:31], v96, s[16:19], s34 offen nt\n\t"
        "s_waitcnt lgkmcnt(0)\n\t"
        "ds_write_b128 v117, v[104:107] offset:12288\n\t"
        "ds_write_b128 v117, v[108:111] offset:13312\n\t"
        "s_waitcnt vmcnt(16)\n\t"
        "ds_write_b128 v97, v[192:195] offset:0\n\t"
        "ds_write_b128 v98, v[196:199] offset:1024\n\t"
        "ds_write_b128 v97, v[200:203] offset:2048\n\t"
        "ds_write_b128 v98, v[204:207] offset:3072\n\t"
        "ds_write_b128 v97, v[208:211] offset:4096\n\t"
        "ds_write_b128 v98, v[212:215] offset:5120\n\t"
        "ds_write_b128 v97, v[216:219] offset:6144\n\t"
        "ds_write_b128 v98, v[220:223] offset:7168\n\t"
        "ds_read_b64_tr_b16 v[136:137], v99 offset:0\n\t"
        "ds_read_b64_tr_b16 v[138:139], v99 offset:256\n\t"
        "ds_read_b64_tr_b16 v[140:141], v99 offset:4096\n\t"
        "ds_read_b64_tr_b16 v[142:143], v99 offset:4352\n\t"
        "s_waitcnt lgkmcnt(8)\n\t"
        "ds_read_b64_tr_b16 v[144:145], v100 offset:0\n\t"
        "ds_read_b64_tr_b16 v[146:147], v100 offset:256\n\t"
        "ds_read_b64_tr_b16 v[148:149], v100 offset:4096\n\t"
        "ds_read_b64_tr_b16 v[150:151], v100 offset:4352\n\t"
        "s_waitcnt lgkmcnt(8)\n\t"
        "ds_read_b64_tr_b16 v[152:153], v101 offset:0\n\t"
        "ds_read_b64_tr_b16 v[154:155], v101 offset:256\n\t"
        "ds_read_b64_tr_b16 v[156:157], v101 offset:4096\n\t"
        "ds_read_b64_tr_b16 v[158:159], v101 offset:4352\n\t"
        "s_waitcnt lgkmcnt(8)\n\t"
        "ds_read_b64_tr_b16 v[104:105], v102 offset:0\n\t"
        "ds_read_b64_tr_b16 v[106:107], v102 offset:256\n\t"
        "ds_read_b64_tr_b16 v[108:109], v102 offset:4096\n\t"
        "ds_read_b64_tr_b16 v[110:111], v102 offset:4352\n\t"
        "s_lshl_b32 s34, s43, 2\n\t"
        "s_add_u32 s34, s34, 14\n\t"
        "s_and_b32 s34, s34, 15\n\t"
        "s_lshl_b32 s34, s34, 19\n\t"
        "buffer_load_dwordx4 v[32:35], v96, s[16:19], s34 offen nt\n\t"
        "s_add_u32 s34, s34, 0x10000\n\t"
        "buffer_load_dwordx4 v[36:39], v96, s[16:19], s34 offen nt\n\t"
        "s_add_u32 s34, s34, 0x10000\n\t"
        "buffer_load_dwordx4 v[40:43], v96, s[16:19], s34 offen nt\n\t"
        "s_add_u32 s34, s34, 0x10000\n\t"
        "buffer_load_dwordx4 v[44:47], v96, s[16:19], s34 offen nt\n\t"
        "s_add_u32 s34, s34, 0x10000\n\t"
        "buffer_load_dwordx4 v[48:51], v96, s[16:19], s34 offen nt\n\t"
        "s_add_u32 s34, s34, 0x10000\n\t"
        "buffer_load_dwordx4 v[52:55], v96, s[16:19], s34 offen nt\n\t"
        "s_add_u32 s34, s34, 0x10000\n\t"
        "buffer_load_dwordx4 v[56:59], v96, s[16:19], s34 offen nt\n\t"
        "s_add_u32 s34, s34, 0x10000\n\t"
        "buffer_load_dwordx4 v[60:63], v96, s[16:19], s34 offen nt\n\t"
        "s_waitcnt lgkmcnt(0)\n\t"
        "ds_write_b128 v117, v[104:107] offset:14336\n\t"
        "ds_write_b128 v117, v[108:111] offset:15360\n\t"
        "s_waitcnt vmcnt(16)\n\t"
        "ds_write_b128 v97, v[224:227] offset:0\n\t"
        "ds_write_b128 v98, v[228:231] offset:1024\n\t"
        "ds_write_b128 v97, v[232:235] offset:2048\n\t"
        "ds_write_b128 v98, v[236:239] offset:3072\n\t"
        "ds_write_b128 v97, v[240:243] offset:4096\n\t"
        "ds_write_b128 v98, v[244:247] offset:5120\n\t"
        "ds_write_b128 v97, v[248:251] offset:6144\n\t"
        "ds_write_b128 v98, v[252:255] offset:7168\n\t"
        "ds_read_b64_tr_b16 v[160:161], v99 offset:0\n\t"
        "ds_read_b64_tr_b16 v[162:163], v99 offset:256\n\t"
        "ds_read_b64_tr_b16 v[164:165], v99 offset:4096\n\t"
        "ds_read_b64_tr_b16 v[166:167], v99 offset:4352\n\t"
        "s_waitcnt lgkmcnt(8)\n\t"
        "ds_read_b64_tr_b16 v[168:169], v100 offset:0\n\t"
        "ds_read_b64_tr_b16 v[170:171], v100 offset:256\n\t"
        "ds_read_b64_tr_b16 v[172:173], v100 offset:4096\n\t"
        "ds_read_b64_tr_b16 v[174:175], v100 offset:4352\n\t"
        "s_waitcnt lgkmcnt(8)\n\t"
        "ds_read_b64_tr_b16 v[176:177], v101 offset:0\n\t"
        "ds_read_b64_tr_b16 v[178:179], v101 offset:256\n\t"
        "ds_read_b64_tr_b16 v[180:181], v101 offset:4096\n\t"
        "ds_read_b64_tr_b16 v[182:183], v101 offset:4352\n\t"
        "s_waitcnt lgkmcnt(8)\n\t"
        "ds_read_b64_tr_b16 v[104:105], v102 offset:0\n\t"
        "ds_read_b64_tr_b16 v[106:107], v102 offset:256\n\t"
        "ds_read_b64_tr_b16 v[108:109], v102 offset:4096\n\t"
        "ds_read_b64_tr_b16 v[110:111], v102 offset:4352\n\t"
        "s_lshl_b32 s34, s43, 2\n\t"
        "s_add_u32 s34, s34, 15\n\t"
        "s_and_b32 s34, s34, 15\n\t"
        "s_lshl_b32 s34, s34, 19\n\t"
        "buffer_load_dwordx4 v[64:67], v96, s[16:19], s34 offen nt\n\t"
        "s_add_u32 s34, s34, 0x10000\n\t"
        "buffer_load_dwordx4 v[68:71], v96, s[16:19], s34 offen nt\n\t"
        "s_add_u32 s34, s34, 0x10000\n\t"
        "buffer_load_dwordx4 v[72:75], v96, s[16:19], s34 offen nt\n\t"
        "s_add_u32 s34, s34, 0x10000\n\t"
        "buffer_load_dwordx4 v[76:79], v96, s[16:19], s34 offen nt\n\t"
        "s_add_u32 s34, s34, 0x10000\n\t"
        "buffer_load_dwordx4 v[80:83], v96, s[16:19], s34 offen nt\n\t"
        "s_add_u32 s34, s34, 0x10000\n\t"
        "buffer_load_dwordx4 v[84:87], v96, s[16:19], s34 offen nt\n\t"
        "s_add_u32 s34, s34, 0x10000\n\t"
        "buffer_load_dwordx4 v[88:91], v96, s[16:19], s34 offen nt\n\t"
        "s_add_u32 s34, s34, 0x10000\n\t"
        "buffer_load_dwordx4 v[92:95], v96, s[16:19], s34 offen nt\n\t"
        "s_waitcnt lgkmcnt(0)\n\t"
        "ds_write_b128 v117, v[104:107] offset:16384\n\t"
        "ds_write_b128 v117, v[108:111] offset:17408\n\t"
        "s_waitcnt vmcnt(16)\n\t"
        "ds_write_b128 v97, v[0:3] offset:0\n\t"
        "ds_write_b128 v98, v[4:7] offset:1024\n\t"
        "ds_write_b128 v97, v[8:11] offset:2048\n\t"
        "ds_write_b128 v98, v[12:15] offset:3072\n\t"
        "ds_write_b128 v97, v[16:19] offset:4096\n\t"
        "ds_write_b128 v98, v[20:23] offset:5120\n\t"
        "ds_write_b128 v97, v[24:27] offset:6144\n\t"
        "ds_write_b128 v98, v[28:31] offset:7168\n\t"
        "ds_read_b64_tr_b16 v[184:185], v99 offset:0\n\t"
        "ds_read_b64_tr_b16 v[186:187], v99 offset:256\n\t"
        "ds_read_b64_tr_b16 v[188:189], v99 offset:4096\n\t"
        "ds_read_b64_tr_b16 v[190:191], v99 offset:4352\n\t"
        "s_waitcnt lgkmcnt(8)\n\t"
        "ds_read_b64_tr_b16 v[192:193], v100 offset:0\n\t"
        "ds_read_b64_tr_b16 v[194:195], v100 offset:256\n\t"
        "ds_read_b64_tr_b16 v[196:197], v100 offset:4096\n\t"
        "ds_read_b64_tr_b16 v[198:199], v100 offset:4352\n\t"
        "s_waitcnt lgkmcnt(8)\n\t"
        "ds_read_b64_tr_b16 v[200:201], v101 offset:0\n\t"
        "ds_read_b64_tr_b16 v[202:203], v101 offset:256\n\t"
        "ds_read_b64_tr_b16 v[204:205], v101 offset:4096\n\t"
        "ds_read_b64_tr_b16 v[206:207], v101 offset:4352\n\t"
        "s_waitcnt lgkmcnt(8)\n\t"
        "ds_read_b64_tr_b16 v[104:105], v102 offset:0\n\t"
        "ds_read_b64_tr_b16 v[106:107], v102 offset:256\n\t"
        "ds_read_b64_tr_b16 v[108:109], v102 offset:4096\n\t"
        "ds_read_b64_tr_b16 v[110:111], v102 offset:4352\n\t"
        "s_waitcnt lgkmcnt(0)\n\t"
        "ds_write_b128 v117, v[104:107] offset:18432\n\t"
        "ds_write_b128 v117, v[108:111] offset:19456\n\t"
        "s_waitcnt vmcnt(8)\n\t"
        "ds_write_b128 v97, v[32:35] offset:0\n\t"
        "ds_write_b128 v98, v[36:39] offset:1024\n\t"
        "ds_write_b128 v97, v[40:43] offset:2048\n\t"
        "ds_write_b128 v98, v[44:47] offset:3072\n\t"
        "ds_write_b128 v97, v[48:51] offset:4096\n\t"
        "ds_write_b128 v98, v[52:55] offset:5120\n\t"
        "ds_write_b128 v97, v[56:59] offset:6144\n\t"
        "ds_write_b128 v98, v[60:63] offset:7168\n\t"
        "ds_read_b64_tr_b16 v[208:209], v99 offset:0\n\t"
        "ds_read_b64_tr_b16 v[210:211], v99 offset:256\n\t"
        "ds_read_b64_tr_b16 v[212:213], v99 offset:4096\n\t"
        "ds_read_b64_tr_b16 v[214:215], v99 offset:4352\n\t"
        "s_waitcnt lgkmcnt(8)\n\t"
        "ds_read_b64_tr_b16 v[216:217], v100 offset:0\n\t"
        "ds_read_b64_tr_b16 v[218:219], v100 offset:256\n\t"
        "ds_read_b64_tr_b16 v[220:221], v100 offset:4096\n\t"
        "ds_read_b64_tr_b16 v[222:223], v100 offset:4352\n\t"
        "s_waitcnt lgkmcnt(8)\n\t"
        "ds_read_b64_tr_b16 v[224:225], v101 offset:0\n\t"
        "ds_read_b64_tr_b16 v[226:227], v101 offset:256\n\t"
        "ds_read_b64_tr_b16 v[228:229], v101 offset:4096\n\t"
        "ds_read_b64_tr_b16 v[230:231], v101 offset:4352\n\t"
        "s_waitcnt lgkmcnt(8)\n\t"
        "ds_read_b64_tr_b16 v[104:105], v102 offset:0\n\t"
        "ds_read_b64_tr_b16 v[106:107], v102 offset:256\n\t"
        "ds_read_b64_tr_b16 v[108:109], v102 offset:4096\n\t"
        "ds_read_b64_tr_b16 v[110:111], v102 offset:4352\n\t"
        "s_waitcnt lgkmcnt(0)\n\t"
        "ds_write_b128 v117, v[104:107] offset:20480\n\t"
        "ds_write_b128 v117, v[108:111] offset:21504\n\t"
        "s_waitcnt vmcnt(0)\n\t"
        "ds_write_b128 v97, v[64:67] offset:0\n\t"
        "ds_write_b128 v98, v[68:71] offset:1024\n\t"
        "ds_write_b128 v97, v[72:75] offset:2048\n\t"
        "ds_write_b128 v98, v[76:79] offset:3072\n\t"
        "ds_write_b128 v97, v[80:83] offset:4096\n\t"
        "ds_write_b128 v98, v[84:87] offset:5120\n\t"
        "ds_write_b128 v97, v[88:91] offset:6144\n\t"
        "ds_write_b128 v98, v[92:95] offset:7168\n\t"
        "ds_read_b64_tr_b16 v[232:233], v99 offset:0\n\t"
        "ds_read_b64_tr_b16 v[234:235], v99 offset:256\n\t"
        "ds_read_b64_tr_b16 v[236:237], v99 offset:4096\n\t"
        "ds_read_b64_tr_b16 v[238:239], v99 offset:4352\n\t"
        "s_waitcnt lgkmcnt(8)\n\t"
        "ds_read_b64_tr_b16 v[240:241], v100 offset:0\n\t"
        "ds_read_b64_tr_b16 v[242:243], v100 offset:256\n\t"
        "ds_read_b64_tr_b16 v[244:245], v100 offset:4096\n\t"
        "ds_read_b64_tr_b16 v[246:247], v100 offset:4352\n\t"
        "s_waitcnt lgkmcnt(8)\n\t"
        "ds_read_b64_tr_b16 v[248:249], v101 offset:0\n\t"
        "ds_read_b64_tr_b16 v[250:251], v101 offset:256\n\t"
        "ds_read_b64_tr_b16 v[252:253], v101 offset:4096\n\t"
        "ds_read_b64_tr_b16 v[254:255], v101 offset:4352\n\t"
        "s_waitcnt lgkmcnt(8)\n\t"
        "ds_read_b64_tr_b16 v[104:105], v102 offset:0\n\t"
        "ds_read_b64_tr_b16 v[106:107], v102 offset:256\n\t"
        "ds_read_b64_tr_b16 v[108:109], v102 offset:4096\n\t"
        "ds_read_b64_tr_b16 v[110:111], v102 offset:4352\n\t"
        "s_mov_b32 s35, 33792\n\t"
        "buffer_load_dword v0, off, s[12:15], s35 sc1\n\t"
        "s_lshl_b32 s35, s44, 10\n\t"
        "v_lshlrev_b32_e32 v1, 2, v122\n\t"
        "v_and_b32_e32 v1, 0x3c0, v1\n\t"
        "buffer_load_dword v1, v1, s[12:15], s35 offen offset:0 sc1\n\t"
        "s_waitcnt lgkmcnt(0)\n\t"
        "ds_write_b128 v126, v[104:107] offset:8192\n\t"
        "ds_write_b128 v126, v[108:111] offset:9216\n\t"
        "s_waitcnt lgkmcnt(0)\n\t"
        "v_mov_b32_e32 v110, v103\n\t"
        "ds_read_b128 v[64:67], v126 offset:0\n\t"
        "ds_read_b128 v[68:71], v126 offset:1024\n\t"
        "ds_read_b128 v[72:75], v126 offset:2048\n\t"
        "ds_read_b128 v[76:79], v126 offset:3072\n\t"
        "ds_read_b128 v[80:83], v126 offset:4096\n\t"
        "ds_read_b128 v[84:87], v126 offset:5120\n\t"
        "ds_read_b128 v[88:91], v126 offset:6144\n\t"
        "ds_read_b128 v[92:95], v126 offset:7168\n\t"
        "ds_read_b128 v[96:99], v126 offset:8192\n\t"
        "ds_read_b128 v[100:103], v126 offset:9216\n\t"
        "s_waitcnt lgkmcnt(0)\n\t"
        "v_mov_b32_e32 v126, 0\n\t"
        "s_barrier\n\t"
        "s_mov_b32 s48, 0\n\t"
        "s_mov_b32 s54, 0\n\t"
        "s_movk_i32 s56, 24576\n\t"
        "s_and_b32 s58, s43, 1\n\t"
        "s_lshl_b32 s58, s58, 12\n\t"
        "s_lshl_b32 s57, s44, 5\n\t"
        "s_add_u32 s57, s57, s43\n\t"
        "s_lshl_b32 s57, s57, 2\n\t"
        "s_add_u32 s57, s57, 458752\n\t"
        "s_waitcnt vmcnt(0)\n\t"
        "v_mov_b32_e32 v111, v1\n\t"
        "s_lshr_b32 s59, s48, 1\n\t"
        "s_cmp_gt_u32 s54, s59\n\t"
        "s_cbranch_scc1 LA_kv0ok%=\n\t"
        "s_mov_b32 s42, 0\n\t"
        "s_waitcnt vmcnt(0)\n\t"
        "s_branch LA_kv0w%=\n\t"
        "LA_kv0%=:\n\t"
        "s_waitcnt vmcnt(0)\n\t"
        "LA_kv0w%=:\n\t"
        "v_min_u32_dpp v111, v111, v111 quad_perm:[1,0,3,2] row_mask:0xf bank_mask:0xf\n\t"
        "s_nop 1\n\t"
        "v_min_u32_dpp v111, v111, v111 quad_perm:[2,3,0,1] row_mask:0xf bank_mask:0xf\n\t"
        "s_nop 1\n\t"
        "v_min_u32_dpp v111, v111, v111 row_ror:4 row_mask:0xf bank_mask:0xf\n\t"
        "s_nop 1\n\t"
        "v_min_u32_dpp v111, v111, v111 row_ror:8 row_mask:0xf bank_mask:0xf\n\t"
        "s_nop 0\n\t"
        "v_readfirstlane_b32 s54, v111\n\t"
        "s_nop 0\n\t"
        "s_cmp_gt_u32 s54, s59\n\t"
        "s_cbranch_scc1 LA_kv0ok%=\n\t"
        "s_sleep 4\n\t"
        "s_lshl_b32 s35, s44, 10\n\t"
        "v_lshlrev_b32_e32 v111, 2, v122\n\t"
        "v_and_b32_e32 v111, 0x3c0, v111\n\t"
        "buffer_load_dword v111, v111, s[12:15], s35 offen offset:0 sc1\n\t"
        "s_add_u32 s42, s42, 1\n\t"
        "s_cmp_lt_u32 s42, 0x100000\n\t"
        "s_cbranch_scc1 LA_kv0%=\n\t"
        "s_movk_i32 s34, 16\n\t"
        "s_branch LA_die%=\n\t"
        "LA_kv0ok%=:\n\t"
        "s_and_b32 s36, s48, 1\n\t"
        "s_mul_i32 s36, s36, 24576\n\t"
        "s_add_u32 s36, s36, s55\n\t"
        "s_lshl_b32 s37, s48, 18\n\t"
        "s_lshl_b32 s34, s43, 11\n\t"
        "s_add_u32 s37, s37, s34\n\t"
        "s_add_u32 m0, s36, 0\n\t"
        "s_add_u32 s34, s37, 29548544\n\t"
        "buffer_load_dwordx4 v122, s[12:15], s34 offen sc1 lds\n\t"
        "s_add_u32 m0, s36, 1024\n\t"
        "s_add_u32 s34, s37, 29549568\n\t"
        "buffer_load_dwordx4 v122, s[12:15], s34 offen sc1 lds\n\t"
        "s_add_u32 m0, s36, 8192\n\t"
        "s_add_u32 s34, s37, 12771328\n\t"
        "buffer_load_dwordx4 v122, s[12:15], s34 offen sc1 lds\n\t"
        "s_add_u32 m0, s36, 9216\n\t"
        "s_add_u32 s34, s37, 12772352\n\t"
        "buffer_load_dwordx4 v122, s[12:15], s34 offen sc1 lds\n\t"
        "s_add_u32 m0, s36, 16384\n\t"
        "s_add_u32 s34, s37, 46325760\n\t"
        "buffer_load_dwordx4 v122, s[12:15], s34 offen sc1 lds\n\t"
        "s_add_u32 m0, s36, 17408\n\t"
        "s_add_u32 s34, s37, 46326784\n\t"
        "buffer_load_dwordx4 v122, s[12:15], s34 offen sc1 lds\n\t"
        "s_mov_b32 s42, 0\n\t"
        "LA_cal%=:\n\t"
        "v_readfirstlane_b32 s34, v0\n\t"
        "s_nop 0\n\t"
        "s_cmp_lg_u32 s34, 0\n\t"
        "s_cbranch_scc1 LA_calok%=\n\t"
        "s_sleep 4\n\t"
        "s_mov_b32 s35, 33792\n\t"
        "buffer_load_dword v0, off, s[12:15], s35 sc1\n\t"
        "s_waitcnt vmcnt(0)\n\t"
        "s_add_u32 s42, s42, 1\n\t"
        "s_cmp_lt_u32 s42, 0x100000\n\t"
        "s_cbranch_scc1 LA_cal%=\n\t"
        "s_movk_i32 s34, 8\n\t"
        "s_branch LA_die%=\n\t"
        "LA_calok%=:\n\t"
        "s_and_b32 s34, s34, 8192\n\t"
        "s_add_u32 s8, s8, s34\n\t"
        "s_addc_u32 s9, s9, 0\n\t"
        "s_barrier\n\t"
        "s_cmp_lg_u32 s43, 0\n\t"
        "s_cbranch_scc1 LA_st%=\n\t"
        "v_mov_b32_e32 v2, 1\n\t"
        "s_mov_b32 s35, 34304\n\t"
        "s_mov_b64 exec, 1\n\t"
        "buffer_atomic_add v2, off, s[12:15], s35 sc0\n\t"
        "s_mov_b64 exec, -1\n\t"
        "s_waitcnt vmcnt(0)\n\t"
        "v_readfirstlane_b32 s34, v2\n\t"
        "s_nop 0\n\t"
        "s_cmp_lg_u32 s34, 95\n\t"
        "s_cbranch_scc1 LA_st%=\n\t"
        "s_memrealtime s[32:33]\n\t"
        "v_mbcnt_lo_u32_b32 v3, -1, 0\n\t"
        "v_lshlrev_b32_e32 v3, 7, v3\n\t"
        "s_waitcnt lgkmcnt(0)\n\t"
        "s_add_u32 s34, s32, 50\n\t"
        "s_or_b32 s34, s34, 1\n\t"
        "v_mov_b32_e32 v2, s34\n\t"
        "s_mov_b32 s35, 36864\n\t"
        "s_mov_b64 exec, 0xffff\n\t"
        "buffer_store_dword v2, v3, s[12:15], s35 offen sc1\n\t"
        "s_mov_b64 exec, -1\n\t"
        "LA_st%=:\n\t"
        "s_mov_b32 s24, 0\n\t"
        "s_mov_b32 s26, 0\n\t"
        "s_mov_b32 s27, 0x80000\n\t"
        "s_mov_b32 s28, 0x180000\n\t"
        "s_mov_b32 s31, 0\n\t"
        "s_mov_b32 s47, 0\n\t"
        "s_mov_b32 s38, 0xff00ff00\n\t"
        "s_mov_b32 s39, 0xff00ff00\n\t"
        "s_mov_b32 s40, 0\n\t"
        "s_mov_b32 s41, -1\n\t"
        "ds_read_b128 v[32:35], v117 offset:0\n\t"
        "ds_read_b128 v[36:39], v117 offset:1024\n\t"
        "ds_read_b128 v[40:43], v117 offset:2048\n\t"
        "ds_read_b128 v[44:47], v117 offset:3072\n\t"
        "ds_read_b128 v[48:51], v117 offset:4096\n\t"
        "ds_read_b128 v[52:55], v117 offset:5120\n\t"
        "ds_read_b128 v[56:59], v117 offset:6144\n\t"
        "ds_read_b128 v[60:63], v117 offset:7168\n\t"
        "v_mov_b64_e32 v[0:1], 0\n\t"
        "v_mov_b64_e32 v[2:3], 0\n\t"
        "v_mov_b64_e32 v[4:5], 0\n\t"
        "v_mov_b64_e32 v[6:7], 0\n\t"
        "v_mov_b64_e32 v[8:9], 0\n\t"
        "v_mov_b64_e32 v[10:11], 0\n\t"
        "v_mov_b64_e32 v[12:13], 0\n\t"
        "v_mov_b64_e32 v[14:15], 0\n\t"
        "s_mov_b32 s42, 0\n\t"
        "LA_gw%=:\n\t"
        "buffer_load_dword v16, off, s[12:15], s45 sc1\n\t"
        "s_waitcnt vmcnt(0)\n\t"
        "v_readfirstlane_b32 s25, v16\n\t"
        "s_nop 0\n\t"
        "s_cmp_lg_u32 s25, 0\n\t"
        "s_cbranch_scc1 LA_loop%=\n\t"
        "s_sleep 2\n\t"
        "s_add_u32 s42, s42, 1\n\t"
        "s_cmp_lt_u32 s42, 0x100000\n\t"
        "s_cbranch_scc1 LA_gw%=\n\t"
        "s_movk_i32 s34, 9\n\t"
        "s_branch LA_die%=\n\t"
        "LA_loop%=:\n\t"
        "s_mov_b32 s42, 0\n\t"
        "s_sub_u32 s35, s25, 1\n\t"
        "LA_wa%=:\n\t"
        "s_memrealtime s[32:33]\n\t"
        "s_waitcnt lgkmcnt(0)\n\t"
        "s_sub_u32 s34, s35, s32\n\t"
        "s_cmpk_lt_u32 s34, 0x2000\n\t"
        "s_cbranch_scc1 LA_wa%=\n\t"
        "LA_rd%=:\n\t"
        "buffer_load_dwordx4 v[16:19], v112, s[8:11], s26 offen offset:0 sc1\n\t"
        "s_nop 12\n\t"
        "buffer_load_dwordx4 v[20:23], v112, s[8:11], s26 offen offset:1024 sc1\n\t"
        "s_nop 12\n\t"
        "buffer_load_dwordx4 v[24:27], v112, s[8:11], s26 offen offset:2048 sc1\n\t"
        "s_nop 12\n\t"
        "buffer_load_dwordx4 v[28:31], v112, s[8:11], s26 offen offset:3072 sc1\n\t"
        "LA_go%=:\n\t"
        "s_waitcnt vmcnt(3)\n\t"
        "v_smfmac_f32_16x16x64_bf16 v[0:3], v[16:19], a[0:7], v110\n\t"
        "ds_write_b128 v113, v[16:19] offset:0\n\t"
        "v_smfmac_f32_16x16x64_bf16 v[4:7], v[16:19], a[8:15], v110\n\t"
        "v_max3_u32 v111, v16, v17, v18\n\t"
        "v_max_u32_e32 v111, v19, v111\n\t"
        "v_smfmac_f32_16x16x64_bf16 v[8:11], v[16:19], a[16:23], v110\n\t"
        "v_smfmac_f32_16x16x64_bf16 v[12:15], v[16:19], v[32:39], v110\n\t"
        "s_waitcnt vmcnt(2)\n\t"
        "v_smfmac_f32_16x16x64_bf16 v[0:3], v[20:23], a[24:31], v110\n\t"
        "ds_write_b128 v113, v[20:23] offset:1024\n\t"
        "v_smfmac_f32_16x16x64_bf16 v[4:7], v[20:23], a[32:39], v110\n\t"
        "v_max3_u32 v111, v20, v21, v111\n\t"
        "v_max3_u32 v111, v22, v23, v111\n\t"
        "v_smfmac_f32_16x16x64_bf16 v[8:11], v[20:23], a[40:47], v110\n\t"
        "s_waitcnt vmcnt(1)\n\t"
        "ds_write_b128 v113, v[24:27] offset:2048\n\t"
        "v_smfmac_f32_16x16x64_bf16 v[12:15], v[20:23], v[40:47], v110\n\t"
        "v_max3_u32 v111, v24, v25, v111\n\t"
        "v_max3_u32 v111, v26, v27, v111\n\t"
        "v_smfmac_f32_16x16x64_bf16 v[0:3], v[24:27], a[48:55], v110\n\t"
        "v_smfmac_f32_16x16x64_bf16 v[4:7], v[24:27], a[56:63], v110\n\t"
        "s_waitcnt vmcnt(0)\n\t"
        "ds_write_b128 v113, v[28:31] offset:3072\n\t"
        "v_max3_u32 v111, v28, v29, v111\n\t"
        "v_max3_u32 v111, v30, v31, v111\n\t"
        "v_smfmac_f32_16x16x64_bf16 v[8:11], v[24:27], a[64:71], v110\n\t"
        "v_cmp_eq_u32_e32 vcc, -1, v111\n\t"
        "v_smfmac_f32_16x16x64_bf16 v[12:15], v[24:27], v[48:55], v110\n\t"
        "s_waitcnt lgkmcnt(0)\n\t"
        "s_cbranch_vccnz LA_rdfail%=\n\t"
        "s_barrier\n\t"
        "ds_read_b128 v[16:19], v114 offset:0\n\t"
        "ds_read_b128 v[20:23], v114 offset:1024\n\t"
        "ds_read_b128 v[24:27], v114 offset:2048\n\t"
        "v_smfmac_f32_16x16x64_bf16 v[0:3], v[28:31], a[72:79], v110\n\t"
        "v_smfmac_f32_16x16x64_bf16 v[4:7], v[28:31], a[80:87], v110\n\t"
        "v_smfmac_f32_16x16x64_bf16 v[8:11], v[28:31], a[88:95], v110\n\t"
        "v_smfmac_f32_16x16x64_bf16 v[12:15], v[28:31], v[56:63], v110\n\t"
        "ds_read_b128 v[28:31], v114 offset:3072\n\t"
        "s_waitcnt lgkmcnt(3)\n\t"
        "v_smfmac_f32_16x16x64_bf16 v[12:15], v[16:19], v[64:71], v110\n\t"
        "ds_read_b128 v[32:35], v117 offset:8192\n\t"
        "v_smfmac_f32_16x16x64_bf16 v[0:3], v[16:19], a[96:103], v110\n\t"
        "ds_read_b128 v[36:39], v117 offset:9216\n\t"
        "v_smfmac_f32_16x16x64_bf16 v[4:7], v[16:19], a[104:111], v110\n\t"
        "v_smfmac_f32_16x16x64_bf16 v[8:11], v[16:19], a[112:119], v110\n\t"
        "ds_read_b128 v[16:19], v115 offset:0\n\t"
        "s_waitcnt lgkmcnt(5)\n\t"
        "v_smfmac_f32_16x16x64_bf16 v[12:15], v[20:23], v[72:79], v110\n\t"
        "ds_read_b128 v[40:43], v117 offset:10240\n\t"
        "v_smfmac_f32_16x16x64_bf16 v[0:3], v[20:23], a[120:127], v110\n\t"
        "ds_read_b128 v[44:47], v117 offset:11264\n\t"
        "v_smfmac_f32_16x16x64_bf16 v[4:7], v[20:23], a[128:135], v110\n\t"
        "v_smfmac_f32_16x16x64_bf16 v[8:11], v[20:23], a[136:143], v110\n\t"
        "ds_read_b128 v[20:23], v115 offset:1024\n\t"
        "s_waitcnt lgkmcnt(7)\n\t"
        "v_smfmac_f32_16x16x64_bf16 v[12:15], v[24:27], v[80:87], v110\n\t"
        "ds_read_b128 v[48:51], v117 offset:12288\n\t"
        "v_smfmac_f32_16x16x64_bf16 v[0:3], v[24:27], a[144:151], v110\n\t"
        "ds_read_b128 v[52:55], v117 offset:13312\n\t"
        "v_smfmac_f32_16x16x64_bf16 v[4:7], v[24:27], a[152:159], v110\n\t"
        "v_smfmac_f32_16x16x64_bf16 v[8:11], v[24:27], a[160:167], v110\n\t"
        "ds_read_b128 v[24:27], v115 offset:2048\n\t"
        "s_waitcnt lgkmcnt(9)\n\t"
        "v_smfmac_f32_16x16x64_bf16 v[12:15], v[28:31], v[88:95], v110\n\t"
        "ds_read_b128 v[56:59], v117 offset:14336\n\t"
        "v_smfmac_f32_16x16x64_bf16 v[0:3], v[28:31], a[168:175], v110\n\t"
        "ds_read_b128 v[60:63], v117 offset:15360\n\t"
        "v_smfmac_f32_16x16x64_bf16 v[4:7], v[28:31], a[176:183], v110\n\t"
        "v_smfmac_f32_16x16x64_bf16 v[8:11], v[28:31], a[184:191], v110\n\t"
        "ds_read_b128 v[28:31], v115 offset:3072\n\t"
        "s_waitcnt lgkmcnt(9)\n\t"
        "v_smfmac_f32_16x16x64_bf16 v[12:15], v[16:19], v[32:39], v110\n\t"
        "ds_read_b128 v[32:35], v117 offset:16384\n\t"
        "v_smfmac_f32_16x16x64_bf16 v[0:3], v[16:19], a[192:199], v110\n\t"
        "ds_read_b128 v[36:39], v117 offset:17408\n\t"
        "v_smfmac_f32_16x16x64_bf16 v[4:7], v[16:19], a[200:207], v110\n\t"
        "v_smfmac_f32_16x16x64_bf16 v[8:11], v[16:19], a[208:215], v110\n\t"
        "ds_read_b128 v[16:19], v116 offset:0\n\t"
        "s_waitcnt lgkmcnt(9)\n\t"
        "v_smfmac_f32_16x16x64_bf16 v[12:15], v[20:23], v[40:47], v110\n\t"
        "ds_read_b128 v[40:43], v117 offset:18432\n\t"
        "v_smfmac_f32_16x16x64_bf16 v[0:3], v[20:23], a[216:223], v110\n\t"
        "ds_read_b128 v[44:47], v117 offset:19456\n\t"
        "v_smfmac_f32_16x16x64_bf16 v[4:7], v[20:23], a[224:231], v110\n\t"
        "v_smfmac_f32_16x16x64_bf16 v[8:11], v[20:23], a[232:239], v110\n\t"
        "ds_read_b128 v[20:23], v116 offset:1024\n\t"
        "s_waitcnt lgkmcnt(9)\n\t"
        "v_smfmac_f32_16x16x64_bf16 v[12:15], v[24:27], v[48:55], v110\n\t"
        "ds_read_b128 v[48:51], v117 offset:20480\n\t"
        "v_smfmac_f32_16x16x64_bf16 v[0:3], v[24:27], a[240:247], v110\n\t"
        "ds_read_b128 v[52:55], v117 offset:21504\n\t"
        "v_smfmac_f32_16x16x64_bf16 v[4:7], v[24:27], a[248:255], v110\n\t"
        "v_smfmac_f32_16x16x64_bf16 v[8:11], v[24:27], v[128:135], v110\n\t"
        "ds_read_b128 v[24:27], v116 offset:2048\n\t"
        "s_waitcnt lgkmcnt(9)\n\t"
        "v_smfmac_f32_16x16x64_bf16 v[12:15], v[28:31], v[56:63], v110\n\t"
        "ds_read_b128 v[56:59], v124 offset:3072\n\t"
        "v_smfmac_f32_16x16x64_bf16 v[0:3], v[28:31], v[136:143], v110\n\t"
        "ds_read_b128 v[60:63], v124 offset:7168\n\t"
        "v_smfmac_f32_16x16x64_bf16 v[4:7], v[28:31], v[144:151], v110\n\t"
        "v_smfmac_f32_16x16x64_bf16 v[8:11], v[28:31], v[152:159], v110\n\t"
        "ds_read_b128 v[28:31], v116 offset:3072\n\t"
        "s_waitcnt lgkmcnt(9)\n\t"
        "v_smfmac_f32_16x16x64_bf16 v[12:15], v[16:19], v[32:39], v110\n\t"
        "ds_read_b128 v[32:35], v124 offset:0\n\t"
        "v_smfmac_f32_16x16x64_bf16 v[0:3], v[16:19], v[160:167], v110\n\t"
        "ds_read_b128 v[36:39], v124 offset:4096\n\t"
        "v_smfmac_f32_16x16x64_bf16 v[4:7], v[16:19], v[168:175], v110\n\t"
        "v_smfmac_f32_16x16x64_bf16 v[8:11], v[16:19], v[176:183], v110\n\t"
        "s_waitcnt lgkmcnt(8)\n\t"
        "v_smfmac_f32_16x16x64_bf16 v[12:15], v[20:23], v[40:47], v110\n\t"
        "ds_read_b128 v[40:43], v124 offset:1024\n\t"
        "v_smfmac_f32_16x16x64_bf16 v[8:11], v[20:23], v[200:207], v110\n\t"
        "ds_read_b128 v[44:47], v124 offset:5120\n\t"
        "s_waitcnt lgkmcnt(7)\n\t"
        "v_smfmac_f32_16x16x64_bf16 v[12:15], v[24:27], v[48:55], v110\n\t"
        "ds_read_b128 v[48:51], v124 offset:2048\n\t"
        "v_smfmac_f32_16x16x64_bf16 v[8:11], v[24:27], v[224:231], v110\n\t"
        "ds_read_b128 v[52:55], v124 offset:6144\n\t"
        "s_waitcnt lgkmcnt(6)\n\t"
        "v_smfmac_f32_16x16x64_bf16 v[12:15], v[28:31], v[96:103], v110\n\t"
        "v_smfmac_f32_16x16x64_bf16 v[8:11], v[28:31], v[248:255], v110\n\t"
        "v_smfmac_f32_16x16x64_bf16 v[0:3], v[20:23], v[184:191], v110\n\t"
        "buffer_load_dwordx4 v[104:107], v120, s[8:11], s26 offen sc1\n\t"
        "s_lshl_b32 s34, s43, 11\n\t"
        "v_add_u32_e32 v108, s34, v124\n\t"
        "v_smfmac_f32_16x16x64_bf16 v[0:3], v[24:27], v[208:215], v110\n\t"
        "v_add_u32_e32 v109, s58, v124\n\t"
        "v_smfmac_f32_16x16x64_bf16 v[4:7], v[20:23], v[192:199], v110\n\t"
        "v_smfmac_f32_16x16x64_bf16 v[0:3], v[28:31], v[232:239], v110\n\t"
        "v_smfmac_f32_16x16x64_bf16 v[4:7], v[24:27], v[216:223], v110\n\t"
        "v_smfmac_f32_16x16x64_bf16 v[4:7], v[28:31], v[240:247], v110\n\t"
        "v_permlane32_swap_b32_e32 v8, v12\n\t"
        "v_permlane32_swap_b32_e32 v9, v13\n\t"
        "v_permlane32_swap_b32_e32 v10, v14\n\t"
        "v_permlane32_swap_b32_e32 v11, v15\n\t"
        "v_add_f32_e32 v8, v8, v12\n\t"
        "v_add_f32_e32 v9, v9, v13\n\t"
        "v_add_f32_e32 v10, v10, v14\n\t"
        "v_add_f32_e32 v11, v11, v15\n\t"
        "v_cvt_pk_bf16_f32 v12, v8, v9\n\t"
        "v_cvt_pk_bf16_f32 v13, v10, v11\n\t"
        "ds_write_b64 v118, v[12:13] offset:256\n\t"
        "v_permlane32_swap_b32_e32 v0, v4\n\t"
        "v_permlane32_swap_b32_e32 v1, v5\n\t"
        "v_permlane32_swap_b32_e32 v2, v6\n\t"
        "v_permlane32_swap_b32_e32 v3, v7\n\t"
        "v_pk_add_f32 v[0:1], v[0:1], v[4:5]\n\t"
        "v_pk_add_f32 v[2:3], v[2:3], v[6:7]\n\t"
        "v_cvt_pk_bf16_f32 v4, v0, v1\n\t"
        "v_cvt_pk_bf16_f32 v5, v2, v3\n\t"
        "ds_write_b64 v118, v[4:5]\n\t"
        "s_cmp_eq_u32 s24, 63\n\t"
        "s_waitcnt lgkmcnt(0)\n\t"
        "s_barrier\n\t"
        "s_cbranch_scc1 LA_skip%=\n\t"
        "ds_read_b64_tr_b16 v[16:17], v119 offset:0\n\t"
        "ds_read_b64_tr_b16 v[18:19], v119 offset:32\n\t"
        "ds_read_b64_tr_b16 v[20:21], v119 offset:256\n\t"
        "ds_read_b64_tr_b16 v[22:23], v119 offset:288\n\t"
        "ds_read_b64_tr_b16 v[24:25], v119 offset:512\n\t"
        "ds_read_b64_tr_b16 v[26:27], v119 offset:544\n\t"
        "ds_read_b64_tr_b16 v[28:29], v119 offset:768\n\t"
        "ds_read_b64_tr_b16 v[30:31], v119 offset:800\n\t"
        "buffer_store_dword v126, v127, s[12:15], 0 offen sc1\n\t"
        "s_waitcnt lgkmcnt(6)\n\t"
        "v_mfma_f32_16x16x32_bf16 v[0:3], v[32:35], v[16:19], 0\n\t"
        "ds_read_b64_tr_b16 v[32:33], v119 offset:2112\n\t"
        "ds_read_b64_tr_b16 v[34:35], v119 offset:2144\n\t"
        "v_mfma_f32_16x16x32_bf16 v[4:7], v[36:39], v[16:19], 0\n\t"
        "ds_read_b64_tr_b16 v[36:37], v119 offset:2368\n\t"
        "ds_read_b64_tr_b16 v[38:39], v119 offset:2400\n\t"
        "s_waitcnt lgkmcnt(8)\n\t"
        "v_mfma_f32_16x16x32_bf16 v[0:3], v[40:43], v[20:23], v[0:3]\n\t"
        "ds_read_b64_tr_b16 v[40:41], v119 offset:2624\n\t"
        "ds_read_b64_tr_b16 v[42:43], v119 offset:2656\n\t"
        "v_mfma_f32_16x16x32_bf16 v[4:7], v[44:47], v[20:23], v[4:7]\n\t"
        "ds_read_b64_tr_b16 v[44:45], v119 offset:2880\n\t"
        "ds_read_b64_tr_b16 v[46:47], v119 offset:2912\n\t"
        "s_waitcnt lgkmcnt(10)\n\t"
        "v_mfma_f32_16x16x32_bf16 v[0:3], v[48:51], v[24:27], v[0:3]\n\t"
        "ds_read_b128 v[48:51], v108 offset:16384\n\t"
        "v_mfma_f32_16x16x32_bf16 v[4:7], v[52:55], v[24:27], v[4:7]\n\t"
        "ds_read_b128 v[52:55], v108 offset:17408\n\t"
        "s_waitcnt lgkmcnt(10)\n\t"
        "v_mfma_f32_16x16x32_bf16 v[0:3], v[56:59], v[28:31], v[0:3]\n\t"
        "v_mfma_f32_16x16x32_bf16 v[4:7], v[60:63], v[28:31], v[4:7]\n\t"
        "s_waitcnt lgkmcnt(2)\n\t"
        "v_mfma_f32_16x16x32_bf16 v[8:11], v[32:35], v[16:19], 0\n\t"
        "s_nop 1\n\t"
        "v_mfma_f32_16x16x32_bf16 v[8:11], v[36:39], v[20:23], v[8:11]\n\t"
        "s_nop 1\n\t"
        "v_mfma_f32_16x16x32_bf16 v[8:11], v[40:43], v[24:27], v[8:11]\n\t"
        "v_max3_f32 v59, v0, v1, v2\n\t"
        "v_max3_f32 v59, v59, v3, v4\n\t"
        "v_mfma_f32_16x16x32_bf16 v[8:11], v[44:47], v[28:31], v[8:11]\n\t"
        "v_max3_f32 v59, v59, v5, v6\n\t"
        "v_max_f32_e32 v60, v59, v7\n\t"
        "v_max_f32_e32 v59, v59, v7\n\t"
        "v_cndmask_b32_e64 v0, v0, v4, s[38:39]\n\t"
        "v_cndmask_b32_e64 v1, v1, v5, s[38:39]\n\t"
        "v_permlane32_swap_b32_e32 v59, v60\n\t"
        "v_cndmask_b32_e64 v2, v2, v6, s[38:39]\n\t"
        "v_cndmask_b32_e64 v3, v3, v7, s[38:39]\n\t"
        "v_max3_f32 v59, v59, v60, v8\n\t"
        "v_max3_f32 v59, v59, v9, v10\n\t"
        "v_max_f32_e32 v60, v59, v11\n\t"
        "v_max_f32_e32 v59, v59, v11\n\t"
        "v_cndmask_b32_e64 v8, v8, v9, s[38:39]\n\t"
        "v_cndmask_b32_e64 v10, v10, v11, s[38:39]\n\t"
        "v_permlane16_swap_b32_e32 v59, v60\n\t"
        "v_max_f32_e32 v59, v59, v60\n\t"
        "v_mul_f32_e32 v59, 0xbe02940c, v59\n\t"
        "v_cndmask_b32_e64 v58, v8, v10, s[40:41]\n\t"
        "v_fmamk_f32 v0, v0, 0x3e02940c, v59\n\t"
        "v_exp_f32_e32 v0, v0\n\t"
        "v_fmamk_f32 v1, v1, 0x3e02940c, v59\n\t"
        "v_exp_f32_e32 v1, v1\n\t"
        "v_fmamk_f32 v2, v2, 0x3e02940c, v59\n\t"
        "v_exp_f32_e32 v2, v2\n\t"
        "v_fmamk_f32 v3, v3, 0x3e02940c, v59\n\t"
        "v_exp_f32_e32 v3, v3\n\t"
        "v_fmamk_f32 v58, v58, 0x3e02940c, v59\n\t"
        "v_cvt_pk_bf16_f32 v12, v0, v1\n\t"
        "v_cvt_pk_bf16_f32 v13, v2, v3\n\t"
        "v_exp_f32_e32 v58, v58\n\t"
        "v_add_f32_e32 v60, v0, v1\n\t"
        "v_mov_b32_dpp v14, v12 row_ror:8 row_mask:0xf bank_mask:0xf\n\t"
        "v_mov_b32_dpp v15, v13 row_ror:8 row_mask:0xf bank_mask:0xf\n\t"
        "v_mov_b32_dpp v59, v58 row_ror:8 row_mask:0xf bank_mask:0xf\n\t"
        "v_add_f32_e32 v61, v2, v3\n\t"
        "s_waitcnt lgkmcnt(0)\n\t"
        "v_mfma_f32_16x16x32_bf16 v[4:7], v[48:51], v[12:15], 0\n\t"
        "v_cvt_pk_bf16_f32 v59, v58, v59\n\t"
        "v_add_f32_e32 v60, v60, v58\n\t"
        "v_mfma_f32_16x16x32_bf16 v[8:11], v[52:55], v[12:15], 0\n\t"
        "v_cndmask_b32_e64 v56, v59, 0, s[40:41]\n\t"
        "v_cndmask_b32_e64 v57, 0, v59, s[40:41]\n\t"
        "v_add_f32_e32 v59, v60, v61\n\t"
        "s_waitcnt vmcnt(1)\n\t"
        "v_max3_u32 v111, v104, v105, v106\n\t"
        "v_mfma_f32_16x16x16_bf16 v[4:7], v[104:105], v[56:57], v[4:7]\n\t"
        "v_add_f32_dpp v60, v59, v59 row_ror:8 row_mask:0xf bank_mask:0xf\n\t"
        "v_add_f32_dpp v61, v59, v59 row_ror:8 row_mask:0xf bank_mask:0xf\n\t"
        "v_mfma_f32_16x16x16_bf16 v[8:11], v[106:107], v[56:57], v[8:11]\n\t"
        "v_max_u32_e32 v111, v111, v107\n\t"
        "v_permlane16_swap_b32_e32 v60, v61\n\t"
        "v_add_f32_e32 v59, v60, v61\n\t"
        "v_add_f32_e32 v60, v60, v61\n\t"
        "v_cmp_eq_u32_e32 vcc, -1, v111\n\t"
        "s_nop 0\n\t"
        "v_permlane32_swap_b32_e32 v59, v60\n\t"
        "v_add_f32_e32 v59, v59, v60\n\t"
        "v_rcp_f32_e32 v59, v59\n\t"
        "s_cbranch_vccnz LA_vmfail%=\n\t"
        "LA_vmok%=:\n\t"
        "v_mul_f32_e32 v4, v4, v59\n\t"
        "v_mul_f32_e32 v5, v5, v59\n\t"
        "v_cvt_pk_bf16_f32 v0, v4, v5\n\t"
        "v_mul_f32_e32 v6, v6, v59\n\t"
        "v_mul_f32_e32 v7, v7, v59\n\t"
        "v_cvt_pk_bf16_f32 v1, v6, v7\n\t"
        "v_mul_f32_e32 v8, v8, v59\n\t"
        "v_mul_f32_e32 v9, v9, v59\n\t"
        "v_cvt_pk_bf16_f32 v2, v8, v9\n\t"
        "v_mul_f32_e32 v10, v10, v59\n\t"
        "v_mul_f32_e32 v11, v11, v59\n\t"
        "v_cvt_pk_bf16_f32 v3, v10, v11\n\t"
        "buffer_store_dwordx4 v[0:3], v121, s[8:11], s26 offen sc1\n\t"
        "LA_x%=:\n\t"
        "s_memrealtime s[52:53]\n\t"
        "ds_read_b128 v[16:19], v109 offset:8192\n\t"
        "ds_read_b128 v[20:23], v109 offset:9216\n\t"
        "ds_read_b128 v[24:27], v109 offset:10240\n\t"
        "ds_read_b128 v[28:31], v109 offset:11264\n\t"
        "ds_read_b128 v[56:59], v124 offset:0\n\t"
        "ds_read_b128 v[60:63], v124 offset:4096\n\t"
        "ds_read_b128 v[12:15], v124 offset:1024\n\t"
        "v_xor_b32_e32 v126, 0x800, v108\n\t"
        "s_add_u32 s48, s24, 1\n\t"
        "s_min_u32 s48, s48, 63\n\t"
        "buffer_load_dwordx2 v[108:109], v125, s[8:11], s26 offen sc1\n\t"
        "s_lshl_b32 s35, s44, 10\n\t"
        "v_lshlrev_b32_e32 v111, 2, v122\n\t"
        "v_and_b32_e32 v111, 0x3c0, v111\n\t"
        "buffer_load_dword v111, v111, s[12:15], s35 offen offset:0 sc1\n\t"
        "s_waitcnt lgkmcnt(6)\n\t"
        "v_mfma_f32_16x16x32_bf16 v[8:11], v[32:35], v[16:19], 0\n\t"
        "ds_read_b128 v[32:35], v124 offset:5120\n\t"
        "s_waitcnt lgkmcnt(6)\n\t"
        "v_mfma_f32_16x16x32_bf16 v[8:11], v[36:39], v[20:23], v[8:11]\n\t"
        "ds_read_b128 v[36:39], v124 offset:2048\n\t"
        "s_waitcnt lgkmcnt(6)\n\t"
        "v_mfma_f32_16x16x32_bf16 v[8:11], v[40:43], v[24:27], v[8:11]\n\t"
        "ds_read_b128 v[40:43], v124 offset:6144\n\t"
        "s_waitcnt lgkmcnt(6)\n\t"
        "v_mfma_f32_16x16x32_bf16 v[8:11], v[44:47], v[28:31], v[8:11]\n\t"
        "ds_read_b128 v[44:47], v124 offset:3072\n\t"
        "s_add_u32 s35, s24, 1\n\t"
        "s_waitcnt lgkmcnt(6)\n\t"
        "v_mfma_f32_16x16x32_bf16 v[0:3], v[56:59], v[16:19], 0\n\t"
        "ds_read_b128 v[56:59], v124 offset:7168\n\t"
        "s_waitcnt lgkmcnt(6)\n\t"
        "v_mfma_f32_16x16x32_bf16 v[4:7], v[60:63], v[16:19], 0\n\t"
        "s_and_b32 s36, s35, 1\n\t"
        "s_mul_i32 s36, s36, 24576\n\t"
        "s_waitcnt lgkmcnt(5)\n\t"
        "v_mfma_f32_16x16x32_bf16 v[0:3], v[12:15], v[20:23], v[0:3]\n\t"
        "s_add_u32 s36, s36, s55\n\t"
        "s_lshl_b32 s37, s48, 18\n\t"
        "s_waitcnt lgkmcnt(4)\n\t"
        "v_mfma_f32_16x16x32_bf16 v[4:7], v[32:35], v[20:23], v[4:7]\n\t"
        "v_xor_b32_e32 v32, 0x200, v120\n\t"
        "buffer_load_dwordx4 v[12:15], v32, s[8:11], s26 offen sc1\n\t"
        "s_waitcnt lgkmcnt(3)\n\t"
        "v_mfma_f32_16x16x32_bf16 v[0:3], v[36:39], v[24:27], v[0:3]\n\t"
        "s_lshl_b32 s34, s43, 11\n\t"
        "s_add_u32 s37, s37, s34\n\t"
        "s_waitcnt lgkmcnt(2)\n\t"
        "v_mfma_f32_16x16x32_bf16 v[4:7], v[40:43], v[24:27], v[4:7]\n\t"
        "v_cvt_pk_bf16_f32 v8, 0, v8\n\t"
        "v_cvt_pk_bf16_f32 v9, 0, v9\n\t"
        "v_cvt_pk_bf16_f32 v10, 0, v10\n\t"
        "v_cvt_pk_bf16_f32 v11, 0, v11\n\t"
        "s_waitcnt lgkmcnt(1)\n\t"
        "v_mfma_f32_16x16x32_bf16 v[0:3], v[44:47], v[28:31], v[0:3]\n\t"
        "v_mul_f32_e32 v8, 0x3db504f3, v8\n\t"
        "v_mul_f32_e32 v9, 0x3db504f3, v9\n\t"
        "v_mul_f32_e32 v10, 0x3db504f3, v10\n\t"
        "v_mul_f32_e32 v11, 0x3db504f3, v11\n\t"
        "s_waitcnt lgkmcnt(0)\n\t"
        "v_mfma_f32_16x16x32_bf16 v[4:7], v[56:59], v[28:31], v[4:7]\n\t"
        "v_cvt_pk_bf16_f32 v8, 0, v8\n\t"
        "v_cvt_pk_bf16_f32 v9, 0, v9\n\t"
        "v_cvt_pk_bf16_f32 v10, 0, v10\n\t"
        "v_cvt_pk_bf16_f32 v11, 0, v11\n\t"
        "ds_read_b128 v[56:59], v126 offset:16384\n\t"
        "ds_read_b128 v[60:63], v126 offset:17408\n\t"
        "v_mov_b64_e32 v[36:37], -1\n\t"
        "v_mov_b64_e32 v[38:39], -1\n\t"
        "v_mov_b32_e32 v40, 0xce6e6b28\n\t"
        "v_mov_b32_e32 v46, 0x3db504f3\n\t"
        "v_mov_b32_e32 v47, 0x3fb8aa3b\n\t"
        "v_cvt_pk_bf16_f32 v0, 0, v0\n\t"
        "v_cvt_pk_bf16_f32 v1, 0, v1\n\t"
        "v_cvt_pk_bf16_f32 v2, 0, v2\n\t"
        "v_cvt_pk_bf16_f32 v3, 0, v3\n\t"
        "v_cvt_pk_bf16_f32 v4, 0, v4\n\t"
        "v_cvt_pk_bf16_f32 v5, 0, v5\n\t"
        "v_cvt_pk_bf16_f32 v6, 0, v6\n\t"
        "v_cvt_pk_bf16_f32 v7, 0, v7\n\t"
        "v_pk_mul_f32 v[0:1], v[0:1], v[46:47] op_sel_hi:[1,0]\n\t"
        "v_pk_mul_f32 v[2:3], v[2:3], v[46:47] op_sel_hi:[1,0]\n\t"
        "v_pk_mul_f32 v[4:5], v[4:5], v[46:47] op_sel_hi:[1,0]\n\t"
        "v_pk_mul_f32 v[6:7], v[6:7], v[46:47] op_sel_hi:[1,0]\n\t"
        "s_lshr_b32 s59, s48, 1\n\t"
        "s_cmp_gt_u32 s54, s59\n\t"
        "s_cbranch_scc1 LA_kvok%=\n\t"
        "s_mov_b32 s42, 0\n\t"
        "s_waitcnt vmcnt(1)\n\t"
        "s_branch LA_kvw%=\n\t"
        "LA_kv%=:\n\t"
        "s_waitcnt vmcnt(0)\n\t"
        "LA_kvw%=:\n\t"
        "v_min_u32_dpp v111, v111, v111 quad_perm:[1,0,3,2] row_mask:0xf bank_mask:0xf\n\t"
        "s_nop 1\n\t"
        "v_min_u32_dpp v111, v111, v111 quad_perm:[2,3,0,1] row_mask:0xf bank_mask:0xf\n\t"
        "s_nop 1\n\t"
        "v_min_u32_dpp v111, v111, v111 row_ror:4 row_mask:0xf bank_mask:0xf\n\t"
        "s_nop 1\n\t"
        "v_min_u32_dpp v111, v111, v111 row_ror:8 row_mask:0xf bank_mask:0xf\n\t"
        "s_nop 0\n\t"
        "v_readfirstlane_b32 s54, v111\n\t"
        "s_nop 0\n\t"
        "s_cmp_gt_u32 s54, s59\n\t"
        "s_cbranch_scc1 LA_kvok%=\n\t"
        "s_sleep 4\n\t"
        "s_lshl_b32 s35, s44, 10\n\t"
        "v_lshlrev_b32_e32 v111, 2, v122\n\t"
        "v_and_b32_e32 v111, 0x3c0, v111\n\t"
        "buffer_load_dword v111, v111, s[12:15], s35 offen offset:0 sc1\n\t"
        "s_add_u32 s42, s42, 1\n\t"
        "s_cmp_lt_u32 s42, 0x100000\n\t"
        "s_cbranch_scc1 LA_kv%=\n\t"
        "s_movk_i32 s34, 16\n\t"
        "s_branch LA_die%=\n\t"
        "LA_kvok%=:\n\t"
        "s_add_u32 m0, s36, 0\n\t"
        "s_add_u32 s34, s37, 29548544\n\t"
        "buffer_load_dwordx4 v122, s[12:15], s34 offen sc1 lds\n\t"
        "v_cvt_pk_bf16_f32 v0, 0, v0\n\t"
        "v_cvt_pk_bf16_f32 v1, 0, v1\n\t"
        "v_cvt_pk_bf16_f32 v2, 0, v2\n\t"
        "v_cvt_pk_bf16_f32 v3, 0, v3\n\t"
        "v_cvt_pk_bf16_f32 v4, 0, v4\n\t"
        "v_cvt_pk_bf16_f32 v5, 0, v5\n\t"
        "v_cvt_pk_bf16_f32 v6, 0, v6\n\t"
        "v_cvt_pk_bf16_f32 v7, 0, v7\n\t"
        "s_add_u32 m0, s36, 1024\n\t"
        "s_add_u32 s34, s37, 29549568\n\t"
        "buffer_load_dwordx4 v122, s[12:15], s34 offen sc1 lds\n\t"
        "v_cndmask_b32_e64 v0, v40, v0, s[60:61]\n\t"
        "v_cndmask_b32_e64 v1, v40, v1, s[62:63]\n\t"
        "v_cndmask_b32_e64 v2, v40, v2, s[64:65]\n\t"
        "v_cndmask_b32_e64 v3, v40, v3, s[66:67]\n\t"
        "v_cndmask_b32_e64 v4, v40, v4, s[68:69]\n\t"
        "v_cndmask_b32_e64 v5, v40, v5, s[70:71]\n\t"
        "v_cndmask_b32_e64 v6, v40, v6, s[72:73]\n\t"
        "v_cndmask_b32_e64 v7, v40, v7, s[74:75]\n\t"
        "s_add_u32 m0, s36, 8192\n\t"
        "s_add_u32 s34, s37, 12771328\n\t"
        "buffer_load_dwordx4 v122, s[12:15], s34 offen sc1 lds\n\t"
        "v_max3_f32 v42, v0, v1, v2\n\t"
        "v_max3_f32 v42, v42, v3, v4\n\t"
        "v_max3_f32 v42, v42, v5, v6\n\t"
        "v_max3_f32 v42, v42, v7, v8\n\t"
        "v_max3_f32 v42, v42, v9, v10\n\t"
        "v_mov_b32_e32 v43, v11\n\t"
        "v_max_f32_e32 v44, v42, v43\n\t"
        "v_max_f32_e32 v42, v42, v43\n\t"
        "s_pack_ll_b32_b16 s34, s52, s24\n\t"
        "v_mov_b32_e32 v40, s34\n\t"
        "s_add_u32 s35, s26, s57\n\t"
        "v_permlane16_swap_b32_e32 v42, v44\n\t"
        "v_max_f32_e32 v45, v42, v44\n\t"
        "v_max_f32_e32 v42, v42, v44\n\t"
        "s_mov_b64 exec, 1\n\t"
        "buffer_store_dword v40, off, s[8:11], s35 sc1\n\t"
        "s_mov_b64 exec, -1\n\t"
        "v_permlane32_swap_b32_e32 v42, v45\n\t"
        "v_max_f32_e32 v42, v42, v45\n\t"
        "v_mul_f32_e32 v42, 0xbfb8aa3b, v42\n\t"
        "v_pk_fma_f32 v[0:1], v[0:1], v[46:47], v[42:43] op_sel:[0,1,0] op_sel_hi:[1,1,0]\n\t"
        "s_add_u32 m0, s36, 9216\n\t"
        "s_add_u32 s34, s37, 12772352\n\t"
        "buffer_load_dwordx4 v122, s[12:15], s34 offen sc1 lds\n\t"
        "v_exp_f32_e32 v0, v0\n\t"
        "v_exp_f32_e32 v1, v1\n\t"
        "v_pk_fma_f32 v[2:3], v[2:3], v[46:47], v[42:43] op_sel:[0,1,0] op_sel_hi:[1,1,0]\n\t"
        "v_exp_f32_e32 v2, v2\n\t"
        "v_exp_f32_e32 v3, v3\n\t"
        "v_pk_fma_f32 v[4:5], v[4:5], v[46:47], v[42:43] op_sel:[0,1,0] op_sel_hi:[1,1,0]\n\t"
        "v_exp_f32_e32 v4, v4\n\t"
        "s_add_u32 m0, s36, 16384\n\t"
        "s_add_u32 s34, s37, 46325760\n\t"
        "buffer_load_dwordx4 v122, s[12:15], s34 offen sc1 lds\n\t"
        "v_exp_f32_e32 v5, v5\n\t"
        "v_pk_fma_f32 v[6:7], v[6:7], v[46:47], v[42:43] op_sel:[0,1,0] op_sel_hi:[1,1,0]\n\t"
        "v_exp_f32_e32 v6, v6\n\t"
        "v_exp_f32_e32 v7, v7\n\t"
        "v_pk_fma_f32 v[8:9], v[8:9], v[46:47], v[42:43] op_sel:[0,1,0] op_sel_hi:[1,1,0]\n\t"
        "v_exp_f32_e32 v8, v8\n\t"
        "v_exp_f32_e32 v9, v9\n\t"
        "s_add_u32 m0, s36, 17408\n\t"
        "s_add_u32 s34, s37, 46326784\n\t"
        "buffer_load_dwordx4 v122, s[12:15], s34 offen sc1 lds\n\t"
        "v_pk_fma_f32 v[10:11], v[10:11], v[46:47], v[42:43] op_sel:[0,1,0] op_sel_hi:[1,1,0]\n\t"
        "v_exp_f32_e32 v10, v10\n\t"
        "v_exp_f32_e32 v11, v11\n\t"
        "v_pk_add_f32 v[42:43], v[0:1], v[2:3]\n\t"
        "v_cndmask_b32_e64 v8, v8, 0, s[40:41]\n\t"
        "v_cndmask_b32_e64 v9, v9, 0, s[40:41]\n\t"
        "v_cndmask_b32_e64 v10, v10, 0, s[40:41]\n\t"
        "buffer_store_dwordx4 v[36:39], v121, s[8:11], s28 offen sc1\n\t"
        "v_cndmask_b32_e64 v11, v11, 0, s[40:41]\n\t"
        "v_pk_add_f32 v[42:43], v[42:43], v[4:5]\n\t"
        "v_pk_add_f32 v[42:43], v[42:43], v[6:7]\n\t"
        "v_pk_add_f32 v[42:43], v[42:43], v[8:9]\n\t"
        "v_pk_add_f32 v[42:43], v[42:43], v[10:11]\n\t"
        "v_add_f32_e32 v44, v42, v43\n\t"
        "v_add_f32_e32 v42, v42, v43\n\t"
        "s_lshl_b32 s34, s24, 18\n\t"
        "s_add_u32 s34, s34, 63102976\n\t"
        "v_permlane16_swap_b32_e32 v42, v44\n\t"
        "v_add_f32_e32 v45, v42, v44\n\t"
        "v_add_f32_e32 v42, v42, v44\n\t"
        "s_add_u32 s35, s24, 1\n\t"
        "v_mov_b32_e32 v126, s35\n\t"
        "v_permlane32_swap_b32_e32 v42, v45\n\t"
        "v_add_f32_e32 v42, v42, v45\n\t"
        "v_rcp_f32_e32 v42, v42\n\t"
        "s_waitcnt vmcnt(8) lgkmcnt(0)\n\t"
        "v_max3_u32 v45, v12, v13, v14\n\t"
        "v_max3_u32 v45, v45, v15, v104\n\t"
        "v_max3_u32 v45, v45, v105, v106\n\t"
        "v_max_u32_e32 v45, v45, v107\n\t"
        "v_cmp_eq_u32_e32 vcc, -1, v45\n\t"
        "s_nop 0\n\t"
        "s_cbranch_vccnz LA_v2fail%=\n\t"
        "LA_v2ok%=:\n\t"
        "v_and_b32_e32 v45, 0xff, v109\n\t"
        "v_cmp_ne_u32_e32 vcc, s24, v45\n\t"
        "s_nop 0\n\t"
        "s_cbranch_vccnz LA_stre%=\n\t"
        "LA_stok%=:\n\t"
        "v_pk_mul_f32 v[0:1], v[0:1], v[42:43] op_sel_hi:[1,0]\n\t"
        "v_subrev_u32_e32 v108, s25, v108\n\t"
        "v_min_u32_dpp v111, v111, v111 quad_perm:[1,0,3,2] row_mask:0xf bank_mask:0xf\n\t"
        "v_cvt_pk_bf16_f32 v32, v0, v1\n\t"
        "v_pk_mul_f32 v[2:3], v[2:3], v[42:43] op_sel_hi:[1,0]\n\t"
        "v_cvt_pk_bf16_f32 v33, v2, v3\n\t"
        "v_max_i32_dpp v108, v108, v108 quad_perm:[1,0,3,2] row_mask:0xf bank_mask:0xf\n\t"
        "v_min_u32_dpp v111, v111, v111 quad_perm:[2,3,0,1] row_mask:0xf bank_mask:0xf\n\t"
        "v_pk_mul_f32 v[4:5], v[4:5], v[42:43] op_sel_hi:[1,0]\n\t"
        "v_cvt_pk_bf16_f32 v34, v4, v5\n\t"
        "v_pk_mul_f32 v[6:7], v[6:7], v[42:43] op_sel_hi:[1,0]\n\t"
        "v_max_i32_dpp v108, v108, v108 quad_perm:[2,3,0,1] row_mask:0xf bank_mask:0xf\n\t"
        "v_min_u32_dpp v111, v111, v111 row_ror:4 row_mask:0xf bank_mask:0xf\n\t"
        "v_cvt_pk_bf16_f32 v35, v6, v7\n\t"
        "v_pk_mul_f32 v[8:9], v[8:9], v[42:43] op_sel_hi:[1,0]\n\t"
        "v_cvt_pk_bf16_f32 v36, v8, v9\n\t"
        "v_max_i32_dpp v108, v108, v108 row_ror:4 row_mask:0xf bank_mask:0xf\n\t"
        "v_min_u32_dpp v111, v111, v111 row_ror:8 row_mask:0xf bank_mask:0xf\n\t"
        "v_pk_mul_f32 v[10:11], v[10:11], v[42:43] op_sel_hi:[1,0]\n\t"
        "v_cvt_pk_bf16_f32 v37, v10, v11\n\t"
        "v_mfma_f32_16x16x32_bf16 v[16:19], v[48:51], v[32:35], 0\n\t"
        "v_max_i32_dpp v108, v108, v108 row_ror:8 row_mask:0xf bank_mask:0xf\n\t"
        "v_mfma_f32_16x16x32_bf16 v[20:23], v[52:55], v[32:35], 0\n\t"
        "v_mfma_f32_16x16x32_bf16 v[24:27], v[56:59], v[32:35], 0\n\t"
        "v_mfma_f32_16x16x32_bf16 v[28:31], v[60:63], v[32:35], 0\n\t"
        "v_max_i32_dpp v108, v108, v108 row_bcast:15 row_mask:0xa bank_mask:0xf\n\t"
        "v_xor_b32_e32 v45, 0x400, v123\n\t"
        "v_add_u32_e32 v124, s56, v124\n\t"
        "v_mfma_f32_16x16x16_bf16 v[16:19], v[104:105], v[36:37], v[16:19]\n\t"
        "v_max_i32_dpp v108, v108, v108 row_bcast:31 row_mask:0xc bank_mask:0xf\n\t"
        "v_mfma_f32_16x16x16_bf16 v[20:23], v[106:107], v[36:37], v[20:23]\n\t"
        "v_mfma_f32_16x16x16_bf16 v[24:27], v[12:13], v[36:37], v[24:27]\n\t"
        "v_mfma_f32_16x16x16_bf16 v[28:31], v[14:15], v[36:37], v[28:31]\n\t"
        "ds_read_b128 v[48:51], v117 offset:4096\n\t"
        "ds_read_b128 v[52:55], v117 offset:5120\n\t"
        "ds_read_b128 v[56:59], v117 offset:6144\n\t"
        "ds_read_b128 v[60:63], v117 offset:7168\n\t"
        "s_sub_u32 s56, 0, s56\n\t"
        "v_cvt_pk_bf16_f32 v0, v16, v17\n\t"
        "v_cvt_pk_bf16_f32 v1, v18, v19\n\t"
        "v_cvt_pk_bf16_f32 v2, v20, v21\n\t"
        "v_cvt_pk_bf16_f32 v3, v22, v23\n\t"
        "buffer_store_dwordx4 v[0:3], v123, s[12:15], s34 offen sc1\n\t"
        "v_cvt_pk_bf16_f32 v4, v24, v25\n\t"
        "v_cvt_pk_bf16_f32 v5, v26, v27\n\t"
        "v_cvt_pk_bf16_f32 v6, v28, v29\n\t"
        "v_cvt_pk_bf16_f32 v7, v30, v31\n\t"
        "buffer_store_dwordx4 v[4:7], v45, s[12:15], s34 offen sc1\n\t"
        "s_cmp_eq_u32 s24, 63\n\t"
        "s_cbranch_scc1 LA_last%=\n\t"
        "ds_read_b128 v[32:35], v117 offset:0\n\t"
        "ds_read_b128 v[36:39], v117 offset:1024\n\t"
        "ds_read_b128 v[40:43], v117 offset:2048\n\t"
        "ds_read_b128 v[44:47], v117 offset:3072\n\t"
        "v_mov_b64_e32 v[14:15], 0\n\t"
        "v_mov_b64_e32 v[12:13], 0\n\t"
        "v_mov_b64_e32 v[10:11], 0\n\t"
        "v_mov_b64_e32 v[8:9], 0\n\t"
        "v_mov_b64_e32 v[6:7], 0\n\t"
        "v_mov_b64_e32 v[4:5], 0\n\t"
        "v_mov_b64_e32 v[2:3], 0\n\t"
        "v_mov_b64_e32 v[0:1], 0\n\t"
        "v_readfirstlane_b32 s54, v111\n\t"
        "v_readfirstlane_b32 s35, v109\n\t"
        "v_readlane_b32 s36, v108, 63\n\t"
        "s_bfe_u32 s29, s35, 0xc0008\n\t"
        "s_lshr_b32 s30, s35, 20\n\t"
        "s_add_u32 s35, s31, s50\n\t"
        "s_mul_i32 s37, s50, 3\n\t"
        "s_min_u32 s35, s35, s37\n\t"
        "s_add_i32 s34, s36, s51\n\t"
        "s_cmp_gt_i32 s34, 0\n\t"
        "s_cselect_b32 s31, s35, 0\n\t"
        "s_cselect_b32 s34, s34, 0\n\t"
        "s_add_u32 s34, s34, s31\n\t"
        "s_add_u32 s37, s25, s34\n\t"
        "s_add_u32 s45, s37, s29\n\t"
        "s_add_u32 s25, s45, s30\n\t"
        "s_add_u32 s24, s24, 1\n\t"
        "s_mov_b32 s28, s26\n\t"
        "s_mov_b32 s26, s27\n\t"
        "s_add_u32 s27, s27, 0x80000\n\t"
        "s_and_b32 s27, s27, 0x1fffff\n\t"
        "s_branch LA_loop%=\n\t"
        "LA_last%=:\n\t"
        "s_waitcnt vmcnt(0)\n\t"
        "s_barrier\n\t"
        "buffer_store_dword v126, v127, s[12:15], 0 offen sc1\n\t"
        "s_endpgm\n\t"
        "LA_skip%=:\n\t"
        "buffer_store_dword v126, v127, s[12:15], 0 offen sc1\n\t"
        "ds_read_b64_tr_b16 v[32:33], v119 offset:2112\n\t"
        "ds_read_b64_tr_b16 v[34:35], v119 offset:2144\n\t"
        "ds_read_b64_tr_b16 v[36:37], v119 offset:2368\n\t"
        "ds_read_b64_tr_b16 v[38:39], v119 offset:2400\n\t"
        "ds_read_b64_tr_b16 v[40:41], v119 offset:2624\n\t"
        "ds_read_b64_tr_b16 v[42:43], v119 offset:2656\n\t"
        "ds_read_b64_tr_b16 v[44:45], v119 offset:2880\n\t"
        "ds_read_b64_tr_b16 v[46:47], v119 offset:2912\n\t"
        "ds_read_b128 v[48:51], v108 offset:16384\n\t"
        "ds_read_b128 v[52:55], v108 offset:17408\n\t"
        "s_waitcnt lgkmcnt(0)\n\t"
        "s_branch LA_x%=\n\t"
        "LA_stre%=:\n\t"
        "s_mov_b32 s42, 0\n\t"
        "LA_str2%=:\n\t"
        "buffer_load_dwordx2 v[108:109], v125, s[8:11], s26 offen sc1\n\t"
        "s_waitcnt vmcnt(0)\n\t"
        "v_and_b32_e32 v45, 0xff, v109\n\t"
        "v_cmp_ne_u32_e32 vcc, s24, v45\n\t"
        "s_nop 0\n\t"
        "s_cbranch_vccz LA_stok%=\n\t"
        "s_add_u32 s42, s42, 1\n\t"
        "s_cmp_lt_u32 s42, 0x100000\n\t"
        "s_cbranch_scc1 LA_str2%=\n\t"
        "s_movk_i32 s34, 5\n\t"
        "s_branch LA_die%=\n\t"
        "LA_v2fail%=:\n\t"
        "s_mov_b32 s42, 0\n\t"
        "LA_v2re%=:\n\t"
        "v_xor_b32_e32 v45, 0x200, v120\n\t"
        "buffer_load_dwordx4 v[12:15], v45, s[8:11], s26 offen sc1\n\t"
        "buffer_load_dwordx4 v[104:107], v120, s[8:11], s26 offen sc1\n\t"
        "s_waitcnt vmcnt(0)\n\t"
        "v_max3_u32 v45, v12, v13, v14\n\t"
        "v_max3_u32 v45, v45, v15, v104\n\t"
        "v_max3_u32 v45, v45, v105, v106\n\t"
        "v_max_u32_e32 v45, v45, v107\n\t"
        "v_cmp_eq_u32_e32 vcc, -1, v45\n\t"
        "s_nop 0\n\t"
        "s_cbranch_vccz LA_v2ok%=\n\t"
        "s_add_u32 s42, s42, 1\n\t"
        "s_cmp_lt_u32 s42, 0x100000\n\t"
        "s_cbranch_scc1 LA_v2re%=\n\t"
        "s_movk_i32 s34, 6\n\t"
        "s_branch LA_die%=\n\t"
        "LA_rdfail%=:\n\t"
        "s_or_b32 s47, s47, 1\n\t"
        "buffer_load_dwordx4 v[16:19], v112, s[8:11], s26 offen offset:0 sc1\n\t"
        "buffer_load_dwordx4 v[20:23], v112, s[8:11], s26 offen offset:1024 sc1\n\t"
        "buffer_load_dwordx4 v[24:27], v112, s[8:11], s26 offen offset:2048 sc1\n\t"
        "buffer_load_dwordx4 v[28:31], v112, s[8:11], s26 offen offset:3072 sc1\n\t"
        "s_nop 7\n\t"
        "v_mov_b64_e32 v[0:1], 0\n\t"
        "v_mov_b64_e32 v[2:3], 0\n\t"
        "v_mov_b64_e32 v[4:5], 0\n\t"
        "v_mov_b64_e32 v[6:7], 0\n\t"
        "v_mov_b64_e32 v[8:9], 0\n\t"
        "v_mov_b64_e32 v[10:11], 0\n\t"
        "v_mov_b64_e32 v[12:13], 0\n\t"
        "v_mov_b64_e32 v[14:15], 0\n\t"
        "s_branch LA_rdre%=w\n\t"
        "LA_rdre%=:\n\t"
        "buffer_load_dwordx4 v[16:19], v112, s[8:11], s26 offen offset:0 sc1\n\t"
        "buffer_load_dwordx4 v[20:23], v112, s[8:11], s26 offen offset:1024 sc1\n\t"
        "buffer_load_dwordx4 v[24:27], v112, s[8:11], s26 offen offset:2048 sc1\n\t"
        "buffer_load_dwordx4 v[28:31], v112, s[8:11], s26 offen offset:3072 sc1\n\t"
        "LA_rdre%=w:\n\t"
        "s_waitcnt vmcnt(0) lgkmcnt(0)\n\t"
        "v_max3_u32 v111, v16, v17, v18\n\t"
        "v_max3_u32 v111, v111, v19, v20\n\t"
        "v_max3_u32 v111, v111, v21, v22\n\t"
        "v_max3_u32 v111, v111, v23, v24\n\t"
        "v_max3_u32 v111, v111, v25, v26\n\t"
        "v_max3_u32 v111, v111, v27, v28\n\t"
        "v_max3_u32 v111, v111, v29, v30\n\t"
        "v_max_u32_e32 v111, v111, v31\n\t"
        "v_cmp_eq_u32_e32 vcc, -1, v111\n\t"
        "s_nop 0\n\t"
        "s_cbranch_vccz LA_go%=\n\t"
        "s_add_u32 s42, s42, 1\n\t"
        "s_cmp_lt_u32 s42, 0x100000\n\t"
        "s_cbranch_scc1 LA_rdre%=\n\t"
        "s_movk_i32 s34, 1\n\t"
        "s_branch LA_die%=\n\t"
        "LA_f2%=:\n\t"
        "s_or_b32 s47, s47, 1\n\t"
        "buffer_load_dwordx4 v[24:27], v112, s[8:11], s26 offen offset:2048 sc1\n\t"
        "s_nop 7\n\t"
        "s_branch LA_r2%=w\n\t"
        "LA_r2%=:\n\t"
        "buffer_load_dwordx4 v[24:27], v112, s[8:11], s26 offen offset:2048 sc1\n\t"
        "LA_r2%=w:\n\t"
        "s_waitcnt vmcnt(0) lgkmcnt(0)\n\t"
        "v_max3_u32 v111, v24, v25, v26\n\t"
        "v_max_u32_e32 v111, v111, v27\n\t"
        "v_cmp_eq_u32_e32 vcc, -1, v111\n\t"
        "s_nop 0\n\t"
        "s_cbranch_vccz LA_go%=\n\t"
        "s_add_u32 s42, s42, 1\n\t"
        "s_cmp_lt_u32 s42, 0x100000\n\t"
        "s_cbranch_scc1 LA_r2%=\n\t"
        "s_movk_i32 s34, 1\n\t"
        "s_branch LA_die%=\n\t"
        "LA_f3%=:\n\t"
        "s_or_b32 s47, s47, 1\n\t"
        "buffer_load_dwordx4 v[28:31], v112, s[8:11], s26 offen offset:3072 sc1\n\t"
        "s_nop 7\n\t"
        "s_branch LA_r3%=w\n\t"
        "LA_r3%=:\n\t"
        "buffer_load_dwordx4 v[28:31], v112, s[8:11], s26 offen offset:3072 sc1\n\t"
        "LA_r3%=w:\n\t"
        "s_waitcnt vmcnt(0) lgkmcnt(0)\n\t"
        "v_max3_u32 v111, v28, v29, v30\n\t"
        "v_max_u32_e32 v111, v111, v31\n\t"
        "v_cmp_eq_u32_e32 vcc, -1, v111\n\t"
        "s_nop 0\n\t"
        "s_cbranch_vccz LA_go%=\n\t"
        "s_add_u32 s42, s42, 1\n\t"
        "s_cmp_lt_u32 s42, 0x100000\n\t"
        "s_cbranch_scc1 LA_r3%=\n\t"
        "s_movk_i32 s34, 1\n\t"
        "s_branch LA_die%=\n\t"
        "LA_vmfail%=:\n\t"
        "s_or_b32 s47, s47, 2\n\t"
        "s_mov_b32 s42, 0\n\t"
        "LA_vmre%=:\n\t"
        "buffer_load_dwordx4 v[104:107], v120, s[8:11], s26 offen sc1\n\t"
        "v_mfma_f32_16x16x32_bf16 v[4:7], v[48:51], v[12:15], 0\n\t"
        "v_mfma_f32_16x16x32_bf16 v[8:11], v[52:55], v[12:15], 0\n\t"
        "s_nop 7\n\t"
        "s_waitcnt vmcnt(0)\n\t"
        "v_mfma_f32_16x16x16_bf16 v[4:7], v[104:105], v[56:57], v[4:7]\n\t"
        "v_mfma_f32_16x16x16_bf16 v[8:11], v[106:107], v[56:57], v[8:11]\n\t"
        "v_max3_u32 v111, v104, v105, v106\n\t"
        "v_max_u32_e32 v111, v111, v107\n\t"
        "v_cmp_eq_u32_e32 vcc, -1, v111\n\t"
        "s_nop 7\n\t"
        "s_cbranch_vccz LA_vmok%=\n\t"
        "s_add_u32 s42, s42, 1\n\t"
        "s_cmp_lt_u32 s42, 0x100000\n\t"
        "s_cbranch_scc1 LA_vmre%=\n\t"
        "s_movk_i32 s34, 2\n\t"
        "s_branch LA_die%=\n\t"
        "LA_die%=:\n\t"
        "v_mov_b32_e32 v30, s34\n\t"
        "s_mov_b32 s35, 33024\n\t"
        "s_mov_b64 exec, 1\n\t"
        "buffer_store_dword v30, off, s[12:15], s35 sc1\n\t"
        "s_endpgm\n\t"
        ".Lk_end_A:\n\t"
        :: REC_S_IN, "{v96}"(g_w), "{v97}"(wb0), "{v98}"(wb1), "{v99}"(rb0), "{v100}"(rb0 ^ 32u), "{v101}"(rb0 ^ 64u), "{v102}"(rb0 ^ 96u),
           "{v112}"(a_mem), "{v113}"(l_ms + w * 4096), "{v114}"(l_ms + ((w + 1) & 3) * 4096), "{v115}"(l_ms + ((w + 2) & 3) * 4096), "{v116}"(l_ms + ((w + 3) & 3) * 4096),
           "{v117}"(lb + w * 22528 + l * 16), "{v118}"(l_iw), "{v119}"(l_iq), "{v120}"(a_vm), "{v121}"(a_o), "{v122}"(h * 8192 + l * 16), "{v123}"(a_ox), "{v124}"(lb + L_KV + l * 16), "{s55}"(lb + L_KV + w * 2048),
           "{v125}"((24 + cp) * 16384 + l * ST_P), "{v126}"(16 * (w & 1) + n - 4 * g), "{v103}"(pp ? 0xEEEEEEEEu : 0x44444444u),
           "{v127}"((l == 0 && w == 0) ? F_PROG + h * 128 : OOB)
        : "memory", "vcc", "scc", "v0", "v255", "a0", "a255", "s101");
    __builtin_unreachable();
}
__device__ __forceinline__ void rec_b(const Params& p, unsigned lb, const int wv, const int id, const int rg, const int cp, const int rot, const int bq) {
    const int w = wv, l = otid() & 63, n = l & 15, g = l >> 4, cs = id, rb = 32 + id * 2 + rg;
    const v4u sh = mk_srd(p.ws + hx_base(p.ws), HX_BYTES - 8192), sw = mk_srd(p.ws, W_TOTAL), s1 = mk_srd(p.Wv, 1024 * 4096 * 2), s2 = mk_srd(p.Wo, 1024 * 4096 * 2);
    const int r8 = l >> 3, pc = l & 7, pp = n >> 3, bb = (n >> 2) & 1, f = (g & 1) | (pp << 1);
    const unsigned stg = lb + LB_STG + w * LB_STGP;
    const unsigned g_v = r8 * 8192 + cs * 256 + rg * 128 + pc * 16, g_o = (l >> 2) * 2048 + cs * 64 + (l & 3) * 16;
    const unsigned wb0 = stg + r8 * 128 + (((pc >> 1) ^ (r8 >> 2)) << 5) + (pc & 1) * 16, wb1 = wb0 ^ 64u;
    const unsigned rb0 = stg + (16 * (g >> 1) + 8 * pp + 4 * (g & 1) + bb) * 128 + (f << 5) + (n & 3) * 8;
    const unsigned o_r = stg + pp * (2048 + 128) + (g & 1) * (1024 + 32) + (bb + 4 * (g >> 1)) * 64 + (n & 3) * 8;
    const unsigned a_mem = cp * 32768 + (w >> 1) * 16384 + (w & 1) * 4096 + (g >> 1) * 512 + ((n & 7) >> 2) * 256 + ((g & 1) * 2 + pp) * 64 + (n & 3) * 16;
    const unsigned a_vmw = w < 2 ? (16 + (cs >> 2)) * 16384 + (cs & 3) * 2048 + (2 * rg + w) * 512 + n * 32 + (g & 1) * 16 + (g >> 1) * 8 : OOB;
    const unsigned a_or = (8 + rg * 4 + w) * 16384 + (n >> 2) * 256 + g * 64 + (n & 3) * 16;
    const unsigned a_mw = (RPL != 0 && RPL != 4 && w == 3) ? OOB : w * 32768 + (cs >> 4) * 16384 + ((cs >> 1) & 7) * 1024 + (cs & 1) * 512 + rg * 256 + l * 4;
    const unsigned a_sw = (w == 0 && l < 4) ? (24 + l) * 16384 + (id * 2 + rg) * ST_P : OOB;
    const bf16_t* m0p = p.mem0 + (4 * rg + ((l >> 2) & 3)) * 1024 + cs * 32 + g * 8 + (l & 3) * 2;
    asm volatile(
        "global_load_dword v109, v[108:109], off\n\t"
        "s_add_u32 s35, s68, 0\n\t"
        "s_and_b32 s35, s35, 7\n\t"
        "s_lshl_b32 s69, s35, 10\n\t"
        "s_add_u32 s35, s68, 2\n\t"
        "s_and_b32 s35, s35, 7\n\t"
        "s_lshl_b32 s70, s35, 10\n\t"
        "s_add_u32 s35, s68, 4\n\t"
        "s_and_b32 s35, s35, 7\n\t"
        "s_lshl_b32 s71, s35, 10\n\t"
        "s_add_u32 s35, s68, 6\n\t"
        "s_and_b32 s35, s35, 7\n\t"
        "s_lshl_b32 s72, s35, 10\n\t"
        "s_lshl_b32 s34, s43, 21\n\t"
        "s_add_u32 s34, s34, 0\n\t"
        "buffer_load_dwordx4 v[128:131], v96, s[16:19], s34 offen nt\n\t"
        "s_add_u32 s34, s34, 0x10000\n\t"
        "buffer_load_dwordx4 v[132:135], v96, s[16:19], s34 offen nt\n\t"
        "s_add_u32 s34, s34, 0x10000\n\t"
        "buffer_load_dwordx4 v[136:139], v96, s[16:19], s34 offen nt\n\t"
        "s_add_u32 s34, s34, 0x10000\n\t"
        "buffer_load_dwordx4 v[140:143], v96, s[16:19], s34 offen nt\n\t"
        "s_add_u32 s34, s34, 0x10000\n\t"
        "buffer_load_dwordx4 v[144:147], v96, s[16:19], s34 offen nt\n\t"
        "s_add_u32 s34, s34, 0x10000\n\t"
        "buffer_load_dwordx4 v[148:151], v96, s[16:19], s34 offen nt\n\t"
        "s_add_u32 s34, s34, 0x10000\n\t"
        "buffer_load_dwordx4 v[152:155], v96, s[16:19], s34 offen nt\n\t"
        "s_add_u32 s34, s34, 0x10000\n\t"
        "buffer_load_dwordx4 v[156:159], v96, s[16:19], s34 offen nt\n\t"
        "s_lshl_b32 s34, s43, 21\n\t"
        "s_add_u32 s34, s34, 524288\n\t"
        "buffer_load_dwordx4 v[160:163], v96, s[16:19], s34 offen nt\n\t"
        "s_add_u32 s34, s34, 0x10000\n\t"
        "buffer_load_dwordx4 v[164:167], v96, s[16:19], s34 offen nt\n\t"
        "s_add_u32 s34, s34, 0x10000\n\t"
        "buffer_load_dwordx4 v[168:171], v96, s[16:19], s34 offen nt\n\t"
        "s_add_u32 s34, s34, 0x10000\n\t"
        "buffer_load_dwordx4 v[172:175], v96, s[16:19], s34 offen nt\n\t"
        "s_add_u32 s34, s34, 0x10000\n\t"
        "buffer_load_dwordx4 v[176:179], v96, s[16:19], s34 offen nt\n\t"
        "s_add_u32 s34, s34, 0x10000\n\t"
        "buffer_load_dwordx4 v[180:183], v96, s[16:19], s34 offen nt\n\t"
        "s_add_u32 s34, s34, 0x10000\n\t"
        "buffer_load_dwordx4 v[184:187], v96, s[16:19], s34 offen nt\n\t"
        "s_add_u32 s34, s34, 0x10000\n\t"
        "buffer_load_dwordx4 v[188:191], v96, s[16:19], s34 offen nt\n\t"
        "s_lshl_b32 s34, s43, 21\n\t"
        "s_add_u32 s34, s34, 1048576\n\t"
        "buffer_load_dwordx4 v[192:195], v96, s[16:19], s34 offen nt\n\t"
        "s_add_u32 s34, s34, 0x10000\n\t"
        "buffer_load_dwordx4 v[196:199], v96, s[16:19], s34 offen nt\n\t"
        "s_add_u32 s34, s34, 0x10000\n\t"
        "buffer_load_dwordx4 v[200:203], v96, s[16:19], s34 offen nt\n\t"
        "s_add_u32 s34, s34, 0x10000\n\t"
        "buffer_load_dwordx4 v[204:207], v96, s[16:19], s34 offen nt\n\t"
        "s_add_u32 s34, s34, 0x10000\n\t"
        "buffer_load_dwordx4 v[208:211], v96, s[16:19], s34 offen nt\n\t"
        "s_add_u32 s34, s34, 0x10000\n\t"
        "buffer_load_dwordx4 v[212:215], v96, s[16:19], s34 offen nt\n\t"
        "s_add_u32 s34, s34, 0x10000\n\t"
        "buffer_load_dwordx4 v[216:219], v96, s[16:19], s34 offen nt\n\t"
        "s_add_u32 s34, s34, 0x10000\n\t"
        "buffer_load_dwordx4 v[220:223], v96, s[16:19], s34 offen nt\n\t"
        "s_waitcnt vmcnt(16)\n\t"
        "ds_write_b128 v98, v[128:131] offset:0\n\t"
        "ds_write_b128 v99, v[132:135] offset:1024\n\t"
        "ds_write_b128 v98, v[136:139] offset:2048\n\t"
        "ds_write_b128 v99, v[140:143] offset:3072\n\t"
        "ds_write_b128 v98, v[144:147] offset:4096\n\t"
        "ds_write_b128 v99, v[148:151] offset:5120\n\t"
        "ds_write_b128 v98, v[152:155] offset:6144\n\t"
        "ds_write_b128 v99, v[156:159] offset:7168\n\t"
        "ds_read_b64_tr_b16 v[128:129], v100 offset:0\n\t"
        "ds_read_b64_tr_b16 v[130:131], v100 offset:256\n\t"
        "ds_read_b64_tr_b16 v[132:133], v100 offset:4096\n\t"
        "ds_read_b64_tr_b16 v[134:135], v100 offset:4352\n\t"
        "s_waitcnt lgkmcnt(8)\n\t"
        "ds_read_b64_tr_b16 v[136:137], v101 offset:0\n\t"
        "ds_read_b64_tr_b16 v[138:139], v101 offset:256\n\t"
        "ds_read_b64_tr_b16 v[140:141], v101 offset:4096\n\t"
        "ds_read_b64_tr_b16 v[142:143], v101 offset:4352\n\t"
        "s_waitcnt lgkmcnt(8)\n\t"
        "ds_read_b64_tr_b16 v[144:145], v102 offset:0\n\t"
        "ds_read_b64_tr_b16 v[146:147], v102 offset:256\n\t"
        "ds_read_b64_tr_b16 v[148:149], v102 offset:4096\n\t"
        "ds_read_b64_tr_b16 v[150:151], v102 offset:4352\n\t"
        "s_waitcnt lgkmcnt(8)\n\t"
        "ds_read_b64_tr_b16 v[152:153], v103 offset:0\n\t"
        "ds_read_b64_tr_b16 v[154:155], v103 offset:256\n\t"
        "ds_read_b64_tr_b16 v[156:157], v103 offset:4096\n\t"
        "ds_read_b64_tr_b16 v[158:159], v103 offset:4352\n\t"
        "s_lshl_b32 s34, s43, 21\n\t"
        "s_add_u32 s34, s34, 1572864\n\t"
        "buffer_load_dwordx4 v[224:227], v96, s[16:19], s34 offen nt\n\t"
        "s_add_u32 s34, s34, 0x10000\n\t"
        "buffer_load_dwordx4 v[228:231], v96, s[16:19], s34 offen nt\n\t"
        "s_add_u32 s34, s34, 0x10000\n\t"
        "buffer_load_dwordx4 v[232:235], v96, s[16:19], s34 offen nt\n\t"
        "s_add_u32 s34, s34, 0x10000\n\t"
        "buffer_load_dwordx4 v[236:239], v96, s[16:19], s34 offen nt\n\t"
        "s_add_u32 s34, s34, 0x10000\n\t"
        "buffer_load_dwordx4 v[240:243], v96, s[16:19], s34 offen nt\n\t"
        "s_add_u32 s34, s34, 0x10000\n\t"
        "buffer_load_dwordx4 v[244:247], v96, s[16:19], s34 offen nt\n\t"
        "s_add_u32 s34, s34, 0x10000\n\t"
        "buffer_load_dwordx4 v[248:251], v96, s[16:19], s34 offen nt\n\t"
        "s_add_u32 s34, s34, 0x10000\n\t"
        "buffer_load_dwordx4 v[252:255], v96, s[16:19], s34 offen nt\n\t"
        "s_waitcnt lgkmcnt(0)\n\t"
        "s_waitcnt vmcnt(16)\n\t"
        "ds_write_b128 v98, v[160:163] offset:0\n\t"
        "ds_write_b128 v99, v[164:167] offset:1024\n\t"
        "ds_write_b128 v98, v[168:171] offset:2048\n\t"
        "ds_write_b128 v99, v[172:175] offset:3072\n\t"
        "ds_write_b128 v98, v[176:179] offset:4096\n\t"
        "ds_write_b128 v99, v[180:183] offset:5120\n\t"
        "ds_write_b128 v98, v[184:187] offset:6144\n\t"
        "ds_write_b128 v99, v[188:191] offset:7168\n\t"
        "ds_read_b64_tr_b16 v[160:161], v100 offset:0\n\t"
        "ds_read_b64_tr_b16 v[162:163], v100 offset:256\n\t"
        "ds_read_b64_tr_b16 v[164:165], v100 offset:4096\n\t"
        "ds_read_b64_tr_b16 v[166:167], v100 offset:4352\n\t"
        "s_waitcnt lgkmcnt(8)\n\t"
        "ds_read_b64_tr_b16 v[168:169], v101 offset:0\n\t"
        "ds_read_b64_tr_b16 v[170:171], v101 offset:256\n\t"
        "ds_read_b64_tr_b16 v[172:173], v101 offset:4096\n\t"
        "ds_read_b64_tr_b16 v[174:175], v101 offset:4352\n\t"
        "s_waitcnt lgkmcnt(8)\n\t"
        "ds_read_b64_tr_b16 v[176:177], v102 offset:0\n\t"
        "ds_read_b64_tr_b16 v[178:179], v102 offset:256\n\t"
        "ds_read_b64_tr_b16 v[180:181], v102 offset:4096\n\t"
        "ds_read_b64_tr_b16 v[182:183], v102 offset:4352\n\t"
        "s_waitcnt lgkmcnt(8)\n\t"
        "ds_read_b64_tr_b16 v[184:185], v103 offset:0\n\t"
        "ds_read_b64_tr_b16 v[186:187], v103 offset:256\n\t"
        "ds_read_b64_tr_b16 v[188:189], v103 offset:4096\n\t"
        "ds_read_b64_tr_b16 v[190:191], v103 offset:4352\n\t"
        "s_add_u32 s35, s68, 0\n\t"
        "s_and_b32 s35, s35, 7\n\t"
        "s_lshl_b32 s35, s35, 18\n\t"
        "s_lshl_b32 s34, s43, 21\n\t"
        "s_add_u32 s34, s34, s35\n\t"
        "buffer_load_dwordx4 a[0:3], v97, s[20:23], s34 offen\n\t"
        "s_add_u32 s34, s34, 0x8000\n\t"
        "buffer_load_dwordx4 a[4:7], v97, s[20:23], s34 offen\n\t"
        "s_add_u32 s34, s34, 0x8000\n\t"
        "buffer_load_dwordx4 a[8:11], v97, s[20:23], s34 offen\n\t"
        "s_add_u32 s34, s34, 0x8000\n\t"
        "buffer_load_dwordx4 a[12:15], v97, s[20:23], s34 offen\n\t"
        "s_add_u32 s34, s34, 0x8000\n\t"
        "buffer_load_dwordx4 a[16:19], v97, s[20:23], s34 offen\n\t"
        "s_add_u32 s34, s34, 0x8000\n\t"
        "buffer_load_dwordx4 a[20:23], v97, s[20:23], s34 offen\n\t"
        "s_add_u32 s34, s34, 0x8000\n\t"
        "buffer_load_dwordx4 a[24:27], v97, s[20:23], s34 offen\n\t"
        "s_add_u32 s34, s34, 0x8000\n\t"
        "buffer_load_dwordx4 a[28:31], v97, s[20:23], s34 offen\n\t"
        "s_waitcnt lgkmcnt(0)\n\t"
        "s_waitcnt vmcnt(16)\n\t"
        "ds_write_b128 v98, v[192:195] offset:0\n\t"
        "ds_write_b128 v99, v[196:199] offset:1024\n\t"
        "ds_write_b128 v98, v[200:203] offset:2048\n\t"
        "ds_write_b128 v99, v[204:207] offset:3072\n\t"
        "ds_write_b128 v98, v[208:211] offset:4096\n\t"
        "ds_write_b128 v99, v[212:215] offset:5120\n\t"
        "ds_write_b128 v98, v[216:219] offset:6144\n\t"
        "ds_write_b128 v99, v[220:223] offset:7168\n\t"
        "ds_read_b64_tr_b16 v[192:193], v100 offset:0\n\t"
        "ds_read_b64_tr_b16 v[194:195], v100 offset:256\n\t"
        "ds_read_b64_tr_b16 v[196:197], v100 offset:4096\n\t"
        "ds_read_b64_tr_b16 v[198:199], v100 offset:4352\n\t"
        "s_waitcnt lgkmcnt(8)\n\t"
        "ds_read_b64_tr_b16 v[200:201], v101 offset:0\n\t"
        "ds_read_b64_tr_b16 v[202:203], v101 offset:256\n\t"
        "ds_read_b64_tr_b16 v[204:205], v101 offset:4096\n\t"
        "ds_read_b64_tr_b16 v[206:207], v101 offset:4352\n\t"
        "s_waitcnt lgkmcnt(8)\n\t"
        "ds_read_b64_tr_b16 v[208:209], v102 offset:0\n\t"
        "ds_read_b64_tr_b16 v[210:211], v102 offset:256\n\t"
        "ds_read_b64_tr_b16 v[212:213], v102 offset:4096\n\t"
        "ds_read_b64_tr_b16 v[214:215], v102 offset:4352\n\t"
        "s_waitcnt lgkmcnt(8)\n\t"
        "ds_read_b64_tr_b16 v[216:217], v103 offset:0\n\t"
        "ds_read_b64_tr_b16 v[218:219], v103 offset:256\n\t"
        "ds_read_b64_tr_b16 v[220:221], v103 offset:4096\n\t"
        "ds_read_b64_tr_b16 v[222:223], v103 offset:4352\n\t"
        "s_add_u32 s35, s68, 1\n\t"
        "s_and_b32 s35, s35, 7\n\t"
        "s_lshl_b32 s35, s35, 18\n\t"
        "s_lshl_b32 s34, s43, 21\n\t"
        "s_add_u32 s34, s34, s35\n\t"
        "buffer_load_dwordx4 a[32:35], v97, s[20:23], s34 offen\n\t"
        "s_add_u32 s34, s34, 0x8000\n\t"
        "buffer_load_dwordx4 a[36:39], v97, s[20:23], s34 offen\n\t"
        "s_add_u32 s34, s34, 0x8000\n\t"
        "buffer_load_dwordx4 a[40:43], v97, s[20:23], s34 offen\n\t"
        "s_add_u32 s34, s34, 0x8000\n\t"
        "buffer_load_dwordx4 a[44:47], v97, s[20:23], s34 offen\n\t"
        "s_add_u32 s34, s34, 0x8000\n\t"
        "buffer_load_dwordx4 a[48:51], v97, s[20:23], s34 offen\n\t"
        "s_add_u32 s34, s34, 0x8000\n\t"
        "buffer_load_dwordx4 a[52:55], v97, s[20:23], s34 offen\n\t"
        "s_add_u32 s34, s34, 0x8000\n\t"
        "buffer_load_dwordx4 a[56:59], v97, s[20:23], s34 offen\n\t"
        "s_add_u32 s34, s34, 0x8000\n\t"
        "buffer_load_dwordx4 a[60:63], v97, s[20:23], s34 offen\n\t"
        "s_waitcnt lgkmcnt(0)\n\t"
        "s_waitcnt vmcnt(16)\n\t"
        "ds_write_b128 v98, v[224:227] offset:0\n\t"
        "ds_write_b128 v99, v[228:231] offset:1024\n\t"
        "ds_write_b128 v98, v[232:235] offset:2048\n\t"
        "ds_write_b128 v99, v[236:239] offset:3072\n\t"
        "ds_write_b128 v98, v[240:243] offset:4096\n\t"
        "ds_write_b128 v99, v[244:247] offset:5120\n\t"
        "ds_write_b128 v98, v[248:251] offset:6144\n\t"
        "ds_write_b128 v99, v[252:255] offset:7168\n\t"
        "ds_read_b64_tr_b16 v[224:225], v100 offset:0\n\t"
        "ds_read_b64_tr_b16 v[226:227], v100 offset:256\n\t"
        "ds_read_b64_tr_b16 v[228:229], v100 offset:4096\n\t"
        "ds_read_b64_tr_b16 v[230:231], v100 offset:4352\n\t"
        "s_waitcnt lgkmcnt(8)\n\t"
        "ds_read_b64_tr_b16 v[232:233], v101 offset:0\n\t"
        "ds_read_b64_tr_b16 v[234:235], v101 offset:256\n\t"
        "ds_read_b64_tr_b16 v[236:237], v101 offset:4096\n\t"
        "ds_read_b64_tr_b16 v[238:239], v101 offset:4352\n\t"
        "s_waitcnt lgkmcnt(8)\n\t"
        "ds_read_b64_tr_b16 v[240:241], v102 offset:0\n\t"
        "ds_read_b64_tr_b16 v[242:243], v102 offset:256\n\t"
        "ds_read_b64_tr_b16 v[244:245], v102 offset:4096\n\t"
        "ds_read_b64_tr_b16 v[246:247], v102 offset:4352\n\t"
        "s_waitcnt lgkmcnt(8)\n\t"
        "ds_read_b64_tr_b16 v[248:249], v103 offset:0\n\t"
        "ds_read_b64_tr_b16 v[250:251], v103 offset:256\n\t"
        "ds_read_b64_tr_b16 v[252:253], v103 offset:4096\n\t"
        "ds_read_b64_tr_b16 v[254:255], v103 offset:4352\n\t"
        "s_add_u32 s35, s68, 2\n\t"
        "s_and_b32 s35, s35, 7\n\t"
        "s_lshl_b32 s35, s35, 18\n\t"
        "s_lshl_b32 s34, s43, 21\n\t"
        "s_add_u32 s34, s34, s35\n\t"
        "buffer_load_dwordx4 a[64:67], v97, s[20:23], s34 offen\n\t"
        "s_add_u32 s34, s34, 0x8000\n\t"
        "buffer_load_dwordx4 a[68:71], v97, s[20:23], s34 offen\n\t"
        "s_add_u32 s34, s34, 0x8000\n\t"
        "buffer_load_dwordx4 a[72:75], v97, s[20:23], s34 offen\n\t"
        "s_add_u32 s34, s34, 0x8000\n\t"
        "buffer_load_dwordx4 a[76:79], v97, s[20:23], s34 offen\n\t"
        "s_add_u32 s34, s34, 0x8000\n\t"
        "buffer_load_dwordx4 a[80:83], v97, s[20:23], s34 offen\n\t"
        "s_add_u32 s34, s34, 0x8000\n\t"
        "buffer_load_dwordx4 a[84:87], v97, s[20:23], s34 offen\n\t"
        "s_add_u32 s34, s34, 0x8000\n\t"
        "buffer_load_dwordx4 a[88:91], v97, s[20:23], s34 offen\n\t"
        "s_add_u32 s34, s34, 0x8000\n\t"
        "buffer_load_dwordx4 a[92:95], v97, s[20:23], s34 offen\n\t"
        "s_waitcnt lgkmcnt(0)\n\t"
        "s_waitcnt vmcnt(16)\n\t"
        "ds_write_b128 v122, a[0:3] offset:0\n\t"
        "ds_write_b128 v122, a[4:7] offset:1056\n\t"
        "ds_write_b128 v122, a[8:11] offset:2176\n\t"
        "ds_write_b128 v122, a[12:15] offset:3232\n\t"
        "ds_write_b128 v122, a[16:19] offset:4352\n\t"
        "ds_write_b128 v122, a[20:23] offset:5408\n\t"
        "ds_write_b128 v122, a[24:27] offset:6528\n\t"
        "ds_write_b128 v122, a[28:31] offset:7584\n\t"
        "ds_read_b64_tr_b16 a[0:1], v123 offset:0\n\t"
        "ds_read_b64_tr_b16 a[2:3], v123 offset:128\n\t"
        "ds_read_b64_tr_b16 a[4:5], v123 offset:512\n\t"
        "ds_read_b64_tr_b16 a[6:7], v123 offset:640\n\t"
        "s_waitcnt lgkmcnt(8)\n\t"
        "ds_read_b64_tr_b16 a[8:9], v123 offset:32\n\t"
        "ds_read_b64_tr_b16 a[10:11], v123 offset:160\n\t"
        "ds_read_b64_tr_b16 a[12:13], v123 offset:544\n\t"
        "ds_read_b64_tr_b16 a[14:15], v123 offset:672\n\t"
        "s_waitcnt lgkmcnt(8)\n\t"
        "ds_read_b64_tr_b16 a[16:17], v123 offset:4352\n\t"
        "ds_read_b64_tr_b16 a[18:19], v123 offset:4480\n\t"
        "ds_read_b64_tr_b16 a[20:21], v123 offset:4864\n\t"
        "ds_read_b64_tr_b16 a[22:23], v123 offset:4992\n\t"
        "s_waitcnt lgkmcnt(8)\n\t"
        "ds_read_b64_tr_b16 a[24:25], v123 offset:4384\n\t"
        "ds_read_b64_tr_b16 a[26:27], v123 offset:4512\n\t"
        "ds_read_b64_tr_b16 a[28:29], v123 offset:4896\n\t"
        "ds_read_b64_tr_b16 a[30:31], v123 offset:5024\n\t"
        "s_waitcnt lgkmcnt(8)\n\t"
        "s_add_u32 s35, s68, 3\n\t"
        "s_and_b32 s35, s35, 7\n\t"
        "s_lshl_b32 s35, s35, 18\n\t"
        "s_lshl_b32 s34, s43, 21\n\t"
        "s_add_u32 s34, s34, s35\n\t"
        "buffer_load_dwordx4 a[96:99], v97, s[20:23], s34 offen\n\t"
        "s_add_u32 s34, s34, 0x8000\n\t"
        "buffer_load_dwordx4 a[100:103], v97, s[20:23], s34 offen\n\t"
        "s_add_u32 s34, s34, 0x8000\n\t"
        "buffer_load_dwordx4 a[104:107], v97, s[20:23], s34 offen\n\t"
        "s_add_u32 s34, s34, 0x8000\n\t"
        "buffer_load_dwordx4 a[108:111], v97, s[20:23], s34 offen\n\t"
        "s_add_u32 s34, s34, 0x8000\n\t"
        "buffer_load_dwordx4 a[112:115], v97, s[20:23], s34 offen\n\t"
        "s_add_u32 s34, s34, 0x8000\n\t"
        "buffer_load_dwordx4 a[116:119], v97, s[20:23], s34 offen\n\t"
        "s_add_u32 s34, s34, 0x8000\n\t"
        "buffer_load_dwordx4 a[120:123], v97, s[20:23], s34 offen\n\t"
        "s_add_u32 s34, s34, 0x8000\n\t"
        "buffer_load_dwordx4 a[124:127], v97, s[20:23], s34 offen\n\t"
        "s_waitcnt lgkmcnt(0)\n\t"
        "s_waitcnt vmcnt(16)\n\t"
        "ds_write_b128 v122, a[32:35] offset:0\n\t"
        "ds_write_b128 v122, a[36:39] offset:1056\n\t"
        "ds_write_b128 v122, a[40:43] offset:2176\n\t"
        "ds_write_b128 v122, a[44:47] offset:3232\n\t"
        "ds_write_b128 v122, a[48:51] offset:4352\n\t"
        "ds_write_b128 v122, a[52:55] offset:5408\n\t"
        "ds_write_b128 v122, a[56:59] offset:6528\n\t"
        "ds_write_b128 v122, a[60:63] offset:7584\n\t"
        "ds_read_b64_tr_b16 a[32:33], v123 offset:0\n\t"
        "ds_read_b64_tr_b16 a[34:35], v123 offset:128\n\t"
        "ds_read_b64_tr_b16 a[36:37], v123 offset:512\n\t"
        "ds_read_b64_tr_b16 a[38:39], v123 offset:640\n\t"
        "s_waitcnt lgkmcnt(8)\n\t"
        "ds_read_b64_tr_b16 a[40:41], v123 offset:32\n\t"
        "ds_read_b64_tr_b16 a[42:43], v123 offset:160\n\t"
        "ds_read_b64_tr_b16 a[44:45], v123 offset:544\n\t"
        "ds_read_b64_tr_b16 a[46:47], v123 offset:672\n\t"
        "s_waitcnt lgkmcnt(8)\n\t"
        "ds_read_b64_tr_b16 a[48:49], v123 offset:4352\n\t"
        "ds_read_b64_tr_b16 a[50:51], v123 offset:4480\n\t"
        "ds_read_b64_tr_b16 a[52:53], v123 offset:4864\n\t"
        "ds_read_b64_tr_b16 a[54:55], v123 offset:4992\n\t"
        "s_waitcnt lgkmcnt(8)\n\t"
        "ds_read_b64_tr_b16 a[56:57], v123 offset:4384\n\t"
        "ds_read_b64_tr_b16 a[58:59], v123 offset:4512\n\t"
        "ds_read_b64_tr_b16 a[60:61], v123 offset:4896\n\t"
        "ds_read_b64_tr_b16 a[62:63], v123 offset:5024\n\t"
        "s_waitcnt lgkmcnt(8)\n\t"
        "s_add_u32 s35, s68, 4\n\t"
        "s_and_b32 s35, s35, 7\n\t"
        "s_lshl_b32 s35, s35, 18\n\t"
        "s_lshl_b32 s34, s43, 21\n\t"
        "s_add_u32 s34, s34, s35\n\t"
        "buffer_load_dwordx4 a[128:131], v97, s[20:23], s34 offen\n\t"
        "s_add_u32 s34, s34, 0x8000\n\t"
        "buffer_load_dwordx4 a[132:135], v97, s[20:23], s34 offen\n\t"
        "s_add_u32 s34, s34, 0x8000\n\t"
        "buffer_load_dwordx4 a[136:139], v97, s[20:23], s34 offen\n\t"
        "s_add_u32 s34, s34, 0x8000\n\t"
        "buffer_load_dwordx4 a[140:143], v97, s[20:23], s34 offen\n\t"
        "s_add_u32 s34, s34, 0x8000\n\t"
        "buffer_load_dwordx4 a[144:147], v97, s[20:23], s34 offen\n\t"
        "s_add_u32 s34, s34, 0x8000\n\t"
        "buffer_load_dwordx4 a[148:151], v97, s[20:23], s34 offen\n\t"
        "s_add_u32 s34, s34, 0x8000\n\t"
        "buffer_load_dwordx4 a[152:155], v97, s[20:23], s34 offen\n\t"
        "s_add_u32 s34, s34, 0x8000\n\t"
        "buffer_load_dwordx4 a[156:159], v97, s[20:23], s34 offen\n\t"
        "s_waitcnt lgkmcnt(0)\n\t"
        "s_waitcnt vmcnt(16)\n\t"
        "ds_write_b128 v122, a[64:67] offset:0\n\t"
        "ds_write_b128 v122, a[68:71] offset:1056\n\t"
        "ds_write_b128 v122, a[72:75] offset:2176\n\t"
        "ds_write_b128 v122, a[76:79] offset:3232\n\t"
        "ds_write_b128 v122, a[80:83] offset:4352\n\t"
        "ds_write_b128 v122, a[84:87] offset:5408\n\t"
        "ds_write_b128 v122, a[88:91] offset:6528\n\t"
        "ds_write_b128 v122, a[92:95] offset:7584\n\t"
        "ds_read_b64_tr_b16 a[64:65], v123 offset:0\n\t"
        "ds_read_b64_tr_b16 a[66:67], v123 offset:128\n\t"
        "ds_read_b64_tr_b16 a[68:69], v123 offset:512\n\t"
        "ds_read_b64_tr_b16 a[70:71], v123 offset:640\n\t"
        "s_waitcnt lgkmcnt(8)\n\t"
        "ds_read_b64_tr_b16 a[72:73], v123 offset:32\n\t"
        "ds_read_b64_tr_b16 a[74:75], v123 offset:160\n\t"
        "ds_read_b64_tr_b16 a[76:77], v123 offset:544\n\t"
        "ds_read_b64_tr_b16 a[78:79], v123 offset:672\n\t"
        "s_waitcnt lgkmcnt(8)\n\t"
        "ds_read_b64_tr_b16 a[80:81], v123 offset:4352\n\t"
        "ds_read_b64_tr_b16 a[82:83], v123 offset:4480\n\t"
        "ds_read_b64_tr_b16 a[84:85], v123 offset:4864\n\t"
        "ds_read_b64_tr_b16 a[86:87], v123 offset:4992\n\t"
        "s_waitcnt lgkmcnt(8)\n\t"
        "ds_read_b64_tr_b16 a[88:89], v123 offset:4384\n\t"
        "ds_read_b64_tr_b16 a[90:91], v123 offset:4512\n\t"
        "ds_read_b64_tr_b16 a[92:93], v123 offset:4896\n\t"
        "ds_read_b64_tr_b16 a[94:95], v123 offset:5024\n\t"
        "s_waitcnt lgkmcnt(8)\n\t"
        "s_add_u32 s35, s68, 5\n\t"
        "s_and_b32 s35, s35, 7\n\t"
        "s_lshl_b32 s35, s35, 18\n\t"
        "s_lshl_b32 s34, s43, 21\n\t"
        "s_add_u32 s34, s34, s35\n\t"
        "buffer_load_dwordx4 a[160:163], v97, s[20:23], s34 offen\n\t"
        "s_add_u32 s34, s34, 0x8000\n\t"
        "buffer_load_dwordx4 a[164:167], v97, s[20:23], s34 offen\n\t"
        "s_add_u32 s34, s34, 0x8000\n\t"
        "buffer_load_dwordx4 a[168:171], v97, s[20:23], s34 offen\n\t"
        "s_add_u32 s34, s34, 0x8000\n\t"
        "buffer_load_dwordx4 a[172:175], v97, s[20:23], s34 offen\n\t"
        "s_add_u32 s34, s34, 0x8000\n\t"
        "buffer_load_dwordx4 a[176:179], v97, s[20:23], s34 offen\n\t"
        "s_add_u32 s34, s34, 0x8000\n\t"
        "buffer_load_dwordx4 a[180:183], v97, s[20:23], s34 offen\n\t"
        "s_add_u32 s34, s34, 0x8000\n\t"
        "buffer_load_dwordx4 a[184:187], v97, s[20:23], s34 offen\n\t"
        "s_add_u32 s34, s34, 0x8000\n\t"
        "buffer_load_dwordx4 a[188:191], v97, s[20:23], s34 offen\n\t"
        "s_waitcnt lgkmcnt(0)\n\t"
        "s_waitcnt vmcnt(16)\n\t"
        "ds_write_b128 v122, a[96:99] offset:0\n\t"
        "ds_write_b128 v122, a[100:103] offset:1056\n\t"
        "ds_write_b128 v122, a[104:107] offset:2176\n\t"
        "ds_write_b128 v122, a[108:111] offset:3232\n\t"
        "ds_write_b128 v122, a[112:115] offset:4352\n\t"
        "ds_write_b128 v122, a[116:119] offset:5408\n\t"
        "ds_write_b128 v122, a[120:123] offset:6528\n\t"
        "ds_write_b128 v122, a[124:127] offset:7584\n\t"
        "ds_read_b64_tr_b16 a[96:97], v123 offset:0\n\t"
        "ds_read_b64_tr_b16 a[98:99], v123 offset:128\n\t"
        "ds_read_b64_tr_b16 a[100:101], v123 offset:512\n\t"
        "ds_read_b64_tr_b16 a[102:103], v123 offset:640\n\t"
        "s_waitcnt lgkmcnt(8)\n\t"
        "ds_read_b64_tr_b16 a[104:105], v123 offset:32\n\t"
        "ds_read_b64_tr_b16 a[106:107], v123 offset:160\n\t"
        "ds_read_b64_tr_b16 a[108:109], v123 offset:544\n\t"
        "ds_read_b64_tr_b16 a[110:111], v123 offset:672\n\t"
        "s_waitcnt lgkmcnt(8)\n\t"
        "ds_read_b64_tr_b16 a[112:113], v123 offset:4352\n\t"
        "ds_read_b64_tr_b16 a[114:115], v123 offset:4480\n\t"
        "ds_read_b64_tr_b16 a[116:117], v123 offset:4864\n\t"
        "ds_read_b64_tr_b16 a[118:119], v123 offset:4992\n\t"
        "s_waitcnt lgkmcnt(8)\n\t"
        "ds_read_b64_tr_b16 a[120:121], v123 offset:4384\n\t"
        "ds_read_b64_tr_b16 a[122:123], v123 offset:4512\n\t"
        "ds_read_b64_tr_b16 a[124:125], v123 offset:4896\n\t"
        "ds_read_b64_tr_b16 a[126:127], v123 offset:5024\n\t"
        "s_waitcnt lgkmcnt(8)\n\t"
        "s_add_u32 s35, s68, 6\n\t"
        "s_and_b32 s35, s35, 7\n\t"
        "s_lshl_b32 s35, s35, 18\n\t"
        "s_lshl_b32 s34, s43, 21\n\t"
        "s_add_u32 s34, s34, s35\n\t"
        "buffer_load_dwordx4 a[192:195], v97, s[20:23], s34 offen\n\t"
        "s_add_u32 s34, s34, 0x8000\n\t"
        "buffer_load_dwordx4 a[196:199], v97, s[20:23], s34 offen\n\t"
        "s_add_u32 s34, s34, 0x8000\n\t"
        "buffer_load_dwordx4 a[200:203], v97, s[20:23], s34 offen\n\t"
        "s_add_u32 s34, s34, 0x8000\n\t"
        "buffer_load_dwordx4 a[204:207], v97, s[20:23], s34 offen\n\t"
        "s_add_u32 s34, s34, 0x8000\n\t"
        "buffer_load_dwordx4 a[208:211], v97, s[20:23], s34 offen\n\t"
        "s_add_u32 s34, s34, 0x8000\n\t"
        "buffer_load_dwordx4 a[212:215], v97, s[20:23], s34 offen\n\t"
        "s_add_u32 s34, s34, 0x8000\n\t"
        "buffer_load_dwordx4 a[216:219], v97, s[20:23], s34 offen\n\t"
        "s_add_u32 s34, s34, 0x8000\n\t"
        "buffer_load_dwordx4 a[220:223], v97, s[20:23], s34 offen\n\t"
        "s_waitcnt lgkmcnt(0)\n\t"
        "s_waitcnt vmcnt(16)\n\t"
        "ds_write_b128 v122, a[128:131] offset:0\n\t"
        "ds_write_b128 v122, a[132:135] offset:1056\n\t"
        "ds_write_b128 v122, a[136:139] offset:2176\n\t"
        "ds_write_b128 v122, a[140:143] offset:3232\n\t"
        "ds_write_b128 v122, a[144:147] offset:4352\n\t"
        "ds_write_b128 v122, a[148:151] offset:5408\n\t"
        "ds_write_b128 v122, a[152:155] offset:6528\n\t"
        "ds_write_b128 v122, a[156:159] offset:7584\n\t"
        "ds_read_b64_tr_b16 a[128:129], v123 offset:0\n\t"
        "ds_read_b64_tr_b16 a[130:131], v123 offset:128\n\t"
        "ds_read_b64_tr_b16 a[132:133], v123 offset:512\n\t"
        "ds_read_b64_tr_b16 a[134:135], v123 offset:640\n\t"
        "s_waitcnt lgkmcnt(8)\n\t"
        "ds_read_b64_tr_b16 a[136:137], v123 offset:32\n\t"
        "ds_read_b64_tr_b16 a[138:139], v123 offset:160\n\t"
        "ds_read_b64_tr_b16 a[140:141], v123 offset:544\n\t"
        "ds_read_b64_tr_b16 a[142:143], v123 offset:672\n\t"
        "s_waitcnt lgkmcnt(8)\n\t"
        "ds_read_b64_tr_b16 a[144:145], v123 offset:4352\n\t"
        "ds_read_b64_tr_b16 a[146:147], v123 offset:4480\n\t"
        "ds_read_b64_tr_b16 a[148:149], v123 offset:4864\n\t"
        "ds_read_b64_tr_b16 a[150:151], v123 offset:4992\n\t"
        "s_waitcnt lgkmcnt(8)\n\t"
        "ds_read_b64_tr_b16 a[152:153], v123 offset:4384\n\t"
        "ds_read_b64_tr_b16 a[154:155], v123 offset:4512\n\t"
        "ds_read_b64_tr_b16 a[156:157], v123 offset:4896\n\t"
        "ds_read_b64_tr_b16 a[158:159], v123 offset:5024\n\t"
        "s_waitcnt lgkmcnt(8)\n\t"
        "s_add_u32 s35, s68, 7\n\t"
        "s_and_b32 s35, s35, 7\n\t"
        "s_lshl_b32 s35, s35, 18\n\t"
        "s_lshl_b32 s34, s43, 21\n\t"
        "s_add_u32 s34, s34, s35\n\t"
        "buffer_load_dwordx4 a[224:227], v97, s[20:23], s34 offen\n\t"
        "s_add_u32 s34, s34, 0x8000\n\t"
        "buffer_load_dwordx4 a[228:231], v97, s[20:23], s34 offen\n\t"
        "s_add_u32 s34, s34, 0x8000\n\t"
        "buffer_load_dwordx4 a[232:235], v97, s[20:23], s34 offen\n\t"
        "s_add_u32 s34, s34, 0x8000\n\t"
        "buffer_load_dwordx4 a[236:239], v97, s[20:23], s34 offen\n\t"
        "s_add_u32 s34, s34, 0x8000\n\t"
        "buffer_load_dwordx4 a[240:243], v97, s[20:23], s34 offen\n\t"
        "s_add_u32 s34, s34, 0x8000\n\t"
        "buffer_load_dwordx4 a[244:247], v97, s[20:23], s34 offen\n\t"
        "s_add_u32 s34, s34, 0x8000\n\t"
        "buffer_load_dwordx4 a[248:251], v97, s[20:23], s34 offen\n\t"
        "s_add_u32 s34, s34, 0x8000\n\t"
        "buffer_load_dwordx4 a[252:255], v97, s[20:23], s34 offen\n\t"
        "s_waitcnt lgkmcnt(0)\n\t"
        "s_waitcnt vmcnt(16)\n\t"
        "ds_write_b128 v122, a[160:163] offset:0\n\t"
        "ds_write_b128 v122, a[164:167] offset:1056\n\t"
        "ds_write_b128 v122, a[168:171] offset:2176\n\t"
        "ds_write_b128 v122, a[172:175] offset:3232\n\t"
        "ds_write_b128 v122, a[176:179] offset:4352\n\t"
        "ds_write_b128 v122, a[180:183] offset:5408\n\t"
        "ds_write_b128 v122, a[184:187] offset:6528\n\t"
        "ds_write_b128 v122, a[188:191] offset:7584\n\t"
        "ds_read_b64_tr_b16 a[160:161], v123 offset:0\n\t"
        "ds_read_b64_tr_b16 a[162:163], v123 offset:128\n\t"
        "ds_read_b64_tr_b16 a[164:165], v123 offset:512\n\t"
        "ds_read_b64_tr_b16 a[166:167], v123 offset:640\n\t"
        "s_waitcnt lgkmcnt(8)\n\t"
        "ds_read_b64_tr_b16 a[168:169], v123 offset:32\n\t"
        "ds_read_b64_tr_b16 a[170:171], v123 offset:160\n\t"
        "ds_read_b64_tr_b16 a[172:173], v123 offset:544\n\t"
        "ds_read_b64_tr_b16 a[174:175], v123 offset:672\n\t"
        "s_waitcnt lgkmcnt(8)\n\t"
        "ds_read_b64_tr_b16 a[176:177], v123 offset:4352\n\t"
        "ds_read_b64_tr_b16 a[178:179], v123 offset:4480\n\t"
        "ds_read_b64_tr_b16 a[180:181], v123 offset:4864\n\t"
        "ds_read_b64_tr_b16 a[182:183], v123 offset:4992\n\t"
        "s_waitcnt lgkmcnt(8)\n\t"
        "ds_read_b64_tr_b16 a[184:185], v123 offset:4384\n\t"
        "ds_read_b64_tr_b16 a[186:187], v123 offset:4512\n\t"
        "ds_read_b64_tr_b16 a[188:189], v123 offset:4896\n\t"
        "ds_read_b64_tr_b16 a[190:191], v123 offset:5024\n\t"
        "s_waitcnt lgkmcnt(8)\n\t"
        "s_waitcnt lgkmcnt(0)\n\t"
        "s_waitcnt vmcnt(8)\n\t"
        "ds_write_b128 v122, a[192:195] offset:0\n\t"
        "ds_write_b128 v122, a[196:199] offset:1056\n\t"
        "ds_write_b128 v122, a[200:203] offset:2176\n\t"
        "ds_write_b128 v122, a[204:207] offset:3232\n\t"
        "ds_write_b128 v122, a[208:211] offset:4352\n\t"
        "ds_write_b128 v122, a[212:215] offset:5408\n\t"
        "ds_write_b128 v122, a[216:219] offset:6528\n\t"
        "ds_write_b128 v122, a[220:223] offset:7584\n\t"
        "ds_read_b64_tr_b16 a[192:193], v123 offset:0\n\t"
        "ds_read_b64_tr_b16 a[194:195], v123 offset:128\n\t"
        "ds_read_b64_tr_b16 a[196:197], v123 offset:512\n\t"
        "ds_read_b64_tr_b16 a[198:199], v123 offset:640\n\t"
        "s_waitcnt lgkmcnt(8)\n\t"
        "ds_read_b64_tr_b16 a[200:201], v123 offset:32\n\t"
        "ds_read_b64_tr_b16 a[202:203], v123 offset:160\n\t"
        "ds_read_b64_tr_b16 a[204:205], v123 offset:544\n\t"
        "ds_read_b64_tr_b16 a[206:207], v123 offset:672\n\t"
        "s_waitcnt lgkmcnt(8)\n\t"
        "ds_read_b64_tr_b16 a[208:209], v123 offset:4352\n\t"
        "ds_read_b64_tr_b16 a[210:211], v123 offset:4480\n\t"
        "ds_read_b64_tr_b16 a[212:213], v123 offset:4864\n\t"
        "ds_read_b64_tr_b16 a[214:215], v123 offset:4992\n\t"
        "s_waitcnt lgkmcnt(8)\n\t"
        "ds_read_b64_tr_b16 a[216:217], v123 offset:4384\n\t"
        "ds_read_b64_tr_b16 a[218:219], v123 offset:4512\n\t"
        "ds_read_b64_tr_b16 a[220:221], v123 offset:4896\n\t"
        "ds_read_b64_tr_b16 a[222:223], v123 offset:5024\n\t"
        "s_waitcnt lgkmcnt(8)\n\t"
        "s_waitcnt lgkmcnt(0)\n\t"
        "s_waitcnt vmcnt(0)\n\t"
        "ds_write_b128 v122, a[224:227] offset:0\n\t"
        "ds_write_b128 v122, a[228:231] offset:1056\n\t"
        "ds_write_b128 v122, a[232:235] offset:2176\n\t"
        "ds_write_b128 v122, a[236:239] offset:3232\n\t"
        "ds_write_b128 v122, a[240:243] offset:4352\n\t"
        "ds_write_b128 v122, a[244:247] offset:5408\n\t"
        "ds_write_b128 v122, a[248:251] offset:6528\n\t"
        "ds_write_b128 v122, a[252:255] offset:7584\n\t"
        "ds_read_b64_tr_b16 a[224:225], v123 offset:0\n\t"
        "ds_read_b64_tr_b16 a[226:227], v123 offset:128\n\t"
        "ds_read_b64_tr_b16 a[228:229], v123 offset:512\n\t"
        "ds_read_b64_tr_b16 a[230:231], v123 offset:640\n\t"
        "s_waitcnt lgkmcnt(8)\n\t"
        "ds_read_b64_tr_b16 a[232:233], v123 offset:32\n\t"
        "ds_read_b64_tr_b16 a[234:235], v123 offset:160\n\t"
        "ds_read_b64_tr_b16 a[236:237], v123 offset:544\n\t"
        "ds_read_b64_tr_b16 a[238:239], v123 offset:672\n\t"
        "s_waitcnt lgkmcnt(8)\n\t"
        "ds_read_b64_tr_b16 a[240:241], v123 offset:4352\n\t"
        "ds_read_b64_tr_b16 a[242:243], v123 offset:4480\n\t"
        "ds_read_b64_tr_b16 a[244:245], v123 offset:4864\n\t"
        "ds_read_b64_tr_b16 a[246:247], v123 offset:4992\n\t"
        "s_waitcnt lgkmcnt(8)\n\t"
        "ds_read_b64_tr_b16 a[248:249], v123 offset:4384\n\t"
        "ds_read_b64_tr_b16 a[250:251], v123 offset:4512\n\t"
        "ds_read_b64_tr_b16 a[252:253], v123 offset:4896\n\t"
        "ds_read_b64_tr_b16 a[254:255], v123 offset:5024\n\t"
        "s_waitcnt lgkmcnt(8)\n\t"
        "s_mov_b32 s35, 33792\n\t"
        "buffer_load_dword v0, off, s[12:15], s35 sc1\n\t"
        "s_waitcnt lgkmcnt(0)\n\t"
        "s_waitcnt vmcnt(0)\n\t"
        "s_mov_b32 s42, 0\n\t"
        "LB_cal%=:\n\t"
        "v_readfirstlane_b32 s34, v0\n\t"
        "s_nop 0\n\t"
        "s_cmp_lg_u32 s34, 0\n\t"
        "s_cbranch_scc1 LB_calok%=\n\t"
        "s_sleep 4\n\t"
        "s_mov_b32 s35, 33792\n\t"
        "buffer_load_dword v0, off, s[12:15], s35 sc1\n\t"
        "s_waitcnt vmcnt(0)\n\t"
        "s_add_u32 s42, s42, 1\n\t"
        "s_cmp_lt_u32 s42, 0x100000\n\t"
        "s_cbranch_scc1 LB_cal%=\n\t"
        "s_movk_i32 s34, 8\n\t"
        "s_branch LB_die%=\n\t"
        "LB_calok%=:\n\t"
        "s_and_b32 s34, s34, 8192\n\t"
        "s_add_u32 s8, s8, s34\n\t"
        "s_addc_u32 s9, s9, 0\n\t"
        "v_min_u32_e32 v109, -2, v109\n\t"
        "buffer_store_dword v109, v116, s[8:11], 0 offen sc1\n\t"
        "s_memrealtime s[32:33]\n\t"
        "s_waitcnt lgkmcnt(0)\n\t"
        "v_mov_b32_e32 v110, s32\n\t"
        "s_lshl_b32 s34, s29, 8\n\t"
        "s_lshl_b32 s35, s30, 20\n\t"
        "s_or_b32 s54, s34, s35\n\t"
        "s_add_u32 s34, s54, 12589056\n\t"
        "v_mov_b32_e32 v111, s34\n\t"
        "s_barrier\n\t"
        "s_cmp_lg_u32 s43, 0\n\t"
        "s_cbranch_scc1 LB_st%=\n\t"
        "v_mov_b32_e32 v2, 1\n\t"
        "s_mov_b32 s35, 34304\n\t"
        "s_mov_b64 exec, 1\n\t"
        "buffer_atomic_add v2, off, s[12:15], s35 sc0\n\t"
        "s_mov_b64 exec, -1\n\t"
        "s_waitcnt vmcnt(0)\n\t"
        "v_readfirstlane_b32 s34, v2\n\t"
        "s_nop 0\n\t"
        "s_cmp_lg_u32 s34, 95\n\t"
        "s_cbranch_scc1 LB_st%=\n\t"
        "s_memrealtime s[32:33]\n\t"
        "v_mbcnt_lo_u32_b32 v3, -1, 0\n\t"
        "v_lshlrev_b32_e32 v3, 7, v3\n\t"
        "s_waitcnt lgkmcnt(0)\n\t"
        "s_add_u32 s34, s32, 50\n\t"
        "s_or_b32 s34, s34, 1\n\t"
        "v_mov_b32_e32 v2, s34\n\t"
        "s_mov_b32 s35, 36864\n\t"
        "s_mov_b64 exec, 0xffff\n\t"
        "buffer_store_dword v2, v3, s[12:15], s35 offen sc1\n\t"
        "s_mov_b64 exec, -1\n\t"
        "LB_st%=:\n\t"
        "s_mov_b32 s24, 0\n\t"
        "s_mov_b32 s26, 0\n\t"
        "s_mov_b32 s27, 0x80000\n\t"
        "s_mov_b32 s28, 0x180000\n\t"
        "s_mov_b32 s31, 0\n\t"
        "s_mov_b32 s47, 0\n\t"
        "s_mov_b32 s38, 0xff00ff00\n\t"
        "s_mov_b32 s39, 0xff00ff00\n\t"
        "s_mov_b32 s40, 0\n\t"
        "s_mov_b32 s41, -1\n\t"
        "v_mov_b64_e32 v[0:1], 0\n\t"
        "v_mov_b64_e32 v[2:3], 0\n\t"
        "v_mov_b64_e32 v[4:5], 0\n\t"
        "v_mov_b64_e32 v[6:7], 0\n\t"
        "v_mov_b64_e32 v[8:9], 0\n\t"
        "v_mov_b64_e32 v[10:11], 0\n\t"
        "v_mov_b64_e32 v[12:13], 0\n\t"
        "v_mov_b64_e32 v[14:15], 0\n\t"
        "s_mov_b32 s42, 0\n\t"
        "LB_gw%=:\n\t"
        "buffer_load_dword v16, off, s[12:15], s45 sc1\n\t"
        "s_waitcnt vmcnt(0)\n\t"
        "v_readfirstlane_b32 s25, v16\n\t"
        "s_nop 0\n\t"
        "s_cmp_lg_u32 s25, 0\n\t"
        "s_cbranch_scc1 LB_loop%=\n\t"
        "s_sleep 2\n\t"
        "s_add_u32 s42, s42, 1\n\t"
        "s_cmp_lt_u32 s42, 0x100000\n\t"
        "s_cbranch_scc1 LB_gw%=\n\t"
        "s_movk_i32 s34, 9\n\t"
        "s_branch LB_die%=\n\t"
        "LB_loop%=:\n\t"
        "s_mov_b32 s42, 0\n\t"
        "s_sub_u32 s35, s25, 1\n\t"
        "LB_wa%=:\n\t"
        "s_memrealtime s[32:33]\n\t"
        "s_waitcnt lgkmcnt(0)\n\t"
        "s_sub_u32 s34, s35, s32\n\t"
        "s_cmpk_lt_u32 s34, 0x2000\n\t"
        "s_cbranch_scc1 LB_wa%=\n\t"
        "LB_rd%=:\n\t"
        "buffer_load_dwordx4 v[16:19], v112, s[8:11], s26 offen offset:0 sc1\n\t"
        "s_nop 9\n\t"
        "buffer_load_dwordx4 v[20:23], v112, s[8:11], s26 offen offset:1024 sc1\n\t"
        "s_nop 9\n\t"
        "buffer_load_dwordx4 v[24:27], v112, s[8:11], s26 offen offset:2048 sc1\n\t"
        "s_nop 9\n\t"
        "buffer_load_dwordx4 v[28:31], v112, s[8:11], s26 offen offset:3072 sc1\n\t"
        "LB_go%=:\n\t"
        "s_waitcnt vmcnt(3)\n\t"
        "v_smfmac_f32_16x16x64_bf16 v[0:3], v[16:19], v[128:135], v106\n\t"
        "v_max3_u32 v108, v16, v17, v18\n\t"
        "v_max_u32_e32 v108, v19, v108\n\t"
        "v_smfmac_f32_16x16x64_bf16 v[4:7], v[16:19], v[136:143], v106\n\t"
        "v_smfmac_f32_16x16x64_bf16 v[8:11], v[16:19], v[144:151], v106\n\t"
        "v_smfmac_f32_16x16x64_bf16 v[12:15], v[16:19], v[152:159], v106\n\t"
        "s_waitcnt vmcnt(2)\n\t"
        "v_smfmac_f32_16x16x64_bf16 v[0:3], v[20:23], v[160:167], v106\n\t"
        "v_max3_u32 v108, v20, v21, v108\n\t"
        "v_max3_u32 v108, v22, v23, v108\n\t"
        "v_smfmac_f32_16x16x64_bf16 v[4:7], v[20:23], v[168:175], v106\n\t"
        "v_smfmac_f32_16x16x64_bf16 v[8:11], v[20:23], v[176:183], v106\n\t"
        "v_smfmac_f32_16x16x64_bf16 v[12:15], v[20:23], v[184:191], v106\n\t"
        "s_waitcnt vmcnt(1)\n\t"
        "v_smfmac_f32_16x16x64_bf16 v[0:3], v[24:27], v[192:199], v106\n\t"
        "v_max3_u32 v108, v24, v25, v108\n\t"
        "v_max3_u32 v108, v26, v27, v108\n\t"
        "v_smfmac_f32_16x16x64_bf16 v[4:7], v[24:27], v[200:207], v106\n\t"
        "v_smfmac_f32_16x16x64_bf16 v[8:11], v[24:27], v[208:215], v106\n\t"
        "v_smfmac_f32_16x16x64_bf16 v[12:15], v[24:27], v[216:223], v106\n\t"
        "s_waitcnt vmcnt(0)\n\t"
        "v_smfmac_f32_16x16x64_bf16 v[0:3], v[28:31], v[224:231], v106\n\t"
        "v_max3_u32 v108, v28, v29, v108\n\t"
        "v_max3_u32 v108, v30, v31, v108\n\t"
        "v_smfmac_f32_16x16x64_bf16 v[4:7], v[28:31], v[232:239], v106\n\t"
        "v_cmp_eq_u32_e32 vcc, -1, v108\n\t"
        "v_smfmac_f32_16x16x64_bf16 v[8:11], v[28:31], v[240:247], v106\n\t"
        "v_smfmac_f32_16x16x64_bf16 v[12:15], v[28:31], v[248:255], v106\n\t"
        "s_cbranch_vccnz LB_rdfail%=\n\t"
        "s_nop 3\n\t"
        "v_permlane32_swap_b32_e32 v0, v4\n\t"
        "v_permlane32_swap_b32_e32 v1, v5\n\t"
        "v_permlane32_swap_b32_e32 v2, v6\n\t"
        "v_permlane32_swap_b32_e32 v3, v7\n\t"
        "v_pk_add_f32 v[0:1], v[0:1], v[4:5]\n\t"
        "v_pk_add_f32 v[2:3], v[2:3], v[6:7]\n\t"
        "ds_write_b128 v118, v[0:3] offset:0\n\t"
        "v_permlane32_swap_b32_e32 v8, v12\n\t"
        "v_permlane32_swap_b32_e32 v9, v13\n\t"
        "v_permlane32_swap_b32_e32 v10, v14\n\t"
        "v_permlane32_swap_b32_e32 v11, v15\n\t"
        "v_pk_add_f32 v[8:9], v[8:9], v[12:13]\n\t"
        "v_pk_add_f32 v[10:11], v[10:11], v[14:15]\n\t"
        "ds_write_b128 v118, v[8:11] offset:1024\n\t"
        "buffer_store_dwordx2 v[110:111], v117, s[8:11], s26 offen sc1\n\t"
        "s_waitcnt lgkmcnt(0)\n\t"
        "s_barrier\n\t"
        "ds_read_b128 v[16:19], v119 offset:0\n\t"
        "ds_read_b128 v[20:23], v119 offset:2048\n\t"
        "ds_read_b128 v[24:27], v119 offset:4096\n\t"
        "ds_read_b128 v[28:31], v119 offset:6144\n\t"
        "buffer_load_dwordx4 v[64:67], v124, s[8:11], s28 offen sc1\n\t"
        "s_waitcnt lgkmcnt(2)\n\t"
        "v_pk_add_f32 v[16:17], v[16:17], v[20:21]\n\t"
        "v_pk_add_f32 v[18:19], v[18:19], v[22:23]\n\t"
        "s_waitcnt lgkmcnt(0)\n\t"
        "v_pk_add_f32 v[24:25], v[24:25], v[28:29]\n\t"
        "v_pk_add_f32 v[26:27], v[26:27], v[30:31]\n\t"
        "v_pk_add_f32 v[16:17], v[16:17], v[24:25]\n\t"
        "v_pk_add_f32 v[18:19], v[18:19], v[26:27]\n\t"
        "v_cvt_pk_bf16_f32 v20, v16, v17\n\t"
        "v_cvt_pk_bf16_f32 v21, v18, v19\n\t"
        "buffer_store_dwordx2 v[20:21], v114, s[8:11], s26 offen sc1\n\t"
        "s_cmp_eq_u32 s24, 63\n\t"
        "s_cbranch_scc1 LB_end%=\n\t"
        "s_memrealtime s[52:53]\n\t"
        "v_mov_b64_e32 v[24:25], -1\n\t"
        "v_mov_b64_e32 v[32:33], 0\n\t"
        "v_mov_b64_e32 v[34:35], 0\n\t"
        "v_mov_b64_e32 v[36:37], 0\n\t"
        "v_mov_b64_e32 v[38:39], 0\n\t"
        "v_mov_b64_e32 v[40:41], 0\n\t"
        "v_mov_b64_e32 v[42:43], 0\n\t"
        "v_mov_b64_e32 v[44:45], 0\n\t"
        "v_mov_b64_e32 v[46:47], 0\n\t"
        "v_mov_b64_e32 v[0:1], 0\n\t"
        "v_mov_b64_e32 v[2:3], 0\n\t"
        "v_mov_b64_e32 v[4:5], 0\n\t"
        "v_mov_b64_e32 v[6:7], 0\n\t"
        "v_mov_b64_e32 v[8:9], 0\n\t"
        "v_mov_b64_e32 v[10:11], 0\n\t"
        "v_mov_b64_e32 v[12:13], 0\n\t"
        "v_mov_b64_e32 v[14:15], 0\n\t"
        "s_add_u32 s46, s26, s69\n\t"
        "s_add_u32 s48, s26, s70\n\t"
        "s_add_u32 s73, s26, s71\n\t"
        "s_add_u32 s74, s26, s72\n\t"
        "s_sleep 1\n\t"
        "buffer_load_dwordx2 v[104:105], v113, s[8:11], s26 offen sc1\n\t"
        "buffer_store_dword v24, v116, s[8:11], s28 offen sc1\n\t"
        "s_xor_b32 s34, s26, 0x100000\n\t"
        "buffer_store_dwordx2 v[24:25], v114, s[8:11], s34 offen sc1\n\t"
        "s_add_u32 s66, s49, s43\n\t"
        "s_cmp_lg_u32 s66, 32\n\t"
        "s_cbranch_scc1 LB_norep%=\n\t"
        "s_waitcnt vmcnt(4)\n\t"
        "s_sub_u32 s66, s24, 1\n\t"
        "v_lshrrev_b32_e32 v68, 16, v64\n\t"
        "v_cmp_eq_u32_e32 vcc, s66, v68\n\t"
        "v_subrev_u32_e32 v64, s59, v64\n\t"
        "v_bfe_i32 v64, v64, 0, 16\n\t"
        "v_cndmask_b32_e32 v64, 0, v64, vcc\n\t"
        "v_lshrrev_b32_e32 v68, 16, v65\n\t"
        "v_cmp_eq_u32_e32 vcc, s66, v68\n\t"
        "v_subrev_u32_e32 v65, s59, v65\n\t"
        "v_bfe_i32 v65, v65, 0, 16\n\t"
        "v_cndmask_b32_e32 v65, 0, v65, vcc\n\t"
        "v_lshrrev_b32_e32 v68, 16, v66\n\t"
        "v_cmp_eq_u32_e32 vcc, s66, v68\n\t"
        "v_subrev_u32_e32 v66, s59, v66\n\t"
        "v_bfe_i32 v66, v66, 0, 16\n\t"
        "v_cndmask_b32_e32 v66, 0, v66, vcc\n\t"
        "v_lshrrev_b32_e32 v68, 16, v67\n\t"
        "v_cmp_eq_u32_e32 vcc, s66, v68\n\t"
        "v_subrev_u32_e32 v67, s59, v67\n\t"
        "v_bfe_i32 v67, v67, 0, 16\n\t"
        "v_cndmask_b32_e32 v67, 0, v67, vcc\n\t"
        "v_max3_i32 v64, v64, v65, v66\n\t"
        "v_max_i32_e32 v64, v64, v67\n\t"
        "s_nop 1\n\t"
        "v_max_i32_dpp v64, v64, v64 quad_perm:[1,0,3,2] row_mask:0xf bank_mask:0xf\n\t"
        "s_nop 1\n\t"
        "v_max_i32_dpp v64, v64, v64 quad_perm:[2,3,0,1] row_mask:0xf bank_mask:0xf\n\t"
        "s_nop 1\n\t"
        "v_max_i32_dpp v64, v64, v64 row_ror:4 row_mask:0xf bank_mask:0xf\n\t"
        "s_nop 1\n\t"
        "v_max_i32_dpp v64, v64, v64 row_ror:8 row_mask:0xf bank_mask:0xf\n\t"
        "v_mov_b32_e32 v65, v64\n\t"
        "s_nop 1\n\t"
        "v_permlane16_swap_b32_e32 v64, v65\n\t"
        "v_max_i32_e32 v64, v64, v65\n\t"
        "v_mov_b32_e32 v65, v64\n\t"
        "s_nop 1\n\t"
        "v_permlane32_swap_b32_e32 v64, v65\n\t"
        "v_max_i32_e32 v64, v64, v65\n\t"
        "s_nop 0\n\t"
        "v_readfirstlane_b32 s64, v64\n\t"
        "LB_norep%=:\n\t"
        "s_waitcnt vmcnt(2) lgkmcnt(0)\n\t"
        "s_mov_b32 s61, s29\n\t"
        "s_mov_b32 s62, s30\n\t"
        "s_mov_b32 s63, s31\n\t"
        "v_and_b32_e32 v30, 0xff, v105\n\t"
        "v_cmp_ne_u32_e32 vcc, s24, v30\n\t"
        "s_nop 0\n\t"
        "s_cbranch_vccnz LB_stre%=\n\t"
        "LB_stok%=:\n\t"
        "v_subrev_u32_e32 v104, s25, v104\n\t"
        "s_nop 1\n\t"
        "v_max_i32_dpp v104, v104, v104 quad_perm:[1,0,3,2] row_mask:0xf bank_mask:0xf\n\t"
        "s_nop 1\n\t"
        "v_max_i32_dpp v104, v104, v104 quad_perm:[2,3,0,1] row_mask:0xf bank_mask:0xf\n\t"
        "s_nop 1\n\t"
        "v_max_i32_dpp v104, v104, v104 row_ror:4 row_mask:0xf bank_mask:0xf\n\t"
        "s_nop 1\n\t"
        "v_max_i32_dpp v104, v104, v104 row_ror:8 row_mask:0xf bank_mask:0xf\n\t"
        "s_nop 1\n\t"
        "v_max_i32_dpp v104, v104, v104 row_bcast:15 row_mask:0xa bank_mask:0xf\n\t"
        "s_nop 1\n\t"
        "v_max_i32_dpp v104, v104, v104 row_bcast:31 row_mask:0xc bank_mask:0xf\n\t"
        "v_readfirstlane_b32 s35, v105\n\t"
        "v_readlane_b32 s36, v104, 63\n\t"
        "s_bfe_u32 s29, s35, 0xc0008\n\t"
        "s_lshr_b32 s30, s35, 20\n\t"
        "s_add_u32 s35, s31, s50\n\t"
        "s_mul_i32 s37, s50, 3\n\t"
        "s_min_u32 s35, s35, s37\n\t"
        "s_add_i32 s34, s36, s51\n\t"
        "s_cmp_gt_i32 s34, 0\n\t"
        "s_cselect_b32 s31, s35, 0\n\t"
        "s_cselect_b32 s34, s34, 0\n\t"
        "s_add_u32 s34, s34, s31\n\t"
        "s_add_u32 s37, s25, s34\n\t"
        "s_add_u32 s45, s37, s29\n\t"
        "s_add_u32 s25, s45, s30\n\t"
        "s_add_u32 s66, s49, s43\n\t"
        "s_cmp_lg_u32 s66, 32\n\t"
        "s_cbranch_scc1 LB_noctl%=\n\t"
        "s_cmp_eq_u32 s24, 0\n\t"
        "s_cbranch_scc1 LB_ctl0%=\n\t"
        "s_cmp_eq_u32 s24, 1\n\t"
        "s_cbranch_scc1 LB_ctl1%=\n\t"
        "s_add_i32 s65, s36, s62\n\t"
        "s_lshl_b32 s66, s65, 1\n\t"
        "s_sub_i32 s66, s66, s56\n\t"
        "s_cmp_gt_i32 s66, 56\n\t"
        "s_cbranch_scc1 LB_c1%=\n\t"
        "s_lshl_b32 s66, s65, 1\n\t"
        "s_add_u32 s34, s56, 8\n\t"
        "s_min_i32 s34, s34, s66\n\t"
        "s_sub_u32 s35, s56, s66\n\t"
        "s_ashr_i32 s35, s35, 2\n\t"
        "s_max_i32 s35, s35, 1\n\t"
        "s_sub_u32 s35, s56, s35\n\t"
        "s_max_i32 s56, s34, s35\n\t"
        "s_branch LB_c2%=\n\t"
        "LB_c1%=:\n\t"
        "s_add_u32 s56, s56, 2\n\t"
        "s_add_i32 s66, s64, s57\n\t"
        "s_cmp_gt_i32 s66, s61\n\t"
        "s_cbranch_scc1 LB_c2%=\n\t"
        "s_add_u32 s57, s57, 4\n\t"
        "s_min_u32 s57, s57, 23\n\t"
        "LB_c2%=:\n\t"
        "s_cmp_lg_u32 s63, 0\n\t"
        "s_cbranch_scc1 LB_c4%=\n\t"
        "s_lshl_b32 s66, s64, 1\n\t"
        "s_sub_i32 s66, s66, s55\n\t"
        "s_cmp_gt_i32 s66, 56\n\t"
        "s_cbranch_scc1 LB_c3%=\n\t"
        "s_lshl_b32 s66, s64, 1\n\t"
        "s_add_u32 s34, s55, 8\n\t"
        "s_min_i32 s34, s34, s66\n\t"
        "s_sub_u32 s35, s55, s66\n\t"
        "s_ashr_i32 s35, s35, 2\n\t"
        "s_max_i32 s35, s35, 1\n\t"
        "s_sub_u32 s35, s55, s35\n\t"
        "s_max_i32 s55, s34, s35\n\t"
        "s_branch LB_c4%=\n\t"
        "LB_c3%=:\n\t"
        "s_add_u32 s55, s55, 2\n\t"
        "s_cmp_lt_i32 s60, s58\n\t"
        "s_cbranch_scc1 LB_c4%=\n\t"
        "s_add_u32 s66, s58, 8\n\t"
        "s_cmp_gt_i32 s60, s66\n\t"
        "s_cbranch_scc1 LB_c4%=\n\t"
        "s_add_u32 s58, s58, 4\n\t"
        "s_min_u32 s58, s58, 20\n\t"
        "LB_c4%=:\n\t"
        "s_and_b32 s66, s24, 3\n\t"
        "s_cmp_eq_u32 s66, 0\n\t"
        "s_cselect_b32 s66, 1, 0\n\t"
        "s_sub_u32 s57, s57, s66\n\t"
        "s_max_u32 s57, s57, 7\n\t"
        "s_sub_u32 s58, s58, s66\n\t"
        "s_max_u32 s58, s58, 4\n\t"
        "s_add_u32 s34, s55, 1\n\t"
        "s_lshr_b32 s34, s34, 1\n\t"
        "s_add_u32 s34, s34, s57\n\t"
        "s_min_u32 s34, s34, 1000\n\t"
        "s_lshl_b32 s34, s34, 8\n\t"
        "s_add_u32 s35, s56, 1\n\t"
        "s_lshr_b32 s35, s35, 1\n\t"
        "s_add_u32 s35, s35, s58\n\t"
        "s_min_u32 s35, s35, 1000\n\t"
        "s_lshl_b32 s35, s35, 20\n\t"
        "s_or_b32 s54, s34, s35\n\t"
        "s_branch LB_ctl1%=\n\t"
        "LB_ctl0%=:\n\t"
        "s_movk_i32 s57, 7\n\t"
        "s_movk_i32 s58, 4\n\t"
        "s_sub_u32 s55, s29, 31\n\t"
        "s_lshl_b32 s55, s55, 1\n\t"
        "s_sub_u32 s56, s30, 16\n\t"
        "s_lshl_b32 s56, s56, 1\n\t"
        "LB_ctl1%=:\n\t"
        "s_mov_b32 s59, s37\n\t"
        "s_sub_u32 s60, 0, s36\n\t"
        "LB_noctl%=:\n\t"
        "s_add_u32 s34, s24, 1\n\t"
        "s_or_b32 s34, s34, s54\n\t"
        "v_mov_b32_e32 v111, s34\n\t"
        "s_mov_b32 s42, 0\n\t"
        "s_sub_u32 s35, s45, 1\n\t"
        "LB_wo%=:\n\t"
        "s_memrealtime s[32:33]\n\t"
        "s_waitcnt lgkmcnt(0)\n\t"
        "s_sub_u32 s34, s35, s32\n\t"
        "s_cmpk_lt_u32 s34, 0x2000\n\t"
        "s_cbranch_scc1 LB_wo%=\n\t"
        "LB_ro%=:\n\t"
        "buffer_load_dwordx4 v[64:67], v115, s[8:11], s46 offen offset:0 sc1\n\t"
        "s_nop 11\n\t"
        "buffer_load_dwordx4 v[68:71], v115, s[8:11], s46 offen offset:1024 sc1\n\t"
        "s_nop 11\n\t"
        "buffer_load_dwordx4 v[72:75], v115, s[8:11], s48 offen offset:0 sc1\n\t"
        "s_nop 11\n\t"
        "buffer_load_dwordx4 v[76:79], v115, s[8:11], s48 offen offset:1024 sc1\n\t"
        "s_nop 11\n\t"
        "buffer_load_dwordx4 v[80:83], v115, s[8:11], s73 offen offset:0 sc1\n\t"
        "s_nop 11\n\t"
        "buffer_load_dwordx4 v[84:87], v115, s[8:11], s73 offen offset:1024 sc1\n\t"
        "s_nop 11\n\t"
        "buffer_load_dwordx4 v[88:91], v115, s[8:11], s74 offen offset:0 sc1\n\t"
        "s_nop 11\n\t"
        "buffer_load_dwordx4 v[92:95], v115, s[8:11], s74 offen offset:1024 sc1\n\t"
        "LB_go2%=:\n\t"
        "s_waitcnt vmcnt(7)\n\t"
        "v_smfmac_f32_16x16x64_bf16 v[40:43], v[64:67], a[16:23], v107\n\t"
        "v_max3_u32 v108, v64, v65, v66\n\t"
        "v_max_u32_e32 v108, v67, v108\n\t"
        "v_smfmac_f32_16x16x64_bf16 v[36:39], v[64:67], a[8:15], v107\n\t"
        "v_smfmac_f32_16x16x64_bf16 v[32:35], v[64:67], a[0:7], v107\n\t"
        "v_smfmac_f32_16x16x64_bf16 v[44:47], v[64:67], a[24:31], v107\n\t"
        "s_waitcnt vmcnt(6)\n\t"
        "v_smfmac_f32_16x16x64_bf16 v[40:43], v[68:71], a[48:55], v107\n\t"
        "v_max3_u32 v108, v68, v69, v108\n\t"
        "v_max3_u32 v108, v70, v71, v108\n\t"
        "v_smfmac_f32_16x16x64_bf16 v[36:39], v[68:71], a[40:47], v107\n\t"
        "v_smfmac_f32_16x16x64_bf16 v[32:35], v[68:71], a[32:39], v107\n\t"
        "v_smfmac_f32_16x16x64_bf16 v[44:47], v[68:71], a[56:63], v107\n\t"
        "s_waitcnt vmcnt(5)\n\t"
        "v_smfmac_f32_16x16x64_bf16 v[40:43], v[72:75], a[80:87], v107\n\t"
        "v_max3_u32 v108, v72, v73, v108\n\t"
        "v_max3_u32 v108, v74, v75, v108\n\t"
        "v_smfmac_f32_16x16x64_bf16 v[36:39], v[72:75], a[72:79], v107\n\t"
        "v_smfmac_f32_16x16x64_bf16 v[32:35], v[72:75], a[64:71], v107\n\t"
        "v_smfmac_f32_16x16x64_bf16 v[44:47], v[72:75], a[88:95], v107\n\t"
        "s_waitcnt vmcnt(4)\n\t"
        "v_smfmac_f32_16x16x64_bf16 v[40:43], v[76:79], a[112:119], v107\n\t"
        "v_max3_u32 v108, v76, v77, v108\n\t"
        "v_max3_u32 v108, v78, v79, v108\n\t"
        "v_smfmac_f32_16x16x64_bf16 v[36:39], v[76:79], a[104:111], v107\n\t"
        "v_smfmac_f32_16x16x64_bf16 v[32:35], v[76:79], a[96:103], v107\n\t"
        "v_smfmac_f32_16x16x64_bf16 v[44:47], v[76:79], a[120:127], v107\n\t"
        "s_waitcnt vmcnt(3)\n\t"
        "v_smfmac_f32_16x16x64_bf16 v[40:43], v[80:83], a[144:151], v107\n\t"
        "v_max3_u32 v108, v80, v81, v108\n\t"
        "v_max3_u32 v108, v82, v83, v108\n\t"
        "v_smfmac_f32_16x16x64_bf16 v[36:39], v[80:83], a[136:143], v107\n\t"
        "v_smfmac_f32_16x16x64_bf16 v[32:35], v[80:83], a[128:135], v107\n\t"
        "v_smfmac_f32_16x16x64_bf16 v[44:47], v[80:83], a[152:159], v107\n\t"
        "s_waitcnt vmcnt(2)\n\t"
        "v_smfmac_f32_16x16x64_bf16 v[40:43], v[84:87], a[176:183], v107\n\t"
        "v_max3_u32 v108, v84, v85, v108\n\t"
        "v_max3_u32 v108, v86, v87, v108\n\t"
        "v_smfmac_f32_16x16x64_bf16 v[36:39], v[84:87], a[168:175], v107\n\t"
        "v_smfmac_f32_16x16x64_bf16 v[32:35], v[84:87], a[160:167], v107\n\t"
        "v_smfmac_f32_16x16x64_bf16 v[44:47], v[84:87], a[184:191], v107\n\t"
        "s_waitcnt vmcnt(1)\n\t"
        "v_smfmac_f32_16x16x64_bf16 v[40:43], v[88:91], a[208:215], v107\n\t"
        "v_max3_u32 v108, v88, v89, v108\n\t"
        "v_max3_u32 v108, v90, v91, v108\n\t"
        "v_smfmac_f32_16x16x64_bf16 v[36:39], v[88:91], a[200:207], v107\n\t"
        "v_smfmac_f32_16x16x64_bf16 v[32:35], v[88:91], a[192:199], v107\n\t"
        "v_smfmac_f32_16x16x64_bf16 v[44:47], v[88:91], a[216:223], v107\n\t"
        "s_waitcnt vmcnt(0)\n\t"
        "v_smfmac_f32_16x16x64_bf16 v[40:43], v[92:95], a[240:247], v107\n\t"
        "v_max3_u32 v108, v92, v93, v108\n\t"
        "v_max3_u32 v108, v94, v95, v108\n\t"
        "v_smfmac_f32_16x16x64_bf16 v[36:39], v[92:95], a[232:239], v107\n\t"
        "v_cmp_eq_u32_e32 vcc, -1, v108\n\t"
        "v_smfmac_f32_16x16x64_bf16 v[32:35], v[92:95], a[224:231], v107\n\t"
        "v_smfmac_f32_16x16x64_bf16 v[44:47], v[92:95], a[248:255], v107\n\t"
        "s_cbranch_vccnz LB_rofail%=\n\t"
        "s_nop 3\n\t"
        "v_permlane32_swap_b32_e32 v40, v36\n\t"
        "v_permlane32_swap_b32_e32 v41, v37\n\t"
        "v_permlane32_swap_b32_e32 v42, v38\n\t"
        "v_permlane32_swap_b32_e32 v43, v39\n\t"
        "v_pk_add_f32 v[32:33], v[32:33], v[36:37]\n\t"
        "v_pk_add_f32 v[34:35], v[34:35], v[38:39]\n\t"
        "s_nop 0\n\t"
        "v_add_f32_dpp v32, v40, v44 quad_perm:[0,1,2,3] row_mask:0xc bank_mask:0xf\n\t"
        "v_add_f32_dpp v33, v41, v45 quad_perm:[0,1,2,3] row_mask:0xc bank_mask:0xf\n\t"
        "v_add_f32_dpp v34, v42, v46 quad_perm:[0,1,2,3] row_mask:0xc bank_mask:0xf\n\t"
        "v_add_f32_dpp v35, v43, v47 quad_perm:[0,1,2,3] row_mask:0xc bank_mask:0xf\n\t"
        "v_permlane16_swap_b32_e32 v32, v33\n\t"
        "s_nop 0\n\t"
        "v_permlane16_swap_b32_e32 v34, v35\n\t"
        "v_add_f32_e32 v32, v32, v33\n\t"
        "v_add_f32_e32 v34, v34, v35\n\t"
        "ds_write2_b32 v120, v32, v34 offset0:0 offset1:16\n\t"
        "s_waitcnt lgkmcnt(0)\n\t"
        "s_barrier\n\t"
        "ds_read_b64 v[64:65], v121 offset:0\n\t"
        "ds_read_b64 v[66:67], v121 offset:512\n\t"
        "ds_read_b64 v[68:69], v121 offset:1024\n\t"
        "ds_read_b64 v[70:71], v121 offset:1536\n\t"
        "s_waitcnt lgkmcnt(2)\n\t"
        "v_pk_add_f32 v[64:65], v[64:65], v[66:67]\n\t"
        "s_waitcnt lgkmcnt(0)\n\t"
        "v_pk_add_f32 v[68:69], v[68:69], v[70:71]\n\t"
        "v_pk_add_f32 v[64:65], v[64:65], v[68:69]\n\t"
        "v_cvt_pk_bf16_f32 v64, v64, v65\n\t"
        "buffer_store_dword v64, v116, s[8:11], s27 offen sc1\n\t"
        "s_memrealtime s[32:33]\n\t"
        "s_waitcnt lgkmcnt(0)\n\t"
        "v_mov_b32_e32 v110, s32\n\t"
        "s_add_u32 s24, s24, 1\n\t"
        "s_mov_b32 s28, s26\n\t"
        "s_mov_b32 s26, s27\n\t"
        "s_add_u32 s27, s27, 0x80000\n\t"
        "s_and_b32 s27, s27, 0x1fffff\n\t"
        "s_branch LB_loop%=\n\t"
        "LB_end%=:\n\t"
        "s_endpgm\n\t"
        "LB_stre%=:\n\t"
        "s_mov_b32 s42, 0\n\t"
        "LB_str2%=:\n\t"
        "buffer_load_dwordx2 v[104:105], v113, s[8:11], s26 offen sc1\n\t"
        "s_waitcnt vmcnt(0)\n\t"
        "v_and_b32_e32 v30, 0xff, v105\n\t"
        "v_cmp_ne_u32_e32 vcc, s24, v30\n\t"
        "s_nop 0\n\t"
        "s_cbranch_vccz LB_stok%=\n\t"
        "s_add_u32 s42, s42, 1\n\t"
        "s_cmp_lt_u32 s42, 0x100000\n\t"
        "s_cbranch_scc1 LB_str2%=\n\t"
        "s_movk_i32 s34, 5\n\t"
        "s_branch LB_die%=\n\t"
        "LB_rdfail%=:\n\t"
        "s_or_b32 s47, s47, 1\n\t"
        "buffer_load_dwordx4 v[16:19], v112, s[8:11], s26 offen offset:0 sc1\n\t"
        "buffer_load_dwordx4 v[20:23], v112, s[8:11], s26 offen offset:1024 sc1\n\t"
        "buffer_load_dwordx4 v[24:27], v112, s[8:11], s26 offen offset:2048 sc1\n\t"
        "buffer_load_dwordx4 v[28:31], v112, s[8:11], s26 offen offset:3072 sc1\n\t"
        "s_nop 7\n\t"
        "v_mov_b64_e32 v[0:1], 0\n\t"
        "v_mov_b64_e32 v[2:3], 0\n\t"
        "v_mov_b64_e32 v[4:5], 0\n\t"
        "v_mov_b64_e32 v[6:7], 0\n\t"
        "v_mov_b64_e32 v[8:9], 0\n\t"
        "v_mov_b64_e32 v[10:11], 0\n\t"
        "v_mov_b64_e32 v[12:13], 0\n\t"
        "v_mov_b64_e32 v[14:15], 0\n\t"
        "s_branch LB_rdre%=w\n\t"
        "LB_rdre%=:\n\t"
        "buffer_load_dwordx4 v[16:19], v112, s[8:11], s26 offen offset:0 sc1\n\t"
        "buffer_load_dwordx4 v[20:23], v112, s[8:11], s26 offen offset:1024 sc1\n\t"
        "buffer_load_dwordx4 v[24:27], v112, s[8:11], s26 offen offset:2048 sc1\n\t"
        "buffer_load_dwordx4 v[28:31], v112, s[8:11], s26 offen offset:3072 sc1\n\t"
        "LB_rdre%=w:\n\t"
        "s_waitcnt vmcnt(0) lgkmcnt(0)\n\t"
        "v_max3_u32 v108, v16, v17, v18\n\t"
        "v_max3_u32 v108, v108, v19, v20\n\t"
        "v_max3_u32 v108, v108, v21, v22\n\t"
        "v_max3_u32 v108, v108, v23, v24\n\t"
        "v_max3_u32 v108, v108, v25, v26\n\t"
        "v_max3_u32 v108, v108, v27, v28\n\t"
        "v_max3_u32 v108, v108, v29, v30\n\t"
        "v_max_u32_e32 v108, v108, v31\n\t"
        "v_cmp_eq_u32_e32 vcc, -1, v108\n\t"
        "s_nop 0\n\t"
        "s_cbranch_vccz LB_go%=\n\t"
        "s_add_u32 s42, s42, 1\n\t"
        "s_cmp_lt_u32 s42, 0x100000\n\t"
        "s_cbranch_scc1 LB_rdre%=\n\t"
        "s_movk_i32 s34, 3\n\t"
        "s_branch LB_die%=\n\t"
        "LB_rofail%=:\n\t"
        "s_or_b32 s47, s47, 4\n\t"
        "buffer_load_dwordx4 v[64:67], v115, s[8:11], s46 offen offset:0 sc1\n\t"
        "buffer_load_dwordx4 v[68:71], v115, s[8:11], s46 offen offset:1024 sc1\n\t"
        "buffer_load_dwordx4 v[72:75], v115, s[8:11], s48 offen offset:0 sc1\n\t"
        "buffer_load_dwordx4 v[76:79], v115, s[8:11], s48 offen offset:1024 sc1\n\t"
        "buffer_load_dwordx4 v[80:83], v115, s[8:11], s73 offen offset:0 sc1\n\t"
        "buffer_load_dwordx4 v[84:87], v115, s[8:11], s73 offen offset:1024 sc1\n\t"
        "buffer_load_dwordx4 v[88:91], v115, s[8:11], s74 offen offset:0 sc1\n\t"
        "buffer_load_dwordx4 v[92:95], v115, s[8:11], s74 offen offset:1024 sc1\n\t"
        "s_nop 7\n\t"
        "v_mov_b64_e32 v[32:33], 0\n\t"
        "v_mov_b64_e32 v[34:35], 0\n\t"
        "v_mov_b64_e32 v[36:37], 0\n\t"
        "v_mov_b64_e32 v[38:39], 0\n\t"
        "v_mov_b64_e32 v[40:41], 0\n\t"
        "v_mov_b64_e32 v[42:43], 0\n\t"
        "v_mov_b64_e32 v[44:45], 0\n\t"
        "v_mov_b64_e32 v[46:47], 0\n\t"
        "s_branch LB_rore%=w\n\t"
        "LB_rore%=:\n\t"
        "buffer_load_dwordx4 v[64:67], v115, s[8:11], s46 offen offset:0 sc1\n\t"
        "buffer_load_dwordx4 v[68:71], v115, s[8:11], s46 offen offset:1024 sc1\n\t"
        "buffer_load_dwordx4 v[72:75], v115, s[8:11], s48 offen offset:0 sc1\n\t"
        "buffer_load_dwordx4 v[76:79], v115, s[8:11], s48 offen offset:1024 sc1\n\t"
        "buffer_load_dwordx4 v[80:83], v115, s[8:11], s73 offen offset:0 sc1\n\t"
        "buffer_load_dwordx4 v[84:87], v115, s[8:11], s73 offen offset:1024 sc1\n\t"
        "buffer_load_dwordx4 v[88:91], v115, s[8:11], s74 offen offset:0 sc1\n\t"
        "buffer_load_dwordx4 v[92:95], v115, s[8:11], s74 offen offset:1024 sc1\n\t"
        "LB_rore%=w:\n\t"
        "s_waitcnt vmcnt(0) lgkmcnt(0)\n\t"
        "v_max3_u32 v108, v64, v65, v66\n\t"
        "v_max3_u32 v108, v108, v67, v68\n\t"
        "v_max3_u32 v108, v108, v69, v70\n\t"
        "v_max3_u32 v108, v108, v71, v72\n\t"
        "v_max3_u32 v108, v108, v73, v74\n\t"
        "v_max3_u32 v108, v108, v75, v76\n\t"
        "v_max3_u32 v108, v108, v77, v78\n\t"
        "v_max3_u32 v108, v108, v79, v80\n\t"
        "v_max3_u32 v108, v108, v81, v82\n\t"
        "v_max3_u32 v108, v108, v83, v84\n\t"
        "v_max3_u32 v108, v108, v85, v86\n\t"
        "v_max3_u32 v108, v108, v87, v88\n\t"
        "v_max3_u32 v108, v108, v89, v90\n\t"
        "v_max3_u32 v108, v108, v91, v92\n\t"
        "v_max3_u32 v108, v108, v93, v94\n\t"
        "v_max_u32_e32 v108, v108, v95\n\t"
        "v_cmp_eq_u32_e32 vcc, -1, v108\n\t"
        "s_nop 0\n\t"
        "s_cbranch_vccz LB_go2%=\n\t"
        "s_add_u32 s42, s42, 1\n\t"
        "s_cmp_lt_u32 s42, 0x100000\n\t"
        "s_cbranch_scc1 LB_rore%=\n\t"
        "s_movk_i32 s34, 4\n\t"
        "s_branch LB_die%=\n\t"
        "LB_die%=:\n\t"
        "v_mov_b32_e32 v30, s34\n\t"
        "s_mov_b32 s35, 33024\n\t"
        "s_mov_b64 exec, 1\n\t"
        "buffer_store_dword v30, off, s[12:15], s35 sc1\n\t"
        "s_endpgm\n\t"
        ".Lk_end_B:\n\t"
        :: REC_S_IN, "{v96}"(g_v), "{v97}"(g_o), "{v98}"(wb0), "{v99}"(wb1), "{v100}"(rb0), "{v101}"(rb0 ^ 32u), "{v102}"(rb0 ^ 64u), "{v103}"(rb0 ^ 96u),
           "{v106}"(pp ? 0xEEEEEEEEu : 0x44444444u), "{v107}"(bb ? 0xEEEEEEEEu : 0x44444444u), "{v[108:109]}"(m0p), "{s68}"(rot),
           "{v112}"(a_mem), "{v113}"((24 + cp) * 16384 + l * ST_P), "{v114}"(a_vmw), "{v115}"(a_or), "{v116}"(a_mw), "{v117}"(a_sw),
           "{v118}"(lb + w * 2048 + l * 16), "{v119}"(lb + (w & 1) * 1024 + l * 16), "{v120}"(lb + LB_R + w * 512 + ((g >> 1) * 2 + (n >> 3)) * 128 + (g & 1) * 32 + (n & 7) * 4),
           "{v121}"(lb + LB_R + l * 8), "{v122}"(stg + l * 16), "{v123}"(o_r), "{v124}"(rb + w == 32 ? 28 * 16384 + (l & 31) * 128 : OOB)
        : "memory", "vcc", "scc", "v0", "v255", "a0", "a255", "s101");
    __builtin_unreachable();
}
__device__ __forceinline__ void rec_helper(const Params& p, rsrc_t r, unsigned char* lds, const int wv, const int hi) {
    const int tid = otid(), l = tid & 63;
    const rsrc_t rh = __builtin_amdgcn_make_buffer_rsrc(p.ws + hx_base(p.ws), 0, HX_BYTES, 0x00020000);
    const v4u ones = {0xFFFFFFFFu, 0xFFFFFFFFu, 0xFFFFFFFFu, 0xFFFFFFFFu};
    unsigned best0 = ~0u, best1 = ~0u, d[5] = {0u, 0u, 0u, 0u, 0u};
    const bool meas = (hi & 7) < 2;
    if (meas) {
        const unsigned ca = l >= 8 ? OOB : (31 + wv * 32) * 16384 + hi * 256 + l * 16;
        const unsigned tend = rtnow() + CAL_T;
#pragma unroll 1
        for (int i = 0; i < 64 && (i < 3 || (int)(rtnow() - tend) < 0); ++i) {
            const unsigned t0 = (unsigned)__builtin_amdgcn_s_memtime(); __builtin_amdgcn_s_waitcnt(0);
            st16(rh, ca, ones); DRAIN();
            const unsigned t1 = (unsigned)__builtin_amdgcn_s_memtime(); __builtin_amdgcn_s_waitcnt(0);
            st16(rh, ca + 8192, ones); DRAIN();
            const unsigned t2 = (unsigned)__builtin_amdgcn_s_memtime(); __builtin_amdgcn_s_waitcnt(0);
            best0 = min(best0, t1 - t0); best1 = min(best1, t2 - t1); }
        if (l < 2) __hip_atomic_fetch_max((unsigned*)(p.ws + F_CAL + 128) + l, 65535u - min(l ? best1 : best0, 65535u), __ATOMIC_RELAXED, __HIP_MEMORY_SCOPE_AGENT);
    } else if (TOUCH) {
        const int xq = hi >> 3, k = ((hi & 7) - 2) * 4 + wv;
#pragma unroll
        for (int i = 0; i < 5; ++i) { const bf16_t* q;
            if (i & 1) { const int z = (i >> 1) * 24 + k, st = z & 3, c = 4 + (z >> 2);
                q = ((st & 1) ? p.Wk : p.Wq) + (size_t)(((((st & 1) * 2 + 1) * 4 + c) & 15) * 64 + l) * 4096 + (xq * 8 + 1 + (st >> 1) * 4) * 128 + 64; }
            else { const int z = min((i >> 1) * 24 + k, 63);
                q = p.Wo + (size_t)(((z & 3) * 8 + (z >> 3)) * 128 + ((z >> 2) & 1) * 64 + l) * 1024 + (RPL == 4 ? (xq >> 1) : (xq & 1)) * 512 + 192; if (RPL == 4 && (xq & 1)) q = p.gate_w; }
            asm volatile("global_load_dword %0, %1, off" : "+v"(d[i]) : "v"(q) : "memory"); }
    }
#pragma unroll
    for (int i = 0; i < 16; ++i) st16(rh, hi * 65536 + i * 4096 + tid * 16, ones);
    asm volatile("s_waitcnt vmcnt(0)" : "+v"(d[0]), "+v"(d[1]), "+v"(d[2]), "+v"(d[3]), "+v"(d[4]) :: "memory");
    __syncthreads();
    if (tid == 0 && __hip_atomic_fetch_add((unsigned*)(p.ws + F_CAL + 256), 1u, __ATOMIC_RELAXED, __HIP_MEMORY_SCOPE_AGENT) == 31u) {
        const v2u c = ld8(r, F_CAL + 128); st4(r, F_CAL, c.x < c.y ? 8193u : 1u); }
}
using f32x16 = __attribute__((ext_vector_type(16))) float;
constexpr int CV_RP = 80;
constexpr int CV_U = 96 * CV_RP;
constexpr int CV_KP = 4416;
constexpr int CV_K0 = 8 * CV_U;
__device__ __forceinline__ float blo(unsigned x) { return __uint_as_float(x << 16); }
__device__ __forceinline__ float bhi(unsigned x) { return __uint_as_float(x & 0xFFFF0000u); }
#define NAP() do { __builtin_amdgcn_sched_barrier(0); __builtin_amdgcn_s_sleep(XSLP); __builtin_amdgcn_sched_barrier(0); } while (0)
__device__ __forceinline__ void gate_block(const Params& p, rsrc_t r, const int hi, const int wv) {
    const int l = otid() & 63, t0 = hi * 64 + wv * 16;
    v4u xg[16][2], gw[2];
#pragma unroll
    for (int q = 0; q < 2; ++q) gw[q] = *(const v4u*)(p.gate_w + q * 512 + l * 8);
#pragma unroll
    for (int i = 0; i < 16; ++i)
#pragma unroll
        for (int q = 0; q < 2; ++q) { xg[i][q] = *(const v4u*)(p.Aq + (size_t)(t0 + i) * D + q * 512 + l * 8); NAP(); }
    float mine = 0.f;
#pragma unroll
    for (int i = 0; i < 16; ++i) { float acc = 0.f;
#pragma unroll
        for (int q = 0; q < 2; ++q)
#pragma unroll
            for (int e = 0; e < 4; ++e) { const unsigned pr = pack2(blo(xg[i][q][e]) * blo(gw[q][e]), bhi(xg[i][q][e]) * bhi(gw[q][e])); acc += blo(pr); acc += bhi(pr); }
        for (int o = 32; o > 0; o >>= 1) acc += __shfl_xor(acc, o);
        if (l == i) mine = acc; }
    if (l < 16) { const float z = rbfh(mine); st4(r, W_GATE + (t0 + l) * 4, __float_as_uint(rbfh(1.f / (1.f + expf(-z))))); }
}
__device__ __forceinline__ void conv_prep(const Params& p, rsrc_t r, const int wv, const int hi) {
    const int tid = otid(), Q = tid >> 2, cg = tid & 3, ch0 = hi * 32 + cg * 8;
    v4u sk[4]; float sv[8][4];
#pragma unroll
    for (int q = 0; q < 4; ++q) sk[q] = *(const v4u*)(p.shift_k + ch0 * 4 + q * 8);
#pragma unroll
    for (int c8 = 0; c8 < 8; ++c8)
#pragma unroll
        for (int tp = 0; tp < 4; ++tp) { const unsigned sw = sk[c8 >> 1][(c8 & 1) * 2 + (tp >> 1)]; sv[c8][tp] = (tp & 1) ? bhi(sw) : blo(sw); }
    const rsrc_t rk = __builtin_amdgcn_make_buffer_rsrc((void*)p.Ak, 0, S * D * 2, 0x00020000), rv = __builtin_amdgcn_make_buffer_rsrc((void*)p.Av, 0, S * D * 2, 0x00020000),
                 rq = __builtin_amdgcn_make_buffer_rsrc((void*)p.Aq, 0, S * D * 2, 0x00020000);
#pragma unroll 1
    for (int G = 0; G < 4; ++G) {
        const unsigned ro = (unsigned)((Q * 32 + G * 8) * D + ch0) * 2u, uo = hi * 131072 + tid * 16;
        v4u kr[11], u[8], qv[8];
#pragma unroll
        for (int i = 0; i < 11; ++i) { kr[i] = __builtin_amdgcn_raw_buffer_load_b128(rk, (i < 3 && Q + G == 0) ? OOB : ro + (i - 3) * 2048, 0, 0); NAP(); }
#pragma unroll
        for (int i = 0; i < 8; ++i) { u[i] = __builtin_amdgcn_raw_buffer_load_b128(rv, ro + i * 2048, 0, 0); NAP(); }
#pragma unroll
        for (int i = 0; i < 8; ++i) { qv[i] = __builtin_amdgcn_raw_buffer_load_b128(rq, ro + i * 2048, 0, 0); NAP(); }
#pragma unroll
        for (int q = 0; q < 4; ++q) {
#pragma unroll
            for (int i = 0; i < 8; ++i) {
                unsigned ks = pack2(blo(kr[i][q]) * sv[2 * q][3], bhi(kr[i][q]) * sv[2 * q + 1][3]), pr;
#pragma unroll
                for (int d = 1; d < 4; ++d) { pr = pack2(blo(kr[i + d][q]) * sv[2 * q][3 - d], bhi(kr[i + d][q]) * sv[2 * q + 1][3 - d]); ks = pack2(blo(ks) + blo(pr), bhi(ks) + bhi(pr)); }
                u[i][q] = pack2(blo(ks) * blo(u[i][q]), bhi(ks) * bhi(u[i][q])); }
            v4u lo, hh;
#pragma unroll
            for (int z = 0; z < 4; ++z) { const unsigned x = u[7 - 2 * z][q], y = u[6 - 2 * z][q]; lo[z] = __builtin_amdgcn_perm(y, x, 0x05040100u); hh[z] = __builtin_amdgcn_perm(y, x, 0x07060302u); }
            st16(r, W_U + uo + q * 32768 + G * 4096, lo); NAP(); st16(r, W_U + uo + q * 32768 + 16384 + G * 4096, hh); NAP();
#pragma unroll
            for (int z = 0; z < 2; ++z) { st16(r, W_Y + uo + q * 32768 + (2 * G + z) * 4096, (v4u){qv[4 * z][q], qv[4 * z + 1][q], qv[4 * z + 2][q], qv[4 * z + 3][q]}); NAP(); }
        }
    }
    DRAIN(); __syncthreads();
    if (tid == 0) __hip_atomic_fetch_add((unsigned*)(p.ws + F_CONV), 1u, __ATOMIC_RELAXED, __HIP_MEMORY_SCOPE_AGENT);
}
__device__ __forceinline__ bool conv_core(const Params& p, rsrc_t r, unsigned char* lds, const int wv, const int un) {
    const int tid = otid(), w = wv, l = tid & 63, Q = tid >> 2, cg = tid & 3;
    const rsrc_t rs = __builtin_amdgcn_make_buffer_rsrc((void*)p.ssm_k, 0, S * D * 2, 0x00020000);
    v4u kraw[4]; v2u kext[4];
    auto kload = [&](int n) { const unsigned o = (unsigned)(((un >> 2) * 32 + w * 8 + (un & 3) * 2 + n) * S + l * 8) * 2u;
#pragma unroll
        for (int i = 0; i < 4; ++i) { kraw[i] = __builtin_amdgcn_raw_buffer_load_b128(rs, o + i * 1024, 0, CV_POL);
            kext[i] = __builtin_amdgcn_raw_buffer_load_b64(rs, (i == 3 && l == 63) ? OOB : o + i * 1024 + 16, 0, 0); } };
    kload(0);
    if (!wait_word(r, F_CONV, 32u, 2048u)) return false;
    v4u ur[8], qr[8];
#pragma unroll
    for (int i = 0; i < 8; ++i) ur[i] = ld16(r, W_U + un * 32768 + i * 4096 + tid * 16);
#pragma unroll
    for (int i = 0; i < 8; ++i) qr[i] = ld16(r, W_Y + un * 32768 + i * 4096 + tid * 16);
    const unsigned dv = *(const unsigned*)(p.ssm_D + (un >> 2) * 32 + cg * 8 + (un & 3) * 2);
    for (int i = tid; i < 8 * 160; i += 256) *(v4u*)(lds + (i / 160) * CV_U + (i % 160) * 16) = (v4u){0u, 0u, 0u, 0u};
    unsigned char* kc = lds + CV_K0 + w * 4 * CV_KP;
    if (l < 32) {
#pragma unroll
        for (int q = 0; q < 4; ++q) *(unsigned*)(kc + q * CV_KP + l * 4) = 0u; }
    const int b = l & 31, kg = l >> 5;
    const unsigned kb = (unsigned)(size_t)kc + (b & 3) * CV_KP + ((b & 28) - 8 * kg + 56 - 16) * 2, ub0 = (unsigned)(size_t)lds + (1 + b) * CV_RP + kg * 16;
    unsigned char* const urow = lds + 2 * cg * CV_U + (32 + Q) * CV_RP;
#pragma unroll
    for (int i = 0; i < 8; ++i) *(v4u*)(urow + (i >> 2) * CV_U + (i & 3) * 16) = ur[i];
    __syncthreads();
#pragma unroll 1
        for (int ci = 0; ci < 2; ++ci) {
            const int c8 = 2 * w + ci;
#pragma unroll
            for (int i = 0; i < 4; ++i) { const v4u e = kraw[i]; const v2u f = kext[i]; unsigned char* q = kc + 128 + (i * 64 + l) * 16;
                const unsigned s0 = __builtin_amdgcn_alignbit(e.y, e.x, 16), s1 = __builtin_amdgcn_alignbit(e.z, e.y, 16), s2 = __builtin_amdgcn_alignbit(e.w, e.z, 16),
                               s3 = __builtin_amdgcn_alignbit(f.x, e.w, 16), s4 = __builtin_amdgcn_alignbit(f.y, f.x, 16);
                *(v4u*)q = (v4u){s0, s1, s2, s3}; *(v4u*)(q + CV_KP) = (v4u){e.y, e.z, e.w, f.x}; *(v4u*)(q + 2 * CV_KP) = (v4u){s1, s2, s3, s4}; *(v4u*)(q + 3 * CV_KP) = (v4u){e.z, e.w, f.x, f.y};
                if (i == 0 && l == 0) {
                    *(v2u*)(q - 8) = (v2u){0u, e.x << 16}; *(v2u*)(q + CV_KP - 8) = (v2u){0u, e.x}; *(v2u*)(q + 2 * CV_KP - 8) = (v2u){e.x << 16, s0}; *(v2u*)(q + 3 * CV_KP - 8) = (v2u){e.x, e.y}; } }
            if (ci == 0) kload(1);
            __builtin_amdgcn_wave_barrier();
            f32x16 acc0, acc1;
            asm volatile(
                "ds_read_b64 v[192:193], %[kb] offset:32\n\t"
                "ds_read_b64 v[194:195], %[kb] offset:40\n\t"
                "ds_read_b128 v[196:199], %[ub] offset:5040\n\t"
                "ds_read_b128 v[200:203], %[ub] offset:2480\n\t"
                "ds_read_b64 v[204:205], %[kb] offset:2080\n\t"
                "ds_read_b64 v[206:207], %[kb] offset:2088\n\t"
                "ds_read_b64 v[208:209], %[kb] offset:0\n\t"
                "ds_read_b64 v[210:211], %[kb] offset:8\n\t"
                "ds_read_b128 v[212:215], %[ub] offset:5072\n\t"
                "ds_read_b128 v[216:219], %[ub] offset:2512\n\t"
                "ds_read_b64 v[220:221], %[kb] offset:2048\n\t"
                "ds_read_b64 v[222:223], %[kb] offset:2056\n\t"
                "s_waitcnt lgkmcnt(9)\n\t"
                "v_mfma_f32_32x32x16_bf16 %[a1], v[192:195], v[196:199], 0\n\t"
                "ds_read_b64 v[224:225], %[kb] offset:96\n\t"
                "ds_read_b64 v[226:227], %[kb] offset:104\n\t"
                "ds_read_b128 v[228:231], %[ub] offset:4960\n\t"
                "s_waitcnt lgkmcnt(11)\n\t"
                "v_mfma_f32_32x32x16_bf16 %[a0], v[192:195], v[200:203], 0\n\t"
                "ds_read_b128 v[232:235], %[ub] offset:2400\n\t"
                "s_waitcnt lgkmcnt(10)\n\t"
                "v_mfma_f32_32x32x16_bf16 %[a1], v[204:207], v[200:203], %[a1]\n\t"
                "ds_read_b64 v[236:237], %[kb] offset:2144\n\t"
                "ds_read_b64 v[238:239], %[kb] offset:2152\n\t"
                "s_waitcnt lgkmcnt(9)\n\t"
                "v_mfma_f32_32x32x16_bf16 %[a1], v[208:211], v[212:215], %[a1]\n\t"
                "ds_read_b64 v[240:241], %[kb] offset:64\n\t"
                "ds_read_b64 v[242:243], %[kb] offset:72\n\t"
                "ds_read_b128 v[244:247], %[ub] offset:4992\n\t"
                "s_waitcnt lgkmcnt(11)\n\t"
                "v_mfma_f32_32x32x16_bf16 %[a0], v[208:211], v[216:219], %[a0]\n\t"
                "ds_read_b128 v[248:251], %[ub] offset:2432\n\t"
                "s_waitcnt lgkmcnt(10)\n\t"
                "v_mfma_f32_32x32x16_bf16 %[a1], v[220:223], v[216:219], %[a1]\n\t"
                "ds_read_b64 v[252:253], %[kb] offset:2112\n\t"
                "ds_read_b64 v[254:255], %[kb] offset:2120\n\t"
                "s_waitcnt lgkmcnt(9)\n\t"
                "v_mfma_f32_32x32x16_bf16 %[a1], v[224:227], v[228:231], %[a1]\n\t"
                "ds_read_b64 v[192:193], %[kb] offset:160\n\t"
                "ds_read_b64 v[194:195], %[kb] offset:168\n\t"
                "ds_read_b128 v[196:199], %[ub] offset:4880\n\t"
                "s_waitcnt lgkmcnt(11)\n\t"
                "v_mfma_f32_32x32x16_bf16 %[a0], v[224:227], v[232:235], %[a0]\n\t"
                "ds_read_b128 v[200:203], %[ub] offset:2320\n\t"
                "s_waitcnt lgkmcnt(10)\n\t"
                "v_mfma_f32_32x32x16_bf16 %[a1], v[236:239], v[232:235], %[a1]\n\t"
                "ds_read_b64 v[204:205], %[kb] offset:2208\n\t"
                "ds_read_b64 v[206:207], %[kb] offset:2216\n\t"
                "s_waitcnt lgkmcnt(9)\n\t"
                "v_mfma_f32_32x32x16_bf16 %[a1], v[240:243], v[244:247], %[a1]\n\t"
                "ds_read_b64 v[208:209], %[kb] offset:128\n\t"
                "ds_read_b64 v[210:211], %[kb] offset:136\n\t"
                "ds_read_b128 v[212:215], %[ub] offset:4912\n\t"
                "s_waitcnt lgkmcnt(11)\n\t"
                "v_mfma_f32_32x32x16_bf16 %[a0], v[240:243], v[248:251], %[a0]\n\t"
                "ds_read_b128 v[216:219], %[ub] offset:2352\n\t"
                "s_waitcnt lgkmcnt(10)\n\t"
                "v_mfma_f32_32x32x16_bf16 %[a1], v[252:255], v[248:251], %[a1]\n\t"
                "ds_read_b64 v[220:221], %[kb] offset:2176\n\t"
                "ds_read_b64 v[222:223], %[kb] offset:2184\n\t"
                "s_waitcnt lgkmcnt(9)\n\t"
                "v_mfma_f32_32x32x16_bf16 %[a1], v[192:195], v[196:199], %[a1]\n\t"
                "ds_read_b64 v[224:225], %[kb] offset:224\n\t"
                "ds_read_b64 v[226:227], %[kb] offset:232\n\t"
                "ds_read_b128 v[228:231], %[ub] offset:4800\n\t"
                "s_waitcnt lgkmcnt(11)\n\t"
                "v_mfma_f32_32x32x16_bf16 %[a0], v[192:195], v[200:203], %[a0]\n\t"
                "ds_read_b128 v[232:235], %[ub] offset:2240\n\t"
                "s_waitcnt lgkmcnt(10)\n\t"
                "v_mfma_f32_32x32x16_bf16 %[a1], v[204:207], v[200:203], %[a1]\n\t"
                "ds_read_b64 v[236:237], %[kb] offset:2272\n\t"
                "ds_read_b64 v[238:239], %[kb] offset:2280\n\t"
                "s_waitcnt lgkmcnt(9)\n\t"
                "v_mfma_f32_32x32x16_bf16 %[a1], v[208:211], v[212:215], %[a1]\n\t"
                "ds_read_b64 v[240:241], %[kb] offset:192\n\t"
                "ds_read_b64 v[242:243], %[kb] offset:200\n\t"
                "ds_read_b128 v[244:247], %[ub] offset:4832\n\t"
                "s_waitcnt lgkmcnt(11)\n\t"
                "v_mfma_f32_32x32x16_bf16 %[a0], v[208:211], v[216:219], %[a0]\n\t"
                "ds_read_b128 v[248:251], %[ub] offset:2272\n\t"
                "s_waitcnt lgkmcnt(10)\n\t"
                "v_mfma_f32_32x32x16_bf16 %[a1], v[220:223], v[216:219], %[a1]\n\t"
                "ds_read_b64 v[252:253], %[kb] offset:2240\n\t"
                "ds_read_b64 v[254:255], %[kb] offset:2248\n\t"
                "s_waitcnt lgkmcnt(9)\n\t"
                "v_mfma_f32_32x32x16_bf16 %[a1], v[224:227], v[228:231], %[a1]\n\t"
                "ds_read_b64 v[192:193], %[kb] offset:288\n\t"
                "ds_read_b64 v[194:195], %[kb] offset:296\n\t"
                "ds_read_b128 v[196:199], %[ub] offset:4720\n\t"
                "s_waitcnt lgkmcnt(11)\n\t"
                "v_mfma_f32_32x32x16_bf16 %[a0], v[224:227], v[232:235], %[a0]\n\t"
                "ds_read_b128 v[200:203], %[ub] offset:2160\n\t"
                "s_waitcnt lgkmcnt(10)\n\t"
                "v_mfma_f32_32x32x16_bf16 %[a1], v[236:239], v[232:235], %[a1]\n\t"
                "ds_read_b64 v[204:205], %[kb] offset:2336\n\t"
                "ds_read_b64 v[206:207], %[kb] offset:2344\n\t"
                "s_waitcnt lgkmcnt(9)\n\t"
                "v_mfma_f32_32x32x16_bf16 %[a1], v[240:243], v[244:247], %[a1]\n\t"
                "ds_read_b64 v[208:209], %[kb] offset:256\n\t"
                "ds_read_b64 v[210:211], %[kb] offset:264\n\t"
                "ds_read_b128 v[212:215], %[ub] offset:4752\n\t"
                "s_waitcnt lgkmcnt(11)\n\t"
                "v_mfma_f32_32x32x16_bf16 %[a0], v[240:243], v[248:251], %[a0]\n\t"
                "ds_read_b128 v[216:219], %[ub] offset:2192\n\t"
                "s_waitcnt lgkmcnt(10)\n\t"
                "v_mfma_f32_32x32x16_bf16 %[a1], v[252:255], v[248:251], %[a1]\n\t"
                "ds_read_b64 v[220:221], %[kb] offset:2304\n\t"
                "ds_read_b64 v[222:223], %[kb] offset:2312\n\t"
                "s_waitcnt lgkmcnt(9)\n\t"
                "v_mfma_f32_32x32x16_bf16 %[a1], v[192:195], v[196:199], %[a1]\n\t"
                "ds_read_b64 v[224:225], %[kb] offset:352\n\t"
                "ds_read_b64 v[226:227], %[kb] offset:360\n\t"
                "ds_read_b128 v[228:231], %[ub] offset:4640\n\t"
                "s_waitcnt lgkmcnt(11)\n\t"
                "v_mfma_f32_32x32x16_bf16 %[a0], v[192:195], v[200:203], %[a0]\n\t"
                "ds_read_b128 v[232:235], %[ub] offset:2080\n\t"
                "s_waitcnt lgkmcnt(10)\n\t"
                "v_mfma_f32_32x32x16_bf16 %[a1], v[204:207], v[200:203], %[a1]\n\t"
                "ds_read_b64 v[236:237], %[kb] offset:2400\n\t"
                "ds_read_b64 v[238:239], %[kb] offset:2408\n\t"
                "s_waitcnt lgkmcnt(9)\n\t"
                "v_mfma_f32_32x32x16_bf16 %[a1], v[208:211], v[212:215], %[a1]\n\t"
                "ds_read_b64 v[240:241], %[kb] offset:320\n\t"
                "ds_read_b64 v[242:243], %[kb] offset:328\n\t"
                "ds_read_b128 v[244:247], %[ub] offset:4672\n\t"
                "s_waitcnt lgkmcnt(11)\n\t"
                "v_mfma_f32_32x32x16_bf16 %[a0], v[208:211], v[216:219], %[a0]\n\t"
                "ds_read_b128 v[248:251], %[ub] offset:2112\n\t"
                "s_waitcnt lgkmcnt(10)\n\t"
                "v_mfma_f32_32x32x16_bf16 %[a1], v[220:223], v[216:219], %[a1]\n\t"
                "ds_read_b64 v[252:253], %[kb] offset:2368\n\t"
                "ds_read_b64 v[254:255], %[kb] offset:2376\n\t"
                "s_waitcnt lgkmcnt(9)\n\t"
                "v_mfma_f32_32x32x16_bf16 %[a1], v[224:227], v[228:231], %[a1]\n\t"
                "ds_read_b64 v[192:193], %[kb] offset:416\n\t"
                "ds_read_b64 v[194:195], %[kb] offset:424\n\t"
                "ds_read_b128 v[196:199], %[ub] offset:4560\n\t"
                "s_waitcnt lgkmcnt(11)\n\t"
                "v_mfma_f32_32x32x16_bf16 %[a0], v[224:227], v[232:235], %[a0]\n\t"
                "ds_read_b128 v[200:203], %[ub] offset:2000\n\t"
                "s_waitcnt lgkmcnt(10)\n\t"
                "v_mfma_f32_32x32x16_bf16 %[a1], v[236:239], v[232:235], %[a1]\n\t"
                "ds_read_b64 v[204:205], %[kb] offset:2464\n\t"
                "ds_read_b64 v[206:207], %[kb] offset:2472\n\t"
                "s_waitcnt lgkmcnt(9)\n\t"
                "v_mfma_f32_32x32x16_bf16 %[a1], v[240:243], v[244:247], %[a1]\n\t"
                "ds_read_b64 v[208:209], %[kb] offset:384\n\t"
                "ds_read_b64 v[210:211], %[kb] offset:392\n\t"
                "ds_read_b128 v[212:215], %[ub] offset:4592\n\t"
                "s_waitcnt lgkmcnt(11)\n\t"
                "v_mfma_f32_32x32x16_bf16 %[a0], v[240:243], v[248:251], %[a0]\n\t"
                "ds_read_b128 v[216:219], %[ub] offset:2032\n\t"
                "s_waitcnt lgkmcnt(10)\n\t"
                "v_mfma_f32_32x32x16_bf16 %[a1], v[252:255], v[248:251], %[a1]\n\t"
                "ds_read_b64 v[220:221], %[kb] offset:2432\n\t"
                "ds_read_b64 v[222:223], %[kb] offset:2440\n\t"
                "s_waitcnt lgkmcnt(9)\n\t"
                "v_mfma_f32_32x32x16_bf16 %[a1], v[192:195], v[196:199], %[a1]\n\t"
                "ds_read_b64 v[224:225], %[kb] offset:480\n\t"
                "ds_read_b64 v[226:227], %[kb] offset:488\n\t"
                "ds_read_b128 v[228:231], %[ub] offset:4480\n\t"
                "s_waitcnt lgkmcnt(11)\n\t"
                "v_mfma_f32_32x32x16_bf16 %[a0], v[192:195], v[200:203], %[a0]\n\t"
                "ds_read_b128 v[232:235], %[ub] offset:1920\n\t"
                "s_waitcnt lgkmcnt(10)\n\t"
                "v_mfma_f32_32x32x16_bf16 %[a1], v[204:207], v[200:203], %[a1]\n\t"
                "ds_read_b64 v[236:237], %[kb] offset:2528\n\t"
                "ds_read_b64 v[238:239], %[kb] offset:2536\n\t"
                "s_waitcnt lgkmcnt(9)\n\t"
                "v_mfma_f32_32x32x16_bf16 %[a1], v[208:211], v[212:215], %[a1]\n\t"
                "ds_read_b64 v[240:241], %[kb] offset:448\n\t"
                "ds_read_b64 v[242:243], %[kb] offset:456\n\t"
                "ds_read_b128 v[244:247], %[ub] offset:4512\n\t"
                "s_waitcnt lgkmcnt(11)\n\t"
                "v_mfma_f32_32x32x16_bf16 %[a0], v[208:211], v[216:219], %[a0]\n\t"
                "ds_read_b128 v[248:251], %[ub] offset:1952\n\t"
                "s_waitcnt lgkmcnt(10)\n\t"
                "v_mfma_f32_32x32x16_bf16 %[a1], v[220:223], v[216:219], %[a1]\n\t"
                "ds_read_b64 v[252:253], %[kb] offset:2496\n\t"
                "ds_read_b64 v[254:255], %[kb] offset:2504\n\t"
                "s_waitcnt lgkmcnt(9)\n\t"
                "v_mfma_f32_32x32x16_bf16 %[a1], v[224:227], v[228:231], %[a1]\n\t"
                "ds_read_b64 v[192:193], %[kb] offset:544\n\t"
                "ds_read_b64 v[194:195], %[kb] offset:552\n\t"
                "ds_read_b128 v[196:199], %[ub] offset:4400\n\t"
                "s_waitcnt lgkmcnt(11)\n\t"
                "v_mfma_f32_32x32x16_bf16 %[a0], v[224:227], v[232:235], %[a0]\n\t"
                "ds_read_b128 v[200:203], %[ub] offset:1840\n\t"
                "s_waitcnt lgkmcnt(10)\n\t"
                "v_mfma_f32_32x32x16_bf16 %[a1], v[236:239], v[232:235], %[a1]\n\t"
                "ds_read_b64 v[204:205], %[kb] offset:2592\n\t"
                "ds_read_b64 v[206:207], %[kb] offset:2600\n\t"
                "s_waitcnt lgkmcnt(9)\n\t"
                "v_mfma_f32_32x32x16_bf16 %[a1], v[240:243], v[244:247], %[a1]\n\t"
                "ds_read_b64 v[208:209], %[kb] offset:512\n\t"
                "ds_read_b64 v[210:211], %[kb] offset:520\n\t"
                "ds_read_b128 v[212:215], %[ub] offset:4432\n\t"
                "s_waitcnt lgkmcnt(11)\n\t"
                "v_mfma_f32_32x32x16_bf16 %[a0], v[240:243], v[248:251], %[a0]\n\t"
                "ds_read_b128 v[216:219], %[ub] offset:1872\n\t"
                "s_waitcnt lgkmcnt(10)\n\t"
                "v_mfma_f32_32x32x16_bf16 %[a1], v[252:255], v[248:251], %[a1]\n\t"
                "ds_read_b64 v[220:221], %[kb] offset:2560\n\t"
                "ds_read_b64 v[222:223], %[kb] offset:2568\n\t"
                "s_waitcnt lgkmcnt(9)\n\t"
                "v_mfma_f32_32x32x16_bf16 %[a1], v[192:195], v[196:199], %[a1]\n\t"
                "ds_read_b64 v[224:225], %[kb] offset:608\n\t"
                "ds_read_b64 v[226:227], %[kb] offset:616\n\t"
                "ds_read_b128 v[228:231], %[ub] offset:4320\n\t"
                "s_waitcnt lgkmcnt(11)\n\t"
                "v_mfma_f32_32x32x16_bf16 %[a0], v[192:195], v[200:203], %[a0]\n\t"
                "ds_read_b128 v[232:235], %[ub] offset:1760\n\t"
                "s_waitcnt lgkmcnt(10)\n\t"
                "v_mfma_f32_32x32x16_bf16 %[a1], v[204:207], v[200:203], %[a1]\n\t"
                "ds_read_b64 v[236:237], %[kb] offset:2656\n\t"
                "ds_read_b64 v[238:239], %[kb] offset:2664\n\t"
                "s_waitcnt lgkmcnt(9)\n\t"
                "v_mfma_f32_32x32x16_bf16 %[a1], v[208:211], v[212:215], %[a1]\n\t"
                "ds_read_b64 v[240:241], %[kb] offset:576\n\t"
                "ds_read_b64 v[242:243], %[kb] offset:584\n\t"
                "ds_read_b128 v[244:247], %[ub] offset:4352\n\t"
                "s_waitcnt lgkmcnt(11)\n\t"
                "v_mfma_f32_32x32x16_bf16 %[a0], v[208:211], v[216:219], %[a0]\n\t"
                "ds_read_b128 v[248:251], %[ub] offset:1792\n\t"
                "s_waitcnt lgkmcnt(10)\n\t"
                "v_mfma_f32_32x32x16_bf16 %[a1], v[220:223], v[216:219], %[a1]\n\t"
                "ds_read_b64 v[252:253], %[kb] offset:2624\n\t"
                "ds_read_b64 v[254:255], %[kb] offset:2632\n\t"
                "s_waitcnt lgkmcnt(9)\n\t"
                "v_mfma_f32_32x32x16_bf16 %[a1], v[224:227], v[228:231], %[a1]\n\t"
                "ds_read_b64 v[192:193], %[kb] offset:672\n\t"
                "ds_read_b64 v[194:195], %[kb] offset:680\n\t"
                "ds_read_b128 v[196:199], %[ub] offset:4240\n\t"
                "s_waitcnt lgkmcnt(11)\n\t"
                "v_mfma_f32_32x32x16_bf16 %[a0], v[224:227], v[232:235], %[a0]\n\t"
                "ds_read_b128 v[200:203], %[ub] offset:1680\n\t"
                "s_waitcnt lgkmcnt(10)\n\t"
                "v_mfma_f32_32x32x16_bf16 %[a1], v[236:239], v[232:235], %[a1]\n\t"
                "ds_read_b64 v[204:205], %[kb] offset:2720\n\t"
                "ds_read_b64 v[206:207], %[kb] offset:2728\n\t"
                "s_waitcnt lgkmcnt(9)\n\t"
                "v_mfma_f32_32x32x16_bf16 %[a1], v[240:243], v[244:247], %[a1]\n\t"
                "ds_read_b64 v[208:209], %[kb] offset:640\n\t"
                "ds_read_b64 v[210:211], %[kb] offset:648\n\t"
                "ds_read_b128 v[212:215], %[ub] offset:4272\n\t"
                "s_waitcnt lgkmcnt(11)\n\t"
                "v_mfma_f32_32x32x16_bf16 %[a0], v[240:243], v[248:251], %[a0]\n\t"
                "ds_read_b128 v[216:219], %[ub] offset:1712\n\t"
                "s_waitcnt lgkmcnt(10)\n\t"
                "v_mfma_f32_32x32x16_bf16 %[a1], v[252:255], v[248:251], %[a1]\n\t"
                "ds_read_b64 v[220:221], %[kb] offset:2688\n\t"
                "ds_read_b64 v[222:223], %[kb] offset:2696\n\t"
                "s_waitcnt lgkmcnt(9)\n\t"
                "v_mfma_f32_32x32x16_bf16 %[a1], v[192:195], v[196:199], %[a1]\n\t"
                "ds_read_b64 v[224:225], %[kb] offset:736\n\t"
                "ds_read_b64 v[226:227], %[kb] offset:744\n\t"
                "ds_read_b128 v[228:231], %[ub] offset:4160\n\t"
                "s_waitcnt lgkmcnt(11)\n\t"
                "v_mfma_f32_32x32x16_bf16 %[a0], v[192:195], v[200:203], %[a0]\n\t"
                "ds_read_b128 v[232:235], %[ub] offset:1600\n\t"
                "s_waitcnt lgkmcnt(10)\n\t"
                "v_mfma_f32_32x32x16_bf16 %[a1], v[204:207], v[200:203], %[a1]\n\t"
                "ds_read_b64 v[236:237], %[kb] offset:2784\n\t"
                "ds_read_b64 v[238:239], %[kb] offset:2792\n\t"
                "s_waitcnt lgkmcnt(9)\n\t"
                "v_mfma_f32_32x32x16_bf16 %[a1], v[208:211], v[212:215], %[a1]\n\t"
                "ds_read_b64 v[240:241], %[kb] offset:704\n\t"
                "ds_read_b64 v[242:243], %[kb] offset:712\n\t"
                "ds_read_b128 v[244:247], %[ub] offset:4192\n\t"
                "s_waitcnt lgkmcnt(11)\n\t"
                "v_mfma_f32_32x32x16_bf16 %[a0], v[208:211], v[216:219], %[a0]\n\t"
                "ds_read_b128 v[248:251], %[ub] offset:1632\n\t"
                "s_waitcnt lgkmcnt(10)\n\t"
                "v_mfma_f32_32x32x16_bf16 %[a1], v[220:223], v[216:219], %[a1]\n\t"
                "ds_read_b64 v[252:253], %[kb] offset:2752\n\t"
                "ds_read_b64 v[254:255], %[kb] offset:2760\n\t"
                "s_waitcnt lgkmcnt(9)\n\t"
                "v_mfma_f32_32x32x16_bf16 %[a1], v[224:227], v[228:231], %[a1]\n\t"
                "ds_read_b64 v[192:193], %[kb] offset:800\n\t"
                "ds_read_b64 v[194:195], %[kb] offset:808\n\t"
                "ds_read_b128 v[196:199], %[ub] offset:4080\n\t"
                "s_waitcnt lgkmcnt(11)\n\t"
                "v_mfma_f32_32x32x16_bf16 %[a0], v[224:227], v[232:235], %[a0]\n\t"
                "ds_read_b128 v[200:203], %[ub] offset:1520\n\t"
                "s_waitcnt lgkmcnt(10)\n\t"
                "v_mfma_f32_32x32x16_bf16 %[a1], v[236:239], v[232:235], %[a1]\n\t"
                "ds_read_b64 v[204:205], %[kb] offset:2848\n\t"
                "ds_read_b64 v[206:207], %[kb] offset:2856\n\t"
                "s_waitcnt lgkmcnt(9)\n\t"
                "v_mfma_f32_32x32x16_bf16 %[a1], v[240:243], v[244:247], %[a1]\n\t"
                "ds_read_b64 v[208:209], %[kb] offset:768\n\t"
                "ds_read_b64 v[210:211], %[kb] offset:776\n\t"
                "ds_read_b128 v[212:215], %[ub] offset:4112\n\t"
                "s_waitcnt lgkmcnt(11)\n\t"
                "v_mfma_f32_32x32x16_bf16 %[a0], v[240:243], v[248:251], %[a0]\n\t"
                "ds_read_b128 v[216:219], %[ub] offset:1552\n\t"
                "s_waitcnt lgkmcnt(10)\n\t"
                "v_mfma_f32_32x32x16_bf16 %[a1], v[252:255], v[248:251], %[a1]\n\t"
                "ds_read_b64 v[220:221], %[kb] offset:2816\n\t"
                "ds_read_b64 v[222:223], %[kb] offset:2824\n\t"
                "s_waitcnt lgkmcnt(9)\n\t"
                "v_mfma_f32_32x32x16_bf16 %[a1], v[192:195], v[196:199], %[a1]\n\t"
                "ds_read_b64 v[224:225], %[kb] offset:864\n\t"
                "ds_read_b64 v[226:227], %[kb] offset:872\n\t"
                "ds_read_b128 v[228:231], %[ub] offset:4000\n\t"
                "s_waitcnt lgkmcnt(11)\n\t"
                "v_mfma_f32_32x32x16_bf16 %[a0], v[192:195], v[200:203], %[a0]\n\t"
                "ds_read_b128 v[232:235], %[ub] offset:1440\n\t"
                "s_waitcnt lgkmcnt(10)\n\t"
                "v_mfma_f32_32x32x16_bf16 %[a1], v[204:207], v[200:203], %[a1]\n\t"
                "ds_read_b64 v[236:237], %[kb] offset:2912\n\t"
                "ds_read_b64 v[238:239], %[kb] offset:2920\n\t"
                "s_waitcnt lgkmcnt(9)\n\t"
                "v_mfma_f32_32x32x16_bf16 %[a1], v[208:211], v[212:215], %[a1]\n\t"
                "ds_read_b64 v[240:241], %[kb] offset:832\n\t"
                "ds_read_b64 v[242:243], %[kb] offset:840\n\t"
                "ds_read_b128 v[244:247], %[ub] offset:4032\n\t"
                "s_waitcnt lgkmcnt(11)\n\t"
                "v_mfma_f32_32x32x16_bf16 %[a0], v[208:211], v[216:219], %[a0]\n\t"
                "ds_read_b128 v[248:251], %[ub] offset:1472\n\t"
                "s_waitcnt lgkmcnt(10)\n\t"
                "v_mfma_f32_32x32x16_bf16 %[a1], v[220:223], v[216:219], %[a1]\n\t"
                "ds_read_b64 v[252:253], %[kb] offset:2880\n\t"
                "ds_read_b64 v[254:255], %[kb] offset:2888\n\t"
                "s_waitcnt lgkmcnt(9)\n\t"
                "v_mfma_f32_32x32x16_bf16 %[a1], v[224:227], v[228:231], %[a1]\n\t"
                "ds_read_b64 v[192:193], %[kb] offset:928\n\t"
                "ds_read_b64 v[194:195], %[kb] offset:936\n\t"
                "ds_read_b128 v[196:199], %[ub] offset:3920\n\t"
                "s_waitcnt lgkmcnt(11)\n\t"
                "v_mfma_f32_32x32x16_bf16 %[a0], v[224:227], v[232:235], %[a0]\n\t"
                "ds_read_b128 v[200:203], %[ub] offset:1360\n\t"
                "s_waitcnt lgkmcnt(10)\n\t"
                "v_mfma_f32_32x32x16_bf16 %[a1], v[236:239], v[232:235], %[a1]\n\t"
                "ds_read_b64 v[204:205], %[kb] offset:2976\n\t"
                "ds_read_b64 v[206:207], %[kb] offset:2984\n\t"
                "s_waitcnt lgkmcnt(9)\n\t"
                "v_mfma_f32_32x32x16_bf16 %[a1], v[240:243], v[244:247], %[a1]\n\t"
                "ds_read_b64 v[208:209], %[kb] offset:896\n\t"
                "ds_read_b64 v[210:211], %[kb] offset:904\n\t"
                "ds_read_b128 v[212:215], %[ub] offset:3952\n\t"
                "s_waitcnt lgkmcnt(11)\n\t"
                "v_mfma_f32_32x32x16_bf16 %[a0], v[240:243], v[248:251], %[a0]\n\t"
                "ds_read_b128 v[216:219], %[ub] offset:1392\n\t"
                "s_waitcnt lgkmcnt(10)\n\t"
                "v_mfma_f32_32x32x16_bf16 %[a1], v[252:255], v[248:251], %[a1]\n\t"
                "ds_read_b64 v[220:221], %[kb] offset:2944\n\t"
                "ds_read_b64 v[222:223], %[kb] offset:2952\n\t"
                "s_waitcnt lgkmcnt(9)\n\t"
                "v_mfma_f32_32x32x16_bf16 %[a1], v[192:195], v[196:199], %[a1]\n\t"
                "ds_read_b64 v[224:225], %[kb] offset:992\n\t"
                "ds_read_b64 v[226:227], %[kb] offset:1000\n\t"
                "ds_read_b128 v[228:231], %[ub] offset:3840\n\t"
                "s_waitcnt lgkmcnt(11)\n\t"
                "v_mfma_f32_32x32x16_bf16 %[a0], v[192:195], v[200:203], %[a0]\n\t"
                "ds_read_b128 v[232:235], %[ub] offset:1280\n\t"
                "s_waitcnt lgkmcnt(10)\n\t"
                "v_mfma_f32_32x32x16_bf16 %[a1], v[204:207], v[200:203], %[a1]\n\t"
                "ds_read_b64 v[236:237], %[kb] offset:3040\n\t"
                "ds_read_b64 v[238:239], %[kb] offset:3048\n\t"
                "s_waitcnt lgkmcnt(9)\n\t"
                "v_mfma_f32_32x32x16_bf16 %[a1], v[208:211], v[212:215], %[a1]\n\t"
                "ds_read_b64 v[240:241], %[kb] offset:960\n\t"
                "ds_read_b64 v[242:243], %[kb] offset:968\n\t"
                "ds_read_b128 v[244:247], %[ub] offset:3872\n\t"
                "s_waitcnt lgkmcnt(11)\n\t"
                "v_mfma_f32_32x32x16_bf16 %[a0], v[208:211], v[216:219], %[a0]\n\t"
                "ds_read_b128 v[248:251], %[ub] offset:1312\n\t"
                "s_waitcnt lgkmcnt(10)\n\t"
                "v_mfma_f32_32x32x16_bf16 %[a1], v[220:223], v[216:219], %[a1]\n\t"
                "ds_read_b64 v[252:253], %[kb] offset:3008\n\t"
                "ds_read_b64 v[254:255], %[kb] offset:3016\n\t"
                "s_waitcnt lgkmcnt(9)\n\t"
                "v_mfma_f32_32x32x16_bf16 %[a1], v[224:227], v[228:231], %[a1]\n\t"
                "ds_read_b64 v[192:193], %[kb] offset:1056\n\t"
                "ds_read_b64 v[194:195], %[kb] offset:1064\n\t"
                "ds_read_b128 v[196:199], %[ub] offset:3760\n\t"
                "s_waitcnt lgkmcnt(11)\n\t"
                "v_mfma_f32_32x32x16_bf16 %[a0], v[224:227], v[232:235], %[a0]\n\t"
                "ds_read_b128 v[200:203], %[ub] offset:1200\n\t"
                "s_waitcnt lgkmcnt(10)\n\t"
                "v_mfma_f32_32x32x16_bf16 %[a1], v[236:239], v[232:235], %[a1]\n\t"
                "ds_read_b64 v[204:205], %[kb] offset:3104\n\t"
                "ds_read_b64 v[206:207], %[kb] offset:3112\n\t"
                "s_waitcnt lgkmcnt(9)\n\t"
                "v_mfma_f32_32x32x16_bf16 %[a1], v[240:243], v[244:247], %[a1]\n\t"
                "ds_read_b64 v[208:209], %[kb] offset:1024\n\t"
                "ds_read_b64 v[210:211], %[kb] offset:1032\n\t"
                "ds_read_b128 v[212:215], %[ub] offset:3792\n\t"
                "s_waitcnt lgkmcnt(11)\n\t"
                "v_mfma_f32_32x32x16_bf16 %[a0], v[240:243], v[248:251], %[a0]\n\t"
                "ds_read_b128 v[216:219], %[ub] offset:1232\n\t"
                "s_waitcnt lgkmcnt(10)\n\t"
                "v_mfma_f32_32x32x16_bf16 %[a1], v[252:255], v[248:251], %[a1]\n\t"
                "ds_read_b64 v[220:221], %[kb] offset:3072\n\t"
                "ds_read_b64 v[222:223], %[kb] offset:3080\n\t"
                "s_waitcnt lgkmcnt(9)\n\t"
                "v_mfma_f32_32x32x16_bf16 %[a1], v[192:195], v[196:199], %[a1]\n\t"
                "ds_read_b64 v[224:225], %[kb] offset:1120\n\t"
                "ds_read_b64 v[226:227], %[kb] offset:1128\n\t"
                "ds_read_b128 v[228:231], %[ub] offset:3680\n\t"
                "s_waitcnt lgkmcnt(11)\n\t"
                "v_mfma_f32_32x32x16_bf16 %[a0], v[192:195], v[200:203], %[a0]\n\t"
                "ds_read_b128 v[232:235], %[ub] offset:1120\n\t"
                "s_waitcnt lgkmcnt(10)\n\t"
                "v_mfma_f32_32x32x16_bf16 %[a1], v[204:207], v[200:203], %[a1]\n\t"
                "ds_read_b64 v[236:237], %[kb] offset:3168\n\t"
                "ds_read_b64 v[238:239], %[kb] offset:3176\n\t"
                "s_waitcnt lgkmcnt(9)\n\t"
                "v_mfma_f32_32x32x16_bf16 %[a1], v[208:211], v[212:215], %[a1]\n\t"
                "ds_read_b64 v[240:241], %[kb] offset:1088\n\t"
                "ds_read_b64 v[242:243], %[kb] offset:1096\n\t"
                "ds_read_b128 v[244:247], %[ub] offset:3712\n\t"
                "s_waitcnt lgkmcnt(11)\n\t"
                "v_mfma_f32_32x32x16_bf16 %[a0], v[208:211], v[216:219], %[a0]\n\t"
                "ds_read_b128 v[248:251], %[ub] offset:1152\n\t"
                "s_waitcnt lgkmcnt(10)\n\t"
                "v_mfma_f32_32x32x16_bf16 %[a1], v[220:223], v[216:219], %[a1]\n\t"
                "ds_read_b64 v[252:253], %[kb] offset:3136\n\t"
                "ds_read_b64 v[254:255], %[kb] offset:3144\n\t"
                "s_waitcnt lgkmcnt(9)\n\t"
                "v_mfma_f32_32x32x16_bf16 %[a1], v[224:227], v[228:231], %[a1]\n\t"
                "ds_read_b64 v[192:193], %[kb] offset:1184\n\t"
                "ds_read_b64 v[194:195], %[kb] offset:1192\n\t"
                "ds_read_b128 v[196:199], %[ub] offset:3600\n\t"
                "s_waitcnt lgkmcnt(11)\n\t"
                "v_mfma_f32_32x32x16_bf16 %[a0], v[224:227], v[232:235], %[a0]\n\t"
                "ds_read_b128 v[200:203], %[ub] offset:1040\n\t"
                "s_waitcnt lgkmcnt(10)\n\t"
                "v_mfma_f32_32x32x16_bf16 %[a1], v[236:239], v[232:235], %[a1]\n\t"
                "ds_read_b64 v[204:205], %[kb] offset:3232\n\t"
                "ds_read_b64 v[206:207], %[kb] offset:3240\n\t"
                "s_waitcnt lgkmcnt(9)\n\t"
                "v_mfma_f32_32x32x16_bf16 %[a1], v[240:243], v[244:247], %[a1]\n\t"
                "ds_read_b64 v[208:209], %[kb] offset:1152\n\t"
                "ds_read_b64 v[210:211], %[kb] offset:1160\n\t"
                "ds_read_b128 v[212:215], %[ub] offset:3632\n\t"
                "s_waitcnt lgkmcnt(11)\n\t"
                "v_mfma_f32_32x32x16_bf16 %[a0], v[240:243], v[248:251], %[a0]\n\t"
                "ds_read_b128 v[216:219], %[ub] offset:1072\n\t"
                "s_waitcnt lgkmcnt(10)\n\t"
                "v_mfma_f32_32x32x16_bf16 %[a1], v[252:255], v[248:251], %[a1]\n\t"
                "ds_read_b64 v[220:221], %[kb] offset:3200\n\t"
                "ds_read_b64 v[222:223], %[kb] offset:3208\n\t"
                "s_waitcnt lgkmcnt(9)\n\t"
                "v_mfma_f32_32x32x16_bf16 %[a1], v[192:195], v[196:199], %[a1]\n\t"
                "ds_read_b64 v[224:225], %[kb] offset:1248\n\t"
                "ds_read_b64 v[226:227], %[kb] offset:1256\n\t"
                "ds_read_b128 v[228:231], %[ub] offset:3520\n\t"
                "s_waitcnt lgkmcnt(11)\n\t"
                "v_mfma_f32_32x32x16_bf16 %[a0], v[192:195], v[200:203], %[a0]\n\t"
                "ds_read_b128 v[232:235], %[ub] offset:960\n\t"
                "s_waitcnt lgkmcnt(10)\n\t"
                "v_mfma_f32_32x32x16_bf16 %[a1], v[204:207], v[200:203], %[a1]\n\t"
                "ds_read_b64 v[236:237], %[kb] offset:3296\n\t"
                "ds_read_b64 v[238:239], %[kb] offset:3304\n\t"
                "s_waitcnt lgkmcnt(9)\n\t"
                "v_mfma_f32_32x32x16_bf16 %[a1], v[208:211], v[212:215], %[a1]\n\t"
                "ds_read_b64 v[240:241], %[kb] offset:1216\n\t"
                "ds_read_b64 v[242:243], %[kb] offset:1224\n\t"
                "ds_read_b128 v[244:247], %[ub] offset:3552\n\t"
                "s_waitcnt lgkmcnt(11)\n\t"
                "v_mfma_f32_32x32x16_bf16 %[a0], v[208:211], v[216:219], %[a0]\n\t"
                "ds_read_b128 v[248:251], %[ub] offset:992\n\t"
                "s_waitcnt lgkmcnt(10)\n\t"
                "v_mfma_f32_32x32x16_bf16 %[a1], v[220:223], v[216:219], %[a1]\n\t"
                "ds_read_b64 v[252:253], %[kb] offset:3264\n\t"
                "ds_read_b64 v[254:255], %[kb] offset:3272\n\t"
                "s_waitcnt lgkmcnt(9)\n\t"
                "v_mfma_f32_32x32x16_bf16 %[a1], v[224:227], v[228:231], %[a1]\n\t"
                "ds_read_b64 v[192:193], %[kb] offset:1312\n\t"
                "ds_read_b64 v[194:195], %[kb] offset:1320\n\t"
                "ds_read_b128 v[196:199], %[ub] offset:3440\n\t"
                "s_waitcnt lgkmcnt(11)\n\t"
                "v_mfma_f32_32x32x16_bf16 %[a0], v[224:227], v[232:235], %[a0]\n\t"
                "ds_read_b128 v[200:203], %[ub] offset:880\n\t"
                "s_waitcnt lgkmcnt(10)\n\t"
                "v_mfma_f32_32x32x16_bf16 %[a1], v[236:239], v[232:235], %[a1]\n\t"
                "ds_read_b64 v[204:205], %[kb] offset:3360\n\t"
                "ds_read_b64 v[206:207], %[kb] offset:3368\n\t"
                "s_waitcnt lgkmcnt(9)\n\t"
                "v_mfma_f32_32x32x16_bf16 %[a1], v[240:243], v[244:247], %[a1]\n\t"
                "ds_read_b64 v[208:209], %[kb] offset:1280\n\t"
                "ds_read_b64 v[210:211], %[kb] offset:1288\n\t"
                "ds_read_b128 v[212:215], %[ub] offset:3472\n\t"
                "s_waitcnt lgkmcnt(11)\n\t"
                "v_mfma_f32_32x32x16_bf16 %[a0], v[240:243], v[248:251], %[a0]\n\t"
                "ds_read_b128 v[216:219], %[ub] offset:912\n\t"
                "s_waitcnt lgkmcnt(10)\n\t"
                "v_mfma_f32_32x32x16_bf16 %[a1], v[252:255], v[248:251], %[a1]\n\t"
                "ds_read_b64 v[220:221], %[kb] offset:3328\n\t"
                "ds_read_b64 v[222:223], %[kb] offset:3336\n\t"
                "s_waitcnt lgkmcnt(9)\n\t"
                "v_mfma_f32_32x32x16_bf16 %[a1], v[192:195], v[196:199], %[a1]\n\t"
                "ds_read_b64 v[224:225], %[kb] offset:1376\n\t"
                "ds_read_b64 v[226:227], %[kb] offset:1384\n\t"
                "ds_read_b128 v[228:231], %[ub] offset:3360\n\t"
                "s_waitcnt lgkmcnt(11)\n\t"
                "v_mfma_f32_32x32x16_bf16 %[a0], v[192:195], v[200:203], %[a0]\n\t"
                "ds_read_b128 v[232:235], %[ub] offset:800\n\t"
                "s_waitcnt lgkmcnt(10)\n\t"
                "v_mfma_f32_32x32x16_bf16 %[a1], v[204:207], v[200:203], %[a1]\n\t"
                "ds_read_b64 v[236:237], %[kb] offset:3424\n\t"
                "ds_read_b64 v[238:239], %[kb] offset:3432\n\t"
                "s_waitcnt lgkmcnt(9)\n\t"
                "v_mfma_f32_32x32x16_bf16 %[a1], v[208:211], v[212:215], %[a1]\n\t"
                "ds_read_b64 v[240:241], %[kb] offset:1344\n\t"
                "ds_read_b64 v[242:243], %[kb] offset:1352\n\t"
                "ds_read_b128 v[244:247], %[ub] offset:3392\n\t"
                "s_waitcnt lgkmcnt(11)\n\t"
                "v_mfma_f32_32x32x16_bf16 %[a0], v[208:211], v[216:219], %[a0]\n\t"
                "ds_read_b128 v[248:251], %[ub] offset:832\n\t"
                "s_waitcnt lgkmcnt(10)\n\t"
                "v_mfma_f32_32x32x16_bf16 %[a1], v[220:223], v[216:219], %[a1]\n\t"
                "ds_read_b64 v[252:253], %[kb] offset:3392\n\t"
                "ds_read_b64 v[254:255], %[kb] offset:3400\n\t"
                "s_waitcnt lgkmcnt(9)\n\t"
                "v_mfma_f32_32x32x16_bf16 %[a1], v[224:227], v[228:231], %[a1]\n\t"
                "ds_read_b64 v[192:193], %[kb] offset:1440\n\t"
                "ds_read_b64 v[194:195], %[kb] offset:1448\n\t"
                "ds_read_b128 v[196:199], %[ub] offset:3280\n\t"
                "s_waitcnt lgkmcnt(11)\n\t"
                "v_mfma_f32_32x32x16_bf16 %[a0], v[224:227], v[232:235], %[a0]\n\t"
                "ds_read_b128 v[200:203], %[ub] offset:720\n\t"
                "s_waitcnt lgkmcnt(10)\n\t"
                "v_mfma_f32_32x32x16_bf16 %[a1], v[236:239], v[232:235], %[a1]\n\t"
                "ds_read_b64 v[204:205], %[kb] offset:3488\n\t"
                "ds_read_b64 v[206:207], %[kb] offset:3496\n\t"
                "s_waitcnt lgkmcnt(9)\n\t"
                "v_mfma_f32_32x32x16_bf16 %[a1], v[240:243], v[244:247], %[a1]\n\t"
                "ds_read_b64 v[208:209], %[kb] offset:1408\n\t"
                "ds_read_b64 v[210:211], %[kb] offset:1416\n\t"
                "ds_read_b128 v[212:215], %[ub] offset:3312\n\t"
                "s_waitcnt lgkmcnt(11)\n\t"
                "v_mfma_f32_32x32x16_bf16 %[a0], v[240:243], v[248:251], %[a0]\n\t"
                "ds_read_b128 v[216:219], %[ub] offset:752\n\t"
                "s_waitcnt lgkmcnt(10)\n\t"
                "v_mfma_f32_32x32x16_bf16 %[a1], v[252:255], v[248:251], %[a1]\n\t"
                "ds_read_b64 v[220:221], %[kb] offset:3456\n\t"
                "ds_read_b64 v[222:223], %[kb] offset:3464\n\t"
                "s_waitcnt lgkmcnt(9)\n\t"
                "v_mfma_f32_32x32x16_bf16 %[a1], v[192:195], v[196:199], %[a1]\n\t"
                "ds_read_b64 v[224:225], %[kb] offset:1504\n\t"
                "ds_read_b64 v[226:227], %[kb] offset:1512\n\t"
                "ds_read_b128 v[228:231], %[ub] offset:3200\n\t"
                "s_waitcnt lgkmcnt(11)\n\t"
                "v_mfma_f32_32x32x16_bf16 %[a0], v[192:195], v[200:203], %[a0]\n\t"
                "ds_read_b128 v[232:235], %[ub] offset:640\n\t"
                "s_waitcnt lgkmcnt(10)\n\t"
                "v_mfma_f32_32x32x16_bf16 %[a1], v[204:207], v[200:203], %[a1]\n\t"
                "ds_read_b64 v[236:237], %[kb] offset:3552\n\t"
                "ds_read_b64 v[238:239], %[kb] offset:3560\n\t"
                "s_waitcnt lgkmcnt(9)\n\t"
                "v_mfma_f32_32x32x16_bf16 %[a1], v[208:211], v[212:215], %[a1]\n\t"
                "ds_read_b64 v[240:241], %[kb] offset:1472\n\t"
                "ds_read_b64 v[242:243], %[kb] offset:1480\n\t"
                "ds_read_b128 v[244:247], %[ub] offset:3232\n\t"
                "s_waitcnt lgkmcnt(11)\n\t"
                "v_mfma_f32_32x32x16_bf16 %[a0], v[208:211], v[216:219], %[a0]\n\t"
                "ds_read_b128 v[248:251], %[ub] offset:672\n\t"
                "s_waitcnt lgkmcnt(10)\n\t"
                "v_mfma_f32_32x32x16_bf16 %[a1], v[220:223], v[216:219], %[a1]\n\t"
                "ds_read_b64 v[252:253], %[kb] offset:3520\n\t"
                "ds_read_b64 v[254:255], %[kb] offset:3528\n\t"
                "s_waitcnt lgkmcnt(9)\n\t"
                "v_mfma_f32_32x32x16_bf16 %[a1], v[224:227], v[228:231], %[a1]\n\t"
                "ds_read_b64 v[192:193], %[kb] offset:1568\n\t"
                "ds_read_b64 v[194:195], %[kb] offset:1576\n\t"
                "ds_read_b128 v[196:199], %[ub] offset:3120\n\t"
                "s_waitcnt lgkmcnt(11)\n\t"
                "v_mfma_f32_32x32x16_bf16 %[a0], v[224:227], v[232:235], %[a0]\n\t"
                "ds_read_b128 v[200:203], %[ub] offset:560\n\t"
                "s_waitcnt lgkmcnt(10)\n\t"
                "v_mfma_f32_32x32x16_bf16 %[a1], v[236:239], v[232:235], %[a1]\n\t"
                "ds_read_b64 v[204:205], %[kb] offset:3616\n\t"
                "ds_read_b64 v[206:207], %[kb] offset:3624\n\t"
                "s_waitcnt lgkmcnt(9)\n\t"
                "v_mfma_f32_32x32x16_bf16 %[a1], v[240:243], v[244:247], %[a1]\n\t"
                "ds_read_b64 v[208:209], %[kb] offset:1536\n\t"
                "ds_read_b64 v[210:211], %[kb] offset:1544\n\t"
                "ds_read_b128 v[212:215], %[ub] offset:3152\n\t"
                "s_waitcnt lgkmcnt(11)\n\t"
                "v_mfma_f32_32x32x16_bf16 %[a0], v[240:243], v[248:251], %[a0]\n\t"
                "ds_read_b128 v[216:219], %[ub] offset:592\n\t"
                "s_waitcnt lgkmcnt(10)\n\t"
                "v_mfma_f32_32x32x16_bf16 %[a1], v[252:255], v[248:251], %[a1]\n\t"
                "ds_read_b64 v[220:221], %[kb] offset:3584\n\t"
                "ds_read_b64 v[222:223], %[kb] offset:3592\n\t"
                "s_waitcnt lgkmcnt(9)\n\t"
                "v_mfma_f32_32x32x16_bf16 %[a1], v[192:195], v[196:199], %[a1]\n\t"
                "ds_read_b64 v[224:225], %[kb] offset:1632\n\t"
                "ds_read_b64 v[226:227], %[kb] offset:1640\n\t"
                "ds_read_b128 v[228:231], %[ub] offset:3040\n\t"
                "s_waitcnt lgkmcnt(11)\n\t"
                "v_mfma_f32_32x32x16_bf16 %[a0], v[192:195], v[200:203], %[a0]\n\t"
                "ds_read_b128 v[232:235], %[ub] offset:480\n\t"
                "s_waitcnt lgkmcnt(10)\n\t"
                "v_mfma_f32_32x32x16_bf16 %[a1], v[204:207], v[200:203], %[a1]\n\t"
                "ds_read_b64 v[236:237], %[kb] offset:3680\n\t"
                "ds_read_b64 v[238:239], %[kb] offset:3688\n\t"
                "s_waitcnt lgkmcnt(9)\n\t"
                "v_mfma_f32_32x32x16_bf16 %[a1], v[208:211], v[212:215], %[a1]\n\t"
                "ds_read_b64 v[240:241], %[kb] offset:1600\n\t"
                "ds_read_b64 v[242:243], %[kb] offset:1608\n\t"
                "ds_read_b128 v[244:247], %[ub] offset:3072\n\t"
                "s_waitcnt lgkmcnt(11)\n\t"
                "v_mfma_f32_32x32x16_bf16 %[a0], v[208:211], v[216:219], %[a0]\n\t"
                "ds_read_b128 v[248:251], %[ub] offset:512\n\t"
                "s_waitcnt lgkmcnt(10)\n\t"
                "v_mfma_f32_32x32x16_bf16 %[a1], v[220:223], v[216:219], %[a1]\n\t"
                "ds_read_b64 v[252:253], %[kb] offset:3648\n\t"
                "ds_read_b64 v[254:255], %[kb] offset:3656\n\t"
                "s_waitcnt lgkmcnt(9)\n\t"
                "v_mfma_f32_32x32x16_bf16 %[a1], v[224:227], v[228:231], %[a1]\n\t"
                "ds_read_b64 v[192:193], %[kb] offset:1696\n\t"
                "ds_read_b64 v[194:195], %[kb] offset:1704\n\t"
                "ds_read_b128 v[196:199], %[ub] offset:2960\n\t"
                "s_waitcnt lgkmcnt(11)\n\t"
                "v_mfma_f32_32x32x16_bf16 %[a0], v[224:227], v[232:235], %[a0]\n\t"
                "ds_read_b128 v[200:203], %[ub] offset:400\n\t"
                "s_waitcnt lgkmcnt(10)\n\t"
                "v_mfma_f32_32x32x16_bf16 %[a1], v[236:239], v[232:235], %[a1]\n\t"
                "ds_read_b64 v[204:205], %[kb] offset:3744\n\t"
                "ds_read_b64 v[206:207], %[kb] offset:3752\n\t"
                "s_waitcnt lgkmcnt(9)\n\t"
                "v_mfma_f32_32x32x16_bf16 %[a1], v[240:243], v[244:247], %[a1]\n\t"
                "ds_read_b64 v[208:209], %[kb] offset:1664\n\t"
                "ds_read_b64 v[210:211], %[kb] offset:1672\n\t"
                "ds_read_b128 v[212:215], %[ub] offset:2992\n\t"
                "s_waitcnt lgkmcnt(11)\n\t"
                "v_mfma_f32_32x32x16_bf16 %[a0], v[240:243], v[248:251], %[a0]\n\t"
                "ds_read_b128 v[216:219], %[ub] offset:432\n\t"
                "s_waitcnt lgkmcnt(10)\n\t"
                "v_mfma_f32_32x32x16_bf16 %[a1], v[252:255], v[248:251], %[a1]\n\t"
                "ds_read_b64 v[220:221], %[kb] offset:3712\n\t"
                "ds_read_b64 v[222:223], %[kb] offset:3720\n\t"
                "s_waitcnt lgkmcnt(9)\n\t"
                "v_mfma_f32_32x32x16_bf16 %[a1], v[192:195], v[196:199], %[a1]\n\t"
                "ds_read_b64 v[224:225], %[kb] offset:1760\n\t"
                "ds_read_b64 v[226:227], %[kb] offset:1768\n\t"
                "ds_read_b128 v[228:231], %[ub] offset:2880\n\t"
                "s_waitcnt lgkmcnt(11)\n\t"
                "v_mfma_f32_32x32x16_bf16 %[a0], v[192:195], v[200:203], %[a0]\n\t"
                "ds_read_b128 v[232:235], %[ub] offset:320\n\t"
                "s_waitcnt lgkmcnt(10)\n\t"
                "v_mfma_f32_32x32x16_bf16 %[a1], v[204:207], v[200:203], %[a1]\n\t"
                "ds_read_b64 v[236:237], %[kb] offset:3808\n\t"
                "ds_read_b64 v[238:239], %[kb] offset:3816\n\t"
                "s_waitcnt lgkmcnt(9)\n\t"
                "v_mfma_f32_32x32x16_bf16 %[a1], v[208:211], v[212:215], %[a1]\n\t"
                "ds_read_b64 v[240:241], %[kb] offset:1728\n\t"
                "ds_read_b64 v[242:243], %[kb] offset:1736\n\t"
                "ds_read_b128 v[244:247], %[ub] offset:2912\n\t"
                "s_waitcnt lgkmcnt(11)\n\t"
                "v_mfma_f32_32x32x16_bf16 %[a0], v[208:211], v[216:219], %[a0]\n\t"
                "ds_read_b128 v[248:251], %[ub] offset:352\n\t"
                "s_waitcnt lgkmcnt(10)\n\t"
                "v_mfma_f32_32x32x16_bf16 %[a1], v[220:223], v[216:219], %[a1]\n\t"
                "ds_read_b64 v[252:253], %[kb] offset:3776\n\t"
                "ds_read_b64 v[254:255], %[kb] offset:3784\n\t"
                "s_waitcnt lgkmcnt(9)\n\t"
                "v_mfma_f32_32x32x16_bf16 %[a1], v[224:227], v[228:231], %[a1]\n\t"
                "ds_read_b64 v[192:193], %[kb] offset:1824\n\t"
                "ds_read_b64 v[194:195], %[kb] offset:1832\n\t"
                "ds_read_b128 v[196:199], %[ub] offset:2800\n\t"
                "s_waitcnt lgkmcnt(11)\n\t"
                "v_mfma_f32_32x32x16_bf16 %[a0], v[224:227], v[232:235], %[a0]\n\t"
                "ds_read_b128 v[200:203], %[ub] offset:240\n\t"
                "s_waitcnt lgkmcnt(10)\n\t"
                "v_mfma_f32_32x32x16_bf16 %[a1], v[236:239], v[232:235], %[a1]\n\t"
                "ds_read_b64 v[204:205], %[kb] offset:3872\n\t"
                "ds_read_b64 v[206:207], %[kb] offset:3880\n\t"
                "s_waitcnt lgkmcnt(9)\n\t"
                "v_mfma_f32_32x32x16_bf16 %[a1], v[240:243], v[244:247], %[a1]\n\t"
                "ds_read_b64 v[208:209], %[kb] offset:1792\n\t"
                "ds_read_b64 v[210:211], %[kb] offset:1800\n\t"
                "ds_read_b128 v[212:215], %[ub] offset:2832\n\t"
                "s_waitcnt lgkmcnt(11)\n\t"
                "v_mfma_f32_32x32x16_bf16 %[a0], v[240:243], v[248:251], %[a0]\n\t"
                "ds_read_b128 v[216:219], %[ub] offset:272\n\t"
                "s_waitcnt lgkmcnt(10)\n\t"
                "v_mfma_f32_32x32x16_bf16 %[a1], v[252:255], v[248:251], %[a1]\n\t"
                "ds_read_b64 v[220:221], %[kb] offset:3840\n\t"
                "ds_read_b64 v[222:223], %[kb] offset:3848\n\t"
                "s_waitcnt lgkmcnt(9)\n\t"
                "v_mfma_f32_32x32x16_bf16 %[a1], v[192:195], v[196:199], %[a1]\n\t"
                "ds_read_b64 v[224:225], %[kb] offset:1888\n\t"
                "ds_read_b64 v[226:227], %[kb] offset:1896\n\t"
                "ds_read_b128 v[228:231], %[ub] offset:2720\n\t"
                "s_waitcnt lgkmcnt(11)\n\t"
                "v_mfma_f32_32x32x16_bf16 %[a0], v[192:195], v[200:203], %[a0]\n\t"
                "ds_read_b128 v[232:235], %[ub] offset:160\n\t"
                "s_waitcnt lgkmcnt(10)\n\t"
                "v_mfma_f32_32x32x16_bf16 %[a1], v[204:207], v[200:203], %[a1]\n\t"
                "ds_read_b64 v[236:237], %[kb] offset:3936\n\t"
                "ds_read_b64 v[238:239], %[kb] offset:3944\n\t"
                "s_waitcnt lgkmcnt(9)\n\t"
                "v_mfma_f32_32x32x16_bf16 %[a1], v[208:211], v[212:215], %[a1]\n\t"
                "ds_read_b64 v[240:241], %[kb] offset:1856\n\t"
                "ds_read_b64 v[242:243], %[kb] offset:1864\n\t"
                "ds_read_b128 v[244:247], %[ub] offset:2752\n\t"
                "s_waitcnt lgkmcnt(11)\n\t"
                "v_mfma_f32_32x32x16_bf16 %[a0], v[208:211], v[216:219], %[a0]\n\t"
                "ds_read_b128 v[248:251], %[ub] offset:192\n\t"
                "s_waitcnt lgkmcnt(10)\n\t"
                "v_mfma_f32_32x32x16_bf16 %[a1], v[220:223], v[216:219], %[a1]\n\t"
                "ds_read_b64 v[252:253], %[kb] offset:3904\n\t"
                "ds_read_b64 v[254:255], %[kb] offset:3912\n\t"
                "s_waitcnt lgkmcnt(9)\n\t"
                "v_mfma_f32_32x32x16_bf16 %[a1], v[224:227], v[228:231], %[a1]\n\t"
                "ds_read_b64 v[192:193], %[kb] offset:1952\n\t"
                "ds_read_b64 v[194:195], %[kb] offset:1960\n\t"
                "ds_read_b128 v[196:199], %[ub] offset:2640\n\t"
                "s_waitcnt lgkmcnt(11)\n\t"
                "v_mfma_f32_32x32x16_bf16 %[a0], v[224:227], v[232:235], %[a0]\n\t"
                "ds_read_b128 v[200:203], %[ub] offset:80\n\t"
                "s_waitcnt lgkmcnt(10)\n\t"
                "v_mfma_f32_32x32x16_bf16 %[a1], v[236:239], v[232:235], %[a1]\n\t"
                "ds_read_b64 v[204:205], %[kb] offset:4000\n\t"
                "ds_read_b64 v[206:207], %[kb] offset:4008\n\t"
                "s_waitcnt lgkmcnt(9)\n\t"
                "v_mfma_f32_32x32x16_bf16 %[a1], v[240:243], v[244:247], %[a1]\n\t"
                "ds_read_b64 v[208:209], %[kb] offset:1920\n\t"
                "ds_read_b64 v[210:211], %[kb] offset:1928\n\t"
                "ds_read_b128 v[212:215], %[ub] offset:2672\n\t"
                "s_waitcnt lgkmcnt(11)\n\t"
                "v_mfma_f32_32x32x16_bf16 %[a0], v[240:243], v[248:251], %[a0]\n\t"
                "ds_read_b128 v[216:219], %[ub] offset:112\n\t"
                "s_waitcnt lgkmcnt(10)\n\t"
                "v_mfma_f32_32x32x16_bf16 %[a1], v[252:255], v[248:251], %[a1]\n\t"
                "ds_read_b64 v[220:221], %[kb] offset:3968\n\t"
                "ds_read_b64 v[222:223], %[kb] offset:3976\n\t"
                "s_waitcnt lgkmcnt(9)\n\t"
                "v_mfma_f32_32x32x16_bf16 %[a1], v[192:195], v[196:199], %[a1]\n\t"
                "ds_read_b64 v[224:225], %[kb] offset:2016\n\t"
                "ds_read_b64 v[226:227], %[kb] offset:2024\n\t"
                "ds_read_b128 v[228:231], %[ub] offset:2560\n\t"
                "s_waitcnt lgkmcnt(11)\n\t"
                "v_mfma_f32_32x32x16_bf16 %[a0], v[192:195], v[200:203], %[a0]\n\t"
                "ds_read_b128 v[232:235], %[ub] offset:0\n\t"
                "s_waitcnt lgkmcnt(10)\n\t"
                "v_mfma_f32_32x32x16_bf16 %[a1], v[204:207], v[200:203], %[a1]\n\t"
                "ds_read_b64 v[236:237], %[kb] offset:4064\n\t"
                "ds_read_b64 v[238:239], %[kb] offset:4072\n\t"
                "s_waitcnt lgkmcnt(9)\n\t"
                "v_mfma_f32_32x32x16_bf16 %[a1], v[208:211], v[212:215], %[a1]\n\t"
                "ds_read_b64 v[240:241], %[kb] offset:1984\n\t"
                "ds_read_b64 v[242:243], %[kb] offset:1992\n\t"
                "ds_read_b128 v[244:247], %[ub] offset:2592\n\t"
                "s_waitcnt lgkmcnt(11)\n\t"
                "v_mfma_f32_32x32x16_bf16 %[a0], v[208:211], v[216:219], %[a0]\n\t"
                "ds_read_b128 v[248:251], %[ub] offset:32\n\t"
                "s_waitcnt lgkmcnt(10)\n\t"
                "v_mfma_f32_32x32x16_bf16 %[a1], v[220:223], v[216:219], %[a1]\n\t"
                "ds_read_b64 v[252:253], %[kb] offset:4032\n\t"
                "ds_read_b64 v[254:255], %[kb] offset:4040\n\t"
                "s_waitcnt lgkmcnt(9)\n\t"
                "v_mfma_f32_32x32x16_bf16 %[a1], v[224:227], v[228:231], %[a1]\n\t"
                "s_waitcnt lgkmcnt(8)\n\t"
                "v_mfma_f32_32x32x16_bf16 %[a0], v[224:227], v[232:235], %[a0]\n\t"
                "s_waitcnt lgkmcnt(6)\n\t"
                "v_mfma_f32_32x32x16_bf16 %[a1], v[236:239], v[232:235], %[a1]\n\t"
                "s_waitcnt lgkmcnt(3)\n\t"
                "v_mfma_f32_32x32x16_bf16 %[a1], v[240:243], v[244:247], %[a1]\n\t"
                "s_waitcnt lgkmcnt(2)\n\t"
                "v_mfma_f32_32x32x16_bf16 %[a0], v[240:243], v[248:251], %[a0]\n\t"
                "s_waitcnt lgkmcnt(0)\n\t"
                "v_mfma_f32_32x32x16_bf16 %[a1], v[252:255], v[248:251], %[a1]\n\t"
                "s_nop 7\n\t"
                "s_nop 3\n\t"
                : [a0] "=&v"(acc0), [a1] "=&v"(acc1) : [kb] "v"(kb), [ub] "v"(ub0 + c8 * CV_U) : "memory", "v192", "v193", "v194", "v195", "v196", "v197", "v198", "v199", "v200", "v201", "v202", "v203", "v204", "v205", "v206", "v207", "v208", "v209", "v210", "v211", "v212", "v213", "v214", "v215", "v216", "v217", "v218", "v219", "v220", "v221", "v222", "v223", "v224", "v225", "v226", "v227", "v228", "v229", "v230", "v231", "v232", "v233", "v234", "v235", "v236", "v237", "v238", "v239", "v240", "v241", "v242", "v243", "v244", "v245", "v246", "v247", "v248", "v249", "v250", "v251", "v252", "v253", "v254", "v255");
            unsigned char* uc = lds + c8 * CV_U + (32 + b) * CV_RP + kg * 8;
#pragma unroll
            for (int q = 0; q < 4; ++q) {
                *(v2u*)(uc + q * 16) = (v2u){pack2(acc0[4 * q], acc0[4 * q + 1]), pack2(acc0[4 * q + 2], acc0[4 * q + 3])};
                *(v2u*)(uc + 32 * CV_RP + q * 16) = (v2u){pack2(acc1[4 * q], acc1[4 * q + 1]), pack2(acc1[4 * q + 2], acc1[4 * q + 3])}; }
            __builtin_amdgcn_wave_barrier();
        }
    __syncthreads();
#pragma unroll
    for (int G = 0; G < 4; ++G) { const v4u lo = *(const v4u*)(urow + G * 16), hh = *(const v4u*)(urow + CV_U + G * 16); v4u o[2];
#pragma unroll
        for (int i = 0; i < 8; ++i) { const unsigned u0 = ur[G][(7 - i) >> 1], u1 = ur[4 + G][(7 - i) >> 1], qq = qr[2 * G + (i >> 2)][i & 3];
            const unsigned ud = pack2(((i & 1) ? blo(u0) : bhi(u0)) * blo(dv), ((i & 1) ? blo(u1) : bhi(u1)) * bhi(dv));
            const unsigned y = pack2(((i & 1) ? bhi(lo[i >> 1]) : blo(lo[i >> 1])) + blo(ud), ((i & 1) ? bhi(hh[i >> 1]) : blo(hh[i >> 1])) + bhi(ud));
            o[i >> 2][i & 3] = pack2(blo(qq) * blo(y), bhi(qq) * bhi(y)); }
        st16(r, W_A + un * 32768 + 2 * G * 4096 + tid * 16, o[0]); st16(r, W_A + un * 32768 + (2 * G + 1) * 4096 + tid * 16, o[1]); }
    DRAIN(); __syncthreads();
    if (tid == 0) __hip_atomic_fetch_add((unsigned*)(p.ws + F_CVY + (un >> 3) * 128), 1u, __ATOMIC_RELAXED, __HIP_MEMORY_SCOPE_AGENT);
    return true;
}

__device__ __forceinline__ void proj_block(const Params& p, unsigned char* lds, const int wv, const int h, const int c, const int j) {
    const int w = wv, l = otid() & 63, n = l & 15, g = l >> 4, tm = w >> 1, tn = w & 1;
    const unsigned lb = __builtin_amdgcn_readfirstlane((unsigned)(size_t)lds);
    const v4u sx = mk_srd(p.Aq, S * D * 2), sq = mk_srd(c < 2 ? p.Wq : p.Wk, 1024 * 4096 * 2), sv = mk_srd(p.Wv, 1024 * 4096 * 2), sw = mk_srd(p.ws, W_TOTAL);
    const unsigned lx = lb + (32 * tm + n) * 128 + ((g ^ (n & 7)) << 4), lr = lb + 131072, rr = 8 * g + (n >> 2);
    const int cv = ((c & 1) ^ 1) * 2 + (c >> 1);
    const unsigned aq = (c < 2 ? W_QX : W_KX) + (tm * 32 + h) * 8192 + (2 * (c & 1) + tn) * 1024 + l * 16;
    const int hq = h & 7, tk = ((hq - (hq > 1) - (hq > 5)) * 4 + c) * 4 + w, ts6 = tk % 6, tch = 15 - tk / 6, tx = tk - 6 * GTOUCH;
    const bf16_t* tp = (h & 3) == 1 ? p.gate_w : tx < 0 ? (ts6 % 3 == 0 ? p.Wq : ts6 % 3 == 1 ? p.Wk : p.Wv) + (size_t)(tch * 64 + l) * 4096 + ((h & 24) + 1 + (ts6 / 3) * 4) * 128 + 64
                     : tx < XTOUCH ? p.Aq + (tx * 64 + l) * 64 : p.gate_w;
    unsigned t0, xo, so, cn; unsigned long long ts, ta, tb, tc, td;
    asm volatile(
        "s_mov_b32 %[xo], 0\n\t"
        "s_mov_b32 %[so], 0\n\t"
        "s_mov_b32 %[cn], 1\n\t"
        "global_load_dword v88, v[114:115], off\n\t"
        "s_add_u32 m0, %[ld], 0\n\t"
        "s_add_u32 %[t0], %[xo], 0\n\t"
        "buffer_load_dwordx4 v100, %[sx], %[t0] offen lds\n\t"
        "s_add_u32 m0, %[ld], 1024\n\t"
        "s_add_u32 %[t0], %[xo], 16384\n\t"
        "buffer_load_dwordx4 v100, %[sx], %[t0] offen lds\n\t"
        "s_add_u32 m0, %[ld], 8192\n\t"
        "s_add_u32 %[t0], %[xo], 128\n\t"
        "buffer_load_dwordx4 v100, %[sx], %[t0] offen lds\n\t"
        "s_add_u32 m0, %[ld], 9216\n\t"
        "s_add_u32 %[t0], %[xo], 16512\n\t"
        "buffer_load_dwordx4 v100, %[sx], %[t0] offen lds\n\t"
        "s_add_u32 m0, %[ld], 16384\n\t"
        "s_add_u32 %[t0], %[xo], 256\n\t"
        "buffer_load_dwordx4 v100, %[sx], %[t0] offen lds\n\t"
        "s_add_u32 m0, %[ld], 17408\n\t"
        "s_add_u32 %[t0], %[xo], 16640\n\t"
        "buffer_load_dwordx4 v100, %[sx], %[t0] offen lds\n\t"
        "s_add_u32 m0, %[ld], 24576\n\t"
        "s_add_u32 %[t0], %[xo], 384\n\t"
        "buffer_load_dwordx4 v100, %[sx], %[t0] offen lds\n\t"
        "s_add_u32 m0, %[ld], 25600\n\t"
        "s_add_u32 %[t0], %[xo], 16768\n\t"
        "buffer_load_dwordx4 v100, %[sx], %[t0] offen lds\n\t"
        "s_mov_b32 %[t0], 0\n\t"
        "buffer_load_dwordx4 a[192:195], v101, %[sq], %[t0] offen\n\t"
        "buffer_load_dwordx4 a[200:203], v112, %[sv], %[t0] offen\n\t"
        "s_mov_b32 %[t0], 262144\n\t"
        "buffer_load_dwordx4 a[196:199], v101, %[sq], %[t0] offen\n\t"
        "s_mov_b32 %[t0], 524288\n\t"
        "buffer_load_dwordx4 a[204:207], v101, %[sq], %[t0] offen\n\t"
        "buffer_load_dwordx4 a[212:215], v112, %[sv], %[t0] offen\n\t"
        "s_mov_b32 %[t0], 786432\n\t"
        "buffer_load_dwordx4 a[208:211], v101, %[sq], %[t0] offen\n\t"
        "s_mov_b32 %[t0], 1048576\n\t"
        "buffer_load_dwordx4 a[216:219], v101, %[sq], %[t0] offen\n\t"
        "buffer_load_dwordx4 a[224:227], v112, %[sv], %[t0] offen\n\t"
        "s_mov_b32 %[t0], 1310720\n\t"
        "buffer_load_dwordx4 a[220:223], v101, %[sq], %[t0] offen\n\t"
        "s_mov_b32 %[t0], 1572864\n\t"
        "buffer_load_dwordx4 a[228:231], v101, %[sq], %[t0] offen\n\t"
        "buffer_load_dwordx4 a[236:239], v112, %[sv], %[t0] offen\n\t"
        "s_mov_b32 %[t0], 1835008\n\t"
        "buffer_load_dwordx4 a[232:235], v101, %[sq], %[t0] offen\n\t"
        "s_waitcnt vmcnt(9)\n\t"
        "ds_write_b128 v102, a[192:195] offset:0\n\t"
        "ds_write_b128 v102, a[196:199] offset:4096\n\t"
        "ds_write_b128 v116, a[200:203] offset:8192\n\t"
        "s_waitcnt lgkmcnt(0)\n\t"
        "s_barrier\n\t"
        "ds_read_b64_tr_b16 a[0:1], v103 offset:0\n\t"
        "ds_read_b64_tr_b16 a[2:3], v103 offset:512\n\t"
        "ds_read_b64_tr_b16 a[4:5], v103 offset:8\n\t"
        "ds_read_b64_tr_b16 a[6:7], v103 offset:520\n\t"
        "ds_read_b64_tr_b16 a[8:9], v104 offset:0\n\t"
        "ds_read_b64_tr_b16 a[10:11], v104 offset:256\n\t"
        "s_waitcnt lgkmcnt(5)\n\t"
        "ds_read_b64_tr_b16 a[12:13], v103 offset:4096\n\t"
        "ds_read_b64_tr_b16 a[14:15], v103 offset:4608\n\t"
        "ds_read_b64_tr_b16 a[16:17], v103 offset:4104\n\t"
        "ds_read_b64_tr_b16 a[18:19], v103 offset:4616\n\t"
        "ds_read_b64_tr_b16 a[20:21], v104 offset:2048\n\t"
        "ds_read_b64_tr_b16 a[22:23], v104 offset:2304\n\t"
        "ds_read_b128 v[56:59], v105 offset:0\n\t"
        "ds_read_b128 v[60:63], v105 offset:2048\n\t"
        "ds_read_b128 v[64:67], v106 offset:0\n\t"
        "ds_read_b128 v[68:71], v106 offset:2048\n\t"
        "s_add_u32 m0, %[ld], 32768\n\t"
        "s_add_u32 %[t0], %[xo], 512\n\t"
        "buffer_load_dwordx4 v100, %[sx], %[t0] offen lds\n\t"
        "s_add_u32 m0, %[ld], 33792\n\t"
        "s_add_u32 %[t0], %[xo], 16896\n\t"
        "buffer_load_dwordx4 v100, %[sx], %[t0] offen lds\n\t"
        "s_add_u32 m0, %[ld], 40960\n\t"
        "s_add_u32 %[t0], %[xo], 640\n\t"
        "buffer_load_dwordx4 v100, %[sx], %[t0] offen lds\n\t"
        "s_add_u32 m0, %[ld], 41984\n\t"
        "s_add_u32 %[t0], %[xo], 17024\n\t"
        "buffer_load_dwordx4 v100, %[sx], %[t0] offen lds\n\t"
        "s_add_u32 m0, %[ld], 49152\n\t"
        "s_add_u32 %[t0], %[xo], 768\n\t"
        "buffer_load_dwordx4 v100, %[sx], %[t0] offen lds\n\t"
        "s_add_u32 m0, %[ld], 50176\n\t"
        "s_add_u32 %[t0], %[xo], 17152\n\t"
        "buffer_load_dwordx4 v100, %[sx], %[t0] offen lds\n\t"
        "s_add_u32 m0, %[ld], 57344\n\t"
        "s_add_u32 %[t0], %[xo], 896\n\t"
        "buffer_load_dwordx4 v100, %[sx], %[t0] offen lds\n\t"
        "s_add_u32 m0, %[ld], 58368\n\t"
        "s_add_u32 %[t0], %[xo], 17280\n\t"
        "buffer_load_dwordx4 v100, %[sx], %[t0] offen lds\n\t"
        "s_mov_b32 %[t0], 2097152\n\t"
        "buffer_load_dwordx4 a[240:243], v101, %[sq], %[t0] offen\n\t"
        "buffer_load_dwordx4 a[248:251], v112, %[sv], %[t0] offen\n\t"
        "s_mov_b32 %[t0], 2359296\n\t"
        "buffer_load_dwordx4 a[244:247], v101, %[sq], %[t0] offen\n\t"
        "s_waitcnt vmcnt(17)\n\t"
        "ds_write_b128 v102, a[204:207] offset:16384\n\t"
        "ds_write_b128 v102, a[208:211] offset:20480\n\t"
        "ds_write_b128 v116, a[212:215] offset:24576\n\t"
        "s_waitcnt lgkmcnt(0)\n\t"
        "s_barrier\n\t"
        "v_mfma_f32_16x16x32_bf16 v[8:11], a[0:3], v[56:59], 0\n\t"
        "ds_read_b64_tr_b16 a[24:25], v103 offset:16384\n\t"
        "ds_read_b64_tr_b16 a[26:27], v103 offset:16896\n\t"
        "v_mfma_f32_16x16x32_bf16 v[12:15], a[4:7], v[56:59], 0\n\t"
        "ds_read_b64_tr_b16 a[28:29], v103 offset:16392\n\t"
        "ds_read_b64_tr_b16 a[30:31], v103 offset:16904\n\t"
        "v_mfma_f32_16x16x32_bf16 v[16:19], v[56:59], a[8:11], 0\n\t"
        "ds_read_b64_tr_b16 a[32:33], v104 offset:16384\n\t"
        "ds_read_b64_tr_b16 a[34:35], v104 offset:16640\n\t"
        "v_mfma_f32_16x16x32_bf16 v[20:23], a[0:3], v[60:63], 0\n\t"
        "s_waitcnt lgkmcnt(5)\n\t"
        "ds_read_b64_tr_b16 a[36:37], v103 offset:20480\n\t"
        "v_mfma_f32_16x16x32_bf16 v[24:27], a[4:7], v[60:63], 0\n\t"
        "ds_read_b64_tr_b16 a[38:39], v103 offset:20992\n\t"
        "ds_read_b64_tr_b16 a[40:41], v103 offset:20488\n\t"
        "v_mfma_f32_16x16x32_bf16 v[28:31], v[60:63], a[8:11], 0\n\t"
        "ds_read_b64_tr_b16 a[42:43], v103 offset:21000\n\t"
        "ds_read_b64_tr_b16 a[44:45], v104 offset:18432\n\t"
        "v_mfma_f32_16x16x32_bf16 v[8:11], a[12:15], v[64:67], v[8:11]\n\t"
        "ds_read_b64_tr_b16 a[46:47], v104 offset:18688\n\t"
        "ds_read_b128 v[72:75], v105 offset:8192\n\t"
        "v_mfma_f32_16x16x32_bf16 v[12:15], a[16:19], v[64:67], v[12:15]\n\t"
        "ds_read_b128 v[76:79], v105 offset:10240\n\t"
        "ds_read_b128 v[80:83], v106 offset:8192\n\t"
        "v_mfma_f32_16x16x32_bf16 v[16:19], v[64:67], a[20:23], v[16:19]\n\t"
        "ds_read_b128 v[84:87], v106 offset:10240\n\t"
        "v_mfma_f32_16x16x32_bf16 v[20:23], a[12:15], v[68:71], v[20:23]\n\t"
        "v_mfma_f32_16x16x32_bf16 v[24:27], a[16:19], v[68:71], v[24:27]\n\t"
        "v_mfma_f32_16x16x32_bf16 v[28:31], v[68:71], a[20:23], v[28:31]\n\t"
        "s_mov_b32 %[t0], 2621440\n\t"
        "buffer_load_dwordx4 a[252:255], v101, %[sq], %[t0] offen\n\t"
        "buffer_load_dwordx4 v[132:135], v112, %[sv], %[t0] offen\n\t"
        "s_mov_b32 %[t0], 2883584\n\t"
        "buffer_load_dwordx4 v[128:131], v101, %[sq], %[t0] offen\n\t"
        "s_waitcnt vmcnt(17)\n\t"
        "ds_write_b128 v102, a[216:219] offset:0\n\t"
        "ds_write_b128 v102, a[220:223] offset:4096\n\t"
        "ds_write_b128 v116, a[224:227] offset:8192\n\t"
        "s_waitcnt lgkmcnt(0)\n\t"
        "s_barrier\n\t"
        "v_mfma_f32_16x16x32_bf16 v[8:11], a[24:27], v[72:75], v[8:11]\n\t"
        "ds_read_b64_tr_b16 a[48:49], v103 offset:0\n\t"
        "ds_read_b64_tr_b16 a[50:51], v103 offset:512\n\t"
        "v_mfma_f32_16x16x32_bf16 v[12:15], a[28:31], v[72:75], v[12:15]\n\t"
        "ds_read_b64_tr_b16 a[52:53], v103 offset:8\n\t"
        "ds_read_b64_tr_b16 a[54:55], v103 offset:520\n\t"
        "v_mfma_f32_16x16x32_bf16 v[16:19], v[72:75], a[32:35], v[16:19]\n\t"
        "ds_read_b64_tr_b16 a[56:57], v104 offset:0\n\t"
        "ds_read_b64_tr_b16 a[58:59], v104 offset:256\n\t"
        "v_mfma_f32_16x16x32_bf16 v[20:23], a[24:27], v[76:79], v[20:23]\n\t"
        "s_waitcnt lgkmcnt(5)\n\t"
        "ds_read_b64_tr_b16 a[60:61], v103 offset:4096\n\t"
        "v_mfma_f32_16x16x32_bf16 v[24:27], a[28:31], v[76:79], v[24:27]\n\t"
        "ds_read_b64_tr_b16 a[62:63], v103 offset:4608\n\t"
        "ds_read_b64_tr_b16 a[64:65], v103 offset:4104\n\t"
        "v_mfma_f32_16x16x32_bf16 v[28:31], v[76:79], a[32:35], v[28:31]\n\t"
        "ds_read_b64_tr_b16 a[66:67], v103 offset:4616\n\t"
        "ds_read_b64_tr_b16 a[68:69], v104 offset:2048\n\t"
        "v_mfma_f32_16x16x32_bf16 v[8:11], a[36:39], v[80:83], v[8:11]\n\t"
        "ds_read_b64_tr_b16 a[70:71], v104 offset:2304\n\t"
        "ds_read_b128 v[56:59], v105 offset:16384\n\t"
        "v_mfma_f32_16x16x32_bf16 v[12:15], a[40:43], v[80:83], v[12:15]\n\t"
        "ds_read_b128 v[60:63], v105 offset:18432\n\t"
        "ds_read_b128 v[64:67], v106 offset:16384\n\t"
        "v_mfma_f32_16x16x32_bf16 v[16:19], v[80:83], a[44:47], v[16:19]\n\t"
        "ds_read_b128 v[68:71], v106 offset:18432\n\t"
        "v_mfma_f32_16x16x32_bf16 v[20:23], a[36:39], v[84:87], v[20:23]\n\t"
        "v_mfma_f32_16x16x32_bf16 v[24:27], a[40:43], v[84:87], v[24:27]\n\t"
        "v_mfma_f32_16x16x32_bf16 v[28:31], v[84:87], a[44:47], v[28:31]\n\t"
        "s_mov_b32 %[t0], 3145728\n\t"
        "buffer_load_dwordx4 v[136:139], v101, %[sq], %[t0] offen\n\t"
        "buffer_load_dwordx4 v[144:147], v112, %[sv], %[t0] offen\n\t"
        "s_mov_b32 %[t0], 3407872\n\t"
        "buffer_load_dwordx4 v[140:143], v101, %[sq], %[t0] offen\n\t"
        "s_waitcnt vmcnt(17)\n\t"
        "ds_write_b128 v102, a[228:231] offset:16384\n\t"
        "ds_write_b128 v102, a[232:235] offset:20480\n\t"
        "ds_write_b128 v116, a[236:239] offset:24576\n\t"
        "s_waitcnt lgkmcnt(0)\n\t"
        "s_barrier\n\t"
        "v_mfma_f32_16x16x32_bf16 v[8:11], a[48:51], v[56:59], v[8:11]\n\t"
        "ds_read_b64_tr_b16 a[72:73], v103 offset:16384\n\t"
        "ds_read_b64_tr_b16 a[74:75], v103 offset:16896\n\t"
        "v_mfma_f32_16x16x32_bf16 v[12:15], a[52:55], v[56:59], v[12:15]\n\t"
        "ds_read_b64_tr_b16 a[76:77], v103 offset:16392\n\t"
        "ds_read_b64_tr_b16 a[78:79], v103 offset:16904\n\t"
        "v_mfma_f32_16x16x32_bf16 v[16:19], v[56:59], a[56:59], v[16:19]\n\t"
        "ds_read_b64_tr_b16 a[80:81], v104 offset:16384\n\t"
        "ds_read_b64_tr_b16 a[82:83], v104 offset:16640\n\t"
        "v_mfma_f32_16x16x32_bf16 v[20:23], a[48:51], v[60:63], v[20:23]\n\t"
        "s_waitcnt lgkmcnt(5)\n\t"
        "ds_read_b64_tr_b16 a[84:85], v103 offset:20480\n\t"
        "v_mfma_f32_16x16x32_bf16 v[24:27], a[52:55], v[60:63], v[24:27]\n\t"
        "ds_read_b64_tr_b16 a[86:87], v103 offset:20992\n\t"
        "ds_read_b64_tr_b16 a[88:89], v103 offset:20488\n\t"
        "v_mfma_f32_16x16x32_bf16 v[28:31], v[60:63], a[56:59], v[28:31]\n\t"
        "ds_read_b64_tr_b16 a[90:91], v103 offset:21000\n\t"
        "ds_read_b64_tr_b16 a[92:93], v104 offset:18432\n\t"
        "v_mfma_f32_16x16x32_bf16 v[8:11], a[60:63], v[64:67], v[8:11]\n\t"
        "ds_read_b64_tr_b16 a[94:95], v104 offset:18688\n\t"
        "ds_read_b128 v[72:75], v105 offset:24576\n\t"
        "v_mfma_f32_16x16x32_bf16 v[12:15], a[64:67], v[64:67], v[12:15]\n\t"
        "ds_read_b128 v[76:79], v105 offset:26624\n\t"
        "ds_read_b128 v[80:83], v106 offset:24576\n\t"
        "v_mfma_f32_16x16x32_bf16 v[16:19], v[64:67], a[68:71], v[16:19]\n\t"
        "ds_read_b128 v[84:87], v106 offset:26624\n\t"
        "v_mfma_f32_16x16x32_bf16 v[20:23], a[60:63], v[68:71], v[20:23]\n\t"
        "v_mfma_f32_16x16x32_bf16 v[24:27], a[64:67], v[68:71], v[24:27]\n\t"
        "v_mfma_f32_16x16x32_bf16 v[28:31], v[68:71], a[68:71], v[28:31]\n\t"
        "s_mov_b32 %[t0], 3670016\n\t"
        "buffer_load_dwordx4 v[148:151], v101, %[sq], %[t0] offen\n\t"
        "buffer_load_dwordx4 v[156:159], v112, %[sv], %[t0] offen\n\t"
        "s_mov_b32 %[t0], 3932160\n\t"
        "buffer_load_dwordx4 v[152:155], v101, %[sq], %[t0] offen\n\t"
        "s_waitcnt vmcnt(9)\n\t"
        "ds_write_b128 v102, a[240:243] offset:0\n\t"
        "ds_write_b128 v102, a[244:247] offset:4096\n\t"
        "ds_write_b128 v116, a[248:251] offset:8192\n\t"
        "s_waitcnt lgkmcnt(0)\n\t"
        "s_barrier\n\t"
        "v_mfma_f32_16x16x32_bf16 v[8:11], a[72:75], v[72:75], v[8:11]\n\t"
        "ds_read_b64_tr_b16 a[96:97], v103 offset:0\n\t"
        "ds_read_b64_tr_b16 a[98:99], v103 offset:512\n\t"
        "v_mfma_f32_16x16x32_bf16 v[12:15], a[76:79], v[72:75], v[12:15]\n\t"
        "ds_read_b64_tr_b16 a[100:101], v103 offset:8\n\t"
        "ds_read_b64_tr_b16 a[102:103], v103 offset:520\n\t"
        "v_mfma_f32_16x16x32_bf16 v[16:19], v[72:75], a[80:83], v[16:19]\n\t"
        "ds_read_b64_tr_b16 a[104:105], v104 offset:0\n\t"
        "ds_read_b64_tr_b16 a[106:107], v104 offset:256\n\t"
        "v_mfma_f32_16x16x32_bf16 v[20:23], a[72:75], v[76:79], v[20:23]\n\t"
        "s_waitcnt lgkmcnt(5)\n\t"
        "ds_read_b64_tr_b16 a[108:109], v103 offset:4096\n\t"
        "v_mfma_f32_16x16x32_bf16 v[24:27], a[76:79], v[76:79], v[24:27]\n\t"
        "ds_read_b64_tr_b16 a[110:111], v103 offset:4608\n\t"
        "ds_read_b64_tr_b16 a[112:113], v103 offset:4104\n\t"
        "v_mfma_f32_16x16x32_bf16 v[28:31], v[76:79], a[80:83], v[28:31]\n\t"
        "ds_read_b64_tr_b16 a[114:115], v103 offset:4616\n\t"
        "ds_read_b64_tr_b16 a[116:117], v104 offset:2048\n\t"
        "v_mfma_f32_16x16x32_bf16 v[8:11], a[84:87], v[80:83], v[8:11]\n\t"
        "ds_read_b64_tr_b16 a[118:119], v104 offset:2304\n\t"
        "ds_read_b128 v[56:59], v105 offset:32768\n\t"
        "v_mfma_f32_16x16x32_bf16 v[12:15], a[88:91], v[80:83], v[12:15]\n\t"
        "ds_read_b128 v[60:63], v105 offset:34816\n\t"
        "ds_read_b128 v[64:67], v106 offset:32768\n\t"
        "v_mfma_f32_16x16x32_bf16 v[16:19], v[80:83], a[92:95], v[16:19]\n\t"
        "ds_read_b128 v[68:71], v106 offset:34816\n\t"
        "v_mfma_f32_16x16x32_bf16 v[20:23], a[84:87], v[84:87], v[20:23]\n\t"
        "v_mfma_f32_16x16x32_bf16 v[24:27], a[88:91], v[84:87], v[24:27]\n\t"
        "v_mfma_f32_16x16x32_bf16 v[28:31], v[84:87], a[92:95], v[28:31]\n\t"
        "s_add_u32 m0, %[ld], 65536\n\t"
        "s_add_u32 %[t0], %[xo], 1024\n\t"
        "buffer_load_dwordx4 v100, %[sx], %[t0] offen lds\n\t"
        "s_add_u32 m0, %[ld], 66560\n\t"
        "s_add_u32 %[t0], %[xo], 17408\n\t"
        "buffer_load_dwordx4 v100, %[sx], %[t0] offen lds\n\t"
        "s_add_u32 m0, %[ld], 73728\n\t"
        "s_add_u32 %[t0], %[xo], 1152\n\t"
        "buffer_load_dwordx4 v100, %[sx], %[t0] offen lds\n\t"
        "s_add_u32 m0, %[ld], 74752\n\t"
        "s_add_u32 %[t0], %[xo], 17536\n\t"
        "buffer_load_dwordx4 v100, %[sx], %[t0] offen lds\n\t"
        "s_add_u32 m0, %[ld], 81920\n\t"
        "s_add_u32 %[t0], %[xo], 1280\n\t"
        "buffer_load_dwordx4 v100, %[sx], %[t0] offen lds\n\t"
        "s_add_u32 m0, %[ld], 82944\n\t"
        "s_add_u32 %[t0], %[xo], 17664\n\t"
        "buffer_load_dwordx4 v100, %[sx], %[t0] offen lds\n\t"
        "s_add_u32 m0, %[ld], 90112\n\t"
        "s_add_u32 %[t0], %[xo], 1408\n\t"
        "buffer_load_dwordx4 v100, %[sx], %[t0] offen lds\n\t"
        "s_add_u32 m0, %[ld], 91136\n\t"
        "s_add_u32 %[t0], %[xo], 17792\n\t"
        "buffer_load_dwordx4 v100, %[sx], %[t0] offen lds\n\t"
        "s_mov_b32 %[t0], 4194304\n\t"
        "buffer_load_dwordx4 v[160:163], v101, %[sq], %[t0] offen\n\t"
        "buffer_load_dwordx4 v[168:171], v112, %[sv], %[t0] offen\n\t"
        "s_mov_b32 %[t0], 4456448\n\t"
        "buffer_load_dwordx4 v[164:167], v101, %[sq], %[t0] offen\n\t"
        "s_waitcnt vmcnt(17)\n\t"
        "ds_write_b128 v102, a[252:255] offset:16384\n\t"
        "ds_write_b128 v102, v[128:131] offset:20480\n\t"
        "ds_write_b128 v116, v[132:135] offset:24576\n\t"
        "s_waitcnt lgkmcnt(0)\n\t"
        "s_barrier\n\t"
        "v_mfma_f32_16x16x32_bf16 v[8:11], a[96:99], v[56:59], v[8:11]\n\t"
        "ds_read_b64_tr_b16 a[120:121], v103 offset:16384\n\t"
        "ds_read_b64_tr_b16 a[122:123], v103 offset:16896\n\t"
        "v_mfma_f32_16x16x32_bf16 v[12:15], a[100:103], v[56:59], v[12:15]\n\t"
        "ds_read_b64_tr_b16 a[124:125], v103 offset:16392\n\t"
        "ds_read_b64_tr_b16 a[126:127], v103 offset:16904\n\t"
        "v_mfma_f32_16x16x32_bf16 v[16:19], v[56:59], a[104:107], v[16:19]\n\t"
        "ds_read_b64_tr_b16 a[128:129], v104 offset:16384\n\t"
        "ds_read_b64_tr_b16 a[130:131], v104 offset:16640\n\t"
        "v_mfma_f32_16x16x32_bf16 v[20:23], a[96:99], v[60:63], v[20:23]\n\t"
        "s_waitcnt lgkmcnt(5)\n\t"
        "ds_read_b64_tr_b16 a[132:133], v103 offset:20480\n\t"
        "v_mfma_f32_16x16x32_bf16 v[24:27], a[100:103], v[60:63], v[24:27]\n\t"
        "ds_read_b64_tr_b16 a[134:135], v103 offset:20992\n\t"
        "ds_read_b64_tr_b16 a[136:137], v103 offset:20488\n\t"
        "v_mfma_f32_16x16x32_bf16 v[28:31], v[60:63], a[104:107], v[28:31]\n\t"
        "ds_read_b64_tr_b16 a[138:139], v103 offset:21000\n\t"
        "ds_read_b64_tr_b16 a[140:141], v104 offset:18432\n\t"
        "v_mfma_f32_16x16x32_bf16 v[8:11], a[108:111], v[64:67], v[8:11]\n\t"
        "ds_read_b64_tr_b16 a[142:143], v104 offset:18688\n\t"
        "ds_read_b128 v[72:75], v105 offset:40960\n\t"
        "v_mfma_f32_16x16x32_bf16 v[12:15], a[112:115], v[64:67], v[12:15]\n\t"
        "ds_read_b128 v[76:79], v105 offset:43008\n\t"
        "ds_read_b128 v[80:83], v106 offset:40960\n\t"
        "v_mfma_f32_16x16x32_bf16 v[16:19], v[64:67], a[116:119], v[16:19]\n\t"
        "ds_read_b128 v[84:87], v106 offset:43008\n\t"
        "v_mfma_f32_16x16x32_bf16 v[20:23], a[108:111], v[68:71], v[20:23]\n\t"
        "v_mfma_f32_16x16x32_bf16 v[24:27], a[112:115], v[68:71], v[24:27]\n\t"
        "v_mfma_f32_16x16x32_bf16 v[28:31], v[68:71], a[116:119], v[28:31]\n\t"
        "s_mov_b32 %[t0], 4718592\n\t"
        "buffer_load_dwordx4 v[172:175], v101, %[sq], %[t0] offen\n\t"
        "buffer_load_dwordx4 v[180:183], v112, %[sv], %[t0] offen\n\t"
        "s_mov_b32 %[t0], 4980736\n\t"
        "buffer_load_dwordx4 v[176:179], v101, %[sq], %[t0] offen\n\t"
        "s_waitcnt vmcnt(17)\n\t"
        "ds_write_b128 v102, v[136:139] offset:0\n\t"
        "ds_write_b128 v102, v[140:143] offset:4096\n\t"
        "ds_write_b128 v116, v[144:147] offset:8192\n\t"
        "s_waitcnt lgkmcnt(0)\n\t"
        "s_barrier\n\t"
        "v_mfma_f32_16x16x32_bf16 v[8:11], a[120:123], v[72:75], v[8:11]\n\t"
        "ds_read_b64_tr_b16 a[144:145], v103 offset:0\n\t"
        "ds_read_b64_tr_b16 a[146:147], v103 offset:512\n\t"
        "v_mfma_f32_16x16x32_bf16 v[12:15], a[124:127], v[72:75], v[12:15]\n\t"
        "ds_read_b64_tr_b16 a[148:149], v103 offset:8\n\t"
        "ds_read_b64_tr_b16 a[150:151], v103 offset:520\n\t"
        "v_mfma_f32_16x16x32_bf16 v[16:19], v[72:75], a[128:131], v[16:19]\n\t"
        "ds_read_b64_tr_b16 a[152:153], v104 offset:0\n\t"
        "ds_read_b64_tr_b16 a[154:155], v104 offset:256\n\t"
        "v_mfma_f32_16x16x32_bf16 v[20:23], a[120:123], v[76:79], v[20:23]\n\t"
        "s_waitcnt lgkmcnt(5)\n\t"
        "ds_read_b64_tr_b16 a[156:157], v103 offset:4096\n\t"
        "v_mfma_f32_16x16x32_bf16 v[24:27], a[124:127], v[76:79], v[24:27]\n\t"
        "ds_read_b64_tr_b16 a[158:159], v103 offset:4608\n\t"
        "ds_read_b64_tr_b16 a[160:161], v103 offset:4104\n\t"
        "v_mfma_f32_16x16x32_bf16 v[28:31], v[76:79], a[128:131], v[28:31]\n\t"
        "ds_read_b64_tr_b16 a[162:163], v103 offset:4616\n\t"
        "ds_read_b64_tr_b16 a[164:165], v104 offset:2048\n\t"
        "v_mfma_f32_16x16x32_bf16 v[8:11], a[132:135], v[80:83], v[8:11]\n\t"
        "ds_read_b64_tr_b16 a[166:167], v104 offset:2304\n\t"
        "ds_read_b128 v[56:59], v105 offset:49152\n\t"
        "v_mfma_f32_16x16x32_bf16 v[12:15], a[136:139], v[80:83], v[12:15]\n\t"
        "ds_read_b128 v[60:63], v105 offset:51200\n\t"
        "ds_read_b128 v[64:67], v106 offset:49152\n\t"
        "v_mfma_f32_16x16x32_bf16 v[16:19], v[80:83], a[140:143], v[16:19]\n\t"
        "ds_read_b128 v[68:71], v106 offset:51200\n\t"
        "v_mfma_f32_16x16x32_bf16 v[20:23], a[132:135], v[84:87], v[20:23]\n\t"
        "v_mfma_f32_16x16x32_bf16 v[24:27], a[136:139], v[84:87], v[24:27]\n\t"
        "v_mfma_f32_16x16x32_bf16 v[28:31], v[84:87], a[140:143], v[28:31]\n\t"
        "s_mov_b32 %[t0], 5242880\n\t"
        "buffer_load_dwordx4 v[184:187], v101, %[sq], %[t0] offen\n\t"
        "buffer_load_dwordx4 v[192:195], v112, %[sv], %[t0] offen\n\t"
        "s_mov_b32 %[t0], 5505024\n\t"
        "buffer_load_dwordx4 v[188:191], v101, %[sq], %[t0] offen\n\t"
        "s_waitcnt vmcnt(17)\n\t"
        "ds_write_b128 v102, v[148:151] offset:16384\n\t"
        "ds_write_b128 v102, v[152:155] offset:20480\n\t"
        "ds_write_b128 v116, v[156:159] offset:24576\n\t"
        "s_waitcnt lgkmcnt(0)\n\t"
        "s_barrier\n\t"
        "v_mfma_f32_16x16x32_bf16 v[8:11], a[144:147], v[56:59], v[8:11]\n\t"
        "ds_read_b64_tr_b16 a[168:169], v103 offset:16384\n\t"
        "ds_read_b64_tr_b16 a[170:171], v103 offset:16896\n\t"
        "v_mfma_f32_16x16x32_bf16 v[12:15], a[148:151], v[56:59], v[12:15]\n\t"
        "ds_read_b64_tr_b16 a[172:173], v103 offset:16392\n\t"
        "ds_read_b64_tr_b16 a[174:175], v103 offset:16904\n\t"
        "v_mfma_f32_16x16x32_bf16 v[16:19], v[56:59], a[152:155], v[16:19]\n\t"
        "ds_read_b64_tr_b16 a[176:177], v104 offset:16384\n\t"
        "ds_read_b64_tr_b16 a[178:179], v104 offset:16640\n\t"
        "v_mfma_f32_16x16x32_bf16 v[20:23], a[144:147], v[60:63], v[20:23]\n\t"
        "s_waitcnt lgkmcnt(5)\n\t"
        "ds_read_b64_tr_b16 a[180:181], v103 offset:20480\n\t"
        "v_mfma_f32_16x16x32_bf16 v[24:27], a[148:151], v[60:63], v[24:27]\n\t"
        "ds_read_b64_tr_b16 a[182:183], v103 offset:20992\n\t"
        "ds_read_b64_tr_b16 a[184:185], v103 offset:20488\n\t"
        "v_mfma_f32_16x16x32_bf16 v[28:31], v[60:63], a[152:155], v[28:31]\n\t"
        "ds_read_b64_tr_b16 a[186:187], v103 offset:21000\n\t"
        "ds_read_b64_tr_b16 a[188:189], v104 offset:18432\n\t"
        "v_mfma_f32_16x16x32_bf16 v[8:11], a[156:159], v[64:67], v[8:11]\n\t"
        "ds_read_b64_tr_b16 a[190:191], v104 offset:18688\n\t"
        "ds_read_b128 v[72:75], v105 offset:57344\n\t"
        "v_mfma_f32_16x16x32_bf16 v[12:15], a[160:163], v[64:67], v[12:15]\n\t"
        "ds_read_b128 v[76:79], v105 offset:59392\n\t"
        "ds_read_b128 v[80:83], v106 offset:57344\n\t"
        "v_mfma_f32_16x16x32_bf16 v[16:19], v[64:67], a[164:167], v[16:19]\n\t"
        "ds_read_b128 v[84:87], v106 offset:59392\n\t"
        "v_mfma_f32_16x16x32_bf16 v[20:23], a[156:159], v[68:71], v[20:23]\n\t"
        "v_mfma_f32_16x16x32_bf16 v[24:27], a[160:163], v[68:71], v[24:27]\n\t"
        "v_mfma_f32_16x16x32_bf16 v[28:31], v[68:71], a[164:167], v[28:31]\n\t"
        "s_mov_b32 %[t0], 5767168\n\t"
        "buffer_load_dwordx4 v[196:199], v101, %[sq], %[t0] offen\n\t"
        "buffer_load_dwordx4 v[204:207], v112, %[sv], %[t0] offen\n\t"
        "s_mov_b32 %[t0], 6029312\n\t"
        "buffer_load_dwordx4 v[200:203], v101, %[sq], %[t0] offen\n\t"
        "s_waitcnt vmcnt(9)\n\t"
        "ds_write_b128 v102, v[160:163] offset:0\n\t"
        "ds_write_b128 v102, v[164:167] offset:4096\n\t"
        "ds_write_b128 v116, v[168:171] offset:8192\n\t"
        "s_waitcnt lgkmcnt(0)\n\t"
        "s_barrier\n\t"
        "v_mfma_f32_16x16x32_bf16 v[8:11], a[168:171], v[72:75], v[8:11]\n\t"
        "ds_read_b64_tr_b16 a[192:193], v103 offset:0\n\t"
        "ds_read_b64_tr_b16 a[194:195], v103 offset:512\n\t"
        "v_mfma_f32_16x16x32_bf16 v[12:15], a[172:175], v[72:75], v[12:15]\n\t"
        "ds_read_b64_tr_b16 a[196:197], v103 offset:8\n\t"
        "ds_read_b64_tr_b16 a[198:199], v103 offset:520\n\t"
        "v_mfma_f32_16x16x32_bf16 v[16:19], v[72:75], a[176:179], v[16:19]\n\t"
        "ds_read_b64_tr_b16 a[200:201], v104 offset:0\n\t"
        "ds_read_b64_tr_b16 a[202:203], v104 offset:256\n\t"
        "v_mfma_f32_16x16x32_bf16 v[20:23], a[168:171], v[76:79], v[20:23]\n\t"
        "s_waitcnt lgkmcnt(5)\n\t"
        "ds_read_b64_tr_b16 a[204:205], v103 offset:4096\n\t"
        "v_mfma_f32_16x16x32_bf16 v[24:27], a[172:175], v[76:79], v[24:27]\n\t"
        "ds_read_b64_tr_b16 a[206:207], v103 offset:4608\n\t"
        "ds_read_b64_tr_b16 a[208:209], v103 offset:4104\n\t"
        "v_mfma_f32_16x16x32_bf16 v[28:31], v[76:79], a[176:179], v[28:31]\n\t"
        "ds_read_b64_tr_b16 a[210:211], v103 offset:4616\n\t"
        "ds_read_b64_tr_b16 a[212:213], v104 offset:2048\n\t"
        "v_mfma_f32_16x16x32_bf16 v[8:11], a[180:183], v[80:83], v[8:11]\n\t"
        "ds_read_b64_tr_b16 a[214:215], v104 offset:2304\n\t"
        "ds_read_b128 v[56:59], v107 offset:0\n\t"
        "v_mfma_f32_16x16x32_bf16 v[12:15], a[184:187], v[80:83], v[12:15]\n\t"
        "ds_read_b128 v[60:63], v107 offset:2048\n\t"
        "ds_read_b128 v[64:67], v108 offset:0\n\t"
        "v_mfma_f32_16x16x32_bf16 v[16:19], v[80:83], a[188:191], v[16:19]\n\t"
        "ds_read_b128 v[68:71], v108 offset:2048\n\t"
        "v_mfma_f32_16x16x32_bf16 v[20:23], a[180:183], v[84:87], v[20:23]\n\t"
        "v_mfma_f32_16x16x32_bf16 v[24:27], a[184:187], v[84:87], v[24:27]\n\t"
        "v_mfma_f32_16x16x32_bf16 v[28:31], v[84:87], a[188:191], v[28:31]\n\t"
        "s_add_u32 m0, %[ld], 98304\n\t"
        "s_add_u32 %[t0], %[xo], 1536\n\t"
        "buffer_load_dwordx4 v100, %[sx], %[t0] offen lds\n\t"
        "s_add_u32 m0, %[ld], 99328\n\t"
        "s_add_u32 %[t0], %[xo], 17920\n\t"
        "buffer_load_dwordx4 v100, %[sx], %[t0] offen lds\n\t"
        "s_add_u32 m0, %[ld], 106496\n\t"
        "s_add_u32 %[t0], %[xo], 1664\n\t"
        "buffer_load_dwordx4 v100, %[sx], %[t0] offen lds\n\t"
        "s_add_u32 m0, %[ld], 107520\n\t"
        "s_add_u32 %[t0], %[xo], 18048\n\t"
        "buffer_load_dwordx4 v100, %[sx], %[t0] offen lds\n\t"
        "s_add_u32 m0, %[ld], 114688\n\t"
        "s_add_u32 %[t0], %[xo], 1792\n\t"
        "buffer_load_dwordx4 v100, %[sx], %[t0] offen lds\n\t"
        "s_add_u32 m0, %[ld], 115712\n\t"
        "s_add_u32 %[t0], %[xo], 18176\n\t"
        "buffer_load_dwordx4 v100, %[sx], %[t0] offen lds\n\t"
        "s_add_u32 m0, %[ld], 122880\n\t"
        "s_add_u32 %[t0], %[xo], 1920\n\t"
        "buffer_load_dwordx4 v100, %[sx], %[t0] offen lds\n\t"
        "s_add_u32 m0, %[ld], 123904\n\t"
        "s_add_u32 %[t0], %[xo], 18304\n\t"
        "buffer_load_dwordx4 v100, %[sx], %[t0] offen lds\n\t"
        "s_mov_b32 %[t0], 6291456\n\t"
        "buffer_load_dwordx4 v[208:211], v101, %[sq], %[t0] offen\n\t"
        "buffer_load_dwordx4 v[216:219], v112, %[sv], %[t0] offen\n\t"
        "s_mov_b32 %[t0], 6553600\n\t"
        "buffer_load_dwordx4 v[212:215], v101, %[sq], %[t0] offen\n\t"
        "s_waitcnt vmcnt(17)\n\t"
        "ds_write_b128 v102, v[172:175] offset:16384\n\t"
        "ds_write_b128 v102, v[176:179] offset:20480\n\t"
        "ds_write_b128 v116, v[180:183] offset:24576\n\t"
        "s_waitcnt lgkmcnt(0)\n\t"
        "s_barrier\n\t"
        "v_mfma_f32_16x16x32_bf16 v[8:11], a[192:195], v[56:59], v[8:11]\n\t"
        "ds_read_b64_tr_b16 a[216:217], v103 offset:16384\n\t"
        "ds_read_b64_tr_b16 a[218:219], v103 offset:16896\n\t"
        "v_mfma_f32_16x16x32_bf16 v[12:15], a[196:199], v[56:59], v[12:15]\n\t"
        "ds_read_b64_tr_b16 a[220:221], v103 offset:16392\n\t"
        "ds_read_b64_tr_b16 a[222:223], v103 offset:16904\n\t"
        "v_mfma_f32_16x16x32_bf16 v[16:19], v[56:59], a[200:203], v[16:19]\n\t"
        "ds_read_b64_tr_b16 a[224:225], v104 offset:16384\n\t"
        "ds_read_b64_tr_b16 a[226:227], v104 offset:16640\n\t"
        "v_mfma_f32_16x16x32_bf16 v[20:23], a[192:195], v[60:63], v[20:23]\n\t"
        "s_waitcnt lgkmcnt(5)\n\t"
        "ds_read_b64_tr_b16 a[228:229], v103 offset:20480\n\t"
        "v_mfma_f32_16x16x32_bf16 v[24:27], a[196:199], v[60:63], v[24:27]\n\t"
        "ds_read_b64_tr_b16 a[230:231], v103 offset:20992\n\t"
        "ds_read_b64_tr_b16 a[232:233], v103 offset:20488\n\t"
        "v_mfma_f32_16x16x32_bf16 v[28:31], v[60:63], a[200:203], v[28:31]\n\t"
        "ds_read_b64_tr_b16 a[234:235], v103 offset:21000\n\t"
        "ds_read_b64_tr_b16 a[236:237], v104 offset:18432\n\t"
        "v_mfma_f32_16x16x32_bf16 v[8:11], a[204:207], v[64:67], v[8:11]\n\t"
        "ds_read_b64_tr_b16 a[238:239], v104 offset:18688\n\t"
        "ds_read_b128 v[72:75], v107 offset:8192\n\t"
        "v_mfma_f32_16x16x32_bf16 v[12:15], a[208:211], v[64:67], v[12:15]\n\t"
        "ds_read_b128 v[76:79], v107 offset:10240\n\t"
        "ds_read_b128 v[80:83], v108 offset:8192\n\t"
        "v_mfma_f32_16x16x32_bf16 v[16:19], v[64:67], a[212:215], v[16:19]\n\t"
        "ds_read_b128 v[84:87], v108 offset:10240\n\t"
        "v_mfma_f32_16x16x32_bf16 v[20:23], a[204:207], v[68:71], v[20:23]\n\t"
        "v_mfma_f32_16x16x32_bf16 v[24:27], a[208:211], v[68:71], v[24:27]\n\t"
        "v_mfma_f32_16x16x32_bf16 v[28:31], v[68:71], a[212:215], v[28:31]\n\t"
        "s_mov_b32 %[t0], 6815744\n\t"
        "buffer_load_dwordx4 v[220:223], v101, %[sq], %[t0] offen\n\t"
        "buffer_load_dwordx4 v[228:231], v112, %[sv], %[t0] offen\n\t"
        "s_mov_b32 %[t0], 7077888\n\t"
        "buffer_load_dwordx4 v[224:227], v101, %[sq], %[t0] offen\n\t"
        "s_waitcnt vmcnt(17)\n\t"
        "ds_write_b128 v102, v[184:187] offset:0\n\t"
        "ds_write_b128 v102, v[188:191] offset:4096\n\t"
        "ds_write_b128 v116, v[192:195] offset:8192\n\t"
        "s_waitcnt lgkmcnt(0)\n\t"
        "s_barrier\n\t"
        "v_mfma_f32_16x16x32_bf16 v[8:11], a[216:219], v[72:75], v[8:11]\n\t"
        "ds_read_b64_tr_b16 a[240:241], v103 offset:0\n\t"
        "ds_read_b64_tr_b16 a[242:243], v103 offset:512\n\t"
        "v_mfma_f32_16x16x32_bf16 v[12:15], a[220:223], v[72:75], v[12:15]\n\t"
        "ds_read_b64_tr_b16 a[244:245], v103 offset:8\n\t"
        "ds_read_b64_tr_b16 a[246:247], v103 offset:520\n\t"
        "v_mfma_f32_16x16x32_bf16 v[16:19], v[72:75], a[224:227], v[16:19]\n\t"
        "ds_read_b64_tr_b16 a[248:249], v104 offset:0\n\t"
        "ds_read_b64_tr_b16 a[250:251], v104 offset:256\n\t"
        "v_mfma_f32_16x16x32_bf16 v[20:23], a[216:219], v[76:79], v[20:23]\n\t"
        "s_waitcnt lgkmcnt(5)\n\t"
        "ds_read_b64_tr_b16 a[252:253], v103 offset:4096\n\t"
        "v_mfma_f32_16x16x32_bf16 v[24:27], a[220:223], v[76:79], v[24:27]\n\t"
        "ds_read_b64_tr_b16 a[254:255], v103 offset:4608\n\t"
        "ds_read_b64_tr_b16 v[128:129], v103 offset:4104\n\t"
        "v_mfma_f32_16x16x32_bf16 v[28:31], v[76:79], a[224:227], v[28:31]\n\t"
        "ds_read_b64_tr_b16 v[130:131], v103 offset:4616\n\t"
        "ds_read_b64_tr_b16 v[132:133], v104 offset:2048\n\t"
        "v_mfma_f32_16x16x32_bf16 v[8:11], a[228:231], v[80:83], v[8:11]\n\t"
        "ds_read_b64_tr_b16 v[134:135], v104 offset:2304\n\t"
        "ds_read_b128 v[56:59], v107 offset:16384\n\t"
        "v_mfma_f32_16x16x32_bf16 v[12:15], a[232:235], v[80:83], v[12:15]\n\t"
        "ds_read_b128 v[60:63], v107 offset:18432\n\t"
        "ds_read_b128 v[64:67], v108 offset:16384\n\t"
        "v_mfma_f32_16x16x32_bf16 v[16:19], v[80:83], a[236:239], v[16:19]\n\t"
        "ds_read_b128 v[68:71], v108 offset:18432\n\t"
        "v_mfma_f32_16x16x32_bf16 v[20:23], a[228:231], v[84:87], v[20:23]\n\t"
        "v_mfma_f32_16x16x32_bf16 v[24:27], a[232:235], v[84:87], v[24:27]\n\t"
        "v_mfma_f32_16x16x32_bf16 v[28:31], v[84:87], a[236:239], v[28:31]\n\t"
        "s_mov_b32 %[t0], 7340032\n\t"
        "buffer_load_dwordx4 v[232:235], v101, %[sq], %[t0] offen\n\t"
        "buffer_load_dwordx4 v[240:243], v112, %[sv], %[t0] offen\n\t"
        "s_mov_b32 %[t0], 7602176\n\t"
        "buffer_load_dwordx4 v[236:239], v101, %[sq], %[t0] offen\n\t"
        "s_waitcnt vmcnt(17)\n\t"
        "ds_write_b128 v102, v[196:199] offset:16384\n\t"
        "ds_write_b128 v102, v[200:203] offset:20480\n\t"
        "ds_write_b128 v116, v[204:207] offset:24576\n\t"
        "s_waitcnt lgkmcnt(0)\n\t"
        "s_barrier\n\t"
        "v_mfma_f32_16x16x32_bf16 v[8:11], a[240:243], v[56:59], v[8:11]\n\t"
        "ds_read_b64_tr_b16 v[136:137], v103 offset:16384\n\t"
        "ds_read_b64_tr_b16 v[138:139], v103 offset:16896\n\t"
        "v_mfma_f32_16x16x32_bf16 v[12:15], a[244:247], v[56:59], v[12:15]\n\t"
        "ds_read_b64_tr_b16 v[140:141], v103 offset:16392\n\t"
        "ds_read_b64_tr_b16 v[142:143], v103 offset:16904\n\t"
        "v_mfma_f32_16x16x32_bf16 v[16:19], v[56:59], a[248:251], v[16:19]\n\t"
        "ds_read_b64_tr_b16 v[144:145], v104 offset:16384\n\t"
        "ds_read_b64_tr_b16 v[146:147], v104 offset:16640\n\t"
        "v_mfma_f32_16x16x32_bf16 v[20:23], a[240:243], v[60:63], v[20:23]\n\t"
        "s_waitcnt lgkmcnt(5)\n\t"
        "ds_read_b64_tr_b16 v[148:149], v103 offset:20480\n\t"
        "v_mfma_f32_16x16x32_bf16 v[24:27], a[244:247], v[60:63], v[24:27]\n\t"
        "ds_read_b64_tr_b16 v[150:151], v103 offset:20992\n\t"
        "ds_read_b64_tr_b16 v[152:153], v103 offset:20488\n\t"
        "v_mfma_f32_16x16x32_bf16 v[28:31], v[60:63], a[248:251], v[28:31]\n\t"
        "ds_read_b64_tr_b16 v[154:155], v103 offset:21000\n\t"
        "ds_read_b64_tr_b16 v[156:157], v104 offset:18432\n\t"
        "v_mfma_f32_16x16x32_bf16 v[8:11], a[252:255], v[64:67], v[8:11]\n\t"
        "ds_read_b64_tr_b16 v[158:159], v104 offset:18688\n\t"
        "ds_read_b128 v[72:75], v107 offset:24576\n\t"
        "v_mfma_f32_16x16x32_bf16 v[12:15], v[128:131], v[64:67], v[12:15]\n\t"
        "ds_read_b128 v[76:79], v107 offset:26624\n\t"
        "ds_read_b128 v[80:83], v108 offset:24576\n\t"
        "v_mfma_f32_16x16x32_bf16 v[16:19], v[64:67], v[132:135], v[16:19]\n\t"
        "ds_read_b128 v[84:87], v108 offset:26624\n\t"
        "v_mfma_f32_16x16x32_bf16 v[20:23], a[252:255], v[68:71], v[20:23]\n\t"
        "v_mfma_f32_16x16x32_bf16 v[24:27], v[128:131], v[68:71], v[24:27]\n\t"
        "v_mfma_f32_16x16x32_bf16 v[28:31], v[68:71], v[132:135], v[28:31]\n\t"
        "s_mov_b32 %[t0], 7864320\n\t"
        "buffer_load_dwordx4 v[244:247], v101, %[sq], %[t0] offen\n\t"
        "buffer_load_dwordx4 v[252:255], v112, %[sv], %[t0] offen\n\t"
        "s_mov_b32 %[t0], 8126464\n\t"
        "buffer_load_dwordx4 v[248:251], v101, %[sq], %[t0] offen\n\t"
        "s_waitcnt vmcnt(9)\n\t"
        "ds_write_b128 v102, v[208:211] offset:0\n\t"
        "ds_write_b128 v102, v[212:215] offset:4096\n\t"
        "ds_write_b128 v116, v[216:219] offset:8192\n\t"
        "s_waitcnt lgkmcnt(0)\n\t"
        "s_barrier\n\t"
        "v_mfma_f32_16x16x32_bf16 v[8:11], v[136:139], v[72:75], v[8:11]\n\t"
        "ds_read_b64_tr_b16 v[160:161], v103 offset:0\n\t"
        "ds_read_b64_tr_b16 v[162:163], v103 offset:512\n\t"
        "v_mfma_f32_16x16x32_bf16 v[12:15], v[140:143], v[72:75], v[12:15]\n\t"
        "ds_read_b64_tr_b16 v[164:165], v103 offset:8\n\t"
        "ds_read_b64_tr_b16 v[166:167], v103 offset:520\n\t"
        "v_mfma_f32_16x16x32_bf16 v[16:19], v[72:75], v[144:147], v[16:19]\n\t"
        "ds_read_b64_tr_b16 v[168:169], v104 offset:0\n\t"
        "ds_read_b64_tr_b16 v[170:171], v104 offset:256\n\t"
        "v_mfma_f32_16x16x32_bf16 v[20:23], v[136:139], v[76:79], v[20:23]\n\t"
        "s_waitcnt lgkmcnt(5)\n\t"
        "ds_read_b64_tr_b16 v[172:173], v103 offset:4096\n\t"
        "v_mfma_f32_16x16x32_bf16 v[24:27], v[140:143], v[76:79], v[24:27]\n\t"
        "ds_read_b64_tr_b16 v[174:175], v103 offset:4608\n\t"
        "ds_read_b64_tr_b16 v[176:177], v103 offset:4104\n\t"
        "v_mfma_f32_16x16x32_bf16 v[28:31], v[76:79], v[144:147], v[28:31]\n\t"
        "ds_read_b64_tr_b16 v[178:179], v103 offset:4616\n\t"
        "ds_read_b64_tr_b16 v[180:181], v104 offset:2048\n\t"
        "v_mfma_f32_16x16x32_bf16 v[8:11], v[148:151], v[80:83], v[8:11]\n\t"
        "ds_read_b64_tr_b16 v[182:183], v104 offset:2304\n\t"
        "ds_read_b128 v[56:59], v107 offset:32768\n\t"
        "v_mfma_f32_16x16x32_bf16 v[12:15], v[152:155], v[80:83], v[12:15]\n\t"
        "ds_read_b128 v[60:63], v107 offset:34816\n\t"
        "ds_read_b128 v[64:67], v108 offset:32768\n\t"
        "v_mfma_f32_16x16x32_bf16 v[16:19], v[80:83], v[156:159], v[16:19]\n\t"
        "ds_read_b128 v[68:71], v108 offset:34816\n\t"
        "v_mfma_f32_16x16x32_bf16 v[20:23], v[148:151], v[84:87], v[20:23]\n\t"
        "v_mfma_f32_16x16x32_bf16 v[24:27], v[152:155], v[84:87], v[24:27]\n\t"
        "v_mfma_f32_16x16x32_bf16 v[28:31], v[84:87], v[156:159], v[28:31]\n\t"
        "s_mov_b32 %[xo], 0x20000\n\t"
        "s_add_u32 m0, %[ld], 0\n\t"
        "s_add_u32 %[t0], %[xo], 0\n\t"
        "buffer_load_dwordx4 v100, %[sx], %[t0] offen lds\n\t"
        "s_add_u32 m0, %[ld], 1024\n\t"
        "s_add_u32 %[t0], %[xo], 16384\n\t"
        "buffer_load_dwordx4 v100, %[sx], %[t0] offen lds\n\t"
        "s_add_u32 m0, %[ld], 8192\n\t"
        "s_add_u32 %[t0], %[xo], 128\n\t"
        "buffer_load_dwordx4 v100, %[sx], %[t0] offen lds\n\t"
        "s_add_u32 m0, %[ld], 9216\n\t"
        "s_add_u32 %[t0], %[xo], 16512\n\t"
        "buffer_load_dwordx4 v100, %[sx], %[t0] offen lds\n\t"
        "s_add_u32 m0, %[ld], 16384\n\t"
        "s_add_u32 %[t0], %[xo], 256\n\t"
        "buffer_load_dwordx4 v100, %[sx], %[t0] offen lds\n\t"
        "s_add_u32 m0, %[ld], 17408\n\t"
        "s_add_u32 %[t0], %[xo], 16640\n\t"
        "buffer_load_dwordx4 v100, %[sx], %[t0] offen lds\n\t"
        "s_add_u32 m0, %[ld], 24576\n\t"
        "s_add_u32 %[t0], %[xo], 384\n\t"
        "buffer_load_dwordx4 v100, %[sx], %[t0] offen lds\n\t"
        "s_add_u32 m0, %[ld], 25600\n\t"
        "s_add_u32 %[t0], %[xo], 16768\n\t"
        "buffer_load_dwordx4 v100, %[sx], %[t0] offen lds\n\t"
        "s_mov_b32 %[xo], 0\n\t"
        "s_waitcnt vmcnt(14)\n\t"
        "ds_write_b128 v102, v[220:223] offset:16384\n\t"
        "ds_write_b128 v102, v[224:227] offset:20480\n\t"
        "ds_write_b128 v116, v[228:231] offset:24576\n\t"
        "s_waitcnt lgkmcnt(0)\n\t"
        "s_barrier\n\t"
        "v_mfma_f32_16x16x32_bf16 v[8:11], v[160:163], v[56:59], v[8:11]\n\t"
        "ds_read_b64_tr_b16 v[184:185], v103 offset:16384\n\t"
        "ds_read_b64_tr_b16 v[186:187], v103 offset:16896\n\t"
        "v_mfma_f32_16x16x32_bf16 v[12:15], v[164:167], v[56:59], v[12:15]\n\t"
        "ds_read_b64_tr_b16 v[188:189], v103 offset:16392\n\t"
        "ds_read_b64_tr_b16 v[190:191], v103 offset:16904\n\t"
        "v_mfma_f32_16x16x32_bf16 v[16:19], v[56:59], v[168:171], v[16:19]\n\t"
        "ds_read_b64_tr_b16 v[192:193], v104 offset:16384\n\t"
        "ds_read_b64_tr_b16 v[194:195], v104 offset:16640\n\t"
        "v_mfma_f32_16x16x32_bf16 v[20:23], v[160:163], v[60:63], v[20:23]\n\t"
        "s_waitcnt lgkmcnt(5)\n\t"
        "ds_read_b64_tr_b16 v[196:197], v103 offset:20480\n\t"
        "v_mfma_f32_16x16x32_bf16 v[24:27], v[164:167], v[60:63], v[24:27]\n\t"
        "ds_read_b64_tr_b16 v[198:199], v103 offset:20992\n\t"
        "ds_read_b64_tr_b16 v[200:201], v103 offset:20488\n\t"
        "v_mfma_f32_16x16x32_bf16 v[28:31], v[60:63], v[168:171], v[28:31]\n\t"
        "ds_read_b64_tr_b16 v[202:203], v103 offset:21000\n\t"
        "ds_read_b64_tr_b16 v[204:205], v104 offset:18432\n\t"
        "v_mfma_f32_16x16x32_bf16 v[8:11], v[172:175], v[64:67], v[8:11]\n\t"
        "ds_read_b64_tr_b16 v[206:207], v104 offset:18688\n\t"
        "ds_read_b128 v[72:75], v107 offset:40960\n\t"
        "v_mfma_f32_16x16x32_bf16 v[12:15], v[176:179], v[64:67], v[12:15]\n\t"
        "ds_read_b128 v[76:79], v107 offset:43008\n\t"
        "ds_read_b128 v[80:83], v108 offset:40960\n\t"
        "v_mfma_f32_16x16x32_bf16 v[16:19], v[64:67], v[180:183], v[16:19]\n\t"
        "ds_read_b128 v[84:87], v108 offset:43008\n\t"
        "v_mfma_f32_16x16x32_bf16 v[20:23], v[172:175], v[68:71], v[20:23]\n\t"
        "v_mfma_f32_16x16x32_bf16 v[24:27], v[176:179], v[68:71], v[24:27]\n\t"
        "v_mfma_f32_16x16x32_bf16 v[28:31], v[68:71], v[180:183], v[28:31]\n\t"
        "s_waitcnt vmcnt(11)\n\t"
        "ds_write_b128 v102, v[232:235] offset:0\n\t"
        "ds_write_b128 v102, v[236:239] offset:4096\n\t"
        "ds_write_b128 v116, v[240:243] offset:8192\n\t"
        "s_waitcnt lgkmcnt(0)\n\t"
        "s_barrier\n\t"
        "v_mfma_f32_16x16x32_bf16 v[8:11], v[184:187], v[72:75], v[8:11]\n\t"
        "ds_read_b64_tr_b16 v[208:209], v103 offset:0\n\t"
        "ds_read_b64_tr_b16 v[210:211], v103 offset:512\n\t"
        "v_mfma_f32_16x16x32_bf16 v[12:15], v[188:191], v[72:75], v[12:15]\n\t"
        "ds_read_b64_tr_b16 v[212:213], v103 offset:8\n\t"
        "ds_read_b64_tr_b16 v[214:215], v103 offset:520\n\t"
        "v_mfma_f32_16x16x32_bf16 v[16:19], v[72:75], v[192:195], v[16:19]\n\t"
        "ds_read_b64_tr_b16 v[216:217], v104 offset:0\n\t"
        "ds_read_b64_tr_b16 v[218:219], v104 offset:256\n\t"
        "v_mfma_f32_16x16x32_bf16 v[20:23], v[184:187], v[76:79], v[20:23]\n\t"
        "s_waitcnt lgkmcnt(5)\n\t"
        "ds_read_b64_tr_b16 v[220:221], v103 offset:4096\n\t"
        "v_mfma_f32_16x16x32_bf16 v[24:27], v[188:191], v[76:79], v[24:27]\n\t"
        "ds_read_b64_tr_b16 v[222:223], v103 offset:4608\n\t"
        "ds_read_b64_tr_b16 v[224:225], v103 offset:4104\n\t"
        "v_mfma_f32_16x16x32_bf16 v[28:31], v[76:79], v[192:195], v[28:31]\n\t"
        "ds_read_b64_tr_b16 v[226:227], v103 offset:4616\n\t"
        "ds_read_b64_tr_b16 v[228:229], v104 offset:2048\n\t"
        "v_mfma_f32_16x16x32_bf16 v[8:11], v[196:199], v[80:83], v[8:11]\n\t"
        "ds_read_b64_tr_b16 v[230:231], v104 offset:2304\n\t"
        "ds_read_b128 v[56:59], v107 offset:49152\n\t"
        "v_mfma_f32_16x16x32_bf16 v[12:15], v[200:203], v[80:83], v[12:15]\n\t"
        "ds_read_b128 v[60:63], v107 offset:51200\n\t"
        "ds_read_b128 v[64:67], v108 offset:49152\n\t"
        "v_mfma_f32_16x16x32_bf16 v[16:19], v[80:83], v[204:207], v[16:19]\n\t"
        "ds_read_b128 v[68:71], v108 offset:51200\n\t"
        "v_mfma_f32_16x16x32_bf16 v[20:23], v[196:199], v[84:87], v[20:23]\n\t"
        "v_mfma_f32_16x16x32_bf16 v[24:27], v[200:203], v[84:87], v[24:27]\n\t"
        "v_mfma_f32_16x16x32_bf16 v[28:31], v[84:87], v[204:207], v[28:31]\n\t"
        "s_waitcnt vmcnt(8)\n\t"
        "ds_write_b128 v102, v[244:247] offset:16384\n\t"
        "ds_write_b128 v102, v[248:251] offset:20480\n\t"
        "ds_write_b128 v116, v[252:255] offset:24576\n\t"
        "s_waitcnt lgkmcnt(0)\n\t"
        "s_barrier\n\t"
        "v_mfma_f32_16x16x32_bf16 v[8:11], v[208:211], v[56:59], v[8:11]\n\t"
        "ds_read_b64_tr_b16 v[232:233], v103 offset:16384\n\t"
        "ds_read_b64_tr_b16 v[234:235], v103 offset:16896\n\t"
        "v_mfma_f32_16x16x32_bf16 v[12:15], v[212:215], v[56:59], v[12:15]\n\t"
        "ds_read_b64_tr_b16 v[236:237], v103 offset:16392\n\t"
        "ds_read_b64_tr_b16 v[238:239], v103 offset:16904\n\t"
        "v_mfma_f32_16x16x32_bf16 v[16:19], v[56:59], v[216:219], v[16:19]\n\t"
        "ds_read_b64_tr_b16 v[240:241], v104 offset:16384\n\t"
        "ds_read_b64_tr_b16 v[242:243], v104 offset:16640\n\t"
        "v_mfma_f32_16x16x32_bf16 v[20:23], v[208:211], v[60:63], v[20:23]\n\t"
        "s_waitcnt lgkmcnt(5)\n\t"
        "ds_read_b64_tr_b16 v[244:245], v103 offset:20480\n\t"
        "v_mfma_f32_16x16x32_bf16 v[24:27], v[212:215], v[60:63], v[24:27]\n\t"
        "ds_read_b64_tr_b16 v[246:247], v103 offset:20992\n\t"
        "ds_read_b64_tr_b16 v[248:249], v103 offset:20488\n\t"
        "v_mfma_f32_16x16x32_bf16 v[28:31], v[60:63], v[216:219], v[28:31]\n\t"
        "ds_read_b64_tr_b16 v[250:251], v103 offset:21000\n\t"
        "ds_read_b64_tr_b16 v[252:253], v104 offset:18432\n\t"
        "v_mfma_f32_16x16x32_bf16 v[8:11], v[220:223], v[64:67], v[8:11]\n\t"
        "ds_read_b64_tr_b16 v[254:255], v104 offset:18688\n\t"
        "ds_read_b128 v[72:75], v107 offset:57344\n\t"
        "v_mfma_f32_16x16x32_bf16 v[12:15], v[224:227], v[64:67], v[12:15]\n\t"
        "ds_read_b128 v[76:79], v107 offset:59392\n\t"
        "ds_read_b128 v[80:83], v108 offset:57344\n\t"
        "v_mfma_f32_16x16x32_bf16 v[16:19], v[64:67], v[228:231], v[16:19]\n\t"
        "ds_read_b128 v[84:87], v108 offset:59392\n\t"
        "v_mfma_f32_16x16x32_bf16 v[20:23], v[220:223], v[68:71], v[20:23]\n\t"
        "v_mfma_f32_16x16x32_bf16 v[24:27], v[224:227], v[68:71], v[24:27]\n\t"
        "v_mfma_f32_16x16x32_bf16 v[28:31], v[68:71], v[228:231], v[28:31]\n\t"
        "s_waitcnt vmcnt(0) lgkmcnt(0)\n\t"
        "s_barrier\n\t"
        "v_mfma_f32_16x16x32_bf16 v[8:11], v[232:235], v[72:75], v[8:11]\n\t"
        "v_mfma_f32_16x16x32_bf16 v[12:15], v[236:239], v[72:75], v[12:15]\n\t"
        "v_mfma_f32_16x16x32_bf16 v[16:19], v[72:75], v[240:243], v[16:19]\n\t"
        "v_mfma_f32_16x16x32_bf16 v[20:23], v[232:235], v[76:79], v[20:23]\n\t"
        "v_mfma_f32_16x16x32_bf16 v[24:27], v[236:239], v[76:79], v[24:27]\n\t"
        "v_mfma_f32_16x16x32_bf16 v[28:31], v[76:79], v[240:243], v[28:31]\n\t"
        "v_mfma_f32_16x16x32_bf16 v[8:11], v[244:247], v[80:83], v[8:11]\n\t"
        "v_mfma_f32_16x16x32_bf16 v[12:15], v[248:251], v[80:83], v[12:15]\n\t"
        "v_mfma_f32_16x16x32_bf16 v[16:19], v[80:83], v[252:255], v[16:19]\n\t"
        "v_mfma_f32_16x16x32_bf16 v[20:23], v[244:247], v[84:87], v[20:23]\n\t"
        "v_mfma_f32_16x16x32_bf16 v[24:27], v[248:251], v[84:87], v[24:27]\n\t"
        "v_mfma_f32_16x16x32_bf16 v[28:31], v[84:87], v[252:255], v[28:31]\n\t"
        "s_mov_b32 %[xo], 0x20000\n\t"
        "ds_read_b128 v[56:59], v105 offset:0\n\t"
        "ds_read_b128 v[60:63], v105 offset:2048\n\t"
        "ds_read_b128 v[64:67], v106 offset:0\n\t"
        "ds_read_b128 v[68:71], v106 offset:2048\n\t"
        "ds_read_b128 v[72:75], v105 offset:8192\n\t"
        "ds_read_b128 v[76:79], v105 offset:10240\n\t"
        "ds_read_b128 v[80:83], v106 offset:8192\n\t"
        "v_cvt_pk_bf16_f32 v88, v8, v9\n\t"
        "v_cvt_pk_bf16_f32 v89, v10, v11\n\t"
        "v_cvt_pk_bf16_f32 v90, v12, v13\n\t"
        "v_cvt_pk_bf16_f32 v91, v14, v15\n\t"
        "buffer_store_dwordx4 v[88:91], v109, %[sw], %[so] offen sc1\n\t"
        "v_cvt_pk_bf16_f32 v92, v20, v21\n\t"
        "v_cvt_pk_bf16_f32 v93, v22, v23\n\t"
        "v_cvt_pk_bf16_f32 v94, v24, v25\n\t"
        "v_cvt_pk_bf16_f32 v95, v26, v27\n\t"
        "buffer_store_dwordx4 v[92:95], v110, %[sw], %[so] offen sc1\n\t"
        "v_cvt_pk_bf16_f32 v96, v16, v17\n\t"
        "v_cvt_pk_bf16_f32 v97, v18, v19\n\t"
        "v_cvt_pk_bf16_f32 v98, v28, v29\n\t"
        "v_cvt_pk_bf16_f32 v99, v30, v31\n\t"
        "buffer_store_dwordx4 v[96:99], v111, %[sw], %[so] offen sc1\n\t"
        "s_mov_b32 %[so], 0xf0000000\n\t"
        "s_waitcnt vmcnt(0)\n\t"
        "v_mov_b32_e32 v117, %[cn]\n\t"
        "buffer_store_dword v117, v113, %[sw], 0 offen sc1\n\t"
        "s_add_u32 m0, %[ld], 32768\n\t"
        "s_add_u32 %[t0], %[xo], 512\n\t"
        "buffer_load_dwordx4 v100, %[sx], %[t0] offen lds\n\t"
        "s_add_u32 m0, %[ld], 33792\n\t"
        "s_add_u32 %[t0], %[xo], 16896\n\t"
        "buffer_load_dwordx4 v100, %[sx], %[t0] offen lds\n\t"
        "s_add_u32 m0, %[ld], 40960\n\t"
        "s_add_u32 %[t0], %[xo], 640\n\t"
        "buffer_load_dwordx4 v100, %[sx], %[t0] offen lds\n\t"
        "s_add_u32 m0, %[ld], 41984\n\t"
        "s_add_u32 %[t0], %[xo], 17024\n\t"
        "buffer_load_dwordx4 v100, %[sx], %[t0] offen lds\n\t"
        "s_add_u32 m0, %[ld], 49152\n\t"
        "s_add_u32 %[t0], %[xo], 768\n\t"
        "buffer_load_dwordx4 v100, %[sx], %[t0] offen lds\n\t"
        "s_add_u32 m0, %[ld], 50176\n\t"
        "s_add_u32 %[t0], %[xo], 17152\n\t"
        "buffer_load_dwordx4 v100, %[sx], %[t0] offen lds\n\t"
        "s_add_u32 m0, %[ld], 57344\n\t"
        "s_add_u32 %[t0], %[xo], 896\n\t"
        "buffer_load_dwordx4 v100, %[sx], %[t0] offen lds\n\t"
        "s_add_u32 m0, %[ld], 58368\n\t"
        "s_add_u32 %[t0], %[xo], 17280\n\t"
        "buffer_load_dwordx4 v100, %[sx], %[t0] offen lds\n\t"
        "s_add_u32 m0, %[ld], 65536\n\t"
        "s_add_u32 %[t0], %[xo], 1024\n\t"
        "buffer_load_dwordx4 v100, %[sx], %[t0] offen lds\n\t"
        "s_add_u32 m0, %[ld], 66560\n\t"
        "s_add_u32 %[t0], %[xo], 17408\n\t"
        "buffer_load_dwordx4 v100, %[sx], %[t0] offen lds\n\t"
        "s_add_u32 m0, %[ld], 73728\n\t"
        "s_add_u32 %[t0], %[xo], 1152\n\t"
        "buffer_load_dwordx4 v100, %[sx], %[t0] offen lds\n\t"
        "s_add_u32 m0, %[ld], 74752\n\t"
        "s_add_u32 %[t0], %[xo], 17536\n\t"
        "buffer_load_dwordx4 v100, %[sx], %[t0] offen lds\n\t"
        "s_add_u32 m0, %[ld], 81920\n\t"
        "s_add_u32 %[t0], %[xo], 1280\n\t"
        "buffer_load_dwordx4 v100, %[sx], %[t0] offen lds\n\t"
        "s_add_u32 m0, %[ld], 82944\n\t"
        "s_add_u32 %[t0], %[xo], 17664\n\t"
        "buffer_load_dwordx4 v100, %[sx], %[t0] offen lds\n\t"
        "s_add_u32 m0, %[ld], 90112\n\t"
        "s_add_u32 %[t0], %[xo], 1408\n\t"
        "buffer_load_dwordx4 v100, %[sx], %[t0] offen lds\n\t"
        "s_add_u32 m0, %[ld], 91136\n\t"
        "s_add_u32 %[t0], %[xo], 17792\n\t"
        "buffer_load_dwordx4 v100, %[sx], %[t0] offen lds\n\t"
        "s_add_u32 m0, %[ld], 98304\n\t"
        "s_add_u32 %[t0], %[xo], 1536\n\t"
        "buffer_load_dwordx4 v100, %[sx], %[t0] offen lds\n\t"
        "s_add_u32 m0, %[ld], 99328\n\t"
        "s_add_u32 %[t0], %[xo], 17920\n\t"
        "buffer_load_dwordx4 v100, %[sx], %[t0] offen lds\n\t"
        "s_add_u32 m0, %[ld], 106496\n\t"
        "s_add_u32 %[t0], %[xo], 1664\n\t"
        "buffer_load_dwordx4 v100, %[sx], %[t0] offen lds\n\t"
        "s_branch LG_mid%=\n\t"
        "LG_loop%=:\n\t"
        "s_waitcnt lgkmcnt(6)\n\t"
        "v_mfma_f32_16x16x32_bf16 v[8:11], a[0:3], v[56:59], 0\n\t"
        "v_mfma_f32_16x16x32_bf16 v[12:15], a[4:7], v[56:59], 0\n\t"
        "ds_read_b128 v[84:87], v106 offset:10240\n\t"
        "v_mfma_f32_16x16x32_bf16 v[16:19], v[56:59], a[8:11], 0\n\t"
        "s_add_u32 m0, %[ld], 107520\n\t"
        "s_add_u32 %[t0], %[xo], 18048\n\t"
        "s_waitcnt lgkmcnt(6)\n\t"
        "v_mfma_f32_16x16x32_bf16 v[20:23], a[0:3], v[60:63], 0\n\t"
        "buffer_load_dwordx4 v100, %[sx], %[t0] offen lds\n\t"
        "v_mfma_f32_16x16x32_bf16 v[24:27], a[4:7], v[60:63], 0\n\t"
        "ds_read_b128 v[56:59], v105 offset:16384\n\t"
        "v_mfma_f32_16x16x32_bf16 v[28:31], v[60:63], a[8:11], 0\n\t"
        "s_waitcnt lgkmcnt(6)\n\t"
        "v_mfma_f32_16x16x32_bf16 v[8:11], a[12:15], v[64:67], v[8:11]\n\t"
        "v_cvt_pk_bf16_f32 v88, v32, v33\n\t"
        "v_cvt_pk_bf16_f32 v89, v34, v35\n\t"
        "v_mfma_f32_16x16x32_bf16 v[12:15], a[16:19], v[64:67], v[12:15]\n\t"
        "ds_read_b128 v[60:63], v105 offset:18432\n\t"
        "v_cvt_pk_bf16_f32 v90, v36, v37\n\t"
        "v_mfma_f32_16x16x32_bf16 v[16:19], v[64:67], a[20:23], v[16:19]\n\t"
        "s_add_u32 m0, %[ld], 114688\n\t"
        "s_add_u32 %[t0], %[xo], 1792\n\t"
        "s_waitcnt lgkmcnt(6)\n\t"
        "v_mfma_f32_16x16x32_bf16 v[20:23], a[12:15], v[68:71], v[20:23]\n\t"
        "buffer_load_dwordx4 v100, %[sx], %[t0] offen lds\n\t"
        "v_mfma_f32_16x16x32_bf16 v[24:27], a[16:19], v[68:71], v[24:27]\n\t"
        "ds_read_b128 v[64:67], v106 offset:16384\n\t"
        "v_cvt_pk_bf16_f32 v91, v38, v39\n\t"
        "v_mfma_f32_16x16x32_bf16 v[28:31], v[68:71], a[20:23], v[28:31]\n\t"
        "buffer_store_dwordx4 v[88:91], v109, %[sw], %[so] offen sc1\n\t"
        "s_waitcnt lgkmcnt(6)\n\t"
        "v_mfma_f32_16x16x32_bf16 v[8:11], a[24:27], v[72:75], v[8:11]\n\t"
        "v_cvt_pk_bf16_f32 v92, v44, v45\n\t"
        "v_cvt_pk_bf16_f32 v93, v46, v47\n\t"
        "v_mfma_f32_16x16x32_bf16 v[12:15], a[28:31], v[72:75], v[12:15]\n\t"
        "ds_read_b128 v[68:71], v106 offset:18432\n\t"
        "v_cvt_pk_bf16_f32 v94, v48, v49\n\t"
        "v_mfma_f32_16x16x32_bf16 v[16:19], v[72:75], a[32:35], v[16:19]\n\t"
        "s_add_u32 m0, %[ld], 115712\n\t"
        "s_add_u32 %[t0], %[xo], 18176\n\t"
        "s_waitcnt lgkmcnt(6)\n\t"
        "v_mfma_f32_16x16x32_bf16 v[20:23], a[24:27], v[76:79], v[20:23]\n\t"
        "buffer_load_dwordx4 v100, %[sx], %[t0] offen lds\n\t"
        "v_mfma_f32_16x16x32_bf16 v[24:27], a[28:31], v[76:79], v[24:27]\n\t"
        "ds_read_b128 v[72:75], v105 offset:24576\n\t"
        "v_cvt_pk_bf16_f32 v95, v50, v51\n\t"
        "v_mfma_f32_16x16x32_bf16 v[28:31], v[76:79], a[32:35], v[28:31]\n\t"
        "buffer_store_dwordx4 v[92:95], v110, %[sw], %[so] offen sc1\n\t"
        "s_waitcnt lgkmcnt(6)\n\t"
        "v_mfma_f32_16x16x32_bf16 v[8:11], a[36:39], v[80:83], v[8:11]\n\t"
        "v_cvt_pk_bf16_f32 v96, v40, v41\n\t"
        "v_cvt_pk_bf16_f32 v97, v42, v43\n\t"
        "v_mfma_f32_16x16x32_bf16 v[12:15], a[40:43], v[80:83], v[12:15]\n\t"
        "ds_read_b128 v[76:79], v105 offset:26624\n\t"
        "v_cvt_pk_bf16_f32 v98, v52, v53\n\t"
        "v_mfma_f32_16x16x32_bf16 v[16:19], v[80:83], a[44:47], v[16:19]\n\t"
        "s_add_u32 m0, %[ld], 122880\n\t"
        "s_add_u32 %[t0], %[xo], 1920\n\t"
        "s_waitcnt lgkmcnt(6)\n\t"
        "v_mfma_f32_16x16x32_bf16 v[20:23], a[36:39], v[84:87], v[20:23]\n\t"
        "buffer_load_dwordx4 v100, %[sx], %[t0] offen lds\n\t"
        "v_mfma_f32_16x16x32_bf16 v[24:27], a[40:43], v[84:87], v[24:27]\n\t"
        "ds_read_b128 v[80:83], v106 offset:24576\n\t"
        "v_cvt_pk_bf16_f32 v99, v54, v55\n\t"
        "v_mfma_f32_16x16x32_bf16 v[28:31], v[84:87], a[44:47], v[28:31]\n\t"
        "buffer_store_dwordx4 v[96:99], v111, %[sw], %[so] offen sc1\n\t"
        "s_waitcnt lgkmcnt(6)\n\t"
        "v_mfma_f32_16x16x32_bf16 v[8:11], a[48:51], v[56:59], v[8:11]\n\t"
        "s_lshl_b32 %[so], %[cn], 19\n\t"
        "v_mfma_f32_16x16x32_bf16 v[12:15], a[52:55], v[56:59], v[12:15]\n\t"
        "ds_read_b128 v[84:87], v106 offset:26624\n\t"
        "v_mfma_f32_16x16x32_bf16 v[16:19], v[56:59], a[56:59], v[16:19]\n\t"
        "s_add_u32 m0, %[ld], 123904\n\t"
        "s_add_u32 %[t0], %[xo], 18304\n\t"
        "s_waitcnt lgkmcnt(6)\n\t"
        "v_mfma_f32_16x16x32_bf16 v[20:23], a[48:51], v[60:63], v[20:23]\n\t"
        "buffer_load_dwordx4 v100, %[sx], %[t0] offen lds\n\t"
        "v_mfma_f32_16x16x32_bf16 v[24:27], a[52:55], v[60:63], v[24:27]\n\t"
        "v_mfma_f32_16x16x32_bf16 v[28:31], v[60:63], a[56:59], v[28:31]\n\t"
        "s_waitcnt lgkmcnt(5)\n\t"
        "v_mfma_f32_16x16x32_bf16 v[8:11], a[60:63], v[64:67], v[8:11]\n\t"
        "s_waitcnt vmcnt(19) lgkmcnt(0)\n\t"
        "s_barrier\n\t"
        "s_add_u32 %[xo], %[xo], 0x20000\n\t"
        "v_mfma_f32_16x16x32_bf16 v[12:15], a[64:67], v[64:67], v[12:15]\n\t"
        "ds_read_b128 v[56:59], v105 offset:32768\n\t"
        "v_mfma_f32_16x16x32_bf16 v[16:19], v[64:67], a[68:71], v[16:19]\n\t"
        "ds_read_b128 v[60:63], v105 offset:34816\n\t"
        "s_add_u32 m0, %[ld], 0\n\t"
        "s_add_u32 %[t0], %[xo], 0\n\t"
        "v_mfma_f32_16x16x32_bf16 v[20:23], a[60:63], v[68:71], v[20:23]\n\t"
        "buffer_load_dwordx4 v100, %[sx], %[t0] offen lds\n\t"
        "v_mfma_f32_16x16x32_bf16 v[24:27], a[64:67], v[68:71], v[24:27]\n\t"
        "ds_read_b128 v[64:67], v106 offset:32768\n\t"
        "v_mfma_f32_16x16x32_bf16 v[28:31], v[68:71], a[68:71], v[28:31]\n\t"
        "v_mfma_f32_16x16x32_bf16 v[8:11], a[72:75], v[72:75], v[8:11]\n\t"
        "v_mfma_f32_16x16x32_bf16 v[12:15], a[76:79], v[72:75], v[12:15]\n\t"
        "ds_read_b128 v[68:71], v106 offset:34816\n\t"
        "v_mfma_f32_16x16x32_bf16 v[16:19], v[72:75], a[80:83], v[16:19]\n\t"
        "s_add_u32 m0, %[ld], 1024\n\t"
        "s_add_u32 %[t0], %[xo], 16384\n\t"
        "v_mfma_f32_16x16x32_bf16 v[20:23], a[72:75], v[76:79], v[20:23]\n\t"
        "buffer_load_dwordx4 v100, %[sx], %[t0] offen lds\n\t"
        "v_mfma_f32_16x16x32_bf16 v[24:27], a[76:79], v[76:79], v[24:27]\n\t"
        "ds_read_b128 v[72:75], v105 offset:40960\n\t"
        "v_mfma_f32_16x16x32_bf16 v[28:31], v[76:79], a[80:83], v[28:31]\n\t"
        "v_mfma_f32_16x16x32_bf16 v[8:11], a[84:87], v[80:83], v[8:11]\n\t"
        "v_mfma_f32_16x16x32_bf16 v[12:15], a[88:91], v[80:83], v[12:15]\n\t"
        "ds_read_b128 v[76:79], v105 offset:43008\n\t"
        "v_mfma_f32_16x16x32_bf16 v[16:19], v[80:83], a[92:95], v[16:19]\n\t"
        "s_add_u32 m0, %[ld], 8192\n\t"
        "s_add_u32 %[t0], %[xo], 128\n\t"
        "v_mfma_f32_16x16x32_bf16 v[20:23], a[84:87], v[84:87], v[20:23]\n\t"
        "buffer_load_dwordx4 v100, %[sx], %[t0] offen lds\n\t"
        "v_mfma_f32_16x16x32_bf16 v[24:27], a[88:91], v[84:87], v[24:27]\n\t"
        "ds_read_b128 v[80:83], v106 offset:40960\n\t"
        "v_mfma_f32_16x16x32_bf16 v[28:31], v[84:87], a[92:95], v[28:31]\n\t"
        "s_waitcnt lgkmcnt(6)\n\t"
        "v_mfma_f32_16x16x32_bf16 v[8:11], a[96:99], v[56:59], v[8:11]\n\t"
        "v_mfma_f32_16x16x32_bf16 v[12:15], a[100:103], v[56:59], v[12:15]\n\t"
        "ds_read_b128 v[84:87], v106 offset:43008\n\t"
        "v_mfma_f32_16x16x32_bf16 v[16:19], v[56:59], a[104:107], v[16:19]\n\t"
        "s_add_u32 m0, %[ld], 9216\n\t"
        "s_add_u32 %[t0], %[xo], 16512\n\t"
        "s_waitcnt lgkmcnt(6)\n\t"
        "v_mfma_f32_16x16x32_bf16 v[20:23], a[96:99], v[60:63], v[20:23]\n\t"
        "buffer_load_dwordx4 v100, %[sx], %[t0] offen lds\n\t"
        "v_mfma_f32_16x16x32_bf16 v[24:27], a[100:103], v[60:63], v[24:27]\n\t"
        "ds_read_b128 v[56:59], v105 offset:49152\n\t"
        "v_mfma_f32_16x16x32_bf16 v[28:31], v[60:63], a[104:107], v[28:31]\n\t"
        "s_waitcnt lgkmcnt(6)\n\t"
        "v_mfma_f32_16x16x32_bf16 v[8:11], a[108:111], v[64:67], v[8:11]\n\t"
        "v_mfma_f32_16x16x32_bf16 v[12:15], a[112:115], v[64:67], v[12:15]\n\t"
        "ds_read_b128 v[60:63], v105 offset:51200\n\t"
        "v_mfma_f32_16x16x32_bf16 v[16:19], v[64:67], a[116:119], v[16:19]\n\t"
        "s_add_u32 m0, %[ld], 16384\n\t"
        "s_add_u32 %[t0], %[xo], 256\n\t"
        "s_waitcnt lgkmcnt(6)\n\t"
        "v_mfma_f32_16x16x32_bf16 v[20:23], a[108:111], v[68:71], v[20:23]\n\t"
        "buffer_load_dwordx4 v100, %[sx], %[t0] offen lds\n\t"
        "v_mfma_f32_16x16x32_bf16 v[24:27], a[112:115], v[68:71], v[24:27]\n\t"
        "ds_read_b128 v[64:67], v106 offset:49152\n\t"
        "v_mfma_f32_16x16x32_bf16 v[28:31], v[68:71], a[116:119], v[28:31]\n\t"
        "s_waitcnt lgkmcnt(6)\n\t"
        "v_mfma_f32_16x16x32_bf16 v[8:11], a[120:123], v[72:75], v[8:11]\n\t"
        "v_mfma_f32_16x16x32_bf16 v[12:15], a[124:127], v[72:75], v[12:15]\n\t"
        "ds_read_b128 v[68:71], v106 offset:51200\n\t"
        "v_mfma_f32_16x16x32_bf16 v[16:19], v[72:75], a[128:131], v[16:19]\n\t"
        "s_add_u32 m0, %[ld], 17408\n\t"
        "s_add_u32 %[t0], %[xo], 16640\n\t"
        "s_waitcnt lgkmcnt(6)\n\t"
        "v_mfma_f32_16x16x32_bf16 v[20:23], a[120:123], v[76:79], v[20:23]\n\t"
        "buffer_load_dwordx4 v100, %[sx], %[t0] offen lds\n\t"
        "v_mfma_f32_16x16x32_bf16 v[24:27], a[124:127], v[76:79], v[24:27]\n\t"
        "ds_read_b128 v[72:75], v105 offset:57344\n\t"
        "v_mfma_f32_16x16x32_bf16 v[28:31], v[76:79], a[128:131], v[28:31]\n\t"
        "s_waitcnt lgkmcnt(6)\n\t"
        "v_mfma_f32_16x16x32_bf16 v[8:11], a[132:135], v[80:83], v[8:11]\n\t"
        "v_mfma_f32_16x16x32_bf16 v[12:15], a[136:139], v[80:83], v[12:15]\n\t"
        "ds_read_b128 v[76:79], v105 offset:59392\n\t"
        "v_mfma_f32_16x16x32_bf16 v[16:19], v[80:83], a[140:143], v[16:19]\n\t"
        "s_add_u32 m0, %[ld], 24576\n\t"
        "s_add_u32 %[t0], %[xo], 384\n\t"
        "s_waitcnt lgkmcnt(6)\n\t"
        "v_mfma_f32_16x16x32_bf16 v[20:23], a[132:135], v[84:87], v[20:23]\n\t"
        "buffer_load_dwordx4 v100, %[sx], %[t0] offen lds\n\t"
        "v_mfma_f32_16x16x32_bf16 v[24:27], a[136:139], v[84:87], v[24:27]\n\t"
        "ds_read_b128 v[80:83], v106 offset:57344\n\t"
        "v_mfma_f32_16x16x32_bf16 v[28:31], v[84:87], a[140:143], v[28:31]\n\t"
        "s_waitcnt lgkmcnt(6)\n\t"
        "v_mfma_f32_16x16x32_bf16 v[8:11], a[144:147], v[56:59], v[8:11]\n\t"
        "v_mfma_f32_16x16x32_bf16 v[12:15], a[148:151], v[56:59], v[12:15]\n\t"
        "ds_read_b128 v[84:87], v106 offset:59392\n\t"
        "v_mfma_f32_16x16x32_bf16 v[16:19], v[56:59], a[152:155], v[16:19]\n\t"
        "s_add_u32 m0, %[ld], 25600\n\t"
        "s_add_u32 %[t0], %[xo], 16768\n\t"
        "s_waitcnt lgkmcnt(6)\n\t"
        "v_mfma_f32_16x16x32_bf16 v[20:23], a[144:147], v[60:63], v[20:23]\n\t"
        "buffer_load_dwordx4 v100, %[sx], %[t0] offen lds\n\t"
        "v_mfma_f32_16x16x32_bf16 v[24:27], a[148:151], v[60:63], v[24:27]\n\t"
        "v_mfma_f32_16x16x32_bf16 v[28:31], v[60:63], a[152:155], v[28:31]\n\t"
        "s_waitcnt lgkmcnt(5)\n\t"
        "v_mfma_f32_16x16x32_bf16 v[8:11], a[156:159], v[64:67], v[8:11]\n\t"
        "s_waitcnt vmcnt(9) lgkmcnt(0)\n\t"
        "s_barrier\n\t"
        "v_mfma_f32_16x16x32_bf16 v[12:15], a[160:163], v[64:67], v[12:15]\n\t"
        "ds_read_b128 v[56:59], v107 offset:0\n\t"
        "v_mfma_f32_16x16x32_bf16 v[16:19], v[64:67], a[164:167], v[16:19]\n\t"
        "ds_read_b128 v[60:63], v107 offset:2048\n\t"
        "s_add_u32 m0, %[ld], 32768\n\t"
        "s_add_u32 %[t0], %[xo], 512\n\t"
        "v_mfma_f32_16x16x32_bf16 v[20:23], a[156:159], v[68:71], v[20:23]\n\t"
        "buffer_load_dwordx4 v100, %[sx], %[t0] offen lds\n\t"
        "v_mfma_f32_16x16x32_bf16 v[24:27], a[160:163], v[68:71], v[24:27]\n\t"
        "ds_read_b128 v[64:67], v108 offset:0\n\t"
        "v_mfma_f32_16x16x32_bf16 v[28:31], v[68:71], a[164:167], v[28:31]\n\t"
        "v_mfma_f32_16x16x32_bf16 v[8:11], a[168:171], v[72:75], v[8:11]\n\t"
        "v_mfma_f32_16x16x32_bf16 v[12:15], a[172:175], v[72:75], v[12:15]\n\t"
        "ds_read_b128 v[68:71], v108 offset:2048\n\t"
        "v_mov_b32_e32 v117, %[cn]\n\t"
        "buffer_store_dword v117, v113, %[sw], 0 offen sc1\n\t"
        "s_add_u32 %[cn], %[cn], 1\n\t"
        "v_mfma_f32_16x16x32_bf16 v[16:19], v[72:75], a[176:179], v[16:19]\n\t"
        "s_add_u32 m0, %[ld], 33792\n\t"
        "s_add_u32 %[t0], %[xo], 16896\n\t"
        "v_mfma_f32_16x16x32_bf16 v[20:23], a[168:171], v[76:79], v[20:23]\n\t"
        "buffer_load_dwordx4 v100, %[sx], %[t0] offen lds\n\t"
        "v_mfma_f32_16x16x32_bf16 v[24:27], a[172:175], v[76:79], v[24:27]\n\t"
        "ds_read_b128 v[72:75], v107 offset:8192\n\t"
        "v_mfma_f32_16x16x32_bf16 v[28:31], v[76:79], a[176:179], v[28:31]\n\t"
        "v_mfma_f32_16x16x32_bf16 v[8:11], a[180:183], v[80:83], v[8:11]\n\t"
        "v_mfma_f32_16x16x32_bf16 v[12:15], a[184:187], v[80:83], v[12:15]\n\t"
        "ds_read_b128 v[76:79], v107 offset:10240\n\t"
        "v_mfma_f32_16x16x32_bf16 v[16:19], v[80:83], a[188:191], v[16:19]\n\t"
        "s_add_u32 m0, %[ld], 40960\n\t"
        "s_add_u32 %[t0], %[xo], 640\n\t"
        "v_mfma_f32_16x16x32_bf16 v[20:23], a[180:183], v[84:87], v[20:23]\n\t"
        "buffer_load_dwordx4 v100, %[sx], %[t0] offen lds\n\t"
        "v_mfma_f32_16x16x32_bf16 v[24:27], a[184:187], v[84:87], v[24:27]\n\t"
        "ds_read_b128 v[80:83], v108 offset:8192\n\t"
        "v_mfma_f32_16x16x32_bf16 v[28:31], v[84:87], a[188:191], v[28:31]\n\t"
        "s_waitcnt lgkmcnt(6)\n\t"
        "v_mfma_f32_16x16x32_bf16 v[8:11], a[192:195], v[56:59], v[8:11]\n\t"
        "v_mfma_f32_16x16x32_bf16 v[12:15], a[196:199], v[56:59], v[12:15]\n\t"
        "ds_read_b128 v[84:87], v108 offset:10240\n\t"
        "v_mfma_f32_16x16x32_bf16 v[16:19], v[56:59], a[200:203], v[16:19]\n\t"
        "s_add_u32 m0, %[ld], 41984\n\t"
        "s_add_u32 %[t0], %[xo], 17024\n\t"
        "s_waitcnt lgkmcnt(6)\n\t"
        "v_mfma_f32_16x16x32_bf16 v[20:23], a[192:195], v[60:63], v[20:23]\n\t"
        "buffer_load_dwordx4 v100, %[sx], %[t0] offen lds\n\t"
        "v_mfma_f32_16x16x32_bf16 v[24:27], a[196:199], v[60:63], v[24:27]\n\t"
        "ds_read_b128 v[56:59], v107 offset:16384\n\t"
        "v_mfma_f32_16x16x32_bf16 v[28:31], v[60:63], a[200:203], v[28:31]\n\t"
        "s_waitcnt lgkmcnt(6)\n\t"
        "v_mfma_f32_16x16x32_bf16 v[8:11], a[204:207], v[64:67], v[8:11]\n\t"
        "v_mfma_f32_16x16x32_bf16 v[12:15], a[208:211], v[64:67], v[12:15]\n\t"
        "ds_read_b128 v[60:63], v107 offset:18432\n\t"
        "v_mfma_f32_16x16x32_bf16 v[16:19], v[64:67], a[212:215], v[16:19]\n\t"
        "s_add_u32 m0, %[ld], 49152\n\t"
        "s_add_u32 %[t0], %[xo], 768\n\t"
        "s_waitcnt lgkmcnt(6)\n\t"
        "v_mfma_f32_16x16x32_bf16 v[20:23], a[204:207], v[68:71], v[20:23]\n\t"
        "buffer_load_dwordx4 v100, %[sx], %[t0] offen lds\n\t"
        "v_mfma_f32_16x16x32_bf16 v[24:27], a[208:211], v[68:71], v[24:27]\n\t"
        "ds_read_b128 v[64:67], v108 offset:16384\n\t"
        "v_mfma_f32_16x16x32_bf16 v[28:31], v[68:71], a[212:215], v[28:31]\n\t"
        "s_waitcnt lgkmcnt(6)\n\t"
        "v_mfma_f32_16x16x32_bf16 v[8:11], a[216:219], v[72:75], v[8:11]\n\t"
        "v_mfma_f32_16x16x32_bf16 v[12:15], a[220:223], v[72:75], v[12:15]\n\t"
        "ds_read_b128 v[68:71], v108 offset:18432\n\t"
        "v_mfma_f32_16x16x32_bf16 v[16:19], v[72:75], a[224:227], v[16:19]\n\t"
        "s_add_u32 m0, %[ld], 50176\n\t"
        "s_add_u32 %[t0], %[xo], 17152\n\t"
        "s_waitcnt lgkmcnt(6)\n\t"
        "v_mfma_f32_16x16x32_bf16 v[20:23], a[216:219], v[76:79], v[20:23]\n\t"
        "buffer_load_dwordx4 v100, %[sx], %[t0] offen lds\n\t"
        "v_mfma_f32_16x16x32_bf16 v[24:27], a[220:223], v[76:79], v[24:27]\n\t"
        "ds_read_b128 v[72:75], v107 offset:24576\n\t"
        "v_mfma_f32_16x16x32_bf16 v[28:31], v[76:79], a[224:227], v[28:31]\n\t"
        "s_waitcnt lgkmcnt(6)\n\t"
        "v_mfma_f32_16x16x32_bf16 v[8:11], a[228:231], v[80:83], v[8:11]\n\t"
        "v_mfma_f32_16x16x32_bf16 v[12:15], a[232:235], v[80:83], v[12:15]\n\t"
        "ds_read_b128 v[76:79], v107 offset:26624\n\t"
        "v_mfma_f32_16x16x32_bf16 v[16:19], v[80:83], a[236:239], v[16:19]\n\t"
        "s_add_u32 m0, %[ld], 57344\n\t"
        "s_add_u32 %[t0], %[xo], 896\n\t"
        "s_waitcnt lgkmcnt(6)\n\t"
        "v_mfma_f32_16x16x32_bf16 v[20:23], a[228:231], v[84:87], v[20:23]\n\t"
        "buffer_load_dwordx4 v100, %[sx], %[t0] offen lds\n\t"
        "v_mfma_f32_16x16x32_bf16 v[24:27], a[232:235], v[84:87], v[24:27]\n\t"
        "ds_read_b128 v[80:83], v108 offset:24576\n\t"
        "v_mfma_f32_16x16x32_bf16 v[28:31], v[84:87], a[236:239], v[28:31]\n\t"
        "s_waitcnt lgkmcnt(6)\n\t"
        "v_mfma_f32_16x16x32_bf16 v[8:11], a[240:243], v[56:59], v[8:11]\n\t"
        "v_mfma_f32_16x16x32_bf16 v[12:15], a[244:247], v[56:59], v[12:15]\n\t"
        "ds_read_b128 v[84:87], v108 offset:26624\n\t"
        "v_mfma_f32_16x16x32_bf16 v[16:19], v[56:59], a[248:251], v[16:19]\n\t"
        "s_add_u32 m0, %[ld], 58368\n\t"
        "s_add_u32 %[t0], %[xo], 17280\n\t"
        "s_waitcnt lgkmcnt(6)\n\t"
        "v_mfma_f32_16x16x32_bf16 v[20:23], a[240:243], v[60:63], v[20:23]\n\t"
        "buffer_load_dwordx4 v100, %[sx], %[t0] offen lds\n\t"
        "v_mfma_f32_16x16x32_bf16 v[24:27], a[244:247], v[60:63], v[24:27]\n\t"
        "v_mfma_f32_16x16x32_bf16 v[28:31], v[60:63], a[248:251], v[28:31]\n\t"
        "s_waitcnt lgkmcnt(5)\n\t"
        "v_mfma_f32_16x16x32_bf16 v[8:11], a[252:255], v[64:67], v[8:11]\n\t"
        "s_waitcnt vmcnt(17) lgkmcnt(0)\n\t"
        "s_barrier\n\t"
        "v_mfma_f32_16x16x32_bf16 v[12:15], v[128:131], v[64:67], v[12:15]\n\t"
        "ds_read_b128 v[56:59], v107 offset:32768\n\t"
        "v_mfma_f32_16x16x32_bf16 v[16:19], v[64:67], v[132:135], v[16:19]\n\t"
        "ds_read_b128 v[60:63], v107 offset:34816\n\t"
        "s_add_u32 m0, %[ld], 65536\n\t"
        "s_add_u32 %[t0], %[xo], 1024\n\t"
        "v_mfma_f32_16x16x32_bf16 v[20:23], a[252:255], v[68:71], v[20:23]\n\t"
        "buffer_load_dwordx4 v100, %[sx], %[t0] offen lds\n\t"
        "v_mfma_f32_16x16x32_bf16 v[24:27], v[128:131], v[68:71], v[24:27]\n\t"
        "ds_read_b128 v[64:67], v108 offset:32768\n\t"
        "v_mfma_f32_16x16x32_bf16 v[28:31], v[68:71], v[132:135], v[28:31]\n\t"
        "v_mfma_f32_16x16x32_bf16 v[8:11], v[136:139], v[72:75], v[8:11]\n\t"
        "v_mfma_f32_16x16x32_bf16 v[12:15], v[140:143], v[72:75], v[12:15]\n\t"
        "ds_read_b128 v[68:71], v108 offset:34816\n\t"
        "v_mfma_f32_16x16x32_bf16 v[16:19], v[72:75], v[144:147], v[16:19]\n\t"
        "s_add_u32 m0, %[ld], 66560\n\t"
        "s_add_u32 %[t0], %[xo], 17408\n\t"
        "v_mfma_f32_16x16x32_bf16 v[20:23], v[136:139], v[76:79], v[20:23]\n\t"
        "buffer_load_dwordx4 v100, %[sx], %[t0] offen lds\n\t"
        "v_mfma_f32_16x16x32_bf16 v[24:27], v[140:143], v[76:79], v[24:27]\n\t"
        "ds_read_b128 v[72:75], v107 offset:40960\n\t"
        "v_mfma_f32_16x16x32_bf16 v[28:31], v[76:79], v[144:147], v[28:31]\n\t"
        "v_mfma_f32_16x16x32_bf16 v[8:11], v[148:151], v[80:83], v[8:11]\n\t"
        "v_mfma_f32_16x16x32_bf16 v[12:15], v[152:155], v[80:83], v[12:15]\n\t"
        "ds_read_b128 v[76:79], v107 offset:43008\n\t"
        "v_mfma_f32_16x16x32_bf16 v[16:19], v[80:83], v[156:159], v[16:19]\n\t"
        "s_add_u32 m0, %[ld], 73728\n\t"
        "s_add_u32 %[t0], %[xo], 1152\n\t"
        "v_mfma_f32_16x16x32_bf16 v[20:23], v[148:151], v[84:87], v[20:23]\n\t"
        "buffer_load_dwordx4 v100, %[sx], %[t0] offen lds\n\t"
        "v_mfma_f32_16x16x32_bf16 v[24:27], v[152:155], v[84:87], v[24:27]\n\t"
        "ds_read_b128 v[80:83], v108 offset:40960\n\t"
        "v_mfma_f32_16x16x32_bf16 v[28:31], v[84:87], v[156:159], v[28:31]\n\t"
        "s_waitcnt lgkmcnt(6)\n\t"
        "v_mfma_f32_16x16x32_bf16 v[8:11], v[160:163], v[56:59], v[8:11]\n\t"
        "v_mfma_f32_16x16x32_bf16 v[12:15], v[164:167], v[56:59], v[12:15]\n\t"
        "ds_read_b128 v[84:87], v108 offset:43008\n\t"
        "v_mfma_f32_16x16x32_bf16 v[16:19], v[56:59], v[168:171], v[16:19]\n\t"
        "s_add_u32 m0, %[ld], 74752\n\t"
        "s_add_u32 %[t0], %[xo], 17536\n\t"
        "s_waitcnt lgkmcnt(6)\n\t"
        "v_mfma_f32_16x16x32_bf16 v[20:23], v[160:163], v[60:63], v[20:23]\n\t"
        "buffer_load_dwordx4 v100, %[sx], %[t0] offen lds\n\t"
        "v_mfma_f32_16x16x32_bf16 v[24:27], v[164:167], v[60:63], v[24:27]\n\t"
        "ds_read_b128 v[56:59], v107 offset:49152\n\t"
        "v_mfma_f32_16x16x32_bf16 v[28:31], v[60:63], v[168:171], v[28:31]\n\t"
        "s_waitcnt lgkmcnt(6)\n\t"
        "v_mfma_f32_16x16x32_bf16 v[8:11], v[172:175], v[64:67], v[8:11]\n\t"
        "v_mfma_f32_16x16x32_bf16 v[12:15], v[176:179], v[64:67], v[12:15]\n\t"
        "ds_read_b128 v[60:63], v107 offset:51200\n\t"
        "v_mfma_f32_16x16x32_bf16 v[16:19], v[64:67], v[180:183], v[16:19]\n\t"
        "s_add_u32 m0, %[ld], 81920\n\t"
        "s_add_u32 %[t0], %[xo], 1280\n\t"
        "s_waitcnt lgkmcnt(6)\n\t"
        "v_mfma_f32_16x16x32_bf16 v[20:23], v[172:175], v[68:71], v[20:23]\n\t"
        "buffer_load_dwordx4 v100, %[sx], %[t0] offen lds\n\t"
        "v_mfma_f32_16x16x32_bf16 v[24:27], v[176:179], v[68:71], v[24:27]\n\t"
        "ds_read_b128 v[64:67], v108 offset:49152\n\t"
        "v_mfma_f32_16x16x32_bf16 v[28:31], v[68:71], v[180:183], v[28:31]\n\t"
        "s_waitcnt lgkmcnt(6)\n\t"
        "v_mfma_f32_16x16x32_bf16 v[8:11], v[184:187], v[72:75], v[8:11]\n\t"
        "v_mfma_f32_16x16x32_bf16 v[12:15], v[188:191], v[72:75], v[12:15]\n\t"
        "ds_read_b128 v[68:71], v108 offset:51200\n\t"
        "v_mfma_f32_16x16x32_bf16 v[16:19], v[72:75], v[192:195], v[16:19]\n\t"
        "s_add_u32 m0, %[ld], 82944\n\t"
        "s_add_u32 %[t0], %[xo], 17664\n\t"
        "s_waitcnt lgkmcnt(6)\n\t"
        "v_mfma_f32_16x16x32_bf16 v[20:23], v[184:187], v[76:79], v[20:23]\n\t"
        "buffer_load_dwordx4 v100, %[sx], %[t0] offen lds\n\t"
        "v_mfma_f32_16x16x32_bf16 v[24:27], v[188:191], v[76:79], v[24:27]\n\t"
        "ds_read_b128 v[72:75], v107 offset:57344\n\t"
        "v_mfma_f32_16x16x32_bf16 v[28:31], v[76:79], v[192:195], v[28:31]\n\t"
        "s_waitcnt lgkmcnt(6)\n\t"
        "v_mfma_f32_16x16x32_bf16 v[8:11], v[196:199], v[80:83], v[8:11]\n\t"
        "v_mfma_f32_16x16x32_bf16 v[12:15], v[200:203], v[80:83], v[12:15]\n\t"
        "ds_read_b128 v[76:79], v107 offset:59392\n\t"
        "v_mfma_f32_16x16x32_bf16 v[16:19], v[80:83], v[204:207], v[16:19]\n\t"
        "s_add_u32 m0, %[ld], 90112\n\t"
        "s_add_u32 %[t0], %[xo], 1408\n\t"
        "s_waitcnt lgkmcnt(6)\n\t"
        "v_mfma_f32_16x16x32_bf16 v[20:23], v[196:199], v[84:87], v[20:23]\n\t"
        "buffer_load_dwordx4 v100, %[sx], %[t0] offen lds\n\t"
        "v_mfma_f32_16x16x32_bf16 v[24:27], v[200:203], v[84:87], v[24:27]\n\t"
        "ds_read_b128 v[80:83], v108 offset:57344\n\t"
        "v_mfma_f32_16x16x32_bf16 v[28:31], v[84:87], v[204:207], v[28:31]\n\t"
        "s_waitcnt lgkmcnt(6)\n\t"
        "v_mfma_f32_16x16x32_bf16 v[8:11], v[208:211], v[56:59], v[8:11]\n\t"
        "v_mfma_f32_16x16x32_bf16 v[12:15], v[212:215], v[56:59], v[12:15]\n\t"
        "ds_read_b128 v[84:87], v108 offset:59392\n\t"
        "v_mfma_f32_16x16x32_bf16 v[16:19], v[56:59], v[216:219], v[16:19]\n\t"
        "s_add_u32 m0, %[ld], 91136\n\t"
        "s_add_u32 %[t0], %[xo], 17792\n\t"
        "s_waitcnt lgkmcnt(6)\n\t"
        "v_mfma_f32_16x16x32_bf16 v[20:23], v[208:211], v[60:63], v[20:23]\n\t"
        "buffer_load_dwordx4 v100, %[sx], %[t0] offen lds\n\t"
        "v_mfma_f32_16x16x32_bf16 v[24:27], v[212:215], v[60:63], v[24:27]\n\t"
        "v_mfma_f32_16x16x32_bf16 v[28:31], v[60:63], v[216:219], v[28:31]\n\t"
        "s_waitcnt lgkmcnt(5)\n\t"
        "v_mfma_f32_16x16x32_bf16 v[8:11], v[220:223], v[64:67], v[8:11]\n\t"
        "s_waitcnt vmcnt(17) lgkmcnt(0)\n\t"
        "s_barrier\n\t"
        "v_mfma_f32_16x16x32_bf16 v[12:15], v[224:227], v[64:67], v[12:15]\n\t"
        "ds_read_b128 v[56:59], v105 offset:0\n\t"
        "v_mfma_f32_16x16x32_bf16 v[16:19], v[64:67], v[228:231], v[16:19]\n\t"
        "ds_read_b128 v[60:63], v105 offset:2048\n\t"
        "s_add_u32 m0, %[ld], 98304\n\t"
        "s_add_u32 %[t0], %[xo], 1536\n\t"
        "v_mfma_f32_16x16x32_bf16 v[20:23], v[220:223], v[68:71], v[20:23]\n\t"
        "buffer_load_dwordx4 v100, %[sx], %[t0] offen lds\n\t"
        "v_mfma_f32_16x16x32_bf16 v[24:27], v[224:227], v[68:71], v[24:27]\n\t"
        "ds_read_b128 v[64:67], v106 offset:0\n\t"
        "v_mfma_f32_16x16x32_bf16 v[28:31], v[68:71], v[228:231], v[28:31]\n\t"
        "v_mfma_f32_16x16x32_bf16 v[8:11], v[232:235], v[72:75], v[8:11]\n\t"
        "v_mfma_f32_16x16x32_bf16 v[12:15], v[236:239], v[72:75], v[12:15]\n\t"
        "ds_read_b128 v[68:71], v106 offset:2048\n\t"
        "v_mfma_f32_16x16x32_bf16 v[16:19], v[72:75], v[240:243], v[16:19]\n\t"
        "s_add_u32 m0, %[ld], 99328\n\t"
        "s_add_u32 %[t0], %[xo], 17920\n\t"
        "v_mfma_f32_16x16x32_bf16 v[20:23], v[232:235], v[76:79], v[20:23]\n\t"
        "buffer_load_dwordx4 v100, %[sx], %[t0] offen lds\n\t"
        "v_mfma_f32_16x16x32_bf16 v[24:27], v[236:239], v[76:79], v[24:27]\n\t"
        "ds_read_b128 v[72:75], v105 offset:8192\n\t"
        "v_mfma_f32_16x16x32_bf16 v[28:31], v[76:79], v[240:243], v[28:31]\n\t"
        "v_mfma_f32_16x16x32_bf16 v[8:11], v[244:247], v[80:83], v[8:11]\n\t"
        "v_mfma_f32_16x16x32_bf16 v[12:15], v[248:251], v[80:83], v[12:15]\n\t"
        "ds_read_b128 v[76:79], v105 offset:10240\n\t"
        "v_mfma_f32_16x16x32_bf16 v[16:19], v[80:83], v[252:255], v[16:19]\n\t"
        "s_add_u32 m0, %[ld], 106496\n\t"
        "s_add_u32 %[t0], %[xo], 1664\n\t"
        "v_mfma_f32_16x16x32_bf16 v[20:23], v[244:247], v[84:87], v[20:23]\n\t"
        "buffer_load_dwordx4 v100, %[sx], %[t0] offen lds\n\t"
        "v_mfma_f32_16x16x32_bf16 v[24:27], v[248:251], v[84:87], v[24:27]\n\t"
        "ds_read_b128 v[80:83], v106 offset:8192\n\t"
        "v_mfma_f32_16x16x32_bf16 v[28:31], v[84:87], v[252:255], v[28:31]\n\t"
        "LG_mid%=:\n\t"
        "s_waitcnt lgkmcnt(6)\n\t"
        "v_mfma_f32_16x16x32_bf16 v[32:35], a[0:3], v[56:59], 0\n\t"
        "v_mfma_f32_16x16x32_bf16 v[36:39], a[4:7], v[56:59], 0\n\t"
        "ds_read_b128 v[84:87], v106 offset:10240\n\t"
        "v_mfma_f32_16x16x32_bf16 v[40:43], v[56:59], a[8:11], 0\n\t"
        "s_add_u32 m0, %[ld], 107520\n\t"
        "s_add_u32 %[t0], %[xo], 18048\n\t"
        "s_waitcnt lgkmcnt(6)\n\t"
        "v_mfma_f32_16x16x32_bf16 v[44:47], a[0:3], v[60:63], 0\n\t"
        "buffer_load_dwordx4 v100, %[sx], %[t0] offen lds\n\t"
        "v_mfma_f32_16x16x32_bf16 v[48:51], a[4:7], v[60:63], 0\n\t"
        "ds_read_b128 v[56:59], v105 offset:16384\n\t"
        "v_mfma_f32_16x16x32_bf16 v[52:55], v[60:63], a[8:11], 0\n\t"
        "s_waitcnt lgkmcnt(6)\n\t"
        "v_mfma_f32_16x16x32_bf16 v[32:35], a[12:15], v[64:67], v[32:35]\n\t"
        "v_cvt_pk_bf16_f32 v88, v8, v9\n\t"
        "v_cvt_pk_bf16_f32 v89, v10, v11\n\t"
        "v_mfma_f32_16x16x32_bf16 v[36:39], a[16:19], v[64:67], v[36:39]\n\t"
        "ds_read_b128 v[60:63], v105 offset:18432\n\t"
        "v_cvt_pk_bf16_f32 v90, v12, v13\n\t"
        "v_mfma_f32_16x16x32_bf16 v[40:43], v[64:67], a[20:23], v[40:43]\n\t"
        "s_add_u32 m0, %[ld], 114688\n\t"
        "s_add_u32 %[t0], %[xo], 1792\n\t"
        "s_waitcnt lgkmcnt(6)\n\t"
        "v_mfma_f32_16x16x32_bf16 v[44:47], a[12:15], v[68:71], v[44:47]\n\t"
        "buffer_load_dwordx4 v100, %[sx], %[t0] offen lds\n\t"
        "v_mfma_f32_16x16x32_bf16 v[48:51], a[16:19], v[68:71], v[48:51]\n\t"
        "ds_read_b128 v[64:67], v106 offset:16384\n\t"
        "v_cvt_pk_bf16_f32 v91, v14, v15\n\t"
        "v_mfma_f32_16x16x32_bf16 v[52:55], v[68:71], a[20:23], v[52:55]\n\t"
        "buffer_store_dwordx4 v[88:91], v109, %[sw], %[so] offen sc1\n\t"
        "s_waitcnt lgkmcnt(6)\n\t"
        "v_mfma_f32_16x16x32_bf16 v[32:35], a[24:27], v[72:75], v[32:35]\n\t"
        "v_cvt_pk_bf16_f32 v92, v20, v21\n\t"
        "v_cvt_pk_bf16_f32 v93, v22, v23\n\t"
        "v_mfma_f32_16x16x32_bf16 v[36:39], a[28:31], v[72:75], v[36:39]\n\t"
        "ds_read_b128 v[68:71], v106 offset:18432\n\t"
        "v_cvt_pk_bf16_f32 v94, v24, v25\n\t"
        "v_mfma_f32_16x16x32_bf16 v[40:43], v[72:75], a[32:35], v[40:43]\n\t"
        "s_add_u32 m0, %[ld], 115712\n\t"
        "s_add_u32 %[t0], %[xo], 18176\n\t"
        "s_waitcnt lgkmcnt(6)\n\t"
        "v_mfma_f32_16x16x32_bf16 v[44:47], a[24:27], v[76:79], v[44:47]\n\t"
        "buffer_load_dwordx4 v100, %[sx], %[t0] offen lds\n\t"
        "v_mfma_f32_16x16x32_bf16 v[48:51], a[28:31], v[76:79], v[48:51]\n\t"
        "ds_read_b128 v[72:75], v105 offset:24576\n\t"
        "v_cvt_pk_bf16_f32 v95, v26, v27\n\t"
        "v_mfma_f32_16x16x32_bf16 v[52:55], v[76:79], a[32:35], v[52:55]\n\t"
        "buffer_store_dwordx4 v[92:95], v110, %[sw], %[so] offen sc1\n\t"
        "s_waitcnt lgkmcnt(6)\n\t"
        "v_mfma_f32_16x16x32_bf16 v[32:35], a[36:39], v[80:83], v[32:35]\n\t"
        "v_cvt_pk_bf16_f32 v96, v16, v17\n\t"
        "v_cvt_pk_bf16_f32 v97, v18, v19\n\t"
        "v_mfma_f32_16x16x32_bf16 v[36:39], a[40:43], v[80:83], v[36:39]\n\t"
        "ds_read_b128 v[76:79], v105 offset:26624\n\t"
        "v_cvt_pk_bf16_f32 v98, v28, v29\n\t"
        "v_mfma_f32_16x16x32_bf16 v[40:43], v[80:83], a[44:47], v[40:43]\n\t"
        "s_add_u32 m0, %[ld], 122880\n\t"
        "s_add_u32 %[t0], %[xo], 1920\n\t"
        "s_waitcnt lgkmcnt(6)\n\t"
        "v_mfma_f32_16x16x32_bf16 v[44:47], a[36:39], v[84:87], v[44:47]\n\t"
        "buffer_load_dwordx4 v100, %[sx], %[t0] offen lds\n\t"
        "v_mfma_f32_16x16x32_bf16 v[48:51], a[40:43], v[84:87], v[48:51]\n\t"
        "ds_read_b128 v[80:83], v106 offset:24576\n\t"
        "v_cvt_pk_bf16_f32 v99, v30, v31\n\t"
        "v_mfma_f32_16x16x32_bf16 v[52:55], v[84:87], a[44:47], v[52:55]\n\t"
        "buffer_store_dwordx4 v[96:99], v111, %[sw], %[so] offen sc1\n\t"
        "s_waitcnt lgkmcnt(6)\n\t"
        "v_mfma_f32_16x16x32_bf16 v[32:35], a[48:51], v[56:59], v[32:35]\n\t"
        "s_lshl_b32 %[so], %[cn], 19\n\t"
        "v_mfma_f32_16x16x32_bf16 v[36:39], a[52:55], v[56:59], v[36:39]\n\t"
        "ds_read_b128 v[84:87], v106 offset:26624\n\t"
        "v_mfma_f32_16x16x32_bf16 v[40:43], v[56:59], a[56:59], v[40:43]\n\t"
        "s_add_u32 m0, %[ld], 123904\n\t"
        "s_add_u32 %[t0], %[xo], 18304\n\t"
        "s_waitcnt lgkmcnt(6)\n\t"
        "v_mfma_f32_16x16x32_bf16 v[44:47], a[48:51], v[60:63], v[44:47]\n\t"
        "buffer_load_dwordx4 v100, %[sx], %[t0] offen lds\n\t"
        "v_mfma_f32_16x16x32_bf16 v[48:51], a[52:55], v[60:63], v[48:51]\n\t"
        "v_mfma_f32_16x16x32_bf16 v[52:55], v[60:63], a[56:59], v[52:55]\n\t"
        "s_waitcnt lgkmcnt(5)\n\t"
        "v_mfma_f32_16x16x32_bf16 v[32:35], a[60:63], v[64:67], v[32:35]\n\t"
        "s_waitcnt vmcnt(19) lgkmcnt(0)\n\t"
        "s_barrier\n\t"
        "s_add_u32 %[xo], %[xo], 0x20000\n\t"
        "v_mfma_f32_16x16x32_bf16 v[36:39], a[64:67], v[64:67], v[36:39]\n\t"
        "ds_read_b128 v[56:59], v105 offset:32768\n\t"
        "v_mfma_f32_16x16x32_bf16 v[40:43], v[64:67], a[68:71], v[40:43]\n\t"
        "ds_read_b128 v[60:63], v105 offset:34816\n\t"
        "s_add_u32 m0, %[ld], 0\n\t"
        "s_add_u32 %[t0], %[xo], 0\n\t"
        "v_mfma_f32_16x16x32_bf16 v[44:47], a[60:63], v[68:71], v[44:47]\n\t"
        "buffer_load_dwordx4 v100, %[sx], %[t0] offen lds\n\t"
        "v_mfma_f32_16x16x32_bf16 v[48:51], a[64:67], v[68:71], v[48:51]\n\t"
        "ds_read_b128 v[64:67], v106 offset:32768\n\t"
        "v_mfma_f32_16x16x32_bf16 v[52:55], v[68:71], a[68:71], v[52:55]\n\t"
        "v_mfma_f32_16x16x32_bf16 v[32:35], a[72:75], v[72:75], v[32:35]\n\t"
        "v_mfma_f32_16x16x32_bf16 v[36:39], a[76:79], v[72:75], v[36:39]\n\t"
        "ds_read_b128 v[68:71], v106 offset:34816\n\t"
        "v_mfma_f32_16x16x32_bf16 v[40:43], v[72:75], a[80:83], v[40:43]\n\t"
        "s_add_u32 m0, %[ld], 1024\n\t"
        "s_add_u32 %[t0], %[xo], 16384\n\t"
        "v_mfma_f32_16x16x32_bf16 v[44:47], a[72:75], v[76:79], v[44:47]\n\t"
        "buffer_load_dwordx4 v100, %[sx], %[t0] offen lds\n\t"
        "v_mfma_f32_16x16x32_bf16 v[48:51], a[76:79], v[76:79], v[48:51]\n\t"
        "ds_read_b128 v[72:75], v105 offset:40960\n\t"
        "v_mfma_f32_16x16x32_bf16 v[52:55], v[76:79], a[80:83], v[52:55]\n\t"
        "v_mfma_f32_16x16x32_bf16 v[32:35], a[84:87], v[80:83], v[32:35]\n\t"
        "v_mfma_f32_16x16x32_bf16 v[36:39], a[88:91], v[80:83], v[36:39]\n\t"
        "ds_read_b128 v[76:79], v105 offset:43008\n\t"
        "v_mfma_f32_16x16x32_bf16 v[40:43], v[80:83], a[92:95], v[40:43]\n\t"
        "s_add_u32 m0, %[ld], 8192\n\t"
        "s_add_u32 %[t0], %[xo], 128\n\t"
        "v_mfma_f32_16x16x32_bf16 v[44:47], a[84:87], v[84:87], v[44:47]\n\t"
        "buffer_load_dwordx4 v100, %[sx], %[t0] offen lds\n\t"
        "v_mfma_f32_16x16x32_bf16 v[48:51], a[88:91], v[84:87], v[48:51]\n\t"
        "ds_read_b128 v[80:83], v106 offset:40960\n\t"
        "v_mfma_f32_16x16x32_bf16 v[52:55], v[84:87], a[92:95], v[52:55]\n\t"
        "s_waitcnt lgkmcnt(6)\n\t"
        "v_mfma_f32_16x16x32_bf16 v[32:35], a[96:99], v[56:59], v[32:35]\n\t"
        "v_mfma_f32_16x16x32_bf16 v[36:39], a[100:103], v[56:59], v[36:39]\n\t"
        "ds_read_b128 v[84:87], v106 offset:43008\n\t"
        "v_mfma_f32_16x16x32_bf16 v[40:43], v[56:59], a[104:107], v[40:43]\n\t"
        "s_add_u32 m0, %[ld], 9216\n\t"
        "s_add_u32 %[t0], %[xo], 16512\n\t"
        "s_waitcnt lgkmcnt(6)\n\t"
        "v_mfma_f32_16x16x32_bf16 v[44:47], a[96:99], v[60:63], v[44:47]\n\t"
        "buffer_load_dwordx4 v100, %[sx], %[t0] offen lds\n\t"
        "v_mfma_f32_16x16x32_bf16 v[48:51], a[100:103], v[60:63], v[48:51]\n\t"
        "ds_read_b128 v[56:59], v105 offset:49152\n\t"
        "v_mfma_f32_16x16x32_bf16 v[52:55], v[60:63], a[104:107], v[52:55]\n\t"
        "s_waitcnt lgkmcnt(6)\n\t"
        "v_mfma_f32_16x16x32_bf16 v[32:35], a[108:111], v[64:67], v[32:35]\n\t"
        "v_mfma_f32_16x16x32_bf16 v[36:39], a[112:115], v[64:67], v[36:39]\n\t"
        "ds_read_b128 v[60:63], v105 offset:51200\n\t"
        "v_mfma_f32_16x16x32_bf16 v[40:43], v[64:67], a[116:119], v[40:43]\n\t"
        "s_add_u32 m0, %[ld], 16384\n\t"
        "s_add_u32 %[t0], %[xo], 256\n\t"
        "s_waitcnt lgkmcnt(6)\n\t"
        "v_mfma_f32_16x16x32_bf16 v[44:47], a[108:111], v[68:71], v[44:47]\n\t"
        "buffer_load_dwordx4 v100, %[sx], %[t0] offen lds\n\t"
        "v_mfma_f32_16x16x32_bf16 v[48:51], a[112:115], v[68:71], v[48:51]\n\t"
        "ds_read_b128 v[64:67], v106 offset:49152\n\t"
        "v_mfma_f32_16x16x32_bf16 v[52:55], v[68:71], a[116:119], v[52:55]\n\t"
        "s_waitcnt lgkmcnt(6)\n\t"
        "v_mfma_f32_16x16x32_bf16 v[32:35], a[120:123], v[72:75], v[32:35]\n\t"
        "v_mfma_f32_16x16x32_bf16 v[36:39], a[124:127], v[72:75], v[36:39]\n\t"
        "ds_read_b128 v[68:71], v106 offset:51200\n\t"
        "v_mfma_f32_16x16x32_bf16 v[40:43], v[72:75], a[128:131], v[40:43]\n\t"
        "s_add_u32 m0, %[ld], 17408\n\t"
        "s_add_u32 %[t0], %[xo], 16640\n\t"
        "s_waitcnt lgkmcnt(6)\n\t"
        "v_mfma_f32_16x16x32_bf16 v[44:47], a[120:123], v[76:79], v[44:47]\n\t"
        "buffer_load_dwordx4 v100, %[sx], %[t0] offen lds\n\t"
        "v_mfma_f32_16x16x32_bf16 v[48:51], a[124:127], v[76:79], v[48:51]\n\t"
        "ds_read_b128 v[72:75], v105 offset:57344\n\t"
        "v_mfma_f32_16x16x32_bf16 v[52:55], v[76:79], a[128:131], v[52:55]\n\t"
        "s_waitcnt lgkmcnt(6)\n\t"
        "v_mfma_f32_16x16x32_bf16 v[32:35], a[132:135], v[80:83], v[32:35]\n\t"
        "v_mfma_f32_16x16x32_bf16 v[36:39], a[136:139], v[80:83], v[36:39]\n\t"
        "ds_read_b128 v[76:79], v105 offset:59392\n\t"
        "v_mfma_f32_16x16x32_bf16 v[40:43], v[80:83], a[140:143], v[40:43]\n\t"
        "s_add_u32 m0, %[ld], 24576\n\t"
        "s_add_u32 %[t0], %[xo], 384\n\t"
        "s_waitcnt lgkmcnt(6)\n\t"
        "v_mfma_f32_16x16x32_bf16 v[44:47], a[132:135], v[84:87], v[44:47]\n\t"
        "buffer_load_dwordx4 v100, %[sx], %[t0] offen lds\n\t"
        "v_mfma_f32_16x16x32_bf16 v[48:51], a[136:139], v[84:87], v[48:51]\n\t"
        "ds_read_b128 v[80:83], v106 offset:57344\n\t"
        "v_mfma_f32_16x16x32_bf16 v[52:55], v[84:87], a[140:143], v[52:55]\n\t"
        "s_waitcnt lgkmcnt(6)\n\t"
        "v_mfma_f32_16x16x32_bf16 v[32:35], a[144:147], v[56:59], v[32:35]\n\t"
        "v_mfma_f32_16x16x32_bf16 v[36:39], a[148:151], v[56:59], v[36:39]\n\t"
        "ds_read_b128 v[84:87], v106 offset:59392\n\t"
        "v_mfma_f32_16x16x32_bf16 v[40:43], v[56:59], a[152:155], v[40:43]\n\t"
        "s_add_u32 m0, %[ld], 25600\n\t"
        "s_add_u32 %[t0], %[xo], 16768\n\t"
        "s_waitcnt lgkmcnt(6)\n\t"
        "v_mfma_f32_16x16x32_bf16 v[44:47], a[144:147], v[60:63], v[44:47]\n\t"
        "buffer_load_dwordx4 v100, %[sx], %[t0] offen lds\n\t"
        "v_mfma_f32_16x16x32_bf16 v[48:51], a[148:151], v[60:63], v[48:51]\n\t"
        "v_mfma_f32_16x16x32_bf16 v[52:55], v[60:63], a[152:155], v[52:55]\n\t"
        "s_waitcnt lgkmcnt(5)\n\t"
        "v_mfma_f32_16x16x32_bf16 v[32:35], a[156:159], v[64:67], v[32:35]\n\t"
        "s_waitcnt vmcnt(9) lgkmcnt(0)\n\t"
        "s_barrier\n\t"
        "v_mfma_f32_16x16x32_bf16 v[36:39], a[160:163], v[64:67], v[36:39]\n\t"
        "ds_read_b128 v[56:59], v107 offset:0\n\t"
        "v_mfma_f32_16x16x32_bf16 v[40:43], v[64:67], a[164:167], v[40:43]\n\t"
        "ds_read_b128 v[60:63], v107 offset:2048\n\t"
        "s_add_u32 m0, %[ld], 32768\n\t"
        "s_add_u32 %[t0], %[xo], 512\n\t"
        "v_mfma_f32_16x16x32_bf16 v[44:47], a[156:159], v[68:71], v[44:47]\n\t"
        "buffer_load_dwordx4 v100, %[sx], %[t0] offen lds\n\t"
        "v_mfma_f32_16x16x32_bf16 v[48:51], a[160:163], v[68:71], v[48:51]\n\t"
        "ds_read_b128 v[64:67], v108 offset:0\n\t"
        "v_mfma_f32_16x16x32_bf16 v[52:55], v[68:71], a[164:167], v[52:55]\n\t"
        "v_mfma_f32_16x16x32_bf16 v[32:35], a[168:171], v[72:75], v[32:35]\n\t"
        "v_mfma_f32_16x16x32_bf16 v[36:39], a[172:175], v[72:75], v[36:39]\n\t"
        "ds_read_b128 v[68:71], v108 offset:2048\n\t"
        "v_mov_b32_e32 v117, %[cn]\n\t"
        "buffer_store_dword v117, v113, %[sw], 0 offen sc1\n\t"
        "s_add_u32 %[cn], %[cn], 1\n\t"
        "v_mfma_f32_16x16x32_bf16 v[40:43], v[72:75], a[176:179], v[40:43]\n\t"
        "s_add_u32 m0, %[ld], 33792\n\t"
        "s_add_u32 %[t0], %[xo], 16896\n\t"
        "v_mfma_f32_16x16x32_bf16 v[44:47], a[168:171], v[76:79], v[44:47]\n\t"
        "buffer_load_dwordx4 v100, %[sx], %[t0] offen lds\n\t"
        "v_mfma_f32_16x16x32_bf16 v[48:51], a[172:175], v[76:79], v[48:51]\n\t"
        "ds_read_b128 v[72:75], v107 offset:8192\n\t"
        "v_mfma_f32_16x16x32_bf16 v[52:55], v[76:79], a[176:179], v[52:55]\n\t"
        "v_mfma_f32_16x16x32_bf16 v[32:35], a[180:183], v[80:83], v[32:35]\n\t"
        "v_mfma_f32_16x16x32_bf16 v[36:39], a[184:187], v[80:83], v[36:39]\n\t"
        "ds_read_b128 v[76:79], v107 offset:10240\n\t"
        "v_mfma_f32_16x16x32_bf16 v[40:43], v[80:83], a[188:191], v[40:43]\n\t"
        "s_add_u32 m0, %[ld], 40960\n\t"
        "s_add_u32 %[t0], %[xo], 640\n\t"
        "v_mfma_f32_16x16x32_bf16 v[44:47], a[180:183], v[84:87], v[44:47]\n\t"
        "buffer_load_dwordx4 v100, %[sx], %[t0] offen lds\n\t"
        "v_mfma_f32_16x16x32_bf16 v[48:51], a[184:187], v[84:87], v[48:51]\n\t"
        "ds_read_b128 v[80:83], v108 offset:8192\n\t"
        "v_mfma_f32_16x16x32_bf16 v[52:55], v[84:87], a[188:191], v[52:55]\n\t"
        "s_waitcnt lgkmcnt(6)\n\t"
        "v_mfma_f32_16x16x32_bf16 v[32:35], a[192:195], v[56:59], v[32:35]\n\t"
        "v_mfma_f32_16x16x32_bf16 v[36:39], a[196:199], v[56:59], v[36:39]\n\t"
        "ds_read_b128 v[84:87], v108 offset:10240\n\t"
        "v_mfma_f32_16x16x32_bf16 v[40:43], v[56:59], a[200:203], v[40:43]\n\t"
        "s_add_u32 m0, %[ld], 41984\n\t"
        "s_add_u32 %[t0], %[xo], 17024\n\t"
        "s_waitcnt lgkmcnt(6)\n\t"
        "v_mfma_f32_16x16x32_bf16 v[44:47], a[192:195], v[60:63], v[44:47]\n\t"
        "buffer_load_dwordx4 v100, %[sx], %[t0] offen lds\n\t"
        "v_mfma_f32_16x16x32_bf16 v[48:51], a[196:199], v[60:63], v[48:51]\n\t"
        "ds_read_b128 v[56:59], v107 offset:16384\n\t"
        "v_mfma_f32_16x16x32_bf16 v[52:55], v[60:63], a[200:203], v[52:55]\n\t"
        "s_waitcnt lgkmcnt(6)\n\t"
        "v_mfma_f32_16x16x32_bf16 v[32:35], a[204:207], v[64:67], v[32:35]\n\t"
        "v_mfma_f32_16x16x32_bf16 v[36:39], a[208:211], v[64:67], v[36:39]\n\t"
        "ds_read_b128 v[60:63], v107 offset:18432\n\t"
        "v_mfma_f32_16x16x32_bf16 v[40:43], v[64:67], a[212:215], v[40:43]\n\t"
        "s_add_u32 m0, %[ld], 49152\n\t"
        "s_add_u32 %[t0], %[xo], 768\n\t"
        "s_waitcnt lgkmcnt(6)\n\t"
        "v_mfma_f32_16x16x32_bf16 v[44:47], a[204:207], v[68:71], v[44:47]\n\t"
        "buffer_load_dwordx4 v100, %[sx], %[t0] offen lds\n\t"
        "v_mfma_f32_16x16x32_bf16 v[48:51], a[208:211], v[68:71], v[48:51]\n\t"
        "ds_read_b128 v[64:67], v108 offset:16384\n\t"
        "v_mfma_f32_16x16x32_bf16 v[52:55], v[68:71], a[212:215], v[52:55]\n\t"
        "s_waitcnt lgkmcnt(6)\n\t"
        "v_mfma_f32_16x16x32_bf16 v[32:35], a[216:219], v[72:75], v[32:35]\n\t"
        "v_mfma_f32_16x16x32_bf16 v[36:39], a[220:223], v[72:75], v[36:39]\n\t"
        "ds_read_b128 v[68:71], v108 offset:18432\n\t"
        "v_mfma_f32_16x16x32_bf16 v[40:43], v[72:75], a[224:227], v[40:43]\n\t"
        "s_add_u32 m0, %[ld], 50176\n\t"
        "s_add_u32 %[t0], %[xo], 17152\n\t"
        "s_waitcnt lgkmcnt(6)\n\t"
        "v_mfma_f32_16x16x32_bf16 v[44:47], a[216:219], v[76:79], v[44:47]\n\t"
        "buffer_load_dwordx4 v100, %[sx], %[t0] offen lds\n\t"
        "v_mfma_f32_16x16x32_bf16 v[48:51], a[220:223], v[76:79], v[48:51]\n\t"
        "ds_read_b128 v[72:75], v107 offset:24576\n\t"
        "v_mfma_f32_16x16x32_bf16 v[52:55], v[76:79], a[224:227], v[52:55]\n\t"
        "s_waitcnt lgkmcnt(6)\n\t"
        "v_mfma_f32_16x16x32_bf16 v[32:35], a[228:231], v[80:83], v[32:35]\n\t"
        "v_mfma_f32_16x16x32_bf16 v[36:39], a[232:235], v[80:83], v[36:39]\n\t"
        "ds_read_b128 v[76:79], v107 offset:26624\n\t"
        "v_mfma_f32_16x16x32_bf16 v[40:43], v[80:83], a[236:239], v[40:43]\n\t"
        "s_add_u32 m0, %[ld], 57344\n\t"
        "s_add_u32 %[t0], %[xo], 896\n\t"
        "s_waitcnt lgkmcnt(6)\n\t"
        "v_mfma_f32_16x16x32_bf16 v[44:47], a[228:231], v[84:87], v[44:47]\n\t"
        "buffer_load_dwordx4 v100, %[sx], %[t0] offen lds\n\t"
        "v_mfma_f32_16x16x32_bf16 v[48:51], a[232:235], v[84:87], v[48:51]\n\t"
        "ds_read_b128 v[80:83], v108 offset:24576\n\t"
        "v_mfma_f32_16x16x32_bf16 v[52:55], v[84:87], a[236:239], v[52:55]\n\t"
        "s_waitcnt lgkmcnt(6)\n\t"
        "v_mfma_f32_16x16x32_bf16 v[32:35], a[240:243], v[56:59], v[32:35]\n\t"
        "v_mfma_f32_16x16x32_bf16 v[36:39], a[244:247], v[56:59], v[36:39]\n\t"
        "ds_read_b128 v[84:87], v108 offset:26624\n\t"
        "v_mfma_f32_16x16x32_bf16 v[40:43], v[56:59], a[248:251], v[40:43]\n\t"
        "s_add_u32 m0, %[ld], 58368\n\t"
        "s_add_u32 %[t0], %[xo], 17280\n\t"
        "s_waitcnt lgkmcnt(6)\n\t"
        "v_mfma_f32_16x16x32_bf16 v[44:47], a[240:243], v[60:63], v[44:47]\n\t"
        "buffer_load_dwordx4 v100, %[sx], %[t0] offen lds\n\t"
        "v_mfma_f32_16x16x32_bf16 v[48:51], a[244:247], v[60:63], v[48:51]\n\t"
        "v_mfma_f32_16x16x32_bf16 v[52:55], v[60:63], a[248:251], v[52:55]\n\t"
        "s_waitcnt lgkmcnt(5)\n\t"
        "v_mfma_f32_16x16x32_bf16 v[32:35], a[252:255], v[64:67], v[32:35]\n\t"
        "s_waitcnt vmcnt(17) lgkmcnt(0)\n\t"
        "s_barrier\n\t"
        "v_mfma_f32_16x16x32_bf16 v[36:39], v[128:131], v[64:67], v[36:39]\n\t"
        "ds_read_b128 v[56:59], v107 offset:32768\n\t"
        "v_mfma_f32_16x16x32_bf16 v[40:43], v[64:67], v[132:135], v[40:43]\n\t"
        "ds_read_b128 v[60:63], v107 offset:34816\n\t"
        "s_add_u32 m0, %[ld], 65536\n\t"
        "s_add_u32 %[t0], %[xo], 1024\n\t"
        "v_mfma_f32_16x16x32_bf16 v[44:47], a[252:255], v[68:71], v[44:47]\n\t"
        "buffer_load_dwordx4 v100, %[sx], %[t0] offen lds\n\t"
        "v_mfma_f32_16x16x32_bf16 v[48:51], v[128:131], v[68:71], v[48:51]\n\t"
        "ds_read_b128 v[64:67], v108 offset:32768\n\t"
        "v_mfma_f32_16x16x32_bf16 v[52:55], v[68:71], v[132:135], v[52:55]\n\t"
        "v_mfma_f32_16x16x32_bf16 v[32:35], v[136:139], v[72:75], v[32:35]\n\t"
        "v_mfma_f32_16x16x32_bf16 v[36:39], v[140:143], v[72:75], v[36:39]\n\t"
        "ds_read_b128 v[68:71], v108 offset:34816\n\t"
        "v_mfma_f32_16x16x32_bf16 v[40:43], v[72:75], v[144:147], v[40:43]\n\t"
        "s_add_u32 m0, %[ld], 66560\n\t"
        "s_add_u32 %[t0], %[xo], 17408\n\t"
        "v_mfma_f32_16x16x32_bf16 v[44:47], v[136:139], v[76:79], v[44:47]\n\t"
        "buffer_load_dwordx4 v100, %[sx], %[t0] offen lds\n\t"
        "v_mfma_f32_16x16x32_bf16 v[48:51], v[140:143], v[76:79], v[48:51]\n\t"
        "ds_read_b128 v[72:75], v107 offset:40960\n\t"
        "v_mfma_f32_16x16x32_bf16 v[52:55], v[76:79], v[144:147], v[52:55]\n\t"
        "v_mfma_f32_16x16x32_bf16 v[32:35], v[148:151], v[80:83], v[32:35]\n\t"
        "v_mfma_f32_16x16x32_bf16 v[36:39], v[152:155], v[80:83], v[36:39]\n\t"
        "ds_read_b128 v[76:79], v107 offset:43008\n\t"
        "v_mfma_f32_16x16x32_bf16 v[40:43], v[80:83], v[156:159], v[40:43]\n\t"
        "s_add_u32 m0, %[ld], 73728\n\t"
        "s_add_u32 %[t0], %[xo], 1152\n\t"
        "v_mfma_f32_16x16x32_bf16 v[44:47], v[148:151], v[84:87], v[44:47]\n\t"
        "buffer_load_dwordx4 v100, %[sx], %[t0] offen lds\n\t"
        "v_mfma_f32_16x16x32_bf16 v[48:51], v[152:155], v[84:87], v[48:51]\n\t"
        "ds_read_b128 v[80:83], v108 offset:40960\n\t"
        "v_mfma_f32_16x16x32_bf16 v[52:55], v[84:87], v[156:159], v[52:55]\n\t"
        "s_waitcnt lgkmcnt(6)\n\t"
        "v_mfma_f32_16x16x32_bf16 v[32:35], v[160:163], v[56:59], v[32:35]\n\t"
        "v_mfma_f32_16x16x32_bf16 v[36:39], v[164:167], v[56:59], v[36:39]\n\t"
        "ds_read_b128 v[84:87], v108 offset:43008\n\t"
        "v_mfma_f32_16x16x32_bf16 v[40:43], v[56:59], v[168:171], v[40:43]\n\t"
        "s_add_u32 m0, %[ld], 74752\n\t"
        "s_add_u32 %[t0], %[xo], 17536\n\t"
        "s_waitcnt lgkmcnt(6)\n\t"
        "v_mfma_f32_16x16x32_bf16 v[44:47], v[160:163], v[60:63], v[44:47]\n\t"
        "buffer_load_dwordx4 v100, %[sx], %[t0] offen lds\n\t"
        "v_mfma_f32_16x16x32_bf16 v[48:51], v[164:167], v[60:63], v[48:51]\n\t"
        "ds_read_b128 v[56:59], v107 offset:49152\n\t"
        "v_mfma_f32_16x16x32_bf16 v[52:55], v[60:63], v[168:171], v[52:55]\n\t"
        "s_waitcnt lgkmcnt(6)\n\t"
        "v_mfma_f32_16x16x32_bf16 v[32:35], v[172:175], v[64:67], v[32:35]\n\t"
        "v_mfma_f32_16x16x32_bf16 v[36:39], v[176:179], v[64:67], v[36:39]\n\t"
        "ds_read_b128 v[60:63], v107 offset:51200\n\t"
        "v_mfma_f32_16x16x32_bf16 v[40:43], v[64:67], v[180:183], v[40:43]\n\t"
        "s_add_u32 m0, %[ld], 81920\n\t"
        "s_add_u32 %[t0], %[xo], 1280\n\t"
        "s_waitcnt lgkmcnt(6)\n\t"
        "v_mfma_f32_16x16x32_bf16 v[44:47], v[172:175], v[68:71], v[44:47]\n\t"
        "buffer_load_dwordx4 v100, %[sx], %[t0] offen lds\n\t"
        "v_mfma_f32_16x16x32_bf16 v[48:51], v[176:179], v[68:71], v[48:51]\n\t"
        "ds_read_b128 v[64:67], v108 offset:49152\n\t"
        "v_mfma_f32_16x16x32_bf16 v[52:55], v[68:71], v[180:183], v[52:55]\n\t"
        "s_waitcnt lgkmcnt(6)\n\t"
        "v_mfma_f32_16x16x32_bf16 v[32:35], v[184:187], v[72:75], v[32:35]\n\t"
        "v_mfma_f32_16x16x32_bf16 v[36:39], v[188:191], v[72:75], v[36:39]\n\t"
        "ds_read_b128 v[68:71], v108 offset:51200\n\t"
        "v_mfma_f32_16x16x32_bf16 v[40:43], v[72:75], v[192:195], v[40:43]\n\t"
        "s_add_u32 m0, %[ld], 82944\n\t"
        "s_add_u32 %[t0], %[xo], 17664\n\t"
        "s_waitcnt lgkmcnt(6)\n\t"
        "v_mfma_f32_16x16x32_bf16 v[44:47], v[184:187], v[76:79], v[44:47]\n\t"
        "buffer_load_dwordx4 v100, %[sx], %[t0] offen lds\n\t"
        "v_mfma_f32_16x16x32_bf16 v[48:51], v[188:191], v[76:79], v[48:51]\n\t"
        "ds_read_b128 v[72:75], v107 offset:57344\n\t"
        "v_mfma_f32_16x16x32_bf16 v[52:55], v[76:79], v[192:195], v[52:55]\n\t"
        "s_waitcnt lgkmcnt(6)\n\t"
        "v_mfma_f32_16x16x32_bf16 v[32:35], v[196:199], v[80:83], v[32:35]\n\t"
        "v_mfma_f32_16x16x32_bf16 v[36:39], v[200:203], v[80:83], v[36:39]\n\t"
        "ds_read_b128 v[76:79], v107 offset:59392\n\t"
        "v_mfma_f32_16x16x32_bf16 v[40:43], v[80:83], v[204:207], v[40:43]\n\t"
        "s_add_u32 m0, %[ld], 90112\n\t"
        "s_add_u32 %[t0], %[xo], 1408\n\t"
        "s_waitcnt lgkmcnt(6)\n\t"
        "v_mfma_f32_16x16x32_bf16 v[44:47], v[196:199], v[84:87], v[44:47]\n\t"
        "buffer_load_dwordx4 v100, %[sx], %[t0] offen lds\n\t"
        "v_mfma_f32_16x16x32_bf16 v[48:51], v[200:203], v[84:87], v[48:51]\n\t"
        "ds_read_b128 v[80:83], v108 offset:57344\n\t"
        "v_mfma_f32_16x16x32_bf16 v[52:55], v[84:87], v[204:207], v[52:55]\n\t"
        "s_waitcnt lgkmcnt(6)\n\t"
        "v_mfma_f32_16x16x32_bf16 v[32:35], v[208:211], v[56:59], v[32:35]\n\t"
        "v_mfma_f32_16x16x32_bf16 v[36:39], v[212:215], v[56:59], v[36:39]\n\t"
        "ds_read_b128 v[84:87], v108 offset:59392\n\t"
        "v_mfma_f32_16x16x32_bf16 v[40:43], v[56:59], v[216:219], v[40:43]\n\t"
        "s_add_u32 m0, %[ld], 91136\n\t"
        "s_add_u32 %[t0], %[xo], 17792\n\t"
        "s_waitcnt lgkmcnt(6)\n\t"
        "v_mfma_f32_16x16x32_bf16 v[44:47], v[208:211], v[60:63], v[44:47]\n\t"
        "buffer_load_dwordx4 v100, %[sx], %[t0] offen lds\n\t"
        "v_mfma_f32_16x16x32_bf16 v[48:51], v[212:215], v[60:63], v[48:51]\n\t"
        "v_mfma_f32_16x16x32_bf16 v[52:55], v[60:63], v[216:219], v[52:55]\n\t"
        "s_waitcnt lgkmcnt(5)\n\t"
        "v_mfma_f32_16x16x32_bf16 v[32:35], v[220:223], v[64:67], v[32:35]\n\t"
        "s_waitcnt vmcnt(17) lgkmcnt(0)\n\t"
        "s_barrier\n\t"
        "v_mfma_f32_16x16x32_bf16 v[36:39], v[224:227], v[64:67], v[36:39]\n\t"
        "ds_read_b128 v[56:59], v105 offset:0\n\t"
        "v_mfma_f32_16x16x32_bf16 v[40:43], v[64:67], v[228:231], v[40:43]\n\t"
        "ds_read_b128 v[60:63], v105 offset:2048\n\t"
        "s_add_u32 m0, %[ld], 98304\n\t"
        "s_add_u32 %[t0], %[xo], 1536\n\t"
        "v_mfma_f32_16x16x32_bf16 v[44:47], v[220:223], v[68:71], v[44:47]\n\t"
        "buffer_load_dwordx4 v100, %[sx], %[t0] offen lds\n\t"
        "v_mfma_f32_16x16x32_bf16 v[48:51], v[224:227], v[68:71], v[48:51]\n\t"
        "ds_read_b128 v[64:67], v106 offset:0\n\t"
        "v_mfma_f32_16x16x32_bf16 v[52:55], v[68:71], v[228:231], v[52:55]\n\t"
        "v_mfma_f32_16x16x32_bf16 v[32:35], v[232:235], v[72:75], v[32:35]\n\t"
        "v_mfma_f32_16x16x32_bf16 v[36:39], v[236:239], v[72:75], v[36:39]\n\t"
        "ds_read_b128 v[68:71], v106 offset:2048\n\t"
        "v_mfma_f32_16x16x32_bf16 v[40:43], v[72:75], v[240:243], v[40:43]\n\t"
        "s_add_u32 m0, %[ld], 99328\n\t"
        "s_add_u32 %[t0], %[xo], 17920\n\t"
        "v_mfma_f32_16x16x32_bf16 v[44:47], v[232:235], v[76:79], v[44:47]\n\t"
        "buffer_load_dwordx4 v100, %[sx], %[t0] offen lds\n\t"
        "v_mfma_f32_16x16x32_bf16 v[48:51], v[236:239], v[76:79], v[48:51]\n\t"
        "ds_read_b128 v[72:75], v105 offset:8192\n\t"
        "v_mfma_f32_16x16x32_bf16 v[52:55], v[76:79], v[240:243], v[52:55]\n\t"
        "v_mfma_f32_16x16x32_bf16 v[32:35], v[244:247], v[80:83], v[32:35]\n\t"
        "v_mfma_f32_16x16x32_bf16 v[36:39], v[248:251], v[80:83], v[36:39]\n\t"
        "ds_read_b128 v[76:79], v105 offset:10240\n\t"
        "v_mfma_f32_16x16x32_bf16 v[40:43], v[80:83], v[252:255], v[40:43]\n\t"
        "s_add_u32 m0, %[ld], 106496\n\t"
        "s_add_u32 %[t0], %[xo], 1664\n\t"
        "v_mfma_f32_16x16x32_bf16 v[44:47], v[244:247], v[84:87], v[44:47]\n\t"
        "buffer_load_dwordx4 v100, %[sx], %[t0] offen lds\n\t"
        "v_mfma_f32_16x16x32_bf16 v[48:51], v[248:251], v[84:87], v[48:51]\n\t"
        "ds_read_b128 v[80:83], v106 offset:8192\n\t"
        "v_mfma_f32_16x16x32_bf16 v[52:55], v[84:87], v[252:255], v[52:55]\n\t"
        "s_cmp_lt_u32 %[cn], 32\n\t"
        "s_cbranch_scc1 LG_loop%=\n\t"
        "s_nop 7\n\t"
        "v_cvt_pk_bf16_f32 v88, v32, v33\n\t"
        "v_cvt_pk_bf16_f32 v89, v34, v35\n\t"
        "v_cvt_pk_bf16_f32 v90, v36, v37\n\t"
        "v_cvt_pk_bf16_f32 v91, v38, v39\n\t"
        "buffer_store_dwordx4 v[88:91], v109, %[sw], %[so] offen sc1\n\t"
        "v_cvt_pk_bf16_f32 v92, v44, v45\n\t"
        "v_cvt_pk_bf16_f32 v93, v46, v47\n\t"
        "v_cvt_pk_bf16_f32 v94, v48, v49\n\t"
        "v_cvt_pk_bf16_f32 v95, v50, v51\n\t"
        "buffer_store_dwordx4 v[92:95], v110, %[sw], %[so] offen sc1\n\t"
        "v_cvt_pk_bf16_f32 v96, v40, v41\n\t"
        "v_cvt_pk_bf16_f32 v97, v42, v43\n\t"
        "v_cvt_pk_bf16_f32 v98, v52, v53\n\t"
        "v_cvt_pk_bf16_f32 v99, v54, v55\n\t"
        "buffer_store_dwordx4 v[96:99], v111, %[sw], %[so] offen sc1\n\t"
        "s_waitcnt vmcnt(0) lgkmcnt(0)\n\t"
        "v_mov_b32_e32 v117, %[cn]\n\t"
        "buffer_store_dword v117, v113, %[sw], 0 offen sc1\n\t"
        "s_barrier\n\t"
        "LG_end%=:\n\t"
        ".Lk_end_G:\n\t"
        : [t0] "=&s"(t0), [xo] "=&s"(xo), [so] "=&s"(so), [cn] "=&s"(cn), [ts] "=&s"(ts), [ta] "=&s"(ta), [tb] "=&s"(tb), [tc] "=&s"(tc), [td] "=&s"(td)
        : [sx] "s"(sx), [sq] "s"(sq), [sv] "s"(sv), [sw] "s"(sw), [ld] "s"(lb + w * 2048),
          "{v100}"((16 * w + (l >> 3)) * 2048 + (((l & 7) ^ (l >> 3)) << 4)), "{v101}"((8 * w + (l >> 3)) * 8192 + h * 256 + (c & 1) * 128 + (l & 7) * 16),
          "{v112}"((16 * w + (l >> 2)) * 8192 + h * 256 + cv * 64 + (l & 3) * 16), "{v102}"(lb + 131072 + w * 1024 + ((l * 16) ^ ((l & 16) << 2))), "{v116}"(lb + 131072 + w * 1024 + ((l * 16) ^ (l & 32))),
          "{v103}"(lr + rr * 128 + ((tn ^ (n >> 3)) * 64) + (n & 3) * 16), "{v104}"(lr + 8192 + rr * 64 + ((tn ^ (g & 1)) * 32) + (n & 3) * 8), "{v105}"(lx), "{v106}"(lx ^ 64u), "{v107}"(lx + 65536), "{v108}"((lx ^ 64u) + 65536),
          "{v109}"(aq), "{v110}"(aq + 4096), "{v111}"(W_VF + (tm * 32 + h) * 8192 + (2 * cv + tn) * 1024 + l * 16), "{v113}"(l == 0 ? F_KVQ + h * 1024 + (c * 4 + w) * 64 : OOB), "{v[114:115]}"(tp)
        : "memory", "scc", "m0", "v8", "v9", "v10", "v11", "v12", "v13", "v14", "v15", "v16", "v17", "v18", "v19", "v20", "v21", "v22", "v23", "v24", "v25", "v26", "v27", "v28", "v29", "v30", "v31", "v32", "v33", "v34", "v35", "v36", "v37", "v38", "v39", "v40", "v41", "v42", "v43", "v44", "v45", "v46", "v47", "v48", "v49", "v50", "v51", "v52", "v53", "v54", "v55", "v56", "v57", "v58", "v59", "v60", "v61", "v62", "v63", "v64", "v65", "v66", "v67", "v68", "v69", "v70", "v71", "v72", "v73", "v74", "v75", "v76", "v77", "v78", "v79", "v80", "v81", "v82", "v83", "v84", "v85", "v86", "v87", "v88", "v89", "v90", "v91", "v92", "v93", "v94", "v95", "v96", "v97", "v98", "v99", "v117", "v118", "v119", "v120", "v121", "v122", "v123", "v124", "v125", "v126", "v127", "v128", "v129", "v130", "v131", "v132", "v133", "v134", "v135", "v136", "v137", "v138", "v139", "v140", "v141", "v142", "v143", "v144", "v145", "v146", "v147", "v148", "v149", "v150", "v151", "v152", "v153", "v154", "v155", "v156", "v157", "v158", "v159", "v160", "v161", "v162", "v163", "v164", "v165", "v166", "v167", "v168", "v169", "v170", "v171", "v172", "v173", "v174", "v175", "v176", "v177", "v178", "v179", "v180", "v181", "v182", "v183", "v184", "v185", "v186", "v187", "v188", "v189", "v190", "v191", "v192", "v193", "v194", "v195", "v196", "v197", "v198", "v199", "v200", "v201", "v202", "v203", "v204", "v205", "v206", "v207", "v208", "v209", "v210", "v211", "v212", "v213", "v214", "v215", "v216", "v217", "v218", "v219", "v220", "v221", "v222", "v223", "v224", "v225", "v226", "v227", "v228", "v229", "v230", "v231", "v232", "v233", "v234", "v235", "v236", "v237", "v238", "v239", "v240", "v241", "v242", "v243", "v244", "v245", "v246", "v247", "v248", "v249", "v250", "v251", "v252", "v253", "v254", "v255", "a0", "a1", "a2", "a3", "a4", "a5", "a6", "a7", "a8", "a9", "a10", "a11", "a12", "a13", "a14", "a15", "a16", "a17", "a18", "a19", "a20", "a21", "a22", "a23", "a24", "a25", "a26", "a27", "a28", "a29", "a30", "a31", "a32", "a33", "a34", "a35", "a36", "a37", "a38", "a39", "a40", "a41", "a42", "a43", "a44", "a45", "a46", "a47", "a48", "a49", "a50", "a51", "a52", "a53", "a54", "a55", "a56", "a57", "a58", "a59", "a60", "a61", "a62", "a63", "a64", "a65", "a66", "a67", "a68", "a69", "a70", "a71", "a72", "a73", "a74", "a75", "a76", "a77", "a78", "a79", "a80", "a81", "a82", "a83", "a84", "a85", "a86", "a87", "a88", "a89", "a90", "a91", "a92", "a93", "a94", "a95", "a96", "a97", "a98", "a99", "a100", "a101", "a102", "a103", "a104", "a105", "a106", "a107", "a108", "a109", "a110", "a111", "a112", "a113", "a114", "a115", "a116", "a117", "a118", "a119", "a120", "a121", "a122", "a123", "a124", "a125", "a126", "a127", "a128", "a129", "a130", "a131", "a132", "a133", "a134", "a135", "a136", "a137", "a138", "a139", "a140", "a141", "a142", "a143", "a144", "a145", "a146", "a147", "a148", "a149", "a150", "a151", "a152", "a153", "a154", "a155", "a156", "a157", "a158", "a159", "a160", "a161", "a162", "a163", "a164", "a165", "a166", "a167", "a168", "a169", "a170", "a171", "a172", "a173", "a174", "a175", "a176", "a177", "a178", "a179", "a180", "a181", "a182", "a183", "a184", "a185", "a186", "a187", "a188", "a189", "a190", "a191", "a192", "a193", "a194", "a195", "a196", "a197", "a198", "a199", "a200", "a201", "a202", "a203", "a204", "a205", "a206", "a207", "a208", "a209", "a210", "a211", "a212", "a213", "a214", "a215", "a216", "a217", "a218", "a219", "a220", "a221", "a222", "a223", "a224", "a225", "a226", "a227", "a228", "a229", "a230", "a231", "a232", "a233", "a234", "a235", "a236", "a237", "a238", "a239", "a240", "a241", "a242", "a243", "a244", "a245", "a246", "a247", "a248", "a249", "a250", "a251", "a252", "a253", "a254", "a255");
}

__device__ __forceinline__ void out_block(const Params& p, unsigned char* lds, const int wv, const int rp, const int nt, const int j) {
    const int w = wv, l = otid() & 63, n = l & 15, g = l >> 4;
    const unsigned lb = __builtin_amdgcn_readfirstlane((unsigned)(size_t)lds), stg = lb + 131072 + w * 8192;
    const v4u so = mk_srd(p.Wo, 1024 * 4096 * 2), sw = mk_srd(p.ws, W_TOTAL), su = mk_srd(p.out, S * D * 2);
    const unsigned lr = stg + (4 * g + (n >> 2)) * 128 + ((2 * (g & 1) + (n >> 3)) << 5) + (n & 3) * 8;
    unsigned t0, t1, tl = rp, pg, cu, nx, sp; unsigned long long tu;
    asm volatile(
        "s_add_u32 %[t1], %[ro], 0\n\t"
        "s_and_b32 %[t1], %[t1], 7\n\t"
        "s_lshl_b32 %[t1], %[t1], 18\n\t"
        "s_add_u32 %[t0], %[t1], 0\n\t"
        "buffer_load_dwordx4 a[0:3], v110, %[so], %[t0] offen\n\t"
        "s_sleep 2\n\t"
        "s_add_u32 %[t0], %[t1], 16384\n\t"
        "buffer_load_dwordx4 a[4:7], v110, %[so], %[t0] offen\n\t"
        "s_sleep 2\n\t"
        "s_add_u32 %[t0], %[t1], 32768\n\t"
        "buffer_load_dwordx4 a[8:11], v110, %[so], %[t0] offen\n\t"
        "s_sleep 2\n\t"
        "s_add_u32 %[t0], %[t1], 49152\n\t"
        "buffer_load_dwordx4 a[12:15], v110, %[so], %[t0] offen\n\t"
        "s_sleep 2\n\t"
        "s_add_u32 %[t0], %[t1], 65536\n\t"
        "buffer_load_dwordx4 a[16:19], v110, %[so], %[t0] offen\n\t"
        "s_sleep 2\n\t"
        "s_add_u32 %[t0], %[t1], 81920\n\t"
        "buffer_load_dwordx4 a[20:23], v110, %[so], %[t0] offen\n\t"
        "s_sleep 2\n\t"
        "s_add_u32 %[t0], %[t1], 98304\n\t"
        "buffer_load_dwordx4 v[24:27], v110, %[so], %[t0] offen\n\t"
        "s_sleep 2\n\t"
        "s_add_u32 %[t0], %[t1], 114688\n\t"
        "buffer_load_dwordx4 v[28:31], v110, %[so], %[t0] offen\n\t"
        "s_sleep 2\n\t"
        "s_add_u32 %[t1], %[ro], 0\n\t"
        "s_and_b32 %[t1], %[t1], 7\n\t"
        "s_lshl_b32 %[t1], %[t1], 18\n\t"
        "s_add_u32 %[t0], %[t1], 131072\n\t"
        "buffer_load_dwordx4 a[24:27], v110, %[so], %[t0] offen\n\t"
        "s_sleep 2\n\t"
        "s_add_u32 %[t0], %[t1], 147456\n\t"
        "buffer_load_dwordx4 a[28:31], v110, %[so], %[t0] offen\n\t"
        "s_sleep 2\n\t"
        "s_add_u32 %[t0], %[t1], 163840\n\t"
        "buffer_load_dwordx4 a[32:35], v110, %[so], %[t0] offen\n\t"
        "s_sleep 2\n\t"
        "s_add_u32 %[t0], %[t1], 180224\n\t"
        "buffer_load_dwordx4 a[36:39], v110, %[so], %[t0] offen\n\t"
        "s_sleep 2\n\t"
        "s_add_u32 %[t0], %[t1], 196608\n\t"
        "buffer_load_dwordx4 a[40:43], v110, %[so], %[t0] offen\n\t"
        "s_sleep 2\n\t"
        "s_add_u32 %[t0], %[t1], 212992\n\t"
        "buffer_load_dwordx4 a[44:47], v110, %[so], %[t0] offen\n\t"
        "s_sleep 2\n\t"
        "s_add_u32 %[t0], %[t1], 229376\n\t"
        "buffer_load_dwordx4 v[32:35], v110, %[so], %[t0] offen\n\t"
        "s_sleep 2\n\t"
        "s_add_u32 %[t0], %[t1], 245760\n\t"
        "buffer_load_dwordx4 v[36:39], v110, %[so], %[t0] offen\n\t"
        "s_sleep 2\n\t"
        "s_waitcnt vmcnt(8)\n\t"
        "ds_write_b128 v111, a[0:3] offset:0\n\t"
        "ds_write_b128 v111, a[4:7] offset:1024\n\t"
        "ds_write_b128 v111, a[8:11] offset:2048\n\t"
        "ds_write_b128 v111, a[12:15] offset:3072\n\t"
        "ds_write_b128 v111, a[16:19] offset:4096\n\t"
        "ds_write_b128 v111, a[20:23] offset:5120\n\t"
        "ds_write_b128 v111, v[24:27] offset:6144\n\t"
        "ds_write_b128 v111, v[28:31] offset:7168\n\t"
        "ds_read_b64_tr_b16 a[0:1], v112 offset:0\n\t"
        "ds_read_b64_tr_b16 a[2:3], v112 offset:2048\n\t"
        "ds_read_b64_tr_b16 a[12:13], v112 offset:4096\n\t"
        "ds_read_b64_tr_b16 a[14:15], v112 offset:6144\n\t"
        "s_waitcnt lgkmcnt(8)\n\t"
        "ds_read_b64_tr_b16 a[4:5], v113 offset:0\n\t"
        "ds_read_b64_tr_b16 a[6:7], v113 offset:2048\n\t"
        "ds_read_b64_tr_b16 a[16:17], v113 offset:4096\n\t"
        "ds_read_b64_tr_b16 a[18:19], v113 offset:6144\n\t"
        "s_waitcnt lgkmcnt(8)\n\t"
        "ds_read_b64_tr_b16 a[8:9], v114 offset:0\n\t"
        "ds_read_b64_tr_b16 a[10:11], v114 offset:2048\n\t"
        "ds_read_b64_tr_b16 a[20:21], v114 offset:4096\n\t"
        "ds_read_b64_tr_b16 a[22:23], v114 offset:6144\n\t"
        "s_waitcnt lgkmcnt(8)\n\t"
        "ds_read_b64_tr_b16 v[56:57], v115 offset:0\n\t"
        "ds_read_b64_tr_b16 v[58:59], v115 offset:2048\n\t"
        "ds_read_b64_tr_b16 v[60:61], v115 offset:4096\n\t"
        "ds_read_b64_tr_b16 v[62:63], v115 offset:6144\n\t"
        "s_add_u32 %[t1], %[ro], 1\n\t"
        "s_and_b32 %[t1], %[t1], 7\n\t"
        "s_lshl_b32 %[t1], %[t1], 18\n\t"
        "s_add_u32 %[t0], %[t1], 0\n\t"
        "buffer_load_dwordx4 a[48:51], v110, %[so], %[t0] offen\n\t"
        "s_sleep 2\n\t"
        "s_add_u32 %[t0], %[t1], 16384\n\t"
        "buffer_load_dwordx4 a[52:55], v110, %[so], %[t0] offen\n\t"
        "s_sleep 2\n\t"
        "s_add_u32 %[t0], %[t1], 32768\n\t"
        "buffer_load_dwordx4 a[56:59], v110, %[so], %[t0] offen\n\t"
        "s_sleep 2\n\t"
        "s_add_u32 %[t0], %[t1], 49152\n\t"
        "buffer_load_dwordx4 a[60:63], v110, %[so], %[t0] offen\n\t"
        "s_sleep 2\n\t"
        "s_add_u32 %[t0], %[t1], 65536\n\t"
        "buffer_load_dwordx4 a[64:67], v110, %[so], %[t0] offen\n\t"
        "s_sleep 2\n\t"
        "s_add_u32 %[t0], %[t1], 81920\n\t"
        "buffer_load_dwordx4 a[68:71], v110, %[so], %[t0] offen\n\t"
        "s_sleep 2\n\t"
        "s_add_u32 %[t0], %[t1], 98304\n\t"
        "buffer_load_dwordx4 v[40:43], v110, %[so], %[t0] offen\n\t"
        "s_sleep 2\n\t"
        "s_add_u32 %[t0], %[t1], 114688\n\t"
        "buffer_load_dwordx4 v[44:47], v110, %[so], %[t0] offen\n\t"
        "s_sleep 2\n\t"
        "s_waitcnt lgkmcnt(0)\n\t"
        "ds_write_b128 v116, v[56:59] offset:0\n\t"
        "ds_write_b128 v116, v[60:63] offset:1024\n\t"
        "s_waitcnt vmcnt(8)\n\t"
        "ds_write_b128 v111, a[24:27] offset:0\n\t"
        "ds_write_b128 v111, a[28:31] offset:1024\n\t"
        "ds_write_b128 v111, a[32:35] offset:2048\n\t"
        "ds_write_b128 v111, a[36:39] offset:3072\n\t"
        "ds_write_b128 v111, a[40:43] offset:4096\n\t"
        "ds_write_b128 v111, a[44:47] offset:5120\n\t"
        "ds_write_b128 v111, v[32:35] offset:6144\n\t"
        "ds_write_b128 v111, v[36:39] offset:7168\n\t"
        "ds_read_b64_tr_b16 a[24:25], v112 offset:0\n\t"
        "ds_read_b64_tr_b16 a[26:27], v112 offset:2048\n\t"
        "ds_read_b64_tr_b16 a[36:37], v112 offset:4096\n\t"
        "ds_read_b64_tr_b16 a[38:39], v112 offset:6144\n\t"
        "s_waitcnt lgkmcnt(8)\n\t"
        "ds_read_b64_tr_b16 a[28:29], v113 offset:0\n\t"
        "ds_read_b64_tr_b16 a[30:31], v113 offset:2048\n\t"
        "ds_read_b64_tr_b16 a[40:41], v113 offset:4096\n\t"
        "ds_read_b64_tr_b16 a[42:43], v113 offset:6144\n\t"
        "s_waitcnt lgkmcnt(8)\n\t"
        "ds_read_b64_tr_b16 a[32:33], v114 offset:0\n\t"
        "ds_read_b64_tr_b16 a[34:35], v114 offset:2048\n\t"
        "ds_read_b64_tr_b16 a[44:45], v114 offset:4096\n\t"
        "ds_read_b64_tr_b16 a[46:47], v114 offset:6144\n\t"
        "s_waitcnt lgkmcnt(8)\n\t"
        "ds_read_b64_tr_b16 v[56:57], v115 offset:0\n\t"
        "ds_read_b64_tr_b16 v[58:59], v115 offset:2048\n\t"
        "ds_read_b64_tr_b16 v[60:61], v115 offset:4096\n\t"
        "ds_read_b64_tr_b16 v[62:63], v115 offset:6144\n\t"
        "s_add_u32 %[t1], %[ro], 1\n\t"
        "s_and_b32 %[t1], %[t1], 7\n\t"
        "s_lshl_b32 %[t1], %[t1], 18\n\t"
        "s_add_u32 %[t0], %[t1], 131072\n\t"
        "buffer_load_dwordx4 a[72:75], v110, %[so], %[t0] offen\n\t"
        "s_sleep 2\n\t"
        "s_add_u32 %[t0], %[t1], 147456\n\t"
        "buffer_load_dwordx4 a[76:79], v110, %[so], %[t0] offen\n\t"
        "s_sleep 2\n\t"
        "s_add_u32 %[t0], %[t1], 163840\n\t"
        "buffer_load_dwordx4 a[80:83], v110, %[so], %[t0] offen\n\t"
        "s_sleep 2\n\t"
        "s_add_u32 %[t0], %[t1], 180224\n\t"
        "buffer_load_dwordx4 a[84:87], v110, %[so], %[t0] offen\n\t"
        "s_sleep 2\n\t"
        "s_add_u32 %[t0], %[t1], 196608\n\t"
        "buffer_load_dwordx4 a[88:91], v110, %[so], %[t0] offen\n\t"
        "s_sleep 2\n\t"
        "s_add_u32 %[t0], %[t1], 212992\n\t"
        "buffer_load_dwordx4 a[92:95], v110, %[so], %[t0] offen\n\t"
        "s_sleep 2\n\t"
        "s_add_u32 %[t0], %[t1], 229376\n\t"
        "buffer_load_dwordx4 v[48:51], v110, %[so], %[t0] offen\n\t"
        "s_sleep 2\n\t"
        "s_add_u32 %[t0], %[t1], 245760\n\t"
        "buffer_load_dwordx4 v[52:55], v110, %[so], %[t0] offen\n\t"
        "s_sleep 2\n\t"
        "s_waitcnt lgkmcnt(0)\n\t"
        "ds_write_b128 v116, v[56:59] offset:2048\n\t"
        "ds_write_b128 v116, v[60:63] offset:3072\n\t"
        "s_waitcnt vmcnt(8)\n\t"
        "ds_write_b128 v111, a[48:51] offset:0\n\t"
        "ds_write_b128 v111, a[52:55] offset:1024\n\t"
        "ds_write_b128 v111, a[56:59] offset:2048\n\t"
        "ds_write_b128 v111, a[60:63] offset:3072\n\t"
        "ds_write_b128 v111, a[64:67] offset:4096\n\t"
        "ds_write_b128 v111, a[68:71] offset:5120\n\t"
        "ds_write_b128 v111, v[40:43] offset:6144\n\t"
        "ds_write_b128 v111, v[44:47] offset:7168\n\t"
        "ds_read_b64_tr_b16 a[48:49], v112 offset:0\n\t"
        "ds_read_b64_tr_b16 a[50:51], v112 offset:2048\n\t"
        "ds_read_b64_tr_b16 a[60:61], v112 offset:4096\n\t"
        "ds_read_b64_tr_b16 a[62:63], v112 offset:6144\n\t"
        "s_waitcnt lgkmcnt(8)\n\t"
        "ds_read_b64_tr_b16 a[52:53], v113 offset:0\n\t"
        "ds_read_b64_tr_b16 a[54:55], v113 offset:2048\n\t"
        "ds_read_b64_tr_b16 a[64:65], v113 offset:4096\n\t"
        "ds_read_b64_tr_b16 a[66:67], v113 offset:6144\n\t"
        "s_waitcnt lgkmcnt(8)\n\t"
        "ds_read_b64_tr_b16 a[56:57], v114 offset:0\n\t"
        "ds_read_b64_tr_b16 a[58:59], v114 offset:2048\n\t"
        "ds_read_b64_tr_b16 a[68:69], v114 offset:4096\n\t"
        "ds_read_b64_tr_b16 a[70:71], v114 offset:6144\n\t"
        "s_waitcnt lgkmcnt(8)\n\t"
        "ds_read_b64_tr_b16 v[56:57], v115 offset:0\n\t"
        "ds_read_b64_tr_b16 v[58:59], v115 offset:2048\n\t"
        "ds_read_b64_tr_b16 v[60:61], v115 offset:4096\n\t"
        "ds_read_b64_tr_b16 v[62:63], v115 offset:6144\n\t"
        "s_add_u32 %[t1], %[ro], 2\n\t"
        "s_and_b32 %[t1], %[t1], 7\n\t"
        "s_lshl_b32 %[t1], %[t1], 18\n\t"
        "s_add_u32 %[t0], %[t1], 0\n\t"
        "buffer_load_dwordx4 a[96:99], v110, %[so], %[t0] offen\n\t"
        "s_sleep 2\n\t"
        "s_add_u32 %[t0], %[t1], 16384\n\t"
        "buffer_load_dwordx4 a[100:103], v110, %[so], %[t0] offen\n\t"
        "s_sleep 2\n\t"
        "s_add_u32 %[t0], %[t1], 32768\n\t"
        "buffer_load_dwordx4 a[104:107], v110, %[so], %[t0] offen\n\t"
        "s_sleep 2\n\t"
        "s_add_u32 %[t0], %[t1], 49152\n\t"
        "buffer_load_dwordx4 a[108:111], v110, %[so], %[t0] offen\n\t"
        "s_sleep 2\n\t"
        "s_add_u32 %[t0], %[t1], 65536\n\t"
        "buffer_load_dwordx4 a[112:115], v110, %[so], %[t0] offen\n\t"
        "s_sleep 2\n\t"
        "s_add_u32 %[t0], %[t1], 81920\n\t"
        "buffer_load_dwordx4 a[116:119], v110, %[so], %[t0] offen\n\t"
        "s_sleep 2\n\t"
        "s_add_u32 %[t0], %[t1], 98304\n\t"
        "buffer_load_dwordx4 v[24:27], v110, %[so], %[t0] offen\n\t"
        "s_sleep 2\n\t"
        "s_add_u32 %[t0], %[t1], 114688\n\t"
        "buffer_load_dwordx4 v[28:31], v110, %[so], %[t0] offen\n\t"
        "s_sleep 2\n\t"
        "s_waitcnt lgkmcnt(0)\n\t"
        "ds_write_b128 v116, v[56:59] offset:4096\n\t"
        "ds_write_b128 v116, v[60:63] offset:5120\n\t"
        "s_waitcnt vmcnt(8)\n\t"
        "ds_write_b128 v111, a[72:75] offset:0\n\t"
        "ds_write_b128 v111, a[76:79] offset:1024\n\t"
        "ds_write_b128 v111, a[80:83] offset:2048\n\t"
        "ds_write_b128 v111, a[84:87] offset:3072\n\t"
        "ds_write_b128 v111, a[88:91] offset:4096\n\t"
        "ds_write_b128 v111, a[92:95] offset:5120\n\t"
        "ds_write_b128 v111, v[48:51] offset:6144\n\t"
        "ds_write_b128 v111, v[52:55] offset:7168\n\t"
        "ds_read_b64_tr_b16 a[72:73], v112 offset:0\n\t"
        "ds_read_b64_tr_b16 a[74:75], v112 offset:2048\n\t"
        "ds_read_b64_tr_b16 a[84:85], v112 offset:4096\n\t"
        "ds_read_b64_tr_b16 a[86:87], v112 offset:6144\n\t"
        "s_waitcnt lgkmcnt(8)\n\t"
        "ds_read_b64_tr_b16 a[76:77], v113 offset:0\n\t"
        "ds_read_b64_tr_b16 a[78:79], v113 offset:2048\n\t"
        "ds_read_b64_tr_b16 a[88:89], v113 offset:4096\n\t"
        "ds_read_b64_tr_b16 a[90:91], v113 offset:6144\n\t"
        "s_waitcnt lgkmcnt(8)\n\t"
        "ds_read_b64_tr_b16 a[80:81], v114 offset:0\n\t"
        "ds_read_b64_tr_b16 a[82:83], v114 offset:2048\n\t"
        "ds_read_b64_tr_b16 a[92:93], v114 offset:4096\n\t"
        "ds_read_b64_tr_b16 a[94:95], v114 offset:6144\n\t"
        "s_waitcnt lgkmcnt(8)\n\t"
        "ds_read_b64_tr_b16 v[56:57], v115 offset:0\n\t"
        "ds_read_b64_tr_b16 v[58:59], v115 offset:2048\n\t"
        "ds_read_b64_tr_b16 v[60:61], v115 offset:4096\n\t"
        "ds_read_b64_tr_b16 v[62:63], v115 offset:6144\n\t"
        "s_add_u32 %[t1], %[ro], 2\n\t"
        "s_and_b32 %[t1], %[t1], 7\n\t"
        "s_lshl_b32 %[t1], %[t1], 18\n\t"
        "s_add_u32 %[t0], %[t1], 131072\n\t"
        "buffer_load_dwordx4 a[120:123], v110, %[so], %[t0] offen\n\t"
        "s_sleep 2\n\t"
        "s_add_u32 %[t0], %[t1], 147456\n\t"
        "buffer_load_dwordx4 a[124:127], v110, %[so], %[t0] offen\n\t"
        "s_sleep 2\n\t"
        "s_add_u32 %[t0], %[t1], 163840\n\t"
        "buffer_load_dwordx4 a[128:131], v110, %[so], %[t0] offen\n\t"
        "s_sleep 2\n\t"
        "s_add_u32 %[t0], %[t1], 180224\n\t"
        "buffer_load_dwordx4 a[132:135], v110, %[so], %[t0] offen\n\t"
        "s_sleep 2\n\t"
        "s_add_u32 %[t0], %[t1], 196608\n\t"
        "buffer_load_dwordx4 a[136:139], v110, %[so], %[t0] offen\n\t"
        "s_sleep 2\n\t"
        "s_add_u32 %[t0], %[t1], 212992\n\t"
        "buffer_load_dwordx4 a[140:143], v110, %[so], %[t0] offen\n\t"
        "s_sleep 2\n\t"
        "s_add_u32 %[t0], %[t1], 229376\n\t"
        "buffer_load_dwordx4 v[32:35], v110, %[so], %[t0] offen\n\t"
        "s_sleep 2\n\t"
        "s_add_u32 %[t0], %[t1], 245760\n\t"
        "buffer_load_dwordx4 v[36:39], v110, %[so], %[t0] offen\n\t"
        "s_sleep 2\n\t"
        "s_waitcnt lgkmcnt(0)\n\t"
        "ds_write_b128 v116, v[56:59] offset:6144\n\t"
        "ds_write_b128 v116, v[60:63] offset:7168\n\t"
        "s_waitcnt vmcnt(8)\n\t"
        "ds_write_b128 v111, a[96:99] offset:0\n\t"
        "ds_write_b128 v111, a[100:103] offset:1024\n\t"
        "ds_write_b128 v111, a[104:107] offset:2048\n\t"
        "ds_write_b128 v111, a[108:111] offset:3072\n\t"
        "ds_write_b128 v111, a[112:115] offset:4096\n\t"
        "ds_write_b128 v111, a[116:119] offset:5120\n\t"
        "ds_write_b128 v111, v[24:27] offset:6144\n\t"
        "ds_write_b128 v111, v[28:31] offset:7168\n\t"
        "ds_read_b64_tr_b16 a[96:97], v112 offset:0\n\t"
        "ds_read_b64_tr_b16 a[98:99], v112 offset:2048\n\t"
        "ds_read_b64_tr_b16 a[108:109], v112 offset:4096\n\t"
        "ds_read_b64_tr_b16 a[110:111], v112 offset:6144\n\t"
        "s_waitcnt lgkmcnt(8)\n\t"
        "ds_read_b64_tr_b16 a[100:101], v113 offset:0\n\t"
        "ds_read_b64_tr_b16 a[102:103], v113 offset:2048\n\t"
        "ds_read_b64_tr_b16 a[112:113], v113 offset:4096\n\t"
        "ds_read_b64_tr_b16 a[114:115], v113 offset:6144\n\t"
        "s_waitcnt lgkmcnt(8)\n\t"
        "ds_read_b64_tr_b16 a[104:105], v114 offset:0\n\t"
        "ds_read_b64_tr_b16 a[106:107], v114 offset:2048\n\t"
        "ds_read_b64_tr_b16 a[116:117], v114 offset:4096\n\t"
        "ds_read_b64_tr_b16 a[118:119], v114 offset:6144\n\t"
        "s_waitcnt lgkmcnt(8)\n\t"
        "ds_read_b64_tr_b16 v[56:57], v115 offset:0\n\t"
        "ds_read_b64_tr_b16 v[58:59], v115 offset:2048\n\t"
        "ds_read_b64_tr_b16 v[60:61], v115 offset:4096\n\t"
        "ds_read_b64_tr_b16 v[62:63], v115 offset:6144\n\t"
        "s_add_u32 %[t1], %[ro], 3\n\t"
        "s_and_b32 %[t1], %[t1], 7\n\t"
        "s_lshl_b32 %[t1], %[t1], 18\n\t"
        "s_add_u32 %[t0], %[t1], 0\n\t"
        "buffer_load_dwordx4 a[144:147], v110, %[so], %[t0] offen\n\t"
        "s_sleep 2\n\t"
        "s_add_u32 %[t0], %[t1], 16384\n\t"
        "buffer_load_dwordx4 a[148:151], v110, %[so], %[t0] offen\n\t"
        "s_sleep 2\n\t"
        "s_add_u32 %[t0], %[t1], 32768\n\t"
        "buffer_load_dwordx4 a[152:155], v110, %[so], %[t0] offen\n\t"
        "s_sleep 2\n\t"
        "s_add_u32 %[t0], %[t1], 49152\n\t"
        "buffer_load_dwordx4 a[156:159], v110, %[so], %[t0] offen\n\t"
        "s_sleep 2\n\t"
        "s_add_u32 %[t0], %[t1], 65536\n\t"
        "buffer_load_dwordx4 a[160:163], v110, %[so], %[t0] offen\n\t"
        "s_sleep 2\n\t"
        "s_add_u32 %[t0], %[t1], 81920\n\t"
        "buffer_load_dwordx4 a[164:167], v110, %[so], %[t0] offen\n\t"
        "s_sleep 2\n\t"
        "s_add_u32 %[t0], %[t1], 98304\n\t"
        "buffer_load_dwordx4 v[40:43], v110, %[so], %[t0] offen\n\t"
        "s_sleep 2\n\t"
        "s_add_u32 %[t0], %[t1], 114688\n\t"
        "buffer_load_dwordx4 v[44:47], v110, %[so], %[t0] offen\n\t"
        "s_sleep 2\n\t"
        "s_waitcnt lgkmcnt(0)\n\t"
        "ds_write_b128 v116, v[56:59] offset:8192\n\t"
        "ds_write_b128 v116, v[60:63] offset:9216\n\t"
        "s_waitcnt vmcnt(8)\n\t"
        "ds_write_b128 v111, a[120:123] offset:0\n\t"
        "ds_write_b128 v111, a[124:127] offset:1024\n\t"
        "ds_write_b128 v111, a[128:131] offset:2048\n\t"
        "ds_write_b128 v111, a[132:135] offset:3072\n\t"
        "ds_write_b128 v111, a[136:139] offset:4096\n\t"
        "ds_write_b128 v111, a[140:143] offset:5120\n\t"
        "ds_write_b128 v111, v[32:35] offset:6144\n\t"
        "ds_write_b128 v111, v[36:39] offset:7168\n\t"
        "ds_read_b64_tr_b16 a[120:121], v112 offset:0\n\t"
        "ds_read_b64_tr_b16 a[122:123], v112 offset:2048\n\t"
        "ds_read_b64_tr_b16 a[132:133], v112 offset:4096\n\t"
        "ds_read_b64_tr_b16 a[134:135], v112 offset:6144\n\t"
        "s_waitcnt lgkmcnt(8)\n\t"
        "ds_read_b64_tr_b16 a[124:125], v113 offset:0\n\t"
        "ds_read_b64_tr_b16 a[126:127], v113 offset:2048\n\t"
        "ds_read_b64_tr_b16 a[136:137], v113 offset:4096\n\t"
        "ds_read_b64_tr_b16 a[138:139], v113 offset:6144\n\t"
        "s_waitcnt lgkmcnt(8)\n\t"
        "ds_read_b64_tr_b16 a[128:129], v114 offset:0\n\t"
        "ds_read_b64_tr_b16 a[130:131], v114 offset:2048\n\t"
        "ds_read_b64_tr_b16 a[140:141], v114 offset:4096\n\t"
        "ds_read_b64_tr_b16 a[142:143], v114 offset:6144\n\t"
        "s_waitcnt lgkmcnt(8)\n\t"
        "ds_read_b64_tr_b16 v[56:57], v115 offset:0\n\t"
        "ds_read_b64_tr_b16 v[58:59], v115 offset:2048\n\t"
        "ds_read_b64_tr_b16 v[60:61], v115 offset:4096\n\t"
        "ds_read_b64_tr_b16 v[62:63], v115 offset:6144\n\t"
        "s_add_u32 %[t1], %[ro], 3\n\t"
        "s_and_b32 %[t1], %[t1], 7\n\t"
        "s_lshl_b32 %[t1], %[t1], 18\n\t"
        "s_add_u32 %[t0], %[t1], 131072\n\t"
        "buffer_load_dwordx4 a[168:171], v110, %[so], %[t0] offen\n\t"
        "s_sleep 2\n\t"
        "s_add_u32 %[t0], %[t1], 147456\n\t"
        "buffer_load_dwordx4 a[172:175], v110, %[so], %[t0] offen\n\t"
        "s_sleep 2\n\t"
        "s_add_u32 %[t0], %[t1], 163840\n\t"
        "buffer_load_dwordx4 a[176:179], v110, %[so], %[t0] offen\n\t"
        "s_sleep 2\n\t"
        "s_add_u32 %[t0], %[t1], 180224\n\t"
        "buffer_load_dwordx4 a[180:183], v110, %[so], %[t0] offen\n\t"
        "s_sleep 2\n\t"
        "s_add_u32 %[t0], %[t1], 196608\n\t"
        "buffer_load_dwordx4 a[184:187], v110, %[so], %[t0] offen\n\t"
        "s_sleep 2\n\t"
        "s_add_u32 %[t0], %[t1], 212992\n\t"
        "buffer_load_dwordx4 a[188:191], v110, %[so], %[t0] offen\n\t"
        "s_sleep 2\n\t"
        "s_add_u32 %[t0], %[t1], 229376\n\t"
        "buffer_load_dwordx4 v[48:51], v110, %[so], %[t0] offen\n\t"
        "s_sleep 2\n\t"
        "s_add_u32 %[t0], %[t1], 245760\n\t"
        "buffer_load_dwordx4 v[52:55], v110, %[so], %[t0] offen\n\t"
        "s_sleep 2\n\t"
        "s_waitcnt lgkmcnt(0)\n\t"
        "ds_write_b128 v116, v[56:59] offset:10240\n\t"
        "ds_write_b128 v116, v[60:63] offset:11264\n\t"
        "s_waitcnt vmcnt(8)\n\t"
        "ds_write_b128 v111, a[144:147] offset:0\n\t"
        "ds_write_b128 v111, a[148:151] offset:1024\n\t"
        "ds_write_b128 v111, a[152:155] offset:2048\n\t"
        "ds_write_b128 v111, a[156:159] offset:3072\n\t"
        "ds_write_b128 v111, a[160:163] offset:4096\n\t"
        "ds_write_b128 v111, a[164:167] offset:5120\n\t"
        "ds_write_b128 v111, v[40:43] offset:6144\n\t"
        "ds_write_b128 v111, v[44:47] offset:7168\n\t"
        "ds_read_b64_tr_b16 a[144:145], v112 offset:0\n\t"
        "ds_read_b64_tr_b16 a[146:147], v112 offset:2048\n\t"
        "ds_read_b64_tr_b16 a[156:157], v112 offset:4096\n\t"
        "ds_read_b64_tr_b16 a[158:159], v112 offset:6144\n\t"
        "s_waitcnt lgkmcnt(8)\n\t"
        "ds_read_b64_tr_b16 a[148:149], v113 offset:0\n\t"
        "ds_read_b64_tr_b16 a[150:151], v113 offset:2048\n\t"
        "ds_read_b64_tr_b16 a[160:161], v113 offset:4096\n\t"
        "ds_read_b64_tr_b16 a[162:163], v113 offset:6144\n\t"
        "s_waitcnt lgkmcnt(8)\n\t"
        "ds_read_b64_tr_b16 a[152:153], v114 offset:0\n\t"
        "ds_read_b64_tr_b16 a[154:155], v114 offset:2048\n\t"
        "ds_read_b64_tr_b16 a[164:165], v114 offset:4096\n\t"
        "ds_read_b64_tr_b16 a[166:167], v114 offset:6144\n\t"
        "s_waitcnt lgkmcnt(8)\n\t"
        "ds_read_b64_tr_b16 v[56:57], v115 offset:0\n\t"
        "ds_read_b64_tr_b16 v[58:59], v115 offset:2048\n\t"
        "ds_read_b64_tr_b16 v[60:61], v115 offset:4096\n\t"
        "ds_read_b64_tr_b16 v[62:63], v115 offset:6144\n\t"
        "s_add_u32 %[t1], %[ro], 4\n\t"
        "s_and_b32 %[t1], %[t1], 7\n\t"
        "s_lshl_b32 %[t1], %[t1], 18\n\t"
        "s_add_u32 %[t0], %[t1], 0\n\t"
        "buffer_load_dwordx4 a[192:195], v110, %[so], %[t0] offen\n\t"
        "s_sleep 2\n\t"
        "s_add_u32 %[t0], %[t1], 16384\n\t"
        "buffer_load_dwordx4 a[196:199], v110, %[so], %[t0] offen\n\t"
        "s_sleep 2\n\t"
        "s_add_u32 %[t0], %[t1], 32768\n\t"
        "buffer_load_dwordx4 a[200:203], v110, %[so], %[t0] offen\n\t"
        "s_sleep 2\n\t"
        "s_add_u32 %[t0], %[t1], 49152\n\t"
        "buffer_load_dwordx4 a[204:207], v110, %[so], %[t0] offen\n\t"
        "s_sleep 2\n\t"
        "s_add_u32 %[t0], %[t1], 65536\n\t"
        "buffer_load_dwordx4 a[208:211], v110, %[so], %[t0] offen\n\t"
        "s_sleep 2\n\t"
        "s_add_u32 %[t0], %[t1], 81920\n\t"
        "buffer_load_dwordx4 a[212:215], v110, %[so], %[t0] offen\n\t"
        "s_sleep 2\n\t"
        "s_add_u32 %[t0], %[t1], 98304\n\t"
        "buffer_load_dwordx4 v[24:27], v110, %[so], %[t0] offen\n\t"
        "s_sleep 2\n\t"
        "s_add_u32 %[t0], %[t1], 114688\n\t"
        "buffer_load_dwordx4 v[28:31], v110, %[so], %[t0] offen\n\t"
        "s_sleep 2\n\t"
        "s_waitcnt lgkmcnt(0)\n\t"
        "ds_write_b128 v116, v[56:59] offset:12288\n\t"
        "ds_write_b128 v116, v[60:63] offset:13312\n\t"
        "s_waitcnt vmcnt(8)\n\t"
        "ds_write_b128 v111, a[168:171] offset:0\n\t"
        "ds_write_b128 v111, a[172:175] offset:1024\n\t"
        "ds_write_b128 v111, a[176:179] offset:2048\n\t"
        "ds_write_b128 v111, a[180:183] offset:3072\n\t"
        "ds_write_b128 v111, a[184:187] offset:4096\n\t"
        "ds_write_b128 v111, a[188:191] offset:5120\n\t"
        "ds_write_b128 v111, v[48:51] offset:6144\n\t"
        "ds_write_b128 v111, v[52:55] offset:7168\n\t"
        "ds_read_b64_tr_b16 a[168:169], v112 offset:0\n\t"
        "ds_read_b64_tr_b16 a[170:171], v112 offset:2048\n\t"
        "ds_read_b64_tr_b16 a[180:181], v112 offset:4096\n\t"
        "ds_read_b64_tr_b16 a[182:183], v112 offset:6144\n\t"
        "s_waitcnt lgkmcnt(8)\n\t"
        "ds_read_b64_tr_b16 a[172:173], v113 offset:0\n\t"
        "ds_read_b64_tr_b16 a[174:175], v113 offset:2048\n\t"
        "ds_read_b64_tr_b16 a[184:185], v113 offset:4096\n\t"
        "ds_read_b64_tr_b16 a[186:187], v113 offset:6144\n\t"
        "s_waitcnt lgkmcnt(8)\n\t"
        "ds_read_b64_tr_b16 a[176:177], v114 offset:0\n\t"
        "ds_read_b64_tr_b16 a[178:179], v114 offset:2048\n\t"
        "ds_read_b64_tr_b16 a[188:189], v114 offset:4096\n\t"
        "ds_read_b64_tr_b16 a[190:191], v114 offset:6144\n\t"
        "s_waitcnt lgkmcnt(8)\n\t"
        "ds_read_b64_tr_b16 v[56:57], v115 offset:0\n\t"
        "ds_read_b64_tr_b16 v[58:59], v115 offset:2048\n\t"
        "ds_read_b64_tr_b16 v[60:61], v115 offset:4096\n\t"
        "ds_read_b64_tr_b16 v[62:63], v115 offset:6144\n\t"
        "s_add_u32 %[t1], %[ro], 4\n\t"
        "s_and_b32 %[t1], %[t1], 7\n\t"
        "s_lshl_b32 %[t1], %[t1], 18\n\t"
        "s_add_u32 %[t0], %[t1], 131072\n\t"
        "buffer_load_dwordx4 a[216:219], v110, %[so], %[t0] offen\n\t"
        "s_sleep 2\n\t"
        "s_add_u32 %[t0], %[t1], 147456\n\t"
        "buffer_load_dwordx4 a[220:223], v110, %[so], %[t0] offen\n\t"
        "s_sleep 2\n\t"
        "s_add_u32 %[t0], %[t1], 163840\n\t"
        "buffer_load_dwordx4 a[224:227], v110, %[so], %[t0] offen\n\t"
        "s_sleep 2\n\t"
        "s_add_u32 %[t0], %[t1], 180224\n\t"
        "buffer_load_dwordx4 a[228:231], v110, %[so], %[t0] offen\n\t"
        "s_sleep 2\n\t"
        "s_add_u32 %[t0], %[t1], 196608\n\t"
        "buffer_load_dwordx4 a[232:235], v110, %[so], %[t0] offen\n\t"
        "s_sleep 2\n\t"
        "s_add_u32 %[t0], %[t1], 212992\n\t"
        "buffer_load_dwordx4 a[236:239], v110, %[so], %[t0] offen\n\t"
        "s_sleep 2\n\t"
        "s_add_u32 %[t0], %[t1], 229376\n\t"
        "buffer_load_dwordx4 v[32:35], v110, %[so], %[t0] offen\n\t"
        "s_sleep 2\n\t"
        "s_add_u32 %[t0], %[t1], 245760\n\t"
        "buffer_load_dwordx4 v[36:39], v110, %[so], %[t0] offen\n\t"
        "s_sleep 2\n\t"
        "s_waitcnt lgkmcnt(0)\n\t"
        "ds_write_b128 v116, v[56:59] offset:14336\n\t"
        "ds_write_b128 v116, v[60:63] offset:15360\n\t"
        "s_waitcnt vmcnt(8)\n\t"
        "ds_write_b128 v111, a[192:195] offset:0\n\t"
        "ds_write_b128 v111, a[196:199] offset:1024\n\t"
        "ds_write_b128 v111, a[200:203] offset:2048\n\t"
        "ds_write_b128 v111, a[204:207] offset:3072\n\t"
        "ds_write_b128 v111, a[208:211] offset:4096\n\t"
        "ds_write_b128 v111, a[212:215] offset:5120\n\t"
        "ds_write_b128 v111, v[24:27] offset:6144\n\t"
        "ds_write_b128 v111, v[28:31] offset:7168\n\t"
        "ds_read_b64_tr_b16 a[192:193], v112 offset:0\n\t"
        "ds_read_b64_tr_b16 a[194:195], v112 offset:2048\n\t"
        "ds_read_b64_tr_b16 a[204:205], v112 offset:4096\n\t"
        "ds_read_b64_tr_b16 a[206:207], v112 offset:6144\n\t"
        "s_waitcnt lgkmcnt(8)\n\t"
        "ds_read_b64_tr_b16 a[196:197], v113 offset:0\n\t"
        "ds_read_b64_tr_b16 a[198:199], v113 offset:2048\n\t"
        "ds_read_b64_tr_b16 a[208:209], v113 offset:4096\n\t"
        "ds_read_b64_tr_b16 a[210:211], v113 offset:6144\n\t"
        "s_waitcnt lgkmcnt(8)\n\t"
        "ds_read_b64_tr_b16 a[200:201], v114 offset:0\n\t"
        "ds_read_b64_tr_b16 a[202:203], v114 offset:2048\n\t"
        "ds_read_b64_tr_b16 a[212:213], v114 offset:4096\n\t"
        "ds_read_b64_tr_b16 a[214:215], v114 offset:6144\n\t"
        "s_waitcnt lgkmcnt(8)\n\t"
        "ds_read_b64_tr_b16 v[56:57], v115 offset:0\n\t"
        "ds_read_b64_tr_b16 v[58:59], v115 offset:2048\n\t"
        "ds_read_b64_tr_b16 v[60:61], v115 offset:4096\n\t"
        "ds_read_b64_tr_b16 v[62:63], v115 offset:6144\n\t"
        "s_add_u32 %[t1], %[ro], 5\n\t"
        "s_and_b32 %[t1], %[t1], 7\n\t"
        "s_lshl_b32 %[t1], %[t1], 18\n\t"
        "s_add_u32 %[t0], %[t1], 0\n\t"
        "buffer_load_dwordx4 a[240:243], v110, %[so], %[t0] offen\n\t"
        "s_sleep 2\n\t"
        "s_add_u32 %[t0], %[t1], 16384\n\t"
        "buffer_load_dwordx4 a[244:247], v110, %[so], %[t0] offen\n\t"
        "s_sleep 2\n\t"
        "s_add_u32 %[t0], %[t1], 32768\n\t"
        "buffer_load_dwordx4 a[248:251], v110, %[so], %[t0] offen\n\t"
        "s_sleep 2\n\t"
        "s_add_u32 %[t0], %[t1], 49152\n\t"
        "buffer_load_dwordx4 a[252:255], v110, %[so], %[t0] offen\n\t"
        "s_sleep 2\n\t"
        "s_add_u32 %[t0], %[t1], 65536\n\t"
        "buffer_load_dwordx4 v[128:131], v110, %[so], %[t0] offen\n\t"
        "s_sleep 2\n\t"
        "s_add_u32 %[t0], %[t1], 81920\n\t"
        "buffer_load_dwordx4 v[132:135], v110, %[so], %[t0] offen\n\t"
        "s_sleep 2\n\t"
        "s_add_u32 %[t0], %[t1], 98304\n\t"
        "buffer_load_dwordx4 v[40:43], v110, %[so], %[t0] offen\n\t"
        "s_sleep 2\n\t"
        "s_add_u32 %[t0], %[t1], 114688\n\t"
        "buffer_load_dwordx4 v[44:47], v110, %[so], %[t0] offen\n\t"
        "s_sleep 2\n\t"
        "s_waitcnt lgkmcnt(0)\n\t"
        "ds_write_b128 v116, v[56:59] offset:16384\n\t"
        "ds_write_b128 v116, v[60:63] offset:17408\n\t"
        "s_waitcnt vmcnt(8)\n\t"
        "ds_write_b128 v111, a[216:219] offset:0\n\t"
        "ds_write_b128 v111, a[220:223] offset:1024\n\t"
        "ds_write_b128 v111, a[224:227] offset:2048\n\t"
        "ds_write_b128 v111, a[228:231] offset:3072\n\t"
        "ds_write_b128 v111, a[232:235] offset:4096\n\t"
        "ds_write_b128 v111, a[236:239] offset:5120\n\t"
        "ds_write_b128 v111, v[32:35] offset:6144\n\t"
        "ds_write_b128 v111, v[36:39] offset:7168\n\t"
        "ds_read_b64_tr_b16 a[216:217], v112 offset:0\n\t"
        "ds_read_b64_tr_b16 a[218:219], v112 offset:2048\n\t"
        "ds_read_b64_tr_b16 a[228:229], v112 offset:4096\n\t"
        "ds_read_b64_tr_b16 a[230:231], v112 offset:6144\n\t"
        "s_waitcnt lgkmcnt(8)\n\t"
        "ds_read_b64_tr_b16 a[220:221], v113 offset:0\n\t"
        "ds_read_b64_tr_b16 a[222:223], v113 offset:2048\n\t"
        "ds_read_b64_tr_b16 a[232:233], v113 offset:4096\n\t"
        "ds_read_b64_tr_b16 a[234:235], v113 offset:6144\n\t"
        "s_waitcnt lgkmcnt(8)\n\t"
        "ds_read_b64_tr_b16 a[224:225], v114 offset:0\n\t"
        "ds_read_b64_tr_b16 a[226:227], v114 offset:2048\n\t"
        "ds_read_b64_tr_b16 a[236:237], v114 offset:4096\n\t"
        "ds_read_b64_tr_b16 a[238:239], v114 offset:6144\n\t"
        "s_waitcnt lgkmcnt(8)\n\t"
        "ds_read_b64_tr_b16 v[56:57], v115 offset:0\n\t"
        "ds_read_b64_tr_b16 v[58:59], v115 offset:2048\n\t"
        "ds_read_b64_tr_b16 v[60:61], v115 offset:4096\n\t"
        "ds_read_b64_tr_b16 v[62:63], v115 offset:6144\n\t"
        "s_add_u32 %[t1], %[ro], 5\n\t"
        "s_and_b32 %[t1], %[t1], 7\n\t"
        "s_lshl_b32 %[t1], %[t1], 18\n\t"
        "s_add_u32 %[t0], %[t1], 131072\n\t"
        "buffer_load_dwordx4 v[136:139], v110, %[so], %[t0] offen\n\t"
        "s_sleep 2\n\t"
        "s_add_u32 %[t0], %[t1], 147456\n\t"
        "buffer_load_dwordx4 v[140:143], v110, %[so], %[t0] offen\n\t"
        "s_sleep 2\n\t"
        "s_add_u32 %[t0], %[t1], 163840\n\t"
        "buffer_load_dwordx4 v[144:147], v110, %[so], %[t0] offen\n\t"
        "s_sleep 2\n\t"
        "s_add_u32 %[t0], %[t1], 180224\n\t"
        "buffer_load_dwordx4 v[148:151], v110, %[so], %[t0] offen\n\t"
        "s_sleep 2\n\t"
        "s_add_u32 %[t0], %[t1], 196608\n\t"
        "buffer_load_dwordx4 v[152:155], v110, %[so], %[t0] offen\n\t"
        "s_sleep 2\n\t"
        "s_add_u32 %[t0], %[t1], 212992\n\t"
        "buffer_load_dwordx4 v[156:159], v110, %[so], %[t0] offen\n\t"
        "s_sleep 2\n\t"
        "s_add_u32 %[t0], %[t1], 229376\n\t"
        "buffer_load_dwordx4 v[48:51], v110, %[so], %[t0] offen\n\t"
        "s_sleep 2\n\t"
        "s_add_u32 %[t0], %[t1], 245760\n\t"
        "buffer_load_dwordx4 v[52:55], v110, %[so], %[t0] offen\n\t"
        "s_sleep 2\n\t"
        "s_waitcnt lgkmcnt(0)\n\t"
        "ds_write_b128 v116, v[56:59] offset:18432\n\t"
        "ds_write_b128 v116, v[60:63] offset:19456\n\t"
        "s_waitcnt vmcnt(8)\n\t"
        "ds_write_b128 v111, a[240:243] offset:0\n\t"
        "ds_write_b128 v111, a[244:247] offset:1024\n\t"
        "ds_write_b128 v111, a[248:251] offset:2048\n\t"
        "ds_write_b128 v111, a[252:255] offset:3072\n\t"
        "ds_write_b128 v111, v[128:131] offset:4096\n\t"
        "ds_write_b128 v111, v[132:135] offset:5120\n\t"
        "ds_write_b128 v111, v[40:43] offset:6144\n\t"
        "ds_write_b128 v111, v[44:47] offset:7168\n\t"
        "ds_read_b64_tr_b16 a[240:241], v112 offset:0\n\t"
        "ds_read_b64_tr_b16 a[242:243], v112 offset:2048\n\t"
        "ds_read_b64_tr_b16 a[252:253], v112 offset:4096\n\t"
        "ds_read_b64_tr_b16 a[254:255], v112 offset:6144\n\t"
        "s_waitcnt lgkmcnt(8)\n\t"
        "ds_read_b64_tr_b16 a[244:245], v113 offset:0\n\t"
        "ds_read_b64_tr_b16 a[246:247], v113 offset:2048\n\t"
        "ds_read_b64_tr_b16 v[128:129], v113 offset:4096\n\t"
        "ds_read_b64_tr_b16 v[130:131], v113 offset:6144\n\t"
        "s_waitcnt lgkmcnt(8)\n\t"
        "ds_read_b64_tr_b16 a[248:249], v114 offset:0\n\t"
        "ds_read_b64_tr_b16 a[250:251], v114 offset:2048\n\t"
        "ds_read_b64_tr_b16 v[132:133], v114 offset:4096\n\t"
        "ds_read_b64_tr_b16 v[134:135], v114 offset:6144\n\t"
        "s_waitcnt lgkmcnt(8)\n\t"
        "ds_read_b64_tr_b16 v[56:57], v115 offset:0\n\t"
        "ds_read_b64_tr_b16 v[58:59], v115 offset:2048\n\t"
        "ds_read_b64_tr_b16 v[60:61], v115 offset:4096\n\t"
        "ds_read_b64_tr_b16 v[62:63], v115 offset:6144\n\t"
        "s_add_u32 %[t1], %[ro], 6\n\t"
        "s_and_b32 %[t1], %[t1], 7\n\t"
        "s_lshl_b32 %[t1], %[t1], 18\n\t"
        "s_add_u32 %[t0], %[t1], 0\n\t"
        "buffer_load_dwordx4 v[160:163], v110, %[so], %[t0] offen\n\t"
        "s_sleep 2\n\t"
        "s_add_u32 %[t0], %[t1], 16384\n\t"
        "buffer_load_dwordx4 v[164:167], v110, %[so], %[t0] offen\n\t"
        "s_sleep 2\n\t"
        "s_add_u32 %[t0], %[t1], 32768\n\t"
        "buffer_load_dwordx4 v[168:171], v110, %[so], %[t0] offen\n\t"
        "s_sleep 2\n\t"
        "s_add_u32 %[t0], %[t1], 49152\n\t"
        "buffer_load_dwordx4 v[172:175], v110, %[so], %[t0] offen\n\t"
        "s_sleep 2\n\t"
        "s_add_u32 %[t0], %[t1], 65536\n\t"
        "buffer_load_dwordx4 v[176:179], v110, %[so], %[t0] offen\n\t"
        "s_sleep 2\n\t"
        "s_add_u32 %[t0], %[t1], 81920\n\t"
        "buffer_load_dwordx4 v[180:183], v110, %[so], %[t0] offen\n\t"
        "s_sleep 2\n\t"
        "s_add_u32 %[t0], %[t1], 98304\n\t"
        "buffer_load_dwordx4 v[24:27], v110, %[so], %[t0] offen\n\t"
        "s_sleep 2\n\t"
        "s_add_u32 %[t0], %[t1], 114688\n\t"
        "buffer_load_dwordx4 v[28:31], v110, %[so], %[t0] offen\n\t"
        "s_sleep 2\n\t"
        "s_waitcnt lgkmcnt(0)\n\t"
        "ds_write_b128 v116, v[56:59] offset:20480\n\t"
        "ds_write_b128 v116, v[60:63] offset:21504\n\t"
        "s_waitcnt vmcnt(8)\n\t"
        "ds_write_b128 v111, v[136:139] offset:0\n\t"
        "ds_write_b128 v111, v[140:143] offset:1024\n\t"
        "ds_write_b128 v111, v[144:147] offset:2048\n\t"
        "ds_write_b128 v111, v[148:151] offset:3072\n\t"
        "ds_write_b128 v111, v[152:155] offset:4096\n\t"
        "ds_write_b128 v111, v[156:159] offset:5120\n\t"
        "ds_write_b128 v111, v[48:51] offset:6144\n\t"
        "ds_write_b128 v111, v[52:55] offset:7168\n\t"
        "ds_read_b64_tr_b16 v[136:137], v112 offset:0\n\t"
        "ds_read_b64_tr_b16 v[138:139], v112 offset:2048\n\t"
        "ds_read_b64_tr_b16 v[148:149], v112 offset:4096\n\t"
        "ds_read_b64_tr_b16 v[150:151], v112 offset:6144\n\t"
        "s_waitcnt lgkmcnt(8)\n\t"
        "ds_read_b64_tr_b16 v[140:141], v113 offset:0\n\t"
        "ds_read_b64_tr_b16 v[142:143], v113 offset:2048\n\t"
        "ds_read_b64_tr_b16 v[152:153], v113 offset:4096\n\t"
        "ds_read_b64_tr_b16 v[154:155], v113 offset:6144\n\t"
        "s_waitcnt lgkmcnt(8)\n\t"
        "ds_read_b64_tr_b16 v[144:145], v114 offset:0\n\t"
        "ds_read_b64_tr_b16 v[146:147], v114 offset:2048\n\t"
        "ds_read_b64_tr_b16 v[156:157], v114 offset:4096\n\t"
        "ds_read_b64_tr_b16 v[158:159], v114 offset:6144\n\t"
        "s_waitcnt lgkmcnt(8)\n\t"
        "ds_read_b64_tr_b16 v[56:57], v115 offset:0\n\t"
        "ds_read_b64_tr_b16 v[58:59], v115 offset:2048\n\t"
        "ds_read_b64_tr_b16 v[60:61], v115 offset:4096\n\t"
        "ds_read_b64_tr_b16 v[62:63], v115 offset:6144\n\t"
        "s_add_u32 %[t1], %[ro], 6\n\t"
        "s_and_b32 %[t1], %[t1], 7\n\t"
        "s_lshl_b32 %[t1], %[t1], 18\n\t"
        "s_add_u32 %[t0], %[t1], 131072\n\t"
        "buffer_load_dwordx4 v[184:187], v110, %[so], %[t0] offen\n\t"
        "s_sleep 2\n\t"
        "s_add_u32 %[t0], %[t1], 147456\n\t"
        "buffer_load_dwordx4 v[188:191], v110, %[so], %[t0] offen\n\t"
        "s_sleep 2\n\t"
        "s_add_u32 %[t0], %[t1], 163840\n\t"
        "buffer_load_dwordx4 v[192:195], v110, %[so], %[t0] offen\n\t"
        "s_sleep 2\n\t"
        "s_add_u32 %[t0], %[t1], 180224\n\t"
        "buffer_load_dwordx4 v[196:199], v110, %[so], %[t0] offen\n\t"
        "s_sleep 2\n\t"
        "s_add_u32 %[t0], %[t1], 196608\n\t"
        "buffer_load_dwordx4 v[200:203], v110, %[so], %[t0] offen\n\t"
        "s_sleep 2\n\t"
        "s_add_u32 %[t0], %[t1], 212992\n\t"
        "buffer_load_dwordx4 v[204:207], v110, %[so], %[t0] offen\n\t"
        "s_sleep 2\n\t"
        "s_add_u32 %[t0], %[t1], 229376\n\t"
        "buffer_load_dwordx4 v[32:35], v110, %[so], %[t0] offen\n\t"
        "s_sleep 2\n\t"
        "s_add_u32 %[t0], %[t1], 245760\n\t"
        "buffer_load_dwordx4 v[36:39], v110, %[so], %[t0] offen\n\t"
        "s_sleep 2\n\t"
        "s_waitcnt lgkmcnt(0)\n\t"
        "ds_write_b128 v116, v[56:59] offset:22528\n\t"
        "ds_write_b128 v116, v[60:63] offset:23552\n\t"
        "s_waitcnt vmcnt(8)\n\t"
        "ds_write_b128 v111, v[160:163] offset:0\n\t"
        "ds_write_b128 v111, v[164:167] offset:1024\n\t"
        "ds_write_b128 v111, v[168:171] offset:2048\n\t"
        "ds_write_b128 v111, v[172:175] offset:3072\n\t"
        "ds_write_b128 v111, v[176:179] offset:4096\n\t"
        "ds_write_b128 v111, v[180:183] offset:5120\n\t"
        "ds_write_b128 v111, v[24:27] offset:6144\n\t"
        "ds_write_b128 v111, v[28:31] offset:7168\n\t"
        "ds_read_b64_tr_b16 v[160:161], v112 offset:0\n\t"
        "ds_read_b64_tr_b16 v[162:163], v112 offset:2048\n\t"
        "ds_read_b64_tr_b16 v[172:173], v112 offset:4096\n\t"
        "ds_read_b64_tr_b16 v[174:175], v112 offset:6144\n\t"
        "s_waitcnt lgkmcnt(8)\n\t"
        "ds_read_b64_tr_b16 v[164:165], v113 offset:0\n\t"
        "ds_read_b64_tr_b16 v[166:167], v113 offset:2048\n\t"
        "ds_read_b64_tr_b16 v[176:177], v113 offset:4096\n\t"
        "ds_read_b64_tr_b16 v[178:179], v113 offset:6144\n\t"
        "s_waitcnt lgkmcnt(8)\n\t"
        "ds_read_b64_tr_b16 v[168:169], v114 offset:0\n\t"
        "ds_read_b64_tr_b16 v[170:171], v114 offset:2048\n\t"
        "ds_read_b64_tr_b16 v[180:181], v114 offset:4096\n\t"
        "ds_read_b64_tr_b16 v[182:183], v114 offset:6144\n\t"
        "s_waitcnt lgkmcnt(8)\n\t"
        "ds_read_b64_tr_b16 v[56:57], v115 offset:0\n\t"
        "ds_read_b64_tr_b16 v[58:59], v115 offset:2048\n\t"
        "ds_read_b64_tr_b16 v[60:61], v115 offset:4096\n\t"
        "ds_read_b64_tr_b16 v[62:63], v115 offset:6144\n\t"
        "s_add_u32 %[t1], %[ro], 7\n\t"
        "s_and_b32 %[t1], %[t1], 7\n\t"
        "s_lshl_b32 %[t1], %[t1], 18\n\t"
        "s_add_u32 %[t0], %[t1], 0\n\t"
        "buffer_load_dwordx4 v[208:211], v110, %[so], %[t0] offen\n\t"
        "s_sleep 2\n\t"
        "s_add_u32 %[t0], %[t1], 16384\n\t"
        "buffer_load_dwordx4 v[212:215], v110, %[so], %[t0] offen\n\t"
        "s_sleep 2\n\t"
        "s_add_u32 %[t0], %[t1], 32768\n\t"
        "buffer_load_dwordx4 v[216:219], v110, %[so], %[t0] offen\n\t"
        "s_sleep 2\n\t"
        "s_add_u32 %[t0], %[t1], 49152\n\t"
        "buffer_load_dwordx4 v[220:223], v110, %[so], %[t0] offen\n\t"
        "s_sleep 2\n\t"
        "s_add_u32 %[t0], %[t1], 65536\n\t"
        "buffer_load_dwordx4 v[224:227], v110, %[so], %[t0] offen\n\t"
        "s_sleep 2\n\t"
        "s_add_u32 %[t0], %[t1], 81920\n\t"
        "buffer_load_dwordx4 v[228:231], v110, %[so], %[t0] offen\n\t"
        "s_sleep 2\n\t"
        "s_add_u32 %[t0], %[t1], 98304\n\t"
        "buffer_load_dwordx4 v[40:43], v110, %[so], %[t0] offen\n\t"
        "s_sleep 2\n\t"
        "s_add_u32 %[t0], %[t1], 114688\n\t"
        "buffer_load_dwordx4 v[44:47], v110, %[so], %[t0] offen\n\t"
        "s_sleep 2\n\t"
        "s_waitcnt lgkmcnt(0)\n\t"
        "ds_write_b128 v116, v[56:59] offset:24576\n\t"
        "ds_write_b128 v116, v[60:63] offset:25600\n\t"
        "s_waitcnt vmcnt(8)\n\t"
        "ds_write_b128 v111, v[184:187] offset:0\n\t"
        "ds_write_b128 v111, v[188:191] offset:1024\n\t"
        "ds_write_b128 v111, v[192:195] offset:2048\n\t"
        "ds_write_b128 v111, v[196:199] offset:3072\n\t"
        "ds_write_b128 v111, v[200:203] offset:4096\n\t"
        "ds_write_b128 v111, v[204:207] offset:5120\n\t"
        "ds_write_b128 v111, v[32:35] offset:6144\n\t"
        "ds_write_b128 v111, v[36:39] offset:7168\n\t"
        "ds_read_b64_tr_b16 v[184:185], v112 offset:0\n\t"
        "ds_read_b64_tr_b16 v[186:187], v112 offset:2048\n\t"
        "ds_read_b64_tr_b16 v[196:197], v112 offset:4096\n\t"
        "ds_read_b64_tr_b16 v[198:199], v112 offset:6144\n\t"
        "s_waitcnt lgkmcnt(8)\n\t"
        "ds_read_b64_tr_b16 v[188:189], v113 offset:0\n\t"
        "ds_read_b64_tr_b16 v[190:191], v113 offset:2048\n\t"
        "ds_read_b64_tr_b16 v[200:201], v113 offset:4096\n\t"
        "ds_read_b64_tr_b16 v[202:203], v113 offset:6144\n\t"
        "s_waitcnt lgkmcnt(8)\n\t"
        "ds_read_b64_tr_b16 v[192:193], v114 offset:0\n\t"
        "ds_read_b64_tr_b16 v[194:195], v114 offset:2048\n\t"
        "ds_read_b64_tr_b16 v[204:205], v114 offset:4096\n\t"
        "ds_read_b64_tr_b16 v[206:207], v114 offset:6144\n\t"
        "s_waitcnt lgkmcnt(8)\n\t"
        "ds_read_b64_tr_b16 v[56:57], v115 offset:0\n\t"
        "ds_read_b64_tr_b16 v[58:59], v115 offset:2048\n\t"
        "ds_read_b64_tr_b16 v[60:61], v115 offset:4096\n\t"
        "ds_read_b64_tr_b16 v[62:63], v115 offset:6144\n\t"
        "s_add_u32 %[t1], %[ro], 7\n\t"
        "s_and_b32 %[t1], %[t1], 7\n\t"
        "s_lshl_b32 %[t1], %[t1], 18\n\t"
        "s_add_u32 %[t0], %[t1], 131072\n\t"
        "buffer_load_dwordx4 v[232:235], v110, %[so], %[t0] offen\n\t"
        "s_sleep 2\n\t"
        "s_add_u32 %[t0], %[t1], 147456\n\t"
        "buffer_load_dwordx4 v[236:239], v110, %[so], %[t0] offen\n\t"
        "s_sleep 2\n\t"
        "s_add_u32 %[t0], %[t1], 163840\n\t"
        "buffer_load_dwordx4 v[240:243], v110, %[so], %[t0] offen\n\t"
        "s_sleep 2\n\t"
        "s_add_u32 %[t0], %[t1], 180224\n\t"
        "buffer_load_dwordx4 v[244:247], v110, %[so], %[t0] offen\n\t"
        "s_sleep 2\n\t"
        "s_add_u32 %[t0], %[t1], 196608\n\t"
        "buffer_load_dwordx4 v[248:251], v110, %[so], %[t0] offen\n\t"
        "s_sleep 2\n\t"
        "s_add_u32 %[t0], %[t1], 212992\n\t"
        "buffer_load_dwordx4 v[252:255], v110, %[so], %[t0] offen\n\t"
        "s_sleep 2\n\t"
        "s_add_u32 %[t0], %[t1], 229376\n\t"
        "buffer_load_dwordx4 v[48:51], v110, %[so], %[t0] offen\n\t"
        "s_sleep 2\n\t"
        "s_add_u32 %[t0], %[t1], 245760\n\t"
        "buffer_load_dwordx4 v[52:55], v110, %[so], %[t0] offen\n\t"
        "s_sleep 2\n\t"
        "s_waitcnt lgkmcnt(0)\n\t"
        "ds_write_b128 v116, v[56:59] offset:26624\n\t"
        "ds_write_b128 v116, v[60:63] offset:27648\n\t"
        "s_waitcnt vmcnt(8)\n\t"
        "ds_write_b128 v111, v[208:211] offset:0\n\t"
        "ds_write_b128 v111, v[212:215] offset:1024\n\t"
        "ds_write_b128 v111, v[216:219] offset:2048\n\t"
        "ds_write_b128 v111, v[220:223] offset:3072\n\t"
        "ds_write_b128 v111, v[224:227] offset:4096\n\t"
        "ds_write_b128 v111, v[228:231] offset:5120\n\t"
        "ds_write_b128 v111, v[40:43] offset:6144\n\t"
        "ds_write_b128 v111, v[44:47] offset:7168\n\t"
        "ds_read_b64_tr_b16 v[208:209], v112 offset:0\n\t"
        "ds_read_b64_tr_b16 v[210:211], v112 offset:2048\n\t"
        "ds_read_b64_tr_b16 v[220:221], v112 offset:4096\n\t"
        "ds_read_b64_tr_b16 v[222:223], v112 offset:6144\n\t"
        "s_waitcnt lgkmcnt(8)\n\t"
        "ds_read_b64_tr_b16 v[212:213], v113 offset:0\n\t"
        "ds_read_b64_tr_b16 v[214:215], v113 offset:2048\n\t"
        "ds_read_b64_tr_b16 v[224:225], v113 offset:4096\n\t"
        "ds_read_b64_tr_b16 v[226:227], v113 offset:6144\n\t"
        "s_waitcnt lgkmcnt(8)\n\t"
        "ds_read_b64_tr_b16 v[216:217], v114 offset:0\n\t"
        "ds_read_b64_tr_b16 v[218:219], v114 offset:2048\n\t"
        "ds_read_b64_tr_b16 v[228:229], v114 offset:4096\n\t"
        "ds_read_b64_tr_b16 v[230:231], v114 offset:6144\n\t"
        "s_waitcnt lgkmcnt(8)\n\t"
        "ds_read_b64_tr_b16 v[56:57], v115 offset:0\n\t"
        "ds_read_b64_tr_b16 v[58:59], v115 offset:2048\n\t"
        "ds_read_b64_tr_b16 v[60:61], v115 offset:4096\n\t"
        "ds_read_b64_tr_b16 v[62:63], v115 offset:6144\n\t"
        "s_waitcnt lgkmcnt(0)\n\t"
        "ds_write_b128 v116, v[56:59] offset:28672\n\t"
        "ds_write_b128 v116, v[60:63] offset:29696\n\t"
        "s_waitcnt vmcnt(0)\n\t"
        "ds_write_b128 v111, v[232:235] offset:0\n\t"
        "ds_write_b128 v111, v[236:239] offset:1024\n\t"
        "ds_write_b128 v111, v[240:243] offset:2048\n\t"
        "ds_write_b128 v111, v[244:247] offset:3072\n\t"
        "ds_write_b128 v111, v[248:251] offset:4096\n\t"
        "ds_write_b128 v111, v[252:255] offset:5120\n\t"
        "ds_write_b128 v111, v[48:51] offset:6144\n\t"
        "ds_write_b128 v111, v[52:55] offset:7168\n\t"
        "ds_read_b64_tr_b16 v[232:233], v112 offset:0\n\t"
        "ds_read_b64_tr_b16 v[234:235], v112 offset:2048\n\t"
        "ds_read_b64_tr_b16 v[244:245], v112 offset:4096\n\t"
        "ds_read_b64_tr_b16 v[246:247], v112 offset:6144\n\t"
        "s_waitcnt lgkmcnt(8)\n\t"
        "ds_read_b64_tr_b16 v[236:237], v113 offset:0\n\t"
        "ds_read_b64_tr_b16 v[238:239], v113 offset:2048\n\t"
        "ds_read_b64_tr_b16 v[248:249], v113 offset:4096\n\t"
        "ds_read_b64_tr_b16 v[250:251], v113 offset:6144\n\t"
        "s_waitcnt lgkmcnt(8)\n\t"
        "ds_read_b64_tr_b16 v[240:241], v114 offset:0\n\t"
        "ds_read_b64_tr_b16 v[242:243], v114 offset:2048\n\t"
        "ds_read_b64_tr_b16 v[252:253], v114 offset:4096\n\t"
        "ds_read_b64_tr_b16 v[254:255], v114 offset:6144\n\t"
        "s_waitcnt lgkmcnt(8)\n\t"
        "ds_read_b64_tr_b16 v[56:57], v115 offset:0\n\t"
        "ds_read_b64_tr_b16 v[58:59], v115 offset:2048\n\t"
        "ds_read_b64_tr_b16 v[60:61], v115 offset:4096\n\t"
        "ds_read_b64_tr_b16 v[62:63], v115 offset:6144\n\t"
        "s_waitcnt lgkmcnt(0)\n\t"
        "ds_write_b128 v116, v[56:59] offset:30720\n\t"
        "ds_write_b128 v116, v[60:63] offset:31744\n\t"
        "ds_read_b128 v[88:91], v116 offset:0\n\t"
        "ds_read_b128 v[92:95], v116 offset:1024\n\t"
        "ds_read_b128 v[96:99], v116 offset:2048\n\t"
        "s_mov_b32 %[sp], 0\n\t"
        "LP_cv%=:\n\t"
        "buffer_load_dword v104, off, %[sw], %[cv] sc1\n\t"
        "s_waitcnt vmcnt(0) lgkmcnt(0)\n\t"
        "v_readfirstlane_b32 %[t0], v104\n\t"
        "s_cmp_ge_u32 %[t0], 8\n\t"
        "s_cbranch_scc1 LP_wait%=\n\t"
        "s_sleep 8\n\t"
        "s_add_u32 %[sp], %[sp], 1\n\t"
        "s_cmp_lt_u32 %[sp], 0x100000\n\t"
        "s_cbranch_scc1 LP_cv%=\n\t"
        "v_mov_b32_e32 v104, 256\n\t"
        "s_mov_b32 %[t0], 33024\n\t"
        "buffer_store_dword v104, off, %[sw], %[t0] sc1\n\t"
        "s_endpgm\n\t"
        "LP_wait%=:\n\t"
        "s_mov_b32 %[sp], 0\n\t"
        "LP_poll%=:\n\t"
        "buffer_load_dword v103, v118, %[sw], 0 offen sc1\n\t"
        "s_lshr_b32 %[t1], %[tl], 1\n\t"
        "s_waitcnt vmcnt(0)\n\t"
        "v_min_u32_dpp v103, v103, v103 quad_perm:[1,0,3,2] row_mask:0xf bank_mask:0xf\n\t"
        "s_nop 1\n\t"
        "v_min_u32_dpp v103, v103, v103 quad_perm:[2,3,0,1] row_mask:0xf bank_mask:0xf\n\t"
        "s_nop 1\n\t"
        "v_min_u32_dpp v103, v103, v103 row_ror:4 row_mask:0xf bank_mask:0xf\n\t"
        "s_nop 0\n\t"
        "v_readfirstlane_b32 %[pg], v103\n\t"
        "s_cmp_gt_u32 %[pg], %[t1]\n\t"
        "s_cbranch_scc1 LP_have%=\n\t"
        "s_cmp_eq_u32 %[pg], %[t1]\n\t"
        "s_cbranch_scc1 LP_near%=\n\t"
        "s_sleep 24\n\t"
        "LP_near%=:\n\t"
        "s_sleep 2\n\t"
        "s_add_u32 %[sp], %[sp], 1\n\t"
        "s_cmp_lt_u32 %[sp], 0x100000\n\t"
        "s_cbranch_scc1 LP_poll%=\n\t"
        "v_mov_b32_e32 v104, 128\n\t"
        "s_mov_b32 %[t0], 33024\n\t"
        "buffer_store_dword v104, off, %[sw], %[t0] sc1\n\t"
        "s_endpgm\n\t"
        "LP_have%=:\n\t"
        "s_lshl_b32 %[nx], %[tl], 17\n\t"
        "s_add_u32 %[t0], %[ro], 0\n\t"
        "s_and_b32 %[t0], %[t0], 7\n\t"
        "s_lshl_b32 %[t0], %[t0], 12\n\t"
        "s_add_u32 %[t0], %[t0], %[nx]\n\t"
        "buffer_load_dwordx4 v[24:27], v117, %[sw], %[t0] offen offset:0 sc1\n\t"
        "buffer_load_dwordx4 v[28:31], v117, %[sw], %[t0] offen offset:1024 sc1\n\t"
        "buffer_load_dwordx4 v[32:35], v117, %[sw], %[t0] offen offset:2048 sc1\n\t"
        "buffer_load_dwordx4 v[36:39], v117, %[sw], %[t0] offen offset:3072 sc1\n\t"
        "s_add_u32 %[t0], %[ro], 1\n\t"
        "s_and_b32 %[t0], %[t0], 7\n\t"
        "s_lshl_b32 %[t0], %[t0], 12\n\t"
        "s_add_u32 %[t0], %[t0], %[nx]\n\t"
        "buffer_load_dwordx4 v[40:43], v117, %[sw], %[t0] offen offset:0 sc1\n\t"
        "buffer_load_dwordx4 v[44:47], v117, %[sw], %[t0] offen offset:1024 sc1\n\t"
        "buffer_load_dwordx4 v[48:51], v117, %[sw], %[t0] offen offset:2048 sc1\n\t"
        "buffer_load_dwordx4 v[52:55], v117, %[sw], %[t0] offen offset:3072 sc1\n\t"
        "s_add_u32 %[t0], %[ro], 2\n\t"
        "s_and_b32 %[t0], %[t0], 7\n\t"
        "s_lshl_b32 %[t0], %[t0], 12\n\t"
        "s_add_u32 %[t0], %[t0], %[nx]\n\t"
        "buffer_load_dwordx4 v[56:59], v117, %[sw], %[t0] offen offset:0 sc1\n\t"
        "buffer_load_dwordx4 v[60:63], v117, %[sw], %[t0] offen offset:1024 sc1\n\t"
        "buffer_load_dwordx4 v[64:67], v117, %[sw], %[t0] offen offset:2048 sc1\n\t"
        "buffer_load_dwordx4 v[68:71], v117, %[sw], %[t0] offen offset:3072 sc1\n\t"
        "s_add_u32 %[t0], %[ro], 3\n\t"
        "s_and_b32 %[t0], %[t0], 7\n\t"
        "s_lshl_b32 %[t0], %[t0], 12\n\t"
        "s_add_u32 %[t0], %[t0], %[nx]\n\t"
        "buffer_load_dwordx4 v[72:75], v117, %[sw], %[t0] offen offset:0 sc1\n\t"
        "buffer_load_dwordx4 v[76:79], v117, %[sw], %[t0] offen offset:1024 sc1\n\t"
        "buffer_load_dwordx4 v[80:83], v117, %[sw], %[t0] offen offset:2048 sc1\n\t"
        "buffer_load_dwordx4 v[84:87], v117, %[sw], %[t0] offen offset:3072 sc1\n\t"
        "buffer_load_dword v103, v118, %[sw], 0 offen sc1\n\t"
        "LP_tile%=:\n\t"
        "s_and_b32 %[t0], %[tl], 1\n\t"
        "s_lshl_b32 %[t0], %[t0], 14\n\t"
        "s_lshr_b32 %[t1], %[tl], 1\n\t"
        "s_lshl_b32 %[t1], %[t1], 6\n\t"
        "s_add_u32 %[t0], %[t0], %[t1]\n\t"
        "buffer_load_dword v100, v119, %[sw], %[t0] offen sc1\n\t"
        "s_add_u32 %[t0], %[t0], 32768\n\t"
        "buffer_load_dword v101, v119, %[sw], %[t0] offen sc1\n\t"
        "s_lshl_b32 %[t0], %[tl], 6\n\t"
        "buffer_load_dword v102, v120, %[sw], %[t0] offen sc1\n\t"
        "buffer_load_dword v103, v118, %[sw], 0 offen sc1\n\t"
        "s_mov_b32 %[cu], %[nx]\n\t"
        "s_waitcnt vmcnt(20) lgkmcnt(2)\n\t"
        "v_mfma_f32_16x16x32_bf16 v[20:23], v[88:91], v[24:27], 0\n\t"
        "ds_read_b128 v[88:91], v116 offset:3072\n\t"
        "v_mfma_f32_16x16x32_bf16 v[8:11], a[0:3], v[24:27], 0\n\t"
        "v_mfma_f32_16x16x32_bf16 v[12:15], a[4:7], v[24:27], 0\n\t"
        "v_mfma_f32_16x16x32_bf16 v[16:19], a[8:11], v[24:27], 0\n\t"
        "s_add_u32 %[t0], %[ro], 4\n\t"
        "s_and_b32 %[t0], %[t0], 7\n\t"
        "s_lshl_b32 %[t0], %[t0], 12\n\t"
        "s_add_u32 %[t0], %[t0], %[cu]\n\t"
        "buffer_load_dwordx4 v[24:27], v117, %[sw], %[t0] offen offset:0 sc1\n\t"
        "s_waitcnt vmcnt(20) lgkmcnt(2)\n\t"
        "v_mfma_f32_16x16x32_bf16 v[20:23], v[92:95], v[28:31], v[20:23]\n\t"
        "ds_read_b128 v[92:95], v116 offset:4096\n\t"
        "v_mfma_f32_16x16x32_bf16 v[8:11], a[12:15], v[28:31], v[8:11]\n\t"
        "v_mfma_f32_16x16x32_bf16 v[12:15], a[16:19], v[28:31], v[12:15]\n\t"
        "v_mfma_f32_16x16x32_bf16 v[16:19], a[20:23], v[28:31], v[16:19]\n\t"
        "buffer_load_dwordx4 v[28:31], v117, %[sw], %[t0] offen offset:1024 sc1\n\t"
        "s_waitcnt vmcnt(20) lgkmcnt(2)\n\t"
        "v_mfma_f32_16x16x32_bf16 v[20:23], v[96:99], v[32:35], v[20:23]\n\t"
        "ds_read_b128 v[96:99], v116 offset:5120\n\t"
        "v_mfma_f32_16x16x32_bf16 v[8:11], a[24:27], v[32:35], v[8:11]\n\t"
        "v_mfma_f32_16x16x32_bf16 v[12:15], a[28:31], v[32:35], v[12:15]\n\t"
        "v_mfma_f32_16x16x32_bf16 v[16:19], a[32:35], v[32:35], v[16:19]\n\t"
        "buffer_load_dwordx4 v[32:35], v117, %[sw], %[t0] offen offset:2048 sc1\n\t"
        "s_waitcnt vmcnt(20) lgkmcnt(2)\n\t"
        "v_mfma_f32_16x16x32_bf16 v[20:23], v[88:91], v[36:39], v[20:23]\n\t"
        "ds_read_b128 v[88:91], v116 offset:6144\n\t"
        "v_mfma_f32_16x16x32_bf16 v[8:11], a[36:39], v[36:39], v[8:11]\n\t"
        "v_mfma_f32_16x16x32_bf16 v[12:15], a[40:43], v[36:39], v[12:15]\n\t"
        "v_mfma_f32_16x16x32_bf16 v[16:19], a[44:47], v[36:39], v[16:19]\n\t"
        "buffer_load_dwordx4 v[36:39], v117, %[sw], %[t0] offen offset:3072 sc1\n\t"
        "s_waitcnt vmcnt(20) lgkmcnt(2)\n\t"
        "v_mfma_f32_16x16x32_bf16 v[20:23], v[92:95], v[40:43], v[20:23]\n\t"
        "ds_read_b128 v[92:95], v116 offset:7168\n\t"
        "v_mfma_f32_16x16x32_bf16 v[8:11], a[48:51], v[40:43], v[8:11]\n\t"
        "v_mfma_f32_16x16x32_bf16 v[12:15], a[52:55], v[40:43], v[12:15]\n\t"
        "v_mfma_f32_16x16x32_bf16 v[16:19], a[56:59], v[40:43], v[16:19]\n\t"
        "s_add_u32 %[t0], %[ro], 5\n\t"
        "s_and_b32 %[t0], %[t0], 7\n\t"
        "s_lshl_b32 %[t0], %[t0], 12\n\t"
        "s_add_u32 %[t0], %[t0], %[cu]\n\t"
        "buffer_load_dwordx4 v[40:43], v117, %[sw], %[t0] offen offset:0 sc1\n\t"
        "s_waitcnt vmcnt(20) lgkmcnt(2)\n\t"
        "v_mfma_f32_16x16x32_bf16 v[20:23], v[96:99], v[44:47], v[20:23]\n\t"
        "ds_read_b128 v[96:99], v116 offset:8192\n\t"
        "v_mfma_f32_16x16x32_bf16 v[8:11], a[60:63], v[44:47], v[8:11]\n\t"
        "v_mfma_f32_16x16x32_bf16 v[12:15], a[64:67], v[44:47], v[12:15]\n\t"
        "v_mfma_f32_16x16x32_bf16 v[16:19], a[68:71], v[44:47], v[16:19]\n\t"
        "buffer_load_dwordx4 v[44:47], v117, %[sw], %[t0] offen offset:1024 sc1\n\t"
        "s_waitcnt vmcnt(20) lgkmcnt(2)\n\t"
        "v_mfma_f32_16x16x32_bf16 v[20:23], v[88:91], v[48:51], v[20:23]\n\t"
        "ds_read_b128 v[88:91], v116 offset:9216\n\t"
        "v_mfma_f32_16x16x32_bf16 v[8:11], a[72:75], v[48:51], v[8:11]\n\t"
        "v_mfma_f32_16x16x32_bf16 v[12:15], a[76:79], v[48:51], v[12:15]\n\t"
        "v_mfma_f32_16x16x32_bf16 v[16:19], a[80:83], v[48:51], v[16:19]\n\t"
        "buffer_load_dwordx4 v[48:51], v117, %[sw], %[t0] offen offset:2048 sc1\n\t"
        "s_waitcnt vmcnt(20) lgkmcnt(2)\n\t"
        "v_mfma_f32_16x16x32_bf16 v[20:23], v[92:95], v[52:55], v[20:23]\n\t"
        "ds_read_b128 v[92:95], v116 offset:10240\n\t"
        "v_mfma_f32_16x16x32_bf16 v[8:11], a[84:87], v[52:55], v[8:11]\n\t"
        "v_mfma_f32_16x16x32_bf16 v[12:15], a[88:91], v[52:55], v[12:15]\n\t"
        "v_mfma_f32_16x16x32_bf16 v[16:19], a[92:95], v[52:55], v[16:19]\n\t"
        "buffer_load_dwordx4 v[52:55], v117, %[sw], %[t0] offen offset:3072 sc1\n\t"
        "s_waitcnt vmcnt(20) lgkmcnt(2)\n\t"
        "s_barrier\n\t"
        "v_mfma_f32_16x16x32_bf16 v[20:23], v[96:99], v[56:59], v[20:23]\n\t"
        "ds_read_b128 v[96:99], v116 offset:11264\n\t"
        "v_mfma_f32_16x16x32_bf16 v[8:11], a[96:99], v[56:59], v[8:11]\n\t"
        "v_mfma_f32_16x16x32_bf16 v[12:15], a[100:103], v[56:59], v[12:15]\n\t"
        "v_mfma_f32_16x16x32_bf16 v[16:19], a[104:107], v[56:59], v[16:19]\n\t"
        "s_add_u32 %[t0], %[ro], 6\n\t"
        "s_and_b32 %[t0], %[t0], 7\n\t"
        "s_lshl_b32 %[t0], %[t0], 12\n\t"
        "s_add_u32 %[t0], %[t0], %[cu]\n\t"
        "buffer_load_dwordx4 v[56:59], v117, %[sw], %[t0] offen offset:0 sc1\n\t"
        "s_waitcnt vmcnt(20) lgkmcnt(2)\n\t"
        "v_mfma_f32_16x16x32_bf16 v[20:23], v[88:91], v[60:63], v[20:23]\n\t"
        "ds_read_b128 v[88:91], v116 offset:12288\n\t"
        "v_mfma_f32_16x16x32_bf16 v[8:11], a[108:111], v[60:63], v[8:11]\n\t"
        "v_mfma_f32_16x16x32_bf16 v[12:15], a[112:115], v[60:63], v[12:15]\n\t"
        "v_mfma_f32_16x16x32_bf16 v[16:19], a[116:119], v[60:63], v[16:19]\n\t"
        "buffer_load_dwordx4 v[60:63], v117, %[sw], %[t0] offen offset:1024 sc1\n\t"
        "s_waitcnt vmcnt(20) lgkmcnt(2)\n\t"
        "v_mfma_f32_16x16x32_bf16 v[20:23], v[92:95], v[64:67], v[20:23]\n\t"
        "ds_read_b128 v[92:95], v116 offset:13312\n\t"
        "v_mfma_f32_16x16x32_bf16 v[8:11], a[120:123], v[64:67], v[8:11]\n\t"
        "v_mfma_f32_16x16x32_bf16 v[12:15], a[124:127], v[64:67], v[12:15]\n\t"
        "v_mfma_f32_16x16x32_bf16 v[16:19], a[128:131], v[64:67], v[16:19]\n\t"
        "buffer_load_dwordx4 v[64:67], v117, %[sw], %[t0] offen offset:2048 sc1\n\t"
        "s_waitcnt vmcnt(20) lgkmcnt(2)\n\t"
        "v_mfma_f32_16x16x32_bf16 v[20:23], v[96:99], v[68:71], v[20:23]\n\t"
        "ds_read_b128 v[96:99], v116 offset:14336\n\t"
        "v_mfma_f32_16x16x32_bf16 v[8:11], a[132:135], v[68:71], v[8:11]\n\t"
        "v_mfma_f32_16x16x32_bf16 v[12:15], a[136:139], v[68:71], v[12:15]\n\t"
        "v_mfma_f32_16x16x32_bf16 v[16:19], a[140:143], v[68:71], v[16:19]\n\t"
        "buffer_load_dwordx4 v[68:71], v117, %[sw], %[t0] offen offset:3072 sc1\n\t"
        "s_waitcnt vmcnt(20) lgkmcnt(2)\n\t"
        "v_mfma_f32_16x16x32_bf16 v[20:23], v[88:91], v[72:75], v[20:23]\n\t"
        "ds_read_b128 v[88:91], v116 offset:15360\n\t"
        "v_mfma_f32_16x16x32_bf16 v[8:11], a[144:147], v[72:75], v[8:11]\n\t"
        "v_mfma_f32_16x16x32_bf16 v[12:15], a[148:151], v[72:75], v[12:15]\n\t"
        "v_mfma_f32_16x16x32_bf16 v[16:19], a[152:155], v[72:75], v[16:19]\n\t"
        "s_add_u32 %[t0], %[ro], 7\n\t"
        "s_and_b32 %[t0], %[t0], 7\n\t"
        "s_lshl_b32 %[t0], %[t0], 12\n\t"
        "s_add_u32 %[t0], %[t0], %[cu]\n\t"
        "buffer_load_dwordx4 v[72:75], v117, %[sw], %[t0] offen offset:0 sc1\n\t"
        "s_waitcnt vmcnt(13) lgkmcnt(2)\n\t"
        "v_mfma_f32_16x16x32_bf16 v[20:23], v[92:95], v[76:79], v[20:23]\n\t"
        "ds_read_b128 v[92:95], v116 offset:16384\n\t"
        "v_min_u32_dpp v103, v103, v103 quad_perm:[1,0,3,2] row_mask:0xf bank_mask:0xf\n\t"
        "s_nop 0\n\t"
        "v_mfma_f32_16x16x32_bf16 v[8:11], a[156:159], v[76:79], v[8:11]\n\t"
        "v_min_u32_dpp v103, v103, v103 quad_perm:[2,3,0,1] row_mask:0xf bank_mask:0xf\n\t"
        "s_nop 0\n\t"
        "v_mfma_f32_16x16x32_bf16 v[12:15], a[160:163], v[76:79], v[12:15]\n\t"
        "v_min_u32_dpp v103, v103, v103 row_ror:4 row_mask:0xf bank_mask:0xf\n\t"
        "s_nop 0\n\t"
        "v_mfma_f32_16x16x32_bf16 v[16:19], a[164:167], v[76:79], v[16:19]\n\t"
        "buffer_load_dwordx4 v[76:79], v117, %[sw], %[t0] offen offset:1024 sc1\n\t"
        "s_waitcnt vmcnt(20) lgkmcnt(2)\n\t"
        "v_mfma_f32_16x16x32_bf16 v[20:23], v[96:99], v[80:83], v[20:23]\n\t"
        "ds_read_b128 v[96:99], v116 offset:17408\n\t"
        "v_readfirstlane_b32 %[pg], v103\n\t"
        "s_add_u32 %[t1], %[tl], 8\n\t"
        "s_lshl_b32 %[nx], %[t1], 17\n\t"
        "s_lshr_b32 %[t1], %[t1], 1\n\t"
        "v_mfma_f32_16x16x32_bf16 v[8:11], a[168:171], v[80:83], v[8:11]\n\t"
        "s_cmp_gt_u32 %[pg], %[t1]\n\t"
        "s_cselect_b32 %[nx], %[nx], 0xf0000000\n\t"
        "v_mfma_f32_16x16x32_bf16 v[12:15], a[172:175], v[80:83], v[12:15]\n\t"
        "v_mfma_f32_16x16x32_bf16 v[16:19], a[176:179], v[80:83], v[16:19]\n\t"
        "buffer_load_dwordx4 v[80:83], v117, %[sw], %[t0] offen offset:2048 sc1\n\t"
        "s_waitcnt vmcnt(20) lgkmcnt(2)\n\t"
        "v_mfma_f32_16x16x32_bf16 v[20:23], v[88:91], v[84:87], v[20:23]\n\t"
        "ds_read_b128 v[88:91], v116 offset:18432\n\t"
        "v_mfma_f32_16x16x32_bf16 v[8:11], a[180:183], v[84:87], v[8:11]\n\t"
        "v_mfma_f32_16x16x32_bf16 v[12:15], a[184:187], v[84:87], v[12:15]\n\t"
        "v_mfma_f32_16x16x32_bf16 v[16:19], a[188:191], v[84:87], v[16:19]\n\t"
        "buffer_load_dwordx4 v[84:87], v117, %[sw], %[t0] offen offset:3072 sc1\n\t"
        "s_waitcnt vmcnt(15) lgkmcnt(2)\n\t"
        "v_mfma_f32_16x16x32_bf16 v[20:23], v[92:95], v[24:27], v[20:23]\n\t"
        "ds_read_b128 v[92:95], v116 offset:19456\n\t"
        "v_mfma_f32_16x16x32_bf16 v[8:11], a[192:195], v[24:27], v[8:11]\n\t"
        "v_mfma_f32_16x16x32_bf16 v[12:15], a[196:199], v[24:27], v[12:15]\n\t"
        "v_mfma_f32_16x16x32_bf16 v[16:19], a[200:203], v[24:27], v[16:19]\n\t"
        "s_add_u32 %[t0], %[ro], 0\n\t"
        "s_and_b32 %[t0], %[t0], 7\n\t"
        "s_lshl_b32 %[t0], %[t0], 12\n\t"
        "s_add_u32 %[t0], %[t0], %[nx]\n\t"
        "buffer_load_dwordx4 v[24:27], v117, %[sw], %[t0] offen offset:0 sc1\n\t"
        "s_waitcnt vmcnt(15) lgkmcnt(2)\n\t"
        "v_mfma_f32_16x16x32_bf16 v[20:23], v[96:99], v[28:31], v[20:23]\n\t"
        "ds_read_b128 v[96:99], v116 offset:20480\n\t"
        "v_mfma_f32_16x16x32_bf16 v[8:11], a[204:207], v[28:31], v[8:11]\n\t"
        "v_mfma_f32_16x16x32_bf16 v[12:15], a[208:211], v[28:31], v[12:15]\n\t"
        "v_sub_f32_e32 v104, 1.0, v102\n\t"
        "v_mfma_f32_16x16x32_bf16 v[16:19], a[212:215], v[28:31], v[16:19]\n\t"
        "buffer_load_dwordx4 v[28:31], v117, %[sw], %[t0] offen offset:1024 sc1\n\t"
        "s_waitcnt vmcnt(15) lgkmcnt(2)\n\t"
        "v_mfma_f32_16x16x32_bf16 v[20:23], v[88:91], v[32:35], v[20:23]\n\t"
        "ds_read_b128 v[88:91], v116 offset:21504\n\t"
        "v_mfma_f32_16x16x32_bf16 v[8:11], a[216:219], v[32:35], v[8:11]\n\t"
        "v_mfma_f32_16x16x32_bf16 v[12:15], a[220:223], v[32:35], v[12:15]\n\t"
        "v_cvt_pk_bf16_f32 v104, 0, v104\n\t"
        "v_mfma_f32_16x16x32_bf16 v[16:19], a[224:227], v[32:35], v[16:19]\n\t"
        "buffer_load_dwordx4 v[32:35], v117, %[sw], %[t0] offen offset:2048 sc1\n\t"
        "s_waitcnt vmcnt(15) lgkmcnt(2)\n\t"
        "v_mfma_f32_16x16x32_bf16 v[20:23], v[92:95], v[36:39], v[20:23]\n\t"
        "ds_read_b128 v[92:95], v116 offset:22528\n\t"
        "v_mfma_f32_16x16x32_bf16 v[8:11], a[228:231], v[36:39], v[8:11]\n\t"
        "v_mfma_f32_16x16x32_bf16 v[12:15], a[232:235], v[36:39], v[12:15]\n\t"
        "v_lshlrev_b32_e32 v106, 16, v100\n\t"
        "v_mfma_f32_16x16x32_bf16 v[16:19], a[236:239], v[36:39], v[16:19]\n\t"
        "buffer_load_dwordx4 v[36:39], v117, %[sw], %[t0] offen offset:3072 sc1\n\t"
        "s_waitcnt vmcnt(15) lgkmcnt(2)\n\t"
        "v_mfma_f32_16x16x32_bf16 v[20:23], v[96:99], v[40:43], v[20:23]\n\t"
        "ds_read_b128 v[96:99], v116 offset:23552\n\t"
        "v_mfma_f32_16x16x32_bf16 v[8:11], a[240:243], v[40:43], v[8:11]\n\t"
        "v_mfma_f32_16x16x32_bf16 v[12:15], a[244:247], v[40:43], v[12:15]\n\t"
        "v_mul_f32_e32 v106, v102, v106\n\t"
        "v_mfma_f32_16x16x32_bf16 v[16:19], a[248:251], v[40:43], v[16:19]\n\t"
        "s_add_u32 %[t0], %[ro], 1\n\t"
        "s_and_b32 %[t0], %[t0], 7\n\t"
        "s_lshl_b32 %[t0], %[t0], 12\n\t"
        "s_add_u32 %[t0], %[t0], %[nx]\n\t"
        "buffer_load_dwordx4 v[40:43], v117, %[sw], %[t0] offen offset:0 sc1\n\t"
        "s_waitcnt vmcnt(15) lgkmcnt(2)\n\t"
        "v_mfma_f32_16x16x32_bf16 v[20:23], v[88:91], v[44:47], v[20:23]\n\t"
        "ds_read_b128 v[88:91], v116 offset:24576\n\t"
        "v_mfma_f32_16x16x32_bf16 v[8:11], a[252:255], v[44:47], v[8:11]\n\t"
        "v_mfma_f32_16x16x32_bf16 v[12:15], v[128:131], v[44:47], v[12:15]\n\t"
        "v_cvt_pk_bf16_f32 v106, 0, v106\n\t"
        "v_mfma_f32_16x16x32_bf16 v[16:19], v[132:135], v[44:47], v[16:19]\n\t"
        "buffer_load_dwordx4 v[44:47], v117, %[sw], %[t0] offen offset:1024 sc1\n\t"
        "s_waitcnt vmcnt(15) lgkmcnt(2)\n\t"
        "v_mfma_f32_16x16x32_bf16 v[20:23], v[92:95], v[48:51], v[20:23]\n\t"
        "ds_read_b128 v[92:95], v116 offset:25600\n\t"
        "v_mfma_f32_16x16x32_bf16 v[8:11], v[136:139], v[48:51], v[8:11]\n\t"
        "v_mfma_f32_16x16x32_bf16 v[12:15], v[140:143], v[48:51], v[12:15]\n\t"
        "v_and_b32_e32 v107, 0xffff0000, v100\n\t"
        "v_mfma_f32_16x16x32_bf16 v[16:19], v[144:147], v[48:51], v[16:19]\n\t"
        "buffer_load_dwordx4 v[48:51], v117, %[sw], %[t0] offen offset:2048 sc1\n\t"
        "s_waitcnt vmcnt(15) lgkmcnt(2)\n\t"
        "v_mfma_f32_16x16x32_bf16 v[20:23], v[96:99], v[52:55], v[20:23]\n\t"
        "ds_read_b128 v[96:99], v116 offset:26624\n\t"
        "v_mfma_f32_16x16x32_bf16 v[8:11], v[148:151], v[52:55], v[8:11]\n\t"
        "v_mfma_f32_16x16x32_bf16 v[12:15], v[152:155], v[52:55], v[12:15]\n\t"
        "v_mul_f32_e32 v107, v102, v107\n\t"
        "v_mfma_f32_16x16x32_bf16 v[16:19], v[156:159], v[52:55], v[16:19]\n\t"
        "buffer_load_dwordx4 v[52:55], v117, %[sw], %[t0] offen offset:3072 sc1\n\t"
        "s_waitcnt vmcnt(15) lgkmcnt(2)\n\t"
        "v_mfma_f32_16x16x32_bf16 v[20:23], v[88:91], v[56:59], v[20:23]\n\t"
        "ds_read_b128 v[88:91], v116 offset:27648\n\t"
        "v_mfma_f32_16x16x32_bf16 v[8:11], v[160:163], v[56:59], v[8:11]\n\t"
        "v_mfma_f32_16x16x32_bf16 v[12:15], v[164:167], v[56:59], v[12:15]\n\t"
        "v_cvt_pk_bf16_f32 v107, 0, v107\n\t"
        "v_mfma_f32_16x16x32_bf16 v[16:19], v[168:171], v[56:59], v[16:19]\n\t"
        "s_add_u32 %[t0], %[ro], 2\n\t"
        "s_and_b32 %[t0], %[t0], 7\n\t"
        "s_lshl_b32 %[t0], %[t0], 12\n\t"
        "s_add_u32 %[t0], %[t0], %[nx]\n\t"
        "buffer_load_dwordx4 v[56:59], v117, %[sw], %[t0] offen offset:0 sc1\n\t"
        "s_waitcnt vmcnt(15) lgkmcnt(2)\n\t"
        "v_mfma_f32_16x16x32_bf16 v[20:23], v[92:95], v[60:63], v[20:23]\n\t"
        "ds_read_b128 v[92:95], v116 offset:28672\n\t"
        "v_mfma_f32_16x16x32_bf16 v[8:11], v[172:175], v[60:63], v[8:11]\n\t"
        "v_mfma_f32_16x16x32_bf16 v[12:15], v[176:179], v[60:63], v[12:15]\n\t"
        "v_lshlrev_b32_e32 v108, 16, v101\n\t"
        "v_mfma_f32_16x16x32_bf16 v[16:19], v[180:183], v[60:63], v[16:19]\n\t"
        "buffer_load_dwordx4 v[60:63], v117, %[sw], %[t0] offen offset:1024 sc1\n\t"
        "s_waitcnt vmcnt(15) lgkmcnt(2)\n\t"
        "v_mfma_f32_16x16x32_bf16 v[20:23], v[96:99], v[64:67], v[20:23]\n\t"
        "ds_read_b128 v[96:99], v116 offset:29696\n\t"
        "v_mfma_f32_16x16x32_bf16 v[8:11], v[184:187], v[64:67], v[8:11]\n\t"
        "v_mfma_f32_16x16x32_bf16 v[12:15], v[188:191], v[64:67], v[12:15]\n\t"
        "v_mul_f32_e32 v108, v102, v108\n\t"
        "v_mfma_f32_16x16x32_bf16 v[16:19], v[192:195], v[64:67], v[16:19]\n\t"
        "buffer_load_dwordx4 v[64:67], v117, %[sw], %[t0] offen offset:2048 sc1\n\t"
        "s_waitcnt vmcnt(15) lgkmcnt(2)\n\t"
        "v_mfma_f32_16x16x32_bf16 v[20:23], v[88:91], v[68:71], v[20:23]\n\t"
        "ds_read_b128 v[88:91], v116 offset:30720\n\t"
        "v_mfma_f32_16x16x32_bf16 v[8:11], v[196:199], v[68:71], v[8:11]\n\t"
        "v_mfma_f32_16x16x32_bf16 v[12:15], v[200:203], v[68:71], v[12:15]\n\t"
        "v_cvt_pk_bf16_f32 v108, 0, v108\n\t"
        "v_mfma_f32_16x16x32_bf16 v[16:19], v[204:207], v[68:71], v[16:19]\n\t"
        "buffer_load_dwordx4 v[68:71], v117, %[sw], %[t0] offen offset:3072 sc1\n\t"
        "s_waitcnt vmcnt(15) lgkmcnt(2)\n\t"
        "v_mfma_f32_16x16x32_bf16 v[20:23], v[92:95], v[72:75], v[20:23]\n\t"
        "ds_read_b128 v[92:95], v116 offset:31744\n\t"
        "v_mfma_f32_16x16x32_bf16 v[8:11], v[208:211], v[72:75], v[8:11]\n\t"
        "v_mfma_f32_16x16x32_bf16 v[12:15], v[212:215], v[72:75], v[12:15]\n\t"
        "v_and_b32_e32 v109, 0xffff0000, v101\n\t"
        "v_mfma_f32_16x16x32_bf16 v[16:19], v[216:219], v[72:75], v[16:19]\n\t"
        "s_add_u32 %[t0], %[ro], 3\n\t"
        "s_and_b32 %[t0], %[t0], 7\n\t"
        "s_lshl_b32 %[t0], %[t0], 12\n\t"
        "s_add_u32 %[t0], %[t0], %[nx]\n\t"
        "buffer_load_dwordx4 v[72:75], v117, %[sw], %[t0] offen offset:0 sc1\n\t"
        "s_waitcnt vmcnt(15) lgkmcnt(2)\n\t"
        "v_mfma_f32_16x16x32_bf16 v[20:23], v[96:99], v[76:79], v[20:23]\n\t"
        "v_mfma_f32_16x16x32_bf16 v[8:11], v[220:223], v[76:79], v[8:11]\n\t"
        "v_mfma_f32_16x16x32_bf16 v[12:15], v[224:227], v[76:79], v[12:15]\n\t"
        "v_mul_f32_e32 v109, v102, v109\n\t"
        "v_mfma_f32_16x16x32_bf16 v[16:19], v[228:231], v[76:79], v[16:19]\n\t"
        "buffer_load_dwordx4 v[76:79], v117, %[sw], %[t0] offen offset:1024 sc1\n\t"
        "s_waitcnt vmcnt(15) lgkmcnt(1)\n\t"
        "v_mfma_f32_16x16x32_bf16 v[20:23], v[88:91], v[80:83], v[20:23]\n\t"
        "ds_read_b128 v[88:91], v116 offset:0\n\t"
        "v_mfma_f32_16x16x32_bf16 v[8:11], v[232:235], v[80:83], v[8:11]\n\t"
        "v_mfma_f32_16x16x32_bf16 v[12:15], v[236:239], v[80:83], v[12:15]\n\t"
        "v_cvt_pk_bf16_f32 v109, 0, v109\n\t"
        "v_mfma_f32_16x16x32_bf16 v[16:19], v[240:243], v[80:83], v[16:19]\n\t"
        "buffer_load_dwordx4 v[80:83], v117, %[sw], %[t0] offen offset:2048 sc1\n\t"
        "s_waitcnt vmcnt(15) lgkmcnt(1)\n\t"
        "v_mfma_f32_16x16x32_bf16 v[20:23], v[92:95], v[84:87], v[20:23]\n\t"
        "ds_read_b128 v[92:95], v116 offset:1024\n\t"
        "v_mfma_f32_16x16x32_bf16 v[8:11], v[244:247], v[84:87], v[8:11]\n\t"
        "ds_read_b128 v[96:99], v116 offset:2048\n\t"
        "v_mfma_f32_16x16x32_bf16 v[12:15], v[248:251], v[84:87], v[12:15]\n\t"
        "v_mfma_f32_16x16x32_bf16 v[16:19], v[252:255], v[84:87], v[16:19]\n\t"
        "buffer_load_dwordx4 v[84:87], v117, %[sw], %[t0] offen offset:3072 sc1\n\t"
        "s_lshl_b32 %[t0], %[tl], 15\n\t"
        "s_nop 0\n\t"
        "ds_write_b128 v122, v[20:23] offset:192\n\t"
        "s_nop 0\n\t"
        "ds_write_b128 v122, v[8:11] offset:0\n\t"
        "s_nop 0\n\t"
        "ds_write_b128 v122, v[12:15] offset:64\n\t"
        "ds_write_b128 v122, v[16:19] offset:128\n\t"
        "s_waitcnt lgkmcnt(0)\n\t"
        "s_barrier\n\t"
        "ds_read_b128 v[8:11], v123 offset:0\n\t"
        "ds_read_b128 v[12:15], v123 offset:4352\n\t"
        "ds_read_b128 v[16:19], v123 offset:8704\n\t"
        "ds_read_b128 v[20:23], v123 offset:13056\n\t"
        "s_waitcnt lgkmcnt(2)\n\t"
        "v_pk_add_f32 v[8:9], v[8:9], v[12:13]\n\t"
        "v_pk_add_f32 v[10:11], v[10:11], v[14:15]\n\t"
        "s_waitcnt lgkmcnt(0)\n\t"
        "v_pk_add_f32 v[16:17], v[16:17], v[20:21]\n\t"
        "v_pk_add_f32 v[18:19], v[18:19], v[22:23]\n\t"
        "v_pk_add_f32 v[8:9], v[8:9], v[16:17]\n\t"
        "v_pk_add_f32 v[10:11], v[10:11], v[18:19]\n\t"
        "v_cvt_pk_bf16_f32 v8, 0, v8\n\t"
        "v_cvt_pk_bf16_f32 v9, 0, v9\n\t"
        "v_cvt_pk_bf16_f32 v10, 0, v10\n\t"
        "v_cvt_pk_bf16_f32 v11, 0, v11\n\t"
        "v_mul_f32_e32 v8, v104, v8\n\t"
        "v_mul_f32_e32 v9, v104, v9\n\t"
        "v_mul_f32_e32 v10, v104, v10\n\t"
        "v_mul_f32_e32 v11, v104, v11\n\t"
        "v_cvt_pk_bf16_f32 v8, 0, v8\n\t"
        "v_cvt_pk_bf16_f32 v9, 0, v9\n\t"
        "v_cvt_pk_bf16_f32 v10, 0, v10\n\t"
        "v_cvt_pk_bf16_f32 v11, 0, v11\n\t"
        "v_add_f32_e32 v8, v106, v8\n\t"
        "v_add_f32_e32 v9, v107, v9\n\t"
        "v_add_f32_e32 v10, v108, v10\n\t"
        "v_add_f32_e32 v11, v109, v11\n\t"
        "v_cvt_pk_bf16_f32 v104, v8, v9\n\t"
        "v_cvt_pk_bf16_f32 v105, v10, v11\n\t"
        "buffer_store_dwordx2 v[104:105], v121, %[su], %[t0] offen sc1\n\t"
        "s_add_u32 %[tl], %[tl], 8\n\t"
        "s_cmp_ge_u32 %[tl], 128\n\t"
        "s_cbranch_scc1 LP_end%=\n\t"
        "s_cmp_lg_u32 %[nx], 0xf0000000\n\t"
        "s_cbranch_scc1 LP_tile%=\n\t"
        "s_branch LP_wait%=\n\t"
        "LP_end%=:\n\t"
        "s_waitcnt vmcnt(0) lgkmcnt(0)\n\t"
        ".Lk_end_P:\n\t"
        : [t0] "=&s"(t0), [t1] "=&s"(t1), [tl] "+s"(tl), [pg] "=&s"(pg), [cu] "=&s"(cu), [nx] "=&s"(nx), [sp] "=&s"(sp), [tu] "=&s"(tu)
        : [so] "s"(so), [sw] "s"(sw), [su] "s"(su), [cv] "s"(F_CVY + nt * 128), [ro] "s"(PROT ? (nt + 2 * rp) & 7 : 0), [db] "s"(W_DBG + (128 + j) * 8192 + 512),
          "{v110}"((w * 1024 + (l >> 3)) * 2048 + nt * 128 + (l & 7) * 16), "{v111}"(stg + ((l * 16) ^ ((l & 48) << 1))), "{v112}"(lr), "{v113}"(lr ^ 32u), "{v114}"(lr ^ 64u), "{v115}"(lr ^ 96u),
          "{v116}"(lb + w * 32768 + l * 16), "{v117}"(W_OX + w * 32768 + l * 16), "{v118}"(F_PROG + (w * 8 + (l & 7)) * 128),
          "{v119}"(W_A + ((nt * 2 + (n >> 3)) * 4 + 2 * (n & 1)) * 32768 + w * 4096 + ((n & 7) >> 1) * 16 + g * 4), "{v120}"(W_GATE + (4 * w + g) * 4), "{v121}"((4 * w + g) * 2048 + nt * 128 + n * 8),
          "{v122}"(lb + 131072 + w * 4352 + n * 272 + g * 16), "{v123}"(lb + 131072 + (4 * w + g) * 272 + n * 16)
        : "memory", "scc", "vcc", "v8", "v9", "v10", "v11", "v12", "v13", "v14", "v15", "v16", "v17", "v18", "v19", "v20", "v21", "v22", "v23", "v24", "v25", "v26", "v27", "v28", "v29", "v30", "v31", "v32", "v33", "v34", "v35", "v36", "v37", "v38", "v39", "v40", "v41", "v42", "v43", "v44", "v45", "v46", "v47", "v48", "v49", "v50", "v51", "v52", "v53", "v54", "v55", "v56", "v57", "v58", "v59", "v60", "v61", "v62", "v63", "v64", "v65", "v66", "v67", "v68", "v69", "v70", "v71", "v72", "v73", "v74", "v75", "v76", "v77", "v78", "v79", "v80", "v81", "v82", "v83", "v84", "v85", "v86", "v87", "v88", "v89", "v90", "v91", "v92", "v93", "v94", "v95", "v96", "v97", "v98", "v99", "v100", "v101", "v102", "v103", "v104", "v105", "v106", "v107", "v108", "v109", "v128", "v129", "v130", "v131", "v132", "v133", "v134", "v135", "v136", "v137", "v138", "v139", "v140", "v141", "v142", "v143", "v144", "v145", "v146", "v147", "v148", "v149", "v150", "v151", "v152", "v153", "v154", "v155", "v156", "v157", "v158", "v159", "v160", "v161", "v162", "v163", "v164", "v165", "v166", "v167", "v168", "v169", "v170", "v171", "v172", "v173", "v174", "v175", "v176", "v177", "v178", "v179", "v180", "v181", "v182", "v183", "v184", "v185", "v186", "v187", "v188", "v189", "v190", "v191", "v192", "v193", "v194", "v195", "v196", "v197", "v198", "v199", "v200", "v201", "v202", "v203", "v204", "v205", "v206", "v207", "v208", "v209", "v210", "v211", "v212", "v213", "v214", "v215", "v216", "v217", "v218", "v219", "v220", "v221", "v222", "v223", "v224", "v225", "v226", "v227", "v228", "v229", "v230", "v231", "v232", "v233", "v234", "v235", "v236", "v237", "v238", "v239", "v240", "v241", "v242", "v243", "v244", "v245", "v246", "v247", "v248", "v249", "v250", "v251", "v252", "v253", "v254", "v255", "a0", "a1", "a2", "a3", "a4", "a5", "a6", "a7", "a8", "a9", "a10", "a11", "a12", "a13", "a14", "a15", "a16", "a17", "a18", "a19", "a20", "a21", "a22", "a23", "a24", "a25", "a26", "a27", "a28", "a29", "a30", "a31", "a32", "a33", "a34", "a35", "a36", "a37", "a38", "a39", "a40", "a41", "a42", "a43", "a44", "a45", "a46", "a47", "a48", "a49", "a50", "a51", "a52", "a53", "a54", "a55", "a56", "a57", "a58", "a59", "a60", "a61", "a62", "a63", "a64", "a65", "a66", "a67", "a68", "a69", "a70", "a71", "a72", "a73", "a74", "a75", "a76", "a77", "a78", "a79", "a80", "a81", "a82", "a83", "a84", "a85", "a86", "a87", "a88", "a89", "a90", "a91", "a92", "a93", "a94", "a95", "a96", "a97", "a98", "a99", "a100", "a101", "a102", "a103", "a104", "a105", "a106", "a107", "a108", "a109", "a110", "a111", "a112", "a113", "a114", "a115", "a116", "a117", "a118", "a119", "a120", "a121", "a122", "a123", "a124", "a125", "a126", "a127", "a128", "a129", "a130", "a131", "a132", "a133", "a134", "a135", "a136", "a137", "a138", "a139", "a140", "a141", "a142", "a143", "a144", "a145", "a146", "a147", "a148", "a149", "a150", "a151", "a152", "a153", "a154", "a155", "a156", "a157", "a158", "a159", "a160", "a161", "a162", "a163", "a164", "a165", "a166", "a167", "a168", "a169", "a170", "a171", "a172", "a173", "a174", "a175", "a176", "a177", "a178", "a179", "a180", "a181", "a182", "a183", "a184", "a185", "a186", "a187", "a188", "a189", "a190", "a191", "a192", "a193", "a194", "a195", "a196", "a197", "a198", "a199", "a200", "a201", "a202", "a203", "a204", "a205", "a206", "a207", "a208", "a209", "a210", "a211", "a212", "a213", "a214", "a215", "a216", "a217", "a218", "a219", "a220", "a221", "a222", "a223", "a224", "a225", "a226", "a227", "a228", "a229", "a230", "a231", "a232", "a233", "a234", "a235", "a236", "a237", "a238", "a239", "a240", "a241", "a242", "a243", "a244", "a245", "a246", "a247", "a248", "a249", "a250", "a251", "a252", "a253", "a254", "a255");
}
__device__ __forceinline__ void bulk_block(const Params& p, rsrc_t r, unsigned char* lds, const int wv, const int j) {
    BSTAMP(0);
    proj_block(p, lds, wv, (j & 6) * 4 + (j >> 4), ((j >> 2) & 2) | (j & 1), j);
    BSTAMP(2);
    if (!conv_core(p, r, lds, wv, j)) return;
    BSTAMP(3);
    if (GDLY) wait_long(rtnow() + GDLY);
    out_block(p, lds, wv, ((j & 7) >> 1) + (j & 1) * 4, j >> 3, j);
    BSTAMP(7);
}

__device__ __forceinline__ void rec_block(const Params& p, rsrc_t r, unsigned char* lds, const int wv, const int b) {
    const int xq = (b & 7) >> 1, t = (b >> 3) * 2 + (b & 1);
    const unsigned lb = __builtin_amdgcn_readfirstlane((unsigned)(size_t)lds);
    int kind, id, rg = 0, rot = 0, bq = 99;
    if (RPL == 0) { kind = t < 8 ? 0 : t < 24 ? 1 : 2; id = t < 8 ? xq * 8 + t : t < 24 ? (xq & 1) * 16 + t - 8 : xq * 8 + t - 24; rg = xq >> 1; }
    else if (RPL == 4) { kind = t < 8 ? 0 : t < 24 ? 1 : 2; id = t < 8 ? xq * 8 + t : t < 24 ? xq * 8 + ((t - 8) >> 1) : xq * 8 + t - 24; rg = t & 1; }
    else if (RPL == 1) { kind = xq == 0 ? 0 : xq < 3 ? 1 : 2; id = t; rg = xq - 1; }
    else if (RPL == 2) { const int na = xq == 2 ? 10 : 11, q = xq * 21 + t - na; kind = xq == 3 ? 2 : t < na ? 0 : 1; id = xq == 3 ? t : t < na ? xq * 11 + t : q >> 1; rg = q & 1; }
    else { const int na = xq == 2 ? 12 : 10, q = t - na; kind = xq == 3 ? 2 : t < na ? 0 : 1; rg = xq < 2 ? xq : q & 1; bq = q;
        const int c2 = q >> 1, cs = xq < 2 ? (q < 6 ? q : q < 20 ? q + 2 : q + 4) : (c2 < 2 ? 6 + c2 : c2 < 4 ? 20 + c2 : 22 + c2);
        id = xq == 3 ? t : t < na ? xq * 10 + t : cs; rot = !ROT ? 0 : xq < 2 ? (cs & 1) * 4 : ((cs & 1) * 2 + rg) * 2; }
    if (kind == 0) rec_a(p, lb, wv, id, xq);
    else if (kind == 1) rec_b(p, lb, wv, id, rg, xq, rot, bq);
    else {
        const int hi = id, j = hi - 32; (void)j;
        BSTAMP(0);
        rec_helper(p, r, lds, wv, hi);
        BSTAMP(6);
        if (XWAIT == 1) { if (!wait_word(r, F_GO + (hi >> 1) * 128, 1u, 1024u)) return; }
        if (XSTG) wait_long(rtnow() + (hi & 7) * XSTG);
        gate_block(p, r, hi, wv);
        BSTAMP(2);
        if (XWAIT == 2) { if (!wait_word(r, F_GO + (hi >> 1) * 128, 1u, 1024u)) return; }
        conv_prep(p, r, wv, hi);
        BSTAMP(3);
    }
}

__global__ void __launch_bounds__(256, 1) k_main(Params p) {
    extern __shared__ __attribute__((aligned(16))) unsigned char lds[];
    rsrc_t r = __builtin_amdgcn_make_buffer_rsrc(p.ws, 0, W_TOTAL, 0x00020000);
    const int wv = __builtin_amdgcn_readfirstlane(threadIdx.x >> 6);
    int role = blockIdx.x;
    unsigned ctouch = 0u;
    if (CODE_T) {
        unsigned long long pc; int ea, eb, eg;
        asm volatile("s_getpc_b64 %0\n.Lk_begin:\n\ts_mov_b32 %1, .Lk_end_A-.Lk_begin\n\ts_mov_b32 %2, .Lk_end_B-.Lk_begin\n\ts_mov_b32 %3, .Lk_end_P-.Lk_begin" : "=s"(pc), "=s"(ea), "=s"(eb), "=s"(eg));
        const int o = ((blockIdx.x >> 3) * 64 + (threadIdx.x >> 2)) * 128;
        if ((threadIdx.x & 3) == 0 && o < max(ea, max(eb, eg)) - 4) asm volatile("global_load_dword %0, %1, off" : "+v"(ctouch) : "v"((const unsigned char*)pc + o) : "memory");
    }
    if (threadIdx.x == 0) {
        unsigned xcc; asm volatile("s_getreg_b32 %0, hwreg(HW_REG_XCC_ID)" : "=s"(xcc)); xcc &= 7u;
        unsigned* tk = (unsigned*)(p.ws + F_TKT);
        unsigned t = __hip_atomic_fetch_add(tk + xcc * 16, 1u, __ATOMIC_RELAXED, __HIP_MEMORY_SCOPE_AGENT);
        if (t >= 32u) {
            unsigned o = __hip_atomic_fetch_add(tk + 128, 1u, __ATOMIC_RELAXED, __HIP_MEMORY_SCOPE_AGENT);
            for (unsigned spin = 0; spin < (1u << 22); ++spin) { unsigned tot = 0u;
                for (int x = 0; x < 8; ++x) tot += __hip_atomic_load(tk + x * 16, __ATOMIC_RELAXED, __HIP_MEMORY_SCOPE_AGENT);
                if (tot >= 256u) break; }
            for (int x = 0; x < 8; ++x) { const unsigned cnt = __hip_atomic_load(tk + x * 16, __ATOMIC_RELAXED, __HIP_MEMORY_SCOPE_AGENT), fr = cnt < 32u ? 32u - cnt : 0u;
                if (o < fr) { xcc = (unsigned)x; t = cnt + o; break; }
                o -= fr; }
        }
        *(volatile int*)lds = (int)((xcc >> 2) * 128u + (t >> 1) * 8u + (xcc & 3u) * 2u + (t & 1u));
    }
    asm volatile("s_waitcnt vmcnt(0)" : "+v"(ctouch) :: "memory");
    __syncthreads();
    role = __builtin_amdgcn_readfirstlane(*(volatile int*)lds) & 255;
    __syncthreads();
    if (role >= 128) bulk_block(p, r, lds, wv, role - 128); else rec_block(p, r, lds, wv, role);
}

extern "C" void kernel_launch(void* const* d_in, const int* in_sizes, int n_in, void* d_out, int out_size, void* d_ws, size_t ws_size, hipStream_t stream) {
    Params p;
    p.Aq = (const bf16_t*)d_in[0]; p.Ak = (const bf16_t*)d_in[1]; p.Av = (const bf16_t*)d_in[2];
    p.shift_k = (const bf16_t*)d_in[3]; p.ssm_k = (const bf16_t*)d_in[4]; p.ssm_D = (const bf16_t*)d_in[5];
    p.mem0 = (const bf16_t*)d_in[7]; p.Wq = (const bf16_t*)d_in[8]; p.Wk = (const bf16_t*)d_in[9];
    p.Wv = (const bf16_t*)d_in[10]; p.Wo = (const bf16_t*)d_in[11]; p.gate_w = (const bf16_t*)d_in[13];
    p.out = (bf16_t*)d_out; p.ws = (unsigned char*)d_ws;
    unsigned char* ws = (unsigned char*)d_ws;
    (void)hipMemsetAsync(ws, 0, FLG_BYTES, stream);
    (void)hipFuncSetAttribute((const void*)k_main, hipFuncAttributeMaxDynamicSharedMemorySize, L_TOTAL);
    k_main<<<256, 256, L_TOTAL, stream>>>(p);
}
```

```cpp
#include <hip/hip_runtime.h>
#include <stdint.h>
#include <type_traits>
typedef unsigned short bf16_t;
using bf16x8 = __attribute__((ext_vector_type(8))) short;
using f32x4 = __attribute__((ext_vector_type(4))) float;
typedef unsigned v4u __attribute__((ext_vector_type(4)));
typedef unsigned v2u __attribute__((ext_vector_type(2)));
typedef __amdgpu_buffer_rsrc_t rsrc_t;

__device__ __forceinline__ float bf2f(bf16_t b) { return __uint_as_float(((unsigned)b) << 16); }
typedef __bf16 hbf2 __attribute__((ext_vector_type(2)));
typedef float f32x2 __attribute__((ext_vector_type(2)));
__device__ __forceinline__ unsigned pack2(float a, float b) { f32x2 v = {a, b}; return __builtin_bit_cast(unsigned, __builtin_convertvector(v, hbf2)); }
__device__ __forceinline__ float rbfh(float f) { return __uint_as_float(pack2(f, 0.f) << 16); }
__device__ __forceinline__ unsigned nosent(unsigned x) {
    typedef unsigned short us2 __attribute__((ext_vector_type(2)));
    const us2 a = __builtin_bit_cast(us2, x), c = {(unsigned short)0xFFFE, (unsigned short)0xFFFE}; return __builtin_bit_cast(unsigned, __builtin_elementwise_min(a, c)); }

constexpr int S = 2048, D = 1024, H = 32, DH = 128, M = 8, SEG = 32, NSEG = 64;

constexpr unsigned W_FLG = 0;
constexpr unsigned F_KVQ = W_FLG;
constexpr unsigned F_CONV = W_FLG + 32768;
constexpr unsigned F_ERR = W_FLG + 32768 + 256;
constexpr unsigned F_PREP = W_FLG + 32768 + 512;
constexpr unsigned F_CAL = W_FLG + 33792;
constexpr unsigned F_TKT = W_FLG + 34816;
constexpr unsigned F_PROG = W_FLG + 49152;
constexpr unsigned F_CVU = W_FLG + 53248, F_CVY = W_FLG + 57344;
constexpr unsigned F_OP = W_FLG + 65536;
constexpr unsigned F_ATT = W_FLG + 131072;
constexpr unsigned F_RDY = W_FLG + 147456;
constexpr unsigned FLG_BYTES = 180224;
constexpr unsigned HO_KVM = W_FLG + FLG_BYTES;
constexpr unsigned W_A = HO_KVM + 64 * 32 * 4096;
constexpr unsigned W_GATE = W_A + S * D * 2;
constexpr unsigned W_QX = W_GATE + 8192;
constexpr unsigned W_KX = W_QX + S * 4096 * 2;
constexpr unsigned W_VF = W_KX + S * 4096 * 2;
constexpr unsigned W_OX = W_VF + 64 * 32 * 8192;
constexpr unsigned W_PART = W_OX + S * 4096 * 2;
constexpr unsigned W_Y = W_PART;
constexpr unsigned W_DBG = W_PART + 32 * 4 * 64 * 1024 * 4;
constexpr unsigned W_U = W_DBG + 256 * 64 * 16 * 8;
constexpr unsigned W_TOTAL = W_U + S * D * 2;

__device__ __forceinline__ v4u ld16(rsrc_t r, unsigned off) { return __builtin_amdgcn_raw_buffer_load_b128(r, off, 0, 16); }
__device__ __forceinline__ v2u ld8(rsrc_t r, unsigned off) { return __builtin_amdgcn_raw_buffer_load_b64(r, off, 0, 16); }
__device__ __forceinline__ unsigned ld4(rsrc_t r, unsigned off) { return __builtin_amdgcn_raw_buffer_load_b32(r, off, 0, 16); }
__device__ __forceinline__ void st16(rsrc_t r, unsigned off, v4u v) { __builtin_amdgcn_raw_buffer_store_b128(v, r, off, 0, 16); }
__device__ __forceinline__ void st8(rsrc_t r, unsigned off, v2u v) { __builtin_amdgcn_raw_buffer_store_b64(v, r, off, 0, 16); }
__device__ __forceinline__ void st4(rsrc_t r, unsigned off, unsigned v) { __builtin_amdgcn_raw_buffer_store_b32(v, r, off, 0, 16); }
__device__ __forceinline__ bool okdw(unsigned x) { return ((x & 0xFFFFu) != 0xFFFFu) & ((x >> 16) != 0xFFFFu); }
typedef unsigned short us2_t __attribute__((ext_vector_type(2)));
__device__ __forceinline__ unsigned pkmax(unsigned a, unsigned b) { return __builtin_bit_cast(unsigned, __builtin_elementwise_max(__builtin_bit_cast(us2_t, a), __builtin_bit_cast(us2_t, b))); }
__device__ __forceinline__ unsigned pkmax4(v4u v) { return pkmax(pkmax(v.x, v.y), pkmax(v.z, v.w)); }
__device__ __forceinline__ bool ok16(v4u v) { return okdw(pkmax4(v)); }
__device__ __forceinline__ bool ok8(v2u v) { return okdw(v.x) & okdw(v.y); }
__device__ __forceinline__ bf16x8 as_frag(v4u v) { return __builtin_bit_cast(bf16x8, v); }
#define MFMA(a, b, c) __builtin_amdgcn_mfma_f32_16x16x32_bf16((a), (b), (c), 0, 0, 0)
#define D1 136
#define D2 85
#define D3 76
#define LT_A 1
#define LB0 12
#define LC0 20
#define LA0 24
#define TR_UP 4
#define TR_DN 2
#define L_DEC 3
#define WARM1 0
#define KROT 0
#define STAG 1
#define UP_SH 2
#define EPI_SLP 0
#define SITA 1
#define PAIRX 4
#define DUE_P 1
#define LATE 1
#define EARLY_FROM 56
#define SKEW 0
#define OUT_WARM 1
#define ADAPT 1
#define RPL 0
#define CV_POL 2
#define POLL_SLP 4
#define TOUCH 1
#define ROT 0
#define CODE_T 1
#define CAL_T 450u
#define GTOUCH 0
#define XTOUCH 32
#define ST_P 32
#define CV_NAP 4000u
#define XSLP 2
#define XDLY 0
#define GDLY 0
#define YDLY 0
#define PROT 3
#define XSTG 0
#define XWAIT 1
#define PEN 48
#define MEMBAR() asm volatile("" ::: "memory")
#define FT(i) do { } while (0)
#define ENDPGM() asm volatile("s_endpgm" ::: "memory")
#define DRAIN() asm volatile("s_waitcnt vmcnt(0)" ::: "memory")

__device__ __forceinline__ float xor16_max(float x) { auto r = __builtin_amdgcn_permlane16_swap(__float_as_uint(x), __float_as_uint(x), false, false); return fmaxf(__uint_as_float(r[0]), __uint_as_float(r[1])); }
__device__ __forceinline__ float xor32_max(float x) { auto r = __builtin_amdgcn_permlane32_swap(__float_as_uint(x), __float_as_uint(x), false, false); return fmaxf(__uint_as_float(r[0]), __uint_as_float(r[1])); }
__device__ __forceinline__ float xor16_add(float x) { auto r = __builtin_amdgcn_permlane16_swap(__float_as_uint(x), __float_as_uint(x), false, false); return __uint_as_float(r[0]) + __uint_as_float(r[1]); }
__device__ __forceinline__ float xor32_add(float x) { auto r = __builtin_amdgcn_permlane32_swap(__float_as_uint(x), __float_as_uint(x), false, false); return __uint_as_float(r[0]) + __uint_as_float(r[1]); }
__device__ __forceinline__ v2u tr_read(unsigned lds_addr) { v2u r; asm volatile("ds_read_b64_tr_b16 %0, %1\n\ts_waitcnt lgkmcnt(0)" : "=&v"(r) : "v"(lds_addr) : "memory"); return r; }

typedef short v4i16_t __attribute__((ext_vector_type(4)));
__device__ __forceinline__ v2u trb(unsigned lds_addr) { return __builtin_bit_cast(v2u, __builtin_amdgcn_ds_read_tr16_b64_v4i16((__attribute__((address_space(3))) v4i16_t*)(size_t)lds_addr)); }
__device__ __forceinline__ int otid_(int wv) { int t; asm volatile("v_mbcnt_lo_u32_b32 %0, -1, 0\n\tv_mbcnt_hi_u32_b32 %0, -1, %0" : "=v"(t)); return wv * 64 + t; }
#define otid() otid_(wv)
__device__ __forceinline__ bool wait_word(rsrc_t r, unsigned off, unsigned want, unsigned code) {
    for (unsigned spin = 0;; ++spin) { MEMBAR();
        const unsigned v = __builtin_amdgcn_readfirstlane(ld4(r, off));
        if (v >= want) return true;
        if (spin > (1u << 20)) { st4(r, F_ERR, code); return false; }
        __builtin_amdgcn_s_sleep(8); }
}

__device__ __forceinline__ unsigned rtnow() { return (unsigned)__builtin_amdgcn_s_memrealtime(); }
__device__ __forceinline__ void wait_until(unsigned target) { for (int i = 0; i < 4096; ++i) { if ((int)(rtnow() - target) >= 0) break; } }
__device__ __forceinline__ void wait_long(unsigned target) { for (int i = 0; i < 4096; ++i) { if ((int)(rtnow() - target) >= 0) break; __builtin_amdgcn_s_sleep(8); } }
constexpr int RED_G = 320;
constexpr int RED_T = 4 * RED_G;
constexpr int L_RED = 0;
constexpr int L_KX = 65536;
constexpr int KXP = 272;
constexpr int L_VF = L_KX + 2 * 32 * KXP;
constexpr int L_QKV = L_VF + 16384;
constexpr int L_OFF = L_QKV + 6144;
constexpr int L_STMP = L_OFF + 256;
constexpr int L_TOTAL = 163840;
constexpr unsigned U_A = 0, U_S = 32, U_B = 36, U_INIT = 113, U_C = 128, U_END = 512;
#define PLACE 1
#define CAL_N 5
#define STEP_MIN 190u

struct Params {
    const bf16_t *Aq, *Ak, *Av, *shift_k, *ssm_k, *ssm_D, *mem0, *Wq, *Wk, *Wv, *Wo, *gate_w;
    bf16_t* out; unsigned char* ws;
};
__host__ __device__ __forceinline__ unsigned hx_base(const void* ws) { const size_t a = (size_t)ws; return (unsigned)((((a + W_TOTAL + 0x7FFFFFu) >> 23) << 23) - a); }

#define STAMP(i) do { } while (0)
#define BSTAMP(i) do { } while (0)
#define EV(code) do { } while (0)
#define SPINS(i) do { } while (0)
typedef short v4s __attribute__((ext_vector_type(4)));
typedef unsigned v32u __attribute__((ext_vector_type(32)));
typedef unsigned v16u __attribute__((ext_vector_type(16)));
static_assert(L_VF - L_KX == 16384 + 1024, "fragment read offsets");
#define MFMA16(a, b, c) __builtin_amdgcn_mfma_f32_16x16x16bf16_1k(__builtin_bit_cast(v4s, (a)), __builtin_bit_cast(v4s, (b)), (c), 0, 0, 0)
__device__ __forceinline__ unsigned ror8(unsigned x) { return (unsigned)__builtin_amdgcn_update_dpp(0, (int)x, 0x128, 0xF, 0xF, false); }
__device__ __forceinline__ unsigned qx1(unsigned x) { return (unsigned)__builtin_amdgcn_update_dpp(0, (int)x, 0xB1, 0xF, 0xF, false); }
__device__ __forceinline__ unsigned wave_pkmax(unsigned d) {
    d = pkmax(d, (unsigned)__builtin_amdgcn_update_dpp((int)d, (int)d, 0xB1, 0xF, 0xF, false)); d = pkmax(d, (unsigned)__builtin_amdgcn_update_dpp((int)d, (int)d, 0x4E, 0xF, 0xF, false));
    d = pkmax(d, (unsigned)__builtin_amdgcn_update_dpp((int)d, (int)d, 0x124, 0xF, 0xF, false)); d = pkmax(d, (unsigned)__builtin_amdgcn_update_dpp((int)d, (int)d, 0x128, 0xF, 0xF, false));
    { auto q = __builtin_amdgcn_permlane16_swap(d, d, false, false); d = pkmax(q[0], q[1]); }
    { auto q = __builtin_amdgcn_permlane32_swap(d, d, false, false); d = pkmax(q[0], q[1]); }
    return d; }
__device__ __forceinline__ int wave_max(int d) {
    d = max(d, __builtin_amdgcn_update_dpp(d, d, 0xB1, 0xF, 0xF, false)); d = max(d, __builtin_amdgcn_update_dpp(d, d, 0x4E, 0xF, 0xF, false));
    d = max(d, __builtin_amdgcn_update_dpp(d, d, 0x124, 0xF, 0xF, false)); d = max(d, __builtin_amdgcn_update_dpp(d, d, 0x128, 0xF, 0xF, false));
    { auto q = __builtin_amdgcn_permlane16_swap((unsigned)d, (unsigned)d, false, false); d = max((int)q[0], (int)q[1]); }
    { auto q = __builtin_amdgcn_permlane32_swap((unsigned)d, (unsigned)d, false, false); d = max((int)q[0], (int)q[1]); }
    return d; }

__device__ __forceinline__ v4u mk_srd(const void* p, unsigned bytes) { const size_t a = (size_t)p; v4u d;
    d.x = __builtin_amdgcn_readfirstlane((unsigned)a); d.y = __builtin_amdgcn_readfirstlane((unsigned)(a >> 32) & 0xFFFFu); d.z = __builtin_amdgcn_readfirstlane(bytes); d.w = 0x00020000u; return d; }
__device__ __forceinline__ bf16x8 ldsa(unsigned addr) { return *(const __attribute__((address_space(3))) bf16x8*)(size_t)addr; }
constexpr unsigned HX_BYTES = 2097152, OOB = 0xF0000000u;
constexpr unsigned F_RDY0 = W_FLG + 34304;
constexpr unsigned F_GO = W_FLG + 36864;
constexpr int L_KV = 90112, L_MS = 139264, L_IMG = 155648, IMG_PS = 1056, IMG_MS = 2112, LB_R = 8192, LB_STG = 16384, LB_STGP = 8704;
static_assert(F_KVQ == 0 && F_ERR == 33024 && F_CONV == 32768 && F_CAL == 33792 && F_RDY0 == 34304 && F_GO == 36864 && F_PROG == 49152 && W_QX == 12771328 && W_KX == 29548544 && W_OX == 63102976 && W_VF == 46325760 && W_DBG == 113434624 && L_KV == 90112 && L_MS == 139264 && L_IMG == 155648 && IMG_PS == 1056 && IMG_MS == 2112 && LB_R == 8192 && LB_STG == 16384 && LB_STGP == 8704, "constants of the generated code");
#define REC_S_IN "{s[8:11]}"(sh), "{s[12:15]}"(sw), "{s[16:19]}"(s1), "{s[20:23]}"(s2), "{s29}"(D1), "{s30}"(D2), "{s43}"(w), "{s44}"(id), "{s49}"(rb), "{s50}"(PEN), "{s51}"(LT_A), "{s45}"(F_GO + (cp * 4 + w) * 128)
__device__ __forceinline__ void rec_a(const Params& p, unsigned lb, const int wv, const int id, const int cp) {
    const int w = wv, l = otid() & 63, n = l & 15, g = l >> 4, h = id, rb = id;
    const v4u sh = mk_srd(p.ws + hx_base(p.ws), HX_BYTES - 8192), sw = mk_srd(p.ws, W_TOTAL), s1 = mk_srd(w < 2 ? p.Wq : p.Wk, 1024 * 4096 * 2), s2 = s1;
    const int r8 = l >> 3, pc = l & 7, pp = n >> 3, bb = (n >> 2) & 1, f = (g & 1) | (pp << 1);
    const unsigned stg = lb + 131072 + w * 8192;
    const unsigned g_w = r8 * 8192 + h * 256 + (w & 1) * 128 + pc * 16;
    const unsigned wb0 = stg + r8 * 128 + (((pc >> 1) ^ (r8 >> 2)) << 5) + (pc & 1) * 16, wb1 = wb0 ^ 64u;
    const unsigned rb0 = stg + (16 * (g >> 1) + 8 * pp + 4 * (g & 1) + bb) * 128 + (f << 5) + (n & 3) * 8;
    const unsigned a_mem = cp * 32768 + (w >> 1) * 16384 + (w & 1) * 4096 + l * 16;
    const unsigned l_ms = lb + L_MS + l * 16;
    const unsigned l_iw = lb + L_IMG + (w >> 1) * IMG_MS + (g >> 1) * IMG_PS + (64 * (w & 1) + n) * 8 + (g & 1) * 4;
    const unsigned l_iq = lb + L_IMG + (n & 1) * IMG_PS + (8 * g + (n >> 2)) * 8;
    const unsigned a_vm = (16 + (h >> 2)) * 16384 + (h & 3) * 2048 + w * 512 + n * 32 + (g & 1) * 16;
    const unsigned a_o = n < 8 ? (8 + (n >> 2) * 4 + (h >> 3)) * 16384 + (h & 7) * 1024 + w * 256 + (n & 3) * 64 + g * 16 : OOB;
    const unsigned a_ox = (((w & 1) * 32 + h) * 4 + w) * 1024 + l * 16;
    asm volatile(
        "s_lshl_b32 s34, s43, 2\n\t"
        "s_add_u32 s34, s34, 0\n\t"
        "s_and_b32 s34, s34, 15\n\t"
        "s_lshl_b32 s34, s34, 19\n\t"
        "buffer_load_dwordx4 v[224:227], v96, s[16:19], s34 offen nt\n\t"
        "s_add_u32 s34, s34, 0x10000\n\t"
        "buffer_load_dwordx4 v[228:231], v96, s[16:19], s34 offen nt\n\t"
        "s_add_u32 s34, s34, 0x10000\n\t"
        "buffer_load_dwordx4 v[232:235], v96, s[16:19], s34 offen nt\n\t"
        "s_add_u32 s34, s34, 0x10000\n\t"
        "buffer_load_dwordx4 v[236:239], v96, s[16:19], s34 offen nt\n\t"
        "s_add_u32 s34, s34, 0x10000\n\t"
        "buffer_load_dwordx4 v[240:243], v96, s[16:19], s34 offen nt\n\t"
        "s_add_u32 s34, s34, 0x10000\n\t"
        "buffer_load_dwordx4 v[244:247], v96, s[16:19], s34 offen nt\n\t"
        "s_add_u32 s34, s34, 0x10000\n\t"
        "buffer_load_dwordx4 v[248:251], v96, s[16:19], s34 offen nt\n\t"
        "s_add_u32 s34, s34, 0x10000\n\t"
        "buffer_load_dwordx4 v[252:255], v96, s[16:19], s34 offen nt\n\t"
        "s_lshl_b32 s34, s43, 2\n\t"
        "s_add_u32 s34, s34, 1\n\t"
        "s_and_b32 s34, s34, 15\n\t"
        "s_lshl_b32 s34, s34, 19\n\t"
        "buffer_load_dwordx4 v[0:3], v96, s[16:19], s34 offen nt\n\t"
        "s_add_u32 s34, s34, 0x10000\n\t"
        "buffer_load_dwordx4 v[4:7], v96, s[16:19], s34 offen nt\n\t"
        "s_add_u32 s34, s34, 0x10000\n\t"
        "buffer_load_dwordx4 v[8:11], v96, s[16:19], s34 offen nt\n\t"
        "s_add_u32 s34, s34, 0x10000\n\t"
        "buffer_load_dwordx4 v[12:15], v96, s[16:19], s34 offen nt\n\t"
        "s_add_u32 s34, s34, 0x10000\n\t"
        "buffer_load_dwordx4 v[16:19], v96, s[16:19], s34 offen nt\n\t"
        "s_add_u32 s34, s34, 0x10000\n\t"
        "buffer_load_dwordx4 v[20:23], v96, s[16:19], s34 offen nt\n\t"
        "s_add_u32 s34, s34, 0x10000\n\t"
        "buffer_load_dwordx4 v[24:27], v96, s[16:19], s34 offen nt\n\t"
        "s_add_u32 s34, s34, 0x10000\n\t"
        "buffer_load_dwordx4 v[28:31], v96, s[16:19], s34 offen nt\n\t"
        "s_lshl_b32 s34, s43, 2\n\t"
        "s_add_u32 s34, s34, 2\n\t"
        "s_and_b32 s34, s34, 15\n\t"
        "s_lshl_b32 s34, s34, 19\n\t"
        "buffer_load_dwordx4 v[32:35], v96, s[16:19], s34 offen nt\n\t"
        "s_add_u32 s34, s34, 0x10000\n\t"
        "buffer_load_dwordx4 v[36:39], v96, s[16:19], s34 offen nt\n\t"
        "s_add_u32 s34, s34, 0x10000\n\t"
        "buffer_load_dwordx4 v[40:43], v96, s[16:19], s34 offen nt\n\t"
        "s_add_u32 s34, s34, 0x10000\n\t"
        "buffer_load_dwordx4 v[44:47], v96, s[16:19], s34 offen nt\n\t"
        "s_add_u32 s34, s34, 0x10000\n\t"
        "buffer_load_dwordx4 v[48:51], v96, s[16:19], s34 offen nt\n\t"
        "s_add_u32 s34, s34, 0x10000\n\t"
        "buffer_load_dwordx4 v[52:55], v96, s[16:19], s34 offen nt\n\t"
        "s_add_u32 s34, s34, 0x10000\n\t"
        "buffer_load_dwordx4 v[56:59], v96, s[16:19], s34 offen nt\n\t"
        "s_add_u32 s34, s34, 0x10000\n\t"
        "buffer_load_dwordx4 v[60:63], v96, s[16:19], s34 offen nt\n\t"
        "v_cmp_le_i32_e64 s[60:61], 0, v126\n\t"
        "v_cmp_le_i32_e64 s[62:63], 1, v126\n\t"
        "v_cmp_le_i32_e64 s[64:65], 2, v126\n\t"
        "v_cmp_le_i32_e64 s[66:67], 3, v126\n\t"
        "v_cmp_le_i32_e64 s[68:69], 16, v126\n\t"
        "v_cmp_le_i32_e64 s[70:71], 17, v126\n\t"
        "v_cmp_le_i32_e64 s[72:73], 18, v126\n\t"
        "v_cmp_le_i32_e64 s[74:75], 19, v126\n\t"
        "s_mul_i32 s35, s43, 10240\n\t"
        "v_add_u32_e32 v126, s35, v124\n\t"
        "s_waitcnt vmcnt(16)\n\t"
        "ds_write_b128 v97, v[224:227] offset:0\n\t"
        "ds_write_b128 v98, v[228:231] offset:1024\n\t"
        "ds_write_b128 v97, v[232:235] offset:2048\n\t"
        "ds_write_b128 v98, v[236:239] offset:3072\n\t"
        "ds_write_b128 v97, v[240:243] offset:4096\n\t"
        "ds_write_b128 v98, v[244:247] offset:5120\n\t"
        "ds_write_b128 v97, v[248:251] offset:6144\n\t"
        "ds_write_b128 v98, v[252:255] offset:7168\n\t"
        "ds_read_b64_tr_b16 a[0:1], v99 offset:0\n\t"
        "ds_read_b64_tr_b16 a[2:3], v99 offset:256\n\t"
        "ds_read_b64_tr_b16 a[4:5], v99 offset:4096\n\t"
        "ds_read_b64_tr_b16 a[6:7], v99 offset:4352\n\t"
        "s_waitcnt lgkmcnt(8)\n\t"
        "ds_read_b64_tr_b16 a[8:9], v100 offset:0\n\t"
        "ds_read_b64_tr_b16 a[10:11], v100 offset:256\n\t"
        "ds_read_b64_tr_b16 a[12:13], v100 offset:4096\n\t"
        "ds_read_b64_tr_b16 a[14:15], v100 offset:4352\n\t"
        "s_waitcnt lgkmcnt(8)\n\t"
        "ds_read_b64_tr_b16 a[16:17], v101 offset:0\n\t"
        "ds_read_b64_tr_b16 a[18:19], v101 offset:256\n\t"
        "ds_read_b64_tr_b16 a[20:21], v101 offset:4096\n\t"
        "ds_read_b64_tr_b16 a[22:23], v101 offset:4352\n\t"
        "s_waitcnt lgkmcnt(8)\n\t"
        "ds_read_b64_tr_b16 v[104:105], v102 offset:0\n\t"
        "ds_read_b64_tr_b16 v[106:107], v102 offset:256\n\t"
        "ds_read_b64_tr_b16 v[108:109], v102 offset:4096\n\t"
        "ds_read_b64_tr_b16 v[110:111], v102 offset:4352\n\t"
        "s_lshl_b32 s34, s43, 2\n\t"
        "s_add_u32 s34, s34, 3\n\t"
        "s_and_b32 s34, s34, 15\n\t"
        "s_lshl_b32 s34, s34, 19\n\t"
        "buffer_load_dwordx4 v[64:67], v96, s[16:19], s34 offen nt\n\t"
        "s_add_u32 s34, s34, 0x10000\n\t"
        "buffer_load_dwordx4 v[68:71], v96, s[16:19], s34 offen nt\n\t"
        "s_add_u32 s34, s34, 0x10000\n\t"
        "buffer_load_dwordx4 v[72:75], v96, s[16:19], s34 offen nt\n\t"
        "s_add_u32 s34, s34, 0x10000\n\t"
        "buffer_load_dwordx4 v[76:79], v96, s[16:19], s34 offen nt\n\t"
        "s_add_u32 s34, s34, 0x10000\n\t"
        "buffer_load_dwordx4 v[80:83], v96, s[16:19], s34 offen nt\n\t"
        "s_add_u32 s34, s34, 0x10000\n\t"
        "buffer_load_dwordx4 v[84:87], v96, s[16:19], s34 offen nt\n\t"
        "s_add_u32 s34, s34, 0x10000\n\t"
        "buffer_load_dwordx4 v[88:91], v96, s[16:19], s34 offen nt\n\t"
        "s_add_u32 s34, s34, 0x10000\n\t"
        "buffer_load_dwordx4 v[92:95], v96, s[16:19], s34 offen nt\n\t"
        "s_waitcnt lgkmcnt(0)\n\t"
        "ds_write_b128 v117, v[104:107] offset:0\n\t"
        "ds_write_b128 v117, v[108:111] offset:1024\n\t"
        "s_waitcnt vmcnt(16)\n\t"
        "ds_write_b128 v97, v[0:3] offset:0\n\t"
        "ds_write_b128 v98, v[4:7] offset:1024\n\t"
        "ds_write_b128 v97, v[8:11] offset:2048\n\t"
        "ds_write_b128 v98, v[12:15] offset:3072\n\t"
        "ds_write_b128 v97, v[16:19] offset:4096\n\t"
        "ds_write_b128 v98, v[20:23] offset:5120\n\t"
        "ds_write_b128 v97, v[24:27] offset:6144\n\t"
        "ds_write_b128 v98, v[28:31] offset:7168\n\t"
        "ds_read_b64_tr_b16 a[24:25], v99 offset:0\n\t"
        "ds_read_b64_tr_b16 a[26:27], v99 offset:256\n\t"
        "ds_read_b64_tr_b16 a[28:29], v99 offset:4096\n\t"
        "ds_read_b64_tr_b16 a[30:31], v99 offset:4352\n\t"
        "s_waitcnt lgkmcnt(8)\n\t"
        "ds_read_b64_tr_b16 a[32:33], v100 offset:0\n\t"
        "ds_read_b64_tr_b16 a[34:35], v100 offset:256\n\t"
        "ds_read_b64_tr_b16 a[36:37], v100 offset:4096\n\t"
        "ds_read_b64_tr_b16 a[38:39], v100 offset:4352\n\t"
        "s_waitcnt lgkmcnt(8)\n\t"
        "ds_read_b64_tr_b16 a[40:41], v101 offset:0\n\t"
        "ds_read_b64_tr_b16 a[42:43], v101 offset:256\n\t"
        "ds_read_b64_tr_b16 a[44:45], v101 offset:4096\n\t"
        "ds_read_b64_tr_b16 a[46:47], v101 offset:4352\n\t"
        "s_waitcnt lgkmcnt(8)\n\t"
        "ds_read_b64_tr_b16 v[104:105], v102 offset:0\n\t"
        "ds_read_b64_tr_b16 v[106:107], v102 offset:256\n\t"
        "ds_read_b64_tr_b16 v[108:109], v102 offset:4096\n\t"
        "ds_read_b64_tr_b16 v[110:111], v102 offset:4352\n\t"
        "s_lshl_b32 s34, s43, 2\n\t"
        "s_add_u32 s34, s34, 4\n\t"
        "s_and_b32 s34, s34, 15\n\t"
        "s_lshl_b32 s34, s34, 19\n\t"
        "buffer_load_dwordx4 a[96:99], v96, s[16:19], s34 offen nt\n\t"
        "s_add_u32 s34, s34, 0x10000\n\t"
        "buffer_load_dwordx4 a[100:103], v96, s[16:19], s34 offen nt\n\t"
        "s_add_u32 s34, s34, 0x10000\n\t"
        "buffer_load_dwordx4 a[104:107], v96, s[16:19], s34 offen nt\n\t"
        "s_add_u32 s34, s34, 0x10000\n\t"
        "buffer_load_dwordx4 a[108:111], v96, s[16:19], s34 offen nt\n\t"
        "s_add_u32 s34, s34, 0x10000\n\t"
        "buffer_load_dwordx4 a[112:115], v96, s[16:19], s34 offen nt\n\t"
        "s_add_u32 s34, s34, 0x10000\n\t"
        "buffer_load_dwordx4 a[116:119], v96, s[16:19], s34 offen nt\n\t"
        "s_add_u32 s34, s34, 0x10000\n\t"
        "buffer_load_dwordx4 a[120:123], v96, s[16:19], s34 offen nt\n\t"
        "s_add_u32 s34, s34, 0x10000\n\t"
        "buffer_load_dwordx4 a[124:127], v96, s[16:19], s34 offen nt\n\t"
        "s_waitcnt lgkmcnt(0)\n\t"
        "ds_write_b128 v117, v[104:107] offset:2048\n\t"
        "ds_write_b128 v117, v[108:111] offset:3072\n\t"
        "s_waitcnt vmcnt(16)\n\t"
        "ds_write_b128 v97, v[32:35] offset:0\n\t"
        "ds_write_b128 v98, v[36:39] offset:1024\n\t"
        "ds_write_b128 v97, v[40:43] offset:2048\n\t"
        "ds_write_b128 v98, v[44:47] offset:3072\n\t"
        "ds_write_b128 v97, v[48:51] offset:4096\n\t"
        "ds_write_b128 v98, v[52:55] offset:5120\n\t"
        "ds_write_b128 v97, v[56:59] offset:6144\n\t"
        "ds_write_b128 v98, v[60:63] offset:7168\n\t"
        "ds_read_b64_tr_b16 a[48:49], v99 offset:0\n\t"
        "ds_read_b64_tr_b16 a[50:51], v99 offset:256\n\t"
        "ds_read_b64_tr_b16 a[52:53], v99 offset:4096\n\t"
        "ds_read_b64_tr_b16 a[54:55], v99 offset:4352\n\t"
        "s_waitcnt lgkmcnt(8)\n\t"
        "ds_read_b64_tr_b16 a[56:57], v100 offset:0\n\t"
        "ds_read_b64_tr_b16 a[58:59], v100 offset:256\n\t"
        "ds_read_b64_tr_b16 a[60:61], v100 offset:4096\n\t"
        "ds_read_b64_tr_b16 a[62:63], v100 offset:4352\n\t"
        "s_waitcnt lgkmcnt(8)\n\t"
        "ds_read_b64_tr_b16 a[64:65], v101 offset:0\n\t"
        "ds_read_b64_tr_b16 a[66:67], v101 offset:256\n\t"
        "ds_read_b64_tr_b16 a[68:69], v101 offset:4096\n\t"
        "ds_read_b64_tr_b16 a[70:71], v101 offset:4352\n\t"
        "s_waitcnt lgkmcnt(8)\n\t"
        "ds_read_b64_tr_b16 v[104:105], v102 offset:0\n\t"
        "ds_read_b64_tr_b16 v[106:107], v102 offset:256\n\t"
        "ds_read_b64_tr_b16 v[108:109], v102 offset:4096\n\t"
        "ds_read_b64_tr_b16 v[110:111], v102 offset:4352\n\t"
        "s_lshl_b32 s34, s43, 2\n\t"
        "s_add_u32 s34, s34, 5\n\t"
        "s_and_b32 s34, s34, 15\n\t"
        "s_lshl_b32 s34, s34, 19\n\t"
        "buffer_load_dwordx4 a[128:131], v96, s[16:19], s34 offen nt\n\t"
        "s_add_u32 s34, s34, 0x10000\n\t"
        "buffer_load_dwordx4 a[132:135], v96, s[16:19], s34 offen nt\n\t"
        "s_add_u32 s34, s34, 0x10000\n\t"
        "buffer_load_dwordx4 a[136:139], v96, s[16:19], s34 offen nt\n\t"
        "s_add_u32 s34, s34, 0x10000\n\t"
        "buffer_load_dwordx4 a[140:143], v96, s[16:19], s34 offen nt\n\t"
        "s_add_u32 s34, s34, 0x10000\n\t"
        "buffer_load_dwordx4 a[144:147], v96, s[16:19], s34 offen nt\n\t"
        "s_add_u32 s34, s34, 0x10000\n\t"
        "buffer_load_dwordx4 a[148:151], v96, s[16:19], s34 offen nt\n\t"
        "s_add_u32 s34, s34, 0x10000\n\t"
        "buffer_load_dwordx4 a[152:155], v96, s[16:19], s34 offen nt\n\t"
        "s_add_u32 s34, s34, 0x10000\n\t"
        "buffer_load_dwordx4 a[156:159], v96, s[16:19], s34 offen nt\n\t"
        "s_waitcnt lgkmcnt(0)\n\t"
        "ds_write_b128 v117, v[104:107] offset:4096\n\t"
        "ds_write_b128 v117, v[108:111] offset:5120\n\t"
        "s_waitcnt vmcnt(16)\n\t"
        "ds_write_b128 v97, v[64:67] offset:0\n\t"
        "ds_write_b128 v98, v[68:71] offset:1024\n\t"
        "ds_write_b128 v97, v[72:75] offset:2048\n\t"
        "ds_write_b128 v98, v[76:79] offset:3072\n\t"
        "ds_write_b128 v97, v[80:83] offset:4096\n\t"
        "ds_write_b128 v98, v[84:87] offset:5120\n\t"
        "ds_write_b128 v97, v[88:91] offset:6144\n\t"
        "ds_write_b128 v98, v[92:95] offset:7168\n\t"
        "ds_read_b64_tr_b16 a[72:73], v99 offset:0\n\t"
        "ds_read_b64_tr_b16 a[74:75], v99 offset:256\n\t"
        "ds_read_b64_tr_b16 a[76:77], v99 offset:4096\n\t"
        "ds_read_b64_tr_b16 a[78:79], v99 offset:4352\n\t"
        "s_waitcnt lgkmcnt(8)\n\t"
        "ds_read_b64_tr_b16 a[80:81], v100 offset:0\n\t"
        "ds_read_b64_tr_b16 a[82:83], v100 offset:256\n\t"
        "ds_read_b64_tr_b16 a[84:85], v100 offset:4096\n\t"
        "ds_read_b64_tr_b16 a[86:87], v100 offset:4352\n\t"
        "s_waitcnt lgkmcnt(8)\n\t"
        "ds_read_b64_tr_b16 a[88:89], v101 offset:0\n\t"
        "ds_read_b64_tr_b16 a[90:91], v101 offset:256\n\t"
        "ds_read_b64_tr_b16 a[92:93], v101 offset:4096\n\t"
        "ds_read_b64_tr_b16 a[94:95], v101 offset:4352\n\t"
        "s_waitcnt lgkmcnt(8)\n\t"
        "ds_read_b64_tr_b16 v[104:105], v102 offset:0\n\t"
        "ds_read_b64_tr_b16 v[106:107], v102 offset:256\n\t"
        "ds_read_b64_tr_b16 v[108:109], v102 offset:4096\n\t"
        "ds_read_b64_tr_b16 v[110:111], v102 offset:4352\n\t"
        "s_lshl_b32 s34, s43, 2\n\t"
        "s_add_u32 s34, s34, 6\n\t"
        "s_and_b32 s34, s34, 15\n\t"
        "s_lshl_b32 s34, s34, 19\n\t"
        "buffer_load_dwordx4 a[160:163], v96, s[16:19], s34 offen nt\n\t"
        "s_add_u32 s34, s34, 0x10000\n\t"
        "buffer_load_dwordx4 a[164:167], v96, s[16:19], s34 offen nt\n\t"
        "s_add_u32 s34, s34, 0x10000\n\t"
        "buffer_load_dwordx4 a[168:171], v96, s[16:19], s34 offen nt\n\t"
        "s_add_u32 s34, s34, 0x10000\n\t"
        "buffer_load_dwordx4 a[172:175], v96, s[16:19], s34 offen nt\n\t"
        "s_add_u32 s34, s34, 0x10000\n\t"
        "buffer_load_dwordx4 a[176:179], v96, s[16:19], s34 offen nt\n\t"
        "s_add_u32 s34, s34, 0x10000\n\t"
        "buffer_load_dwordx4 a[180:183], v96, s[16:19], s34 offen nt\n\t"
        "s_add_u32 s34, s34, 0x10000\n\t"
        "buffer_load_dwordx4 a[184:187], v96, s[16:19], s34 offen nt\n\t"
        "s_add_u32 s34, s34, 0x10000\n\t"
        "buffer_load_dwordx4 a[188:191], v96, s[16:19], s34 offen nt\n\t"
        "s_waitcnt lgkmcnt(0)\n\t"
        "ds_write_b128 v117, v[104:107] offset:6144\n\t"
        "ds_write_b128 v117, v[108:111] offset:7168\n\t"
        "s_waitcnt vmcnt(16)\n\t"
        "ds_write_b128 v97, a[96:99] offset:0\n\t"
        "ds_write_b128 v98, a[100:103] offset:1024\n\t"
        "ds_write_b128 v97, a[104:107] offset:2048\n\t"
        "ds_write_b128 v98, a[108:111] offset:3072\n\t"
        "ds_write_b128 v97, a[112:115] offset:4096\n\t"
        "ds_write_b128 v98, a[116:119] offset:5120\n\t"
        "ds_write_b128 v97, a[120:123] offset:6144\n\t"
        "ds_write_b128 v98, a[124:127] offset:7168\n\t"
        "ds_read_b64_tr_b16 a[96:97], v99 offset:0\n\t"
        "ds_read_b64_tr_b16 a[98:99], v99 offset:256\n\t"
        "ds_read_b64_tr_b16 a[100:101], v99 offset:4096\n\t"
        "ds_read_b64_tr_b16 a[102:103], v99 offset:4352\n\t"
        "s_waitcnt lgkmcnt(8)\n\t"
        "ds_read_b64_tr_b16 a[104:105], v100 offset:0\n\t"
        "ds_read_b64_tr_b16 a[106:107], v100 offset:256\n\t"
        "ds_read_b64_tr_b16 a[108:109], v100 offset:4096\n\t"
        "ds_read_b64_tr_b16 a[110:111], v100 offset:4352\n\t"
        "s_waitcnt lgkmcnt(8)\n\t"
        "ds_read_b64_tr_b16 a[112:113], v101 offset:0\n\t"
        "ds_read_b64_tr_b16 a[114:115], v101 offset:256\n\t"
        "ds_read_b64_tr_b16 a[116:117], v101 offset:4096\n\t"
        "ds_read_b64_tr_b16 a[118:119], v101 offset:4352\n\t"
        "s_waitcnt lgkmcnt(8)\n\t"
        "ds_read_b64_tr_b16 v[104:105], v102 offset:0\n\t"
        "ds_read_b64_tr_b16 v[106:107], v102 offset:256\n\t"
        "ds_read_b64_tr_b16 v[108:109], v102 offset:4096\n\t"
        "ds_read_b64_tr_b16 v[110:111], v102 offset:4352\n\t"
        "s_lshl_b32 s34, s43, 2\n\t"
        "s_add_u32 s34, s34, 7\n\t"
        "s_and_b32 s34, s34, 15\n\t"
        "s_lshl_b32 s34, s34, 19\n\t"
        "buffer_load_dwordx4 a[192:195], v96, s[16:19], s34 offen nt\n\t"
        "s_add_u32 s34, s34, 0x10000\n\t"
        "buffer_load_dwordx4 a[196:199], v96, s[16:19], s34 offen nt\n\t"
        "s_add_u32 s34, s34, 0x10000\n\t"
        "buffer_load_dwordx4 a[200:203], v96, s[16:19], s34 offen nt\n\t"
        "s_add_u32 s34, s34, 0x10000\n\t"
        "buffer_load_dwordx4 a[204:207], v96, s[16:19], s34 offen nt\n\t"
        "s_add_u32 s34, s34, 0x10000\n\t"
        "buffer_load_dwordx4 a[208:211], v96, s[16:19], s34 offen nt\n\t"
        "s_add_u32 s34, s34, 0x10000\n\t"
        "buffer_load_dwordx4 a[212:215], v96, s[16:19], s34 offen nt\n\t"
        "s_add_u32 s34, s34, 0x10000\n\t"
        "buffer_load_dwordx4 a[216:219], v96, s[16:19], s34 offen nt\n\t"
        "s_add_u32 s34, s34, 0x10000\n\t"
        "buffer_load_dwordx4 a[220:223], v96, s[16:19], s34 offen nt\n\t"
        "s_waitcnt lgkmcnt(0)\n\t"
        "ds_write_b128 v126, v[104:107] offset:0\n\t"
        "ds_write_b128 v126, v[108:111] offset:1024\n\t"
        "s_waitcnt vmcnt(16)\n\t"
        "ds_write_b128 v97, a[128:131] offset:0\n\t"
        "ds_write_b128 v98, a[132:135] offset:1024\n\t"
        "ds_write_b128 v97, a[136:139] offset:2048\n\t"
        "ds_write_b128 v98, a[140:143] offset:3072\n\t"
        "ds_write_b128 v97, a[144:147] offset:4096\n\t"
        "ds_write_b128 v98, a[148:151] offset:5120\n\t"
        "ds_write_b128 v97, a[152:155] offset:6144\n\t"
        "ds_write_b128 v98, a[156:159] offset:7168\n\t"
        "ds_read_b64_tr_b16 a[120:121], v99 offset:0\n\t"
        "ds_read_b64_tr_b16 a[122:123], v99 offset:256\n\t"
        "ds_read_b64_tr_b16 a[124:125], v99 offset:4096\n\t"
        "ds_read_b64_tr_b16 a[126:127], v99 offset:4352\n\t"
        "s_waitcnt lgkmcnt(8)\n\t"
        "ds_read_b64_tr_b16 a[128:129], v100 offset:0\n\t"
        "ds_read_b64_tr_b16 a[130:131], v100 offset:256\n\t"
        "ds_read_b64_tr_b16 a[132:133], v100 offset:4096\n\t"
        "ds_read_b64_tr_b16 a[134:135], v100 offset:4352\n\t"
        "s_waitcnt lgkmcnt(8)\n\t"
        "ds_read_b64_tr_b16 a[136:137], v101 offset:0\n\t"
        "ds_read_b64_tr_b16 a[138:139], v101 offset:256\n\t"
        "ds_read_b64_tr_b16 a[140:141], v101 offset:4096\n\t"
        "ds_read_b64_tr_b16 a[142:143], v101 offset:4352\n\t"
        "s_waitcnt lgkmcnt(8)\n\t"
        "ds_read_b64_tr_b16 v[104:105], v102 offset:0\n\t"
        "ds_read_b64_tr_b16 v[106:107], v102 offset:256\n\t"
        "ds_read_b64_tr_b16 v[108:109], v102 offset:4096\n\t"
        "ds_read_b64_tr_b16 v[110:111], v102 offset:4352\n\t"
        "s_lshl_b32 s34, s43, 2\n\t"
        "s_add_u32 s34, s34, 8\n\t"
        "s_and_b32 s34, s34, 15\n\t"
        "s_lshl_b32 s34, s34, 19\n\t"
        "buffer_load_dwordx4 a[224:227], v96, s[16:19], s34 offen nt\n\t"
        "s_add_u32 s34, s34, 0x10000\n\t"
        "buffer_load_dwordx4 a[228:231], v96, s[16:19], s34 offen nt\n\t"
        "s_add_u32 s34, s34, 0x10000\n\t"
        "buffer_load_dwordx4 a[232:235], v96, s[16:19], s34 offen nt\n\t"
        "s_add_u32 s34, s34, 0x10000\n\t"
        "buffer_load_dwordx4 a[236:239], v96, s[16:19], s34 offen nt\n\t"
        "s_add_u32 s34, s34, 0x10000\n\t"
        "buffer_load_dwordx4 a[240:243], v96, s[16:19], s34 offen nt\n\t"
        "s_add_u32 s34, s34, 0x10000\n\t"
        "buffer_load_dwordx4 a[244:247], v96, s[16:19], s34 offen nt\n\t"
        "s_add_u32 s34, s34, 0x10000\n\t"
        "buffer_load_dwordx4 a[248:251], v96, s[16:19], s34 offen nt\n\t"
        "s_add_u32 s34, s34, 0x10000\n\t"
        "buffer_load_dwordx4 a[252:255], v96, s[16:19], s34 offen nt\n\t"
        "s_waitcnt lgkmcnt(0)\n\t"
        "ds_write_b128 v126, v[104:107] offset:2048\n\t"
        "ds_write_b128 v126, v[108:111] offset:3072\n\t"
        "s_waitcnt vmcnt(16)\n\t"
        "ds_write_b128 v97, a[160:163] offset:0\n\t"
        "ds_write_b128 v98, a[164:167] offset:1024\n\t"
        "ds_write_b128 v97, a[168:171] offset:2048\n\t"
        "ds_write_b128 v98, a[172:175] offset:3072\n\t"
        "ds_write_b128 v97, a[176:179] offset:4096\n\t"
        "ds_write_b128 v98, a[180:183] offset:5120\n\t"
        "ds_write_b128 v97, a[184:187] offset:6144\n\t"
        "ds_write_b128 v98, a[188:191] offset:7168\n\t"
        "ds_read_b64_tr_b16 a[144:145], v99 offset:0\n\t"
        "ds_read_b64_tr_b16 a[146:147], v99 offset:256\n\t"
        "ds_read_b64_tr_b16 a[148:149], v99 offset:4096\n\t"
        "ds_read_b64_tr_b16 a[150:151], v99 offset:4352\n\t"
        "s_waitcnt lgkmcnt(8)\n\t"
        "ds_read_b64_tr_b16 a[152:153], v100 offset:0\n\t"
        "ds_read_b64_tr_b16 a[154:155], v100 offset:256\n\t"
        "ds_read_b64_tr_b16 a[156:157], v100 offset:4096\n\t"
        "ds_read_b64_tr_b16 a[158:159], v100 offset:4352\n\t"
        "s_waitcnt lgkmcnt(8)\n\t"
        "ds_read_b64_tr_b16 a[160:161], v101 offset:0\n\t"
        "ds_read_b64_tr_b16 a[162:163], v101 offset:256\n\t"
        "ds_read_b64_tr_b16 a[164:165], v101 offset:4096\n\t"
        "ds_read_b64_tr_b16 a[166:167], v101 offset:4352\n\t"
        "s_waitcnt lgkmcnt(8)\n\t"
        "ds_read_b64_tr_b16 v[104:105], v102 offset:0\n\t"
        "ds_read_b64_tr_b16 v[106:107], v102 offset:256\n\t"
        "ds_read_b64_tr_b16 v[108:109], v102 offset:4096\n\t"
        "ds_read_b64_tr_b16 v[110:111], v102 offset:4352\n\t"
        "s_lshl_b32 s34, s43, 2\n\t"
        "s_add_u32 s34, s34, 9\n\t"
        "s_and_b32 s34, s34, 15\n\t"
        "s_lshl_b32 s34, s34, 19\n\t"
        "buffer_load_dwordx4 v[128:131], v96, s[16:19], s34 offen nt\n\t"
        "s_add_u32 s34, s34, 0x10000\n\t"
        "buffer_load_dwordx4 v[132:135], v96, s[16:19], s34 offen nt\n\t"
        "s_add_u32 s34, s34, 0x10000\n\t"
        "buffer_load_dwordx4 v[136:139], v96, s[16:19], s34 offen nt\n\t"
        "s_add_u32 s34, s34, 0x10000\n\t"
        "buffer_load_dwordx4 v[140:143], v96, s[16:19], s34 offen nt\n\t"
        "s_add_u32 s34, s34, 0x10000\n\t"
        "buffer_load_dwordx4 v[144:147], v96, s[16:19], s34 offen nt\n\t"
        "s_add_u32 s34, s34, 0x10000\n\t"
        "buffer_load_dwordx4 v[148:151], v96, s[16:19], s34 offen nt\n\t"
        "s_add_u32 s34, s34, 0x10000\n\t"
        "buffer_load_dwordx4 v[152:155], v96, s[16:19], s34 offen nt\n\t"
        "s_add_u32 s34, s34, 0x10000\n\t"
        "buffer_load_dwordx4 v[156:159], v96, s[16:19], s34 offen nt\n\t"
        "s_waitcnt lgkmcnt(0)\n\t"
        "ds_write_b128 v126, v[104:107] offset:4096\n\t"
        "ds_write_b128 v126, v[108:111] offset:5120\n\t"
        "s_waitcnt vmcnt(16)\n\t"
        "ds_write_b128 v97, a[192:195] offset:0\n\t"
        "ds_write_b128 v98, a[196:199] offset:1024\n\t"
        "ds_write_b128 v97, a[200:203] offset:2048\n\t"
        "ds_write_b128 v98, a[204:207] offset:3072\n\t"
        "ds_write_b128 v97, a[208:211] offset:4096\n\t"
        "ds_write_b128 v98, a[212:215] offset:5120\n\t"
        "ds_write_b128 v97, a[216:219] offset:6144\n\t"
        "ds_write_b128 v98, a[220:223] offset:7168\n\t"
        "ds_read_b64_tr_b16 a[168:169], v99 offset:0\n\t"
        "ds_read_b64_tr_b16 a[170:171], v99 offset:256\n\t"
        "ds_read_b64_tr_b16 a[172:173], v99 offset:4096\n\t"
        "ds_read_b64_tr_b16 a[174:175], v99 offset:4352\n\t"
        "s_waitcnt lgkmcnt(8)\n\t"
        "ds_read_b64_tr_b16 a[176:177], v100 offset:0\n\t"
        "ds_read_b64_tr_b16 a[178:179], v100 offset:256\n\t"
        "ds_read_b64_tr_b16 a[180:181], v100 offset:4096\n\t"
        "ds_read_b64_tr_b16 a[182:183], v100 offset:4352\n\t"
        "s_waitcnt lgkmcnt(8)\n\t"
        "ds_read_b64_tr_b16 a[184:185], v101 offset:0\n\t"
        "ds_read_b64_tr_b16 a[186:187], v101 offset:256\n\t"
        "ds_read_b64_tr_b16 a[188:189], v101 offset:4096\n\t"
        "ds_read_b64_tr_b16 a[190:191], v101 offset:4352\n\t"
        "s_waitcnt lgkmcnt(8)\n\t"
        "ds_read_b64_tr_b16 v[104:105], v102 offset:0\n\t"
        "ds_read_b64_tr_b16 v[106:107], v102 offset:256\n\t"
        "ds_read_b64_tr_b16 v[108:109], v102 offset:4096\n\t"
        "ds_read_b64_tr_b16 v[110:111], v102 offset:4352\n\t"
        "s_lshl_b32 s34, s43, 2\n\t"
        "s_add_u32 s34, s34, 10\n\t"
        "s_and_b32 s34, s34, 15\n\t"
        "s_lshl_b32 s34, s34, 19\n\t"
        "buffer_load_dwordx4 v[160:163], v96, s[16:19], s34 offen nt\n\t"
        "s_add_u32 s34, s34, 0x10000\n\t"
        "buffer_load_dwordx4 v[164:167], v96, s[16:19], s34 offen nt\n\t"
        "s_add_u32 s34, s34, 0x10000\n\t"
        "buffer_load_dwordx4 v[168:171], v96, s[16:19], s34 offen nt\n\t"
        "s_add_u32 s34, s34, 0x10000\n\t"
        "buffer_load_dwordx4 v[172:175], v96, s[16:19], s34 offen nt\n\t"
        "s_add_u32 s34, s34, 0x10000\n\t"
        "buffer_load_dwordx4 v[176:179], v96, s[16:19], s34 offen nt\n\t"
        "s_add_u32 s34, s34, 0x10000\n\t"
        "buffer_load_dwordx4 v[180:183], v96, s[16:19], s34 offen nt\n\t"
        "s_add_u32 s34, s34, 0x10000\n\t"
        "buffer_load_dwordx4 v[184:187], v96, s[16:19], s34 offen nt\n\t"
        "s_add_u32 s34, s34, 0x10000\n\t"
        "buffer_load_dwordx4 v[188:191], v96, s[16:19], s34 offen nt\n\t"
        "s_waitcnt lgkmcnt(0)\n\t"
        "ds_write_b128 v126, v[104:107] offset:6144\n\t"
        "ds_write_b128 v126, v[108:111] offset:7168\n\t"
        "s_waitcnt vmcnt(16)\n\t"
        "ds_write_b128 v97, a[224:227] offset:0\n\t"
        "ds_write_b128 v98, a[228:231] offset:1024\n\t"
        "ds_write_b128 v97, a[232:235] offset:2048\n\t"
        "ds_write_b128 v98, a[236:239] offset:3072\n\t"
        "ds_write_b128 v97, a[240:243] offset:4096\n\t"
        "ds_write_b128 v98, a[244:247] offset:5120\n\t"
        "ds_write_b128 v97, a[248:251] offset:6144\n\t"
        "ds_write_b128 v98, a[252:255] offset:7168\n\t"
        "ds_read_b64_tr_b16 a[192:193], v99 offset:0\n\t"
        "ds_read_b64_tr_b16 a[194:195], v99 offset:256\n\t"
        "ds_read_b64_tr_b16 a[196:197], v99 offset:4096\n\t"
        "ds_read_b64_tr_b16 a[198:199], v99 offset:4352\n\t"
        "s_waitcnt lgkmcnt(8)\n\t"
        "ds_read_b64_tr_b16 a[200:201], v100 offset:0\n\t"
        "ds_read_b64_tr_b16 a[202:203], v100 offset:256\n\t"
        "ds_read_b64_tr_b16 a[204:205], v100 offset:4096\n\t"
        "ds_read_b64_tr_b16 a[206:207], v100 offset:4352\n\t"
        "s_waitcnt lgkmcnt(8)\n\t"
        "ds_read_b64_tr_b16 a[208:209], v101 offset:0\n\t"
        "ds_read_b64_tr_b16 a[210:211], v101 offset:256\n\t"
        "ds_read_b64_tr_b16 a[212:213], v101 offset:4096\n\t"
        "ds_read_b64_tr_b16 a[214:215], v101 offset:4352\n\t"
        "s_waitcnt lgkmcnt(8)\n\t"
        "ds_read_b64_tr_b16 v[104:105], v102 offset:0\n\t"
        "ds_read_b64_tr_b16 v[106:107], v102 offset:256\n\t"
        "ds_read_b64_tr_b16 v[108:109], v102 offset:4096\n\t"
        "ds_read_b64_tr_b16 v[110:111], v102 offset:4352\n\t"
        "s_lshl_b32 s34, s43, 2\n\t"
        "s_add_u32 s34, s34, 11\n\t"
        "s_and_b32 s34, s34, 15\n\t"
        "s_lshl_b32 s34, s34, 19\n\t"
        "buffer_load_dwordx4 v[192:195], v96, s[16:19], s34 offen nt\n\t"
        "s_add_u32 s34, s34, 0x10000\n\t"
        "buffer_load_dwordx4 v[196:199], v96, s[16:19], s34 offen nt\n\t"
        "s_add_u32 s34, s34, 0x10000\n\t"
        "buffer_load_dwordx4 v[200:203], v96, s[16:19], s34 offen nt\n\t"
        "s_add_u32 s34, s34, 0x10000\n\t"
        "buffer_load_dwordx4 v[204:207], v96, s[16:19], s34 offen nt\n\t"
        "s_add_u32 s34, s34, 0x10000\n\t"
        "buffer_load_dwordx4 v[208:211], v96, s[16:19], s34 offen nt\n\t"
        "s_add_u32 s34, s34, 0x10000\n\t"
        "buffer_load_dwordx4 v[212:215], v96, s[16:19], s34 offen nt\n\t"
        "s_add_u32 s34, s34, 0x10000\n\t"
        "buffer_load_dwordx4 v[216:219], v96, s[16:19], s34 offen nt\n\t"
        "s_add_u32 s34, s34, 0x10000\n\t"
        "buffer_load_dwordx4 v[220:223], v96, s[16:19], s34 offen nt\n\t"
        "s_waitcnt lgkmcnt(0)\n\t"
        "ds_write_b128 v117, v[104:107] offset:8192\n\t"
        "ds_write_b128 v117, v[108:111] offset:9216\n\t"
        "s_waitcnt vmcnt(16)\n\t"
        "ds_write_b128 v97, v[128:131] offset:0\n\t"
        "ds_write_b128 v98, v[132:135] offset:1024\n\t"
        "ds_write_b128 v97, v[136:139] offset:2048\n\t"
        "ds_write_b128 v98, v[140:143] offset:3072\n\t"
        "ds_write_b128 v97, v[144:147] offset:4096\n\t"
        "ds_write_b128 v98, v[148:151] offset:5120\n\t"
        "ds_write_b128 v97, v[152:155] offset:6144\n\t"
        "ds_write_b128 v98, v[156:159] offset:7168\n\t"
        "ds_read_b64_tr_b16 a[216:217], v99 offset:0\n\t"
        "ds_read_b64_tr_b16 a[218:219], v99 offset:256\n\t"
        "ds_read_b64_tr_b16 a[220:221], v99 offset:4096\n\t"
        "ds_read_b64_tr_b16 a[222:223], v99 offset:4352\n\t"
        "s_waitcnt lgkmcnt(8)\n\t"
        "ds_read_b64_tr_b16 a[224:225], v100 offset:0\n\t"
        "ds_read_b64_tr_b16 a[226:227], v100 offset:256\n\t"
        "ds_read_b64_tr_b16 a[228:229], v100 offset:4096\n\t"
        "ds_read_b64_tr_b16 a[230:231], v100 offset:4352\n\t"
        "s_waitcnt lgkmcnt(8)\n\t"
        "ds_read_b64_tr_b16 a[232:233], v101 offset:0\n\t"
        "ds_read_b64_tr_b16 a[234:235], v101 offset:256\n\t"
        "ds_read_b64_tr_b16 a[236:237], v101 offset:4096\n\t"
        "ds_read_b64_tr_b16 a[238:239], v101 offset:4352\n\t"
        "s_waitcnt lgkmcnt(8)\n\t"
        "ds_read_b64_tr_b16 v[104:105], v102 offset:0\n\t"
        "ds_read_b64_tr_b16 v[106:107], v102 offset:256\n\t"
        "ds_read_b64_tr_b16 v[108:109], v102 offset:4096\n\t"
        "ds_read_b64_tr_b16 v[110:111], v102 offset:4352\n\t"
        "s_lshl_b32 s34, s43, 2\n\t"
        "s_add_u32 s34, s34, 12\n\t"
        "s_and_b32 s34, s34, 15\n\t"
        "s_lshl_b32 s34, s34, 19\n\t"
        "buffer_load_dwordx4 v[224:227], v96, s[16:19], s34 offen nt\n\t"
        "s_add_u32 s34, s34, 0x10000\n\t"
        "buffer_load_dwordx4 v[228:231], v96, s[16:19], s34 offen nt\n\t"
        "s_add_u32 s34, s34, 0x10000\n\t"
        "buffer_load_dwordx4 v[232:235], v96, s[16:19], s34 offen nt\n\t"
        "s_add_u32 s34, s34, 0x10000\n\t"
        "buffer_load_dwordx4 v[236:239], v96, s[16:19], s34 offen nt\n\t"
        "s_add_u32 s34, s34, 0x10000\n\t"
        "buffer_load_dwordx4 v[240:243], v96, s[16:19], s34 offen nt\n\t"
        "s_add_u32 s34, s34, 0x10000\n\t"
        "buffer_load_dwordx4 v[244:247], v96, s[16:19], s34 offen nt\n\t"
        "s_add_u32 s34, s34, 0x10000\n\t"
        "buffer_load_dwordx4 v[248:251], v96, s[16:19], s34 offen nt\n\t"
        "s_add_u32 s34, s34, 0x10000\n\t"
        "buffer_load_dwordx4 v[252:255], v96, s[16:19], s34 offen nt\n\t"
        "s_waitcnt lgkmcnt(0)\n\t"
        "ds_write_b128 v117, v[104:107] offset:10240\n\t"
        "ds_write_b128 v117, v[108:111] offset:11264\n\t"
        "s_waitcnt vmcnt(16)\n\t"
        "ds_write_b128 v97, v[160:163] offset:0\n\t"
        "ds_write_b128 v98, v[164:167] offset:1024\n\t"
        "ds_write_b128 v97, v[168:171] offset:2048\n\t"
        "ds_write_b128 v98, v[172:175] offset:3072\n\t"
        "ds_write_b128 v97, v[176:179] offset:4096\n\t"
        "ds_write_b128 v98, v[180:183] offset:5120\n\t"
        "ds_write_b128 v97, v[184:187] offset:6144\n\t"
        "ds_write_b128 v98, v[188:191] offset:7168\n\t"
        "ds_read_b64_tr_b16 a[240:241], v99 offset:0\n\t"
        "ds_read_b64_tr_b16 a[242:243], v99 offset:256\n\t"
        "ds_read_b64_tr_b16 a[244:245], v99 offset:4096\n\t"
        "ds_read_b64_tr_b16 a[246:247], v99 offset:4352\n\t"
        "s_waitcnt lgkmcnt(8)\n\t"
        "ds_read_b64_tr_b16 a[248:249], v100 offset:0\n\t"
        "ds_read_b64_tr_b16 a[250:251], v100 offset:256\n\t"
        "ds_read_b64_tr_b16 a[252:253], v100 offset:4096\n\t"
        "ds_read_b64_tr_b16 a[254:255], v100 offset:4352\n\t"
        "s_waitcnt lgkmcnt(8)\n\t"
        "ds_read_b64_tr_b16 v[128:129], v101 offset:0\n\t"
        "ds_read_b64_tr_b16 v[130:131], v101 offset:256\n\t"
        "ds_read_b64_tr_b16 v[132:133], v101 offset:4096\n\t"
        "ds_read_b64_tr_b16 v[134:135], v101 offset:4352\n\t"
        "s_waitcnt lgkmcnt(8)\n\t"
        "ds_read_b64_tr_b16 v[104:105], v102 offset:0\n\t"
        "ds_read_b64_tr_b16 v[106:107], v102 offset:256\n\t"
        "ds_read_b64_tr_b16 v[108:109], v102 offset:4096\n\t"
        "ds_read_b64_tr_b16 v[110:111], v102 offset:4352\n\t"
        "s_lshl_b32 s34, s43, 2\n\t"
        "s_add_u32 s34, s34, 13\n\t"
        "s_and_b32 s34, s34, 15\n\t"
        "s_lshl_b32 s34, s34, 19\n\t"
        "buffer_load_dwordx4 v[0:3], v96, s[16:19], s34 offen nt\n\t"
        "s_add_u32 s34, s34, 0x10000\n\t"
        "buffer_load_dwordx4 v[4:7], v96, s[16:19], s34 offen nt\n\t"
        "s_add_u32 s34, s34, 0x10000\n\t"
        "buffer_load_dwordx4 v[8:11], v96, s[16:19], s34 offen nt\n\t"
        "s_add_u32 s34, s34, 0x10000\n\t"
        "buffer_load_dwordx4 v[12:15], v96, s[16:19], s34 offen nt\n\t"
        "s_add_u32 s34, s34, 0x10000\n\t"
        "buffer_load_dwordx4 v[16:19], v96, s[16:19], s34 offen nt\n\t"
        "s_add_u32 s34, s34, 0x10000\n\t"
        "buffer_load_dwordx4 v[20:23], v96, s[16:19], s34 offen nt\n\t"
        "s_add_u32 s34, s34, 0x10000\n\t"
        "buffer_load_dwordx4 v[24:27], v96, s[16:19], s34 offen nt\n\t"
        "s_add_u32 s34, s34, 0x10000\n\t"
        "buffer_load_dwordx4 v[28:31], v96, s[16:19], s34 offen nt\n\t"
        "s_waitcnt lgkmcnt(0)\n\t"
        "ds_write_b128 v117, v[104:107] offset:12288\n\t"
        "ds_write_b128 v117, v[108:111] offset:13312\n\t"
        "s_waitcnt vmcnt(16)\n\t"
        "ds_write_b128 v97, v[192:195] offset:0\n\t"
        "ds_write_b128 v98, v[196:199] offset:1024\n\t"
        "ds_write_b128 v97, v[200:203] offset:2048\n\t"
        "ds_write_b128 v98, v[204:207] offset:3072\n\t"
        "ds_write_b128 v97, v[208:211] offset:4096\n\t"
        "ds_write_b128 v98, v[212:215] offset:5120\n\t"
        "ds_write_b128 v97, v[216:219] offset:6144\n\t"
        "ds_write_b128 v98, v[220:223] offset:7168\n\t"
        "ds_read_b64_tr_b16 v[136:137], v99 offset:0\n\t"
        "ds_read_b64_tr_b16 v[138:139], v99 offset:256\n\t"
        "ds_read_b64_tr_b16 v[140:141], v99 offset:4096\n\t"
        "ds_read_b64_tr_b16 v[142:143], v99 offset:4352\n\t"
        "s_waitcnt lgkmcnt(8)\n\t"
        "ds_read_b64_tr_b16 v[144:145], v100 offset:0\n\t"
        "ds_read_b64_tr_b16 v[146:147], v100 offset:256\n\t"
        "ds_read_b64_tr_b16 v[148:149], v100 offset:4096\n\t"
        "ds_read_b64_tr_b16 v[150:151], v100 offset:4352\n\t"
        "s_waitcnt lgkmcnt(8)\n\t"
        "ds_read_b64_tr_b16 v[152:153], v101 offset:0\n\t"
        "ds_read_b64_tr_b16 v[154:155], v101 offset:256\n\t"
        "ds_read_b64_tr_b16 v[156:157], v101 offset:4096\n\t"
        "ds_read_b64_tr_b16 v[158:159], v101 offset:4352\n\t"
        "s_waitcnt lgkmcnt(8)\n\t"
        "ds_read_b64_tr_b16 v[104:105], v102 offset:0\n\t"
        "ds_read_b64_tr_b16 v[106:107], v102 offset:256\n\t"
        "ds_read_b64_tr_b16 v[108:109], v102 offset:4096\n\t"
        "ds_read_b64_tr_b16 v[110:111], v102 offset:4352\n\t"
        "s_lshl_b32 s34, s43, 2\n\t"
        "s_add_u32 s34, s34, 14\n\t"
        "s_and_b32 s34, s34, 15\n\t"
        "s_lshl_b32 s34, s34, 19\n\t"
        "buffer_load_dwordx4 v[32:35], v96, s[16:19], s34 offen nt\n\t"
        "s_add_u32 s34, s34, 0x10000\n\t"
        "buffer_load_dwordx4 v[36:39], v96, s[16:19], s34 offen nt\n\t"
        "s_add_u32 s34, s34, 0x10000\n\t"
        "buffer_load_dwordx4 v[40:43], v96, s[16:19], s34 offen nt\n\t"
        "s_add_u32 s34, s34, 0x10000\n\t"
        "buffer_load_dwordx4 v[44:47], v96, s[16:19], s34 offen nt\n\t"
        "s_add_u32 s34, s34, 0x10000\n\t"
        "buffer_load_dwordx4 v[48:51], v96, s[16:19], s34 offen nt\n\t"
        "s_add_u32 s34, s34, 0x10000\n\t"
        "buffer_load_dwordx4 v[52:55], v96, s[16:19], s34 offen nt\n\t"
        "s_add_u32 s34, s34, 0x10000\n\t"
        "buffer_load_dwordx4 v[56:59], v96, s[16:19], s34 offen nt\n\t"
        "s_add_u32 s34, s34, 0x10000\n\t"
        "buffer_load_dwordx4 v[60:63], v96, s[16:19], s34 offen nt\n\t"
        "s_waitcnt lgkmcnt(0)\n\t"
        "ds_write_b128 v117, v[104:107] offset:14336\n\t"
        "ds_write_b128 v117, v[108:111] offset:15360\n\t"
        "s_waitcnt vmcnt(16)\n\t"
        "ds_write_b128 v97, v[224:227] offset:0\n\t"
        "ds_write_b128 v98, v[228:231] offset:1024\n\t"
        "ds_write_b128 v97, v[232:235] offset:2048\n\t"
        "ds_write_b128 v98, v[236:239] offset:3072\n\t"
        "ds_write_b128 v97, v[240:243] offset:4096\n\t"
        "ds_write_b128 v98, v[244:247] offset:5120\n\t"
        "ds_write_b128 v97, v[248:251] offset:6144\n\t"
        "ds_write_b128 v98, v[252:255] offset:7168\n\t"
        "ds_read_b64_tr_b16 v[160:161], v99 offset:0\n\t"
        "ds_read_b64_tr_b16 v[162:163], v99 offset:256\n\t"
        "ds_read_b64_tr_b16 v[164:165], v99 offset:4096\n\t"
        "ds_read_b64_tr_b16 v[166:167], v99 offset:4352\n\t"
        "s_waitcnt lgkmcnt(8)\n\t"
        "ds_read_b64_tr_b16 v[168:169], v100 offset:0\n\t"
        "ds_read_b64_tr_b16 v[170:171], v100 offset:256\n\t"
        "ds_read_b64_tr_b16 v[172:173], v100 offset:4096\n\t"
        "ds_read_b64_tr_b16 v[174:175], v100 offset:4352\n\t"
        "s_waitcnt lgkmcnt(8)\n\t"
        "ds_read_b64_tr_b16 v[176:177], v101 offset:0\n\t"
        "ds_read_b64_tr_b16 v[178:179], v101 offset:256\n\t"
        "ds_read_b64_tr_b16 v[180:181], v101 offset:4096\n\t"
        "ds_read_b64_tr_b16 v[182:183], v101 offset:4352\n\t"
        "s_waitcnt lgkmcnt(8)\n\t"
        "ds_read_b64_tr_b16 v[104:105], v102 offset:0\n\t"
        "ds_read_b64_tr_b16 v[106:107], v102 offset:256\n\t"
        "ds_read_b64_tr_b16 v[108:109], v102 offset:4096\n\t"
        "ds_read_b64_tr_b16 v[110:111], v102 offset:4352\n\t"
        "s_lshl_b32 s34, s43, 2\n\t"
        "s_add_u32 s34, s34, 15\n\t"
        "s_and_b32 s34, s34, 15\n\t"
        "s_lshl_b32 s34, s34, 19\n\t"
        "buffer_load_dwordx4 v[64:67], v96, s[16:19], s34 offen nt\n\t"
        "s_add_u32 s34, s34, 0x10000\n\t"
        "buffer_load_dwordx4 v[68:71], v96, s[16:19], s34 offen nt\n\t"
        "s_add_u32 s34, s34, 0x10000\n\t"
        "buffer_load_dwordx4 v[72:75], v96, s[16:19], s34 offen nt\n\t"
        "s_add_u32 s34, s34, 0x10000\n\t"
        "buffer_load_dwordx4 v[76:79], v96, s[16:19], s34 offen nt\n\t"
        "s_add_u32 s34, s34, 0x10000\n\t"
        "buffer_load_dwordx4 v[80:83], v96, s[16:19], s34 offen nt\n\t"
        "s_add_u32 s34, s34, 0x10000\n\t"
        "buffer_load_dwordx4 v[84:87], v96, s[16:19], s34 offen nt\n\t"
        "s_add_u32 s34, s34, 0x10000\n\t"
        "buffer_load_dwordx4 v[88:91], v96, s[16:19], s34 offen nt\n\t"
        "s_add_u32 s34, s34, 0x10000\n\t"
        "buffer_load_dwordx4 v[92:95], v96, s[16:19], s34 offen nt\n\t"
        "s_waitcnt lgkmcnt(0)\n\t"
        "ds_write_b128 v117, v[104:107] offset:16384\n\t"
        "ds_write_b128 v117, v[108:111] offset:17408\n\t"
        "s_waitcnt vmcnt(16)\n\t"
        "ds_write_b128 v97, v[0:3] offset:0\n\t"
        "ds_write_b128 v98, v[4:7] offset:1024\n\t"
        "ds_write_b128 v97, v[8:11] offset:2048\n\t"
        "ds_write_b128 v98, v[12:15] offset:3072\n\t"
        "ds_write_b128 v97, v[16:19] offset:4096\n\t"
        "ds_write_b128 v98, v[20:23] offset:5120\n\t"
        "ds_write_b128 v97, v[24:27] offset:6144\n\t"
        "ds_write_b128 v98, v[28:31] offset:7168\n\t"
        "ds_read_b64_tr_b16 v[184:185], v99 offset:0\n\t"
        "ds_read_b64_tr_b16 v[186:187], v99 offset:256\n\t"
        "ds_read_b64_tr_b16 v[188:189], v99 offset:4096\n\t"
        "ds_read_b64_tr_b16 v[190:191], v99 offset:4352\n\t"
        "s_waitcnt lgkmcnt(8)\n\t"
        "ds_read_b64_tr_b16 v[192:193], v100 offset:0\n\t"
        "ds_read_b64_tr_b16 v[194:195], v100 offset:256\n\t"
        "ds_read_b64_tr_b16 v[196:197], v100 offset:4096\n\t"
        "ds_read_b64_tr_b16 v[198:199], v100 offset:4352\n\t"
        "s_waitcnt lgkmcnt(8)\n\t"
        "ds_read_b64_tr_b16 v[200:201], v101 offset:0\n\t"
        "ds_read_b64_tr_b16 v[202:203], v101 offset:256\n\t"
        "ds_read_b64_tr_b16 v[204:205], v101 offset:4096\n\t"
        "ds_read_b64_tr_b16 v[206:207], v101 offset:4352\n\t"
        "s_waitcnt lgkmcnt(8)\n\t"
        "ds_read_b64_tr_b16 v[104:105], v102 offset:0\n\t"
        "ds_read_b64_tr_b16 v[106:107], v102 offset:256\n\t"
        "ds_read_b64_tr_b16 v[108:109], v102 offset:4096\n\t"
        "ds_read_b64_tr_b16 v[110:111], v102 offset:4352\n\t"
        "s_waitcnt lgkmcnt(0)\n\t"
        "ds_write_b128 v117, v[104:107] offset:18432\n\t"
        "ds_write_b128 v117, v[108:111] offset:19456\n\t"
        "s_waitcnt vmcnt(8)\n\t"
        "ds_write_b128 v97, v[32:35] offset:0\n\t"
        "ds_write_b128 v98, v[36:39] offset:1024\n\t"
        "ds_write_b128 v97, v[40:43] offset:2048\n\t"
        "ds_write_b128 v98, v[44:47] offset:3072\n\t"
        "ds_write_b128 v97, v[48:51] offset:4096\n\t"
        "ds_write_b128 v98, v[52:55] offset:5120\n\t"
        "ds_write_b128 v97, v[56:59] offset:6144\n\t"
        "ds_write_b128 v98, v[60:63] offset:7168\n\t"
        "ds_read_b64_tr_b16 v[208:209], v99 offset:0\n\t"
        "ds_read_b64_tr_b16 v[210:211], v99 offset:256\n\t"
        "ds_read_b64_tr_b16 v[212:213], v99 offset:4096\n\t"
        "ds_read_b64_tr_b16 v[214:215], v99 offset:4352\n\t"
        "s_waitcnt lgkmcnt(8)\n\t"
        "ds_read_b64_tr_b16 v[216:217], v100 offset:0\n\t"
        "ds_read_b64_tr_b16 v[218:219], v100 offset:256\n\t"
        "ds_read_b64_tr_b16 v[220:221], v100 offset:4096\n\t"
        "ds_read_b64_tr_b16 v[222:223], v100 offset:4352\n\t"
        "s_waitcnt lgkmcnt(8)\n\t"
        "ds_read_b64_tr_b16 v[224:225], v101 offset:0\n\t"
        "ds_read_b64_tr_b16 v[226:227], v101 offset:256\n\t"
        "ds_read_b64_tr_b16 v[228:229], v101 offset:4096\n\t"
        "ds_read_b64_tr_b16 v[230:231], v101 offset:4352\n\t"
        "s_waitcnt lgkmcnt(8)\n\t"
        "ds_read_b64_tr_b16 v[104:105], v102 offset:0\n\t"
        "ds_read_b64_tr_b16 v[106:107], v102 offset:256\n\t"
        "ds_read_b64_tr_b16 v[108:109], v102 offset:4096\n\t"
        "ds_read_b64_tr_b16 v[110:111], v102 offset:4352\n\t"
        "s_waitcnt lgkmcnt(0)\n\t"
        "ds_write_b128 v117, v[104:107] offset:20480\n\t"
        "ds_write_b128 v117, v[108:111] offset:21504\n\t"
        "s_waitcnt vmcnt(0)\n\t"
        "ds_write_b128 v97, v[64:67] offset:0\n\t"
        "ds_write_b128 v98, v[68:71] offset:1024\n\t"
        "ds_write_b128 v97, v[72:75] offset:2048\n\t"
        "ds_write_b128 v98, v[76:79] offset:3072\n\t"
        "ds_write_b128 v97, v[80:83] offset:4096\n\t"
        "ds_write_b128 v98, v[84:87] offset:5120\n\t"
        "ds_write_b128 v97, v[88:91] offset:6144\n\t"
        "ds_write_b128 v98, v[92:95] offset:7168\n\t"
        "ds_read_b64_tr_b16 v[232:233], v99 offset:0\n\t"
        "ds_read_b64_tr_b16 v[234:235], v99 offset:256\n\t"
        "ds_read_b64_tr_b16 v[236:237], v99 offset:4096\n\t"
        "ds_read_b64_tr_b16 v[238:239], v99 offset:4352\n\t"
        "s_waitcnt lgkmcnt(8)\n\t"
        "ds_read_b64_tr_b16 v[240:241], v100 offset:0\n\t"
        "ds_read_b64_tr_b16 v[242:243], v100 offset:256\n\t"
        "ds_read_b64_tr_b16 v[244:245], v100 offset:4096\n\t"
        "ds_read_b64_tr_b16 v[246:247], v100 offset:4352\n\t"
        "s_waitcnt lgkmcnt(8)\n\t"
        "ds_read_b64_tr_b16 v[248:249], v101 offset:0\n\t"
        "ds_read_b64_tr_b16 v[250:251], v101 offset:256\n\t"
        "ds_read_b64_tr_b16 v[252:253], v101 offset:4096\n\t"
        "ds_read_b64_tr_b16 v[254:255], v101 offset:4352\n\t"
        "s_waitcnt lgkmcnt(8)\n\t"
        "ds_read_b64_tr_b16 v[104:105], v102 offset:0\n\t"
        "ds_read_b64_tr_b16 v[106:107], v102 offset:256\n\t"
        "ds_read_b64_tr_b16 v[108:109], v102 offset:4096\n\t"
        "ds_read_b64_tr_b16 v[110:111], v102 offset:4352\n\t"
        "s_mov_b32 s35, 33792\n\t"
        "buffer_load_dword v0, off, s[12:15], s35 sc1\n\t"
        "s_lshl_b32 s35, s44, 10\n\t"
        "v_lshlrev_b32_e32 v1, 2, v122\n\t"
        "v_and_b32_e32 v1, 0x3c0, v1\n\t"
        "buffer_load_dword v1, v1, s[12:15], s35 offen offset:0 sc1\n\t"
        "s_waitcnt lgkmcnt(0)\n\t"
        "ds_write_b128 v126, v[104:107] offset:8192\n\t"
        "ds_write_b128 v126, v[108:111] offset:9216\n\t"
        "s_waitcnt lgkmcnt(0)\n\t"
        "v_mov_b32_e32 v110, v103\n\t"
        "ds_read_b128 v[64:67], v126 offset:0\n\t"
        "ds_read_b128 v[68:71], v126 offset:1024\n\t"
        "ds_read_b128 v[72:75], v126 offset:2048\n\t"
        "ds_read_b128 v[76:79], v126 offset:3072\n\t"
        "ds_read_b128 v[80:83], v126 offset:4096\n\t"
        "ds_read_b128 v[84:87], v126 offset:5120\n\t"
        "ds_read_b128 v[88:91], v126 offset:6144\n\t"
        "ds_read_b128 v[92:95], v126 offset:7168\n\t"
        "ds_read_b128 v[96:99], v126 offset:8192\n\t"
        "ds_read_b128 v[100:103], v126 offset:9216\n\t"
        "s_waitcnt lgkmcnt(0)\n\t"
        "v_mov_b32_e32 v126, 0\n\t"
        "s_barrier\n\t"
        "s_mov_b32 s48, 0\n\t"
        "s_mov_b32 s54, 0\n\t"
        "s_movk_i32 s56, 24576\n\t"
        "s_and_b32 s58, s43, 1\n\t"
        "s_lshl_b32 s58, s58, 12\n\t"
        "s_lshl_b32 s57, s44, 5\n\t"
        "s_add_u32 s57, s57, s43\n\t"
        "s_lshl_b32 s57, s57, 2\n\t"
        "s_add_u32 s57, s57, 458752\n\t"
        "s_waitcnt vmcnt(0)\n\t"
        "v_mov_b32_e32 v111, v1\n\t"
        "s_lshr_b32 s59, s48, 1\n\t"
        "s_cmp_gt_u32 s54, s59\n\t"
        "s_cbranch_scc1 LA_kv0ok%=\n\t"
        "s_mov_b32 s42, 0\n\t"
        "s_waitcnt vmcnt(0)\n\t"
        "s_branch LA_kv0w%=\n\t"
        "LA_kv0%=:\n\t"
        "s_waitcnt vmcnt(0)\n\t"
        "LA_kv0w%=:\n\t"
        "v_min_u32_dpp v111, v111, v111 quad_perm:[1,0,3,2] row_mask:0xf bank_mask:0xf\n\t"
        "s_nop 1\n\t"
        "v_min_u32_dpp v111, v111, v111 quad_perm:[2,3,0,1] row_mask:0xf bank_mask:0xf\n\t"
        "s_nop 1\n\t"
        "v_min_u32_dpp v111, v111, v111 row_ror:4 row_mask:0xf bank_mask:0xf\n\t"
        "s_nop 1\n\t"
        "v_min_u32_dpp v111, v111, v111 row_ror:8 row_mask:0xf bank_mask:0xf\n\t"
        "s_nop 0\n\t"
        "v_readfirstlane_b32 s54, v111\n\t"
        "s_nop 0\n\t"
        "s_cmp_gt_u32 s54, s59\n\t"
        "s_cbranch_scc1 LA_kv0ok%=\n\t"
        "s_sleep 4\n\t"
        "s_lshl_b32 s35, s44, 10\n\t"
        "v_lshlrev_b32_e32 v111, 2, v122\n\t"
        "v_and_b32_e32 v111, 0x3c0, v111\n\t"
        "buffer_load_dword v111, v111, s[12:15], s35 offen offset:0 sc1\n\t"
        "s_add_u32 s42, s42, 1\n\t"
        "s_cmp_lt_u32 s42, 0x100000\n\t"
        "s_cbranch_scc1 LA_kv0%=\n\t"
        "s_movk_i32 s34, 16\n\t"
        "s_branch LA_die%=\n\t"
        "LA_kv0ok%=:\n\t"
        "s_and_b32 s36, s48, 1\n\t"
        "s_mul_i32 s36, s36, 24576\n\t"
        "s_add_u32 s36, s36, s55\n\t"
        "s_lshl_b32 s37, s48, 18\n\t"
        "s_lshl_b32 s34, s43, 11\n\t"
        "s_add_u32 s37, s37, s34\n\t"
        "s_add_u32 m0, s36, 0\n\t"
        "s_add_u32 s34, s37, 29548544\n\t"
        "buffer_load_dwordx4 v122, s[12:15], s34 offen sc1 lds\n\t"
        "s_add_u32 m0, s36, 1024\n\t"
        "s_add_u32 s34, s37, 29549568\n\t"
        "buffer_load_dwordx4 v122, s[12:15], s34 offen sc1 lds\n\t"
        "s_add_u32 m0, s36, 8192\n\t"
        "s_add_u32 s34, s37, 12771328\n\t"
        "buffer_load_dwordx4 v122, s[12:15], s34 offen sc1 lds\n\t"
        "s_add_u32 m0, s36, 9216\n\t"
        "s_add_u32 s34, s37, 12772352\n\t"
        "buffer_load_dwordx4 v122, s[12:15], s34 offen sc1 lds\n\t"
        "s_add_u32 m0, s36, 16384\n\t"
        "s_add_u32 s34, s37, 46325760\n\t"
        "buffer_load_dwordx4 v122, s[12:15], s34 offen sc1 lds\n\t"
        "s_add_u32 m0, s36, 17408\n\t"
        "s_add_u32 s34, s37, 46326784\n\t"
        "buffer_load_dwordx4 v122, s[12:15], s34 offen sc1 lds\n\t"
        "s_mov_b32 s42, 0\n\t"
        "LA_cal%=:\n\t"
        "v_readfirstlane_b32 s34, v0\n\t"
        "s_nop 0\n\t"
        "s_cmp_lg_u32 s34, 0\n\t"
        "s_cbranch_scc1 LA_calok%=\n\t"
        "s_sleep 4\n\t"
        "s_mov_b32 s35, 33792\n\t"
        "buffer_load_dword v0, off, s[12:15], s35 sc1\n\t"
        "s_waitcnt vmcnt(0)\n\t"
        "s_add_u32 s42, s42, 1\n\t"
        "s_cmp_lt_u32 s42, 0x100000\n\t"
        "s_cbranch_scc1 LA_cal%=\n\t"
        "s_movk_i32 s34, 8\n\t"
        "s_branch LA_die%=\n\t"
        "LA_calok%=:\n\t"
        "s_and_b32 s34, s34, 8192\n\t"
        "s_add_u32 s8, s8, s34\n\t"
        "s_addc_u32 s9, s9, 0\n\t"
        "s_barrier\n\t"
        "s_cmp_lg_u32 s43, 0\n\t"
        "s_cbranch_scc1 LA_st%=\n\t"
        "v_mov_b32_e32 v2, 1\n\t"
        "s_mov_b32 s35, 34304\n\t"
        "s_mov_b64 exec, 1\n\t"
        "buffer_atomic_add v2, off, s[12:15], s35 sc0\n\t"
        "s_mov_b64 exec, -1\n\t"
        "s_waitcnt vmcnt(0)\n\t"
        "v_readfirstlane_b32 s34, v2\n\t"
        "s_nop 0\n\t"
        "s_cmp_lg_u32 s34, 95\n\t"
        "s_cbranch_scc1 LA_st%=\n\t"
        "s_memrealtime s[32:33]\n\t"
        "v_mbcnt_lo_u32_b32 v3, -1, 0\n\t"
        "v_lshlrev_b32_e32 v3, 7, v3\n\t"
        "s_waitcnt lgkmcnt(0)\n\t"
        "s_add_u32 s34, s32, 50\n\t"
        "s_or_b32 s34, s34, 1\n\t"
        "v_mov_b32_e32 v2, s34\n\t"
        "s_mov_b32 s35, 36864\n\t"
        "s_mov_b64 exec, 0xffff\n\t"
        "buffer_store_dword v2, v3, s[12:15], s35 offen sc1\n\t"
        "s_mov_b64 exec, -1\n\t"
        "LA_st%=:\n\t"
        "s_mov_b32 s24, 0\n\t"
        "s_mov_b32 s26, 0\n\t"
        "s_mov_b32 s27, 0x80000\n\t"
        "s_mov_b32 s28, 0x180000\n\t"
        "s_mov_b32 s31, 0\n\t"
        "s_mov_b32 s47, 0\n\t"
        "s_mov_b32 s38, 0xff00ff00\n\t"
        "s_mov_b32 s39, 0xff00ff00\n\t"
        "s_mov_b32 s40, 0\n\t"
        "s_mov_b32 s41, -1\n\t"
        "ds_read_b128 v[32:35], v117 offset:0\n\t"
        "ds_read_b128 v[36:39], v117 offset:1024\n\t"
        "ds_read_b128 v[40:43], v117 offset:2048\n\t"
        "ds_read_b128 v[44:47], v117 offset:3072\n\t"
        "ds_read_b128 v[48:51], v117 offset:4096\n\t"
        "ds_read_b128 v[52:55], v117 offset:5120\n\t"
        "ds_read_b128 v[56:59], v117 offset:6144\n\t"
        "ds_read_b128 v[60:63], v117 offset:7168\n\t"
        "v_mov_b64_e32 v[0:1], 0\n\t"
        "v_mov_b64_e32 v[2:3], 0\n\t"
        "v_mov_b64_e32 v[4:5], 0\n\t"
        "v_mov_b64_e32 v[6:7], 0\n\t"
        "v_mov_b64_e32 v[8:9], 0\n\t"
        "v_mov_b64_e32 v[10:11], 0\n\t"
        "v_mov_b64_e32 v[12:13], 0\n\t"
        "v_mov_b64_e32 v[14:15], 0\n\t"
        "s_mov_b32 s42, 0\n\t"
        "LA_gw%=:\n\t"
        "buffer_load_dword v16, off, s[12:15], s45 sc1\n\t"
        "s_waitcnt vmcnt(0)\n\t"
        "v_readfirstlane_b32 s25, v16\n\t"
        "s_nop 0\n\t"
        "s_cmp_lg_u32 s25, 0\n\t"
        "s_cbranch_scc1 LA_loop%=\n\t"
        "s_sleep 2\n\t"
        "s_add_u32 s42, s42, 1\n\t"
        "s_cmp_lt_u32 s42, 0x100000\n\t"
        "s_cbranch_scc1 LA_gw%=\n\t"
        "s_movk_i32 s34, 9\n\t"
        "s_branch LA_die%=\n\t"
        "LA_loop%=:\n\t"
        "s_mov_b32 s42, 0\n\t"
        "s_sub_u32 s35, s25, 1\n\t"
        "LA_wa%=:\n\t"
        "s_memrealtime s[32:33]\n\t"
        "s_waitcnt lgkmcnt(0)\n\t"
        "s_sub_u32 s34, s35, s32\n\t"
        "s_cmpk_lt_u32 s34, 0x2000\n\t"
        "s_cbranch_scc1 LA_wa%=\n\t"
        "LA_rd%=:\n\t"
        "buffer_load_dwordx4 v[16:19], v112, s[8:11], s26 offen offset:0 sc1\n\t"
        "s_nop 12\n\t"
        "buffer_load_dwordx4 v[20:23], v112, s[8:11], s26 offen offset:1024 sc1\n\t"
        "s_nop 12\n\t"
        "buffer_load_dwordx4 v[24:27], v112, s[8:11], s26 offen offset:2048 sc1\n\t"
        "s_nop 12\n\t"
        "buffer_load_dwordx4 v[28:31], v112, s[8:11], s26 offen offset:3072 sc1\n\t"
        "LA_go%=:\n\t"
        "s_waitcnt vmcnt(3)\n\t"
        "v_smfmac_f32_16x16x64_bf16 v[0:3], v[16:19], a[0:7], v110\n\t"
        "ds_write_b128 v113, v[16:19] offset:0\n\t"
        "v_smfmac_f32_16x16x64_bf16 v[4:7], v[16:19], a[8:15], v110\n\t"
        "v_max3_u32 v111, v16, v17, v18\n\t"
        "v_max_u32_e32 v111, v19, v111\n\t"
        "v_smfmac_f32_16x16x64_bf16 v[8:11], v[16:19], a[16:23], v110\n\t"
        "v_smfmac_f32_16x16x64_bf16 v[12:15], v[16:19], v[32:39], v110\n\t"
        "s_waitcnt vmcnt(2)\n\t"
        "v_smfmac_f32_16x16x64_bf16 v[0:3], v[20:23], a[24:31], v110\n\t"
        "ds_write_b128 v113, v[20:23] offset:1024\n\t"
        "v_smfmac_f32_16x16x64_bf16 v[4:7], v[20:23], a[32:39], v110\n\t"
        "v_max3_u32 v111, v20, v21, v111\n\t"
        "v_max3_u32 v111, v22, v23, v111\n\t"
        "v_smfmac_f32_16x16x64_bf16 v[8:11], v[20:23], a[40:47], v110\n\t"
        "s_waitcnt vmcnt(1)\n\t"
        "ds_write_b128 v113, v[24:27] offset:2048\n\t"
        "v_smfmac_f32_16x16x64_bf16 v[12:15], v[20:23], v[40:47], v110\n\t"
        "v_max3_u32 v111, v24, v25, v111\n\t"
        "v_max3_u32 v111, v26, v27, v111\n\t"
        "v_smfmac_f32_16x16x64_bf16 v[0:3], v[24:27], a[48:55], v110\n\t"
        "v_smfmac_f32_16x16x64_bf16 v[4:7], v[24:27], a[56:63], v110\n\t"
        "s_waitcnt vmcnt(0)\n\t"
        "ds_write_b128 v113, v[28:31] offset:3072\n\t"
        "v_max3_u32 v111, v28, v29, v111\n\t"
        "v_max3_u32 v111, v30, v31, v111\n\t"
        "v_smfmac_f32_16x16x64_bf16 v[8:11], v[24:27], a[64:71], v110\n\t"
        "v_cmp_eq_u32_e32 vcc, -1, v111\n\t"
        "v_smfmac_f32_16x16x64_bf16 v[12:15], v[24:27], v[48:55], v110\n\t"
        "s_waitcnt lgkmcnt(0)\n\t"
        "s_cbranch_vccnz LA_rdfail%=\n\t"
        "s_barrier\n\t"
        "ds_read_b128 v[16:19], v114 offset:0\n\t"
        "ds_read_b128 v[20:23], v114 offset:1024\n\t"
        "ds_read_b128 v[24:27], v114 offset:2048\n\t"
        "v_smfmac_f32_16x16x64_bf16 v[0:3], v[28:31], a[72:79], v110\n\t"
        "v_smfmac_f32_16x16x64_bf16 v[4:7], v[28:31], a[80:87], v110\n\t"
        "v_smfmac_f32_16x16x64_bf16 v[8:11], v[28:31], a[88:95], v110\n\t"
        "v_smfmac_f32_16x16x64_bf16 v[12:15], v[28:31], v[56:63], v110\n\t"
        "ds_read_b128 v[28:31], v114 offset:3072\n\t"
        "s_waitcnt lgkmcnt(3)\n\t"
        "v_smfmac_f32_16x16x64_bf16 v[12:15], v[16:19], v[64:71], v110\n\t"
        "ds_read_b128 v[32:35], v117 offset:8192\n\t"
        "v_smfmac_f32_16x16x64_bf16 v[0:3], v[16:19], a[96:103], v110\n\t"
        "ds_read_b128 v[36:39], v117 offset:9216\n\t"
        "v_smfmac_f32_16x16x64_bf16 v[4:7], v[16:19], a[104:111], v110\n\t"
        "v_smfmac_f32_16x16x64_bf16 v[8:11], v[16:19], a[112:119], v110\n\t"
        "ds_read_b128 v[16:19], v115 offset:0\n\t"
        "s_waitcnt lgkmcnt(5)\n\t"
        "v_smfmac_f32_16x16x64_bf16 v[12:15], v[20:23], v[72:79], v110\n\t"
        "ds_read_b128 v[40:43], v117 offset:10240\n\t"
        "v_smfmac_f32_16x16x64_bf16 v[0:3], v[20:23], a[120:127], v110\n\t"
        "ds_read_b128 v[44:47], v117 offset:11264\n\t"
        "v_smfmac_f32_16x16x64_bf16 v[4:7], v[20:23], a[128:135], v110\n\t"
        "v_smfmac_f32_16x16x64_bf16 v[8:11], v[20:23], a[136:143], v110\n\t"
        "ds_read_b128 v[20:23], v115 offset:1024\n\t"
        "s_waitcnt lgkmcnt(7)\n\t"
        "v_smfmac_f32_16x16x64_bf16 v[12:15], v[24:27], v[80:87], v110\n\t"
        "ds_read_b128 v[48:51], v117 offset:12288\n\t"
        "v_smfmac_f32_16x16x64_bf16 v[0:3], v[24:27], a[144:151], v110\n\t"
        "ds_read_b128 v[52:55], v117 offset:13312\n\t"
        "v_smfmac_f32_16x16x64_bf16 v[4:7], v[24:27], a[152:159], v110\n\t"
        "v_smfmac_f32_16x16x64_bf16 v[8:11], v[24:27], a[160:167], v110\n\t"
        "ds_read_b128 v[24:27], v115 offset:2048\n\t"
        "s_waitcnt lgkmcnt(9)\n\t"
        "v_smfmac_f32_16x16x64_bf16 v[12:15], v[28:31], v[88:95], v110\n\t"
        "ds_read_b128 v[56:59], v117 offset:14336\n\t"
        "v_smfmac_f32_16x16x64_bf16 v[0:3], v[28:31], a[168:175], v110\n\t"
        "ds_read_b128 v[60:63], v117 offset:15360\n\t"
        "v_smfmac_f32_16x16x64_bf16 v[4:7], v[28:31], a[176:183], v110\n\t"
        "v_smfmac_f32_16x16x64_bf16 v[8:11], v[28:31], a[184:191], v110\n\t"
        "ds_read_b128 v[28:31], v115 offset:3072\n\t"
        "s_waitcnt lgkmcnt(9)\n\t"
        "v_smfmac_f32_16x16x64_bf16 v[12:15], v[16:19], v[32:39], v110\n\t"
        "ds_read_b128 v[32:35], v117 offset:16384\n\t"
        "v_smfmac_f32_16x16x64_bf16 v[0:3], v[16:19], a[192:199], v110\n\t"
        "ds_read_b128 v[36:39], v117 offset:17408\n\t"
        "v_smfmac_f32_16x16x64_bf16 v[4:7], v[16:19], a[200:207], v110\n\t"
        "v_smfmac_f32_16x16x64_bf16 v[8:11], v[16:19], a[208:215], v110\n\t"
        "ds_read_b128 v[16:19], v116 offset:0\n\t"
        "s_waitcnt lgkmcnt(9)\n\t"
        "v_smfmac_f32_16x16x64_bf16 v[12:15], v[20:23], v[40:47], v110\n\t"
        "ds_read_b128 v[40:43], v117 offset:18432\n\t"
        "v_smfmac_f32_16x16x64_bf16 v[0:3], v[20:23], a[216:223], v110\n\t"
        "ds_read_b128 v[44:47], v117 offset:19456\n\t"
        "v_smfmac_f32_16x16x64_bf16 v[4:7], v[20:23], a[224:231], v110\n\t"
        "v_smfmac_f32_16x16x64_bf16 v[8:11], v[20:23], a[232:239], v110\n\t"
        "ds_read_b128 v[20:23], v116 offset:1024\n\t"
        "s_waitcnt lgkmcnt(9)\n\t"
        "v_smfmac_f32_16x16x64_bf16 v[12:15], v[24:27], v[48:55], v110\n\t"
        "ds_read_b128 v[48:51], v117 offset:20480\n\t"
        "v_smfmac_f32_16x16x64_bf16 v[0:3], v[24:27], a[240:247], v110\n\t"
        "ds_read_b128 v[52:55], v117 offset:21504\n\t"
        "v_smfmac_f32_16x16x64_bf16 v[4:7], v[24:27], a[248:255], v110\n\t"
        "v_smfmac_f32_16x16x64_bf16 v[8:11], v[24:27], v[128:135], v110\n\t"
        "ds_read_b128 v[24:27], v116 offset:2048\n\t"
        "s_waitcnt lgkmcnt(9)\n\t"
        "v_smfmac_f32_16x16x64_bf16 v[12:15], v[28:31], v[56:63], v110\n\t"
        "ds_read_b128 v[56:59], v124 offset:3072\n\t"
        "v_smfmac_f32_16x16x64_bf16 v[0:3], v[28:31], v[136:143], v110\n\t"
        "ds_read_b128 v[60:63], v124 offset:7168\n\t"
        "v_smfmac_f32_16x16x64_bf16 v[4:7], v[28:31], v[144:151], v110\n\t"
        "v_smfmac_f32_16x16x64_bf16 v[8:11], v[28:31], v[152:159], v110\n\t"
        "ds_read_b128 v[28:31], v116 offset:3072\n\t"
        "s_waitcnt lgkmcnt(9)\n\t"
        "v_smfmac_f32_16x16x64_bf16 v[12:15], v[16:19], v[32:39], v110\n\t"
        "ds_read_b128 v[32:35], v124 offset:0\n\t"
        "v_smfmac_f32_16x16x64_bf16 v[0:3], v[16:19], v[160:167], v110\n\t"
        "ds_read_b128 v[36:39], v124 offset:4096\n\t"
        "v_smfmac_f32_16x16x64_bf16 v[4:7], v[16:19], v[168:175], v110\n\t"
        "v_smfmac_f32_16x16x64_bf16 v[8:11], v[16:19], v[176:183], v110\n\t"
        "s_waitcnt lgkmcnt(8)\n\t"
        "v_smfmac_f32_16x16x64_bf16 v[12:15], v[20:23], v[40:47], v110\n\t"
        "ds_read_b128 v[40:43], v124 offset:1024\n\t"
        "v_smfmac_f32_16x16x64_bf16 v[8:11], v[20:23], v[200:207], v110\n\t"
        "ds_read_b128 v[44:47], v124 offset:5120\n\t"
        "s_waitcnt lgkmcnt(7)\n\t"
        "v_smfmac_f32_16x16x64_bf16 v[12:15], v[24:27], v[48:55], v110\n\t"
        "ds_read_b128 v[48:51], v124 offset:2048\n\t"
        "v_smfmac_f32_16x16x64_bf16 v[8:11], v[24:27], v[224:231], v110\n\t"
        "ds_read_b128 v[52:55], v124 offset:6144\n\t"
        "buffer_load_dwordx4 v[104:107], v120, s[8:11], s26 offen sc1\n\t"
        "s_waitcnt lgkmcnt(6)\n\t"
        "v_smfmac_f32_16x16x64_bf16 v[12:15], v[28:31], v[96:103], v110\n\t"
        "v_smfmac_f32_16x16x64_bf16 v[8:11], v[28:31], v[248:255], v110\n\t"
        "v_smfmac_f32_16x16x64_bf16 v[0:3], v[20:23], v[184:191], v110\n\t"
        "s_lshl_b32 s34, s43, 11\n\t"
        "v_add_u32_e32 v108, s34, v124\n\t"
        "v_smfmac_f32_16x16x64_bf16 v[0:3], v[24:27], v[208:215], v110\n\t"
        "v_add_u32_e32 v109, s58, v124\n\t"
        "v_smfmac_f32_16x16x64_bf16 v[4:7], v[20:23], v[192:199], v110\n\t"
        "v_smfmac_f32_16x16x64_bf16 v[0:3], v[28:31], v[232:239], v110\n\t"
        "v_smfmac_f32_16x16x64_bf16 v[4:7], v[24:27], v[216:223], v110\n\t"
        "v_smfmac_f32_16x16x64_bf16 v[4:7], v[28:31], v[240:247], v110\n\t"
        "v_add_f32_e32 v12, v12, v13\n\t"
        "v_add_f32_e32 v14, v14, v15\n\t"
        "v_cvt_pk_bf16_f32 v12, v12, v14\n\t"
        "v_add_f32_e32 v8, v8, v9\n\t"
        "v_add_f32_e32 v10, v10, v11\n\t"
        "v_cvt_pk_bf16_f32 v8, v8, v10\n\t"
        "ds_write2_b32 v118, v8, v12 offset0:64 offset1:96\n\t"
        "v_add_f32_e32 v0, v0, v1\n\t"
        "v_add_f32_e32 v2, v2, v3\n\t"
        "v_cvt_pk_bf16_f32 v0, v0, v2\n\t"
        "v_add_f32_e32 v4, v4, v5\n\t"
        "v_add_f32_e32 v6, v6, v7\n\t"
        "v_cvt_pk_bf16_f32 v4, v4, v6\n\t"
        "ds_write2_b32 v118, v0, v4 offset0:0 offset1:32\n\t"
        "s_cmp_eq_u32 s24, 63\n\t"
        "s_waitcnt lgkmcnt(0)\n\t"
        "s_barrier\n\t"
        "s_cbranch_scc1 LA_skip%=\n\t"
        "ds_read_b64_tr_b16 v[16:17], v119 offset:0\n\t"
        "ds_read_b64_tr_b16 v[18:19], v119 offset:32\n\t"
        "ds_read_b64_tr_b16 v[20:21], v119 offset:256\n\t"
        "ds_read_b64_tr_b16 v[22:23], v119 offset:288\n\t"
        "ds_read_b64_tr_b16 v[24:25], v119 offset:512\n\t"
        "ds_read_b64_tr_b16 v[26:27], v119 offset:544\n\t"
        "ds_read_b64_tr_b16 v[28:29], v119 offset:768\n\t"
        "ds_read_b64_tr_b16 v[30:31], v119 offset:800\n\t"
        "buffer_store_dword v126, v127, s[12:15], 0 offen sc1\n\t"
        "s_waitcnt lgkmcnt(6)\n\t"
        "v_mfma_f32_16x16x32_bf16 v[0:3], v[32:35], v[16:19], 0\n\t"
        "ds_read_b64_tr_b16 v[32:33], v119 offset:2112\n\t"
        "ds_read_b64_tr_b16 v[34:35], v119 offset:2144\n\t"
        "v_mfma_f32_16x16x32_bf16 v[4:7], v[36:39], v[16:19], 0\n\t"
        "ds_read_b64_tr_b16 v[36:37], v119 offset:2368\n\t"
        "ds_read_b64_tr_b16 v[38:39], v119 offset:2400\n\t"
        "s_waitcnt lgkmcnt(8)\n\t"
        "v_mfma_f32_16x16x32_bf16 v[0:3], v[40:43], v[20:23], v[0:3]\n\t"
        "ds_read_b64_tr_b16 v[40:41], v119 offset:2624\n\t"
        "ds_read_b64_tr_b16 v[42:43], v119 offset:2656\n\t"
        "v_mfma_f32_16x16x32_bf16 v[4:7], v[44:47], v[20:23], v[4:7]\n\t"
        "ds_read_b64_tr_b16 v[44:45], v119 offset:2880\n\t"
        "ds_read_b64_tr_b16 v[46:47], v119 offset:2912\n\t"
        "s_waitcnt lgkmcnt(10)\n\t"
        "v_mfma_f32_16x16x32_bf16 v[0:3], v[48:51], v[24:27], v[0:3]\n\t"
        "ds_read_b128 v[48:51], v108 offset:16384\n\t"
        "v_mfma_f32_16x16x32_bf16 v[4:7], v[52:55], v[24:27], v[4:7]\n\t"
        "ds_read_b128 v[52:55], v108 offset:17408\n\t"
        "s_waitcnt lgkmcnt(10)\n\t"
        "v_mfma_f32_16x16x32_bf16 v[0:3], v[56:59], v[28:31], v[0:3]\n\t"
        "v_mfma_f32_16x16x32_bf16 v[4:7], v[60:63], v[28:31], v[4:7]\n\t"
        "s_waitcnt lgkmcnt(2)\n\t"
        "v_mfma_f32_16x16x32_bf16 v[8:11], v[32:35], v[16:19], 0\n\t"
        "s_nop 1\n\t"
        "v_mfma_f32_16x16x32_bf16 v[8:11], v[36:39], v[20:23], v[8:11]\n\t"
        "s_nop 1\n\t"
        "v_mfma_f32_16x16x32_bf16 v[8:11], v[40:43], v[24:27], v[8:11]\n\t"
        "v_max3_f32 v59, v0, v1, v2\n\t"
        "v_max3_f32 v59, v59, v3, v4\n\t"
        "v_mfma_f32_16x16x32_bf16 v[8:11], v[44:47], v[28:31], v[8:11]\n\t"
        "v_max3_f32 v59, v59, v5, v6\n\t"
        "v_max_f32_e32 v60, v59, v7\n\t"
        "v_max_f32_e32 v59, v59, v7\n\t"
        "v_cndmask_b32_e64 v0, v0, v4, s[38:39]\n\t"
        "v_cndmask_b32_e64 v1, v1, v5, s[38:39]\n\t"
        "v_permlane32_swap_b32_e32 v59, v60\n\t"
        "v_cndmask_b32_e64 v2, v2, v6, s[38:39]\n\t"
        "v_cndmask_b32_e64 v3, v3, v7, s[38:39]\n\t"
        "v_max3_f32 v59, v59, v60, v8\n\t"
        "v_max3_f32 v59, v59, v9, v10\n\t"
        "v_max_f32_e32 v60, v59, v11\n\t"
        "v_max_f32_e32 v59, v59, v11\n\t"
        "v_cndmask_b32_e64 v8, v8, v9, s[38:39]\n\t"
        "v_cndmask_b32_e64 v10, v10, v11, s[38:39]\n\t"
        "v_permlane16_swap_b32_e32 v59, v60\n\t"
        "v_max_f32_e32 v59, v59, v60\n\t"
        "v_mul_f32_e32 v59, 0xbe02940c, v59\n\t"
        "v_cndmask_b32_e64 v58, v8, v10, s[40:41]\n\t"
        "v_fmamk_f32 v0, v0, 0x3e02940c, v59\n\t"
        "v_exp_f32_e32 v0, v0\n\t"
        "v_fmamk_f32 v1, v1, 0x3e02940c, v59\n\t"
        "v_exp_f32_e32 v1, v1\n\t"
        "v_fmamk_f32 v2, v2, 0x3e02940c, v59\n\t"
        "v_exp_f32_e32 v2, v2\n\t"
        "v_fmamk_f32 v3, v3, 0x3e02940c, v59\n\t"
        "v_exp_f32_e32 v3, v3\n\t"
        "v_fmamk_f32 v58, v58, 0x3e02940c, v59\n\t"
        "v_cvt_pk_bf16_f32 v12, v0, v1\n\t"
        "v_cvt_pk_bf16_f32 v13, v2, v3\n\t"
        "v_exp_f32_e32 v58, v58\n\t"
        "v_add_f32_e32 v60, v0, v1\n\t"
        "v_mov_b32_dpp v14, v12 row_ror:8 row_mask:0xf bank_mask:0xf\n\t"
        "v_mov_b32_dpp v15, v13 row_ror:8 row_mask:0xf bank_mask:0xf\n\t"
        "v_mov_b32_dpp v59, v58 row_ror:8 row_mask:0xf bank_mask:0xf\n\t"
        "v_add_f32_e32 v61, v2, v3\n\t"
        "s_waitcnt lgkmcnt(0)\n\t"
        "v_mfma_f32_16x16x32_bf16 v[4:7], v[48:51], v[12:15], 0\n\t"
        "v_cvt_pk_bf16_f32 v59, v58, v59\n\t"
        "v_add_f32_e32 v60, v60, v58\n\t"
        "v_mfma_f32_16x16x32_bf16 v[8:11], v[52:55], v[12:15], 0\n\t"
        "v_cndmask_b32_e64 v56, v59, 0, s[40:41]\n\t"
        "v_cndmask_b32_e64 v57, 0, v59, s[40:41]\n\t"
        "v_add_f32_e32 v59, v60, v61\n\t"
        "s_waitcnt vmcnt(1)\n\t"
        "v_max3_u32 v111, v104, v105, v106\n\t"
        "v_mfma_f32_16x16x16_bf16 v[4:7], v[104:105], v[56:57], v[4:7]\n\t"
        "v_add_f32_dpp v60, v59, v59 row_ror:8 row_mask:0xf bank_mask:0xf\n\t"
        "v_add_f32_dpp v61, v59, v59 row_ror:8 row_mask:0xf bank_mask:0xf\n\t"
        "v_mfma_f32_16x16x16_bf16 v[8:11], v[106:107], v[56:57], v[8:11]\n\t"
        "v_max_u32_e32 v111, v111, v107\n\t"
        "v_permlane16_swap_b32_e32 v60, v61\n\t"
        "v_add_f32_e32 v59, v60, v61\n\t"
        "v_add_f32_e32 v60, v60, v61\n\t"
        "v_cmp_eq_u32_e32 vcc, -1, v111\n\t"
        "s_nop 0\n\t"
        "v_permlane32_swap_b32_e32 v59, v60\n\t"
        "v_add_f32_e32 v59, v59, v60\n\t"
        "v_rcp_f32_e32 v59, v59\n\t"
        "s_cbranch_vccnz LA_vmfail%=\n\t"
        "LA_vmok%=:\n\t"
        "v_mul_f32_e32 v4, v4, v59\n\t"
        "v_mul_f32_e32 v5, v5, v59\n\t"
        "v_cvt_pk_bf16_f32 v0, v4, v5\n\t"
        "v_mul_f32_e32 v6, v6, v59\n\t"
        "v_mul_f32_e32 v7, v7, v59\n\t"
        "v_cvt_pk_bf16_f32 v1, v6, v7\n\t"
        "v_mul_f32_e32 v8, v8, v59\n\t"
        "v_mul_f32_e32 v9, v9, v59\n\t"
        "v_cvt_pk_bf16_f32 v2, v8, v9\n\t"
        "v_mul_f32_e32 v10, v10, v59\n\t"
        "v_mul_f32_e32 v11, v11, v59\n\t"
        "v_cvt_pk_bf16_f32 v3, v10, v11\n\t"
        "buffer_store_dwordx4 v[0:3], v121, s[8:11], s26 offen sc1\n\t"
        "LA_x%=:\n\t"
        "s_memrealtime s[52:53]\n\t"
        "ds_read_b128 v[16:19], v109 offset:8192\n\t"
        "ds_read_b128 v[20:23], v109 offset:9216\n\t"
        "ds_read_b128 v[24:27], v109 offset:10240\n\t"
        "ds_read_b128 v[28:31], v109 offset:11264\n\t"
        "ds_read_b128 v[56:59], v124 offset:0\n\t"
        "ds_read_b128 v[60:63], v124 offset:4096\n\t"
        "ds_read_b128 v[12:15], v124 offset:1024\n\t"
        "v_xor_b32_e32 v126, 0x800, v108\n\t"
        "s_add_u32 s48, s24, 1\n\t"
        "s_min_u32 s48, s48, 63\n\t"
        "buffer_load_dwordx2 v[108:109], v125, s[8:11], s26 offen sc1\n\t"
        "s_lshl_b32 s35, s44, 10\n\t"
        "v_lshlrev_b32_e32 v111, 2, v122\n\t"
        "v_and_b32_e32 v111, 0x3c0, v111\n\t"
        "buffer_load_dword v111, v111, s[12:15], s35 offen offset:0 sc1\n\t"
        "s_waitcnt lgkmcnt(6)\n\t"
        "v_mfma_f32_16x16x32_bf16 v[8:11], v[32:35], v[16:19], 0\n\t"
        "ds_read_b128 v[32:35], v124 offset:5120\n\t"
        "s_waitcnt lgkmcnt(6)\n\t"
        "v_mfma_f32_16x16x32_bf16 v[8:11], v[36:39], v[20:23], v[8:11]\n\t"
        "ds_read_b128 v[36:39], v124 offset:2048\n\t"
        "s_waitcnt lgkmcnt(6)\n\t"
        "v_mfma_f32_16x16x32_bf16 v[8:11], v[40:43], v[24:27], v[8:11]\n\t"
        "ds_read_b128 v[40:43], v124 offset:6144\n\t"
        "s_waitcnt lgkmcnt(6)\n\t"
        "v_mfma_f32_16x16x32_bf16 v[8:11], v[44:47], v[28:31], v[8:11]\n\t"
        "ds_read_b128 v[44:47], v124 offset:3072\n\t"
        "s_add_u32 s35, s24, 1\n\t"
        "s_waitcnt lgkmcnt(6)\n\t"
        "v_mfma_f32_16x16x32_bf16 v[0:3], v[56:59], v[16:19], 0\n\t"
        "ds_read_b128 v[56:59], v124 offset:7168\n\t"
        "s_waitcnt lgkmcnt(6)\n\t"
        "v_mfma_f32_16x16x32_bf16 v[4:7], v[60:63], v[16:19], 0\n\t"
        "s_and_b32 s36, s35, 1\n\t"
        "s_mul_i32 s36, s36, 24576\n\t"
        "s_waitcnt lgkmcnt(5)\n\t"
        "v_mfma_f32_16x16x32_bf16 v[0:3], v[12:15], v[20:23], v[0:3]\n\t"
        "s_add_u32 s36, s36, s55\n\t"
        "s_lshl_b32 s37, s48, 18\n\t"
        "s_waitcnt lgkmcnt(4)\n\t"
        "v_mfma_f32_16x16x32_bf16 v[4:7], v[32:35], v[20:23], v[4:7]\n\t"
        "v_xor_b32_e32 v32, 0x200, v120\n\t"
        "buffer_load_dwordx4 v[12:15], v32, s[8:11], s26 offen sc1\n\t"
        "s_waitcnt lgkmcnt(3)\n\t"
        "v_mfma_f32_16x16x32_bf16 v[0:3], v[36:39], v[24:27], v[0:3]\n\t"
        "s_lshl_b32 s34, s43, 11\n\t"
        "s_add_u32 s37, s37, s34\n\t"
        "s_waitcnt lgkmcnt(2)\n\t"
        "v_mfma_f32_16x16x32_bf16 v[4:7], v[40:43], v[24:27], v[4:7]\n\t"
        "v_cvt_pk_bf16_f32 v8, 0, v8\n\t"
        "v_cvt_pk_bf16_f32 v9, 0, v9\n\t"
        "v_cvt_pk_bf16_f32 v10, 0, v10\n\t"
        "v_cvt_pk_bf16_f32 v11, 0, v11\n\t"
        "s_waitcnt lgkmcnt(1)\n\t"
        "v_mfma_f32_16x16x32_bf16 v[0:3], v[44:47], v[28:31], v[0:3]\n\t"
        "v_mul_f32_e32 v8, 0x3db504f3, v8\n\t"
        "v_mul_f32_e32 v9, 0x3db504f3, v9\n\t"
        "v_mul_f32_e32 v10, 0x3db504f3, v10\n\t"
        "v_mul_f32_e32 v11, 0x3db504f3, v11\n\t"
        "s_waitcnt lgkmcnt(0)\n\t"
        "v_mfma_f32_16x16x32_bf16 v[4:7], v[56:59], v[28:31], v[4:7]\n\t"
        "v_cvt_pk_bf16_f32 v8, 0, v8\n\t"
        "v_cvt_pk_bf16_f32 v9, 0, v9\n\t"
        "v_cvt_pk_bf16_f32 v10, 0, v10\n\t"
        "v_cvt_pk_bf16_f32 v11, 0, v11\n\t"
        "ds_read_b128 v[56:59], v126 offset:16384\n\t"
        "ds_read_b128 v[60:63], v126 offset:17408\n\t"
        "v_mov_b64_e32 v[36:37], -1\n\t"
        "v_mov_b64_e32 v[38:39], -1\n\t"
        "v_mov_b32_e32 v40, 0xce6e6b28\n\t"
        "v_mov_b32_e32 v46, 0x3db504f3\n\t"
        "v_mov_b32_e32 v47, 0x3fb8aa3b\n\t"
        "v_cvt_pk_bf16_f32 v0, 0, v0\n\t"
        "v_cvt_pk_bf16_f32 v1, 0, v1\n\t"
        "v_cvt_pk_bf16_f32 v2, 0, v2\n\t"
        "v_cvt_pk_bf16_f32 v3, 0, v3\n\t"
        "v_cvt_pk_bf16_f32 v4, 0, v4\n\t"
        "v_cvt_pk_bf16_f32 v5, 0, v5\n\t"
        "v_cvt_pk_bf16_f32 v6, 0, v6\n\t"
        "v_cvt_pk_bf16_f32 v7, 0, v7\n\t"
        "v_pk_mul_f32 v[0:1], v[0:1], v[46:47] op_sel_hi:[1,0]\n\t"
        "v_pk_mul_f32 v[2:3], v[2:3], v[46:47] op_sel_hi:[1,0]\n\t"
        "v_pk_mul_f32 v[4:5], v[4:5], v[46:47] op_sel_hi:[1,0]\n\t"
        "v_pk_mul_f32 v[6:7], v[6:7], v[46:47] op_sel_hi:[1,0]\n\t"
        "s_lshr_b32 s59, s48, 1\n\t"
        "s_cmp_gt_u32 s54, s59\n\t"
        "s_cbranch_scc1 LA_kvok%=\n\t"
        "s_mov_b32 s42, 0\n\t"
        "s_waitcnt vmcnt(1)\n\t"
        "s_branch LA_kvw%=\n\t"
        "LA_kv%=:\n\t"
        "s_waitcnt vmcnt(0)\n\t"
        "LA_kvw%=:\n\t"
        "v_min_u32_dpp v111, v111, v111 quad_perm:[1,0,3,2] row_mask:0xf bank_mask:0xf\n\t"
        "s_nop 1\n\t"
        "v_min_u32_dpp v111, v111, v111 quad_perm:[2,3,0,1] row_mask:0xf bank_mask:0xf\n\t"
        "s_nop 1\n\t"
        "v_min_u32_dpp v111, v111, v111 row_ror:4 row_mask:0xf bank_mask:0xf\n\t"
        "s_nop 1\n\t"
        "v_min_u32_dpp v111, v111, v111 row_ror:8 row_mask:0xf bank_mask:0xf\n\t"
        "s_nop 0\n\t"
        "v_readfirstlane_b32 s54, v111\n\t"
        "s_nop 0\n\t"
        "s_cmp_gt_u32 s54, s59\n\t"
        "s_cbranch_scc1 LA_kvok%=\n\t"
        "s_sleep 4\n\t"
        "s_lshl_b32 s35, s44, 10\n\t"
        "v_lshlrev_b32_e32 v111, 2, v122\n\t"
        "v_and_b32_e32 v111, 0x3c0, v111\n\t"
        "buffer_load_dword v111, v111, s[12:15], s35 offen offset:0 sc1\n\t"
        "s_add_u32 s42, s42, 1\n\t"
        "s_cmp_lt_u32 s42, 0x100000\n\t"
        "s_cbranch_scc1 LA_kv%=\n\t"
        "s_movk_i32 s34, 16\n\t"
        "s_branch LA_die%=\n\t"
        "LA_kvok%=:\n\t"
        "s_add_u32 m0, s36, 0\n\t"
        "s_add_u32 s34, s37, 29548544\n\t"
        "buffer_load_dwordx4 v122, s[12:15], s34 offen sc1 lds\n\t"
        "v_cvt_pk_bf16_f32 v0, 0, v0\n\t"
        "v_cvt_pk_bf16_f32 v1, 0, v1\n\t"
        "v_cvt_pk_bf16_f32 v2, 0, v2\n\t"
        "v_cvt_pk_bf16_f32 v3, 0, v3\n\t"
        "v_cvt_pk_bf16_f32 v4, 0, v4\n\t"
        "v_cvt_pk_bf16_f32 v5, 0, v5\n\t"
        "v_cvt_pk_bf16_f32 v6, 0, v6\n\t"
        "v_cvt_pk_bf16_f32 v7, 0, v7\n\t"
        "s_add_u32 m0, s36, 1024\n\t"
        "s_add_u32 s34, s37, 29549568\n\t"
        "buffer_load_dwordx4 v122, s[12:15], s34 offen sc1 lds\n\t"
        "v_cndmask_b32_e64 v0, v40, v0, s[60:61]\n\t"
        "v_cndmask_b32_e64 v1, v40, v1, s[62:63]\n\t"
        "v_cndmask_b32_e64 v2, v40, v2, s[64:65]\n\t"
        "v_cndmask_b32_e64 v3, v40, v3, s[66:67]\n\t"
        "v_cndmask_b32_e64 v4, v40, v4, s[68:69]\n\t"
        "v_cndmask_b32_e64 v5, v40, v5, s[70:71]\n\t"
        "v_cndmask_b32_e64 v6, v40, v6, s[72:73]\n\t"
        "v_cndmask_b32_e64 v7, v40, v7, s[74:75]\n\t"
        "s_add_u32 m0, s36, 8192\n\t"
        "s_add_u32 s34, s37, 12771328\n\t"
        "buffer_load_dwordx4 v122, s[12:15], s34 offen sc1 lds\n\t"
        "v_max3_f32 v42, v0, v1, v2\n\t"
        "v_max3_f32 v42, v42, v3, v4\n\t"
        "v_max3_f32 v42, v42, v5, v6\n\t"
        "v_max3_f32 v42, v42, v7, v8\n\t"
        "v_max3_f32 v42, v42, v9, v10\n\t"
        "v_mov_b32_e32 v43, v11\n\t"
        "v_max_f32_e32 v44, v42, v43\n\t"
        "v_max_f32_e32 v42, v42, v43\n\t"
        "s_pack_ll_b32_b16 s34, s52, s24\n\t"
        "v_mov_b32_e32 v40, s34\n\t"
        "s_add_u32 s35, s26, s57\n\t"
        "v_permlane16_swap_b32_e32 v42, v44\n\t"
        "v_max_f32_e32 v45, v42, v44\n\t"
        "v_max_f32_e32 v42, v42, v44\n\t"
        "s_mov_b64 exec, 1\n\t"
        "buffer_store_dword v40, off, s[8:11], s35 sc1\n\t"
        "s_mov_b64 exec, -1\n\t"
        "v_permlane32_swap_b32_e32 v42, v45\n\t"
        "v_max_f32_e32 v42, v42, v45\n\t"
        "v_mul_f32_e32 v42, 0xbfb8aa3b, v42\n\t"
        "v_pk_fma_f32 v[0:1], v[0:1], v[46:47], v[42:43] op_sel:[0,1,0] op_sel_hi:[1,1,0]\n\t"
        "s_add_u32 m0, s36, 9216\n\t"
        "s_add_u32 s34, s37, 12772352\n\t"
        "buffer_load_dwordx4 v122, s[12:15], s34 offen sc1 lds\n\t"
        "v_exp_f32_e32 v0, v0\n\t"
        "v_exp_f32_e32 v1, v1\n\t"
        "v_pk_fma_f32 v[2:3], v[2:3], v[46:47], v[42:43] op_sel:[0,1,0] op_sel_hi:[1,1,0]\n\t"
        "v_exp_f32_e32 v2, v2\n\t"
        "v_exp_f32_e32 v3, v3\n\t"
        "v_pk_fma_f32 v[4:5], v[4:5], v[46:47], v[42:43] op_sel:[0,1,0] op_sel_hi:[1,1,0]\n\t"
        "v_exp_f32_e32 v4, v4\n\t"
        "s_add_u32 m0, s36, 16384\n\t"
        "s_add_u32 s34, s37, 46325760\n\t"
        "buffer_load_dwordx4 v122, s[12:15], s34 offen sc1 lds\n\t"
        "v_exp_f32_e32 v5, v5\n\t"
        "v_pk_fma_f32 v[6:7], v[6:7], v[46:47], v[42:43] op_sel:[0,1,0] op_sel_hi:[1,1,0]\n\t"
        "v_exp_f32_e32 v6, v6\n\t"
        "v_exp_f32_e32 v7, v7\n\t"
        "v_pk_fma_f32 v[8:9], v[8:9], v[46:47], v[42:43] op_sel:[0,1,0] op_sel_hi:[1,1,0]\n\t"
        "v_exp_f32_e32 v8, v8\n\t"
        "v_exp_f32_e32 v9, v9\n\t"
        "s_add_u32 m0, s36, 17408\n\t"
        "s_add_u32 s34, s37, 46326784\n\t"
        "buffer_load_dwordx4 v122, s[12:15], s34 offen sc1 lds\n\t"
        "v_pk_fma_f32 v[10:11], v[10:11], v[46:47], v[42:43] op_sel:[0,1,0] op_sel_hi:[1,1,0]\n\t"
        "v_exp_f32_e32 v10, v10\n\t"
        "v_exp_f32_e32 v11, v11\n\t"
        "v_pk_add_f32 v[42:43], v[0:1], v[2:3]\n\t"
        "v_cndmask_b32_e64 v8, v8, 0, s[40:41]\n\t"
        "v_cndmask_b32_e64 v9, v9, 0, s[40:41]\n\t"
        "v_cndmask_b32_e64 v10, v10, 0, s[40:41]\n\t"
        "buffer_store_dwordx4 v[36:39], v121, s[8:11], s28 offen sc1\n\t"
        "v_cndmask_b32_e64 v11, v11, 0, s[40:41]\n\t"
        "v_pk_add_f32 v[42:43], v[42:43], v[4:5]\n\t"
        "v_pk_add_f32 v[42:43], v[42:43], v[6:7]\n\t"
        "v_pk_add_f32 v[42:43], v[42:43], v[8:9]\n\t"
        "v_pk_add_f32 v[42:43], v[42:43], v[10:11]\n\t"
        "v_add_f32_e32 v44, v42, v43\n\t"
        "v_add_f32_e32 v42, v42, v43\n\t"
        "s_lshl_b32 s34, s24, 18\n\t"
        "s_add_u32 s34, s34, 63102976\n\t"
        "v_permlane16_swap_b32_e32 v42, v44\n\t"
        "v_add_f32_e32 v45, v42, v44\n\t"
        "v_add_f32_e32 v42, v42, v44\n\t"
        "s_add_u32 s35, s24, 1\n\t"
        "v_mov_b32_e32 v126, s35\n\t"
        "v_permlane32_swap_b32_e32 v42, v45\n\t"
        "v_add_f32_e32 v42, v42, v45\n\t"
        "v_rcp_f32_e32 v42, v42\n\t"
        "s_waitcnt vmcnt(8) lgkmcnt(0)\n\t"
        "v_max3_u32 v45, v12, v13, v14\n\t"
        "v_max3_u32 v45, v45, v15, v104\n\t"
        "v_max3_u32 v45, v45, v105, v106\n\t"
        "v_max_u32_e32 v45, v45, v107\n\t"
        "v_cmp_eq_u32_e32 vcc, -1, v45\n\t"
        "s_nop 0\n\t"
        "s_cbranch_vccnz LA_v2fail%=\n\t"
        "LA_v2ok%=:\n\t"
        "v_and_b32_e32 v45, 0xff, v109\n\t"
        "v_cmp_ne_u32_e32 vcc, s24, v45\n\t"
        "s_nop 0\n\t"
        "s_cbranch_vccnz LA_stre%=\n\t"
        "LA_stok%=:\n\t"
        "v_pk_mul_f32 v[0:1], v[0:1], v[42:43] op_sel_hi:[1,0]\n\t"
        "v_subrev_u32_e32 v108, s25, v108\n\t"
        "v_min_u32_dpp v111, v111, v111 quad_perm:[1,0,3,2] row_mask:0xf bank_mask:0xf\n\t"
        "v_cvt_pk_bf16_f32 v32, v0, v1\n\t"
        "v_pk_mul_f32 v[2:3], v[2:3], v[42:43] op_sel_hi:[1,0]\n\t"
        "v_cvt_pk_bf16_f32 v33, v2, v3\n\t"
        "v_max_i32_dpp v108, v108, v108 quad_perm:[1,0,3,2] row_mask:0xf bank_mask:0xf\n\t"
        "v_min_u32_dpp v111, v111, v111 quad_perm:[2,3,0,1] row_mask:0xf bank_mask:0xf\n\t"
        "v_pk_mul_f32 v[4:5], v[4:5], v[42:43] op_sel_hi:[1,0]\n\t"
        "v_cvt_pk_bf16_f32 v34, v4, v5\n\t"
        "v_pk_mul_f32 v[6:7], v[6:7], v[42:43] op_sel_hi:[1,0]\n\t"
        "v_max_i32_dpp v108, v108, v108 quad_perm:[2,3,0,1] row_mask:0xf bank_mask:0xf\n\t"
        "v_min_u32_dpp v111, v111, v111 row_ror:4 row_mask:0xf bank_mask:0xf\n\t"
        "v_cvt_pk_bf16_f32 v35, v6, v7\n\t"
        "v_pk_mul_f32 v[8:9], v[8:9], v[42:43] op_sel_hi:[1,0]\n\t"
        "v_cvt_pk_bf16_f32 v36, v8, v9\n\t"
        "v_max_i32_dpp v108, v108, v108 row_ror:4 row_mask:0xf bank_mask:0xf\n\t"
        "v_min_u32_dpp v111, v111, v111 row_ror:8 row_mask:0xf bank_mask:0xf\n\t"
        "v_pk_mul_f32 v[10:11], v[10:11], v[42:43] op_sel_hi:[1,0]\n\t"
        "v_cvt_pk_bf16_f32 v37, v10, v11\n\t"
        "v_mfma_f32_16x16x32_bf16 v[16:19], v[48:51], v[32:35], 0\n\t"
        "v_max_i32_dpp v108, v108, v108 row_ror:8 row_mask:0xf bank_mask:0xf\n\t"
        "v_mfma_f32_16x16x32_bf16 v[20:23], v[52:55], v[32:35], 0\n\t"
        "v_mfma_f32_16x16x32_bf16 v[24:27], v[56:59], v[32:35], 0\n\t"
        "v_mfma_f32_16x16x32_bf16 v[28:31], v[60:63], v[32:35], 0\n\t"
        "v_max_i32_dpp v108, v108, v108 row_bcast:15 row_mask:0xa bank_mask:0xf\n\t"
        "v_xor_b32_e32 v45, 0x400, v123\n\t"
        "v_add_u32_e32 v124, s56, v124\n\t"
        "v_mfma_f32_16x16x16_bf16 v[16:19], v[104:105], v[36:37], v[16:19]\n\t"
        "v_max_i32_dpp v108, v108, v108 row_bcast:31 row_mask:0xc bank_mask:0xf\n\t"
        "v_mfma_f32_16x16x16_bf16 v[20:23], v[106:107], v[36:37], v[20:23]\n\t"
        "v_mfma_f32_16x16x16_bf16 v[24:27], v[12:13], v[36:37], v[24:27]\n\t"
        "v_mfma_f32_16x16x16_bf16 v[28:31], v[14:15], v[36:37], v[28:31]\n\t"
        "ds_read_b128 v[48:51], v117 offset:4096\n\t"
        "ds_read_b128 v[52:55], v117 offset:5120\n\t"
        "ds_read_b128 v[56:59], v117 offset:6144\n\t"
        "ds_read_b128 v[60:63], v117 offset:7168\n\t"
        "s_sub_u32 s56, 0, s56\n\t"
        "v_cvt_pk_bf16_f32 v0, v16, v17\n\t"
        "v_cvt_pk_bf16_f32 v1, v18, v19\n\t"
        "v_cvt_pk_bf16_f32 v2, v20, v21\n\t"
        "v_cvt_pk_bf16_f32 v3, v22, v23\n\t"
        "buffer_store_dwordx4 v[0:3], v123, s[12:15], s34 offen sc1\n\t"
        "v_cvt_pk_bf16_f32 v4, v24, v25\n\t"
        "v_cvt_pk_bf16_f32 v5, v26, v27\n\t"
        "v_cvt_pk_bf16_f32 v6, v28, v29\n\t"
        "v_cvt_pk_bf16_f32 v7, v30, v31\n\t"
        "buffer_store_dwordx4 v[4:7], v45, s[12:15], s34 offen sc1\n\t"
        "s_cmp_eq_u32 s24, 63\n\t"
        "s_cbranch_scc1 LA_last%=\n\t"
        "ds_read_b128 v[32:35], v117 offset:0\n\t"
        "ds_read_b128 v[36:39], v117 offset:1024\n\t"
        "ds_read_b128 v[40:43], v117 offset:2048\n\t"
        "ds_read_b128 v[44:47], v117 offset:3072\n\t"
        "v_mov_b64_e32 v[14:15], 0\n\t"
        "v_mov_b64_e32 v[12:13], 0\n\t"
        "v_mov_b64_e32 v[10:11], 0\n\t"
        "v_mov_b64_e32 v[8:9], 0\n\t"
        "v_mov_b64_e32 v[6:7], 0\n\t"
        "v_mov_b64_e32 v[4:5], 0\n\t"
        "v_mov_b64_e32 v[2:3], 0\n\t"
        "v_mov_b64_e32 v[0:1], 0\n\t"
        "v_readfirstlane_b32 s54, v111\n\t"
        "v_readfirstlane_b32 s35, v109\n\t"
        "v_readlane_b32 s36, v108, 63\n\t"
        "s_bfe_u32 s29, s35, 0xc0008\n\t"
        "s_lshr_b32 s30, s35, 20\n\t"
        "s_add_u32 s35, s31, s50\n\t"
        "s_mul_i32 s37, s50, 3\n\t"
        "s_min_u32 s35, s35, s37\n\t"
        "s_add_i32 s34, s36, s51\n\t"
        "s_cmp_gt_i32 s34, 0\n\t"
        "s_cselect_b32 s31, s35, 0\n\t"
        "s_cselect_b32 s34, s34, 0\n\t"
        "s_add_u32 s34, s34, s31\n\t"
        "s_add_u32 s37, s25, s34\n\t"
        "s_add_u32 s45, s37, s29\n\t"
        "s_add_u32 s25, s45, s30\n\t"
        "s_add_u32 s24, s24, 1\n\t"
        "s_mov_b32 s28, s26\n\t"
        "s_mov_b32 s26, s27\n\t"
        "s_add_u32 s27, s27, 0x80000\n\t"
        "s_and_b32 s27, s27, 0x1fffff\n\t"
        "s_branch LA_loop%=\n\t"
        "LA_last%=:\n\t"
        "s_waitcnt vmcnt(0)\n\t"
        "s_barrier\n\t"
        "buffer_store_dword v126, v127, s[12:15], 0 offen sc1\n\t"
        "s_endpgm\n\t"
        "LA_skip%=:\n\t"
        "buffer_store_dword v126, v127, s[12:15], 0 offen sc1\n\t"
        "ds_read_b64_tr_b16 v[32:33], v119 offset:2112\n\t"
        "ds_read_b64_tr_b16 v[34:35], v119 offset:2144\n\t"
        "ds_read_b64_tr_b16 v[36:37], v119 offset:2368\n\t"
        "ds_read_b64_tr_b16 v[38:39], v119 offset:2400\n\t"
        "ds_read_b64_tr_b16 v[40:41], v119 offset:2624\n\t"
        "ds_read_b64_tr_b16 v[42:43], v119 offset:2656\n\t"
        "ds_read_b64_tr_b16 v[44:45], v119 offset:2880\n\t"
        "ds_read_b64_tr_b16 v[46:47], v119 offset:2912\n\t"
        "ds_read_b128 v[48:51], v108 offset:16384\n\t"
        "ds_read_b128 v[52:55], v108 offset:17408\n\t"
        "s_waitcnt lgkmcnt(0)\n\t"
        "s_branch LA_x%=\n\t"
        "LA_stre%=:\n\t"
        "s_mov_b32 s42, 0\n\t"
        "LA_str2%=:\n\t"
        "buffer_load_dwordx2 v[108:109], v125, s[8:11], s26 offen sc1\n\t"
        "s_waitcnt vmcnt(0)\n\t"
        "v_and_b32_e32 v45, 0xff, v109\n\t"
        "v_cmp_ne_u32_e32 vcc, s24, v45\n\t"
        "s_nop 0\n\t"
        "s_cbranch_vccz LA_stok%=\n\t"
        "s_add_u32 s42, s42, 1\n\t"
        "s_cmp_lt_u32 s42, 0x100000\n\t"
        "s_cbranch_scc1 LA_str2%=\n\t"
        "s_movk_i32 s34, 5\n\t"
        "s_branch LA_die%=\n\t"
        "LA_v2fail%=:\n\t"
        "s_mov_b32 s42, 0\n\t"
        "LA_v2re%=:\n\t"
        "v_xor_b32_e32 v45, 0x200, v120\n\t"
        "buffer_load_dwordx4 v[12:15], v45, s[8:11], s26 offen sc1\n\t"
        "buffer_load_dwordx4 v[104:107], v120, s[8:11], s26 offen sc1\n\t"
        "s_waitcnt vmcnt(0)\n\t"
        "v_max3_u32 v45, v12, v13, v14\n\t"
        "v_max3_u32 v45, v45, v15, v104\n\t"
        "v_max3_u32 v45, v45, v105, v106\n\t"
        "v_max_u32_e32 v45, v45, v107\n\t"
        "v_cmp_eq_u32_e32 vcc, -1, v45\n\t"
        "s_nop 0\n\t"
        "s_cbranch_vccz LA_v2ok%=\n\t"
        "s_add_u32 s42, s42, 1\n\t"
        "s_cmp_lt_u32 s42, 0x100000\n\t"
        "s_cbranch_scc1 LA_v2re%=\n\t"
        "s_movk_i32 s34, 6\n\t"
        "s_branch LA_die%=\n\t"
        "LA_rdfail%=:\n\t"
        "s_or_b32 s47, s47, 1\n\t"
        "buffer_load_dwordx4 v[16:19], v112, s[8:11], s26 offen offset:0 sc1\n\t"
        "buffer_load_dwordx4 v[20:23], v112, s[8:11], s26 offen offset:1024 sc1\n\t"
        "buffer_load_dwordx4 v[24:27], v112, s[8:11], s26 offen offset:2048 sc1\n\t"
        "buffer_load_dwordx4 v[28:31], v112, s[8:11], s26 offen offset:3072 sc1\n\t"
        "s_nop 7\n\t"
        "v_mov_b64_e32 v[0:1], 0\n\t"
        "v_mov_b64_e32 v[2:3], 0\n\t"
        "v_mov_b64_e32 v[4:5], 0\n\t"
        "v_mov_b64_e32 v[6:7], 0\n\t"
        "v_mov_b64_e32 v[8:9], 0\n\t"
        "v_mov_b64_e32 v[10:11], 0\n\t"
        "v_mov_b64_e32 v[12:13], 0\n\t"
        "v_mov_b64_e32 v[14:15], 0\n\t"
        "s_branch LA_rdre%=w\n\t"
        "LA_rdre%=:\n\t"
        "buffer_load_dwordx4 v[16:19], v112, s[8:11], s26 offen offset:0 sc1\n\t"
        "buffer_load_dwordx4 v[20:23], v112, s[8:11], s26 offen offset:1024 sc1\n\t"
        "buffer_load_dwordx4 v[24:27], v112, s[8:11], s26 offen offset:2048 sc1\n\t"
        "buffer_load_dwordx4 v[28:31], v112, s[8:11], s26 offen offset:3072 sc1\n\t"
        "LA_rdre%=w:\n\t"
        "s_waitcnt vmcnt(0) lgkmcnt(0)\n\t"
        "v_max3_u32 v111, v16, v17, v18\n\t"
        "v_max3_u32 v111, v111, v19, v20\n\t"
        "v_max3_u32 v111, v111, v21, v22\n\t"
        "v_max3_u32 v111, v111, v23, v24\n\t"
        "v_max3_u32 v111, v111, v25, v26\n\t"
        "v_max3_u32 v111, v111, v27, v28\n\t"
        "v_max3_u32 v111, v111, v29, v30\n\t"
        "v_max_u32_e32 v111, v111, v31\n\t"
        "v_cmp_eq_u32_e32 vcc, -1, v111\n\t"
        "s_nop 0\n\t"
        "s_cbranch_vccz LA_go%=\n\t"
        "s_add_u32 s42, s42, 1\n\t"
        "s_cmp_lt_u32 s42, 0x100000\n\t"
        "s_cbranch_scc1 LA_rdre%=\n\t"
        "s_movk_i32 s34, 1\n\t"
        "s_branch LA_die%=\n\t"
        "LA_f2%=:\n\t"
        "s_or_b32 s47, s47, 1\n\t"
        "buffer_load_dwordx4 v[24:27], v112, s[8:11], s26 offen offset:2048 sc1\n\t"
        "s_nop 7\n\t"
        "s_branch LA_r2%=w\n\t"
        "LA_r2%=:\n\t"
        "buffer_load_dwordx4 v[24:27], v112, s[8:11], s26 offen offset:2048 sc1\n\t"
        "LA_r2%=w:\n\t"
        "s_waitcnt vmcnt(0) lgkmcnt(0)\n\t"
        "v_max3_u32 v111, v24, v25, v26\n\t"
        "v_max_u32_e32 v111, v111, v27\n\t"
        "v_cmp_eq_u32_e32 vcc, -1, v111\n\t"
        "s_nop 0\n\t"
        "s_cbranch_vccz LA_go%=\n\t"
        "s_add_u32 s42, s42, 1\n\t"
        "s_cmp_lt_u32 s42, 0x100000\n\t"
        "s_cbranch_scc1 LA_r2%=\n\t"
        "s_movk_i32 s34, 1\n\t"
        "s_branch LA_die%=\n\t"
        "LA_f3%=:\n\t"
        "s_or_b32 s47, s47, 1\n\t"
        "buffer_load_dwordx4 v[28:31], v112, s[8:11], s26 offen offset:3072 sc1\n\t"
        "s_nop 7\n\t"
        "s_branch LA_r3%=w\n\t"
        "LA_r3%=:\n\t"
        "buffer_load_dwordx4 v[28:31], v112, s[8:11], s26 offen offset:3072 sc1\n\t"
        "LA_r3%=w:\n\t"
        "s_waitcnt vmcnt(0) lgkmcnt(0)\n\t"
        "v_max3_u32 v111, v28, v29, v30\n\t"
        "v_max_u32_e32 v111, v111, v31\n\t"
        "v_cmp_eq_u32_e32 vcc, -1, v111\n\t"
        "s_nop 0\n\t"
        "s_cbranch_vccz LA_go%=\n\t"
        "s_add_u32 s42, s42, 1\n\t"
        "s_cmp_lt_u32 s42, 0x100000\n\t"
        "s_cbranch_scc1 LA_r3%=\n\t"
        "s_movk_i32 s34, 1\n\t"
        "s_branch LA_die%=\n\t"
        "LA_vmfail%=:\n\t"
        "s_or_b32 s47, s47, 2\n\t"
        "s_mov_b32 s42, 0\n\t"
        "LA_vmre%=:\n\t"
        "buffer_load_dwordx4 v[104:107], v120, s[8:11], s26 offen sc1\n\t"
        "v_mfma_f32_16x16x32_bf16 v[4:7], v[48:51], v[12:15], 0\n\t"
        "v_mfma_f32_16x16x32_bf16 v[8:11], v[52:55], v[12:15], 0\n\t"
        "s_nop 7\n\t"
        "s_waitcnt vmcnt(0)\n\t"
        "v_mfma_f32_16x16x16_bf16 v[4:7], v[104:105], v[56:57], v[4:7]\n\t"
        "v_mfma_f32_16x16x16_bf16 v[8:11], v[106:107], v[56:57], v[8:11]\n\t"
        "v_max3_u32 v111, v104, v105, v106\n\t"
        "v_max_u32_e32 v111, v111, v107\n\t"
        "v_cmp_eq_u32_e32 vcc, -1, v111\n\t"
        "s_nop 7\n\t"
        "s_cbranch_vccz LA_vmok%=\n\t"
        "s_add_u32 s42, s42, 1\n\t"
        "s_cmp_lt_u32 s42, 0x100000\n\t"
        "s_cbranch_scc1 LA_vmre%=\n\t"
        "s_movk_i32 s34, 2\n\t"
        "s_branch LA_die%=\n\t"
        "LA_die%=:\n\t"
        "v_mov_b32_e32 v30, s34\n\t"
        "s_mov_b32 s35, 33024\n\t"
        "s_mov_b64 exec, 1\n\t"
        "buffer_store_dword v30, off, s[12:15], s35 sc1\n\t"
        "s_endpgm\n\t"
        ".Lk_end_A:\n\t"
        :: REC_S_IN, "{v96}"(g_w), "{v97}"(wb0), "{v98}"(wb1), "{v99}"(rb0), "{v100}"(rb0 ^ 32u), "{v101}"(rb0 ^ 64u), "{v102}"(rb0 ^ 96u),
           "{v112}"(a_mem), "{v113}"(l_ms + w * 4096), "{v114}"(l_ms + ((w + 1) & 3) * 4096), "{v115}"(l_ms + ((w + 2) & 3) * 4096), "{v116}"(l_ms + ((w + 3) & 3) * 4096),
           "{v117}"(lb + w * 22528 + l * 16), "{v118}"(l_iw), "{v119}"(l_iq), "{v120}"(a_vm), "{v121}"(a_o), "{v122}"(h * 8192 + l * 16), "{v123}"(a_ox), "{v124}"(lb + L_KV + l * 16), "{s55}"(lb + L_KV + w * 2048),
           "{v125}"((24 + cp) * 16384 + l * ST_P), "{v126}"(16 * (w & 1) + n - 4 * g), "{v103}"((l & 1) ? 0xEEEEEEEEu : 0x44444444u),
           "{v127}"((l == 0 && w == 0) ? F_PROG + h * 128 : OOB)
        : "memory", "vcc", "scc", "v0", "v255", "a0", "a255", "s101");
    __builtin_unreachable();
}
__device__ __forceinline__ void rec_b(const Params& p, unsigned lb, const int wv, const int id, const int rg, const int cp, const int rot, const int bq) {
    const int w = wv, l = otid() & 63, n = l & 15, g = l >> 4, cs = id, rb = 32 + id * 2 + rg;
    const v4u sh = mk_srd(p.ws + hx_base(p.ws), HX_BYTES - 8192), sw = mk_srd(p.ws, W_TOTAL), s1 = mk_srd(p.Wv, 1024 * 4096 * 2), s2 = mk_srd(p.Wo, 1024 * 4096 * 2);
    const int r8 = l >> 3, pc = l & 7, pp = n >> 3, bb = (n >> 2) & 1, f = (g & 1) | (pp << 1);
    const unsigned stg = lb + LB_STG + w * LB_STGP;
    const unsigned g_v = r8 * 8192 + cs * 256 + rg * 128 + pc * 16, g_o = (l >> 2) * 2048 + cs * 64 + (l & 3) * 16;
    const unsigned wb0 = stg + r8 * 128 + (((pc >> 1) ^ (r8 >> 2)) << 5) + (pc & 1) * 16, wb1 = wb0 ^ 64u;
    const unsigned rb0 = stg + (16 * (g >> 1) + 8 * pp + 4 * (g & 1) + bb) * 128 + (f << 5) + (n & 3) * 8;
    const unsigned o_r = stg + (g >> 1) * (2048 + 128) + (g & 1) * (1024 + 32) + (bb + 4 * pp) * 64 + (n & 3) * 8;
    const unsigned a_mem = cp * 32768 + (w >> 1) * 16384 + (w & 1) * 4096 + l * 16;
    const unsigned a_vmw = w < 2 ? (16 + (cs >> 2)) * 16384 + (cs & 3) * 2048 + (2 * rg + w) * 512 + n * 32 + (g & 1) * 16 + (g >> 1) * 8 : OOB;
    const unsigned a_or = (8 + rg * 4 + w) * 16384 + l * 16;
    const unsigned a_mw = (RPL != 0 && RPL != 4 && w == 3) ? OOB : w * 32768 + (cs >> 4) * 16384 + ((cs >> 1) & 7) * 1024 + (cs & 1) * 512 + (l >> 5) * 256 + rg * 128 + (l & 31) * 4;
    const unsigned a_sw = (w == 0 && l < 4) ? (24 + l) * 16384 + (id * 2 + rg) * ST_P : OOB;
    const bf16_t* m0p = p.mem0 + (4 * rg + ((l >> 3) & 3)) * 1024 + cs * 32 + (l >> 5) * 16 + (l & 7) * 2;
    asm volatile(
        "global_load_dword v109, v[108:109], off\n\t"
        "s_add_u32 s35, s68, 0\n\t"
        "s_and_b32 s35, s35, 7\n\t"
        "s_lshl_b32 s69, s35, 10\n\t"
        "s_add_u32 s35, s68, 2\n\t"
        "s_and_b32 s35, s35, 7\n\t"
        "s_lshl_b32 s70, s35, 10\n\t"
        "s_add_u32 s35, s68, 4\n\t"
        "s_and_b32 s35, s35, 7\n\t"
        "s_lshl_b32 s71, s35, 10\n\t"
        "s_add_u32 s35, s68, 6\n\t"
        "s_and_b32 s35, s35, 7\n\t"
        "s_lshl_b32 s72, s35, 10\n\t"
        "s_lshl_b32 s34, s43, 21\n\t"
        "s_add_u32 s34, s34, 0\n\t"
        "buffer_load_dwordx4 v[128:131], v96, s[16:19], s34 offen nt\n\t"
        "s_add_u32 s34, s34, 0x10000\n\t"
        "buffer_load_dwordx4 v[132:135], v96, s[16:19], s34 offen nt\n\t"
        "s_add_u32 s34, s34, 0x10000\n\t"
        "buffer_load_dwordx4 v[136:139], v96, s[16:19], s34 offen nt\n\t"
        "s_add_u32 s34, s34, 0x10000\n\t"
        "buffer_load_dwordx4 v[140:143], v96, s[16:19], s34 offen nt\n\t"
        "s_add_u32 s34, s34, 0x10000\n\t"
        "buffer_load_dwordx4 v[144:147], v96, s[16:19], s34 offen nt\n\t"
        "s_add_u32 s34, s34, 0x10000\n\t"
        "buffer_load_dwordx4 v[148:151], v96, s[16:19], s34 offen nt\n\t"
        "s_add_u32 s34, s34, 0x10000\n\t"
        "buffer_load_dwordx4 v[152:155], v96, s[16:19], s34 offen nt\n\t"
        "s_add_u32 s34, s34, 0x10000\n\t"
        "buffer_load_dwordx4 v[156:159], v96, s[16:19], s34 offen nt\n\t"
        "s_lshl_b32 s34, s43, 21\n\t"
        "s_add_u32 s34, s34, 524288\n\t"
        "buffer_load_dwordx4 v[160:163], v96, s[16:19], s34 offen nt\n\t"
        "s_add_u32 s34, s34, 0x10000\n\t"
        "buffer_load_dwordx4 v[164:167], v96, s[16:19], s34 offen nt\n\t"
        "s_add_u32 s34, s34, 0x10000\n\t"
        "buffer_load_dwordx4 v[168:171], v96, s[16:19], s34 offen nt\n\t"
        "s_add_u32 s34, s34, 0x10000\n\t"
        "buffer_load_dwordx4 v[172:175], v96, s[16:19], s34 offen nt\n\t"
        "s_add_u32 s34, s34, 0x10000\n\t"
        "buffer_load_dwordx4 v[176:179], v96, s[16:19], s34 offen nt\n\t"
        "s_add_u32 s34, s34, 0x10000\n\t"
        "buffer_load_dwordx4 v[180:183], v96, s[16:19], s34 offen nt\n\t"
        "s_add_u32 s34, s34, 0x10000\n\t"
        "buffer_load_dwordx4 v[184:187], v96, s[16:19], s34 offen nt\n\t"
        "s_add_u32 s34, s34, 0x10000\n\t"
        "buffer_load_dwordx4 v[188:191], v96, s[16:19], s34 offen nt\n\t"
        "s_lshl_b32 s34, s43, 21\n\t"
        "s_add_u32 s34, s34, 1048576\n\t"
        "buffer_load_dwordx4 v[192:195], v96, s[16:19], s34 offen nt\n\t"
        "s_add_u32 s34, s34, 0x10000\n\t"
        "buffer_load_dwordx4 v[196:199], v96, s[16:19], s34 offen nt\n\t"
        "s_add_u32 s34, s34, 0x10000\n\t"
        "buffer_load_dwordx4 v[200:203], v96, s[16:19], s34 offen nt\n\t"
        "s_add_u32 s34, s34, 0x10000\n\t"
        "buffer_load_dwordx4 v[204:207], v96, s[16:19], s34 offen nt\n\t"
        "s_add_u32 s34, s34, 0x10000\n\t"
        "buffer_load_dwordx4 v[208:211], v96, s[16:19], s34 offen nt\n\t"
        "s_add_u32 s34, s34, 0x10000\n\t"
        "buffer_load_dwordx4 v[212:215], v96, s[16:19], s34 offen nt\n\t"
        "s_add_u32 s34, s34, 0x10000\n\t"
        "buffer_load_dwordx4 v[216:219], v96, s[16:19], s34 offen nt\n\t"
        "s_add_u32 s34, s34, 0x10000\n\t"
        "buffer_load_dwordx4 v[220:223], v96, s[16:19], s34 offen nt\n\t"
        "s_waitcnt vmcnt(16)\n\t"
        "ds_write_b128 v98, v[128:131] offset:0\n\t"
        "ds_write_b128 v99, v[132:135] offset:1024\n\t"
        "ds_write_b128 v98, v[136:139] offset:2048\n\t"
        "ds_write_b128 v99, v[140:143] offset:3072\n\t"
        "ds_write_b128 v98, v[144:147] offset:4096\n\t"
        "ds_write_b128 v99, v[148:151] offset:5120\n\t"
        "ds_write_b128 v98, v[152:155] offset:6144\n\t"
        "ds_write_b128 v99, v[156:159] offset:7168\n\t"
        "ds_read_b64_tr_b16 v[128:129], v100 offset:0\n\t"
        "ds_read_b64_tr_b16 v[130:131], v100 offset:256\n\t"
        "ds_read_b64_tr_b16 v[132:133], v100 offset:4096\n\t"
        "ds_read_b64_tr_b16 v[134:135], v100 offset:4352\n\t"
        "s_waitcnt lgkmcnt(8)\n\t"
        "ds_read_b64_tr_b16 v[136:137], v101 offset:0\n\t"
        "ds_read_b64_tr_b16 v[138:139], v101 offset:256\n\t"
        "ds_read_b64_tr_b16 v[140:141], v101 offset:4096\n\t"
        "ds_read_b64_tr_b16 v[142:143], v101 offset:4352\n\t"
        "s_waitcnt lgkmcnt(8)\n\t"
        "ds_read_b64_tr_b16 v[144:145], v102 offset:0\n\t"
        "ds_read_b64_tr_b16 v[146:147], v102 offset:256\n\t"
        "ds_read_b64_tr_b16 v[148:149], v102 offset:4096\n\t"
        "ds_read_b64_tr_b16 v[150:151], v102 offset:4352\n\t"
        "s_waitcnt lgkmcnt(8)\n\t"
        "ds_read_b64_tr_b16 v[152:153], v103 offset:0\n\t"
        "ds_read_b64_tr_b16 v[154:155], v103 offset:256\n\t"
        "ds_read_b64_tr_b16 v[156:157], v103 offset:4096\n\t"
        "ds_read_b64_tr_b16 v[158:159], v103 offset:4352\n\t"
        "s_lshl_b32 s34, s43, 21\n\t"
        "s_add_u32 s34, s34, 1572864\n\t"
        "buffer_load_dwordx4 v[224:227], v96, s[16:19], s34 offen nt\n\t"
        "s_add_u32 s34, s34, 0x10000\n\t"
        "buffer_load_dwordx4 v[228:231], v96, s[16:19], s34 offen nt\n\t"
        "s_add_u32 s34, s34, 0x10000\n\t"
        "buffer_load_dwordx4 v[232:235], v96, s[16:19], s34 offen nt\n\t"
        "s_add_u32 s34, s34, 0x10000\n\t"
        "buffer_load_dwordx4 v[236:239], v96, s[16:19], s34 offen nt\n\t"
        "s_add_u32 s34, s34, 0x10000\n\t"
        "buffer_load_dwordx4 v[240:243], v96, s[16:19], s34 offen nt\n\t"
        "s_add_u32 s34, s34, 0x10000\n\t"
        "buffer_load_dwordx4 v[244:247], v96, s[16:19], s34 offen nt\n\t"
        "s_add_u32 s34, s34, 0x10000\n\t"
        "buffer_load_dwordx4 v[248:251], v96, s[16:19], s34 offen nt\n\t"
        "s_add_u32 s34, s34, 0x10000\n\t"
        "buffer_load_dwordx4 v[252:255], v96, s[16:19], s34 offen nt\n\t"
        "s_waitcnt lgkmcnt(0)\n\t"
        "s_waitcnt vmcnt(16)\n\t"
        "ds_write_b128 v98, v[160:163] offset:0\n\t"
        "ds_write_b128 v99, v[164:167] offset:1024\n\t"
        "ds_write_b128 v98, v[168:171] offset:2048\n\t"
        "ds_write_b128 v99, v[172:175] offset:3072\n\t"
        "ds_write_b128 v98, v[176:179] offset:4096\n\t"
        "ds_write_b128 v99, v[180:183] offset:5120\n\t"
        "ds_write_b128 v98, v[184:187] offset:6144\n\t"
        "ds_write_b128 v99, v[188:191] offset:7168\n\t"
        "ds_read_b64_tr_b16 v[160:161], v100 offset:0\n\t"
        "ds_read_b64_tr_b16 v[162:163], v100 offset:256\n\t"
        "ds_read_b64_tr_b16 v[164:165], v100 offset:4096\n\t"
        "ds_read_b64_tr_b16 v[166:167], v100 offset:4352\n\t"
        "s_waitcnt lgkmcnt(8)\n\t"
        "ds_read_b64_tr_b16 v[168:169], v101 offset:0\n\t"
        "ds_read_b64_tr_b16 v[170:171], v101 offset:256\n\t"
        "ds_read_b64_tr_b16 v[172:173], v101 offset:4096\n\t"
        "ds_read_b64_tr_b16 v[174:175], v101 offset:4352\n\t"
        "s_waitcnt lgkmcnt(8)\n\t"
        "ds_read_b64_tr_b16 v[176:177], v102 offset:0\n\t"
        "ds_read_b64_tr_b16 v[178:179], v102 offset:256\n\t"
        "ds_read_b64_tr_b16 v[180:181], v102 offset:4096\n\t"
        "ds_read_b64_tr_b16 v[182:183], v102 offset:4352\n\t"
        "s_waitcnt lgkmcnt(8)\n\t"
        "ds_read_b64_tr_b16 v[184:185], v103 offset:0\n\t"
        "ds_read_b64_tr_b16 v[186:187], v103 offset:256\n\t"
        "ds_read_b64_tr_b16 v[188:189], v103 offset:4096\n\t"
        "ds_read_b64_tr_b16 v[190:191], v103 offset:4352\n\t"
        "s_add_u32 s35, s68, 0\n\t"
        "s_and_b32 s35, s35, 7\n\t"
        "s_lshl_b32 s35, s35, 18\n\t"
        "s_lshl_b32 s34, s43, 21\n\t"
        "s_add_u32 s34, s34, s35\n\t"
        "buffer_load_dwordx4 a[0:3], v97, s[20:23], s34 offen\n\t"
        "s_add_u32 s34, s34, 0x8000\n\t"
        "buffer_load_dwordx4 a[4:7], v97, s[20:23], s34 offen\n\t"
        "s_add_u32 s34, s34, 0x8000\n\t"
        "buffer_load_dwordx4 a[8:11], v97, s[20:23], s34 offen\n\t"
        "s_add_u32 s34, s34, 0x8000\n\t"
        "buffer_load_dwordx4 a[12:15], v97, s[20:23], s34 offen\n\t"
        "s_add_u32 s34, s34, 0x8000\n\t"
        "buffer_load_dwordx4 a[16:19], v97, s[20:23], s34 offen\n\t"
        "s_add_u32 s34, s34, 0x8000\n\t"
        "buffer_load_dwordx4 a[20:23], v97, s[20:23], s34 offen\n\t"
        "s_add_u32 s34, s34, 0x8000\n\t"
        "buffer_load_dwordx4 a[24:27], v97, s[20:23], s34 offen\n\t"
        "s_add_u32 s34, s34, 0x8000\n\t"
        "buffer_load_dwordx4 a[28:31], v97, s[20:23], s34 offen\n\t"
        "s_waitcnt lgkmcnt(0)\n\t"
        "s_waitcnt vmcnt(16)\n\t"
        "ds_write_b128 v98, v[192:195] offset:0\n\t"
        "ds_write_b128 v99, v[196:199] offset:1024\n\t"
        "ds_write_b128 v98, v[200:203] offset:2048\n\t"
        "ds_write_b128 v99, v[204:207] offset:3072\n\t"
        "ds_write_b128 v98, v[208:211] offset:4096\n\t"
        "ds_write_b128 v99, v[212:215] offset:5120\n\t"
        "ds_write_b128 v98, v[216:219] offset:6144\n\t"
        "ds_write_b128 v99, v[220:223] offset:7168\n\t"
        "ds_read_b64_tr_b16 v[192:193], v100 offset:0\n\t"
        "ds_read_b64_tr_b16 v[194:195], v100 offset:256\n\t"
        "ds_read_b64_tr_b16 v[196:197], v100 offset:4096\n\t"
        "ds_read_b64_tr_b16 v[198:199], v100 offset:4352\n\t"
        "s_waitcnt lgkmcnt(8)\n\t"
        "ds_read_b64_tr_b16 v[200:201], v101 offset:0\n\t"
        "ds_read_b64_tr_b16 v[202:203], v101 offset:256\n\t"
        "ds_read_b64_tr_b16 v[204:205], v101 offset:4096\n\t"
        "ds_read_b64_tr_b16 v[206:207], v101 offset:4352\n\t"
        "s_waitcnt lgkmcnt(8)\n\t"
        "ds_read_b64_tr_b16 v[208:209], v102 offset:0\n\t"
        "ds_read_b64_tr_b16 v[210:211], v102 offset:256\n\t"
        "ds_read_b64_tr_b16 v[212:213], v102 offset:4096\n\t"
        "ds_read_b64_tr_b16 v[214:215], v102 offset:4352\n\t"
        "s_waitcnt lgkmcnt(8)\n\t"
        "ds_read_b64_tr_b16 v[216:217], v103 offset:0\n\t"
        "ds_read_b64_tr_b16 v[218:219], v103 offset:256\n\t"
        "ds_read_b64_tr_b16 v[220:221], v103 offset:4096\n\t"
        "ds_read_b64_tr_b16 v[222:223], v103 offset:4352\n\t"
        "s_add_u32 s35, s68, 1\n\t"
        "s_and_b32 s35, s35, 7\n\t"
        "s_lshl_b32 s35, s35, 18\n\t"
        "s_lshl_b32 s34, s43, 21\n\t"
        "s_add_u32 s34, s34, s35\n\t"
        "buffer_load_dwordx4 a[32:35], v97, s[20:23], s34 offen\n\t"
        "s_add_u32 s34, s34, 0x8000\n\t"
        "buffer_load_dwordx4 a[36:39], v97, s[20:23], s34 offen\n\t"
        "s_add_u32 s34, s34, 0x8000\n\t"
        "buffer_load_dwordx4 a[40:43], v97, s[20:23], s34 offen\n\t"
        "s_add_u32 s34, s34, 0x8000\n\t"
        "buffer_load_dwordx4 a[44:47], v97, s[20:23], s34 offen\n\t"
        "s_add_u32 s34, s34, 0x8000\n\t"
        "buffer_load_dwordx4 a[48:51], v97, s[20:23], s34 offen\n\t"
        "s_add_u32 s34, s34, 0x8000\n\t"
        "buffer_load_dwordx4 a[52:55], v97, s[20:23], s34 offen\n\t"
        "s_add_u32 s34, s34, 0x8000\n\t"
        "buffer_load_dwordx4 a[56:59], v97, s[20:23], s34 offen\n\t"
        "s_add_u32 s34, s34, 0x8000\n\t"
        "buffer_load_dwordx4 a[60:63], v97, s[20:23], s34 offen\n\t"
        "s_waitcnt lgkmcnt(0)\n\t"
        "s_waitcnt vmcnt(16)\n\t"
        "ds_write_b128 v98, v[224:227] offset:0\n\t"
        "ds_write_b128 v99, v[228:231] offset:1024\n\t"
        "ds_write_b128 v98, v[232:235] offset:2048\n\t"
        "ds_write_b128 v99, v[236:239] offset:3072\n\t"
        "ds_write_b128 v98, v[240:243] offset:4096\n\t"
        "ds_write_b128 v99, v[244:247] offset:5120\n\t"
        "ds_write_b128 v98, v[248:251] offset:6144\n\t"
        "ds_write_b128 v99, v[252:255] offset:7168\n\t"
        "ds_read_b64_tr_b16 v[224:225], v100 offset:0\n\t"
        "ds_read_b64_tr_b16 v[226:227], v100 offset:256\n\t"
        "ds_read_b64_tr_b16 v[228:229], v100 offset:4096\n\t"
        "ds_read_b64_tr_b16 v[230:231], v100 offset:4352\n\t"
        "s_waitcnt lgkmcnt(8)\n\t"
        "ds_read_b64_tr_b16 v[232:233], v101 offset:0\n\t"
        "ds_read_b64_tr_b16 v[234:235], v101 offset:256\n\t"
        "ds_read_b64_tr_b16 v[236:237], v101 offset:4096\n\t"
        "ds_read_b64_tr_b16 v[238:239], v101 offset:4352\n\t"
        "s_waitcnt lgkmcnt(8)\n\t"
        "ds_read_b64_tr_b16 v[240:241], v102 offset:0\n\t"
        "ds_read_b64_tr_b16 v[242:243], v102 offset:256\n\t"
        "ds_read_b64_tr_b16 v[244:245], v102 offset:4096\n\t"
        "ds_read_b64_tr_b16 v[246:247], v102 offset:4352\n\t"
        "s_waitcnt lgkmcnt(8)\n\t"
        "ds_read_b64_tr_b16 v[248:249], v103 offset:0\n\t"
        "ds_read_b64_tr_b16 v[250:251], v103 offset:256\n\t"
        "ds_read_b64_tr_b16 v[252:253], v103 offset:4096\n\t"
        "ds_read_b64_tr_b16 v[254:255], v103 offset:4352\n\t"
        "s_add_u32 s35, s68, 2\n\t"
        "s_and_b32 s35, s35, 7\n\t"
        "s_lshl_b32 s35, s35, 18\n\t"
        "s_lshl_b32 s34, s43, 21\n\t"
        "s_add_u32 s34, s34, s35\n\t"
        "buffer_load_dwordx4 a[64:67], v97, s[20:23], s34 offen\n\t"
        "s_add_u32 s34, s34, 0x8000\n\t"
        "buffer_load_dwordx4 a[68:71], v97, s[20:23], s34 offen\n\t"
        "s_add_u32 s34, s34, 0x8000\n\t"
        "buffer_load_dwordx4 a[72:75], v97, s[20:23], s34 offen\n\t"
        "s_add_u32 s34, s34, 0x8000\n\t"
        "buffer_load_dwordx4 a[76:79], v97, s[20:23], s34 offen\n\t"
        "s_add_u32 s34, s34, 0x8000\n\t"
        "buffer_load_dwordx4 a[80:83], v97, s[20:23], s34 offen\n\t"
        "s_add_u32 s34, s34, 0x8000\n\t"
        "buffer_load_dwordx4 a[84:87], v97, s[20:23], s34 offen\n\t"
        "s_add_u32 s34, s34, 0x8000\n\t"
        "buffer_load_dwordx4 a[88:91], v97, s[20:23], s34 offen\n\t"
        "s_add_u32 s34, s34, 0x8000\n\t"
        "buffer_load_dwordx4 a[92:95], v97, s[20:23], s34 offen\n\t"
        "s_waitcnt lgkmcnt(0)\n\t"
        "s_waitcnt vmcnt(16)\n\t"
        "ds_write_b128 v122, a[0:3] offset:0\n\t"
        "ds_write_b128 v122, a[4:7] offset:1056\n\t"
        "ds_write_b128 v122, a[8:11] offset:2176\n\t"
        "ds_write_b128 v122, a[12:15] offset:3232\n\t"
        "ds_write_b128 v122, a[16:19] offset:4352\n\t"
        "ds_write_b128 v122, a[20:23] offset:5408\n\t"
        "ds_write_b128 v122, a[24:27] offset:6528\n\t"
        "ds_write_b128 v122, a[28:31] offset:7584\n\t"
        "ds_read_b64_tr_b16 a[0:1], v123 offset:0\n\t"
        "ds_read_b64_tr_b16 a[2:3], v123 offset:128\n\t"
        "ds_read_b64_tr_b16 a[4:5], v123 offset:4352\n\t"
        "ds_read_b64_tr_b16 a[6:7], v123 offset:4480\n\t"
        "s_waitcnt lgkmcnt(8)\n\t"
        "ds_read_b64_tr_b16 a[8:9], v123 offset:32\n\t"
        "ds_read_b64_tr_b16 a[10:11], v123 offset:160\n\t"
        "ds_read_b64_tr_b16 a[12:13], v123 offset:4384\n\t"
        "ds_read_b64_tr_b16 a[14:15], v123 offset:4512\n\t"
        "s_waitcnt lgkmcnt(8)\n\t"
        "ds_read_b64_tr_b16 a[16:17], v123 offset:512\n\t"
        "ds_read_b64_tr_b16 a[18:19], v123 offset:640\n\t"
        "ds_read_b64_tr_b16 a[20:21], v123 offset:4864\n\t"
        "ds_read_b64_tr_b16 a[22:23], v123 offset:4992\n\t"
        "s_waitcnt lgkmcnt(8)\n\t"
        "ds_read_b64_tr_b16 a[24:25], v123 offset:544\n\t"
        "ds_read_b64_tr_b16 a[26:27], v123 offset:672\n\t"
        "ds_read_b64_tr_b16 a[28:29], v123 offset:4896\n\t"
        "ds_read_b64_tr_b16 a[30:31], v123 offset:5024\n\t"
        "s_waitcnt lgkmcnt(8)\n\t"
        "s_add_u32 s35, s68, 3\n\t"
        "s_and_b32 s35, s35, 7\n\t"
        "s_lshl_b32 s35, s35, 18\n\t"
        "s_lshl_b32 s34, s43, 21\n\t"
        "s_add_u32 s34, s34, s35\n\t"
        "buffer_load_dwordx4 a[96:99], v97, s[20:23], s34 offen\n\t"
        "s_add_u32 s34, s34, 0x8000\n\t"
        "buffer_load_dwordx4 a[100:103], v97, s[20:23], s34 offen\n\t"
        "s_add_u32 s34, s34, 0x8000\n\t"
        "buffer_load_dwordx4 a[104:107], v97, s[20:23], s34 offen\n\t"
        "s_add_u32 s34, s34, 0x8000\n\t"
        "buffer_load_dwordx4 a[108:111], v97, s[20:23], s34 offen\n\t"
        "s_add_u32 s34, s34, 0x8000\n\t"
        "buffer_load_dwordx4 a[112:115], v97, s[20:23], s34 offen\n\t"
        "s_add_u32 s34, s34, 0x8000\n\t"
        "buffer_load_dwordx4 a[116:119], v97, s[20:23], s34 offen\n\t"
        "s_add_u32 s34, s34, 0x8000\n\t"
        "buffer_load_dwordx4 a[120:123], v97, s[20:23], s34 offen\n\t"
        "s_add_u32 s34, s34, 0x8000\n\t"
        "buffer_load_dwordx4 a[124:127], v97, s[20:23], s34 offen\n\t"
        "s_waitcnt lgkmcnt(0)\n\t"
        "s_waitcnt vmcnt(16)\n\t"
        "ds_write_b128 v122, a[32:35] offset:0\n\t"
        "ds_write_b128 v122, a[36:39] offset:1056\n\t"
        "ds_write_b128 v122, a[40:43] offset:2176\n\t"
        "ds_write_b128 v122, a[44:47] offset:3232\n\t"
        "ds_write_b128 v122, a[48:51] offset:4352\n\t"
        "ds_write_b128 v122, a[52:55] offset:5408\n\t"
        "ds_write_b128 v122, a[56:59] offset:6528\n\t"
        "ds_write_b128 v122, a[60:63] offset:7584\n\t"
        "ds_read_b64_tr_b16 a[32:33], v123 offset:0\n\t"
        "ds_read_b64_tr_b16 a[34:35], v123 offset:128\n\t"
        "ds_read_b64_tr_b16 a[36:37], v123 offset:4352\n\t"
        "ds_read_b64_tr_b16 a[38:39], v123 offset:4480\n\t"
        "s_waitcnt lgkmcnt(8)\n\t"
        "ds_read_b64_tr_b16 a[40:41], v123 offset:32\n\t"
        "ds_read_b64_tr_b16 a[42:43], v123 offset:160\n\t"
        "ds_read_b64_tr_b16 a[44:45], v123 offset:4384\n\t"
        "ds_read_b64_tr_b16 a[46:47], v123 offset:4512\n\t"
        "s_waitcnt lgkmcnt(8)\n\t"
        "ds_read_b64_tr_b16 a[48:49], v123 offset:512\n\t"
        "ds_read_b64_tr_b16 a[50:51], v123 offset:640\n\t"
        "ds_read_b64_tr_b16 a[52:53], v123 offset:4864\n\t"
        "ds_read_b64_tr_b16 a[54:55], v123 offset:4992\n\t"
        "s_waitcnt lgkmcnt(8)\n\t"
        "ds_read_b64_tr_b16 a[56:57], v123 offset:544\n\t"
        "ds_read_b64_tr_b16 a[58:59], v123 offset:672\n\t"
        "ds_read_b64_tr_b16 a[60:61], v123 offset:4896\n\t"
        "ds_read_b64_tr_b16 a[62:63], v123 offset:5024\n\t"
        "s_waitcnt lgkmcnt(8)\n\t"
        "s_add_u32 s35, s68, 4\n\t"
        "s_and_b32 s35, s35, 7\n\t"
        "s_lshl_b32 s35, s35, 18\n\t"
        "s_lshl_b32 s34, s43, 21\n\t"
        "s_add_u32 s34, s34, s35\n\t"
        "buffer_load_dwordx4 a[128:131], v97, s[20:23], s34 offen\n\t"
        "s_add_u32 s34, s34, 0x8000\n\t"
        "buffer_load_dwordx4 a[132:135], v97, s[20:23], s34 offen\n\t"
        "s_add_u32 s34, s34, 0x8000\n\t"
        "buffer_load_dwordx4 a[136:139], v97, s[20:23], s34 offen\n\t"
        "s_add_u32 s34, s34, 0x8000\n\t"
        "buffer_load_dwordx4 a[140:143], v97, s[20:23], s34 offen\n\t"
        "s_add_u32 s34, s34, 0x8000\n\t"
        "buffer_load_dwordx4 a[144:147], v97, s[20:23], s34 offen\n\t"
        "s_add_u32 s34, s34, 0x8000\n\t"
        "buffer_load_dwordx4 a[148:151], v97, s[20:23], s34 offen\n\t"
        "s_add_u32 s34, s34, 0x8000\n\t"
        "buffer_load_dwordx4 a[152:155], v97, s[20:23], s34 offen\n\t"
        "s_add_u32 s34, s34, 0x8000\n\t"
        "buffer_load_dwordx4 a[156:159], v97, s[20:23], s34 offen\n\t"
        "s_waitcnt lgkmcnt(0)\n\t"
        "s_waitcnt vmcnt(16)\n\t"
        "ds_write_b128 v122, a[64:67] offset:0\n\t"
        "ds_write_b128 v122, a[68:71] offset:1056\n\t"
        "ds_write_b128 v122, a[72:75] offset:2176\n\t"
        "ds_write_b128 v122, a[76:79] offset:3232\n\t"
        "ds_write_b128 v122, a[80:83] offset:4352\n\t"
        "ds_write_b128 v122, a[84:87] offset:5408\n\t"
        "ds_write_b128 v122, a[88:91] offset:6528\n\t"
        "ds_write_b128 v122, a[92:95] offset:7584\n\t"
        "ds_read_b64_tr_b16 a[64:65], v123 offset:0\n\t"
        "ds_read_b64_tr_b16 a[66:67], v123 offset:128\n\t"
        "ds_read_b64_tr_b16 a[68:69], v123 offset:4352\n\t"
        "ds_read_b64_tr_b16 a[70:71], v123 offset:4480\n\t"
        "s_waitcnt lgkmcnt(8)\n\t"
        "ds_read_b64_tr_b16 a[72:73], v123 offset:32\n\t"
        "ds_read_b64_tr_b16 a[74:75], v123 offset:160\n\t"
        "ds_read_b64_tr_b16 a[76:77], v123 offset:4384\n\t"
        "ds_read_b64_tr_b16 a[78:79], v123 offset:4512\n\t"
        "s_waitcnt lgkmcnt(8)\n\t"
        "ds_read_b64_tr_b16 a[80:81], v123 offset:512\n\t"
        "ds_read_b64_tr_b16 a[82:83], v123 offset:640\n\t"
        "ds_read_b64_tr_b16 a[84:85], v123 offset:4864\n\t"
        "ds_read_b64_tr_b16 a[86:87], v123 offset:4992\n\t"
        "s_waitcnt lgkmcnt(8)\n\t"
        "ds_read_b64_tr_b16 a[88:89], v123 offset:544\n\t"
        "ds_read_b64_tr_b16 a[90:91], v123 offset:672\n\t"
        "ds_read_b64_tr_b16 a[92:93], v123 offset:4896\n\t"
        "ds_read_b64_tr_b16 a[94:95], v123 offset:5024\n\t"
        "s_waitcnt lgkmcnt(8)\n\t"
        "s_add_u32 s35, s68, 5\n\t"
        "s_and_b32 s35, s35, 7\n\t"
        "s_lshl_b32 s35, s35, 18\n\t"
        "s_lshl_b32 s34, s43, 21\n\t"
        "s_add_u32 s34, s34, s35\n\t"
        "buffer_load_dwordx4 a[160:163], v97, s[20:23], s34 offen\n\t"
        "s_add_u32 s34, s34, 0x8000\n\t"
        "buffer_load_dwordx4 a[164:167], v97, s[20:23], s34 offen\n\t"
        "s_add_u32 s34, s34, 0x8000\n\t"
        "buffer_load_dwordx4 a[168:171], v97, s[20:23], s34 offen\n\t"
        "s_add_u32 s34, s34, 0x8000\n\t"
        "buffer_load_dwordx4 a[172:175], v97, s[20:23], s34 offen\n\t"
        "s_add_u32 s34, s34, 0x8000\n\t"
        "buffer_load_dwordx4 a[176:179], v97, s[20:23], s34 offen\n\t"
        "s_add_u32 s34, s34, 0x8000\n\t"
        "buffer_load_dwordx4 a[180:183], v97, s[20:23], s34 offen\n\t"
        "s_add_u32 s34, s34, 0x8000\n\t"
        "buffer_load_dwordx4 a[184:187], v97, s[20:23], s34 offen\n\t"
        "s_add_u32 s34, s34, 0x8000\n\t"
        "buffer_load_dwordx4 a[188:191], v97, s[20:23], s34 offen\n\t"
        "s_waitcnt lgkmcnt(0)\n\t"
        "s_waitcnt vmcnt(16)\n\t"
        "ds_write_b128 v122, a[96:99] offset:0\n\t"
        "ds_write_b128 v122, a[100:103] offset:1056\n\t"
        "ds_write_b128 v122, a[104:107] offset:2176\n\t"
        "ds_write_b128 v122, a[108:111] offset:3232\n\t"
        "ds_write_b128 v122, a[112:115] offset:4352\n\t"
        "ds_write_b128 v122, a[116:119] offset:5408\n\t"
        "ds_write_b128 v122, a[120:123] offset:6528\n\t"
        "ds_write_b128 v122, a[124:127] offset:7584\n\t"
        "ds_read_b64_tr_b16 a[96:97], v123 offset:0\n\t"
        "ds_read_b64_tr_b16 a[98:99], v123 offset:128\n\t"
        "ds_read_b64_tr_b16 a[100:101], v123 offset:4352\n\t"
        "ds_read_b64_tr_b16 a[102:103], v123 offset:4480\n\t"
        "s_waitcnt lgkmcnt(8)\n\t"
        "ds_read_b64_tr_b16 a[104:105], v123 offset:32\n\t"
        "ds_read_b64_tr_b16 a[106:107], v123 offset:160\n\t"
        "ds_read_b64_tr_b16 a[108:109], v123 offset:4384\n\t"
        "ds_read_b64_tr_b16 a[110:111], v123 offset:4512\n\t"
        "s_waitcnt lgkmcnt(8)\n\t"
        "ds_read_b64_tr_b16 a[112:113], v123 offset:512\n\t"
        "ds_read_b64_tr_b16 a[114:115], v123 offset:640\n\t"
        "ds_read_b64_tr_b16 a[116:117], v123 offset:4864\n\t"
        "ds_read_b64_tr_b16 a[118:119], v123 offset:4992\n\t"
        "s_waitcnt lgkmcnt(8)\n\t"
        "ds_read_b64_tr_b16 a[120:121], v123 offset:544\n\t"
        "ds_read_b64_tr_b16 a[122:123], v123 offset:672\n\t"
        "ds_read_b64_tr_b16 a[124:125], v123 offset:4896\n\t"
        "ds_read_b64_tr_b16 a[126:127], v123 offset:5024\n\t"
        "s_waitcnt lgkmcnt(8)\n\t"
        "s_add_u32 s35, s68, 6\n\t"
        "s_and_b32 s35, s35, 7\n\t"
        "s_lshl_b32 s35, s35, 18\n\t"
        "s_lshl_b32 s34, s43, 21\n\t"
        "s_add_u32 s34, s34, s35\n\t"
        "buffer_load_dwordx4 a[192:195], v97, s[20:23], s34 offen\n\t"
        "s_add_u32 s34, s34, 0x8000\n\t"
        "buffer_load_dwordx4 a[196:199], v97, s[20:23], s34 offen\n\t"
        "s_add_u32 s34, s34, 0x8000\n\t"
        "buffer_load_dwordx4 a[200:203], v97, s[20:23], s34 offen\n\t"
        "s_add_u32 s34, s34, 0x8000\n\t"
        "buffer_load_dwordx4 a[204:207], v97, s[20:23], s34 offen\n\t"
        "s_add_u32 s34, s34, 0x8000\n\t"
        "buffer_load_dwordx4 a[208:211], v97, s[20:23], s34 offen\n\t"
        "s_add_u32 s34, s34, 0x8000\n\t"
        "buffer_load_dwordx4 a[212:215], v97, s[20:23], s34 offen\n\t"
        "s_add_u32 s34, s34, 0x8000\n\t"
        "buffer_load_dwordx4 a[216:219], v97, s[20:23], s34 offen\n\t"
        "s_add_u32 s34, s34, 0x8000\n\t"
        "buffer_load_dwordx4 a[220:223], v97, s[20:23], s34 offen\n\t"
        "s_waitcnt lgkmcnt(0)\n\t"
        "s_waitcnt vmcnt(16)\n\t"
        "ds_write_b128 v122, a[128:131] offset:0\n\t"
        "ds_write_b128 v122, a[132:135] offset:1056\n\t"
        "ds_write_b128 v122, a[136:139] offset:2176\n\t"
        "ds_write_b128 v122, a[140:143] offset:3232\n\t"
        "ds_write_b128 v122, a[144:147] offset:4352\n\t"
        "ds_write_b128 v122, a[148:151] offset:5408\n\t"
        "ds_write_b128 v122, a[152:155] offset:6528\n\t"
        "ds_write_b128 v122, a[156:159] offset:7584\n\t"
        "ds_read_b64_tr_b16 a[128:129], v123 offset:0\n\t"
        "ds_read_b64_tr_b16 a[130:131], v123 offset:128\n\t"
        "ds_read_b64_tr_b16 a[132:133], v123 offset:4352\n\t"
        "ds_read_b64_tr_b16 a[134:135], v123 offset:4480\n\t"
        "s_waitcnt lgkmcnt(8)\n\t"
        "ds_read_b64_tr_b16 a[136:137], v123 offset:32\n\t"
        "ds_read_b64_tr_b16 a[138:139], v123 offset:160\n\t"
        "ds_read_b64_tr_b16 a[140:141], v123 offset:4384\n\t"
        "ds_read_b64_tr_b16 a[142:143], v123 offset:4512\n\t"
        "s_waitcnt lgkmcnt(8)\n\t"
        "ds_read_b64_tr_b16 a[144:145], v123 offset:512\n\t"
        "ds_read_b64_tr_b16 a[146:147], v123 offset:640\n\t"
        "ds_read_b64_tr_b16 a[148:149], v123 offset:4864\n\t"
        "ds_read_b64_tr_b16 a[150:151], v123 offset:4992\n\t"
        "s_waitcnt lgkmcnt(8)\n\t"
        "ds_read_b64_tr_b16 a[152:153], v123 offset:544\n\t"
        "ds_read_b64_tr_b16 a[154:155], v123 offset:672\n\t"
        "ds_read_b64_tr_b16 a[156:157], v123 offset:4896\n\t"
        "ds_read_b64_tr_b16 a[158:159], v123 offset:5024\n\t"
        "s_waitcnt lgkmcnt(8)\n\t"
        "s_add_u32 s35, s68, 7\n\t"
        "s_and_b32 s35, s35, 7\n\t"
        "s_lshl_b32 s35, s35, 18\n\t"
        "s_lshl_b32 s34, s43, 21\n\t"
        "s_add_u32 s34, s34, s35\n\t"
        "buffer_load_dwordx4 a[224:227], v97, s[20:23], s34 offen\n\t"
        "s_add_u32 s34, s34, 0x8000\n\t"
        "buffer_load_dwordx4 a[228:231], v97, s[20:23], s34 offen\n\t"
        "s_add_u32 s34, s34, 0x8000\n\t"
        "buffer_load_dwordx4 a[232:235], v97, s[20:23], s34 offen\n\t"
        "s_add_u32 s34, s34, 0x8000\n\t"
        "buffer_load_dwordx4 a[236:239], v97, s[20:23], s34 offen\n\t"
        "s_add_u32 s34, s34, 0x8000\n\t"
        "buffer_load_dwordx4 a[240:243], v97, s[20:23], s34 offen\n\t"
        "s_add_u32 s34, s34, 0x8000\n\t"
        "buffer_load_dwordx4 a[244:247], v97, s[20:23], s34 offen\n\t"
        "s_add_u32 s34, s34, 0x8000\n\t"
        "buffer_load_dwordx4 a[248:251], v97, s[20:23], s34 offen\n\t"
        "s_add_u32 s34, s34, 0x8000\n\t"
        "buffer_load_dwordx4 a[252:255], v97, s[20:23], s34 offen\n\t"
        "s_waitcnt lgkmcnt(0)\n\t"
        "s_waitcnt vmcnt(16)\n\t"
        "ds_write_b128 v122, a[160:163] offset:0\n\t"
        "ds_write_b128 v122, a[164:167] offset:1056\n\t"
        "ds_write_b128 v122, a[168:171] offset:2176\n\t"
        "ds_write_b128 v122, a[172:175] offset:3232\n\t"
        "ds_write_b128 v122, a[176:179] offset:4352\n\t"
        "ds_write_b128 v122, a[180:183] offset:5408\n\t"
        "ds_write_b128 v122, a[184:187] offset:6528\n\t"
        "ds_write_b128 v122, a[188:191] offset:7584\n\t"
        "ds_read_b64_tr_b16 a[160:161], v123 offset:0\n\t"
        "ds_read_b64_tr_b16 a[162:163], v123 offset:128\n\t"
        "ds_read_b64_tr_b16 a[164:165], v123 offset:4352\n\t"
        "ds_read_b64_tr_b16 a[166:167], v123 offset:4480\n\t"
        "s_waitcnt lgkmcnt(8)\n\t"
        "ds_read_b64_tr_b16 a[168:169], v123 offset:32\n\t"
        "ds_read_b64_tr_b16 a[170:171], v123 offset:160\n\t"
        "ds_read_b64_tr_b16 a[172:173], v123 offset:4384\n\t"
        "ds_read_b64_tr_b16 a[174:175], v123 offset:4512\n\t"
        "s_waitcnt lgkmcnt(8)\n\t"
        "ds_read_b64_tr_b16 a[176:177], v123 offset:512\n\t"
        "ds_read_b64_tr_b16 a[178:179], v123 offset:640\n\t"
        "ds_read_b64_tr_b16 a[180:181], v123 offset:4864\n\t"
        "ds_read_b64_tr_b16 a[182:183], v123 offset:4992\n\t"
        "s_waitcnt lgkmcnt(8)\n\t"
        "ds_read_b64_tr_b16 a[184:185], v123 offset:544\n\t"
        "ds_read_b64_tr_b16 a[186:187], v123 offset:672\n\t"
        "ds_read_b64_tr_b16 a[188:189], v123 offset:4896\n\t"
        "ds_read_b64_tr_b16 a[190:191], v123 offset:5024\n\t"
        "s_waitcnt lgkmcnt(8)\n\t"
        "s_waitcnt lgkmcnt(0)\n\t"
        "s_waitcnt vmcnt(8)\n\t"
        "ds_write_b128 v122, a[192:195] offset:0\n\t"
        "ds_write_b128 v122, a[196:199] offset:1056\n\t"
        "ds_write_b128 v122, a[200:203] offset:2176\n\t"
        "ds_write_b128 v122, a[204:207] offset:3232\n\t"
        "ds_write_b128 v122, a[208:211] offset:4352\n\t"
        "ds_write_b128 v122, a[212:215] offset:5408\n\t"
        "ds_write_b128 v122, a[216:219] offset:6528\n\t"
        "ds_write_b128 v122, a[220:223] offset:7584\n\t"
        "ds_read_b64_tr_b16 a[192:193], v123 offset:0\n\t"
        "ds_read_b64_tr_b16 a[194:195], v123 offset:128\n\t"
        "ds_read_b64_tr_b16 a[196:197], v123 offset:4352\n\t"
        "ds_read_b64_tr_b16 a[198:199], v123 offset:4480\n\t"
        "s_waitcnt lgkmcnt(8)\n\t"
        "ds_read_b64_tr_b16 a[200:201], v123 offset:32\n\t"
        "ds_read_b64_tr_b16 a[202:203], v123 offset:160\n\t"
        "ds_read_b64_tr_b16 a[204:205], v123 offset:4384\n\t"
        "ds_read_b64_tr_b16 a[206:207], v123 offset:4512\n\t"
        "s_waitcnt lgkmcnt(8)\n\t"
        "ds_read_b64_tr_b16 a[208:209], v123 offset:512\n\t"
        "ds_read_b64_tr_b16 a[210:211], v123 offset:640\n\t"
        "ds_read_b64_tr_b16 a[212:213], v123 offset:4864\n\t"
        "ds_read_b64_tr_b16 a[214:215], v123 offset:4992\n\t"
        "s_waitcnt lgkmcnt(8)\n\t"
        "ds_read_b64_tr_b16 a[216:217], v123 offset:544\n\t"
        "ds_read_b64_tr_b16 a[218:219], v123 offset:672\n\t"
        "ds_read_b64_tr_b16 a[220:221], v123 offset:4896\n\t"
        "ds_read_b64_tr_b16 a[222:223], v123 offset:5024\n\t"
        "s_waitcnt lgkmcnt(8)\n\t"
        "s_waitcnt lgkmcnt(0)\n\t"
        "s_waitcnt vmcnt(0)\n\t"
        "ds_write_b128 v122, a[224:227] offset:0\n\t"
        "ds_write_b128 v122, a[228:231] offset:1056\n\t"
        "ds_write_b128 v122, a[232:235] offset:2176\n\t"
        "ds_write_b128 v122, a[236:239] offset:3232\n\t"
        "ds_write_b128 v122, a[240:243] offset:4352\n\t"
        "ds_write_b128 v122, a[244:247] offset:5408\n\t"
        "ds_write_b128 v122, a[248:251] offset:6528\n\t"
        "ds_write_b128 v122, a[252:255] offset:7584\n\t"
        "ds_read_b64_tr_b16 a[224:225], v123 offset:0\n\t"
        "ds_read_b64_tr_b16 a[226:227], v123 offset:128\n\t"
        "ds_read_b64_tr_b16 a[228:229], v123 offset:4352\n\t"
        "ds_read_b64_tr_b16 a[230:231], v123 offset:4480\n\t"
        "s_waitcnt lgkmcnt(8)\n\t"
        "ds_read_b64_tr_b16 a[232:233], v123 offset:32\n\t"
        "ds_read_b64_tr_b16 a[234:235], v123 offset:160\n\t"
        "ds_read_b64_tr_b16 a[236:237], v123 offset:4384\n\t"
        "ds_read_b64_tr_b16 a[238:239], v123 offset:4512\n\t"
        "s_waitcnt lgkmcnt(8)\n\t"
        "ds_read_b64_tr_b16 a[240:241], v123 offset:512\n\t"
        "ds_read_b64_tr_b16 a[242:243], v123 offset:640\n\t"
        "ds_read_b64_tr_b16 a[244:245], v123 offset:4864\n\t"
        "ds_read_b64_tr_b16 a[246:247], v123 offset:4992\n\t"
        "s_waitcnt lgkmcnt(8)\n\t"
        "ds_read_b64_tr_b16 a[248:249], v123 offset:544\n\t"
        "ds_read_b64_tr_b16 a[250:251], v123 offset:672\n\t"
        "ds_read_b64_tr_b16 a[252:253], v123 offset:4896\n\t"
        "ds_read_b64_tr_b16 a[254:255], v123 offset:5024\n\t"
        "s_waitcnt lgkmcnt(8)\n\t"
        "s_mov_b32 s35, 33792\n\t"
        "buffer_load_dword v0, off, s[12:15], s35 sc1\n\t"
        "s_waitcnt lgkmcnt(0)\n\t"
        "s_waitcnt vmcnt(0)\n\t"
        "s_mov_b32 s42, 0\n\t"
        "LB_cal%=:\n\t"
        "v_readfirstlane_b32 s34, v0\n\t"
        "s_nop 0\n\t"
        "s_cmp_lg_u32 s34, 0\n\t"
        "s_cbranch_scc1 LB_calok%=\n\t"
        "s_sleep 4\n\t"
        "s_mov_b32 s35, 33792\n\t"
        "buffer_load_dword v0, off, s[12:15], s35 sc1\n\t"
        "s_waitcnt vmcnt(0)\n\t"
        "s_add_u32 s42, s42, 1\n\t"
        "s_cmp_lt_u32 s42, 0x100000\n\t"
        "s_cbranch_scc1 LB_cal%=\n\t"
        "s_movk_i32 s34, 8\n\t"
        "s_branch LB_die%=\n\t"
        "LB_calok%=:\n\t"
        "s_and_b32 s34, s34, 8192\n\t"
        "s_add_u32 s8, s8, s34\n\t"
        "s_addc_u32 s9, s9, 0\n\t"
        "v_min_u32_e32 v109, -2, v109\n\t"
        "buffer_store_dword v109, v116, s[8:11], 0 offen sc1\n\t"
        "s_memrealtime s[32:33]\n\t"
        "s_waitcnt lgkmcnt(0)\n\t"
        "v_mov_b32_e32 v110, s32\n\t"
        "s_lshl_b32 s34, s29, 8\n\t"
        "s_lshl_b32 s35, s30, 20\n\t"
        "s_or_b32 s54, s34, s35\n\t"
        "s_add_u32 s34, s54, 12589056\n\t"
        "v_mov_b32_e32 v111, s34\n\t"
        "s_barrier\n\t"
        "s_cmp_lg_u32 s43, 0\n\t"
        "s_cbranch_scc1 LB_st%=\n\t"
        "v_mov_b32_e32 v2, 1\n\t"
        "s_mov_b32 s35, 34304\n\t"
        "s_mov_b64 exec, 1\n\t"
        "buffer_atomic_add v2, off, s[12:15], s35 sc0\n\t"
        "s_mov_b64 exec, -1\n\t"
        "s_waitcnt vmcnt(0)\n\t"
        "v_readfirstlane_b32 s34, v2\n\t"
        "s_nop 0\n\t"
        "s_cmp_lg_u32 s34, 95\n\t"
        "s_cbranch_scc1 LB_st%=\n\t"
        "s_memrealtime s[32:33]\n\t"
        "v_mbcnt_lo_u32_b32 v3, -1, 0\n\t"
        "v_lshlrev_b32_e32 v3, 7, v3\n\t"
        "s_waitcnt lgkmcnt(0)\n\t"
        "s_add_u32 s34, s32, 50\n\t"
        "s_or_b32 s34, s34, 1\n\t"
        "v_mov_b32_e32 v2, s34\n\t"
        "s_mov_b32 s35, 36864\n\t"
        "s_mov_b64 exec, 0xffff\n\t"
        "buffer_store_dword v2, v3, s[12:15], s35 offen sc1\n\t"
        "s_mov_b64 exec, -1\n\t"
        "LB_st%=:\n\t"
        "s_mov_b32 s24, 0\n\t"
        "s_mov_b32 s26, 0\n\t"
        "s_mov_b32 s27, 0x80000\n\t"
        "s_mov_b32 s28, 0x180000\n\t"
        "s_mov_b32 s31, 0\n\t"
        "s_mov_b32 s47, 0\n\t"
        "s_mov_b32 s38, 0xff00ff00\n\t"
        "s_mov_b32 s39, 0xff00ff00\n\t"
        "s_mov_b32 s40, 0\n\t"
        "s_mov_b32 s41, -1\n\t"
        "v_mov_b64_e32 v[0:1], 0\n\t"
        "v_mov_b64_e32 v[2:3], 0\n\t"
        "v_mov_b64_e32 v[4:5], 0\n\t"
        "v_mov_b64_e32 v[6:7], 0\n\t"
        "v_mov_b64_e32 v[8:9], 0\n\t"
        "v_mov_b64_e32 v[10:11], 0\n\t"
        "v_mov_b64_e32 v[12:13], 0\n\t"
        "v_mov_b64_e32 v[14:15], 0\n\t"
        "s_mov_b32 s42, 0\n\t"
        "LB_gw%=:\n\t"
        "buffer_load_dword v16, off, s[12:15], s45 sc1\n\t"
        "s_waitcnt vmcnt(0)\n\t"
        "v_readfirstlane_b32 s25, v16\n\t"
        "s_nop 0\n\t"
        "s_cmp_lg_u32 s25, 0\n\t"
        "s_cbranch_scc1 LB_loop%=\n\t"
        "s_sleep 2\n\t"
        "s_add_u32 s42, s42, 1\n\t"
        "s_cmp_lt_u32 s42, 0x100000\n\t"
        "s_cbranch_scc1 LB_gw%=\n\t"
        "s_movk_i32 s34, 9\n\t"
        "s_branch LB_die%=\n\t"
        "LB_loop%=:\n\t"
        "s_mov_b32 s42, 0\n\t"
        "s_sub_u32 s35, s25, 1\n\t"
        "LB_wa%=:\n\t"
        "s_memrealtime s[32:33]\n\t"
        "s_waitcnt lgkmcnt(0)\n\t"
        "s_sub_u32 s34, s35, s32\n\t"
        "s_cmpk_lt_u32 s34, 0x2000\n\t"
        "s_cbranch_scc1 LB_wa%=\n\t"
        "LB_rd%=:\n\t"
        "buffer_load_dwordx4 v[16:19], v112, s[8:11], s26 offen offset:0 sc1\n\t"
        "s_nop 9\n\t"
        "buffer_load_dwordx4 v[20:23], v112, s[8:11], s26 offen offset:1024 sc1\n\t"
        "s_nop 9\n\t"
        "buffer_load_dwordx4 v[24:27], v112, s[8:11], s26 offen offset:2048 sc1\n\t"
        "s_nop 9\n\t"
        "buffer_load_dwordx4 v[28:31], v112, s[8:11], s26 offen offset:3072 sc1\n\t"
        "LB_go%=:\n\t"
        "s_waitcnt vmcnt(3)\n\t"
        "v_smfmac_f32_16x16x64_bf16 v[0:3], v[16:19], v[128:135], v106\n\t"
        "v_max3_u32 v108, v16, v17, v18\n\t"
        "v_max_u32_e32 v108, v19, v108\n\t"
        "v_smfmac_f32_16x16x64_bf16 v[4:7], v[16:19], v[136:143], v106\n\t"
        "v_smfmac_f32_16x16x64_bf16 v[8:11], v[16:19], v[144:151], v106\n\t"
        "v_smfmac_f32_16x16x64_bf16 v[12:15], v[16:19], v[152:159], v106\n\t"
        "s_waitcnt vmcnt(2)\n\t"
        "v_smfmac_f32_16x16x64_bf16 v[0:3], v[20:23], v[160:167], v106\n\t"
        "v_max3_u32 v108, v20, v21, v108\n\t"
        "v_max3_u32 v108, v22, v23, v108\n\t"
        "v_smfmac_f32_16x16x64_bf16 v[4:7], v[20:23], v[168:175], v106\n\t"
        "v_smfmac_f32_16x16x64_bf16 v[8:11], v[20:23], v[176:183], v106\n\t"
        "v_smfmac_f32_16x16x64_bf16 v[12:15], v[20:23], v[184:191], v106\n\t"
        "s_waitcnt vmcnt(1)\n\t"
        "v_smfmac_f32_16x16x64_bf16 v[0:3], v[24:27], v[192:199], v106\n\t"
        "v_max3_u32 v108, v24, v25, v108\n\t"
        "v_max3_u32 v108, v26, v27, v108\n\t"
        "v_smfmac_f32_16x16x64_bf16 v[4:7], v[24:27], v[200:207], v106\n\t"
        "v_smfmac_f32_16x16x64_bf16 v[8:11], v[24:27], v[208:215], v106\n\t"
        "v_smfmac_f32_16x16x64_bf16 v[12:15], v[24:27], v[216:223], v106\n\t"
        "s_waitcnt vmcnt(0)\n\t"
        "v_smfmac_f32_16x16x64_bf16 v[0:3], v[28:31], v[224:231], v106\n\t"
        "v_max3_u32 v108, v28, v29, v108\n\t"
        "v_max3_u32 v108, v30, v31, v108\n\t"
        "v_smfmac_f32_16x16x64_bf16 v[4:7], v[28:31], v[232:239], v106\n\t"
        "v_cmp_eq_u32_e32 vcc, -1, v108\n\t"
        "v_smfmac_f32_16x16x64_bf16 v[8:11], v[28:31], v[240:247], v106\n\t"
        "v_smfmac_f32_16x16x64_bf16 v[12:15], v[28:31], v[248:255], v106\n\t"
        "s_cbranch_vccnz LB_rdfail%=\n\t"
        "s_nop 0\n\t"
        "v_add_f32_e32 v0, v0, v1\n\t"
        "v_add_f32_e32 v1, v2, v3\n\t"
        "ds_write_b64 v118, v[0:1] offset:0\n\t"
        "v_add_f32_e32 v4, v4, v5\n\t"
        "v_add_f32_e32 v5, v6, v7\n\t"
        "ds_write_b64 v118, v[4:5] offset:512\n\t"
        "v_add_f32_e32 v8, v8, v9\n\t"
        "v_add_f32_e32 v9, v10, v11\n\t"
        "ds_write_b64 v118, v[8:9] offset:1024\n\t"
        "v_add_f32_e32 v12, v12, v13\n\t"
        "v_add_f32_e32 v13, v14, v15\n\t"
        "ds_write_b64 v118, v[12:13] offset:1536\n\t"
        "buffer_store_dwordx2 v[110:111], v117, s[8:11], s26 offen sc1\n\t"
        "s_waitcnt lgkmcnt(0)\n\t"
        "s_barrier\n\t"
        "ds_read_b128 v[16:19], v119 offset:0\n\t"
        "ds_read_b128 v[20:23], v119 offset:2048\n\t"
        "ds_read_b128 v[24:27], v119 offset:4096\n\t"
        "ds_read_b128 v[28:31], v119 offset:6144\n\t"
        "buffer_load_dwordx4 v[64:67], v124, s[8:11], s28 offen sc1\n\t"
        "s_waitcnt lgkmcnt(2)\n\t"
        "v_pk_add_f32 v[16:17], v[16:17], v[20:21]\n\t"
        "v_pk_add_f32 v[18:19], v[18:19], v[22:23]\n\t"
        "s_waitcnt lgkmcnt(0)\n\t"
        "v_pk_add_f32 v[24:25], v[24:25], v[28:29]\n\t"
        "v_pk_add_f32 v[26:27], v[26:27], v[30:31]\n\t"
        "v_pk_add_f32 v[16:17], v[16:17], v[24:25]\n\t"
        "v_pk_add_f32 v[18:19], v[18:19], v[26:27]\n\t"
        "v_cvt_pk_bf16_f32 v20, v16, v17\n\t"
        "v_cvt_pk_bf16_f32 v21, v18, v19\n\t"
        "buffer_store_dwordx2 v[20:21], v114, s[8:11], s26 offen sc1\n\t"
        "s_cmp_eq_u32 s24, 63\n\t"
        "s_cbranch_scc1 LB_end%=\n\t"
        "s_memrealtime s[52:53]\n\t"
        "v_mov_b64_e32 v[24:25], -1\n\t"
        "v_mov_b64_e32 v[32:33], 0\n\t"
        "v_mov_b64_e32 v[34:35], 0\n\t"
        "v_mov_b64_e32 v[36:37], 0\n\t"
        "v_mov_b64_e32 v[38:39], 0\n\t"
        "v_mov_b64_e32 v[40:41], 0\n\t"
        "v_mov_b64_e32 v[42:43], 0\n\t"
        "v_mov_b64_e32 v[44:45], 0\n\t"
        "v_mov_b64_e32 v[46:47], 0\n\t"
        "v_mov_b64_e32 v[0:1], 0\n\t"
        "v_mov_b64_e32 v[2:3], 0\n\t"
        "v_mov_b64_e32 v[4:5], 0\n\t"
        "v_mov_b64_e32 v[6:7], 0\n\t"
        "v_mov_b64_e32 v[8:9], 0\n\t"
        "v_mov_b64_e32 v[10:11], 0\n\t"
        "v_mov_b64_e32 v[12:13], 0\n\t"
        "v_mov_b64_e32 v[14:15], 0\n\t"
        "s_add_u32 s46, s26, s69\n\t"
        "s_add_u32 s48, s26, s70\n\t"
        "s_add_u32 s73, s26, s71\n\t"
        "s_add_u32 s74, s26, s72\n\t"
        "s_sleep 1\n\t"
        "buffer_load_dwordx2 v[104:105], v113, s[8:11], s26 offen sc1\n\t"
        "buffer_store_dword v24, v116, s[8:11], s28 offen sc1\n\t"
        "s_xor_b32 s34, s26, 0x100000\n\t"
        "buffer_store_dwordx2 v[24:25], v114, s[8:11], s34 offen sc1\n\t"
        "s_add_u32 s66, s49, s43\n\t"
        "s_cmp_lg_u32 s66, 32\n\t"
        "s_cbranch_scc1 LB_norep%=\n\t"
        "s_waitcnt vmcnt(4)\n\t"
        "s_sub_u32 s66, s24, 1\n\t"
        "v_lshrrev_b32_e32 v68, 16, v64\n\t"
        "v_cmp_eq_u32_e32 vcc, s66, v68\n\t"
        "v_subrev_u32_e32 v64, s59, v64\n\t"
        "v_bfe_i32 v64, v64, 0, 16\n\t"
        "v_cndmask_b32_e32 v64, 0, v64, vcc\n\t"
        "v_lshrrev_b32_e32 v68, 16, v65\n\t"
        "v_cmp_eq_u32_e32 vcc, s66, v68\n\t"
        "v_subrev_u32_e32 v65, s59, v65\n\t"
        "v_bfe_i32 v65, v65, 0, 16\n\t"
        "v_cndmask_b32_e32 v65, 0, v65, vcc\n\t"
        "v_lshrrev_b32_e32 v68, 16, v66\n\t"
        "v_cmp_eq_u32_e32 vcc, s66, v68\n\t"
        "v_subrev_u32_e32 v66, s59, v66\n\t"
        "v_bfe_i32 v66, v66, 0, 16\n\t"
        "v_cndmask_b32_e32 v66, 0, v66, vcc\n\t"
        "v_lshrrev_b32_e32 v68, 16, v67\n\t"
        "v_cmp_eq_u32_e32 vcc, s66, v68\n\t"
        "v_subrev_u32_e32 v67, s59, v67\n\t"
        "v_bfe_i32 v67, v67, 0, 16\n\t"
        "v_cndmask_b32_e32 v67, 0, v67, vcc\n\t"
        "v_max3_i32 v64, v64, v65, v66\n\t"
        "v_max_i32_e32 v64, v64, v67\n\t"
        "s_nop 1\n\t"
        "v_max_i32_dpp v64, v64, v64 quad_perm:[1,0,3,2] row_mask:0xf bank_mask:0xf\n\t"
        "s_nop 1\n\t"
        "v_max_i32_dpp v64, v64, v64 quad_perm:[2,3,0,1] row_mask:0xf bank_mask:0xf\n\t"
        "s_nop 1\n\t"
        "v_max_i32_dpp v64, v64, v64 row_ror:4 row_mask:0xf bank_mask:0xf\n\t"
        "s_nop 1\n\t"
        "v_max_i32_dpp v64, v64, v64 row_ror:8 row_mask:0xf bank_mask:0xf\n\t"
        "v_mov_b32_e32 v65, v64\n\t"
        "s_nop 1\n\t"
        "v_permlane16_swap_b32_e32 v64, v65\n\t"
        "v_max_i32_e32 v64, v64, v65\n\t"
        "v_mov_b32_e32 v65, v64\n\t"
        "s_nop 1\n\t"
        "v_permlane32_swap_b32_e32 v64, v65\n\t"
        "v_max_i32_e32 v64, v64, v65\n\t"
        "s_nop 0\n\t"
        "v_readfirstlane_b32 s64, v64\n\t"
        "LB_norep%=:\n\t"
        "s_waitcnt vmcnt(2) lgkmcnt(0)\n\t"
        "s_mov_b32 s61, s29\n\t"
        "s_mov_b32 s62, s30\n\t"
        "s_mov_b32 s63, s31\n\t"
        "v_and_b32_e32 v30, 0xff, v105\n\t"
        "v_cmp_ne_u32_e32 vcc, s24, v30\n\t"
        "s_nop 0\n\t"
        "s_cbranch_vccnz LB_stre%=\n\t"
        "LB_stok%=:\n\t"
        "v_subrev_u32_e32 v104, s25, v104\n\t"
        "s_nop 1\n\t"
        "v_max_i32_dpp v104, v104, v104 quad_perm:[1,0,3,2] row_mask:0xf bank_mask:0xf\n\t"
        "s_nop 1\n\t"
        "v_max_i32_dpp v104, v104, v104 quad_perm:[2,3,0,1] row_mask:0xf bank_mask:0xf\n\t"
        "s_nop 1\n\t"
        "v_max_i32_dpp v104, v104, v104 row_ror:4 row_mask:0xf bank_mask:0xf\n\t"
        "s_nop 1\n\t"
        "v_max_i32_dpp v104, v104, v104 row_ror:8 row_mask:0xf bank_mask:0xf\n\t"
        "s_nop 1\n\t"
        "v_max_i32_dpp v104, v104, v104 row_bcast:15 row_mask:0xa bank_mask:0xf\n\t"
        "s_nop 1\n\t"
        "v_max_i32_dpp v104, v104, v104 row_bcast:31 row_mask:0xc bank_mask:0xf\n\t"
        "v_readfirstlane_b32 s35, v105\n\t"
        "v_readlane_b32 s36, v104, 63\n\t"
        "s_bfe_u32 s29, s35, 0xc0008\n\t"
        "s_lshr_b32 s30, s35, 20\n\t"
        "s_add_u32 s35, s31, s50\n\t"
        "s_mul_i32 s37, s50, 3\n\t"
        "s_min_u32 s35, s35, s37\n\t"
        "s_add_i32 s34, s36, s51\n\t"
        "s_cmp_gt_i32 s34, 0\n\t"
        "s_cselect_b32 s31, s35, 0\n\t"
        "s_cselect_b32 s34, s34, 0\n\t"
        "s_add_u32 s34, s34, s31\n\t"
        "s_add_u32 s37, s25, s34\n\t"
        "s_add_u32 s45, s37, s29\n\t"
        "s_add_u32 s25, s45, s30\n\t"
        "s_add_u32 s66, s49, s43\n\t"
        "s_cmp_lg_u32 s66, 32\n\t"
        "s_cbranch_scc1 LB_noctl%=\n\t"
        "s_cmp_eq_u32 s24, 0\n\t"
        "s_cbranch_scc1 LB_ctl0%=\n\t"
        "s_cmp_eq_u32 s24, 1\n\t"
        "s_cbranch_scc1 LB_ctl1%=\n\t"
        "s_add_i32 s65, s36, s62\n\t"
        "s_lshl_b32 s66, s65, 1\n\t"
        "s_sub_i32 s66, s66, s56\n\t"
        "s_cmp_gt_i32 s66, 56\n\t"
        "s_cbranch_scc1 LB_c1%=\n\t"
        "s_lshl_b32 s66, s65, 1\n\t"
        "s_add_u32 s34, s56, 8\n\t"
        "s_min_i32 s34, s34, s66\n\t"
        "s_sub_u32 s35, s56, s66\n\t"
        "s_ashr_i32 s35, s35, 2\n\t"
        "s_max_i32 s35, s35, 1\n\t"
        "s_sub_u32 s35, s56, s35\n\t"
        "s_max_i32 s56, s34, s35\n\t"
        "s_branch LB_c2%=\n\t"
        "LB_c1%=:\n\t"
        "s_add_u32 s56, s56, 2\n\t"
        "s_add_i32 s66, s64, s57\n\t"
        "s_cmp_gt_i32 s66, s61\n\t"
        "s_cbranch_scc1 LB_c2%=\n\t"
        "s_add_u32 s57, s57, 4\n\t"
        "s_min_u32 s57, s57, 23\n\t"
        "LB_c2%=:\n\t"
        "s_cmp_lg_u32 s63, 0\n\t"
        "s_cbranch_scc1 LB_c4%=\n\t"
        "s_lshl_b32 s66, s64, 1\n\t"
        "s_sub_i32 s66, s66, s55\n\t"
        "s_cmp_gt_i32 s66, 56\n\t"
        "s_cbranch_scc1 LB_c3%=\n\t"
        "s_lshl_b32 s66, s64, 1\n\t"
        "s_add_u32 s34, s55, 8\n\t"
        "s_min_i32 s34, s34, s66\n\t"
        "s_sub_u32 s35, s55, s66\n\t"
        "s_ashr_i32 s35, s35, 2\n\t"
        "s_max_i32 s35, s35, 1\n\t"
        "s_sub_u32 s35, s55, s35\n\t"
        "s_max_i32 s55, s34, s35\n\t"
        "s_branch LB_c4%=\n\t"
        "LB_c3%=:\n\t"
        "s_add_u32 s55, s55, 2\n\t"
        "s_cmp_lt_i32 s60, s58\n\t"
        "s_cbranch_scc1 LB_c4%=\n\t"
        "s_add_u32 s66, s58, 8\n\t"
        "s_cmp_gt_i32 s60, s66\n\t"
        "s_cbranch_scc1 LB_c4%=\n\t"
        "s_add_u32 s58, s58, 4\n\t"
        "s_min_u32 s58, s58, 20\n\t"
        "LB_c4%=:\n\t"
        "s_and_b32 s66, s24, 3\n\t"
        "s_cmp_eq_u32 s66, 0\n\t"
        "s_cselect_b32 s66, 1, 0\n\t"
        "s_sub_u32 s57, s57, s66\n\t"
        "s_max_u32 s57, s57, 7\n\t"
        "s_sub_u32 s58, s58, s66\n\t"
        "s_max_u32 s58, s58, 4\n\t"
        "s_add_u32 s34, s55, 1\n\t"
        "s_lshr_b32 s34, s34, 1\n\t"
        "s_add_u32 s34, s34, s57\n\t"
        "s_min_u32 s34, s34, 1000\n\t"
        "s_lshl_b32 s34, s34, 8\n\t"
        "s_add_u32 s35, s56, 1\n\t"
        "s_lshr_b32 s35, s35, 1\n\t"
        "s_add_u32 s35, s35, s58\n\t"
        "s_min_u32 s35, s35, 1000\n\t"
        "s_lshl_b32 s35, s35, 20\n\t"
        "s_or_b32 s54, s34, s35\n\t"
        "s_branch LB_ctl1%=\n\t"
        "LB_ctl0%=:\n\t"
        "s_movk_i32 s57, 7\n\t"
        "s_movk_i32 s58, 4\n\t"
        "s_sub_u32 s55, s29, 31\n\t"
        "s_lshl_b32 s55, s55, 1\n\t"
        "s_sub_u32 s56, s30, 16\n\t"
        "s_lshl_b32 s56, s56, 1\n\t"
        "LB_ctl1%=:\n\t"
        "s_mov_b32 s59, s37\n\t"
        "s_sub_u32 s60, 0, s36\n\t"
        "LB_noctl%=:\n\t"
        "s_add_u32 s34, s24, 1\n\t"
        "s_or_b32 s34, s34, s54\n\t"
        "v_mov_b32_e32 v111, s34\n\t"
        "s_mov_b32 s42, 0\n\t"
        "s_sub_u32 s35, s45, 1\n\t"
        "LB_wo%=:\n\t"
        "s_memrealtime s[32:33]\n\t"
        "s_waitcnt lgkmcnt(0)\n\t"
        "s_sub_u32 s34, s35, s32\n\t"
        "s_cmpk_lt_u32 s34, 0x2000\n\t"
        "s_cbranch_scc1 LB_wo%=\n\t"
        "LB_ro%=:\n\t"
        "buffer_load_dwordx4 v[64:67], v115, s[8:11], s46 offen offset:0 sc1\n\t"
        "s_nop 11\n\t"
        "buffer_load_dwordx4 v[68:71], v115, s[8:11], s46 offen offset:1024 sc1\n\t"
        "s_nop 11\n\t"
        "buffer_load_dwordx4 v[72:75], v115, s[8:11], s48 offen offset:0 sc1\n\t"
        "s_nop 11\n\t"
        "buffer_load_dwordx4 v[76:79], v115, s[8:11], s48 offen offset:1024 sc1\n\t"
        "s_nop 11\n\t"
        "buffer_load_dwordx4 v[80:83], v115, s[8:11], s73 offen offset:0 sc1\n\t"
        "s_nop 11\n\t"
        "buffer_load_dwordx4 v[84:87], v115, s[8:11], s73 offen offset:1024 sc1\n\t"
        "s_nop 11\n\t"
        "buffer_load_dwordx4 v[88:91], v115, s[8:11], s74 offen offset:0 sc1\n\t"
        "s_nop 11\n\t"
        "buffer_load_dwordx4 v[92:95], v115, s[8:11], s74 offen offset:1024 sc1\n\t"
        "LB_go2%=:\n\t"
        "s_waitcnt vmcnt(7)\n\t"
        "v_smfmac_f32_16x16x64_bf16 v[32:35], v[64:67], a[0:7], v106\n\t"
        "v_max3_u32 v108, v64, v65, v66\n\t"
        "v_max_u32_e32 v108, v67, v108\n\t"
        "v_smfmac_f32_16x16x64_bf16 v[40:43], v[64:67], a[16:23], v106\n\t"
        "v_smfmac_f32_16x16x64_bf16 v[36:39], v[64:67], a[8:15], v106\n\t"
        "v_smfmac_f32_16x16x64_bf16 v[44:47], v[64:67], a[24:31], v106\n\t"
        "s_waitcnt vmcnt(6)\n\t"
        "v_smfmac_f32_16x16x64_bf16 v[32:35], v[68:71], a[32:39], v106\n\t"
        "v_max3_u32 v108, v68, v69, v108\n\t"
        "v_max3_u32 v108, v70, v71, v108\n\t"
        "v_smfmac_f32_16x16x64_bf16 v[40:43], v[68:71], a[48:55], v106\n\t"
        "v_smfmac_f32_16x16x64_bf16 v[36:39], v[68:71], a[40:47], v106\n\t"
        "v_smfmac_f32_16x16x64_bf16 v[44:47], v[68:71], a[56:63], v106\n\t"
        "s_waitcnt vmcnt(5)\n\t"
        "v_smfmac_f32_16x16x64_bf16 v[32:35], v[72:75], a[64:71], v106\n\t"
        "v_max3_u32 v108, v72, v73, v108\n\t"
        "v_max3_u32 v108, v74, v75, v108\n\t"
        "v_smfmac_f32_16x16x64_bf16 v[40:43], v[72:75], a[80:87], v106\n\t"
        "v_smfmac_f32_16x16x64_bf16 v[36:39], v[72:75], a[72:79], v106\n\t"
        "v_smfmac_f32_16x16x64_bf16 v[44:47], v[72:75], a[88:95], v106\n\t"
        "s_waitcnt vmcnt(4)\n\t"
        "v_smfmac_f32_16x16x64_bf16 v[32:35], v[76:79], a[96:103], v106\n\t"
        "v_max3_u32 v108, v76, v77, v108\n\t"
        "v_max3_u32 v108, v78, v79, v108\n\t"
        "v_smfmac_f32_16x16x64_bf16 v[40:43], v[76:79], a[112:119], v106\n\t"
        "v_smfmac_f32_16x16x64_bf16 v[36:39], v[76:79], a[104:111], v106\n\t"
        "v_smfmac_f32_16x16x64_bf16 v[44:47], v[76:79], a[120:127], v106\n\t"
        "s_waitcnt vmcnt(3)\n\t"
        "v_smfmac_f32_16x16x64_bf16 v[32:35], v[80:83], a[128:135], v106\n\t"
        "v_max3_u32 v108, v80, v81, v108\n\t"
        "v_max3_u32 v108, v82, v83, v108\n\t"
        "v_smfmac_f32_16x16x64_bf16 v[40:43], v[80:83], a[144:151], v106\n\t"
        "v_smfmac_f32_16x16x64_bf16 v[36:39], v[80:83], a[136:143], v106\n\t"
        "v_smfmac_f32_16x16x64_bf16 v[44:47], v[80:83], a[152:159], v106\n\t"
        "s_waitcnt vmcnt(2)\n\t"
        "v_smfmac_f32_16x16x64_bf16 v[32:35], v[84:87], a[160:167], v106\n\t"
        "v_max3_u32 v108, v84, v85, v108\n\t"
        "v_max3_u32 v108, v86, v87, v108\n\t"
        "v_smfmac_f32_16x16x64_bf16 v[40:43], v[84:87], a[176:183], v106\n\t"
        "v_smfmac_f32_16x16x64_bf16 v[36:39], v[84:87], a[168:175], v106\n\t"
        "v_smfmac_f32_16x16x64_bf16 v[44:47], v[84:87], a[184:191], v106\n\t"
        "s_waitcnt vmcnt(1)\n\t"
        "v_smfmac_f32_16x16x64_bf16 v[32:35], v[88:91], a[192:199], v106\n\t"
        "v_max3_u32 v108, v88, v89, v108\n\t"
        "v_max3_u32 v108, v90, v91, v108\n\t"
        "v_smfmac_f32_16x16x64_bf16 v[40:43], v[88:91], a[208:215], v106\n\t"
        "v_smfmac_f32_16x16x64_bf16 v[36:39], v[88:91], a[200:207], v106\n\t"
        "v_smfmac_f32_16x16x64_bf16 v[44:47], v[88:91], a[216:223], v106\n\t"
        "s_waitcnt vmcnt(0)\n\t"
        "v_smfmac_f32_16x16x64_bf16 v[32:35], v[92:95], a[224:231], v106\n\t"
        "v_max3_u32 v108, v92, v93, v108\n\t"
        "v_max3_u32 v108, v94, v95, v108\n\t"
        "v_smfmac_f32_16x16x64_bf16 v[40:43], v[92:95], a[240:247], v106\n\t"
        "v_cmp_eq_u32_e32 vcc, -1, v108\n\t"
        "v_smfmac_f32_16x16x64_bf16 v[36:39], v[92:95], a[232:239], v106\n\t"
        "v_smfmac_f32_16x16x64_bf16 v[44:47], v[92:95], a[248:255], v106\n\t"
        "s_cbranch_vccnz LB_rofail%=\n\t"
        "s_nop 0\n\t"
        "v_add_f32_e32 v32, v32, v33\n\t"
        "s_nop 1\n\t"
        "v_add_f32_e32 v40, v42, v43\n\t"
        "v_add_f32_e32 v32, v32, v40\n\t"
        "v_add_f32_e32 v36, v36, v37\n\t"
        "v_add_f32_e32 v44, v46, v47\n\t"
        "v_add_f32_e32 v36, v36, v44\n\t"
        "ds_write2_b32 v120, v32, v36 offset0:0 offset1:64\n\t"
        "s_waitcnt lgkmcnt(0)\n\t"
        "s_barrier\n\t"
        "ds_read_b64 v[64:65], v121 offset:0\n\t"
        "ds_read_b64 v[66:67], v121 offset:512\n\t"
        "ds_read_b64 v[68:69], v121 offset:1024\n\t"
        "ds_read_b64 v[70:71], v121 offset:1536\n\t"
        "s_waitcnt lgkmcnt(2)\n\t"
        "v_pk_add_f32 v[64:65], v[64:65], v[66:67]\n\t"
        "s_waitcnt lgkmcnt(0)\n\t"
        "v_pk_add_f32 v[68:69], v[68:69], v[70:71]\n\t"
        "v_pk_add_f32 v[64:65], v[64:65], v[68:69]\n\t"
        "v_cvt_pk_bf16_f32 v64, v64, v65\n\t"
        "buffer_store_dword v64, v116, s[8:11], s27 offen sc1\n\t"
        "s_memrealtime s[32:33]\n\t"
        "s_waitcnt lgkmcnt(0)\n\t"
        "v_mov_b32_e32 v110, s32\n\t"
        "s_add_u32 s24, s24, 1\n\t"
        "s_mov_b32 s28, s26\n\t"
        "s_mov_b32 s26, s27\n\t"
        "s_add_u32 s27, s27, 0x80000\n\t"
        "s_and_b32 s27, s27, 0x1fffff\n\t"
        "s_branch LB_loop%=\n\t"
        "LB_end%=:\n\t"
        "s_endpgm\n\t"
        "LB_stre%=:\n\t"
        "s_mov_b32 s42, 0\n\t"
        "LB_str2%=:\n\t"
        "buffer_load_dwordx2 v[104:105], v113, s[8:11], s26 offen sc1\n\t"
        "s_waitcnt vmcnt(0)\n\t"
        "v_and_b32_e32 v30, 0xff, v105\n\t"
        "v_cmp_ne_u32_e32 vcc, s24, v30\n\t"
        "s_nop 0\n\t"
        "s_cbranch_vccz LB_stok%=\n\t"
        "s_add_u32 s42, s42, 1\n\t"
        "s_cmp_lt_u32 s42, 0x100000\n\t"
        "s_cbranch_scc1 LB_str2%=\n\t"
        "s_movk_i32 s34, 5\n\t"
        "s_branch LB_die%=\n\t"
        "LB_rdfail%=:\n\t"
        "s_or_b32 s47, s47, 1\n\t"
        "buffer_load_dwordx4 v[16:19], v112, s[8:11], s26 offen offset:0 sc1\n\t"
        "buffer_load_dwordx4 v[20:23], v112, s[8:11], s26 offen offset:1024 sc1\n\t"
        "buffer_load_dwordx4 v[24:27], v112, s[8:11], s26 offen offset:2048 sc1\n\t"
        "buffer_load_dwordx4 v[28:31], v112, s[8:11], s26 offen offset:3072 sc1\n\t"
        "s_nop 7\n\t"
        "v_mov_b64_e32 v[0:1], 0\n\t"
        "v_mov_b64_e32 v[2:3], 0\n\t"
        "v_mov_b64_e32 v[4:5], 0\n\t"
        "v_mov_b64_e32 v[6:7], 0\n\t"
        "v_mov_b64_e32 v[8:9], 0\n\t"
        "v_mov_b64_e32 v[10:11], 0\n\t"
        "v_mov_b64_e32 v[12:13], 0\n\t"
        "v_mov_b64_e32 v[14:15], 0\n\t"
        "s_branch LB_rdre%=w\n\t"
        "LB_rdre%=:\n\t"
        "buffer_load_dwordx4 v[16:19], v112, s[8:11], s26 offen offset:0 sc1\n\t"
        "buffer_load_dwordx4 v[20:23], v112, s[8:11], s26 offen offset:1024 sc1\n\t"
        "buffer_load_dwordx4 v[24:27], v112, s[8:11], s26 offen offset:2048 sc1\n\t"
        "buffer_load_dwordx4 v[28:31], v112, s[8:11], s26 offen offset:3072 sc1\n\t"
        "LB_rdre%=w:\n\t"
        "s_waitcnt vmcnt(0) lgkmcnt(0)\n\t"
        "v_max3_u32 v108, v16, v17, v18\n\t"
        "v_max3_u32 v108, v108, v19, v20\n\t"
        "v_max3_u32 v108, v108, v21, v22\n\t"
        "v_max3_u32 v108, v108, v23, v24\n\t"
        "v_max3_u32 v108, v108, v25, v26\n\t"
        "v_max3_u32 v108, v108, v27, v28\n\t"
        "v_max3_u32 v108, v108, v29, v30\n\t"
        "v_max_u32_e32 v108, v108, v31\n\t"
        "v_cmp_eq_u32_e32 vcc, -1, v108\n\t"
        "s_nop 0\n\t"
        "s_cbranch_vccz LB_go%=\n\t"
        "s_add_u32 s42, s42, 1\n\t"
        "s_cmp_lt_u32 s42, 0x100000\n\t"
        "s_cbranch_scc1 LB_rdre%=\n\t"
        "s_movk_i32 s34, 3\n\t"
        "s_branch LB_die%=\n\t"
        "LB_rofail%=:\n\t"
        "s_or_b32 s47, s47, 4\n\t"
        "buffer_load_dwordx4 v[64:67], v115, s[8:11], s46 offen offset:0 sc1\n\t"
        "buffer_load_dwordx4 v[68:71], v115, s[8:11], s46 offen offset:1024 sc1\n\t"
        "buffer_load_dwordx4 v[72:75], v115, s[8:11], s48 offen offset:0 sc1\n\t"
        "buffer_load_dwordx4 v[76:79], v115, s[8:11], s48 offen offset:1024 sc1\n\t"
        "buffer_load_dwordx4 v[80:83], v115, s[8:11], s73 offen offset:0 sc1\n\t"
        "buffer_load_dwordx4 v[84:87], v115, s[8:11], s73 offen offset:1024 sc1\n\t"
        "buffer_load_dwordx4 v[88:91], v115, s[8:11], s74 offen offset:0 sc1\n\t"
        "buffer_load_dwordx4 v[92:95], v115, s[8:11], s74 offen offset:1024 sc1\n\t"
        "s_nop 7\n\t"
        "v_mov_b64_e32 v[32:33], 0\n\t"
        "v_mov_b64_e32 v[34:35], 0\n\t"
        "v_mov_b64_e32 v[36:37], 0\n\t"
        "v_mov_b64_e32 v[38:39], 0\n\t"
        "v_mov_b64_e32 v[40:41], 0\n\t"
        "v_mov_b64_e32 v[42:43], 0\n\t"
        "v_mov_b64_e32 v[44:45], 0\n\t"
        "v_mov_b64_e32 v[46:47], 0\n\t"
        "s_branch LB_rore%=w\n\t"
        "LB_rore%=:\n\t"
        "buffer_load_dwordx4 v[64:67], v115, s[8:11], s46 offen offset:0 sc1\n\t"
        "buffer_load_dwordx4 v[68:71], v115, s[8:11], s46 offen offset:1024 sc1\n\t"
        "buffer_load_dwordx4 v[72:75], v115, s[8:11], s48 offen offset:0 sc1\n\t"
        "buffer_load_dwordx4 v[76:79], v115, s[8:11], s48 offen offset:1024 sc1\n\t"
        "buffer_load_dwordx4 v[80:83], v115, s[8:11], s73 offen offset:0 sc1\n\t"
        "buffer_load_dwordx4 v[84:87], v115, s[8:11], s73 offen offset:1024 sc1\n\t"
        "buffer_load_dwordx4 v[88:91], v115, s[8:11], s74 offen offset:0 sc1\n\t"
        "buffer_load_dwordx4 v[92:95], v115, s[8:11], s74 offen offset:1024 sc1\n\t"
        "LB_rore%=w:\n\t"
        "s_waitcnt vmcnt(0) lgkmcnt(0)\n\t"
        "v_max3_u32 v108, v64, v65, v66\n\t"
        "v_max3_u32 v108, v108, v67, v68\n\t"
        "v_max3_u32 v108, v108, v69, v70\n\t"
        "v_max3_u32 v108, v108, v71, v72\n\t"
        "v_max3_u32 v108, v108, v73, v74\n\t"
        "v_max3_u32 v108, v108, v75, v76\n\t"
        "v_max3_u32 v108, v108, v77, v78\n\t"
        "v_max3_u32 v108, v108, v79, v80\n\t"
        "v_max3_u32 v108, v108, v81, v82\n\t"
        "v_max3_u32 v108, v108, v83, v84\n\t"
        "v_max3_u32 v108, v108, v85, v86\n\t"
        "v_max3_u32 v108, v108, v87, v88\n\t"
        "v_max3_u32 v108, v108, v89, v90\n\t"
        "v_max3_u32 v108, v108, v91, v92\n\t"
        "v_max3_u32 v108, v108, v93, v94\n\t"
        "v_max_u32_e32 v108, v108, v95\n\t"
        "v_cmp_eq_u32_e32 vcc, -1, v108\n\t"
        "s_nop 0\n\t"
        "s_cbranch_vccz LB_go2%=\n\t"
        "s_add_u32 s42, s42, 1\n\t"
        "s_cmp_lt_u32 s42, 0x100000\n\t"
        "s_cbranch_scc1 LB_rore%=\n\t"
        "s_movk_i32 s34, 4\n\t"
        "s_branch LB_die%=\n\t"
        "LB_die%=:\n\t"
        "v_mov_b32_e32 v30, s34\n\t"
        "s_mov_b32 s35, 33024\n\t"
        "s_mov_b64 exec, 1\n\t"
        "buffer_store_dword v30, off, s[12:15], s35 sc1\n\t"
        "s_endpgm\n\t"
        ".Lk_end_B:\n\t"
        :: REC_S_IN, "{v96}"(g_v), "{v97}"(g_o), "{v98}"(wb0), "{v99}"(wb1), "{v100}"(rb0), "{v101}"(rb0 ^ 32u), "{v102}"(rb0 ^ 64u), "{v103}"(rb0 ^ 96u),
           "{v106}"((l & 1) ? 0xEEEEEEEEu : 0x44444444u), "{v[108:109]}"(m0p), "{s68}"(rot),
           "{v112}"(a_mem), "{v113}"((24 + cp) * 16384 + l * ST_P), "{v114}"(a_vmw), "{v115}"(a_or), "{v116}"(a_mw), "{v117}"(a_sw),
           "{v118}"(lb + w * 2048 + (g >> 1) * 256 + n * 16 + (g & 1) * 8), "{v119}"(lb + (w & 1) * 1024 + l * 16), "{v120}"(lb + LB_R + w * 512 + l * 4),
           "{v121}"(lb + LB_R + l * 8), "{v122}"(stg + l * 16), "{v123}"(o_r), "{v124}"(rb + w == 32 ? 28 * 16384 + (l & 31) * 128 : OOB)
        : "memory", "vcc", "scc", "v0", "v255", "a0", "a255", "s101");
    __builtin_unreachable();
}
__device__ __forceinline__ void rec_helper(const Params& p, rsrc_t r, unsigned char* lds, const int wv, const int hi) {
    const int tid = otid(), l = tid & 63;
    const rsrc_t rh = __builtin_amdgcn_make_buffer_rsrc(p.ws + hx_base(p.ws), 0, HX_BYTES, 0x00020000);
    const v4u ones = {0xFFFFFFFFu, 0xFFFFFFFFu, 0xFFFFFFFFu, 0xFFFFFFFFu};
    unsigned best0 = ~0u, best1 = ~0u, d[5] = {0u, 0u, 0u, 0u, 0u};
    const bool meas = (hi & 7) < 2;
    if (meas) {
        const unsigned ca = l >= 8 ? OOB : (31 + wv * 32) * 16384 + hi * 256 + l * 16;
        const unsigned tend = rtnow() + CAL_T;
#pragma unroll 1
        for (int i = 0; i < 64 && (i < 3 || (int)(rtnow() - tend) < 0); ++i) {
            const unsigned t0 = (unsigned)__builtin_amdgcn_s_memtime(); __builtin_amdgcn_s_waitcnt(0);
            st16(rh, ca, ones); DRAIN();
            const unsigned t1 = (unsigned)__builtin_amdgcn_s_memtime(); __builtin_amdgcn_s_waitcnt(0);
            st16(rh, ca + 8192, ones); DRAIN();
            const unsigned t2 = (unsigned)__builtin_amdgcn_s_memtime(); __builtin_amdgcn_s_waitcnt(0);
            best0 = min(best0, t1 - t0); best1 = min(best1, t2 - t1); }
        if (l < 2) __hip_atomic_fetch_max((unsigned*)(p.ws + F_CAL + 128) + l, 65535u - min(l ? best1 : best0, 65535u), __ATOMIC_RELAXED, __HIP_MEMORY_SCOPE_AGENT);
    } else if (TOUCH) {
        const int xq = hi >> 3, k = ((hi & 7) - 2) * 4 + wv;
#pragma unroll
        for (int i = 0; i < 5; ++i) { const bf16_t* q;
            if (i & 1) { const int z = (i >> 1) * 24 + k, st = z & 3, c = 4 + (z >> 2);
                q = ((st & 1) ? p.Wk : p.Wq) + (size_t)(((((st & 1) * 2 + 1) * 4 + c) & 15) * 64 + l) * 4096 + (xq * 8 + 1 + (st >> 1) * 4) * 128 + 64; }
            else { const int z = min((i >> 1) * 24 + k, 63);
                q = p.Wo + (size_t)(((z & 3) * 8 + (z >> 3)) * 128 + ((z >> 2) & 1) * 64 + l) * 1024 + (RPL == 4 ? (xq >> 1) : (xq & 1)) * 512 + 192; if (RPL == 4 && (xq & 1)) q = p.gate_w; }
            asm volatile("global_load_dword %0, %1, off" : "+v"(d[i]) : "v"(q) : "memory"); }
    }
#pragma unroll
    for (int i = 0; i < 16; ++i) st16(rh, hi * 65536 + i * 4096 + tid * 16, ones);
    asm volatile("s_waitcnt vmcnt(0)" : "+v"(d[0]), "+v"(d[1]), "+v"(d[2]), "+v"(d[3]), "+v"(d[4]) :: "memory");
    __syncthreads();
    if (tid == 0 && __hip_atomic_fetch_add((unsigned*)(p.ws + F_CAL + 256), 1u, __ATOMIC_RELAXED, __HIP_MEMORY_SCOPE_AGENT) == 31u) {
        const v2u c = ld8(r, F_CAL + 128); st4(r, F_CAL, c.x < c.y ? 8193u : 1u); }
}
using f32x16 = __attribute__((ext_vector_type(16))) float;
constexpr int CV_RP = 80;
constexpr int CV_U = 96 * CV_RP;
constexpr int CV_KP = 4416;
constexpr int CV_K0 = 8 * CV_U;
__device__ __forceinline__ float blo(unsigned x) { return __uint_as_float(x << 16); }
__device__ __forceinline__ float bhi(unsigned x) { return __uint_as_float(x & 0xFFFF0000u); }
#define NAP() do { __builtin_amdgcn_sched_barrier(0); __builtin_amdgcn_s_sleep(XSLP); __builtin_amdgcn_sched_barrier(0); } while (0)
__device__ __forceinline__ void gate_block(const Params& p, rsrc_t r, const int hi, const int wv) {
    const int l = otid() & 63, t0 = hi * 64 + wv * 16;
    v4u xg[16][2], gw[2];
#pragma unroll
    for (int q = 0; q < 2; ++q) gw[q] = *(const v4u*)(p.gate_w + q * 512 + l * 8);
#pragma unroll
    for (int i = 0; i < 16; ++i)
#pragma unroll
        for (int q = 0; q < 2; ++q) { xg[i][q] = *(const v4u*)(p.Aq + (size_t)(t0 + i) * D + q * 512 + l * 8); NAP(); }
    float mine = 0.f;
#pragma unroll
    for (int i = 0; i < 16; ++i) { float acc = 0.f;
#pragma unroll
        for (int q = 0; q < 2; ++q)
#pragma unroll
            for (int e = 0; e < 4; ++e) { const unsigned pr = pack2(blo(xg[i][q][e]) * blo(gw[q][e]), bhi(xg[i][q][e]) * bhi(gw[q][e])); acc += blo(pr); acc += bhi(pr); }
        for (int o = 32; o > 0; o >>= 1) acc += __shfl_xor(acc, o);
        if (l == i) mine = acc; }
    if (l < 16) { const float z = rbfh(mine); st4(r, W_GATE + (t0 + l) * 4, __float_as_uint(rbfh(1.f / (1.f + expf(-z))))); }
}
__device__ __forceinline__ void conv_prep(const Params& p, rsrc_t r, const int wv, const int hi) {
    const int tid = otid(), Q = tid >> 2, cg = tid & 3, ch0 = hi * 32 + cg * 8;
    v4u sk[4]; float sv[8][4];
#pragma unroll
    for (int q = 0; q < 4; ++q) sk[q] = *(const v4u*)(p.shift_k + ch0 * 4 + q * 8);
#pragma unroll
    for (int c8 = 0; c8 < 8; ++c8)
#pragma unroll
        for (int tp = 0; tp < 4; ++tp) { const unsigned sw = sk[c8 >> 1][(c8 & 1) * 2 + (tp >> 1)]; sv[c8][tp] = (tp & 1) ? bhi(sw) : blo(sw); }
    const rsrc_t rk = __builtin_amdgcn_make_buffer_rsrc((void*)p.Ak, 0, S * D * 2, 0x00020000), rv = __builtin_amdgcn_make_buffer_rsrc((void*)p.Av, 0, S * D * 2, 0x00020000),
                 rq = __builtin_amdgcn_make_buffer_rsrc((void*)p.Aq, 0, S * D * 2, 0x00020000);
#pragma unroll 1
    for (int G = 0; G < 4; ++G) {
        const unsigned ro = (unsigned)((Q * 32 + G * 8) * D + ch0) * 2u, uo = hi * 131072 + tid * 16;
        v4u kr[11], u[8], qv[8];
#pragma unroll
        for (int i = 0; i < 11; ++i) { kr[i] = __builtin_amdgcn_raw_buffer_load_b128(rk, (i < 3 && Q + G == 0) ? OOB : ro + (i - 3) * 2048, 0, 0); NAP(); }
#pragma unroll
        for (int i = 0; i < 8; ++i) { u[i] = __builtin_amdgcn_raw_buffer_load_b128(rv, ro + i * 2048, 0, 0); NAP(); }
#pragma unroll
        for (int i = 0; i < 8; ++i) { qv[i] = __builtin_amdgcn_raw_buffer_load_b128(rq, ro + i * 2048, 0, 0); NAP(); }
#pragma unroll
        for (int q = 0; q < 4; ++q) {
#pragma unroll
            for (int i = 0; i < 8; ++i) {
                unsigned ks = pack2(blo(kr[i][q]) * sv[2 * q][3], bhi(kr[i][q]) * sv[2 * q + 1][3]), pr;
#pragma unroll
                for (int d = 1; d < 4; ++d) { pr = pack2(blo(kr[i + d][q]) * sv[2 * q][3 - d], bhi(kr[i + d][q]) * sv[2 * q + 1][3 - d]); ks = pack2(blo(ks) + blo(pr), bhi(ks) + bhi(pr)); }
                u[i][q] = pack2(blo(ks) * blo(u[i][q]), bhi(ks) * bhi(u[i][q])); }
            v4u lo, hh;
#pragma unroll
            for (int z = 0; z < 4; ++z) { const unsigned x = u[7 - 2 * z][q], y = u[6 - 2 * z][q]; lo[z] = __builtin_amdgcn_perm(y, x, 0x05040100u); hh[z] = __builtin_amdgcn_perm(y, x, 0x07060302u); }
            st16(r, W_U + uo + q * 32768 + G * 4096, lo); NAP(); st16(r, W_U + uo + q * 32768 + 16384 + G * 4096, hh); NAP();
#pragma unroll
            for (int z = 0; z < 2; ++z) { st16(r, W_Y + uo + q * 32768 + (2 * G + z) * 4096, (v4u){qv[4 * z][q], qv[4 * z + 1][q], qv[4 * z + 2][q], qv[4 * z + 3][q]}); NAP(); }
        }
    }
    DRAIN(); __syncthreads();
    if (tid == 0) __hip_atomic_fetch_add((unsigned*)(p.ws + F_CONV), 1u, __ATOMIC_RELAXED, __HIP_MEMORY_SCOPE_AGENT);
}
__device__ __forceinline__ bool conv_core(const Params& p, rsrc_t r, unsigned char* lds, const int wv, const int un) {
    const int tid = otid(), w = wv, l = tid & 63, Q = tid >> 2, cg = tid & 3;
    const rsrc_t rs = __builtin_amdgcn_make_buffer_rsrc((void*)p.ssm_k, 0, S * D * 2, 0x00020000);
    v4u kraw[4]; v2u kext[4];
    auto kload = [&](int n) { const unsigned o = (unsigned)(((un >> 2) * 32 + w * 8 + (un & 3) * 2 + n) * S + l * 8) * 2u;
#pragma unroll
        for (int i = 0; i < 4; ++i) { kraw[i] = __builtin_amdgcn_raw_buffer_load_b128(rs, o + i * 1024, 0, CV_POL);
            kext[i] = __builtin_amdgcn_raw_buffer_load_b64(rs, (i == 3 && l == 63) ? OOB : o + i * 1024 + 16, 0, 0); } };
    kload(0);
    if (!wait_word(r, F_CONV, 32u, 2048u)) return false;
    v4u ur[8], qr[8];
#pragma unroll
    for (int i = 0; i < 8; ++i) ur[i] = ld16(r, W_U + un * 32768 + i * 4096 + tid * 16);
#pragma unroll
    for (int i = 0; i < 8; ++i) qr[i] = ld16(r, W_Y + un * 32768 + i * 4096 + tid * 16);
    const unsigned dv = *(const unsigned*)(p.ssm_D + (un >> 2) * 32 + cg * 8 + (un & 3) * 2);
    for (int i = tid; i < 8 * 160; i += 256) *(v4u*)(lds + (i / 160) * CV_U + (i % 160) * 16) = (v4u){0u, 0u, 0u, 0u};
    unsigned char* kc = lds + CV_K0 + w * 4 * CV_KP;
    if (l < 32) {
#pragma unroll
        for (int q = 0; q < 4; ++q) *(unsigned*)(kc + q * CV_KP + l * 4) = 0u; }
    const int b = l & 31, kg = l >> 5;
    const unsigned kb = (unsigned)(size_t)kc + (b & 3) * CV_KP + ((b & 28) - 8 * kg + 56 - 16) * 2, ub0 = (unsigned)(size_t)lds + (1 + b) * CV_RP + kg * 16;
    unsigned char* const urow = lds + 2 * cg * CV_U + (32 + Q) * CV_RP;
#pragma unroll
    for (int i = 0; i < 8; ++i) *(v4u*)(urow + (i >> 2) * CV_U + (i & 3) * 16) = ur[i];
    __syncthreads();
#pragma unroll 1
        for (int ci = 0; ci < 2; ++ci) {
            const int c8 = 2 * w + ci;
#pragma unroll
            for (int i = 0; i < 4; ++i) { const v4u e = kraw[i]; const v2u f = kext[i]; unsigned char* q = kc + 128 + (i * 64 + l) * 16;
                const unsigned s0 = __builtin_amdgcn_alignbit(e.y, e.x, 16), s1 = __builtin_amdgcn_alignbit(e.z, e.y, 16), s2 = __builtin_amdgcn_alignbit(e.w, e.z, 16),
                               s3 = __builtin_amdgcn_alignbit(f.x, e.w, 16), s4 = __builtin_amdgcn_alignbit(f.y, f.x, 16);
                *(v4u*)q = (v4u){s0, s1, s2, s3}; *(v4u*)(q + CV_KP) = (v4u){e.y, e.z, e.w, f.x}; *(v4u*)(q + 2 * CV_KP) = (v4u){s1, s2, s3, s4}; *(v4u*)(q + 3 * CV_KP) = (v4u){e.z, e.w, f.x, f.y};
                if (i == 0 && l == 0) {
                    *(v2u*)(q - 8) = (v2u){0u, e.x << 16}; *(v2u*)(q + CV_KP - 8) = (v2u){0u, e.x}; *(v2u*)(q + 2 * CV_KP - 8) = (v2u){e.x << 16, s0}; *(v2u*)(q + 3 * CV_KP - 8) = (v2u){e.x, e.y}; } }
            if (ci == 0) kload(1);
            __builtin_amdgcn_wave_barrier();
            f32x16 acc0, acc1;
            asm volatile(
                "ds_read_b64 v[192:193], %[kb] offset:32\n\t"
                "ds_read_b64 v[194:195], %[kb] offset:40\n\t"
                "ds_read_b128 v[196:199], %[ub] offset:5040\n\t"
                "ds_read_b128 v[200:203], %[ub] offset:2480\n\t"
                "ds_read_b64 v[204:205], %[kb] offset:2080\n\t"
                "ds_read_b64 v[206:207], %[kb] offset:2088\n\t"
                "ds_read_b64 v[208:209], %[kb] offset:0\n\t"
                "ds_read_b64 v[210:211], %[kb] offset:8\n\t"
                "ds_read_b128 v[212:215], %[ub] offset:5072\n\t"
                "ds_read_b128 v[216:219], %[ub] offset:2512\n\t"
                "ds_read_b64 v[220:221], %[kb] offset:2048\n\t"
                "ds_read_b64 v[222:223], %[kb] offset:2056\n\t"
                "s_waitcnt lgkmcnt(9)\n\t"
                "v_mfma_f32_32x32x16_bf16 %[a1], v[192:195], v[196:199], 0\n\t"
                "ds_read_b64 v[224:225], %[kb] offset:96\n\t"
                "ds_read_b64 v[226:227], %[kb] offset:104\n\t"
                "ds_read_b128 v[228:231], %[ub] offset:4960\n\t"
                "s_waitcnt lgkmcnt(11)\n\t"
                "v_mfma_f32_32x32x16_bf16 %[a0], v[192:195], v[200:203], 0\n\t"
                "ds_read_b128 v[232:235], %[ub] offset:2400\n\t"
                "s_waitcnt lgkmcnt(10)\n\t"
                "v_mfma_f32_32x32x16_bf16 %[a1], v[204:207], v[200:203], %[a1]\n\t"
                "ds_read_b64 v[236:237], %[kb] offset:2144\n\t"
                "ds_read_b64 v[238:239], %[kb] offset:2152\n\t"
                "s_waitcnt lgkmcnt(9)\n\t"
                "v_mfma_f32_32x32x16_bf16 %[a1], v[208:211], v[212:215], %[a1]\n\t"
                "ds_read_b64 v[240:241], %[kb] offset:64\n\t"
                "ds_read_b64 v[242:243], %[kb] offset:72\n\t"
                "ds_read_b128 v[244:247], %[ub] offset:4992\n\t"
                "s_waitcnt lgkmcnt(11)\n\t"
                "v_mfma_f32_32x32x16_bf16 %[a0], v[208:211], v[216:219], %[a0]\n\t"
                "ds_read_b128 v[248:251], %[ub] offset:2432\n\t"
                "s_waitcnt lgkmcnt(10)\n\t"
                "v_mfma_f32_32x32x16_bf16 %[a1], v[220:223], v[216:219], %[a1]\n\t"
                "ds_read_b64 v[252:253], %[kb] offset:2112\n\t"
                "ds_read_b64 v[254:255], %[kb] offset:2120\n\t"
                "s_waitcnt lgkmcnt(9)\n\t"
                "v_mfma_f32_32x32x16_bf16 %[a1], v[224:227], v[228:231], %[a1]\n\t"
                "ds_read_b64 v[192:193], %[kb] offset:160\n\t"
                "ds_read_b64 v[194:195], %[kb] offset:168\n\t"
                "ds_read_b128 v[196:199], %[ub] offset:4880\n\t"
                "s_waitcnt lgkmcnt(11)\n\t"
                "v_mfma_f32_32x32x16_bf16 %[a0], v[224:227], v[232:235], %[a0]\n\t"
                "ds_read_b128 v[200:203], %[ub] offset:2320\n\t"
                "s_waitcnt lgkmcnt(10)\n\t"
                "v_mfma_f32_32x32x16_bf16 %[a1], v[236:239], v[232:235], %[a1]\n\t"
                "ds_read_b64 v[204:205], %[kb] offset:2208\n\t"
                "ds_read_b64 v[206:207], %[kb] offset:2216\n\t"
                "s_waitcnt lgkmcnt(9)\n\t"
                "v_mfma_f32_32x32x16_bf16 %[a1], v[240:243], v[244:247], %[a1]\n\t"
                "ds_read_b64 v[208:209], %[kb] offset:128\n\t"
                "ds_read_b64 v[210:211], %[kb] offset:136\n\t"
                "ds_read_b128 v[212:215], %[ub] offset:4912\n\t"
                "s_waitcnt lgkmcnt(11)\n\t"
                "v_mfma_f32_32x32x16_bf16 %[a0], v[240:243], v[248:251], %[a0]\n\t"
                "ds_read_b128 v[216:219], %[ub] offset:2352\n\t"
                "s_waitcnt lgkmcnt(10)\n\t"
                "v_mfma_f32_32x32x16_bf16 %[a1], v[252:255], v[248:251], %[a1]\n\t"
                "ds_read_b64 v[220:221], %[kb] offset:2176\n\t"
                "ds_read_b64 v[222:223], %[kb] offset:2184\n\t"
                "s_waitcnt lgkmcnt(9)\n\t"
                "v_mfma_f32_32x32x16_bf16 %[a1], v[192:195], v[196:199], %[a1]\n\t"
                "ds_read_b64 v[224:225], %[kb] offset:224\n\t"
                "ds_read_b64 v[226:227], %[kb] offset:232\n\t"
                "ds_read_b128 v[228:231], %[ub] offset:4800\n\t"
                "s_waitcnt lgkmcnt(11)\n\t"
                "v_mfma_f32_32x32x16_bf16 %[a0], v[192:195], v[200:203], %[a0]\n\t"
                "ds_read_b128 v[232:235], %[ub] offset:2240\n\t"
                "s_waitcnt lgkmcnt(10)\n\t"
                "v_mfma_f32_32x32x16_bf16 %[a1], v[204:207], v[200:203], %[a1]\n\t"
                "ds_read_b64 v[236:237], %[kb] offset:2272\n\t"
                "ds_read_b64 v[238:239], %[kb] offset:2280\n\t"
                "s_waitcnt lgkmcnt(9)\n\t"
                "v_mfma_f32_32x32x16_bf16 %[a1], v[208:211], v[212:215], %[a1]\n\t"
                "ds_read_b64 v[240:241], %[kb] offset:192\n\t"
                "ds_read_b64 v[242:243], %[kb] offset:200\n\t"
                "ds_read_b128 v[244:247], %[ub] offset:4832\n\t"
                "s_waitcnt lgkmcnt(11)\n\t"
                "v_mfma_f32_32x32x16_bf16 %[a0], v[208:211], v[216:219], %[a0]\n\t"
                "ds_read_b128 v[248:251], %[ub] offset:2272\n\t"
                "s_waitcnt lgkmcnt(10)\n\t"
                "v_mfma_f32_32x32x16_bf16 %[a1], v[220:223], v[216:219], %[a1]\n\t"
                "ds_read_b64 v[252:253], %[kb] offset:2240\n\t"
                "ds_read_b64 v[254:255], %[kb] offset:2248\n\t"
                "s_waitcnt lgkmcnt(9)\n\t"
                "v_mfma_f32_32x32x16_bf16 %[a1], v[224:227], v[228:231], %[a1]\n\t"
                "ds_read_b64 v[192:193], %[kb] offset:288\n\t"
                "ds_read_b64 v[194:195], %[kb] offset:296\n\t"
                "ds_read_b128 v[196:199], %[ub] offset:4720\n\t"
                "s_waitcnt lgkmcnt(11)\n\t"
                "v_mfma_f32_32x32x16_bf16 %[a0], v[224:227], v[232:235], %[a0]\n\t"
                "ds_read_b128 v[200:203], %[ub] offset:2160\n\t"
                "s_waitcnt lgkmcnt(10)\n\t"
                "v_mfma_f32_32x32x16_bf16 %[a1], v[236:239], v[232:235], %[a1]\n\t"
                "ds_read_b64 v[204:205], %[kb] offset:2336\n\t"
                "ds_read_b64 v[206:207], %[kb] offset:2344\n\t"
                "s_waitcnt lgkmcnt(9)\n\t"
                "v_mfma_f32_32x32x16_bf16 %[a1], v[240:243], v[244:247], %[a1]\n\t"
                "ds_read_b64 v[208:209], %[kb] offset:256\n\t"
                "ds_read_b64 v[210:211], %[kb] offset:264\n\t"
                "ds_read_b128 v[212:215], %[ub] offset:4752\n\t"
                "s_waitcnt lgkmcnt(11)\n\t"
                "v_mfma_f32_32x32x16_bf16 %[a0], v[240:243], v[248:251], %[a0]\n\t"
                "ds_read_b128 v[216:219], %[ub] offset:2192\n\t"
                "s_waitcnt lgkmcnt(10)\n\t"
                "v_mfma_f32_32x32x16_bf16 %[a1], v[252:255], v[248:251], %[a1]\n\t"
                "ds_read_b64 v[220:221], %[kb] offset:2304\n\t"
                "ds_read_b64 v[222:223], %[kb] offset:2312\n\t"
                "s_waitcnt lgkmcnt(9)\n\t"
                "v_mfma_f32_32x32x16_bf16 %[a1], v[192:195], v[196:199], %[a1]\n\t"
                "ds_read_b64 v[224:225], %[kb] offset:352\n\t"
                "ds_read_b64 v[226:227], %[kb] offset:360\n\t"
                "ds_read_b128 v[228:231], %[ub] offset:4640\n\t"
                "s_waitcnt lgkmcnt(11)\n\t"
                "v_mfma_f32_32x32x16_bf16 %[a0], v[192:195], v[200:203], %[a0]\n\t"
                "ds_read_b128 v[232:235], %[ub] offset:2080\n\t"
                "s_waitcnt lgkmcnt(10)\n\t"
                "v_mfma_f32_32x32x16_bf16 %[a1], v[204:207], v[200:203], %[a1]\n\t"
                "ds_read_b64 v[236:237], %[kb] offset:2400\n\t"
                "ds_read_b64 v[238:239], %[kb] offset:2408\n\t"
                "s_waitcnt lgkmcnt(9)\n\t"
                "v_mfma_f32_32x32x16_bf16 %[a1], v[208:211], v[212:215], %[a1]\n\t"
                "ds_read_b64 v[240:241], %[kb] offset:320\n\t"
                "ds_read_b64 v[242:243], %[kb] offset:328\n\t"
                "ds_read_b128 v[244:247], %[ub] offset:4672\n\t"
                "s_waitcnt lgkmcnt(11)\n\t"
                "v_mfma_f32_32x32x16_bf16 %[a0], v[208:211], v[216:219], %[a0]\n\t"
                "ds_read_b128 v[248:251], %[ub] offset:2112\n\t"
                "s_waitcnt lgkmcnt(10)\n\t"
                "v_mfma_f32_32x32x16_bf16 %[a1], v[220:223], v[216:219], %[a1]\n\t"
                "ds_read_b64 v[252:253], %[kb] offset:2368\n\t"
                "ds_read_b64 v[254:255], %[kb] offset:2376\n\t"
                "s_waitcnt lgkmcnt(9)\n\t"
                "v_mfma_f32_32x32x16_bf16 %[a1], v[224:227], v[228:231], %[a1]\n\t"
                "ds_read_b64 v[192:193], %[kb] offset:416\n\t"
                "ds_read_b64 v[194:195], %[kb] offset:424\n\t"
                "ds_read_b128 v[196:199], %[ub] offset:4560\n\t"
                "s_waitcnt lgkmcnt(11)\n\t"
                "v_mfma_f32_32x32x16_bf16 %[a0], v[224:227], v[232:235], %[a0]\n\t"
                "ds_read_b128 v[200:203], %[ub] offset:2000\n\t"
                "s_waitcnt lgkmcnt(10)\n\t"
                "v_mfma_f32_32x32x16_bf16 %[a1], v[236:239], v[232:235], %[a1]\n\t"
                "ds_read_b64 v[204:205], %[kb] offset:2464\n\t"
                "ds_read_b64 v[206:207], %[kb] offset:2472\n\t"
                "s_waitcnt lgkmcnt(9)\n\t"
                "v_mfma_f32_32x32x16_bf16 %[a1], v[240:243], v[244:247], %[a1]\n\t"
                "ds_read_b64 v[208:209], %[kb] offset:384\n\t"
                "ds_read_b64 v[210:211], %[kb] offset:392\n\t"
                "ds_read_b128 v[212:215], %[ub] offset:4592\n\t"
                "s_waitcnt lgkmcnt(11)\n\t"
                "v_mfma_f32_32x32x16_bf16 %[a0], v[240:243], v[248:251], %[a0]\n\t"
                "ds_read_b128 v[216:219], %[ub] offset:2032\n\t"
                "s_waitcnt lgkmcnt(10)\n\t"
                "v_mfma_f32_32x32x16_bf16 %[a1], v[252:255], v[248:251], %[a1]\n\t"
                "ds_read_b64 v[220:221], %[kb] offset:2432\n\t"
                "ds_read_b64 v[222:223], %[kb] offset:2440\n\t"
                "s_waitcnt lgkmcnt(9)\n\t"
                "v_mfma_f32_32x32x16_bf16 %[a1], v[192:195], v[196:199], %[a1]\n\t"
                "ds_read_b64 v[224:225], %[kb] offset:480\n\t"
                "ds_read_b64 v[226:227], %[kb] offset:488\n\t"
                "ds_read_b128 v[228:231], %[ub] offset:4480\n\t"
                "s_waitcnt lgkmcnt(11)\n\t"
                "v_mfma_f32_32x32x16_bf16 %[a0], v[192:195], v[200:203], %[a0]\n\t"
                "ds_read_b128 v[232:235], %[ub] offset:1920\n\t"
                "s_waitcnt lgkmcnt(10)\n\t"
                "v_mfma_f32_32x32x16_bf16 %[a1], v[204:207], v[200:203], %[a1]\n\t"
                "ds_read_b64 v[236:237], %[kb] offset:2528\n\t"
                "ds_read_b64 v[238:239], %[kb] offset:2536\n\t"
                "s_waitcnt lgkmcnt(9)\n\t"
                "v_mfma_f32_32x32x16_bf16 %[a1], v[208:211], v[212:215], %[a1]\n\t"
                "ds_read_b64 v[240:241], %[kb] offset:448\n\t"
                "ds_read_b64 v[242:243], %[kb] offset:456\n\t"
                "ds_read_b128 v[244:247], %[ub] offset:4512\n\t"
                "s_waitcnt lgkmcnt(11)\n\t"
                "v_mfma_f32_32x32x16_bf16 %[a0], v[208:211], v[216:219], %[a0]\n\t"
                "ds_read_b128 v[248:251], %[ub] offset:1952\n\t"
                "s_waitcnt lgkmcnt(10)\n\t"
                "v_mfma_f32_32x32x16_bf16 %[a1], v[220:223], v[216:219], %[a1]\n\t"
                "ds_read_b64 v[252:253], %[kb] offset:2496\n\t"
                "ds_read_b64 v[254:255], %[kb] offset:2504\n\t"
                "s_waitcnt lgkmcnt(9)\n\t"
                "v_mfma_f32_32x32x16_bf16 %[a1], v[224:227], v[228:231], %[a1]\n\t"
                "ds_read_b64 v[192:193], %[kb] offset:544\n\t"
                "ds_read_b64 v[194:195], %[kb] offset:552\n\t"
                "ds_read_b128 v[196:199], %[ub] offset:4400\n\t"
                "s_waitcnt lgkmcnt(11)\n\t"
                "v_mfma_f32_32x32x16_bf16 %[a0], v[224:227], v[232:235], %[a0]\n\t"
                "ds_read_b128 v[200:203], %[ub] offset:1840\n\t"
                "s_waitcnt lgkmcnt(10)\n\t"
                "v_mfma_f32_32x32x16_bf16 %[a1], v[236:239], v[232:235], %[a1]\n\t"
                "ds_read_b64 v[204:205], %[kb] offset:2592\n\t"
                "ds_read_b64 v[206:207], %[kb] offset:2600\n\t"
                "s_waitcnt lgkmcnt(9)\n\t"
                "v_mfma_f32_32x32x16_bf16 %[a1], v[240:243], v[244:247], %[a1]\n\t"
                "ds_read_b64 v[208:209], %[kb] offset:512\n\t"
                "ds_read_b64 v[210:211], %[kb] offset:520\n\t"
                "ds_read_b128 v[212:215], %[ub] offset:4432\n\t"
                "s_waitcnt lgkmcnt(11)\n\t"
                "v_mfma_f32_32x32x16_bf16 %[a0], v[240:243], v[248:251], %[a0]\n\t"
                "ds_read_b128 v[216:219], %[ub] offset:1872\n\t"
                "s_waitcnt lgkmcnt(10)\n\t"
                "v_mfma_f32_32x32x16_bf16 %[a1], v[252:255], v[248:251], %[a1]\n\t"
                "ds_read_b64 v[220:221], %[kb] offset:2560\n\t"
                "ds_read_b64 v[222:223], %[kb] offset:2568\n\t"
                "s_waitcnt lgkmcnt(9)\n\t"
                "v_mfma_f32_32x32x16_bf16 %[a1], v[192:195], v[196:199], %[a1]\n\t"
                "ds_read_b64 v[224:225], %[kb] offset:608\n\t"
                "ds_read_b64 v[226:227], %[kb] offset:616\n\t"
                "ds_read_b128 v[228:231], %[ub] offset:4320\n\t"
                "s_waitcnt lgkmcnt(11)\n\t"
                "v_mfma_f32_32x32x16_bf16 %[a0], v[192:195], v[200:203], %[a0]\n\t"
                "ds_read_b128 v[232:235], %[ub] offset:1760\n\t"
                "s_waitcnt lgkmcnt(10)\n\t"
                "v_mfma_f32_32x32x16_bf16 %[a1], v[204:207], v[200:203], %[a1]\n\t"
                "ds_read_b64 v[236:237], %[kb] offset:2656\n\t"
                "ds_read_b64 v[238:239], %[kb] offset:2664\n\t"
                "s_waitcnt lgkmcnt(9)\n\t"
                "v_mfma_f32_32x32x16_bf16 %[a1], v[208:211], v[212:215], %[a1]\n\t"
                "ds_read_b64 v[240:241], %[kb] offset:576\n\t"
                "ds_read_b64 v[242:243], %[kb] offset:584\n\t"
                "ds_read_b128 v[244:247], %[ub] offset:4352\n\t"
                "s_waitcnt lgkmcnt(11)\n\t"
                "v_mfma_f32_32x32x16_bf16 %[a0], v[208:211], v[216:219], %[a0]\n\t"
                "ds_read_b128 v[248:251], %[ub] offset:1792\n\t"
                "s_waitcnt lgkmcnt(10)\n\t"
                "v_mfma_f32_32x32x16_bf16 %[a1], v[220:223], v[216:219], %[a1]\n\t"
                "ds_read_b64 v[252:253], %[kb] offset:2624\n\t"
                "ds_read_b64 v[254:255], %[kb] offset:2632\n\t"
                "s_waitcnt lgkmcnt(9)\n\t"
                "v_mfma_f32_32x32x16_bf16 %[a1], v[224:227], v[228:231], %[a1]\n\t"
                "ds_read_b64 v[192:193], %[kb] offset:672\n\t"
                "ds_read_b64 v[194:195], %[kb] offset:680\n\t"
                "ds_read_b128 v[196:199], %[ub] offset:4240\n\t"
                "s_waitcnt lgkmcnt(11)\n\t"
                "v_mfma_f32_32x32x16_bf16 %[a0], v[224:227], v[232:235], %[a0]\n\t"
                "ds_read_b128 v[200:203], %[ub] offset:1680\n\t"
                "s_waitcnt lgkmcnt(10)\n\t"
                "v_mfma_f32_32x32x16_bf16 %[a1], v[236:239], v[232:235], %[a1]\n\t"
                "ds_read_b64 v[204:205], %[kb] offset:2720\n\t"
                "ds_read_b64 v[206:207], %[kb] offset:2728\n\t"
                "s_waitcnt lgkmcnt(9)\n\t"
                "v_mfma_f32_32x32x16_bf16 %[a1], v[240:243], v[244:247], %[a1]\n\t"
                "ds_read_b64 v[208:209], %[kb] offset:640\n\t"
                "ds_read_b64 v[210:211], %[kb] offset:648\n\t"
                "ds_read_b128 v[212:215], %[ub] offset:4272\n\t"
                "s_waitcnt lgkmcnt(11)\n\t"
                "v_mfma_f32_32x32x16_bf16 %[a0], v[240:243], v[248:251], %[a0]\n\t"
                "ds_read_b128 v[216:219], %[ub] offset:1712\n\t"
                "s_waitcnt lgkmcnt(10)\n\t"
                "v_mfma_f32_32x32x16_bf16 %[a1], v[252:255], v[248:251], %[a1]\n\t"
                "ds_read_b64 v[220:221], %[kb] offset:2688\n\t"
                "ds_read_b64 v[222:223], %[kb] offset:2696\n\t"
                "s_waitcnt lgkmcnt(9)\n\t"
                "v_mfma_f32_32x32x16_bf16 %[a1], v[192:195], v[196:199], %[a1]\n\t"
                "ds_read_b64 v[224:225], %[kb] offset:736\n\t"
                "ds_read_b64 v[226:227], %[kb] offset:744\n\t"
                "ds_read_b128 v[228:231], %[ub] offset:4160\n\t"
                "s_waitcnt lgkmcnt(11)\n\t"
                "v_mfma_f32_32x32x16_bf16 %[a0], v[192:195], v[200:203], %[a0]\n\t"
                "ds_read_b128 v[232:235], %[ub] offset:1600\n\t"
                "s_waitcnt lgkmcnt(10)\n\t"
                "v_mfma_f32_32x32x16_bf16 %[a1], v[204:207], v[200:203], %[a1]\n\t"
                "ds_read_b64 v[236:237], %[kb] offset:2784\n\t"
                "ds_read_b64 v[238:239], %[kb] offset:2792\n\t"
                "s_waitcnt lgkmcnt(9)\n\t"
                "v_mfma_f32_32x32x16_bf16 %[a1], v[208:211], v[212:215], %[a1]\n\t"
                "ds_read_b64 v[240:241], %[kb] offset:704\n\t"
                "ds_read_b64 v[242:243], %[kb] offset:712\n\t"
                "ds_read_b128 v[244:247], %[ub] offset:4192\n\t"
                "s_waitcnt lgkmcnt(11)\n\t"
                "v_mfma_f32_32x32x16_bf16 %[a0], v[208:211], v[216:219], %[a0]\n\t"
                "ds_read_b128 v[248:251], %[ub] offset:1632\n\t"
                "s_waitcnt lgkmcnt(10)\n\t"
                "v_mfma_f32_32x32x16_bf16 %[a1], v[220:223], v[216:219], %[a1]\n\t"
                "ds_read_b64 v[252:253], %[kb] offset:2752\n\t"
                "ds_read_b64 v[254:255], %[kb] offset:2760\n\t"
                "s_waitcnt lgkmcnt(9)\n\t"
                "v_mfma_f32_32x32x16_bf16 %[a1], v[224:227], v[228:231], %[a1]\n\t"
                "ds_read_b64 v[192:193], %[kb] offset:800\n\t"
                "ds_read_b64 v[194:195], %[kb] offset:808\n\t"
                "ds_read_b128 v[196:199], %[ub] offset:4080\n\t"
                "s_waitcnt lgkmcnt(11)\n\t"
                "v_mfma_f32_32x32x16_bf16 %[a0], v[224:227], v[232:235], %[a0]\n\t"
                "ds_read_b128 v[200:203], %[ub] offset:1520\n\t"
                "s_waitcnt lgkmcnt(10)\n\t"
                "v_mfma_f32_32x32x16_bf16 %[a1], v[236:239], v[232:235], %[a1]\n\t"
                "ds_read_b64 v[204:205], %[kb] offset:2848\n\t"
                "ds_read_b64 v[206:207], %[kb] offset:2856\n\t"
                "s_waitcnt lgkmcnt(9)\n\t"
                "v_mfma_f32_32x32x16_bf16 %[a1], v[240:243], v[244:247], %[a1]\n\t"
                "ds_read_b64 v[208:209], %[kb] offset:768\n\t"
                "ds_read_b64 v[210:211], %[kb] offset:776\n\t"
                "ds_read_b128 v[212:215], %[ub] offset:4112\n\t"
                "s_waitcnt lgkmcnt(11)\n\t"
                "v_mfma_f32_32x32x16_bf16 %[a0], v[240:243], v[248:251], %[a0]\n\t"
                "ds_read_b128 v[216:219], %[ub] offset:1552\n\t"
                "s_waitcnt lgkmcnt(10)\n\t"
                "v_mfma_f32_32x32x16_bf16 %[a1], v[252:255], v[248:251], %[a1]\n\t"
                "ds_read_b64 v[220:221], %[kb] offset:2816\n\t"
                "ds_read_b64 v[222:223], %[kb] offset:2824\n\t"
                "s_waitcnt lgkmcnt(9)\n\t"
                "v_mfma_f32_32x32x16_bf16 %[a1], v[192:195], v[196:199], %[a1]\n\t"
                "ds_read_b64 v[224:225], %[kb] offset:864\n\t"
                "ds_read_b64 v[226:227], %[kb] offset:872\n\t"
                "ds_read_b128 v[228:231], %[ub] offset:4000\n\t"
                "s_waitcnt lgkmcnt(11)\n\t"
                "v_mfma_f32_32x32x16_bf16 %[a0], v[192:195], v[200:203], %[a0]\n\t"
                "ds_read_b128 v[232:235], %[ub] offset:1440\n\t"
                "s_waitcnt lgkmcnt(10)\n\t"
                "v_mfma_f32_32x32x16_bf16 %[a1], v[204:207], v[200:203], %[a1]\n\t"
                "ds_read_b64 v[236:237], %[kb] offset:2912\n\t"
                "ds_read_b64 v[238:239], %[kb] offset:2920\n\t"
                "s_waitcnt lgkmcnt(9)\n\t"
                "v_mfma_f32_32x32x16_bf16 %[a1], v[208:211], v[212:215], %[a1]\n\t"
                "ds_read_b64 v[240:241], %[kb] offset:832\n\t"
                "ds_read_b64 v[242:243], %[kb] offset:840\n\t"
                "ds_read_b128 v[244:247], %[ub] offset:4032\n\t"
                "s_waitcnt lgkmcnt(11)\n\t"
                "v_mfma_f32_32x32x16_bf16 %[a0], v[208:211], v[216:219], %[a0]\n\t"
                "ds_read_b128 v[248:251], %[ub] offset:1472\n\t"
                "s_waitcnt lgkmcnt(10)\n\t"
                "v_mfma_f32_32x32x16_bf16 %[a1], v[220:223], v[216:219], %[a1]\n\t"
                "ds_read_b64 v[252:253], %[kb] offset:2880\n\t"
                "ds_read_b64 v[254:255], %[kb] offset:2888\n\t"
                "s_waitcnt lgkmcnt(9)\n\t"
                "v_mfma_f32_32x32x16_bf16 %[a1], v[224:227], v[228:231], %[a1]\n\t"
                "ds_read_b64 v[192:193], %[kb] offset:928\n\t"
                "ds_read_b64 v[194:195], %[kb] offset:936\n\t"
                "ds_read_b128 v[196:199], %[ub] offset:3920\n\t"
                "s_waitcnt lgkmcnt(11)\n\t"
                "v_mfma_f32_32x32x16_bf16 %[a0], v[224:227], v[232:235], %[a0]\n\t"
                "ds_read_b128 v[200:203], %[ub] offset:1360\n\t"
                "s_waitcnt lgkmcnt(10)\n\t"
                "v_mfma_f32_32x32x16_bf16 %[a1], v[236:239], v[232:235], %[a1]\n\t"
                "ds_read_b64 v[204:205], %[kb] offset:2976\n\t"
                "ds_read_b64 v[206:207], %[kb] offset:2984\n\t"
                "s_waitcnt lgkmcnt(9)\n\t"
                "v_mfma_f32_32x32x16_bf16 %[a1], v[240:243], v[244:247], %[a1]\n\t"
                "ds_read_b64 v[208:209], %[kb] offset:896\n\t"
                "ds_read_b64 v[210:211], %[kb] offset:904\n\t"
                "ds_read_b128 v[212:215], %[ub] offset:3952\n\t"
                "s_waitcnt lgkmcnt(11)\n\t"
                "v_mfma_f32_32x32x16_bf16 %[a0], v[240:243], v[248:251], %[a0]\n\t"
                "ds_read_b128 v[216:219], %[ub] offset:1392\n\t"
                "s_waitcnt lgkmcnt(10)\n\t"
                "v_mfma_f32_32x32x16_bf16 %[a1], v[252:255], v[248:251], %[a1]\n\t"
                "ds_read_b64 v[220:221], %[kb] offset:2944\n\t"
                "ds_read_b64 v[222:223], %[kb] offset:2952\n\t"
                "s_waitcnt lgkmcnt(9)\n\t"
                "v_mfma_f32_32x32x16_bf16 %[a1], v[192:195], v[196:199], %[a1]\n\t"
                "ds_read_b64 v[224:225], %[kb] offset:992\n\t"
                "ds_read_b64 v[226:227], %[kb] offset:1000\n\t"
                "ds_read_b128 v[228:231], %[ub] offset:3840\n\t"
                "s_waitcnt lgkmcnt(11)\n\t"
                "v_mfma_f32_32x32x16_bf16 %[a0], v[192:195], v[200:203], %[a0]\n\t"
                "ds_read_b128 v[232:235], %[ub] offset:1280\n\t"
                "s_waitcnt lgkmcnt(10)\n\t"
                "v_mfma_f32_32x32x16_bf16 %[a1], v[204:207], v[200:203], %[a1]\n\t"
                "ds_read_b64 v[236:237], %[kb] offset:3040\n\t"
                "ds_read_b64 v[238:239], %[kb] offset:3048\n\t"
                "s_waitcnt lgkmcnt(9)\n\t"
                "v_mfma_f32_32x32x16_bf16 %[a1], v[208:211], v[212:215], %[a1]\n\t"
                "ds_read_b64 v[240:241], %[kb] offset:960\n\t"
                "ds_read_b64 v[242:243], %[kb] offset:968\n\t"
                "ds_read_b128 v[244:247], %[ub] offset:3872\n\t"
                "s_waitcnt lgkmcnt(11)\n\t"
                "v_mfma_f32_32x32x16_bf16 %[a0], v[208:211], v[216:219], %[a0]\n\t"
                "ds_read_b128 v[248:251], %[ub] offset:1312\n\t"
                "s_waitcnt lgkmcnt(10)\n\t"
                "v_mfma_f32_32x32x16_bf16 %[a1], v[220:223], v[216:219], %[a1]\n\t"
                "ds_read_b64 v[252:253], %[kb] offset:3008\n\t"
                "ds_read_b64 v[254:255], %[kb] offset:3016\n\t"
                "s_waitcnt lgkmcnt(9)\n\t"
                "v_mfma_f32_32x32x16_bf16 %[a1], v[224:227], v[228:231], %[a1]\n\t"
                "ds_read_b64 v[192:193], %[kb] offset:1056\n\t"
                "ds_read_b64 v[194:195], %[kb] offset:1064\n\t"
                "ds_read_b128 v[196:199], %[ub] offset:3760\n\t"
                "s_waitcnt lgkmcnt(11)\n\t"
                "v_mfma_f32_32x32x16_bf16 %[a0], v[224:227], v[232:235], %[a0]\n\t"
                "ds_read_b128 v[200:203], %[ub] offset:1200\n\t"
                "s_waitcnt lgkmcnt(10)\n\t"
                "v_mfma_f32_32x32x16_bf16 %[a1], v[236:239], v[232:235], %[a1]\n\t"
                "ds_read_b64 v[204:205], %[kb] offset:3104\n\t"
                "ds_read_b64 v[206:207], %[kb] offset:3112\n\t"
                "s_waitcnt lgkmcnt(9)\n\t"
                "v_mfma_f32_32x32x16_bf16 %[a1], v[240:243], v[244:247], %[a1]\n\t"
                "ds_read_b64 v[208:209], %[kb] offset:1024\n\t"
                "ds_read_b64 v[210:211], %[kb] offset:1032\n\t"
                "ds_read_b128 v[212:215], %[ub] offset:3792\n\t"
                "s_waitcnt lgkmcnt(11)\n\t"
                "v_mfma_f32_32x32x16_bf16 %[a0], v[240:243], v[248:251], %[a0]\n\t"
                "ds_read_b128 v[216:219], %[ub] offset:1232\n\t"
                "s_waitcnt lgkmcnt(10)\n\t"
                "v_mfma_f32_32x32x16_bf16 %[a1], v[252:255], v[248:251], %[a1]\n\t"
                "ds_read_b64 v[220:221], %[kb] offset:3072\n\t"
                "ds_read_b64 v[222:223], %[kb] offset:3080\n\t"
                "s_waitcnt lgkmcnt(9)\n\t"
                "v_mfma_f32_32x32x16_bf16 %[a1], v[192:195], v[196:199], %[a1]\n\t"
                "ds_read_b64 v[224:225], %[kb] offset:1120\n\t"
                "ds_read_b64 v[226:227], %[kb] offset:1128\n\t"
                "ds_read_b128 v[228:231], %[ub] offset:3680\n\t"
                "s_waitcnt lgkmcnt(11)\n\t"
                "v_mfma_f32_32x32x16_bf16 %[a0], v[192:195], v[200:203], %[a0]\n\t"
                "ds_read_b128 v[232:235], %[ub] offset:1120\n\t"
                "s_waitcnt lgkmcnt(10)\n\t"
                "v_mfma_f32_32x32x16_bf16 %[a1], v[204:207], v[200:203], %[a1]\n\t"
                "ds_read_b64 v[236:237], %[kb] offset:3168\n\t"
                "ds_read_b64 v[238:239], %[kb] offset:3176\n\t"
                "s_waitcnt lgkmcnt(9)\n\t"
                "v_mfma_f32_32x32x16_bf16 %[a1], v[208:211], v[212:215], %[a1]\n\t"
                "ds_read_b64 v[240:241], %[kb] offset:1088\n\t"
                "ds_read_b64 v[242:243], %[kb] offset:1096\n\t"
                "ds_read_b128 v[244:247], %[ub] offset:3712\n\t"
                "s_waitcnt lgkmcnt(11)\n\t"
                "v_mfma_f32_32x32x16_bf16 %[a0], v[208:211], v[216:219], %[a0]\n\t"
                "ds_read_b128 v[248:251], %[ub] offset:1152\n\t"
                "s_waitcnt lgkmcnt(10)\n\t"
                "v_mfma_f32_32x32x16_bf16 %[a1], v[220:223], v[216:219], %[a1]\n\t"
                "ds_read_b64 v[252:253], %[kb] offset:3136\n\t"
                "ds_read_b64 v[254:255], %[kb] offset:3144\n\t"
                "s_waitcnt lgkmcnt(9)\n\t"
                "v_mfma_f32_32x32x16_bf16 %[a1], v[224:227], v[228:231], %[a1]\n\t"
                "ds_read_b64 v[192:193], %[kb] offset:1184\n\t"
                "ds_read_b64 v[194:195], %[kb] offset:1192\n\t"
                "ds_read_b128 v[196:199], %[ub] offset:3600\n\t"
                "s_waitcnt lgkmcnt(11)\n\t"
                "v_mfma_f32_32x32x16_bf16 %[a0], v[224:227], v[232:235], %[a0]\n\t"
                "ds_read_b128 v[200:203], %[ub] offset:1040\n\t"
                "s_waitcnt lgkmcnt(10)\n\t"
                "v_mfma_f32_32x32x16_bf16 %[a1], v[236:239], v[232:235], %[a1]\n\t"
                "ds_read_b64 v[204:205], %[kb] offset:3232\n\t"
                "ds_read_b64 v[206:207], %[kb] offset:3240\n\t"
                "s_waitcnt lgkmcnt(9)\n\t"
                "v_mfma_f32_32x32x16_bf16 %[a1], v[240:243], v[244:247], %[a1]\n\t"
                "ds_read_b64 v[208:209], %[kb] offset:1152\n\t"
                "ds_read_b64 v[210:211], %[kb] offset:1160\n\t"
                "ds_read_b128 v[212:215], %[ub] offset:3632\n\t"
                "s_waitcnt lgkmcnt(11)\n\t"
                "v_mfma_f32_32x32x16_bf16 %[a0], v[240:243], v[248:251], %[a0]\n\t"
                "ds_read_b128 v[216:219], %[ub] offset:1072\n\t"
                "s_waitcnt lgkmcnt(10)\n\t"
                "v_mfma_f32_32x32x16_bf16 %[a1], v[252:255], v[248:251], %[a1]\n\t"
                "ds_read_b64 v[220:221], %[kb] offset:3200\n\t"
                "ds_read_b64 v[222:223], %[kb] offset:3208\n\t"
                "s_waitcnt lgkmcnt(9)\n\t"
                "v_mfma_f32_32x32x16_bf16 %[a1], v[192:195], v[196:199], %[a1]\n\t"
                "ds_read_b64 v[224:225], %[kb] offset:1248\n\t"
                "ds_read_b64 v[226:227], %[kb] offset:1256\n\t"
                "ds_read_b128 v[228:231], %[ub] offset:3520\n\t"
                "s_waitcnt lgkmcnt(11)\n\t"
                "v_mfma_f32_32x32x16_bf16 %[a0], v[192:195], v[200:203], %[a0]\n\t"
                "ds_read_b128 v[232:235], %[ub] offset:960\n\t"
                "s_waitcnt lgkmcnt(10)\n\t"
                "v_mfma_f32_32x32x16_bf16 %[a1], v[204:207], v[200:203], %[a1]\n\t"
                "ds_read_b64 v[236:237], %[kb] offset:3296\n\t"
                "ds_read_b64 v[238:239], %[kb] offset:3304\n\t"
                "s_waitcnt lgkmcnt(9)\n\t"
                "v_mfma_f32_32x32x16_bf16 %[a1], v[208:211], v[212:215], %[a1]\n\t"
                "ds_read_b64 v[240:241], %[kb] offset:1216\n\t"
                "ds_read_b64 v[242:243], %[kb] offset:1224\n\t"
                "ds_read_b128 v[244:247], %[ub] offset:3552\n\t"
                "s_waitcnt lgkmcnt(11)\n\t"
                "v_mfma_f32_32x32x16_bf16 %[a0], v[208:211], v[216:219], %[a0]\n\t"
                "ds_read_b128 v[248:251], %[ub] offset:992\n\t"
                "s_waitcnt lgkmcnt(10)\n\t"
                "v_mfma_f32_32x32x16_bf16 %[a1], v[220:223], v[216:219], %[a1]\n\t"
                "ds_read_b64 v[252:253], %[kb] offset:3264\n\t"
                "ds_read_b64 v[254:255], %[kb] offset:3272\n\t"
                "s_waitcnt lgkmcnt(9)\n\t"
                "v_mfma_f32_32x32x16_bf16 %[a1], v[224:227], v[228:231], %[a1]\n\t"
                "ds_read_b64 v[192:193], %[kb] offset:1312\n\t"
                "ds_read_b64 v[194:195], %[kb] offset:1320\n\t"
                "ds_read_b128 v[196:199], %[ub] offset:3440\n\t"
                "s_waitcnt lgkmcnt(11)\n\t"
                "v_mfma_f32_32x32x16_bf16 %[a0], v[224:227], v[232:235], %[a0]\n\t"
                "ds_read_b128 v[200:203], %[ub] offset:880\n\t"
                "s_waitcnt lgkmcnt(10)\n\t"
                "v_mfma_f32_32x32x16_bf16 %[a1], v[236:239], v[232:235], %[a1]\n\t"
                "ds_read_b64 v[204:205], %[kb] offset:3360\n\t"
                "ds_read_b64 v[206:207], %[kb] offset:3368\n\t"
                "s_waitcnt lgkmcnt(9)\n\t"
                "v_mfma_f32_32x32x16_bf16 %[a1], v[240:243], v[244:247], %[a1]\n\t"
                "ds_read_b64 v[208:209], %[kb] offset:1280\n\t"
                "ds_read_b64 v[210:211], %[kb] offset:1288\n\t"
                "ds_read_b128 v[212:215], %[ub] offset:3472\n\t"
                "s_waitcnt lgkmcnt(11)\n\t"
                "v_mfma_f32_32x32x16_bf16 %[a0], v[240:243], v[248:251], %[a0]\n\t"
                "ds_read_b128 v[216:219], %[ub] offset:912\n\t"
                "s_waitcnt lgkmcnt(10)\n\t"
                "v_mfma_f32_32x32x16_bf16 %[a1], v[252:255], v[248:251], %[a1]\n\t"
                "ds_read_b64 v[220:221], %[kb] offset:3328\n\t"
                "ds_read_b64 v[222:223], %[kb] offset:3336\n\t"
                "s_waitcnt lgkmcnt(9)\n\t"
                "v_mfma_f32_32x32x16_bf16 %[a1], v[192:195], v[196:199], %[a1]\n\t"
                "ds_read_b64 v[224:225], %[kb] offset:1376\n\t"
                "ds_read_b64 v[226:227], %[kb] offset:1384\n\t"
                "ds_read_b128 v[228:231], %[ub] offset:3360\n\t"
                "s_waitcnt lgkmcnt(11)\n\t"
                "v_mfma_f32_32x32x16_bf16 %[a0], v[192:195], v[200:203], %[a0]\n\t"
                "ds_read_b128 v[232:235], %[ub] offset:800\n\t"
                "s_waitcnt lgkmcnt(10)\n\t"
                "v_mfma_f32_32x32x16_bf16 %[a1], v[204:207], v[200:203], %[a1]\n\t"
                "ds_read_b64 v[236:237], %[kb] offset:3424\n\t"
                "ds_read_b64 v[238:239], %[kb] offset:3432\n\t"
                "s_waitcnt lgkmcnt(9)\n\t"
                "v_mfma_f32_32x32x16_bf16 %[a1], v[208:211], v[212:215], %[a1]\n\t"
                "ds_read_b64 v[240:241], %[kb] offset:1344\n\t"
                "ds_read_b64 v[242:243], %[kb] offset:1352\n\t"
                "ds_read_b128 v[244:247], %[ub] offset:3392\n\t"
                "s_waitcnt lgkmcnt(11)\n\t"
                "v_mfma_f32_32x32x16_bf16 %[a0], v[208:211], v[216:219], %[a0]\n\t"
                "ds_read_b128 v[248:251], %[ub] offset:832\n\t"
                "s_waitcnt lgkmcnt(10)\n\t"
                "v_mfma_f32_32x32x16_bf16 %[a1], v[220:223], v[216:219], %[a1]\n\t"
                "ds_read_b64 v[252:253], %[kb] offset:3392\n\t"
                "ds_read_b64 v[254:255], %[kb] offset:3400\n\t"
                "s_waitcnt lgkmcnt(9)\n\t"
                "v_mfma_f32_32x32x16_bf16 %[a1], v[224:227], v[228:231], %[a1]\n\t"
                "ds_read_b64 v[192:193], %[kb] offset:1440\n\t"
                "ds_read_b64 v[194:195], %[kb] offset:1448\n\t"
                "ds_read_b128 v[196:199], %[ub] offset:3280\n\t"
                "s_waitcnt lgkmcnt(11)\n\t"
                "v_mfma_f32_32x32x16_bf16 %[a0], v[224:227], v[232:235], %[a0]\n\t"
                "ds_read_b128 v[200:203], %[ub] offset:720\n\t"
                "s_waitcnt lgkmcnt(10)\n\t"
                "v_mfma_f32_32x32x16_bf16 %[a1], v[236:239], v[232:235], %[a1]\n\t"
                "ds_read_b64 v[204:205], %[kb] offset:3488\n\t"
                "ds_read_b64 v[206:207], %[kb] offset:3496\n\t"
                "s_waitcnt lgkmcnt(9)\n\t"
                "v_mfma_f32_32x32x16_bf16 %[a1], v[240:243], v[244:247], %[a1]\n\t"
                "ds_read_b64 v[208:209], %[kb] offset:1408\n\t"
                "ds_read_b64 v[210:211], %[kb] offset:1416\n\t"
                "ds_read_b128 v[212:215], %[ub] offset:3312\n\t"
                "s_waitcnt lgkmcnt(11)\n\t"
                "v_mfma_f32_32x32x16_bf16 %[a0], v[240:243], v[248:251], %[a0]\n\t"
                "ds_read_b128 v[216:219], %[ub] offset:752\n\t"
                "s_waitcnt lgkmcnt(10)\n\t"
                "v_mfma_f32_32x32x16_bf16 %[a1], v[252:255], v[248:251], %[a1]\n\t"
                "ds_read_b64 v[220:221], %[kb] offset:3456\n\t"
                "ds_read_b64 v[222:223], %[kb] offset:3464\n\t"
                "s_waitcnt lgkmcnt(9)\n\t"
                "v_mfma_f32_32x32x16_bf16 %[a1], v[192:195], v[196:199], %[a1]\n\t"
                "ds_read_b64 v[224:225], %[kb] offset:1504\n\t"
                "ds_read_b64 v[226:227], %[kb] offset:1512\n\t"
                "ds_read_b128 v[228:231], %[ub] offset:3200\n\t"
                "s_waitcnt lgkmcnt(11)\n\t"
                "v_mfma_f32_32x32x16_bf16 %[a0], v[192:195], v[200:203], %[a0]\n\t"
                "ds_read_b128 v[232:235], %[ub] offset:640\n\t"
                "s_waitcnt lgkmcnt(10)\n\t"
                "v_mfma_f32_32x32x16_bf16 %[a1], v[204:207], v[200:203], %[a1]\n\t"
                "ds_read_b64 v[236:237], %[kb] offset:3552\n\t"
                "ds_read_b64 v[238:239], %[kb] offset:3560\n\t"
                "s_waitcnt lgkmcnt(9)\n\t"
                "v_mfma_f32_32x32x16_bf16 %[a1], v[208:211], v[212:215], %[a1]\n\t"
                "ds_read_b64 v[240:241], %[kb] offset:1472\n\t"
                "ds_read_b64 v[242:243], %[kb] offset:1480\n\t"
                "ds_read_b128 v[244:247], %[ub] offset:3232\n\t"
                "s_waitcnt lgkmcnt(11)\n\t"
                "v_mfma_f32_32x32x16_bf16 %[a0], v[208:211], v[216:219], %[a0]\n\t"
                "ds_read_b128 v[248:251], %[ub] offset:672\n\t"
                "s_waitcnt lgkmcnt(10)\n\t"
                "v_mfma_f32_32x32x16_bf16 %[a1], v[220:223], v[216:219], %[a1]\n\t"
                "ds_read_b64 v[252:253], %[kb] offset:3520\n\t"
                "ds_read_b64 v[254:255], %[kb] offset:3528\n\t"
                "s_waitcnt lgkmcnt(9)\n\t"
                "v_mfma_f32_32x32x16_bf16 %[a1], v[224:227], v[228:231], %[a1]\n\t"
                "ds_read_b64 v[192:193], %[kb] offset:1568\n\t"
                "ds_read_b64 v[194:195], %[kb] offset:1576\n\t"
                "ds_read_b128 v[196:199], %[ub] offset:3120\n\t"
                "s_waitcnt lgkmcnt(11)\n\t"
                "v_mfma_f32_32x32x16_bf16 %[a0], v[224:227], v[232:235], %[a0]\n\t"
                "ds_read_b128 v[200:203], %[ub] offset:560\n\t"
                "s_waitcnt lgkmcnt(10)\n\t"
                "v_mfma_f32_32x32x16_bf16 %[a1], v[236:239], v[232:235], %[a1]\n\t"
                "ds_read_b64 v[204:205], %[kb] offset:3616\n\t"
                "ds_read_b64 v[206:207], %[kb] offset:3624\n\t"
                "s_waitcnt lgkmcnt(9)\n\t"
                "v_mfma_f32_32x32x16_bf16 %[a1], v[240:243], v[244:247], %[a1]\n\t"
                "ds_read_b64 v[208:209], %[kb] offset:1536\n\t"
                "ds_read_b64 v[210:211], %[kb] offset:1544\n\t"
                "ds_read_b128 v[212:215], %[ub] offset:3152\n\t"
                "s_waitcnt lgkmcnt(11)\n\t"
                "v_mfma_f32_32x32x16_bf16 %[a0], v[240:243], v[248:251], %[a0]\n\t"
                "ds_read_b128 v[216:219], %[ub] offset:592\n\t"
                "s_waitcnt lgkmcnt(10)\n\t"
                "v_mfma_f32_32x32x16_bf16 %[a1], v[252:255], v[248:251], %[a1]\n\t"
                "ds_read_b64 v[220:221], %[kb] offset:3584\n\t"
                "ds_read_b64 v[222:223], %[kb] offset:3592\n\t"
                "s_waitcnt lgkmcnt(9)\n\t"
                "v_mfma_f32_32x32x16_bf16 %[a1], v[192:195], v[196:199], %[a1]\n\t"
                "ds_read_b64 v[224:225], %[kb] offset:1632\n\t"
                "ds_read_b64 v[226:227], %[kb] offset:1640\n\t"
                "ds_read_b128 v[228:231], %[ub] offset:3040\n\t"
                "s_waitcnt lgkmcnt(11)\n\t"
                "v_mfma_f32_32x32x16_bf16 %[a0], v[192:195], v[200:203], %[a0]\n\t"
                "ds_read_b128 v[232:235], %[ub] offset:480\n\t"
                "s_waitcnt lgkmcnt(10)\n\t"
                "v_mfma_f32_32x32x16_bf16 %[a1], v[204:207], v[200:203], %[a1]\n\t"
                "ds_read_b64 v[236:237], %[kb] offset:3680\n\t"
                "ds_read_b64 v[238:239], %[kb] offset:3688\n\t"
                "s_waitcnt lgkmcnt(9)\n\t"
                "v_mfma_f32_32x32x16_bf16 %[a1], v[208:211], v[212:215], %[a1]\n\t"
                "ds_read_b64 v[240:241], %[kb] offset:1600\n\t"
                "ds_read_b64 v[242:243], %[kb] offset:1608\n\t"
                "ds_read_b128 v[244:247], %[ub] offset:3072\n\t"
                "s_waitcnt lgkmcnt(11)\n\t"
                "v_mfma_f32_32x32x16_bf16 %[a0], v[208:211], v[216:219], %[a0]\n\t"
                "ds_read_b128 v[248:251], %[ub] offset:512\n\t"
                "s_waitcnt lgkmcnt(10)\n\t"
                "v_mfma_f32_32x32x16_bf16 %[a1], v[220:223], v[216:219], %[a1]\n\t"
                "ds_read_b64 v[252:253], %[kb] offset:3648\n\t"
                "ds_read_b64 v[254:255], %[kb] offset:3656\n\t"
                "s_waitcnt lgkmcnt(9)\n\t"
                "v_mfma_f32_32x32x16_bf16 %[a1], v[224:227], v[228:231], %[a1]\n\t"
                "ds_read_b64 v[192:193], %[kb] offset:1696\n\t"
                "ds_read_b64 v[194:195], %[kb] offset:1704\n\t"
                "ds_read_b128 v[196:199], %[ub] offset:2960\n\t"
                "s_waitcnt lgkmcnt(11)\n\t"
                "v_mfma_f32_32x32x16_bf16 %[a0], v[224:227], v[232:235], %[a0]\n\t"
                "ds_read_b128 v[200:203], %[ub] offset:400\n\t"
                "s_waitcnt lgkmcnt(10)\n\t"
                "v_mfma_f32_32x32x16_bf16 %[a1], v[236:239], v[232:235], %[a1]\n\t"
                "ds_read_b64 v[204:205], %[kb] offset:3744\n\t"
                "ds_read_b64 v[206:207], %[kb] offset:3752\n\t"
                "s_waitcnt lgkmcnt(9)\n\t"
                "v_mfma_f32_32x32x16_bf16 %[a1], v[240:243], v[244:247], %[a1]\n\t"
                "ds_read_b64 v[208:209], %[kb] offset:1664\n\t"
                "ds_read_b64 v[210:211], %[kb] offset:1672\n\t"
                "ds_read_b128 v[212:215], %[ub] offset:2992\n\t"
                "s_waitcnt lgkmcnt(11)\n\t"
                "v_mfma_f32_32x32x16_bf16 %[a0], v[240:243], v[248:251], %[a0]\n\t"
                "ds_read_b128 v[216:219], %[ub] offset:432\n\t"
                "s_waitcnt lgkmcnt(10)\n\t"
                "v_mfma_f32_32x32x16_bf16 %[a1], v[252:255], v[248:251], %[a1]\n\t"
                "ds_read_b64 v[220:221], %[kb] offset:3712\n\t"
                "ds_read_b64 v[222:223], %[kb] offset:3720\n\t"
                "s_waitcnt lgkmcnt(9)\n\t"
                "v_mfma_f32_32x32x16_bf16 %[a1], v[192:195], v[196:199], %[a1]\n\t"
                "ds_read_b64 v[224:225], %[kb] offset:1760\n\t"
                "ds_read_b64 v[226:227], %[kb] offset:1768\n\t"
                "ds_read_b128 v[228:231], %[ub] offset:2880\n\t"
                "s_waitcnt lgkmcnt(11)\n\t"
                "v_mfma_f32_32x32x16_bf16 %[a0], v[192:195], v[200:203], %[a0]\n\t"
                "ds_read_b128 v[232:235], %[ub] offset:320\n\t"
                "s_waitcnt lgkmcnt(10)\n\t"
                "v_mfma_f32_32x32x16_bf16 %[a1], v[204:207], v[200:203], %[a1]\n\t"
                "ds_read_b64 v[236:237], %[kb] offset:3808\n\t"
                "ds_read_b64 v[238:239], %[kb] offset:3816\n\t"
                "s_waitcnt lgkmcnt(9)\n\t"
                "v_mfma_f32_32x32x16_bf16 %[a1], v[208:211], v[212:215], %[a1]\n\t"
                "ds_read_b64 v[240:241], %[kb] offset:1728\n\t"
                "ds_read_b64 v[242:243], %[kb] offset:1736\n\t"
                "ds_read_b128 v[244:247], %[ub] offset:2912\n\t"
                "s_waitcnt lgkmcnt(11)\n\t"
                "v_mfma_f32_32x32x16_bf16 %[a0], v[208:211], v[216:219], %[a0]\n\t"
                "ds_read_b128 v[248:251], %[ub] offset:352\n\t"
                "s_waitcnt lgkmcnt(10)\n\t"
                "v_mfma_f32_32x32x16_bf16 %[a1], v[220:223], v[216:219], %[a1]\n\t"
                "ds_read_b64 v[252:253], %[kb] offset:3776\n\t"
                "ds_read_b64 v[254:255], %[kb] offset:3784\n\t"
                "s_waitcnt lgkmcnt(9)\n\t"
                "v_mfma_f32_32x32x16_bf16 %[a1], v[224:227], v[228:231], %[a1]\n\t"
                "ds_read_b64 v[192:193], %[kb] offset:1824\n\t"
                "ds_read_b64 v[194:195], %[kb] offset:1832\n\t"
                "ds_read_b128 v[196:199], %[ub] offset:2800\n\t"
                "s_waitcnt lgkmcnt(11)\n\t"
                "v_mfma_f32_32x32x16_bf16 %[a0], v[224:227], v[232:235], %[a0]\n\t"
                "ds_read_b128 v[200:203], %[ub] offset:240\n\t"
                "s_waitcnt lgkmcnt(10)\n\t"
                "v_mfma_f32_32x32x16_bf16 %[a1], v[236:239], v[232:235], %[a1]\n\t"
                "ds_read_b64 v[204:205], %[kb] offset:3872\n\t"
                "ds_read_b64 v[206:207], %[kb] offset:3880\n\t"
                "s_waitcnt lgkmcnt(9)\n\t"
                "v_mfma_f32_32x32x16_bf16 %[a1], v[240:243], v[244:247], %[a1]\n\t"
                "ds_read_b64 v[208:209], %[kb] offset:1792\n\t"
                "ds_read_b64 v[210:211], %[kb] offset:1800\n\t"
                "ds_read_b128 v[212:215], %[ub] offset:2832\n\t"
                "s_waitcnt lgkmcnt(11)\n\t"
                "v_mfma_f32_32x32x16_bf16 %[a0], v[240:243], v[248:251], %[a0]\n\t"
                "ds_read_b128 v[216:219], %[ub] offset:272\n\t"
                "s_waitcnt lgkmcnt(10)\n\t"
                "v_mfma_f32_32x32x16_bf16 %[a1], v[252:255], v[248:251], %[a1]\n\t"
                "ds_read_b64 v[220:221], %[kb] offset:3840\n\t"
                "ds_read_b64 v[222:223], %[kb] offset:3848\n\t"
                "s_waitcnt lgkmcnt(9)\n\t"
                "v_mfma_f32_32x32x16_bf16 %[a1], v[192:195], v[196:199], %[a1]\n\t"
                "ds_read_b64 v[224:225], %[kb] offset:1888\n\t"
                "ds_read_b64 v[226:227], %[kb] offset:1896\n\t"
                "ds_read_b128 v[228:231], %[ub] offset:2720\n\t"
                "s_waitcnt lgkmcnt(11)\n\t"
                "v_mfma_f32_32x32x16_bf16 %[a0], v[192:195], v[200:203], %[a0]\n\t"
                "ds_read_b128 v[232:235], %[ub] offset:160\n\t"
                "s_waitcnt lgkmcnt(10)\n\t"
                "v_mfma_f32_32x32x16_bf16 %[a1], v[204:207], v[200:203], %[a1]\n\t"
                "ds_read_b64 v[236:237], %[kb] offset:3936\n\t"
                "ds_read_b64 v[238:239], %[kb] offset:3944\n\t"
                "s_waitcnt lgkmcnt(9)\n\t"
                "v_mfma_f32_32x32x16_bf16 %[a1], v[208:211], v[212:215], %[a1]\n\t"
                "ds_read_b64 v[240:241], %[kb] offset:1856\n\t"
                "ds_read_b64 v[242:243], %[kb] offset:1864\n\t"
                "ds_read_b128 v[244:247], %[ub] offset:2752\n\t"
                "s_waitcnt lgkmcnt(11)\n\t"
                "v_mfma_f32_32x32x16_bf16 %[a0], v[208:211], v[216:219], %[a0]\n\t"
                "ds_read_b128 v[248:251], %[ub] offset:192\n\t"
                "s_waitcnt lgkmcnt(10)\n\t"
                "v_mfma_f32_32x32x16_bf16 %[a1], v[220:223], v[216:219], %[a1]\n\t"
                "ds_read_b64 v[252:253], %[kb] offset:3904\n\t"
                "ds_read_b64 v[254:255], %[kb] offset:3912\n\t"
                "s_waitcnt lgkmcnt(9)\n\t"
                "v_mfma_f32_32x32x16_bf16 %[a1], v[224:227], v[228:231], %[a1]\n\t"
                "ds_read_b64 v[192:193], %[kb] offset:1952\n\t"
                "ds_read_b64 v[194:195], %[kb] offset:1960\n\t"
                "ds_read_b128 v[196:199], %[ub] offset:2640\n\t"
                "s_waitcnt lgkmcnt(11)\n\t"
                "v_mfma_f32_32x32x16_bf16 %[a0], v[224:227], v[232:235], %[a0]\n\t"
                "ds_read_b128 v[200:203], %[ub] offset:80\n\t"
                "s_waitcnt lgkmcnt(10)\n\t"
                "v_mfma_f32_32x32x16_bf16 %[a1], v[236:239], v[232:235], %[a1]\n\t"
                "ds_read_b64 v[204:205], %[kb] offset:4000\n\t"
                "ds_read_b64 v[206:207], %[kb] offset:4008\n\t"
                "s_waitcnt lgkmcnt(9)\n\t"
                "v_mfma_f32_32x32x16_bf16 %[a1], v[240:243], v[244:247], %[a1]\n\t"
                "ds_read_b64 v[208:209], %[kb] offset:1920\n\t"
                "ds_read_b64 v[210:211], %[kb] offset:1928\n\t"
                "ds_read_b128 v[212:215], %[ub] offset:2672\n\t"
                "s_waitcnt lgkmcnt(11)\n\t"
                "v_mfma_f32_32x32x16_bf16 %[a0], v[240:243], v[248:251], %[a0]\n\t"
                "ds_read_b128 v[216:219], %[ub] offset:112\n\t"
                "s_waitcnt lgkmcnt(10)\n\t"
                "v_mfma_f32_32x32x16_bf16 %[a1], v[252:255], v[248:251], %[a1]\n\t"
                "ds_read_b64 v[220:221], %[kb] offset:3968\n\t"
                "ds_read_b64 v[222:223], %[kb] offset:3976\n\t"
                "s_waitcnt lgkmcnt(9)\n\t"
                "v_mfma_f32_32x32x16_bf16 %[a1], v[192:195], v[196:199], %[a1]\n\t"
                "ds_read_b64 v[224:225], %[kb] offset:2016\n\t"
                "ds_read_b64 v[226:227], %[kb] offset:2024\n\t"
                "ds_read_b128 v[228:231], %[ub] offset:2560\n\t"
                "s_waitcnt lgkmcnt(11)\n\t"
                "v_mfma_f32_32x32x16_bf16 %[a0], v[192:195], v[200:203], %[a0]\n\t"
                "ds_read_b128 v[232:235], %[ub] offset:0\n\t"
                "s_waitcnt lgkmcnt(10)\n\t"
                "v_mfma_f32_32x32x16_bf16 %[a1], v[204:207], v[200:203], %[a1]\n\t"
                "ds_read_b64 v[236:237], %[kb] offset:4064\n\t"
                "ds_read_b64 v[238:239], %[kb] offset:4072\n\t"
                "s_waitcnt lgkmcnt(9)\n\t"
                "v_mfma_f32_32x32x16_bf16 %[a1], v[208:211], v[212:215], %[a1]\n\t"
                "ds_read_b64 v[240:241], %[kb] offset:1984\n\t"
                "ds_read_b64 v[242:243], %[kb] offset:1992\n\t"
                "ds_read_b128 v[244:247], %[ub] offset:2592\n\t"
                "s_waitcnt lgkmcnt(11)\n\t"
                "v_mfma_f32_32x32x16_bf16 %[a0], v[208:211], v[216:219], %[a0]\n\t"
                "ds_read_b128 v[248:251], %[ub] offset:32\n\t"
                "s_waitcnt lgkmcnt(10)\n\t"
                "v_mfma_f32_32x32x16_bf16 %[a1], v[220:223], v[216:219], %[a1]\n\t"
                "ds_read_b64 v[252:253], %[kb] offset:4032\n\t"
                "ds_read_b64 v[254:255], %[kb] offset:4040\n\t"
                "s_waitcnt lgkmcnt(9)\n\t"
                "v_mfma_f32_32x32x16_bf16 %[a1], v[224:227], v[228:231], %[a1]\n\t"
                "s_waitcnt lgkmcnt(8)\n\t"
                "v_mfma_f32_32x32x16_bf16 %[a0], v[224:227], v[232:235], %[a0]\n\t"
                "s_waitcnt lgkmcnt(6)\n\t"
                "v_mfma_f32_32x32x16_bf16 %[a1], v[236:239], v[232:235], %[a1]\n\t"
                "s_waitcnt lgkmcnt(3)\n\t"
                "v_mfma_f32_32x32x16_bf16 %[a1], v[240:243], v[244:247], %[a1]\n\t"
                "s_waitcnt lgkmcnt(2)\n\t"
                "v_mfma_f32_32x32x16_bf16 %[a0], v[240:243], v[248:251], %[a0]\n\t"
                "s_waitcnt lgkmcnt(0)\n\t"
                "v_mfma_f32_32x32x16_bf16 %[a1], v[252:255], v[248:251], %[a1]\n\t"
                "s_nop 7\n\t"
                "s_nop 3\n\t"
                : [a0] "=&v"(acc0), [a1] "=&v"(acc1) : [kb] "v"(kb), [ub] "v"(ub0 + c8 * CV_U) : "memory", "v192", "v193", "v194", "v195", "v196", "v197", "v198", "v199", "v200", "v201", "v202", "v203", "v204", "v205", "v206", "v207", "v208", "v209", "v210", "v211", "v212", "v213", "v214", "v215", "v216", "v217", "v218", "v219", "v220", "v221", "v222", "v223", "v224", "v225", "v226", "v227", "v228", "v229", "v230", "v231", "v232", "v233", "v234", "v235", "v236", "v237", "v238", "v239", "v240", "v241", "v242", "v243", "v244", "v245", "v246", "v247", "v248", "v249", "v250", "v251", "v252", "v253", "v254", "v255");
            unsigned char* uc = lds + c8 * CV_U + (32 + b) * CV_RP + kg * 8;
#pragma unroll
            for (int q = 0; q < 4; ++q) {
                *(v2u*)(uc + q * 16) = (v2u){pack2(acc0[4 * q], acc0[4 * q + 1]), pack2(acc0[4 * q + 2], acc0[4 * q + 3])};
                *(v2u*)(uc + 32 * CV_RP + q * 16) = (v2u){pack2(acc1[4 * q], acc1[4 * q + 1]), pack2(acc1[4 * q + 2], acc1[4 * q + 3])}; }
            __builtin_amdgcn_wave_barrier();
        }
    __syncthreads();
#pragma unroll
    for (int G = 0; G < 4; ++G) { const v4u lo = *(const v4u*)(urow + G * 16), hh = *(const v4u*)(urow + CV_U + G * 16); v4u o[2];
#pragma unroll
        for (int i = 0; i < 8; ++i) { const unsigned u0 = ur[G][(7 - i) >> 1], u1 = ur[4 + G][(7 - i) >> 1], qq = qr[2 * G + (i >> 2)][i & 3];
            const unsigned ud = pack2(((i & 1) ? blo(u0) : bhi(u0)) * blo(dv), ((i & 1) ? blo(u1) : bhi(u1)) * bhi(dv));
            const unsigned y = pack2(((i & 1) ? bhi(lo[i >> 1]) : blo(lo[i >> 1])) + blo(ud), ((i & 1) ? bhi(hh[i >> 1]) : blo(hh[i >> 1])) + bhi(ud));
            o[i >> 2][i & 3] = pack2(blo(qq) * blo(y), bhi(qq) * bhi(y)); }
        st16(r, W_A + un * 32768 + 2 * G * 4096 + tid * 16, o[0]); st16(r, W_A + un * 32768 + (2 * G + 1) * 4096 + tid * 16, o[1]); }
    DRAIN(); __syncthreads();
    if (tid == 0) __hip_atomic_fetch_add((unsigned*)(p.ws + F_CVY + (un >> 3) * 128), 1u, __ATOMIC_RELAXED, __HIP_MEMORY_SCOPE_AGENT);
    return true;
}

__device__ __forceinline__ void proj_block(const Params& p, unsigned char* lds, const int wv, const int h, const int c, const int j) {
    const int w = wv, l = otid() & 63, n = l & 15, g = l >> 4, tm = w >> 1, tn = w & 1;
    const unsigned lb = __builtin_amdgcn_readfirstlane((unsigned)(size_t)lds);
    const v4u sx = mk_srd(p.Aq, S * D * 2), sq = mk_srd(c < 2 ? p.Wq : p.Wk, 1024 * 4096 * 2), sv = mk_srd(p.Wv, 1024 * 4096 * 2), sw = mk_srd(p.ws, W_TOTAL);
    const unsigned lx = lb + (32 * tm + n) * 128 + ((g ^ (n & 7)) << 4), lr = lb + 131072, rr = 8 * g + (n >> 2);
    const int cv = ((c & 1) ^ 1) * 2 + (c >> 1);
    const unsigned aq = (c < 2 ? W_QX : W_KX) + (tm * 32 + h) * 8192 + (2 * (c & 1) + tn) * 1024 + l * 16;
    const int hq = h & 7, tk = ((hq - (hq > 1) - (hq > 5)) * 4 + c) * 4 + w, ts6 = tk % 6, tch = 15 - tk / 6, tx = tk - 6 * GTOUCH;
    const bf16_t* tp = (h & 3) == 1 ? p.gate_w : tx < 0 ? (ts6 % 3 == 0 ? p.Wq : ts6 % 3 == 1 ? p.Wk : p.Wv) + (size_t)(tch * 64 + l) * 4096 + ((h & 24) + 1 + (ts6 / 3) * 4) * 128 + 64
                     : tx < XTOUCH ? p.Aq + (tx * 64 + l) * 64 : p.gate_w;
    unsigned t0, xo, so, cn; unsigned long long ts, ta, tb, tc, td;
    asm volatile(
        "s_mov_b32 %[xo], 0\n\t"
        "s_mov_b32 %[so], 0\n\t"
        "s_mov_b32 %[cn], 1\n\t"
        "global_load_dword v88, v[114:115], off\n\t"
        "s_add_u32 m0, %[ld], 0\n\t"
        "s_add_u32 %[t0], %[xo], 0\n\t"
        "buffer_load_dwordx4 v100, %[sx], %[t0] offen lds\n\t"
        "s_add_u32 m0, %[ld], 1024\n\t"
        "s_add_u32 %[t0], %[xo], 16384\n\t"
        "buffer_load_dwordx4 v100, %[sx], %[t0] offen lds\n\t"
        "s_add_u32 m0, %[ld], 8192\n\t"
        "s_add_u32 %[t0], %[xo], 128\n\t"
        "buffer_load_dwordx4 v100, %[sx], %[t0] offen lds\n\t"
        "s_add_u32 m0, %[ld], 9216\n\t"
        "s_add_u32 %[t0], %[xo], 16512\n\t"
        "buffer_load_dwordx4 v100, %[sx], %[t0] offen lds\n\t"
        "s_add_u32 m0, %[ld], 16384\n\t"
        "s_add_u32 %[t0], %[xo], 256\n\t"
        "buffer_load_dwordx4 v100, %[sx], %[t0] offen lds\n\t"
        "s_add_u32 m0, %[ld], 17408\n\t"
        "s_add_u32 %[t0], %[xo], 16640\n\t"
        "buffer_load_dwordx4 v100, %[sx], %[t0] offen lds\n\t"
        "s_add_u32 m0, %[ld], 24576\n\t"
        "s_add_u32 %[t0], %[xo], 384\n\t"
        "buffer_load_dwordx4 v100, %[sx], %[t0] offen lds\n\t"
        "s_add_u32 m0, %[ld], 25600\n\t"
        "s_add_u32 %[t0], %[xo], 16768\n\t"
        "buffer_load_dwordx4 v100, %[sx], %[t0] offen lds\n\t"
        "s_mov_b32 %[t0], 0\n\t"
        "buffer_load_dwordx4 a[192:195], v101, %[sq], %[t0] offen\n\t"
        "buffer_load_dwordx4 a[200:203], v112, %[sv], %[t0] offen\n\t"
        "s_mov_b32 %[t0], 262144\n\t"
        "buffer_load_dwordx4 a[196:199], v101, %[sq], %[t0] offen\n\t"
        "s_mov_b32 %[t0], 524288\n\t"
        "buffer_load_dwordx4 a[204:207], v101, %[sq], %[t0] offen\n\t"
        "buffer_load_dwordx4 a[212:215], v112, %[sv], %[t0] offen\n\t"
        "s_mov_b32 %[t0], 786432\n\t"
        "buffer_load_dwordx4 a[208:211], v101, %[sq], %[t0] offen\n\t"
        "s_mov_b32 %[t0], 1048576\n\t"
        "buffer_load_dwordx4 a[216:219], v101, %[sq], %[t0] offen\n\t"
        "buffer_load_dwordx4 a[224:227], v112, %[sv], %[t0] offen\n\t"
        "s_mov_b32 %[t0], 1310720\n\t"
        "buffer_load_dwordx4 a[220:223], v101, %[sq], %[t0] offen\n\t"
        "s_mov_b32 %[t0], 1572864\n\t"
        "buffer_load_dwordx4 a[228:231], v101, %[sq], %[t0] offen\n\t"
        "buffer_load_dwordx4 a[236:239], v112, %[sv], %[t0] offen\n\t"
        "s_mov_b32 %[t0], 1835008\n\t"
        "buffer_load_dwordx4 a[232:235], v101, %[sq], %[t0] offen\n\t"
        "s_waitcnt vmcnt(9)\n\t"
        "ds_write_b128 v102, a[192:195] offset:0\n\t"
        "ds_write_b128 v102, a[196:199] offset:4096\n\t"
        "ds_write_b128 v116, a[200:203] offset:8192\n\t"
        "s_waitcnt lgkmcnt(0)\n\t"
        "s_barrier\n\t"
        "ds_read_b64_tr_b16 a[0:1], v103 offset:0\n\t"
        "ds_read_b64_tr_b16 a[2:3], v103 offset:512\n\t"
        "ds_read_b64_tr_b16 a[4:5], v103 offset:8\n\t"
        "ds_read_b64_tr_b16 a[6:7], v103 offset:520\n\t"
        "ds_read_b64_tr_b16 a[8:9], v104 offset:0\n\t"
        "ds_read_b64_tr_b16 a[10:11], v104 offset:256\n\t"
        "s_waitcnt lgkmcnt(5)\n\t"
        "ds_read_b64_tr_b16 a[12:13], v103 offset:4096\n\t"
        "ds_read_b64_tr_b16 a[14:15], v103 offset:4608\n\t"
        "ds_read_b64_tr_b16 a[16:17], v103 offset:4104\n\t"
        "ds_read_b64_tr_b16 a[18:19], v103 offset:4616\n\t"
        "ds_read_b64_tr_b16 a[20:21], v104 offset:2048\n\t"
        "ds_read_b64_tr_b16 a[22:23], v104 offset:2304\n\t"
        "ds_read_b128 v[56:59], v105 offset:0\n\t"
        "ds_read_b128 v[60:63], v105 offset:2048\n\t"
        "ds_read_b128 v[64:67], v106 offset:0\n\t"
        "ds_read_b128 v[68:71], v106 offset:2048\n\t"
        "s_add_u32 m0, %[ld], 32768\n\t"
        "s_add_u32 %[t0], %[xo], 512\n\t"
        "buffer_load_dwordx4 v100, %[sx], %[t0] offen lds\n\t"
        "s_add_u32 m0, %[ld], 33792\n\t"
        "s_add_u32 %[t0], %[xo], 16896\n\t"
        "buffer_load_dwordx4 v100, %[sx], %[t0] offen lds\n\t"
        "s_add_u32 m0, %[ld], 40960\n\t"
        "s_add_u32 %[t0], %[xo], 640\n\t"
        "buffer_load_dwordx4 v100, %[sx], %[t0] offen lds\n\t"
        "s_add_u32 m0, %[ld], 41984\n\t"
        "s_add_u32 %[t0], %[xo], 17024\n\t"
        "buffer_load_dwordx4 v100, %[sx], %[t0] offen lds\n\t"
        "s_add_u32 m0, %[ld], 49152\n\t"
        "s_add_u32 %[t0], %[xo], 768\n\t"
        "buffer_load_dwordx4 v100, %[sx], %[t0] offen lds\n\t"
        "s_add_u32 m0, %[ld], 50176\n\t"
        "s_add_u32 %[t0], %[xo], 17152\n\t"
        "buffer_load_dwordx4 v100, %[sx], %[t0] offen lds\n\t"
        "s_add_u32 m0, %[ld], 57344\n\t"
        "s_add_u32 %[t0], %[xo], 896\n\t"
        "buffer_load_dwordx4 v100, %[sx], %[t0] offen lds\n\t"
        "s_add_u32 m0, %[ld], 58368\n\t"
        "s_add_u32 %[t0], %[xo], 17280\n\t"
        "buffer_load_dwordx4 v100, %[sx], %[t0] offen lds\n\t"
        "s_mov_b32 %[t0], 2097152\n\t"
        "buffer_load_dwordx4 a[240:243], v101, %[sq], %[t0] offen\n\t"
        "buffer_load_dwordx4 a[248:251], v112, %[sv], %[t0] offen\n\t"
        "s_mov_b32 %[t0], 2359296\n\t"
        "buffer_load_dwordx4 a[244:247], v101, %[sq], %[t0] offen\n\t"
        "s_waitcnt vmcnt(17)\n\t"
        "ds_write_b128 v102, a[204:207] offset:16384\n\t"
        "ds_write_b128 v102, a[208:211] offset:20480\n\t"
        "ds_write_b128 v116, a[212:215] offset:24576\n\t"
        "s_waitcnt lgkmcnt(0)\n\t"
        "s_barrier\n\t"
        "v_mfma_f32_16x16x32_bf16 v[8:11], a[0:3], v[56:59], 0\n\t"
        "ds_read_b64_tr_b16 a[24:25], v103 offset:16384\n\t"
        "ds_read_b64_tr_b16 a[26:27], v103 offset:16896\n\t"
        "v_mfma_f32_16x16x32_bf16 v[12:15], a[4:7], v[56:59], 0\n\t"
        "ds_read_b64_tr_b16 a[28:29], v103 offset:16392\n\t"
        "ds_read_b64_tr_b16 a[30:31], v103 offset:16904\n\t"
        "v_mfma_f32_16x16x32_bf16 v[16:19], v[56:59], a[8:11], 0\n\t"
        "ds_read_b64_tr_b16 a[32:33], v104 offset:16384\n\t"
        "ds_read_b64_tr_b16 a[34:35], v104 offset:16640\n\t"
        "v_mfma_f32_16x16x32_bf16 v[20:23], a[0:3], v[60:63], 0\n\t"
        "s_waitcnt lgkmcnt(5)\n\t"
        "ds_read_b64_tr_b16 a[36:37], v103 offset:20480\n\t"
        "v_mfma_f32_16x16x32_bf16 v[24:27], a[4:7], v[60:63], 0\n\t"
        "ds_read_b64_tr_b16 a[38:39], v103 offset:20992\n\t"
        "ds_read_b64_tr_b16 a[40:41], v103 offset:20488\n\t"
        "v_mfma_f32_16x16x32_bf16 v[28:31], v[60:63], a[8:11], 0\n\t"
        "ds_read_b64_tr_b16 a[42:43], v103 offset:21000\n\t"
        "ds_read_b64_tr_b16 a[44:45], v104 offset:18432\n\t"
        "v_mfma_f32_16x16x32_bf16 v[8:11], a[12:15], v[64:67], v[8:11]\n\t"
        "ds_read_b64_tr_b16 a[46:47], v104 offset:18688\n\t"
        "ds_read_b128 v[72:75], v105 offset:8192\n\t"
        "v_mfma_f32_16x16x32_bf16 v[12:15], a[16:19], v[64:67], v[12:15]\n\t"
        "ds_read_b128 v[76:79], v105 offset:10240\n\t"
        "ds_read_b128 v[80:83], v106 offset:8192\n\t"
        "v_mfma_f32_16x16x32_bf16 v[16:19], v[64:67], a[20:23], v[16:19]\n\t"
        "ds_read_b128 v[84:87], v106 offset:10240\n\t"
        "v_mfma_f32_16x16x32_bf16 v[20:23], a[12:15], v[68:71], v[20:23]\n\t"
        "v_mfma_f32_16x16x32_bf16 v[24:27], a[16:19], v[68:71], v[24:27]\n\t"
        "v_mfma_f32_16x16x32_bf16 v[28:31], v[68:71], a[20:23], v[28:31]\n\t"
        "s_mov_b32 %[t0], 2621440\n\t"
        "buffer_load_dwordx4 a[252:255], v101, %[sq], %[t0] offen\n\t"
        "buffer_load_dwordx4 v[132:135], v112, %[sv], %[t0] offen\n\t"
        "s_mov_b32 %[t0], 2883584\n\t"
        "buffer_load_dwordx4 v[128:131], v101, %[sq], %[t0] offen\n\t"
        "s_waitcnt vmcnt(17)\n\t"
        "ds_write_b128 v102, a[216:219] offset:0\n\t"
        "ds_write_b128 v102, a[220:223] offset:4096\n\t"
        "ds_write_b128 v116, a[224:227] offset:8192\n\t"
        "s_waitcnt lgkmcnt(0)\n\t"
        "s_barrier\n\t"
        "v_mfma_f32_16x16x32_bf16 v[8:11], a[24:27], v[72:75], v[8:11]\n\t"
        "ds_read_b64_tr_b16 a[48:49], v103 offset:0\n\t"
        "ds_read_b64_tr_b16 a[50:51], v103 offset:512\n\t"
        "v_mfma_f32_16x16x32_bf16 v[12:15], a[28:31], v[72:75], v[12:15]\n\t"
        "ds_read_b64_tr_b16 a[52:53], v103 offset:8\n\t"
        "ds_read_b64_tr_b16 a[54:55], v103 offset:520\n\t"
        "v_mfma_f32_16x16x32_bf16 v[16:19], v[72:75], a[32:35], v[16:19]\n\t"
        "ds_read_b64_tr_b16 a[56:57], v104 offset:0\n\t"
        "ds_read_b64_tr_b16 a[58:59], v104 offset:256\n\t"
        "v_mfma_f32_16x16x32_bf16 v[20:23], a[24:27], v[76:79], v[20:23]\n\t"
        "s_waitcnt lgkmcnt(5)\n\t"
        "ds_read_b64_tr_b16 a[60:61], v103 offset:4096\n\t"
        "v_mfma_f32_16x16x32_bf16 v[24:27], a[28:31], v[76:79], v[24:27]\n\t"
        "ds_read_b64_tr_b16 a[62:63], v103 offset:4608\n\t"
        "ds_read_b64_tr_b16 a[64:65], v103 offset:4104\n\t"
        "v_mfma_f32_16x16x32_bf16 v[28:31], v[76:79], a[32:35], v[28:31]\n\t"
        "ds_read_b64_tr_b16 a[66:67], v103 offset:4616\n\t"
        "ds_read_b64_tr_b16 a[68:69], v104 offset:2048\n\t"
        "v_mfma_f32_16x16x32_bf16 v[8:11], a[36:39], v[80:83], v[8:11]\n\t"
        "ds_read_b64_tr_b16 a[70:71], v104 offset:2304\n\t"
        "ds_read_b128 v[56:59], v105 offset:16384\n\t"
        "v_mfma_f32_16x16x32_bf16 v[12:15], a[40:43], v[80:83], v[12:15]\n\t"
        "ds_read_b128 v[60:63], v105 offset:18432\n\t"
        "ds_read_b128 v[64:67], v106 offset:16384\n\t"
        "v_mfma_f32_16x16x32_bf16 v[16:19], v[80:83], a[44:47], v[16:19]\n\t"
        "ds_read_b128 v[68:71], v106 offset:18432\n\t"
        "v_mfma_f32_16x16x32_bf16 v[20:23], a[36:39], v[84:87], v[20:23]\n\t"
        "v_mfma_f32_16x16x32_bf16 v[24:27], a[40:43], v[84:87], v[24:27]\n\t"
        "v_mfma_f32_16x16x32_bf16 v[28:31], v[84:87], a[44:47], v[28:31]\n\t"
        "s_mov_b32 %[t0], 3145728\n\t"
        "buffer_load_dwordx4 v[136:139], v101, %[sq], %[t0] offen\n\t"
        "buffer_load_dwordx4 v[144:147], v112, %[sv], %[t0] offen\n\t"
        "s_mov_b32 %[t0], 3407872\n\t"
        "buffer_load_dwordx4 v[140:143], v101, %[sq], %[t0] offen\n\t"
        "s_waitcnt vmcnt(17)\n\t"
        "ds_write_b128 v102, a[228:231] offset:16384\n\t"
        "ds_write_b128 v102, a[232:235] offset:20480\n\t"
        "ds_write_b128 v116, a[236:239] offset:24576\n\t"
        "s_waitcnt lgkmcnt(0)\n\t"
        "s_barrier\n\t"
        "v_mfma_f32_16x16x32_bf16 v[8:11], a[48:51], v[56:59], v[8:11]\n\t"
        "ds_read_b64_tr_b16 a[72:73], v103 offset:16384\n\t"
        "ds_read_b64_tr_b16 a[74:75], v103 offset:16896\n\t"
        "v_mfma_f32_16x16x32_bf16 v[12:15], a[52:55], v[56:59], v[12:15]\n\t"
        "ds_read_b64_tr_b16 a[76:77], v103 offset:16392\n\t"
        "ds_read_b64_tr_b16 a[78:79], v103 offset:16904\n\t"
        "v_mfma_f32_16x16x32_bf16 v[16:19], v[56:59], a[56:59], v[16:19]\n\t"
        "ds_read_b64_tr_b16 a[80:81], v104 offset:16384\n\t"
        "ds_read_b64_tr_b16 a[82:83], v104 offset:16640\n\t"
        "v_mfma_f32_16x16x32_bf16 v[20:23], a[48:51], v[60:63], v[20:23]\n\t"
        "s_waitcnt lgkmcnt(5)\n\t"
        "ds_read_b64_tr_b16 a[84:85], v103 offset:20480\n\t"
        "v_mfma_f32_16x16x32_bf16 v[24:27], a[52:55], v[60:63], v[24:27]\n\t"
        "ds_read_b64_tr_b16 a[86:87], v103 offset:20992\n\t"
        "ds_read_b64_tr_b16 a[88:89], v103 offset:20488\n\t"
        "v_mfma_f32_16x16x32_bf16 v[28:31], v[60:63], a[56:59], v[28:31]\n\t"
        "ds_read_b64_tr_b16 a[90:91], v103 offset:21000\n\t"
        "ds_read_b64_tr_b16 a[92:93], v104 offset:18432\n\t"
        "v_mfma_f32_16x16x32_bf16 v[8:11], a[60:63], v[64:67], v[8:11]\n\t"
        "ds_read_b64_tr_b16 a[94:95], v104 offset:18688\n\t"
        "ds_read_b128 v[72:75], v105 offset:24576\n\t"
        "v_mfma_f32_16x16x32_bf16 v[12:15], a[64:67], v[64:67], v[12:15]\n\t"
        "ds_read_b128 v[76:79], v105 offset:26624\n\t"
        "ds_read_b128 v[80:83], v106 offset:24576\n\t"
        "v_mfma_f32_16x16x32_bf16 v[16:19], v[64:67], a[68:71], v[16:19]\n\t"
        "ds_read_b128 v[84:87], v106 offset:26624\n\t"
        "v_mfma_f32_16x16x32_bf16 v[20:23], a[60:63], v[68:71], v[20:23]\n\t"
        "v_mfma_f32_16x16x32_bf16 v[24:27], a[64:67], v[68:71], v[24:27]\n\t"
        "v_mfma_f32_16x16x32_bf16 v[28:31], v[68:71], a[68:71], v[28:31]\n\t"
        "s_mov_b32 %[t0], 3670016\n\t"
        "buffer_load_dwordx4 v[148:151], v101, %[sq], %[t0] offen\n\t"
        "buffer_load_dwordx4 v[156:159], v112, %[sv], %[t0] offen\n\t"
        "s_mov_b32 %[t0], 3932160\n\t"
        "buffer_load_dwordx4 v[152:155], v101, %[sq], %[t0] offen\n\t"
        "s_waitcnt vmcnt(9)\n\t"
        "ds_write_b128 v102, a[240:243] offset:0\n\t"
        "ds_write_b128 v102, a[244:247] offset:4096\n\t"
        "ds_write_b128 v116, a[248:251] offset:8192\n\t"
        "s_waitcnt lgkmcnt(0)\n\t"
        "s_barrier\n\t"
        "v_mfma_f32_16x16x32_bf16 v[8:11], a[72:75], v[72:75], v[8:11]\n\t"
        "ds_read_b64_tr_b16 a[96:97], v103 offset:0\n\t"
        "ds_read_b64_tr_b16 a[98:99], v103 offset:512\n\t"
        "v_mfma_f32_16x16x32_bf16 v[12:15], a[76:79], v[72:75], v[12:15]\n\t"
        "ds_read_b64_tr_b16 a[100:101], v103 offset:8\n\t"
        "ds_read_b64_tr_b16 a[102:103], v103 offset:520\n\t"
        "v_mfma_f32_16x16x32_bf16 v[16:19], v[72:75], a[80:83], v[16:19]\n\t"
        "ds_read_b64_tr_b16 a[104:105], v104 offset:0\n\t"
        "ds_read_b64_tr_b16 a[106:107], v104 offset:256\n\t"
        "v_mfma_f32_16x16x32_bf16 v[20:23], a[72:75], v[76:79], v[20:23]\n\t"
        "s_waitcnt lgkmcnt(5)\n\t"
        "ds_read_b64_tr_b16 a[108:109], v103 offset:4096\n\t"
        "v_mfma_f32_16x16x32_bf16 v[24:27], a[76:79], v[76:79], v[24:27]\n\t"
        "ds_read_b64_tr_b16 a[110:111], v103 offset:4608\n\t"
        "ds_read_b64_tr_b16 a[112:113], v103 offset:4104\n\t"
        "v_mfma_f32_16x16x32_bf16 v[28:31], v[76:79], a[80:83], v[28:31]\n\t"
        "ds_read_b64_tr_b16 a[114:115], v103 offset:4616\n\t"
        "ds_read_b64_tr_b16 a[116:117], v104 offset:2048\n\t"
        "v_mfma_f32_16x16x32_bf16 v[8:11], a[84:87], v[80:83], v[8:11]\n\t"
        "ds_read_b64_tr_b16 a[118:119], v104 offset:2304\n\t"
        "ds_read_b128 v[56:59], v105 offset:32768\n\t"
        "v_mfma_f32_16x16x32_bf16 v[12:15], a[88:91], v[80:83], v[12:15]\n\t"
        "ds_read_b128 v[60:63], v105 offset:34816\n\t"
        "ds_read_b128 v[64:67], v106 offset:32768\n\t"
        "v_mfma_f32_16x16x32_bf16 v[16:19], v[80:83], a[92:95], v[16:19]\n\t"
        "ds_read_b128 v[68:71], v106 offset:34816\n\t"
        "v_mfma_f32_16x16x32_bf16 v[20:23], a[84:87], v[84:87], v[20:23]\n\t"
        "v_mfma_f32_16x16x32_bf16 v[24:27], a[88:91], v[84:87], v[24:27]\n\t"
        "v_mfma_f32_16x16x32_bf16 v[28:31], v[84:87], a[92:95], v[28:31]\n\t"
        "s_add_u32 m0, %[ld], 65536\n\t"
        "s_add_u32 %[t0], %[xo], 1024\n\t"
        "buffer_load_dwordx4 v100, %[sx], %[t0] offen lds\n\t"
        "s_add_u32 m0, %[ld], 66560\n\t"
        "s_add_u32 %[t0], %[xo], 17408\n\t"
        "buffer_load_dwordx4 v100, %[sx], %[t0] offen lds\n\t"
        "s_add_u32 m0, %[ld], 73728\n\t"
        "s_add_u32 %[t0], %[xo], 1152\n\t"
        "buffer_load_dwordx4 v100, %[sx], %[t0] offen lds\n\t"
        "s_add_u32 m0, %[ld], 74752\n\t"
        "s_add_u32 %[t0], %[xo], 17536\n\t"
        "buffer_load_dwordx4 v100, %[sx], %[t0] offen lds\n\t"
        "s_add_u32 m0, %[ld], 81920\n\t"
        "s_add_u32 %[t0], %[xo], 1280\n\t"
        "buffer_load_dwordx4 v100, %[sx], %[t0] offen lds\n\t"
        "s_add_u32 m0, %[ld], 82944\n\t"
        "s_add_u32 %[t0], %[xo], 17664\n\t"
        "buffer_load_dwordx4 v100, %[sx], %[t0] offen lds\n\t"
        "s_add_u32 m0, %[ld], 90112\n\t"
        "s_add_u32 %[t0], %[xo], 1408\n\t"
        "buffer_load_dwordx4 v100, %[sx], %[t0] offen lds\n\t"
        "s_add_u32 m0, %[ld], 91136\n\t"
        "s_add_u32 %[t0], %[xo], 17792\n\t"
        "buffer_load_dwordx4 v100, %[sx], %[t0] offen lds\n\t"
        "s_mov_b32 %[t0], 4194304\n\t"
        "buffer_load_dwordx4 v[160:163], v101, %[sq], %[t0] offen\n\t"
        "buffer_load_dwordx4 v[168:171], v112, %[sv], %[t0] offen\n\t"
        "s_mov_b32 %[t0], 4456448\n\t"
        "buffer_load_dwordx4 v[164:167], v101, %[sq], %[t0] offen\n\t"
        "s_waitcnt vmcnt(17)\n\t"
        "ds_write_b128 v102, a[252:255] offset:16384\n\t"
        "ds_write_b128 v102, v[128:131] offset:20480\n\t"
        "ds_write_b128 v116, v[132:135] offset:24576\n\t"
        "s_waitcnt lgkmcnt(0)\n\t"
        "s_barrier\n\t"
        "v_mfma_f32_16x16x32_bf16 v[8:11], a[96:99], v[56:59], v[8:11]\n\t"
        "ds_read_b64_tr_b16 a[120:121], v103 offset:16384\n\t"
        "ds_read_b64_tr_b16 a[122:123], v103 offset:16896\n\t"
        "v_mfma_f32_16x16x32_bf16 v[12:15], a[100:103], v[56:59], v[12:15]\n\t"
        "ds_read_b64_tr_b16 a[124:125], v103 offset:16392\n\t"
        "ds_read_b64_tr_b16 a[126:127], v103 offset:16904\n\t"
        "v_mfma_f32_16x16x32_bf16 v[16:19], v[56:59], a[104:107], v[16:19]\n\t"
        "ds_read_b64_tr_b16 a[128:129], v104 offset:16384\n\t"
        "ds_read_b64_tr_b16 a[130:131], v104 offset:16640\n\t"
        "v_mfma_f32_16x16x32_bf16 v[20:23], a[96:99], v[60:63], v[20:23]\n\t"
        "s_waitcnt lgkmcnt(5)\n\t"
        "ds_read_b64_tr_b16 a[132:133], v103 offset:20480\n\t"
        "v_mfma_f32_16x16x32_bf16 v[24:27], a[100:103], v[60:63], v[24:27]\n\t"
        "ds_read_b64_tr_b16 a[134:135], v103 offset:20992\n\t"
        "ds_read_b64_tr_b16 a[136:137], v103 offset:20488\n\t"
        "v_mfma_f32_16x16x32_bf16 v[28:31], v[60:63], a[104:107], v[28:31]\n\t"
        "ds_read_b64_tr_b16 a[138:139], v103 offset:21000\n\t"
        "ds_read_b64_tr_b16 a[140:141], v104 offset:18432\n\t"
        "v_mfma_f32_16x16x32_bf16 v[8:11], a[108:111], v[64:67], v[8:11]\n\t"
        "ds_read_b64_tr_b16 a[142:143], v104 offset:18688\n\t"
        "ds_read_b128 v[72:75], v105 offset:40960\n\t"
        "v_mfma_f32_16x16x32_bf16 v[12:15], a[112:115], v[64:67], v[12:15]\n\t"
        "ds_read_b128 v[76:79], v105 offset:43008\n\t"
        "ds_read_b128 v[80:83], v106 offset:40960\n\t"
        "v_mfma_f32_16x16x32_bf16 v[16:19], v[64:67], a[116:119], v[16:19]\n\t"
        "ds_read_b128 v[84:87], v106 offset:43008\n\t"
        "v_mfma_f32_16x16x32_bf16 v[20:23], a[108:111], v[68:71], v[20:23]\n\t"
        "v_mfma_f32_16x16x32_bf16 v[24:27], a[112:115], v[68:71], v[24:27]\n\t"
        "v_mfma_f32_16x16x32_bf16 v[28:31], v[68:71], a[116:119], v[28:31]\n\t"
        "s_mov_b32 %[t0], 4718592\n\t"
        "buffer_load_dwordx4 v[172:175], v101, %[sq], %[t0] offen\n\t"
        "buffer_load_dwordx4 v[180:183], v112, %[sv], %[t0] offen\n\t"
        "s_mov_b32 %[t0], 4980736\n\t"
        "buffer_load_dwordx4 v[176:179], v101, %[sq], %[t0] offen\n\t"
        "s_waitcnt vmcnt(17)\n\t"
        "ds_write_b128 v102, v[136:139] offset:0\n\t"
        "ds_write_b128 v102, v[140:143] offset:4096\n\t"
        "ds_write_b128 v116, v[144:147] offset:8192\n\t"
        "s_waitcnt lgkmcnt(0)\n\t"
        "s_barrier\n\t"
        "v_mfma_f32_16x16x32_bf16 v[8:11], a[120:123], v[72:75], v[8:11]\n\t"
        "ds_read_b64_tr_b16 a[144:145], v103 offset:0\n\t"
        "ds_read_b64_tr_b16 a[146:147], v103 offset:512\n\t"
        "v_mfma_f32_16x16x32_bf16 v[12:15], a[124:127], v[72:75], v[12:15]\n\t"
        "ds_read_b64_tr_b16 a[148:149], v103 offset:8\n\t"
        "ds_read_b64_tr_b16 a[150:151], v103 offset:520\n\t"
        "v_mfma_f32_16x16x32_bf16 v[16:19], v[72:75], a[128:131], v[16:19]\n\t"
        "ds_read_b64_tr_b16 a[152:153], v104 offset:0\n\t"
        "ds_read_b64_tr_b16 a[154:155], v104 offset:256\n\t"
        "v_mfma_f32_16x16x32_bf16 v[20:23], a[120:123], v[76:79], v[20:23]\n\t"
        "s_waitcnt lgkmcnt(5)\n\t"
        "ds_read_b64_tr_b16 a[156:157], v103 offset:4096\n\t"
        "v_mfma_f32_16x16x32_bf16 v[24:27], a[124:127], v[76:79], v[24:27]\n\t"
        "ds_read_b64_tr_b16 a[158:159], v103 offset:4608\n\t"
        "ds_read_b64_tr_b16 a[160:161], v103 offset:4104\n\t"
        "v_mfma_f32_16x16x32_bf16 v[28:31], v[76:79], a[128:131], v[28:31]\n\t"
        "ds_read_b64_tr_b16 a[162:163], v103 offset:4616\n\t"
        "ds_read_b64_tr_b16 a[164:165], v104 offset:2048\n\t"
        "v_mfma_f32_16x16x32_bf16 v[8:11], a[132:135], v[80:83], v[8:11]\n\t"
        "ds_read_b64_tr_b16 a[166:167], v104 offset:2304\n\t"
        "ds_read_b128 v[56:59], v105 offset:49152\n\t"
        "v_mfma_f32_16x16x32_bf16 v[12:15], a[136:139], v[80:83], v[12:15]\n\t"
        "ds_read_b128 v[60:63], v105 offset:51200\n\t"
        "ds_read_b128 v[64:67], v106 offset:49152\n\t"
        "v_mfma_f32_16x16x32_bf16 v[16:19], v[80:83], a[140:143], v[16:19]\n\t"
        "ds_read_b128 v[68:71], v106 offset:51200\n\t"
        "v_mfma_f32_16x16x32_bf16 v[20:23], a[132:135], v[84:87], v[20:23]\n\t"
        "v_mfma_f32_16x16x32_bf16 v[24:27], a[136:139], v[84:87], v[24:27]\n\t"
        "v_mfma_f32_16x16x32_bf16 v[28:31], v[84:87], a[140:143], v[28:31]\n\t"
        "s_mov_b32 %[t0], 5242880\n\t"
        "buffer_load_dwordx4 v[184:187], v101, %[sq], %[t0] offen\n\t"
        "buffer_load_dwordx4 v[192:195], v112, %[sv], %[t0] offen\n\t"
        "s_mov_b32 %[t0], 5505024\n\t"
        "buffer_load_dwordx4 v[188:191], v101, %[sq], %[t0] offen\n\t"
        "s_waitcnt vmcnt(17)\n\t"
        "ds_write_b128 v102, v[148:151] offset:16384\n\t"
        "ds_write_b128 v102, v[152:155] offset:20480\n\t"
        "ds_write_b128 v116, v[156:159] offset:24576\n\t"
        "s_waitcnt lgkmcnt(0)\n\t"
        "s_barrier\n\t"
        "v_mfma_f32_16x16x32_bf16 v[8:11], a[144:147], v[56:59], v[8:11]\n\t"
        "ds_read_b64_tr_b16 a[168:169], v103 offset:16384\n\t"
        "ds_read_b64_tr_b16 a[170:171], v103 offset:16896\n\t"
        "v_mfma_f32_16x16x32_bf16 v[12:15], a[148:151], v[56:59], v[12:15]\n\t"
        "ds_read_b64_tr_b16 a[172:173], v103 offset:16392\n\t"
        "ds_read_b64_tr_b16 a[174:175], v103 offset:16904\n\t"
        "v_mfma_f32_16x16x32_bf16 v[16:19], v[56:59], a[152:155], v[16:19]\n\t"
        "ds_read_b64_tr_b16 a[176:177], v104 offset:16384\n\t"
        "ds_read_b64_tr_b16 a[178:179], v104 offset:16640\n\t"
        "v_mfma_f32_16x16x32_bf16 v[20:23], a[144:147], v[60:63], v[20:23]\n\t"
        "s_waitcnt lgkmcnt(5)\n\t"
        "ds_read_b64_tr_b16 a[180:181], v103 offset:20480\n\t"
        "v_mfma_f32_16x16x32_bf16 v[24:27], a[148:151], v[60:63], v[24:27]\n\t"
        "ds_read_b64_tr_b16 a[182:183], v103 offset:20992\n\t"
        "ds_read_b64_tr_b16 a[184:185], v103 offset:20488\n\t"
        "v_mfma_f32_16x16x32_bf16 v[28:31], v[60:63], a[152:155], v[28:31]\n\t"
        "ds_read_b64_tr_b16 a[186:187], v103 offset:21000\n\t"
        "ds_read_b64_tr_b16 a[188:189], v104 offset:18432\n\t"
        "v_mfma_f32_16x16x32_bf16 v[8:11], a[156:159], v[64:67], v[8:11]\n\t"
        "ds_read_b64_tr_b16 a[190:191], v104 offset:18688\n\t"
        "ds_read_b128 v[72:75], v105 offset:57344\n\t"
        "v_mfma_f32_16x16x32_bf16 v[12:15], a[160:163], v[64:67], v[12:15]\n\t"
        "ds_read_b128 v[76:79], v105 offset:59392\n\t"
        "ds_read_b128 v[80:83], v106 offset:57344\n\t"
        "v_mfma_f32_16x16x32_bf16 v[16:19], v[64:67], a[164:167], v[16:19]\n\t"
        "ds_read_b128 v[84:87], v106 offset:59392\n\t"
        "v_mfma_f32_16x16x32_bf16 v[20:23], a[156:159], v[68:71], v[20:23]\n\t"
        "v_mfma_f32_16x16x32_bf16 v[24:27], a[160:163], v[68:71], v[24:27]\n\t"
        "v_mfma_f32_16x16x32_bf16 v[28:31], v[68:71], a[164:167], v[28:31]\n\t"
        "s_mov_b32 %[t0], 5767168\n\t"
        "buffer_load_dwordx4 v[196:199], v101, %[sq], %[t0] offen\n\t"
        "buffer_load_dwordx4 v[204:207], v112, %[sv], %[t0] offen\n\t"
        "s_mov_b32 %[t0], 6029312\n\t"
        "buffer_load_dwordx4 v[200:203], v101, %[sq], %[t0] offen\n\t"
        "s_waitcnt vmcnt(9)\n\t"
        "ds_write_b128 v102, v[160:163] offset:0\n\t"
        "ds_write_b128 v102, v[164:167] offset:4096\n\t"
        "ds_write_b128 v116, v[168:171] offset:8192\n\t"
        "s_waitcnt lgkmcnt(0)\n\t"
        "s_barrier\n\t"
        "v_mfma_f32_16x16x32_bf16 v[8:11], a[168:171], v[72:75], v[8:11]\n\t"
        "ds_read_b64_tr_b16 a[192:193], v103 offset:0\n\t"
        "ds_read_b64_tr_b16 a[194:195], v103 offset:512\n\t"
        "v_mfma_f32_16x16x32_bf16 v[12:15], a[172:175], v[72:75], v[12:15]\n\t"
        "ds_read_b64_tr_b16 a[196:197], v103 offset:8\n\t"
        "ds_read_b64_tr_b16 a[198:199], v103 offset:520\n\t"
        "v_mfma_f32_16x16x32_bf16 v[16:19], v[72:75], a[176:179], v[16:19]\n\t"
        "ds_read_b64_tr_b16 a[200:201], v104 offset:0\n\t"
        "ds_read_b64_tr_b16 a[202:203], v104 offset:256\n\t"
        "v_mfma_f32_16x16x32_bf16 v[20:23], a[168:171], v[76:79], v[20:23]\n\t"
        "s_waitcnt lgkmcnt(5)\n\t"
        "ds_read_b64_tr_b16 a[204:205], v103 offset:4096\n\t"
        "v_mfma_f32_16x16x32_bf16 v[24:27], a[172:175], v[76:79], v[24:27]\n\t"
        "ds_read_b64_tr_b16 a[206:207], v103 offset:4608\n\t"
        "ds_read_b64_tr_b16 a[208:209], v103 offset:4104\n\t"
        "v_mfma_f32_16x16x32_bf16 v[28:31], v[76:79], a[176:179], v[28:31]\n\t"
        "ds_read_b64_tr_b16 a[210:211], v103 offset:4616\n\t"
        "ds_read_b64_tr_b16 a[212:213], v104 offset:2048\n\t"
        "v_mfma_f32_16x16x32_bf16 v[8:11], a[180:183], v[80:83], v[8:11]\n\t"
        "ds_read_b64_tr_b16 a[214:215], v104 offset:2304\n\t"
        "ds_read_b128 v[56:59], v107 offset:0\n\t"
        "v_mfma_f32_16x16x32_bf16 v[12:15], a[184:187], v[80:83], v[12:15]\n\t"
        "ds_read_b128 v[60:63], v107 offset:2048\n\t"
        "ds_read_b128 v[64:67], v108 offset:0\n\t"
        "v_mfma_f32_16x16x32_bf16 v[16:19], v[80:83], a[188:191], v[16:19]\n\t"
        "ds_read_b128 v[68:71], v108 offset:2048\n\t"
        "v_mfma_f32_16x16x32_bf16 v[20:23], a[180:183], v[84:87], v[20:23]\n\t"
        "v_mfma_f32_16x16x32_bf16 v[24:27], a[184:187], v[84:87], v[24:27]\n\t"
        "v_mfma_f32_16x16x32_bf16 v[28:31], v[84:87], a[188:191], v[28:31]\n\t"
        "s_add_u32 m0, %[ld], 98304\n\t"
        "s_add_u32 %[t0], %[xo], 1536\n\t"
        "buffer_load_dwordx4 v100, %[sx], %[t0] offen lds\n\t"
        "s_add_u32 m0, %[ld], 99328\n\t"
        "s_add_u32 %[t0], %[xo], 17920\n\t"
        "buffer_load_dwordx4 v100, %[sx], %[t0] offen lds\n\t"
        "s_add_u32 m0, %[ld], 106496\n\t"
        "s_add_u32 %[t0], %[xo], 1664\n\t"
        "buffer_load_dwordx4 v100, %[sx], %[t0] offen lds\n\t"
        "s_add_u32 m0, %[ld], 107520\n\t"
        "s_add_u32 %[t0], %[xo], 18048\n\t"
        "buffer_load_dwordx4 v100, %[sx], %[t0] offen lds\n\t"
        "s_add_u32 m0, %[ld], 114688\n\t"
        "s_add_u32 %[t0], %[xo], 1792\n\t"
        "buffer_load_dwordx4 v100, %[sx], %[t0] offen lds\n\t"
        "s_add_u32 m0, %[ld], 115712\n\t"
        "s_add_u32 %[t0], %[xo], 18176\n\t"
        "buffer_load_dwordx4 v100, %[sx], %[t0] offen lds\n\t"
        "s_add_u32 m0, %[ld], 122880\n\t"
        "s_add_u32 %[t0], %[xo], 1920\n\t"
        "buffer_load_dwordx4 v100, %[sx], %[t0] offen lds\n\t"
        "s_add_u32 m0, %[ld], 123904\n\t"
        "s_add_u32 %[t0], %[xo], 18304\n\t"
        "buffer_load_dwordx4 v100, %[sx], %[t0] offen lds\n\t"
        "s_mov_b32 %[t0], 6291456\n\t"
        "buffer_load_dwordx4 v[208:211], v101, %[sq], %[t0] offen\n\t"
        "buffer_load_dwordx4 v[216:219], v112, %[sv], %[t0] offen\n\t"
        "s_mov_b32 %[t0], 6553600\n\t"
        "buffer_load_dwordx4 v[212:215], v101, %[sq], %[t0] offen\n\t"
        "s_waitcnt vmcnt(17)\n\t"
        "ds_write_b128 v102, v[172:175] offset:16384\n\t"
        "ds_write_b128 v102, v[176:179] offset:20480\n\t"
        "ds_write_b128 v116, v[180:183] offset:24576\n\t"
        "s_waitcnt lgkmcnt(0)\n\t"
        "s_barrier\n\t"
        "v_mfma_f32_16x16x32_bf16 v[8:11], a[192:195], v[56:59], v[8:11]\n\t"
        "ds_read_b64_tr_b16 a[216:217], v103 offset:16384\n\t"
        "ds_read_b64_tr_b16 a[218:219], v103 offset:16896\n\t"
        "v_mfma_f32_16x16x32_bf16 v[12:15], a[196:199], v[56:59], v[12:15]\n\t"
        "ds_read_b64_tr_b16 a[220:221], v103 offset:16392\n\t"
        "ds_read_b64_tr_b16 a[222:223], v103 offset:16904\n\t"
        "v_mfma_f32_16x16x32_bf16 v[16:19], v[56:59], a[200:203], v[16:19]\n\t"
        "ds_read_b64_tr_b16 a[224:225], v104 offset:16384\n\t"
        "ds_read_b64_tr_b16 a[226:227], v104 offset:16640\n\t"
        "v_mfma_f32_16x16x32_bf16 v[20:23], a[192:195], v[60:63], v[20:23]\n\t"
        "s_waitcnt lgkmcnt(5)\n\t"
        "ds_read_b64_tr_b16 a[228:229], v103 offset:20480\n\t"
        "v_mfma_f32_16x16x32_bf16 v[24:27], a[196:199], v[60:63], v[24:27]\n\t"
        "ds_read_b64_tr_b16 a[230:231], v103 offset:20992\n\t"
        "ds_read_b64_tr_b16 a[232:233], v103 offset:20488\n\t"
        "v_mfma_f32_16x16x32_bf16 v[28:31], v[60:63], a[200:203], v[28:31]\n\t"
        "ds_read_b64_tr_b16 a[234:235], v103 offset:21000\n\t"
        "ds_read_b64_tr_b16 a[236:237], v104 offset:18432\n\t"
        "v_mfma_f32_16x16x32_bf16 v[8:11], a[204:207], v[64:67], v[8:11]\n\t"
        "ds_read_b64_tr_b16 a[238:239], v104 offset:18688\n\t"
        "ds_read_b128 v[72:75], v107 offset:8192\n\t"
        "v_mfma_f32_16x16x32_bf16 v[12:15], a[208:211], v[64:67], v[12:15]\n\t"
        "ds_read_b128 v[76:79], v107 offset:10240\n\t"
        "ds_read_b128 v[80:83], v108 offset:8192\n\t"
        "v_mfma_f32_16x16x32_bf16 v[16:19], v[64:67], a[212:215], v[16:19]\n\t"
        "ds_read_b128 v[84:87], v108 offset:10240\n\t"
        "v_mfma_f32_16x16x32_bf16 v[20:23], a[204:207], v[68:71], v[20:23]\n\t"
        "v_mfma_f32_16x16x32_bf16 v[24:27], a[208:211], v[68:71], v[24:27]\n\t"
        "v_mfma_f32_16x16x32_bf16 v[28:31], v[68:71], a[212:215], v[28:31]\n\t"
        "s_mov_b32 %[t0], 6815744\n\t"
        "buffer_load_dwordx4 v[220:223], v101, %[sq], %[t0] offen\n\t"
        "buffer_load_dwordx4 v[228:231], v112, %[sv], %[t0] offen\n\t"
        "s_mov_b32 %[t0], 7077888\n\t"
        "buffer_load_dwordx4 v[224:227], v101, %[sq], %[t0] offen\n\t"
        "s_waitcnt vmcnt(17)\n\t"
        "ds_write_b128 v102, v[184:187] offset:0\n\t"
        "ds_write_b128 v102, v[188:191] offset:4096\n\t"
        "ds_write_b128 v116, v[192:195] offset:8192\n\t"
        "s_waitcnt lgkmcnt(0)\n\t"
        "s_barrier\n\t"
        "v_mfma_f32_16x16x32_bf16 v[8:11], a[216:219], v[72:75], v[8:11]\n\t"
        "ds_read_b64_tr_b16 a[240:241], v103 offset:0\n\t"
        "ds_read_b64_tr_b16 a[242:243], v103 offset:512\n\t"
        "v_mfma_f32_16x16x32_bf16 v[12:15], a[220:223], v[72:75], v[12:15]\n\t"
        "ds_read_b64_tr_b16 a[244:245], v103 offset:8\n\t"
        "ds_read_b64_tr_b16 a[246:247], v103 offset:520\n\t"
        "v_mfma_f32_16x16x32_bf16 v[16:19], v[72:75], a[224:227], v[16:19]\n\t"
        "ds_read_b64_tr_b16 a[248:249], v104 offset:0\n\t"
        "ds_read_b64_tr_b16 a[250:251], v104 offset:256\n\t"
        "v_mfma_f32_16x16x32_bf16 v[20:23], a[216:219], v[76:79], v[20:23]\n\t"
        "s_waitcnt lgkmcnt(5)\n\t"
        "ds_read_b64_tr_b16 a[252:253], v103 offset:4096\n\t"
        "v_mfma_f32_16x16x32_bf16 v[24:27], a[220:223], v[76:79], v[24:27]\n\t"
        "ds_read_b64_tr_b16 a[254:255], v103 offset:4608\n\t"
        "ds_read_b64_tr_b16 v[128:129], v103 offset:4104\n\t"
        "v_mfma_f32_16x16x32_bf16 v[28:31], v[76:79], a[224:227], v[28:31]\n\t"
        "ds_read_b64_tr_b16 v[130:131], v103 offset:4616\n\t"
        "ds_read_b64_tr_b16 v[132:133], v104 offset:2048\n\t"
        "v_mfma_f32_16x16x32_bf16 v[8:11], a[228:231], v[80:83], v[8:11]\n\t"
        "ds_read_b64_tr_b16 v[134:135], v104 offset:2304\n\t"
        "ds_read_b128 v[56:59], v107 offset:16384\n\t"
        "v_mfma_f32_16x16x32_bf16 v[12:15], a[232:235], v[80:83], v[12:15]\n\t"
        "ds_read_b128 v[60:63], v107 offset:18432\n\t"
        "ds_read_b128 v[64:67], v108 offset:16384\n\t"
        "v_mfma_f32_16x16x32_bf16 v[16:19], v[80:83], a[236:239], v[16:19]\n\t"
        "ds_read_b128 v[68:71], v108 offset:18432\n\t"
        "v_mfma_f32_16x16x32_bf16 v[20:23], a[228:231], v[84:87], v[20:23]\n\t"
        "v_mfma_f32_16x16x32_bf16 v[24:27], a[232:235], v[84:87], v[24:27]\n\t"
        "v_mfma_f32_16x16x32_bf16 v[28:31], v[84:87], a[236:239], v[28:31]\n\t"
        "s_mov_b32 %[t0], 7340032\n\t"
        "buffer_load_dwordx4 v[232:235], v101, %[sq], %[t0] offen\n\t"
        "buffer_load_dwordx4 v[240:243], v112, %[sv], %[t0] offen\n\t"
        "s_mov_b32 %[t0], 7602176\n\t"
        "buffer_load_dwordx4 v[236:239], v101, %[sq], %[t0] offen\n\t"
        "s_waitcnt vmcnt(17)\n\t"
        "ds_write_b128 v102, v[196:199] offset:16384\n\t"
        "ds_write_b128 v102, v[200:203] offset:20480\n\t"
        "ds_write_b128 v116, v[204:207] offset:24576\n\t"
        "s_waitcnt lgkmcnt(0)\n\t"
        "s_barrier\n\t"
        "v_mfma_f32_16x16x32_bf16 v[8:11], a[240:243], v[56:59], v[8:11]\n\t"
        "ds_read_b64_tr_b16 v[136:137], v103 offset:16384\n\t"
        "ds_read_b64_tr_b16 v[138:139], v103 offset:16896\n\t"
        "v_mfma_f32_16x16x32_bf16 v[12:15], a[244:247], v[56:59], v[12:15]\n\t"
        "ds_read_b64_tr_b16 v[140:141], v103 offset:16392\n\t"
        "ds_read_b64_tr_b16 v[142:143], v103 offset:16904\n\t"
        "v_mfma_f32_16x16x32_bf16 v[16:19], v[56:59], a[248:251], v[16:19]\n\t"
        "ds_read_b64_tr_b16 v[144:145], v104 offset:16384\n\t"
        "ds_read_b64_tr_b16 v[146:147], v104 offset:16640\n\t"
        "v_mfma_f32_16x16x32_bf16 v[20:23], a[240:243], v[60:63], v[20:23]\n\t"
        "s_waitcnt lgkmcnt(5)\n\t"
        "ds_read_b64_tr_b16 v[148:149], v103 offset:20480\n\t"
        "v_mfma_f32_16x16x32_bf16 v[24:27], a[244:247], v[60:63], v[24:27]\n\t"
        "ds_read_b64_tr_b16 v[150:151], v103 offset:20992\n\t"
        "ds_read_b64_tr_b16 v[152:153], v103 offset:20488\n\t"
        "v_mfma_f32_16x16x32_bf16 v[28:31], v[60:63], a[248:251], v[28:31]\n\t"
        "ds_read_b64_tr_b16 v[154:155], v103 offset:21000\n\t"
        "ds_read_b64_tr_b16 v[156:157], v104 offset:18432\n\t"
        "v_mfma_f32_16x16x32_bf16 v[8:11], a[252:255], v[64:67], v[8:11]\n\t"
        "ds_read_b64_tr_b16 v[158:159], v104 offset:18688\n\t"
        "ds_read_b128 v[72:75], v107 offset:24576\n\t"
        "v_mfma_f32_16x16x32_bf16 v[12:15], v[128:131], v[64:67], v[12:15]\n\t"
        "ds_read_b128 v[76:79], v107 offset:26624\n\t"
        "ds_read_b128 v[80:83], v108 offset:24576\n\t"
        "v_mfma_f32_16x16x32_bf16 v[16:19], v[64:67], v[132:135], v[16:19]\n\t"
        "ds_read_b128 v[84:87], v108 offset:26624\n\t"
        "v_mfma_f32_16x16x32_bf16 v[20:23], a[252:255], v[68:71], v[20:23]\n\t"
        "v_mfma_f32_16x16x32_bf16 v[24:27], v[128:131], v[68:71], v[24:27]\n\t"
        "v_mfma_f32_16x16x32_bf16 v[28:31], v[68:71], v[132:135], v[28:31]\n\t"
        "s_mov_b32 %[t0], 7864320\n\t"
        "buffer_load_dwordx4 v[244:247], v101, %[sq], %[t0] offen\n\t"
        "buffer_load_dwordx4 v[252:255], v112, %[sv], %[t0] offen\n\t"
        "s_mov_b32 %[t0], 8126464\n\t"
        "buffer_load_dwordx4 v[248:251], v101, %[sq], %[t0] offen\n\t"
        "s_waitcnt vmcnt(9)\n\t"
        "ds_write_b128 v102, v[208:211] offset:0\n\t"
        "ds_write_b128 v102, v[212:215] offset:4096\n\t"
        "ds_write_b128 v116, v[216:219] offset:8192\n\t"
        "s_waitcnt lgkmcnt(0)\n\t"
        "s_barrier\n\t"
        "v_mfma_f32_16x16x32_bf16 v[8:11], v[136:139], v[72:75], v[8:11]\n\t"
        "ds_read_b64_tr_b16 v[160:161], v103 offset:0\n\t"
        "ds_read_b64_tr_b16 v[162:163], v103 offset:512\n\t"
        "v_mfma_f32_16x16x32_bf16 v[12:15], v[140:143], v[72:75], v[12:15]\n\t"
        "ds_read_b64_tr_b16 v[164:165], v103 offset:8\n\t"
        "ds_read_b64_tr_b16 v[166:167], v103 offset:520\n\t"
        "v_mfma_f32_16x16x32_bf16 v[16:19], v[72:75], v[144:147], v[16:19]\n\t"
        "ds_read_b64_tr_b16 v[168:169], v104 offset:0\n\t"
        "ds_read_b64_tr_b16 v[170:171], v104 offset:256\n\t"
        "v_mfma_f32_16x16x32_bf16 v[20:23], v[136:139], v[76:79], v[20:23]\n\t"
        "s_waitcnt lgkmcnt(5)\n\t"
        "ds_read_b64_tr_b16 v[172:173], v103 offset:4096\n\t"
        "v_mfma_f32_16x16x32_bf16 v[24:27], v[140:143], v[76:79], v[24:27]\n\t"
        "ds_read_b64_tr_b16 v[174:175], v103 offset:4608\n\t"
        "ds_read_b64_tr_b16 v[176:177], v103 offset:4104\n\t"
        "v_mfma_f32_16x16x32_bf16 v[28:31], v[76:79], v[144:147], v[28:31]\n\t"
        "ds_read_b64_tr_b16 v[178:179], v103 offset:4616\n\t"
        "ds_read_b64_tr_b16 v[180:181], v104 offset:2048\n\t"
        "v_mfma_f32_16x16x32_bf16 v[8:11], v[148:151], v[80:83], v[8:11]\n\t"
        "ds_read_b64_tr_b16 v[182:183], v104 offset:2304\n\t"
        "ds_read_b128 v[56:59], v107 offset:32768\n\t"
        "v_mfma_f32_16x16x32_bf16 v[12:15], v[152:155], v[80:83], v[12:15]\n\t"
        "ds_read_b128 v[60:63], v107 offset:34816\n\t"
        "ds_read_b128 v[64:67], v108 offset:32768\n\t"
        "v_mfma_f32_16x16x32_bf16 v[16:19], v[80:83], v[156:159], v[16:19]\n\t"
        "ds_read_b128 v[68:71], v108 offset:34816\n\t"
        "v_mfma_f32_16x16x32_bf16 v[20:23], v[148:151], v[84:87], v[20:23]\n\t"
        "v_mfma_f32_16x16x32_bf16 v[24:27], v[152:155], v[84:87], v[24:27]\n\t"
        "v_mfma_f32_16x16x32_bf16 v[28:31], v[84:87], v[156:159], v[28:31]\n\t"
        "s_mov_b32 %[xo], 0x20000\n\t"
        "s_add_u32 m0, %[ld], 0\n\t"
        "s_add_u32 %[t0], %[xo], 0\n\t"
        "buffer_load_dwordx4 v100, %[sx], %[t0] offen lds\n\t"
        "s_add_u32 m0, %[ld], 1024\n\t"
        "s_add_u32 %[t0], %[xo], 16384\n\t"
        "buffer_load_dwordx4 v100, %[sx], %[t0] offen lds\n\t"
        "s_add_u32 m0, %[ld], 8192\n\t"
        "s_add_u32 %[t0], %[xo], 128\n\t"
        "buffer_load_dwordx4 v100, %[sx], %[t0] offen lds\n\t"
        "s_add_u32 m0, %[ld], 9216\n\t"
        "s_add_u32 %[t0], %[xo], 16512\n\t"
        "buffer_load_dwordx4 v100, %[sx], %[t0] offen lds\n\t"
        "s_add_u32 m0, %[ld], 16384\n\t"
        "s_add_u32 %[t0], %[xo], 256\n\t"
        "buffer_load_dwordx4 v100, %[sx], %[t0] offen lds\n\t"
        "s_add_u32 m0, %[ld], 17408\n\t"
        "s_add_u32 %[t0], %[xo], 16640\n\t"
        "buffer_load_dwordx4 v100, %[sx], %[t0] offen lds\n\t"
        "s_add_u32 m0, %[ld], 24576\n\t"
        "s_add_u32 %[t0], %[xo], 384\n\t"
        "buffer_load_dwordx4 v100, %[sx], %[t0] offen lds\n\t"
        "s_add_u32 m0, %[ld], 25600\n\t"
        "s_add_u32 %[t0], %[xo], 16768\n\t"
        "buffer_load_dwordx4 v100, %[sx], %[t0] offen lds\n\t"
        "s_mov_b32 %[xo], 0\n\t"
        "s_waitcnt vmcnt(14)\n\t"
        "ds_write_b128 v102, v[220:223] offset:16384\n\t"
        "ds_write_b128 v102, v[224:227] offset:20480\n\t"
        "ds_write_b128 v116, v[228:231] offset:24576\n\t"
        "s_waitcnt lgkmcnt(0)\n\t"
        "s_barrier\n\t"
        "v_mfma_f32_16x16x32_bf16 v[8:11], v[160:163], v[56:59], v[8:11]\n\t"
        "ds_read_b64_tr_b16 v[184:185], v103 offset:16384\n\t"
        "ds_read_b64_tr_b16 v[186:187], v103 offset:16896\n\t"
        "v_mfma_f32_16x16x32_bf16 v[12:15], v[164:167], v[56:59], v[12:15]\n\t"
        "ds_read_b64_tr_b16 v[188:189], v103 offset:16392\n\t"
        "ds_read_b64_tr_b16 v[190:191], v103 offset:16904\n\t"
        "v_mfma_f32_16x16x32_bf16 v[16:19], v[56:59], v[168:171], v[16:19]\n\t"
        "ds_read_b64_tr_b16 v[192:193], v104 offset:16384\n\t"
        "ds_read_b64_tr_b16 v[194:195], v104 offset:16640\n\t"
        "v_mfma_f32_16x16x32_bf16 v[20:23], v[160:163], v[60:63], v[20:23]\n\t"
        "s_waitcnt lgkmcnt(5)\n\t"
        "ds_read_b64_tr_b16 v[196:197], v103 offset:20480\n\t"
        "v_mfma_f32_16x16x32_bf16 v[24:27], v[164:167], v[60:63], v[24:27]\n\t"
        "ds_read_b64_tr_b16 v[198:199], v103 offset:20992\n\t"
        "ds_read_b64_tr_b16 v[200:201], v103 offset:20488\n\t"
        "v_mfma_f32_16x16x32_bf16 v[28:31], v[60:63], v[168:171], v[28:31]\n\t"
        "ds_read_b64_tr_b16 v[202:203], v103 offset:21000\n\t"
        "ds_read_b64_tr_b16 v[204:205], v104 offset:18432\n\t"
        "v_mfma_f32_16x16x32_bf16 v[8:11], v[172:175], v[64:67], v[8:11]\n\t"
        "ds_read_b64_tr_b16 v[206:207], v104 offset:18688\n\t"
        "ds_read_b128 v[72:75], v107 offset:40960\n\t"
        "v_mfma_f32_16x16x32_bf16 v[12:15], v[176:179], v[64:67], v[12:15]\n\t"
        "ds_read_b128 v[76:79], v107 offset:43008\n\t"
        "ds_read_b128 v[80:83], v108 offset:40960\n\t"
        "v_mfma_f32_16x16x32_bf16 v[16:19], v[64:67], v[180:183], v[16:19]\n\t"
        "ds_read_b128 v[84:87], v108 offset:43008\n\t"
        "v_mfma_f32_16x16x32_bf16 v[20:23], v[172:175], v[68:71], v[20:23]\n\t"
        "v_mfma_f32_16x16x32_bf16 v[24:27], v[176:179], v[68:71], v[24:27]\n\t"
        "v_mfma_f32_16x16x32_bf16 v[28:31], v[68:71], v[180:183], v[28:31]\n\t"
        "s_waitcnt vmcnt(11)\n\t"
        "ds_write_b128 v102, v[232:235] offset:0\n\t"
        "ds_write_b128 v102, v[236:239] offset:4096\n\t"
        "ds_write_b128 v116, v[240:243] offset:8192\n\t"
        "s_waitcnt lgkmcnt(0)\n\t"
        "s_barrier\n\t"
        "v_mfma_f32_16x16x32_bf16 v[8:11], v[184:187], v[72:75], v[8:11]\n\t"
        "ds_read_b64_tr_b16 v[208:209], v103 offset:0\n\t"
        "ds_read_b64_tr_b16 v[210:211], v103 offset:512\n\t"
        "v_mfma_f32_16x16x32_bf16 v[12:15], v[188:191], v[72:75], v[12:15]\n\t"
        "ds_read_b64_tr_b16 v[212:213], v103 offset:8\n\t"
        "ds_read_b64_tr_b16 v[214:215], v103 offset:520\n\t"
        "v_mfma_f32_16x16x32_bf16 v[16:19], v[72:75], v[192:195], v[16:19]\n\t"
        "ds_read_b64_tr_b16 v[216:217], v104 offset:0\n\t"
        "ds_read_b64_tr_b16 v[218:219], v104 offset:256\n\t"
        "v_mfma_f32_16x16x32_bf16 v[20:23], v[184:187], v[76:79], v[20:23]\n\t"
        "s_waitcnt lgkmcnt(5)\n\t"
        "ds_read_b64_tr_b16 v[220:221], v103 offset:4096\n\t"
        "v_mfma_f32_16x16x32_bf16 v[24:27], v[188:191], v[76:79], v[24:27]\n\t"
        "ds_read_b64_tr_b16 v[222:223], v103 offset:4608\n\t"
        "ds_read_b64_tr_b16 v[224:225], v103 offset:4104\n\t"
        "v_mfma_f32_16x16x32_bf16 v[28:31], v[76:79], v[192:195], v[28:31]\n\t"
        "ds_read_b64_tr_b16 v[226:227], v103 offset:4616\n\t"
        "ds_read_b64_tr_b16 v[228:229], v104 offset:2048\n\t"
        "v_mfma_f32_16x16x32_bf16 v[8:11], v[196:199], v[80:83], v[8:11]\n\t"
        "ds_read_b64_tr_b16 v[230:231], v104 offset:2304\n\t"
        "ds_read_b128 v[56:59], v107 offset:49152\n\t"
        "v_mfma_f32_16x16x32_bf16 v[12:15], v[200:203], v[80:83], v[12:15]\n\t"
        "ds_read_b128 v[60:63], v107 offset:51200\n\t"
        "ds_read_b128 v[64:67], v108 offset:49152\n\t"
        "v_mfma_f32_16x16x32_bf16 v[16:19], v[80:83], v[204:207], v[16:19]\n\t"
        "ds_read_b128 v[68:71], v108 offset:51200\n\t"
        "v_mfma_f32_16x16x32_bf16 v[20:23], v[196:199], v[84:87], v[20:23]\n\t"
        "v_mfma_f32_16x16x32_bf16 v[24:27], v[200:203], v[84:87], v[24:27]\n\t"
        "v_mfma_f32_16x16x32_bf16 v[28:31], v[84:87], v[204:207], v[28:31]\n\t"
        "s_waitcnt vmcnt(8)\n\t"
        "ds_write_b128 v102, v[244:247] offset:16384\n\t"
        "ds_write_b128 v102, v[248:251] offset:20480\n\t"
        "ds_write_b128 v116, v[252:255] offset:24576\n\t"
        "s_waitcnt lgkmcnt(0)\n\t"
        "s_barrier\n\t"
        "v_mfma_f32_16x16x32_bf16 v[8:11], v[208:211], v[56:59], v[8:11]\n\t"
        "ds_read_b64_tr_b16 v[232:233], v103 offset:16384\n\t"
        "ds_read_b64_tr_b16 v[234:235], v103 offset:16896\n\t"
        "v_mfma_f32_16x16x32_bf16 v[12:15], v[212:215], v[56:59], v[12:15]\n\t"
        "ds_read_b64_tr_b16 v[236:237], v103 offset:16392\n\t"
        "ds_read_b64_tr_b16 v[238:239], v103 offset:16904\n\t"
        "v_mfma_f32_16x16x32_bf16 v[16:19], v[56:59], v[216:219], v[16:19]\n\t"
        "ds_read_b64_tr_b16 v[240:241], v104 offset:16384\n\t"
        "ds_read_b64_tr_b16 v[242:243], v104 offset:16640\n\t"
        "v_mfma_f32_16x16x32_bf16 v[20:23], v[208:211], v[60:63], v[20:23]\n\t"
        "s_waitcnt lgkmcnt(5)\n\t"
        "ds_read_b64_tr_b16 v[244:245], v103 offset:20480\n\t"
        "v_mfma_f32_16x16x32_bf16 v[24:27], v[212:215], v[60:63], v[24:27]\n\t"
        "ds_read_b64_tr_b16 v[246:247], v103 offset:20992\n\t"
        "ds_read_b64_tr_b16 v[248:249], v103 offset:20488\n\t"
        "v_mfma_f32_16x16x32_bf16 v[28:31], v[60:63], v[216:219], v[28:31]\n\t"
        "ds_read_b64_tr_b16 v[250:251], v103 offset:21000\n\t"
        "ds_read_b64_tr_b16 v[252:253], v104 offset:18432\n\t"
        "v_mfma_f32_16x16x32_bf16 v[8:11], v[220:223], v[64:67], v[8:11]\n\t"
        "ds_read_b64_tr_b16 v[254:255], v104 offset:18688\n\t"
        "ds_read_b128 v[72:75], v107 offset:57344\n\t"
        "v_mfma_f32_16x16x32_bf16 v[12:15], v[224:227], v[64:67], v[12:15]\n\t"
        "ds_read_b128 v[76:79], v107 offset:59392\n\t"
        "ds_read_b128 v[80:83], v108 offset:57344\n\t"
        "v_mfma_f32_16x16x32_bf16 v[16:19], v[64:67], v[228:231], v[16:19]\n\t"
        "ds_read_b128 v[84:87], v108 offset:59392\n\t"
        "v_mfma_f32_16x16x32_bf16 v[20:23], v[220:223], v[68:71], v[20:23]\n\t"
        "v_mfma_f32_16x16x32_bf16 v[24:27], v[224:227], v[68:71], v[24:27]\n\t"
        "v_mfma_f32_16x16x32_bf16 v[28:31], v[68:71], v[228:231], v[28:31]\n\t"
        "s_waitcnt vmcnt(0) lgkmcnt(0)\n\t"
        "s_barrier\n\t"
        "v_mfma_f32_16x16x32_bf16 v[8:11], v[232:235], v[72:75], v[8:11]\n\t"
        "v_mfma_f32_16x16x32_bf16 v[12:15], v[236:239], v[72:75], v[12:15]\n\t"
        "v_mfma_f32_16x16x32_bf16 v[16:19], v[72:75], v[240:243], v[16:19]\n\t"
        "v_mfma_f32_16x16x32_bf16 v[20:23], v[232:235], v[76:79], v[20:23]\n\t"
        "v_mfma_f32_16x16x32_bf16 v[24:27], v[236:239], v[76:79], v[24:27]\n\t"
        "v_mfma_f32_16x16x32_bf16 v[28:31], v[76:79], v[240:243], v[28:31]\n\t"
        "v_mfma_f32_16x16x32_bf16 v[8:11], v[244:247], v[80:83], v[8:11]\n\t"
        "v_mfma_f32_16x16x32_bf16 v[12:15], v[248:251], v[80:83], v[12:15]\n\t"
        "v_mfma_f32_16x16x32_bf16 v[16:19], v[80:83], v[252:255], v[16:19]\n\t"
        "v_mfma_f32_16x16x32_bf16 v[20:23], v[244:247], v[84:87], v[20:23]\n\t"
        "v_mfma_f32_16x16x32_bf16 v[24:27], v[248:251], v[84:87], v[24:27]\n\t"
        "v_mfma_f32_16x16x32_bf16 v[28:31], v[84:87], v[252:255], v[28:31]\n\t"
        "s_mov_b32 %[xo], 0x20000\n\t"
        "ds_read_b128 v[56:59], v105 offset:0\n\t"
        "ds_read_b128 v[60:63], v105 offset:2048\n\t"
        "ds_read_b128 v[64:67], v106 offset:0\n\t"
        "ds_read_b128 v[68:71], v106 offset:2048\n\t"
        "ds_read_b128 v[72:75], v105 offset:8192\n\t"
        "ds_read_b128 v[76:79], v105 offset:10240\n\t"
        "ds_read_b128 v[80:83], v106 offset:8192\n\t"
        "v_cvt_pk_bf16_f32 v88, v8, v9\n\t"
        "v_cvt_pk_bf16_f32 v89, v10, v11\n\t"
        "v_cvt_pk_bf16_f32 v90, v12, v13\n\t"
        "v_cvt_pk_bf16_f32 v91, v14, v15\n\t"
        "buffer_store_dwordx4 v[88:91], v109, %[sw], %[so] offen sc1\n\t"
        "v_cvt_pk_bf16_f32 v92, v20, v21\n\t"
        "v_cvt_pk_bf16_f32 v93, v22, v23\n\t"
        "v_cvt_pk_bf16_f32 v94, v24, v25\n\t"
        "v_cvt_pk_bf16_f32 v95, v26, v27\n\t"
        "buffer_store_dwordx4 v[92:95], v110, %[sw], %[so] offen sc1\n\t"
        "v_cvt_pk_bf16_f32 v96, v16, v17\n\t"
        "v_cvt_pk_bf16_f32 v97, v18, v19\n\t"
        "v_cvt_pk_bf16_f32 v98, v28, v29\n\t"
        "v_cvt_pk_bf16_f32 v99, v30, v31\n\t"
        "buffer_store_dwordx4 v[96:99], v111, %[sw], %[so] offen sc1\n\t"
        "s_mov_b32 %[so], 0xf0000000\n\t"
        "s_waitcnt vmcnt(0)\n\t"
        "v_mov_b32_e32 v117, %[cn]\n\t"
        "buffer_store_dword v117, v113, %[sw], 0 offen sc1\n\t"
        "s_add_u32 m0, %[ld], 32768\n\t"
        "s_add_u32 %[t0], %[xo], 512\n\t"
        "buffer_load_dwordx4 v100, %[sx], %[t0] offen lds\n\t"
        "s_add_u32 m0, %[ld], 33792\n\t"
        "s_add_u32 %[t0], %[xo], 16896\n\t"
        "buffer_load_dwordx4 v100, %[sx], %[t0] offen lds\n\t"
        "s_add_u32 m0, %[ld], 40960\n\t"
        "s_add_u32 %[t0], %[xo], 640\n\t"
        "buffer_load_dwordx4 v100, %[sx], %[t0] offen lds\n\t"
        "s_add_u32 m0, %[ld], 41984\n\t"
        "s_add_u32 %[t0], %[xo], 17024\n\t"
        "buffer_load_dwordx4 v100, %[sx], %[t0] offen lds\n\t"
        "s_add_u32 m0, %[ld], 49152\n\t"
        "s_add_u32 %[t0], %[xo], 768\n\t"
        "buffer_load_dwordx4 v100, %[sx], %[t0] offen lds\n\t"
        "s_add_u32 m0, %[ld], 50176\n\t"
        "s_add_u32 %[t0], %[xo], 17152\n\t"
        "buffer_load_dwordx4 v100, %[sx], %[t0] offen lds\n\t"
        "s_add_u32 m0, %[ld], 57344\n\t"
        "s_add_u32 %[t0], %[xo], 896\n\t"
        "buffer_load_dwordx4 v100, %[sx], %[t0] offen lds\n\t"
        "s_add_u32 m0, %[ld], 58368\n\t"
        "s_add_u32 %[t0], %[xo], 17280\n\t"
        "buffer_load_dwordx4 v100, %[sx], %[t0] offen lds\n\t"
        "s_add_u32 m0, %[ld], 65536\n\t"
        "s_add_u32 %[t0], %[xo], 1024\n\t"
        "buffer_load_dwordx4 v100, %[sx], %[t0] offen lds\n\t"
        "s_add_u32 m0, %[ld], 66560\n\t"
        "s_add_u32 %[t0], %[xo], 17408\n\t"
        "buffer_load_dwordx4 v100, %[sx], %[t0] offen lds\n\t"
        "s_add_u32 m0, %[ld], 73728\n\t"
        "s_add_u32 %[t0], %[xo], 1152\n\t"
        "buffer_load_dwordx4 v100, %[sx], %[t0] offen lds\n\t"
        "s_add_u32 m0, %[ld], 74752\n\t"
        "s_add_u32 %[t0], %[xo], 17536\n\t"
        "buffer_load_dwordx4 v100, %[sx], %[t0] offen lds\n\t"
        "s_add_u32 m0, %[ld], 81920\n\t"
        "s_add_u32 %[t0], %[xo], 1280\n\t"
        "buffer_load_dwordx4 v100, %[sx], %[t0] offen lds\n\t"
        "s_add_u32 m0, %[ld], 82944\n\t"
        "s_add_u32 %[t0], %[xo], 17664\n\t"
        "buffer_load_dwordx4 v100, %[sx], %[t0] offen lds\n\t"
        "s_add_u32 m0, %[ld], 90112\n\t"
        "s_add_u32 %[t0], %[xo], 1408\n\t"
        "buffer_load_dwordx4 v100, %[sx], %[t0] offen lds\n\t"
        "s_add_u32 m0, %[ld], 91136\n\t"
        "s_add_u32 %[t0], %[xo], 17792\n\t"
        "buffer_load_dwordx4 v100, %[sx], %[t0] offen lds\n\t"
        "s_add_u32 m0, %[ld], 98304\n\t"
        "s_add_u32 %[t0], %[xo], 1536\n\t"
        "buffer_load_dwordx4 v100, %[sx], %[t0] offen lds\n\t"
        "s_add_u32 m0, %[ld], 99328\n\t"
        "s_add_u32 %[t0], %[xo], 17920\n\t"
        "buffer_load_dwordx4 v100, %[sx], %[t0] offen lds\n\t"
        "s_add_u32 m0, %[ld], 106496\n\t"
        "s_add_u32 %[t0], %[xo], 1664\n\t"
        "buffer_load_dwordx4 v100, %[sx], %[t0] offen lds\n\t"
        "s_branch LG_mid%=\n\t"
        "LG_loop%=:\n\t"
        "s_waitcnt lgkmcnt(6)\n\t"
        "v_mfma_f32_16x16x32_bf16 v[8:11], a[0:3], v[56:59], 0\n\t"
        "v_mfma_f32_16x16x32_bf16 v[12:15], a[4:7], v[56:59], 0\n\t"
        "ds_read_b128 v[84:87], v106 offset:10240\n\t"
        "v_mfma_f32_16x16x32_bf16 v[16:19], v[56:59], a[8:11], 0\n\t"
        "s_add_u32 m0, %[ld], 107520\n\t"
        "s_add_u32 %[t0], %[xo], 18048\n\t"
        "s_waitcnt lgkmcnt(6)\n\t"
        "v_mfma_f32_16x16x32_bf16 v[20:23], a[0:3], v[60:63], 0\n\t"
        "buffer_load_dwordx4 v100, %[sx], %[t0] offen lds\n\t"
        "v_mfma_f32_16x16x32_bf16 v[24:27], a[4:7], v[60:63], 0\n\t"
        "ds_read_b128 v[56:59], v105 offset:16384\n\t"
        "v_mfma_f32_16x16x32_bf16 v[28:31], v[60:63], a[8:11], 0\n\t"
        "s_waitcnt lgkmcnt(6)\n\t"
        "v_mfma_f32_16x16x32_bf16 v[8:11], a[12:15], v[64:67], v[8:11]\n\t"
        "v_cvt_pk_bf16_f32 v88, v32, v33\n\t"
        "v_cvt_pk_bf16_f32 v89, v34, v35\n\t"
        "v_mfma_f32_16x16x32_bf16 v[12:15], a[16:19], v[64:67], v[12:15]\n\t"
        "ds_read_b128 v[60:63], v105 offset:18432\n\t"
        "v_cvt_pk_bf16_f32 v90, v36, v37\n\t"
        "v_mfma_f32_16x16x32_bf16 v[16:19], v[64:67], a[20:23], v[16:19]\n\t"
        "s_add_u32 m0, %[ld], 114688\n\t"
        "s_add_u32 %[t0], %[xo], 1792\n\t"
        "s_waitcnt lgkmcnt(6)\n\t"
        "v_mfma_f32_16x16x32_bf16 v[20:23], a[12:15], v[68:71], v[20:23]\n\t"
        "buffer_load_dwordx4 v100, %[sx], %[t0] offen lds\n\t"
        "v_mfma_f32_16x16x32_bf16 v[24:27], a[16:19], v[68:71], v[24:27]\n\t"
        "ds_read_b128 v[64:67], v106 offset:16384\n\t"
        "v_cvt_pk_bf16_f32 v91, v38, v39\n\t"
        "v_mfma_f32_16x16x32_bf16 v[28:31], v[68:71], a[20:23], v[28:31]\n\t"
        "buffer_store_dwordx4 v[88:91], v109, %[sw], %[so] offen sc1\n\t"
        "s_waitcnt lgkmcnt(6)\n\t"
        "v_mfma_f32_16x16x32_bf16 v[8:11], a[24:27], v[72:75], v[8:11]\n\t"
        "v_cvt_pk_bf16_f32 v92, v44, v45\n\t"
        "v_cvt_pk_bf16_f32 v93, v46, v47\n\t"
        "v_mfma_f32_16x16x32_bf16 v[12:15], a[28:31], v[72:75], v[12:15]\n\t"
        "ds_read_b128 v[68:71], v106 offset:18432\n\t"
        "v_cvt_pk_bf16_f32 v94, v48, v49\n\t"
        "v_mfma_f32_16x16x32_bf16 v[16:19], v[72:75], a[32:35], v[16:19]\n\t"
        "s_add_u32 m0, %[ld], 115712\n\t"
        "s_add_u32 %[t0], %[xo], 18176\n\t"
        "s_waitcnt lgkmcnt(6)\n\t"
        "v_mfma_f32_16x16x32_bf16 v[20:23], a[24:27], v[76:79], v[20:23]\n\t"
        "buffer_load_dwordx4 v100, %[sx], %[t0] offen lds\n\t"
        "v_mfma_f32_16x16x32_bf16 v[24:27], a[28:31], v[76:79], v[24:27]\n\t"
        "ds_read_b128 v[72:75], v105 offset:24576\n\t"
        "v_cvt_pk_bf16_f32 v95, v50, v51\n\t"
        "v_mfma_f32_16x16x32_bf16 v[28:31], v[76:79], a[32:35], v[28:31]\n\t"
        "buffer_store_dwordx4 v[92:95], v110, %[sw], %[so] offen sc1\n\t"
        "s_waitcnt lgkmcnt(6)\n\t"
        "v_mfma_f32_16x16x32_bf16 v[8:11], a[36:39], v[80:83], v[8:11]\n\t"
        "v_cvt_pk_bf16_f32 v96, v40, v41\n\t"
        "v_cvt_pk_bf16_f32 v97, v42, v43\n\t"
        "v_mfma_f32_16x16x32_bf16 v[12:15], a[40:43], v[80:83], v[12:15]\n\t"
        "ds_read_b128 v[76:79], v105 offset:26624\n\t"
        "v_cvt_pk_bf16_f32 v98, v52, v53\n\t"
        "v_mfma_f32_16x16x32_bf16 v[16:19], v[80:83], a[44:47], v[16:19]\n\t"
        "s_add_u32 m0, %[ld], 122880\n\t"
        "s_add_u32 %[t0], %[xo], 1920\n\t"
        "s_waitcnt lgkmcnt(6)\n\t"
        "v_mfma_f32_16x16x32_bf16 v[20:23], a[36:39], v[84:87], v[20:23]\n\t"
        "buffer_load_dwordx4 v100, %[sx], %[t0] offen lds\n\t"
        "v_mfma_f32_16x16x32_bf16 v[24:27], a[40:43], v[84:87], v[24:27]\n\t"
        "ds_read_b128 v[80:83], v106 offset:24576\n\t"
        "v_cvt_pk_bf16_f32 v99, v54, v55\n\t"
        "v_mfma_f32_16x16x32_bf16 v[28:31], v[84:87], a[44:47], v[28:31]\n\t"
        "buffer_store_dwordx4 v[96:99], v111, %[sw], %[so] offen sc1\n\t"
        "s_waitcnt lgkmcnt(6)\n\t"
        "v_mfma_f32_16x16x32_bf16 v[8:11], a[48:51], v[56:59], v[8:11]\n\t"
        "s_lshl_b32 %[so], %[cn], 19\n\t"
        "v_mfma_f32_16x16x32_bf16 v[12:15], a[52:55], v[56:59], v[12:15]\n\t"
        "ds_read_b128 v[84:87], v106 offset:26624\n\t"
        "v_mfma_f32_16x16x32_bf16 v[16:19], v[56:59], a[56:59], v[16:19]\n\t"
        "s_add_u32 m0, %[ld], 123904\n\t"
        "s_add_u32 %[t0], %[xo], 18304\n\t"
        "s_waitcnt lgkmcnt(6)\n\t"
        "v_mfma_f32_16x16x32_bf16 v[20:23], a[48:51], v[60:63], v[20:23]\n\t"
        "buffer_load_dwordx4 v100, %[sx], %[t0] offen lds\n\t"
        "v_mfma_f32_16x16x32_bf16 v[24:27], a[52:55], v[60:63], v[24:27]\n\t"
        "v_mfma_f32_16x16x32_bf16 v[28:31], v[60:63], a[56:59], v[28:31]\n\t"
        "s_waitcnt lgkmcnt(5)\n\t"
        "v_mfma_f32_16x16x32_bf16 v[8:11], a[60:63], v[64:67], v[8:11]\n\t"
        "s_waitcnt vmcnt(19) lgkmcnt(0)\n\t"
        "s_barrier\n\t"
        "s_add_u32 %[xo], %[xo], 0x20000\n\t"
        "v_mfma_f32_16x16x32_bf16 v[12:15], a[64:67], v[64:67], v[12:15]\n\t"
        "ds_read_b128 v[56:59], v105 offset:32768\n\t"
        "v_mfma_f32_16x16x32_bf16 v[16:19], v[64:67], a[68:71], v[16:19]\n\t"
        "ds_read_b128 v[60:63], v105 offset:34816\n\t"
        "s_add_u32 m0, %[ld], 0\n\t"
        "s_add_u32 %[t0], %[xo], 0\n\t"
        "v_mfma_f32_16x16x32_bf16 v[20:23], a[60:63], v[68:71], v[20:23]\n\t"
        "buffer_load_dwordx4 v100, %[sx], %[t0] offen lds\n\t"
        "v_mfma_f32_16x16x32_bf16 v[24:27], a[64:67], v[68:71], v[24:27]\n\t"
        "ds_read_b128 v[64:67], v106 offset:32768\n\t"
        "v_mfma_f32_16x16x32_bf16 v[28:31], v[68:71], a[68:71], v[28:31]\n\t"
        "v_mfma_f32_16x16x32_bf16 v[8:11], a[72:75], v[72:75], v[8:11]\n\t"
        "v_mfma_f32_16x16x32_bf16 v[12:15], a[76:79], v[72:75], v[12:15]\n\t"
        "ds_read_b128 v[68:71], v106 offset:34816\n\t"
        "v_mfma_f32_16x16x32_bf16 v[16:19], v[72:75], a[80:83], v[16:19]\n\t"
        "s_add_u32 m0, %[ld], 1024\n\t"
        "s_add_u32 %[t0], %[xo], 16384\n\t"
        "v_mfma_f32_16x16x32_bf16 v[20:23], a[72:75], v[76:79], v[20:23]\n\t"
        "buffer_load_dwordx4 v100, %[sx], %[t0] offen lds\n\t"
        "v_mfma_f32_16x16x32_bf16 v[24:27], a[76:79], v[76:79], v[24:27]\n\t"
        "ds_read_b128 v[72:75], v105 offset:40960\n\t"
        "v_mfma_f32_16x16x32_bf16 v[28:31], v[76:79], a[80:83], v[28:31]\n\t"
        "v_mfma_f32_16x16x32_bf16 v[8:11], a[84:87], v[80:83], v[8:11]\n\t"
        "v_mfma_f32_16x16x32_bf16 v[12:15], a[88:91], v[80:83], v[12:15]\n\t"
        "ds_read_b128 v[76:79], v105 offset:43008\n\t"
        "v_mfma_f32_16x16x32_bf16 v[16:19], v[80:83], a[92:95], v[16:19]\n\t"
        "s_add_u32 m0, %[ld], 8192\n\t"
        "s_add_u32 %[t0], %[xo], 128\n\t"
        "v_mfma_f32_16x16x32_bf16 v[20:23], a[84:87], v[84:87], v[20:23]\n\t"
        "buffer_load_dwordx4 v100, %[sx], %[t0] offen lds\n\t"
        "v_mfma_f32_16x16x32_bf16 v[24:27], a[88:91], v[84:87], v[24:27]\n\t"
        "ds_read_b128 v[80:83], v106 offset:40960\n\t"
        "v_mfma_f32_16x16x32_bf16 v[28:31], v[84:87], a[92:95], v[28:31]\n\t"
        "s_waitcnt lgkmcnt(6)\n\t"
        "v_mfma_f32_16x16x32_bf16 v[8:11], a[96:99], v[56:59], v[8:11]\n\t"
        "v_mfma_f32_16x16x32_bf16 v[12:15], a[100:103], v[56:59], v[12:15]\n\t"
        "ds_read_b128 v[84:87], v106 offset:43008\n\t"
        "v_mfma_f32_16x16x32_bf16 v[16:19], v[56:59], a[104:107], v[16:19]\n\t"
        "s_add_u32 m0, %[ld], 9216\n\t"
        "s_add_u32 %[t0], %[xo], 16512\n\t"
        "s_waitcnt lgkmcnt(6)\n\t"
        "v_mfma_f32_16x16x32_bf16 v[20:23], a[96:99], v[60:63], v[20:23]\n\t"
        "buffer_load_dwordx4 v100, %[sx], %[t0] offen lds\n\t"
        "v_mfma_f32_16x16x32_bf16 v[24:27], a[100:103], v[60:63], v[24:27]\n\t"
        "ds_read_b128 v[56:59], v105 offset:49152\n\t"
        "v_mfma_f32_16x16x32_bf16 v[28:31], v[60:63], a[104:107], v[28:31]\n\t"
        "s_waitcnt lgkmcnt(6)\n\t"
        "v_mfma_f32_16x16x32_bf16 v[8:11], a[108:111], v[64:67], v[8:11]\n\t"
        "v_mfma_f32_16x16x32_bf16 v[12:15], a[112:115], v[64:67], v[12:15]\n\t"
        "ds_read_b128 v[60:63], v105 offset:51200\n\t"
        "v_mfma_f32_16x16x32_bf16 v[16:19], v[64:67], a[116:119], v[16:19]\n\t"
        "s_add_u32 m0, %[ld], 16384\n\t"
        "s_add_u32 %[t0], %[xo], 256\n\t"
        "s_waitcnt lgkmcnt(6)\n\t"
        "v_mfma_f32_16x16x32_bf16 v[20:23], a[108:111], v[68:71], v[20:23]\n\t"
        "buffer_load_dwordx4 v100, %[sx], %[t0] offen lds\n\t"
        "v_mfma_f32_16x16x32_bf16 v[24:27], a[112:115], v[68:71], v[24:27]\n\t"
        "ds_read_b128 v[64:67], v106 offset:49152\n\t"
        "v_mfma_f32_16x16x32_bf16 v[28:31], v[68:71], a[116:119], v[28:31]\n\t"
        "s_waitcnt lgkmcnt(6)\n\t"
        "v_mfma_f32_16x16x32_bf16 v[8:11], a[120:123], v[72:75], v[8:11]\n\t"
        "v_mfma_f32_16x16x32_bf16 v[12:15], a[124:127], v[72:75], v[12:15]\n\t"
        "ds_read_b128 v[68:71], v106 offset:51200\n\t"
        "v_mfma_f32_16x16x32_bf16 v[16:19], v[72:75], a[128:131], v[16:19]\n\t"
        "s_add_u32 m0, %[ld], 17408\n\t"
        "s_add_u32 %[t0], %[xo], 16640\n\t"
        "s_waitcnt lgkmcnt(6)\n\t"
        "v_mfma_f32_16x16x32_bf16 v[20:23], a[120:123], v[76:79], v[20:23]\n\t"
        "buffer_load_dwordx4 v100, %[sx], %[t0] offen lds\n\t"
        "v_mfma_f32_16x16x32_bf16 v[24:27], a[124:127], v[76:79], v[24:27]\n\t"
        "ds_read_b128 v[72:75], v105 offset:57344\n\t"
        "v_mfma_f32_16x16x32_bf16 v[28:31], v[76:79], a[128:131], v[28:31]\n\t"
        "s_waitcnt lgkmcnt(6)\n\t"
        "v_mfma_f32_16x16x32_bf16 v[8:11], a[132:135], v[80:83], v[8:11]\n\t"
        "v_mfma_f32_16x16x32_bf16 v[12:15], a[136:139], v[80:83], v[12:15]\n\t"
        "ds_read_b128 v[76:79], v105 offset:59392\n\t"
        "v_mfma_f32_16x16x32_bf16 v[16:19], v[80:83], a[140:143], v[16:19]\n\t"
        "s_add_u32 m0, %[ld], 24576\n\t"
        "s_add_u32 %[t0], %[xo], 384\n\t"
        "s_waitcnt lgkmcnt(6)\n\t"
        "v_mfma_f32_16x16x32_bf16 v[20:23], a[132:135], v[84:87], v[20:23]\n\t"
        "buffer_load_dwordx4 v100, %[sx], %[t0] offen lds\n\t"
        "v_mfma_f32_16x16x32_bf16 v[24:27], a[136:139], v[84:87], v[24:27]\n\t"
        "ds_read_b128 v[80:83], v106 offset:57344\n\t"
        "v_mfma_f32_16x16x32_bf16 v[28:31], v[84:87], a[140:143], v[28:31]\n\t"
        "s_waitcnt lgkmcnt(6)\n\t"
        "v_mfma_f32_16x16x32_bf16 v[8:11], a[144:147], v[56:59], v[8:11]\n\t"
        "v_mfma_f32_16x16x32_bf16 v[12:15], a[148:151], v[56:59], v[12:15]\n\t"
        "ds_read_b128 v[84:87], v106 offset:59392\n\t"
        "v_mfma_f32_16x16x32_bf16 v[16:19], v[56:59], a[152:155], v[16:19]\n\t"
        "s_add_u32 m0, %[ld], 25600\n\t"
        "s_add_u32 %[t0], %[xo], 16768\n\t"
        "s_waitcnt lgkmcnt(6)\n\t"
        "v_mfma_f32_16x16x32_bf16 v[20:23], a[144:147], v[60:63], v[20:23]\n\t"
        "buffer_load_dwordx4 v100, %[sx], %[t0] offen lds\n\t"
        "v_mfma_f32_16x16x32_bf16 v[24:27], a[148:151], v[60:63], v[24:27]\n\t"
        "v_mfma_f32_16x16x32_bf16 v[28:31], v[60:63], a[152:155], v[28:31]\n\t"
        "s_waitcnt lgkmcnt(5)\n\t"
        "v_mfma_f32_16x16x32_bf16 v[8:11], a[156:159], v[64:67], v[8:11]\n\t"
        "s_waitcnt vmcnt(9) lgkmcnt(0)\n\t"
        "s_barrier\n\t"
        "v_mfma_f32_16x16x32_bf16 v[12:15], a[160:163], v[64:67], v[12:15]\n\t"
        "ds_read_b128 v[56:59], v107 offset:0\n\t"
        "v_mfma_f32_16x16x32_bf16 v[16:19], v[64:67], a[164:167], v[16:19]\n\t"
        "ds_read_b128 v[60:63], v107 offset:2048\n\t"
        "s_add_u32 m0, %[ld], 32768\n\t"
        "s_add_u32 %[t0], %[xo], 512\n\t"
        "v_mfma_f32_16x16x32_bf16 v[20:23], a[156:159], v[68:71], v[20:23]\n\t"
        "buffer_load_dwordx4 v100, %[sx], %[t0] offen lds\n\t"
        "v_mfma_f32_16x16x32_bf16 v[24:27], a[160:163], v[68:71], v[24:27]\n\t"
        "ds_read_b128 v[64:67], v108 offset:0\n\t"
        "v_mfma_f32_16x16x32_bf16 v[28:31], v[68:71], a[164:167], v[28:31]\n\t"
        "v_mfma_f32_16x16x32_bf16 v[8:11], a[168:171], v[72:75], v[8:11]\n\t"
        "v_mfma_f32_16x16x32_bf16 v[12:15], a[172:175], v[72:75], v[12:15]\n\t"
        "ds_read_b128 v[68:71], v108 offset:2048\n\t"
        "v_mov_b32_e32 v117, %[cn]\n\t"
        "buffer_store_dword v117, v113, %[sw], 0 offen sc1\n\t"
        "s_add_u32 %[cn], %[cn], 1\n\t"
        "v_mfma_f32_16x16x32_bf16 v[16:19], v[72:75], a[176:179], v[16:19]\n\t"
        "s_add_u32 m0, %[ld], 33792\n\t"
        "s_add_u32 %[t0], %[xo], 16896\n\t"
        "v_mfma_f32_16x16x32_bf16 v[20:23], a[168:171], v[76:79], v[20:23]\n\t"
        "buffer_load_dwordx4 v100, %[sx], %[t0] offen lds\n\t"
        "v_mfma_f32_16x16x32_bf16 v[24:27], a[172:175], v[76:79], v[24:27]\n\t"
        "ds_read_b128 v[72:75], v107 offset:8192\n\t"
        "v_mfma_f32_16x16x32_bf16 v[28:31], v[76:79], a[176:179], v[28:31]\n\t"
        "v_mfma_f32_16x16x32_bf16 v[8:11], a[180:183], v[80:83], v[8:11]\n\t"
        "v_mfma_f32_16x16x32_bf16 v[12:15], a[184:187], v[80:83], v[12:15]\n\t"
        "ds_read_b128 v[76:79], v107 offset:10240\n\t"
        "v_mfma_f32_16x16x32_bf16 v[16:19], v[80:83], a[188:191], v[16:19]\n\t"
        "s_add_u32 m0, %[ld], 40960\n\t"
        "s_add_u32 %[t0], %[xo], 640\n\t"
        "v_mfma_f32_16x16x32_bf16 v[20:23], a[180:183], v[84:87], v[20:23]\n\t"
        "buffer_load_dwordx4 v100, %[sx], %[t0] offen lds\n\t"
        "v_mfma_f32_16x16x32_bf16 v[24:27], a[184:187], v[84:87], v[24:27]\n\t"
        "ds_read_b128 v[80:83], v108 offset:8192\n\t"
        "v_mfma_f32_16x16x32_bf16 v[28:31], v[84:87], a[188:191], v[28:31]\n\t"
        "s_waitcnt lgkmcnt(6)\n\t"
        "v_mfma_f32_16x16x32_bf16 v[8:11], a[192:195], v[56:59], v[8:11]\n\t"
        "v_mfma_f32_16x16x32_bf16 v[12:15], a[196:199], v[56:59], v[12:15]\n\t"
        "ds_read_b128 v[84:87], v108 offset:10240\n\t"
        "v_mfma_f32_16x16x32_bf16 v[16:19], v[56:59], a[200:203], v[16:19]\n\t"
        "s_add_u32 m0, %[ld], 41984\n\t"
        "s_add_u32 %[t0], %[xo], 17024\n\t"
        "s_waitcnt lgkmcnt(6)\n\t"
        "v_mfma_f32_16x16x32_bf16 v[20:23], a[192:195], v[60:63], v[20:23]\n\t"
        "buffer_load_dwordx4 v100, %[sx], %[t0] offen lds\n\t"
        "v_mfma_f32_16x16x32_bf16 v[24:27], a[196:199], v[60:63], v[24:27]\n\t"
        "ds_read_b128 v[56:59], v107 offset:16384\n\t"
        "v_mfma_f32_16x16x32_bf16 v[28:31], v[60:63], a[200:203], v[28:31]\n\t"
        "s_waitcnt lgkmcnt(6)\n\t"
        "v_mfma_f32_16x16x32_bf16 v[8:11], a[204:207], v[64:67], v[8:11]\n\t"
        "v_mfma_f32_16x16x32_bf16 v[12:15], a[208:211], v[64:67], v[12:15]\n\t"
        "ds_read_b128 v[60:63], v107 offset:18432\n\t"
        "v_mfma_f32_16x16x32_bf16 v[16:19], v[64:67], a[212:215], v[16:19]\n\t"
        "s_add_u32 m0, %[ld], 49152\n\t"
        "s_add_u32 %[t0], %[xo], 768\n\t"
        "s_waitcnt lgkmcnt(6)\n\t"
        "v_mfma_f32_16x16x32_bf16 v[20:23], a[204:207], v[68:71], v[20:23]\n\t"
        "buffer_load_dwordx4 v100, %[sx], %[t0] offen lds\n\t"
        "v_mfma_f32_16x16x32_bf16 v[24:27], a[208:211], v[68:71], v[24:27]\n\t"
        "ds_read_b128 v[64:67], v108 offset:16384\n\t"
        "v_mfma_f32_16x16x32_bf16 v[28:31], v[68:71], a[212:215], v[28:31]\n\t"
        "s_waitcnt lgkmcnt(6)\n\t"
        "v_mfma_f32_16x16x32_bf16 v[8:11], a[216:219], v[72:75], v[8:11]\n\t"
        "v_mfma_f32_16x16x32_bf16 v[12:15], a[220:223], v[72:75], v[12:15]\n\t"
        "ds_read_b128 v[68:71], v108 offset:18432\n\t"
        "v_mfma_f32_16x16x32_bf16 v[16:19], v[72:75], a[224:227], v[16:19]\n\t"
        "s_add_u32 m0, %[ld], 50176\n\t"
        "s_add_u32 %[t0], %[xo], 17152\n\t"
        "s_waitcnt lgkmcnt(6)\n\t"
        "v_mfma_f32_16x16x32_bf16 v[20:23], a[216:219], v[76:79], v[20:23]\n\t"
        "buffer_load_dwordx4 v100, %[sx], %[t0] offen lds\n\t"
        "v_mfma_f32_16x16x32_bf16 v[24:27], a[220:223], v[76:79], v[24:27]\n\t"
        "ds_read_b128 v[72:75], v107 offset:24576\n\t"
        "v_mfma_f32_16x16x32_bf16 v[28:31], v[76:79], a[224:227], v[28:31]\n\t"
        "s_waitcnt lgkmcnt(6)\n\t"
        "v_mfma_f32_16x16x32_bf16 v[8:11], a[228:231], v[80:83], v[8:11]\n\t"
        "v_mfma_f32_16x16x32_bf16 v[12:15], a[232:235], v[80:83], v[12:15]\n\t"
        "ds_read_b128 v[76:79], v107 offset:26624\n\t"
        "v_mfma_f32_16x16x32_bf16 v[16:19], v[80:83], a[236:239], v[16:19]\n\t"
        "s_add_u32 m0, %[ld], 57344\n\t"
        "s_add_u32 %[t0], %[xo], 896\n\t"
        "s_waitcnt lgkmcnt(6)\n\t"
        "v_mfma_f32_16x16x32_bf16 v[20:23], a[228:231], v[84:87], v[20:23]\n\t"
        "buffer_load_dwordx4 v100, %[sx], %[t0] offen lds\n\t"
        "v_mfma_f32_16x16x32_bf16 v[24:27], a[232:235], v[84:87], v[24:27]\n\t"
        "ds_read_b128 v[80:83], v108 offset:24576\n\t"
        "v_mfma_f32_16x16x32_bf16 v[28:31], v[84:87], a[236:239], v[28:31]\n\t"
        "s_waitcnt lgkmcnt(6)\n\t"
        "v_mfma_f32_16x16x32_bf16 v[8:11], a[240:243], v[56:59], v[8:11]\n\t"
        "v_mfma_f32_16x16x32_bf16 v[12:15], a[244:247], v[56:59], v[12:15]\n\t"
        "ds_read_b128 v[84:87], v108 offset:26624\n\t"
        "v_mfma_f32_16x16x32_bf16 v[16:19], v[56:59], a[248:251], v[16:19]\n\t"
        "s_add_u32 m0, %[ld], 58368\n\t"
        "s_add_u32 %[t0], %[xo], 17280\n\t"
        "s_waitcnt lgkmcnt(6)\n\t"
        "v_mfma_f32_16x16x32_bf16 v[20:23], a[240:243], v[60:63], v[20:23]\n\t"
        "buffer_load_dwordx4 v100, %[sx], %[t0] offen lds\n\t"
        "v_mfma_f32_16x16x32_bf16 v[24:27], a[244:247], v[60:63], v[24:27]\n\t"
        "v_mfma_f32_16x16x32_bf16 v[28:31], v[60:63], a[248:251], v[28:31]\n\t"
        "s_waitcnt lgkmcnt(5)\n\t"
        "v_mfma_f32_16x16x32_bf16 v[8:11], a[252:255], v[64:67], v[8:11]\n\t"
        "s_waitcnt vmcnt(17) lgkmcnt(0)\n\t"
        "s_barrier\n\t"
        "v_mfma_f32_16x16x32_bf16 v[12:15], v[128:131], v[64:67], v[12:15]\n\t"
        "ds_read_b128 v[56:59], v107 offset:32768\n\t"
        "v_mfma_f32_16x16x32_bf16 v[16:19], v[64:67], v[132:135], v[16:19]\n\t"
        "ds_read_b128 v[60:63], v107 offset:34816\n\t"
        "s_add_u32 m0, %[ld], 65536\n\t"
        "s_add_u32 %[t0], %[xo], 1024\n\t"
        "v_mfma_f32_16x16x32_bf16 v[20:23], a[252:255], v[68:71], v[20:23]\n\t"
        "buffer_load_dwordx4 v100, %[sx], %[t0] offen lds\n\t"
        "v_mfma_f32_16x16x32_bf16 v[24:27], v[128:131], v[68:71], v[24:27]\n\t"
        "ds_read_b128 v[64:67], v108 offset:32768\n\t"
        "v_mfma_f32_16x16x32_bf16 v[28:31], v[68:71], v[132:135], v[28:31]\n\t"
        "v_mfma_f32_16x16x32_bf16 v[8:11], v[136:139], v[72:75], v[8:11]\n\t"
        "v_mfma_f32_16x16x32_bf16 v[12:15], v[140:143], v[72:75], v[12:15]\n\t"
        "ds_read_b128 v[68:71], v108 offset:34816\n\t"
        "v_mfma_f32_16x16x32_bf16 v[16:19], v[72:75], v[144:147], v[16:19]\n\t"
        "s_add_u32 m0, %[ld], 66560\n\t"
        "s_add_u32 %[t0], %[xo], 17408\n\t"
        "v_mfma_f32_16x16x32_bf16 v[20:23], v[136:139], v[76:79], v[20:23]\n\t"
        "buffer_load_dwordx4 v100, %[sx], %[t0] offen lds\n\t"
        "v_mfma_f32_16x16x32_bf16 v[24:27], v[140:143], v[76:79], v[24:27]\n\t"
        "ds_read_b128 v[72:75], v107 offset:40960\n\t"
        "v_mfma_f32_16x16x32_bf16 v[28:31], v[76:79], v[144:147], v[28:31]\n\t"
        "v_mfma_f32_16x16x32_bf16 v[8:11], v[148:151], v[80:83], v[8:11]\n\t"
        "v_mfma_f32_16x16x32_bf16 v[12:15], v[152:155], v[80:83], v[12:15]\n\t"
        "ds_read_b128 v[76:79], v107 offset:43008\n\t"
        "v_mfma_f32_16x16x32_bf16 v[16:19], v[80:83], v[156:159], v[16:19]\n\t"
        "s_add_u32 m0, %[ld], 73728\n\t"
        "s_add_u32 %[t0], %[xo], 1152\n\t"
        "v_mfma_f32_16x16x32_bf16 v[20:23], v[148:151], v[84:87], v[20:23]\n\t"
        "buffer_load_dwordx4 v100, %[sx], %[t0] offen lds\n\t"
        "v_mfma_f32_16x16x32_bf16 v[24:27], v[152:155], v[84:87], v[24:27]\n\t"
        "ds_read_b128 v[80:83], v108 offset:40960\n\t"
        "v_mfma_f32_16x16x32_bf16 v[28:31], v[84:87], v[156:159], v[28:31]\n\t"
        "s_waitcnt lgkmcnt(6)\n\t"
        "v_mfma_f32_16x16x32_bf16 v[8:11], v[160:163], v[56:59], v[8:11]\n\t"
        "v_mfma_f32_16x16x32_bf16 v[12:15], v[164:167], v[56:59], v[12:15]\n\t"
        "ds_read_b128 v[84:87], v108 offset:43008\n\t"
        "v_mfma_f32_16x16x32_bf16 v[16:19], v[56:59], v[168:171], v[16:19]\n\t"
        "s_add_u32 m0, %[ld], 74752\n\t"
        "s_add_u32 %[t0], %[xo], 17536\n\t"
        "s_waitcnt lgkmcnt(6)\n\t"
        "v_mfma_f32_16x16x32_bf16 v[20:23], v[160:163], v[60:63], v[20:23]\n\t"
        "buffer_load_dwordx4 v100, %[sx], %[t0] offen lds\n\t"
        "v_mfma_f32_16x16x32_bf16 v[24:27], v[164:167], v[60:63], v[24:27]\n\t"
        "ds_read_b128 v[56:59], v107 offset:49152\n\t"
        "v_mfma_f32_16x16x32_bf16 v[28:31], v[60:63], v[168:171], v[28:31]\n\t"
        "s_waitcnt lgkmcnt(6)\n\t"
        "v_mfma_f32_16x16x32_bf16 v[8:11], v[172:175], v[64:67], v[8:11]\n\t"
        "v_mfma_f32_16x16x32_bf16 v[12:15], v[176:179], v[64:67], v[12:15]\n\t"
        "ds_read_b128 v[60:63], v107 offset:51200\n\t"
        "v_mfma_f32_16x16x32_bf16 v[16:19], v[64:67], v[180:183], v[16:19]\n\t"
        "s_add_u32 m0, %[ld], 81920\n\t"
        "s_add_u32 %[t0], %[xo], 1280\n\t"
        "s_waitcnt lgkmcnt(6)\n\t"
        "v_mfma_f32_16x16x32_bf16 v[20:23], v[172:175], v[68:71], v[20:23]\n\t"
        "buffer_load_dwordx4 v100, %[sx], %[t0] offen lds\n\t"
        "v_mfma_f32_16x16x32_bf16 v[24:27], v[176:179], v[68:71], v[24:27]\n\t"
        "ds_read_b128 v[64:67], v108 offset:49152\n\t"
        "v_mfma_f32_16x16x32_bf16 v[28:31], v[68:71], v[180:183], v[28:31]\n\t"
        "s_waitcnt lgkmcnt(6)\n\t"
        "v_mfma_f32_16x16x32_bf16 v[8:11], v[184:187], v[72:75], v[8:11]\n\t"
        "v_mfma_f32_16x16x32_bf16 v[12:15], v[188:191], v[72:75], v[12:15]\n\t"
        "ds_read_b128 v[68:71], v108 offset:51200\n\t"
        "v_mfma_f32_16x16x32_bf16 v[16:19], v[72:75], v[192:195], v[16:19]\n\t"
        "s_add_u32 m0, %[ld], 82944\n\t"
        "s_add_u32 %[t0], %[xo], 17664\n\t"
        "s_waitcnt lgkmcnt(6)\n\t"
        "v_mfma_f32_16x16x32_bf16 v[20:23], v[184:187], v[76:79], v[20:23]\n\t"
        "buffer_load_dwordx4 v100, %[sx], %[t0] offen lds\n\t"
        "v_mfma_f32_16x16x32_bf16 v[24:27], v[188:191], v[76:79], v[24:27]\n\t"
        "ds_read_b128 v[72:75], v107 offset:57344\n\t"
        "v_mfma_f32_16x16x32_bf16 v[28:31], v[76:79], v[192:195], v[28:31]\n\t"
        "s_waitcnt lgkmcnt(6)\n\t"
        "v_mfma_f32_16x16x32_bf16 v[8:11], v[196:199], v[80:83], v[8:11]\n\t"
        "v_mfma_f32_16x16x32_bf16 v[12:15], v[200:203], v[80:83], v[12:15]\n\t"
        "ds_read_b128 v[76:79], v107 offset:59392\n\t"
        "v_mfma_f32_16x16x32_bf16 v[16:19], v[80:83], v[204:207], v[16:19]\n\t"
        "s_add_u32 m0, %[ld], 90112\n\t"
        "s_add_u32 %[t0], %[xo], 1408\n\t"
        "s_waitcnt lgkmcnt(6)\n\t"
        "v_mfma_f32_16x16x32_bf16 v[20:23], v[196:199], v[84:87], v[20:23]\n\t"
        "buffer_load_dwordx4 v100, %[sx], %[t0] offen lds\n\t"
        "v_mfma_f32_16x16x32_bf16 v[24:27], v[200:203], v[84:87], v[24:27]\n\t"
        "ds_read_b128 v[80:83], v108 offset:57344\n\t"
        "v_mfma_f32_16x16x32_bf16 v[28:31], v[84:87], v[204:207], v[28:31]\n\t"
        "s_waitcnt lgkmcnt(6)\n\t"
        "v_mfma_f32_16x16x32_bf16 v[8:11], v[208:211], v[56:59], v[8:11]\n\t"
        "v_mfma_f32_16x16x32_bf16 v[12:15], v[212:215], v[56:59], v[12:15]\n\t"
        "ds_read_b128 v[84:87], v108 offset:59392\n\t"
        "v_mfma_f32_16x16x32_bf16 v[16:19], v[56:59], v[216:219], v[16:19]\n\t"
        "s_add_u32 m0, %[ld], 91136\n\t"
        "s_add_u32 %[t0], %[xo], 17792\n\t"
        "s_waitcnt lgkmcnt(6)\n\t"
        "v_mfma_f32_16x16x32_bf16 v[20:23], v[208:211], v[60:63], v[20:23]\n\t"
        "buffer_load_dwordx4 v100, %[sx], %[t0] offen lds\n\t"
        "v_mfma_f32_16x16x32_bf16 v[24:27], v[212:215], v[60:63], v[24:27]\n\t"
        "v_mfma_f32_16x16x32_bf16 v[28:31], v[60:63], v[216:219], v[28:31]\n\t"
        "s_waitcnt lgkmcnt(5)\n\t"
        "v_mfma_f32_16x16x32_bf16 v[8:11], v[220:223], v[64:67], v[8:11]\n\t"
        "s_waitcnt vmcnt(17) lgkmcnt(0)\n\t"
        "s_barrier\n\t"
        "v_mfma_f32_16x16x32_bf16 v[12:15], v[224:227], v[64:67], v[12:15]\n\t"
        "ds_read_b128 v[56:59], v105 offset:0\n\t"
        "v_mfma_f32_16x16x32_bf16 v[16:19], v[64:67], v[228:231], v[16:19]\n\t"
        "ds_read_b128 v[60:63], v105 offset:2048\n\t"
        "s_add_u32 m0, %[ld], 98304\n\t"
        "s_add_u32 %[t0], %[xo], 1536\n\t"
        "v_mfma_f32_16x16x32_bf16 v[20:23], v[220:223], v[68:71], v[20:23]\n\t"
        "buffer_load_dwordx4 v100, %[sx], %[t0] offen lds\n\t"
        "v_mfma_f32_16x16x32_bf16 v[24:27], v[224:227], v[68:71], v[24:27]\n\t"
        "ds_read_b128 v[64:67], v106 offset:0\n\t"
        "v_mfma_f32_16x16x32_bf16 v[28:31], v[68:71], v[228:231], v[28:31]\n\t"
        "v_mfma_f32_16x16x32_bf16 v[8:11], v[232:235], v[72:75], v[8:11]\n\t"
        "v_mfma_f32_16x16x32_bf16 v[12:15], v[236:239], v[72:75], v[12:15]\n\t"
        "ds_read_b128 v[68:71], v106 offset:2048\n\t"
        "v_mfma_f32_16x16x32_bf16 v[16:19], v[72:75], v[240:243], v[16:19]\n\t"
        "s_add_u32 m0, %[ld], 99328\n\t"
        "s_add_u32 %[t0], %[xo], 17920\n\t"
        "v_mfma_f32_16x16x32_bf16 v[20:23], v[232:235], v[76:79], v[20:23]\n\t"
        "buffer_load_dwordx4 v100, %[sx], %[t0] offen lds\n\t"
        "v_mfma_f32_16x16x32_bf16 v[24:27], v[236:239], v[76:79], v[24:27]\n\t"
        "ds_read_b128 v[72:75], v105 offset:8192\n\t"
        "v_mfma_f32_16x16x32_bf16 v[28:31], v[76:79], v[240:243], v[28:31]\n\t"
        "v_mfma_f32_16x16x32_bf16 v[8:11], v[244:247], v[80:83], v[8:11]\n\t"
        "v_mfma_f32_16x16x32_bf16 v[12:15], v[248:251], v[80:83], v[12:15]\n\t"
        "ds_read_b128 v[76:79], v105 offset:10240\n\t"
        "v_mfma_f32_16x16x32_bf16 v[16:19], v[80:83], v[252:255], v[16:19]\n\t"
        "s_add_u32 m0, %[ld], 106496\n\t"
        "s_add_u32 %[t0], %[xo], 1664\n\t"
        "v_mfma_f32_16x16x32_bf16 v[20:23], v[244:247], v[84:87], v[20:23]\n\t"
        "buffer_load_dwordx4 v100, %[sx], %[t0] offen lds\n\t"
        "v_mfma_f32_16x16x32_bf16 v[24:27], v[248:251], v[84:87], v[24:27]\n\t"
        "ds_read_b128 v[80:83], v106 offset:8192\n\t"
        "v_mfma_f32_16x16x32_bf16 v[28:31], v[84:87], v[252:255], v[28:31]\n\t"
        "LG_mid%=:\n\t"
        "s_waitcnt lgkmcnt(6)\n\t"
        "v_mfma_f32_16x16x32_bf16 v[32:35], a[0:3], v[56:59], 0\n\t"
        "v_mfma_f32_16x16x32_bf16 v[36:39], a[4:7], v[56:59], 0\n\t"
        "ds_read_b128 v[84:87], v106 offset:10240\n\t"
        "v_mfma_f32_16x16x32_bf16 v[40:43], v[56:59], a[8:11], 0\n\t"
        "s_add_u32 m0, %[ld], 107520\n\t"
        "s_add_u32 %[t0], %[xo], 18048\n\t"
        "s_waitcnt lgkmcnt(6)\n\t"
        "v_mfma_f32_16x16x32_bf16 v[44:47], a[0:3], v[60:63], 0\n\t"
        "buffer_load_dwordx4 v100, %[sx], %[t0] offen lds\n\t"
        "v_mfma_f32_16x16x32_bf16 v[48:51], a[4:7], v[60:63], 0\n\t"
        "ds_read_b128 v[56:59], v105 offset:16384\n\t"
        "v_mfma_f32_16x16x32_bf16 v[52:55], v[60:63], a[8:11], 0\n\t"
        "s_waitcnt lgkmcnt(6)\n\t"
        "v_mfma_f32_16x16x32_bf16 v[32:35], a[12:15], v[64:67], v[32:35]\n\t"
        "v_cvt_pk_bf16_f32 v88, v8, v9\n\t"
        "v_cvt_pk_bf16_f32 v89, v10, v11\n\t"
        "v_mfma_f32_16x16x32_bf16 v[36:39], a[16:19], v[64:67], v[36:39]\n\t"
        "ds_read_b128 v[60:63], v105 offset:18432\n\t"
        "v_cvt_pk_bf16_f32 v90, v12, v13\n\t"
        "v_mfma_f32_16x16x32_bf16 v[40:43], v[64:67], a[20:23], v[40:43]\n\t"
        "s_add_u32 m0, %[ld], 114688\n\t"
        "s_add_u32 %[t0], %[xo], 1792\n\t"
        "s_waitcnt lgkmcnt(6)\n\t"
        "v_mfma_f32_16x16x32_bf16 v[44:47], a[12:15], v[68:71], v[44:47]\n\t"
        "buffer_load_dwordx4 v100, %[sx], %[t0] offen lds\n\t"
        "v_mfma_f32_16x16x32_bf16 v[48:51], a[16:19], v[68:71], v[48:51]\n\t"
        "ds_read_b128 v[64:67], v106 offset:16384\n\t"
        "v_cvt_pk_bf16_f32 v91, v14, v15\n\t"
        "v_mfma_f32_16x16x32_bf16 v[52:55], v[68:71], a[20:23], v[52:55]\n\t"
        "buffer_store_dwordx4 v[88:91], v109, %[sw], %[so] offen sc1\n\t"
        "s_waitcnt lgkmcnt(6)\n\t"
        "v_mfma_f32_16x16x32_bf16 v[32:35], a[24:27], v[72:75], v[32:35]\n\t"
        "v_cvt_pk_bf16_f32 v92, v20, v21\n\t"
        "v_cvt_pk_bf16_f32 v93, v22, v23\n\t"
        "v_mfma_f32_16x16x32_bf16 v[36:39], a[28:31], v[72:75], v[36:39]\n\t"
        "ds_read_b128 v[68:71], v106 offset:18432\n\t"
        "v_cvt_pk_bf16_f32 v94, v24, v25\n\t"
        "v_mfma_f32_16x16x32_bf16 v[40:43], v[72:75], a[32:35], v[40:43]\n\t"
        "s_add_u32 m0, %[ld], 115712\n\t"
        "s_add_u32 %[t0], %[xo], 18176\n\t"
        "s_waitcnt lgkmcnt(6)\n\t"
        "v_mfma_f32_16x16x32_bf16 v[44:47], a[24:27], v[76:79], v[44:47]\n\t"
        "buffer_load_dwordx4 v100, %[sx], %[t0] offen lds\n\t"
        "v_mfma_f32_16x16x32_bf16 v[48:51], a[28:31], v[76:79], v[48:51]\n\t"
        "ds_read_b128 v[72:75], v105 offset:24576\n\t"
        "v_cvt_pk_bf16_f32 v95, v26, v27\n\t"
        "v_mfma_f32_16x16x32_bf16 v[52:55], v[76:79], a[32:35], v[52:55]\n\t"
        "buffer_store_dwordx4 v[92:95], v110, %[sw], %[so] offen sc1\n\t"
        "s_waitcnt lgkmcnt(6)\n\t"
        "v_mfma_f32_16x16x32_bf16 v[32:35], a[36:39], v[80:83], v[32:35]\n\t"
        "v_cvt_pk_bf16_f32 v96, v16, v17\n\t"
        "v_cvt_pk_bf16_f32 v97, v18, v19\n\t"
        "v_mfma_f32_16x16x32_bf16 v[36:39], a[40:43], v[80:83], v[36:39]\n\t"
        "ds_read_b128 v[76:79], v105 offset:26624\n\t"
        "v_cvt_pk_bf16_f32 v98, v28, v29\n\t"
        "v_mfma_f32_16x16x32_bf16 v[40:43], v[80:83], a[44:47], v[40:43]\n\t"
        "s_add_u32 m0, %[ld], 122880\n\t"
        "s_add_u32 %[t0], %[xo], 1920\n\t"
        "s_waitcnt lgkmcnt(6)\n\t"
        "v_mfma_f32_16x16x32_bf16 v[44:47], a[36:39], v[84:87], v[44:47]\n\t"
        "buffer_load_dwordx4 v100, %[sx], %[t0] offen lds\n\t"
        "v_mfma_f32_16x16x32_bf16 v[48:51], a[40:43], v[84:87], v[48:51]\n\t"
        "ds_read_b128 v[80:83], v106 offset:24576\n\t"
        "v_cvt_pk_bf16_f32 v99, v30, v31\n\t"
        "v_mfma_f32_16x16x32_bf16 v[52:55], v[84:87], a[44:47], v[52:55]\n\t"
        "buffer_store_dwordx4 v[96:99], v111, %[sw], %[so] offen sc1\n\t"
        "s_waitcnt lgkmcnt(6)\n\t"
        "v_mfma_f32_16x16x32_bf16 v[32:35], a[48:51], v[56:59], v[32:35]\n\t"
        "s_lshl_b32 %[so], %[cn], 19\n\t"
        "v_mfma_f32_16x16x32_bf16 v[36:39], a[52:55], v[56:59], v[36:39]\n\t"
        "ds_read_b128 v[84:87], v106 offset:26624\n\t"
        "v_mfma_f32_16x16x32_bf16 v[40:43], v[56:59], a[56:59], v[40:43]\n\t"
        "s_add_u32 m0, %[ld], 123904\n\t"
        "s_add_u32 %[t0], %[xo], 18304\n\t"
        "s_waitcnt lgkmcnt(6)\n\t"
        "v_mfma_f32_16x16x32_bf16 v[44:47], a[48:51], v[60:63], v[44:47]\n\t"
        "buffer_load_dwordx4 v100, %[sx], %[t0] offen lds\n\t"
        "v_mfma_f32_16x16x32_bf16 v[48:51], a[52:55], v[60:63], v[48:51]\n\t"
        "v_mfma_f32_16x16x32_bf16 v[52:55], v[60:63], a[56:59], v[52:55]\n\t"
        "s_waitcnt lgkmcnt(5)\n\t"
        "v_mfma_f32_16x16x32_bf16 v[32:35], a[60:63], v[64:67], v[32:35]\n\t"
        "s_waitcnt vmcnt(19) lgkmcnt(0)\n\t"
        "s_barrier\n\t"
        "s_add_u32 %[xo], %[xo], 0x20000\n\t"
        "v_mfma_f32_16x16x32_bf16 v[36:39], a[64:67], v[64:67], v[36:39]\n\t"
        "ds_read_b128 v[56:59], v105 offset:32768\n\t"
        "v_mfma_f32_16x16x32_bf16 v[40:43], v[64:67], a[68:71], v[40:43]\n\t"
        "ds_read_b128 v[60:63], v105 offset:34816\n\t"
        "s_add_u32 m0, %[ld], 0\n\t"
        "s_add_u32 %[t0], %[xo], 0\n\t"
        "v_mfma_f32_16x16x32_bf16 v[44:47], a[60:63], v[68:71], v[44:47]\n\t"
        "buffer_load_dwordx4 v100, %[sx], %[t0] offen lds\n\t"
        "v_mfma_f32_16x16x32_bf16 v[48:51], a[64:67], v[68:71], v[48:51]\n\t"
        "ds_read_b128 v[64:67], v106 offset:32768\n\t"
        "v_mfma_f32_16x16x32_bf16 v[52:55], v[68:71], a[68:71], v[52:55]\n\t"
        "v_mfma_f32_16x16x32_bf16 v[32:35], a[72:75], v[72:75], v[32:35]\n\t"
        "v_mfma_f32_16x16x32_bf16 v[36:39], a[76:79], v[72:75], v[36:39]\n\t"
        "ds_read_b128 v[68:71], v106 offset:34816\n\t"
        "v_mfma_f32_16x16x32_bf16 v[40:43], v[72:75], a[80:83], v[40:43]\n\t"
        "s_add_u32 m0, %[ld], 1024\n\t"
        "s_add_u32 %[t0], %[xo], 16384\n\t"
        "v_mfma_f32_16x16x32_bf16 v[44:47], a[72:75], v[76:79], v[44:47]\n\t"
        "buffer_load_dwordx4 v100, %[sx], %[t0] offen lds\n\t"
        "v_mfma_f32_16x16x32_bf16 v[48:51], a[76:79], v[76:79], v[48:51]\n\t"
        "ds_read_b128 v[72:75], v105 offset:40960\n\t"
        "v_mfma_f32_16x16x32_bf16 v[52:55], v[76:79], a[80:83], v[52:55]\n\t"
        "v_mfma_f32_16x16x32_bf16 v[32:35], a[84:87], v[80:83], v[32:35]\n\t"
        "v_mfma_f32_16x16x32_bf16 v[36:39], a[88:91], v[80:83], v[36:39]\n\t"
        "ds_read_b128 v[76:79], v105 offset:43008\n\t"
        "v_mfma_f32_16x16x32_bf16 v[40:43], v[80:83], a[92:95], v[40:43]\n\t"
        "s_add_u32 m0, %[ld], 8192\n\t"
        "s_add_u32 %[t0], %[xo], 128\n\t"
        "v_mfma_f32_16x16x32_bf16 v[44:47], a[84:87], v[84:87], v[44:47]\n\t"
        "buffer_load_dwordx4 v100, %[sx], %[t0] offen lds\n\t"
        "v_mfma_f32_16x16x32_bf16 v[48:51], a[88:91], v[84:87], v[48:51]\n\t"
        "ds_read_b128 v[80:83], v106 offset:40960\n\t"
        "v_mfma_f32_16x16x32_bf16 v[52:55], v[84:87], a[92:95], v[52:55]\n\t"
        "s_waitcnt lgkmcnt(6)\n\t"
        "v_mfma_f32_16x16x32_bf16 v[32:35], a[96:99], v[56:59], v[32:35]\n\t"
        "v_mfma_f32_16x16x32_bf16 v[36:39], a[100:103], v[56:59], v[36:39]\n\t"
        "ds_read_b128 v[84:87], v106 offset:43008\n\t"
        "v_mfma_f32_16x16x32_bf16 v[40:43], v[56:59], a[104:107], v[40:43]\n\t"
        "s_add_u32 m0, %[ld], 9216\n\t"
        "s_add_u32 %[t0], %[xo], 16512\n\t"
        "s_waitcnt lgkmcnt(6)\n\t"
        "v_mfma_f32_16x16x32_bf16 v[44:47], a[96:99], v[60:63], v[44:47]\n\t"
        "buffer_load_dwordx4 v100, %[sx], %[t0] offen lds\n\t"
        "v_mfma_f32_16x16x32_bf16 v[48:51], a[100:103], v[60:63], v[48:51]\n\t"
        "ds_read_b128 v[56:59], v105 offset:49152\n\t"
        "v_mfma_f32_16x16x32_bf16 v[52:55], v[60:63], a[104:107], v[52:55]\n\t"
        "s_waitcnt lgkmcnt(6)\n\t"
        "v_mfma_f32_16x16x32_bf16 v[32:35], a[108:111], v[64:67], v[32:35]\n\t"
        "v_mfma_f32_16x16x32_bf16 v[36:39], a[112:115], v[64:67], v[36:39]\n\t"
        "ds_read_b128 v[60:63], v105 offset:51200\n\t"
        "v_mfma_f32_16x16x32_bf16 v[40:43], v[64:67], a[116:119], v[40:43]\n\t"
        "s_add_u32 m0, %[ld], 16384\n\t"
        "s_add_u32 %[t0], %[xo], 256\n\t"
        "s_waitcnt lgkmcnt(6)\n\t"
        "v_mfma_f32_16x16x32_bf16 v[44:47], a[108:111], v[68:71], v[44:47]\n\t"
        "buffer_load_dwordx4 v100, %[sx], %[t0] offen lds\n\t"
        "v_mfma_f32_16x16x32_bf16 v[48:51], a[112:115], v[68:71], v[48:51]\n\t"
        "ds_read_b128 v[64:67], v106 offset:49152\n\t"
        "v_mfma_f32_16x16x32_bf16 v[52:55], v[68:71], a[116:119], v[52:55]\n\t"
        "s_waitcnt lgkmcnt(6)\n\t"
        "v_mfma_f32_16x16x32_bf16 v[32:35], a[120:123], v[72:75], v[32:35]\n\t"
        "v_mfma_f32_16x16x32_bf16 v[36:39], a[124:127], v[72:75], v[36:39]\n\t"
        "ds_read_b128 v[68:71], v106 offset:51200\n\t"
        "v_mfma_f32_16x16x32_bf16 v[40:43], v[72:75], a[128:131], v[40:43]\n\t"
        "s_add_u32 m0, %[ld], 17408\n\t"
        "s_add_u32 %[t0], %[xo], 16640\n\t"
        "s_waitcnt lgkmcnt(6)\n\t"
        "v_mfma_f32_16x16x32_bf16 v[44:47], a[120:123], v[76:79], v[44:47]\n\t"
        "buffer_load_dwordx4 v100, %[sx], %[t0] offen lds\n\t"
        "v_mfma_f32_16x16x32_bf16 v[48:51], a[124:127], v[76:79], v[48:51]\n\t"
        "ds_read_b128 v[72:75], v105 offset:57344\n\t"
        "v_mfma_f32_16x16x32_bf16 v[52:55], v[76:79], a[128:131], v[52:55]\n\t"
        "s_waitcnt lgkmcnt(6)\n\t"
        "v_mfma_f32_16x16x32_bf16 v[32:35], a[132:135], v[80:83], v[32:35]\n\t"
        "v_mfma_f32_16x16x32_bf16 v[36:39], a[136:139], v[80:83], v[36:39]\n\t"
        "ds_read_b128 v[76:79], v105 offset:59392\n\t"
        "v_mfma_f32_16x16x32_bf16 v[40:43], v[80:83], a[140:143], v[40:43]\n\t"
        "s_add_u32 m0, %[ld], 24576\n\t"
        "s_add_u32 %[t0], %[xo], 384\n\t"
        "s_waitcnt lgkmcnt(6)\n\t"
        "v_mfma_f32_16x16x32_bf16 v[44:47], a[132:135], v[84:87], v[44:47]\n\t"
        "buffer_load_dwordx4 v100, %[sx], %[t0] offen lds\n\t"
        "v_mfma_f32_16x16x32_bf16 v[48:51], a[136:139], v[84:87], v[48:51]\n\t"
        "ds_read_b128 v[80:83], v106 offset:57344\n\t"
        "v_mfma_f32_16x16x32_bf16 v[52:55], v[84:87], a[140:143], v[52:55]\n\t"
        "s_waitcnt lgkmcnt(6)\n\t"
        "v_mfma_f32_16x16x32_bf16 v[32:35], a[144:147], v[56:59], v[32:35]\n\t"
        "v_mfma_f32_16x16x32_bf16 v[36:39], a[148:151], v[56:59], v[36:39]\n\t"
        "ds_read_b128 v[84:87], v106 offset:59392\n\t"
        "v_mfma_f32_16x16x32_bf16 v[40:43], v[56:59], a[152:155], v[40:43]\n\t"
        "s_add_u32 m0, %[ld], 25600\n\t"
        "s_add_u32 %[t0], %[xo], 16768\n\t"
        "s_waitcnt lgkmcnt(6)\n\t"
        "v_mfma_f32_16x16x32_bf16 v[44:47], a[144:147], v[60:63], v[44:47]\n\t"
        "buffer_load_dwordx4 v100, %[sx], %[t0] offen lds\n\t"
        "v_mfma_f32_16x16x32_bf16 v[48:51], a[148:151], v[60:63], v[48:51]\n\t"
        "v_mfma_f32_16x16x32_bf16 v[52:55], v[60:63], a[152:155], v[52:55]\n\t"
        "s_waitcnt lgkmcnt(5)\n\t"
        "v_mfma_f32_16x16x32_bf16 v[32:35], a[156:159], v[64:67], v[32:35]\n\t"
        "s_waitcnt vmcnt(9) lgkmcnt(0)\n\t"
        "s_barrier\n\t"
        "v_mfma_f32_16x16x32_bf16 v[36:39], a[160:163], v[64:67], v[36:39]\n\t"
        "ds_read_b128 v[56:59], v107 offset:0\n\t"
        "v_mfma_f32_16x16x32_bf16 v[40:43], v[64:67], a[164:167], v[40:43]\n\t"
        "ds_read_b128 v[60:63], v107 offset:2048\n\t"
        "s_add_u32 m0, %[ld], 32768\n\t"
        "s_add_u32 %[t0], %[xo], 512\n\t"
        "v_mfma_f32_16x16x32_bf16 v[44:47], a[156:159], v[68:71], v[44:47]\n\t"
        "buffer_load_dwordx4 v100, %[sx], %[t0] offen lds\n\t"
        "v_mfma_f32_16x16x32_bf16 v[48:51], a[160:163], v[68:71], v[48:51]\n\t"
        "ds_read_b128 v[64:67], v108 offset:0\n\t"
        "v_mfma_f32_16x16x32_bf16 v[52:55], v[68:71], a[164:167], v[52:55]\n\t"
        "v_mfma_f32_16x16x32_bf16 v[32:35], a[168:171], v[72:75], v[32:35]\n\t"
        "v_mfma_f32_16x16x32_bf16 v[36:39], a[172:175], v[72:75], v[36:39]\n\t"
        "ds_read_b128 v[68:71], v108 offset:2048\n\t"
        "v_mov_b32_e32 v117, %[cn]\n\t"
        "buffer_store_dword v117, v113, %[sw], 0 offen sc1\n\t"
        "s_add_u32 %[cn], %[cn], 1\n\t"
        "v_mfma_f32_16x16x32_bf16 v[40:43], v[72:75], a[176:179], v[40:43]\n\t"
        "s_add_u32 m0, %[ld], 33792\n\t"
        "s_add_u32 %[t0], %[xo], 16896\n\t"
        "v_mfma_f32_16x16x32_bf16 v[44:47], a[168:171], v[76:79], v[44:47]\n\t"
        "buffer_load_dwordx4 v100, %[sx], %[t0] offen lds\n\t"
        "v_mfma_f32_16x16x32_bf16 v[48:51], a[172:175], v[76:79], v[48:51]\n\t"
        "ds_read_b128 v[72:75], v107 offset:8192\n\t"
        "v_mfma_f32_16x16x32_bf16 v[52:55], v[76:79], a[176:179], v[52:55]\n\t"
        "v_mfma_f32_16x16x32_bf16 v[32:35], a[180:183], v[80:83], v[32:35]\n\t"
        "v_mfma_f32_16x16x32_bf16 v[36:39], a[184:187], v[80:83], v[36:39]\n\t"
        "ds_read_b128 v[76:79], v107 offset:10240\n\t"
        "v_mfma_f32_16x16x32_bf16 v[40:43], v[80:83], a[188:191], v[40:43]\n\t"
        "s_add_u32 m0, %[ld], 40960\n\t"
        "s_add_u32 %[t0], %[xo], 640\n\t"
        "v_mfma_f32_16x16x32_bf16 v[44:47], a[180:183], v[84:87], v[44:47]\n\t"
        "buffer_load_dwordx4 v100, %[sx], %[t0] offen lds\n\t"
        "v_mfma_f32_16x16x32_bf16 v[48:51], a[184:187], v[84:87], v[48:51]\n\t"
        "ds_read_b128 v[80:83], v108 offset:8192\n\t"
        "v_mfma_f32_16x16x32_bf16 v[52:55], v[84:87], a[188:191], v[52:55]\n\t"
        "s_waitcnt lgkmcnt(6)\n\t"
        "v_mfma_f32_16x16x32_bf16 v[32:35], a[192:195], v[56:59], v[32:35]\n\t"
        "v_mfma_f32_16x16x32_bf16 v[36:39], a[196:199], v[56:59], v[36:39]\n\t"
        "ds_read_b128 v[84:87], v108 offset:10240\n\t"
        "v_mfma_f32_16x16x32_bf16 v[40:43], v[56:59], a[200:203], v[40:43]\n\t"
        "s_add_u32 m0, %[ld], 41984\n\t"
        "s_add_u32 %[t0], %[xo], 17024\n\t"
        "s_waitcnt lgkmcnt(6)\n\t"
        "v_mfma_f32_16x16x32_bf16 v[44:47], a[192:195], v[60:63], v[44:47]\n\t"
        "buffer_load_dwordx4 v100, %[sx], %[t0] offen lds\n\t"
        "v_mfma_f32_16x16x32_bf16 v[48:51], a[196:199], v[60:63], v[48:51]\n\t"
        "ds_read_b128 v[56:59], v107 offset:16384\n\t"
        "v_mfma_f32_16x16x32_bf16 v[52:55], v[60:63], a[200:203], v[52:55]\n\t"
        "s_waitcnt lgkmcnt(6)\n\t"
        "v_mfma_f32_16x16x32_bf16 v[32:35], a[204:207], v[64:67], v[32:35]\n\t"
        "v_mfma_f32_16x16x32_bf16 v[36:39], a[208:211], v[64:67], v[36:39]\n\t"
        "ds_read_b128 v[60:63], v107 offset:18432\n\t"
        "v_mfma_f32_16x16x32_bf16 v[40:43], v[64:67], a[212:215], v[40:43]\n\t"
        "s_add_u32 m0, %[ld], 49152\n\t"
        "s_add_u32 %[t0], %[xo], 768\n\t"
        "s_waitcnt lgkmcnt(6)\n\t"
        "v_mfma_f32_16x16x32_bf16 v[44:47], a[204:207], v[68:71], v[44:47]\n\t"
        "buffer_load_dwordx4 v100, %[sx], %[t0] offen lds\n\t"
        "v_mfma_f32_16x16x32_bf16 v[48:51], a[208:211], v[68:71], v[48:51]\n\t"
        "ds_read_b128 v[64:67], v108 offset:16384\n\t"
        "v_mfma_f32_16x16x32_bf16 v[52:55], v[68:71], a[212:215], v[52:55]\n\t"
        "s_waitcnt lgkmcnt(6)\n\t"
        "v_mfma_f32_16x16x32_bf16 v[32:35], a[216:219], v[72:75], v[32:35]\n\t"
        "v_mfma_f32_16x16x32_bf16 v[36:39], a[220:223], v[72:75], v[36:39]\n\t"
        "ds_read_b128 v[68:71], v108 offset:18432\n\t"
        "v_mfma_f32_16x16x32_bf16 v[40:43], v[72:75], a[224:227], v[40:43]\n\t"
        "s_add_u32 m0, %[ld], 50176\n\t"
        "s_add_u32 %[t0], %[xo], 17152\n\t"
        "s_waitcnt lgkmcnt(6)\n\t"
        "v_mfma_f32_16x16x32_bf16 v[44:47], a[216:219], v[76:79], v[44:47]\n\t"
        "buffer_load_dwordx4 v100, %[sx], %[t0] offen lds\n\t"
        "v_mfma_f32_16x16x32_bf16 v[48:51], a[220:223], v[76:79], v[48:51]\n\t"
        "ds_read_b128 v[72:75], v107 offset:24576\n\t"
        "v_mfma_f32_16x16x32_bf16 v[52:55], v[76:79], a[224:227], v[52:55]\n\t"
        "s_waitcnt lgkmcnt(6)\n\t"
        "v_mfma_f32_16x16x32_bf16 v[32:35], a[228:231], v[80:83], v[32:35]\n\t"
        "v_mfma_f32_16x16x32_bf16 v[36:39], a[232:235], v[80:83], v[36:39]\n\t"
        "ds_read_b128 v[76:79], v107 offset:26624\n\t"
        "v_mfma_f32_16x16x32_bf16 v[40:43], v[80:83], a[236:239], v[40:43]\n\t"
        "s_add_u32 m0, %[ld], 57344\n\t"
        "s_add_u32 %[t0], %[xo], 896\n\t"
        "s_waitcnt lgkmcnt(6)\n\t"
        "v_mfma_f32_16x16x32_bf16 v[44:47], a[228:231], v[84:87], v[44:47]\n\t"
        "buffer_load_dwordx4 v100, %[sx], %[t0] offen lds\n\t"
        "v_mfma_f32_16x16x32_bf16 v[48:51], a[232:235], v[84:87], v[48:51]\n\t"
        "ds_read_b128 v[80:83], v108 offset:24576\n\t"
        "v_mfma_f32_16x16x32_bf16 v[52:55], v[84:87], a[236:239], v[52:55]\n\t"
        "s_waitcnt lgkmcnt(6)\n\t"
        "v_mfma_f32_16x16x32_bf16 v[32:35], a[240:243], v[56:59], v[32:35]\n\t"
        "v_mfma_f32_16x16x32_bf16 v[36:39], a[244:247], v[56:59], v[36:39]\n\t"
        "ds_read_b128 v[84:87], v108 offset:26624\n\t"
        "v_mfma_f32_16x16x32_bf16 v[40:43], v[56:59], a[248:251], v[40:43]\n\t"
        "s_add_u32 m0, %[ld], 58368\n\t"
        "s_add_u32 %[t0], %[xo], 17280\n\t"
        "s_waitcnt lgkmcnt(6)\n\t"
        "v_mfma_f32_16x16x32_bf16 v[44:47], a[240:243], v[60:63], v[44:47]\n\t"
        "buffer_load_dwordx4 v100, %[sx], %[t0] offen lds\n\t"
        "v_mfma_f32_16x16x32_bf16 v[48:51], a[244:247], v[60:63], v[48:51]\n\t"
        "v_mfma_f32_16x16x32_bf16 v[52:55], v[60:63], a[248:251], v[52:55]\n\t"
        "s_waitcnt lgkmcnt(5)\n\t"
        "v_mfma_f32_16x16x32_bf16 v[32:35], a[252:255], v[64:67], v[32:35]\n\t"
        "s_waitcnt vmcnt(17) lgkmcnt(0)\n\t"
        "s_barrier\n\t"
        "v_mfma_f32_16x16x32_bf16 v[36:39], v[128:131], v[64:67], v[36:39]\n\t"
        "ds_read_b128 v[56:59], v107 offset:32768\n\t"
        "v_mfma_f32_16x16x32_bf16 v[40:43], v[64:67], v[132:135], v[40:43]\n\t"
        "ds_read_b128 v[60:63], v107 offset:34816\n\t"
        "s_add_u32 m0, %[ld], 65536\n\t"
        "s_add_u32 %[t0], %[xo], 1024\n\t"
        "v_mfma_f32_16x16x32_bf16 v[44:47], a[252:255], v[68:71], v[44:47]\n\t"
        "buffer_load_dwordx4 v100, %[sx], %[t0] offen lds\n\t"
        "v_mfma_f32_16x16x32_bf16 v[48:51], v[128:131], v[68:71], v[48:51]\n\t"
        "ds_read_b128 v[64:67], v108 offset:32768\n\t"
        "v_mfma_f32_16x16x32_bf16 v[52:55], v[68:71], v[132:135], v[52:55]\n\t"
        "v_mfma_f32_16x16x32_bf16 v[32:35], v[136:139], v[72:75], v[32:35]\n\t"
        "v_mfma_f32_16x16x32_bf16 v[36:39], v[140:143], v[72:75], v[36:39]\n\t"
        "ds_read_b128 v[68:71], v108 offset:34816\n\t"
        "v_mfma_f32_16x16x32_bf16 v[40:43], v[72:75], v[144:147], v[40:43]\n\t"
        "s_add_u32 m0, %[ld], 66560\n\t"
        "s_add_u32 %[t0], %[xo], 17408\n\t"
        "v_mfma_f32_16x16x32_bf16 v[44:47], v[136:139], v[76:79], v[44:47]\n\t"
        "buffer_load_dwordx4 v100, %[sx], %[t0] offen lds\n\t"
        "v_mfma_f32_16x16x32_bf16 v[48:51], v[140:143], v[76:79], v[48:51]\n\t"
        "ds_read_b128 v[72:75], v107 offset:40960\n\t"
        "v_mfma_f32_16x16x32_bf16 v[52:55], v[76:79], v[144:147], v[52:55]\n\t"
        "v_mfma_f32_16x16x32_bf16 v[32:35], v[148:151], v[80:83], v[32:35]\n\t"
        "v_mfma_f32_16x16x32_bf16 v[36:39], v[152:155], v[80:83], v[36:39]\n\t"
        "ds_read_b128 v[76:79], v107 offset:43008\n\t"
        "v_mfma_f32_16x16x32_bf16 v[40:43], v[80:83], v[156:159], v[40:43]\n\t"
        "s_add_u32 m0, %[ld], 73728\n\t"
        "s_add_u32 %[t0], %[xo], 1152\n\t"
        "v_mfma_f32_16x16x32_bf16 v[44:47], v[148:151], v[84:87], v[44:47]\n\t"
        "buffer_load_dwordx4 v100, %[sx], %[t0] offen lds\n\t"
        "v_mfma_f32_16x16x32_bf16 v[48:51], v[152:155], v[84:87], v[48:51]\n\t"
        "ds_read_b128 v[80:83], v108 offset:40960\n\t"
        "v_mfma_f32_16x16x32_bf16 v[52:55], v[84:87], v[156:159], v[52:55]\n\t"
        "s_waitcnt lgkmcnt(6)\n\t"
        "v_mfma_f32_16x16x32_bf16 v[32:35], v[160:163], v[56:59], v[32:35]\n\t"
        "v_mfma_f32_16x16x32_bf16 v[36:39], v[164:167], v[56:59], v[36:39]\n\t"
        "ds_read_b128 v[84:87], v108 offset:43008\n\t"
        "v_mfma_f32_16x16x32_bf16 v[40:43], v[56:59], v[168:171], v[40:43]\n\t"
        "s_add_u32 m0, %[ld], 74752\n\t"
        "s_add_u32 %[t0], %[xo], 17536\n\t"
        "s_waitcnt lgkmcnt(6)\n\t"
        "v_mfma_f32_16x16x32_bf16 v[44:47], v[160:163], v[60:63], v[44:47]\n\t"
        "buffer_load_dwordx4 v100, %[sx], %[t0] offen lds\n\t"
        "v_mfma_f32_16x16x32_bf16 v[48:51], v[164:167], v[60:63], v[48:51]\n\t"
        "ds_read_b128 v[56:59], v107 offset:49152\n\t"
        "v_mfma_f32_16x16x32_bf16 v[52:55], v[60:63], v[168:171], v[52:55]\n\t"
        "s_waitcnt lgkmcnt(6)\n\t"
        "v_mfma_f32_16x16x32_bf16 v[32:35], v[172:175], v[64:67], v[32:35]\n\t"
        "v_mfma_f32_16x16x32_bf16 v[36:39], v[176:179], v[64:67], v[36:39]\n\t"
        "ds_read_b128 v[60:63], v107 offset:51200\n\t"
        "v_mfma_f32_16x16x32_bf16 v[40:43], v[64:67], v[180:183], v[40:43]\n\t"
        "s_add_u32 m0, %[ld], 81920\n\t"
        "s_add_u32 %[t0], %[xo], 1280\n\t"
        "s_waitcnt lgkmcnt(6)\n\t"
        "v_mfma_f32_16x16x32_bf16 v[44:47], v[172:175], v[68:71], v[44:47]\n\t"
        "buffer_load_dwordx4 v100, %[sx], %[t0] offen lds\n\t"
        "v_mfma_f32_16x16x32_bf16 v[48:51], v[176:179], v[68:71], v[48:51]\n\t"
        "ds_read_b128 v[64:67], v108 offset:49152\n\t"
        "v_mfma_f32_16x16x32_bf16 v[52:55], v[68:71], v[180:183], v[52:55]\n\t"
        "s_waitcnt lgkmcnt(6)\n\t"
        "v_mfma_f32_16x16x32_bf16 v[32:35], v[184:187], v[72:75], v[32:35]\n\t"
        "v_mfma_f32_16x16x32_bf16 v[36:39], v[188:191], v[72:75], v[36:39]\n\t"
        "ds_read_b128 v[68:71], v108 offset:51200\n\t"
        "v_mfma_f32_16x16x32_bf16 v[40:43], v[72:75], v[192:195], v[40:43]\n\t"
        "s_add_u32 m0, %[ld], 82944\n\t"
        "s_add_u32 %[t0], %[xo], 17664\n\t"
        "s_waitcnt lgkmcnt(6)\n\t"
        "v_mfma_f32_16x16x32_bf16 v[44:47], v[184:187], v[76:79], v[44:47]\n\t"
        "buffer_load_dwordx4 v100, %[sx], %[t0] offen lds\n\t"
        "v_mfma_f32_16x16x32_bf16 v[48:51], v[188:191], v[76:79], v[48:51]\n\t"
        "ds_read_b128 v[72:75], v107 offset:57344\n\t"
        "v_mfma_f32_16x16x32_bf16 v[52:55], v[76:79], v[192:195], v[52:55]\n\t"
        "s_waitcnt lgkmcnt(6)\n\t"
        "v_mfma_f32_16x16x32_bf16 v[32:35], v[196:199], v[80:83], v[32:35]\n\t"
        "v_mfma_f32_16x16x32_bf16 v[36:39], v[200:203], v[80:83], v[36:39]\n\t"
        "ds_read_b128 v[76:79], v107 offset:59392\n\t"
        "v_mfma_f32_16x16x32_bf16 v[40:43], v[80:83], v[204:207], v[40:43]\n\t"
        "s_add_u32 m0, %[ld], 90112\n\t"
        "s_add_u32 %[t0], %[xo], 1408\n\t"
        "s_waitcnt lgkmcnt(6)\n\t"
        "v_mfma_f32_16x16x32_bf16 v[44:47], v[196:199], v[84:87], v[44:47]\n\t"
        "buffer_load_dwordx4 v100, %[sx], %[t0] offen lds\n\t"
        "v_mfma_f32_16x16x32_bf16 v[48:51], v[200:203], v[84:87], v[48:51]\n\t"
        "ds_read_b128 v[80:83], v108 offset:57344\n\t"
        "v_mfma_f32_16x16x32_bf16 v[52:55], v[84:87], v[204:207], v[52:55]\n\t"
        "s_waitcnt lgkmcnt(6)\n\t"
        "v_mfma_f32_16x16x32_bf16 v[32:35], v[208:211], v[56:59], v[32:35]\n\t"
        "v_mfma_f32_16x16x32_bf16 v[36:39], v[212:215], v[56:59], v[36:39]\n\t"
        "ds_read_b128 v[84:87], v108 offset:59392\n\t"
        "v_mfma_f32_16x16x32_bf16 v[40:43], v[56:59], v[216:219], v[40:43]\n\t"
        "s_add_u32 m0, %[ld], 91136\n\t"
        "s_add_u32 %[t0], %[xo], 17792\n\t"
        "s_waitcnt lgkmcnt(6)\n\t"
        "v_mfma_f32_16x16x32_bf16 v[44:47], v[208:211], v[60:63], v[44:47]\n\t"
        "buffer_load_dwordx4 v100, %[sx], %[t0] offen lds\n\t"
        "v_mfma_f32_16x16x32_bf16 v[48:51], v[212:215], v[60:63], v[48:51]\n\t"
        "v_mfma_f32_16x16x32_bf16 v[52:55], v[60:63], v[216:219], v[52:55]\n\t"
        "s_waitcnt lgkmcnt(5)\n\t"
        "v_mfma_f32_16x16x32_bf16 v[32:35], v[220:223], v[64:67], v[32:35]\n\t"
        "s_waitcnt vmcnt(17) lgkmcnt(0)\n\t"
        "s_barrier\n\t"
        "v_mfma_f32_16x16x32_bf16 v[36:39], v[224:227], v[64:67], v[36:39]\n\t"
        "ds_read_b128 v[56:59], v105 offset:0\n\t"
        "v_mfma_f32_16x16x32_bf16 v[40:43], v[64:67], v[228:231], v[40:43]\n\t"
        "ds_read_b128 v[60:63], v105 offset:2048\n\t"
        "s_add_u32 m0, %[ld], 98304\n\t"
        "s_add_u32 %[t0], %[xo], 1536\n\t"
        "v_mfma_f32_16x16x32_bf16 v[44:47], v[220:223], v[68:71], v[44:47]\n\t"
        "buffer_load_dwordx4 v100, %[sx], %[t0] offen lds\n\t"
        "v_mfma_f32_16x16x32_bf16 v[48:51], v[224:227], v[68:71], v[48:51]\n\t"
        "ds_read_b128 v[64:67], v106 offset:0\n\t"
        "v_mfma_f32_16x16x32_bf16 v[52:55], v[68:71], v[228:231], v[52:55]\n\t"
        "v_mfma_f32_16x16x32_bf16 v[32:35], v[232:235], v[72:75], v[32:35]\n\t"
        "v_mfma_f32_16x16x32_bf16 v[36:39], v[236:239], v[72:75], v[36:39]\n\t"
        "ds_read_b128 v[68:71], v106 offset:2048\n\t"
        "v_mfma_f32_16x16x32_bf16 v[40:43], v[72:75], v[240:243], v[40:43]\n\t"
        "s_add_u32 m0, %[ld], 99328\n\t"
        "s_add_u32 %[t0], %[xo], 17920\n\t"
        "v_mfma_f32_16x16x32_bf16 v[44:47], v[232:235], v[76:79], v[44:47]\n\t"
        "buffer_load_dwordx4 v100, %[sx], %[t0] offen lds\n\t"
        "v_mfma_f32_16x16x32_bf16 v[48:51], v[236:239], v[76:79], v[48:51]\n\t"
        "ds_read_b128 v[72:75], v105 offset:8192\n\t"
        "v_mfma_f32_16x16x32_bf16 v[52:55], v[76:79], v[240:243], v[52:55]\n\t"
        "v_mfma_f32_16x16x32_bf16 v[32:35], v[244:247], v[80:83], v[32:35]\n\t"
        "v_mfma_f32_16x16x32_bf16 v[36:39], v[248:251], v[80:83], v[36:39]\n\t"
        "ds_read_b128 v[76:79], v105 offset:10240\n\t"
        "v_mfma_f32_16x16x32_bf16 v[40:43], v[80:83], v[252:255], v[40:43]\n\t"
        "s_add_u32 m0, %[ld], 106496\n\t"
        "s_add_u32 %[t0], %[xo], 1664\n\t"
        "v_mfma_f32_16x16x32_bf16 v[44:47], v[244:247], v[84:87], v[44:47]\n\t"
        "buffer_load_dwordx4 v100, %[sx], %[t0] offen lds\n\t"
        "v_mfma_f32_16x16x32_bf16 v[48:51], v[248:251], v[84:87], v[48:51]\n\t"
        "ds_read_b128 v[80:83], v106 offset:8192\n\t"
        "v_mfma_f32_16x16x32_bf16 v[52:55], v[84:87], v[252:255], v[52:55]\n\t"
        "s_cmp_lt_u32 %[cn], 32\n\t"
        "s_cbranch_scc1 LG_loop%=\n\t"
        "s_nop 7\n\t"
        "v_cvt_pk_bf16_f32 v88, v32, v33\n\t"
        "v_cvt_pk_bf16_f32 v89, v34, v35\n\t"
        "v_cvt_pk_bf16_f32 v90, v36, v37\n\t"
        "v_cvt_pk_bf16_f32 v91, v38, v39\n\t"
        "buffer_store_dwordx4 v[88:91], v109, %[sw], %[so] offen sc1\n\t"
        "v_cvt_pk_bf16_f32 v92, v44, v45\n\t"
        "v_cvt_pk_bf16_f32 v93, v46, v47\n\t"
        "v_cvt_pk_bf16_f32 v94, v48, v49\n\t"
        "v_cvt_pk_bf16_f32 v95, v50, v51\n\t"
        "buffer_store_dwordx4 v[92:95], v110, %[sw], %[so] offen sc1\n\t"
        "v_cvt_pk_bf16_f32 v96, v40, v41\n\t"
        "v_cvt_pk_bf16_f32 v97, v42, v43\n\t"
        "v_cvt_pk_bf16_f32 v98, v52, v53\n\t"
        "v_cvt_pk_bf16_f32 v99, v54, v55\n\t"
        "buffer_store_dwordx4 v[96:99], v111, %[sw], %[so] offen sc1\n\t"
        "s_waitcnt vmcnt(0) lgkmcnt(0)\n\t"
        "v_mov_b32_e32 v117, %[cn]\n\t"
        "buffer_store_dword v117, v113, %[sw], 0 offen sc1\n\t"
        "s_barrier\n\t"
        "LG_end%=:\n\t"
        ".Lk_end_G:\n\t"
        : [t0] "=&s"(t0), [xo] "=&s"(xo), [so] "=&s"(so), [cn] "=&s"(cn), [ts] "=&s"(ts), [ta] "=&s"(ta), [tb] "=&s"(tb), [tc] "=&s"(tc), [td] "=&s"(td)
        : [sx] "s"(sx), [sq] "s"(sq), [sv] "s"(sv), [sw] "s"(sw), [ld] "s"(lb + w * 2048),
          "{v100}"((16 * w + (l >> 3)) * 2048 + (((l & 7) ^ (l >> 3)) << 4)), "{v101}"((8 * w + (l >> 3)) * 8192 + h * 256 + (c & 1) * 128 + (l & 7) * 16),
          "{v112}"((16 * w + (l >> 2)) * 8192 + h * 256 + cv * 64 + (l & 3) * 16), "{v102}"(lb + 131072 + w * 1024 + ((l * 16) ^ ((l & 16) << 2))), "{v116}"(lb + 131072 + w * 1024 + ((l * 16) ^ (l & 32))),
          "{v103}"(lr + rr * 128 + ((tn ^ (n >> 3)) * 64) + (n & 3) * 16), "{v104}"(lr + 8192 + rr * 64 + ((tn ^ (g & 1)) * 32) + (n & 3) * 8), "{v105}"(lx), "{v106}"(lx ^ 64u), "{v107}"(lx + 65536), "{v108}"((lx ^ 64u) + 65536),
          "{v109}"(aq), "{v110}"(aq + 4096), "{v111}"(W_VF + (tm * 32 + h) * 8192 + (2 * cv + tn) * 1024 + l * 16), "{v113}"(l == 0 ? F_KVQ + h * 1024 + (c * 4 + w) * 64 : OOB), "{v[114:115]}"(tp)
        : "memory", "scc", "m0", "v8", "v9", "v10", "v11", "v12", "v13", "v14", "v15", "v16", "v17", "v18", "v19", "v20", "v21", "v22", "v23", "v24", "v25", "v26", "v27", "v28", "v29", "v30", "v31", "v32", "v33", "v34", "v35", "v36", "v37", "v38", "v39", "v40", "v41", "v42", "v43", "v44", "v45", "v46", "v47", "v48", "v49", "v50", "v51", "v52", "v53", "v54", "v55", "v56", "v57", "v58", "v59", "v60", "v61", "v62", "v63", "v64", "v65", "v66", "v67", "v68", "v69", "v70", "v71", "v72", "v73", "v74", "v75", "v76", "v77", "v78", "v79", "v80", "v81", "v82", "v83", "v84", "v85", "v86", "v87", "v88", "v89", "v90", "v91", "v92", "v93", "v94", "v95", "v96", "v97", "v98", "v99", "v117", "v118", "v119", "v120", "v121", "v122", "v123", "v124", "v125", "v126", "v127", "v128", "v129", "v130", "v131", "v132", "v133", "v134", "v135", "v136", "v137", "v138", "v139", "v140", "v141", "v142", "v143", "v144", "v145", "v146", "v147", "v148", "v149", "v150", "v151", "v152", "v153", "v154", "v155", "v156", "v157", "v158", "v159", "v160", "v161", "v162", "v163", "v164", "v165", "v166", "v167", "v168", "v169", "v170", "v171", "v172", "v173", "v174", "v175", "v176", "v177", "v178", "v179", "v180", "v181", "v182", "v183", "v184", "v185", "v186", "v187", "v188", "v189", "v190", "v191", "v192", "v193", "v194", "v195", "v196", "v197", "v198", "v199", "v200", "v201", "v202", "v203", "v204", "v205", "v206", "v207", "v208", "v209", "v210", "v211", "v212", "v213", "v214", "v215", "v216", "v217", "v218", "v219", "v220", "v221", "v222", "v223", "v224", "v225", "v226", "v227", "v228", "v229", "v230", "v231", "v232", "v233", "v234", "v235", "v236", "v237", "v238", "v239", "v240", "v241", "v242", "v243", "v244", "v245", "v246", "v247", "v248", "v249", "v250", "v251", "v252", "v253", "v254", "v255", "a0", "a1", "a2", "a3", "a4", "a5", "a6", "a7", "a8", "a9", "a10", "a11", "a12", "a13", "a14", "a15", "a16", "a17", "a18", "a19", "a20", "a21", "a22", "a23", "a24", "a25", "a26", "a27", "a28", "a29", "a30", "a31", "a32", "a33", "a34", "a35", "a36", "a37", "a38", "a39", "a40", "a41", "a42", "a43", "a44", "a45", "a46", "a47", "a48", "a49", "a50", "a51", "a52", "a53", "a54", "a55", "a56", "a57", "a58", "a59", "a60", "a61", "a62", "a63", "a64", "a65", "a66", "a67", "a68", "a69", "a70", "a71", "a72", "a73", "a74", "a75", "a76", "a77", "a78", "a79", "a80", "a81", "a82", "a83", "a84", "a85", "a86", "a87", "a88", "a89", "a90", "a91", "a92", "a93", "a94", "a95", "a96", "a97", "a98", "a99", "a100", "a101", "a102", "a103", "a104", "a105", "a106", "a107", "a108", "a109", "a110", "a111", "a112", "a113", "a114", "a115", "a116", "a117", "a118", "a119", "a120", "a121", "a122", "a123", "a124", "a125", "a126", "a127", "a128", "a129", "a130", "a131", "a132", "a133", "a134", "a135", "a136", "a137", "a138", "a139", "a140", "a141", "a142", "a143", "a144", "a145", "a146", "a147", "a148", "a149", "a150", "a151", "a152", "a153", "a154", "a155", "a156", "a157", "a158", "a159", "a160", "a161", "a162", "a163", "a164", "a165", "a166", "a167", "a168", "a169", "a170", "a171", "a172", "a173", "a174", "a175", "a176", "a177", "a178", "a179", "a180", "a181", "a182", "a183", "a184", "a185", "a186", "a187", "a188", "a189", "a190", "a191", "a192", "a193", "a194", "a195", "a196", "a197", "a198", "a199", "a200", "a201", "a202", "a203", "a204", "a205", "a206", "a207", "a208", "a209", "a210", "a211", "a212", "a213", "a214", "a215", "a216", "a217", "a218", "a219", "a220", "a221", "a222", "a223", "a224", "a225", "a226", "a227", "a228", "a229", "a230", "a231", "a232", "a233", "a234", "a235", "a236", "a237", "a238", "a239", "a240", "a241", "a242", "a243", "a244", "a245", "a246", "a247", "a248", "a249", "a250", "a251", "a252", "a253", "a254", "a255");
}

__device__ __forceinline__ void out_block(const Params& p, unsigned char* lds, const int wv, const int rp, const int nt, const int j) {
    const int w = wv, l = otid() & 63, n = l & 15, g = l >> 4;
    const unsigned lb = __builtin_amdgcn_readfirstlane((unsigned)(size_t)lds), stg = lb + 131072 + w * 8192;
    const v4u so = mk_srd(p.Wo, 1024 * 4096 * 2), sw = mk_srd(p.ws, W_TOTAL), su = mk_srd(p.out, S * D * 2);
    const unsigned lr = stg + (4 * g + (n >> 2)) * 128 + ((2 * (g & 1) + (n >> 3)) << 5) + (n & 3) * 8;
    unsigned t0, t1, tl = rp, pg, cu, nx, sp; unsigned long long tu;
    asm volatile(
        "s_add_u32 %[t1], %[ro], 0\n\t"
        "s_and_b32 %[t1], %[t1], 7\n\t"
        "s_lshl_b32 %[t1], %[t1], 18\n\t"
        "s_add_u32 %[t0], %[t1], 0\n\t"
        "buffer_load_dwordx4 a[0:3], v110, %[so], %[t0] offen\n\t"
        "s_sleep 2\n\t"
        "s_add_u32 %[t0], %[t1], 16384\n\t"
        "buffer_load_dwordx4 a[4:7], v110, %[so], %[t0] offen\n\t"
        "s_sleep 2\n\t"
        "s_add_u32 %[t0], %[t1], 32768\n\t"
        "buffer_load_dwordx4 a[8:11], v110, %[so], %[t0] offen\n\t"
        "s_sleep 2\n\t"
        "s_add_u32 %[t0], %[t1], 49152\n\t"
        "buffer_load_dwordx4 a[12:15], v110, %[so], %[t0] offen\n\t"
        "s_sleep 2\n\t"
        "s_add_u32 %[t0], %[t1], 65536\n\t"
        "buffer_load_dwordx4 a[16:19], v110, %[so], %[t0] offen\n\t"
        "s_sleep 2\n\t"
        "s_add_u32 %[t0], %[t1], 81920\n\t"
        "buffer_load_dwordx4 a[20:23], v110, %[so], %[t0] offen\n\t"
        "s_sleep 2\n\t"
        "s_add_u32 %[t0], %[t1], 98304\n\t"
        "buffer_load_dwordx4 v[24:27], v110, %[so], %[t0] offen\n\t"
        "s_sleep 2\n\t"
        "s_add_u32 %[t0], %[t1], 114688\n\t"
        "buffer_load_dwordx4 v[28:31], v110, %[so], %[t0] offen\n\t"
        "s_sleep 2\n\t"
        "s_add_u32 %[t1], %[ro], 0\n\t"
        "s_and_b32 %[t1], %[t1], 7\n\t"
        "s_lshl_b32 %[t1], %[t1], 18\n\t"
        "s_add_u32 %[t0], %[t1], 131072\n\t"
        "buffer_load_dwordx4 a[24:27], v110, %[so], %[t0] offen\n\t"
        "s_sleep 2\n\t"
        "s_add_u32 %[t0], %[t1], 147456\n\t"
        "buffer_load_dwordx4 a[28:31], v110, %[so], %[t0] offen\n\t"
        "s_sleep 2\n\t"
        "s_add_u32 %[t0], %[t1], 163840\n\t"
        "buffer_load_dwordx4 a[32:35], v110, %[so], %[t0] offen\n\t"
        "s_sleep 2\n\t"
        "s_add_u32 %[t0], %[t1], 180224\n\t"
        "buffer_load_dwordx4 a[36:39], v110, %[so], %[t0] offen\n\t"
        "s_sleep 2\n\t"
        "s_add_u32 %[t0], %[t1], 196608\n\t"
        "buffer_load_dwordx4 a[40:43], v110, %[so], %[t0] offen\n\t"
        "s_sleep 2\n\t"
        "s_add_u32 %[t0], %[t1], 212992\n\t"
        "buffer_load_dwordx4 a[44:47], v110, %[so], %[t0] offen\n\t"
        "s_sleep 2\n\t"
        "s_add_u32 %[t0], %[t1], 229376\n\t"
        "buffer_load_dwordx4 v[32:35], v110, %[so], %[t0] offen\n\t"
        "s_sleep 2\n\t"
        "s_add_u32 %[t0], %[t1], 245760\n\t"
        "buffer_load_dwordx4 v[36:39], v110, %[so], %[t0] offen\n\t"
        "s_sleep 2\n\t"
        "s_waitcnt vmcnt(8)\n\t"
        "ds_write_b128 v111, a[0:3] offset:0\n\t"
        "ds_write_b128 v111, a[4:7] offset:1024\n\t"
        "ds_write_b128 v111, a[8:11] offset:2048\n\t"
        "ds_write_b128 v111, a[12:15] offset:3072\n\t"
        "ds_write_b128 v111, a[16:19] offset:4096\n\t"
        "ds_write_b128 v111, a[20:23] offset:5120\n\t"
        "ds_write_b128 v111, v[24:27] offset:6144\n\t"
        "ds_write_b128 v111, v[28:31] offset:7168\n\t"
        "ds_read_b64_tr_b16 a[0:1], v112 offset:0\n\t"
        "ds_read_b64_tr_b16 a[2:3], v112 offset:2048\n\t"
        "ds_read_b64_tr_b16 a[12:13], v112 offset:4096\n\t"
        "ds_read_b64_tr_b16 a[14:15], v112 offset:6144\n\t"
        "s_waitcnt lgkmcnt(8)\n\t"
        "ds_read_b64_tr_b16 a[4:5], v113 offset:0\n\t"
        "ds_read_b64_tr_b16 a[6:7], v113 offset:2048\n\t"
        "ds_read_b64_tr_b16 a[16:17], v113 offset:4096\n\t"
        "ds_read_b64_tr_b16 a[18:19], v113 offset:6144\n\t"
        "s_waitcnt lgkmcnt(8)\n\t"
        "ds_read_b64_tr_b16 a[8:9], v114 offset:0\n\t"
        "ds_read_b64_tr_b16 a[10:11], v114 offset:2048\n\t"
        "ds_read_b64_tr_b16 a[20:21], v114 offset:4096\n\t"
        "ds_read_b64_tr_b16 a[22:23], v114 offset:6144\n\t"
        "s_waitcnt lgkmcnt(8)\n\t"
        "ds_read_b64_tr_b16 v[56:57], v115 offset:0\n\t"
        "ds_read_b64_tr_b16 v[58:59], v115 offset:2048\n\t"
        "ds_read_b64_tr_b16 v[60:61], v115 offset:4096\n\t"
        "ds_read_b64_tr_b16 v[62:63], v115 offset:6144\n\t"
        "s_add_u32 %[t1], %[ro], 1\n\t"
        "s_and_b32 %[t1], %[t1], 7\n\t"
        "s_lshl_b32 %[t1], %[t1], 18\n\t"
        "s_add_u32 %[t0], %[t1], 0\n\t"
        "buffer_load_dwordx4 a[48:51], v110, %[so], %[t0] offen\n\t"
        "s_sleep 2\n\t"
        "s_add_u32 %[t0], %[t1], 16384\n\t"
        "buffer_load_dwordx4 a[52:55], v110, %[so], %[t0] offen\n\t"
        "s_sleep 2\n\t"
        "s_add_u32 %[t0], %[t1], 32768\n\t"
        "buffer_load_dwordx4 a[56:59], v110, %[so], %[t0] offen\n\t"
        "s_sleep 2\n\t"
        "s_add_u32 %[t0], %[t1], 49152\n\t"
        "buffer_load_dwordx4 a[60:63], v110, %[so], %[t0] offen\n\t"
        "s_sleep 2\n\t"
        "s_add_u32 %[t0], %[t1], 65536\n\t"
        "buffer_load_dwordx4 a[64:67], v110, %[so], %[t0] offen\n\t"
        "s_sleep 2\n\t"
        "s_add_u32 %[t0], %[t1], 81920\n\t"
        "buffer_load_dwordx4 a[68:71], v110, %[so], %[t0] offen\n\t"
        "s_sleep 2\n\t"
        "s_add_u32 %[t0], %[t1], 98304\n\t"
        "buffer_load_dwordx4 v[40:43], v110, %[so], %[t0] offen\n\t"
        "s_sleep 2\n\t"
        "s_add_u32 %[t0], %[t1], 114688\n\t"
        "buffer_load_dwordx4 v[44:47], v110, %[so], %[t0] offen\n\t"
        "s_sleep 2\n\t"
        "s_waitcnt lgkmcnt(0)\n\t"
        "ds_write_b128 v116, v[56:59] offset:0\n\t"
        "ds_write_b128 v116, v[60:63] offset:1024\n\t"
        "s_waitcnt vmcnt(8)\n\t"
        "ds_write_b128 v111, a[24:27] offset:0\n\t"
        "ds_write_b128 v111, a[28:31] offset:1024\n\t"
        "ds_write_b128 v111, a[32:35] offset:2048\n\t"
        "ds_write_b128 v111, a[36:39] offset:3072\n\t"
        "ds_write_b128 v111, a[40:43] offset:4096\n\t"
        "ds_write_b128 v111, a[44:47] offset:5120\n\t"
        "ds_write_b128 v111, v[32:35] offset:6144\n\t"
        "ds_write_b128 v111, v[36:39] offset:7168\n\t"
        "ds_read_b64_tr_b16 a[24:25], v112 offset:0\n\t"
        "ds_read_b64_tr_b16 a[26:27], v112 offset:2048\n\t"
        "ds_read_b64_tr_b16 a[36:37], v112 offset:4096\n\t"
        "ds_read_b64_tr_b16 a[38:39], v112 offset:6144\n\t"
        "s_waitcnt lgkmcnt(8)\n\t"
        "ds_read_b64_tr_b16 a[28:29], v113 offset:0\n\t"
        "ds_read_b64_tr_b16 a[30:31], v113 offset:2048\n\t"
        "ds_read_b64_tr_b16 a[40:41], v113 offset:4096\n\t"
        "ds_read_b64_tr_b16 a[42:43], v113 offset:6144\n\t"
        "s_waitcnt lgkmcnt(8)\n\t"
        "ds_read_b64_tr_b16 a[32:33], v114 offset:0\n\t"
        "ds_read_b64_tr_b16 a[34:35], v114 offset:2048\n\t"
        "ds_read_b64_tr_b16 a[44:45], v114 offset:4096\n\t"
        "ds_read_b64_tr_b16 a[46:47], v114 offset:6144\n\t"
        "s_waitcnt lgkmcnt(8)\n\t"
        "ds_read_b64_tr_b16 v[56:57], v115 offset:0\n\t"
        "ds_read_b64_tr_b16 v[58:59], v115 offset:2048\n\t"
        "ds_read_b64_tr_b16 v[60:61], v115 offset:4096\n\t"
        "ds_read_b64_tr_b16 v[62:63], v115 offset:6144\n\t"
        "s_add_u32 %[t1], %[ro], 1\n\t"
        "s_and_b32 %[t1], %[t1], 7\n\t"
        "s_lshl_b32 %[t1], %[t1], 18\n\t"
        "s_add_u32 %[t0], %[t1], 131072\n\t"
        "buffer_load_dwordx4 a[72:75], v110, %[so], %[t0] offen\n\t"
        "s_sleep 2\n\t"
        "s_add_u32 %[t0], %[t1], 147456\n\t"
        "buffer_load_dwordx4 a[76:79], v110, %[so], %[t0] offen\n\t"
        "s_sleep 2\n\t"
        "s_add_u32 %[t0], %[t1], 163840\n\t"
        "buffer_load_dwordx4 a[80:83], v110, %[so], %[t0] offen\n\t"
        "s_sleep 2\n\t"
        "s_add_u32 %[t0], %[t1], 180224\n\t"
        "buffer_load_dwordx4 a[84:87], v110, %[so], %[t0] offen\n\t"
        "s_sleep 2\n\t"
        "s_add_u32 %[t0], %[t1], 196608\n\t"
        "buffer_load_dwordx4 a[88:91], v110, %[so], %[t0] offen\n\t"
        "s_sleep 2\n\t"
        "s_add_u32 %[t0], %[t1], 212992\n\t"
        "buffer_load_dwordx4 a[92:95], v110, %[so], %[t0] offen\n\t"
        "s_sleep 2\n\t"
        "s_add_u32 %[t0], %[t1], 229376\n\t"
        "buffer_load_dwordx4 v[48:51], v110, %[so], %[t0] offen\n\t"
        "s_sleep 2\n\t"
        "s_add_u32 %[t0], %[t1], 245760\n\t"
        "buffer_load_dwordx4 v[52:55], v110, %[so], %[t0] offen\n\t"
        "s_sleep 2\n\t"
        "s_waitcnt lgkmcnt(0)\n\t"
        "ds_write_b128 v116, v[56:59] offset:2048\n\t"
        "ds_write_b128 v116, v[60:63] offset:3072\n\t"
        "s_waitcnt vmcnt(8)\n\t"
        "ds_write_b128 v111, a[48:51] offset:0\n\t"
        "ds_write_b128 v111, a[52:55] offset:1024\n\t"
        "ds_write_b128 v111, a[56:59] offset:2048\n\t"
        "ds_write_b128 v111, a[60:63] offset:3072\n\t"
        "ds_write_b128 v111, a[64:67] offset:4096\n\t"
        "ds_write_b128 v111, a[68:71] offset:5120\n\t"
        "ds_write_b128 v111, v[40:43] offset:6144\n\t"
        "ds_write_b128 v111, v[44:47] offset:7168\n\t"
        "ds_read_b64_tr_b16 a[48:49], v112 offset:0\n\t"
        "ds_read_b64_tr_b16 a[50:51], v112 offset:2048\n\t"
        "ds_read_b64_tr_b16 a[60:61], v112 offset:4096\n\t"
        "ds_read_b64_tr_b16 a[62:63], v112 offset:6144\n\t"
        "s_waitcnt lgkmcnt(8)\n\t"
        "ds_read_b64_tr_b16 a[52:53], v113 offset:0\n\t"
        "ds_read_b64_tr_b16 a[54:55], v113 offset:2048\n\t"
        "ds_read_b64_tr_b16 a[64:65], v113 offset:4096\n\t"
        "ds_read_b64_tr_b16 a[66:67], v113 offset:6144\n\t"
        "s_waitcnt lgkmcnt(8)\n\t"
        "ds_read_b64_tr_b16 a[56:57], v114 offset:0\n\t"
        "ds_read_b64_tr_b16 a[58:59], v114 offset:2048\n\t"
        "ds_read_b64_tr_b16 a[68:69], v114 offset:4096\n\t"
        "ds_read_b64_tr_b16 a[70:71], v114 offset:6144\n\t"
        "s_waitcnt lgkmcnt(8)\n\t"
        "ds_read_b64_tr_b16 v[56:57], v115 offset:0\n\t"
        "ds_read_b64_tr_b16 v[58:59], v115 offset:2048\n\t"
        "ds_read_b64_tr_b16 v[60:61], v115 offset:4096\n\t"
        "ds_read_b64_tr_b16 v[62:63], v115 offset:6144\n\t"
        "s_add_u32 %[t1], %[ro], 2\n\t"
        "s_and_b32 %[t1], %[t1], 7\n\t"
        "s_lshl_b32 %[t1], %[t1], 18\n\t"
        "s_add_u32 %[t0], %[t1], 0\n\t"
        "buffer_load_dwordx4 a[96:99], v110, %[so], %[t0] offen\n\t"
        "s_sleep 2\n\t"
        "s_add_u32 %[t0], %[t1], 16384\n\t"
        "buffer_load_dwordx4 a[100:103], v110, %[so], %[t0] offen\n\t"
        "s_sleep 2\n\t"
        "s_add_u32 %[t0], %[t1], 32768\n\t"
        "buffer_load_dwordx4 a[104:107], v110, %[so], %[t0] offen\n\t"
        "s_sleep 2\n\t"
        "s_add_u32 %[t0], %[t1], 49152\n\t"
        "buffer_load_dwordx4 a[108:111], v110, %[so], %[t0] offen\n\t"
        "s_sleep 2\n\t"
        "s_add_u32 %[t0], %[t1], 65536\n\t"
        "buffer_load_dwordx4 a[112:115], v110, %[so], %[t0] offen\n\t"
        "s_sleep 2\n\t"
        "s_add_u32 %[t0], %[t1], 81920\n\t"
        "buffer_load_dwordx4 a[116:119], v110, %[so], %[t0] offen\n\t"
        "s_sleep 2\n\t"
        "s_add_u32 %[t0], %[t1], 98304\n\t"
        "buffer_load_dwordx4 v[24:27], v110, %[so], %[t0] offen\n\t"
        "s_sleep 2\n\t"
        "s_add_u32 %[t0], %[t1], 114688\n\t"
        "buffer_load_dwordx4 v[28:31], v110, %[so], %[t0] offen\n\t"
        "s_sleep 2\n\t"
        "s_waitcnt lgkmcnt(0)\n\t"
        "ds_write_b128 v116, v[56:59] offset:4096\n\t"
        "ds_write_b128 v116, v[60:63] offset:5120\n\t"
        "s_waitcnt vmcnt(8)\n\t"
        "ds_write_b128 v111, a[72:75] offset:0\n\t"
        "ds_write_b128 v111, a[76:79] offset:1024\n\t"
        "ds_write_b128 v111, a[80:83] offset:2048\n\t"
        "ds_write_b128 v111, a[84:87] offset:3072\n\t"
        "ds_write_b128 v111, a[88:91] offset:4096\n\t"
        "ds_write_b128 v111, a[92:95] offset:5120\n\t"
        "ds_write_b128 v111, v[48:51] offset:6144\n\t"
        "ds_write_b128 v111, v[52:55] offset:7168\n\t"
        "ds_read_b64_tr_b16 a[72:73], v112 offset:0\n\t"
        "ds_read_b64_tr_b16 a[74:75], v112 offset:2048\n\t"
        "ds_read_b64_tr_b16 a[84:85], v112 offset:4096\n\t"
        "ds_read_b64_tr_b16 a[86:87], v112 offset:6144\n\t"
        "s_waitcnt lgkmcnt(8)\n\t"
        "ds_read_b64_tr_b16 a[76:77], v113 offset:0\n\t"
        "ds_read_b64_tr_b16 a[78:79], v113 offset:2048\n\t"
        "ds_read_b64_tr_b16 a[88:89], v113 offset:4096\n\t"
        "ds_read_b64_tr_b16 a[90:91], v113 offset:6144\n\t"
        "s_waitcnt lgkmcnt(8)\n\t"
        "ds_read_b64_tr_b16 a[80:81], v114 offset:0\n\t"
        "ds_read_b64_tr_b16 a[82:83], v114 offset:2048\n\t"
        "ds_read_b64_tr_b16 a[92:93], v114 offset:4096\n\t"
        "ds_read_b64_tr_b16 a[94:95], v114 offset:6144\n\t"
        "s_waitcnt lgkmcnt(8)\n\t"
        "ds_read_b64_tr_b16 v[56:57], v115 offset:0\n\t"
        "ds_read_b64_tr_b16 v[58:59], v115 offset:2048\n\t"
        "ds_read_b64_tr_b16 v[60:61], v115 offset:4096\n\t"
        "ds_read_b64_tr_b16 v[62:63], v115 offset:6144\n\t"
        "s_add_u32 %[t1], %[ro], 2\n\t"
        "s_and_b32 %[t1], %[t1], 7\n\t"
        "s_lshl_b32 %[t1], %[t1], 18\n\t"
        "s_add_u32 %[t0], %[t1], 131072\n\t"
        "buffer_load_dwordx4 a[120:123], v110, %[so], %[t0] offen\n\t"
        "s_sleep 2\n\t"
        "s_add_u32 %[t0], %[t1], 147456\n\t"
        "buffer_load_dwordx4 a[124:127], v110, %[so], %[t0] offen\n\t"
        "s_sleep 2\n\t"
        "s_add_u32 %[t0], %[t1], 163840\n\t"
        "buffer_load_dwordx4 a[128:131], v110, %[so], %[t0] offen\n\t"
        "s_sleep 2\n\t"
        "s_add_u32 %[t0], %[t1], 180224\n\t"
        "buffer_load_dwordx4 a[132:135], v110, %[so], %[t0] offen\n\t"
        "s_sleep 2\n\t"
        "s_add_u32 %[t0], %[t1], 196608\n\t"
        "buffer_load_dwordx4 a[136:139], v110, %[so], %[t0] offen\n\t"
        "s_sleep 2\n\t"
        "s_add_u32 %[t0], %[t1], 212992\n\t"
        "buffer_load_dwordx4 a[140:143], v110, %[so], %[t0] offen\n\t"
        "s_sleep 2\n\t"
        "s_add_u32 %[t0], %[t1], 229376\n\t"
        "buffer_load_dwordx4 v[32:35], v110, %[so], %[t0] offen\n\t"
        "s_sleep 2\n\t"
        "s_add_u32 %[t0], %[t1], 245760\n\t"
        "buffer_load_dwordx4 v[36:39], v110, %[so], %[t0] offen\n\t"
        "s_sleep 2\n\t"
        "s_waitcnt lgkmcnt(0)\n\t"
        "ds_write_b128 v116, v[56:59] offset:6144\n\t"
        "ds_write_b128 v116, v[60:63] offset:7168\n\t"
        "s_waitcnt vmcnt(8)\n\t"
        "ds_write_b128 v111, a[96:99] offset:0\n\t"
        "ds_write_b128 v111, a[100:103] offset:1024\n\t"
        "ds_write_b128 v111, a[104:107] offset:2048\n\t"
        "ds_write_b128 v111, a[108:111] offset:3072\n\t"
        "ds_write_b128 v111, a[112:115] offset:4096\n\t"
        "ds_write_b128 v111, a[116:119] offset:5120\n\t"
        "ds_write_b128 v111, v[24:27] offset:6144\n\t"
        "ds_write_b128 v111, v[28:31] offset:7168\n\t"
        "ds_read_b64_tr_b16 a[96:97], v112 offset:0\n\t"
        "ds_read_b64_tr_b16 a[98:99], v112 offset:2048\n\t"
        "ds_read_b64_tr_b16 a[108:109], v112 offset:4096\n\t"
        "ds_read_b64_tr_b16 a[110:111], v112 offset:6144\n\t"
        "s_waitcnt lgkmcnt(8)\n\t"
        "ds_read_b64_tr_b16 a[100:101], v113 offset:0\n\t"
        "ds_read_b64_tr_b16 a[102:103], v113 offset:2048\n\t"
        "ds_read_b64_tr_b16 a[112:113], v113 offset:4096\n\t"
        "ds_read_b64_tr_b16 a[114:115], v113 offset:6144\n\t"
        "s_waitcnt lgkmcnt(8)\n\t"
        "ds_read_b64_tr_b16 a[104:105], v114 offset:0\n\t"
        "ds_read_b64_tr_b16 a[106:107], v114 offset:2048\n\t"
        "ds_read_b64_tr_b16 a[116:117], v114 offset:4096\n\t"
        "ds_read_b64_tr_b16 a[118:119], v114 offset:6144\n\t"
        "s_waitcnt lgkmcnt(8)\n\t"
        "ds_read_b64_tr_b16 v[56:57], v115 offset:0\n\t"
        "ds_read_b64_tr_b16 v[58:59], v115 offset:2048\n\t"
        "ds_read_b64_tr_b16 v[60:61], v115 offset:4096\n\t"
        "ds_read_b64_tr_b16 v[62:63], v115 offset:6144\n\t"
        "s_add_u32 %[t1], %[ro], 3\n\t"
        "s_and_b32 %[t1], %[t1], 7\n\t"
        "s_lshl_b32 %[t1], %[t1], 18\n\t"
        "s_add_u32 %[t0], %[t1], 0\n\t"
        "buffer_load_dwordx4 a[144:147], v110, %[so], %[t0] offen\n\t"
        "s_sleep 2\n\t"
        "s_add_u32 %[t0], %[t1], 16384\n\t"
        "buffer_load_dwordx4 a[148:151], v110, %[so], %[t0] offen\n\t"
        "s_sleep 2\n\t"
        "s_add_u32 %[t0], %[t1], 32768\n\t"
        "buffer_load_dwordx4 a[152:155], v110, %[so], %[t0] offen\n\t"
        "s_sleep 2\n\t"
        "s_add_u32 %[t0], %[t1], 49152\n\t"
        "buffer_load_dwordx4 a[156:159], v110, %[so], %[t0] offen\n\t"
        "s_sleep 2\n\t"
        "s_add_u32 %[t0], %[t1], 65536\n\t"
        "buffer_load_dwordx4 a[160:163], v110, %[so], %[t0] offen\n\t"
        "s_sleep 2\n\t"
        "s_add_u32 %[t0], %[t1], 81920\n\t"
        "buffer_load_dwordx4 a[164:167], v110, %[so], %[t0] offen\n\t"
        "s_sleep 2\n\t"
        "s_add_u32 %[t0], %[t1], 98304\n\t"
        "buffer_load_dwordx4 v[40:43], v110, %[so], %[t0] offen\n\t"
        "s_sleep 2\n\t"
        "s_add_u32 %[t0], %[t1], 114688\n\t"
        "buffer_load_dwordx4 v[44:47], v110, %[so], %[t0] offen\n\t"
        "s_sleep 2\n\t"
        "s_waitcnt lgkmcnt(0)\n\t"
        "ds_write_b128 v116, v[56:59] offset:8192\n\t"
        "ds_write_b128 v116, v[60:63] offset:9216\n\t"
        "s_waitcnt vmcnt(8)\n\t"
        "ds_write_b128 v111, a[120:123] offset:0\n\t"
        "ds_write_b128 v111, a[124:127] offset:1024\n\t"
        "ds_write_b128 v111, a[128:131] offset:2048\n\t"
        "ds_write_b128 v111, a[132:135] offset:3072\n\t"
        "ds_write_b128 v111, a[136:139] offset:4096\n\t"
        "ds_write_b128 v111, a[140:143] offset:5120\n\t"
        "ds_write_b128 v111, v[32:35] offset:6144\n\t"
        "ds_write_b128 v111, v[36:39] offset:7168\n\t"
        "ds_read_b64_tr_b16 a[120:121], v112 offset:0\n\t"
        "ds_read_b64_tr_b16 a[122:123], v112 offset:2048\n\t"
        "ds_read_b64_tr_b16 a[132:133], v112 offset:4096\n\t"
        "ds_read_b64_tr_b16 a[134:135], v112 offset:6144\n\t"
        "s_waitcnt lgkmcnt(8)\n\t"
        "ds_read_b64_tr_b16 a[124:125], v113 offset:0\n\t"
        "ds_read_b64_tr_b16 a[126:127], v113 offset:2048\n\t"
        "ds_read_b64_tr_b16 a[136:137], v113 offset:4096\n\t"
        "ds_read_b64_tr_b16 a[138:139], v113 offset:6144\n\t"
        "s_waitcnt lgkmcnt(8)\n\t"
        "ds_read_b64_tr_b16 a[128:129], v114 offset:0\n\t"
        "ds_read_b64_tr_b16 a[130:131], v114 offset:2048\n\t"
        "ds_read_b64_tr_b16 a[140:141], v114 offset:4096\n\t"
        "ds_read_b64_tr_b16 a[142:143], v114 offset:6144\n\t"
        "s_waitcnt lgkmcnt(8)\n\t"
        "ds_read_b64_tr_b16 v[56:57], v115 offset:0\n\t"
        "ds_read_b64_tr_b16 v[58:59], v115 offset:2048\n\t"
        "ds_read_b64_tr_b16 v[60:61], v115 offset:4096\n\t"
        "ds_read_b64_tr_b16 v[62:63], v115 offset:6144\n\t"
        "s_add_u32 %[t1], %[ro], 3\n\t"
        "s_and_b32 %[t1], %[t1], 7\n\t"
        "s_lshl_b32 %[t1], %[t1], 18\n\t"
        "s_add_u32 %[t0], %[t1], 131072\n\t"
        "buffer_load_dwordx4 a[168:171], v110, %[so], %[t0] offen\n\t"
        "s_sleep 2\n\t"
        "s_add_u32 %[t0], %[t1], 147456\n\t"
        "buffer_load_dwordx4 a[172:175], v110, %[so], %[t0] offen\n\t"
        "s_sleep 2\n\t"
        "s_add_u32 %[t0], %[t1], 163840\n\t"
        "buffer_load_dwordx4 a[176:179], v110, %[so], %[t0] offen\n\t"
        "s_sleep 2\n\t"
        "s_add_u32 %[t0], %[t1], 180224\n\t"
        "buffer_load_dwordx4 a[180:183], v110, %[so], %[t0] offen\n\t"
        "s_sleep 2\n\t"
        "s_add_u32 %[t0], %[t1], 196608\n\t"
        "buffer_load_dwordx4 a[184:187], v110, %[so], %[t0] offen\n\t"
        "s_sleep 2\n\t"
        "s_add_u32 %[t0], %[t1], 212992\n\t"
        "buffer_load_dwordx4 a[188:191], v110, %[so], %[t0] offen\n\t"
        "s_sleep 2\n\t"
        "s_add_u32 %[t0], %[t1], 229376\n\t"
        "buffer_load_dwordx4 v[48:51], v110, %[so], %[t0] offen\n\t"
        "s_sleep 2\n\t"
        "s_add_u32 %[t0], %[t1], 245760\n\t"
        "buffer_load_dwordx4 v[52:55], v110, %[so], %[t0] offen\n\t"
        "s_sleep 2\n\t"
        "s_waitcnt lgkmcnt(0)\n\t"
        "ds_write_b128 v116, v[56:59] offset:10240\n\t"
        "ds_write_b128 v116, v[60:63] offset:11264\n\t"
        "s_waitcnt vmcnt(8)\n\t"
        "ds_write_b128 v111, a[144:147] offset:0\n\t"
        "ds_write_b128 v111, a[148:151] offset:1024\n\t"
        "ds_write_b128 v111, a[152:155] offset:2048\n\t"
        "ds_write_b128 v111, a[156:159] offset:3072\n\t"
        "ds_write_b128 v111, a[160:163] offset:4096\n\t"
        "ds_write_b128 v111, a[164:167] offset:5120\n\t"
        "ds_write_b128 v111, v[40:43] offset:6144\n\t"
        "ds_write_b128 v111, v[44:47] offset:7168\n\t"
        "ds_read_b64_tr_b16 a[144:145], v112 offset:0\n\t"
        "ds_read_b64_tr_b16 a[146:147], v112 offset:2048\n\t"
        "ds_read_b64_tr_b16 a[156:157], v112 offset:4096\n\t"
        "ds_read_b64_tr_b16 a[158:159], v112 offset:6144\n\t"
        "s_waitcnt lgkmcnt(8)\n\t"
        "ds_read_b64_tr_b16 a[148:149], v113 offset:0\n\t"
        "ds_read_b64_tr_b16 a[150:151], v113 offset:2048\n\t"
        "ds_read_b64_tr_b16 a[160:161], v113 offset:4096\n\t"
        "ds_read_b64_tr_b16 a[162:163], v113 offset:6144\n\t"
        "s_waitcnt lgkmcnt(8)\n\t"
        "ds_read_b64_tr_b16 a[152:153], v114 offset:0\n\t"
        "ds_read_b64_tr_b16 a[154:155], v114 offset:2048\n\t"
        "ds_read_b64_tr_b16 a[164:165], v114 offset:4096\n\t"
        "ds_read_b64_tr_b16 a[166:167], v114 offset:6144\n\t"
        "s_waitcnt lgkmcnt(8)\n\t"
        "ds_read_b64_tr_b16 v[56:57], v115 offset:0\n\t"
        "ds_read_b64_tr_b16 v[58:59], v115 offset:2048\n\t"
        "ds_read_b64_tr_b16 v[60:61], v115 offset:4096\n\t"
        "ds_read_b64_tr_b16 v[62:63], v115 offset:6144\n\t"
        "s_add_u32 %[t1], %[ro], 4\n\t"
        "s_and_b32 %[t1], %[t1], 7\n\t"
        "s_lshl_b32 %[t1], %[t1], 18\n\t"
        "s_add_u32 %[t0], %[t1], 0\n\t"
        "buffer_load_dwordx4 a[192:195], v110, %[so], %[t0] offen\n\t"
        "s_sleep 2\n\t"
        "s_add_u32 %[t0], %[t1], 16384\n\t"
        "buffer_load_dwordx4 a[196:199], v110, %[so], %[t0] offen\n\t"
        "s_sleep 2\n\t"
        "s_add_u32 %[t0], %[t1], 32768\n\t"
        "buffer_load_dwordx4 a[200:203], v110, %[so], %[t0] offen\n\t"
        "s_sleep 2\n\t"
        "s_add_u32 %[t0], %[t1], 49152\n\t"
        "buffer_load_dwordx4 a[204:207], v110, %[so], %[t0] offen\n\t"
        "s_sleep 2\n\t"
        "s_add_u32 %[t0], %[t1], 65536\n\t"
        "buffer_load_dwordx4 a[208:211], v110, %[so], %[t0] offen\n\t"
        "s_sleep 2\n\t"
        "s_add_u32 %[t0], %[t1], 81920\n\t"
        "buffer_load_dwordx4 a[212:215], v110, %[so], %[t0] offen\n\t"
        "s_sleep 2\n\t"
        "s_add_u32 %[t0], %[t1], 98304\n\t"
        "buffer_load_dwordx4 v[24:27], v110, %[so], %[t0] offen\n\t"
        "s_sleep 2\n\t"
        "s_add_u32 %[t0], %[t1], 114688\n\t"
        "buffer_load_dwordx4 v[28:31], v110, %[so], %[t0] offen\n\t"
        "s_sleep 2\n\t"
        "s_waitcnt lgkmcnt(0)\n\t"
        "ds_write_b128 v116, v[56:59] offset:12288\n\t"
        "ds_write_b128 v116, v[60:63] offset:13312\n\t"
        "s_waitcnt vmcnt(8)\n\t"
        "ds_write_b128 v111, a[168:171] offset:0\n\t"
        "ds_write_b128 v111, a[172:175] offset:1024\n\t"
        "ds_write_b128 v111, a[176:179] offset:2048\n\t"
        "ds_write_b128 v111, a[180:183] offset:3072\n\t"
        "ds_write_b128 v111, a[184:187] offset:4096\n\t"
        "ds_write_b128 v111, a[188:191] offset:5120\n\t"
        "ds_write_b128 v111, v[48:51] offset:6144\n\t"
        "ds_write_b128 v111, v[52:55] offset:7168\n\t"
        "ds_read_b64_tr_b16 a[168:169], v112 offset:0\n\t"
        "ds_read_b64_tr_b16 a[170:171], v112 offset:2048\n\t"
        "ds_read_b64_tr_b16 a[180:181], v112 offset:4096\n\t"
        "ds_read_b64_tr_b16 a[182:183], v112 offset:6144\n\t"
        "s_waitcnt lgkmcnt(8)\n\t"
        "ds_read_b64_tr_b16 a[172:173], v113 offset:0\n\t"
        "ds_read_b64_tr_b16 a[174:175], v113 offset:2048\n\t"
        "ds_read_b64_tr_b16 a[184:185], v113 offset:4096\n\t"
        "ds_read_b64_tr_b16 a[186:187], v113 offset:6144\n\t"
        "s_waitcnt lgkmcnt(8)\n\t"
        "ds_read_b64_tr_b16 a[176:177], v114 offset:0\n\t"
        "ds_read_b64_tr_b16 a[178:179], v114 offset:2048\n\t"
        "ds_read_b64_tr_b16 a[188:189], v114 offset:4096\n\t"
        "ds_read_b64_tr_b16 a[190:191], v114 offset:6144\n\t"
        "s_waitcnt lgkmcnt(8)\n\t"
        "ds_read_b64_tr_b16 v[56:57], v115 offset:0\n\t"
        "ds_read_b64_tr_b16 v[58:59], v115 offset:2048\n\t"
        "ds_read_b64_tr_b16 v[60:61], v115 offset:4096\n\t"
        "ds_read_b64_tr_b16 v[62:63], v115 offset:6144\n\t"
        "s_add_u32 %[t1], %[ro], 4\n\t"
        "s_and_b32 %[t1], %[t1], 7\n\t"
        "s_lshl_b32 %[t1], %[t1], 18\n\t"
        "s_add_u32 %[t0], %[t1], 131072\n\t"
        "buffer_load_dwordx4 a[216:219], v110, %[so], %[t0] offen\n\t"
        "s_sleep 2\n\t"
        "s_add_u32 %[t0], %[t1], 147456\n\t"
        "buffer_load_dwordx4 a[220:223], v110, %[so], %[t0] offen\n\t"
        "s_sleep 2\n\t"
        "s_add_u32 %[t0], %[t1], 163840\n\t"
        "buffer_load_dwordx4 a[224:227], v110, %[so], %[t0] offen\n\t"
        "s_sleep 2\n\t"
        "s_add_u32 %[t0], %[t1], 180224\n\t"
        "buffer_load_dwordx4 a[228:231], v110, %[so], %[t0] offen\n\t"
        "s_sleep 2\n\t"
        "s_add_u32 %[t0], %[t1], 196608\n\t"
        "buffer_load_dwordx4 a[232:235], v110, %[so], %[t0] offen\n\t"
        "s_sleep 2\n\t"
        "s_add_u32 %[t0], %[t1], 212992\n\t"
        "buffer_load_dwordx4 a[236:239], v110, %[so], %[t0] offen\n\t"
        "s_sleep 2\n\t"
        "s_add_u32 %[t0], %[t1], 229376\n\t"
        "buffer_load_dwordx4 v[32:35], v110, %[so], %[t0] offen\n\t"
        "s_sleep 2\n\t"
        "s_add_u32 %[t0], %[t1], 245760\n\t"
        "buffer_load_dwordx4 v[36:39], v110, %[so], %[t0] offen\n\t"
        "s_sleep 2\n\t"
        "s_waitcnt lgkmcnt(0)\n\t"
        "ds_write_b128 v116, v[56:59] offset:14336\n\t"
        "ds_write_b128 v116, v[60:63] offset:15360\n\t"
        "s_waitcnt vmcnt(8)\n\t"
        "ds_write_b128 v111, a[192:195] offset:0\n\t"
        "ds_write_b128 v111, a[196:199] offset:1024\n\t"
        "ds_write_b128 v111, a[200:203] offset:2048\n\t"
        "ds_write_b128 v111, a[204:207] offset:3072\n\t"
        "ds_write_b128 v111, a[208:211] offset:4096\n\t"
        "ds_write_b128 v111, a[212:215] offset:5120\n\t"
        "ds_write_b128 v111, v[24:27] offset:6144\n\t"
        "ds_write_b128 v111, v[28:31] offset:7168\n\t"
        "ds_read_b64_tr_b16 a[192:193], v112 offset:0\n\t"
        "ds_read_b64_tr_b16 a[194:195], v112 offset:2048\n\t"
        "ds_read_b64_tr_b16 a[204:205], v112 offset:4096\n\t"
        "ds_read_b64_tr_b16 a[206:207], v112 offset:6144\n\t"
        "s_waitcnt lgkmcnt(8)\n\t"
        "ds_read_b64_tr_b16 a[196:197], v113 offset:0\n\t"
        "ds_read_b64_tr_b16 a[198:199], v113 offset:2048\n\t"
        "ds_read_b64_tr_b16 a[208:209], v113 offset:4096\n\t"
        "ds_read_b64_tr_b16 a[210:211], v113 offset:6144\n\t"
        "s_waitcnt lgkmcnt(8)\n\t"
        "ds_read_b64_tr_b16 a[200:201], v114 offset:0\n\t"
        "ds_read_b64_tr_b16 a[202:203], v114 offset:2048\n\t"
        "ds_read_b64_tr_b16 a[212:213], v114 offset:4096\n\t"
        "ds_read_b64_tr_b16 a[214:215], v114 offset:6144\n\t"
        "s_waitcnt lgkmcnt(8)\n\t"
        "ds_read_b64_tr_b16 v[56:57], v115 offset:0\n\t"
        "ds_read_b64_tr_b16 v[58:59], v115 offset:2048\n\t"
        "ds_read_b64_tr_b16 v[60:61], v115 offset:4096\n\t"
        "ds_read_b64_tr_b16 v[62:63], v115 offset:6144\n\t"
        "s_add_u32 %[t1], %[ro], 5\n\t"
        "s_and_b32 %[t1], %[t1], 7\n\t"
        "s_lshl_b32 %[t1], %[t1], 18\n\t"
        "s_add_u32 %[t0], %[t1], 0\n\t"
        "buffer_load_dwordx4 a[240:243], v110, %[so], %[t0] offen\n\t"
        "s_sleep 2\n\t"
        "s_add_u32 %[t0], %[t1], 16384\n\t"
        "buffer_load_dwordx4 a[244:247], v110, %[so], %[t0] offen\n\t"
        "s_sleep 2\n\t"
        "s_add_u32 %[t0], %[t1], 32768\n\t"
        "buffer_load_dwordx4 a[248:251], v110, %[so], %[t0] offen\n\t"
        "s_sleep 2\n\t"
        "s_add_u32 %[t0], %[t1], 49152\n\t"
        "buffer_load_dwordx4 a[252:255], v110, %[so], %[t0] offen\n\t"
        "s_sleep 2\n\t"
        "s_add_u32 %[t0], %[t1], 65536\n\t"
        "buffer_load_dwordx4 v[128:131], v110, %[so], %[t0] offen\n\t"
        "s_sleep 2\n\t"
        "s_add_u32 %[t0], %[t1], 81920\n\t"
        "buffer_load_dwordx4 v[132:135], v110, %[so], %[t0] offen\n\t"
        "s_sleep 2\n\t"
        "s_add_u32 %[t0], %[t1], 98304\n\t"
        "buffer_load_dwordx4 v[40:43], v110, %[so], %[t0] offen\n\t"
        "s_sleep 2\n\t"
        "s_add_u32 %[t0], %[t1], 114688\n\t"
        "buffer_load_dwordx4 v[44:47], v110, %[so], %[t0] offen\n\t"
        "s_sleep 2\n\t"
        "s_waitcnt lgkmcnt(0)\n\t"
        "ds_write_b128 v116, v[56:59] offset:16384\n\t"
        "ds_write_b128 v116, v[60:63] offset:17408\n\t"
        "s_waitcnt vmcnt(8)\n\t"
        "ds_write_b128 v111, a[216:219] offset:0\n\t"
        "ds_write_b128 v111, a[220:223] offset:1024\n\t"
        "ds_write_b128 v111, a[224:227] offset:2048\n\t"
        "ds_write_b128 v111, a[228:231] offset:3072\n\t"
        "ds_write_b128 v111, a[232:235] offset:4096\n\t"
        "ds_write_b128 v111, a[236:239] offset:5120\n\t"
        "ds_write_b128 v111, v[32:35] offset:6144\n\t"
        "ds_write_b128 v111, v[36:39] offset:7168\n\t"
        "ds_read_b64_tr_b16 a[216:217], v112 offset:0\n\t"
        "ds_read_b64_tr_b16 a[218:219], v112 offset:2048\n\t"
        "ds_read_b64_tr_b16 a[228:229], v112 offset:4096\n\t"
        "ds_read_b64_tr_b16 a[230:231], v112 offset:6144\n\t"
        "s_waitcnt lgkmcnt(8)\n\t"
        "ds_read_b64_tr_b16 a[220:221], v113 offset:0\n\t"
        "ds_read_b64_tr_b16 a[222:223], v113 offset:2048\n\t"
        "ds_read_b64_tr_b16 a[232:233], v113 offset:4096\n\t"
        "ds_read_b64_tr_b16 a[234:235], v113 offset:6144\n\t"
        "s_waitcnt lgkmcnt(8)\n\t"
        "ds_read_b64_tr_b16 a[224:225], v114 offset:0\n\t"
        "ds_read_b64_tr_b16 a[226:227], v114 offset:2048\n\t"
        "ds_read_b64_tr_b16 a[236:237], v114 offset:4096\n\t"
        "ds_read_b64_tr_b16 a[238:239], v114 offset:6144\n\t"
        "s_waitcnt lgkmcnt(8)\n\t"
        "ds_read_b64_tr_b16 v[56:57], v115 offset:0\n\t"
        "ds_read_b64_tr_b16 v[58:59], v115 offset:2048\n\t"
        "ds_read_b64_tr_b16 v[60:61], v115 offset:4096\n\t"
        "ds_read_b64_tr_b16 v[62:63], v115 offset:6144\n\t"
        "s_add_u32 %[t1], %[ro], 5\n\t"
        "s_and_b32 %[t1], %[t1], 7\n\t"
        "s_lshl_b32 %[t1], %[t1], 18\n\t"
        "s_add_u32 %[t0], %[t1], 131072\n\t"
        "buffer_load_dwordx4 v[136:139], v110, %[so], %[t0] offen\n\t"
        "s_sleep 2\n\t"
        "s_add_u32 %[t0], %[t1], 147456\n\t"
        "buffer_load_dwordx4 v[140:143], v110, %[so], %[t0] offen\n\t"
        "s_sleep 2\n\t"
        "s_add_u32 %[t0], %[t1], 163840\n\t"
        "buffer_load_dwordx4 v[144:147], v110, %[so], %[t0] offen\n\t"
        "s_sleep 2\n\t"
        "s_add_u32 %[t0], %[t1], 180224\n\t"
        "buffer_load_dwordx4 v[148:151], v110, %[so], %[t0] offen\n\t"
        "s_sleep 2\n\t"
        "s_add_u32 %[t0], %[t1], 196608\n\t"
        "buffer_load_dwordx4 v[152:155], v110, %[so], %[t0] offen\n\t"
        "s_sleep 2\n\t"
        "s_add_u32 %[t0], %[t1], 212992\n\t"
        "buffer_load_dwordx4 v[156:159], v110, %[so], %[t0] offen\n\t"
        "s_sleep 2\n\t"
        "s_add_u32 %[t0], %[t1], 229376\n\t"
        "buffer_load_dwordx4 v[48:51], v110, %[so], %[t0] offen\n\t"
        "s_sleep 2\n\t"
        "s_add_u32 %[t0], %[t1], 245760\n\t"
        "buffer_load_dwordx4 v[52:55], v110, %[so], %[t0] offen\n\t"
        "s_sleep 2\n\t"
        "s_waitcnt lgkmcnt(0)\n\t"
        "ds_write_b128 v116, v[56:59] offset:18432\n\t"
        "ds_write_b128 v116, v[60:63] offset:19456\n\t"
        "s_waitcnt vmcnt(8)\n\t"
        "ds_write_b128 v111, a[240:243] offset:0\n\t"
        "ds_write_b128 v111, a[244:247] offset:1024\n\t"
        "ds_write_b128 v111, a[248:251] offset:2048\n\t"
        "ds_write_b128 v111, a[252:255] offset:3072\n\t"
        "ds_write_b128 v111, v[128:131] offset:4096\n\t"
        "ds_write_b128 v111, v[132:135] offset:5120\n\t"
        "ds_write_b128 v111, v[40:43] offset:6144\n\t"
        "ds_write_b128 v111, v[44:47] offset:7168\n\t"
        "ds_read_b64_tr_b16 a[240:241], v112 offset:0\n\t"
        "ds_read_b64_tr_b16 a[242:243], v112 offset:2048\n\t"
        "ds_read_b64_tr_b16 a[252:253], v112 offset:4096\n\t"
        "ds_read_b64_tr_b16 a[254:255], v112 offset:6144\n\t"
        "s_waitcnt lgkmcnt(8)\n\t"
        "ds_read_b64_tr_b16 a[244:245], v113 offset:0\n\t"
        "ds_read_b64_tr_b16 a[246:247], v113 offset:2048\n\t"
        "ds_read_b64_tr_b16 v[128:129], v113 offset:4096\n\t"
        "ds_read_b64_tr_b16 v[130:131], v113 offset:6144\n\t"
        "s_waitcnt lgkmcnt(8)\n\t"
        "ds_read_b64_tr_b16 a[248:249], v114 offset:0\n\t"
        "ds_read_b64_tr_b16 a[250:251], v114 offset:2048\n\t"
        "ds_read_b64_tr_b16 v[132:133], v114 offset:4096\n\t"
        "ds_read_b64_tr_b16 v[134:135], v114 offset:6144\n\t"
        "s_waitcnt lgkmcnt(8)\n\t"
        "ds_read_b64_tr_b16 v[56:57], v115 offset:0\n\t"
        "ds_read_b64_tr_b16 v[58:59], v115 offset:2048\n\t"
        "ds_read_b64_tr_b16 v[60:61], v115 offset:4096\n\t"
        "ds_read_b64_tr_b16 v[62:63], v115 offset:6144\n\t"
        "s_add_u32 %[t1], %[ro], 6\n\t"
        "s_and_b32 %[t1], %[t1], 7\n\t"
        "s_lshl_b32 %[t1], %[t1], 18\n\t"
        "s_add_u32 %[t0], %[t1], 0\n\t"
        "buffer_load_dwordx4 v[160:163], v110, %[so], %[t0] offen\n\t"
        "s_sleep 2\n\t"
        "s_add_u32 %[t0], %[t1], 16384\n\t"
        "buffer_load_dwordx4 v[164:167], v110, %[so], %[t0] offen\n\t"
        "s_sleep 2\n\t"
        "s_add_u32 %[t0], %[t1], 32768\n\t"
        "buffer_load_dwordx4 v[168:171], v110, %[so], %[t0] offen\n\t"
        "s_sleep 2\n\t"
        "s_add_u32 %[t0], %[t1], 49152\n\t"
        "buffer_load_dwordx4 v[172:175], v110, %[so], %[t0] offen\n\t"
        "s_sleep 2\n\t"
        "s_add_u32 %[t0], %[t1], 65536\n\t"
        "buffer_load_dwordx4 v[176:179], v110, %[so], %[t0] offen\n\t"
        "s_sleep 2\n\t"
        "s_add_u32 %[t0], %[t1], 81920\n\t"
        "buffer_load_dwordx4 v[180:183], v110, %[so], %[t0] offen\n\t"
        "s_sleep 2\n\t"
        "s_add_u32 %[t0], %[t1], 98304\n\t"
        "buffer_load_dwordx4 v[24:27], v110, %[so], %[t0] offen\n\t"
        "s_sleep 2\n\t"
        "s_add_u32 %[t0], %[t1], 114688\n\t"
        "buffer_load_dwordx4 v[28:31], v110, %[so], %[t0] offen\n\t"
        "s_sleep 2\n\t"
        "s_waitcnt lgkmcnt(0)\n\t"
        "ds_write_b128 v116, v[56:59] offset:20480\n\t"
        "ds_write_b128 v116, v[60:63] offset:21504\n\t"
        "s_waitcnt vmcnt(8)\n\t"
        "ds_write_b128 v111, v[136:139] offset:0\n\t"
        "ds_write_b128 v111, v[140:143] offset:1024\n\t"
        "ds_write_b128 v111, v[144:147] offset:2048\n\t"
        "ds_write_b128 v111, v[148:151] offset:3072\n\t"
        "ds_write_b128 v111, v[152:155] offset:4096\n\t"
        "ds_write_b128 v111, v[156:159] offset:5120\n\t"
        "ds_write_b128 v111, v[48:51] offset:6144\n\t"
        "ds_write_b128 v111, v[52:55] offset:7168\n\t"
        "ds_read_b64_tr_b16 v[136:137], v112 offset:0\n\t"
        "ds_read_b64_tr_b16 v[138:139], v112 offset:2048\n\t"
        "ds_read_b64_tr_b16 v[148:149], v112 offset:4096\n\t"
        "ds_read_b64_tr_b16 v[150:151], v112 offset:6144\n\t"
        "s_waitcnt lgkmcnt(8)\n\t"
        "ds_read_b64_tr_b16 v[140:141], v113 offset:0\n\t"
        "ds_read_b64_tr_b16 v[142:143], v113 offset:2048\n\t"
        "ds_read_b64_tr_b16 v[152:153], v113 offset:4096\n\t"
        "ds_read_b64_tr_b16 v[154:155], v113 offset:6144\n\t"
        "s_waitcnt lgkmcnt(8)\n\t"
        "ds_read_b64_tr_b16 v[144:145], v114 offset:0\n\t"
        "ds_read_b64_tr_b16 v[146:147], v114 offset:2048\n\t"
        "ds_read_b64_tr_b16 v[156:157], v114 offset:4096\n\t"
        "ds_read_b64_tr_b16 v[158:159], v114 offset:6144\n\t"
        "s_waitcnt lgkmcnt(8)\n\t"
        "ds_read_b64_tr_b16 v[56:57], v115 offset:0\n\t"
        "ds_read_b64_tr_b16 v[58:59], v115 offset:2048\n\t"
        "ds_read_b64_tr_b16 v[60:61], v115 offset:4096\n\t"
        "ds_read_b64_tr_b16 v[62:63], v115 offset:6144\n\t"
        "s_add_u32 %[t1], %[ro], 6\n\t"
        "s_and_b32 %[t1], %[t1], 7\n\t"
        "s_lshl_b32 %[t1], %[t1], 18\n\t"
        "s_add_u32 %[t0], %[t1], 131072\n\t"
        "buffer_load_dwordx4 v[184:187], v110, %[so], %[t0] offen\n\t"
        "s_sleep 2\n\t"
        "s_add_u32 %[t0], %[t1], 147456\n\t"
        "buffer_load_dwordx4 v[188:191], v110, %[so], %[t0] offen\n\t"
        "s_sleep 2\n\t"
        "s_add_u32 %[t0], %[t1], 163840\n\t"
        "buffer_load_dwordx4 v[192:195], v110, %[so], %[t0] offen\n\t"
        "s_sleep 2\n\t"
        "s_add_u32 %[t0], %[t1], 180224\n\t"
        "buffer_load_dwordx4 v[196:199], v110, %[so], %[t0] offen\n\t"
        "s_sleep 2\n\t"
        "s_add_u32 %[t0], %[t1], 196608\n\t"
        "buffer_load_dwordx4 v[200:203], v110, %[so], %[t0] offen\n\t"
        "s_sleep 2\n\t"
        "s_add_u32 %[t0], %[t1], 212992\n\t"
        "buffer_load_dwordx4 v[204:207], v110, %[so], %[t0] offen\n\t"
        "s_sleep 2\n\t"
        "s_add_u32 %[t0], %[t1], 229376\n\t"
        "buffer_load_dwordx4 v[32:35], v110, %[so], %[t0] offen\n\t"
        "s_sleep 2\n\t"
        "s_add_u32 %[t0], %[t1], 245760\n\t"
        "buffer_load_dwordx4 v[36:39], v110, %[so], %[t0] offen\n\t"
        "s_sleep 2\n\t"
        "s_waitcnt lgkmcnt(0)\n\t"
        "ds_write_b128 v116, v[56:59] offset:22528\n\t"
        "ds_write_b128 v116, v[60:63] offset:23552\n\t"
        "s_waitcnt vmcnt(8)\n\t"
        "ds_write_b128 v111, v[160:163] offset:0\n\t"
        "ds_write_b128 v111, v[164:167] offset:1024\n\t"
        "ds_write_b128 v111, v[168:171] offset:2048\n\t"
        "ds_write_b128 v111, v[172:175] offset:3072\n\t"
        "ds_write_b128 v111, v[176:179] offset:4096\n\t"
        "ds_write_b128 v111, v[180:183] offset:5120\n\t"
        "ds_write_b128 v111, v[24:27] offset:6144\n\t"
        "ds_write_b128 v111, v[28:31] offset:7168\n\t"
        "ds_read_b64_tr_b16 v[160:161], v112 offset:0\n\t"
        "ds_read_b64_tr_b16 v[162:163], v112 offset:2048\n\t"
        "ds_read_b64_tr_b16 v[172:173], v112 offset:4096\n\t"
        "ds_read_b64_tr_b16 v[174:175], v112 offset:6144\n\t"
        "s_waitcnt lgkmcnt(8)\n\t"
        "ds_read_b64_tr_b16 v[164:165], v113 offset:0\n\t"
        "ds_read_b64_tr_b16 v[166:167], v113 offset:2048\n\t"
        "ds_read_b64_tr_b16 v[176:177], v113 offset:4096\n\t"
        "ds_read_b64_tr_b16 v[178:179], v113 offset:6144\n\t"
        "s_waitcnt lgkmcnt(8)\n\t"
        "ds_read_b64_tr_b16 v[168:169], v114 offset:0\n\t"
        "ds_read_b64_tr_b16 v[170:171], v114 offset:2048\n\t"
        "ds_read_b64_tr_b16 v[180:181], v114 offset:4096\n\t"
        "ds_read_b64_tr_b16 v[182:183], v114 offset:6144\n\t"
        "s_waitcnt lgkmcnt(8)\n\t"
        "ds_read_b64_tr_b16 v[56:57], v115 offset:0\n\t"
        "ds_read_b64_tr_b16 v[58:59], v115 offset:2048\n\t"
        "ds_read_b64_tr_b16 v[60:61], v115 offset:4096\n\t"
        "ds_read_b64_tr_b16 v[62:63], v115 offset:6144\n\t"
        "s_add_u32 %[t1], %[ro], 7\n\t"
        "s_and_b32 %[t1], %[t1], 7\n\t"
        "s_lshl_b32 %[t1], %[t1], 18\n\t"
        "s_add_u32 %[t0], %[t1], 0\n\t"
        "buffer_load_dwordx4 v[208:211], v110, %[so], %[t0] offen\n\t"
        "s_sleep 2\n\t"
        "s_add_u32 %[t0], %[t1], 16384\n\t"
        "buffer_load_dwordx4 v[212:215], v110, %[so], %[t0] offen\n\t"
        "s_sleep 2\n\t"
        "s_add_u32 %[t0], %[t1], 32768\n\t"
        "buffer_load_dwordx4 v[216:219], v110, %[so], %[t0] offen\n\t"
        "s_sleep 2\n\t"
        "s_add_u32 %[t0], %[t1], 49152\n\t"
        "buffer_load_dwordx4 v[220:223], v110, %[so], %[t0] offen\n\t"
        "s_sleep 2\n\t"
        "s_add_u32 %[t0], %[t1], 65536\n\t"
        "buffer_load_dwordx4 v[224:227], v110, %[so], %[t0] offen\n\t"
        "s_sleep 2\n\t"
        "s_add_u32 %[t0], %[t1], 81920\n\t"
        "buffer_load_dwordx4 v[228:231], v110, %[so], %[t0] offen\n\t"
        "s_sleep 2\n\t"
        "s_add_u32 %[t0], %[t1], 98304\n\t"
        "buffer_load_dwordx4 v[40:43], v110, %[so], %[t0] offen\n\t"
        "s_sleep 2\n\t"
        "s_add_u32 %[t0], %[t1], 114688\n\t"
        "buffer_load_dwordx4 v[44:47], v110, %[so], %[t0] offen\n\t"
        "s_sleep 2\n\t"
        "s_waitcnt lgkmcnt(0)\n\t"
        "ds_write_b128 v116, v[56:59] offset:24576\n\t"
        "ds_write_b128 v116, v[60:63] offset:25600\n\t"
        "s_waitcnt vmcnt(8)\n\t"
        "ds_write_b128 v111, v[184:187] offset:0\n\t"
        "ds_write_b128 v111, v[188:191] offset:1024\n\t"
        "ds_write_b128 v111, v[192:195] offset:2048\n\t"
        "ds_write_b128 v111, v[196:199] offset:3072\n\t"
        "ds_write_b128 v111, v[200:203] offset:4096\n\t"
        "ds_write_b128 v111, v[204:207] offset:5120\n\t"
        "ds_write_b128 v111, v[32:35] offset:6144\n\t"
        "ds_write_b128 v111, v[36:39] offset:7168\n\t"
        "ds_read_b64_tr_b16 v[184:185], v112 offset:0\n\t"
        "ds_read_b64_tr_b16 v[186:187], v112 offset:2048\n\t"
        "ds_read_b64_tr_b16 v[196:197], v112 offset:4096\n\t"
        "ds_read_b64_tr_b16 v[198:199], v112 offset:6144\n\t"
        "s_waitcnt lgkmcnt(8)\n\t"
        "ds_read_b64_tr_b16 v[188:189], v113 offset:0\n\t"
        "ds_read_b64_tr_b16 v[190:191], v113 offset:2048\n\t"
        "ds_read_b64_tr_b16 v[200:201], v113 offset:4096\n\t"
        "ds_read_b64_tr_b16 v[202:203], v113 offset:6144\n\t"
        "s_waitcnt lgkmcnt(8)\n\t"
        "ds_read_b64_tr_b16 v[192:193], v114 offset:0\n\t"
        "ds_read_b64_tr_b16 v[194:195], v114 offset:2048\n\t"
        "ds_read_b64_tr_b16 v[204:205], v114 offset:4096\n\t"
        "ds_read_b64_tr_b16 v[206:207], v114 offset:6144\n\t"
        "s_waitcnt lgkmcnt(8)\n\t"
        "ds_read_b64_tr_b16 v[56:57], v115 offset:0\n\t"
        "ds_read_b64_tr_b16 v[58:59], v115 offset:2048\n\t"
        "ds_read_b64_tr_b16 v[60:61], v115 offset:4096\n\t"
        "ds_read_b64_tr_b16 v[62:63], v115 offset:6144\n\t"
        "s_add_u32 %[t1], %[ro], 7\n\t"
        "s_and_b32 %[t1], %[t1], 7\n\t"
        "s_lshl_b32 %[t1], %[t1], 18\n\t"
        "s_add_u32 %[t0], %[t1], 131072\n\t"
        "buffer_load_dwordx4 v[232:235], v110, %[so], %[t0] offen\n\t"
        "s_sleep 2\n\t"
        "s_add_u32 %[t0], %[t1], 147456\n\t"
        "buffer_load_dwordx4 v[236:239], v110, %[so], %[t0] offen\n\t"
        "s_sleep 2\n\t"
        "s_add_u32 %[t0], %[t1], 163840\n\t"
        "buffer_load_dwordx4 v[240:243], v110, %[so], %[t0] offen\n\t"
        "s_sleep 2\n\t"
        "s_add_u32 %[t0], %[t1], 180224\n\t"
        "buffer_load_dwordx4 v[244:247], v110, %[so], %[t0] offen\n\t"
        "s_sleep 2\n\t"
        "s_add_u32 %[t0], %[t1], 196608\n\t"
        "buffer_load_dwordx4 v[248:251], v110, %[so], %[t0] offen\n\t"
        "s_sleep 2\n\t"
        "s_add_u32 %[t0], %[t1], 212992\n\t"
        "buffer_load_dwordx4 v[252:255], v110, %[so], %[t0] offen\n\t"
        "s_sleep 2\n\t"
        "s_add_u32 %[t0], %[t1], 229376\n\t"
        "buffer_load_dwordx4 v[48:51], v110, %[so], %[t0] offen\n\t"
        "s_sleep 2\n\t"
        "s_add_u32 %[t0], %[t1], 245760\n\t"
        "buffer_load_dwordx4 v[52:55], v110, %[so], %[t0] offen\n\t"
        "s_sleep 2\n\t"
        "s_waitcnt lgkmcnt(0)\n\t"
        "ds_write_b128 v116, v[56:59] offset:26624\n\t"
        "ds_write_b128 v116, v[60:63] offset:27648\n\t"
        "s_waitcnt vmcnt(8)\n\t"
        "ds_write_b128 v111, v[208:211] offset:0\n\t"
        "ds_write_b128 v111, v[212:215] offset:1024\n\t"
        "ds_write_b128 v111, v[216:219] offset:2048\n\t"
        "ds_write_b128 v111, v[220:223] offset:3072\n\t"
        "ds_write_b128 v111, v[224:227] offset:4096\n\t"
        "ds_write_b128 v111, v[228:231] offset:5120\n\t"
        "ds_write_b128 v111, v[40:43] offset:6144\n\t"
        "ds_write_b128 v111, v[44:47] offset:7168\n\t"
        "ds_read_b64_tr_b16 v[208:209], v112 offset:0\n\t"
        "ds_read_b64_tr_b16 v[210:211], v112 offset:2048\n\t"
        "ds_read_b64_tr_b16 v[220:221], v112 offset:4096\n\t"
        "ds_read_b64_tr_b16 v[222:223], v112 offset:6144\n\t"
        "s_waitcnt lgkmcnt(8)\n\t"
        "ds_read_b64_tr_b16 v[212:213], v113 offset:0\n\t"
        "ds_read_b64_tr_b16 v[214:215], v113 offset:2048\n\t"
        "ds_read_b64_tr_b16 v[224:225], v113 offset:4096\n\t"
        "ds_read_b64_tr_b16 v[226:227], v113 offset:6144\n\t"
        "s_waitcnt lgkmcnt(8)\n\t"
        "ds_read_b64_tr_b16 v[216:217], v114 offset:0\n\t"
        "ds_read_b64_tr_b16 v[218:219], v114 offset:2048\n\t"
        "ds_read_b64_tr_b16 v[228:229], v114 offset:4096\n\t"
        "ds_read_b64_tr_b16 v[230:231], v114 offset:6144\n\t"
        "s_waitcnt lgkmcnt(8)\n\t"
        "ds_read_b64_tr_b16 v[56:57], v115 offset:0\n\t"
        "ds_read_b64_tr_b16 v[58:59], v115 offset:2048\n\t"
        "ds_read_b64_tr_b16 v[60:61], v115 offset:4096\n\t"
        "ds_read_b64_tr_b16 v[62:63], v115 offset:6144\n\t"
        "s_waitcnt lgkmcnt(0)\n\t"
        "ds_write_b128 v116, v[56:59] offset:28672\n\t"
        "ds_write_b128 v116, v[60:63] offset:29696\n\t"
        "s_waitcnt vmcnt(0)\n\t"
        "ds_write_b128 v111, v[232:235] offset:0\n\t"
        "ds_write_b128 v111, v[236:239] offset:1024\n\t"
        "ds_write_b128 v111, v[240:243] offset:2048\n\t"
        "ds_write_b128 v111, v[244:247] offset:3072\n\t"
        "ds_write_b128 v111, v[248:251] offset:4096\n\t"
        "ds_write_b128 v111, v[252:255] offset:5120\n\t"
        "ds_write_b128 v111, v[48:51] offset:6144\n\t"
        "ds_write_b128 v111, v[52:55] offset:7168\n\t"
        "ds_read_b64_tr_b16 v[232:233], v112 offset:0\n\t"
        "ds_read_b64_tr_b16 v[234:235], v112 offset:2048\n\t"
        "ds_read_b64_tr_b16 v[244:245], v112 offset:4096\n\t"
        "ds_read_b64_tr_b16 v[246:247], v112 offset:6144\n\t"
        "s_waitcnt lgkmcnt(8)\n\t"
        "ds_read_b64_tr_b16 v[236:237], v113 offset:0\n\t"
        "ds_read_b64_tr_b16 v[238:239], v113 offset:2048\n\t"
        "ds_read_b64_tr_b16 v[248:249], v113 offset:4096\n\t"
        "ds_read_b64_tr_b16 v[250:251], v113 offset:6144\n\t"
        "s_waitcnt lgkmcnt(8)\n\t"
        "ds_read_b64_tr_b16 v[240:241], v114 offset:0\n\t"
        "ds_read_b64_tr_b16 v[242:243], v114 offset:2048\n\t"
        "ds_read_b64_tr_b16 v[252:253], v114 offset:4096\n\t"
        "ds_read_b64_tr_b16 v[254:255], v114 offset:6144\n\t"
        "s_waitcnt lgkmcnt(8)\n\t"
        "ds_read_b64_tr_b16 v[56:57], v115 offset:0\n\t"
        "ds_read_b64_tr_b16 v[58:59], v115 offset:2048\n\t"
        "ds_read_b64_tr_b16 v[60:61], v115 offset:4096\n\t"
        "ds_read_b64_tr_b16 v[62:63], v115 offset:6144\n\t"
        "s_waitcnt lgkmcnt(0)\n\t"
        "ds_write_b128 v116, v[56:59] offset:30720\n\t"
        "ds_write_b128 v116, v[60:63] offset:31744\n\t"
        "ds_read_b128 v[88:91], v116 offset:0\n\t"
        "ds_read_b128 v[92:95], v116 offset:1024\n\t"
        "ds_read_b128 v[96:99], v116 offset:2048\n\t"
        "s_mov_b32 %[sp], 0\n\t"
        "LP_cv%=:\n\t"
        "buffer_load_dword v104, off, %[sw], %[cv] sc1\n\t"
        "s_waitcnt vmcnt(0) lgkmcnt(0)\n\t"
        "v_readfirstlane_b32 %[t0], v104\n\t"
        "s_cmp_ge_u32 %[t0], 8\n\t"
        "s_cbranch_scc1 LP_wait%=\n\t"
        "s_sleep 8\n\t"
        "s_add_u32 %[sp], %[sp], 1\n\t"
        "s_cmp_lt_u32 %[sp], 0x100000\n\t"
        "s_cbranch_scc1 LP_cv%=\n\t"
        "v_mov_b32_e32 v104, 256\n\t"
        "s_mov_b32 %[t0], 33024\n\t"
        "buffer_store_dword v104, off, %[sw], %[t0] sc1\n\t"
        "s_endpgm\n\t"
        "LP_wait%=:\n\t"
        "s_mov_b32 %[sp], 0\n\t"
        "LP_poll%=:\n\t"
        "buffer_load_dword v103, v118, %[sw], 0 offen sc1\n\t"
        "s_lshr_b32 %[t1], %[tl], 1\n\t"
        "s_waitcnt vmcnt(0)\n\t"
        "v_min_u32_dpp v103, v103, v103 quad_perm:[1,0,3,2] row_mask:0xf bank_mask:0xf\n\t"
        "s_nop 1\n\t"
        "v_min_u32_dpp v103, v103, v103 quad_perm:[2,3,0,1] row_mask:0xf bank_mask:0xf\n\t"
        "s_nop 1\n\t"
        "v_min_u32_dpp v103, v103, v103 row_ror:4 row_mask:0xf bank_mask:0xf\n\t"
        "s_nop 0\n\t"
        "v_readfirstlane_b32 %[pg], v103\n\t"
        "s_cmp_gt_u32 %[pg], %[t1]\n\t"
        "s_cbranch_scc1 LP_have%=\n\t"
        "s_cmp_eq_u32 %[pg], %[t1]\n\t"
        "s_cbranch_scc1 LP_near%=\n\t"
        "s_sleep 24\n\t"
        "LP_near%=:\n\t"
        "s_sleep 2\n\t"
        "s_add_u32 %[sp], %[sp], 1\n\t"
        "s_cmp_lt_u32 %[sp], 0x100000\n\t"
        "s_cbranch_scc1 LP_poll%=\n\t"
        "v_mov_b32_e32 v104, 128\n\t"
        "s_mov_b32 %[t0], 33024\n\t"
        "buffer_store_dword v104, off, %[sw], %[t0] sc1\n\t"
        "s_endpgm\n\t"
        "LP_have%=:\n\t"
        "s_lshl_b32 %[nx], %[tl], 17\n\t"
        "s_add_u32 %[t0], %[ro], 0\n\t"
        "s_and_b32 %[t0], %[t0], 7\n\t"
        "s_lshl_b32 %[t0], %[t0], 12\n\t"
        "s_add_u32 %[t0], %[t0], %[nx]\n\t"
        "buffer_load_dwordx4 v[24:27], v117, %[sw], %[t0] offen offset:0 sc1\n\t"
        "buffer_load_dwordx4 v[28:31], v117, %[sw], %[t0] offen offset:1024 sc1\n\t"
        "buffer_load_dwordx4 v[32:35], v117, %[sw], %[t0] offen offset:2048 sc1\n\t"
        "buffer_load_dwordx4 v[36:39], v117, %[sw], %[t0] offen offset:3072 sc1\n\t"
        "s_add_u32 %[t0], %[ro], 1\n\t"
        "s_and_b32 %[t0], %[t0], 7\n\t"
        "s_lshl_b32 %[t0], %[t0], 12\n\t"
        "s_add_u32 %[t0], %[t0], %[nx]\n\t"
        "buffer_load_dwordx4 v[40:43], v117, %[sw], %[t0] offen offset:0 sc1\n\t"
        "buffer_load_dwordx4 v[44:47], v117, %[sw], %[t0] offen offset:1024 sc1\n\t"
        "buffer_load_dwordx4 v[48:51], v117, %[sw], %[t0] offen offset:2048 sc1\n\t"
        "buffer_load_dwordx4 v[52:55], v117, %[sw], %[t0] offen offset:3072 sc1\n\t"
        "s_add_u32 %[t0], %[ro], 2\n\t"
        "s_and_b32 %[t0], %[t0], 7\n\t"
        "s_lshl_b32 %[t0], %[t0], 12\n\t"
        "s_add_u32 %[t0], %[t0], %[nx]\n\t"
        "buffer_load_dwordx4 v[56:59], v117, %[sw], %[t0] offen offset:0 sc1\n\t"
        "buffer_load_dwordx4 v[60:63], v117, %[sw], %[t0] offen offset:1024 sc1\n\t"
        "buffer_load_dwordx4 v[64:67], v117, %[sw], %[t0] offen offset:2048 sc1\n\t"
        "buffer_load_dwordx4 v[68:71], v117, %[sw], %[t0] offen offset:3072 sc1\n\t"
        "s_add_u32 %[t0], %[ro], 3\n\t"
        "s_and_b32 %[t0], %[t0], 7\n\t"
        "s_lshl_b32 %[t0], %[t0], 12\n\t"
        "s_add_u32 %[t0], %[t0], %[nx]\n\t"
        "buffer_load_dwordx4 v[72:75], v117, %[sw], %[t0] offen offset:0 sc1\n\t"
        "buffer_load_dwordx4 v[76:79], v117, %[sw], %[t0] offen offset:1024 sc1\n\t"
        "buffer_load_dwordx4 v[80:83], v117, %[sw], %[t0] offen offset:2048 sc1\n\t"
        "buffer_load_dwordx4 v[84:87], v117, %[sw], %[t0] offen offset:3072 sc1\n\t"
        "buffer_load_dword v103, v118, %[sw], 0 offen sc1\n\t"
        "LP_tile%=:\n\t"
        "s_and_b32 %[t0], %[tl], 1\n\t"
        "s_lshl_b32 %[t0], %[t0], 14\n\t"
        "s_lshr_b32 %[t1], %[tl], 1\n\t"
        "s_lshl_b32 %[t1], %[t1], 6\n\t"
        "s_add_u32 %[t0], %[t0], %[t1]\n\t"
        "buffer_load_dword v100, v119, %[sw], %[t0] offen sc1\n\t"
        "s_add_u32 %[t0], %[t0], 32768\n\t"
        "buffer_load_dword v101, v119, %[sw], %[t0] offen sc1\n\t"
        "s_lshl_b32 %[t0], %[tl], 6\n\t"
        "buffer_load_dword v102, v120, %[sw], %[t0] offen sc1\n\t"
        "buffer_load_dword v103, v118, %[sw], 0 offen sc1\n\t"
        "s_mov_b32 %[cu], %[nx]\n\t"
        "s_waitcnt vmcnt(20) lgkmcnt(2)\n\t"
        "v_mfma_f32_16x16x32_bf16 v[20:23], v[88:91], v[24:27], 0\n\t"
        "ds_read_b128 v[88:91], v116 offset:3072\n\t"
        "v_mfma_f32_16x16x32_bf16 v[8:11], a[0:3], v[24:27], 0\n\t"
        "v_mfma_f32_16x16x32_bf16 v[12:15], a[4:7], v[24:27], 0\n\t"
        "v_mfma_f32_16x16x32_bf16 v[16:19], a[8:11], v[24:27], 0\n\t"
        "s_add_u32 %[t0], %[ro], 4\n\t"
        "s_and_b32 %[t0], %[t0], 7\n\t"
        "s_lshl_b32 %[t0], %[t0], 12\n\t"
        "s_add_u32 %[t0], %[t0], %[cu]\n\t"
        "buffer_load_dwordx4 v[24:27], v117, %[sw], %[t0] offen offset:0 sc1\n\t"
        "s_waitcnt vmcnt(20) lgkmcnt(2)\n\t"
        "v_mfma_f32_16x16x32_bf16 v[20:23], v[92:95], v[28:31], v[20:23]\n\t"
        "ds_read_b128 v[92:95], v116 offset:4096\n\t"
        "v_mfma_f32_16x16x32_bf16 v[8:11], a[12:15], v[28:31], v[8:11]\n\t"
        "v_mfma_f32_16x16x32_bf16 v[12:15], a[16:19], v[28:31], v[12:15]\n\t"
        "v_mfma_f32_16x16x32_bf16 v[16:19], a[20:23], v[28:31], v[16:19]\n\t"
        "buffer_load_dwordx4 v[28:31], v117, %[sw], %[t0] offen offset:1024 sc1\n\t"
        "s_waitcnt vmcnt(20) lgkmcnt(2)\n\t"
        "v_mfma_f32_16x16x32_bf16 v[20:23], v[96:99], v[32:35], v[20:23]\n\t"
        "ds_read_b128 v[96:99], v116 offset:5120\n\t"
        "v_mfma_f32_16x16x32_bf16 v[8:11], a[24:27], v[32:35], v[8:11]\n\t"
        "v_mfma_f32_16x16x32_bf16 v[12:15], a[28:31], v[32:35], v[12:15]\n\t"
        "v_mfma_f32_16x16x32_bf16 v[16:19], a[32:35], v[32:35], v[16:19]\n\t"
        "buffer_load_dwordx4 v[32:35], v117, %[sw], %[t0] offen offset:2048 sc1\n\t"
        "s_waitcnt vmcnt(20) lgkmcnt(2)\n\t"
        "v_mfma_f32_16x16x32_bf16 v[20:23], v[88:91], v[36:39], v[20:23]\n\t"
        "ds_read_b128 v[88:91], v116 offset:6144\n\t"
        "v_mfma_f32_16x16x32_bf16 v[8:11], a[36:39], v[36:39], v[8:11]\n\t"
        "v_mfma_f32_16x16x32_bf16 v[12:15], a[40:43], v[36:39], v[12:15]\n\t"
        "v_mfma_f32_16x16x32_bf16 v[16:19], a[44:47], v[36:39], v[16:19]\n\t"
        "buffer_load_dwordx4 v[36:39], v117, %[sw], %[t0] offen offset:3072 sc1\n\t"
        "s_waitcnt vmcnt(20) lgkmcnt(2)\n\t"
        "v_mfma_f32_16x16x32_bf16 v[20:23], v[92:95], v[40:43], v[20:23]\n\t"
        "ds_read_b128 v[92:95], v116 offset:7168\n\t"
        "v_mfma_f32_16x16x32_bf16 v[8:11], a[48:51], v[40:43], v[8:11]\n\t"
        "v_mfma_f32_16x16x32_bf16 v[12:15], a[52:55], v[40:43], v[12:15]\n\t"
        "v_mfma_f32_16x16x32_bf16 v[16:19], a[56:59], v[40:43], v[16:19]\n\t"
        "s_add_u32 %[t0], %[ro], 5\n\t"
        "s_and_b32 %[t0], %[t0], 7\n\t"
        "s_lshl_b32 %[t0], %[t0], 12\n\t"
        "s_add_u32 %[t0], %[t0], %[cu]\n\t"
        "buffer_load_dwordx4 v[40:43], v117, %[sw], %[t0] offen offset:0 sc1\n\t"
        "s_waitcnt vmcnt(20) lgkmcnt(2)\n\t"
        "v_mfma_f32_16x16x32_bf16 v[20:23], v[96:99], v[44:47], v[20:23]\n\t"
        "ds_read_b128 v[96:99], v116 offset:8192\n\t"
        "v_mfma_f32_16x16x32_bf16 v[8:11], a[60:63], v[44:47], v[8:11]\n\t"
        "v_mfma_f32_16x16x32_bf16 v[12:15], a[64:67], v[44:47], v[12:15]\n\t"
        "v_mfma_f32_16x16x32_bf16 v[16:19], a[68:71], v[44:47], v[16:19]\n\t"
        "buffer_load_dwordx4 v[44:47], v117, %[sw], %[t0] offen offset:1024 sc1\n\t"
        "s_waitcnt vmcnt(20) lgkmcnt(2)\n\t"
        "v_mfma_f32_16x16x32_bf16 v[20:23], v[88:91], v[48:51], v[20:23]\n\t"
        "ds_read_b128 v[88:91], v116 offset:9216\n\t"
        "v_mfma_f32_16x16x32_bf16 v[8:11], a[72:75], v[48:51], v[8:11]\n\t"
        "v_mfma_f32_16x16x32_bf16 v[12:15], a[76:79], v[48:51], v[12:15]\n\t"
        "v_mfma_f32_16x16x32_bf16 v[16:19], a[80:83], v[48:51], v[16:19]\n\t"
        "buffer_load_dwordx4 v[48:51], v117, %[sw], %[t0] offen offset:2048 sc1\n\t"
        "s_waitcnt vmcnt(20) lgkmcnt(2)\n\t"
        "v_mfma_f32_16x16x32_bf16 v[20:23], v[92:95], v[52:55], v[20:23]\n\t"
        "ds_read_b128 v[92:95], v116 offset:10240\n\t"
        "v_mfma_f32_16x16x32_bf16 v[8:11], a[84:87], v[52:55], v[8:11]\n\t"
        "v_mfma_f32_16x16x32_bf16 v[12:15], a[88:91], v[52:55], v[12:15]\n\t"
        "v_mfma_f32_16x16x32_bf16 v[16:19], a[92:95], v[52:55], v[16:19]\n\t"
        "buffer_load_dwordx4 v[52:55], v117, %[sw], %[t0] offen offset:3072 sc1\n\t"
        "s_waitcnt vmcnt(20) lgkmcnt(2)\n\t"
        "s_barrier\n\t"
        "v_mfma_f32_16x16x32_bf16 v[20:23], v[96:99], v[56:59], v[20:23]\n\t"
        "ds_read_b128 v[96:99], v116 offset:11264\n\t"
        "v_mfma_f32_16x16x32_bf16 v[8:11], a[96:99], v[56:59], v[8:11]\n\t"
        "v_mfma_f32_16x16x32_bf16 v[12:15], a[100:103], v[56:59], v[12:15]\n\t"
        "v_mfma_f32_16x16x32_bf16 v[16:19], a[104:107], v[56:59], v[16:19]\n\t"
        "s_add_u32 %[t0], %[ro], 6\n\t"
        "s_and_b32 %[t0], %[t0], 7\n\t"
        "s_lshl_b32 %[t0], %[t0], 12\n\t"
        "s_add_u32 %[t0], %[t0], %[cu]\n\t"
        "buffer_load_dwordx4 v[56:59], v117, %[sw], %[t0] offen offset:0 sc1\n\t"
        "s_waitcnt vmcnt(20) lgkmcnt(2)\n\t"
        "v_mfma_f32_16x16x32_bf16 v[20:23], v[88:91], v[60:63], v[20:23]\n\t"
        "ds_read_b128 v[88:91], v116 offset:12288\n\t"
        "v_mfma_f32_16x16x32_bf16 v[8:11], a[108:111], v[60:63], v[8:11]\n\t"
        "v_mfma_f32_16x16x32_bf16 v[12:15], a[112:115], v[60:63], v[12:15]\n\t"
        "v_mfma_f32_16x16x32_bf16 v[16:19], a[116:119], v[60:63], v[16:19]\n\t"
        "buffer_load_dwordx4 v[60:63], v117, %[sw], %[t0] offen offset:1024 sc1\n\t"
        "s_waitcnt vmcnt(20) lgkmcnt(2)\n\t"
        "v_mfma_f32_16x16x32_bf16 v[20:23], v[92:95], v[64:67], v[20:23]\n\t"
        "ds_read_b128 v[92:95], v116 offset:13312\n\t"
        "v_mfma_f32_16x16x32_bf16 v[8:11], a[120:123], v[64:67], v[8:11]\n\t"
        "v_mfma_f32_16x16x32_bf16 v[12:15], a[124:127], v[64:67], v[12:15]\n\t"
        "v_mfma_f32_16x16x32_bf16 v[16:19], a[128:131], v[64:67], v[16:19]\n\t"
        "buffer_load_dwordx4 v[64:67], v117, %[sw], %[t0] offen offset:2048 sc1\n\t"
        "s_waitcnt vmcnt(20) lgkmcnt(2)\n\t"
        "v_mfma_f32_16x16x32_bf16 v[20:23], v[96:99], v[68:71], v[20:23]\n\t"
        "ds_read_b128 v[96:99], v116 offset:14336\n\t"
        "v_mfma_f32_16x16x32_bf16 v[8:11], a[132:135], v[68:71], v[8:11]\n\t"
        "v_mfma_f32_16x16x32_bf16 v[12:15], a[136:139], v[68:71], v[12:15]\n\t"
        "v_mfma_f32_16x16x32_bf16 v[16:19], a[140:143], v[68:71], v[16:19]\n\t"
        "buffer_load_dwordx4 v[68:71], v117, %[sw], %[t0] offen offset:3072 sc1\n\t"
        "s_waitcnt vmcnt(20) lgkmcnt(2)\n\t"
        "v_mfma_f32_16x16x32_bf16 v[20:23], v[88:91], v[72:75], v[20:23]\n\t"
        "ds_read_b128 v[88:91], v116 offset:15360\n\t"
        "v_mfma_f32_16x16x32_bf16 v[8:11], a[144:147], v[72:75], v[8:11]\n\t"
        "v_mfma_f32_16x16x32_bf16 v[12:15], a[148:151], v[72:75], v[12:15]\n\t"
        "v_mfma_f32_16x16x32_bf16 v[16:19], a[152:155], v[72:75], v[16:19]\n\t"
        "s_add_u32 %[t0], %[ro], 7\n\t"
        "s_and_b32 %[t0], %[t0], 7\n\t"
        "s_lshl_b32 %[t0], %[t0], 12\n\t"
        "s_add_u32 %[t0], %[t0], %[cu]\n\t"
        "buffer_load_dwordx4 v[72:75], v117, %[sw], %[t0] offen offset:0 sc1\n\t"
        "s_waitcnt vmcnt(13) lgkmcnt(2)\n\t"
        "v_mfma_f32_16x16x32_bf16 v[20:23], v[92:95], v[76:79], v[20:23]\n\t"
        "ds_read_b128 v[92:95], v116 offset:16384\n\t"
        "v_min_u32_dpp v103, v103, v103 quad_perm:[1,0,3,2] row_mask:0xf bank_mask:0xf\n\t"
        "s_nop 0\n\t"
        "v_mfma_f32_16x16x32_bf16 v[8:11], a[156:159], v[76:79], v[8:11]\n\t"
        "v_min_u32_dpp v103, v103, v103 quad_perm:[2,3,0,1] row_mask:0xf bank_mask:0xf\n\t"
        "s_nop 0\n\t"
        "v_mfma_f32_16x16x32_bf16 v[12:15], a[160:163], v[76:79], v[12:15]\n\t"
        "v_min_u32_dpp v103, v103, v103 row_ror:4 row_mask:0xf bank_mask:0xf\n\t"
        "s_nop 0\n\t"
        "v_mfma_f32_16x16x32_bf16 v[16:19], a[164:167], v[76:79], v[16:19]\n\t"
        "buffer_load_dwordx4 v[76:79], v117, %[sw], %[t0] offen offset:1024 sc1\n\t"
        "s_waitcnt vmcnt(20) lgkmcnt(2)\n\t"
        "v_mfma_f32_16x16x32_bf16 v[20:23], v[96:99], v[80:83], v[20:23]\n\t"
        "ds_read_b128 v[96:99], v116 offset:17408\n\t"
        "v_readfirstlane_b32 %[pg], v103\n\t"
        "s_add_u32 %[t1], %[tl], 8\n\t"
        "s_lshl_b32 %[nx], %[t1], 17\n\t"
        "s_lshr_b32 %[t1], %[t1], 1\n\t"
        "v_mfma_f32_16x16x32_bf16 v[8:11], a[168:171], v[80:83], v[8:11]\n\t"
        "s_cmp_gt_u32 %[pg], %[t1]\n\t"
        "s_cselect_b32 %[nx], %[nx], 0xf0000000\n\t"
        "v_mfma_f32_16x16x32_bf16 v[12:15], a[172:175], v[80:83], v[12:15]\n\t"
        "v_mfma_f32_16x16x32_bf16 v[16:19], a[176:179], v[80:83], v[16:19]\n\t"
        "buffer_load_dwordx4 v[80:83], v117, %[sw], %[t0] offen offset:2048 sc1\n\t"
        "s_waitcnt vmcnt(20) lgkmcnt(2)\n\t"
        "v_mfma_f32_16x16x32_bf16 v[20:23], v[88:91], v[84:87], v[20:23]\n\t"
        "ds_read_b128 v[88:91], v116 offset:18432\n\t"
        "v_mfma_f32_16x16x32_bf16 v[8:11], a[180:183], v[84:87], v[8:11]\n\t"
        "v_mfma_f32_16x16x32_bf16 v[12:15], a[184:187], v[84:87], v[12:15]\n\t"
        "v_mfma_f32_16x16x32_bf16 v[16:19], a[188:191], v[84:87], v[16:19]\n\t"
        "buffer_load_dwordx4 v[84:87], v117, %[sw], %[t0] offen offset:3072 sc1\n\t"
        "s_waitcnt vmcnt(15) lgkmcnt(2)\n\t"
        "v_mfma_f32_16x16x32_bf16 v[20:23], v[92:95], v[24:27], v[20:23]\n\t"
        "ds_read_b128 v[92:95], v116 offset:19456\n\t"
        "v_mfma_f32_16x16x32_bf16 v[8:11], a[192:195], v[24:27], v[8:11]\n\t"
        "v_mfma_f32_16x16x32_bf16 v[12:15], a[196:199], v[24:27], v[12:15]\n\t"
        "v_mfma_f32_16x16x32_bf16 v[16:19], a[200:203], v[24:27], v[16:19]\n\t"
        "s_add_u32 %[t0], %[ro], 0\n\t"
        "s_and_b32 %[t0], %[t0], 7\n\t"
        "s_lshl_b32 %[t0], %[t0], 12\n\t"
        "s_add_u32 %[t0], %[t0], %[nx]\n\t"
        "buffer_load_dwordx4 v[24:27], v117, %[sw], %[t0] offen offset:0 sc1\n\t"
        "s_waitcnt vmcnt(15) lgkmcnt(2)\n\t"
        "v_mfma_f32_16x16x32_bf16 v[20:23], v[96:99], v[28:31], v[20:23]\n\t"
        "ds_read_b128 v[96:99], v116 offset:20480\n\t"
        "v_mfma_f32_16x16x32_bf16 v[8:11], a[204:207], v[28:31], v[8:11]\n\t"
        "v_mfma_f32_16x16x32_bf16 v[12:15], a[208:211], v[28:31], v[12:15]\n\t"
        "v_sub_f32_e32 v104, 1.0, v102\n\t"
        "v_mfma_f32_16x16x32_bf16 v[16:19], a[212:215], v[28:31], v[16:19]\n\t"
        "buffer_load_dwordx4 v[28:31], v117, %[sw], %[t0] offen offset:1024 sc1\n\t"
        "s_waitcnt vmcnt(15) lgkmcnt(2)\n\t"
        "v_mfma_f32_16x16x32_bf16 v[20:23], v[88:91], v[32:35], v[20:23]\n\t"
        "ds_read_b128 v[88:91], v116 offset:21504\n\t"
        "v_mfma_f32_16x16x32_bf16 v[8:11], a[216:219], v[32:35], v[8:11]\n\t"
        "v_mfma_f32_16x16x32_bf16 v[12:15], a[220:223], v[32:35], v[12:15]\n\t"
        "v_cvt_pk_bf16_f32 v104, 0, v104\n\t"
        "v_mfma_f32_16x16x32_bf16 v[16:19], a[224:227], v[32:35], v[16:19]\n\t"
        "buffer_load_dwordx4 v[32:35], v117, %[sw], %[t0] offen offset:2048 sc1\n\t"
        "s_waitcnt vmcnt(15) lgkmcnt(2)\n\t"
        "v_mfma_f32_16x16x32_bf16 v[20:23], v[92:95], v[36:39], v[20:23]\n\t"
        "ds_read_b128 v[92:95], v116 offset:22528\n\t"
        "v_mfma_f32_16x16x32_bf16 v[8:11], a[228:231], v[36:39], v[8:11]\n\t"
        "v_mfma_f32_16x16x32_bf16 v[12:15], a[232:235], v[36:39], v[12:15]\n\t"
        "v_lshlrev_b32_e32 v106, 16, v100\n\t"
        "v_mfma_f32_16x16x32_bf16 v[16:19], a[236:239], v[36:39], v[16:19]\n\t"
        "buffer_load_dwordx4 v[36:39], v117, %[sw], %[t0] offen offset:3072 sc1\n\t"
        "s_waitcnt vmcnt(15) lgkmcnt(2)\n\t"
        "v_mfma_f32_16x16x32_bf16 v[20:23], v[96:99], v[40:43], v[20:23]\n\t"
        "ds_read_b128 v[96:99], v116 offset:23552\n\t"
        "v_mfma_f32_16x16x32_bf16 v[8:11], a[240:243], v[40:43], v[8:11]\n\t"
        "v_mfma_f32_16x16x32_bf16 v[12:15], a[244:247], v[40:43], v[12:15]\n\t"
        "v_mul_f32_e32 v106, v102, v106\n\t"
        "v_mfma_f32_16x16x32_bf16 v[16:19], a[248:251], v[40:43], v[16:19]\n\t"
        "s_add_u32 %[t0], %[ro], 1\n\t"
        "s_and_b32 %[t0], %[t0], 7\n\t"
        "s_lshl_b32 %[t0], %[t0], 12\n\t"
        "s_add_u32 %[t0], %[t0], %[nx]\n\t"
        "buffer_load_dwordx4 v[40:43], v117, %[sw], %[t0] offen offset:0 sc1\n\t"
        "s_waitcnt vmcnt(15) lgkmcnt(2)\n\t"
        "v_mfma_f32_16x16x32_bf16 v[20:23], v[88:91], v[44:47], v[20:23]\n\t"
        "ds_read_b128 v[88:91], v116 offset:24576\n\t"
        "v_mfma_f32_16x16x32_bf16 v[8:11], a[252:255], v[44:47], v[8:11]\n\t"
        "v_mfma_f32_16x16x32_bf16 v[12:15], v[128:131], v[44:47], v[12:15]\n\t"
        "v_cvt_pk_bf16_f32 v106, 0, v106\n\t"
        "v_mfma_f32_16x16x32_bf16 v[16:19], v[132:135], v[44:47], v[16:19]\n\t"
        "buffer_load_dwordx4 v[44:47], v117, %[sw], %[t0] offen offset:1024 sc1\n\t"
        "s_waitcnt vmcnt(15) lgkmcnt(2)\n\t"
        "v_mfma_f32_16x16x32_bf16 v[20:23], v[92:95], v[48:51], v[20:23]\n\t"
        "ds_read_b128 v[92:95], v116 offset:25600\n\t"
        "v_mfma_f32_16x16x32_bf16 v[8:11], v[136:139], v[48:51], v[8:11]\n\t"
        "v_mfma_f32_16x16x32_bf16 v[12:15], v[140:143], v[48:51], v[12:15]\n\t"
        "v_and_b32_e32 v107, 0xffff0000, v100\n\t"
        "v_mfma_f32_16x16x32_bf16 v[16:19], v[144:147], v[48:51], v[16:19]\n\t"
        "buffer_load_dwordx4 v[48:51], v117, %[sw], %[t0] offen offset:2048 sc1\n\t"
        "s_waitcnt vmcnt(15) lgkmcnt(2)\n\t"
        "v_mfma_f32_16x16x32_bf16 v[20:23], v[96:99], v[52:55], v[20:23]\n\t"
        "ds_read_b128 v[96:99], v116 offset:26624\n\t"
        "v_mfma_f32_16x16x32_bf16 v[8:11], v[148:151], v[52:55], v[8:11]\n\t"
        "v_mfma_f32_16x16x32_bf16 v[12:15], v[152:155], v[52:55], v[12:15]\n\t"
        "v_mul_f32_e32 v107, v102, v107\n\t"
        "v_mfma_f32_16x16x32_bf16 v[16:19], v[156:159], v[52:55], v[16:19]\n\t"
        "buffer_load_dwordx4 v[52:55], v117, %[sw], %[t0] offen offset:3072 sc1\n\t"
        "s_waitcnt vmcnt(15) lgkmcnt(2)\n\t"
        "v_mfma_f32_16x16x32_bf16 v[20:23], v[88:91], v[56:59], v[20:23]\n\t"
        "ds_read_b128 v[88:91], v116 offset:27648\n\t"
        "v_mfma_f32_16x16x32_bf16 v[8:11], v[160:163], v[56:59], v[8:11]\n\t"
        "v_mfma_f32_16x16x32_bf16 v[12:15], v[164:167], v[56:59], v[12:15]\n\t"
        "v_cvt_pk_bf16_f32 v107, 0, v107\n\t"
        "v_mfma_f32_16x16x32_bf16 v[16:19], v[168:171], v[56:59], v[16:19]\n\t"
        "s_add_u32 %[t0], %[ro], 2\n\t"
        "s_and_b32 %[t0], %[t0], 7\n\t"
        "s_lshl_b32 %[t0], %[t0], 12\n\t"
        "s_add_u32 %[t0], %[t0], %[nx]\n\t"
        "buffer_load_dwordx4 v[56:59], v117, %[sw], %[t0] offen offset:0 sc1\n\t"
        "s_waitcnt vmcnt(15) lgkmcnt(2)\n\t"
        "v_mfma_f32_16x16x32_bf16 v[20:23], v[92:95], v[60:63], v[20:23]\n\t"
        "ds_read_b128 v[92:95], v116 offset:28672\n\t"
        "v_mfma_f32_16x16x32_bf16 v[8:11], v[172:175], v[60:63], v[8:11]\n\t"
        "v_mfma_f32_16x16x32_bf16 v[12:15], v[176:179], v[60:63], v[12:15]\n\t"
        "v_lshlrev_b32_e32 v108, 16, v101\n\t"
        "v_mfma_f32_16x16x32_bf16 v[16:19], v[180:183], v[60:63], v[16:19]\n\t"
        "buffer_load_dwordx4 v[60:63], v117, %[sw], %[t0] offen offset:1024 sc1\n\t"
        "s_waitcnt vmcnt(15) lgkmcnt(2)\n\t"
        "v_mfma_f32_16x16x32_bf16 v[20:23], v[96:99], v[64:67], v[20:23]\n\t"
        "ds_read_b128 v[96:99], v116 offset:29696\n\t"
        "v_mfma_f32_16x16x32_bf16 v[8:11], v[184:187], v[64:67], v[8:11]\n\t"
        "v_mfma_f32_16x16x32_bf16 v[12:15], v[188:191], v[64:67], v[12:15]\n\t"
        "v_mul_f32_e32 v108, v102, v108\n\t"
        "v_mfma_f32_16x16x32_bf16 v[16:19], v[192:195], v[64:67], v[16:19]\n\t"
        "buffer_load_dwordx4 v[64:67], v117, %[sw], %[t0] offen offset:2048 sc1\n\t"
        "s_waitcnt vmcnt(15) lgkmcnt(2)\n\t"
        "v_mfma_f32_16x16x32_bf16 v[20:23], v[88:91], v[68:71], v[20:23]\n\t"
        "ds_read_b128 v[88:91], v116 offset:30720\n\t"
        "v_mfma_f32_16x16x32_bf16 v[8:11], v[196:199], v[68:71], v[8:11]\n\t"
        "v_mfma_f32_16x16x32_bf16 v[12:15], v[200:203], v[68:71], v[12:15]\n\t"
        "v_cvt_pk_bf16_f32 v108, 0, v108\n\t"
        "v_mfma_f32_16x16x32_bf16 v[16:19], v[204:207], v[68:71], v[16:19]\n\t"
        "buffer_load_dwordx4 v[68:71], v117, %[sw], %[t0] offen offset:3072 sc1\n\t"
        "s_waitcnt vmcnt(15) lgkmcnt(2)\n\t"
        "v_mfma_f32_16x16x32_bf16 v[20:23], v[92:95], v[72:75], v[20:23]\n\t"
        "ds_read_b128 v[92:95], v116 offset:31744\n\t"
        "v_mfma_f32_16x16x32_bf16 v[8:11], v[208:211], v[72:75], v[8:11]\n\t"
        "v_mfma_f32_16x16x32_bf16 v[12:15], v[212:215], v[72:75], v[12:15]\n\t"
        "v_and_b32_e32 v109, 0xffff0000, v101\n\t"
        "v_mfma_f32_16x16x32_bf16 v[16:19], v[216:219], v[72:75], v[16:19]\n\t"
        "s_add_u32 %[t0], %[ro], 3\n\t"
        "s_and_b32 %[t0], %[t0], 7\n\t"
        "s_lshl_b32 %[t0], %[t0], 12\n\t"
        "s_add_u32 %[t0], %[t0], %[nx]\n\t"
        "buffer_load_dwordx4 v[72:75], v117, %[sw], %[t0] offen offset:0 sc1\n\t"
        "s_waitcnt vmcnt(15) lgkmcnt(2)\n\t"
        "v_mfma_f32_16x16x32_bf16 v[20:23], v[96:99], v[76:79], v[20:23]\n\t"
        "v_mfma_f32_16x16x32_bf16 v[8:11], v[220:223], v[76:79], v[8:11]\n\t"
        "v_mfma_f32_16x16x32_bf16 v[12:15], v[224:227], v[76:79], v[12:15]\n\t"
        "v_mul_f32_e32 v109, v102, v109\n\t"
        "v_mfma_f32_16x16x32_bf16 v[16:19], v[228:231], v[76:79], v[16:19]\n\t"
        "buffer_load_dwordx4 v[76:79], v117, %[sw], %[t0] offen offset:1024 sc1\n\t"
        "s_waitcnt vmcnt(15) lgkmcnt(1)\n\t"
        "v_mfma_f32_16x16x32_bf16 v[20:23], v[88:91], v[80:83], v[20:23]\n\t"
        "ds_read_b128 v[88:91], v116 offset:0\n\t"
        "v_mfma_f32_16x16x32_bf16 v[8:11], v[232:235], v[80:83], v[8:11]\n\t"
        "v_mfma_f32_16x16x32_bf16 v[12:15], v[236:239], v[80:83], v[12:15]\n\t"
        "v_cvt_pk_bf16_f32 v109, 0, v109\n\t"
        "v_mfma_f32_16x16x32_bf16 v[16:19], v[240:243], v[80:83], v[16:19]\n\t"
        "buffer_load_dwordx4 v[80:83], v117, %[sw], %[t0] offen offset:2048 sc1\n\t"
        "s_waitcnt vmcnt(15) lgkmcnt(1)\n\t"
        "v_mfma_f32_16x16x32_bf16 v[20:23], v[92:95], v[84:87], v[20:23]\n\t"
        "ds_read_b128 v[92:95], v116 offset:1024\n\t"
        "v_mfma_f32_16x16x32_bf16 v[8:11], v[244:247], v[84:87], v[8:11]\n\t"
        "ds_read_b128 v[96:99], v116 offset:2048\n\t"
        "v_mfma_f32_16x16x32_bf16 v[12:15], v[248:251], v[84:87], v[12:15]\n\t"
        "v_mfma_f32_16x16x32_bf16 v[16:19], v[252:255], v[84:87], v[16:19]\n\t"
        "buffer_load_dwordx4 v[84:87], v117, %[sw], %[t0] offen offset:3072 sc1\n\t"
        "s_lshl_b32 %[t0], %[tl], 15\n\t"
        "s_nop 0\n\t"
        "ds_write_b128 v122, v[20:23] offset:192\n\t"
        "s_nop 0\n\t"
        "ds_write_b128 v122, v[8:11] offset:0\n\t"
        "s_nop 0\n\t"
        "ds_write_b128 v122, v[12:15] offset:64\n\t"
        "ds_write_b128 v122, v[16:19] offset:128\n\t"
        "s_waitcnt lgkmcnt(0)\n\t"
        "s_barrier\n\t"
        "ds_read_b128 v[8:11], v123 offset:0\n\t"
        "ds_read_b128 v[12:15], v123 offset:4352\n\t"
        "ds_read_b128 v[16:19], v123 offset:8704\n\t"
        "ds_read_b128 v[20:23], v123 offset:13056\n\t"
        "s_waitcnt lgkmcnt(2)\n\t"
        "v_pk_add_f32 v[8:9], v[8:9], v[12:13]\n\t"
        "v_pk_add_f32 v[10:11], v[10:11], v[14:15]\n\t"
        "s_waitcnt lgkmcnt(0)\n\t"
        "v_pk_add_f32 v[16:17], v[16:17], v[20:21]\n\t"
        "v_pk_add_f32 v[18:19], v[18:19], v[22:23]\n\t"
        "v_pk_add_f32 v[8:9], v[8:9], v[16:17]\n\t"
        "v_pk_add_f32 v[10:11], v[10:11], v[18:19]\n\t"
        "v_cvt_pk_bf16_f32 v8, 0, v8\n\t"
        "v_cvt_pk_bf16_f32 v9, 0, v9\n\t"
        "v_cvt_pk_bf16_f32 v10, 0, v10\n\t"
        "v_cvt_pk_bf16_f32 v11, 0, v11\n\t"
        "v_mul_f32_e32 v8, v104, v8\n\t"
        "v_mul_f32_e32 v9, v104, v9\n\t"
        "v_mul_f32_e32 v10, v104, v10\n\t"
        "v_mul_f32_e32 v11, v104, v11\n\t"
        "v_cvt_pk_bf16_f32 v8, 0, v8\n\t"
        "v_cvt_pk_bf16_f32 v9, 0, v9\n\t"
        "v_cvt_pk_bf16_f32 v10, 0, v10\n\t"
        "v_cvt_pk_bf16_f32 v11, 0, v11\n\t"
        "v_add_f32_e32 v8, v106, v8\n\t"
        "v_add_f32_e32 v9, v107, v9\n\t"
        "v_add_f32_e32 v10, v108, v10\n\t"
        "v_add_f32_e32 v11, v109, v11\n\t"
        "v_cvt_pk_bf16_f32 v104, v8, v9\n\t"
        "v_cvt_pk_bf16_f32 v105, v10, v11\n\t"
        "buffer_store_dwordx2 v[104:105], v121, %[su], %[t0] offen sc1\n\t"
        "s_add_u32 %[tl], %[tl], 8\n\t"
        "s_cmp_ge_u32 %[tl], 128\n\t"
        "s_cbranch_scc1 LP_end%=\n\t"
        "s_cmp_lg_u32 %[nx], 0xf0000000\n\t"
        "s_cbranch_scc1 LP_tile%=\n\t"
        "s_branch LP_wait%=\n\t"
        "LP_end%=:\n\t"
        "s_waitcnt vmcnt(0) lgkmcnt(0)\n\t"
        ".Lk_end_P:\n\t"
        : [t0] "=&s"(t0), [t1] "=&s"(t1), [tl] "+s"(tl), [pg] "=&s"(pg), [cu] "=&s"(cu), [nx] "=&s"(nx), [sp] "=&s"(sp), [tu] "=&s"(tu)
        : [so] "s"(so), [sw] "s"(sw), [su] "s"(su), [cv] "s"(F_CVY + nt * 128), [ro] "s"(PROT ? (nt + 2 * rp) & 7 : 0), [db] "s"(W_DBG + (128 + j) * 8192 + 512),
          "{v110}"((w * 1024 + (l >> 3)) * 2048 + nt * 128 + (l & 7) * 16), "{v111}"(stg + ((l * 16) ^ ((l & 48) << 1))), "{v112}"(lr), "{v113}"(lr ^ 32u), "{v114}"(lr ^ 64u), "{v115}"(lr ^ 96u),
          "{v116}"(lb + w * 32768 + l * 16), "{v117}"(W_OX + w * 32768 + l * 16), "{v118}"(F_PROG + (w * 8 + (l & 7)) * 128),
          "{v119}"(W_A + ((nt * 2 + (n >> 3)) * 4 + 2 * (n & 1)) * 32768 + w * 4096 + ((n & 7) >> 1) * 16 + g * 4), "{v120}"(W_GATE + (4 * w + g) * 4), "{v121}"((4 * w + g) * 2048 + nt * 128 + n * 8),
          "{v122}"(lb + 131072 + w * 4352 + n * 272 + g * 16), "{v123}"(lb + 131072 + (4 * w + g) * 272 + n * 16)
        : "memory", "scc", "vcc", "v8", "v9", "v10", "v11", "v12", "v13", "v14", "v15", "v16", "v17", "v18", "v19", "v20", "v21", "v22", "v23", "v24", "v25", "v26", "v27", "v28", "v29", "v30", "v31", "v32", "v33", "v34", "v35", "v36", "v37", "v38", "v39", "v40", "v41", "v42", "v43", "v44", "v45", "v46", "v47", "v48", "v49", "v50", "v51", "v52", "v53", "v54", "v55", "v56", "v57", "v58", "v59", "v60", "v61", "v62", "v63", "v64", "v65", "v66", "v67", "v68", "v69", "v70", "v71", "v72", "v73", "v74", "v75", "v76", "v77", "v78", "v79", "v80", "v81", "v82", "v83", "v84", "v85", "v86", "v87", "v88", "v89", "v90", "v91", "v92", "v93", "v94", "v95", "v96", "v97", "v98", "v99", "v100", "v101", "v102", "v103", "v104", "v105", "v106", "v107", "v108", "v109", "v128", "v129", "v130", "v131", "v132", "v133", "v134", "v135", "v136", "v137", "v138", "v139", "v140", "v141", "v142", "v143", "v144", "v145", "v146", "v147", "v148", "v149", "v150", "v151", "v152", "v153", "v154", "v155", "v156", "v157", "v158", "v159", "v160", "v161", "v162", "v163", "v164", "v165", "v166", "v167", "v168", "v169", "v170", "v171", "v172", "v173", "v174", "v175", "v176", "v177", "v178", "v179", "v180", "v181", "v182", "v183", "v184", "v185", "v186", "v187", "v188", "v189", "v190", "v191", "v192", "v193", "v194", "v195", "v196", "v197", "v198", "v199", "v200", "v201", "v202", "v203", "v204", "v205", "v206", "v207", "v208", "v209", "v210", "v211", "v212", "v213", "v214", "v215", "v216", "v217", "v218", "v219", "v220", "v221", "v222", "v223", "v224", "v225", "v226", "v227", "v228", "v229", "v230", "v231", "v232", "v233", "v234", "v235", "v236", "v237", "v238", "v239", "v240", "v241", "v242", "v243", "v244", "v245", "v246", "v247", "v248", "v249", "v250", "v251", "v252", "v253", "v254", "v255", "a0", "a1", "a2", "a3", "a4", "a5", "a6", "a7", "a8", "a9", "a10", "a11", "a12", "a13", "a14", "a15", "a16", "a17", "a18", "a19", "a20", "a21", "a22", "a23", "a24", "a25", "a26", "a27", "a28", "a29", "a30", "a31", "a32", "a33", "a34", "a35", "a36", "a37", "a38", "a39", "a40", "a41", "a42", "a43", "a44", "a45", "a46", "a47", "a48", "a49", "a50", "a51", "a52", "a53", "a54", "a55", "a56", "a57", "a58", "a59", "a60", "a61", "a62", "a63", "a64", "a65", "a66", "a67", "a68", "a69", "a70", "a71", "a72", "a73", "a74", "a75", "a76", "a77", "a78", "a79", "a80", "a81", "a82", "a83", "a84", "a85", "a86", "a87", "a88", "a89", "a90", "a91", "a92", "a93", "a94", "a95", "a96", "a97", "a98", "a99", "a100", "a101", "a102", "a103", "a104", "a105", "a106", "a107", "a108", "a109", "a110", "a111", "a112", "a113", "a114", "a115", "a116", "a117", "a118", "a119", "a120", "a121", "a122", "a123", "a124", "a125", "a126", "a127", "a128", "a129", "a130", "a131", "a132", "a133", "a134", "a135", "a136", "a137", "a138", "a139", "a140", "a141", "a142", "a143", "a144", "a145", "a146", "a147", "a148", "a149", "a150", "a151", "a152", "a153", "a154", "a155", "a156", "a157", "a158", "a159", "a160", "a161", "a162", "a163", "a164", "a165", "a166", "a167", "a168", "a169", "a170", "a171", "a172", "a173", "a174", "a175", "a176", "a177", "a178", "a179", "a180", "a181", "a182", "a183", "a184", "a185", "a186", "a187", "a188", "a189", "a190", "a191", "a192", "a193", "a194", "a195", "a196", "a197", "a198", "a199", "a200", "a201", "a202", "a203", "a204", "a205", "a206", "a207", "a208", "a209", "a210", "a211", "a212", "a213", "a214", "a215", "a216", "a217", "a218", "a219", "a220", "a221", "a222", "a223", "a224", "a225", "a226", "a227", "a228", "a229", "a230", "a231", "a232", "a233", "a234", "a235", "a236", "a237", "a238", "a239", "a240", "a241", "a242", "a243", "a244", "a245", "a246", "a247", "a248", "a249", "a250", "a251", "a252", "a253", "a254", "a255");
}
__device__ __forceinline__ void bulk_block(const Params& p, rsrc_t r, unsigned char* lds, const int wv, const int j) {
    BSTAMP(0);
    proj_block(p, lds, wv, (j & 6) * 4 + (j >> 4), ((j >> 2) & 2) | (j & 1), j);
    BSTAMP(2);
    if (!conv_core(p, r, lds, wv, j)) return;
    BSTAMP(3);
    if (GDLY) wait_long(rtnow() + GDLY);
    out_block(p, lds, wv, ((j & 7) >> 1) + (j & 1) * 4, j >> 3, j);
    BSTAMP(7);
}

__device__ __forceinline__ void rec_block(const Params& p, rsrc_t r, unsigned char* lds, const int wv, const int b) {
    const int xq = (b & 7) >> 1, t = (b >> 3) * 2 + (b & 1);
    const unsigned lb = __builtin_amdgcn_readfirstlane((unsigned)(size_t)lds);
    int kind, id, rg = 0, rot = 0, bq = 99;
    if (RPL == 0) { kind = t < 8 ? 0 : t < 24 ? 1 : 2; id = t < 8 ? xq * 8 + t : t < 24 ? (xq & 1) * 16 + t - 8 : xq * 8 + t - 24; rg = xq >> 1; }
    else if (RPL == 4) { kind = t < 8 ? 0 : t < 24 ? 1 : 2; id = t < 8 ? xq * 8 + t : t < 24 ? xq * 8 + ((t - 8) >> 1) : xq * 8 + t - 24; rg = t & 1; }
    else if (RPL == 1) { kind = xq == 0 ? 0 : xq < 3 ? 1 : 2; id = t; rg = xq - 1; }
    else if (RPL == 2) { const int na = xq == 2 ? 10 : 11, q = xq * 21 + t - na; kind = xq == 3 ? 2 : t < na ? 0 : 1; id = xq == 3 ? t : t < na ? xq * 11 + t : q >> 1; rg = q & 1; }
    else { const int na = xq == 2 ? 12 : 10, q = t - na; kind = xq == 3 ? 2 : t < na ? 0 : 1; rg = xq < 2 ? xq : q & 1; bq = q;
        const int c2 = q >> 1, cs = xq < 2 ? (q < 6 ? q : q < 20 ? q + 2 : q + 4) : (c2 < 2 ? 6 + c2 : c2 < 4 ? 20 + c2 : 22 + c2);
        id = xq == 3 ? t : t < na ? xq * 10 + t : cs; rot = !ROT ? 0 : xq < 2 ? (cs & 1) * 4 : ((cs & 1) * 2 + rg) * 2; }
    if (kind == 0) rec_a(p, lb, wv, id, xq);
    else if (kind == 1) rec_b(p, lb, wv, id, rg, xq, rot, bq);
    else {
        const int hi = id, j = hi - 32; (void)j;
        BSTAMP(0);
        rec_helper(p, r, lds, wv, hi);
        BSTAMP(6);
        if (XWAIT == 1) { if (!wait_word(r, F_GO + (hi >> 1) * 128, 1u, 1024u)) return; }
        if (XSTG) wait_long(rtnow() + (hi & 7) * XSTG);
        gate_block(p, r, hi, wv);
        BSTAMP(2);
        if (XWAIT == 2) { if (!wait_word(r, F_GO + (hi >> 1) * 128, 1u, 1024u)) return; }
        conv_prep(p, r, wv, hi);
        BSTAMP(3);
    }
}

__global__ void __launch_bounds__(256, 1) k_main(Params p) {
    extern __shared__ __attribute__((aligned(16))) unsigned char lds[];
    rsrc_t r = __builtin_amdgcn_make_buffer_rsrc(p.ws, 0, W_TOTAL, 0x00020000);
    const int wv = __builtin_amdgcn_readfirstlane(threadIdx.x >> 6);
    int role = blockIdx.x;
    unsigned ctouch = 0u;
    if (CODE_T) {
        unsigned long long pc; int ea, eb, eg;
        asm volatile("s_getpc_b64 %0\n.Lk_begin:\n\ts_mov_b32 %1, .Lk_end_A-.Lk_begin\n\ts_mov_b32 %2, .Lk_end_B-.Lk_begin\n\ts_mov_b32 %3, .Lk_end_P-.Lk_begin" : "=s"(pc), "=s"(ea), "=s"(eb), "=s"(eg));
        const int o = ((blockIdx.x >> 3) * 64 + (threadIdx.x >> 2)) * 128;
        if ((threadIdx.x & 3) == 0 && o < max(ea, max(eb, eg)) - 4) asm volatile("global_load_dword %0, %1, off" : "+v"(ctouch) : "v"((const unsigned char*)pc + o) : "memory");
    }
    if (threadIdx.x == 0) {
        unsigned xcc; asm volatile("s_getreg_b32 %0, hwreg(HW_REG_XCC_ID)" : "=s"(xcc)); xcc &= 7u;
        unsigned* tk = (unsigned*)(p.ws + F_TKT);
        unsigned t = __hip_atomic_fetch_add(tk + xcc * 16, 1u, __ATOMIC_RELAXED, __HIP_MEMORY_SCOPE_AGENT);
        if (t >= 32u) {
            unsigned o = __hip_atomic_fetch_add(tk + 128, 1u, __ATOMIC_RELAXED, __HIP_MEMORY_SCOPE_AGENT);
            for (unsigned spin = 0; spin < (1u << 22); ++spin) { unsigned tot = 0u;
                for (int x = 0; x < 8; ++x) tot += __hip_atomic_load(tk + x * 16, __ATOMIC_RELAXED, __HIP_MEMORY_SCOPE_AGENT);
                if (tot >= 256u) break; }
            for (int x = 0; x < 8; ++x) { const unsigned cnt = __hip_atomic_load(tk + x * 16, __ATOMIC_RELAXED, __HIP_MEMORY_SCOPE_AGENT), fr = cnt < 32u ? 32u - cnt : 0u;
                if (o < fr) { xcc = (unsigned)x; t = cnt + o; break; }
                o -= fr; }
        }
        *(volatile int*)lds = (int)((xcc >> 2) * 128u + (t >> 1) * 8u + (xcc & 3u) * 2u + (t & 1u));
    }
    asm volatile("s_waitcnt vmcnt(0)" : "+v"(ctouch) :: "memory");
    __syncthreads();
    role = __builtin_amdgcn_readfirstlane(*(volatile int*)lds) & 255;
    __syncthreads();
    if (role >= 128) bulk_block(p, r, lds, wv, role - 128); else rec_block(p, r, lds, wv, role);
}

extern "C" void kernel_launch(void* const* d_in, const int* in_sizes, int n_in, void* d_out, int out_size, void* d_ws, size_t ws_size, hipStream_t stream) {
    Params p;
    p.Aq = (const bf16_t*)d_in[0]; p.Ak = (const bf16_t*)d_in[1]; p.Av = (const bf16_t*)d_in[2];
    p.shift_k = (const bf16_t*)d_in[3]; p.ssm_k = (const bf16_t*)d_in[4]; p.ssm_D = (const bf16_t*)d_in[5];
    p.mem0 = (const bf16_t*)d_in[7]; p.Wq = (const bf16_t*)d_in[8]; p.Wk = (const bf16_t*)d_in[9];
    p.Wv = (const bf16_t*)d_in[10]; p.Wo = (const bf16_t*)d_in[11]; p.gate_w = (const bf16_t*)d_in[13];
    p.out = (bf16_t*)d_out; p.ws = (unsigned char*)d_ws;
    unsigned char* ws = (unsigned char*)d_ws;
    (void)hipMemsetAsync(ws, 0, FLG_BYTES, stream);
    (void)hipFuncSetAttribute((const void*)k_main, hipFuncAttributeMaxDynamicSharedMemorySize, L_TOTAL);
    k_main<<<256, 256, L_TOTAL, stream>>>(p);
}
```

```cpp
#include <hip/hip_runtime.h>
#include <stdint.h>
#include <type_traits>
typedef unsigned short bf16_t;
using bf16x8 = __attribute__((ext_vector_type(8))) short;
using f32x4 = __attribute__((ext_vector_type(4))) float;
typedef unsigned v4u __attribute__((ext_vector_type(4)));
typedef unsigned v2u __attribute__((ext_vector_type(2)));
typedef __amdgpu_buffer_rsrc_t rsrc_t;

__device__ __forceinline__ float bf2f(bf16_t b) { return __uint_as_float(((unsigned)b) << 16); }
typedef __bf16 hbf2 __attribute__((ext_vector_type(2)));
typedef float f32x2 __attribute__((ext_vector_type(2)));
__device__ __forceinline__ unsigned pack2(float a, float b) { f32x2 v = {a, b}; return __builtin_bit_cast(unsigned, __builtin_convertvector(v, hbf2)); }
__device__ __forceinline__ float rbfh(float f) { return __uint_as_float(pack2(f, 0.f) << 16); }
__device__ __forceinline__ unsigned nosent(unsigned x) {
    typedef unsigned short us2 __attribute__((ext_vector_type(2)));
    const us2 a = __builtin_bit_cast(us2, x), c = {(unsigned short)0xFFFE, (unsigned short)0xFFFE}; return __builtin_bit_cast(unsigned, __builtin_elementwise_min(a, c)); }

constexpr int S = 2048, D = 1024, H = 32, DH = 128, M = 8, SEG = 32, NSEG = 64;

constexpr unsigned W_FLG = 0;
constexpr unsigned F_KVQ = W_FLG;
constexpr unsigned F_CONV = W_FLG + 32768;
constexpr unsigned F_ERR = W_FLG + 32768 + 256;
constexpr unsigned F_PREP = W_FLG + 32768 + 512;
constexpr unsigned F_CAL = W_FLG + 33792;
constexpr unsigned F_TKT = W_FLG + 34816;
constexpr unsigned F_BEAT = W_FLG + 40960;
constexpr unsigned F_PROG = W_FLG + 49152;
constexpr unsigned F_CVU = W_FLG + 53248, F_CVY = W_FLG + 57344;
constexpr unsigned F_OP = W_FLG + 65536;
constexpr unsigned F_ATT = W_FLG + 131072;
constexpr unsigned F_RDY = W_FLG + 147456;
constexpr unsigned FLG_BYTES = 180224;
constexpr unsigned HO_KVM = W_FLG + FLG_BYTES;
constexpr unsigned W_A = HO_KVM + 64 * 32 * 4096;
constexpr unsigned W_GATE = W_A + S * D * 2;
constexpr unsigned W_QX = W_GATE + 8192;
constexpr unsigned W_KX = W_QX + S * 4096 * 2;
constexpr unsigned W_VF = W_KX + S * 4096 * 2;
constexpr unsigned W_OX = W_VF + 64 * 32 * 8192;
constexpr unsigned W_PART = W_OX + S * 4096 * 2;
constexpr unsigned W_Y = W_PART;
constexpr unsigned W_DBG = W_PART + 32 * 4 * 64 * 1024 * 4;
constexpr unsigned W_U = W_DBG + 256 * 64 * 16 * 8;
constexpr unsigned W_TOTAL = W_U + S * D * 2;

__device__ __forceinline__ v4u ld16(rsrc_t r, unsigned off) { return __builtin_amdgcn_raw_buffer_load_b128(r, off, 0, 16); }
__device__ __forceinline__ v2u ld8(rsrc_t r, unsigned off) { return __builtin_amdgcn_raw_buffer_load_b64(r, off, 0, 16); }
__device__ __forceinline__ unsigned ld4(rsrc_t r, unsigned off) { return __builtin_amdgcn_raw_buffer_load_b32(r, off, 0, 16); }
__device__ __forceinline__ void st16(rsrc_t r, unsigned off, v4u v) { __builtin_amdgcn_raw_buffer_store_b128(v, r, off, 0, 16); }
__device__ __forceinline__ void st8(rsrc_t r, unsigned off, v2u v) { __builtin_amdgcn_raw_buffer_store_b64(v, r, off, 0, 16); }
__device__ __forceinline__ void st4(rsrc_t r, unsigned off, unsigned v) { __builtin_amdgcn_raw_buffer_store_b32(v, r, off, 0, 16); }
__device__ __forceinline__ bool okdw(unsigned x) { return ((x & 0xFFFFu) != 0xFFFFu) & ((x >> 16) != 0xFFFFu); }
typedef unsigned short us2_t __attribute__((ext_vector_type(2)));
__device__ __forceinline__ unsigned pkmax(unsigned a, unsigned b) { return __builtin_bit_cast(unsigned, __builtin_elementwise_max(__builtin_bit_cast(us2_t, a), __builtin_bit_cast(us2_t, b))); }
__device__ __forceinline__ unsigned pkmax4(v4u v) { return pkmax(pkmax(v.x, v.y), pkmax(v.z, v.w)); }
__device__ __forceinline__ bool ok16(v4u v) { return okdw(pkmax4(v)); }
__device__ __forceinline__ bool ok8(v2u v) { return okdw(v.x) & okdw(v.y); }
__device__ __forceinline__ bf16x8 as_frag(v4u v) { return __builtin_bit_cast(bf16x8, v); }
#define MFMA(a, b, c) __builtin_amdgcn_mfma_f32_16x16x32_bf16((a), (b), (c), 0, 0, 0)
#define D1 130
#define D2 80
#define D3 76
#define LT_A 1
#define LB0 12
#define LC0 20
#define LA0 24
#define TR_UP 4
#define TR_DN 2
#define L_DEC 3
#define WARM1 0
#define KROT 0
#define STAG 1
#define UP_SH 2
#define EPI_SLP 0
#define SITA 1
#define PAIRX 4
#define DUE_P 1
#define LATE 1
#define EARLY_FROM 56
#define SKEW 0
#define OUT_WARM 1
#define ADAPT 1
#define RPL 0
#define CV_POL 2
#define POLL_SLP 4
#define TOUCH 1
#define ROT 0
#define CODE_T 1
#define CAL_T 450u
#define GTOUCH 0
#define XTOUCH 32
#define ST_P 32
#define CV_NAP 4000u
#define XSLP 2
#define XDLY 0
#define GDLY 0
#define YDLY 0
#define PROT 3
#define XSTG 0
#define XG 0
#define MS_BYTES 65536
#define BEAT_SLP 0
#define T2U 2
#define T2A 0
#define T2F 0
#define XWAIT 1
#define PEN 48
#define MEMBAR() asm volatile("" ::: "memory")
#define FT(i) do { } while (0)
#define ENDPGM() asm volatile("s_endpgm" ::: "memory")
#define DRAIN() asm volatile("s_waitcnt vmcnt(0)" ::: "memory")

__device__ __forceinline__ float xor16_max(float x) { auto r = __builtin_amdgcn_permlane16_swap(__float_as_uint(x), __float_as_uint(x), false, false); return fmaxf(__uint_as_float(r[0]), __uint_as_float(r[1])); }
__device__ __forceinline__ float xor32_max(float x) { auto r = __builtin_amdgcn_permlane32_swap(__float_as_uint(x), __float_as_uint(x), false, false); return fmaxf(__uint_as_float(r[0]), __uint_as_float(r[1])); }
__device__ __forceinline__ float xor16_add(float x) { auto r = __builtin_amdgcn_permlane16_swap(__float_as_uint(x), __float_as_uint(x), false, false); return __uint_as_float(r[0]) + __uint_as_float(r[1]); }
__device__ __forceinline__ float xor32_add(float x) { auto r = __builtin_amdgcn_permlane32_swap(__float_as_uint(x), __float_as_uint(x), false, false); return __uint_as_float(r[0]) + __uint_as_float(r[1]); }
__device__ __forceinline__ v2u tr_read(unsigned lds_addr) { v2u r; asm volatile("ds_read_b64_tr_b16 %0, %1\n\ts_waitcnt lgkmcnt(0)" : "=&v"(r) : "v"(lds_addr) : "memory"); return r; }

typedef short v4i16_t __attribute__((ext_vector_type(4)));
__device__ __forceinline__ v2u trb(unsigned lds_addr) { return __builtin_bit_cast(v2u, __builtin_amdgcn_ds_read_tr16_b64_v4i16((__attribute__((address_space(3))) v4i16_t*)(size_t)lds_addr)); }
__device__ __forceinline__ int otid_(int wv) { int t; asm volatile("v_mbcnt_lo_u32_b32 %0, -1, 0\n\tv_mbcnt_hi_u32_b32 %0, -1, %0" : "=v"(t)); return wv * 64 + t; }
#define otid() otid_(wv)
__device__ __forceinline__ bool wait_word(rsrc_t r, unsigned off, unsigned want, unsigned code) {
    for (unsigned spin = 0;; ++spin) { MEMBAR();
        const unsigned v = __builtin_amdgcn_readfirstlane(ld4(r, off));
        if (v >= want) return true;
        if (spin > (1u << 20)) { st4(r, F_ERR, code); return false; }
        __builtin_amdgcn_s_sleep(8); }
}

__device__ __forceinline__ unsigned rtnow() { return (unsigned)__builtin_amdgcn_s_memrealtime(); }
__device__ __forceinline__ void wait_until(unsigned target) { for (int i = 0; i < 4096; ++i) { if ((int)(rtnow() - target) >= 0) break; } }
__device__ __forceinline__ void wait_long(unsigned target) { for (int i = 0; i < 4096; ++i) { if ((int)(rtnow() - target) >= 0) break; __builtin_amdgcn_s_sleep(8); } }
constexpr int RED_G = 320;
constexpr int RED_T = 4 * RED_G;
constexpr int L_RED = 0;
constexpr int L_KX = 65536;
constexpr int KXP = 272;
constexpr int L_VF = L_KX + 2 * 32 * KXP;
constexpr int L_QKV = L_VF + 16384;
constexpr int L_OFF = L_QKV + 6144;
constexpr int L_STMP = L_OFF + 256;
constexpr int L_TOTAL = 163840;
constexpr unsigned U_A = 0, U_S = 32, U_B = 36, U_INIT = 113, U_C = 128, U_END = 512;
#define PLACE 1
#define CAL_N 5
#define STEP_MIN 190u

struct Params {
    const bf16_t *Aq, *Ak, *Av, *shift_k, *ssm_k, *ssm_D, *mem0, *Wq, *Wk, *Wv, *Wo, *gate_w;
    bf16_t* out; unsigned char* ws;
};
__host__ __device__ __forceinline__ unsigned hx_base(const void* ws) { const size_t a = (size_t)ws; return (unsigned)((((a + W_TOTAL + 0x7FFFFFu) >> 23) << 23) - a); }

#define STAMP(i) do { } while (0)
#define BSTAMP(i) do { } while (0)
#define EV(code) do { } while (0)
#define SPINS(i) do { } while (0)
typedef short v4s __attribute__((ext_vector_type(4)));
typedef unsigned v32u __attribute__((ext_vector_type(32)));
typedef unsigned v16u __attribute__((ext_vector_type(16)));
static_assert(L_VF - L_KX == 16384 + 1024, "fragment read offsets");
#define MFMA16(a, b, c) __builtin_amdgcn_mfma_f32_16x16x16bf16_1k(__builtin_bit_cast(v4s, (a)), __builtin_bit_cast(v4s, (b)), (c), 0, 0, 0)
__device__ __forceinline__ unsigned ror8(unsigned x) { return (unsigned)__builtin_amdgcn_update_dpp(0, (int)x, 0x128, 0xF, 0xF, false); }
__device__ __forceinline__ unsigned qx1(unsigned x) { return (unsigned)__builtin_amdgcn_update_dpp(0, (int)x, 0xB1, 0xF, 0xF, false); }
__device__ __forceinline__ unsigned wave_pkmax(unsigned d) {
    d = pkmax(d, (unsigned)__builtin_amdgcn_update_dpp((int)d, (int)d, 0xB1, 0xF, 0xF, false)); d = pkmax(d, (unsigned)__builtin_amdgcn_update_dpp((int)d, (int)d, 0x4E, 0xF, 0xF, false));
    d = pkmax(d, (unsigned)__builtin_amdgcn_update_dpp((int)d, (int)d, 0x124, 0xF, 0xF, false)); d = pkmax(d, (unsigned)__builtin_amdgcn_update_dpp((int)d, (int)d, 0x128, 0xF, 0xF, false));
    { auto q = __builtin_amdgcn_permlane16_swap(d, d, false, false); d = pkmax(q[0], q[1]); }
    { auto q = __builtin_amdgcn_permlane32_swap(d, d, false, false); d = pkmax(q[0], q[1]); }
    return d; }
__device__ __forceinline__ int wave_max(int d) {
    d = max(d, __builtin_amdgcn_update_dpp(d, d, 0xB1, 0xF, 0xF, false)); d = max(d, __builtin_amdgcn_update_dpp(d, d, 0x4E, 0xF, 0xF, false));
    d = max(d, __builtin_amdgcn_update_dpp(d, d, 0x124, 0xF, 0xF, false)); d = max(d, __builtin_amdgcn_update_dpp(d, d, 0x128, 0xF, 0xF, false));
    { auto q = __builtin_amdgcn_permlane16_swap((unsigned)d, (unsigned)d, false, false); d = max((int)q[0], (int)q[1]); }
    { auto q = __builtin_amdgcn_permlane32_swap((unsigned)d, (unsigned)d, false, false); d = max((int)q[0], (int)q[1]); }
    return d; }

__device__ __forceinline__ v4u mk_srd(const void* p, unsigned bytes) { const size_t a = (size_t)p; v4u d;
    d.x = __builtin_amdgcn_readfirstlane((unsigned)a); d.y = __builtin_amdgcn_readfirstlane((unsigned)(a >> 32) & 0xFFFFu); d.z = __builtin_amdgcn_readfirstlane(bytes); d.w = 0x00020000u; return d; }
__device__ __forceinline__ bf16x8 ldsa(unsigned addr) { return *(const __attribute__((address_space(3))) bf16x8*)(size_t)addr; }
constexpr unsigned HX_BYTES = 2097152, OOB = 0xF0000000u;
constexpr unsigned F_RDY0 = W_FLG + 34304;
constexpr unsigned F_GO = W_FLG + 36864;
constexpr int L_KV = 90112, L_MS = 139264, L_IMG = 155648, IMG_PS = 1056, IMG_MS = 2112, LB_R = 8192, LB_STG = 16384, LB_STGP = 8704;
static_assert(F_KVQ == 0 && F_ERR == 33024 && F_CONV == 32768 && F_CAL == 33792 && F_RDY0 == 34304 && F_GO == 36864 && F_PROG == 49152 && W_QX == 12771328 && W_KX == 29548544 && W_OX == 63102976 && W_VF == 46325760 && W_DBG == 113434624 && L_KV == 90112 && L_MS == 139264 && L_IMG == 155648 && IMG_PS == 1056 && IMG_MS == 2112 && LB_R == 8192 && LB_STG == 16384 && LB_STGP == 8704, "constants of the generated code");
#define REC_S_IN "{s[8:11]}"(sh), "{s[12:15]}"(sw), "{s[16:19]}"(s1), "{s[20:23]}"(s2), "{s29}"(D1), "{s30}"(D2), "{s43}"(w), "{s44}"(id), "{s49}"(rb), "{s50}"(PEN), "{s51}"(LT_A), "{s45}"(F_GO + (cp * 4 + w) * 128)
__device__ __forceinline__ void rec_a(const Params& p, unsigned lb, const int wv, const int id, const int cp) {
    const int w = wv, l = otid() & 63, n = l & 15, g = l >> 4, h = id, rb = id;
    const v4u sh = mk_srd(p.ws + hx_base(p.ws), HX_BYTES - 8192), sw = mk_srd(p.ws, W_TOTAL), s1 = mk_srd(w < 2 ? p.Wq : p.Wk, 1024 * 4096 * 2), s2 = s1;
    const int r8 = l >> 3, pc = l & 7, pp = n >> 3, bb = (n >> 2) & 1, f = (g & 1) | (pp << 1);
    const unsigned stg = lb + 131072 + w * 8192;
    const unsigned g_w = r8 * 8192 + h * 256 + (w & 1) * 128 + pc * 16;
    const unsigned wb0 = stg + r8 * 128 + (((pc >> 1) ^ (r8 >> 2)) << 5) + (pc & 1) * 16, wb1 = wb0 ^ 64u;
    const unsigned rb0 = stg + (16 * (g >> 1) + 8 * pp + 4 * (g & 1) + bb) * 128 + (f << 5) + (n & 3) * 8;
    const unsigned a_mem = cp * 32768 + (w >> 1) * 16384 + (w & 1) * 4096 + l * 16;
    const unsigned l_ms = lb + L_MS + l * 16;
    const unsigned l_iw = lb + L_IMG + (w >> 1) * IMG_MS + (g >> 1) * IMG_PS + (64 * (w & 1) + n) * 8 + (g & 1) * 4;
    const unsigned l_iq = lb + L_IMG + (n & 1) * IMG_PS + (8 * g + (n >> 2)) * 8;
    const unsigned a_vm = (16 + (h >> 2)) * 16384 + (h & 3) * 2048 + w * 512 + (l & 31) * 16;
    const unsigned a_o = n < 8 ? (8 + (n >> 2) * 4 + (h >> 3)) * 16384 + (h & 7) * 1024 + w * 256 + (n & 3) * 64 + g * 16 : OOB;
    const unsigned a_ox = (((w & 1) * 32 + h) * 4 + w) * 1024 + l * 16;
    asm volatile(
        "s_lshl_b32 s34, s43, 2\n\t"
        "s_add_u32 s34, s34, 0\n\t"
        "s_and_b32 s34, s34, 15\n\t"
        "s_lshl_b32 s34, s34, 19\n\t"
        "buffer_load_dwordx4 v[224:227], v96, s[16:19], s34 offen nt\n\t"
        "s_add_u32 s34, s34, 0x10000\n\t"
        "buffer_load_dwordx4 v[228:231], v96, s[16:19], s34 offen nt\n\t"
        "s_add_u32 s34, s34, 0x10000\n\t"
        "buffer_load_dwordx4 v[232:235], v96, s[16:19], s34 offen nt\n\t"
        "s_add_u32 s34, s34, 0x10000\n\t"
        "buffer_load_dwordx4 v[236:239], v96, s[16:19], s34 offen nt\n\t"
        "s_add_u32 s34, s34, 0x10000\n\t"
        "buffer_load_dwordx4 v[240:243], v96, s[16:19], s34 offen nt\n\t"
        "s_add_u32 s34, s34, 0x10000\n\t"
        "buffer_load_dwordx4 v[244:247], v96, s[16:19], s34 offen nt\n\t"
        "s_add_u32 s34, s34, 0x10000\n\t"
        "buffer_load_dwordx4 v[248:251], v96, s[16:19], s34 offen nt\n\t"
        "s_add_u32 s34, s34, 0x10000\n\t"
        "buffer_load_dwordx4 v[252:255], v96, s[16:19], s34 offen nt\n\t"
        "s_lshl_b32 s34, s43, 2\n\t"
        "s_add_u32 s34, s34, 1\n\t"
        "s_and_b32 s34, s34, 15\n\t"
        "s_lshl_b32 s34, s34, 19\n\t"
        "buffer_load_dwordx4 v[0:3], v96, s[16:19], s34 offen nt\n\t"
        "s_add_u32 s34, s34, 0x10000\n\t"
        "buffer_load_dwordx4 v[4:7], v96, s[16:19], s34 offen nt\n\t"
        "s_add_u32 s34, s34, 0x10000\n\t"
        "buffer_load_dwordx4 v[8:11], v96, s[16:19], s34 offen nt\n\t"
        "s_add_u32 s34, s34, 0x10000\n\t"
        "buffer_load_dwordx4 v[12:15], v96, s[16:19], s34 offen nt\n\t"
        "s_add_u32 s34, s34, 0x10000\n\t"
        "buffer_load_dwordx4 v[16:19], v96, s[16:19], s34 offen nt\n\t"
        "s_add_u32 s34, s34, 0x10000\n\t"
        "buffer_load_dwordx4 v[20:23], v96, s[16:19], s34 offen nt\n\t"
        "s_add_u32 s34, s34, 0x10000\n\t"
        "buffer_load_dwordx4 v[24:27], v96, s[16:19], s34 offen nt\n\t"
        "s_add_u32 s34, s34, 0x10000\n\t"
        "buffer_load_dwordx4 v[28:31], v96, s[16:19], s34 offen nt\n\t"
        "s_lshl_b32 s34, s43, 2\n\t"
        "s_add_u32 s34, s34, 2\n\t"
        "s_and_b32 s34, s34, 15\n\t"
        "s_lshl_b32 s34, s34, 19\n\t"
        "buffer_load_dwordx4 v[32:35], v96, s[16:19], s34 offen nt\n\t"
        "s_add_u32 s34, s34, 0x10000\n\t"
        "buffer_load_dwordx4 v[36:39], v96, s[16:19], s34 offen nt\n\t"
        "s_add_u32 s34, s34, 0x10000\n\t"
        "buffer_load_dwordx4 v[40:43], v96, s[16:19], s34 offen nt\n\t"
        "s_add_u32 s34, s34, 0x10000\n\t"
        "buffer_load_dwordx4 v[44:47], v96, s[16:19], s34 offen nt\n\t"
        "s_add_u32 s34, s34, 0x10000\n\t"
        "buffer_load_dwordx4 v[48:51], v96, s[16:19], s34 offen nt\n\t"
        "s_add_u32 s34, s34, 0x10000\n\t"
        "buffer_load_dwordx4 v[52:55], v96, s[16:19], s34 offen nt\n\t"
        "s_add_u32 s34, s34, 0x10000\n\t"
        "buffer_load_dwordx4 v[56:59], v96, s[16:19], s34 offen nt\n\t"
        "s_add_u32 s34, s34, 0x10000\n\t"
        "buffer_load_dwordx4 v[60:63], v96, s[16:19], s34 offen nt\n\t"
        "v_cmp_le_i32_e64 s[60:61], 0, v126\n\t"
        "v_cmp_le_i32_e64 s[62:63], 1, v126\n\t"
        "v_cmp_le_i32_e64 s[64:65], 2, v126\n\t"
        "v_cmp_le_i32_e64 s[66:67], 3, v126\n\t"
        "v_cmp_le_i32_e64 s[68:69], 16, v126\n\t"
        "v_cmp_le_i32_e64 s[70:71], 17, v126\n\t"
        "v_cmp_le_i32_e64 s[72:73], 18, v126\n\t"
        "v_cmp_le_i32_e64 s[74:75], 19, v126\n\t"
        "s_mul_i32 s35, s43, 10240\n\t"
        "v_add_u32_e32 v126, s35, v124\n\t"
        "s_waitcnt vmcnt(16)\n\t"
        "ds_write_b128 v97, v[224:227] offset:0\n\t"
        "ds_write_b128 v98, v[228:231] offset:1024\n\t"
        "ds_write_b128 v97, v[232:235] offset:2048\n\t"
        "ds_write_b128 v98, v[236:239] offset:3072\n\t"
        "ds_write_b128 v97, v[240:243] offset:4096\n\t"
        "ds_write_b128 v98, v[244:247] offset:5120\n\t"
        "ds_write_b128 v97, v[248:251] offset:6144\n\t"
        "ds_write_b128 v98, v[252:255] offset:7168\n\t"
        "ds_read_b64_tr_b16 a[0:1], v99 offset:0\n\t"
        "ds_read_b64_tr_b16 a[2:3], v99 offset:256\n\t"
        "ds_read_b64_tr_b16 a[4:5], v99 offset:4096\n\t"
        "ds_read_b64_tr_b16 a[6:7], v99 offset:4352\n\t"
        "s_waitcnt lgkmcnt(8)\n\t"
        "ds_read_b64_tr_b16 a[8:9], v100 offset:0\n\t"
        "ds_read_b64_tr_b16 a[10:11], v100 offset:256\n\t"
        "ds_read_b64_tr_b16 a[12:13], v100 offset:4096\n\t"
        "ds_read_b64_tr_b16 a[14:15], v100 offset:4352\n\t"
        "s_waitcnt lgkmcnt(8)\n\t"
        "ds_read_b64_tr_b16 a[16:17], v101 offset:0\n\t"
        "ds_read_b64_tr_b16 a[18:19], v101 offset:256\n\t"
        "ds_read_b64_tr_b16 a[20:21], v101 offset:4096\n\t"
        "ds_read_b64_tr_b16 a[22:23], v101 offset:4352\n\t"
        "s_waitcnt lgkmcnt(8)\n\t"
        "ds_read_b64_tr_b16 v[104:105], v102 offset:0\n\t"
        "ds_read_b64_tr_b16 v[106:107], v102 offset:256\n\t"
        "ds_read_b64_tr_b16 v[108:109], v102 offset:4096\n\t"
        "ds_read_b64_tr_b16 v[110:111], v102 offset:4352\n\t"
        "s_lshl_b32 s34, s43, 2\n\t"
        "s_add_u32 s34, s34, 3\n\t"
        "s_and_b32 s34, s34, 15\n\t"
        "s_lshl_b32 s34, s34, 19\n\t"
        "buffer_load_dwordx4 v[64:67], v96, s[16:19], s34 offen nt\n\t"
        "s_add_u32 s34, s34, 0x10000\n\t"
        "buffer_load_dwordx4 v[68:71], v96, s[16:19], s34 offen nt\n\t"
        "s_add_u32 s34, s34, 0x10000\n\t"
        "buffer_load_dwordx4 v[72:75], v96, s[16:19], s34 offen nt\n\t"
        "s_add_u32 s34, s34, 0x10000\n\t"
        "buffer_load_dwordx4 v[76:79], v96, s[16:19], s34 offen nt\n\t"
        "s_add_u32 s34, s34, 0x10000\n\t"
        "buffer_load_dwordx4 v[80:83], v96, s[16:19], s34 offen nt\n\t"
        "s_add_u32 s34, s34, 0x10000\n\t"
        "buffer_load_dwordx4 v[84:87], v96, s[16:19], s34 offen nt\n\t"
        "s_add_u32 s34, s34, 0x10000\n\t"
        "buffer_load_dwordx4 v[88:91], v96, s[16:19], s34 offen nt\n\t"
        "s_add_u32 s34, s34, 0x10000\n\t"
        "buffer_load_dwordx4 v[92:95], v96, s[16:19], s34 offen nt\n\t"
        "s_waitcnt lgkmcnt(0)\n\t"
        "ds_write_b128 v117, v[104:107] offset:0\n\t"
        "ds_write_b128 v117, v[108:111] offset:1024\n\t"
        "s_waitcnt vmcnt(16)\n\t"
        "ds_write_b128 v97, v[0:3] offset:0\n\t"
        "ds_write_b128 v98, v[4:7] offset:1024\n\t"
        "ds_write_b128 v97, v[8:11] offset:2048\n\t"
        "ds_write_b128 v98, v[12:15] offset:3072\n\t"
        "ds_write_b128 v97, v[16:19] offset:4096\n\t"
        "ds_write_b128 v98, v[20:23] offset:5120\n\t"
        "ds_write_b128 v97, v[24:27] offset:6144\n\t"
        "ds_write_b128 v98, v[28:31] offset:7168\n\t"
        "ds_read_b64_tr_b16 a[24:25], v99 offset:0\n\t"
        "ds_read_b64_tr_b16 a[26:27], v99 offset:256\n\t"
        "ds_read_b64_tr_b16 a[28:29], v99 offset:4096\n\t"
        "ds_read_b64_tr_b16 a[30:31], v99 offset:4352\n\t"
        "s_waitcnt lgkmcnt(8)\n\t"
        "ds_read_b64_tr_b16 a[32:33], v100 offset:0\n\t"
        "ds_read_b64_tr_b16 a[34:35], v100 offset:256\n\t"
        "ds_read_b64_tr_b16 a[36:37], v100 offset:4096\n\t"
        "ds_read_b64_tr_b16 a[38:39], v100 offset:4352\n\t"
        "s_waitcnt lgkmcnt(8)\n\t"
        "ds_read_b64_tr_b16 a[40:41], v101 offset:0\n\t"
        "ds_read_b64_tr_b16 a[42:43], v101 offset:256\n\t"
        "ds_read_b64_tr_b16 a[44:45], v101 offset:4096\n\t"
        "ds_read_b64_tr_b16 a[46:47], v101 offset:4352\n\t"
        "s_waitcnt lgkmcnt(8)\n\t"
        "ds_read_b64_tr_b16 v[104:105], v102 offset:0\n\t"
        "ds_read_b64_tr_b16 v[106:107], v102 offset:256\n\t"
        "ds_read_b64_tr_b16 v[108:109], v102 offset:4096\n\t"
        "ds_read_b64_tr_b16 v[110:111], v102 offset:4352\n\t"
        "s_lshl_b32 s34, s43, 2\n\t"
        "s_add_u32 s34, s34, 4\n\t"
        "s_and_b32 s34, s34, 15\n\t"
        "s_lshl_b32 s34, s34, 19\n\t"
        "buffer_load_dwordx4 a[96:99], v96, s[16:19], s34 offen nt\n\t"
        "s_add_u32 s34, s34, 0x10000\n\t"
        "buffer_load_dwordx4 a[100:103], v96, s[16:19], s34 offen nt\n\t"
        "s_add_u32 s34, s34, 0x10000\n\t"
        "buffer_load_dwordx4 a[104:107], v96, s[16:19], s34 offen nt\n\t"
        "s_add_u32 s34, s34, 0x10000\n\t"
        "buffer_load_dwordx4 a[108:111], v96, s[16:19], s34 offen nt\n\t"
        "s_add_u32 s34, s34, 0x10000\n\t"
        "buffer_load_dwordx4 a[112:115], v96, s[16:19], s34 offen nt\n\t"
        "s_add_u32 s34, s34, 0x10000\n\t"
        "buffer_load_dwordx4 a[116:119], v96, s[16:19], s34 offen nt\n\t"
        "s_add_u32 s34, s34, 0x10000\n\t"
        "buffer_load_dwordx4 a[120:123], v96, s[16:19], s34 offen nt\n\t"
        "s_add_u32 s34, s34, 0x10000\n\t"
        "buffer_load_dwordx4 a[124:127], v96, s[16:19], s34 offen nt\n\t"
        "s_waitcnt lgkmcnt(0)\n\t"
        "ds_write_b128 v117, v[104:107] offset:2048\n\t"
        "ds_write_b128 v117, v[108:111] offset:3072\n\t"
        "s_waitcnt vmcnt(16)\n\t"
        "ds_write_b128 v97, v[32:35] offset:0\n\t"
        "ds_write_b128 v98, v[36:39] offset:1024\n\t"
        "ds_write_b128 v97, v[40:43] offset:2048\n\t"
        "ds_write_b128 v98, v[44:47] offset:3072\n\t"
        "ds_write_b128 v97, v[48:51] offset:4096\n\t"
        "ds_write_b128 v98, v[52:55] offset:5120\n\t"
        "ds_write_b128 v97, v[56:59] offset:6144\n\t"
        "ds_write_b128 v98, v[60:63] offset:7168\n\t"
        "ds_read_b64_tr_b16 a[48:49], v99 offset:0\n\t"
        "ds_read_b64_tr_b16 a[50:51], v99 offset:256\n\t"
        "ds_read_b64_tr_b16 a[52:53], v99 offset:4096\n\t"
        "ds_read_b64_tr_b16 a[54:55], v99 offset:4352\n\t"
        "s_waitcnt lgkmcnt(8)\n\t"
        "ds_read_b64_tr_b16 a[56:57], v100 offset:0\n\t"
        "ds_read_b64_tr_b16 a[58:59], v100 offset:256\n\t"
        "ds_read_b64_tr_b16 a[60:61], v100 offset:4096\n\t"
        "ds_read_b64_tr_b16 a[62:63], v100 offset:4352\n\t"
        "s_waitcnt lgkmcnt(8)\n\t"
        "ds_read_b64_tr_b16 a[64:65], v101 offset:0\n\t"
        "ds_read_b64_tr_b16 a[66:67], v101 offset:256\n\t"
        "ds_read_b64_tr_b16 a[68:69], v101 offset:4096\n\t"
        "ds_read_b64_tr_b16 a[70:71], v101 offset:4352\n\t"
        "s_waitcnt lgkmcnt(8)\n\t"
        "ds_read_b64_tr_b16 v[104:105], v102 offset:0\n\t"
        "ds_read_b64_tr_b16 v[106:107], v102 offset:256\n\t"
        "ds_read_b64_tr_b16 v[108:109], v102 offset:4096\n\t"
        "ds_read_b64_tr_b16 v[110:111], v102 offset:4352\n\t"
        "s_lshl_b32 s34, s43, 2\n\t"
        "s_add_u32 s34, s34, 5\n\t"
        "s_and_b32 s34, s34, 15\n\t"
        "s_lshl_b32 s34, s34, 19\n\t"
        "buffer_load_dwordx4 a[128:131], v96, s[16:19], s34 offen nt\n\t"
        "s_add_u32 s34, s34, 0x10000\n\t"
        "buffer_load_dwordx4 a[132:135], v96, s[16:19], s34 offen nt\n\t"
        "s_add_u32 s34, s34, 0x10000\n\t"
        "buffer_load_dwordx4 a[136:139], v96, s[16:19], s34 offen nt\n\t"
        "s_add_u32 s34, s34, 0x10000\n\t"
        "buffer_load_dwordx4 a[140:143], v96, s[16:19], s34 offen nt\n\t"
        "s_add_u32 s34, s34, 0x10000\n\t"
        "buffer_load_dwordx4 a[144:147], v96, s[16:19], s34 offen nt\n\t"
        "s_add_u32 s34, s34, 0x10000\n\t"
        "buffer_load_dwordx4 a[148:151], v96, s[16:19], s34 offen nt\n\t"
        "s_add_u32 s34, s34, 0x10000\n\t"
        "buffer_load_dwordx4 a[152:155], v96, s[16:19], s34 offen nt\n\t"
        "s_add_u32 s34, s34, 0x10000\n\t"
        "buffer_load_dwordx4 a[156:159], v96, s[16:19], s34 offen nt\n\t"
        "s_waitcnt lgkmcnt(0)\n\t"
        "ds_write_b128 v117, v[104:107] offset:4096\n\t"
        "ds_write_b128 v117, v[108:111] offset:5120\n\t"
        "s_waitcnt vmcnt(16)\n\t"
        "ds_write_b128 v97, v[64:67] offset:0\n\t"
        "ds_write_b128 v98, v[68:71] offset:1024\n\t"
        "ds_write_b128 v97, v[72:75] offset:2048\n\t"
        "ds_write_b128 v98, v[76:79] offset:3072\n\t"
        "ds_write_b128 v97, v[80:83] offset:4096\n\t"
        "ds_write_b128 v98, v[84:87] offset:5120\n\t"
        "ds_write_b128 v97, v[88:91] offset:6144\n\t"
        "ds_write_b128 v98, v[92:95] offset:7168\n\t"
        "ds_read_b64_tr_b16 a[72:73], v99 offset:0\n\t"
        "ds_read_b64_tr_b16 a[74:75], v99 offset:256\n\t"
        "ds_read_b64_tr_b16 a[76:77], v99 offset:4096\n\t"
        "ds_read_b64_tr_b16 a[78:79], v99 offset:4352\n\t"
        "s_waitcnt lgkmcnt(8)\n\t"
        "ds_read_b64_tr_b16 a[80:81], v100 offset:0\n\t"
        "ds_read_b64_tr_b16 a[82:83], v100 offset:256\n\t"
        "ds_read_b64_tr_b16 a[84:85], v100 offset:4096\n\t"
        "ds_read_b64_tr_b16 a[86:87], v100 offset:4352\n\t"
        "s_waitcnt lgkmcnt(8)\n\t"
        "ds_read_b64_tr_b16 a[88:89], v101 offset:0\n\t"
        "ds_read_b64_tr_b16 a[90:91], v101 offset:256\n\t"
        "ds_read_b64_tr_b16 a[92:93], v101 offset:4096\n\t"
        "ds_read_b64_tr_b16 a[94:95], v101 offset:4352\n\t"
        "s_waitcnt lgkmcnt(8)\n\t"
        "ds_read_b64_tr_b16 v[104:105], v102 offset:0\n\t"
        "ds_read_b64_tr_b16 v[106:107], v102 offset:256\n\t"
        "ds_read_b64_tr_b16 v[108:109], v102 offset:4096\n\t"
        "ds_read_b64_tr_b16 v[110:111], v102 offset:4352\n\t"
        "s_lshl_b32 s34, s43, 2\n\t"
        "s_add_u32 s34, s34, 6\n\t"
        "s_and_b32 s34, s34, 15\n\t"
        "s_lshl_b32 s34, s34, 19\n\t"
        "buffer_load_dwordx4 a[160:163], v96, s[16:19], s34 offen nt\n\t"
        "s_add_u32 s34, s34, 0x10000\n\t"
        "buffer_load_dwordx4 a[164:167], v96, s[16:19], s34 offen nt\n\t"
        "s_add_u32 s34, s34, 0x10000\n\t"
        "buffer_load_dwordx4 a[168:171], v96, s[16:19], s34 offen nt\n\t"
        "s_add_u32 s34, s34, 0x10000\n\t"
        "buffer_load_dwordx4 a[172:175], v96, s[16:19], s34 offen nt\n\t"
        "s_add_u32 s34, s34, 0x10000\n\t"
        "buffer_load_dwordx4 a[176:179], v96, s[16:19], s34 offen nt\n\t"
        "s_add_u32 s34, s34, 0x10000\n\t"
        "buffer_load_dwordx4 a[180:183], v96, s[16:19], s34 offen nt\n\t"
        "s_add_u32 s34, s34, 0x10000\n\t"
        "buffer_load_dwordx4 a[184:187], v96, s[16:19], s34 offen nt\n\t"
        "s_add_u32 s34, s34, 0x10000\n\t"
        "buffer_load_dwordx4 a[188:191], v96, s[16:19], s34 offen nt\n\t"
        "s_waitcnt lgkmcnt(0)\n\t"
        "ds_write_b128 v117, v[104:107] offset:6144\n\t"
        "ds_write_b128 v117, v[108:111] offset:7168\n\t"
        "s_waitcnt vmcnt(16)\n\t"
        "ds_write_b128 v97, a[96:99] offset:0\n\t"
        "ds_write_b128 v98, a[100:103] offset:1024\n\t"
        "ds_write_b128 v97, a[104:107] offset:2048\n\t"
        "ds_write_b128 v98, a[108:111] offset:3072\n\t"
        "ds_write_b128 v97, a[112:115] offset:4096\n\t"
        "ds_write_b128 v98, a[116:119] offset:5120\n\t"
        "ds_write_b128 v97, a[120:123] offset:6144\n\t"
        "ds_write_b128 v98, a[124:127] offset:7168\n\t"
        "ds_read_b64_tr_b16 a[96:97], v99 offset:0\n\t"
        "ds_read_b64_tr_b16 a[98:99], v99 offset:256\n\t"
        "ds_read_b64_tr_b16 a[100:101], v99 offset:4096\n\t"
        "ds_read_b64_tr_b16 a[102:103], v99 offset:4352\n\t"
        "s_waitcnt lgkmcnt(8)\n\t"
        "ds_read_b64_tr_b16 a[104:105], v100 offset:0\n\t"
        "ds_read_b64_tr_b16 a[106:107], v100 offset:256\n\t"
        "ds_read_b64_tr_b16 a[108:109], v100 offset:4096\n\t"
        "ds_read_b64_tr_b16 a[110:111], v100 offset:4352\n\t"
        "s_waitcnt lgkmcnt(8)\n\t"
        "ds_read_b64_tr_b16 a[112:113], v101 offset:0\n\t"
        "ds_read_b64_tr_b16 a[114:115], v101 offset:256\n\t"
        "ds_read_b64_tr_b16 a[116:117], v101 offset:4096\n\t"
        "ds_read_b64_tr_b16 a[118:119], v101 offset:4352\n\t"
        "s_waitcnt lgkmcnt(8)\n\t"
        "ds_read_b64_tr_b16 v[104:105], v102 offset:0\n\t"
        "ds_read_b64_tr_b16 v[106:107], v102 offset:256\n\t"
        "ds_read_b64_tr_b16 v[108:109], v102 offset:4096\n\t"
        "ds_read_b64_tr_b16 v[110:111], v102 offset:4352\n\t"
        "s_lshl_b32 s34, s43, 2\n\t"
        "s_add_u32 s34, s34, 7\n\t"
        "s_and_b32 s34, s34, 15\n\t"
        "s_lshl_b32 s34, s34, 19\n\t"
        "buffer_load_dwordx4 a[192:195], v96, s[16:19], s34 offen nt\n\t"
        "s_add_u32 s34, s34, 0x10000\n\t"
        "buffer_load_dwordx4 a[196:199], v96, s[16:19], s34 offen nt\n\t"
        "s_add_u32 s34, s34, 0x10000\n\t"
        "buffer_load_dwordx4 a[200:203], v96, s[16:19], s34 offen nt\n\t"
        "s_add_u32 s34, s34, 0x10000\n\t"
        "buffer_load_dwordx4 a[204:207], v96, s[16:19], s34 offen nt\n\t"
        "s_add_u32 s34, s34, 0x10000\n\t"
        "buffer_load_dwordx4 a[208:211], v96, s[16:19], s34 offen nt\n\t"
        "s_add_u32 s34, s34, 0x10000\n\t"
        "buffer_load_dwordx4 a[212:215], v96, s[16:19], s34 offen nt\n\t"
        "s_add_u32 s34, s34, 0x10000\n\t"
        "buffer_load_dwordx4 a[216:219], v96, s[16:19], s34 offen nt\n\t"
        "s_add_u32 s34, s34, 0x10000\n\t"
        "buffer_load_dwordx4 a[220:223], v96, s[16:19], s34 offen nt\n\t"
        "s_waitcnt lgkmcnt(0)\n\t"
        "ds_write_b128 v126, v[104:107] offset:0\n\t"
        "ds_write_b128 v126, v[108:111] offset:1024\n\t"
        "s_waitcnt vmcnt(16)\n\t"
        "ds_write_b128 v97, a[128:131] offset:0\n\t"
        "ds_write_b128 v98, a[132:135] offset:1024\n\t"
        "ds_write_b128 v97, a[136:139] offset:2048\n\t"
        "ds_write_b128 v98, a[140:143] offset:3072\n\t"
        "ds_write_b128 v97, a[144:147] offset:4096\n\t"
        "ds_write_b128 v98, a[148:151] offset:5120\n\t"
        "ds_write_b128 v97, a[152:155] offset:6144\n\t"
        "ds_write_b128 v98, a[156:159] offset:7168\n\t"
        "ds_read_b64_tr_b16 a[120:121], v99 offset:0\n\t"
        "ds_read_b64_tr_b16 a[122:123], v99 offset:256\n\t"
        "ds_read_b64_tr_b16 a[124:125], v99 offset:4096\n\t"
        "ds_read_b64_tr_b16 a[126:127], v99 offset:4352\n\t"
        "s_waitcnt lgkmcnt(8)\n\t"
        "ds_read_b64_tr_b16 a[128:129], v100 offset:0\n\t"
        "ds_read_b64_tr_b16 a[130:131], v100 offset:256\n\t"
        "ds_read_b64_tr_b16 a[132:133], v100 offset:4096\n\t"
        "ds_read_b64_tr_b16 a[134:135], v100 offset:4352\n\t"
        "s_waitcnt lgkmcnt(8)\n\t"
        "ds_read_b64_tr_b16 a[136:137], v101 offset:0\n\t"
        "ds_read_b64_tr_b16 a[138:139], v101 offset:256\n\t"
        "ds_read_b64_tr_b16 a[140:141], v101 offset:4096\n\t"
        "ds_read_b64_tr_b16 a[142:143], v101 offset:4352\n\t"
        "s_waitcnt lgkmcnt(8)\n\t"
        "ds_read_b64_tr_b16 v[104:105], v102 offset:0\n\t"
        "ds_read_b64_tr_b16 v[106:107], v102 offset:256\n\t"
        "ds_read_b64_tr_b16 v[108:109], v102 offset:4096\n\t"
        "ds_read_b64_tr_b16 v[110:111], v102 offset:4352\n\t"
        "s_lshl_b32 s34, s43, 2\n\t"
        "s_add_u32 s34, s34, 8\n\t"
        "s_and_b32 s34, s34, 15\n\t"
        "s_lshl_b32 s34, s34, 19\n\t"
        "buffer_load_dwordx4 a[224:227], v96, s[16:19], s34 offen nt\n\t"
        "s_add_u32 s34, s34, 0x10000\n\t"
        "buffer_load_dwordx4 a[228:231], v96, s[16:19], s34 offen nt\n\t"
        "s_add_u32 s34, s34, 0x10000\n\t"
        "buffer_load_dwordx4 a[232:235], v96, s[16:19], s34 offen nt\n\t"
        "s_add_u32 s34, s34, 0x10000\n\t"
        "buffer_load_dwordx4 a[236:239], v96, s[16:19], s34 offen nt\n\t"
        "s_add_u32 s34, s34, 0x10000\n\t"
        "buffer_load_dwordx4 a[240:243], v96, s[16:19], s34 offen nt\n\t"
        "s_add_u32 s34, s34, 0x10000\n\t"
        "buffer_load_dwordx4 a[244:247], v96, s[16:19], s34 offen nt\n\t"
        "s_add_u32 s34, s34, 0x10000\n\t"
        "buffer_load_dwordx4 a[248:251], v96, s[16:19], s34 offen nt\n\t"
        "s_add_u32 s34, s34, 0x10000\n\t"
        "buffer_load_dwordx4 a[252:255], v96, s[16:19], s34 offen nt\n\t"
        "s_waitcnt lgkmcnt(0)\n\t"
        "ds_write_b128 v126, v[104:107] offset:2048\n\t"
        "ds_write_b128 v126, v[108:111] offset:3072\n\t"
        "s_waitcnt vmcnt(16)\n\t"
        "ds_write_b128 v97, a[160:163] offset:0\n\t"
        "ds_write_b128 v98, a[164:167] offset:1024\n\t"
        "ds_write_b128 v97, a[168:171] offset:2048\n\t"
        "ds_write_b128 v98, a[172:175] offset:3072\n\t"
        "ds_write_b128 v97, a[176:179] offset:4096\n\t"
        "ds_write_b128 v98, a[180:183] offset:5120\n\t"
        "ds_write_b128 v97, a[184:187] offset:6144\n\t"
        "ds_write_b128 v98, a[188:191] offset:7168\n\t"
        "ds_read_b64_tr_b16 a[144:145], v99 offset:0\n\t"
        "ds_read_b64_tr_b16 a[146:147], v99 offset:256\n\t"
        "ds_read_b64_tr_b16 a[148:149], v99 offset:4096\n\t"
        "ds_read_b64_tr_b16 a[150:151], v99 offset:4352\n\t"
        "s_waitcnt lgkmcnt(8)\n\t"
        "ds_read_b64_tr_b16 a[152:153], v100 offset:0\n\t"
        "ds_read_b64_tr_b16 a[154:155], v100 offset:256\n\t"
        "ds_read_b64_tr_b16 a[156:157], v100 offset:4096\n\t"
        "ds_read_b64_tr_b16 a[158:159], v100 offset:4352\n\t"
        "s_waitcnt lgkmcnt(8)\n\t"
        "ds_read_b64_tr_b16 a[160:161], v101 offset:0\n\t"
        "ds_read_b64_tr_b16 a[162:163], v101 offset:256\n\t"
        "ds_read_b64_tr_b16 a[164:165], v101 offset:4096\n\t"
        "ds_read_b64_tr_b16 a[166:167], v101 offset:4352\n\t"
        "s_waitcnt lgkmcnt(8)\n\t"
        "ds_read_b64_tr_b16 v[104:105], v102 offset:0\n\t"
        "ds_read_b64_tr_b16 v[106:107], v102 offset:256\n\t"
        "ds_read_b64_tr_b16 v[108:109], v102 offset:4096\n\t"
        "ds_read_b64_tr_b16 v[110:111], v102 offset:4352\n\t"
        "s_lshl_b32 s34, s43, 2\n\t"
        "s_add_u32 s34, s34, 9\n\t"
        "s_and_b32 s34, s34, 15\n\t"
        "s_lshl_b32 s34, s34, 19\n\t"
        "buffer_load_dwordx4 v[128:131], v96, s[16:19], s34 offen nt\n\t"
        "s_add_u32 s34, s34, 0x10000\n\t"
        "buffer_load_dwordx4 v[132:135], v96, s[16:19], s34 offen nt\n\t"
        "s_add_u32 s34, s34, 0x10000\n\t"
        "buffer_load_dwordx4 v[136:139], v96, s[16:19], s34 offen nt\n\t"
        "s_add_u32 s34, s34, 0x10000\n\t"
        "buffer_load_dwordx4 v[140:143], v96, s[16:19], s34 offen nt\n\t"
        "s_add_u32 s34, s34, 0x10000\n\t"
        "buffer_load_dwordx4 v[144:147], v96, s[16:19], s34 offen nt\n\t"
        "s_add_u32 s34, s34, 0x10000\n\t"
        "buffer_load_dwordx4 v[148:151], v96, s[16:19], s34 offen nt\n\t"
        "s_add_u32 s34, s34, 0x10000\n\t"
        "buffer_load_dwordx4 v[152:155], v96, s[16:19], s34 offen nt\n\t"
        "s_add_u32 s34, s34, 0x10000\n\t"
        "buffer_load_dwordx4 v[156:159], v96, s[16:19], s34 offen nt\n\t"
        "s_waitcnt lgkmcnt(0)\n\t"
        "ds_write_b128 v126, v[104:107] offset:4096\n\t"
        "ds_write_b128 v126, v[108:111] offset:5120\n\t"
        "s_waitcnt vmcnt(16)\n\t"
        "ds_write_b128 v97, a[192:195] offset:0\n\t"
        "ds_write_b128 v98, a[196:199] offset:1024\n\t"
        "ds_write_b128 v97, a[200:203] offset:2048\n\t"
        "ds_write_b128 v98, a[204:207] offset:3072\n\t"
        "ds_write_b128 v97, a[208:211] offset:4096\n\t"
        "ds_write_b128 v98, a[212:215] offset:5120\n\t"
        "ds_write_b128 v97, a[216:219] offset:6144\n\t"
        "ds_write_b128 v98, a[220:223] offset:7168\n\t"
        "ds_read_b64_tr_b16 a[168:169], v99 offset:0\n\t"
        "ds_read_b64_tr_b16 a[170:171], v99 offset:256\n\t"
        "ds_read_b64_tr_b16 a[172:173], v99 offset:4096\n\t"
        "ds_read_b64_tr_b16 a[174:175], v99 offset:4352\n\t"
        "s_waitcnt lgkmcnt(8)\n\t"
        "ds_read_b64_tr_b16 a[176:177], v100 offset:0\n\t"
        "ds_read_b64_tr_b16 a[178:179], v100 offset:256\n\t"
        "ds_read_b64_tr_b16 a[180:181], v100 offset:4096\n\t"
        "ds_read_b64_tr_b16 a[182:183], v100 offset:4352\n\t"
        "s_waitcnt lgkmcnt(8)\n\t"
        "ds_read_b64_tr_b16 a[184:185], v101 offset:0\n\t"
        "ds_read_b64_tr_b16 a[186:187], v101 offset:256\n\t"
        "ds_read_b64_tr_b16 a[188:189], v101 offset:4096\n\t"
        "ds_read_b64_tr_b16 a[190:191], v101 offset:4352\n\t"
        "s_waitcnt lgkmcnt(8)\n\t"
        "ds_read_b64_tr_b16 v[104:105], v102 offset:0\n\t"
        "ds_read_b64_tr_b16 v[106:107], v102 offset:256\n\t"
        "ds_read_b64_tr_b16 v[108:109], v102 offset:4096\n\t"
        "ds_read_b64_tr_b16 v[110:111], v102 offset:4352\n\t"
        "s_lshl_b32 s34, s43, 2\n\t"
        "s_add_u32 s34, s34, 10\n\t"
        "s_and_b32 s34, s34, 15\n\t"
        "s_lshl_b32 s34, s34, 19\n\t"
        "buffer_load_dwordx4 v[160:163], v96, s[16:19], s34 offen nt\n\t"
        "s_add_u32 s34, s34, 0x10000\n\t"
        "buffer_load_dwordx4 v[164:167], v96, s[16:19], s34 offen nt\n\t"
        "s_add_u32 s34, s34, 0x10000\n\t"
        "buffer_load_dwordx4 v[168:171], v96, s[16:19], s34 offen nt\n\t"
        "s_add_u32 s34, s34, 0x10000\n\t"
        "buffer_load_dwordx4 v[172:175], v96, s[16:19], s34 offen nt\n\t"
        "s_add_u32 s34, s34, 0x10000\n\t"
        "buffer_load_dwordx4 v[176:179], v96, s[16:19], s34 offen nt\n\t"
        "s_add_u32 s34, s34, 0x10000\n\t"
        "buffer_load_dwordx4 v[180:183], v96, s[16:19], s34 offen nt\n\t"
        "s_add_u32 s34, s34, 0x10000\n\t"
        "buffer_load_dwordx4 v[184:187], v96, s[16:19], s34 offen nt\n\t"
        "s_add_u32 s34, s34, 0x10000\n\t"
        "buffer_load_dwordx4 v[188:191], v96, s[16:19], s34 offen nt\n\t"
        "s_waitcnt lgkmcnt(0)\n\t"
        "ds_write_b128 v126, v[104:107] offset:6144\n\t"
        "ds_write_b128 v126, v[108:111] offset:7168\n\t"
        "s_waitcnt vmcnt(16)\n\t"
        "ds_write_b128 v97, a[224:227] offset:0\n\t"
        "ds_write_b128 v98, a[228:231] offset:1024\n\t"
        "ds_write_b128 v97, a[232:235] offset:2048\n\t"
        "ds_write_b128 v98, a[236:239] offset:3072\n\t"
        "ds_write_b128 v97, a[240:243] offset:4096\n\t"
        "ds_write_b128 v98, a[244:247] offset:5120\n\t"
        "ds_write_b128 v97, a[248:251] offset:6144\n\t"
        "ds_write_b128 v98, a[252:255] offset:7168\n\t"
        "ds_read_b64_tr_b16 a[192:193], v99 offset:0\n\t"
        "ds_read_b64_tr_b16 a[194:195], v99 offset:256\n\t"
        "ds_read_b64_tr_b16 a[196:197], v99 offset:4096\n\t"
        "ds_read_b64_tr_b16 a[198:199], v99 offset:4352\n\t"
        "s_waitcnt lgkmcnt(8)\n\t"
        "ds_read_b64_tr_b16 a[200:201], v100 offset:0\n\t"
        "ds_read_b64_tr_b16 a[202:203], v100 offset:256\n\t"
        "ds_read_b64_tr_b16 a[204:205], v100 offset:4096\n\t"
        "ds_read_b64_tr_b16 a[206:207], v100 offset:4352\n\t"
        "s_waitcnt lgkmcnt(8)\n\t"
        "ds_read_b64_tr_b16 a[208:209], v101 offset:0\n\t"
        "ds_read_b64_tr_b16 a[210:211], v101 offset:256\n\t"
        "ds_read_b64_tr_b16 a[212:213], v101 offset:4096\n\t"
        "ds_read_b64_tr_b16 a[214:215], v101 offset:4352\n\t"
        "s_waitcnt lgkmcnt(8)\n\t"
        "ds_read_b64_tr_b16 v[104:105], v102 offset:0\n\t"
        "ds_read_b64_tr_b16 v[106:107], v102 offset:256\n\t"
        "ds_read_b64_tr_b16 v[108:109], v102 offset:4096\n\t"
        "ds_read_b64_tr_b16 v[110:111], v102 offset:4352\n\t"
        "s_lshl_b32 s34, s43, 2\n\t"
        "s_add_u32 s34, s34, 11\n\t"
        "s_and_b32 s34, s34, 15\n\t"
        "s_lshl_b32 s34, s34, 19\n\t"
        "buffer_load_dwordx4 v[192:195], v96, s[16:19], s34 offen nt\n\t"
        "s_add_u32 s34, s34, 0x10000\n\t"
        "buffer_load_dwordx4 v[196:199], v96, s[16:19], s34 offen nt\n\t"
        "s_add_u32 s34, s34, 0x10000\n\t"
        "buffer_load_dwordx4 v[200:203], v96, s[16:19], s34 offen nt\n\t"
        "s_add_u32 s34, s34, 0x10000\n\t"
        "buffer_load_dwordx4 v[204:207], v96, s[16:19], s34 offen nt\n\t"
        "s_add_u32 s34, s34, 0x10000\n\t"
        "buffer_load_dwordx4 v[208:211], v96, s[16:19], s34 offen nt\n\t"
        "s_add_u32 s34, s34, 0x10000\n\t"
        "buffer_load_dwordx4 v[212:215], v96, s[16:19], s34 offen nt\n\t"
        "s_add_u32 s34, s34, 0x10000\n\t"
        "buffer_load_dwordx4 v[216:219], v96, s[16:19], s34 offen nt\n\t"
        "s_add_u32 s34, s34, 0x10000\n\t"
        "buffer_load_dwordx4 v[220:223], v96, s[16:19], s34 offen nt\n\t"
        "s_waitcnt lgkmcnt(0)\n\t"
        "ds_write_b128 v117, v[104:107] offset:8192\n\t"
        "ds_write_b128 v117, v[108:111] offset:9216\n\t"
        "s_waitcnt vmcnt(16)\n\t"
        "ds_write_b128 v97, v[128:131] offset:0\n\t"
        "ds_write_b128 v98, v[132:135] offset:1024\n\t"
        "ds_write_b128 v97, v[136:139] offset:2048\n\t"
        "ds_write_b128 v98, v[140:143] offset:3072\n\t"
        "ds_write_b128 v97, v[144:147] offset:4096\n\t"
        "ds_write_b128 v98, v[148:151] offset:5120\n\t"
        "ds_write_b128 v97, v[152:155] offset:6144\n\t"
        "ds_write_b128 v98, v[156:159] offset:7168\n\t"
        "ds_read_b64_tr_b16 a[216:217], v99 offset:0\n\t"
        "ds_read_b64_tr_b16 a[218:219], v99 offset:256\n\t"
        "ds_read_b64_tr_b16 a[220:221], v99 offset:4096\n\t"
        "ds_read_b64_tr_b16 a[222:223], v99 offset:4352\n\t"
        "s_waitcnt lgkmcnt(8)\n\t"
        "ds_read_b64_tr_b16 a[224:225], v100 offset:0\n\t"
        "ds_read_b64_tr_b16 a[226:227], v100 offset:256\n\t"
        "ds_read_b64_tr_b16 a[228:229], v100 offset:4096\n\t"
        "ds_read_b64_tr_b16 a[230:231], v100 offset:4352\n\t"
        "s_waitcnt lgkmcnt(8)\n\t"
        "ds_read_b64_tr_b16 a[232:233], v101 offset:0\n\t"
        "ds_read_b64_tr_b16 a[234:235], v101 offset:256\n\t"
        "ds_read_b64_tr_b16 a[236:237], v101 offset:4096\n\t"
        "ds_read_b64_tr_b16 a[238:239], v101 offset:4352\n\t"
        "s_waitcnt lgkmcnt(8)\n\t"
        "ds_read_b64_tr_b16 v[104:105], v102 offset:0\n\t"
        "ds_read_b64_tr_b16 v[106:107], v102 offset:256\n\t"
        "ds_read_b64_tr_b16 v[108:109], v102 offset:4096\n\t"
        "ds_read_b64_tr_b16 v[110:111], v102 offset:4352\n\t"
        "s_lshl_b32 s34, s43, 2\n\t"
        "s_add_u32 s34, s34, 12\n\t"
        "s_and_b32 s34, s34, 15\n\t"
        "s_lshl_b32 s34, s34, 19\n\t"
        "buffer_load_dwordx4 v[224:227], v96, s[16:19], s34 offen nt\n\t"
        "s_add_u32 s34, s34, 0x10000\n\t"
        "buffer_load_dwordx4 v[228:231], v96, s[16:19], s34 offen nt\n\t"
        "s_add_u32 s34, s34, 0x10000\n\t"
        "buffer_load_dwordx4 v[232:235], v96, s[16:19], s34 offen nt\n\t"
        "s_add_u32 s34, s34, 0x10000\n\t"
        "buffer_load_dwordx4 v[236:239], v96, s[16:19], s34 offen nt\n\t"
        "s_add_u32 s34, s34, 0x10000\n\t"
        "buffer_load_dwordx4 v[240:243], v96, s[16:19], s34 offen nt\n\t"
        "s_add_u32 s34, s34, 0x10000\n\t"
        "buffer_load_dwordx4 v[244:247], v96, s[16:19], s34 offen nt\n\t"
        "s_add_u32 s34, s34, 0x10000\n\t"
        "buffer_load_dwordx4 v[248:251], v96, s[16:19], s34 offen nt\n\t"
        "s_add_u32 s34, s34, 0x10000\n\t"
        "buffer_load_dwordx4 v[252:255], v96, s[16:19], s34 offen nt\n\t"
        "s_waitcnt lgkmcnt(0)\n\t"
        "ds_write_b128 v117, v[104:107] offset:10240\n\t"
        "ds_write_b128 v117, v[108:111] offset:11264\n\t"
        "s_waitcnt vmcnt(16)\n\t"
        "ds_write_b128 v97, v[160:163] offset:0\n\t"
        "ds_write_b128 v98, v[164:167] offset:1024\n\t"
        "ds_write_b128 v97, v[168:171] offset:2048\n\t"
        "ds_write_b128 v98, v[172:175] offset:3072\n\t"
        "ds_write_b128 v97, v[176:179] offset:4096\n\t"
        "ds_write_b128 v98, v[180:183] offset:5120\n\t"
        "ds_write_b128 v97, v[184:187] offset:6144\n\t"
        "ds_write_b128 v98, v[188:191] offset:7168\n\t"
        "ds_read_b64_tr_b16 a[240:241], v99 offset:0\n\t"
        "ds_read_b64_tr_b16 a[242:243], v99 offset:256\n\t"
        "ds_read_b64_tr_b16 a[244:245], v99 offset:4096\n\t"
        "ds_read_b64_tr_b16 a[246:247], v99 offset:4352\n\t"
        "s_waitcnt lgkmcnt(8)\n\t"
        "ds_read_b64_tr_b16 a[248:249], v100 offset:0\n\t"
        "ds_read_b64_tr_b16 a[250:251], v100 offset:256\n\t"
        "ds_read_b64_tr_b16 a[252:253], v100 offset:4096\n\t"
        "ds_read_b64_tr_b16 a[254:255], v100 offset:4352\n\t"
        "s_waitcnt lgkmcnt(8)\n\t"
        "ds_read_b64_tr_b16 v[128:129], v101 offset:0\n\t"
        "ds_read_b64_tr_b16 v[130:131], v101 offset:256\n\t"
        "ds_read_b64_tr_b16 v[132:133], v101 offset:4096\n\t"
        "ds_read_b64_tr_b16 v[134:135], v101 offset:4352\n\t"
        "s_waitcnt lgkmcnt(8)\n\t"
        "ds_read_b64_tr_b16 v[104:105], v102 offset:0\n\t"
        "ds_read_b64_tr_b16 v[106:107], v102 offset:256\n\t"
        "ds_read_b64_tr_b16 v[108:109], v102 offset:4096\n\t"
        "ds_read_b64_tr_b16 v[110:111], v102 offset:4352\n\t"
        "s_lshl_b32 s34, s43, 2\n\t"
        "s_add_u32 s34, s34, 13\n\t"
        "s_and_b32 s34, s34, 15\n\t"
        "s_lshl_b32 s34, s34, 19\n\t"
        "buffer_load_dwordx4 v[0:3], v96, s[16:19], s34 offen nt\n\t"
        "s_add_u32 s34, s34, 0x10000\n\t"
        "buffer_load_dwordx4 v[4:7], v96, s[16:19], s34 offen nt\n\t"
        "s_add_u32 s34, s34, 0x10000\n\t"
        "buffer_load_dwordx4 v[8:11], v96, s[16:19], s34 offen nt\n\t"
        "s_add_u32 s34, s34, 0x10000\n\t"
        "buffer_load_dwordx4 v[12:15], v96, s[16:19], s34 offen nt\n\t"
        "s_add_u32 s34, s34, 0x10000\n\t"
        "buffer_load_dwordx4 v[16:19], v96, s[16:19], s34 offen nt\n\t"
        "s_add_u32 s34, s34, 0x10000\n\t"
        "buffer_load_dwordx4 v[20:23], v96, s[16:19], s34 offen nt\n\t"
        "s_add_u32 s34, s34, 0x10000\n\t"
        "buffer_load_dwordx4 v[24:27], v96, s[16:19], s34 offen nt\n\t"
        "s_add_u32 s34, s34, 0x10000\n\t"
        "buffer_load_dwordx4 v[28:31], v96, s[16:19], s34 offen nt\n\t"
        "s_waitcnt lgkmcnt(0)\n\t"
        "ds_write_b128 v117, v[104:107] offset:12288\n\t"
        "ds_write_b128 v117, v[108:111] offset:13312\n\t"
        "s_waitcnt vmcnt(16)\n\t"
        "ds_write_b128 v97, v[192:195] offset:0\n\t"
        "ds_write_b128 v98, v[196:199] offset:1024\n\t"
        "ds_write_b128 v97, v[200:203] offset:2048\n\t"
        "ds_write_b128 v98, v[204:207] offset:3072\n\t"
        "ds_write_b128 v97, v[208:211] offset:4096\n\t"
        "ds_write_b128 v98, v[212:215] offset:5120\n\t"
        "ds_write_b128 v97, v[216:219] offset:6144\n\t"
        "ds_write_b128 v98, v[220:223] offset:7168\n\t"
        "ds_read_b64_tr_b16 v[136:137], v99 offset:0\n\t"
        "ds_read_b64_tr_b16 v[138:139], v99 offset:256\n\t"
        "ds_read_b64_tr_b16 v[140:141], v99 offset:4096\n\t"
        "ds_read_b64_tr_b16 v[142:143], v99 offset:4352\n\t"
        "s_waitcnt lgkmcnt(8)\n\t"
        "ds_read_b64_tr_b16 v[144:145], v100 offset:0\n\t"
        "ds_read_b64_tr_b16 v[146:147], v100 offset:256\n\t"
        "ds_read_b64_tr_b16 v[148:149], v100 offset:4096\n\t"
        "ds_read_b64_tr_b16 v[150:151], v100 offset:4352\n\t"
        "s_waitcnt lgkmcnt(8)\n\t"
        "ds_read_b64_tr_b16 v[152:153], v101 offset:0\n\t"
        "ds_read_b64_tr_b16 v[154:155], v101 offset:256\n\t"
        "ds_read_b64_tr_b16 v[156:157], v101 offset:4096\n\t"
        "ds_read_b64_tr_b16 v[158:159], v101 offset:4352\n\t"
        "s_waitcnt lgkmcnt(8)\n\t"
        "ds_read_b64_tr_b16 v[104:105], v102 offset:0\n\t"
        "ds_read_b64_tr_b16 v[106:107], v102 offset:256\n\t"
        "ds_read_b64_tr_b16 v[108:109], v102 offset:4096\n\t"
        "ds_read_b64_tr_b16 v[110:111], v102 offset:4352\n\t"
        "s_lshl_b32 s34, s43, 2\n\t"
        "s_add_u32 s34, s34, 14\n\t"
        "s_and_b32 s34, s34, 15\n\t"
        "s_lshl_b32 s34, s34, 19\n\t"
        "buffer_load_dwordx4 v[32:35], v96, s[16:19], s34 offen nt\n\t"
        "s_add_u32 s34, s34, 0x10000\n\t"
        "buffer_load_dwordx4 v[36:39], v96, s[16:19], s34 offen nt\n\t"
        "s_add_u32 s34, s34, 0x10000\n\t"
        "buffer_load_dwordx4 v[40:43], v96, s[16:19], s34 offen nt\n\t"
        "s_add_u32 s34, s34, 0x10000\n\t"
        "buffer_load_dwordx4 v[44:47], v96, s[16:19], s34 offen nt\n\t"
        "s_add_u32 s34, s34, 0x10000\n\t"
        "buffer_load_dwordx4 v[48:51], v96, s[16:19], s34 offen nt\n\t"
        "s_add_u32 s34, s34, 0x10000\n\t"
        "buffer_load_dwordx4 v[52:55], v96, s[16:19], s34 offen nt\n\t"
        "s_add_u32 s34, s34, 0x10000\n\t"
        "buffer_load_dwordx4 v[56:59], v96, s[16:19], s34 offen nt\n\t"
        "s_add_u32 s34, s34, 0x10000\n\t"
        "buffer_load_dwordx4 v[60:63], v96, s[16:19], s34 offen nt\n\t"
        "s_waitcnt lgkmcnt(0)\n\t"
        "ds_write_b128 v117, v[104:107] offset:14336\n\t"
        "ds_write_b128 v117, v[108:111] offset:15360\n\t"
        "s_waitcnt vmcnt(16)\n\t"
        "ds_write_b128 v97, v[224:227] offset:0\n\t"
        "ds_write_b128 v98, v[228:231] offset:1024\n\t"
        "ds_write_b128 v97, v[232:235] offset:2048\n\t"
        "ds_write_b128 v98, v[236:239] offset:3072\n\t"
        "ds_write_b128 v97, v[240:243] offset:4096\n\t"
        "ds_write_b128 v98, v[244:247] offset:5120\n\t"
        "ds_write_b128 v97, v[248:251] offset:6144\n\t"
        "ds_write_b128 v98, v[252:255] offset:7168\n\t"
        "ds_read_b64_tr_b16 v[160:161], v99 offset:0\n\t"
        "ds_read_b64_tr_b16 v[162:163], v99 offset:256\n\t"
        "ds_read_b64_tr_b16 v[164:165], v99 offset:4096\n\t"
        "ds_read_b64_tr_b16 v[166:167], v99 offset:4352\n\t"
        "s_waitcnt lgkmcnt(8)\n\t"
        "ds_read_b64_tr_b16 v[168:169], v100 offset:0\n\t"
        "ds_read_b64_tr_b16 v[170:171], v100 offset:256\n\t"
        "ds_read_b64_tr_b16 v[172:173], v100 offset:4096\n\t"
        "ds_read_b64_tr_b16 v[174:175], v100 offset:4352\n\t"
        "s_waitcnt lgkmcnt(8)\n\t"
        "ds_read_b64_tr_b16 v[176:177], v101 offset:0\n\t"
        "ds_read_b64_tr_b16 v[178:179], v101 offset:256\n\t"
        "ds_read_b64_tr_b16 v[180:181], v101 offset:4096\n\t"
        "ds_read_b64_tr_b16 v[182:183], v101 offset:4352\n\t"
        "s_waitcnt lgkmcnt(8)\n\t"
        "ds_read_b64_tr_b16 v[104:105], v102 offset:0\n\t"
        "ds_read_b64_tr_b16 v[106:107], v102 offset:256\n\t"
        "ds_read_b64_tr_b16 v[108:109], v102 offset:4096\n\t"
        "ds_read_b64_tr_b16 v[110:111], v102 offset:4352\n\t"
        "s_lshl_b32 s34, s43, 2\n\t"
        "s_add_u32 s34, s34, 15\n\t"
        "s_and_b32 s34, s34, 15\n\t"
        "s_lshl_b32 s34, s34, 19\n\t"
        "buffer_load_dwordx4 v[64:67], v96, s[16:19], s34 offen nt\n\t"
        "s_add_u32 s34, s34, 0x10000\n\t"
        "buffer_load_dwordx4 v[68:71], v96, s[16:19], s34 offen nt\n\t"
        "s_add_u32 s34, s34, 0x10000\n\t"
        "buffer_load_dwordx4 v[72:75], v96, s[16:19], s34 offen nt\n\t"
        "s_add_u32 s34, s34, 0x10000\n\t"
        "buffer_load_dwordx4 v[76:79], v96, s[16:19], s34 offen nt\n\t"
        "s_add_u32 s34, s34, 0x10000\n\t"
        "buffer_load_dwordx4 v[80:83], v96, s[16:19], s34 offen nt\n\t"
        "s_add_u32 s34, s34, 0x10000\n\t"
        "buffer_load_dwordx4 v[84:87], v96, s[16:19], s34 offen nt\n\t"
        "s_add_u32 s34, s34, 0x10000\n\t"
        "buffer_load_dwordx4 v[88:91], v96, s[16:19], s34 offen nt\n\t"
        "s_add_u32 s34, s34, 0x10000\n\t"
        "buffer_load_dwordx4 v[92:95], v96, s[16:19], s34 offen nt\n\t"
        "s_waitcnt lgkmcnt(0)\n\t"
        "ds_write_b128 v117, v[104:107] offset:16384\n\t"
        "ds_write_b128 v117, v[108:111] offset:17408\n\t"
        "s_waitcnt vmcnt(16)\n\t"
        "ds_write_b128 v97, v[0:3] offset:0\n\t"
        "ds_write_b128 v98, v[4:7] offset:1024\n\t"
        "ds_write_b128 v97, v[8:11] offset:2048\n\t"
        "ds_write_b128 v98, v[12:15] offset:3072\n\t"
        "ds_write_b128 v97, v[16:19] offset:4096\n\t"
        "ds_write_b128 v98, v[20:23] offset:5120\n\t"
        "ds_write_b128 v97, v[24:27] offset:6144\n\t"
        "ds_write_b128 v98, v[28:31] offset:7168\n\t"
        "ds_read_b64_tr_b16 v[184:185], v99 offset:0\n\t"
        "ds_read_b64_tr_b16 v[186:187], v99 offset:256\n\t"
        "ds_read_b64_tr_b16 v[188:189], v99 offset:4096\n\t"
        "ds_read_b64_tr_b16 v[190:191], v99 offset:4352\n\t"
        "s_waitcnt lgkmcnt(8)\n\t"
        "ds_read_b64_tr_b16 v[192:193], v100 offset:0\n\t"
        "ds_read_b64_tr_b16 v[194:195], v100 offset:256\n\t"
        "ds_read_b64_tr_b16 v[196:197], v100 offset:4096\n\t"
        "ds_read_b64_tr_b16 v[198:199], v100 offset:4352\n\t"
        "s_waitcnt lgkmcnt(8)\n\t"
        "ds_read_b64_tr_b16 v[200:201], v101 offset:0\n\t"
        "ds_read_b64_tr_b16 v[202:203], v101 offset:256\n\t"
        "ds_read_b64_tr_b16 v[204:205], v101 offset:4096\n\t"
        "ds_read_b64_tr_b16 v[206:207], v101 offset:4352\n\t"
        "s_waitcnt lgkmcnt(8)\n\t"
        "ds_read_b64_tr_b16 v[104:105], v102 offset:0\n\t"
        "ds_read_b64_tr_b16 v[106:107], v102 offset:256\n\t"
        "ds_read_b64_tr_b16 v[108:109], v102 offset:4096\n\t"
        "ds_read_b64_tr_b16 v[110:111], v102 offset:4352\n\t"
        "s_waitcnt lgkmcnt(0)\n\t"
        "ds_write_b128 v117, v[104:107] offset:18432\n\t"
        "ds_write_b128 v117, v[108:111] offset:19456\n\t"
        "s_waitcnt vmcnt(8)\n\t"
        "ds_write_b128 v97, v[32:35] offset:0\n\t"
        "ds_write_b128 v98, v[36:39] offset:1024\n\t"
        "ds_write_b128 v97, v[40:43] offset:2048\n\t"
        "ds_write_b128 v98, v[44:47] offset:3072\n\t"
        "ds_write_b128 v97, v[48:51] offset:4096\n\t"
        "ds_write_b128 v98, v[52:55] offset:5120\n\t"
        "ds_write_b128 v97, v[56:59] offset:6144\n\t"
        "ds_write_b128 v98, v[60:63] offset:7168\n\t"
        "ds_read_b64_tr_b16 v[208:209], v99 offset:0\n\t"
        "ds_read_b64_tr_b16 v[210:211], v99 offset:256\n\t"
        "ds_read_b64_tr_b16 v[212:213], v99 offset:4096\n\t"
        "ds_read_b64_tr_b16 v[214:215], v99 offset:4352\n\t"
        "s_waitcnt lgkmcnt(8)\n\t"
        "ds_read_b64_tr_b16 v[216:217], v100 offset:0\n\t"
        "ds_read_b64_tr_b16 v[218:219], v100 offset:256\n\t"
        "ds_read_b64_tr_b16 v[220:221], v100 offset:4096\n\t"
        "ds_read_b64_tr_b16 v[222:223], v100 offset:4352\n\t"
        "s_waitcnt lgkmcnt(8)\n\t"
        "ds_read_b64_tr_b16 v[224:225], v101 offset:0\n\t"
        "ds_read_b64_tr_b16 v[226:227], v101 offset:256\n\t"
        "ds_read_b64_tr_b16 v[228:229], v101 offset:4096\n\t"
        "ds_read_b64_tr_b16 v[230:231], v101 offset:4352\n\t"
        "s_waitcnt lgkmcnt(8)\n\t"
        "ds_read_b64_tr_b16 v[104:105], v102 offset:0\n\t"
        "ds_read_b64_tr_b16 v[106:107], v102 offset:256\n\t"
        "ds_read_b64_tr_b16 v[108:109], v102 offset:4096\n\t"
        "ds_read_b64_tr_b16 v[110:111], v102 offset:4352\n\t"
        "s_mov_b32 s35, 33792\n\t"
        "buffer_load_dword v0, off, s[12:15], s35 sc1\n\t"
        "s_waitcnt lgkmcnt(0)\n\t"
        "ds_write_b128 v117, v[104:107] offset:20480\n\t"
        "ds_write_b128 v117, v[108:111] offset:21504\n\t"
        "s_waitcnt vmcnt(0)\n\t"
        "ds_write_b128 v97, v[64:67] offset:0\n\t"
        "ds_write_b128 v98, v[68:71] offset:1024\n\t"
        "ds_write_b128 v97, v[72:75] offset:2048\n\t"
        "ds_write_b128 v98, v[76:79] offset:3072\n\t"
        "ds_write_b128 v97, v[80:83] offset:4096\n\t"
        "ds_write_b128 v98, v[84:87] offset:5120\n\t"
        "ds_write_b128 v97, v[88:91] offset:6144\n\t"
        "ds_write_b128 v98, v[92:95] offset:7168\n\t"
        "ds_read_b64_tr_b16 v[232:233], v99 offset:0\n\t"
        "ds_read_b64_tr_b16 v[234:235], v99 offset:256\n\t"
        "ds_read_b64_tr_b16 v[236:237], v99 offset:4096\n\t"
        "ds_read_b64_tr_b16 v[238:239], v99 offset:4352\n\t"
        "s_waitcnt lgkmcnt(8)\n\t"
        "ds_read_b64_tr_b16 v[240:241], v100 offset:0\n\t"
        "ds_read_b64_tr_b16 v[242:243], v100 offset:256\n\t"
        "ds_read_b64_tr_b16 v[244:245], v100 offset:4096\n\t"
        "ds_read_b64_tr_b16 v[246:247], v100 offset:4352\n\t"
        "s_waitcnt lgkmcnt(8)\n\t"
        "ds_read_b64_tr_b16 v[248:249], v101 offset:0\n\t"
        "ds_read_b64_tr_b16 v[250:251], v101 offset:256\n\t"
        "ds_read_b64_tr_b16 v[252:253], v101 offset:4096\n\t"
        "ds_read_b64_tr_b16 v[254:255], v101 offset:4352\n\t"
        "s_waitcnt lgkmcnt(8)\n\t"
        "ds_read_b64_tr_b16 v[104:105], v102 offset:0\n\t"
        "ds_read_b64_tr_b16 v[106:107], v102 offset:256\n\t"
        "ds_read_b64_tr_b16 v[108:109], v102 offset:4096\n\t"
        "ds_read_b64_tr_b16 v[110:111], v102 offset:4352\n\t"
        "s_mov_b32 s42, 0\n\t"
        "LA_cal%=:\n\t"
        "v_readfirstlane_b32 s34, v0\n\t"
        "s_nop 0\n\t"
        "s_cmp_lg_u32 s34, 0\n\t"
        "s_cbranch_scc1 LA_calok%=\n\t"
        "s_sleep 4\n\t"
        "s_mov_b32 s35, 33792\n\t"
        "buffer_load_dword v0, off, s[12:15], s35 sc1\n\t"
        "s_waitcnt vmcnt(0)\n\t"
        "s_add_u32 s42, s42, 1\n\t"
        "s_cmp_lt_u32 s42, 0x100000\n\t"
        "s_cbranch_scc1 LA_cal%=\n\t"
        "s_movk_i32 s34, 8\n\t"
        "s_branch LA_die%=\n\t"
        "LA_calok%=:\n\t"
        "s_lshl_b32 s35, s44, 10\n\t"
        "v_lshlrev_b32_e32 v1, 2, v122\n\t"
        "v_and_b32_e32 v1, 0x3c0, v1\n\t"
        "buffer_load_dword v1, v1, s[12:15], s35 offen offset:0 sc1\n\t"
        "s_waitcnt lgkmcnt(0)\n\t"
        "ds_write_b128 v126, v[104:107] offset:8192\n\t"
        "ds_write_b128 v126, v[108:111] offset:9216\n\t"
        "s_waitcnt lgkmcnt(0)\n\t"
        "v_mov_b32_e32 v110, v103\n\t"
        "ds_read_b128 v[64:67], v126 offset:0\n\t"
        "ds_read_b128 v[68:71], v126 offset:1024\n\t"
        "ds_read_b128 v[72:75], v126 offset:2048\n\t"
        "ds_read_b128 v[76:79], v126 offset:3072\n\t"
        "ds_read_b128 v[80:83], v126 offset:4096\n\t"
        "ds_read_b128 v[84:87], v126 offset:5120\n\t"
        "ds_read_b128 v[88:91], v126 offset:6144\n\t"
        "ds_read_b128 v[92:95], v126 offset:7168\n\t"
        "ds_read_b128 v[96:99], v126 offset:8192\n\t"
        "ds_read_b128 v[100:103], v126 offset:9216\n\t"
        "s_waitcnt lgkmcnt(0)\n\t"
        "v_mov_b32_e32 v126, 0\n\t"
        "s_barrier\n\t"
        "s_mov_b32 s48, 0\n\t"
        "s_mov_b32 s54, 0\n\t"
        "s_mov_b32 s0, 0x3f803f80\n\t"
        "s_mov_b32 s1, s0\n\t"
        "s_movk_i32 s56, 24576\n\t"
        "s_and_b32 s58, s43, 1\n\t"
        "s_lshl_b32 s58, s58, 12\n\t"
        "s_lshl_b32 s57, s44, 5\n\t"
        "s_add_u32 s57, s57, s43\n\t"
        "s_lshl_b32 s57, s57, 2\n\t"
        "s_add_u32 s57, s57, 458752\n\t"
        "s_waitcnt vmcnt(0)\n\t"
        "v_mov_b32_e32 v111, v1\n\t"
        "s_lshr_b32 s59, s48, 1\n\t"
        "s_cmp_gt_u32 s54, s59\n\t"
        "s_cbranch_scc1 LA_kv0ok%=\n\t"
        "s_mov_b32 s42, 0\n\t"
        "s_waitcnt vmcnt(0)\n\t"
        "s_branch LA_kv0w%=\n\t"
        "LA_kv0%=:\n\t"
        "s_waitcnt vmcnt(0)\n\t"
        "LA_kv0w%=:\n\t"
        "v_min_u32_dpp v111, v111, v111 quad_perm:[1,0,3,2] row_mask:0xf bank_mask:0xf\n\t"
        "s_nop 1\n\t"
        "v_min_u32_dpp v111, v111, v111 quad_perm:[2,3,0,1] row_mask:0xf bank_mask:0xf\n\t"
        "s_nop 1\n\t"
        "v_min_u32_dpp v111, v111, v111 row_ror:4 row_mask:0xf bank_mask:0xf\n\t"
        "s_nop 1\n\t"
        "v_min_u32_dpp v111, v111, v111 row_ror:8 row_mask:0xf bank_mask:0xf\n\t"
        "s_nop 0\n\t"
        "v_readfirstlane_b32 s54, v111\n\t"
        "s_nop 0\n\t"
        "s_cmp_gt_u32 s54, s59\n\t"
        "s_cbranch_scc1 LA_kv0ok%=\n\t"
        "s_sleep 4\n\t"
        "s_lshl_b32 s35, s44, 10\n\t"
        "v_lshlrev_b32_e32 v111, 2, v122\n\t"
        "v_and_b32_e32 v111, 0x3c0, v111\n\t"
        "buffer_load_dword v111, v111, s[12:15], s35 offen offset:0 sc1\n\t"
        "s_add_u32 s42, s42, 1\n\t"
        "s_cmp_lt_u32 s42, 0x100000\n\t"
        "s_cbranch_scc1 LA_kv0%=\n\t"
        "s_movk_i32 s34, 16\n\t"
        "s_branch LA_die%=\n\t"
        "LA_kv0ok%=:\n\t"
        "s_and_b32 s36, s48, 1\n\t"
        "s_mul_i32 s36, s36, 24576\n\t"
        "s_add_u32 s36, s36, s55\n\t"
        "s_lshl_b32 s37, s48, 18\n\t"
        "s_lshl_b32 s34, s43, 11\n\t"
        "s_add_u32 s37, s37, s34\n\t"
        "s_add_u32 m0, s36, 0\n\t"
        "s_add_u32 s34, s37, 29548544\n\t"
        "buffer_load_dwordx4 v122, s[12:15], s34 offen sc1 lds\n\t"
        "s_add_u32 m0, s36, 1024\n\t"
        "s_add_u32 s34, s37, 29549568\n\t"
        "buffer_load_dwordx4 v122, s[12:15], s34 offen sc1 lds\n\t"
        "s_add_u32 m0, s36, 8192\n\t"
        "s_add_u32 s34, s37, 12771328\n\t"
        "buffer_load_dwordx4 v122, s[12:15], s34 offen sc1 lds\n\t"
        "s_add_u32 m0, s36, 9216\n\t"
        "s_add_u32 s34, s37, 12772352\n\t"
        "buffer_load_dwordx4 v122, s[12:15], s34 offen sc1 lds\n\t"
        "s_add_u32 m0, s36, 16384\n\t"
        "s_add_u32 s34, s37, 46325760\n\t"
        "buffer_load_dwordx4 v122, s[12:15], s34 offen sc1 lds\n\t"
        "s_add_u32 m0, s36, 17408\n\t"
        "s_add_u32 s34, s37, 46326784\n\t"
        "buffer_load_dwordx4 v122, s[12:15], s34 offen sc1 lds\n\t"
        "v_readfirstlane_b32 s34, v0\n\t"
        "s_nop 0\n\t"
        "s_and_b32 s34, s34, 8192\n\t"
        "s_add_u32 s8, s8, s34\n\t"
        "s_addc_u32 s9, s9, 0\n\t"
        "s_barrier\n\t"
        "s_cmp_lg_u32 s43, 0\n\t"
        "s_cbranch_scc1 LA_st%=\n\t"
        "v_mov_b32_e32 v2, 1\n\t"
        "s_mov_b32 s35, 34304\n\t"
        "s_mov_b64 exec, 1\n\t"
        "buffer_atomic_add v2, off, s[12:15], s35 sc0\n\t"
        "s_mov_b64 exec, -1\n\t"
        "s_waitcnt vmcnt(0)\n\t"
        "v_readfirstlane_b32 s34, v2\n\t"
        "s_nop 0\n\t"
        "s_cmp_lg_u32 s34, 95\n\t"
        "s_cbranch_scc1 LA_st%=\n\t"
        "s_memrealtime s[32:33]\n\t"
        "v_mbcnt_lo_u32_b32 v3, -1, 0\n\t"
        "v_lshlrev_b32_e32 v3, 7, v3\n\t"
        "s_waitcnt lgkmcnt(0)\n\t"
        "s_add_u32 s34, s32, 50\n\t"
        "s_or_b32 s34, s34, 1\n\t"
        "v_mov_b32_e32 v2, s34\n\t"
        "s_mov_b32 s35, 36864\n\t"
        "s_mov_b64 exec, 0xffff\n\t"
        "buffer_store_dword v2, v3, s[12:15], s35 offen sc1\n\t"
        "s_mov_b64 exec, -1\n\t"
        "LA_st%=:\n\t"
        "s_mov_b32 s24, 0\n\t"
        "s_mov_b32 s26, 0\n\t"
        "s_mov_b32 s27, 0x80000\n\t"
        "s_mov_b32 s28, 0x180000\n\t"
        "s_mov_b32 s31, 0\n\t"
        "s_mov_b32 s47, 0\n\t"
        "s_mov_b32 s38, 0xff00ff00\n\t"
        "s_mov_b32 s39, 0xff00ff00\n\t"
        "s_mov_b32 s40, 0\n\t"
        "s_mov_b32 s41, -1\n\t"
        "ds_read_b128 v[32:35], v117 offset:0\n\t"
        "ds_read_b128 v[36:39], v117 offset:1024\n\t"
        "ds_read_b128 v[40:43], v117 offset:2048\n\t"
        "ds_read_b128 v[44:47], v117 offset:3072\n\t"
        "ds_read_b128 v[48:51], v117 offset:4096\n\t"
        "ds_read_b128 v[52:55], v117 offset:5120\n\t"
        "ds_read_b128 v[56:59], v117 offset:6144\n\t"
        "ds_read_b128 v[60:63], v117 offset:7168\n\t"
        "v_mov_b64_e32 v[0:1], 0\n\t"
        "v_mov_b64_e32 v[2:3], 0\n\t"
        "v_mov_b64_e32 v[4:5], 0\n\t"
        "v_mov_b64_e32 v[6:7], 0\n\t"
        "v_mov_b64_e32 v[8:9], 0\n\t"
        "v_mov_b64_e32 v[10:11], 0\n\t"
        "v_mov_b64_e32 v[12:13], 0\n\t"
        "v_mov_b64_e32 v[14:15], 0\n\t"
        "s_mov_b32 s42, 0\n\t"
        "LA_gw%=:\n\t"
        "buffer_load_dword v16, off, s[12:15], s45 sc1\n\t"
        "s_waitcnt vmcnt(0)\n\t"
        "v_readfirstlane_b32 s25, v16\n\t"
        "s_nop 0\n\t"
        "s_cmp_lg_u32 s25, 0\n\t"
        "s_cbranch_scc1 LA_loop%=\n\t"
        "s_sleep 2\n\t"
        "s_add_u32 s42, s42, 1\n\t"
        "s_cmp_lt_u32 s42, 0x100000\n\t"
        "s_cbranch_scc1 LA_gw%=\n\t"
        "s_movk_i32 s34, 9\n\t"
        "s_branch LA_die%=\n\t"
        "LA_loop%=:\n\t"
        "s_mov_b32 s42, 0\n\t"
        "s_sub_u32 s35, s25, 1\n\t"
        "LA_wa%=:\n\t"
        "s_memrealtime s[32:33]\n\t"
        "s_waitcnt lgkmcnt(0)\n\t"
        "s_sub_u32 s34, s35, s32\n\t"
        "s_cmpk_lt_u32 s34, 0x2000\n\t"
        "s_cbranch_scc1 LA_wa%=\n\t"
        "LA_rd%=:\n\t"
        "buffer_load_dwordx4 v[16:19], v112, s[8:11], s26 offen offset:0 sc1\n\t"
        "s_nop 12\n\t"
        "buffer_load_dwordx4 v[20:23], v112, s[8:11], s26 offen offset:1024 sc1\n\t"
        "s_nop 12\n\t"
        "buffer_load_dwordx4 v[24:27], v112, s[8:11], s26 offen offset:2048 sc1\n\t"
        "s_nop 12\n\t"
        "buffer_load_dwordx4 v[28:31], v112, s[8:11], s26 offen offset:3072 sc1\n\t"
        "LA_go%=:\n\t"
        "s_waitcnt vmcnt(3)\n\t"
        "v_smfmac_f32_16x16x64_bf16 v[0:3], v[16:19], a[0:7], v110\n\t"
        "ds_write_b128 v113, v[16:19] offset:0\n\t"
        "v_smfmac_f32_16x16x64_bf16 v[4:7], v[16:19], a[8:15], v110\n\t"
        "v_max3_u32 v111, v16, v17, v18\n\t"
        "v_max_u32_e32 v111, v19, v111\n\t"
        "v_smfmac_f32_16x16x64_bf16 v[8:11], v[16:19], a[16:23], v110\n\t"
        "v_smfmac_f32_16x16x64_bf16 v[12:15], v[16:19], v[32:39], v110\n\t"
        "s_waitcnt vmcnt(2)\n\t"
        "v_smfmac_f32_16x16x64_bf16 v[0:3], v[20:23], a[24:31], v110\n\t"
        "ds_write_b128 v113, v[20:23] offset:1024\n\t"
        "v_smfmac_f32_16x16x64_bf16 v[4:7], v[20:23], a[32:39], v110\n\t"
        "v_max3_u32 v111, v20, v21, v111\n\t"
        "v_max3_u32 v111, v22, v23, v111\n\t"
        "v_smfmac_f32_16x16x64_bf16 v[8:11], v[20:23], a[40:47], v110\n\t"
        "s_waitcnt vmcnt(1)\n\t"
        "ds_write_b128 v113, v[24:27] offset:2048\n\t"
        "v_smfmac_f32_16x16x64_bf16 v[12:15], v[20:23], v[40:47], v110\n\t"
        "v_max3_u32 v111, v24, v25, v111\n\t"
        "v_max3_u32 v111, v26, v27, v111\n\t"
        "v_smfmac_f32_16x16x64_bf16 v[0:3], v[24:27], a[48:55], v110\n\t"
        "v_smfmac_f32_16x16x64_bf16 v[4:7], v[24:27], a[56:63], v110\n\t"
        "s_waitcnt vmcnt(0)\n\t"
        "ds_write_b128 v113, v[28:31] offset:3072\n\t"
        "v_max3_u32 v111, v28, v29, v111\n\t"
        "v_max3_u32 v111, v30, v31, v111\n\t"
        "v_smfmac_f32_16x16x64_bf16 v[8:11], v[24:27], a[64:71], v110\n\t"
        "v_cmp_eq_u32_e32 vcc, -1, v111\n\t"
        "v_smfmac_f32_16x16x64_bf16 v[12:15], v[24:27], v[48:55], v110\n\t"
        "s_waitcnt lgkmcnt(0)\n\t"
        "s_cbranch_vccnz LA_rdfail%=\n\t"
        "s_barrier\n\t"
        "ds_read_b128 v[16:19], v114 offset:0\n\t"
        "ds_read_b128 v[20:23], v114 offset:1024\n\t"
        "ds_read_b128 v[24:27], v114 offset:2048\n\t"
        "v_smfmac_f32_16x16x64_bf16 v[0:3], v[28:31], a[72:79], v110\n\t"
        "v_smfmac_f32_16x16x64_bf16 v[4:7], v[28:31], a[80:87], v110\n\t"
        "v_smfmac_f32_16x16x64_bf16 v[8:11], v[28:31], a[88:95], v110\n\t"
        "v_smfmac_f32_16x16x64_bf16 v[12:15], v[28:31], v[56:63], v110\n\t"
        "ds_read_b128 v[28:31], v114 offset:3072\n\t"
        "s_waitcnt lgkmcnt(3)\n\t"
        "v_smfmac_f32_16x16x64_bf16 v[12:15], v[16:19], v[64:71], v110\n\t"
        "ds_read_b128 v[32:35], v117 offset:8192\n\t"
        "v_smfmac_f32_16x16x64_bf16 v[0:3], v[16:19], a[96:103], v110\n\t"
        "ds_read_b128 v[36:39], v117 offset:9216\n\t"
        "v_smfmac_f32_16x16x64_bf16 v[4:7], v[16:19], a[104:111], v110\n\t"
        "v_smfmac_f32_16x16x64_bf16 v[8:11], v[16:19], a[112:119], v110\n\t"
        "ds_read_b128 v[16:19], v115 offset:0\n\t"
        "s_waitcnt lgkmcnt(5)\n\t"
        "v_smfmac_f32_16x16x64_bf16 v[12:15], v[20:23], v[72:79], v110\n\t"
        "ds_read_b128 v[40:43], v117 offset:10240\n\t"
        "v_smfmac_f32_16x16x64_bf16 v[0:3], v[20:23], a[120:127], v110\n\t"
        "ds_read_b128 v[44:47], v117 offset:11264\n\t"
        "v_smfmac_f32_16x16x64_bf16 v[4:7], v[20:23], a[128:135], v110\n\t"
        "v_smfmac_f32_16x16x64_bf16 v[8:11], v[20:23], a[136:143], v110\n\t"
        "ds_read_b128 v[20:23], v115 offset:1024\n\t"
        "s_waitcnt lgkmcnt(7)\n\t"
        "v_smfmac_f32_16x16x64_bf16 v[12:15], v[24:27], v[80:87], v110\n\t"
        "ds_read_b128 v[48:51], v117 offset:12288\n\t"
        "v_smfmac_f32_16x16x64_bf16 v[0:3], v[24:27], a[144:151], v110\n\t"
        "ds_read_b128 v[52:55], v117 offset:13312\n\t"
        "v_smfmac_f32_16x16x64_bf16 v[4:7], v[24:27], a[152:159], v110\n\t"
        "v_smfmac_f32_16x16x64_bf16 v[8:11], v[24:27], a[160:167], v110\n\t"
        "ds_read_b128 v[24:27], v115 offset:2048\n\t"
        "s_waitcnt lgkmcnt(9)\n\t"
        "v_smfmac_f32_16x16x64_bf16 v[12:15], v[28:31], v[88:95], v110\n\t"
        "ds_read_b128 v[56:59], v117 offset:14336\n\t"
        "v_smfmac_f32_16x16x64_bf16 v[0:3], v[28:31], a[168:175], v110\n\t"
        "ds_read_b128 v[60:63], v117 offset:15360\n\t"
        "v_smfmac_f32_16x16x64_bf16 v[4:7], v[28:31], a[176:183], v110\n\t"
        "v_smfmac_f32_16x16x64_bf16 v[8:11], v[28:31], a[184:191], v110\n\t"
        "ds_read_b128 v[28:31], v115 offset:3072\n\t"
        "s_waitcnt lgkmcnt(9)\n\t"
        "v_smfmac_f32_16x16x64_bf16 v[12:15], v[16:19], v[32:39], v110\n\t"
        "ds_read_b128 v[32:35], v117 offset:16384\n\t"
        "v_smfmac_f32_16x16x64_bf16 v[0:3], v[16:19], a[192:199], v110\n\t"
        "ds_read_b128 v[36:39], v117 offset:17408\n\t"
        "v_smfmac_f32_16x16x64_bf16 v[4:7], v[16:19], a[200:207], v110\n\t"
        "v_smfmac_f32_16x16x64_bf16 v[8:11], v[16:19], a[208:215], v110\n\t"
        "ds_read_b128 v[16:19], v116 offset:0\n\t"
        "s_waitcnt lgkmcnt(9)\n\t"
        "v_smfmac_f32_16x16x64_bf16 v[12:15], v[20:23], v[40:47], v110\n\t"
        "ds_read_b128 v[40:43], v117 offset:18432\n\t"
        "v_smfmac_f32_16x16x64_bf16 v[0:3], v[20:23], a[216:223], v110\n\t"
        "ds_read_b128 v[44:47], v117 offset:19456\n\t"
        "v_smfmac_f32_16x16x64_bf16 v[4:7], v[20:23], a[224:231], v110\n\t"
        "v_smfmac_f32_16x16x64_bf16 v[8:11], v[20:23], a[232:239], v110\n\t"
        "ds_read_b128 v[20:23], v116 offset:1024\n\t"
        "s_waitcnt lgkmcnt(9)\n\t"
        "v_smfmac_f32_16x16x64_bf16 v[12:15], v[24:27], v[48:55], v110\n\t"
        "ds_read_b128 v[48:51], v117 offset:20480\n\t"
        "v_smfmac_f32_16x16x64_bf16 v[0:3], v[24:27], a[240:247], v110\n\t"
        "ds_read_b128 v[52:55], v117 offset:21504\n\t"
        "v_smfmac_f32_16x16x64_bf16 v[4:7], v[24:27], a[248:255], v110\n\t"
        "v_smfmac_f32_16x16x64_bf16 v[8:11], v[24:27], v[128:135], v110\n\t"
        "ds_read_b128 v[24:27], v116 offset:2048\n\t"
        "s_waitcnt lgkmcnt(9)\n\t"
        "v_smfmac_f32_16x16x64_bf16 v[12:15], v[28:31], v[56:63], v110\n\t"
        "ds_read_b128 v[56:59], v124 offset:3072\n\t"
        "v_smfmac_f32_16x16x64_bf16 v[0:3], v[28:31], v[136:143], v110\n\t"
        "ds_read_b128 v[60:63], v124 offset:7168\n\t"
        "v_smfmac_f32_16x16x64_bf16 v[4:7], v[28:31], v[144:151], v110\n\t"
        "v_smfmac_f32_16x16x64_bf16 v[8:11], v[28:31], v[152:159], v110\n\t"
        "ds_read_b128 v[28:31], v116 offset:3072\n\t"
        "s_waitcnt lgkmcnt(9)\n\t"
        "v_smfmac_f32_16x16x64_bf16 v[12:15], v[16:19], v[32:39], v110\n\t"
        "ds_read_b128 v[32:35], v124 offset:0\n\t"
        "v_smfmac_f32_16x16x64_bf16 v[0:3], v[16:19], v[160:167], v110\n\t"
        "ds_read_b128 v[36:39], v124 offset:4096\n\t"
        "v_smfmac_f32_16x16x64_bf16 v[4:7], v[16:19], v[168:175], v110\n\t"
        "v_smfmac_f32_16x16x64_bf16 v[8:11], v[16:19], v[176:183], v110\n\t"
        "s_waitcnt lgkmcnt(8)\n\t"
        "v_smfmac_f32_16x16x64_bf16 v[12:15], v[20:23], v[40:47], v110\n\t"
        "ds_read_b128 v[40:43], v124 offset:1024\n\t"
        "v_smfmac_f32_16x16x64_bf16 v[8:11], v[20:23], v[200:207], v110\n\t"
        "ds_read_b128 v[44:47], v124 offset:5120\n\t"
        "s_waitcnt lgkmcnt(7)\n\t"
        "v_smfmac_f32_16x16x64_bf16 v[12:15], v[24:27], v[48:55], v110\n\t"
        "ds_read_b128 v[48:51], v124 offset:2048\n\t"
        "v_smfmac_f32_16x16x64_bf16 v[8:11], v[24:27], v[224:231], v110\n\t"
        "ds_read_b128 v[52:55], v124 offset:6144\n\t"
        "s_waitcnt lgkmcnt(6)\n\t"
        "v_smfmac_f32_16x16x64_bf16 v[12:15], v[28:31], v[96:103], v110\n\t"
        "v_smfmac_f32_16x16x64_bf16 v[8:11], v[28:31], v[248:255], v110\n\t"
        "v_smfmac_f32_16x16x64_bf16 v[0:3], v[20:23], v[184:191], v110\n\t"
        "s_lshl_b32 s34, s43, 11\n\t"
        "v_add_u32_e32 v108, s34, v124\n\t"
        "v_smfmac_f32_16x16x64_bf16 v[0:3], v[24:27], v[208:215], v110\n\t"
        "v_add_u32_e32 v109, s58, v124\n\t"
        "v_smfmac_f32_16x16x64_bf16 v[4:7], v[20:23], v[192:199], v110\n\t"
        "v_smfmac_f32_16x16x64_bf16 v[0:3], v[28:31], v[232:239], v110\n\t"
        "v_smfmac_f32_16x16x64_bf16 v[4:7], v[24:27], v[216:223], v110\n\t"
        "buffer_load_dwordx4 v[104:107], v120, s[8:11], s26 offen sc1\n\t"
        "v_smfmac_f32_16x16x64_bf16 v[4:7], v[28:31], v[240:247], v110\n\t"
        "v_add_f32_e32 v12, v12, v13\n\t"
        "v_add_f32_e32 v14, v14, v15\n\t"
        "v_cvt_pk_bf16_f32 v12, v12, v14\n\t"
        "v_add_f32_e32 v8, v8, v9\n\t"
        "v_add_f32_e32 v10, v10, v11\n\t"
        "v_cvt_pk_bf16_f32 v8, v8, v10\n\t"
        "ds_write2_b32 v118, v8, v12 offset0:64 offset1:96\n\t"
        "v_add_f32_e32 v0, v0, v1\n\t"
        "v_add_f32_e32 v2, v2, v3\n\t"
        "v_cvt_pk_bf16_f32 v0, v0, v2\n\t"
        "v_add_f32_e32 v4, v4, v5\n\t"
        "v_add_f32_e32 v6, v6, v7\n\t"
        "v_cvt_pk_bf16_f32 v4, v4, v6\n\t"
        "ds_write2_b32 v118, v0, v4 offset0:0 offset1:32\n\t"
        "s_cmp_eq_u32 s24, 63\n\t"
        "s_waitcnt lgkmcnt(0)\n\t"
        "s_barrier\n\t"
        "s_cbranch_scc1 LA_skip%=\n\t"
        "ds_read_b64_tr_b16 v[16:17], v119 offset:0\n\t"
        "ds_read_b64_tr_b16 v[18:19], v119 offset:32\n\t"
        "ds_read_b64_tr_b16 v[20:21], v119 offset:256\n\t"
        "ds_read_b64_tr_b16 v[22:23], v119 offset:288\n\t"
        "ds_read_b64_tr_b16 v[24:25], v119 offset:512\n\t"
        "ds_read_b64_tr_b16 v[26:27], v119 offset:544\n\t"
        "ds_read_b64_tr_b16 v[28:29], v119 offset:768\n\t"
        "ds_read_b64_tr_b16 v[30:31], v119 offset:800\n\t"
        "buffer_store_dword v126, v127, s[12:15], 0 offen sc1\n\t"
        "s_waitcnt lgkmcnt(6)\n\t"
        "v_mfma_f32_16x16x32_bf16 v[0:3], v[32:35], v[16:19], 0\n\t"
        "ds_read_b64_tr_b16 v[32:33], v119 offset:2112\n\t"
        "ds_read_b64_tr_b16 v[34:35], v119 offset:2144\n\t"
        "v_mfma_f32_16x16x32_bf16 v[4:7], v[36:39], v[16:19], 0\n\t"
        "ds_read_b64_tr_b16 v[36:37], v119 offset:2368\n\t"
        "ds_read_b64_tr_b16 v[38:39], v119 offset:2400\n\t"
        "s_waitcnt lgkmcnt(8)\n\t"
        "v_mfma_f32_16x16x32_bf16 v[0:3], v[40:43], v[20:23], v[0:3]\n\t"
        "ds_read_b64_tr_b16 v[40:41], v119 offset:2624\n\t"
        "ds_read_b64_tr_b16 v[42:43], v119 offset:2656\n\t"
        "v_mfma_f32_16x16x32_bf16 v[4:7], v[44:47], v[20:23], v[4:7]\n\t"
        "ds_read_b64_tr_b16 v[44:45], v119 offset:2880\n\t"
        "ds_read_b64_tr_b16 v[46:47], v119 offset:2912\n\t"
        "s_waitcnt lgkmcnt(10)\n\t"
        "v_mfma_f32_16x16x32_bf16 v[0:3], v[48:51], v[24:27], v[0:3]\n\t"
        "ds_read_b128 v[48:51], v108 offset:16384\n\t"
        "v_mfma_f32_16x16x32_bf16 v[4:7], v[52:55], v[24:27], v[4:7]\n\t"
        "ds_read_b128 v[52:55], v108 offset:17408\n\t"
        "s_waitcnt lgkmcnt(10)\n\t"
        "v_mfma_f32_16x16x32_bf16 v[0:3], v[56:59], v[28:31], v[0:3]\n\t"
        "v_mfma_f32_16x16x32_bf16 v[4:7], v[60:63], v[28:31], v[4:7]\n\t"
        "s_waitcnt lgkmcnt(2)\n\t"
        "v_mfma_f32_16x16x32_bf16 v[8:11], v[32:35], v[16:19], 0\n\t"
        "s_nop 1\n\t"
        "v_mfma_f32_16x16x32_bf16 v[8:11], v[36:39], v[20:23], v[8:11]\n\t"
        "v_mov_b64_e32 v[16:17], s[0:1]\n\t"
        "v_mov_b64_e32 v[18:19], s[0:1]\n\t"
        "v_mfma_f32_16x16x32_bf16 v[8:11], v[40:43], v[24:27], v[8:11]\n\t"
        "v_max3_f32 v59, v0, v1, v2\n\t"
        "v_max3_f32 v59, v59, v3, v4\n\t"
        "v_mfma_f32_16x16x32_bf16 v[8:11], v[44:47], v[28:31], v[8:11]\n\t"
        "v_max3_f32 v59, v59, v5, v6\n\t"
        "v_max_f32_e32 v60, v59, v7\n\t"
        "v_max_f32_e32 v59, v59, v7\n\t"
        "v_cndmask_b32_e64 v0, v0, v4, s[38:39]\n\t"
        "v_cndmask_b32_e64 v1, v1, v5, s[38:39]\n\t"
        "v_permlane32_swap_b32_e32 v59, v60\n\t"
        "v_cndmask_b32_e64 v2, v2, v6, s[38:39]\n\t"
        "v_cndmask_b32_e64 v3, v3, v7, s[38:39]\n\t"
        "v_max3_f32 v59, v59, v60, v8\n\t"
        "v_max3_f32 v59, v59, v9, v10\n\t"
        "v_max_f32_e32 v60, v59, v11\n\t"
        "v_max_f32_e32 v59, v59, v11\n\t"
        "v_cndmask_b32_e64 v8, v8, v9, s[38:39]\n\t"
        "v_cndmask_b32_e64 v10, v10, v11, s[38:39]\n\t"
        "v_permlane16_swap_b32_e32 v59, v60\n\t"
        "v_max_f32_e32 v59, v59, v60\n\t"
        "v_mul_f32_e32 v59, 0xbe02940c, v59\n\t"
        "v_cndmask_b32_e64 v58, v8, v10, s[40:41]\n\t"
        "v_fmamk_f32 v0, v0, 0x3e02940c, v59\n\t"
        "v_exp_f32_e32 v0, v0\n\t"
        "v_fmamk_f32 v1, v1, 0x3e02940c, v59\n\t"
        "v_exp_f32_e32 v1, v1\n\t"
        "v_fmamk_f32 v2, v2, 0x3e02940c, v59\n\t"
        "v_exp_f32_e32 v2, v2\n\t"
        "v_fmamk_f32 v3, v3, 0x3e02940c, v59\n\t"
        "v_exp_f32_e32 v3, v3\n\t"
        "v_fmamk_f32 v58, v58, 0x3e02940c, v59\n\t"
        "v_cvt_pk_bf16_f32 v12, v0, v1\n\t"
        "v_cvt_pk_bf16_f32 v13, v2, v3\n\t"
        "v_exp_f32_e32 v58, v58\n\t"
        "s_waitcnt lgkmcnt(0)\n\t"
        "v_mov_b32_dpp v14, v12 row_ror:8 row_mask:0xf bank_mask:0xf\n\t"
        "v_mov_b32_dpp v15, v13 row_ror:8 row_mask:0xf bank_mask:0xf\n\t"
        "v_mov_b32_dpp v59, v58 row_ror:8 row_mask:0xf bank_mask:0xf\n\t"
        "v_cvt_pk_bf16_f32 v59, v58, v59\n\t"
        "v_mfma_f32_16x16x32_bf16 v[20:23], v[16:19], v[12:15], 0\n\t"
        "v_cndmask_b32_e64 v56, v59, 0, s[40:41]\n\t"
        "v_cndmask_b32_e64 v57, 0, v59, s[40:41]\n\t"
        "v_mfma_f32_16x16x32_bf16 v[4:7], v[48:51], v[12:15], 0\n\t"
        "s_waitcnt vmcnt(1)\n\t"
        "v_max3_u32 v111, v104, v105, v106\n\t"
        "v_mfma_f32_16x16x32_bf16 v[8:11], v[52:55], v[12:15], 0\n\t"
        "v_max_u32_e32 v111, v111, v107\n\t"
        "v_mfma_f32_16x16x16_bf16 v[20:23], v[16:17], v[56:57], v[20:23]\n\t"
        "v_cmp_eq_u32_e32 vcc, -1, v111\n\t"
        "v_mfma_f32_16x16x16_bf16 v[4:7], v[104:105], v[56:57], v[4:7]\n\t"
        "s_nop 1\n\t"
        "v_mfma_f32_16x16x16_bf16 v[8:11], v[106:107], v[56:57], v[8:11]\n\t"
        "s_cbranch_vccnz LA_vmfail%=\n\t"
        "LA_vmok%=:\n\t"
        "s_nop 1\n\t"
        "v_rcp_f32_e32 v59, v20\n\t"
        "s_nop 0\n\t"
        "v_pk_mul_f32 v[4:5], v[4:5], v[58:59] op_sel:[0,1] op_sel_hi:[1,1]\n\t"
        "v_cvt_pk_bf16_f32 v0, v4, v5\n\t"
        "v_pk_mul_f32 v[6:7], v[6:7], v[58:59] op_sel:[0,1] op_sel_hi:[1,1]\n\t"
        "v_cvt_pk_bf16_f32 v1, v6, v7\n\t"
        "v_pk_mul_f32 v[8:9], v[8:9], v[58:59] op_sel:[0,1] op_sel_hi:[1,1]\n\t"
        "v_cvt_pk_bf16_f32 v2, v8, v9\n\t"
        "v_pk_mul_f32 v[10:11], v[10:11], v[58:59] op_sel:[0,1] op_sel_hi:[1,1]\n\t"
        "v_cvt_pk_bf16_f32 v3, v10, v11\n\t"
        "buffer_store_dwordx4 v[0:3], v121, s[8:11], s26 offen sc1\n\t"
        "LA_x%=:\n\t"
        "s_memrealtime s[52:53]\n\t"
        "ds_read_b128 v[16:19], v109 offset:8192\n\t"
        "ds_read_b128 v[20:23], v109 offset:9216\n\t"
        "ds_read_b128 v[24:27], v109 offset:10240\n\t"
        "ds_read_b128 v[28:31], v109 offset:11264\n\t"
        "ds_read_b128 v[56:59], v124 offset:0\n\t"
        "ds_read_b128 v[60:63], v124 offset:4096\n\t"
        "ds_read_b128 v[12:15], v124 offset:1024\n\t"
        "v_xor_b32_e32 v126, 0x800, v108\n\t"
        "s_add_u32 s48, s24, 1\n\t"
        "s_min_u32 s48, s48, 63\n\t"
        "buffer_load_dwordx2 v[108:109], v125, s[8:11], s26 offen sc1\n\t"
        "s_lshl_b32 s35, s44, 10\n\t"
        "v_lshlrev_b32_e32 v111, 2, v122\n\t"
        "v_and_b32_e32 v111, 0x3c0, v111\n\t"
        "buffer_load_dword v111, v111, s[12:15], s35 offen offset:0 sc1\n\t"
        "s_waitcnt lgkmcnt(6)\n\t"
        "v_mfma_f32_16x16x32_bf16 v[8:11], v[32:35], v[16:19], 0\n\t"
        "ds_read_b128 v[32:35], v124 offset:5120\n\t"
        "s_waitcnt lgkmcnt(6)\n\t"
        "v_mfma_f32_16x16x32_bf16 v[8:11], v[36:39], v[20:23], v[8:11]\n\t"
        "ds_read_b128 v[36:39], v124 offset:2048\n\t"
        "s_waitcnt lgkmcnt(6)\n\t"
        "v_mfma_f32_16x16x32_bf16 v[8:11], v[40:43], v[24:27], v[8:11]\n\t"
        "ds_read_b128 v[40:43], v124 offset:6144\n\t"
        "s_waitcnt lgkmcnt(6)\n\t"
        "v_mfma_f32_16x16x32_bf16 v[8:11], v[44:47], v[28:31], v[8:11]\n\t"
        "ds_read_b128 v[44:47], v124 offset:3072\n\t"
        "s_add_u32 s35, s24, 1\n\t"
        "s_waitcnt lgkmcnt(6)\n\t"
        "v_mfma_f32_16x16x32_bf16 v[0:3], v[56:59], v[16:19], 0\n\t"
        "ds_read_b128 v[56:59], v124 offset:7168\n\t"
        "s_waitcnt lgkmcnt(6)\n\t"
        "v_mfma_f32_16x16x32_bf16 v[4:7], v[60:63], v[16:19], 0\n\t"
        "s_and_b32 s36, s35, 1\n\t"
        "s_mul_i32 s36, s36, 24576\n\t"
        "s_waitcnt lgkmcnt(5)\n\t"
        "v_mfma_f32_16x16x32_bf16 v[0:3], v[12:15], v[20:23], v[0:3]\n\t"
        "s_add_u32 s36, s36, s55\n\t"
        "s_lshl_b32 s37, s48, 18\n\t"
        "s_waitcnt lgkmcnt(4)\n\t"
        "v_mfma_f32_16x16x32_bf16 v[4:7], v[32:35], v[20:23], v[4:7]\n\t"
        "v_xor_b32_e32 v32, 0x200, v120\n\t"
        "buffer_load_dwordx4 v[12:15], v32, s[8:11], s26 offen sc1\n\t"
        "s_waitcnt lgkmcnt(3)\n\t"
        "v_mfma_f32_16x16x32_bf16 v[0:3], v[36:39], v[24:27], v[0:3]\n\t"
        "s_lshl_b32 s34, s43, 11\n\t"
        "s_add_u32 s37, s37, s34\n\t"
        "s_waitcnt lgkmcnt(2)\n\t"
        "v_mfma_f32_16x16x32_bf16 v[4:7], v[40:43], v[24:27], v[4:7]\n\t"
        "v_cvt_pk_bf16_f32 v8, 0, v8\n\t"
        "v_cvt_pk_bf16_f32 v9, 0, v9\n\t"
        "v_cvt_pk_bf16_f32 v10, 0, v10\n\t"
        "v_cvt_pk_bf16_f32 v11, 0, v11\n\t"
        "s_waitcnt lgkmcnt(1)\n\t"
        "v_mfma_f32_16x16x32_bf16 v[0:3], v[44:47], v[28:31], v[0:3]\n\t"
        "v_mul_f32_e32 v8, 0x3db504f3, v8\n\t"
        "v_mul_f32_e32 v9, 0x3db504f3, v9\n\t"
        "v_mul_f32_e32 v10, 0x3db504f3, v10\n\t"
        "v_mul_f32_e32 v11, 0x3db504f3, v11\n\t"
        "s_waitcnt lgkmcnt(0)\n\t"
        "v_mfma_f32_16x16x32_bf16 v[4:7], v[56:59], v[28:31], v[4:7]\n\t"
        "v_cvt_pk_bf16_f32 v8, 0, v8\n\t"
        "v_cvt_pk_bf16_f32 v9, 0, v9\n\t"
        "v_cvt_pk_bf16_f32 v10, 0, v10\n\t"
        "v_cvt_pk_bf16_f32 v11, 0, v11\n\t"
        "ds_read_b128 v[56:59], v126 offset:16384\n\t"
        "ds_read_b128 v[60:63], v126 offset:17408\n\t"
        "v_mov_b64_e32 v[36:37], -1\n\t"
        "v_mov_b64_e32 v[38:39], -1\n\t"
        "v_mov_b32_e32 v40, 0xce6e6b28\n\t"
        "v_mov_b32_e32 v46, 0x3db504f3\n\t"
        "v_mov_b32_e32 v47, 0x3fb8aa3b\n\t"
        "v_cvt_pk_bf16_f32 v0, 0, v0\n\t"
        "v_cvt_pk_bf16_f32 v1, 0, v1\n\t"
        "v_cvt_pk_bf16_f32 v2, 0, v2\n\t"
        "v_cvt_pk_bf16_f32 v3, 0, v3\n\t"
        "v_cvt_pk_bf16_f32 v4, 0, v4\n\t"
        "v_cvt_pk_bf16_f32 v5, 0, v5\n\t"
        "v_cvt_pk_bf16_f32 v6, 0, v6\n\t"
        "v_cvt_pk_bf16_f32 v7, 0, v7\n\t"
        "v_pk_mul_f32 v[0:1], v[0:1], v[46:47] op_sel_hi:[1,0]\n\t"
        "v_pk_mul_f32 v[2:3], v[2:3], v[46:47] op_sel_hi:[1,0]\n\t"
        "v_pk_mul_f32 v[4:5], v[4:5], v[46:47] op_sel_hi:[1,0]\n\t"
        "v_pk_mul_f32 v[6:7], v[6:7], v[46:47] op_sel_hi:[1,0]\n\t"
        "s_lshr_b32 s59, s48, 1\n\t"
        "s_cmp_gt_u32 s54, s59\n\t"
        "s_cbranch_scc1 LA_kvok%=\n\t"
        "s_mov_b32 s42, 0\n\t"
        "s_waitcnt vmcnt(1)\n\t"
        "s_branch LA_kvw%=\n\t"
        "LA_kv%=:\n\t"
        "s_waitcnt vmcnt(0)\n\t"
        "LA_kvw%=:\n\t"
        "v_min_u32_dpp v111, v111, v111 quad_perm:[1,0,3,2] row_mask:0xf bank_mask:0xf\n\t"
        "s_nop 1\n\t"
        "v_min_u32_dpp v111, v111, v111 quad_perm:[2,3,0,1] row_mask:0xf bank_mask:0xf\n\t"
        "s_nop 1\n\t"
        "v_min_u32_dpp v111, v111, v111 row_ror:4 row_mask:0xf bank_mask:0xf\n\t"
        "s_nop 1\n\t"
        "v_min_u32_dpp v111, v111, v111 row_ror:8 row_mask:0xf bank_mask:0xf\n\t"
        "s_nop 0\n\t"
        "v_readfirstlane_b32 s54, v111\n\t"
        "s_nop 0\n\t"
        "s_cmp_gt_u32 s54, s59\n\t"
        "s_cbranch_scc1 LA_kvok%=\n\t"
        "s_sleep 4\n\t"
        "s_lshl_b32 s35, s44, 10\n\t"
        "v_lshlrev_b32_e32 v111, 2, v122\n\t"
        "v_and_b32_e32 v111, 0x3c0, v111\n\t"
        "buffer_load_dword v111, v111, s[12:15], s35 offen offset:0 sc1\n\t"
        "s_add_u32 s42, s42, 1\n\t"
        "s_cmp_lt_u32 s42, 0x100000\n\t"
        "s_cbranch_scc1 LA_kv%=\n\t"
        "s_movk_i32 s34, 16\n\t"
        "s_branch LA_die%=\n\t"
        "LA_kvok%=:\n\t"
        "s_add_u32 m0, s36, 0\n\t"
        "s_add_u32 s34, s37, 29548544\n\t"
        "buffer_load_dwordx4 v122, s[12:15], s34 offen sc1 lds\n\t"
        "v_cvt_pk_bf16_f32 v0, 0, v0\n\t"
        "v_cvt_pk_bf16_f32 v1, 0, v1\n\t"
        "v_cvt_pk_bf16_f32 v2, 0, v2\n\t"
        "v_cvt_pk_bf16_f32 v3, 0, v3\n\t"
        "v_cvt_pk_bf16_f32 v4, 0, v4\n\t"
        "v_cvt_pk_bf16_f32 v5, 0, v5\n\t"
        "v_cvt_pk_bf16_f32 v6, 0, v6\n\t"
        "v_cvt_pk_bf16_f32 v7, 0, v7\n\t"
        "s_add_u32 m0, s36, 1024\n\t"
        "s_add_u32 s34, s37, 29549568\n\t"
        "buffer_load_dwordx4 v122, s[12:15], s34 offen sc1 lds\n\t"
        "v_cndmask_b32_e64 v0, v40, v0, s[60:61]\n\t"
        "v_cndmask_b32_e64 v1, v40, v1, s[62:63]\n\t"
        "v_cndmask_b32_e64 v2, v40, v2, s[64:65]\n\t"
        "v_cndmask_b32_e64 v3, v40, v3, s[66:67]\n\t"
        "v_cndmask_b32_e64 v4, v40, v4, s[68:69]\n\t"
        "v_cndmask_b32_e64 v5, v40, v5, s[70:71]\n\t"
        "v_cndmask_b32_e64 v6, v40, v6, s[72:73]\n\t"
        "v_cndmask_b32_e64 v7, v40, v7, s[74:75]\n\t"
        "s_add_u32 m0, s36, 8192\n\t"
        "s_add_u32 s34, s37, 12771328\n\t"
        "buffer_load_dwordx4 v122, s[12:15], s34 offen sc1 lds\n\t"
        "v_max3_f32 v42, v0, v1, v2\n\t"
        "v_max3_f32 v42, v42, v3, v4\n\t"
        "v_max3_f32 v42, v42, v5, v6\n\t"
        "v_max3_f32 v42, v42, v7, v8\n\t"
        "v_max3_f32 v42, v42, v9, v10\n\t"
        "v_mov_b32_e32 v43, v11\n\t"
        "v_max_f32_e32 v44, v42, v43\n\t"
        "v_max_f32_e32 v42, v42, v43\n\t"
        "s_pack_ll_b32_b16 s34, s52, s24\n\t"
        "v_mov_b32_e32 v40, s34\n\t"
        "s_add_u32 s35, s26, s57\n\t"
        "v_permlane16_swap_b32_e32 v42, v44\n\t"
        "v_max_f32_e32 v45, v42, v44\n\t"
        "v_max_f32_e32 v42, v42, v44\n\t"
        "s_mov_b64 exec, 1\n\t"
        "buffer_store_dword v40, off, s[8:11], s35 sc1\n\t"
        "s_mov_b64 exec, -1\n\t"
        "v_permlane32_swap_b32_e32 v42, v45\n\t"
        "v_max_f32_e32 v42, v42, v45\n\t"
        "v_mul_f32_e32 v42, 0xbfb8aa3b, v42\n\t"
        "v_pk_fma_f32 v[0:1], v[0:1], v[46:47], v[42:43] op_sel:[0,1,0] op_sel_hi:[1,1,0]\n\t"
        "s_add_u32 m0, s36, 9216\n\t"
        "s_add_u32 s34, s37, 12772352\n\t"
        "buffer_load_dwordx4 v122, s[12:15], s34 offen sc1 lds\n\t"
        "v_exp_f32_e32 v0, v0\n\t"
        "v_exp_f32_e32 v1, v1\n\t"
        "v_pk_fma_f32 v[2:3], v[2:3], v[46:47], v[42:43] op_sel:[0,1,0] op_sel_hi:[1,1,0]\n\t"
        "v_exp_f32_e32 v2, v2\n\t"
        "v_exp_f32_e32 v3, v3\n\t"
        "v_pk_fma_f32 v[4:5], v[4:5], v[46:47], v[42:43] op_sel:[0,1,0] op_sel_hi:[1,1,0]\n\t"
        "v_exp_f32_e32 v4, v4\n\t"
        "s_add_u32 m0, s36, 16384\n\t"
        "s_add_u32 s34, s37, 46325760\n\t"
        "buffer_load_dwordx4 v122, s[12:15], s34 offen sc1 lds\n\t"
        "v_exp_f32_e32 v5, v5\n\t"
        "v_pk_fma_f32 v[6:7], v[6:7], v[46:47], v[42:43] op_sel:[0,1,0] op_sel_hi:[1,1,0]\n\t"
        "v_exp_f32_e32 v6, v6\n\t"
        "v_exp_f32_e32 v7, v7\n\t"
        "v_pk_fma_f32 v[8:9], v[8:9], v[46:47], v[42:43] op_sel:[0,1,0] op_sel_hi:[1,1,0]\n\t"
        "v_exp_f32_e32 v8, v8\n\t"
        "v_exp_f32_e32 v9, v9\n\t"
        "s_add_u32 m0, s36, 17408\n\t"
        "s_add_u32 s34, s37, 46326784\n\t"
        "buffer_load_dwordx4 v122, s[12:15], s34 offen sc1 lds\n\t"
        "v_pk_fma_f32 v[10:11], v[10:11], v[46:47], v[42:43] op_sel:[0,1,0] op_sel_hi:[1,1,0]\n\t"
        "v_exp_f32_e32 v10, v10\n\t"
        "v_exp_f32_e32 v11, v11\n\t"
        "v_pk_add_f32 v[42:43], v[0:1], v[2:3]\n\t"
        "v_cndmask_b32_e64 v8, v8, 0, s[40:41]\n\t"
        "v_cndmask_b32_e64 v9, v9, 0, s[40:41]\n\t"
        "v_cndmask_b32_e64 v10, v10, 0, s[40:41]\n\t"
        "buffer_store_dwordx4 v[36:39], v121, s[8:11], s28 offen sc1\n\t"
        "v_cndmask_b32_e64 v11, v11, 0, s[40:41]\n\t"
        "v_pk_add_f32 v[42:43], v[42:43], v[4:5]\n\t"
        "v_pk_add_f32 v[42:43], v[42:43], v[6:7]\n\t"
        "v_pk_add_f32 v[42:43], v[42:43], v[8:9]\n\t"
        "v_pk_add_f32 v[42:43], v[42:43], v[10:11]\n\t"
        "v_add_f32_e32 v44, v42, v43\n\t"
        "v_add_f32_e32 v42, v42, v43\n\t"
        "s_lshl_b32 s34, s24, 18\n\t"
        "s_add_u32 s34, s34, 63102976\n\t"
        "v_permlane16_swap_b32_e32 v42, v44\n\t"
        "v_add_f32_e32 v45, v42, v44\n\t"
        "v_add_f32_e32 v42, v42, v44\n\t"
        "s_add_u32 s35, s24, 1\n\t"
        "v_mov_b32_e32 v126, s35\n\t"
        "v_permlane32_swap_b32_e32 v42, v45\n\t"
        "v_add_f32_e32 v42, v42, v45\n\t"
        "v_rcp_f32_e32 v42, v42\n\t"
        "s_waitcnt vmcnt(8) lgkmcnt(0)\n\t"
        "v_max3_u32 v45, v12, v13, v14\n\t"
        "v_max3_u32 v45, v45, v15, v104\n\t"
        "v_max3_u32 v45, v45, v105, v106\n\t"
        "v_max_u32_e32 v45, v45, v107\n\t"
        "v_cmp_eq_u32_e32 vcc, -1, v45\n\t"
        "s_nop 0\n\t"
        "s_cbranch_vccnz LA_v2fail%=\n\t"
        "LA_v2ok%=:\n\t"
        "v_and_b32_e32 v45, 0xff, v109\n\t"
        "v_cmp_ne_u32_e32 vcc, s24, v45\n\t"
        "s_nop 0\n\t"
        "s_cbranch_vccnz LA_stre%=\n\t"
        "LA_stok%=:\n\t"
        "v_pk_mul_f32 v[0:1], v[0:1], v[42:43] op_sel_hi:[1,0]\n\t"
        "v_subrev_u32_e32 v108, s25, v108\n\t"
        "v_min_u32_dpp v111, v111, v111 quad_perm:[1,0,3,2] row_mask:0xf bank_mask:0xf\n\t"
        "v_cvt_pk_bf16_f32 v32, v0, v1\n\t"
        "v_pk_mul_f32 v[2:3], v[2:3], v[42:43] op_sel_hi:[1,0]\n\t"
        "v_cvt_pk_bf16_f32 v33, v2, v3\n\t"
        "v_max_i32_dpp v108, v108, v108 quad_perm:[1,0,3,2] row_mask:0xf bank_mask:0xf\n\t"
        "v_min_u32_dpp v111, v111, v111 quad_perm:[2,3,0,1] row_mask:0xf bank_mask:0xf\n\t"
        "v_pk_mul_f32 v[4:5], v[4:5], v[42:43] op_sel_hi:[1,0]\n\t"
        "v_cvt_pk_bf16_f32 v34, v4, v5\n\t"
        "v_pk_mul_f32 v[6:7], v[6:7], v[42:43] op_sel_hi:[1,0]\n\t"
        "v_max_i32_dpp v108, v108, v108 quad_perm:[2,3,0,1] row_mask:0xf bank_mask:0xf\n\t"
        "v_min_u32_dpp v111, v111, v111 row_ror:4 row_mask:0xf bank_mask:0xf\n\t"
        "v_cvt_pk_bf16_f32 v35, v6, v7\n\t"
        "v_pk_mul_f32 v[8:9], v[8:9], v[42:43] op_sel_hi:[1,0]\n\t"
        "v_cvt_pk_bf16_f32 v36, v8, v9\n\t"
        "v_max_i32_dpp v108, v108, v108 row_ror:4 row_mask:0xf bank_mask:0xf\n\t"
        "v_min_u32_dpp v111, v111, v111 row_ror:8 row_mask:0xf bank_mask:0xf\n\t"
        "v_pk_mul_f32 v[10:11], v[10:11], v[42:43] op_sel_hi:[1,0]\n\t"
        "v_cvt_pk_bf16_f32 v37, v10, v11\n\t"
        "v_mfma_f32_16x16x32_bf16 v[16:19], v[48:51], v[32:35], 0\n\t"
        "v_max_i32_dpp v108, v108, v108 row_ror:8 row_mask:0xf bank_mask:0xf\n\t"
        "v_mfma_f32_16x16x32_bf16 v[20:23], v[52:55], v[32:35], 0\n\t"
        "v_mfma_f32_16x16x32_bf16 v[24:27], v[56:59], v[32:35], 0\n\t"
        "v_mfma_f32_16x16x32_bf16 v[28:31], v[60:63], v[32:35], 0\n\t"
        "v_max_i32_dpp v108, v108, v108 row_bcast:15 row_mask:0xa bank_mask:0xf\n\t"
        "v_xor_b32_e32 v45, 0x400, v123\n\t"
        "v_add_u32_e32 v124, s56, v124\n\t"
        "v_mfma_f32_16x16x16_bf16 v[16:19], v[104:105], v[36:37], v[16:19]\n\t"
        "v_max_i32_dpp v108, v108, v108 row_bcast:31 row_mask:0xc bank_mask:0xf\n\t"
        "v_mfma_f32_16x16x16_bf16 v[20:23], v[106:107], v[36:37], v[20:23]\n\t"
        "v_mfma_f32_16x16x16_bf16 v[24:27], v[12:13], v[36:37], v[24:27]\n\t"
        "v_mfma_f32_16x16x16_bf16 v[28:31], v[14:15], v[36:37], v[28:31]\n\t"
        "ds_read_b128 v[48:51], v117 offset:4096\n\t"
        "ds_read_b128 v[52:55], v117 offset:5120\n\t"
        "ds_read_b128 v[56:59], v117 offset:6144\n\t"
        "ds_read_b128 v[60:63], v117 offset:7168\n\t"
        "s_sub_u32 s56, 0, s56\n\t"
        "v_cvt_pk_bf16_f32 v0, v16, v17\n\t"
        "v_cvt_pk_bf16_f32 v1, v18, v19\n\t"
        "v_cvt_pk_bf16_f32 v2, v20, v21\n\t"
        "v_cvt_pk_bf16_f32 v3, v22, v23\n\t"
        "buffer_store_dwordx4 v[0:3], v123, s[12:15], s34 offen sc1\n\t"
        "v_cvt_pk_bf16_f32 v4, v24, v25\n\t"
        "v_cvt_pk_bf16_f32 v5, v26, v27\n\t"
        "v_cvt_pk_bf16_f32 v6, v28, v29\n\t"
        "v_cvt_pk_bf16_f32 v7, v30, v31\n\t"
        "buffer_store_dwordx4 v[4:7], v45, s[12:15], s34 offen sc1\n\t"
        "s_cmp_eq_u32 s24, 63\n\t"
        "s_cbranch_scc1 LA_last%=\n\t"
        "ds_read_b128 v[32:35], v117 offset:0\n\t"
        "ds_read_b128 v[36:39], v117 offset:1024\n\t"
        "ds_read_b128 v[40:43], v117 offset:2048\n\t"
        "ds_read_b128 v[44:47], v117 offset:3072\n\t"
        "v_mov_b64_e32 v[14:15], 0\n\t"
        "v_mov_b64_e32 v[12:13], 0\n\t"
        "v_mov_b64_e32 v[10:11], 0\n\t"
        "v_mov_b64_e32 v[8:9], 0\n\t"
        "v_mov_b64_e32 v[6:7], 0\n\t"
        "v_mov_b64_e32 v[4:5], 0\n\t"
        "v_mov_b64_e32 v[2:3], 0\n\t"
        "v_mov_b64_e32 v[0:1], 0\n\t"
        "v_readfirstlane_b32 s54, v111\n\t"
        "v_readfirstlane_b32 s35, v109\n\t"
        "v_readlane_b32 s36, v108, 63\n\t"
        "s_bfe_u32 s29, s35, 0xc0008\n\t"
        "s_lshr_b32 s30, s35, 20\n\t"
        "s_add_u32 s35, s31, s50\n\t"
        "s_mul_i32 s37, s50, 3\n\t"
        "s_min_u32 s35, s35, s37\n\t"
        "s_add_i32 s34, s36, s51\n\t"
        "s_cmp_gt_i32 s34, 0\n\t"
        "s_cselect_b32 s31, s35, 0\n\t"
        "s_cselect_b32 s34, s34, 0\n\t"
        "s_add_u32 s34, s34, s31\n\t"
        "s_add_u32 s37, s25, s34\n\t"
        "s_add_u32 s45, s37, s29\n\t"
        "s_add_u32 s25, s45, s30\n\t"
        "s_add_u32 s24, s24, 1\n\t"
        "s_mov_b32 s28, s26\n\t"
        "s_mov_b32 s26, s27\n\t"
        "s_add_u32 s27, s27, 0x80000\n\t"
        "s_and_b32 s27, s27, 0x1fffff\n\t"
        "s_branch LA_loop%=\n\t"
        "LA_last%=:\n\t"
        "s_waitcnt vmcnt(0)\n\t"
        "s_barrier\n\t"
        "buffer_store_dword v126, v127, s[12:15], 0 offen sc1\n\t"
        "s_endpgm\n\t"
        "LA_skip%=:\n\t"
        "buffer_store_dword v126, v127, s[12:15], 0 offen sc1\n\t"
        "ds_read_b64_tr_b16 v[32:33], v119 offset:2112\n\t"
        "ds_read_b64_tr_b16 v[34:35], v119 offset:2144\n\t"
        "ds_read_b64_tr_b16 v[36:37], v119 offset:2368\n\t"
        "ds_read_b64_tr_b16 v[38:39], v119 offset:2400\n\t"
        "ds_read_b64_tr_b16 v[40:41], v119 offset:2624\n\t"
        "ds_read_b64_tr_b16 v[42:43], v119 offset:2656\n\t"
        "ds_read_b64_tr_b16 v[44:45], v119 offset:2880\n\t"
        "ds_read_b64_tr_b16 v[46:47], v119 offset:2912\n\t"
        "ds_read_b128 v[48:51], v108 offset:16384\n\t"
        "ds_read_b128 v[52:55], v108 offset:17408\n\t"
        "s_waitcnt lgkmcnt(0)\n\t"
        "s_branch LA_x%=\n\t"
        "LA_stre%=:\n\t"
        "s_mov_b32 s42, 0\n\t"
        "LA_str2%=:\n\t"
        "buffer_load_dwordx2 v[108:109], v125, s[8:11], s26 offen sc1\n\t"
        "s_waitcnt vmcnt(0)\n\t"
        "v_and_b32_e32 v45, 0xff, v109\n\t"
        "v_cmp_ne_u32_e32 vcc, s24, v45\n\t"
        "s_nop 0\n\t"
        "s_cbranch_vccz LA_stok%=\n\t"
        "s_add_u32 s42, s42, 1\n\t"
        "s_cmp_lt_u32 s42, 0x100000\n\t"
        "s_cbranch_scc1 LA_str2%=\n\t"
        "s_movk_i32 s34, 5\n\t"
        "s_branch LA_die%=\n\t"
        "LA_v2fail%=:\n\t"
        "s_mov_b32 s42, 0\n\t"
        "LA_v2re%=:\n\t"
        "v_xor_b32_e32 v45, 0x200, v120\n\t"
        "buffer_load_dwordx4 v[12:15], v45, s[8:11], s26 offen sc1\n\t"
        "buffer_load_dwordx4 v[104:107], v120, s[8:11], s26 offen sc1\n\t"
        "s_waitcnt vmcnt(0)\n\t"
        "v_max3_u32 v45, v12, v13, v14\n\t"
        "v_max3_u32 v45, v45, v15, v104\n\t"
        "v_max3_u32 v45, v45, v105, v106\n\t"
        "v_max_u32_e32 v45, v45, v107\n\t"
        "v_cmp_eq_u32_e32 vcc, -1, v45\n\t"
        "s_nop 0\n\t"
        "s_cbranch_vccz LA_v2ok%=\n\t"
        "s_add_u32 s42, s42, 1\n\t"
        "s_cmp_lt_u32 s42, 0x100000\n\t"
        "s_cbranch_scc1 LA_v2re%=\n\t"
        "s_movk_i32 s34, 6\n\t"
        "s_branch LA_die%=\n\t"
        "LA_rdfail%=:\n\t"
        "s_or_b32 s47, s47, 1\n\t"
        "buffer_load_dwordx4 v[16:19], v112, s[8:11], s26 offen offset:0 sc1\n\t"
        "buffer_load_dwordx4 v[20:23], v112, s[8:11], s26 offen offset:1024 sc1\n\t"
        "buffer_load_dwordx4 v[24:27], v112, s[8:11], s26 offen offset:2048 sc1\n\t"
        "buffer_load_dwordx4 v[28:31], v112, s[8:11], s26 offen offset:3072 sc1\n\t"
        "s_nop 7\n\t"
        "v_mov_b64_e32 v[0:1], 0\n\t"
        "v_mov_b64_e32 v[2:3], 0\n\t"
        "v_mov_b64_e32 v[4:5], 0\n\t"
        "v_mov_b64_e32 v[6:7], 0\n\t"
        "v_mov_b64_e32 v[8:9], 0\n\t"
        "v_mov_b64_e32 v[10:11], 0\n\t"
        "v_mov_b64_e32 v[12:13], 0\n\t"
        "v_mov_b64_e32 v[14:15], 0\n\t"
        "s_branch LA_rdre%=w\n\t"
        "LA_rdre%=:\n\t"
        "buffer_load_dwordx4 v[16:19], v112, s[8:11], s26 offen offset:0 sc1\n\t"
        "buffer_load_dwordx4 v[20:23], v112, s[8:11], s26 offen offset:1024 sc1\n\t"
        "buffer_load_dwordx4 v[24:27], v112, s[8:11], s26 offen offset:2048 sc1\n\t"
        "buffer_load_dwordx4 v[28:31], v112, s[8:11], s26 offen offset:3072 sc1\n\t"
        "LA_rdre%=w:\n\t"
        "s_waitcnt vmcnt(0) lgkmcnt(0)\n\t"
        "v_max3_u32 v111, v16, v17, v18\n\t"
        "v_max3_u32 v111, v111, v19, v20\n\t"
        "v_max3_u32 v111, v111, v21, v22\n\t"
        "v_max3_u32 v111, v111, v23, v24\n\t"
        "v_max3_u32 v111, v111, v25, v26\n\t"
        "v_max3_u32 v111, v111, v27, v28\n\t"
        "v_max3_u32 v111, v111, v29, v30\n\t"
        "v_max_u32_e32 v111, v111, v31\n\t"
        "v_cmp_eq_u32_e32 vcc, -1, v111\n\t"
        "s_nop 0\n\t"
        "s_cbranch_vccz LA_go%=\n\t"
        "s_add_u32 s42, s42, 1\n\t"
        "s_cmp_lt_u32 s42, 0x100000\n\t"
        "s_cbranch_scc1 LA_rdre%=\n\t"
        "s_movk_i32 s34, 1\n\t"
        "s_branch LA_die%=\n\t"
        "LA_f2%=:\n\t"
        "s_or_b32 s47, s47, 1\n\t"
        "buffer_load_dwordx4 v[24:27], v112, s[8:11], s26 offen offset:2048 sc1\n\t"
        "s_nop 7\n\t"
        "s_branch LA_r2%=w\n\t"
        "LA_r2%=:\n\t"
        "buffer_load_dwordx4 v[24:27], v112, s[8:11], s26 offen offset:2048 sc1\n\t"
        "LA_r2%=w:\n\t"
        "s_waitcnt vmcnt(0) lgkmcnt(0)\n\t"
        "v_max3_u32 v111, v24, v25, v26\n\t"
        "v_max_u32_e32 v111, v111, v27\n\t"
        "v_cmp_eq_u32_e32 vcc, -1, v111\n\t"
        "s_nop 0\n\t"
        "s_cbranch_vccz LA_go%=\n\t"
        "s_add_u32 s42, s42, 1\n\t"
        "s_cmp_lt_u32 s42, 0x100000\n\t"
        "s_cbranch_scc1 LA_r2%=\n\t"
        "s_movk_i32 s34, 1\n\t"
        "s_branch LA_die%=\n\t"
        "LA_f3%=:\n\t"
        "s_or_b32 s47, s47, 1\n\t"
        "buffer_load_dwordx4 v[28:31], v112, s[8:11], s26 offen offset:3072 sc1\n\t"
        "s_nop 7\n\t"
        "s_branch LA_r3%=w\n\t"
        "LA_r3%=:\n\t"
        "buffer_load_dwordx4 v[28:31], v112, s[8:11], s26 offen offset:3072 sc1\n\t"
        "LA_r3%=w:\n\t"
        "s_waitcnt vmcnt(0) lgkmcnt(0)\n\t"
        "v_max3_u32 v111, v28, v29, v30\n\t"
        "v_max_u32_e32 v111, v111, v31\n\t"
        "v_cmp_eq_u32_e32 vcc, -1, v111\n\t"
        "s_nop 0\n\t"
        "s_cbranch_vccz LA_go%=\n\t"
        "s_add_u32 s42, s42, 1\n\t"
        "s_cmp_lt_u32 s42, 0x100000\n\t"
        "s_cbranch_scc1 LA_r3%=\n\t"
        "s_movk_i32 s34, 1\n\t"
        "s_branch LA_die%=\n\t"
        "LA_vmfail%=:\n\t"
        "s_or_b32 s47, s47, 2\n\t"
        "s_mov_b32 s42, 0\n\t"
        "LA_vmre%=:\n\t"
        "buffer_load_dwordx4 v[104:107], v120, s[8:11], s26 offen sc1\n\t"
        "v_mfma_f32_16x16x32_bf16 v[4:7], v[48:51], v[12:15], 0\n\t"
        "v_mfma_f32_16x16x32_bf16 v[8:11], v[52:55], v[12:15], 0\n\t"
        "s_nop 7\n\t"
        "s_waitcnt vmcnt(0)\n\t"
        "v_mfma_f32_16x16x16_bf16 v[4:7], v[104:105], v[56:57], v[4:7]\n\t"
        "v_mfma_f32_16x16x16_bf16 v[8:11], v[106:107], v[56:57], v[8:11]\n\t"
        "v_max3_u32 v111, v104, v105, v106\n\t"
        "v_max_u32_e32 v111, v111, v107\n\t"
        "v_cmp_eq_u32_e32 vcc, -1, v111\n\t"
        "s_nop 7\n\t"
        "s_cbranch_vccz LA_vmok%=\n\t"
        "s_add_u32 s42, s42, 1\n\t"
        "s_cmp_lt_u32 s42, 0x100000\n\t"
        "s_cbranch_scc1 LA_vmre%=\n\t"
        "s_movk_i32 s34, 2\n\t"
        "s_branch LA_die%=\n\t"
        "LA_die%=:\n\t"
        "v_mov_b32_e32 v30, s34\n\t"
        "s_mov_b32 s35, 33024\n\t"
        "s_mov_b64 exec, 1\n\t"
        "buffer_store_dword v30, off, s[12:15], s35 sc1\n\t"
        "s_endpgm\n\t"
        ".Lk_end_A:\n\t"
        :: REC_S_IN, "{v96}"(g_w), "{v97}"(wb0), "{v98}"(wb1), "{v99}"(rb0), "{v100}"(rb0 ^ 32u), "{v101}"(rb0 ^ 64u), "{v102}"(rb0 ^ 96u),
           "{v112}"(a_mem), "{v113}"(l_ms + w * 4096), "{v114}"(l_ms + ((w + 1) & 3) * 4096), "{v115}"(l_ms + ((w + 2) & 3) * 4096), "{v116}"(l_ms + ((w + 3) & 3) * 4096),
           "{v117}"(lb + w * 22528 + l * 16), "{v118}"(l_iw), "{v119}"(l_iq), "{v120}"(a_vm), "{v121}"(a_o), "{v122}"(h * 8192 + l * 16), "{v123}"(a_ox), "{v124}"(lb + L_KV + l * 16), "{s55}"(lb + L_KV + w * 2048),
           "{v125}"((24 + cp) * 16384 + l * ST_P), "{v126}"(16 * (w & 1) + n - 4 * g), "{v103}"((l & 1) ? 0xEEEEEEEEu : 0x44444444u),
           "{v127}"((l == 0 && w == 0) ? F_PROG + h * 128 : OOB)
        : "memory", "vcc", "scc", "v0", "v255", "a0", "a255", "s101");
    __builtin_unreachable();
}
__device__ __forceinline__ void rec_b(const Params& p, unsigned lb, const int wv, const int id, const int rg, const int cp, const int rot, const int bq) {
    const int w = wv, l = otid() & 63, n = l & 15, g = l >> 4, cs = id, rb = 32 + id * 2 + rg;
    const v4u sh = mk_srd(p.ws + hx_base(p.ws), HX_BYTES - 8192), sw = mk_srd(p.ws, W_TOTAL), s1 = mk_srd(p.Wv, 1024 * 4096 * 2), s2 = mk_srd(p.Wo, 1024 * 4096 * 2);
    const int r8 = l >> 3, pc = l & 7, pp = n >> 3, bb = (n >> 2) & 1, f = (g & 1) | (pp << 1);
    const unsigned stg = lb + LB_STG + w * LB_STGP;
    const unsigned g_v = r8 * 8192 + cs * 256 + rg * 128 + pc * 16, g_o = (l >> 2) * 2048 + cs * 64 + (l & 3) * 16;
    const unsigned wb0 = stg + r8 * 128 + (((pc >> 1) ^ (r8 >> 2)) << 5) + (pc & 1) * 16, wb1 = wb0 ^ 64u;
    const unsigned rb0 = stg + (16 * (g >> 1) + 8 * pp + 4 * (g & 1) + bb) * 128 + (f << 5) + (n & 3) * 8;
    const unsigned o_r = stg + (g >> 1) * (2048 + 128) + (g & 1) * (1024 + 32) + (bb + 4 * pp) * 64 + (n & 3) * 8;
    const unsigned a_mem = cp * 32768 + (w >> 1) * 16384 + (w & 1) * 4096 + l * 16;
    const unsigned a_vmw = w < 2 ? (16 + (cs >> 2)) * 16384 + (cs & 3) * 2048 + (2 * rg + w) * 512 + (l & 31) * 16 + (g >> 1) * 8 : OOB;
    const unsigned a_or = (8 + rg * 4 + w) * 16384 + l * 16;
    const unsigned a_mw = (RPL != 0 && RPL != 4 && w == 3) ? OOB : w * 32768 + (cs >> 4) * 16384 + ((cs >> 1) & 7) * 1024 + (cs & 1) * 512 + (l >> 5) * 256 + rg * 128 + (l & 31) * 4;
    const unsigned a_sw = (w == 0 && l < 4) ? (24 + l) * 16384 + (id * 2 + rg) * ST_P : OOB;
    const bf16_t* m0p = p.mem0 + (4 * rg + ((l >> 3) & 3)) * 1024 + cs * 32 + (l >> 5) * 16 + (l & 7) * 2;
    asm volatile(
        "global_load_dword v109, v[108:109], off\n\t"
        "s_add_u32 s35, s68, 0\n\t"
        "s_and_b32 s35, s35, 7\n\t"
        "s_lshl_b32 s69, s35, 10\n\t"
        "s_add_u32 s35, s68, 2\n\t"
        "s_and_b32 s35, s35, 7\n\t"
        "s_lshl_b32 s70, s35, 10\n\t"
        "s_add_u32 s35, s68, 4\n\t"
        "s_and_b32 s35, s35, 7\n\t"
        "s_lshl_b32 s71, s35, 10\n\t"
        "s_add_u32 s35, s68, 6\n\t"
        "s_and_b32 s35, s35, 7\n\t"
        "s_lshl_b32 s72, s35, 10\n\t"
        "s_lshl_b32 s34, s43, 21\n\t"
        "s_add_u32 s34, s34, 0\n\t"
        "buffer_load_dwordx4 v[128:131], v96, s[16:19], s34 offen nt\n\t"
        "s_add_u32 s34, s34, 0x10000\n\t"
        "buffer_load_dwordx4 v[132:135], v96, s[16:19], s34 offen nt\n\t"
        "s_add_u32 s34, s34, 0x10000\n\t"
        "buffer_load_dwordx4 v[136:139], v96, s[16:19], s34 offen nt\n\t"
        "s_add_u32 s34, s34, 0x10000\n\t"
        "buffer_load_dwordx4 v[140:143], v96, s[16:19], s34 offen nt\n\t"
        "s_add_u32 s34, s34, 0x10000\n\t"
        "buffer_load_dwordx4 v[144:147], v96, s[16:19], s34 offen nt\n\t"
        "s_add_u32 s34, s34, 0x10000\n\t"
        "buffer_load_dwordx4 v[148:151], v96, s[16:19], s34 offen nt\n\t"
        "s_add_u32 s34, s34, 0x10000\n\t"
        "buffer_load_dwordx4 v[152:155], v96, s[16:19], s34 offen nt\n\t"
        "s_add_u32 s34, s34, 0x10000\n\t"
        "buffer_load_dwordx4 v[156:159], v96, s[16:19], s34 offen nt\n\t"
        "s_lshl_b32 s34, s43, 21\n\t"
        "s_add_u32 s34, s34, 524288\n\t"
        "buffer_load_dwordx4 v[160:163], v96, s[16:19], s34 offen nt\n\t"
        "s_add_u32 s34, s34, 0x10000\n\t"
        "buffer_load_dwordx4 v[164:167], v96, s[16:19], s34 offen nt\n\t"
        "s_add_u32 s34, s34, 0x10000\n\t"
        "buffer_load_dwordx4 v[168:171], v96, s[16:19], s34 offen nt\n\t"
        "s_add_u32 s34, s34, 0x10000\n\t"
        "buffer_load_dwordx4 v[172:175], v96, s[16:19], s34 offen nt\n\t"
        "s_add_u32 s34, s34, 0x10000\n\t"
        "buffer_load_dwordx4 v[176:179], v96, s[16:19], s34 offen nt\n\t"
        "s_add_u32 s34, s34, 0x10000\n\t"
        "buffer_load_dwordx4 v[180:183], v96, s[16:19], s34 offen nt\n\t"
        "s_add_u32 s34, s34, 0x10000\n\t"
        "buffer_load_dwordx4 v[184:187], v96, s[16:19], s34 offen nt\n\t"
        "s_add_u32 s34, s34, 0x10000\n\t"
        "buffer_load_dwordx4 v[188:191], v96, s[16:19], s34 offen nt\n\t"
        "s_lshl_b32 s34, s43, 21\n\t"
        "s_add_u32 s34, s34, 1048576\n\t"
        "buffer_load_dwordx4 v[192:195], v96, s[16:19], s34 offen nt\n\t"
        "s_add_u32 s34, s34, 0x10000\n\t"
        "buffer_load_dwordx4 v[196:199], v96, s[16:19], s34 offen nt\n\t"
        "s_add_u32 s34, s34, 0x10000\n\t"
        "buffer_load_dwordx4 v[200:203], v96, s[16:19], s34 offen nt\n\t"
        "s_add_u32 s34, s34, 0x10000\n\t"
        "buffer_load_dwordx4 v[204:207], v96, s[16:19], s34 offen nt\n\t"
        "s_add_u32 s34, s34, 0x10000\n\t"
        "buffer_load_dwordx4 v[208:211], v96, s[16:19], s34 offen nt\n\t"
        "s_add_u32 s34, s34, 0x10000\n\t"
        "buffer_load_dwordx4 v[212:215], v96, s[16:19], s34 offen nt\n\t"
        "s_add_u32 s34, s34, 0x10000\n\t"
        "buffer_load_dwordx4 v[216:219], v96, s[16:19], s34 offen nt\n\t"
        "s_add_u32 s34, s34, 0x10000\n\t"
        "buffer_load_dwordx4 v[220:223], v96, s[16:19], s34 offen nt\n\t"
        "s_waitcnt vmcnt(16)\n\t"
        "ds_write_b128 v98, v[128:131] offset:0\n\t"
        "ds_write_b128 v99, v[132:135] offset:1024\n\t"
        "ds_write_b128 v98, v[136:139] offset:2048\n\t"
        "ds_write_b128 v99, v[140:143] offset:3072\n\t"
        "ds_write_b128 v98, v[144:147] offset:4096\n\t"
        "ds_write_b128 v99, v[148:151] offset:5120\n\t"
        "ds_write_b128 v98, v[152:155] offset:6144\n\t"
        "ds_write_b128 v99, v[156:159] offset:7168\n\t"
        "ds_read_b64_tr_b16 v[128:129], v100 offset:0\n\t"
        "ds_read_b64_tr_b16 v[130:131], v100 offset:256\n\t"
        "ds_read_b64_tr_b16 v[132:133], v100 offset:4096\n\t"
        "ds_read_b64_tr_b16 v[134:135], v100 offset:4352\n\t"
        "s_waitcnt lgkmcnt(8)\n\t"
        "ds_read_b64_tr_b16 v[136:137], v101 offset:0\n\t"
        "ds_read_b64_tr_b16 v[138:139], v101 offset:256\n\t"
        "ds_read_b64_tr_b16 v[140:141], v101 offset:4096\n\t"
        "ds_read_b64_tr_b16 v[142:143], v101 offset:4352\n\t"
        "s_waitcnt lgkmcnt(8)\n\t"
        "ds_read_b64_tr_b16 v[144:145], v102 offset:0\n\t"
        "ds_read_b64_tr_b16 v[146:147], v102 offset:256\n\t"
        "ds_read_b64_tr_b16 v[148:149], v102 offset:4096\n\t"
        "ds_read_b64_tr_b16 v[150:151], v102 offset:4352\n\t"
        "s_waitcnt lgkmcnt(8)\n\t"
        "ds_read_b64_tr_b16 v[152:153], v103 offset:0\n\t"
        "ds_read_b64_tr_b16 v[154:155], v103 offset:256\n\t"
        "ds_read_b64_tr_b16 v[156:157], v103 offset:4096\n\t"
        "ds_read_b64_tr_b16 v[158:159], v103 offset:4352\n\t"
        "s_lshl_b32 s34, s43, 21\n\t"
        "s_add_u32 s34, s34, 1572864\n\t"
        "buffer_load_dwordx4 v[224:227], v96, s[16:19], s34 offen nt\n\t"
        "s_add_u32 s34, s34, 0x10000\n\t"
        "buffer_load_dwordx4 v[228:231], v96, s[16:19], s34 offen nt\n\t"
        "s_add_u32 s34, s34, 0x10000\n\t"
        "buffer_load_dwordx4 v[232:235], v96, s[16:19], s34 offen nt\n\t"
        "s_add_u32 s34, s34, 0x10000\n\t"
        "buffer_load_dwordx4 v[236:239], v96, s[16:19], s34 offen nt\n\t"
        "s_add_u32 s34, s34, 0x10000\n\t"
        "buffer_load_dwordx4 v[240:243], v96, s[16:19], s34 offen nt\n\t"
        "s_add_u32 s34, s34, 0x10000\n\t"
        "buffer_load_dwordx4 v[244:247], v96, s[16:19], s34 offen nt\n\t"
        "s_add_u32 s34, s34, 0x10000\n\t"
        "buffer_load_dwordx4 v[248:251], v96, s[16:19], s34 offen nt\n\t"
        "s_add_u32 s34, s34, 0x10000\n\t"
        "buffer_load_dwordx4 v[252:255], v96, s[16:19], s34 offen nt\n\t"
        "s_waitcnt lgkmcnt(0)\n\t"
        "s_waitcnt vmcnt(16)\n\t"
        "ds_write_b128 v98, v[160:163] offset:0\n\t"
        "ds_write_b128 v99, v[164:167] offset:1024\n\t"
        "ds_write_b128 v98, v[168:171] offset:2048\n\t"
        "ds_write_b128 v99, v[172:175] offset:3072\n\t"
        "ds_write_b128 v98, v[176:179] offset:4096\n\t"
        "ds_write_b128 v99, v[180:183] offset:5120\n\t"
        "ds_write_b128 v98, v[184:187] offset:6144\n\t"
        "ds_write_b128 v99, v[188:191] offset:7168\n\t"
        "ds_read_b64_tr_b16 v[160:161], v100 offset:0\n\t"
        "ds_read_b64_tr_b16 v[162:163], v100 offset:256\n\t"
        "ds_read_b64_tr_b16 v[164:165], v100 offset:4096\n\t"
        "ds_read_b64_tr_b16 v[166:167], v100 offset:4352\n\t"
        "s_waitcnt lgkmcnt(8)\n\t"
        "ds_read_b64_tr_b16 v[168:169], v101 offset:0\n\t"
        "ds_read_b64_tr_b16 v[170:171], v101 offset:256\n\t"
        "ds_read_b64_tr_b16 v[172:173], v101 offset:4096\n\t"
        "ds_read_b64_tr_b16 v[174:175], v101 offset:4352\n\t"
        "s_waitcnt lgkmcnt(8)\n\t"
        "ds_read_b64_tr_b16 v[176:177], v102 offset:0\n\t"
        "ds_read_b64_tr_b16 v[178:179], v102 offset:256\n\t"
        "ds_read_b64_tr_b16 v[180:181], v102 offset:4096\n\t"
        "ds_read_b64_tr_b16 v[182:183], v102 offset:4352\n\t"
        "s_waitcnt lgkmcnt(8)\n\t"
        "ds_read_b64_tr_b16 v[184:185], v103 offset:0\n\t"
        "ds_read_b64_tr_b16 v[186:187], v103 offset:256\n\t"
        "ds_read_b64_tr_b16 v[188:189], v103 offset:4096\n\t"
        "ds_read_b64_tr_b16 v[190:191], v103 offset:4352\n\t"
        "s_add_u32 s35, s68, 0\n\t"
        "s_and_b32 s35, s35, 7\n\t"
        "s_lshl_b32 s35, s35, 18\n\t"
        "s_lshl_b32 s34, s43, 21\n\t"
        "s_add_u32 s34, s34, s35\n\t"
        "buffer_load_dwordx4 a[0:3], v97, s[20:23], s34 offen\n\t"
        "s_add_u32 s34, s34, 0x8000\n\t"
        "buffer_load_dwordx4 a[4:7], v97, s[20:23], s34 offen\n\t"
        "s_add_u32 s34, s34, 0x8000\n\t"
        "buffer_load_dwordx4 a[8:11], v97, s[20:23], s34 offen\n\t"
        "s_add_u32 s34, s34, 0x8000\n\t"
        "buffer_load_dwordx4 a[12:15], v97, s[20:23], s34 offen\n\t"
        "s_add_u32 s34, s34, 0x8000\n\t"
        "buffer_load_dwordx4 a[16:19], v97, s[20:23], s34 offen\n\t"
        "s_add_u32 s34, s34, 0x8000\n\t"
        "buffer_load_dwordx4 a[20:23], v97, s[20:23], s34 offen\n\t"
        "s_add_u32 s34, s34, 0x8000\n\t"
        "buffer_load_dwordx4 a[24:27], v97, s[20:23], s34 offen\n\t"
        "s_add_u32 s34, s34, 0x8000\n\t"
        "buffer_load_dwordx4 a[28:31], v97, s[20:23], s34 offen\n\t"
        "s_waitcnt lgkmcnt(0)\n\t"
        "s_waitcnt vmcnt(16)\n\t"
        "ds_write_b128 v98, v[192:195] offset:0\n\t"
        "ds_write_b128 v99, v[196:199] offset:1024\n\t"
        "ds_write_b128 v98, v[200:203] offset:2048\n\t"
        "ds_write_b128 v99, v[204:207] offset:3072\n\t"
        "ds_write_b128 v98, v[208:211] offset:4096\n\t"
        "ds_write_b128 v99, v[212:215] offset:5120\n\t"
        "ds_write_b128 v98, v[216:219] offset:6144\n\t"
        "ds_write_b128 v99, v[220:223] offset:7168\n\t"
        "ds_read_b64_tr_b16 v[192:193], v100 offset:0\n\t"
        "ds_read_b64_tr_b16 v[194:195], v100 offset:256\n\t"
        "ds_read_b64_tr_b16 v[196:197], v100 offset:4096\n\t"
        "ds_read_b64_tr_b16 v[198:199], v100 offset:4352\n\t"
        "s_waitcnt lgkmcnt(8)\n\t"
        "ds_read_b64_tr_b16 v[200:201], v101 offset:0\n\t"
        "ds_read_b64_tr_b16 v[202:203], v101 offset:256\n\t"
        "ds_read_b64_tr_b16 v[204:205], v101 offset:4096\n\t"
        "ds_read_b64_tr_b16 v[206:207], v101 offset:4352\n\t"
        "s_waitcnt lgkmcnt(8)\n\t"
        "ds_read_b64_tr_b16 v[208:209], v102 offset:0\n\t"
        "ds_read_b64_tr_b16 v[210:211], v102 offset:256\n\t"
        "ds_read_b64_tr_b16 v[212:213], v102 offset:4096\n\t"
        "ds_read_b64_tr_b16 v[214:215], v102 offset:4352\n\t"
        "s_waitcnt lgkmcnt(8)\n\t"
        "ds_read_b64_tr_b16 v[216:217], v103 offset:0\n\t"
        "ds_read_b64_tr_b16 v[218:219], v103 offset:256\n\t"
        "ds_read_b64_tr_b16 v[220:221], v103 offset:4096\n\t"
        "ds_read_b64_tr_b16 v[222:223], v103 offset:4352\n\t"
        "s_add_u32 s35, s68, 1\n\t"
        "s_and_b32 s35, s35, 7\n\t"
        "s_lshl_b32 s35, s35, 18\n\t"
        "s_lshl_b32 s34, s43, 21\n\t"
        "s_add_u32 s34, s34, s35\n\t"
        "buffer_load_dwordx4 a[32:35], v97, s[20:23], s34 offen\n\t"
        "s_add_u32 s34, s34, 0x8000\n\t"
        "buffer_load_dwordx4 a[36:39], v97, s[20:23], s34 offen\n\t"
        "s_add_u32 s34, s34, 0x8000\n\t"
        "buffer_load_dwordx4 a[40:43], v97, s[20:23], s34 offen\n\t"
        "s_add_u32 s34, s34, 0x8000\n\t"
        "buffer_load_dwordx4 a[44:47], v97, s[20:23], s34 offen\n\t"
        "s_add_u32 s34, s34, 0x8000\n\t"
        "buffer_load_dwordx4 a[48:51], v97, s[20:23], s34 offen\n\t"
        "s_add_u32 s34, s34, 0x8000\n\t"
        "buffer_load_dwordx4 a[52:55], v97, s[20:23], s34 offen\n\t"
        "s_add_u32 s34, s34, 0x8000\n\t"
        "buffer_load_dwordx4 a[56:59], v97, s[20:23], s34 offen\n\t"
        "s_add_u32 s34, s34, 0x8000\n\t"
        "buffer_load_dwordx4 a[60:63], v97, s[20:23], s34 offen\n\t"
        "s_waitcnt lgkmcnt(0)\n\t"
        "s_waitcnt vmcnt(16)\n\t"
        "ds_write_b128 v98, v[224:227] offset:0\n\t"
        "ds_write_b128 v99, v[228:231] offset:1024\n\t"
        "ds_write_b128 v98, v[232:235] offset:2048\n\t"
        "ds_write_b128 v99, v[236:239] offset:3072\n\t"
        "ds_write_b128 v98, v[240:243] offset:4096\n\t"
        "ds_write_b128 v99, v[244:247] offset:5120\n\t"
        "ds_write_b128 v98, v[248:251] offset:6144\n\t"
        "ds_write_b128 v99, v[252:255] offset:7168\n\t"
        "ds_read_b64_tr_b16 v[224:225], v100 offset:0\n\t"
        "ds_read_b64_tr_b16 v[226:227], v100 offset:256\n\t"
        "ds_read_b64_tr_b16 v[228:229], v100 offset:4096\n\t"
        "ds_read_b64_tr_b16 v[230:231], v100 offset:4352\n\t"
        "s_waitcnt lgkmcnt(8)\n\t"
        "ds_read_b64_tr_b16 v[232:233], v101 offset:0\n\t"
        "ds_read_b64_tr_b16 v[234:235], v101 offset:256\n\t"
        "ds_read_b64_tr_b16 v[236:237], v101 offset:4096\n\t"
        "ds_read_b64_tr_b16 v[238:239], v101 offset:4352\n\t"
        "s_waitcnt lgkmcnt(8)\n\t"
        "ds_read_b64_tr_b16 v[240:241], v102 offset:0\n\t"
        "ds_read_b64_tr_b16 v[242:243], v102 offset:256\n\t"
        "ds_read_b64_tr_b16 v[244:245], v102 offset:4096\n\t"
        "ds_read_b64_tr_b16 v[246:247], v102 offset:4352\n\t"
        "s_waitcnt lgkmcnt(8)\n\t"
        "ds_read_b64_tr_b16 v[248:249], v103 offset:0\n\t"
        "ds_read_b64_tr_b16 v[250:251], v103 offset:256\n\t"
        "ds_read_b64_tr_b16 v[252:253], v103 offset:4096\n\t"
        "ds_read_b64_tr_b16 v[254:255], v103 offset:4352\n\t"
        "s_add_u32 s35, s68, 2\n\t"
        "s_and_b32 s35, s35, 7\n\t"
        "s_lshl_b32 s35, s35, 18\n\t"
        "s_lshl_b32 s34, s43, 21\n\t"
        "s_add_u32 s34, s34, s35\n\t"
        "buffer_load_dwordx4 a[64:67], v97, s[20:23], s34 offen\n\t"
        "s_add_u32 s34, s34, 0x8000\n\t"
        "buffer_load_dwordx4 a[68:71], v97, s[20:23], s34 offen\n\t"
        "s_add_u32 s34, s34, 0x8000\n\t"
        "buffer_load_dwordx4 a[72:75], v97, s[20:23], s34 offen\n\t"
        "s_add_u32 s34, s34, 0x8000\n\t"
        "buffer_load_dwordx4 a[76:79], v97, s[20:23], s34 offen\n\t"
        "s_add_u32 s34, s34, 0x8000\n\t"
        "buffer_load_dwordx4 a[80:83], v97, s[20:23], s34 offen\n\t"
        "s_add_u32 s34, s34, 0x8000\n\t"
        "buffer_load_dwordx4 a[84:87], v97, s[20:23], s34 offen\n\t"
        "s_add_u32 s34, s34, 0x8000\n\t"
        "buffer_load_dwordx4 a[88:91], v97, s[20:23], s34 offen\n\t"
        "s_add_u32 s34, s34, 0x8000\n\t"
        "buffer_load_dwordx4 a[92:95], v97, s[20:23], s34 offen\n\t"
        "s_waitcnt lgkmcnt(0)\n\t"
        "s_waitcnt vmcnt(16)\n\t"
        "ds_write_b128 v122, a[0:3] offset:0\n\t"
        "ds_write_b128 v122, a[4:7] offset:1056\n\t"
        "ds_write_b128 v122, a[8:11] offset:2176\n\t"
        "ds_write_b128 v122, a[12:15] offset:3232\n\t"
        "ds_write_b128 v122, a[16:19] offset:4352\n\t"
        "ds_write_b128 v122, a[20:23] offset:5408\n\t"
        "ds_write_b128 v122, a[24:27] offset:6528\n\t"
        "ds_write_b128 v122, a[28:31] offset:7584\n\t"
        "ds_read_b64_tr_b16 a[0:1], v123 offset:0\n\t"
        "ds_read_b64_tr_b16 a[2:3], v123 offset:128\n\t"
        "ds_read_b64_tr_b16 a[4:5], v123 offset:4352\n\t"
        "ds_read_b64_tr_b16 a[6:7], v123 offset:4480\n\t"
        "s_waitcnt lgkmcnt(8)\n\t"
        "ds_read_b64_tr_b16 a[8:9], v123 offset:32\n\t"
        "ds_read_b64_tr_b16 a[10:11], v123 offset:160\n\t"
        "ds_read_b64_tr_b16 a[12:13], v123 offset:4384\n\t"
        "ds_read_b64_tr_b16 a[14:15], v123 offset:4512\n\t"
        "s_waitcnt lgkmcnt(8)\n\t"
        "ds_read_b64_tr_b16 a[16:17], v123 offset:512\n\t"
        "ds_read_b64_tr_b16 a[18:19], v123 offset:640\n\t"
        "ds_read_b64_tr_b16 a[20:21], v123 offset:4864\n\t"
        "ds_read_b64_tr_b16 a[22:23], v123 offset:4992\n\t"
        "s_waitcnt lgkmcnt(8)\n\t"
        "ds_read_b64_tr_b16 a[24:25], v123 offset:544\n\t"
        "ds_read_b64_tr_b16 a[26:27], v123 offset:672\n\t"
        "ds_read_b64_tr_b16 a[28:29], v123 offset:4896\n\t"
        "ds_read_b64_tr_b16 a[30:31], v123 offset:5024\n\t"
        "s_waitcnt lgkmcnt(8)\n\t"
        "s_add_u32 s35, s68, 3\n\t"
        "s_and_b32 s35, s35, 7\n\t"
        "s_lshl_b32 s35, s35, 18\n\t"
        "s_lshl_b32 s34, s43, 21\n\t"
        "s_add_u32 s34, s34, s35\n\t"
        "buffer_load_dwordx4 a[96:99], v97, s[20:23], s34 offen\n\t"
        "s_add_u32 s34, s34, 0x8000\n\t"
        "buffer_load_dwordx4 a[100:103], v97, s[20:23], s34 offen\n\t"
        "s_add_u32 s34, s34, 0x8000\n\t"
        "buffer_load_dwordx4 a[104:107], v97, s[20:23], s34 offen\n\t"
        "s_add_u32 s34, s34, 0x8000\n\t"
        "buffer_load_dwordx4 a[108:111], v97, s[20:23], s34 offen\n\t"
        "s_add_u32 s34, s34, 0x8000\n\t"
        "buffer_load_dwordx4 a[112:115], v97, s[20:23], s34 offen\n\t"
        "s_add_u32 s34, s34, 0x8000\n\t"
        "buffer_load_dwordx4 a[116:119], v97, s[20:23], s34 offen\n\t"
        "s_add_u32 s34, s34, 0x8000\n\t"
        "buffer_load_dwordx4 a[120:123], v97, s[20:23], s34 offen\n\t"
        "s_add_u32 s34, s34, 0x8000\n\t"
        "buffer_load_dwordx4 a[124:127], v97, s[20:23], s34 offen\n\t"
        "s_waitcnt lgkmcnt(0)\n\t"
        "s_waitcnt vmcnt(16)\n\t"
        "ds_write_b128 v122, a[32:35] offset:0\n\t"
        "ds_write_b128 v122, a[36:39] offset:1056\n\t"
        "ds_write_b128 v122, a[40:43] offset:2176\n\t"
        "ds_write_b128 v122, a[44:47] offset:3232\n\t"
        "ds_write_b128 v122, a[48:51] offset:4352\n\t"
        "ds_write_b128 v122, a[52:55] offset:5408\n\t"
        "ds_write_b128 v122, a[56:59] offset:6528\n\t"
        "ds_write_b128 v122, a[60:63] offset:7584\n\t"
        "ds_read_b64_tr_b16 a[32:33], v123 offset:0\n\t"
        "ds_read_b64_tr_b16 a[34:35], v123 offset:128\n\t"
        "ds_read_b64_tr_b16 a[36:37], v123 offset:4352\n\t"
        "ds_read_b64_tr_b16 a[38:39], v123 offset:4480\n\t"
        "s_waitcnt lgkmcnt(8)\n\t"
        "ds_read_b64_tr_b16 a[40:41], v123 offset:32\n\t"
        "ds_read_b64_tr_b16 a[42:43], v123 offset:160\n\t"
        "ds_read_b64_tr_b16 a[44:45], v123 offset:4384\n\t"
        "ds_read_b64_tr_b16 a[46:47], v123 offset:4512\n\t"
        "s_waitcnt lgkmcnt(8)\n\t"
        "ds_read_b64_tr_b16 a[48:49], v123 offset:512\n\t"
        "ds_read_b64_tr_b16 a[50:51], v123 offset:640\n\t"
        "ds_read_b64_tr_b16 a[52:53], v123 offset:4864\n\t"
        "ds_read_b64_tr_b16 a[54:55], v123 offset:4992\n\t"
        "s_waitcnt lgkmcnt(8)\n\t"
        "ds_read_b64_tr_b16 a[56:57], v123 offset:544\n\t"
        "ds_read_b64_tr_b16 a[58:59], v123 offset:672\n\t"
        "ds_read_b64_tr_b16 a[60:61], v123 offset:4896\n\t"
        "ds_read_b64_tr_b16 a[62:63], v123 offset:5024\n\t"
        "s_waitcnt lgkmcnt(8)\n\t"
        "s_add_u32 s35, s68, 4\n\t"
        "s_and_b32 s35, s35, 7\n\t"
        "s_lshl_b32 s35, s35, 18\n\t"
        "s_lshl_b32 s34, s43, 21\n\t"
        "s_add_u32 s34, s34, s35\n\t"
        "buffer_load_dwordx4 a[128:131], v97, s[20:23], s34 offen\n\t"
        "s_add_u32 s34, s34, 0x8000\n\t"
        "buffer_load_dwordx4 a[132:135], v97, s[20:23], s34 offen\n\t"
        "s_add_u32 s34, s34, 0x8000\n\t"
        "buffer_load_dwordx4 a[136:139], v97, s[20:23], s34 offen\n\t"
        "s_add_u32 s34, s34, 0x8000\n\t"
        "buffer_load_dwordx4 a[140:143], v97, s[20:23], s34 offen\n\t"
        "s_add_u32 s34, s34, 0x8000\n\t"
        "buffer_load_dwordx4 a[144:147], v97, s[20:23], s34 offen\n\t"
        "s_add_u32 s34, s34, 0x8000\n\t"
        "buffer_load_dwordx4 a[148:151], v97, s[20:23], s34 offen\n\t"
        "s_add_u32 s34, s34, 0x8000\n\t"
        "buffer_load_dwordx4 a[152:155], v97, s[20:23], s34 offen\n\t"
        "s_add_u32 s34, s34, 0x8000\n\t"
        "buffer_load_dwordx4 a[156:159], v97, s[20:23], s34 offen\n\t"
        "s_waitcnt lgkmcnt(0)\n\t"
        "s_waitcnt vmcnt(16)\n\t"
        "ds_write_b128 v122, a[64:67] offset:0\n\t"
        "ds_write_b128 v122, a[68:71] offset:1056\n\t"
        "ds_write_b128 v122, a[72:75] offset:2176\n\t"
        "ds_write_b128 v122, a[76:79] offset:3232\n\t"
        "ds_write_b128 v122, a[80:83] offset:4352\n\t"
        "ds_write_b128 v122, a[84:87] offset:5408\n\t"
        "ds_write_b128 v122, a[88:91] offset:6528\n\t"
        "ds_write_b128 v122, a[92:95] offset:7584\n\t"
        "ds_read_b64_tr_b16 a[64:65], v123 offset:0\n\t"
        "ds_read_b64_tr_b16 a[66:67], v123 offset:128\n\t"
        "ds_read_b64_tr_b16 a[68:69], v123 offset:4352\n\t"
        "ds_read_b64_tr_b16 a[70:71], v123 offset:4480\n\t"
        "s_waitcnt lgkmcnt(8)\n\t"
        "ds_read_b64_tr_b16 a[72:73], v123 offset:32\n\t"
        "ds_read_b64_tr_b16 a[74:75], v123 offset:160\n\t"
        "ds_read_b64_tr_b16 a[76:77], v123 offset:4384\n\t"
        "ds_read_b64_tr_b16 a[78:79], v123 offset:4512\n\t"
        "s_waitcnt lgkmcnt(8)\n\t"
        "ds_read_b64_tr_b16 a[80:81], v123 offset:512\n\t"
        "ds_read_b64_tr_b16 a[82:83], v123 offset:640\n\t"
        "ds_read_b64_tr_b16 a[84:85], v123 offset:4864\n\t"
        "ds_read_b64_tr_b16 a[86:87], v123 offset:4992\n\t"
        "s_waitcnt lgkmcnt(8)\n\t"
        "ds_read_b64_tr_b16 a[88:89], v123 offset:544\n\t"
        "ds_read_b64_tr_b16 a[90:91], v123 offset:672\n\t"
        "ds_read_b64_tr_b16 a[92:93], v123 offset:4896\n\t"
        "ds_read_b64_tr_b16 a[94:95], v123 offset:5024\n\t"
        "s_waitcnt lgkmcnt(8)\n\t"
        "s_add_u32 s35, s68, 5\n\t"
        "s_and_b32 s35, s35, 7\n\t"
        "s_lshl_b32 s35, s35, 18\n\t"
        "s_lshl_b32 s34, s43, 21\n\t"
        "s_add_u32 s34, s34, s35\n\t"
        "buffer_load_dwordx4 a[160:163], v97, s[20:23], s34 offen\n\t"
        "s_add_u32 s34, s34, 0x8000\n\t"
        "buffer_load_dwordx4 a[164:167], v97, s[20:23], s34 offen\n\t"
        "s_add_u32 s34, s34, 0x8000\n\t"
        "buffer_load_dwordx4 a[168:171], v97, s[20:23], s34 offen\n\t"
        "s_add_u32 s34, s34, 0x8000\n\t"
        "buffer_load_dwordx4 a[172:175], v97, s[20:23], s34 offen\n\t"
        "s_add_u32 s34, s34, 0x8000\n\t"
        "buffer_load_dwordx4 a[176:179], v97, s[20:23], s34 offen\n\t"
        "s_add_u32 s34, s34, 0x8000\n\t"
        "buffer_load_dwordx4 a[180:183], v97, s[20:23], s34 offen\n\t"
        "s_add_u32 s34, s34, 0x8000\n\t"
        "buffer_load_dwordx4 a[184:187], v97, s[20:23], s34 offen\n\t"
        "s_add_u32 s34, s34, 0x8000\n\t"
        "buffer_load_dwordx4 a[188:191], v97, s[20:23], s34 offen\n\t"
        "s_waitcnt lgkmcnt(0)\n\t"
        "s_waitcnt vmcnt(16)\n\t"
        "ds_write_b128 v122, a[96:99] offset:0\n\t"
        "ds_write_b128 v122, a[100:103] offset:1056\n\t"
        "ds_write_b128 v122, a[104:107] offset:2176\n\t"
        "ds_write_b128 v122, a[108:111] offset:3232\n\t"
        "ds_write_b128 v122, a[112:115] offset:4352\n\t"
        "ds_write_b128 v122, a[116:119] offset:5408\n\t"
        "ds_write_b128 v122, a[120:123] offset:6528\n\t"
        "ds_write_b128 v122, a[124:127] offset:7584\n\t"
        "ds_read_b64_tr_b16 a[96:97], v123 offset:0\n\t"
        "ds_read_b64_tr_b16 a[98:99], v123 offset:128\n\t"
        "ds_read_b64_tr_b16 a[100:101], v123 offset:4352\n\t"
        "ds_read_b64_tr_b16 a[102:103], v123 offset:4480\n\t"
        "s_waitcnt lgkmcnt(8)\n\t"
        "ds_read_b64_tr_b16 a[104:105], v123 offset:32\n\t"
        "ds_read_b64_tr_b16 a[106:107], v123 offset:160\n\t"
        "ds_read_b64_tr_b16 a[108:109], v123 offset:4384\n\t"
        "ds_read_b64_tr_b16 a[110:111], v123 offset:4512\n\t"
        "s_waitcnt lgkmcnt(8)\n\t"
        "ds_read_b64_tr_b16 a[112:113], v123 offset:512\n\t"
        "ds_read_b64_tr_b16 a[114:115], v123 offset:640\n\t"
        "ds_read_b64_tr_b16 a[116:117], v123 offset:4864\n\t"
        "ds_read_b64_tr_b16 a[118:119], v123 offset:4992\n\t"
        "s_waitcnt lgkmcnt(8)\n\t"
        "ds_read_b64_tr_b16 a[120:121], v123 offset:544\n\t"
        "ds_read_b64_tr_b16 a[122:123], v123 offset:672\n\t"
        "ds_read_b64_tr_b16 a[124:125], v123 offset:4896\n\t"
        "ds_read_b64_tr_b16 a[126:127], v123 offset:5024\n\t"
        "s_waitcnt lgkmcnt(8)\n\t"
        "s_add_u32 s35, s68, 6\n\t"
        "s_and_b32 s35, s35, 7\n\t"
        "s_lshl_b32 s35, s35, 18\n\t"
        "s_lshl_b32 s34, s43, 21\n\t"
        "s_add_u32 s34, s34, s35\n\t"
        "buffer_load_dwordx4 a[192:195], v97, s[20:23], s34 offen\n\t"
        "s_add_u32 s34, s34, 0x8000\n\t"
        "buffer_load_dwordx4 a[196:199], v97, s[20:23], s34 offen\n\t"
        "s_add_u32 s34, s34, 0x8000\n\t"
        "buffer_load_dwordx4 a[200:203], v97, s[20:23], s34 offen\n\t"
        "s_add_u32 s34, s34, 0x8000\n\t"
        "buffer_load_dwordx4 a[204:207], v97, s[20:23], s34 offen\n\t"
        "s_add_u32 s34, s34, 0x8000\n\t"
        "buffer_load_dwordx4 a[208:211], v97, s[20:23], s34 offen\n\t"
        "s_add_u32 s34, s34, 0x8000\n\t"
        "buffer_load_dwordx4 a[212:215], v97, s[20:23], s34 offen\n\t"
        "s_add_u32 s34, s34, 0x8000\n\t"
        "buffer_load_dwordx4 a[216:219], v97, s[20:23], s34 offen\n\t"
        "s_add_u32 s34, s34, 0x8000\n\t"
        "buffer_load_dwordx4 a[220:223], v97, s[20:23], s34 offen\n\t"
        "s_waitcnt lgkmcnt(0)\n\t"
        "s_waitcnt vmcnt(16)\n\t"
        "ds_write_b128 v122, a[128:131] offset:0\n\t"
        "ds_write_b128 v122, a[132:135] offset:1056\n\t"
        "ds_write_b128 v122, a[136:139] offset:2176\n\t"
        "ds_write_b128 v122, a[140:143] offset:3232\n\t"
        "ds_write_b128 v122, a[144:147] offset:4352\n\t"
        "ds_write_b128 v122, a[148:151] offset:5408\n\t"
        "ds_write_b128 v122, a[152:155] offset:6528\n\t"
        "ds_write_b128 v122, a[156:159] offset:7584\n\t"
        "ds_read_b64_tr_b16 a[128:129], v123 offset:0\n\t"
        "ds_read_b64_tr_b16 a[130:131], v123 offset:128\n\t"
        "ds_read_b64_tr_b16 a[132:133], v123 offset:4352\n\t"
        "ds_read_b64_tr_b16 a[134:135], v123 offset:4480\n\t"
        "s_waitcnt lgkmcnt(8)\n\t"
        "ds_read_b64_tr_b16 a[136:137], v123 offset:32\n\t"
        "ds_read_b64_tr_b16 a[138:139], v123 offset:160\n\t"
        "ds_read_b64_tr_b16 a[140:141], v123 offset:4384\n\t"
        "ds_read_b64_tr_b16 a[142:143], v123 offset:4512\n\t"
        "s_waitcnt lgkmcnt(8)\n\t"
        "ds_read_b64_tr_b16 a[144:145], v123 offset:512\n\t"
        "ds_read_b64_tr_b16 a[146:147], v123 offset:640\n\t"
        "ds_read_b64_tr_b16 a[148:149], v123 offset:4864\n\t"
        "ds_read_b64_tr_b16 a[150:151], v123 offset:4992\n\t"
        "s_waitcnt lgkmcnt(8)\n\t"
        "ds_read_b64_tr_b16 a[152:153], v123 offset:544\n\t"
        "ds_read_b64_tr_b16 a[154:155], v123 offset:672\n\t"
        "ds_read_b64_tr_b16 a[156:157], v123 offset:4896\n\t"
        "ds_read_b64_tr_b16 a[158:159], v123 offset:5024\n\t"
        "s_waitcnt lgkmcnt(8)\n\t"
        "s_add_u32 s35, s68, 7\n\t"
        "s_and_b32 s35, s35, 7\n\t"
        "s_lshl_b32 s35, s35, 18\n\t"
        "s_lshl_b32 s34, s43, 21\n\t"
        "s_add_u32 s34, s34, s35\n\t"
        "buffer_load_dwordx4 a[224:227], v97, s[20:23], s34 offen\n\t"
        "s_add_u32 s34, s34, 0x8000\n\t"
        "buffer_load_dwordx4 a[228:231], v97, s[20:23], s34 offen\n\t"
        "s_add_u32 s34, s34, 0x8000\n\t"
        "buffer_load_dwordx4 a[232:235], v97, s[20:23], s34 offen\n\t"
        "s_add_u32 s34, s34, 0x8000\n\t"
        "buffer_load_dwordx4 a[236:239], v97, s[20:23], s34 offen\n\t"
        "s_add_u32 s34, s34, 0x8000\n\t"
        "buffer_load_dwordx4 a[240:243], v97, s[20:23], s34 offen\n\t"
        "s_add_u32 s34, s34, 0x8000\n\t"
        "buffer_load_dwordx4 a[244:247], v97, s[20:23], s34 offen\n\t"
        "s_add_u32 s34, s34, 0x8000\n\t"
        "buffer_load_dwordx4 a[248:251], v97, s[20:23], s34 offen\n\t"
        "s_add_u32 s34, s34, 0x8000\n\t"
        "buffer_load_dwordx4 a[252:255], v97, s[20:23], s34 offen\n\t"
        "s_waitcnt lgkmcnt(0)\n\t"
        "s_waitcnt vmcnt(16)\n\t"
        "ds_write_b128 v122, a[160:163] offset:0\n\t"
        "ds_write_b128 v122, a[164:167] offset:1056\n\t"
        "ds_write_b128 v122, a[168:171] offset:2176\n\t"
        "ds_write_b128 v122, a[172:175] offset:3232\n\t"
        "ds_write_b128 v122, a[176:179] offset:4352\n\t"
        "ds_write_b128 v122, a[180:183] offset:5408\n\t"
        "ds_write_b128 v122, a[184:187] offset:6528\n\t"
        "ds_write_b128 v122, a[188:191] offset:7584\n\t"
        "ds_read_b64_tr_b16 a[160:161], v123 offset:0\n\t"
        "ds_read_b64_tr_b16 a[162:163], v123 offset:128\n\t"
        "ds_read_b64_tr_b16 a[164:165], v123 offset:4352\n\t"
        "ds_read_b64_tr_b16 a[166:167], v123 offset:4480\n\t"
        "s_waitcnt lgkmcnt(8)\n\t"
        "ds_read_b64_tr_b16 a[168:169], v123 offset:32\n\t"
        "ds_read_b64_tr_b16 a[170:171], v123 offset:160\n\t"
        "ds_read_b64_tr_b16 a[172:173], v123 offset:4384\n\t"
        "ds_read_b64_tr_b16 a[174:175], v123 offset:4512\n\t"
        "s_waitcnt lgkmcnt(8)\n\t"
        "ds_read_b64_tr_b16 a[176:177], v123 offset:512\n\t"
        "ds_read_b64_tr_b16 a[178:179], v123 offset:640\n\t"
        "ds_read_b64_tr_b16 a[180:181], v123 offset:4864\n\t"
        "ds_read_b64_tr_b16 a[182:183], v123 offset:4992\n\t"
        "s_waitcnt lgkmcnt(8)\n\t"
        "ds_read_b64_tr_b16 a[184:185], v123 offset:544\n\t"
        "ds_read_b64_tr_b16 a[186:187], v123 offset:672\n\t"
        "ds_read_b64_tr_b16 a[188:189], v123 offset:4896\n\t"
        "ds_read_b64_tr_b16 a[190:191], v123 offset:5024\n\t"
        "s_waitcnt lgkmcnt(8)\n\t"
        "s_waitcnt lgkmcnt(0)\n\t"
        "s_waitcnt vmcnt(8)\n\t"
        "ds_write_b128 v122, a[192:195] offset:0\n\t"
        "ds_write_b128 v122, a[196:199] offset:1056\n\t"
        "ds_write_b128 v122, a[200:203] offset:2176\n\t"
        "ds_write_b128 v122, a[204:207] offset:3232\n\t"
        "ds_write_b128 v122, a[208:211] offset:4352\n\t"
        "ds_write_b128 v122, a[212:215] offset:5408\n\t"
        "ds_write_b128 v122, a[216:219] offset:6528\n\t"
        "ds_write_b128 v122, a[220:223] offset:7584\n\t"
        "ds_read_b64_tr_b16 a[192:193], v123 offset:0\n\t"
        "ds_read_b64_tr_b16 a[194:195], v123 offset:128\n\t"
        "ds_read_b64_tr_b16 a[196:197], v123 offset:4352\n\t"
        "ds_read_b64_tr_b16 a[198:199], v123 offset:4480\n\t"
        "s_waitcnt lgkmcnt(8)\n\t"
        "ds_read_b64_tr_b16 a[200:201], v123 offset:32\n\t"
        "ds_read_b64_tr_b16 a[202:203], v123 offset:160\n\t"
        "ds_read_b64_tr_b16 a[204:205], v123 offset:4384\n\t"
        "ds_read_b64_tr_b16 a[206:207], v123 offset:4512\n\t"
        "s_waitcnt lgkmcnt(8)\n\t"
        "ds_read_b64_tr_b16 a[208:209], v123 offset:512\n\t"
        "ds_read_b64_tr_b16 a[210:211], v123 offset:640\n\t"
        "ds_read_b64_tr_b16 a[212:213], v123 offset:4864\n\t"
        "ds_read_b64_tr_b16 a[214:215], v123 offset:4992\n\t"
        "s_waitcnt lgkmcnt(8)\n\t"
        "ds_read_b64_tr_b16 a[216:217], v123 offset:544\n\t"
        "ds_read_b64_tr_b16 a[218:219], v123 offset:672\n\t"
        "ds_read_b64_tr_b16 a[220:221], v123 offset:4896\n\t"
        "ds_read_b64_tr_b16 a[222:223], v123 offset:5024\n\t"
        "s_waitcnt lgkmcnt(8)\n\t"
        "s_waitcnt lgkmcnt(0)\n\t"
        "s_waitcnt vmcnt(0)\n\t"
        "ds_write_b128 v122, a[224:227] offset:0\n\t"
        "ds_write_b128 v122, a[228:231] offset:1056\n\t"
        "ds_write_b128 v122, a[232:235] offset:2176\n\t"
        "ds_write_b128 v122, a[236:239] offset:3232\n\t"
        "ds_write_b128 v122, a[240:243] offset:4352\n\t"
        "ds_write_b128 v122, a[244:247] offset:5408\n\t"
        "ds_write_b128 v122, a[248:251] offset:6528\n\t"
        "ds_write_b128 v122, a[252:255] offset:7584\n\t"
        "ds_read_b64_tr_b16 a[224:225], v123 offset:0\n\t"
        "ds_read_b64_tr_b16 a[226:227], v123 offset:128\n\t"
        "ds_read_b64_tr_b16 a[228:229], v123 offset:4352\n\t"
        "ds_read_b64_tr_b16 a[230:231], v123 offset:4480\n\t"
        "s_waitcnt lgkmcnt(8)\n\t"
        "ds_read_b64_tr_b16 a[232:233], v123 offset:32\n\t"
        "ds_read_b64_tr_b16 a[234:235], v123 offset:160\n\t"
        "ds_read_b64_tr_b16 a[236:237], v123 offset:4384\n\t"
        "ds_read_b64_tr_b16 a[238:239], v123 offset:4512\n\t"
        "s_waitcnt lgkmcnt(8)\n\t"
        "ds_read_b64_tr_b16 a[240:241], v123 offset:512\n\t"
        "ds_read_b64_tr_b16 a[242:243], v123 offset:640\n\t"
        "ds_read_b64_tr_b16 a[244:245], v123 offset:4864\n\t"
        "ds_read_b64_tr_b16 a[246:247], v123 offset:4992\n\t"
        "s_waitcnt lgkmcnt(8)\n\t"
        "ds_read_b64_tr_b16 a[248:249], v123 offset:544\n\t"
        "ds_read_b64_tr_b16 a[250:251], v123 offset:672\n\t"
        "ds_read_b64_tr_b16 a[252:253], v123 offset:4896\n\t"
        "ds_read_b64_tr_b16 a[254:255], v123 offset:5024\n\t"
        "s_waitcnt lgkmcnt(8)\n\t"
        "s_mov_b32 s35, 33792\n\t"
        "buffer_load_dword v0, off, s[12:15], s35 sc1\n\t"
        "s_waitcnt lgkmcnt(0)\n\t"
        "s_waitcnt vmcnt(0)\n\t"
        "s_mov_b32 s42, 0\n\t"
        "LB_cal%=:\n\t"
        "v_readfirstlane_b32 s34, v0\n\t"
        "s_nop 0\n\t"
        "s_cmp_lg_u32 s34, 0\n\t"
        "s_cbranch_scc1 LB_calok%=\n\t"
        "s_sleep 4\n\t"
        "s_mov_b32 s35, 33792\n\t"
        "buffer_load_dword v0, off, s[12:15], s35 sc1\n\t"
        "s_waitcnt vmcnt(0)\n\t"
        "s_add_u32 s42, s42, 1\n\t"
        "s_cmp_lt_u32 s42, 0x100000\n\t"
        "s_cbranch_scc1 LB_cal%=\n\t"
        "s_movk_i32 s34, 8\n\t"
        "s_branch LB_die%=\n\t"
        "LB_calok%=:\n\t"
        "v_readfirstlane_b32 s34, v0\n\t"
        "s_nop 0\n\t"
        "s_and_b32 s34, s34, 8192\n\t"
        "s_add_u32 s8, s8, s34\n\t"
        "s_addc_u32 s9, s9, 0\n\t"
        "v_min_u32_e32 v109, -2, v109\n\t"
        "buffer_store_dword v109, v116, s[8:11], 0 offen sc1\n\t"
        "s_memrealtime s[32:33]\n\t"
        "s_waitcnt lgkmcnt(0)\n\t"
        "v_mov_b32_e32 v110, s32\n\t"
        "s_lshl_b32 s34, s29, 8\n\t"
        "s_lshl_b32 s35, s30, 20\n\t"
        "s_or_b32 s54, s34, s35\n\t"
        "s_add_u32 s34, s54, 12589056\n\t"
        "v_mov_b32_e32 v111, s34\n\t"
        "s_barrier\n\t"
        "s_cmp_lg_u32 s43, 0\n\t"
        "s_cbranch_scc1 LB_st%=\n\t"
        "v_mov_b32_e32 v2, 1\n\t"
        "s_mov_b32 s35, 34304\n\t"
        "s_mov_b64 exec, 1\n\t"
        "buffer_atomic_add v2, off, s[12:15], s35 sc0\n\t"
        "s_mov_b64 exec, -1\n\t"
        "s_waitcnt vmcnt(0)\n\t"
        "v_readfirstlane_b32 s34, v2\n\t"
        "s_nop 0\n\t"
        "s_cmp_lg_u32 s34, 95\n\t"
        "s_cbranch_scc1 LB_st%=\n\t"
        "s_memrealtime s[32:33]\n\t"
        "v_mbcnt_lo_u32_b32 v3, -1, 0\n\t"
        "v_lshlrev_b32_e32 v3, 7, v3\n\t"
        "s_waitcnt lgkmcnt(0)\n\t"
        "s_add_u32 s34, s32, 50\n\t"
        "s_or_b32 s34, s34, 1\n\t"
        "v_mov_b32_e32 v2, s34\n\t"
        "s_mov_b32 s35, 36864\n\t"
        "s_mov_b64 exec, 0xffff\n\t"
        "buffer_store_dword v2, v3, s[12:15], s35 offen sc1\n\t"
        "s_mov_b64 exec, -1\n\t"
        "LB_st%=:\n\t"
        "s_mov_b32 s24, 0\n\t"
        "s_mov_b32 s26, 0\n\t"
        "s_mov_b32 s27, 0x80000\n\t"
        "s_mov_b32 s28, 0x180000\n\t"
        "s_mov_b32 s31, 0\n\t"
        "s_mov_b32 s47, 0\n\t"
        "s_mov_b32 s38, 0xff00ff00\n\t"
        "s_mov_b32 s39, 0xff00ff00\n\t"
        "s_mov_b32 s40, 0\n\t"
        "s_mov_b32 s41, -1\n\t"
        "v_mov_b64_e32 v[0:1], 0\n\t"
        "v_mov_b64_e32 v[2:3], 0\n\t"
        "v_mov_b64_e32 v[4:5], 0\n\t"
        "v_mov_b64_e32 v[6:7], 0\n\t"
        "v_mov_b64_e32 v[8:9], 0\n\t"
        "v_mov_b64_e32 v[10:11], 0\n\t"
        "v_mov_b64_e32 v[12:13], 0\n\t"
        "v_mov_b64_e32 v[14:15], 0\n\t"
        "s_mov_b32 s42, 0\n\t"
        "LB_gw%=:\n\t"
        "buffer_load_dword v16, off, s[12:15], s45 sc1\n\t"
        "s_waitcnt vmcnt(0)\n\t"
        "v_readfirstlane_b32 s25, v16\n\t"
        "s_nop 0\n\t"
        "s_cmp_lg_u32 s25, 0\n\t"
        "s_cbranch_scc1 LB_loop%=\n\t"
        "s_sleep 2\n\t"
        "s_add_u32 s42, s42, 1\n\t"
        "s_cmp_lt_u32 s42, 0x100000\n\t"
        "s_cbranch_scc1 LB_gw%=\n\t"
        "s_movk_i32 s34, 9\n\t"
        "s_branch LB_die%=\n\t"
        "LB_loop%=:\n\t"
        "s_mov_b32 s42, 0\n\t"
        "s_sub_u32 s35, s25, 1\n\t"
        "LB_wa%=:\n\t"
        "s_memrealtime s[32:33]\n\t"
        "s_waitcnt lgkmcnt(0)\n\t"
        "s_sub_u32 s34, s35, s32\n\t"
        "s_cmpk_lt_u32 s34, 0x2000\n\t"
        "s_cbranch_scc1 LB_wa%=\n\t"
        "LB_rd%=:\n\t"
        "buffer_load_dwordx4 v[16:19], v112, s[8:11], s26 offen offset:0 sc1\n\t"
        "s_nop 9\n\t"
        "buffer_load_dwordx4 v[20:23], v112, s[8:11], s26 offen offset:1024 sc1\n\t"
        "s_nop 9\n\t"
        "buffer_load_dwordx4 v[24:27], v112, s[8:11], s26 offen offset:2048 sc1\n\t"
        "s_nop 9\n\t"
        "buffer_load_dwordx4 v[28:31], v112, s[8:11], s26 offen offset:3072 sc1\n\t"
        "LB_go%=:\n\t"
        "s_waitcnt vmcnt(3)\n\t"
        "v_smfmac_f32_16x16x64_bf16 v[0:3], v[16:19], v[128:135], v106\n\t"
        "v_max3_u32 v108, v16, v17, v18\n\t"
        "v_max_u32_e32 v108, v19, v108\n\t"
        "v_smfmac_f32_16x16x64_bf16 v[4:7], v[16:19], v[136:143], v106\n\t"
        "v_smfmac_f32_16x16x64_bf16 v[8:11], v[16:19], v[144:151], v106\n\t"
        "v_smfmac_f32_16x16x64_bf16 v[12:15], v[16:19], v[152:159], v106\n\t"
        "s_waitcnt vmcnt(2)\n\t"
        "v_smfmac_f32_16x16x64_bf16 v[0:3], v[20:23], v[160:167], v106\n\t"
        "v_max3_u32 v108, v20, v21, v108\n\t"
        "v_max3_u32 v108, v22, v23, v108\n\t"
        "v_smfmac_f32_16x16x64_bf16 v[4:7], v[20:23], v[168:175], v106\n\t"
        "v_smfmac_f32_16x16x64_bf16 v[8:11], v[20:23], v[176:183], v106\n\t"
        "v_smfmac_f32_16x16x64_bf16 v[12:15], v[20:23], v[184:191], v106\n\t"
        "s_waitcnt vmcnt(1)\n\t"
        "v_smfmac_f32_16x16x64_bf16 v[0:3], v[24:27], v[192:199], v106\n\t"
        "v_max3_u32 v108, v24, v25, v108\n\t"
        "v_max3_u32 v108, v26, v27, v108\n\t"
        "v_smfmac_f32_16x16x64_bf16 v[4:7], v[24:27], v[200:207], v106\n\t"
        "v_smfmac_f32_16x16x64_bf16 v[8:11], v[24:27], v[208:215], v106\n\t"
        "v_smfmac_f32_16x16x64_bf16 v[12:15], v[24:27], v[216:223], v106\n\t"
        "s_waitcnt vmcnt(0)\n\t"
        "v_smfmac_f32_16x16x64_bf16 v[0:3], v[28:31], v[224:231], v106\n\t"
        "v_max3_u32 v108, v28, v29, v108\n\t"
        "v_max3_u32 v108, v30, v31, v108\n\t"
        "v_smfmac_f32_16x16x64_bf16 v[4:7], v[28:31], v[232:239], v106\n\t"
        "v_cmp_eq_u32_e32 vcc, -1, v108\n\t"
        "v_smfmac_f32_16x16x64_bf16 v[8:11], v[28:31], v[240:247], v106\n\t"
        "v_smfmac_f32_16x16x64_bf16 v[12:15], v[28:31], v[248:255], v106\n\t"
        "s_cbranch_vccnz LB_rdfail%=\n\t"
        "s_nop 0\n\t"
        "v_add_f32_e32 v0, v0, v1\n\t"
        "v_add_f32_e32 v1, v2, v3\n\t"
        "ds_write_b64 v118, v[0:1] offset:0\n\t"
        "v_add_f32_e32 v4, v4, v5\n\t"
        "v_add_f32_e32 v5, v6, v7\n\t"
        "ds_write_b64 v118, v[4:5] offset:512\n\t"
        "v_add_f32_e32 v8, v8, v9\n\t"
        "v_add_f32_e32 v9, v10, v11\n\t"
        "ds_write_b64 v118, v[8:9] offset:1024\n\t"
        "v_add_f32_e32 v12, v12, v13\n\t"
        "v_add_f32_e32 v13, v14, v15\n\t"
        "ds_write_b64 v118, v[12:13] offset:1536\n\t"
        "buffer_store_dwordx2 v[110:111], v117, s[8:11], s26 offen sc1\n\t"
        "s_waitcnt lgkmcnt(0)\n\t"
        "s_barrier\n\t"
        "ds_read_b128 v[16:19], v119 offset:0\n\t"
        "ds_read_b128 v[20:23], v119 offset:2048\n\t"
        "ds_read_b128 v[24:27], v119 offset:4096\n\t"
        "ds_read_b128 v[28:31], v119 offset:6144\n\t"
        "buffer_load_dwordx4 v[64:67], v124, s[8:11], s28 offen sc1\n\t"
        "s_waitcnt lgkmcnt(2)\n\t"
        "v_pk_add_f32 v[16:17], v[16:17], v[20:21]\n\t"
        "v_pk_add_f32 v[18:19], v[18:19], v[22:23]\n\t"
        "s_waitcnt lgkmcnt(0)\n\t"
        "v_pk_add_f32 v[24:25], v[24:25], v[28:29]\n\t"
        "v_pk_add_f32 v[26:27], v[26:27], v[30:31]\n\t"
        "v_pk_add_f32 v[16:17], v[16:17], v[24:25]\n\t"
        "v_pk_add_f32 v[18:19], v[18:19], v[26:27]\n\t"
        "v_cvt_pk_bf16_f32 v20, v16, v17\n\t"
        "v_cvt_pk_bf16_f32 v21, v18, v19\n\t"
        "buffer_store_dwordx2 v[20:21], v114, s[8:11], s26 offen sc1\n\t"
        "s_cmp_eq_u32 s24, 63\n\t"
        "s_cbranch_scc1 LB_end%=\n\t"
        "s_memrealtime s[52:53]\n\t"
        "v_mov_b64_e32 v[24:25], -1\n\t"
        "v_mov_b64_e32 v[32:33], 0\n\t"
        "v_mov_b64_e32 v[34:35], 0\n\t"
        "v_mov_b64_e32 v[36:37], 0\n\t"
        "v_mov_b64_e32 v[38:39], 0\n\t"
        "v_mov_b64_e32 v[40:41], 0\n\t"
        "v_mov_b64_e32 v[42:43], 0\n\t"
        "v_mov_b64_e32 v[44:45], 0\n\t"
        "v_mov_b64_e32 v[46:47], 0\n\t"
        "v_mov_b64_e32 v[0:1], 0\n\t"
        "v_mov_b64_e32 v[2:3], 0\n\t"
        "v_mov_b64_e32 v[4:5], 0\n\t"
        "v_mov_b64_e32 v[6:7], 0\n\t"
        "v_mov_b64_e32 v[8:9], 0\n\t"
        "v_mov_b64_e32 v[10:11], 0\n\t"
        "v_mov_b64_e32 v[12:13], 0\n\t"
        "v_mov_b64_e32 v[14:15], 0\n\t"
        "s_add_u32 s46, s26, s69\n\t"
        "s_add_u32 s48, s26, s70\n\t"
        "s_add_u32 s73, s26, s71\n\t"
        "s_add_u32 s74, s26, s72\n\t"
        "s_sleep 1\n\t"
        "buffer_load_dwordx2 v[104:105], v113, s[8:11], s26 offen sc1\n\t"
        "buffer_store_dword v24, v116, s[8:11], s28 offen sc1\n\t"
        "s_xor_b32 s34, s26, 0x100000\n\t"
        "buffer_store_dwordx2 v[24:25], v114, s[8:11], s34 offen sc1\n\t"
        "s_add_u32 s66, s49, s43\n\t"
        "s_cmp_lg_u32 s66, 32\n\t"
        "s_cbranch_scc1 LB_norep%=\n\t"
        "s_waitcnt vmcnt(4)\n\t"
        "s_sub_u32 s66, s24, 1\n\t"
        "v_lshrrev_b32_e32 v68, 16, v64\n\t"
        "v_cmp_eq_u32_e32 vcc, s66, v68\n\t"
        "v_subrev_u32_e32 v64, s59, v64\n\t"
        "v_bfe_i32 v64, v64, 0, 16\n\t"
        "v_cndmask_b32_e32 v64, 0, v64, vcc\n\t"
        "v_lshrrev_b32_e32 v68, 16, v65\n\t"
        "v_cmp_eq_u32_e32 vcc, s66, v68\n\t"
        "v_subrev_u32_e32 v65, s59, v65\n\t"
        "v_bfe_i32 v65, v65, 0, 16\n\t"
        "v_cndmask_b32_e32 v65, 0, v65, vcc\n\t"
        "v_lshrrev_b32_e32 v68, 16, v66\n\t"
        "v_cmp_eq_u32_e32 vcc, s66, v68\n\t"
        "v_subrev_u32_e32 v66, s59, v66\n\t"
        "v_bfe_i32 v66, v66, 0, 16\n\t"
        "v_cndmask_b32_e32 v66, 0, v66, vcc\n\t"
        "v_lshrrev_b32_e32 v68, 16, v67\n\t"
        "v_cmp_eq_u32_e32 vcc, s66, v68\n\t"
        "v_subrev_u32_e32 v67, s59, v67\n\t"
        "v_bfe_i32 v67, v67, 0, 16\n\t"
        "v_cndmask_b32_e32 v67, 0, v67, vcc\n\t"
        "v_max3_i32 v64, v64, v65, v66\n\t"
        "v_max_i32_e32 v64, v64, v67\n\t"
        "s_nop 1\n\t"
        "v_max_i32_dpp v64, v64, v64 quad_perm:[1,0,3,2] row_mask:0xf bank_mask:0xf\n\t"
        "s_nop 1\n\t"
        "v_max_i32_dpp v64, v64, v64 quad_perm:[2,3,0,1] row_mask:0xf bank_mask:0xf\n\t"
        "s_nop 1\n\t"
        "v_max_i32_dpp v64, v64, v64 row_ror:4 row_mask:0xf bank_mask:0xf\n\t"
        "s_nop 1\n\t"
        "v_max_i32_dpp v64, v64, v64 row_ror:8 row_mask:0xf bank_mask:0xf\n\t"
        "v_mov_b32_e32 v65, v64\n\t"
        "s_nop 1\n\t"
        "v_permlane16_swap_b32_e32 v64, v65\n\t"
        "v_max_i32_e32 v64, v64, v65\n\t"
        "v_mov_b32_e32 v65, v64\n\t"
        "s_nop 1\n\t"
        "v_permlane32_swap_b32_e32 v64, v65\n\t"
        "v_max_i32_e32 v64, v64, v65\n\t"
        "s_nop 0\n\t"
        "v_readfirstlane_b32 s64, v64\n\t"
        "LB_norep%=:\n\t"
        "s_waitcnt vmcnt(2) lgkmcnt(0)\n\t"
        "s_mov_b32 s61, s29\n\t"
        "s_mov_b32 s62, s30\n\t"
        "s_mov_b32 s63, s31\n\t"
        "v_and_b32_e32 v30, 0xff, v105\n\t"
        "v_cmp_ne_u32_e32 vcc, s24, v30\n\t"
        "s_nop 0\n\t"
        "s_cbranch_vccnz LB_stre%=\n\t"
        "LB_stok%=:\n\t"
        "v_subrev_u32_e32 v104, s25, v104\n\t"
        "s_nop 1\n\t"
        "v_max_i32_dpp v104, v104, v104 quad_perm:[1,0,3,2] row_mask:0xf bank_mask:0xf\n\t"
        "s_nop 1\n\t"
        "v_max_i32_dpp v104, v104, v104 quad_perm:[2,3,0,1] row_mask:0xf bank_mask:0xf\n\t"
        "s_nop 1\n\t"
        "v_max_i32_dpp v104, v104, v104 row_ror:4 row_mask:0xf bank_mask:0xf\n\t"
        "s_nop 1\n\t"
        "v_max_i32_dpp v104, v104, v104 row_ror:8 row_mask:0xf bank_mask:0xf\n\t"
        "s_nop 1\n\t"
        "v_max_i32_dpp v104, v104, v104 row_bcast:15 row_mask:0xa bank_mask:0xf\n\t"
        "s_nop 1\n\t"
        "v_max_i32_dpp v104, v104, v104 row_bcast:31 row_mask:0xc bank_mask:0xf\n\t"
        "v_readfirstlane_b32 s35, v105\n\t"
        "v_readlane_b32 s36, v104, 63\n\t"
        "s_bfe_u32 s29, s35, 0xc0008\n\t"
        "s_lshr_b32 s30, s35, 20\n\t"
        "s_add_u32 s35, s31, s50\n\t"
        "s_mul_i32 s37, s50, 3\n\t"
        "s_min_u32 s35, s35, s37\n\t"
        "s_add_i32 s34, s36, s51\n\t"
        "s_cmp_gt_i32 s34, 0\n\t"
        "s_cselect_b32 s31, s35, 0\n\t"
        "s_cselect_b32 s34, s34, 0\n\t"
        "s_add_u32 s34, s34, s31\n\t"
        "s_add_u32 s37, s25, s34\n\t"
        "s_add_u32 s45, s37, s29\n\t"
        "s_add_u32 s25, s45, s30\n\t"
        "s_add_u32 s66, s49, s43\n\t"
        "s_cmp_lg_u32 s66, 32\n\t"
        "s_cbranch_scc1 LB_noctl%=\n\t"
        "s_cmp_eq_u32 s24, 0\n\t"
        "s_cbranch_scc1 LB_ctl0%=\n\t"
        "s_cmp_eq_u32 s24, 1\n\t"
        "s_cbranch_scc1 LB_ctl1%=\n\t"
        "s_add_i32 s65, s36, s62\n\t"
        "s_lshl_b32 s66, s65, 1\n\t"
        "s_sub_i32 s66, s66, s56\n\t"
        "s_cmp_gt_i32 s66, 56\n\t"
        "s_cbranch_scc1 LB_c1%=\n\t"
        "s_lshl_b32 s66, s65, 1\n\t"
        "s_add_u32 s34, s56, 8\n\t"
        "s_min_i32 s34, s34, s66\n\t"
        "s_sub_u32 s35, s56, s66\n\t"
        "s_ashr_i32 s35, s35, 2\n\t"
        "s_max_i32 s35, s35, 1\n\t"
        "s_sub_u32 s35, s56, s35\n\t"
        "s_max_i32 s56, s34, s35\n\t"
        "s_branch LB_c2%=\n\t"
        "LB_c1%=:\n\t"
        "s_add_u32 s56, s56, 2\n\t"
        "s_add_i32 s66, s64, s57\n\t"
        "s_cmp_gt_i32 s66, s61\n\t"
        "s_cbranch_scc1 LB_c2%=\n\t"
        "s_add_u32 s57, s57, 4\n\t"
        "s_min_u32 s57, s57, 24\n\t"
        "LB_c2%=:\n\t"
        "s_cmp_lg_u32 s63, 0\n\t"
        "s_cbranch_scc1 LB_c4%=\n\t"
        "s_lshl_b32 s66, s64, 1\n\t"
        "s_sub_i32 s66, s66, s55\n\t"
        "s_cmp_gt_i32 s66, 56\n\t"
        "s_cbranch_scc1 LB_c3%=\n\t"
        "s_lshl_b32 s66, s64, 1\n\t"
        "s_add_u32 s34, s55, 8\n\t"
        "s_min_i32 s34, s34, s66\n\t"
        "s_sub_u32 s35, s55, s66\n\t"
        "s_ashr_i32 s35, s35, 2\n\t"
        "s_max_i32 s35, s35, 1\n\t"
        "s_sub_u32 s35, s55, s35\n\t"
        "s_max_i32 s55, s34, s35\n\t"
        "s_branch LB_c4%=\n\t"
        "LB_c3%=:\n\t"
        "s_add_u32 s55, s55, 2\n\t"
        "s_cmp_lt_i32 s60, s58\n\t"
        "s_cbranch_scc1 LB_c4%=\n\t"
        "s_add_u32 s66, s58, 8\n\t"
        "s_cmp_gt_i32 s60, s66\n\t"
        "s_cbranch_scc1 LB_c4%=\n\t"
        "s_add_u32 s58, s58, 4\n\t"
        "s_min_u32 s58, s58, 21\n\t"
        "LB_c4%=:\n\t"
        "s_and_b32 s66, s24, 3\n\t"
        "s_cmp_eq_u32 s66, 0\n\t"
        "s_cselect_b32 s66, 1, 0\n\t"
        "s_sub_u32 s57, s57, s66\n\t"
        "s_max_u32 s57, s57, 8\n\t"
        "s_sub_u32 s58, s58, s66\n\t"
        "s_max_u32 s58, s58, 5\n\t"
        "s_add_u32 s34, s55, 1\n\t"
        "s_lshr_b32 s34, s34, 1\n\t"
        "s_add_u32 s34, s34, s57\n\t"
        "s_min_u32 s34, s34, 1000\n\t"
        "s_lshl_b32 s34, s34, 8\n\t"
        "s_add_u32 s35, s56, 1\n\t"
        "s_lshr_b32 s35, s35, 1\n\t"
        "s_add_u32 s35, s35, s58\n\t"
        "s_min_u32 s35, s35, 1000\n\t"
        "s_lshl_b32 s35, s35, 20\n\t"
        "s_or_b32 s54, s34, s35\n\t"
        "s_branch LB_ctl1%=\n\t"
        "LB_ctl0%=:\n\t"
        "s_movk_i32 s57, 8\n\t"
        "s_movk_i32 s58, 5\n\t"
        "s_sub_u32 s55, s29, 32\n\t"
        "s_lshl_b32 s55, s55, 1\n\t"
        "s_sub_u32 s56, s30, 17\n\t"
        "s_lshl_b32 s56, s56, 1\n\t"
        "LB_ctl1%=:\n\t"
        "s_mov_b32 s59, s37\n\t"
        "s_sub_u32 s60, 0, s36\n\t"
        "LB_noctl%=:\n\t"
        "s_cmp_eq_u32 s24, 0\n\t"
        "s_cselect_b32 s34, 3073, 1\n\t"
        "s_add_u32 s34, s34, s24\n\t"
        "s_add_u32 s34, s34, s54\n\t"
        "v_mov_b32_e32 v111, s34\n\t"
        "s_mov_b32 s42, 0\n\t"
        "s_sub_u32 s35, s45, 1\n\t"
        "LB_wo%=:\n\t"
        "s_memrealtime s[32:33]\n\t"
        "s_waitcnt lgkmcnt(0)\n\t"
        "s_sub_u32 s34, s35, s32\n\t"
        "s_cmpk_lt_u32 s34, 0x2000\n\t"
        "s_cbranch_scc1 LB_wo%=\n\t"
        "LB_ro%=:\n\t"
        "buffer_load_dwordx4 v[64:67], v115, s[8:11], s46 offen offset:0 sc1\n\t"
        "s_nop 12\n\t"
        "buffer_load_dwordx4 v[68:71], v115, s[8:11], s46 offen offset:1024 sc1\n\t"
        "s_nop 12\n\t"
        "buffer_load_dwordx4 v[72:75], v115, s[8:11], s48 offen offset:0 sc1\n\t"
        "s_nop 12\n\t"
        "buffer_load_dwordx4 v[76:79], v115, s[8:11], s48 offen offset:1024 sc1\n\t"
        "s_nop 12\n\t"
        "buffer_load_dwordx4 v[80:83], v115, s[8:11], s73 offen offset:0 sc1\n\t"
        "s_nop 12\n\t"
        "buffer_load_dwordx4 v[84:87], v115, s[8:11], s73 offen offset:1024 sc1\n\t"
        "s_nop 12\n\t"
        "buffer_load_dwordx4 v[88:91], v115, s[8:11], s74 offen offset:0 sc1\n\t"
        "s_nop 12\n\t"
        "buffer_load_dwordx4 v[92:95], v115, s[8:11], s74 offen offset:1024 sc1\n\t"
        "LB_go2%=:\n\t"
        "s_waitcnt vmcnt(7)\n\t"
        "v_smfmac_f32_16x16x64_bf16 v[32:35], v[64:67], a[0:7], v106\n\t"
        "v_max3_u32 v108, v64, v65, v66\n\t"
        "v_max_u32_e32 v108, v67, v108\n\t"
        "v_smfmac_f32_16x16x64_bf16 v[40:43], v[64:67], a[16:23], v106\n\t"
        "v_smfmac_f32_16x16x64_bf16 v[36:39], v[64:67], a[8:15], v106\n\t"
        "v_smfmac_f32_16x16x64_bf16 v[44:47], v[64:67], a[24:31], v106\n\t"
        "s_waitcnt vmcnt(6)\n\t"
        "v_smfmac_f32_16x16x64_bf16 v[32:35], v[68:71], a[32:39], v106\n\t"
        "v_max3_u32 v108, v68, v69, v108\n\t"
        "v_max3_u32 v108, v70, v71, v108\n\t"
        "v_smfmac_f32_16x16x64_bf16 v[40:43], v[68:71], a[48:55], v106\n\t"
        "v_smfmac_f32_16x16x64_bf16 v[36:39], v[68:71], a[40:47], v106\n\t"
        "v_smfmac_f32_16x16x64_bf16 v[44:47], v[68:71], a[56:63], v106\n\t"
        "s_waitcnt vmcnt(5)\n\t"
        "v_smfmac_f32_16x16x64_bf16 v[32:35], v[72:75], a[64:71], v106\n\t"
        "v_max3_u32 v108, v72, v73, v108\n\t"
        "v_max3_u32 v108, v74, v75, v108\n\t"
        "v_smfmac_f32_16x16x64_bf16 v[40:43], v[72:75], a[80:87], v106\n\t"
        "v_smfmac_f32_16x16x64_bf16 v[36:39], v[72:75], a[72:79], v106\n\t"
        "v_smfmac_f32_16x16x64_bf16 v[44:47], v[72:75], a[88:95], v106\n\t"
        "s_waitcnt vmcnt(4)\n\t"
        "v_smfmac_f32_16x16x64_bf16 v[32:35], v[76:79], a[96:103], v106\n\t"
        "v_max3_u32 v108, v76, v77, v108\n\t"
        "v_max3_u32 v108, v78, v79, v108\n\t"
        "v_smfmac_f32_16x16x64_bf16 v[40:43], v[76:79], a[112:119], v106\n\t"
        "v_smfmac_f32_16x16x64_bf16 v[36:39], v[76:79], a[104:111], v106\n\t"
        "v_smfmac_f32_16x16x64_bf16 v[44:47], v[76:79], a[120:127], v106\n\t"
        "s_waitcnt vmcnt(3)\n\t"
        "v_smfmac_f32_16x16x64_bf16 v[32:35], v[80:83], a[128:135], v106\n\t"
        "v_max3_u32 v108, v80, v81, v108\n\t"
        "v_max3_u32 v108, v82, v83, v108\n\t"
        "v_smfmac_f32_16x16x64_bf16 v[40:43], v[80:83], a[144:151], v106\n\t"
        "v_smfmac_f32_16x16x64_bf16 v[36:39], v[80:83], a[136:143], v106\n\t"
        "v_smfmac_f32_16x16x64_bf16 v[44:47], v[80:83], a[152:159], v106\n\t"
        "s_waitcnt vmcnt(2)\n\t"
        "v_smfmac_f32_16x16x64_bf16 v[32:35], v[84:87], a[160:167], v106\n\t"
        "v_max3_u32 v108, v84, v85, v108\n\t"
        "v_max3_u32 v108, v86, v87, v108\n\t"
        "v_smfmac_f32_16x16x64_bf16 v[40:43], v[84:87], a[176:183], v106\n\t"
        "v_smfmac_f32_16x16x64_bf16 v[36:39], v[84:87], a[168:175], v106\n\t"
        "v_smfmac_f32_16x16x64_bf16 v[44:47], v[84:87], a[184:191], v106\n\t"
        "s_waitcnt vmcnt(1)\n\t"
        "v_smfmac_f32_16x16x64_bf16 v[32:35], v[88:91], a[192:199], v106\n\t"
        "v_max3_u32 v108, v88, v89, v108\n\t"
        "v_max3_u32 v108, v90, v91, v108\n\t"
        "v_smfmac_f32_16x16x64_bf16 v[40:43], v[88:91], a[208:215], v106\n\t"
        "v_smfmac_f32_16x16x64_bf16 v[36:39], v[88:91], a[200:207], v106\n\t"
        "v_smfmac_f32_16x16x64_bf16 v[44:47], v[88:91], a[216:223], v106\n\t"
        "s_waitcnt vmcnt(0)\n\t"
        "v_smfmac_f32_16x16x64_bf16 v[32:35], v[92:95], a[224:231], v106\n\t"
        "v_max3_u32 v108, v92, v93, v108\n\t"
        "v_max3_u32 v108, v94, v95, v108\n\t"
        "v_smfmac_f32_16x16x64_bf16 v[40:43], v[92:95], a[240:247], v106\n\t"
        "v_cmp_eq_u32_e32 vcc, -1, v108\n\t"
        "v_smfmac_f32_16x16x64_bf16 v[36:39], v[92:95], a[232:239], v106\n\t"
        "v_smfmac_f32_16x16x64_bf16 v[44:47], v[92:95], a[248:255], v106\n\t"
        "s_cbranch_vccnz LB_rofail%=\n\t"
        "s_nop 0\n\t"
        "v_add_f32_e32 v32, v32, v33\n\t"
        "s_nop 1\n\t"
        "v_add_f32_e32 v40, v42, v43\n\t"
        "v_add_f32_e32 v32, v32, v40\n\t"
        "v_add_f32_e32 v36, v36, v37\n\t"
        "v_add_f32_e32 v44, v46, v47\n\t"
        "v_add_f32_e32 v36, v36, v44\n\t"
        "ds_write2_b32 v120, v32, v36 offset0:0 offset1:64\n\t"
        "s_waitcnt lgkmcnt(0)\n\t"
        "s_barrier\n\t"
        "ds_read_b64 v[64:65], v121 offset:0\n\t"
        "ds_read_b64 v[66:67], v121 offset:512\n\t"
        "ds_read_b64 v[68:69], v121 offset:1024\n\t"
        "ds_read_b64 v[70:71], v121 offset:1536\n\t"
        "s_waitcnt lgkmcnt(2)\n\t"
        "v_pk_add_f32 v[64:65], v[64:65], v[66:67]\n\t"
        "s_waitcnt lgkmcnt(0)\n\t"
        "v_pk_add_f32 v[68:69], v[68:69], v[70:71]\n\t"
        "v_pk_add_f32 v[64:65], v[64:65], v[68:69]\n\t"
        "v_cvt_pk_bf16_f32 v64, v64, v65\n\t"
        "buffer_store_dword v64, v116, s[8:11], s27 offen sc1\n\t"
        "s_memrealtime s[32:33]\n\t"
        "s_waitcnt lgkmcnt(0)\n\t"
        "v_mov_b32_e32 v110, s32\n\t"
        "s_add_u32 s24, s24, 1\n\t"
        "s_mov_b32 s28, s26\n\t"
        "s_mov_b32 s26, s27\n\t"
        "s_add_u32 s27, s27, 0x80000\n\t"
        "s_and_b32 s27, s27, 0x1fffff\n\t"
        "s_branch LB_loop%=\n\t"
        "LB_end%=:\n\t"
        "s_endpgm\n\t"
        "LB_stre%=:\n\t"
        "s_mov_b32 s42, 0\n\t"
        "LB_str2%=:\n\t"
        "buffer_load_dwordx2 v[104:105], v113, s[8:11], s26 offen sc1\n\t"
        "s_waitcnt vmcnt(0)\n\t"
        "v_and_b32_e32 v30, 0xff, v105\n\t"
        "v_cmp_ne_u32_e32 vcc, s24, v30\n\t"
        "s_nop 0\n\t"
        "s_cbranch_vccz LB_stok%=\n\t"
        "s_add_u32 s42, s42, 1\n\t"
        "s_cmp_lt_u32 s42, 0x100000\n\t"
        "s_cbranch_scc1 LB_str2%=\n\t"
        "s_movk_i32 s34, 5\n\t"
        "s_branch LB_die%=\n\t"
        "LB_rdfail%=:\n\t"
        "s_or_b32 s47, s47, 1\n\t"
        "buffer_load_dwordx4 v[16:19], v112, s[8:11], s26 offen offset:0 sc1\n\t"
        "buffer_load_dwordx4 v[20:23], v112, s[8:11], s26 offen offset:1024 sc1\n\t"
        "buffer_load_dwordx4 v[24:27], v112, s[8:11], s26 offen offset:2048 sc1\n\t"
        "buffer_load_dwordx4 v[28:31], v112, s[8:11], s26 offen offset:3072 sc1\n\t"
        "s_nop 7\n\t"
        "v_mov_b64_e32 v[0:1], 0\n\t"
        "v_mov_b64_e32 v[2:3], 0\n\t"
        "v_mov_b64_e32 v[4:5], 0\n\t"
        "v_mov_b64_e32 v[6:7], 0\n\t"
        "v_mov_b64_e32 v[8:9], 0\n\t"
        "v_mov_b64_e32 v[10:11], 0\n\t"
        "v_mov_b64_e32 v[12:13], 0\n\t"
        "v_mov_b64_e32 v[14:15], 0\n\t"
        "s_branch LB_rdre%=w\n\t"
        "LB_rdre%=:\n\t"
        "buffer_load_dwordx4 v[16:19], v112, s[8:11], s26 offen offset:0 sc1\n\t"
        "buffer_load_dwordx4 v[20:23], v112, s[8:11], s26 offen offset:1024 sc1\n\t"
        "buffer_load_dwordx4 v[24:27], v112, s[8:11], s26 offen offset:2048 sc1\n\t"
        "buffer_load_dwordx4 v[28:31], v112, s[8:11], s26 offen offset:3072 sc1\n\t"
        "LB_rdre%=w:\n\t"
        "s_waitcnt vmcnt(0) lgkmcnt(0)\n\t"
        "v_max3_u32 v108, v16, v17, v18\n\t"
        "v_max3_u32 v108, v108, v19, v20\n\t"
        "v_max3_u32 v108, v108, v21, v22\n\t"
        "v_max3_u32 v108, v108, v23, v24\n\t"
        "v_max3_u32 v108, v108, v25, v26\n\t"
        "v_max3_u32 v108, v108, v27, v28\n\t"
        "v_max3_u32 v108, v108, v29, v30\n\t"
        "v_max_u32_e32 v108, v108, v31\n\t"
        "v_cmp_eq_u32_e32 vcc, -1, v108\n\t"
        "s_nop 0\n\t"
        "s_cbranch_vccz LB_go%=\n\t"
        "s_add_u32 s42, s42, 1\n\t"
        "s_cmp_lt_u32 s42, 0x100000\n\t"
        "s_cbranch_scc1 LB_rdre%=\n\t"
        "s_movk_i32 s34, 3\n\t"
        "s_branch LB_die%=\n\t"
        "LB_rofail%=:\n\t"
        "s_or_b32 s47, s47, 4\n\t"
        "buffer_load_dwordx4 v[64:67], v115, s[8:11], s46 offen offset:0 sc1\n\t"
        "buffer_load_dwordx4 v[68:71], v115, s[8:11], s46 offen offset:1024 sc1\n\t"
        "buffer_load_dwordx4 v[72:75], v115, s[8:11], s48 offen offset:0 sc1\n\t"
        "buffer_load_dwordx4 v[76:79], v115, s[8:11], s48 offen offset:1024 sc1\n\t"
        "buffer_load_dwordx4 v[80:83], v115, s[8:11], s73 offen offset:0 sc1\n\t"
        "buffer_load_dwordx4 v[84:87], v115, s[8:11], s73 offen offset:1024 sc1\n\t"
        "buffer_load_dwordx4 v[88:91], v115, s[8:11], s74 offen offset:0 sc1\n\t"
        "buffer_load_dwordx4 v[92:95], v115, s[8:11], s74 offen offset:1024 sc1\n\t"
        "s_nop 7\n\t"
        "v_mov_b64_e32 v[32:33], 0\n\t"
        "v_mov_b64_e32 v[34:35], 0\n\t"
        "v_mov_b64_e32 v[36:37], 0\n\t"
        "v_mov_b64_e32 v[38:39], 0\n\t"
        "v_mov_b64_e32 v[40:41], 0\n\t"
        "v_mov_b64_e32 v[42:43], 0\n\t"
        "v_mov_b64_e32 v[44:45], 0\n\t"
        "v_mov_b64_e32 v[46:47], 0\n\t"
        "s_branch LB_rore%=w\n\t"
        "LB_rore%=:\n\t"
        "buffer_load_dwordx4 v[64:67], v115, s[8:11], s46 offen offset:0 sc1\n\t"
        "buffer_load_dwordx4 v[68:71], v115, s[8:11], s46 offen offset:1024 sc1\n\t"
        "buffer_load_dwordx4 v[72:75], v115, s[8:11], s48 offen offset:0 sc1\n\t"
        "buffer_load_dwordx4 v[76:79], v115, s[8:11], s48 offen offset:1024 sc1\n\t"
        "buffer_load_dwordx4 v[80:83], v115, s[8:11], s73 offen offset:0 sc1\n\t"
        "buffer_load_dwordx4 v[84:87], v115, s[8:11], s73 offen offset:1024 sc1\n\t"
        "buffer_load_dwordx4 v[88:91], v115, s[8:11], s74 offen offset:0 sc1\n\t"
        "buffer_load_dwordx4 v[92:95], v115, s[8:11], s74 offen offset:1024 sc1\n\t"
        "LB_rore%=w:\n\t"
        "s_waitcnt vmcnt(0) lgkmcnt(0)\n\t"
        "v_max3_u32 v108, v64, v65, v66\n\t"
        "v_max3_u32 v108, v108, v67, v68\n\t"
        "v_max3_u32 v108, v108, v69, v70\n\t"
        "v_max3_u32 v108, v108, v71, v72\n\t"
        "v_max3_u32 v108, v108, v73, v74\n\t"
        "v_max3_u32 v108, v108, v75, v76\n\t"
        "v_max3_u32 v108, v108, v77, v78\n\t"
        "v_max3_u32 v108, v108, v79, v80\n\t"
        "v_max3_u32 v108, v108, v81, v82\n\t"
        "v_max3_u32 v108, v108, v83, v84\n\t"
        "v_max3_u32 v108, v108, v85, v86\n\t"
        "v_max3_u32 v108, v108, v87, v88\n\t"
        "v_max3_u32 v108, v108, v89, v90\n\t"
        "v_max3_u32 v108, v108, v91, v92\n\t"
        "v_max3_u32 v108, v108, v93, v94\n\t"
        "v_max_u32_e32 v108, v108, v95\n\t"
        "v_cmp_eq_u32_e32 vcc, -1, v108\n\t"
        "s_nop 0\n\t"
        "s_cbranch_vccz LB_go2%=\n\t"
        "s_add_u32 s42, s42, 1\n\t"
        "s_cmp_lt_u32 s42, 0x100000\n\t"
        "s_cbranch_scc1 LB_rore%=\n\t"
        "s_movk_i32 s34, 4\n\t"
        "s_branch LB_die%=\n\t"
        "LB_die%=:\n\t"
        "v_mov_b32_e32 v30, s34\n\t"
        "s_mov_b32 s35, 33024\n\t"
        "s_mov_b64 exec, 1\n\t"
        "buffer_store_dword v30, off, s[12:15], s35 sc1\n\t"
        "s_endpgm\n\t"
        ".Lk_end_B:\n\t"
        :: REC_S_IN, "{v96}"(g_v), "{v97}"(g_o), "{v98}"(wb0), "{v99}"(wb1), "{v100}"(rb0), "{v101}"(rb0 ^ 32u), "{v102}"(rb0 ^ 64u), "{v103}"(rb0 ^ 96u),
           "{v106}"((l & 1) ? 0xEEEEEEEEu : 0x44444444u), "{v[108:109]}"(m0p), "{s68}"(rot),
           "{v112}"(a_mem), "{v113}"((24 + cp) * 16384 + l * ST_P), "{v114}"(a_vmw), "{v115}"(a_or), "{v116}"(a_mw), "{v117}"(a_sw),
           "{v118}"(lb + w * 2048 + (g >> 1) * 256 + n * 16 + (g & 1) * 8), "{v119}"(lb + (w & 1) * 1024 + l * 16), "{v120}"(lb + LB_R + w * 512 + l * 4),
           "{v121}"(lb + LB_R + l * 8), "{v122}"(stg + l * 16), "{v123}"(o_r), "{v124}"(rb + w == 32 ? 28 * 16384 + (l & 31) * 128 : OOB)
        : "memory", "vcc", "scc", "v0", "v255", "a0", "a255", "s101");
    __builtin_unreachable();
}
__device__ __forceinline__ void rec_helper(const Params& p, rsrc_t r, unsigned char* lds, const int wv, const int hi) {
    const int tid = otid(), l = tid & 63;
    const rsrc_t rh = __builtin_amdgcn_make_buffer_rsrc(p.ws + hx_base(p.ws), 0, HX_BYTES, 0x00020000);
    const v4u ones = {0xFFFFFFFFu, 0xFFFFFFFFu, 0xFFFFFFFFu, 0xFFFFFFFFu};
    unsigned best0 = ~0u, best1 = ~0u, d[5] = {0u, 0u, 0u, 0u, 0u};
    const bool meas = (hi & 7) < 2;
    if (meas) {
        const unsigned ca = l >= 8 ? OOB : (31 + wv * 32) * 16384 + hi * 256 + l * 16;
        const unsigned tend = rtnow() + CAL_T;
#pragma unroll 1
        for (int i = 0; i < 64 && (i < 3 || (int)(rtnow() - tend) < 0); ++i) {
            const unsigned t0 = (unsigned)__builtin_amdgcn_s_memtime(); __builtin_amdgcn_s_waitcnt(0);
            st16(rh, ca, ones); DRAIN();
            const unsigned t1 = (unsigned)__builtin_amdgcn_s_memtime(); __builtin_amdgcn_s_waitcnt(0);
            st16(rh, ca + 8192, ones); DRAIN();
            const unsigned t2 = (unsigned)__builtin_amdgcn_s_memtime(); __builtin_amdgcn_s_waitcnt(0);
            best0 = min(best0, t1 - t0); best1 = min(best1, t2 - t1); }
        if (l < 2) __hip_atomic_fetch_max((unsigned*)(p.ws + F_CAL + 128) + l, 65535u - min(l ? best1 : best0, 65535u), __ATOMIC_RELAXED, __HIP_MEMORY_SCOPE_AGENT);
    } else if (TOUCH) {
        const int xq = hi >> 3, k = ((hi & 7) - 2) * 4 + wv;
#pragma unroll
        for (int i = 0; i < 5; ++i) { const bf16_t* q;
            if (i & 1) { const int z = (i >> 1) * 24 + k, st = z & 3, c = 4 + (z >> 2);
                q = ((st & 1) ? p.Wk : p.Wq) + (size_t)(((((st & 1) * 2 + 1) * 4 + c) & 15) * 64 + l) * 4096 + (xq * 8 + 1 + (st >> 1) * 4) * 128 + 64; }
            else { const int z = min((i >> 1) * 24 + k, 63);
                q = p.Wo + (size_t)(((z & 3) * 8 + (z >> 3)) * 128 + ((z >> 2) & 1) * 64 + l) * 1024 + (RPL == 4 ? (xq >> 1) : (xq & 1)) * 512 + 192; if (RPL == 4 && (xq & 1)) q = p.gate_w; }
            asm volatile("global_load_dword %0, %1, off" : "+v"(d[i]) : "v"(q) : "memory"); }
    }
    auto more = [&]() {
        const int xq = hi >> 3, k = (hi & 7) * 4 + wv;
#pragma unroll 1
        for (int z = k; z < 56 * T2U; z += 32) { const int s7 = z % 7, rr = z / 7;
            const bf16_t* q = p.Wo + (size_t)(((rr & 3) * 8 + 8 - T2U + (rr >> 3)) * 128 + ((rr >> 2) & 1) * 64 + l) * 1024 + (xq & 1) * 512 + (s7 + (s7 >= 3)) * 64;
            asm volatile("global_load_dword %0, %1, off" : "+v"(d[0]) : "v"(q) : "memory"); }
#pragma unroll 1
        for (int z = k; z < 28 * T2A; z += 32) { const int st = z % 28, c = 16 - T2A + z / 28, s2 = st + (st >= 5) + (st >= 6) + (st >= 19) + (st >= 20);
            const bf16_t* q = ((s2 & 2) ? p.Wk : p.Wq) + (size_t)((((s2 & 3) * 4 + c) & 15) * 64 + l) * 4096 + (xq * 8 + (s2 >> 2)) * 128 + (s2 & 1) * 64;
            asm volatile("global_load_dword %0, %1, off" : "+v"(d[0]) : "v"(q) : "memory"); } };
    if (T2F) more();
    st16(r, tid < 64 ? F_KVQ + hi * 1024 + tid * 16 : tid < 72 ? F_PROG + hi * 128 + (tid - 64) * 16 : tid < 80 ? F_CVY + hi * 128 + (tid - 72) * 16 : OOB, (v4u){0u, 0u, 0u, 0u});
#pragma unroll
    for (int i = 0; i < 16; ++i) st16(rh, hi * 65536 + i * 4096 + tid * 16, ones);
    asm volatile("s_waitcnt vmcnt(0)" : "+v"(d[0]), "+v"(d[1]), "+v"(d[2]), "+v"(d[3]), "+v"(d[4]) :: "memory");
    __syncthreads();
    if (tid == 0 && __hip_atomic_fetch_add((unsigned*)(p.ws + F_CAL + 256), 1u, __ATOMIC_RELAXED, __HIP_MEMORY_SCOPE_AGENT) == 31u) {
        const v2u c = ld8(r, F_CAL + 128); st4(r, F_CAL, c.x < c.y ? 8193u : 1u); }
    if (!T2F) more();
    asm volatile("s_waitcnt vmcnt(0)" : "+v"(d[0]) :: "memory");
}
using f32x16 = __attribute__((ext_vector_type(16))) float;
constexpr int CV_RP = 80;
constexpr int CV_U = 96 * CV_RP;
constexpr int CV_KP = 4416;
constexpr int CV_K0 = 8 * CV_U;
__device__ __forceinline__ float blo(unsigned x) { return __uint_as_float(x << 16); }
__device__ __forceinline__ float bhi(unsigned x) { return __uint_as_float(x & 0xFFFF0000u); }
#define NAP() do { __builtin_amdgcn_sched_barrier(0); __builtin_amdgcn_s_sleep(XSLP); __builtin_amdgcn_sched_barrier(0); } while (0)
template <int NT>
__device__ __forceinline__ void gate_block(const Params& p, rsrc_t r, const int hi, const int wv) {
    const int l = otid() & 63, t0 = (hi * 4 + wv) * NT;
    v4u xg[NT][2], gw[2];
#pragma unroll
    for (int q = 0; q < 2; ++q) gw[q] = *(const v4u*)(p.gate_w + q * 512 + l * 8);
#pragma unroll
    for (int i = 0; i < NT; ++i)
#pragma unroll
        for (int q = 0; q < 2; ++q) { xg[i][q] = *(const v4u*)(p.Aq + (size_t)(t0 + i) * D + q * 512 + l * 8); NAP(); }
    float mine = 0.f;
#pragma unroll
    for (int i = 0; i < NT; ++i) { float acc = 0.f;
#pragma unroll
        for (int q = 0; q < 2; ++q)
#pragma unroll
            for (int e = 0; e < 4; ++e) { const unsigned pr = pack2(blo(xg[i][q][e]) * blo(gw[q][e]), bhi(xg[i][q][e]) * bhi(gw[q][e])); acc += blo(pr); acc += bhi(pr); }
        for (int o = 32; o > 0; o >>= 1) acc += __shfl_xor(acc, o);
        if (l == i) mine = acc; }
    if (l < NT) { const float z = rbfh(mine); st4(r, W_GATE + (t0 + l) * 4, __float_as_uint(rbfh(1.f / (1.f + expf(-z))))); }
}
__device__ __forceinline__ void conv_prep(const Params& p, rsrc_t r, const int wv, const int hi, const int G0, const int G1) {
    const int tid = otid(), Q = tid >> 2, cg = tid & 3, ch0 = hi * 32 + cg * 8;
    v4u sk[4]; float sv[8][4];
#pragma unroll
    for (int q = 0; q < 4; ++q) sk[q] = *(const v4u*)(p.shift_k + ch0 * 4 + q * 8);
#pragma unroll
    for (int c8 = 0; c8 < 8; ++c8)
#pragma unroll
        for (int tp = 0; tp < 4; ++tp) { const unsigned sw = sk[c8 >> 1][(c8 & 1) * 2 + (tp >> 1)]; sv[c8][tp] = (tp & 1) ? bhi(sw) : blo(sw); }
    const rsrc_t rk = __builtin_amdgcn_make_buffer_rsrc((void*)p.Ak, 0, S * D * 2, 0x00020000), rv = __builtin_amdgcn_make_buffer_rsrc((void*)p.Av, 0, S * D * 2, 0x00020000),
                 rq = __builtin_amdgcn_make_buffer_rsrc((void*)p.Aq, 0, S * D * 2, 0x00020000);
#pragma unroll 1
    for (int G = G0; G < G1; ++G) {
        const unsigned ro = (unsigned)((Q * 32 + G * 8) * D + ch0) * 2u, uo = hi * 131072 + tid * 16;
        v4u kr[11], u[8], qv[8];
#pragma unroll
        for (int i = 0; i < 11; ++i) { kr[i] = __builtin_amdgcn_raw_buffer_load_b128(rk, (i < 3 && Q + G == 0) ? OOB : ro + (i - 3) * 2048, 0, 0); NAP(); }
#pragma unroll
        for (int i = 0; i < 8; ++i) { u[i] = __builtin_amdgcn_raw_buffer_load_b128(rv, ro + i * 2048, 0, 0); NAP(); }
#pragma unroll
        for (int i = 0; i < 8; ++i) { qv[i] = __builtin_amdgcn_raw_buffer_load_b128(rq, ro + i * 2048, 0, 0); NAP(); }
#pragma unroll
        for (int q = 0; q < 4; ++q) {
#pragma unroll
            for (int i = 0; i < 8; ++i) {
                unsigned ks = pack2(blo(kr[i][q]) * sv[2 * q][3], bhi(kr[i][q]) * sv[2 * q + 1][3]), pr;
#pragma unroll
                for (int d = 1; d < 4; ++d) { pr = pack2(blo(kr[i + d][q]) * sv[2 * q][3 - d], bhi(kr[i + d][q]) * sv[2 * q + 1][3 - d]); ks = pack2(blo(ks) + blo(pr), bhi(ks) + bhi(pr)); }
                u[i][q] = pack2(blo(ks) * blo(u[i][q]), bhi(ks) * bhi(u[i][q])); }
            v4u lo, hh;
#pragma unroll
            for (int z = 0; z < 4; ++z) { const unsigned x = u[7 - 2 * z][q], y = u[6 - 2 * z][q]; lo[z] = __builtin_amdgcn_perm(y, x, 0x05040100u); hh[z] = __builtin_amdgcn_perm(y, x, 0x07060302u); }
            st16(r, W_U + uo + q * 32768 + G * 4096, lo); NAP(); st16(r, W_U + uo + q * 32768 + 16384 + G * 4096, hh); NAP();
#pragma unroll
            for (int z = 0; z < 2; ++z) { st16(r, W_Y + uo + q * 32768 + (2 * G + z) * 4096, (v4u){qv[4 * z][q], qv[4 * z + 1][q], qv[4 * z + 2][q], qv[4 * z + 3][q]}); NAP(); }
        }
    }
    DRAIN(); __syncthreads();
    if (tid == 0) __hip_atomic_fetch_add((unsigned*)(p.ws + F_CONV), 1u, __ATOMIC_RELAXED, __HIP_MEMORY_SCOPE_AGENT);
}
__device__ __forceinline__ bool conv_core(const Params& p, rsrc_t r, unsigned char* lds, const int wv, const int un) {
    const int tid = otid(), w = wv, l = tid & 63, Q = tid >> 2, cg = tid & 3;
    const rsrc_t rs = __builtin_amdgcn_make_buffer_rsrc((void*)p.ssm_k, 0, S * D * 2, 0x00020000);
    v4u kraw[4]; v2u kext[4];
    auto kload = [&](int n) { const unsigned o = (unsigned)(((un >> 2) * 32 + w * 8 + (un & 3) * 2 + n) * S + l * 8) * 2u;
#pragma unroll
        for (int i = 0; i < 4; ++i) { kraw[i] = __builtin_amdgcn_raw_buffer_load_b128(rs, o + i * 1024, 0, CV_POL);
            kext[i] = __builtin_amdgcn_raw_buffer_load_b64(rs, (i == 3 && l == 63) ? OOB : o + i * 1024 + 16, 0, 0); } };
    kload(0);
    if (!wait_word(r, F_CONV, XG ? 128u : 32u, 2048u)) return false;
    v4u ur[8], qr[8];
#pragma unroll
    for (int i = 0; i < 8; ++i) ur[i] = ld16(r, W_U + un * 32768 + i * 4096 + tid * 16);
#pragma unroll
    for (int i = 0; i < 8; ++i) qr[i] = ld16(r, W_Y + un * 32768 + i * 4096 + tid * 16);
    const unsigned dv = *(const unsigned*)(p.ssm_D + (un >> 2) * 32 + cg * 8 + (un & 3) * 2);
    for (int i = tid; i < 8 * 160; i += 256) *(v4u*)(lds + (i / 160) * CV_U + (i % 160) * 16) = (v4u){0u, 0u, 0u, 0u};
    unsigned char* kc = lds + CV_K0 + w * 4 * CV_KP;
    if (l < 32) {
#pragma unroll
        for (int q = 0; q < 4; ++q) *(unsigned*)(kc + q * CV_KP + l * 4) = 0u; }
    const int b = l & 31, kg = l >> 5;
    const unsigned kb = (unsigned)(size_t)kc + (b & 3) * CV_KP + ((b & 28) - 8 * kg + 56 - 16) * 2, ub0 = (unsigned)(size_t)lds + (1 + b) * CV_RP + kg * 16;
    unsigned char* const urow = lds + 2 * cg * CV_U + (32 + Q) * CV_RP;
#pragma unroll
    for (int i = 0; i < 8; ++i) *(v4u*)(urow + (i >> 2) * CV_U + (i & 3) * 16) = ur[i];
    __syncthreads();
#pragma unroll 1
        for (int ci = 0; ci < 2; ++ci) {
            const int c8 = 2 * w + ci;
#pragma unroll
            for (int i = 0; i < 4; ++i) { const v4u e = kraw[i]; const v2u f = kext[i]; unsigned char* q = kc + 128 + (i * 64 + l) * 16;
                const unsigned s0 = __builtin_amdgcn_alignbit(e.y, e.x, 16), s1 = __builtin_amdgcn_alignbit(e.z, e.y, 16), s2 = __builtin_amdgcn_alignbit(e.w, e.z, 16),
                               s3 = __builtin_amdgcn_alignbit(f.x, e.w, 16), s4 = __builtin_amdgcn_alignbit(f.y, f.x, 16);
                *(v4u*)q = (v4u){s0, s1, s2, s3}; *(v4u*)(q + CV_KP) = (v4u){e.y, e.z, e.w, f.x}; *(v4u*)(q + 2 * CV_KP) = (v4u){s1, s2, s3, s4}; *(v4u*)(q + 3 * CV_KP) = (v4u){e.z, e.w, f.x, f.y};
                if (i == 0 && l == 0) {
                    *(v2u*)(q - 8) = (v2u){0u, e.x << 16}; *(v2u*)(q + CV_KP - 8) = (v2u){0u, e.x}; *(v2u*)(q + 2 * CV_KP - 8) = (v2u){e.x << 16, s0}; *(v2u*)(q + 3 * CV_KP - 8) = (v2u){e.x, e.y}; } }
            if (ci == 0) kload(1);
            __builtin_amdgcn_wave_barrier();
            f32x16 acc0, acc1;
            asm volatile(
                "ds_read_b64 v[192:193], %[kb] offset:32\n\t"
                "ds_read_b64 v[194:195], %[kb] offset:40\n\t"
                "ds_read_b128 v[196:199], %[ub] offset:5040\n\t"
                "ds_read_b128 v[200:203], %[ub] offset:2480\n\t"
                "ds_read_b64 v[204:205], %[kb] offset:2080\n\t"
                "ds_read_b64 v[206:207], %[kb] offset:2088\n\t"
                "ds_read_b64 v[208:209], %[kb] offset:0\n\t"
                "ds_read_b64 v[210:211], %[kb] offset:8\n\t"
                "ds_read_b128 v[212:215], %[ub] offset:5072\n\t"
                "ds_read_b128 v[216:219], %[ub] offset:2512\n\t"
                "ds_read_b64 v[220:221], %[kb] offset:2048\n\t"
                "ds_read_b64 v[222:223], %[kb] offset:2056\n\t"
                "s_waitcnt lgkmcnt(9)\n\t"
                "v_mfma_f32_32x32x16_bf16 %[a1], v[192:195], v[196:199], 0\n\t"
                "ds_read_b64 v[224:225], %[kb] offset:96\n\t"
                "ds_read_b64 v[226:227], %[kb] offset:104\n\t"
                "ds_read_b128 v[228:231], %[ub] offset:4960\n\t"
                "s_waitcnt lgkmcnt(11)\n\t"
                "v_mfma_f32_32x32x16_bf16 %[a0], v[192:195], v[200:203], 0\n\t"
                "ds_read_b128 v[232:235], %[ub] offset:2400\n\t"
                "s_waitcnt lgkmcnt(10)\n\t"
                "v_mfma_f32_32x32x16_bf16 %[a1], v[204:207], v[200:203], %[a1]\n\t"
                "ds_read_b64 v[236:237], %[kb] offset:2144\n\t"
                "ds_read_b64 v[238:239], %[kb] offset:2152\n\t"
                "s_waitcnt lgkmcnt(9)\n\t"
                "v_mfma_f32_32x32x16_bf16 %[a1], v[208:211], v[212:215], %[a1]\n\t"
                "ds_read_b64 v[240:241], %[kb] offset:64\n\t"
                "ds_read_b64 v[242:243], %[kb] offset:72\n\t"
                "ds_read_b128 v[244:247], %[ub] offset:4992\n\t"
                "s_waitcnt lgkmcnt(11)\n\t"
                "v_mfma_f32_32x32x16_bf16 %[a0], v[208:211], v[216:219], %[a0]\n\t"
                "ds_read_b128 v[248:251], %[ub] offset:2432\n\t"
                "s_waitcnt lgkmcnt(10)\n\t"
                "v_mfma_f32_32x32x16_bf16 %[a1], v[220:223], v[216:219], %[a1]\n\t"
                "ds_read_b64 v[252:253], %[kb] offset:2112\n\t"
                "ds_read_b64 v[254:255], %[kb] offset:2120\n\t"
                "s_waitcnt lgkmcnt(9)\n\t"
                "v_mfma_f32_32x32x16_bf16 %[a1], v[224:227], v[228:231], %[a1]\n\t"
                "ds_read_b64 v[192:193], %[kb] offset:160\n\t"
                "ds_read_b64 v[194:195], %[kb] offset:168\n\t"
                "ds_read_b128 v[196:199], %[ub] offset:4880\n\t"
                "s_waitcnt lgkmcnt(11)\n\t"
                "v_mfma_f32_32x32x16_bf16 %[a0], v[224:227], v[232:235], %[a0]\n\t"
                "ds_read_b128 v[200:203], %[ub] offset:2320\n\t"
                "s_waitcnt lgkmcnt(10)\n\t"
                "v_mfma_f32_32x32x16_bf16 %[a1], v[236:239], v[232:235], %[a1]\n\t"
                "ds_read_b64 v[204:205], %[kb] offset:2208\n\t"
                "ds_read_b64 v[206:207], %[kb] offset:2216\n\t"
                "s_waitcnt lgkmcnt(9)\n\t"
                "v_mfma_f32_32x32x16_bf16 %[a1], v[240:243], v[244:247], %[a1]\n\t"
                "ds_read_b64 v[208:209], %[kb] offset:128\n\t"
                "ds_read_b64 v[210:211], %[kb] offset:136\n\t"
                "ds_read_b128 v[212:215], %[ub] offset:4912\n\t"
                "s_waitcnt lgkmcnt(11)\n\t"
                "v_mfma_f32_32x32x16_bf16 %[a0], v[240:243], v[248:251], %[a0]\n\t"
                "ds_read_b128 v[216:219], %[ub] offset:2352\n\t"
                "s_waitcnt lgkmcnt(10)\n\t"
                "v_mfma_f32_32x32x16_bf16 %[a1], v[252:255], v[248:251], %[a1]\n\t"
                "ds_read_b64 v[220:221], %[kb] offset:2176\n\t"
                "ds_read_b64 v[222:223], %[kb] offset:2184\n\t"
                "s_waitcnt lgkmcnt(9)\n\t"
                "v_mfma_f32_32x32x16_bf16 %[a1], v[192:195], v[196:199], %[a1]\n\t"
                "ds_read_b64 v[224:225], %[kb] offset:224\n\t"
                "ds_read_b64 v[226:227], %[kb] offset:232\n\t"
                "ds_read_b128 v[228:231], %[ub] offset:4800\n\t"
                "s_waitcnt lgkmcnt(11)\n\t"
                "v_mfma_f32_32x32x16_bf16 %[a0], v[192:195], v[200:203], %[a0]\n\t"
                "ds_read_b128 v[232:235], %[ub] offset:2240\n\t"
                "s_waitcnt lgkmcnt(10)\n\t"
                "v_mfma_f32_32x32x16_bf16 %[a1], v[204:207], v[200:203], %[a1]\n\t"
                "ds_read_b64 v[236:237], %[kb] offset:2272\n\t"
                "ds_read_b64 v[238:239], %[kb] offset:2280\n\t"
                "s_waitcnt lgkmcnt(9)\n\t"
                "v_mfma_f32_32x32x16_bf16 %[a1], v[208:211], v[212:215], %[a1]\n\t"
                "ds_read_b64 v[240:241], %[kb] offset:192\n\t"
                "ds_read_b64 v[242:243], %[kb] offset:200\n\t"
                "ds_read_b128 v[244:247], %[ub] offset:4832\n\t"
                "s_waitcnt lgkmcnt(11)\n\t"
                "v_mfma_f32_32x32x16_bf16 %[a0], v[208:211], v[216:219], %[a0]\n\t"
                "ds_read_b128 v[248:251], %[ub] offset:2272\n\t"
                "s_waitcnt lgkmcnt(10)\n\t"
                "v_mfma_f32_32x32x16_bf16 %[a1], v[220:223], v[216:219], %[a1]\n\t"
                "ds_read_b64 v[252:253], %[kb] offset:2240\n\t"
                "ds_read_b64 v[254:255], %[kb] offset:2248\n\t"
                "s_waitcnt lgkmcnt(9)\n\t"
                "v_mfma_f32_32x32x16_bf16 %[a1], v[224:227], v[228:231], %[a1]\n\t"
                "ds_read_b64 v[192:193], %[kb] offset:288\n\t"
                "ds_read_b64 v[194:195], %[kb] offset:296\n\t"
                "ds_read_b128 v[196:199], %[ub] offset:4720\n\t"
                "s_waitcnt lgkmcnt(11)\n\t"
                "v_mfma_f32_32x32x16_bf16 %[a0], v[224:227], v[232:235], %[a0]\n\t"
                "ds_read_b128 v[200:203], %[ub] offset:2160\n\t"
                "s_waitcnt lgkmcnt(10)\n\t"
                "v_mfma_f32_32x32x16_bf16 %[a1], v[236:239], v[232:235], %[a1]\n\t"
                "ds_read_b64 v[204:205], %[kb] offset:2336\n\t"
                "ds_read_b64 v[206:207], %[kb] offset:2344\n\t"
                "s_waitcnt lgkmcnt(9)\n\t"
                "v_mfma_f32_32x32x16_bf16 %[a1], v[240:243], v[244:247], %[a1]\n\t"
                "ds_read_b64 v[208:209], %[kb] offset:256\n\t"
                "ds_read_b64 v[210:211], %[kb] offset:264\n\t"
                "ds_read_b128 v[212:215], %[ub] offset:4752\n\t"
                "s_waitcnt lgkmcnt(11)\n\t"
                "v_mfma_f32_32x32x16_bf16 %[a0], v[240:243], v[248:251], %[a0]\n\t"
                "ds_read_b128 v[216:219], %[ub] offset:2192\n\t"
                "s_waitcnt lgkmcnt(10)\n\t"
                "v_mfma_f32_32x32x16_bf16 %[a1], v[252:255], v[248:251], %[a1]\n\t"
                "ds_read_b64 v[220:221], %[kb] offset:2304\n\t"
                "ds_read_b64 v[222:223], %[kb] offset:2312\n\t"
                "s_waitcnt lgkmcnt(9)\n\t"
                "v_mfma_f32_32x32x16_bf16 %[a1], v[192:195], v[196:199], %[a1]\n\t"
                "ds_read_b64 v[224:225], %[kb] offset:352\n\t"
                "ds_read_b64 v[226:227], %[kb] offset:360\n\t"
                "ds_read_b128 v[228:231], %[ub] offset:4640\n\t"
                "s_waitcnt lgkmcnt(11)\n\t"
                "v_mfma_f32_32x32x16_bf16 %[a0], v[192:195], v[200:203], %[a0]\n\t"
                "ds_read_b128 v[232:235], %[ub] offset:2080\n\t"
                "s_waitcnt lgkmcnt(10)\n\t"
                "v_mfma_f32_32x32x16_bf16 %[a1], v[204:207], v[200:203], %[a1]\n\t"
                "ds_read_b64 v[236:237], %[kb] offset:2400\n\t"
                "ds_read_b64 v[238:239], %[kb] offset:2408\n\t"
                "s_waitcnt lgkmcnt(9)\n\t"
                "v_mfma_f32_32x32x16_bf16 %[a1], v[208:211], v[212:215], %[a1]\n\t"
                "ds_read_b64 v[240:241], %[kb] offset:320\n\t"
                "ds_read_b64 v[242:243], %[kb] offset:328\n\t"
                "ds_read_b128 v[244:247], %[ub] offset:4672\n\t"
                "s_waitcnt lgkmcnt(11)\n\t"
                "v_mfma_f32_32x32x16_bf16 %[a0], v[208:211], v[216:219], %[a0]\n\t"
                "ds_read_b128 v[248:251], %[ub] offset:2112\n\t"
                "s_waitcnt lgkmcnt(10)\n\t"
                "v_mfma_f32_32x32x16_bf16 %[a1], v[220:223], v[216:219], %[a1]\n\t"
                "ds_read_b64 v[252:253], %[kb] offset:2368\n\t"
                "ds_read_b64 v[254:255], %[kb] offset:2376\n\t"
                "s_waitcnt lgkmcnt(9)\n\t"
                "v_mfma_f32_32x32x16_bf16 %[a1], v[224:227], v[228:231], %[a1]\n\t"
                "ds_read_b64 v[192:193], %[kb] offset:416\n\t"
                "ds_read_b64 v[194:195], %[kb] offset:424\n\t"
                "ds_read_b128 v[196:199], %[ub] offset:4560\n\t"
                "s_waitcnt lgkmcnt(11)\n\t"
                "v_mfma_f32_32x32x16_bf16 %[a0], v[224:227], v[232:235], %[a0]\n\t"
                "ds_read_b128 v[200:203], %[ub] offset:2000\n\t"
                "s_waitcnt lgkmcnt(10)\n\t"
                "v_mfma_f32_32x32x16_bf16 %[a1], v[236:239], v[232:235], %[a1]\n\t"
                "ds_read_b64 v[204:205], %[kb] offset:2464\n\t"
                "ds_read_b64 v[206:207], %[kb] offset:2472\n\t"
                "s_waitcnt lgkmcnt(9)\n\t"
                "v_mfma_f32_32x32x16_bf16 %[a1], v[240:243], v[244:247], %[a1]\n\t"
                "ds_read_b64 v[208:209], %[kb] offset:384\n\t"
                "ds_read_b64 v[210:211], %[kb] offset:392\n\t"
                "ds_read_b128 v[212:215], %[ub] offset:4592\n\t"
                "s_waitcnt lgkmcnt(11)\n\t"
                "v_mfma_f32_32x32x16_bf16 %[a0], v[240:243], v[248:251], %[a0]\n\t"
                "ds_read_b128 v[216:219], %[ub] offset:2032\n\t"
                "s_waitcnt lgkmcnt(10)\n\t"
                "v_mfma_f32_32x32x16_bf16 %[a1], v[252:255], v[248:251], %[a1]\n\t"
                "ds_read_b64 v[220:221], %[kb] offset:2432\n\t"
                "ds_read_b64 v[222:223], %[kb] offset:2440\n\t"
                "s_waitcnt lgkmcnt(9)\n\t"
                "v_mfma_f32_32x32x16_bf16 %[a1], v[192:195], v[196:199], %[a1]\n\t"
                "ds_read_b64 v[224:225], %[kb] offset:480\n\t"
                "ds_read_b64 v[226:227], %[kb] offset:488\n\t"
                "ds_read_b128 v[228:231], %[ub] offset:4480\n\t"
                "s_waitcnt lgkmcnt(11)\n\t"
                "v_mfma_f32_32x32x16_bf16 %[a0], v[192:195], v[200:203], %[a0]\n\t"
                "ds_read_b128 v[232:235], %[ub] offset:1920\n\t"
                "s_waitcnt lgkmcnt(10)\n\t"
                "v_mfma_f32_32x32x16_bf16 %[a1], v[204:207], v[200:203], %[a1]\n\t"
                "ds_read_b64 v[236:237], %[kb] offset:2528\n\t"
                "ds_read_b64 v[238:239], %[kb] offset:2536\n\t"
                "s_waitcnt lgkmcnt(9)\n\t"
                "v_mfma_f32_32x32x16_bf16 %[a1], v[208:211], v[212:215], %[a1]\n\t"
                "ds_read_b64 v[240:241], %[kb] offset:448\n\t"
                "ds_read_b64 v[242:243], %[kb] offset:456\n\t"
                "ds_read_b128 v[244:247], %[ub] offset:4512\n\t"
                "s_waitcnt lgkmcnt(11)\n\t"
                "v_mfma_f32_32x32x16_bf16 %[a0], v[208:211], v[216:219], %[a0]\n\t"
                "ds_read_b128 v[248:251], %[ub] offset:1952\n\t"
                "s_waitcnt lgkmcnt(10)\n\t"
                "v_mfma_f32_32x32x16_bf16 %[a1], v[220:223], v[216:219], %[a1]\n\t"
                "ds_read_b64 v[252:253], %[kb] offset:2496\n\t"
                "ds_read_b64 v[254:255], %[kb] offset:2504\n\t"
                "s_waitcnt lgkmcnt(9)\n\t"
                "v_mfma_f32_32x32x16_bf16 %[a1], v[224:227], v[228:231], %[a1]\n\t"
                "ds_read_b64 v[192:193], %[kb] offset:544\n\t"
                "ds_read_b64 v[194:195], %[kb] offset:552\n\t"
                "ds_read_b128 v[196:199], %[ub] offset:4400\n\t"
                "s_waitcnt lgkmcnt(11)\n\t"
                "v_mfma_f32_32x32x16_bf16 %[a0], v[224:227], v[232:235], %[a0]\n\t"
                "ds_read_b128 v[200:203], %[ub] offset:1840\n\t"
                "s_waitcnt lgkmcnt(10)\n\t"
                "v_mfma_f32_32x32x16_bf16 %[a1], v[236:239], v[232:235], %[a1]\n\t"
                "ds_read_b64 v[204:205], %[kb] offset:2592\n\t"
                "ds_read_b64 v[206:207], %[kb] offset:2600\n\t"
                "s_waitcnt lgkmcnt(9)\n\t"
                "v_mfma_f32_32x32x16_bf16 %[a1], v[240:243], v[244:247], %[a1]\n\t"
                "ds_read_b64 v[208:209], %[kb] offset:512\n\t"
                "ds_read_b64 v[210:211], %[kb] offset:520\n\t"
                "ds_read_b128 v[212:215], %[ub] offset:4432\n\t"
                "s_waitcnt lgkmcnt(11)\n\t"
                "v_mfma_f32_32x32x16_bf16 %[a0], v[240:243], v[248:251], %[a0]\n\t"
                "ds_read_b128 v[216:219], %[ub] offset:1872\n\t"
                "s_waitcnt lgkmcnt(10)\n\t"
                "v_mfma_f32_32x32x16_bf16 %[a1], v[252:255], v[248:251], %[a1]\n\t"
                "ds_read_b64 v[220:221], %[kb] offset:2560\n\t"
                "ds_read_b64 v[222:223], %[kb] offset:2568\n\t"
                "s_waitcnt lgkmcnt(9)\n\t"
                "v_mfma_f32_32x32x16_bf16 %[a1], v[192:195], v[196:199], %[a1]\n\t"
                "ds_read_b64 v[224:225], %[kb] offset:608\n\t"
                "ds_read_b64 v[226:227], %[kb] offset:616\n\t"
                "ds_read_b128 v[228:231], %[ub] offset:4320\n\t"
                "s_waitcnt lgkmcnt(11)\n\t"
                "v_mfma_f32_32x32x16_bf16 %[a0], v[192:195], v[200:203], %[a0]\n\t"
                "ds_read_b128 v[232:235], %[ub] offset:1760\n\t"
                "s_waitcnt lgkmcnt(10)\n\t"
                "v_mfma_f32_32x32x16_bf16 %[a1], v[204:207], v[200:203], %[a1]\n\t"
                "ds_read_b64 v[236:237], %[kb] offset:2656\n\t"
                "ds_read_b64 v[238:239], %[kb] offset:2664\n\t"
                "s_waitcnt lgkmcnt(9)\n\t"
                "v_mfma_f32_32x32x16_bf16 %[a1], v[208:211], v[212:215], %[a1]\n\t"
                "ds_read_b64 v[240:241], %[kb] offset:576\n\t"
                "ds_read_b64 v[242:243], %[kb] offset:584\n\t"
                "ds_read_b128 v[244:247], %[ub] offset:4352\n\t"
                "s_waitcnt lgkmcnt(11)\n\t"
                "v_mfma_f32_32x32x16_bf16 %[a0], v[208:211], v[216:219], %[a0]\n\t"
                "ds_read_b128 v[248:251], %[ub] offset:1792\n\t"
                "s_waitcnt lgkmcnt(10)\n\t"
                "v_mfma_f32_32x32x16_bf16 %[a1], v[220:223], v[216:219], %[a1]\n\t"
                "ds_read_b64 v[252:253], %[kb] offset:2624\n\t"
                "ds_read_b64 v[254:255], %[kb] offset:2632\n\t"
                "s_waitcnt lgkmcnt(9)\n\t"
                "v_mfma_f32_32x32x16_bf16 %[a1], v[224:227], v[228:231], %[a1]\n\t"
                "ds_read_b64 v[192:193], %[kb] offset:672\n\t"
                "ds_read_b64 v[194:195], %[kb] offset:680\n\t"
                "ds_read_b128 v[196:199], %[ub] offset:4240\n\t"
                "s_waitcnt lgkmcnt(11)\n\t"
                "v_mfma_f32_32x32x16_bf16 %[a0], v[224:227], v[232:235], %[a0]\n\t"
                "ds_read_b128 v[200:203], %[ub] offset:1680\n\t"
                "s_waitcnt lgkmcnt(10)\n\t"
                "v_mfma_f32_32x32x16_bf16 %[a1], v[236:239], v[232:235], %[a1]\n\t"
                "ds_read_b64 v[204:205], %[kb] offset:2720\n\t"
                "ds_read_b64 v[206:207], %[kb] offset:2728\n\t"
                "s_waitcnt lgkmcnt(9)\n\t"
                "v_mfma_f32_32x32x16_bf16 %[a1], v[240:243], v[244:247], %[a1]\n\t"
                "ds_read_b64 v[208:209], %[kb] offset:640\n\t"
                "ds_read_b64 v[210:211], %[kb] offset:648\n\t"
                "ds_read_b128 v[212:215], %[ub] offset:4272\n\t"
                "s_waitcnt lgkmcnt(11)\n\t"
                "v_mfma_f32_32x32x16_bf16 %[a0], v[240:243], v[248:251], %[a0]\n\t"
                "ds_read_b128 v[216:219], %[ub] offset:1712\n\t"
                "s_waitcnt lgkmcnt(10)\n\t"
                "v_mfma_f32_32x32x16_bf16 %[a1], v[252:255], v[248:251], %[a1]\n\t"
                "ds_read_b64 v[220:221], %[kb] offset:2688\n\t"
                "ds_read_b64 v[222:223], %[kb] offset:2696\n\t"
                "s_waitcnt lgkmcnt(9)\n\t"
                "v_mfma_f32_32x32x16_bf16 %[a1], v[192:195], v[196:199], %[a1]\n\t"
                "ds_read_b64 v[224:225], %[kb] offset:736\n\t"
                "ds_read_b64 v[226:227], %[kb] offset:744\n\t"
                "ds_read_b128 v[228:231], %[ub] offset:4160\n\t"
                "s_waitcnt lgkmcnt(11)\n\t"
                "v_mfma_f32_32x32x16_bf16 %[a0], v[192:195], v[200:203], %[a0]\n\t"
                "ds_read_b128 v[232:235], %[ub] offset:1600\n\t"
                "s_waitcnt lgkmcnt(10)\n\t"
                "v_mfma_f32_32x32x16_bf16 %[a1], v[204:207], v[200:203], %[a1]\n\t"
                "ds_read_b64 v[236:237], %[kb] offset:2784\n\t"
                "ds_read_b64 v[238:239], %[kb] offset:2792\n\t"
                "s_waitcnt lgkmcnt(9)\n\t"
                "v_mfma_f32_32x32x16_bf16 %[a1], v[208:211], v[212:215], %[a1]\n\t"
                "ds_read_b64 v[240:241], %[kb] offset:704\n\t"
                "ds_read_b64 v[242:243], %[kb] offset:712\n\t"
                "ds_read_b128 v[244:247], %[ub] offset:4192\n\t"
                "s_waitcnt lgkmcnt(11)\n\t"
                "v_mfma_f32_32x32x16_bf16 %[a0], v[208:211], v[216:219], %[a0]\n\t"
                "ds_read_b128 v[248:251], %[ub] offset:1632\n\t"
                "s_waitcnt lgkmcnt(10)\n\t"
                "v_mfma_f32_32x32x16_bf16 %[a1], v[220:223], v[216:219], %[a1]\n\t"
                "ds_read_b64 v[252:253], %[kb] offset:2752\n\t"
                "ds_read_b64 v[254:255], %[kb] offset:2760\n\t"
                "s_waitcnt lgkmcnt(9)\n\t"
                "v_mfma_f32_32x32x16_bf16 %[a1], v[224:227], v[228:231], %[a1]\n\t"
                "ds_read_b64 v[192:193], %[kb] offset:800\n\t"
                "ds_read_b64 v[194:195], %[kb] offset:808\n\t"
                "ds_read_b128 v[196:199], %[ub] offset:4080\n\t"
                "s_waitcnt lgkmcnt(11)\n\t"
                "v_mfma_f32_32x32x16_bf16 %[a0], v[224:227], v[232:235], %[a0]\n\t"
                "ds_read_b128 v[200:203], %[ub] offset:1520\n\t"
                "s_waitcnt lgkmcnt(10)\n\t"
                "v_mfma_f32_32x32x16_bf16 %[a1], v[236:239], v[232:235], %[a1]\n\t"
                "ds_read_b64 v[204:205], %[kb] offset:2848\n\t"
                "ds_read_b64 v[206:207], %[kb] offset:2856\n\t"
                "s_waitcnt lgkmcnt(9)\n\t"
                "v_mfma_f32_32x32x16_bf16 %[a1], v[240:243], v[244:247], %[a1]\n\t"
                "ds_read_b64 v[208:209], %[kb] offset:768\n\t"
                "ds_read_b64 v[210:211], %[kb] offset:776\n\t"
                "ds_read_b128 v[212:215], %[ub] offset:4112\n\t"
                "s_waitcnt lgkmcnt(11)\n\t"
                "v_mfma_f32_32x32x16_bf16 %[a0], v[240:243], v[248:251], %[a0]\n\t"
                "ds_read_b128 v[216:219], %[ub] offset:1552\n\t"
                "s_waitcnt lgkmcnt(10)\n\t"
                "v_mfma_f32_32x32x16_bf16 %[a1], v[252:255], v[248:251], %[a1]\n\t"
                "ds_read_b64 v[220:221], %[kb] offset:2816\n\t"
                "ds_read_b64 v[222:223], %[kb] offset:2824\n\t"
                "s_waitcnt lgkmcnt(9)\n\t"
                "v_mfma_f32_32x32x16_bf16 %[a1], v[192:195], v[196:199], %[a1]\n\t"
                "ds_read_b64 v[224:225], %[kb] offset:864\n\t"
                "ds_read_b64 v[226:227], %[kb] offset:872\n\t"
                "ds_read_b128 v[228:231], %[ub] offset:4000\n\t"
                "s_waitcnt lgkmcnt(11)\n\t"
                "v_mfma_f32_32x32x16_bf16 %[a0], v[192:195], v[200:203], %[a0]\n\t"
                "ds_read_b128 v[232:235], %[ub] offset:1440\n\t"
                "s_waitcnt lgkmcnt(10)\n\t"
                "v_mfma_f32_32x32x16_bf16 %[a1], v[204:207], v[200:203], %[a1]\n\t"
                "ds_read_b64 v[236:237], %[kb] offset:2912\n\t"
                "ds_read_b64 v[238:239], %[kb] offset:2920\n\t"
                "s_waitcnt lgkmcnt(9)\n\t"
                "v_mfma_f32_32x32x16_bf16 %[a1], v[208:211], v[212:215], %[a1]\n\t"
                "ds_read_b64 v[240:241], %[kb] offset:832\n\t"
                "ds_read_b64 v[242:243], %[kb] offset:840\n\t"
                "ds_read_b128 v[244:247], %[ub] offset:4032\n\t"
                "s_waitcnt lgkmcnt(11)\n\t"
                "v_mfma_f32_32x32x16_bf16 %[a0], v[208:211], v[216:219], %[a0]\n\t"
                "ds_read_b128 v[248:251], %[ub] offset:1472\n\t"
                "s_waitcnt lgkmcnt(10)\n\t"
                "v_mfma_f32_32x32x16_bf16 %[a1], v[220:223], v[216:219], %[a1]\n\t"
                "ds_read_b64 v[252:253], %[kb] offset:2880\n\t"
                "ds_read_b64 v[254:255], %[kb] offset:2888\n\t"
                "s_waitcnt lgkmcnt(9)\n\t"
                "v_mfma_f32_32x32x16_bf16 %[a1], v[224:227], v[228:231], %[a1]\n\t"
                "ds_read_b64 v[192:193], %[kb] offset:928\n\t"
                "ds_read_b64 v[194:195], %[kb] offset:936\n\t"
                "ds_read_b128 v[196:199], %[ub] offset:3920\n\t"
                "s_waitcnt lgkmcnt(11)\n\t"
                "v_mfma_f32_32x32x16_bf16 %[a0], v[224:227], v[232:235], %[a0]\n\t"
                "ds_read_b128 v[200:203], %[ub] offset:1360\n\t"
                "s_waitcnt lgkmcnt(10)\n\t"
                "v_mfma_f32_32x32x16_bf16 %[a1], v[236:239], v[232:235], %[a1]\n\t"
                "ds_read_b64 v[204:205], %[kb] offset:2976\n\t"
                "ds_read_b64 v[206:207], %[kb] offset:2984\n\t"
                "s_waitcnt lgkmcnt(9)\n\t"
                "v_mfma_f32_32x32x16_bf16 %[a1], v[240:243], v[244:247], %[a1]\n\t"
                "ds_read_b64 v[208:209], %[kb] offset:896\n\t"
                "ds_read_b64 v[210:211], %[kb] offset:904\n\t"
                "ds_read_b128 v[212:215], %[ub] offset:3952\n\t"
                "s_waitcnt lgkmcnt(11)\n\t"
                "v_mfma_f32_32x32x16_bf16 %[a0], v[240:243], v[248:251], %[a0]\n\t"
                "ds_read_b128 v[216:219], %[ub] offset:1392\n\t"
                "s_waitcnt lgkmcnt(10)\n\t"
                "v_mfma_f32_32x32x16_bf16 %[a1], v[252:255], v[248:251], %[a1]\n\t"
                "ds_read_b64 v[220:221], %[kb] offset:2944\n\t"
                "ds_read_b64 v[222:223], %[kb] offset:2952\n\t"
                "s_waitcnt lgkmcnt(9)\n\t"
                "v_mfma_f32_32x32x16_bf16 %[a1], v[192:195], v[196:199], %[a1]\n\t"
                "ds_read_b64 v[224:225], %[kb] offset:992\n\t"
                "ds_read_b64 v[226:227], %[kb] offset:1000\n\t"
                "ds_read_b128 v[228:231], %[ub] offset:3840\n\t"
                "s_waitcnt lgkmcnt(11)\n\t"
                "v_mfma_f32_32x32x16_bf16 %[a0], v[192:195], v[200:203], %[a0]\n\t"
                "ds_read_b128 v[232:235], %[ub] offset:1280\n\t"
                "s_waitcnt lgkmcnt(10)\n\t"
                "v_mfma_f32_32x32x16_bf16 %[a1], v[204:207], v[200:203], %[a1]\n\t"
                "ds_read_b64 v[236:237], %[kb] offset:3040\n\t"
                "ds_read_b64 v[238:239], %[kb] offset:3048\n\t"
                "s_waitcnt lgkmcnt(9)\n\t"
                "v_mfma_f32_32x32x16_bf16 %[a1], v[208:211], v[212:215], %[a1]\n\t"
                "ds_read_b64 v[240:241], %[kb] offset:960\n\t"
                "ds_read_b64 v[242:243], %[kb] offset:968\n\t"
                "ds_read_b128 v[244:247], %[ub] offset:3872\n\t"
                "s_waitcnt lgkmcnt(11)\n\t"
                "v_mfma_f32_32x32x16_bf16 %[a0], v[208:211], v[216:219], %[a0]\n\t"
                "ds_read_b128 v[248:251], %[ub] offset:1312\n\t"
                "s_waitcnt lgkmcnt(10)\n\t"
                "v_mfma_f32_32x32x16_bf16 %[a1], v[220:223], v[216:219], %[a1]\n\t"
                "ds_read_b64 v[252:253], %[kb] offset:3008\n\t"
                "ds_read_b64 v[254:255], %[kb] offset:3016\n\t"
                "s_waitcnt lgkmcnt(9)\n\t"
                "v_mfma_f32_32x32x16_bf16 %[a1], v[224:227], v[228:231], %[a1]\n\t"
                "ds_read_b64 v[192:193], %[kb] offset:1056\n\t"
                "ds_read_b64 v[194:195], %[kb] offset:1064\n\t"
                "ds_read_b128 v[196:199], %[ub] offset:3760\n\t"
                "s_waitcnt lgkmcnt(11)\n\t"
                "v_mfma_f32_32x32x16_bf16 %[a0], v[224:227], v[232:235], %[a0]\n\t"
                "ds_read_b128 v[200:203], %[ub] offset:1200\n\t"
                "s_waitcnt lgkmcnt(10)\n\t"
                "v_mfma_f32_32x32x16_bf16 %[a1], v[236:239], v[232:235], %[a1]\n\t"
                "ds_read_b64 v[204:205], %[kb] offset:3104\n\t"
                "ds_read_b64 v[206:207], %[kb] offset:3112\n\t"
                "s_waitcnt lgkmcnt(9)\n\t"
                "v_mfma_f32_32x32x16_bf16 %[a1], v[240:243], v[244:247], %[a1]\n\t"
                "ds_read_b64 v[208:209], %[kb] offset:1024\n\t"
                "ds_read_b64 v[210:211], %[kb] offset:1032\n\t"
                "ds_read_b128 v[212:215], %[ub] offset:3792\n\t"
                "s_waitcnt lgkmcnt(11)\n\t"
                "v_mfma_f32_32x32x16_bf16 %[a0], v[240:243], v[248:251], %[a0]\n\t"
                "ds_read_b128 v[216:219], %[ub] offset:1232\n\t"
                "s_waitcnt lgkmcnt(10)\n\t"
                "v_mfma_f32_32x32x16_bf16 %[a1], v[252:255], v[248:251], %[a1]\n\t"
                "ds_read_b64 v[220:221], %[kb] offset:3072\n\t"
                "ds_read_b64 v[222:223], %[kb] offset:3080\n\t"
                "s_waitcnt lgkmcnt(9)\n\t"
                "v_mfma_f32_32x32x16_bf16 %[a1], v[192:195], v[196:199], %[a1]\n\t"
                "ds_read_b64 v[224:225], %[kb] offset:1120\n\t"
                "ds_read_b64 v[226:227], %[kb] offset:1128\n\t"
                "ds_read_b128 v[228:231], %[ub] offset:3680\n\t"
                "s_waitcnt lgkmcnt(11)\n\t"
                "v_mfma_f32_32x32x16_bf16 %[a0], v[192:195], v[200:203], %[a0]\n\t"
                "ds_read_b128 v[232:235], %[ub] offset:1120\n\t"
                "s_waitcnt lgkmcnt(10)\n\t"
                "v_mfma_f32_32x32x16_bf16 %[a1], v[204:207], v[200:203], %[a1]\n\t"
                "ds_read_b64 v[236:237], %[kb] offset:3168\n\t"
                "ds_read_b64 v[238:239], %[kb] offset:3176\n\t"
                "s_waitcnt lgkmcnt(9)\n\t"
                "v_mfma_f32_32x32x16_bf16 %[a1], v[208:211], v[212:215], %[a1]\n\t"
                "ds_read_b64 v[240:241], %[kb] offset:1088\n\t"
                "ds_read_b64 v[242:243], %[kb] offset:1096\n\t"
                "ds_read_b128 v[244:247], %[ub] offset:3712\n\t"
                "s_waitcnt lgkmcnt(11)\n\t"
                "v_mfma_f32_32x32x16_bf16 %[a0], v[208:211], v[216:219], %[a0]\n\t"
                "ds_read_b128 v[248:251], %[ub] offset:1152\n\t"
                "s_waitcnt lgkmcnt(10)\n\t"
                "v_mfma_f32_32x32x16_bf16 %[a1], v[220:223], v[216:219], %[a1]\n\t"
                "ds_read_b64 v[252:253], %[kb] offset:3136\n\t"
                "ds_read_b64 v[254:255], %[kb] offset:3144\n\t"
                "s_waitcnt lgkmcnt(9)\n\t"
                "v_mfma_f32_32x32x16_bf16 %[a1], v[224:227], v[228:231], %[a1]\n\t"
                "ds_read_b64 v[192:193], %[kb] offset:1184\n\t"
                "ds_read_b64 v[194:195], %[kb] offset:1192\n\t"
                "ds_read_b128 v[196:199], %[ub] offset:3600\n\t"
                "s_waitcnt lgkmcnt(11)\n\t"
                "v_mfma_f32_32x32x16_bf16 %[a0], v[224:227], v[232:235], %[a0]\n\t"
                "ds_read_b128 v[200:203], %[ub] offset:1040\n\t"
                "s_waitcnt lgkmcnt(10)\n\t"
                "v_mfma_f32_32x32x16_bf16 %[a1], v[236:239], v[232:235], %[a1]\n\t"
                "ds_read_b64 v[204:205], %[kb] offset:3232\n\t"
                "ds_read_b64 v[206:207], %[kb] offset:3240\n\t"
                "s_waitcnt lgkmcnt(9)\n\t"
                "v_mfma_f32_32x32x16_bf16 %[a1], v[240:243], v[244:247], %[a1]\n\t"
                "ds_read_b64 v[208:209], %[kb] offset:1152\n\t"
                "ds_read_b64 v[210:211], %[kb] offset:1160\n\t"
                "ds_read_b128 v[212:215], %[ub] offset:3632\n\t"
                "s_waitcnt lgkmcnt(11)\n\t"
                "v_mfma_f32_32x32x16_bf16 %[a0], v[240:243], v[248:251], %[a0]\n\t"
                "ds_read_b128 v[216:219], %[ub] offset:1072\n\t"
                "s_waitcnt lgkmcnt(10)\n\t"
                "v_mfma_f32_32x32x16_bf16 %[a1], v[252:255], v[248:251], %[a1]\n\t"
                "ds_read_b64 v[220:221], %[kb] offset:3200\n\t"
                "ds_read_b64 v[222:223], %[kb] offset:3208\n\t"
                "s_waitcnt lgkmcnt(9)\n\t"
                "v_mfma_f32_32x32x16_bf16 %[a1], v[192:195], v[196:199], %[a1]\n\t"
                "ds_read_b64 v[224:225], %[kb] offset:1248\n\t"
                "ds_read_b64 v[226:227], %[kb] offset:1256\n\t"
                "ds_read_b128 v[228:231], %[ub] offset:3520\n\t"
                "s_waitcnt lgkmcnt(11)\n\t"
                "v_mfma_f32_32x32x16_bf16 %[a0], v[192:195], v[200:203], %[a0]\n\t"
                "ds_read_b128 v[232:235], %[ub] offset:960\n\t"
                "s_waitcnt lgkmcnt(10)\n\t"
                "v_mfma_f32_32x32x16_bf16 %[a1], v[204:207], v[200:203], %[a1]\n\t"
                "ds_read_b64 v[236:237], %[kb] offset:3296\n\t"
                "ds_read_b64 v[238:239], %[kb] offset:3304\n\t"
                "s_waitcnt lgkmcnt(9)\n\t"
                "v_mfma_f32_32x32x16_bf16 %[a1], v[208:211], v[212:215], %[a1]\n\t"
                "ds_read_b64 v[240:241], %[kb] offset:1216\n\t"
                "ds_read_b64 v[242:243], %[kb] offset:1224\n\t"
                "ds_read_b128 v[244:247], %[ub] offset:3552\n\t"
                "s_waitcnt lgkmcnt(11)\n\t"
                "v_mfma_f32_32x32x16_bf16 %[a0], v[208:211], v[216:219], %[a0]\n\t"
                "ds_read_b128 v[248:251], %[ub] offset:992\n\t"
                "s_waitcnt lgkmcnt(10)\n\t"
                "v_mfma_f32_32x32x16_bf16 %[a1], v[220:223], v[216:219], %[a1]\n\t"
                "ds_read_b64 v[252:253], %[kb] offset:3264\n\t"
                "ds_read_b64 v[254:255], %[kb] offset:3272\n\t"
                "s_waitcnt lgkmcnt(9)\n\t"
                "v_mfma_f32_32x32x16_bf16 %[a1], v[224:227], v[228:231], %[a1]\n\t"
                "ds_read_b64 v[192:193], %[kb] offset:1312\n\t"
                "ds_read_b64 v[194:195], %[kb] offset:1320\n\t"
                "ds_read_b128 v[196:199], %[ub] offset:3440\n\t"
                "s_waitcnt lgkmcnt(11)\n\t"
                "v_mfma_f32_32x32x16_bf16 %[a0], v[224:227], v[232:235], %[a0]\n\t"
                "ds_read_b128 v[200:203], %[ub] offset:880\n\t"
                "s_waitcnt lgkmcnt(10)\n\t"
                "v_mfma_f32_32x32x16_bf16 %[a1], v[236:239], v[232:235], %[a1]\n\t"
                "ds_read_b64 v[204:205], %[kb] offset:3360\n\t"
                "ds_read_b64 v[206:207], %[kb] offset:3368\n\t"
                "s_waitcnt lgkmcnt(9)\n\t"
                "v_mfma_f32_32x32x16_bf16 %[a1], v[240:243], v[244:247], %[a1]\n\t"
                "ds_read_b64 v[208:209], %[kb] offset:1280\n\t"
                "ds_read_b64 v[210:211], %[kb] offset:1288\n\t"
                "ds_read_b128 v[212:215], %[ub] offset:3472\n\t"
                "s_waitcnt lgkmcnt(11)\n\t"
                "v_mfma_f32_32x32x16_bf16 %[a0], v[240:243], v[248:251], %[a0]\n\t"
                "ds_read_b128 v[216:219], %[ub] offset:912\n\t"
                "s_waitcnt lgkmcnt(10)\n\t"
                "v_mfma_f32_32x32x16_bf16 %[a1], v[252:255], v[248:251], %[a1]\n\t"
                "ds_read_b64 v[220:221], %[kb] offset:3328\n\t"
                "ds_read_b64 v[222:223], %[kb] offset:3336\n\t"
                "s_waitcnt lgkmcnt(9)\n\t"
                "v_mfma_f32_32x32x16_bf16 %[a1], v[192:195], v[196:199], %[a1]\n\t"
                "ds_read_b64 v[224:225], %[kb] offset:1376\n\t"
                "ds_read_b64 v[226:227], %[kb] offset:1384\n\t"
                "ds_read_b128 v[228:231], %[ub] offset:3360\n\t"
                "s_waitcnt lgkmcnt(11)\n\t"
                "v_mfma_f32_32x32x16_bf16 %[a0], v[192:195], v[200:203], %[a0]\n\t"
                "ds_read_b128 v[232:235], %[ub] offset:800\n\t"
                "s_waitcnt lgkmcnt(10)\n\t"
                "v_mfma_f32_32x32x16_bf16 %[a1], v[204:207], v[200:203], %[a1]\n\t"
                "ds_read_b64 v[236:237], %[kb] offset:3424\n\t"
                "ds_read_b64 v[238:239], %[kb] offset:3432\n\t"
                "s_waitcnt lgkmcnt(9)\n\t"
                "v_mfma_f32_32x32x16_bf16 %[a1], v[208:211], v[212:215], %[a1]\n\t"
                "ds_read_b64 v[240:241], %[kb] offset:1344\n\t"
                "ds_read_b64 v[242:243], %[kb] offset:1352\n\t"
                "ds_read_b128 v[244:247], %[ub] offset:3392\n\t"
                "s_waitcnt lgkmcnt(11)\n\t"
                "v_mfma_f32_32x32x16_bf16 %[a0], v[208:211], v[216:219], %[a0]\n\t"
                "ds_read_b128 v[248:251], %[ub] offset:832\n\t"
                "s_waitcnt lgkmcnt(10)\n\t"
                "v_mfma_f32_32x32x16_bf16 %[a1], v[220:223], v[216:219], %[a1]\n\t"
                "ds_read_b64 v[252:253], %[kb] offset:3392\n\t"
                "ds_read_b64 v[254:255], %[kb] offset:3400\n\t"
                "s_waitcnt lgkmcnt(9)\n\t"
                "v_mfma_f32_32x32x16_bf16 %[a1], v[224:227], v[228:231], %[a1]\n\t"
                "ds_read_b64 v[192:193], %[kb] offset:1440\n\t"
                "ds_read_b64 v[194:195], %[kb] offset:1448\n\t"
                "ds_read_b128 v[196:199], %[ub] offset:3280\n\t"
                "s_waitcnt lgkmcnt(11)\n\t"
                "v_mfma_f32_32x32x16_bf16 %[a0], v[224:227], v[232:235], %[a0]\n\t"
                "ds_read_b128 v[200:203], %[ub] offset:720\n\t"
                "s_waitcnt lgkmcnt(10)\n\t"
                "v_mfma_f32_32x32x16_bf16 %[a1], v[236:239], v[232:235], %[a1]\n\t"
                "ds_read_b64 v[204:205], %[kb] offset:3488\n\t"
                "ds_read_b64 v[206:207], %[kb] offset:3496\n\t"
                "s_waitcnt lgkmcnt(9)\n\t"
                "v_mfma_f32_32x32x16_bf16 %[a1], v[240:243], v[244:247], %[a1]\n\t"
                "ds_read_b64 v[208:209], %[kb] offset:1408\n\t"
                "ds_read_b64 v[210:211], %[kb] offset:1416\n\t"
                "ds_read_b128 v[212:215], %[ub] offset:3312\n\t"
                "s_waitcnt lgkmcnt(11)\n\t"
                "v_mfma_f32_32x32x16_bf16 %[a0], v[240:243], v[248:251], %[a0]\n\t"
                "ds_read_b128 v[216:219], %[ub] offset:752\n\t"
                "s_waitcnt lgkmcnt(10)\n\t"
                "v_mfma_f32_32x32x16_bf16 %[a1], v[252:255], v[248:251], %[a1]\n\t"
                "ds_read_b64 v[220:221], %[kb] offset:3456\n\t"
                "ds_read_b64 v[222:223], %[kb] offset:3464\n\t"
                "s_waitcnt lgkmcnt(9)\n\t"
                "v_mfma_f32_32x32x16_bf16 %[a1], v[192:195], v[196:199], %[a1]\n\t"
                "ds_read_b64 v[224:225], %[kb] offset:1504\n\t"
                "ds_read_b64 v[226:227], %[kb] offset:1512\n\t"
                "ds_read_b128 v[228:231], %[ub] offset:3200\n\t"
                "s_waitcnt lgkmcnt(11)\n\t"
                "v_mfma_f32_32x32x16_bf16 %[a0], v[192:195], v[200:203], %[a0]\n\t"
                "ds_read_b128 v[232:235], %[ub] offset:640\n\t"
                "s_waitcnt lgkmcnt(10)\n\t"
                "v_mfma_f32_32x32x16_bf16 %[a1], v[204:207], v[200:203], %[a1]\n\t"
                "ds_read_b64 v[236:237], %[kb] offset:3552\n\t"
                "ds_read_b64 v[238:239], %[kb] offset:3560\n\t"
                "s_waitcnt lgkmcnt(9)\n\t"
                "v_mfma_f32_32x32x16_bf16 %[a1], v[208:211], v[212:215], %[a1]\n\t"
                "ds_read_b64 v[240:241], %[kb] offset:1472\n\t"
                "ds_read_b64 v[242:243], %[kb] offset:1480\n\t"
                "ds_read_b128 v[244:247], %[ub] offset:3232\n\t"
                "s_waitcnt lgkmcnt(11)\n\t"
                "v_mfma_f32_32x32x16_bf16 %[a0], v[208:211], v[216:219], %[a0]\n\t"
                "ds_read_b128 v[248:251], %[ub] offset:672\n\t"
                "s_waitcnt lgkmcnt(10)\n\t"
                "v_mfma_f32_32x32x16_bf16 %[a1], v[220:223], v[216:219], %[a1]\n\t"
                "ds_read_b64 v[252:253], %[kb] offset:3520\n\t"
                "ds_read_b64 v[254:255], %[kb] offset:3528\n\t"
                "s_waitcnt lgkmcnt(9)\n\t"
                "v_mfma_f32_32x32x16_bf16 %[a1], v[224:227], v[228:231], %[a1]\n\t"
                "ds_read_b64 v[192:193], %[kb] offset:1568\n\t"
                "ds_read_b64 v[194:195], %[kb] offset:1576\n\t"
                "ds_read_b128 v[196:199], %[ub] offset:3120\n\t"
                "s_waitcnt lgkmcnt(11)\n\t"
                "v_mfma_f32_32x32x16_bf16 %[a0], v[224:227], v[232:235], %[a0]\n\t"
                "ds_read_b128 v[200:203], %[ub] offset:560\n\t"
                "s_waitcnt lgkmcnt(10)\n\t"
                "v_mfma_f32_32x32x16_bf16 %[a1], v[236:239], v[232:235], %[a1]\n\t"
                "ds_read_b64 v[204:205], %[kb] offset:3616\n\t"
                "ds_read_b64 v[206:207], %[kb] offset:3624\n\t"
                "s_waitcnt lgkmcnt(9)\n\t"
                "v_mfma_f32_32x32x16_bf16 %[a1], v[240:243], v[244:247], %[a1]\n\t"
                "ds_read_b64 v[208:209], %[kb] offset:1536\n\t"
                "ds_read_b64 v[210:211], %[kb] offset:1544\n\t"
                "ds_read_b128 v[212:215], %[ub] offset:3152\n\t"
                "s_waitcnt lgkmcnt(11)\n\t"
                "v_mfma_f32_32x32x16_bf16 %[a0], v[240:243], v[248:251], %[a0]\n\t"
                "ds_read_b128 v[216:219], %[ub] offset:592\n\t"
                "s_waitcnt lgkmcnt(10)\n\t"
                "v_mfma_f32_32x32x16_bf16 %[a1], v[252:255], v[248:251], %[a1]\n\t"
                "ds_read_b64 v[220:221], %[kb] offset:3584\n\t"
                "ds_read_b64 v[222:223], %[kb] offset:3592\n\t"
                "s_waitcnt lgkmcnt(9)\n\t"
                "v_mfma_f32_32x32x16_bf16 %[a1], v[192:195], v[196:199], %[a1]\n\t"
                "ds_read_b64 v[224:225], %[kb] offset:1632\n\t"
                "ds_read_b64 v[226:227], %[kb] offset:1640\n\t"
                "ds_read_b128 v[228:231], %[ub] offset:3040\n\t"
                "s_waitcnt lgkmcnt(11)\n\t"
                "v_mfma_f32_32x32x16_bf16 %[a0], v[192:195], v[200:203], %[a0]\n\t"
                "ds_read_b128 v[232:235], %[ub] offset:480\n\t"
                "s_waitcnt lgkmcnt(10)\n\t"
                "v_mfma_f32_32x32x16_bf16 %[a1], v[204:207], v[200:203], %[a1]\n\t"
                "ds_read_b64 v[236:237], %[kb] offset:3680\n\t"
                "ds_read_b64 v[238:239], %[kb] offset:3688\n\t"
                "s_waitcnt lgkmcnt(9)\n\t"
                "v_mfma_f32_32x32x16_bf16 %[a1], v[208:211], v[212:215], %[a1]\n\t"
                "ds_read_b64 v[240:241], %[kb] offset:1600\n\t"
                "ds_read_b64 v[242:243], %[kb] offset:1608\n\t"
                "ds_read_b128 v[244:247], %[ub] offset:3072\n\t"
                "s_waitcnt lgkmcnt(11)\n\t"
                "v_mfma_f32_32x32x16_bf16 %[a0], v[208:211], v[216:219], %[a0]\n\t"
                "ds_read_b128 v[248:251], %[ub] offset:512\n\t"
                "s_waitcnt lgkmcnt(10)\n\t"
                "v_mfma_f32_32x32x16_bf16 %[a1], v[220:223], v[216:219], %[a1]\n\t"
                "ds_read_b64 v[252:253], %[kb] offset:3648\n\t"
                "ds_read_b64 v[254:255], %[kb] offset:3656\n\t"
                "s_waitcnt lgkmcnt(9)\n\t"
                "v_mfma_f32_32x32x16_bf16 %[a1], v[224:227], v[228:231], %[a1]\n\t"
                "ds_read_b64 v[192:193], %[kb] offset:1696\n\t"
                "ds_read_b64 v[194:195], %[kb] offset:1704\n\t"
                "ds_read_b128 v[196:199], %[ub] offset:2960\n\t"
                "s_waitcnt lgkmcnt(11)\n\t"
                "v_mfma_f32_32x32x16_bf16 %[a0], v[224:227], v[232:235], %[a0]\n\t"
                "ds_read_b128 v[200:203], %[ub] offset:400\n\t"
                "s_waitcnt lgkmcnt(10)\n\t"
                "v_mfma_f32_32x32x16_bf16 %[a1], v[236:239], v[232:235], %[a1]\n\t"
                "ds_read_b64 v[204:205], %[kb] offset:3744\n\t"
                "ds_read_b64 v[206:207], %[kb] offset:3752\n\t"
                "s_waitcnt lgkmcnt(9)\n\t"
                "v_mfma_f32_32x32x16_bf16 %[a1], v[240:243], v[244:247], %[a1]\n\t"
                "ds_read_b64 v[208:209], %[kb] offset:1664\n\t"
                "ds_read_b64 v[210:211], %[kb] offset:1672\n\t"
                "ds_read_b128 v[212:215], %[ub] offset:2992\n\t"
                "s_waitcnt lgkmcnt(11)\n\t"
                "v_mfma_f32_32x32x16_bf16 %[a0], v[240:243], v[248:251], %[a0]\n\t"
                "ds_read_b128 v[216:219], %[ub] offset:432\n\t"
                "s_waitcnt lgkmcnt(10)\n\t"
                "v_mfma_f32_32x32x16_bf16 %[a1], v[252:255], v[248:251], %[a1]\n\t"
                "ds_read_b64 v[220:221], %[kb] offset:3712\n\t"
                "ds_read_b64 v[222:223], %[kb] offset:3720\n\t"
                "s_waitcnt lgkmcnt(9)\n\t"
                "v_mfma_f32_32x32x16_bf16 %[a1], v[192:195], v[196:199], %[a1]\n\t"
                "ds_read_b64 v[224:225], %[kb] offset:1760\n\t"
                "ds_read_b64 v[226:227], %[kb] offset:1768\n\t"
                "ds_read_b128 v[228:231], %[ub] offset:2880\n\t"
                "s_waitcnt lgkmcnt(11)\n\t"
                "v_mfma_f32_32x32x16_bf16 %[a0], v[192:195], v[200:203], %[a0]\n\t"
                "ds_read_b128 v[232:235], %[ub] offset:320\n\t"
                "s_waitcnt lgkmcnt(10)\n\t"
                "v_mfma_f32_32x32x16_bf16 %[a1], v[204:207], v[200:203], %[a1]\n\t"
                "ds_read_b64 v[236:237], %[kb] offset:3808\n\t"
                "ds_read_b64 v[238:239], %[kb] offset:3816\n\t"
                "s_waitcnt lgkmcnt(9)\n\t"
                "v_mfma_f32_32x32x16_bf16 %[a1], v[208:211], v[212:215], %[a1]\n\t"
                "ds_read_b64 v[240:241], %[kb] offset:1728\n\t"
                "ds_read_b64 v[242:243], %[kb] offset:1736\n\t"
                "ds_read_b128 v[244:247], %[ub] offset:2912\n\t"
                "s_waitcnt lgkmcnt(11)\n\t"
                "v_mfma_f32_32x32x16_bf16 %[a0], v[208:211], v[216:219], %[a0]\n\t"
                "ds_read_b128 v[248:251], %[ub] offset:352\n\t"
                "s_waitcnt lgkmcnt(10)\n\t"
                "v_mfma_f32_32x32x16_bf16 %[a1], v[220:223], v[216:219], %[a1]\n\t"
                "ds_read_b64 v[252:253], %[kb] offset:3776\n\t"
                "ds_read_b64 v[254:255], %[kb] offset:3784\n\t"
                "s_waitcnt lgkmcnt(9)\n\t"
                "v_mfma_f32_32x32x16_bf16 %[a1], v[224:227], v[228:231], %[a1]\n\t"
                "ds_read_b64 v[192:193], %[kb] offset:1824\n\t"
                "ds_read_b64 v[194:195], %[kb] offset:1832\n\t"
                "ds_read_b128 v[196:199], %[ub] offset:2800\n\t"
                "s_waitcnt lgkmcnt(11)\n\t"
                "v_mfma_f32_32x32x16_bf16 %[a0], v[224:227], v[232:235], %[a0]\n\t"
                "ds_read_b128 v[200:203], %[ub] offset:240\n\t"
                "s_waitcnt lgkmcnt(10)\n\t"
                "v_mfma_f32_32x32x16_bf16 %[a1], v[236:239], v[232:235], %[a1]\n\t"
                "ds_read_b64 v[204:205], %[kb] offset:3872\n\t"
                "ds_read_b64 v[206:207], %[kb] offset:3880\n\t"
                "s_waitcnt lgkmcnt(9)\n\t"
                "v_mfma_f32_32x32x16_bf16 %[a1], v[240:243], v[244:247], %[a1]\n\t"
                "ds_read_b64 v[208:209], %[kb] offset:1792\n\t"
                "ds_read_b64 v[210:211], %[kb] offset:1800\n\t"
                "ds_read_b128 v[212:215], %[ub] offset:2832\n\t"
                "s_waitcnt lgkmcnt(11)\n\t"
                "v_mfma_f32_32x32x16_bf16 %[a0], v[240:243], v[248:251], %[a0]\n\t"
                "ds_read_b128 v[216:219], %[ub] offset:272\n\t"
                "s_waitcnt lgkmcnt(10)\n\t"
                "v_mfma_f32_32x32x16_bf16 %[a1], v[252:255], v[248:251], %[a1]\n\t"
                "ds_read_b64 v[220:221], %[kb] offset:3840\n\t"
                "ds_read_b64 v[222:223], %[kb] offset:3848\n\t"
                "s_waitcnt lgkmcnt(9)\n\t"
                "v_mfma_f32_32x32x16_bf16 %[a1], v[192:195], v[196:199], %[a1]\n\t"
                "ds_read_b64 v[224:225], %[kb] offset:1888\n\t"
                "ds_read_b64 v[226:227], %[kb] offset:1896\n\t"
                "ds_read_b128 v[228:231], %[ub] offset:2720\n\t"
                "s_waitcnt lgkmcnt(11)\n\t"
                "v_mfma_f32_32x32x16_bf16 %[a0], v[192:195], v[200:203], %[a0]\n\t"
                "ds_read_b128 v[232:235], %[ub] offset:160\n\t"
                "s_waitcnt lgkmcnt(10)\n\t"
                "v_mfma_f32_32x32x16_bf16 %[a1], v[204:207], v[200:203], %[a1]\n\t"
                "ds_read_b64 v[236:237], %[kb] offset:3936\n\t"
                "ds_read_b64 v[238:239], %[kb] offset:3944\n\t"
                "s_waitcnt lgkmcnt(9)\n\t"
                "v_mfma_f32_32x32x16_bf16 %[a1], v[208:211], v[212:215], %[a1]\n\t"
                "ds_read_b64 v[240:241], %[kb] offset:1856\n\t"
                "ds_read_b64 v[242:243], %[kb] offset:1864\n\t"
                "ds_read_b128 v[244:247], %[ub] offset:2752\n\t"
                "s_waitcnt lgkmcnt(11)\n\t"
                "v_mfma_f32_32x32x16_bf16 %[a0], v[208:211], v[216:219], %[a0]\n\t"
                "ds_read_b128 v[248:251], %[ub] offset:192\n\t"
                "s_waitcnt lgkmcnt(10)\n\t"
                "v_mfma_f32_32x32x16_bf16 %[a1], v[220:223], v[216:219], %[a1]\n\t"
                "ds_read_b64 v[252:253], %[kb] offset:3904\n\t"
                "ds_read_b64 v[254:255], %[kb] offset:3912\n\t"
                "s_waitcnt lgkmcnt(9)\n\t"
                "v_mfma_f32_32x32x16_bf16 %[a1], v[224:227], v[228:231], %[a1]\n\t"
                "ds_read_b64 v[192:193], %[kb] offset:1952\n\t"
                "ds_read_b64 v[194:195], %[kb] offset:1960\n\t"
                "ds_read_b128 v[196:199], %[ub] offset:2640\n\t"
                "s_waitcnt lgkmcnt(11)\n\t"
                "v_mfma_f32_32x32x16_bf16 %[a0], v[224:227], v[232:235], %[a0]\n\t"
                "ds_read_b128 v[200:203], %[ub] offset:80\n\t"
                "s_waitcnt lgkmcnt(10)\n\t"
                "v_mfma_f32_32x32x16_bf16 %[a1], v[236:239], v[232:235], %[a1]\n\t"
                "ds_read_b64 v[204:205], %[kb] offset:4000\n\t"
                "ds_read_b64 v[206:207], %[kb] offset:4008\n\t"
                "s_waitcnt lgkmcnt(9)\n\t"
                "v_mfma_f32_32x32x16_bf16 %[a1], v[240:243], v[244:247], %[a1]\n\t"
                "ds_read_b64 v[208:209], %[kb] offset:1920\n\t"
                "ds_read_b64 v[210:211], %[kb] offset:1928\n\t"
                "ds_read_b128 v[212:215], %[ub] offset:2672\n\t"
                "s_waitcnt lgkmcnt(11)\n\t"
                "v_mfma_f32_32x32x16_bf16 %[a0], v[240:243], v[248:251], %[a0]\n\t"
                "ds_read_b128 v[216:219], %[ub] offset:112\n\t"
                "s_waitcnt lgkmcnt(10)\n\t"
                "v_mfma_f32_32x32x16_bf16 %[a1], v[252:255], v[248:251], %[a1]\n\t"
                "ds_read_b64 v[220:221], %[kb] offset:3968\n\t"
                "ds_read_b64 v[222:223], %[kb] offset:3976\n\t"
                "s_waitcnt lgkmcnt(9)\n\t"
                "v_mfma_f32_32x32x16_bf16 %[a1], v[192:195], v[196:199], %[a1]\n\t"
                "ds_read_b64 v[224:225], %[kb] offset:2016\n\t"
                "ds_read_b64 v[226:227], %[kb] offset:2024\n\t"
                "ds_read_b128 v[228:231], %[ub] offset:2560\n\t"
                "s_waitcnt lgkmcnt(11)\n\t"
                "v_mfma_f32_32x32x16_bf16 %[a0], v[192:195], v[200:203], %[a0]\n\t"
                "ds_read_b128 v[232:235], %[ub] offset:0\n\t"
                "s_waitcnt lgkmcnt(10)\n\t"
                "v_mfma_f32_32x32x16_bf16 %[a1], v[204:207], v[200:203], %[a1]\n\t"
                "ds_read_b64 v[236:237], %[kb] offset:4064\n\t"
                "ds_read_b64 v[238:239], %[kb] offset:4072\n\t"
                "s_waitcnt lgkmcnt(9)\n\t"
                "v_mfma_f32_32x32x16_bf16 %[a1], v[208:211], v[212:215], %[a1]\n\t"
                "ds_read_b64 v[240:241], %[kb] offset:1984\n\t"
                "ds_read_b64 v[242:243], %[kb] offset:1992\n\t"
                "ds_read_b128 v[244:247], %[ub] offset:2592\n\t"
                "s_waitcnt lgkmcnt(11)\n\t"
                "v_mfma_f32_32x32x16_bf16 %[a0], v[208:211], v[216:219], %[a0]\n\t"
                "ds_read_b128 v[248:251], %[ub] offset:32\n\t"
                "s_waitcnt lgkmcnt(10)\n\t"
                "v_mfma_f32_32x32x16_bf16 %[a1], v[220:223], v[216:219], %[a1]\n\t"
                "ds_read_b64 v[252:253], %[kb] offset:4032\n\t"
                "ds_read_b64 v[254:255], %[kb] offset:4040\n\t"
                "s_waitcnt lgkmcnt(9)\n\t"
                "v_mfma_f32_32x32x16_bf16 %[a1], v[224:227], v[228:231], %[a1]\n\t"
                "s_waitcnt lgkmcnt(8)\n\t"
                "v_mfma_f32_32x32x16_bf16 %[a0], v[224:227], v[232:235], %[a0]\n\t"
                "s_waitcnt lgkmcnt(6)\n\t"
                "v_mfma_f32_32x32x16_bf16 %[a1], v[236:239], v[232:235], %[a1]\n\t"
                "s_waitcnt lgkmcnt(3)\n\t"
                "v_mfma_f32_32x32x16_bf16 %[a1], v[240:243], v[244:247], %[a1]\n\t"
                "s_waitcnt lgkmcnt(2)\n\t"
                "v_mfma_f32_32x32x16_bf16 %[a0], v[240:243], v[248:251], %[a0]\n\t"
                "s_waitcnt lgkmcnt(0)\n\t"
                "v_mfma_f32_32x32x16_bf16 %[a1], v[252:255], v[248:251], %[a1]\n\t"
                "s_nop 7\n\t"
                "s_nop 3\n\t"
                : [a0] "=&v"(acc0), [a1] "=&v"(acc1) : [kb] "v"(kb), [ub] "v"(ub0 + c8 * CV_U) : "memory", "v192", "v193", "v194", "v195", "v196", "v197", "v198", "v199", "v200", "v201", "v202", "v203", "v204", "v205", "v206", "v207", "v208", "v209", "v210", "v211", "v212", "v213", "v214", "v215", "v216", "v217", "v218", "v219", "v220", "v221", "v222", "v223", "v224", "v225", "v226", "v227", "v228", "v229", "v230", "v231", "v232", "v233", "v234", "v235", "v236", "v237", "v238", "v239", "v240", "v241", "v242", "v243", "v244", "v245", "v246", "v247", "v248", "v249", "v250", "v251", "v252", "v253", "v254", "v255");
            unsigned char* uc = lds + c8 * CV_U + (32 + b) * CV_RP + kg * 8;
#pragma unroll
            for (int q = 0; q < 4; ++q) {
                *(v2u*)(uc + q * 16) = (v2u){pack2(acc0[4 * q], acc0[4 * q + 1]), pack2(acc0[4 * q + 2], acc0[4 * q + 3])};
                *(v2u*)(uc + 32 * CV_RP + q * 16) = (v2u){pack2(acc1[4 * q], acc1[4 * q + 1]), pack2(acc1[4 * q + 2], acc1[4 * q + 3])}; }
            __builtin_amdgcn_wave_barrier();
        }
    __syncthreads();
#pragma unroll
    for (int G = 0; G < 4; ++G) { const v4u lo = *(const v4u*)(urow + G * 16), hh = *(const v4u*)(urow + CV_U + G * 16); v4u o[2];
#pragma unroll
        for (int i = 0; i < 8; ++i) { const unsigned u0 = ur[G][(7 - i) >> 1], u1 = ur[4 + G][(7 - i) >> 1], qq = qr[2 * G + (i >> 2)][i & 3];
            const unsigned ud = pack2(((i & 1) ? blo(u0) : bhi(u0)) * blo(dv), ((i & 1) ? blo(u1) : bhi(u1)) * bhi(dv));
            const unsigned y = pack2(((i & 1) ? bhi(lo[i >> 1]) : blo(lo[i >> 1])) + blo(ud), ((i & 1) ? bhi(hh[i >> 1]) : blo(hh[i >> 1])) + bhi(ud));
            o[i >> 2][i & 3] = pack2(blo(qq) * blo(y), bhi(qq) * bhi(y)); }
        st16(r, W_A + un * 32768 + 2 * G * 4096 + tid * 16, o[0]); st16(r, W_A + un * 32768 + (2 * G + 1) * 4096 + tid * 16, o[1]); }
    DRAIN(); __syncthreads();
    if (tid == 0) __hip_atomic_fetch_add((unsigned*)(p.ws + F_CVY + (un >> 3) * 128), 1u, __ATOMIC_RELAXED, __HIP_MEMORY_SCOPE_AGENT);
    return true;
}

__device__ __forceinline__ void proj_block(const Params& p, unsigned char* lds, const int wv, const int h, const int c, const int j) {
    const int w = wv, l = otid() & 63, n = l & 15, g = l >> 4, tm = w >> 1, tn = w & 1;
    const unsigned lb = __builtin_amdgcn_readfirstlane((unsigned)(size_t)lds);
    const v4u sx = mk_srd(p.Aq, S * D * 2), sq = mk_srd(c < 2 ? p.Wq : p.Wk, 1024 * 4096 * 2), sv = mk_srd(p.Wv, 1024 * 4096 * 2), sw = mk_srd(p.ws, W_TOTAL);
    const unsigned lx = lb + (32 * tm + n) * 128 + ((g ^ (n & 7)) << 4), lr = lb + 131072, rr = 8 * g + (n >> 2);
    const int cv = ((c & 1) ^ 1) * 2 + (c >> 1);
    const unsigned aq = (c < 2 ? W_QX : W_KX) + (tm * 32 + h) * 8192 + (2 * (c & 1) + tn) * 1024 + l * 16;
    const int hq = h & 7, tk = ((hq - (hq > 1) - (hq > 5)) * 4 + c) * 4 + w, ts6 = tk % 6, tch = 15 - tk / 6, tx = tk - 6 * GTOUCH;
    const bf16_t* tp = (h & 3) == 1 ? p.gate_w : tx < 0 ? (ts6 % 3 == 0 ? p.Wq : ts6 % 3 == 1 ? p.Wk : p.Wv) + (size_t)(tch * 64 + l) * 4096 + ((h & 24) + 1 + (ts6 / 3) * 4) * 128 + 64
                     : tx < XTOUCH ? p.Aq + (tx * 64 + l) * 64 : p.gate_w;
    unsigned t0, xo, so, cn; unsigned long long ts, ta, tb, tc, td;
    asm volatile(
        "s_mov_b32 %[xo], 0\n\t"
        "s_mov_b32 %[so], 0\n\t"
        "s_mov_b32 %[cn], 1\n\t"
        "global_load_dword v88, v[114:115], off\n\t"
        "s_add_u32 m0, %[ld], 0\n\t"
        "s_add_u32 %[t0], %[xo], 0\n\t"
        "buffer_load_dwordx4 v100, %[sx], %[t0] offen lds\n\t"
        "s_add_u32 m0, %[ld], 1024\n\t"
        "s_add_u32 %[t0], %[xo], 16384\n\t"
        "buffer_load_dwordx4 v100, %[sx], %[t0] offen lds\n\t"
        "s_add_u32 m0, %[ld], 8192\n\t"
        "s_add_u32 %[t0], %[xo], 128\n\t"
        "buffer_load_dwordx4 v100, %[sx], %[t0] offen lds\n\t"
        "s_add_u32 m0, %[ld], 9216\n\t"
        "s_add_u32 %[t0], %[xo], 16512\n\t"
        "buffer_load_dwordx4 v100, %[sx], %[t0] offen lds\n\t"
        "s_add_u32 m0, %[ld], 16384\n\t"
        "s_add_u32 %[t0], %[xo], 256\n\t"
        "buffer_load_dwordx4 v100, %[sx], %[t0] offen lds\n\t"
        "s_add_u32 m0, %[ld], 17408\n\t"
        "s_add_u32 %[t0], %[xo], 16640\n\t"
        "buffer_load_dwordx4 v100, %[sx], %[t0] offen lds\n\t"
        "s_add_u32 m0, %[ld], 24576\n\t"
        "s_add_u32 %[t0], %[xo], 384\n\t"
        "buffer_load_dwordx4 v100, %[sx], %[t0] offen lds\n\t"
        "s_add_u32 m0, %[ld], 25600\n\t"
        "s_add_u32 %[t0], %[xo], 16768\n\t"
        "buffer_load_dwordx4 v100, %[sx], %[t0] offen lds\n\t"
        "s_mov_b32 %[t0], 0\n\t"
        "buffer_load_dwordx4 a[192:195], v101, %[sq], %[t0] offen\n\t"
        "buffer_load_dwordx4 a[200:203], v112, %[sv], %[t0] offen\n\t"
        "s_mov_b32 %[t0], 262144\n\t"
        "buffer_load_dwordx4 a[196:199], v101, %[sq], %[t0] offen\n\t"
        "s_mov_b32 %[t0], 524288\n\t"
        "buffer_load_dwordx4 a[204:207], v101, %[sq], %[t0] offen\n\t"
        "buffer_load_dwordx4 a[212:215], v112, %[sv], %[t0] offen\n\t"
        "s_mov_b32 %[t0], 786432\n\t"
        "buffer_load_dwordx4 a[208:211], v101, %[sq], %[t0] offen\n\t"
        "s_mov_b32 %[t0], 1048576\n\t"
        "buffer_load_dwordx4 a[216:219], v101, %[sq], %[t0] offen\n\t"
        "buffer_load_dwordx4 a[224:227], v112, %[sv], %[t0] offen\n\t"
        "s_mov_b32 %[t0], 1310720\n\t"
        "buffer_load_dwordx4 a[220:223], v101, %[sq], %[t0] offen\n\t"
        "s_mov_b32 %[t0], 1572864\n\t"
        "buffer_load_dwordx4 a[228:231], v101, %[sq], %[t0] offen\n\t"
        "buffer_load_dwordx4 a[236:239], v112, %[sv], %[t0] offen\n\t"
        "s_mov_b32 %[t0], 1835008\n\t"
        "buffer_load_dwordx4 a[232:235], v101, %[sq], %[t0] offen\n\t"
        "s_waitcnt vmcnt(9)\n\t"
        "ds_write_b128 v102, a[192:195] offset:0\n\t"
        "ds_write_b128 v102, a[196:199] offset:4096\n\t"
        "ds_write_b128 v116, a[200:203] offset:8192\n\t"
        "s_waitcnt lgkmcnt(0)\n\t"
        "s_barrier\n\t"
        "ds_read_b64_tr_b16 a[0:1], v103 offset:0\n\t"
        "ds_read_b64_tr_b16 a[2:3], v103 offset:512\n\t"
        "ds_read_b64_tr_b16 a[4:5], v103 offset:8\n\t"
        "ds_read_b64_tr_b16 a[6:7], v103 offset:520\n\t"
        "ds_read_b64_tr_b16 a[8:9], v104 offset:0\n\t"
        "ds_read_b64_tr_b16 a[10:11], v104 offset:256\n\t"
        "s_waitcnt lgkmcnt(5)\n\t"
        "ds_read_b64_tr_b16 a[12:13], v103 offset:4096\n\t"
        "ds_read_b64_tr_b16 a[14:15], v103 offset:4608\n\t"
        "ds_read_b64_tr_b16 a[16:17], v103 offset:4104\n\t"
        "ds_read_b64_tr_b16 a[18:19], v103 offset:4616\n\t"
        "ds_read_b64_tr_b16 a[20:21], v104 offset:2048\n\t"
        "ds_read_b64_tr_b16 a[22:23], v104 offset:2304\n\t"
        "ds_read_b128 v[56:59], v105 offset:0\n\t"
        "ds_read_b128 v[60:63], v105 offset:2048\n\t"
        "ds_read_b128 v[64:67], v106 offset:0\n\t"
        "ds_read_b128 v[68:71], v106 offset:2048\n\t"
        "s_add_u32 m0, %[ld], 32768\n\t"
        "s_add_u32 %[t0], %[xo], 512\n\t"
        "buffer_load_dwordx4 v100, %[sx], %[t0] offen lds\n\t"
        "s_add_u32 m0, %[ld], 33792\n\t"
        "s_add_u32 %[t0], %[xo], 16896\n\t"
        "buffer_load_dwordx4 v100, %[sx], %[t0] offen lds\n\t"
        "s_add_u32 m0, %[ld], 40960\n\t"
        "s_add_u32 %[t0], %[xo], 640\n\t"
        "buffer_load_dwordx4 v100, %[sx], %[t0] offen lds\n\t"
        "s_add_u32 m0, %[ld], 41984\n\t"
        "s_add_u32 %[t0], %[xo], 17024\n\t"
        "buffer_load_dwordx4 v100, %[sx], %[t0] offen lds\n\t"
        "s_add_u32 m0, %[ld], 49152\n\t"
        "s_add_u32 %[t0], %[xo], 768\n\t"
        "buffer_load_dwordx4 v100, %[sx], %[t0] offen lds\n\t"
        "s_add_u32 m0, %[ld], 50176\n\t"
        "s_add_u32 %[t0], %[xo], 17152\n\t"
        "buffer_load_dwordx4 v100, %[sx], %[t0] offen lds\n\t"
        "s_add_u32 m0, %[ld], 57344\n\t"
        "s_add_u32 %[t0], %[xo], 896\n\t"
        "buffer_load_dwordx4 v100, %[sx], %[t0] offen lds\n\t"
        "s_add_u32 m0, %[ld], 58368\n\t"
        "s_add_u32 %[t0], %[xo], 17280\n\t"
        "buffer_load_dwordx4 v100, %[sx], %[t0] offen lds\n\t"
        "s_mov_b32 %[t0], 2097152\n\t"
        "buffer_load_dwordx4 a[240:243], v101, %[sq], %[t0] offen\n\t"
        "buffer_load_dwordx4 a[248:251], v112, %[sv], %[t0] offen\n\t"
        "s_mov_b32 %[t0], 2359296\n\t"
        "buffer_load_dwordx4 a[244:247], v101, %[sq], %[t0] offen\n\t"
        "s_waitcnt vmcnt(17)\n\t"
        "ds_write_b128 v102, a[204:207] offset:16384\n\t"
        "ds_write_b128 v102, a[208:211] offset:20480\n\t"
        "ds_write_b128 v116, a[212:215] offset:24576\n\t"
        "s_waitcnt lgkmcnt(0)\n\t"
        "s_barrier\n\t"
        "v_mfma_f32_16x16x32_bf16 v[8:11], a[0:3], v[56:59], 0\n\t"
        "ds_read_b64_tr_b16 a[24:25], v103 offset:16384\n\t"
        "ds_read_b64_tr_b16 a[26:27], v103 offset:16896\n\t"
        "v_mfma_f32_16x16x32_bf16 v[12:15], a[4:7], v[56:59], 0\n\t"
        "ds_read_b64_tr_b16 a[28:29], v103 offset:16392\n\t"
        "ds_read_b64_tr_b16 a[30:31], v103 offset:16904\n\t"
        "v_mfma_f32_16x16x32_bf16 v[16:19], v[56:59], a[8:11], 0\n\t"
        "ds_read_b64_tr_b16 a[32:33], v104 offset:16384\n\t"
        "ds_read_b64_tr_b16 a[34:35], v104 offset:16640\n\t"
        "v_mfma_f32_16x16x32_bf16 v[20:23], a[0:3], v[60:63], 0\n\t"
        "s_waitcnt lgkmcnt(5)\n\t"
        "ds_read_b64_tr_b16 a[36:37], v103 offset:20480\n\t"
        "v_mfma_f32_16x16x32_bf16 v[24:27], a[4:7], v[60:63], 0\n\t"
        "ds_read_b64_tr_b16 a[38:39], v103 offset:20992\n\t"
        "ds_read_b64_tr_b16 a[40:41], v103 offset:20488\n\t"
        "v_mfma_f32_16x16x32_bf16 v[28:31], v[60:63], a[8:11], 0\n\t"
        "ds_read_b64_tr_b16 a[42:43], v103 offset:21000\n\t"
        "ds_read_b64_tr_b16 a[44:45], v104 offset:18432\n\t"
        "v_mfma_f32_16x16x32_bf16 v[8:11], a[12:15], v[64:67], v[8:11]\n\t"
        "ds_read_b64_tr_b16 a[46:47], v104 offset:18688\n\t"
        "ds_read_b128 v[72:75], v105 offset:8192\n\t"
        "v_mfma_f32_16x16x32_bf16 v[12:15], a[16:19], v[64:67], v[12:15]\n\t"
        "ds_read_b128 v[76:79], v105 offset:10240\n\t"
        "ds_read_b128 v[80:83], v106 offset:8192\n\t"
        "v_mfma_f32_16x16x32_bf16 v[16:19], v[64:67], a[20:23], v[16:19]\n\t"
        "ds_read_b128 v[84:87], v106 offset:10240\n\t"
        "v_mfma_f32_16x16x32_bf16 v[20:23], a[12:15], v[68:71], v[20:23]\n\t"
        "v_mfma_f32_16x16x32_bf16 v[24:27], a[16:19], v[68:71], v[24:27]\n\t"
        "v_mfma_f32_16x16x32_bf16 v[28:31], v[68:71], a[20:23], v[28:31]\n\t"
        "s_mov_b32 %[t0], 2621440\n\t"
        "buffer_load_dwordx4 a[252:255], v101, %[sq], %[t0] offen\n\t"
        "buffer_load_dwordx4 v[132:135], v112, %[sv], %[t0] offen\n\t"
        "s_mov_b32 %[t0], 2883584\n\t"
        "buffer_load_dwordx4 v[128:131], v101, %[sq], %[t0] offen\n\t"
        "s_waitcnt vmcnt(17)\n\t"
        "ds_write_b128 v102, a[216:219] offset:0\n\t"
        "ds_write_b128 v102, a[220:223] offset:4096\n\t"
        "ds_write_b128 v116, a[224:227] offset:8192\n\t"
        "s_waitcnt lgkmcnt(0)\n\t"
        "s_barrier\n\t"
        "v_mfma_f32_16x16x32_bf16 v[8:11], a[24:27], v[72:75], v[8:11]\n\t"
        "ds_read_b64_tr_b16 a[48:49], v103 offset:0\n\t"
        "ds_read_b64_tr_b16 a[50:51], v103 offset:512\n\t"
        "v_mfma_f32_16x16x32_bf16 v[12:15], a[28:31], v[72:75], v[12:15]\n\t"
        "ds_read_b64_tr_b16 a[52:53], v103 offset:8\n\t"
        "ds_read_b64_tr_b16 a[54:55], v103 offset:520\n\t"
        "v_mfma_f32_16x16x32_bf16 v[16:19], v[72:75], a[32:35], v[16:19]\n\t"
        "ds_read_b64_tr_b16 a[56:57], v104 offset:0\n\t"
        "ds_read_b64_tr_b16 a[58:59], v104 offset:256\n\t"
        "v_mfma_f32_16x16x32_bf16 v[20:23], a[24:27], v[76:79], v[20:23]\n\t"
        "s_waitcnt lgkmcnt(5)\n\t"
        "ds_read_b64_tr_b16 a[60:61], v103 offset:4096\n\t"
        "v_mfma_f32_16x16x32_bf16 v[24:27], a[28:31], v[76:79], v[24:27]\n\t"
        "ds_read_b64_tr_b16 a[62:63], v103 offset:4608\n\t"
        "ds_read_b64_tr_b16 a[64:65], v103 offset:4104\n\t"
        "v_mfma_f32_16x16x32_bf16 v[28:31], v[76:79], a[32:35], v[28:31]\n\t"
        "ds_read_b64_tr_b16 a[66:67], v103 offset:4616\n\t"
        "ds_read_b64_tr_b16 a[68:69], v104 offset:2048\n\t"
        "v_mfma_f32_16x16x32_bf16 v[8:11], a[36:39], v[80:83], v[8:11]\n\t"
        "ds_read_b64_tr_b16 a[70:71], v104 offset:2304\n\t"
        "ds_read_b128 v[56:59], v105 offset:16384\n\t"
        "v_mfma_f32_16x16x32_bf16 v[12:15], a[40:43], v[80:83], v[12:15]\n\t"
        "ds_read_b128 v[60:63], v105 offset:18432\n\t"
        "ds_read_b128 v[64:67], v106 offset:16384\n\t"
        "v_mfma_f32_16x16x32_bf16 v[16:19], v[80:83], a[44:47], v[16:19]\n\t"
        "ds_read_b128 v[68:71], v106 offset:18432\n\t"
        "v_mfma_f32_16x16x32_bf16 v[20:23], a[36:39], v[84:87], v[20:23]\n\t"
        "v_mfma_f32_16x16x32_bf16 v[24:27], a[40:43], v[84:87], v[24:27]\n\t"
        "v_mfma_f32_16x16x32_bf16 v[28:31], v[84:87], a[44:47], v[28:31]\n\t"
        "s_mov_b32 %[t0], 3145728\n\t"
        "buffer_load_dwordx4 v[136:139], v101, %[sq], %[t0] offen\n\t"
        "buffer_load_dwordx4 v[144:147], v112, %[sv], %[t0] offen\n\t"
        "s_mov_b32 %[t0], 3407872\n\t"
        "buffer_load_dwordx4 v[140:143], v101, %[sq], %[t0] offen\n\t"
        "s_waitcnt vmcnt(17)\n\t"
        "ds_write_b128 v102, a[228:231] offset:16384\n\t"
        "ds_write_b128 v102, a[232:235] offset:20480\n\t"
        "ds_write_b128 v116, a[236:239] offset:24576\n\t"
        "s_waitcnt lgkmcnt(0)\n\t"
        "s_barrier\n\t"
        "v_mfma_f32_16x16x32_bf16 v[8:11], a[48:51], v[56:59], v[8:11]\n\t"
        "ds_read_b64_tr_b16 a[72:73], v103 offset:16384\n\t"
        "ds_read_b64_tr_b16 a[74:75], v103 offset:16896\n\t"
        "v_mfma_f32_16x16x32_bf16 v[12:15], a[52:55], v[56:59], v[12:15]\n\t"
        "ds_read_b64_tr_b16 a[76:77], v103 offset:16392\n\t"
        "ds_read_b64_tr_b16 a[78:79], v103 offset:16904\n\t"
        "v_mfma_f32_16x16x32_bf16 v[16:19], v[56:59], a[56:59], v[16:19]\n\t"
        "ds_read_b64_tr_b16 a[80:81], v104 offset:16384\n\t"
        "ds_read_b64_tr_b16 a[82:83], v104 offset:16640\n\t"
        "v_mfma_f32_16x16x32_bf16 v[20:23], a[48:51], v[60:63], v[20:23]\n\t"
        "s_waitcnt lgkmcnt(5)\n\t"
        "ds_read_b64_tr_b16 a[84:85], v103 offset:20480\n\t"
        "v_mfma_f32_16x16x32_bf16 v[24:27], a[52:55], v[60:63], v[24:27]\n\t"
        "ds_read_b64_tr_b16 a[86:87], v103 offset:20992\n\t"
        "ds_read_b64_tr_b16 a[88:89], v103 offset:20488\n\t"
        "v_mfma_f32_16x16x32_bf16 v[28:31], v[60:63], a[56:59], v[28:31]\n\t"
        "ds_read_b64_tr_b16 a[90:91], v103 offset:21000\n\t"
        "ds_read_b64_tr_b16 a[92:93], v104 offset:18432\n\t"
        "v_mfma_f32_16x16x32_bf16 v[8:11], a[60:63], v[64:67], v[8:11]\n\t"
        "ds_read_b64_tr_b16 a[94:95], v104 offset:18688\n\t"
        "ds_read_b128 v[72:75], v105 offset:24576\n\t"
        "v_mfma_f32_16x16x32_bf16 v[12:15], a[64:67], v[64:67], v[12:15]\n\t"
        "ds_read_b128 v[76:79], v105 offset:26624\n\t"
        "ds_read_b128 v[80:83], v106 offset:24576\n\t"
        "v_mfma_f32_16x16x32_bf16 v[16:19], v[64:67], a[68:71], v[16:19]\n\t"
        "ds_read_b128 v[84:87], v106 offset:26624\n\t"
        "v_mfma_f32_16x16x32_bf16 v[20:23], a[60:63], v[68:71], v[20:23]\n\t"
        "v_mfma_f32_16x16x32_bf16 v[24:27], a[64:67], v[68:71], v[24:27]\n\t"
        "v_mfma_f32_16x16x32_bf16 v[28:31], v[68:71], a[68:71], v[28:31]\n\t"
        "s_mov_b32 %[t0], 3670016\n\t"
        "buffer_load_dwordx4 v[148:151], v101, %[sq], %[t0] offen\n\t"
        "buffer_load_dwordx4 v[156:159], v112, %[sv], %[t0] offen\n\t"
        "s_mov_b32 %[t0], 3932160\n\t"
        "buffer_load_dwordx4 v[152:155], v101, %[sq], %[t0] offen\n\t"
        "s_waitcnt vmcnt(9)\n\t"
        "ds_write_b128 v102, a[240:243] offset:0\n\t"
        "ds_write_b128 v102, a[244:247] offset:4096\n\t"
        "ds_write_b128 v116, a[248:251] offset:8192\n\t"
        "s_waitcnt lgkmcnt(0)\n\t"
        "s_barrier\n\t"
        "v_mfma_f32_16x16x32_bf16 v[8:11], a[72:75], v[72:75], v[8:11]\n\t"
        "ds_read_b64_tr_b16 a[96:97], v103 offset:0\n\t"
        "ds_read_b64_tr_b16 a[98:99], v103 offset:512\n\t"
        "v_mfma_f32_16x16x32_bf16 v[12:15], a[76:79], v[72:75], v[12:15]\n\t"
        "ds_read_b64_tr_b16 a[100:101], v103 offset:8\n\t"
        "ds_read_b64_tr_b16 a[102:103], v103 offset:520\n\t"
        "v_mfma_f32_16x16x32_bf16 v[16:19], v[72:75], a[80:83], v[16:19]\n\t"
        "ds_read_b64_tr_b16 a[104:105], v104 offset:0\n\t"
        "ds_read_b64_tr_b16 a[106:107], v104 offset:256\n\t"
        "v_mfma_f32_16x16x32_bf16 v[20:23], a[72:75], v[76:79], v[20:23]\n\t"
        "s_waitcnt lgkmcnt(5)\n\t"
        "ds_read_b64_tr_b16 a[108:109], v103 offset:4096\n\t"
        "v_mfma_f32_16x16x32_bf16 v[24:27], a[76:79], v[76:79], v[24:27]\n\t"
        "ds_read_b64_tr_b16 a[110:111], v103 offset:4608\n\t"
        "ds_read_b64_tr_b16 a[112:113], v103 offset:4104\n\t"
        "v_mfma_f32_16x16x32_bf16 v[28:31], v[76:79], a[80:83], v[28:31]\n\t"
        "ds_read_b64_tr_b16 a[114:115], v103 offset:4616\n\t"
        "ds_read_b64_tr_b16 a[116:117], v104 offset:2048\n\t"
        "v_mfma_f32_16x16x32_bf16 v[8:11], a[84:87], v[80:83], v[8:11]\n\t"
        "ds_read_b64_tr_b16 a[118:119], v104 offset:2304\n\t"
        "ds_read_b128 v[56:59], v105 offset:32768\n\t"
        "v_mfma_f32_16x16x32_bf16 v[12:15], a[88:91], v[80:83], v[12:15]\n\t"
        "ds_read_b128 v[60:63], v105 offset:34816\n\t"
        "ds_read_b128 v[64:67], v106 offset:32768\n\t"
        "v_mfma_f32_16x16x32_bf16 v[16:19], v[80:83], a[92:95], v[16:19]\n\t"
        "ds_read_b128 v[68:71], v106 offset:34816\n\t"
        "v_mfma_f32_16x16x32_bf16 v[20:23], a[84:87], v[84:87], v[20:23]\n\t"
        "v_mfma_f32_16x16x32_bf16 v[24:27], a[88:91], v[84:87], v[24:27]\n\t"
        "v_mfma_f32_16x16x32_bf16 v[28:31], v[84:87], a[92:95], v[28:31]\n\t"
        "s_add_u32 m0, %[ld], 65536\n\t"
        "s_add_u32 %[t0], %[xo], 1024\n\t"
        "buffer_load_dwordx4 v100, %[sx], %[t0] offen lds\n\t"
        "s_add_u32 m0, %[ld], 66560\n\t"
        "s_add_u32 %[t0], %[xo], 17408\n\t"
        "buffer_load_dwordx4 v100, %[sx], %[t0] offen lds\n\t"
        "s_add_u32 m0, %[ld], 73728\n\t"
        "s_add_u32 %[t0], %[xo], 1152\n\t"
        "buffer_load_dwordx4 v100, %[sx], %[t0] offen lds\n\t"
        "s_add_u32 m0, %[ld], 74752\n\t"
        "s_add_u32 %[t0], %[xo], 17536\n\t"
        "buffer_load_dwordx4 v100, %[sx], %[t0] offen lds\n\t"
        "s_add_u32 m0, %[ld], 81920\n\t"
        "s_add_u32 %[t0], %[xo], 1280\n\t"
        "buffer_load_dwordx4 v100, %[sx], %[t0] offen lds\n\t"
        "s_add_u32 m0, %[ld], 82944\n\t"
        "s_add_u32 %[t0], %[xo], 17664\n\t"
        "buffer_load_dwordx4 v100, %[sx], %[t0] offen lds\n\t"
        "s_add_u32 m0, %[ld], 90112\n\t"
        "s_add_u32 %[t0], %[xo], 1408\n\t"
        "buffer_load_dwordx4 v100, %[sx], %[t0] offen lds\n\t"
        "s_add_u32 m0, %[ld], 91136\n\t"
        "s_add_u32 %[t0], %[xo], 17792\n\t"
        "buffer_load_dwordx4 v100, %[sx], %[t0] offen lds\n\t"
        "s_mov_b32 %[t0], 4194304\n\t"
        "buffer_load_dwordx4 v[160:163], v101, %[sq], %[t0] offen\n\t"
        "buffer_load_dwordx4 v[168:171], v112, %[sv], %[t0] offen\n\t"
        "s_mov_b32 %[t0], 4456448\n\t"
        "buffer_load_dwordx4 v[164:167], v101, %[sq], %[t0] offen\n\t"
        "s_waitcnt vmcnt(17)\n\t"
        "ds_write_b128 v102, a[252:255] offset:16384\n\t"
        "ds_write_b128 v102, v[128:131] offset:20480\n\t"
        "ds_write_b128 v116, v[132:135] offset:24576\n\t"
        "s_waitcnt lgkmcnt(0)\n\t"
        "s_barrier\n\t"
        "v_mfma_f32_16x16x32_bf16 v[8:11], a[96:99], v[56:59], v[8:11]\n\t"
        "ds_read_b64_tr_b16 a[120:121], v103 offset:16384\n\t"
        "ds_read_b64_tr_b16 a[122:123], v103 offset:16896\n\t"
        "v_mfma_f32_16x16x32_bf16 v[12:15], a[100:103], v[56:59], v[12:15]\n\t"
        "ds_read_b64_tr_b16 a[124:125], v103 offset:16392\n\t"
        "ds_read_b64_tr_b16 a[126:127], v103 offset:16904\n\t"
        "v_mfma_f32_16x16x32_bf16 v[16:19], v[56:59], a[104:107], v[16:19]\n\t"
        "ds_read_b64_tr_b16 a[128:129], v104 offset:16384\n\t"
        "ds_read_b64_tr_b16 a[130:131], v104 offset:16640\n\t"
        "v_mfma_f32_16x16x32_bf16 v[20:23], a[96:99], v[60:63], v[20:23]\n\t"
        "s_waitcnt lgkmcnt(5)\n\t"
        "ds_read_b64_tr_b16 a[132:133], v103 offset:20480\n\t"
        "v_mfma_f32_16x16x32_bf16 v[24:27], a[100:103], v[60:63], v[24:27]\n\t"
        "ds_read_b64_tr_b16 a[134:135], v103 offset:20992\n\t"
        "ds_read_b64_tr_b16 a[136:137], v103 offset:20488\n\t"
        "v_mfma_f32_16x16x32_bf16 v[28:31], v[60:63], a[104:107], v[28:31]\n\t"
        "ds_read_b64_tr_b16 a[138:139], v103 offset:21000\n\t"
        "ds_read_b64_tr_b16 a[140:141], v104 offset:18432\n\t"
        "v_mfma_f32_16x16x32_bf16 v[8:11], a[108:111], v[64:67], v[8:11]\n\t"
        "ds_read_b64_tr_b16 a[142:143], v104 offset:18688\n\t"
        "ds_read_b128 v[72:75], v105 offset:40960\n\t"
        "v_mfma_f32_16x16x32_bf16 v[12:15], a[112:115], v[64:67], v[12:15]\n\t"
        "ds_read_b128 v[76:79], v105 offset:43008\n\t"
        "ds_read_b128 v[80:83], v106 offset:40960\n\t"
        "v_mfma_f32_16x16x32_bf16 v[16:19], v[64:67], a[116:119], v[16:19]\n\t"
        "ds_read_b128 v[84:87], v106 offset:43008\n\t"
        "v_mfma_f32_16x16x32_bf16 v[20:23], a[108:111], v[68:71], v[20:23]\n\t"
        "v_mfma_f32_16x16x32_bf16 v[24:27], a[112:115], v[68:71], v[24:27]\n\t"
        "v_mfma_f32_16x16x32_bf16 v[28:31], v[68:71], a[116:119], v[28:31]\n\t"
        "s_mov_b32 %[t0], 4718592\n\t"
        "buffer_load_dwordx4 v[172:175], v101, %[sq], %[t0] offen\n\t"
        "buffer_load_dwordx4 v[180:183], v112, %[sv], %[t0] offen\n\t"
        "s_mov_b32 %[t0], 4980736\n\t"
        "buffer_load_dwordx4 v[176:179], v101, %[sq], %[t0] offen\n\t"
        "s_waitcnt vmcnt(17)\n\t"
        "ds_write_b128 v102, v[136:139] offset:0\n\t"
        "ds_write_b128 v102, v[140:143] offset:4096\n\t"
        "ds_write_b128 v116, v[144:147] offset:8192\n\t"
        "s_waitcnt lgkmcnt(0)\n\t"
        "s_barrier\n\t"
        "v_mfma_f32_16x16x32_bf16 v[8:11], a[120:123], v[72:75], v[8:11]\n\t"
        "ds_read_b64_tr_b16 a[144:145], v103 offset:0\n\t"
        "ds_read_b64_tr_b16 a[146:147], v103 offset:512\n\t"
        "v_mfma_f32_16x16x32_bf16 v[12:15], a[124:127], v[72:75], v[12:15]\n\t"
        "ds_read_b64_tr_b16 a[148:149], v103 offset:8\n\t"
        "ds_read_b64_tr_b16 a[150:151], v103 offset:520\n\t"
        "v_mfma_f32_16x16x32_bf16 v[16:19], v[72:75], a[128:131], v[16:19]\n\t"
        "ds_read_b64_tr_b16 a[152:153], v104 offset:0\n\t"
        "ds_read_b64_tr_b16 a[154:155], v104 offset:256\n\t"
        "v_mfma_f32_16x16x32_bf16 v[20:23], a[120:123], v[76:79], v[20:23]\n\t"
        "s_waitcnt lgkmcnt(5)\n\t"
        "ds_read_b64_tr_b16 a[156:157], v103 offset:4096\n\t"
        "v_mfma_f32_16x16x32_bf16 v[24:27], a[124:127], v[76:79], v[24:27]\n\t"
        "ds_read_b64_tr_b16 a[158:159], v103 offset:4608\n\t"
        "ds_read_b64_tr_b16 a[160:161], v103 offset:4104\n\t"
        "v_mfma_f32_16x16x32_bf16 v[28:31], v[76:79], a[128:131], v[28:31]\n\t"
        "ds_read_b64_tr_b16 a[162:163], v103 offset:4616\n\t"
        "ds_read_b64_tr_b16 a[164:165], v104 offset:2048\n\t"
        "v_mfma_f32_16x16x32_bf16 v[8:11], a[132:135], v[80:83], v[8:11]\n\t"
        "ds_read_b64_tr_b16 a[166:167], v104 offset:2304\n\t"
        "ds_read_b128 v[56:59], v105 offset:49152\n\t"
        "v_mfma_f32_16x16x32_bf16 v[12:15], a[136:139], v[80:83], v[12:15]\n\t"
        "ds_read_b128 v[60:63], v105 offset:51200\n\t"
        "ds_read_b128 v[64:67], v106 offset:49152\n\t"
        "v_mfma_f32_16x16x32_bf16 v[16:19], v[80:83], a[140:143], v[16:19]\n\t"
        "ds_read_b128 v[68:71], v106 offset:51200\n\t"
        "v_mfma_f32_16x16x32_bf16 v[20:23], a[132:135], v[84:87], v[20:23]\n\t"
        "v_mfma_f32_16x16x32_bf16 v[24:27], a[136:139], v[84:87], v[24:27]\n\t"
        "v_mfma_f32_16x16x32_bf16 v[28:31], v[84:87], a[140:143], v[28:31]\n\t"
        "s_mov_b32 %[t0], 5242880\n\t"
        "buffer_load_dwordx4 v[184:187], v101, %[sq], %[t0] offen\n\t"
        "buffer_load_dwordx4 v[192:195], v112, %[sv], %[t0] offen\n\t"
        "s_mov_b32 %[t0], 5505024\n\t"
        "buffer_load_dwordx4 v[188:191], v101, %[sq], %[t0] offen\n\t"
        "s_waitcnt vmcnt(17)\n\t"
        "ds_write_b128 v102, v[148:151] offset:16384\n\t"
        "ds_write_b128 v102, v[152:155] offset:20480\n\t"
        "ds_write_b128 v116, v[156:159] offset:24576\n\t"
        "s_waitcnt lgkmcnt(0)\n\t"
        "s_barrier\n\t"
        "v_mfma_f32_16x16x32_bf16 v[8:11], a[144:147], v[56:59], v[8:11]\n\t"
        "ds_read_b64_tr_b16 a[168:169], v103 offset:16384\n\t"
        "ds_read_b64_tr_b16 a[170:171], v103 offset:16896\n\t"
        "v_mfma_f32_16x16x32_bf16 v[12:15], a[148:151], v[56:59], v[12:15]\n\t"
        "ds_read_b64_tr_b16 a[172:173], v103 offset:16392\n\t"
        "ds_read_b64_tr_b16 a[174:175], v103 offset:16904\n\t"
        "v_mfma_f32_16x16x32_bf16 v[16:19], v[56:59], a[152:155], v[16:19]\n\t"
        "ds_read_b64_tr_b16 a[176:177], v104 offset:16384\n\t"
        "ds_read_b64_tr_b16 a[178:179], v104 offset:16640\n\t"
        "v_mfma_f32_16x16x32_bf16 v[20:23], a[144:147], v[60:63], v[20:23]\n\t"
        "s_waitcnt lgkmcnt(5)\n\t"
        "ds_read_b64_tr_b16 a[180:181], v103 offset:20480\n\t"
        "v_mfma_f32_16x16x32_bf16 v[24:27], a[148:151], v[60:63], v[24:27]\n\t"
        "ds_read_b64_tr_b16 a[182:183], v103 offset:20992\n\t"
        "ds_read_b64_tr_b16 a[184:185], v103 offset:20488\n\t"
        "v_mfma_f32_16x16x32_bf16 v[28:31], v[60:63], a[152:155], v[28:31]\n\t"
        "ds_read_b64_tr_b16 a[186:187], v103 offset:21000\n\t"
        "ds_read_b64_tr_b16 a[188:189], v104 offset:18432\n\t"
        "v_mfma_f32_16x16x32_bf16 v[8:11], a[156:159], v[64:67], v[8:11]\n\t"
        "ds_read_b64_tr_b16 a[190:191], v104 offset:18688\n\t"
        "ds_read_b128 v[72:75], v105 offset:57344\n\t"
        "v_mfma_f32_16x16x32_bf16 v[12:15], a[160:163], v[64:67], v[12:15]\n\t"
        "ds_read_b128 v[76:79], v105 offset:59392\n\t"
        "ds_read_b128 v[80:83], v106 offset:57344\n\t"
        "v_mfma_f32_16x16x32_bf16 v[16:19], v[64:67], a[164:167], v[16:19]\n\t"
        "ds_read_b128 v[84:87], v106 offset:59392\n\t"
        "v_mfma_f32_16x16x32_bf16 v[20:23], a[156:159], v[68:71], v[20:23]\n\t"
        "v_mfma_f32_16x16x32_bf16 v[24:27], a[160:163], v[68:71], v[24:27]\n\t"
        "v_mfma_f32_16x16x32_bf16 v[28:31], v[68:71], a[164:167], v[28:31]\n\t"
        "s_mov_b32 %[t0], 5767168\n\t"
        "buffer_load_dwordx4 v[196:199], v101, %[sq], %[t0] offen\n\t"
        "buffer_load_dwordx4 v[204:207], v112, %[sv], %[t0] offen\n\t"
        "s_mov_b32 %[t0], 6029312\n\t"
        "buffer_load_dwordx4 v[200:203], v101, %[sq], %[t0] offen\n\t"
        "s_waitcnt vmcnt(9)\n\t"
        "ds_write_b128 v102, v[160:163] offset:0\n\t"
        "ds_write_b128 v102, v[164:167] offset:4096\n\t"
        "ds_write_b128 v116, v[168:171] offset:8192\n\t"
        "s_waitcnt lgkmcnt(0)\n\t"
        "s_barrier\n\t"
        "v_mfma_f32_16x16x32_bf16 v[8:11], a[168:171], v[72:75], v[8:11]\n\t"
        "ds_read_b64_tr_b16 a[192:193], v103 offset:0\n\t"
        "ds_read_b64_tr_b16 a[194:195], v103 offset:512\n\t"
        "v_mfma_f32_16x16x32_bf16 v[12:15], a[172:175], v[72:75], v[12:15]\n\t"
        "ds_read_b64_tr_b16 a[196:197], v103 offset:8\n\t"
        "ds_read_b64_tr_b16 a[198:199], v103 offset:520\n\t"
        "v_mfma_f32_16x16x32_bf16 v[16:19], v[72:75], a[176:179], v[16:19]\n\t"
        "ds_read_b64_tr_b16 a[200:201], v104 offset:0\n\t"
        "ds_read_b64_tr_b16 a[202:203], v104 offset:256\n\t"
        "v_mfma_f32_16x16x32_bf16 v[20:23], a[168:171], v[76:79], v[20:23]\n\t"
        "s_waitcnt lgkmcnt(5)\n\t"
        "ds_read_b64_tr_b16 a[204:205], v103 offset:4096\n\t"
        "v_mfma_f32_16x16x32_bf16 v[24:27], a[172:175], v[76:79], v[24:27]\n\t"
        "ds_read_b64_tr_b16 a[206:207], v103 offset:4608\n\t"
        "ds_read_b64_tr_b16 a[208:209], v103 offset:4104\n\t"
        "v_mfma_f32_16x16x32_bf16 v[28:31], v[76:79], a[176:179], v[28:31]\n\t"
        "ds_read_b64_tr_b16 a[210:211], v103 offset:4616\n\t"
        "ds_read_b64_tr_b16 a[212:213], v104 offset:2048\n\t"
        "v_mfma_f32_16x16x32_bf16 v[8:11], a[180:183], v[80:83], v[8:11]\n\t"
        "ds_read_b64_tr_b16 a[214:215], v104 offset:2304\n\t"
        "ds_read_b128 v[56:59], v107 offset:0\n\t"
        "v_mfma_f32_16x16x32_bf16 v[12:15], a[184:187], v[80:83], v[12:15]\n\t"
        "ds_read_b128 v[60:63], v107 offset:2048\n\t"
        "ds_read_b128 v[64:67], v108 offset:0\n\t"
        "v_mfma_f32_16x16x32_bf16 v[16:19], v[80:83], a[188:191], v[16:19]\n\t"
        "ds_read_b128 v[68:71], v108 offset:2048\n\t"
        "v_mfma_f32_16x16x32_bf16 v[20:23], a[180:183], v[84:87], v[20:23]\n\t"
        "v_mfma_f32_16x16x32_bf16 v[24:27], a[184:187], v[84:87], v[24:27]\n\t"
        "v_mfma_f32_16x16x32_bf16 v[28:31], v[84:87], a[188:191], v[28:31]\n\t"
        "s_add_u32 m0, %[ld], 98304\n\t"
        "s_add_u32 %[t0], %[xo], 1536\n\t"
        "buffer_load_dwordx4 v100, %[sx], %[t0] offen lds\n\t"
        "s_add_u32 m0, %[ld], 99328\n\t"
        "s_add_u32 %[t0], %[xo], 17920\n\t"
        "buffer_load_dwordx4 v100, %[sx], %[t0] offen lds\n\t"
        "s_add_u32 m0, %[ld], 106496\n\t"
        "s_add_u32 %[t0], %[xo], 1664\n\t"
        "buffer_load_dwordx4 v100, %[sx], %[t0] offen lds\n\t"
        "s_add_u32 m0, %[ld], 107520\n\t"
        "s_add_u32 %[t0], %[xo], 18048\n\t"
        "buffer_load_dwordx4 v100, %[sx], %[t0] offen lds\n\t"
        "s_add_u32 m0, %[ld], 114688\n\t"
        "s_add_u32 %[t0], %[xo], 1792\n\t"
        "buffer_load_dwordx4 v100, %[sx], %[t0] offen lds\n\t"
        "s_add_u32 m0, %[ld], 115712\n\t"
        "s_add_u32 %[t0], %[xo], 18176\n\t"
        "buffer_load_dwordx4 v100, %[sx], %[t0] offen lds\n\t"
        "s_add_u32 m0, %[ld], 122880\n\t"
        "s_add_u32 %[t0], %[xo], 1920\n\t"
        "buffer_load_dwordx4 v100, %[sx], %[t0] offen lds\n\t"
        "s_add_u32 m0, %[ld], 123904\n\t"
        "s_add_u32 %[t0], %[xo], 18304\n\t"
        "buffer_load_dwordx4 v100, %[sx], %[t0] offen lds\n\t"
        "s_mov_b32 %[t0], 6291456\n\t"
        "buffer_load_dwordx4 v[208:211], v101, %[sq], %[t0] offen\n\t"
        "buffer_load_dwordx4 v[216:219], v112, %[sv], %[t0] offen\n\t"
        "s_mov_b32 %[t0], 6553600\n\t"
        "buffer_load_dwordx4 v[212:215], v101, %[sq], %[t0] offen\n\t"
        "s_waitcnt vmcnt(17)\n\t"
        "ds_write_b128 v102, v[172:175] offset:16384\n\t"
        "ds_write_b128 v102, v[176:179] offset:20480\n\t"
        "ds_write_b128 v116, v[180:183] offset:24576\n\t"
        "s_waitcnt lgkmcnt(0)\n\t"
        "s_barrier\n\t"
        "v_mfma_f32_16x16x32_bf16 v[8:11], a[192:195], v[56:59], v[8:11]\n\t"
        "ds_read_b64_tr_b16 a[216:217], v103 offset:16384\n\t"
        "ds_read_b64_tr_b16 a[218:219], v103 offset:16896\n\t"
        "v_mfma_f32_16x16x32_bf16 v[12:15], a[196:199], v[56:59], v[12:15]\n\t"
        "ds_read_b64_tr_b16 a[220:221], v103 offset:16392\n\t"
        "ds_read_b64_tr_b16 a[222:223], v103 offset:16904\n\t"
        "v_mfma_f32_16x16x32_bf16 v[16:19], v[56:59], a[200:203], v[16:19]\n\t"
        "ds_read_b64_tr_b16 a[224:225], v104 offset:16384\n\t"
        "ds_read_b64_tr_b16 a[226:227], v104 offset:16640\n\t"
        "v_mfma_f32_16x16x32_bf16 v[20:23], a[192:195], v[60:63], v[20:23]\n\t"
        "s_waitcnt lgkmcnt(5)\n\t"
        "ds_read_b64_tr_b16 a[228:229], v103 offset:20480\n\t"
        "v_mfma_f32_16x16x32_bf16 v[24:27], a[196:199], v[60:63], v[24:27]\n\t"
        "ds_read_b64_tr_b16 a[230:231], v103 offset:20992\n\t"
        "ds_read_b64_tr_b16 a[232:233], v103 offset:20488\n\t"
        "v_mfma_f32_16x16x32_bf16 v[28:31], v[60:63], a[200:203], v[28:31]\n\t"
        "ds_read_b64_tr_b16 a[234:235], v103 offset:21000\n\t"
        "ds_read_b64_tr_b16 a[236:237], v104 offset:18432\n\t"
        "v_mfma_f32_16x16x32_bf16 v[8:11], a[204:207], v[64:67], v[8:11]\n\t"
        "ds_read_b64_tr_b16 a[238:239], v104 offset:18688\n\t"
        "ds_read_b128 v[72:75], v107 offset:8192\n\t"
        "v_mfma_f32_16x16x32_bf16 v[12:15], a[208:211], v[64:67], v[12:15]\n\t"
        "ds_read_b128 v[76:79], v107 offset:10240\n\t"
        "ds_read_b128 v[80:83], v108 offset:8192\n\t"
        "v_mfma_f32_16x16x32_bf16 v[16:19], v[64:67], a[212:215], v[16:19]\n\t"
        "ds_read_b128 v[84:87], v108 offset:10240\n\t"
        "v_mfma_f32_16x16x32_bf16 v[20:23], a[204:207], v[68:71], v[20:23]\n\t"
        "v_mfma_f32_16x16x32_bf16 v[24:27], a[208:211], v[68:71], v[24:27]\n\t"
        "v_mfma_f32_16x16x32_bf16 v[28:31], v[68:71], a[212:215], v[28:31]\n\t"
        "s_mov_b32 %[t0], 6815744\n\t"
        "buffer_load_dwordx4 v[220:223], v101, %[sq], %[t0] offen\n\t"
        "buffer_load_dwordx4 v[228:231], v112, %[sv], %[t0] offen\n\t"
        "s_mov_b32 %[t0], 7077888\n\t"
        "buffer_load_dwordx4 v[224:227], v101, %[sq], %[t0] offen\n\t"
        "s_waitcnt vmcnt(17)\n\t"
        "ds_write_b128 v102, v[184:187] offset:0\n\t"
        "ds_write_b128 v102, v[188:191] offset:4096\n\t"
        "ds_write_b128 v116, v[192:195] offset:8192\n\t"
        "s_waitcnt lgkmcnt(0)\n\t"
        "s_barrier\n\t"
        "v_mfma_f32_16x16x32_bf16 v[8:11], a[216:219], v[72:75], v[8:11]\n\t"
        "ds_read_b64_tr_b16 a[240:241], v103 offset:0\n\t"
        "ds_read_b64_tr_b16 a[242:243], v103 offset:512\n\t"
        "v_mfma_f32_16x16x32_bf16 v[12:15], a[220:223], v[72:75], v[12:15]\n\t"
        "ds_read_b64_tr_b16 a[244:245], v103 offset:8\n\t"
        "ds_read_b64_tr_b16 a[246:247], v103 offset:520\n\t"
        "v_mfma_f32_16x16x32_bf16 v[16:19], v[72:75], a[224:227], v[16:19]\n\t"
        "ds_read_b64_tr_b16 a[248:249], v104 offset:0\n\t"
        "ds_read_b64_tr_b16 a[250:251], v104 offset:256\n\t"
        "v_mfma_f32_16x16x32_bf16 v[20:23], a[216:219], v[76:79], v[20:23]\n\t"
        "s_waitcnt lgkmcnt(5)\n\t"
        "ds_read_b64_tr_b16 a[252:253], v103 offset:4096\n\t"
        "v_mfma_f32_16x16x32_bf16 v[24:27], a[220:223], v[76:79], v[24:27]\n\t"
        "ds_read_b64_tr_b16 a[254:255], v103 offset:4608\n\t"
        "ds_read_b64_tr_b16 v[128:129], v103 offset:4104\n\t"
        "v_mfma_f32_16x16x32_bf16 v[28:31], v[76:79], a[224:227], v[28:31]\n\t"
        "ds_read_b64_tr_b16 v[130:131], v103 offset:4616\n\t"
        "ds_read_b64_tr_b16 v[132:133], v104 offset:2048\n\t"
        "v_mfma_f32_16x16x32_bf16 v[8:11], a[228:231], v[80:83], v[8:11]\n\t"
        "ds_read_b64_tr_b16 v[134:135], v104 offset:2304\n\t"
        "ds_read_b128 v[56:59], v107 offset:16384\n\t"
        "v_mfma_f32_16x16x32_bf16 v[12:15], a[232:235], v[80:83], v[12:15]\n\t"
        "ds_read_b128 v[60:63], v107 offset:18432\n\t"
        "ds_read_b128 v[64:67], v108 offset:16384\n\t"
        "v_mfma_f32_16x16x32_bf16 v[16:19], v[80:83], a[236:239], v[16:19]\n\t"
        "ds_read_b128 v[68:71], v108 offset:18432\n\t"
        "v_mfma_f32_16x16x32_bf16 v[20:23], a[228:231], v[84:87], v[20:23]\n\t"
        "v_mfma_f32_16x16x32_bf16 v[24:27], a[232:235], v[84:87], v[24:27]\n\t"
        "v_mfma_f32_16x16x32_bf16 v[28:31], v[84:87], a[236:239], v[28:31]\n\t"
        "s_mov_b32 %[t0], 7340032\n\t"
        "buffer_load_dwordx4 v[232:235], v101, %[sq], %[t0] offen\n\t"
        "buffer_load_dwordx4 v[240:243], v112, %[sv], %[t0] offen\n\t"
        "s_mov_b32 %[t0], 7602176\n\t"
        "buffer_load_dwordx4 v[236:239], v101, %[sq], %[t0] offen\n\t"
        "s_waitcnt vmcnt(17)\n\t"
        "ds_write_b128 v102, v[196:199] offset:16384\n\t"
        "ds_write_b128 v102, v[200:203] offset:20480\n\t"
        "ds_write_b128 v116, v[204:207] offset:24576\n\t"
        "s_waitcnt lgkmcnt(0)\n\t"
        "s_barrier\n\t"
        "v_mfma_f32_16x16x32_bf16 v[8:11], a[240:243], v[56:59], v[8:11]\n\t"
        "ds_read_b64_tr_b16 v[136:137], v103 offset:16384\n\t"
        "ds_read_b64_tr_b16 v[138:139], v103 offset:16896\n\t"
        "v_mfma_f32_16x16x32_bf16 v[12:15], a[244:247], v[56:59], v[12:15]\n\t"
        "ds_read_b64_tr_b16 v[140:141], v103 offset:16392\n\t"
        "ds_read_b64_tr_b16 v[142:143], v103 offset:16904\n\t"
        "v_mfma_f32_16x16x32_bf16 v[16:19], v[56:59], a[248:251], v[16:19]\n\t"
        "ds_read_b64_tr_b16 v[144:145], v104 offset:16384\n\t"
        "ds_read_b64_tr_b16 v[146:147], v104 offset:16640\n\t"
        "v_mfma_f32_16x16x32_bf16 v[20:23], a[240:243], v[60:63], v[20:23]\n\t"
        "s_waitcnt lgkmcnt(5)\n\t"
        "ds_read_b64_tr_b16 v[148:149], v103 offset:20480\n\t"
        "v_mfma_f32_16x16x32_bf16 v[24:27], a[244:247], v[60:63], v[24:27]\n\t"
        "ds_read_b64_tr_b16 v[150:151], v103 offset:20992\n\t"
        "ds_read_b64_tr_b16 v[152:153], v103 offset:20488\n\t"
        "v_mfma_f32_16x16x32_bf16 v[28:31], v[60:63], a[248:251], v[28:31]\n\t"
        "ds_read_b64_tr_b16 v[154:155], v103 offset:21000\n\t"
        "ds_read_b64_tr_b16 v[156:157], v104 offset:18432\n\t"
        "v_mfma_f32_16x16x32_bf16 v[8:11], a[252:255], v[64:67], v[8:11]\n\t"
        "ds_read_b64_tr_b16 v[158:159], v104 offset:18688\n\t"
        "ds_read_b128 v[72:75], v107 offset:24576\n\t"
        "v_mfma_f32_16x16x32_bf16 v[12:15], v[128:131], v[64:67], v[12:15]\n\t"
        "ds_read_b128 v[76:79], v107 offset:26624\n\t"
        "ds_read_b128 v[80:83], v108 offset:24576\n\t"
        "v_mfma_f32_16x16x32_bf16 v[16:19], v[64:67], v[132:135], v[16:19]\n\t"
        "ds_read_b128 v[84:87], v108 offset:26624\n\t"
        "v_mfma_f32_16x16x32_bf16 v[20:23], a[252:255], v[68:71], v[20:23]\n\t"
        "v_mfma_f32_16x16x32_bf16 v[24:27], v[128:131], v[68:71], v[24:27]\n\t"
        "v_mfma_f32_16x16x32_bf16 v[28:31], v[68:71], v[132:135], v[28:31]\n\t"
        "s_mov_b32 %[t0], 7864320\n\t"
        "buffer_load_dwordx4 v[244:247], v101, %[sq], %[t0] offen\n\t"
        "buffer_load_dwordx4 v[252:255], v112, %[sv], %[t0] offen\n\t"
        "s_mov_b32 %[t0], 8126464\n\t"
        "buffer_load_dwordx4 v[248:251], v101, %[sq], %[t0] offen\n\t"
        "s_waitcnt vmcnt(9)\n\t"
        "ds_write_b128 v102, v[208:211] offset:0\n\t"
        "ds_write_b128 v102, v[212:215] offset:4096\n\t"
        "ds_write_b128 v116, v[216:219] offset:8192\n\t"
        "s_waitcnt lgkmcnt(0)\n\t"
        "s_barrier\n\t"
        "v_mfma_f32_16x16x32_bf16 v[8:11], v[136:139], v[72:75], v[8:11]\n\t"
        "ds_read_b64_tr_b16 v[160:161], v103 offset:0\n\t"
        "ds_read_b64_tr_b16 v[162:163], v103 offset:512\n\t"
        "v_mfma_f32_16x16x32_bf16 v[12:15], v[140:143], v[72:75], v[12:15]\n\t"
        "ds_read_b64_tr_b16 v[164:165], v103 offset:8\n\t"
        "ds_read_b64_tr_b16 v[166:167], v103 offset:520\n\t"
        "v_mfma_f32_16x16x32_bf16 v[16:19], v[72:75], v[144:147], v[16:19]\n\t"
        "ds_read_b64_tr_b16 v[168:169], v104 offset:0\n\t"
        "ds_read_b64_tr_b16 v[170:171], v104 offset:256\n\t"
        "v_mfma_f32_16x16x32_bf16 v[20:23], v[136:139], v[76:79], v[20:23]\n\t"
        "s_waitcnt lgkmcnt(5)\n\t"
        "ds_read_b64_tr_b16 v[172:173], v103 offset:4096\n\t"
        "v_mfma_f32_16x16x32_bf16 v[24:27], v[140:143], v[76:79], v[24:27]\n\t"
        "ds_read_b64_tr_b16 v[174:175], v103 offset:4608\n\t"
        "ds_read_b64_tr_b16 v[176:177], v103 offset:4104\n\t"
        "v_mfma_f32_16x16x32_bf16 v[28:31], v[76:79], v[144:147], v[28:31]\n\t"
        "ds_read_b64_tr_b16 v[178:179], v103 offset:4616\n\t"
        "ds_read_b64_tr_b16 v[180:181], v104 offset:2048\n\t"
        "v_mfma_f32_16x16x32_bf16 v[8:11], v[148:151], v[80:83], v[8:11]\n\t"
        "ds_read_b64_tr_b16 v[182:183], v104 offset:2304\n\t"
        "ds_read_b128 v[56:59], v107 offset:32768\n\t"
        "v_mfma_f32_16x16x32_bf16 v[12:15], v[152:155], v[80:83], v[12:15]\n\t"
        "ds_read_b128 v[60:63], v107 offset:34816\n\t"
        "ds_read_b128 v[64:67], v108 offset:32768\n\t"
        "v_mfma_f32_16x16x32_bf16 v[16:19], v[80:83], v[156:159], v[16:19]\n\t"
        "ds_read_b128 v[68:71], v108 offset:34816\n\t"
        "v_mfma_f32_16x16x32_bf16 v[20:23], v[148:151], v[84:87], v[20:23]\n\t"
        "v_mfma_f32_16x16x32_bf16 v[24:27], v[152:155], v[84:87], v[24:27]\n\t"
        "v_mfma_f32_16x16x32_bf16 v[28:31], v[84:87], v[156:159], v[28:31]\n\t"
        "s_mov_b32 %[xo], 0x20000\n\t"
        "s_add_u32 m0, %[ld], 0\n\t"
        "s_add_u32 %[t0], %[xo], 0\n\t"
        "buffer_load_dwordx4 v100, %[sx], %[t0] offen lds\n\t"
        "s_add_u32 m0, %[ld], 1024\n\t"
        "s_add_u32 %[t0], %[xo], 16384\n\t"
        "buffer_load_dwordx4 v100, %[sx], %[t0] offen lds\n\t"
        "s_add_u32 m0, %[ld], 8192\n\t"
        "s_add_u32 %[t0], %[xo], 128\n\t"
        "buffer_load_dwordx4 v100, %[sx], %[t0] offen lds\n\t"
        "s_add_u32 m0, %[ld], 9216\n\t"
        "s_add_u32 %[t0], %[xo], 16512\n\t"
        "buffer_load_dwordx4 v100, %[sx], %[t0] offen lds\n\t"
        "s_add_u32 m0, %[ld], 16384\n\t"
        "s_add_u32 %[t0], %[xo], 256\n\t"
        "buffer_load_dwordx4 v100, %[sx], %[t0] offen lds\n\t"
        "s_add_u32 m0, %[ld], 17408\n\t"
        "s_add_u32 %[t0], %[xo], 16640\n\t"
        "buffer_load_dwordx4 v100, %[sx], %[t0] offen lds\n\t"
        "s_add_u32 m0, %[ld], 24576\n\t"
        "s_add_u32 %[t0], %[xo], 384\n\t"
        "buffer_load_dwordx4 v100, %[sx], %[t0] offen lds\n\t"
        "s_add_u32 m0, %[ld], 25600\n\t"
        "s_add_u32 %[t0], %[xo], 16768\n\t"
        "buffer_load_dwordx4 v100, %[sx], %[t0] offen lds\n\t"
        "s_mov_b32 %[xo], 0\n\t"
        "s_waitcnt vmcnt(14)\n\t"
        "ds_write_b128 v102, v[220:223] offset:16384\n\t"
        "ds_write_b128 v102, v[224:227] offset:20480\n\t"
        "ds_write_b128 v116, v[228:231] offset:24576\n\t"
        "s_waitcnt lgkmcnt(0)\n\t"
        "s_barrier\n\t"
        "v_mfma_f32_16x16x32_bf16 v[8:11], v[160:163], v[56:59], v[8:11]\n\t"
        "ds_read_b64_tr_b16 v[184:185], v103 offset:16384\n\t"
        "ds_read_b64_tr_b16 v[186:187], v103 offset:16896\n\t"
        "v_mfma_f32_16x16x32_bf16 v[12:15], v[164:167], v[56:59], v[12:15]\n\t"
        "ds_read_b64_tr_b16 v[188:189], v103 offset:16392\n\t"
        "ds_read_b64_tr_b16 v[190:191], v103 offset:16904\n\t"
        "v_mfma_f32_16x16x32_bf16 v[16:19], v[56:59], v[168:171], v[16:19]\n\t"
        "ds_read_b64_tr_b16 v[192:193], v104 offset:16384\n\t"
        "ds_read_b64_tr_b16 v[194:195], v104 offset:16640\n\t"
        "v_mfma_f32_16x16x32_bf16 v[20:23], v[160:163], v[60:63], v[20:23]\n\t"
        "s_waitcnt lgkmcnt(5)\n\t"
        "ds_read_b64_tr_b16 v[196:197], v103 offset:20480\n\t"
        "v_mfma_f32_16x16x32_bf16 v[24:27], v[164:167], v[60:63], v[24:27]\n\t"
        "ds_read_b64_tr_b16 v[198:199], v103 offset:20992\n\t"
        "ds_read_b64_tr_b16 v[200:201], v103 offset:20488\n\t"
        "v_mfma_f32_16x16x32_bf16 v[28:31], v[60:63], v[168:171], v[28:31]\n\t"
        "ds_read_b64_tr_b16 v[202:203], v103 offset:21000\n\t"
        "ds_read_b64_tr_b16 v[204:205], v104 offset:18432\n\t"
        "v_mfma_f32_16x16x32_bf16 v[8:11], v[172:175], v[64:67], v[8:11]\n\t"
        "ds_read_b64_tr_b16 v[206:207], v104 offset:18688\n\t"
        "ds_read_b128 v[72:75], v107 offset:40960\n\t"
        "v_mfma_f32_16x16x32_bf16 v[12:15], v[176:179], v[64:67], v[12:15]\n\t"
        "ds_read_b128 v[76:79], v107 offset:43008\n\t"
        "ds_read_b128 v[80:83], v108 offset:40960\n\t"
        "v_mfma_f32_16x16x32_bf16 v[16:19], v[64:67], v[180:183], v[16:19]\n\t"
        "ds_read_b128 v[84:87], v108 offset:43008\n\t"
        "v_mfma_f32_16x16x32_bf16 v[20:23], v[172:175], v[68:71], v[20:23]\n\t"
        "v_mfma_f32_16x16x32_bf16 v[24:27], v[176:179], v[68:71], v[24:27]\n\t"
        "v_mfma_f32_16x16x32_bf16 v[28:31], v[68:71], v[180:183], v[28:31]\n\t"
        "s_waitcnt vmcnt(11)\n\t"
        "ds_write_b128 v102, v[232:235] offset:0\n\t"
        "ds_write_b128 v102, v[236:239] offset:4096\n\t"
        "ds_write_b128 v116, v[240:243] offset:8192\n\t"
        "s_waitcnt lgkmcnt(0)\n\t"
        "s_barrier\n\t"
        "v_mfma_f32_16x16x32_bf16 v[8:11], v[184:187], v[72:75], v[8:11]\n\t"
        "ds_read_b64_tr_b16 v[208:209], v103 offset:0\n\t"
        "ds_read_b64_tr_b16 v[210:211], v103 offset:512\n\t"
        "v_mfma_f32_16x16x32_bf16 v[12:15], v[188:191], v[72:75], v[12:15]\n\t"
        "ds_read_b64_tr_b16 v[212:213], v103 offset:8\n\t"
        "ds_read_b64_tr_b16 v[214:215], v103 offset:520\n\t"
        "v_mfma_f32_16x16x32_bf16 v[16:19], v[72:75], v[192:195], v[16:19]\n\t"
        "ds_read_b64_tr_b16 v[216:217], v104 offset:0\n\t"
        "ds_read_b64_tr_b16 v[218:219], v104 offset:256\n\t"
        "v_mfma_f32_16x16x32_bf16 v[20:23], v[184:187], v[76:79], v[20:23]\n\t"
        "s_waitcnt lgkmcnt(5)\n\t"
        "ds_read_b64_tr_b16 v[220:221], v103 offset:4096\n\t"
        "v_mfma_f32_16x16x32_bf16 v[24:27], v[188:191], v[76:79], v[24:27]\n\t"
        "ds_read_b64_tr_b16 v[222:223], v103 offset:4608\n\t"
        "ds_read_b64_tr_b16 v[224:225], v103 offset:4104\n\t"
        "v_mfma_f32_16x16x32_bf16 v[28:31], v[76:79], v[192:195], v[28:31]\n\t"
        "ds_read_b64_tr_b16 v[226:227], v103 offset:4616\n\t"
        "ds_read_b64_tr_b16 v[228:229], v104 offset:2048\n\t"
        "v_mfma_f32_16x16x32_bf16 v[8:11], v[196:199], v[80:83], v[8:11]\n\t"
        "ds_read_b64_tr_b16 v[230:231], v104 offset:2304\n\t"
        "ds_read_b128 v[56:59], v107 offset:49152\n\t"
        "v_mfma_f32_16x16x32_bf16 v[12:15], v[200:203], v[80:83], v[12:15]\n\t"
        "ds_read_b128 v[60:63], v107 offset:51200\n\t"
        "ds_read_b128 v[64:67], v108 offset:49152\n\t"
        "v_mfma_f32_16x16x32_bf16 v[16:19], v[80:83], v[204:207], v[16:19]\n\t"
        "ds_read_b128 v[68:71], v108 offset:51200\n\t"
        "v_mfma_f32_16x16x32_bf16 v[20:23], v[196:199], v[84:87], v[20:23]\n\t"
        "v_mfma_f32_16x16x32_bf16 v[24:27], v[200:203], v[84:87], v[24:27]\n\t"
        "v_mfma_f32_16x16x32_bf16 v[28:31], v[84:87], v[204:207], v[28:31]\n\t"
        "s_waitcnt vmcnt(8)\n\t"
        "ds_write_b128 v102, v[244:247] offset:16384\n\t"
        "ds_write_b128 v102, v[248:251] offset:20480\n\t"
        "ds_write_b128 v116, v[252:255] offset:24576\n\t"
        "s_waitcnt lgkmcnt(0)\n\t"
        "s_barrier\n\t"
        "v_mfma_f32_16x16x32_bf16 v[8:11], v[208:211], v[56:59], v[8:11]\n\t"
        "ds_read_b64_tr_b16 v[232:233], v103 offset:16384\n\t"
        "ds_read_b64_tr_b16 v[234:235], v103 offset:16896\n\t"
        "v_mfma_f32_16x16x32_bf16 v[12:15], v[212:215], v[56:59], v[12:15]\n\t"
        "ds_read_b64_tr_b16 v[236:237], v103 offset:16392\n\t"
        "ds_read_b64_tr_b16 v[238:239], v103 offset:16904\n\t"
        "v_mfma_f32_16x16x32_bf16 v[16:19], v[56:59], v[216:219], v[16:19]\n\t"
        "ds_read_b64_tr_b16 v[240:241], v104 offset:16384\n\t"
        "ds_read_b64_tr_b16 v[242:243], v104 offset:16640\n\t"
        "v_mfma_f32_16x16x32_bf16 v[20:23], v[208:211], v[60:63], v[20:23]\n\t"
        "s_waitcnt lgkmcnt(5)\n\t"
        "ds_read_b64_tr_b16 v[244:245], v103 offset:20480\n\t"
        "v_mfma_f32_16x16x32_bf16 v[24:27], v[212:215], v[60:63], v[24:27]\n\t"
        "ds_read_b64_tr_b16 v[246:247], v103 offset:20992\n\t"
        "ds_read_b64_tr_b16 v[248:249], v103 offset:20488\n\t"
        "v_mfma_f32_16x16x32_bf16 v[28:31], v[60:63], v[216:219], v[28:31]\n\t"
        "ds_read_b64_tr_b16 v[250:251], v103 offset:21000\n\t"
        "ds_read_b64_tr_b16 v[252:253], v104 offset:18432\n\t"
        "v_mfma_f32_16x16x32_bf16 v[8:11], v[220:223], v[64:67], v[8:11]\n\t"
        "ds_read_b64_tr_b16 v[254:255], v104 offset:18688\n\t"
        "ds_read_b128 v[72:75], v107 offset:57344\n\t"
        "v_mfma_f32_16x16x32_bf16 v[12:15], v[224:227], v[64:67], v[12:15]\n\t"
        "ds_read_b128 v[76:79], v107 offset:59392\n\t"
        "ds_read_b128 v[80:83], v108 offset:57344\n\t"
        "v_mfma_f32_16x16x32_bf16 v[16:19], v[64:67], v[228:231], v[16:19]\n\t"
        "ds_read_b128 v[84:87], v108 offset:59392\n\t"
        "v_mfma_f32_16x16x32_bf16 v[20:23], v[220:223], v[68:71], v[20:23]\n\t"
        "v_mfma_f32_16x16x32_bf16 v[24:27], v[224:227], v[68:71], v[24:27]\n\t"
        "v_mfma_f32_16x16x32_bf16 v[28:31], v[68:71], v[228:231], v[28:31]\n\t"
        "s_waitcnt vmcnt(0) lgkmcnt(0)\n\t"
        "s_barrier\n\t"
        "v_mfma_f32_16x16x32_bf16 v[8:11], v[232:235], v[72:75], v[8:11]\n\t"
        "v_mfma_f32_16x16x32_bf16 v[12:15], v[236:239], v[72:75], v[12:15]\n\t"
        "v_mfma_f32_16x16x32_bf16 v[16:19], v[72:75], v[240:243], v[16:19]\n\t"
        "v_mfma_f32_16x16x32_bf16 v[20:23], v[232:235], v[76:79], v[20:23]\n\t"
        "v_mfma_f32_16x16x32_bf16 v[24:27], v[236:239], v[76:79], v[24:27]\n\t"
        "v_mfma_f32_16x16x32_bf16 v[28:31], v[76:79], v[240:243], v[28:31]\n\t"
        "v_mfma_f32_16x16x32_bf16 v[8:11], v[244:247], v[80:83], v[8:11]\n\t"
        "v_mfma_f32_16x16x32_bf16 v[12:15], v[248:251], v[80:83], v[12:15]\n\t"
        "v_mfma_f32_16x16x32_bf16 v[16:19], v[80:83], v[252:255], v[16:19]\n\t"
        "v_mfma_f32_16x16x32_bf16 v[20:23], v[244:247], v[84:87], v[20:23]\n\t"
        "v_mfma_f32_16x16x32_bf16 v[24:27], v[248:251], v[84:87], v[24:27]\n\t"
        "v_mfma_f32_16x16x32_bf16 v[28:31], v[84:87], v[252:255], v[28:31]\n\t"
        "s_mov_b32 %[xo], 0x20000\n\t"
        "ds_read_b128 v[56:59], v105 offset:0\n\t"
        "ds_read_b128 v[60:63], v105 offset:2048\n\t"
        "ds_read_b128 v[64:67], v106 offset:0\n\t"
        "ds_read_b128 v[68:71], v106 offset:2048\n\t"
        "ds_read_b128 v[72:75], v105 offset:8192\n\t"
        "ds_read_b128 v[76:79], v105 offset:10240\n\t"
        "ds_read_b128 v[80:83], v106 offset:8192\n\t"
        "v_cvt_pk_bf16_f32 v88, v8, v9\n\t"
        "v_cvt_pk_bf16_f32 v89, v10, v11\n\t"
        "v_cvt_pk_bf16_f32 v90, v12, v13\n\t"
        "v_cvt_pk_bf16_f32 v91, v14, v15\n\t"
        "buffer_store_dwordx4 v[88:91], v109, %[sw], %[so] offen sc1\n\t"
        "v_cvt_pk_bf16_f32 v92, v20, v21\n\t"
        "v_cvt_pk_bf16_f32 v93, v22, v23\n\t"
        "v_cvt_pk_bf16_f32 v94, v24, v25\n\t"
        "v_cvt_pk_bf16_f32 v95, v26, v27\n\t"
        "buffer_store_dwordx4 v[92:95], v110, %[sw], %[so] offen sc1\n\t"
        "v_cvt_pk_bf16_f32 v96, v16, v17\n\t"
        "v_cvt_pk_bf16_f32 v97, v18, v19\n\t"
        "v_cvt_pk_bf16_f32 v98, v28, v29\n\t"
        "v_cvt_pk_bf16_f32 v99, v30, v31\n\t"
        "buffer_store_dwordx4 v[96:99], v111, %[sw], %[so] offen sc1\n\t"
        "s_mov_b32 %[so], 0xf0000000\n\t"
        "s_mov_b32 %[xo], 33792\n\t"
        "s_mov_b32 %[cn], 0\n\t"
        "LG_cal%=:\n\t"
        "buffer_load_dword v118, off, %[sw], %[xo] sc1\n\t"
        "s_waitcnt vmcnt(0)\n\t"
        "v_readfirstlane_b32 %[t0], v118\n\t"
        "s_cmp_lg_u32 %[t0], 0\n\t"
        "s_cbranch_scc1 LG_calok%=\n\t"
        "s_sleep 4\n\t"
        "s_add_u32 %[cn], %[cn], 1\n\t"
        "s_cmp_lt_u32 %[cn], 0x100000\n\t"
        "s_cbranch_scc1 LG_cal%=\n\t"
        "s_endpgm\n\t"
        "LG_calok%=:\n\t"
        "s_mov_b32 %[xo], 0x20000\n\t"
        "s_mov_b32 %[cn], 1\n\t"
        "v_mov_b32_e32 v117, %[cn]\n\t"
        "buffer_store_dword v117, v113, %[sw], 0 offen sc1\n\t"
        "s_add_u32 m0, %[ld], 32768\n\t"
        "s_add_u32 %[t0], %[xo], 512\n\t"
        "buffer_load_dwordx4 v100, %[sx], %[t0] offen lds\n\t"
        "s_add_u32 m0, %[ld], 33792\n\t"
        "s_add_u32 %[t0], %[xo], 16896\n\t"
        "buffer_load_dwordx4 v100, %[sx], %[t0] offen lds\n\t"
        "s_add_u32 m0, %[ld], 40960\n\t"
        "s_add_u32 %[t0], %[xo], 640\n\t"
        "buffer_load_dwordx4 v100, %[sx], %[t0] offen lds\n\t"
        "s_add_u32 m0, %[ld], 41984\n\t"
        "s_add_u32 %[t0], %[xo], 17024\n\t"
        "buffer_load_dwordx4 v100, %[sx], %[t0] offen lds\n\t"
        "s_add_u32 m0, %[ld], 49152\n\t"
        "s_add_u32 %[t0], %[xo], 768\n\t"
        "buffer_load_dwordx4 v100, %[sx], %[t0] offen lds\n\t"
        "s_add_u32 m0, %[ld], 50176\n\t"
        "s_add_u32 %[t0], %[xo], 17152\n\t"
        "buffer_load_dwordx4 v100, %[sx], %[t0] offen lds\n\t"
        "s_add_u32 m0, %[ld], 57344\n\t"
        "s_add_u32 %[t0], %[xo], 896\n\t"
        "buffer_load_dwordx4 v100, %[sx], %[t0] offen lds\n\t"
        "s_add_u32 m0, %[ld], 58368\n\t"
        "s_add_u32 %[t0], %[xo], 17280\n\t"
        "buffer_load_dwordx4 v100, %[sx], %[t0] offen lds\n\t"
        "s_add_u32 m0, %[ld], 65536\n\t"
        "s_add_u32 %[t0], %[xo], 1024\n\t"
        "buffer_load_dwordx4 v100, %[sx], %[t0] offen lds\n\t"
        "s_add_u32 m0, %[ld], 66560\n\t"
        "s_add_u32 %[t0], %[xo], 17408\n\t"
        "buffer_load_dwordx4 v100, %[sx], %[t0] offen lds\n\t"
        "s_add_u32 m0, %[ld], 73728\n\t"
        "s_add_u32 %[t0], %[xo], 1152\n\t"
        "buffer_load_dwordx4 v100, %[sx], %[t0] offen lds\n\t"
        "s_add_u32 m0, %[ld], 74752\n\t"
        "s_add_u32 %[t0], %[xo], 17536\n\t"
        "buffer_load_dwordx4 v100, %[sx], %[t0] offen lds\n\t"
        "s_add_u32 m0, %[ld], 81920\n\t"
        "s_add_u32 %[t0], %[xo], 1280\n\t"
        "buffer_load_dwordx4 v100, %[sx], %[t0] offen lds\n\t"
        "s_add_u32 m0, %[ld], 82944\n\t"
        "s_add_u32 %[t0], %[xo], 17664\n\t"
        "buffer_load_dwordx4 v100, %[sx], %[t0] offen lds\n\t"
        "s_add_u32 m0, %[ld], 90112\n\t"
        "s_add_u32 %[t0], %[xo], 1408\n\t"
        "buffer_load_dwordx4 v100, %[sx], %[t0] offen lds\n\t"
        "s_add_u32 m0, %[ld], 91136\n\t"
        "s_add_u32 %[t0], %[xo], 17792\n\t"
        "buffer_load_dwordx4 v100, %[sx], %[t0] offen lds\n\t"
        "s_add_u32 m0, %[ld], 98304\n\t"
        "s_add_u32 %[t0], %[xo], 1536\n\t"
        "buffer_load_dwordx4 v100, %[sx], %[t0] offen lds\n\t"
        "s_add_u32 m0, %[ld], 99328\n\t"
        "s_add_u32 %[t0], %[xo], 17920\n\t"
        "buffer_load_dwordx4 v100, %[sx], %[t0] offen lds\n\t"
        "s_add_u32 m0, %[ld], 106496\n\t"
        "s_add_u32 %[t0], %[xo], 1664\n\t"
        "buffer_load_dwordx4 v100, %[sx], %[t0] offen lds\n\t"
        "s_branch LG_mid%=\n\t"
        "LG_loop%=:\n\t"
        "s_waitcnt lgkmcnt(6)\n\t"
        "v_mfma_f32_16x16x32_bf16 v[8:11], a[0:3], v[56:59], 0\n\t"
        "v_mfma_f32_16x16x32_bf16 v[12:15], a[4:7], v[56:59], 0\n\t"
        "ds_read_b128 v[84:87], v106 offset:10240\n\t"
        "v_mfma_f32_16x16x32_bf16 v[16:19], v[56:59], a[8:11], 0\n\t"
        "s_add_u32 m0, %[ld], 107520\n\t"
        "s_add_u32 %[t0], %[xo], 18048\n\t"
        "s_waitcnt lgkmcnt(6)\n\t"
        "v_mfma_f32_16x16x32_bf16 v[20:23], a[0:3], v[60:63], 0\n\t"
        "buffer_load_dwordx4 v100, %[sx], %[t0] offen lds\n\t"
        "v_mfma_f32_16x16x32_bf16 v[24:27], a[4:7], v[60:63], 0\n\t"
        "ds_read_b128 v[56:59], v105 offset:16384\n\t"
        "v_mfma_f32_16x16x32_bf16 v[28:31], v[60:63], a[8:11], 0\n\t"
        "s_waitcnt lgkmcnt(6)\n\t"
        "v_mfma_f32_16x16x32_bf16 v[8:11], a[12:15], v[64:67], v[8:11]\n\t"
        "v_cvt_pk_bf16_f32 v88, v32, v33\n\t"
        "v_cvt_pk_bf16_f32 v89, v34, v35\n\t"
        "v_mfma_f32_16x16x32_bf16 v[12:15], a[16:19], v[64:67], v[12:15]\n\t"
        "ds_read_b128 v[60:63], v105 offset:18432\n\t"
        "v_cvt_pk_bf16_f32 v90, v36, v37\n\t"
        "v_mfma_f32_16x16x32_bf16 v[16:19], v[64:67], a[20:23], v[16:19]\n\t"
        "s_add_u32 m0, %[ld], 114688\n\t"
        "s_add_u32 %[t0], %[xo], 1792\n\t"
        "s_waitcnt lgkmcnt(6)\n\t"
        "v_mfma_f32_16x16x32_bf16 v[20:23], a[12:15], v[68:71], v[20:23]\n\t"
        "buffer_load_dwordx4 v100, %[sx], %[t0] offen lds\n\t"
        "v_mfma_f32_16x16x32_bf16 v[24:27], a[16:19], v[68:71], v[24:27]\n\t"
        "ds_read_b128 v[64:67], v106 offset:16384\n\t"
        "v_cvt_pk_bf16_f32 v91, v38, v39\n\t"
        "v_mfma_f32_16x16x32_bf16 v[28:31], v[68:71], a[20:23], v[28:31]\n\t"
        "buffer_store_dwordx4 v[88:91], v109, %[sw], %[so] offen sc1\n\t"
        "s_waitcnt lgkmcnt(6)\n\t"
        "v_mfma_f32_16x16x32_bf16 v[8:11], a[24:27], v[72:75], v[8:11]\n\t"
        "v_cvt_pk_bf16_f32 v92, v44, v45\n\t"
        "v_cvt_pk_bf16_f32 v93, v46, v47\n\t"
        "v_mfma_f32_16x16x32_bf16 v[12:15], a[28:31], v[72:75], v[12:15]\n\t"
        "ds_read_b128 v[68:71], v106 offset:18432\n\t"
        "v_cvt_pk_bf16_f32 v94, v48, v49\n\t"
        "v_mfma_f32_16x16x32_bf16 v[16:19], v[72:75], a[32:35], v[16:19]\n\t"
        "s_add_u32 m0, %[ld], 115712\n\t"
        "s_add_u32 %[t0], %[xo], 18176\n\t"
        "s_waitcnt lgkmcnt(6)\n\t"
        "v_mfma_f32_16x16x32_bf16 v[20:23], a[24:27], v[76:79], v[20:23]\n\t"
        "buffer_load_dwordx4 v100, %[sx], %[t0] offen lds\n\t"
        "v_mfma_f32_16x16x32_bf16 v[24:27], a[28:31], v[76:79], v[24:27]\n\t"
        "ds_read_b128 v[72:75], v105 offset:24576\n\t"
        "v_cvt_pk_bf16_f32 v95, v50, v51\n\t"
        "v_mfma_f32_16x16x32_bf16 v[28:31], v[76:79], a[32:35], v[28:31]\n\t"
        "buffer_store_dwordx4 v[92:95], v110, %[sw], %[so] offen sc1\n\t"
        "s_waitcnt lgkmcnt(6)\n\t"
        "v_mfma_f32_16x16x32_bf16 v[8:11], a[36:39], v[80:83], v[8:11]\n\t"
        "v_cvt_pk_bf16_f32 v96, v40, v41\n\t"
        "v_cvt_pk_bf16_f32 v97, v42, v43\n\t"
        "v_mfma_f32_16x16x32_bf16 v[12:15], a[40:43], v[80:83], v[12:15]\n\t"
        "ds_read_b128 v[76:79], v105 offset:26624\n\t"
        "v_cvt_pk_bf16_f32 v98, v52, v53\n\t"
        "v_mfma_f32_16x16x32_bf16 v[16:19], v[80:83], a[44:47], v[16:19]\n\t"
        "s_add_u32 m0, %[ld], 122880\n\t"
        "s_add_u32 %[t0], %[xo], 1920\n\t"
        "s_waitcnt lgkmcnt(6)\n\t"
        "v_mfma_f32_16x16x32_bf16 v[20:23], a[36:39], v[84:87], v[20:23]\n\t"
        "buffer_load_dwordx4 v100, %[sx], %[t0] offen lds\n\t"
        "v_mfma_f32_16x16x32_bf16 v[24:27], a[40:43], v[84:87], v[24:27]\n\t"
        "ds_read_b128 v[80:83], v106 offset:24576\n\t"
        "v_cvt_pk_bf16_f32 v99, v54, v55\n\t"
        "v_mfma_f32_16x16x32_bf16 v[28:31], v[84:87], a[44:47], v[28:31]\n\t"
        "buffer_store_dwordx4 v[96:99], v111, %[sw], %[so] offen sc1\n\t"
        "s_waitcnt lgkmcnt(6)\n\t"
        "v_mfma_f32_16x16x32_bf16 v[8:11], a[48:51], v[56:59], v[8:11]\n\t"
        "s_lshl_b32 %[so], %[cn], 19\n\t"
        "v_mfma_f32_16x16x32_bf16 v[12:15], a[52:55], v[56:59], v[12:15]\n\t"
        "ds_read_b128 v[84:87], v106 offset:26624\n\t"
        "v_mfma_f32_16x16x32_bf16 v[16:19], v[56:59], a[56:59], v[16:19]\n\t"
        "s_add_u32 m0, %[ld], 123904\n\t"
        "s_add_u32 %[t0], %[xo], 18304\n\t"
        "s_waitcnt lgkmcnt(6)\n\t"
        "v_mfma_f32_16x16x32_bf16 v[20:23], a[48:51], v[60:63], v[20:23]\n\t"
        "buffer_load_dwordx4 v100, %[sx], %[t0] offen lds\n\t"
        "v_mfma_f32_16x16x32_bf16 v[24:27], a[52:55], v[60:63], v[24:27]\n\t"
        "v_mfma_f32_16x16x32_bf16 v[28:31], v[60:63], a[56:59], v[28:31]\n\t"
        "s_waitcnt lgkmcnt(5)\n\t"
        "v_mfma_f32_16x16x32_bf16 v[8:11], a[60:63], v[64:67], v[8:11]\n\t"
        "s_waitcnt vmcnt(19) lgkmcnt(0)\n\t"
        "s_barrier\n\t"
        "s_add_u32 %[xo], %[xo], 0x20000\n\t"
        "v_mfma_f32_16x16x32_bf16 v[12:15], a[64:67], v[64:67], v[12:15]\n\t"
        "ds_read_b128 v[56:59], v105 offset:32768\n\t"
        "v_mfma_f32_16x16x32_bf16 v[16:19], v[64:67], a[68:71], v[16:19]\n\t"
        "ds_read_b128 v[60:63], v105 offset:34816\n\t"
        "s_add_u32 m0, %[ld], 0\n\t"
        "s_add_u32 %[t0], %[xo], 0\n\t"
        "v_mfma_f32_16x16x32_bf16 v[20:23], a[60:63], v[68:71], v[20:23]\n\t"
        "buffer_load_dwordx4 v100, %[sx], %[t0] offen lds\n\t"
        "v_mfma_f32_16x16x32_bf16 v[24:27], a[64:67], v[68:71], v[24:27]\n\t"
        "ds_read_b128 v[64:67], v106 offset:32768\n\t"
        "v_mfma_f32_16x16x32_bf16 v[28:31], v[68:71], a[68:71], v[28:31]\n\t"
        "v_mfma_f32_16x16x32_bf16 v[8:11], a[72:75], v[72:75], v[8:11]\n\t"
        "v_mfma_f32_16x16x32_bf16 v[12:15], a[76:79], v[72:75], v[12:15]\n\t"
        "ds_read_b128 v[68:71], v106 offset:34816\n\t"
        "v_mfma_f32_16x16x32_bf16 v[16:19], v[72:75], a[80:83], v[16:19]\n\t"
        "s_add_u32 m0, %[ld], 1024\n\t"
        "s_add_u32 %[t0], %[xo], 16384\n\t"
        "v_mfma_f32_16x16x32_bf16 v[20:23], a[72:75], v[76:79], v[20:23]\n\t"
        "buffer_load_dwordx4 v100, %[sx], %[t0] offen lds\n\t"
        "v_mfma_f32_16x16x32_bf16 v[24:27], a[76:79], v[76:79], v[24:27]\n\t"
        "ds_read_b128 v[72:75], v105 offset:40960\n\t"
        "v_mfma_f32_16x16x32_bf16 v[28:31], v[76:79], a[80:83], v[28:31]\n\t"
        "v_mfma_f32_16x16x32_bf16 v[8:11], a[84:87], v[80:83], v[8:11]\n\t"
        "v_mfma_f32_16x16x32_bf16 v[12:15], a[88:91], v[80:83], v[12:15]\n\t"
        "ds_read_b128 v[76:79], v105 offset:43008\n\t"
        "v_mfma_f32_16x16x32_bf16 v[16:19], v[80:83], a[92:95], v[16:19]\n\t"
        "s_add_u32 m0, %[ld], 8192\n\t"
        "s_add_u32 %[t0], %[xo], 128\n\t"
        "v_mfma_f32_16x16x32_bf16 v[20:23], a[84:87], v[84:87], v[20:23]\n\t"
        "buffer_load_dwordx4 v100, %[sx], %[t0] offen lds\n\t"
        "v_mfma_f32_16x16x32_bf16 v[24:27], a[88:91], v[84:87], v[24:27]\n\t"
        "ds_read_b128 v[80:83], v106 offset:40960\n\t"
        "v_mfma_f32_16x16x32_bf16 v[28:31], v[84:87], a[92:95], v[28:31]\n\t"
        "s_waitcnt lgkmcnt(6)\n\t"
        "v_mfma_f32_16x16x32_bf16 v[8:11], a[96:99], v[56:59], v[8:11]\n\t"
        "v_mfma_f32_16x16x32_bf16 v[12:15], a[100:103], v[56:59], v[12:15]\n\t"
        "ds_read_b128 v[84:87], v106 offset:43008\n\t"
        "v_mfma_f32_16x16x32_bf16 v[16:19], v[56:59], a[104:107], v[16:19]\n\t"
        "s_add_u32 m0, %[ld], 9216\n\t"
        "s_add_u32 %[t0], %[xo], 16512\n\t"
        "s_waitcnt lgkmcnt(6)\n\t"
        "v_mfma_f32_16x16x32_bf16 v[20:23], a[96:99], v[60:63], v[20:23]\n\t"
        "buffer_load_dwordx4 v100, %[sx], %[t0] offen lds\n\t"
        "v_mfma_f32_16x16x32_bf16 v[24:27], a[100:103], v[60:63], v[24:27]\n\t"
        "ds_read_b128 v[56:59], v105 offset:49152\n\t"
        "v_mfma_f32_16x16x32_bf16 v[28:31], v[60:63], a[104:107], v[28:31]\n\t"
        "s_waitcnt lgkmcnt(6)\n\t"
        "v_mfma_f32_16x16x32_bf16 v[8:11], a[108:111], v[64:67], v[8:11]\n\t"
        "v_mfma_f32_16x16x32_bf16 v[12:15], a[112:115], v[64:67], v[12:15]\n\t"
        "ds_read_b128 v[60:63], v105 offset:51200\n\t"
        "v_mfma_f32_16x16x32_bf16 v[16:19], v[64:67], a[116:119], v[16:19]\n\t"
        "s_add_u32 m0, %[ld], 16384\n\t"
        "s_add_u32 %[t0], %[xo], 256\n\t"
        "s_waitcnt lgkmcnt(6)\n\t"
        "v_mfma_f32_16x16x32_bf16 v[20:23], a[108:111], v[68:71], v[20:23]\n\t"
        "buffer_load_dwordx4 v100, %[sx], %[t0] offen lds\n\t"
        "v_mfma_f32_16x16x32_bf16 v[24:27], a[112:115], v[68:71], v[24:27]\n\t"
        "ds_read_b128 v[64:67], v106 offset:49152\n\t"
        "v_mfma_f32_16x16x32_bf16 v[28:31], v[68:71], a[116:119], v[28:31]\n\t"
        "s_waitcnt lgkmcnt(6)\n\t"
        "v_mfma_f32_16x16x32_bf16 v[8:11], a[120:123], v[72:75], v[8:11]\n\t"
        "v_mfma_f32_16x16x32_bf16 v[12:15], a[124:127], v[72:75], v[12:15]\n\t"
        "ds_read_b128 v[68:71], v106 offset:51200\n\t"
        "v_mfma_f32_16x16x32_bf16 v[16:19], v[72:75], a[128:131], v[16:19]\n\t"
        "s_add_u32 m0, %[ld], 17408\n\t"
        "s_add_u32 %[t0], %[xo], 16640\n\t"
        "s_waitcnt lgkmcnt(6)\n\t"
        "v_mfma_f32_16x16x32_bf16 v[20:23], a[120:123], v[76:79], v[20:23]\n\t"
        "buffer_load_dwordx4 v100, %[sx], %[t0] offen lds\n\t"
        "v_mfma_f32_16x16x32_bf16 v[24:27], a[124:127], v[76:79], v[24:27]\n\t"
        "ds_read_b128 v[72:75], v105 offset:57344\n\t"
        "v_mfma_f32_16x16x32_bf16 v[28:31], v[76:79], a[128:131], v[28:31]\n\t"
        "s_waitcnt lgkmcnt(6)\n\t"
        "v_mfma_f32_16x16x32_bf16 v[8:11], a[132:135], v[80:83], v[8:11]\n\t"
        "v_mfma_f32_16x16x32_bf16 v[12:15], a[136:139], v[80:83], v[12:15]\n\t"
        "ds_read_b128 v[76:79], v105 offset:59392\n\t"
        "v_mfma_f32_16x16x32_bf16 v[16:19], v[80:83], a[140:143], v[16:19]\n\t"
        "s_add_u32 m0, %[ld], 24576\n\t"
        "s_add_u32 %[t0], %[xo], 384\n\t"
        "s_waitcnt lgkmcnt(6)\n\t"
        "v_mfma_f32_16x16x32_bf16 v[20:23], a[132:135], v[84:87], v[20:23]\n\t"
        "buffer_load_dwordx4 v100, %[sx], %[t0] offen lds\n\t"
        "v_mfma_f32_16x16x32_bf16 v[24:27], a[136:139], v[84:87], v[24:27]\n\t"
        "ds_read_b128 v[80:83], v106 offset:57344\n\t"
        "v_mfma_f32_16x16x32_bf16 v[28:31], v[84:87], a[140:143], v[28:31]\n\t"
        "s_waitcnt lgkmcnt(6)\n\t"
        "v_mfma_f32_16x16x32_bf16 v[8:11], a[144:147], v[56:59], v[8:11]\n\t"
        "v_mfma_f32_16x16x32_bf16 v[12:15], a[148:151], v[56:59], v[12:15]\n\t"
        "ds_read_b128 v[84:87], v106 offset:59392\n\t"
        "v_mfma_f32_16x16x32_bf16 v[16:19], v[56:59], a[152:155], v[16:19]\n\t"
        "s_add_u32 m0, %[ld], 25600\n\t"
        "s_add_u32 %[t0], %[xo], 16768\n\t"
        "s_waitcnt lgkmcnt(6)\n\t"
        "v_mfma_f32_16x16x32_bf16 v[20:23], a[144:147], v[60:63], v[20:23]\n\t"
        "buffer_load_dwordx4 v100, %[sx], %[t0] offen lds\n\t"
        "v_mfma_f32_16x16x32_bf16 v[24:27], a[148:151], v[60:63], v[24:27]\n\t"
        "v_mfma_f32_16x16x32_bf16 v[28:31], v[60:63], a[152:155], v[28:31]\n\t"
        "s_waitcnt lgkmcnt(5)\n\t"
        "v_mfma_f32_16x16x32_bf16 v[8:11], a[156:159], v[64:67], v[8:11]\n\t"
        "s_waitcnt vmcnt(9) lgkmcnt(0)\n\t"
        "s_barrier\n\t"
        "v_mfma_f32_16x16x32_bf16 v[12:15], a[160:163], v[64:67], v[12:15]\n\t"
        "ds_read_b128 v[56:59], v107 offset:0\n\t"
        "v_mfma_f32_16x16x32_bf16 v[16:19], v[64:67], a[164:167], v[16:19]\n\t"
        "ds_read_b128 v[60:63], v107 offset:2048\n\t"
        "s_add_u32 m0, %[ld], 32768\n\t"
        "s_add_u32 %[t0], %[xo], 512\n\t"
        "v_mfma_f32_16x16x32_bf16 v[20:23], a[156:159], v[68:71], v[20:23]\n\t"
        "buffer_load_dwordx4 v100, %[sx], %[t0] offen lds\n\t"
        "v_mfma_f32_16x16x32_bf16 v[24:27], a[160:163], v[68:71], v[24:27]\n\t"
        "ds_read_b128 v[64:67], v108 offset:0\n\t"
        "v_mfma_f32_16x16x32_bf16 v[28:31], v[68:71], a[164:167], v[28:31]\n\t"
        "v_mfma_f32_16x16x32_bf16 v[8:11], a[168:171], v[72:75], v[8:11]\n\t"
        "v_mfma_f32_16x16x32_bf16 v[12:15], a[172:175], v[72:75], v[12:15]\n\t"
        "ds_read_b128 v[68:71], v108 offset:2048\n\t"
        "v_mov_b32_e32 v117, %[cn]\n\t"
        "buffer_store_dword v117, v113, %[sw], 0 offen sc1\n\t"
        "s_add_u32 %[cn], %[cn], 1\n\t"
        "v_mfma_f32_16x16x32_bf16 v[16:19], v[72:75], a[176:179], v[16:19]\n\t"
        "s_add_u32 m0, %[ld], 33792\n\t"
        "s_add_u32 %[t0], %[xo], 16896\n\t"
        "v_mfma_f32_16x16x32_bf16 v[20:23], a[168:171], v[76:79], v[20:23]\n\t"
        "buffer_load_dwordx4 v100, %[sx], %[t0] offen lds\n\t"
        "v_mfma_f32_16x16x32_bf16 v[24:27], a[172:175], v[76:79], v[24:27]\n\t"
        "ds_read_b128 v[72:75], v107 offset:8192\n\t"
        "v_mfma_f32_16x16x32_bf16 v[28:31], v[76:79], a[176:179], v[28:31]\n\t"
        "v_mfma_f32_16x16x32_bf16 v[8:11], a[180:183], v[80:83], v[8:11]\n\t"
        "v_mfma_f32_16x16x32_bf16 v[12:15], a[184:187], v[80:83], v[12:15]\n\t"
        "ds_read_b128 v[76:79], v107 offset:10240\n\t"
        "v_mfma_f32_16x16x32_bf16 v[16:19], v[80:83], a[188:191], v[16:19]\n\t"
        "s_add_u32 m0, %[ld], 40960\n\t"
        "s_add_u32 %[t0], %[xo], 640\n\t"
        "v_mfma_f32_16x16x32_bf16 v[20:23], a[180:183], v[84:87], v[20:23]\n\t"
        "buffer_load_dwordx4 v100, %[sx], %[t0] offen lds\n\t"
        "v_mfma_f32_16x16x32_bf16 v[24:27], a[184:187], v[84:87], v[24:27]\n\t"
        "ds_read_b128 v[80:83], v108 offset:8192\n\t"
        "v_mfma_f32_16x16x32_bf16 v[28:31], v[84:87], a[188:191], v[28:31]\n\t"
        "s_waitcnt lgkmcnt(6)\n\t"
        "v_mfma_f32_16x16x32_bf16 v[8:11], a[192:195], v[56:59], v[8:11]\n\t"
        "v_mfma_f32_16x16x32_bf16 v[12:15], a[196:199], v[56:59], v[12:15]\n\t"
        "ds_read_b128 v[84:87], v108 offset:10240\n\t"
        "v_mfma_f32_16x16x32_bf16 v[16:19], v[56:59], a[200:203], v[16:19]\n\t"
        "s_add_u32 m0, %[ld], 41984\n\t"
        "s_add_u32 %[t0], %[xo], 17024\n\t"
        "s_waitcnt lgkmcnt(6)\n\t"
        "v_mfma_f32_16x16x32_bf16 v[20:23], a[192:195], v[60:63], v[20:23]\n\t"
        "buffer_load_dwordx4 v100, %[sx], %[t0] offen lds\n\t"
        "v_mfma_f32_16x16x32_bf16 v[24:27], a[196:199], v[60:63], v[24:27]\n\t"
        "ds_read_b128 v[56:59], v107 offset:16384\n\t"
        "v_mfma_f32_16x16x32_bf16 v[28:31], v[60:63], a[200:203], v[28:31]\n\t"
        "s_waitcnt lgkmcnt(6)\n\t"
        "v_mfma_f32_16x16x32_bf16 v[8:11], a[204:207], v[64:67], v[8:11]\n\t"
        "v_mfma_f32_16x16x32_bf16 v[12:15], a[208:211], v[64:67], v[12:15]\n\t"
        "ds_read_b128 v[60:63], v107 offset:18432\n\t"
        "v_mfma_f32_16x16x32_bf16 v[16:19], v[64:67], a[212:215], v[16:19]\n\t"
        "s_add_u32 m0, %[ld], 49152\n\t"
        "s_add_u32 %[t0], %[xo], 768\n\t"
        "s_waitcnt lgkmcnt(6)\n\t"
        "v_mfma_f32_16x16x32_bf16 v[20:23], a[204:207], v[68:71], v[20:23]\n\t"
        "buffer_load_dwordx4 v100, %[sx], %[t0] offen lds\n\t"
        "v_mfma_f32_16x16x32_bf16 v[24:27], a[208:211], v[68:71], v[24:27]\n\t"
        "ds_read_b128 v[64:67], v108 offset:16384\n\t"
        "v_mfma_f32_16x16x32_bf16 v[28:31], v[68:71], a[212:215], v[28:31]\n\t"
        "s_waitcnt lgkmcnt(6)\n\t"
        "v_mfma_f32_16x16x32_bf16 v[8:11], a[216:219], v[72:75], v[8:11]\n\t"
        "v_mfma_f32_16x16x32_bf16 v[12:15], a[220:223], v[72:75], v[12:15]\n\t"
        "ds_read_b128 v[68:71], v108 offset:18432\n\t"
        "v_mfma_f32_16x16x32_bf16 v[16:19], v[72:75], a[224:227], v[16:19]\n\t"
        "s_add_u32 m0, %[ld], 50176\n\t"
        "s_add_u32 %[t0], %[xo], 17152\n\t"
        "s_waitcnt lgkmcnt(6)\n\t"
        "v_mfma_f32_16x16x32_bf16 v[20:23], a[216:219], v[76:79], v[20:23]\n\t"
        "buffer_load_dwordx4 v100, %[sx], %[t0] offen lds\n\t"
        "v_mfma_f32_16x16x32_bf16 v[24:27], a[220:223], v[76:79], v[24:27]\n\t"
        "ds_read_b128 v[72:75], v107 offset:24576\n\t"
        "v_mfma_f32_16x16x32_bf16 v[28:31], v[76:79], a[224:227], v[28:31]\n\t"
        "s_waitcnt lgkmcnt(6)\n\t"
        "v_mfma_f32_16x16x32_bf16 v[8:11], a[228:231], v[80:83], v[8:11]\n\t"
        "v_mfma_f32_16x16x32_bf16 v[12:15], a[232:235], v[80:83], v[12:15]\n\t"
        "ds_read_b128 v[76:79], v107 offset:26624\n\t"
        "v_mfma_f32_16x16x32_bf16 v[16:19], v[80:83], a[236:239], v[16:19]\n\t"
        "s_add_u32 m0, %[ld], 57344\n\t"
        "s_add_u32 %[t0], %[xo], 896\n\t"
        "s_waitcnt lgkmcnt(6)\n\t"
        "v_mfma_f32_16x16x32_bf16 v[20:23], a[228:231], v[84:87], v[20:23]\n\t"
        "buffer_load_dwordx4 v100, %[sx], %[t0] offen lds\n\t"
        "v_mfma_f32_16x16x32_bf16 v[24:27], a[232:235], v[84:87], v[24:27]\n\t"
        "ds_read_b128 v[80:83], v108 offset:24576\n\t"
        "v_mfma_f32_16x16x32_bf16 v[28:31], v[84:87], a[236:239], v[28:31]\n\t"
        "s_waitcnt lgkmcnt(6)\n\t"
        "v_mfma_f32_16x16x32_bf16 v[8:11], a[240:243], v[56:59], v[8:11]\n\t"
        "v_mfma_f32_16x16x32_bf16 v[12:15], a[244:247], v[56:59], v[12:15]\n\t"
        "ds_read_b128 v[84:87], v108 offset:26624\n\t"
        "v_mfma_f32_16x16x32_bf16 v[16:19], v[56:59], a[248:251], v[16:19]\n\t"
        "s_add_u32 m0, %[ld], 58368\n\t"
        "s_add_u32 %[t0], %[xo], 17280\n\t"
        "s_waitcnt lgkmcnt(6)\n\t"
        "v_mfma_f32_16x16x32_bf16 v[20:23], a[240:243], v[60:63], v[20:23]\n\t"
        "buffer_load_dwordx4 v100, %[sx], %[t0] offen lds\n\t"
        "v_mfma_f32_16x16x32_bf16 v[24:27], a[244:247], v[60:63], v[24:27]\n\t"
        "v_mfma_f32_16x16x32_bf16 v[28:31], v[60:63], a[248:251], v[28:31]\n\t"
        "s_waitcnt lgkmcnt(5)\n\t"
        "v_mfma_f32_16x16x32_bf16 v[8:11], a[252:255], v[64:67], v[8:11]\n\t"
        "s_waitcnt vmcnt(17) lgkmcnt(0)\n\t"
        "s_barrier\n\t"
        "v_mfma_f32_16x16x32_bf16 v[12:15], v[128:131], v[64:67], v[12:15]\n\t"
        "ds_read_b128 v[56:59], v107 offset:32768\n\t"
        "v_mfma_f32_16x16x32_bf16 v[16:19], v[64:67], v[132:135], v[16:19]\n\t"
        "ds_read_b128 v[60:63], v107 offset:34816\n\t"
        "s_add_u32 m0, %[ld], 65536\n\t"
        "s_add_u32 %[t0], %[xo], 1024\n\t"
        "v_mfma_f32_16x16x32_bf16 v[20:23], a[252:255], v[68:71], v[20:23]\n\t"
        "buffer_load_dwordx4 v100, %[sx], %[t0] offen lds\n\t"
        "v_mfma_f32_16x16x32_bf16 v[24:27], v[128:131], v[68:71], v[24:27]\n\t"
        "ds_read_b128 v[64:67], v108 offset:32768\n\t"
        "v_mfma_f32_16x16x32_bf16 v[28:31], v[68:71], v[132:135], v[28:31]\n\t"
        "v_mfma_f32_16x16x32_bf16 v[8:11], v[136:139], v[72:75], v[8:11]\n\t"
        "v_mfma_f32_16x16x32_bf16 v[12:15], v[140:143], v[72:75], v[12:15]\n\t"
        "ds_read_b128 v[68:71], v108 offset:34816\n\t"
        "v_mfma_f32_16x16x32_bf16 v[16:19], v[72:75], v[144:147], v[16:19]\n\t"
        "s_add_u32 m0, %[ld], 66560\n\t"
        "s_add_u32 %[t0], %[xo], 17408\n\t"
        "v_mfma_f32_16x16x32_bf16 v[20:23], v[136:139], v[76:79], v[20:23]\n\t"
        "buffer_load_dwordx4 v100, %[sx], %[t0] offen lds\n\t"
        "v_mfma_f32_16x16x32_bf16 v[24:27], v[140:143], v[76:79], v[24:27]\n\t"
        "ds_read_b128 v[72:75], v107 offset:40960\n\t"
        "v_mfma_f32_16x16x32_bf16 v[28:31], v[76:79], v[144:147], v[28:31]\n\t"
        "v_mfma_f32_16x16x32_bf16 v[8:11], v[148:151], v[80:83], v[8:11]\n\t"
        "v_mfma_f32_16x16x32_bf16 v[12:15], v[152:155], v[80:83], v[12:15]\n\t"
        "ds_read_b128 v[76:79], v107 offset:43008\n\t"
        "v_mfma_f32_16x16x32_bf16 v[16:19], v[80:83], v[156:159], v[16:19]\n\t"
        "s_add_u32 m0, %[ld], 73728\n\t"
        "s_add_u32 %[t0], %[xo], 1152\n\t"
        "v_mfma_f32_16x16x32_bf16 v[20:23], v[148:151], v[84:87], v[20:23]\n\t"
        "buffer_load_dwordx4 v100, %[sx], %[t0] offen lds\n\t"
        "v_mfma_f32_16x16x32_bf16 v[24:27], v[152:155], v[84:87], v[24:27]\n\t"
        "ds_read_b128 v[80:83], v108 offset:40960\n\t"
        "v_mfma_f32_16x16x32_bf16 v[28:31], v[84:87], v[156:159], v[28:31]\n\t"
        "s_waitcnt lgkmcnt(6)\n\t"
        "v_mfma_f32_16x16x32_bf16 v[8:11], v[160:163], v[56:59], v[8:11]\n\t"
        "v_mfma_f32_16x16x32_bf16 v[12:15], v[164:167], v[56:59], v[12:15]\n\t"
        "ds_read_b128 v[84:87], v108 offset:43008\n\t"
        "v_mfma_f32_16x16x32_bf16 v[16:19], v[56:59], v[168:171], v[16:19]\n\t"
        "s_add_u32 m0, %[ld], 74752\n\t"
        "s_add_u32 %[t0], %[xo], 17536\n\t"
        "s_waitcnt lgkmcnt(6)\n\t"
        "v_mfma_f32_16x16x32_bf16 v[20:23], v[160:163], v[60:63], v[20:23]\n\t"
        "buffer_load_dwordx4 v100, %[sx], %[t0] offen lds\n\t"
        "v_mfma_f32_16x16x32_bf16 v[24:27], v[164:167], v[60:63], v[24:27]\n\t"
        "ds_read_b128 v[56:59], v107 offset:49152\n\t"
        "v_mfma_f32_16x16x32_bf16 v[28:31], v[60:63], v[168:171], v[28:31]\n\t"
        "s_waitcnt lgkmcnt(6)\n\t"
        "v_mfma_f32_16x16x32_bf16 v[8:11], v[172:175], v[64:67], v[8:11]\n\t"
        "v_mfma_f32_16x16x32_bf16 v[12:15], v[176:179], v[64:67], v[12:15]\n\t"
        "ds_read_b128 v[60:63], v107 offset:51200\n\t"
        "v_mfma_f32_16x16x32_bf16 v[16:19], v[64:67], v[180:183], v[16:19]\n\t"
        "s_add_u32 m0, %[ld], 81920\n\t"
        "s_add_u32 %[t0], %[xo], 1280\n\t"
        "s_waitcnt lgkmcnt(6)\n\t"
        "v_mfma_f32_16x16x32_bf16 v[20:23], v[172:175], v[68:71], v[20:23]\n\t"
        "buffer_load_dwordx4 v100, %[sx], %[t0] offen lds\n\t"
        "v_mfma_f32_16x16x32_bf16 v[24:27], v[176:179], v[68:71], v[24:27]\n\t"
        "ds_read_b128 v[64:67], v108 offset:49152\n\t"
        "v_mfma_f32_16x16x32_bf16 v[28:31], v[68:71], v[180:183], v[28:31]\n\t"
        "s_waitcnt lgkmcnt(6)\n\t"
        "v_mfma_f32_16x16x32_bf16 v[8:11], v[184:187], v[72:75], v[8:11]\n\t"
        "v_mfma_f32_16x16x32_bf16 v[12:15], v[188:191], v[72:75], v[12:15]\n\t"
        "ds_read_b128 v[68:71], v108 offset:51200\n\t"
        "v_mfma_f32_16x16x32_bf16 v[16:19], v[72:75], v[192:195], v[16:19]\n\t"
        "s_add_u32 m0, %[ld], 82944\n\t"
        "s_add_u32 %[t0], %[xo], 17664\n\t"
        "s_waitcnt lgkmcnt(6)\n\t"
        "v_mfma_f32_16x16x32_bf16 v[20:23], v[184:187], v[76:79], v[20:23]\n\t"
        "buffer_load_dwordx4 v100, %[sx], %[t0] offen lds\n\t"
        "v_mfma_f32_16x16x32_bf16 v[24:27], v[188:191], v[76:79], v[24:27]\n\t"
        "ds_read_b128 v[72:75], v107 offset:57344\n\t"
        "v_mfma_f32_16x16x32_bf16 v[28:31], v[76:79], v[192:195], v[28:31]\n\t"
        "s_waitcnt lgkmcnt(6)\n\t"
        "v_mfma_f32_16x16x32_bf16 v[8:11], v[196:199], v[80:83], v[8:11]\n\t"
        "v_mfma_f32_16x16x32_bf16 v[12:15], v[200:203], v[80:83], v[12:15]\n\t"
        "ds_read_b128 v[76:79], v107 offset:59392\n\t"
        "v_mfma_f32_16x16x32_bf16 v[16:19], v[80:83], v[204:207], v[16:19]\n\t"
        "s_add_u32 m0, %[ld], 90112\n\t"
        "s_add_u32 %[t0], %[xo], 1408\n\t"
        "s_waitcnt lgkmcnt(6)\n\t"
        "v_mfma_f32_16x16x32_bf16 v[20:23], v[196:199], v[84:87], v[20:23]\n\t"
        "buffer_load_dwordx4 v100, %[sx], %[t0] offen lds\n\t"
        "v_mfma_f32_16x16x32_bf16 v[24:27], v[200:203], v[84:87], v[24:27]\n\t"
        "ds_read_b128 v[80:83], v108 offset:57344\n\t"
        "v_mfma_f32_16x16x32_bf16 v[28:31], v[84:87], v[204:207], v[28:31]\n\t"
        "s_waitcnt lgkmcnt(6)\n\t"
        "v_mfma_f32_16x16x32_bf16 v[8:11], v[208:211], v[56:59], v[8:11]\n\t"
        "v_mfma_f32_16x16x32_bf16 v[12:15], v[212:215], v[56:59], v[12:15]\n\t"
        "ds_read_b128 v[84:87], v108 offset:59392\n\t"
        "v_mfma_f32_16x16x32_bf16 v[16:19], v[56:59], v[216:219], v[16:19]\n\t"
        "s_add_u32 m0, %[ld], 91136\n\t"
        "s_add_u32 %[t0], %[xo], 17792\n\t"
        "s_waitcnt lgkmcnt(6)\n\t"
        "v_mfma_f32_16x16x32_bf16 v[20:23], v[208:211], v[60:63], v[20:23]\n\t"
        "buffer_load_dwordx4 v100, %[sx], %[t0] offen lds\n\t"
        "v_mfma_f32_16x16x32_bf16 v[24:27], v[212:215], v[60:63], v[24:27]\n\t"
        "v_mfma_f32_16x16x32_bf16 v[28:31], v[60:63], v[216:219], v[28:31]\n\t"
        "s_waitcnt lgkmcnt(5)\n\t"
        "v_mfma_f32_16x16x32_bf16 v[8:11], v[220:223], v[64:67], v[8:11]\n\t"
        "s_waitcnt vmcnt(17) lgkmcnt(0)\n\t"
        "s_barrier\n\t"
        "v_mfma_f32_16x16x32_bf16 v[12:15], v[224:227], v[64:67], v[12:15]\n\t"
        "ds_read_b128 v[56:59], v105 offset:0\n\t"
        "v_mfma_f32_16x16x32_bf16 v[16:19], v[64:67], v[228:231], v[16:19]\n\t"
        "ds_read_b128 v[60:63], v105 offset:2048\n\t"
        "s_add_u32 m0, %[ld], 98304\n\t"
        "s_add_u32 %[t0], %[xo], 1536\n\t"
        "v_mfma_f32_16x16x32_bf16 v[20:23], v[220:223], v[68:71], v[20:23]\n\t"
        "buffer_load_dwordx4 v100, %[sx], %[t0] offen lds\n\t"
        "v_mfma_f32_16x16x32_bf16 v[24:27], v[224:227], v[68:71], v[24:27]\n\t"
        "ds_read_b128 v[64:67], v106 offset:0\n\t"
        "v_mfma_f32_16x16x32_bf16 v[28:31], v[68:71], v[228:231], v[28:31]\n\t"
        "v_mfma_f32_16x16x32_bf16 v[8:11], v[232:235], v[72:75], v[8:11]\n\t"
        "v_mfma_f32_16x16x32_bf16 v[12:15], v[236:239], v[72:75], v[12:15]\n\t"
        "ds_read_b128 v[68:71], v106 offset:2048\n\t"
        "v_mfma_f32_16x16x32_bf16 v[16:19], v[72:75], v[240:243], v[16:19]\n\t"
        "s_add_u32 m0, %[ld], 99328\n\t"
        "s_add_u32 %[t0], %[xo], 17920\n\t"
        "v_mfma_f32_16x16x32_bf16 v[20:23], v[232:235], v[76:79], v[20:23]\n\t"
        "buffer_load_dwordx4 v100, %[sx], %[t0] offen lds\n\t"
        "v_mfma_f32_16x16x32_bf16 v[24:27], v[236:239], v[76:79], v[24:27]\n\t"
        "ds_read_b128 v[72:75], v105 offset:8192\n\t"
        "v_mfma_f32_16x16x32_bf16 v[28:31], v[76:79], v[240:243], v[28:31]\n\t"
        "v_mfma_f32_16x16x32_bf16 v[8:11], v[244:247], v[80:83], v[8:11]\n\t"
        "v_mfma_f32_16x16x32_bf16 v[12:15], v[248:251], v[80:83], v[12:15]\n\t"
        "ds_read_b128 v[76:79], v105 offset:10240\n\t"
        "v_mfma_f32_16x16x32_bf16 v[16:19], v[80:83], v[252:255], v[16:19]\n\t"
        "s_add_u32 m0, %[ld], 106496\n\t"
        "s_add_u32 %[t0], %[xo], 1664\n\t"
        "v_mfma_f32_16x16x32_bf16 v[20:23], v[244:247], v[84:87], v[20:23]\n\t"
        "buffer_load_dwordx4 v100, %[sx], %[t0] offen lds\n\t"
        "v_mfma_f32_16x16x32_bf16 v[24:27], v[248:251], v[84:87], v[24:27]\n\t"
        "ds_read_b128 v[80:83], v106 offset:8192\n\t"
        "v_mfma_f32_16x16x32_bf16 v[28:31], v[84:87], v[252:255], v[28:31]\n\t"
        "LG_mid%=:\n\t"
        "s_waitcnt lgkmcnt(6)\n\t"
        "v_mfma_f32_16x16x32_bf16 v[32:35], a[0:3], v[56:59], 0\n\t"
        "v_mfma_f32_16x16x32_bf16 v[36:39], a[4:7], v[56:59], 0\n\t"
        "ds_read_b128 v[84:87], v106 offset:10240\n\t"
        "v_mfma_f32_16x16x32_bf16 v[40:43], v[56:59], a[8:11], 0\n\t"
        "s_add_u32 m0, %[ld], 107520\n\t"
        "s_add_u32 %[t0], %[xo], 18048\n\t"
        "s_waitcnt lgkmcnt(6)\n\t"
        "v_mfma_f32_16x16x32_bf16 v[44:47], a[0:3], v[60:63], 0\n\t"
        "buffer_load_dwordx4 v100, %[sx], %[t0] offen lds\n\t"
        "v_mfma_f32_16x16x32_bf16 v[48:51], a[4:7], v[60:63], 0\n\t"
        "ds_read_b128 v[56:59], v105 offset:16384\n\t"
        "v_mfma_f32_16x16x32_bf16 v[52:55], v[60:63], a[8:11], 0\n\t"
        "s_waitcnt lgkmcnt(6)\n\t"
        "v_mfma_f32_16x16x32_bf16 v[32:35], a[12:15], v[64:67], v[32:35]\n\t"
        "v_cvt_pk_bf16_f32 v88, v8, v9\n\t"
        "v_cvt_pk_bf16_f32 v89, v10, v11\n\t"
        "v_mfma_f32_16x16x32_bf16 v[36:39], a[16:19], v[64:67], v[36:39]\n\t"
        "ds_read_b128 v[60:63], v105 offset:18432\n\t"
        "v_cvt_pk_bf16_f32 v90, v12, v13\n\t"
        "v_mfma_f32_16x16x32_bf16 v[40:43], v[64:67], a[20:23], v[40:43]\n\t"
        "s_add_u32 m0, %[ld], 114688\n\t"
        "s_add_u32 %[t0], %[xo], 1792\n\t"
        "s_waitcnt lgkmcnt(6)\n\t"
        "v_mfma_f32_16x16x32_bf16 v[44:47], a[12:15], v[68:71], v[44:47]\n\t"
        "buffer_load_dwordx4 v100, %[sx], %[t0] offen lds\n\t"
        "v_mfma_f32_16x16x32_bf16 v[48:51], a[16:19], v[68:71], v[48:51]\n\t"
        "ds_read_b128 v[64:67], v106 offset:16384\n\t"
        "v_cvt_pk_bf16_f32 v91, v14, v15\n\t"
        "v_mfma_f32_16x16x32_bf16 v[52:55], v[68:71], a[20:23], v[52:55]\n\t"
        "buffer_store_dwordx4 v[88:91], v109, %[sw], %[so] offen sc1\n\t"
        "s_waitcnt lgkmcnt(6)\n\t"
        "v_mfma_f32_16x16x32_bf16 v[32:35], a[24:27], v[72:75], v[32:35]\n\t"
        "v_cvt_pk_bf16_f32 v92, v20, v21\n\t"
        "v_cvt_pk_bf16_f32 v93, v22, v23\n\t"
        "v_mfma_f32_16x16x32_bf16 v[36:39], a[28:31], v[72:75], v[36:39]\n\t"
        "ds_read_b128 v[68:71], v106 offset:18432\n\t"
        "v_cvt_pk_bf16_f32 v94, v24, v25\n\t"
        "v_mfma_f32_16x16x32_bf16 v[40:43], v[72:75], a[32:35], v[40:43]\n\t"
        "s_add_u32 m0, %[ld], 115712\n\t"
        "s_add_u32 %[t0], %[xo], 18176\n\t"
        "s_waitcnt lgkmcnt(6)\n\t"
        "v_mfma_f32_16x16x32_bf16 v[44:47], a[24:27], v[76:79], v[44:47]\n\t"
        "buffer_load_dwordx4 v100, %[sx], %[t0] offen lds\n\t"
        "v_mfma_f32_16x16x32_bf16 v[48:51], a[28:31], v[76:79], v[48:51]\n\t"
        "ds_read_b128 v[72:75], v105 offset:24576\n\t"
        "v_cvt_pk_bf16_f32 v95, v26, v27\n\t"
        "v_mfma_f32_16x16x32_bf16 v[52:55], v[76:79], a[32:35], v[52:55]\n\t"
        "buffer_store_dwordx4 v[92:95], v110, %[sw], %[so] offen sc1\n\t"
        "s_waitcnt lgkmcnt(6)\n\t"
        "v_mfma_f32_16x16x32_bf16 v[32:35], a[36:39], v[80:83], v[32:35]\n\t"
        "v_cvt_pk_bf16_f32 v96, v16, v17\n\t"
        "v_cvt_pk_bf16_f32 v97, v18, v19\n\t"
        "v_mfma_f32_16x16x32_bf16 v[36:39], a[40:43], v[80:83], v[36:39]\n\t"
        "ds_read_b128 v[76:79], v105 offset:26624\n\t"
        "v_cvt_pk_bf16_f32 v98, v28, v29\n\t"
        "v_mfma_f32_16x16x32_bf16 v[40:43], v[80:83], a[44:47], v[40:43]\n\t"
        "s_add_u32 m0, %[ld], 122880\n\t"
        "s_add_u32 %[t0], %[xo], 1920\n\t"
        "s_waitcnt lgkmcnt(6)\n\t"
        "v_mfma_f32_16x16x32_bf16 v[44:47], a[36:39], v[84:87], v[44:47]\n\t"
        "buffer_load_dwordx4 v100, %[sx], %[t0] offen lds\n\t"
        "v_mfma_f32_16x16x32_bf16 v[48:51], a[40:43], v[84:87], v[48:51]\n\t"
        "ds_read_b128 v[80:83], v106 offset:24576\n\t"
        "v_cvt_pk_bf16_f32 v99, v30, v31\n\t"
        "v_mfma_f32_16x16x32_bf16 v[52:55], v[84:87], a[44:47], v[52:55]\n\t"
        "buffer_store_dwordx4 v[96:99], v111, %[sw], %[so] offen sc1\n\t"
        "s_waitcnt lgkmcnt(6)\n\t"
        "v_mfma_f32_16x16x32_bf16 v[32:35], a[48:51], v[56:59], v[32:35]\n\t"
        "s_lshl_b32 %[so], %[cn], 19\n\t"
        "v_mfma_f32_16x16x32_bf16 v[36:39], a[52:55], v[56:59], v[36:39]\n\t"
        "ds_read_b128 v[84:87], v106 offset:26624\n\t"
        "v_mfma_f32_16x16x32_bf16 v[40:43], v[56:59], a[56:59], v[40:43]\n\t"
        "s_add_u32 m0, %[ld], 123904\n\t"
        "s_add_u32 %[t0], %[xo], 18304\n\t"
        "s_waitcnt lgkmcnt(6)\n\t"
        "v_mfma_f32_16x16x32_bf16 v[44:47], a[48:51], v[60:63], v[44:47]\n\t"
        "buffer_load_dwordx4 v100, %[sx], %[t0] offen lds\n\t"
        "v_mfma_f32_16x16x32_bf16 v[48:51], a[52:55], v[60:63], v[48:51]\n\t"
        "v_mfma_f32_16x16x32_bf16 v[52:55], v[60:63], a[56:59], v[52:55]\n\t"
        "s_waitcnt lgkmcnt(5)\n\t"
        "v_mfma_f32_16x16x32_bf16 v[32:35], a[60:63], v[64:67], v[32:35]\n\t"
        "s_waitcnt vmcnt(19) lgkmcnt(0)\n\t"
        "s_barrier\n\t"
        "s_add_u32 %[xo], %[xo], 0x20000\n\t"
        "v_mfma_f32_16x16x32_bf16 v[36:39], a[64:67], v[64:67], v[36:39]\n\t"
        "ds_read_b128 v[56:59], v105 offset:32768\n\t"
        "v_mfma_f32_16x16x32_bf16 v[40:43], v[64:67], a[68:71], v[40:43]\n\t"
        "ds_read_b128 v[60:63], v105 offset:34816\n\t"
        "s_add_u32 m0, %[ld], 0\n\t"
        "s_add_u32 %[t0], %[xo], 0\n\t"
        "v_mfma_f32_16x16x32_bf16 v[44:47], a[60:63], v[68:71], v[44:47]\n\t"
        "buffer_load_dwordx4 v100, %[sx], %[t0] offen lds\n\t"
        "v_mfma_f32_16x16x32_bf16 v[48:51], a[64:67], v[68:71], v[48:51]\n\t"
        "ds_read_b128 v[64:67], v106 offset:32768\n\t"
        "v_mfma_f32_16x16x32_bf16 v[52:55], v[68:71], a[68:71], v[52:55]\n\t"
        "v_mfma_f32_16x16x32_bf16 v[32:35], a[72:75], v[72:75], v[32:35]\n\t"
        "v_mfma_f32_16x16x32_bf16 v[36:39], a[76:79], v[72:75], v[36:39]\n\t"
        "ds_read_b128 v[68:71], v106 offset:34816\n\t"
        "v_mfma_f32_16x16x32_bf16 v[40:43], v[72:75], a[80:83], v[40:43]\n\t"
        "s_add_u32 m0, %[ld], 1024\n\t"
        "s_add_u32 %[t0], %[xo], 16384\n\t"
        "v_mfma_f32_16x16x32_bf16 v[44:47], a[72:75], v[76:79], v[44:47]\n\t"
        "buffer_load_dwordx4 v100, %[sx], %[t0] offen lds\n\t"
        "v_mfma_f32_16x16x32_bf16 v[48:51], a[76:79], v[76:79], v[48:51]\n\t"
        "ds_read_b128 v[72:75], v105 offset:40960\n\t"
        "v_mfma_f32_16x16x32_bf16 v[52:55], v[76:79], a[80:83], v[52:55]\n\t"
        "v_mfma_f32_16x16x32_bf16 v[32:35], a[84:87], v[80:83], v[32:35]\n\t"
        "v_mfma_f32_16x16x32_bf16 v[36:39], a[88:91], v[80:83], v[36:39]\n\t"
        "ds_read_b128 v[76:79], v105 offset:43008\n\t"
        "v_mfma_f32_16x16x32_bf16 v[40:43], v[80:83], a[92:95], v[40:43]\n\t"
        "s_add_u32 m0, %[ld], 8192\n\t"
        "s_add_u32 %[t0], %[xo], 128\n\t"
        "v_mfma_f32_16x16x32_bf16 v[44:47], a[84:87], v[84:87], v[44:47]\n\t"
        "buffer_load_dwordx4 v100, %[sx], %[t0] offen lds\n\t"
        "v_mfma_f32_16x16x32_bf16 v[48:51], a[88:91], v[84:87], v[48:51]\n\t"
        "ds_read_b128 v[80:83], v106 offset:40960\n\t"
        "v_mfma_f32_16x16x32_bf16 v[52:55], v[84:87], a[92:95], v[52:55]\n\t"
        "s_waitcnt lgkmcnt(6)\n\t"
        "v_mfma_f32_16x16x32_bf16 v[32:35], a[96:99], v[56:59], v[32:35]\n\t"
        "v_mfma_f32_16x16x32_bf16 v[36:39], a[100:103], v[56:59], v[36:39]\n\t"
        "ds_read_b128 v[84:87], v106 offset:43008\n\t"
        "v_mfma_f32_16x16x32_bf16 v[40:43], v[56:59], a[104:107], v[40:43]\n\t"
        "s_add_u32 m0, %[ld], 9216\n\t"
        "s_add_u32 %[t0], %[xo], 16512\n\t"
        "s_waitcnt lgkmcnt(6)\n\t"
        "v_mfma_f32_16x16x32_bf16 v[44:47], a[96:99], v[60:63], v[44:47]\n\t"
        "buffer_load_dwordx4 v100, %[sx], %[t0] offen lds\n\t"
        "v_mfma_f32_16x16x32_bf16 v[48:51], a[100:103], v[60:63], v[48:51]\n\t"
        "ds_read_b128 v[56:59], v105 offset:49152\n\t"
        "v_mfma_f32_16x16x32_bf16 v[52:55], v[60:63], a[104:107], v[52:55]\n\t"
        "s_waitcnt lgkmcnt(6)\n\t"
        "v_mfma_f32_16x16x32_bf16 v[32:35], a[108:111], v[64:67], v[32:35]\n\t"
        "v_mfma_f32_16x16x32_bf16 v[36:39], a[112:115], v[64:67], v[36:39]\n\t"
        "ds_read_b128 v[60:63], v105 offset:51200\n\t"
        "v_mfma_f32_16x16x32_bf16 v[40:43], v[64:67], a[116:119], v[40:43]\n\t"
        "s_add_u32 m0, %[ld], 16384\n\t"
        "s_add_u32 %[t0], %[xo], 256\n\t"
        "s_waitcnt lgkmcnt(6)\n\t"
        "v_mfma_f32_16x16x32_bf16 v[44:47], a[108:111], v[68:71], v[44:47]\n\t"
        "buffer_load_dwordx4 v100, %[sx], %[t0] offen lds\n\t"
        "v_mfma_f32_16x16x32_bf16 v[48:51], a[112:115], v[68:71], v[48:51]\n\t"
        "ds_read_b128 v[64:67], v106 offset:49152\n\t"
        "v_mfma_f32_16x16x32_bf16 v[52:55], v[68:71], a[116:119], v[52:55]\n\t"
        "s_waitcnt lgkmcnt(6)\n\t"
        "v_mfma_f32_16x16x32_bf16 v[32:35], a[120:123], v[72:75], v[32:35]\n\t"
        "v_mfma_f32_16x16x32_bf16 v[36:39], a[124:127], v[72:75], v[36:39]\n\t"
        "ds_read_b128 v[68:71], v106 offset:51200\n\t"
        "v_mfma_f32_16x16x32_bf16 v[40:43], v[72:75], a[128:131], v[40:43]\n\t"
        "s_add_u32 m0, %[ld], 17408\n\t"
        "s_add_u32 %[t0], %[xo], 16640\n\t"
        "s_waitcnt lgkmcnt(6)\n\t"
        "v_mfma_f32_16x16x32_bf16 v[44:47], a[120:123], v[76:79], v[44:47]\n\t"
        "buffer_load_dwordx4 v100, %[sx], %[t0] offen lds\n\t"
        "v_mfma_f32_16x16x32_bf16 v[48:51], a[124:127], v[76:79], v[48:51]\n\t"
        "ds_read_b128 v[72:75], v105 offset:57344\n\t"
        "v_mfma_f32_16x16x32_bf16 v[52:55], v[76:79], a[128:131], v[52:55]\n\t"
        "s_waitcnt lgkmcnt(6)\n\t"
        "v_mfma_f32_16x16x32_bf16 v[32:35], a[132:135], v[80:83], v[32:35]\n\t"
        "v_mfma_f32_16x16x32_bf16 v[36:39], a[136:139], v[80:83], v[36:39]\n\t"
        "ds_read_b128 v[76:79], v105 offset:59392\n\t"
        "v_mfma_f32_16x16x32_bf16 v[40:43], v[80:83], a[140:143], v[40:43]\n\t"
        "s_add_u32 m0, %[ld], 24576\n\t"
        "s_add_u32 %[t0], %[xo], 384\n\t"
        "s_waitcnt lgkmcnt(6)\n\t"
        "v_mfma_f32_16x16x32_bf16 v[44:47], a[132:135], v[84:87], v[44:47]\n\t"
        "buffer_load_dwordx4 v100, %[sx], %[t0] offen lds\n\t"
        "v_mfma_f32_16x16x32_bf16 v[48:51], a[136:139], v[84:87], v[48:51]\n\t"
        "ds_read_b128 v[80:83], v106 offset:57344\n\t"
        "v_mfma_f32_16x16x32_bf16 v[52:55], v[84:87], a[140:143], v[52:55]\n\t"
        "s_waitcnt lgkmcnt(6)\n\t"
        "v_mfma_f32_16x16x32_bf16 v[32:35], a[144:147], v[56:59], v[32:35]\n\t"
        "v_mfma_f32_16x16x32_bf16 v[36:39], a[148:151], v[56:59], v[36:39]\n\t"
        "ds_read_b128 v[84:87], v106 offset:59392\n\t"
        "v_mfma_f32_16x16x32_bf16 v[40:43], v[56:59], a[152:155], v[40:43]\n\t"
        "s_add_u32 m0, %[ld], 25600\n\t"
        "s_add_u32 %[t0], %[xo], 16768\n\t"
        "s_waitcnt lgkmcnt(6)\n\t"
        "v_mfma_f32_16x16x32_bf16 v[44:47], a[144:147], v[60:63], v[44:47]\n\t"
        "buffer_load_dwordx4 v100, %[sx], %[t0] offen lds\n\t"
        "v_mfma_f32_16x16x32_bf16 v[48:51], a[148:151], v[60:63], v[48:51]\n\t"
        "v_mfma_f32_16x16x32_bf16 v[52:55], v[60:63], a[152:155], v[52:55]\n\t"
        "s_waitcnt lgkmcnt(5)\n\t"
        "v_mfma_f32_16x16x32_bf16 v[32:35], a[156:159], v[64:67], v[32:35]\n\t"
        "s_waitcnt vmcnt(9) lgkmcnt(0)\n\t"
        "s_barrier\n\t"
        "v_mfma_f32_16x16x32_bf16 v[36:39], a[160:163], v[64:67], v[36:39]\n\t"
        "ds_read_b128 v[56:59], v107 offset:0\n\t"
        "v_mfma_f32_16x16x32_bf16 v[40:43], v[64:67], a[164:167], v[40:43]\n\t"
        "ds_read_b128 v[60:63], v107 offset:2048\n\t"
        "s_add_u32 m0, %[ld], 32768\n\t"
        "s_add_u32 %[t0], %[xo], 512\n\t"
        "v_mfma_f32_16x16x32_bf16 v[44:47], a[156:159], v[68:71], v[44:47]\n\t"
        "buffer_load_dwordx4 v100, %[sx], %[t0] offen lds\n\t"
        "v_mfma_f32_16x16x32_bf16 v[48:51], a[160:163], v[68:71], v[48:51]\n\t"
        "ds_read_b128 v[64:67], v108 offset:0\n\t"
        "v_mfma_f32_16x16x32_bf16 v[52:55], v[68:71], a[164:167], v[52:55]\n\t"
        "v_mfma_f32_16x16x32_bf16 v[32:35], a[168:171], v[72:75], v[32:35]\n\t"
        "v_mfma_f32_16x16x32_bf16 v[36:39], a[172:175], v[72:75], v[36:39]\n\t"
        "ds_read_b128 v[68:71], v108 offset:2048\n\t"
        "v_mov_b32_e32 v117, %[cn]\n\t"
        "buffer_store_dword v117, v113, %[sw], 0 offen sc1\n\t"
        "s_add_u32 %[cn], %[cn], 1\n\t"
        "v_mfma_f32_16x16x32_bf16 v[40:43], v[72:75], a[176:179], v[40:43]\n\t"
        "s_add_u32 m0, %[ld], 33792\n\t"
        "s_add_u32 %[t0], %[xo], 16896\n\t"
        "v_mfma_f32_16x16x32_bf16 v[44:47], a[168:171], v[76:79], v[44:47]\n\t"
        "buffer_load_dwordx4 v100, %[sx], %[t0] offen lds\n\t"
        "v_mfma_f32_16x16x32_bf16 v[48:51], a[172:175], v[76:79], v[48:51]\n\t"
        "ds_read_b128 v[72:75], v107 offset:8192\n\t"
        "v_mfma_f32_16x16x32_bf16 v[52:55], v[76:79], a[176:179], v[52:55]\n\t"
        "v_mfma_f32_16x16x32_bf16 v[32:35], a[180:183], v[80:83], v[32:35]\n\t"
        "v_mfma_f32_16x16x32_bf16 v[36:39], a[184:187], v[80:83], v[36:39]\n\t"
        "ds_read_b128 v[76:79], v107 offset:10240\n\t"
        "v_mfma_f32_16x16x32_bf16 v[40:43], v[80:83], a[188:191], v[40:43]\n\t"
        "s_add_u32 m0, %[ld], 40960\n\t"
        "s_add_u32 %[t0], %[xo], 640\n\t"
        "v_mfma_f32_16x16x32_bf16 v[44:47], a[180:183], v[84:87], v[44:47]\n\t"
        "buffer_load_dwordx4 v100, %[sx], %[t0] offen lds\n\t"
        "v_mfma_f32_16x16x32_bf16 v[48:51], a[184:187], v[84:87], v[48:51]\n\t"
        "ds_read_b128 v[80:83], v108 offset:8192\n\t"
        "v_mfma_f32_16x16x32_bf16 v[52:55], v[84:87], a[188:191], v[52:55]\n\t"
        "s_waitcnt lgkmcnt(6)\n\t"
        "v_mfma_f32_16x16x32_bf16 v[32:35], a[192:195], v[56:59], v[32:35]\n\t"
        "v_mfma_f32_16x16x32_bf16 v[36:39], a[196:199], v[56:59], v[36:39]\n\t"
        "ds_read_b128 v[84:87], v108 offset:10240\n\t"
        "v_mfma_f32_16x16x32_bf16 v[40:43], v[56:59], a[200:203], v[40:43]\n\t"
        "s_add_u32 m0, %[ld], 41984\n\t"
        "s_add_u32 %[t0], %[xo], 17024\n\t"
        "s_waitcnt lgkmcnt(6)\n\t"
        "v_mfma_f32_16x16x32_bf16 v[44:47], a[192:195], v[60:63], v[44:47]\n\t"
        "buffer_load_dwordx4 v100, %[sx], %[t0] offen lds\n\t"
        "v_mfma_f32_16x16x32_bf16 v[48:51], a[196:199], v[60:63], v[48:51]\n\t"
        "ds_read_b128 v[56:59], v107 offset:16384\n\t"
        "v_mfma_f32_16x16x32_bf16 v[52:55], v[60:63], a[200:203], v[52:55]\n\t"
        "s_waitcnt lgkmcnt(6)\n\t"
        "v_mfma_f32_16x16x32_bf16 v[32:35], a[204:207], v[64:67], v[32:35]\n\t"
        "v_mfma_f32_16x16x32_bf16 v[36:39], a[208:211], v[64:67], v[36:39]\n\t"
        "ds_read_b128 v[60:63], v107 offset:18432\n\t"
        "v_mfma_f32_16x16x32_bf16 v[40:43], v[64:67], a[212:215], v[40:43]\n\t"
        "s_add_u32 m0, %[ld], 49152\n\t"
        "s_add_u32 %[t0], %[xo], 768\n\t"
        "s_waitcnt lgkmcnt(6)\n\t"
        "v_mfma_f32_16x16x32_bf16 v[44:47], a[204:207], v[68:71], v[44:47]\n\t"
        "buffer_load_dwordx4 v100, %[sx], %[t0] offen lds\n\t"
        "v_mfma_f32_16x16x32_bf16 v[48:51], a[208:211], v[68:71], v[48:51]\n\t"
        "ds_read_b128 v[64:67], v108 offset:16384\n\t"
        "v_mfma_f32_16x16x32_bf16 v[52:55], v[68:71], a[212:215], v[52:55]\n\t"
        "s_waitcnt lgkmcnt(6)\n\t"
        "v_mfma_f32_16x16x32_bf16 v[32:35], a[216:219], v[72:75], v[32:35]\n\t"
        "v_mfma_f32_16x16x32_bf16 v[36:39], a[220:223], v[72:75], v[36:39]\n\t"
        "ds_read_b128 v[68:71], v108 offset:18432\n\t"
        "v_mfma_f32_16x16x32_bf16 v[40:43], v[72:75], a[224:227], v[40:43]\n\t"
        "s_add_u32 m0, %[ld], 50176\n\t"
        "s_add_u32 %[t0], %[xo], 17152\n\t"
        "s_waitcnt lgkmcnt(6)\n\t"
        "v_mfma_f32_16x16x32_bf16 v[44:47], a[216:219], v[76:79], v[44:47]\n\t"
        "buffer_load_dwordx4 v100, %[sx], %[t0] offen lds\n\t"
        "v_mfma_f32_16x16x32_bf16 v[48:51], a[220:223], v[76:79], v[48:51]\n\t"
        "ds_read_b128 v[72:75], v107 offset:24576\n\t"
        "v_mfma_f32_16x16x32_bf16 v[52:55], v[76:79], a[224:227], v[52:55]\n\t"
        "s_waitcnt lgkmcnt(6)\n\t"
        "v_mfma_f32_16x16x32_bf16 v[32:35], a[228:231], v[80:83], v[32:35]\n\t"
        "v_mfma_f32_16x16x32_bf16 v[36:39], a[232:235], v[80:83], v[36:39]\n\t"
        "ds_read_b128 v[76:79], v107 offset:26624\n\t"
        "v_mfma_f32_16x16x32_bf16 v[40:43], v[80:83], a[236:239], v[40:43]\n\t"
        "s_add_u32 m0, %[ld], 57344\n\t"
        "s_add_u32 %[t0], %[xo], 896\n\t"
        "s_waitcnt lgkmcnt(6)\n\t"
        "v_mfma_f32_16x16x32_bf16 v[44:47], a[228:231], v[84:87], v[44:47]\n\t"
        "buffer_load_dwordx4 v100, %[sx], %[t0] offen lds\n\t"
        "v_mfma_f32_16x16x32_bf16 v[48:51], a[232:235], v[84:87], v[48:51]\n\t"
        "ds_read_b128 v[80:83], v108 offset:24576\n\t"
        "v_mfma_f32_16x16x32_bf16 v[52:55], v[84:87], a[236:239], v[52:55]\n\t"
        "s_waitcnt lgkmcnt(6)\n\t"
        "v_mfma_f32_16x16x32_bf16 v[32:35], a[240:243], v[56:59], v[32:35]\n\t"
        "v_mfma_f32_16x16x32_bf16 v[36:39], a[244:247], v[56:59], v[36:39]\n\t"
        "ds_read_b128 v[84:87], v108 offset:26624\n\t"
        "v_mfma_f32_16x16x32_bf16 v[40:43], v[56:59], a[248:251], v[40:43]\n\t"
        "s_add_u32 m0, %[ld], 58368\n\t"
        "s_add_u32 %[t0], %[xo], 17280\n\t"
        "s_waitcnt lgkmcnt(6)\n\t"
        "v_mfma_f32_16x16x32_bf16 v[44:47], a[240:243], v[60:63], v[44:47]\n\t"
        "buffer_load_dwordx4 v100, %[sx], %[t0] offen lds\n\t"
        "v_mfma_f32_16x16x32_bf16 v[48:51], a[244:247], v[60:63], v[48:51]\n\t"
        "v_mfma_f32_16x16x32_bf16 v[52:55], v[60:63], a[248:251], v[52:55]\n\t"
        "s_waitcnt lgkmcnt(5)\n\t"
        "v_mfma_f32_16x16x32_bf16 v[32:35], a[252:255], v[64:67], v[32:35]\n\t"
        "s_waitcnt vmcnt(17) lgkmcnt(0)\n\t"
        "s_barrier\n\t"
        "v_mfma_f32_16x16x32_bf16 v[36:39], v[128:131], v[64:67], v[36:39]\n\t"
        "ds_read_b128 v[56:59], v107 offset:32768\n\t"
        "v_mfma_f32_16x16x32_bf16 v[40:43], v[64:67], v[132:135], v[40:43]\n\t"
        "ds_read_b128 v[60:63], v107 offset:34816\n\t"
        "s_add_u32 m0, %[ld], 65536\n\t"
        "s_add_u32 %[t0], %[xo], 1024\n\t"
        "v_mfma_f32_16x16x32_bf16 v[44:47], a[252:255], v[68:71], v[44:47]\n\t"
        "buffer_load_dwordx4 v100, %[sx], %[t0] offen lds\n\t"
        "v_mfma_f32_16x16x32_bf16 v[48:51], v[128:131], v[68:71], v[48:51]\n\t"
        "ds_read_b128 v[64:67], v108 offset:32768\n\t"
        "v_mfma_f32_16x16x32_bf16 v[52:55], v[68:71], v[132:135], v[52:55]\n\t"
        "v_mfma_f32_16x16x32_bf16 v[32:35], v[136:139], v[72:75], v[32:35]\n\t"
        "v_mfma_f32_16x16x32_bf16 v[36:39], v[140:143], v[72:75], v[36:39]\n\t"
        "ds_read_b128 v[68:71], v108 offset:34816\n\t"
        "v_mfma_f32_16x16x32_bf16 v[40:43], v[72:75], v[144:147], v[40:43]\n\t"
        "s_add_u32 m0, %[ld], 66560\n\t"
        "s_add_u32 %[t0], %[xo], 17408\n\t"
        "v_mfma_f32_16x16x32_bf16 v[44:47], v[136:139], v[76:79], v[44:47]\n\t"
        "buffer_load_dwordx4 v100, %[sx], %[t0] offen lds\n\t"
        "v_mfma_f32_16x16x32_bf16 v[48:51], v[140:143], v[76:79], v[48:51]\n\t"
        "ds_read_b128 v[72:75], v107 offset:40960\n\t"
        "v_mfma_f32_16x16x32_bf16 v[52:55], v[76:79], v[144:147], v[52:55]\n\t"
        "v_mfma_f32_16x16x32_bf16 v[32:35], v[148:151], v[80:83], v[32:35]\n\t"
        "v_mfma_f32_16x16x32_bf16 v[36:39], v[152:155], v[80:83], v[36:39]\n\t"
        "ds_read_b128 v[76:79], v107 offset:43008\n\t"
        "v_mfma_f32_16x16x32_bf16 v[40:43], v[80:83], v[156:159], v[40:43]\n\t"
        "s_add_u32 m0, %[ld], 73728\n\t"
        "s_add_u32 %[t0], %[xo], 1152\n\t"
        "v_mfma_f32_16x16x32_bf16 v[44:47], v[148:151], v[84:87], v[44:47]\n\t"
        "buffer_load_dwordx4 v100, %[sx], %[t0] offen lds\n\t"
        "v_mfma_f32_16x16x32_bf16 v[48:51], v[152:155], v[84:87], v[48:51]\n\t"
        "ds_read_b128 v[80:83], v108 offset:40960\n\t"
        "v_mfma_f32_16x16x32_bf16 v[52:55], v[84:87], v[156:159], v[52:55]\n\t"
        "s_waitcnt lgkmcnt(6)\n\t"
        "v_mfma_f32_16x16x32_bf16 v[32:35], v[160:163], v[56:59], v[32:35]\n\t"
        "v_mfma_f32_16x16x32_bf16 v[36:39], v[164:167], v[56:59], v[36:39]\n\t"
        "ds_read_b128 v[84:87], v108 offset:43008\n\t"
        "v_mfma_f32_16x16x32_bf16 v[40:43], v[56:59], v[168:171], v[40:43]\n\t"
        "s_add_u32 m0, %[ld], 74752\n\t"
        "s_add_u32 %[t0], %[xo], 17536\n\t"
        "s_waitcnt lgkmcnt(6)\n\t"
        "v_mfma_f32_16x16x32_bf16 v[44:47], v[160:163], v[60:63], v[44:47]\n\t"
        "buffer_load_dwordx4 v100, %[sx], %[t0] offen lds\n\t"
        "v_mfma_f32_16x16x32_bf16 v[48:51], v[164:167], v[60:63], v[48:51]\n\t"
        "ds_read_b128 v[56:59], v107 offset:49152\n\t"
        "v_mfma_f32_16x16x32_bf16 v[52:55], v[60:63], v[168:171], v[52:55]\n\t"
        "s_waitcnt lgkmcnt(6)\n\t"
        "v_mfma_f32_16x16x32_bf16 v[32:35], v[172:175], v[64:67], v[32:35]\n\t"
        "v_mfma_f32_16x16x32_bf16 v[36:39], v[176:179], v[64:67], v[36:39]\n\t"
        "ds_read_b128 v[60:63], v107 offset:51200\n\t"
        "v_mfma_f32_16x16x32_bf16 v[40:43], v[64:67], v[180:183], v[40:43]\n\t"
        "s_add_u32 m0, %[ld], 81920\n\t"
        "s_add_u32 %[t0], %[xo], 1280\n\t"
        "s_waitcnt lgkmcnt(6)\n\t"
        "v_mfma_f32_16x16x32_bf16 v[44:47], v[172:175], v[68:71], v[44:47]\n\t"
        "buffer_load_dwordx4 v100, %[sx], %[t0] offen lds\n\t"
        "v_mfma_f32_16x16x32_bf16 v[48:51], v[176:179], v[68:71], v[48:51]\n\t"
        "ds_read_b128 v[64:67], v108 offset:49152\n\t"
        "v_mfma_f32_16x16x32_bf16 v[52:55], v[68:71], v[180:183], v[52:55]\n\t"
        "s_waitcnt lgkmcnt(6)\n\t"
        "v_mfma_f32_16x16x32_bf16 v[32:35], v[184:187], v[72:75], v[32:35]\n\t"
        "v_mfma_f32_16x16x32_bf16 v[36:39], v[188:191], v[72:75], v[36:39]\n\t"
        "ds_read_b128 v[68:71], v108 offset:51200\n\t"
        "v_mfma_f32_16x16x32_bf16 v[40:43], v[72:75], v[192:195], v[40:43]\n\t"
        "s_add_u32 m0, %[ld], 82944\n\t"
        "s_add_u32 %[t0], %[xo], 17664\n\t"
        "s_waitcnt lgkmcnt(6)\n\t"
        "v_mfma_f32_16x16x32_bf16 v[44:47], v[184:187], v[76:79], v[44:47]\n\t"
        "buffer_load_dwordx4 v100, %[sx], %[t0] offen lds\n\t"
        "v_mfma_f32_16x16x32_bf16 v[48:51], v[188:191], v[76:79], v[48:51]\n\t"
        "ds_read_b128 v[72:75], v107 offset:57344\n\t"
        "v_mfma_f32_16x16x32_bf16 v[52:55], v[76:79], v[192:195], v[52:55]\n\t"
        "s_waitcnt lgkmcnt(6)\n\t"
        "v_mfma_f32_16x16x32_bf16 v[32:35], v[196:199], v[80:83], v[32:35]\n\t"
        "v_mfma_f32_16x16x32_bf16 v[36:39], v[200:203], v[80:83], v[36:39]\n\t"
        "ds_read_b128 v[76:79], v107 offset:59392\n\t"
        "v_mfma_f32_16x16x32_bf16 v[40:43], v[80:83], v[204:207], v[40:43]\n\t"
        "s_add_u32 m0, %[ld], 90112\n\t"
        "s_add_u32 %[t0], %[xo], 1408\n\t"
        "s_waitcnt lgkmcnt(6)\n\t"
        "v_mfma_f32_16x16x32_bf16 v[44:47], v[196:199], v[84:87], v[44:47]\n\t"
        "buffer_load_dwordx4 v100, %[sx], %[t0] offen lds\n\t"
        "v_mfma_f32_16x16x32_bf16 v[48:51], v[200:203], v[84:87], v[48:51]\n\t"
        "ds_read_b128 v[80:83], v108 offset:57344\n\t"
        "v_mfma_f32_16x16x32_bf16 v[52:55], v[84:87], v[204:207], v[52:55]\n\t"
        "s_waitcnt lgkmcnt(6)\n\t"
        "v_mfma_f32_16x16x32_bf16 v[32:35], v[208:211], v[56:59], v[32:35]\n\t"
        "v_mfma_f32_16x16x32_bf16 v[36:39], v[212:215], v[56:59], v[36:39]\n\t"
        "ds_read_b128 v[84:87], v108 offset:59392\n\t"
        "v_mfma_f32_16x16x32_bf16 v[40:43], v[56:59], v[216:219], v[40:43]\n\t"
        "s_add_u32 m0, %[ld], 91136\n\t"
        "s_add_u32 %[t0], %[xo], 17792\n\t"
        "s_waitcnt lgkmcnt(6)\n\t"
        "v_mfma_f32_16x16x32_bf16 v[44:47], v[208:211], v[60:63], v[44:47]\n\t"
        "buffer_load_dwordx4 v100, %[sx], %[t0] offen lds\n\t"
        "v_mfma_f32_16x16x32_bf16 v[48:51], v[212:215], v[60:63], v[48:51]\n\t"
        "v_mfma_f32_16x16x32_bf16 v[52:55], v[60:63], v[216:219], v[52:55]\n\t"
        "s_waitcnt lgkmcnt(5)\n\t"
        "v_mfma_f32_16x16x32_bf16 v[32:35], v[220:223], v[64:67], v[32:35]\n\t"
        "s_waitcnt vmcnt(17) lgkmcnt(0)\n\t"
        "s_barrier\n\t"
        "v_mfma_f32_16x16x32_bf16 v[36:39], v[224:227], v[64:67], v[36:39]\n\t"
        "ds_read_b128 v[56:59], v105 offset:0\n\t"
        "v_mfma_f32_16x16x32_bf16 v[40:43], v[64:67], v[228:231], v[40:43]\n\t"
        "ds_read_b128 v[60:63], v105 offset:2048\n\t"
        "s_add_u32 m0, %[ld], 98304\n\t"
        "s_add_u32 %[t0], %[xo], 1536\n\t"
        "v_mfma_f32_16x16x32_bf16 v[44:47], v[220:223], v[68:71], v[44:47]\n\t"
        "buffer_load_dwordx4 v100, %[sx], %[t0] offen lds\n\t"
        "v_mfma_f32_16x16x32_bf16 v[48:51], v[224:227], v[68:71], v[48:51]\n\t"
        "ds_read_b128 v[64:67], v106 offset:0\n\t"
        "v_mfma_f32_16x16x32_bf16 v[52:55], v[68:71], v[228:231], v[52:55]\n\t"
        "v_mfma_f32_16x16x32_bf16 v[32:35], v[232:235], v[72:75], v[32:35]\n\t"
        "v_mfma_f32_16x16x32_bf16 v[36:39], v[236:239], v[72:75], v[36:39]\n\t"
        "ds_read_b128 v[68:71], v106 offset:2048\n\t"
        "v_mfma_f32_16x16x32_bf16 v[40:43], v[72:75], v[240:243], v[40:43]\n\t"
        "s_add_u32 m0, %[ld], 99328\n\t"
        "s_add_u32 %[t0], %[xo], 17920\n\t"
        "v_mfma_f32_16x16x32_bf16 v[44:47], v[232:235], v[76:79], v[44:47]\n\t"
        "buffer_load_dwordx4 v100, %[sx], %[t0] offen lds\n\t"
        "v_mfma_f32_16x16x32_bf16 v[48:51], v[236:239], v[76:79], v[48:51]\n\t"
        "ds_read_b128 v[72:75], v105 offset:8192\n\t"
        "v_mfma_f32_16x16x32_bf16 v[52:55], v[76:79], v[240:243], v[52:55]\n\t"
        "v_mfma_f32_16x16x32_bf16 v[32:35], v[244:247], v[80:83], v[32:35]\n\t"
        "v_mfma_f32_16x16x32_bf16 v[36:39], v[248:251], v[80:83], v[36:39]\n\t"
        "ds_read_b128 v[76:79], v105 offset:10240\n\t"
        "v_mfma_f32_16x16x32_bf16 v[40:43], v[80:83], v[252:255], v[40:43]\n\t"
        "s_add_u32 m0, %[ld], 106496\n\t"
        "s_add_u32 %[t0], %[xo], 1664\n\t"
        "v_mfma_f32_16x16x32_bf16 v[44:47], v[244:247], v[84:87], v[44:47]\n\t"
        "buffer_load_dwordx4 v100, %[sx], %[t0] offen lds\n\t"
        "v_mfma_f32_16x16x32_bf16 v[48:51], v[248:251], v[84:87], v[48:51]\n\t"
        "ds_read_b128 v[80:83], v106 offset:8192\n\t"
        "v_mfma_f32_16x16x32_bf16 v[52:55], v[84:87], v[252:255], v[52:55]\n\t"
        "s_cmp_lt_u32 %[cn], 32\n\t"
        "s_cbranch_scc1 LG_loop%=\n\t"
        "s_nop 7\n\t"
        "v_cvt_pk_bf16_f32 v88, v32, v33\n\t"
        "v_cvt_pk_bf16_f32 v89, v34, v35\n\t"
        "v_cvt_pk_bf16_f32 v90, v36, v37\n\t"
        "v_cvt_pk_bf16_f32 v91, v38, v39\n\t"
        "buffer_store_dwordx4 v[88:91], v109, %[sw], %[so] offen sc1\n\t"
        "v_cvt_pk_bf16_f32 v92, v44, v45\n\t"
        "v_cvt_pk_bf16_f32 v93, v46, v47\n\t"
        "v_cvt_pk_bf16_f32 v94, v48, v49\n\t"
        "v_cvt_pk_bf16_f32 v95, v50, v51\n\t"
        "buffer_store_dwordx4 v[92:95], v110, %[sw], %[so] offen sc1\n\t"
        "v_cvt_pk_bf16_f32 v96, v40, v41\n\t"
        "v_cvt_pk_bf16_f32 v97, v42, v43\n\t"
        "v_cvt_pk_bf16_f32 v98, v52, v53\n\t"
        "v_cvt_pk_bf16_f32 v99, v54, v55\n\t"
        "buffer_store_dwordx4 v[96:99], v111, %[sw], %[so] offen sc1\n\t"
        "s_waitcnt vmcnt(0) lgkmcnt(0)\n\t"
        "v_mov_b32_e32 v117, %[cn]\n\t"
        "buffer_store_dword v117, v113, %[sw], 0 offen sc1\n\t"
        "s_barrier\n\t"
        "LG_end%=:\n\t"
        ".Lk_end_G:\n\t"
        : [t0] "=&s"(t0), [xo] "=&s"(xo), [so] "=&s"(so), [cn] "=&s"(cn), [ts] "=&s"(ts), [ta] "=&s"(ta), [tb] "=&s"(tb), [tc] "=&s"(tc), [td] "=&s"(td)
        : [sx] "s"(sx), [sq] "s"(sq), [sv] "s"(sv), [sw] "s"(sw), [ld] "s"(lb + w * 2048),
          "{v100}"((16 * w + (l >> 3)) * 2048 + (((l & 7) ^ (l >> 3)) << 4)), "{v101}"((8 * w + (l >> 3)) * 8192 + h * 256 + (c & 1) * 128 + (l & 7) * 16),
          "{v112}"((16 * w + (l >> 2)) * 8192 + h * 256 + cv * 64 + (l & 3) * 16), "{v102}"(lb + 131072 + w * 1024 + ((l * 16) ^ ((l & 16) << 2))), "{v116}"(lb + 131072 + w * 1024 + ((l * 16) ^ (l & 32))),
          "{v103}"(lr + rr * 128 + ((tn ^ (n >> 3)) * 64) + (n & 3) * 16), "{v104}"(lr + 8192 + rr * 64 + ((tn ^ (g & 1)) * 32) + (n & 3) * 8), "{v105}"(lx), "{v106}"(lx ^ 64u), "{v107}"(lx + 65536), "{v108}"((lx ^ 64u) + 65536),
          "{v109}"(aq), "{v110}"(aq + 4096), "{v111}"(W_VF + (tm * 32 + h) * 8192 + (2 * cv + tn) * 1024 + l * 16), "{v113}"(l == 0 ? F_KVQ + h * 1024 + (c * 4 + w) * 64 : OOB), "{v[114:115]}"(tp)
        : "memory", "scc", "m0", "v8", "v9", "v10", "v11", "v12", "v13", "v14", "v15", "v16", "v17", "v18", "v19", "v20", "v21", "v22", "v23", "v24", "v25", "v26", "v27", "v28", "v29", "v30", "v31", "v32", "v33", "v34", "v35", "v36", "v37", "v38", "v39", "v40", "v41", "v42", "v43", "v44", "v45", "v46", "v47", "v48", "v49", "v50", "v51", "v52", "v53", "v54", "v55", "v56", "v57", "v58", "v59", "v60", "v61", "v62", "v63", "v64", "v65", "v66", "v67", "v68", "v69", "v70", "v71", "v72", "v73", "v74", "v75", "v76", "v77", "v78", "v79", "v80", "v81", "v82", "v83", "v84", "v85", "v86", "v87", "v88", "v89", "v90", "v91", "v92", "v93", "v94", "v95", "v96", "v97", "v98", "v99", "v117", "v118", "v119", "v120", "v121", "v122", "v123", "v124", "v125", "v126", "v127", "v128", "v129", "v130", "v131", "v132", "v133", "v134", "v135", "v136", "v137", "v138", "v139", "v140", "v141", "v142", "v143", "v144", "v145", "v146", "v147", "v148", "v149", "v150", "v151", "v152", "v153", "v154", "v155", "v156", "v157", "v158", "v159", "v160", "v161", "v162", "v163", "v164", "v165", "v166", "v167", "v168", "v169", "v170", "v171", "v172", "v173", "v174", "v175", "v176", "v177", "v178", "v179", "v180", "v181", "v182", "v183", "v184", "v185", "v186", "v187", "v188", "v189", "v190", "v191", "v192", "v193", "v194", "v195", "v196", "v197", "v198", "v199", "v200", "v201", "v202", "v203", "v204", "v205", "v206", "v207", "v208", "v209", "v210", "v211", "v212", "v213", "v214", "v215", "v216", "v217", "v218", "v219", "v220", "v221", "v222", "v223", "v224", "v225", "v226", "v227", "v228", "v229", "v230", "v231", "v232", "v233", "v234", "v235", "v236", "v237", "v238", "v239", "v240", "v241", "v242", "v243", "v244", "v245", "v246", "v247", "v248", "v249", "v250", "v251", "v252", "v253", "v254", "v255", "a0", "a1", "a2", "a3", "a4", "a5", "a6", "a7", "a8", "a9", "a10", "a11", "a12", "a13", "a14", "a15", "a16", "a17", "a18", "a19", "a20", "a21", "a22", "a23", "a24", "a25", "a26", "a27", "a28", "a29", "a30", "a31", "a32", "a33", "a34", "a35", "a36", "a37", "a38", "a39", "a40", "a41", "a42", "a43", "a44", "a45", "a46", "a47", "a48", "a49", "a50", "a51", "a52", "a53", "a54", "a55", "a56", "a57", "a58", "a59", "a60", "a61", "a62", "a63", "a64", "a65", "a66", "a67", "a68", "a69", "a70", "a71", "a72", "a73", "a74", "a75", "a76", "a77", "a78", "a79", "a80", "a81", "a82", "a83", "a84", "a85", "a86", "a87", "a88", "a89", "a90", "a91", "a92", "a93", "a94", "a95", "a96", "a97", "a98", "a99", "a100", "a101", "a102", "a103", "a104", "a105", "a106", "a107", "a108", "a109", "a110", "a111", "a112", "a113", "a114", "a115", "a116", "a117", "a118", "a119", "a120", "a121", "a122", "a123", "a124", "a125", "a126", "a127", "a128", "a129", "a130", "a131", "a132", "a133", "a134", "a135", "a136", "a137", "a138", "a139", "a140", "a141", "a142", "a143", "a144", "a145", "a146", "a147", "a148", "a149", "a150", "a151", "a152", "a153", "a154", "a155", "a156", "a157", "a158", "a159", "a160", "a161", "a162", "a163", "a164", "a165", "a166", "a167", "a168", "a169", "a170", "a171", "a172", "a173", "a174", "a175", "a176", "a177", "a178", "a179", "a180", "a181", "a182", "a183", "a184", "a185", "a186", "a187", "a188", "a189", "a190", "a191", "a192", "a193", "a194", "a195", "a196", "a197", "a198", "a199", "a200", "a201", "a202", "a203", "a204", "a205", "a206", "a207", "a208", "a209", "a210", "a211", "a212", "a213", "a214", "a215", "a216", "a217", "a218", "a219", "a220", "a221", "a222", "a223", "a224", "a225", "a226", "a227", "a228", "a229", "a230", "a231", "a232", "a233", "a234", "a235", "a236", "a237", "a238", "a239", "a240", "a241", "a242", "a243", "a244", "a245", "a246", "a247", "a248", "a249", "a250", "a251", "a252", "a253", "a254", "a255");
}

__device__ __forceinline__ void out_block(const Params& p, unsigned char* lds, const int wv, const int rp, const int nt, const int j) {
    const int w = wv, l = otid() & 63, n = l & 15, g = l >> 4;
    const unsigned lb = __builtin_amdgcn_readfirstlane((unsigned)(size_t)lds), stg = lb + 131072 + w * 8192;
    const v4u so = mk_srd(p.Wo, 1024 * 4096 * 2), sw = mk_srd(p.ws, W_TOTAL), su = mk_srd(p.out, S * D * 2);
    const unsigned lr = stg + (4 * g + (n >> 2)) * 128 + ((2 * (g & 1) + (n >> 3)) << 5) + (n & 3) * 8;
    unsigned t0, t1, tl = rp, pg, cu, nx, sp; unsigned long long tu;
    asm volatile(
        "s_add_u32 %[t1], %[ro], 0\n\t"
        "s_and_b32 %[t1], %[t1], 7\n\t"
        "s_lshl_b32 %[t1], %[t1], 18\n\t"
        "s_add_u32 %[t0], %[t1], 0\n\t"
        "buffer_load_dwordx4 a[0:3], v110, %[so], %[t0] offen\n\t"
        "s_sleep 2\n\t"
        "s_add_u32 %[t0], %[t1], 16384\n\t"
        "buffer_load_dwordx4 a[4:7], v110, %[so], %[t0] offen\n\t"
        "s_sleep 2\n\t"
        "s_add_u32 %[t0], %[t1], 32768\n\t"
        "buffer_load_dwordx4 a[8:11], v110, %[so], %[t0] offen\n\t"
        "s_sleep 2\n\t"
        "s_add_u32 %[t0], %[t1], 49152\n\t"
        "buffer_load_dwordx4 a[12:15], v110, %[so], %[t0] offen\n\t"
        "s_sleep 2\n\t"
        "s_add_u32 %[t0], %[t1], 65536\n\t"
        "buffer_load_dwordx4 a[16:19], v110, %[so], %[t0] offen\n\t"
        "s_sleep 2\n\t"
        "s_add_u32 %[t0], %[t1], 81920\n\t"
        "buffer_load_dwordx4 a[20:23], v110, %[so], %[t0] offen\n\t"
        "s_sleep 2\n\t"
        "s_add_u32 %[t0], %[t1], 98304\n\t"
        "buffer_load_dwordx4 v[24:27], v110, %[so], %[t0] offen\n\t"
        "s_sleep 2\n\t"
        "s_add_u32 %[t0], %[t1], 114688\n\t"
        "buffer_load_dwordx4 v[28:31], v110, %[so], %[t0] offen\n\t"
        "s_sleep 2\n\t"
        "s_add_u32 %[t1], %[ro], 0\n\t"
        "s_and_b32 %[t1], %[t1], 7\n\t"
        "s_lshl_b32 %[t1], %[t1], 18\n\t"
        "s_add_u32 %[t0], %[t1], 131072\n\t"
        "buffer_load_dwordx4 a[24:27], v110, %[so], %[t0] offen\n\t"
        "s_sleep 2\n\t"
        "s_add_u32 %[t0], %[t1], 147456\n\t"
        "buffer_load_dwordx4 a[28:31], v110, %[so], %[t0] offen\n\t"
        "s_sleep 2\n\t"
        "s_add_u32 %[t0], %[t1], 163840\n\t"
        "buffer_load_dwordx4 a[32:35], v110, %[so], %[t0] offen\n\t"
        "s_sleep 2\n\t"
        "s_add_u32 %[t0], %[t1], 180224\n\t"
        "buffer_load_dwordx4 a[36:39], v110, %[so], %[t0] offen\n\t"
        "s_sleep 2\n\t"
        "s_add_u32 %[t0], %[t1], 196608\n\t"
        "buffer_load_dwordx4 a[40:43], v110, %[so], %[t0] offen\n\t"
        "s_sleep 2\n\t"
        "s_add_u32 %[t0], %[t1], 212992\n\t"
        "buffer_load_dwordx4 a[44:47], v110, %[so], %[t0] offen\n\t"
        "s_sleep 2\n\t"
        "s_add_u32 %[t0], %[t1], 229376\n\t"
        "buffer_load_dwordx4 v[32:35], v110, %[so], %[t0] offen\n\t"
        "s_sleep 2\n\t"
        "s_add_u32 %[t0], %[t1], 245760\n\t"
        "buffer_load_dwordx4 v[36:39], v110, %[so], %[t0] offen\n\t"
        "s_sleep 2\n\t"
        "s_waitcnt vmcnt(8)\n\t"
        "ds_write_b128 v111, a[0:3] offset:0\n\t"
        "ds_write_b128 v111, a[4:7] offset:1024\n\t"
        "ds_write_b128 v111, a[8:11] offset:2048\n\t"
        "ds_write_b128 v111, a[12:15] offset:3072\n\t"
        "ds_write_b128 v111, a[16:19] offset:4096\n\t"
        "ds_write_b128 v111, a[20:23] offset:5120\n\t"
        "ds_write_b128 v111, v[24:27] offset:6144\n\t"
        "ds_write_b128 v111, v[28:31] offset:7168\n\t"
        "ds_read_b64_tr_b16 a[0:1], v112 offset:0\n\t"
        "ds_read_b64_tr_b16 a[2:3], v112 offset:2048\n\t"
        "ds_read_b64_tr_b16 a[12:13], v112 offset:4096\n\t"
        "ds_read_b64_tr_b16 a[14:15], v112 offset:6144\n\t"
        "s_waitcnt lgkmcnt(8)\n\t"
        "ds_read_b64_tr_b16 a[4:5], v113 offset:0\n\t"
        "ds_read_b64_tr_b16 a[6:7], v113 offset:2048\n\t"
        "ds_read_b64_tr_b16 a[16:17], v113 offset:4096\n\t"
        "ds_read_b64_tr_b16 a[18:19], v113 offset:6144\n\t"
        "s_waitcnt lgkmcnt(8)\n\t"
        "ds_read_b64_tr_b16 a[8:9], v114 offset:0\n\t"
        "ds_read_b64_tr_b16 a[10:11], v114 offset:2048\n\t"
        "ds_read_b64_tr_b16 a[20:21], v114 offset:4096\n\t"
        "ds_read_b64_tr_b16 a[22:23], v114 offset:6144\n\t"
        "s_waitcnt lgkmcnt(8)\n\t"
        "ds_read_b64_tr_b16 v[56:57], v115 offset:0\n\t"
        "ds_read_b64_tr_b16 v[58:59], v115 offset:2048\n\t"
        "ds_read_b64_tr_b16 v[60:61], v115 offset:4096\n\t"
        "ds_read_b64_tr_b16 v[62:63], v115 offset:6144\n\t"
        "s_add_u32 %[t1], %[ro], 1\n\t"
        "s_and_b32 %[t1], %[t1], 7\n\t"
        "s_lshl_b32 %[t1], %[t1], 18\n\t"
        "s_add_u32 %[t0], %[t1], 0\n\t"
        "buffer_load_dwordx4 a[48:51], v110, %[so], %[t0] offen\n\t"
        "s_sleep 2\n\t"
        "s_add_u32 %[t0], %[t1], 16384\n\t"
        "buffer_load_dwordx4 a[52:55], v110, %[so], %[t0] offen\n\t"
        "s_sleep 2\n\t"
        "s_add_u32 %[t0], %[t1], 32768\n\t"
        "buffer_load_dwordx4 a[56:59], v110, %[so], %[t0] offen\n\t"
        "s_sleep 2\n\t"
        "s_add_u32 %[t0], %[t1], 49152\n\t"
        "buffer_load_dwordx4 a[60:63], v110, %[so], %[t0] offen\n\t"
        "s_sleep 2\n\t"
        "s_add_u32 %[t0], %[t1], 65536\n\t"
        "buffer_load_dwordx4 a[64:67], v110, %[so], %[t0] offen\n\t"
        "s_sleep 2\n\t"
        "s_add_u32 %[t0], %[t1], 81920\n\t"
        "buffer_load_dwordx4 a[68:71], v110, %[so], %[t0] offen\n\t"
        "s_sleep 2\n\t"
        "s_add_u32 %[t0], %[t1], 98304\n\t"
        "buffer_load_dwordx4 v[40:43], v110, %[so], %[t0] offen\n\t"
        "s_sleep 2\n\t"
        "s_add_u32 %[t0], %[t1], 114688\n\t"
        "buffer_load_dwordx4 v[44:47], v110, %[so], %[t0] offen\n\t"
        "s_sleep 2\n\t"
        "s_waitcnt lgkmcnt(0)\n\t"
        "ds_write_b128 v116, v[56:59] offset:0\n\t"
        "ds_write_b128 v116, v[60:63] offset:1024\n\t"
        "s_waitcnt vmcnt(8)\n\t"
        "ds_write_b128 v111, a[24:27] offset:0\n\t"
        "ds_write_b128 v111, a[28:31] offset:1024\n\t"
        "ds_write_b128 v111, a[32:35] offset:2048\n\t"
        "ds_write_b128 v111, a[36:39] offset:3072\n\t"
        "ds_write_b128 v111, a[40:43] offset:4096\n\t"
        "ds_write_b128 v111, a[44:47] offset:5120\n\t"
        "ds_write_b128 v111, v[32:35] offset:6144\n\t"
        "ds_write_b128 v111, v[36:39] offset:7168\n\t"
        "ds_read_b64_tr_b16 a[24:25], v112 offset:0\n\t"
        "ds_read_b64_tr_b16 a[26:27], v112 offset:2048\n\t"
        "ds_read_b64_tr_b16 a[36:37], v112 offset:4096\n\t"
        "ds_read_b64_tr_b16 a[38:39], v112 offset:6144\n\t"
        "s_waitcnt lgkmcnt(8)\n\t"
        "ds_read_b64_tr_b16 a[28:29], v113 offset:0\n\t"
        "ds_read_b64_tr_b16 a[30:31], v113 offset:2048\n\t"
        "ds_read_b64_tr_b16 a[40:41], v113 offset:4096\n\t"
        "ds_read_b64_tr_b16 a[42:43], v113 offset:6144\n\t"
        "s_waitcnt lgkmcnt(8)\n\t"
        "ds_read_b64_tr_b16 a[32:33], v114 offset:0\n\t"
        "ds_read_b64_tr_b16 a[34:35], v114 offset:2048\n\t"
        "ds_read_b64_tr_b16 a[44:45], v114 offset:4096\n\t"
        "ds_read_b64_tr_b16 a[46:47], v114 offset:6144\n\t"
        "s_waitcnt lgkmcnt(8)\n\t"
        "ds_read_b64_tr_b16 v[56:57], v115 offset:0\n\t"
        "ds_read_b64_tr_b16 v[58:59], v115 offset:2048\n\t"
        "ds_read_b64_tr_b16 v[60:61], v115 offset:4096\n\t"
        "ds_read_b64_tr_b16 v[62:63], v115 offset:6144\n\t"
        "s_add_u32 %[t1], %[ro], 1\n\t"
        "s_and_b32 %[t1], %[t1], 7\n\t"
        "s_lshl_b32 %[t1], %[t1], 18\n\t"
        "s_add_u32 %[t0], %[t1], 131072\n\t"
        "buffer_load_dwordx4 a[72:75], v110, %[so], %[t0] offen\n\t"
        "s_sleep 2\n\t"
        "s_add_u32 %[t0], %[t1], 147456\n\t"
        "buffer_load_dwordx4 a[76:79], v110, %[so], %[t0] offen\n\t"
        "s_sleep 2\n\t"
        "s_add_u32 %[t0], %[t1], 163840\n\t"
        "buffer_load_dwordx4 a[80:83], v110, %[so], %[t0] offen\n\t"
        "s_sleep 2\n\t"
        "s_add_u32 %[t0], %[t1], 180224\n\t"
        "buffer_load_dwordx4 a[84:87], v110, %[so], %[t0] offen\n\t"
        "s_sleep 2\n\t"
        "s_add_u32 %[t0], %[t1], 196608\n\t"
        "buffer_load_dwordx4 a[88:91], v110, %[so], %[t0] offen\n\t"
        "s_sleep 2\n\t"
        "s_add_u32 %[t0], %[t1], 212992\n\t"
        "buffer_load_dwordx4 a[92:95], v110, %[so], %[t0] offen\n\t"
        "s_sleep 2\n\t"
        "s_add_u32 %[t0], %[t1], 229376\n\t"
        "buffer_load_dwordx4 v[48:51], v110, %[so], %[t0] offen\n\t"
        "s_sleep 2\n\t"
        "s_add_u32 %[t0], %[t1], 245760\n\t"
        "buffer_load_dwordx4 v[52:55], v110, %[so], %[t0] offen\n\t"
        "s_sleep 2\n\t"
        "s_waitcnt lgkmcnt(0)\n\t"
        "ds_write_b128 v116, v[56:59] offset:2048\n\t"
        "ds_write_b128 v116, v[60:63] offset:3072\n\t"
        "s_waitcnt vmcnt(8)\n\t"
        "ds_write_b128 v111, a[48:51] offset:0\n\t"
        "ds_write_b128 v111, a[52:55] offset:1024\n\t"
        "ds_write_b128 v111, a[56:59] offset:2048\n\t"
        "ds_write_b128 v111, a[60:63] offset:3072\n\t"
        "ds_write_b128 v111, a[64:67] offset:4096\n\t"
        "ds_write_b128 v111, a[68:71] offset:5120\n\t"
        "ds_write_b128 v111, v[40:43] offset:6144\n\t"
        "ds_write_b128 v111, v[44:47] offset:7168\n\t"
        "ds_read_b64_tr_b16 a[48:49], v112 offset:0\n\t"
        "ds_read_b64_tr_b16 a[50:51], v112 offset:2048\n\t"
        "ds_read_b64_tr_b16 a[60:61], v112 offset:4096\n\t"
        "ds_read_b64_tr_b16 a[62:63], v112 offset:6144\n\t"
        "s_waitcnt lgkmcnt(8)\n\t"
        "ds_read_b64_tr_b16 a[52:53], v113 offset:0\n\t"
        "ds_read_b64_tr_b16 a[54:55], v113 offset:2048\n\t"
        "ds_read_b64_tr_b16 a[64:65], v113 offset:4096\n\t"
        "ds_read_b64_tr_b16 a[66:67], v113 offset:6144\n\t"
        "s_waitcnt lgkmcnt(8)\n\t"
        "ds_read_b64_tr_b16 a[56:57], v114 offset:0\n\t"
        "ds_read_b64_tr_b16 a[58:59], v114 offset:2048\n\t"
        "ds_read_b64_tr_b16 a[68:69], v114 offset:4096\n\t"
        "ds_read_b64_tr_b16 a[70:71], v114 offset:6144\n\t"
        "s_waitcnt lgkmcnt(8)\n\t"
        "ds_read_b64_tr_b16 v[56:57], v115 offset:0\n\t"
        "ds_read_b64_tr_b16 v[58:59], v115 offset:2048\n\t"
        "ds_read_b64_tr_b16 v[60:61], v115 offset:4096\n\t"
        "ds_read_b64_tr_b16 v[62:63], v115 offset:6144\n\t"
        "s_add_u32 %[t1], %[ro], 2\n\t"
        "s_and_b32 %[t1], %[t1], 7\n\t"
        "s_lshl_b32 %[t1], %[t1], 18\n\t"
        "s_add_u32 %[t0], %[t1], 0\n\t"
        "buffer_load_dwordx4 a[96:99], v110, %[so], %[t0] offen\n\t"
        "s_sleep 2\n\t"
        "s_add_u32 %[t0], %[t1], 16384\n\t"
        "buffer_load_dwordx4 a[100:103], v110, %[so], %[t0] offen\n\t"
        "s_sleep 2\n\t"
        "s_add_u32 %[t0], %[t1], 32768\n\t"
        "buffer_load_dwordx4 a[104:107], v110, %[so], %[t0] offen\n\t"
        "s_sleep 2\n\t"
        "s_add_u32 %[t0], %[t1], 49152\n\t"
        "buffer_load_dwordx4 a[108:111], v110, %[so], %[t0] offen\n\t"
        "s_sleep 2\n\t"
        "s_add_u32 %[t0], %[t1], 65536\n\t"
        "buffer_load_dwordx4 a[112:115], v110, %[so], %[t0] offen\n\t"
        "s_sleep 2\n\t"
        "s_add_u32 %[t0], %[t1], 81920\n\t"
        "buffer_load_dwordx4 a[116:119], v110, %[so], %[t0] offen\n\t"
        "s_sleep 2\n\t"
        "s_add_u32 %[t0], %[t1], 98304\n\t"
        "buffer_load_dwordx4 v[24:27], v110, %[so], %[t0] offen\n\t"
        "s_sleep 2\n\t"
        "s_add_u32 %[t0], %[t1], 114688\n\t"
        "buffer_load_dwordx4 v[28:31], v110, %[so], %[t0] offen\n\t"
        "s_sleep 2\n\t"
        "s_waitcnt lgkmcnt(0)\n\t"
        "ds_write_b128 v116, v[56:59] offset:4096\n\t"
        "ds_write_b128 v116, v[60:63] offset:5120\n\t"
        "s_waitcnt vmcnt(8)\n\t"
        "ds_write_b128 v111, a[72:75] offset:0\n\t"
        "ds_write_b128 v111, a[76:79] offset:1024\n\t"
        "ds_write_b128 v111, a[80:83] offset:2048\n\t"
        "ds_write_b128 v111, a[84:87] offset:3072\n\t"
        "ds_write_b128 v111, a[88:91] offset:4096\n\t"
        "ds_write_b128 v111, a[92:95] offset:5120\n\t"
        "ds_write_b128 v111, v[48:51] offset:6144\n\t"
        "ds_write_b128 v111, v[52:55] offset:7168\n\t"
        "ds_read_b64_tr_b16 a[72:73], v112 offset:0\n\t"
        "ds_read_b64_tr_b16 a[74:75], v112 offset:2048\n\t"
        "ds_read_b64_tr_b16 a[84:85], v112 offset:4096\n\t"
        "ds_read_b64_tr_b16 a[86:87], v112 offset:6144\n\t"
        "s_waitcnt lgkmcnt(8)\n\t"
        "ds_read_b64_tr_b16 a[76:77], v113 offset:0\n\t"
        "ds_read_b64_tr_b16 a[78:79], v113 offset:2048\n\t"
        "ds_read_b64_tr_b16 a[88:89], v113 offset:4096\n\t"
        "ds_read_b64_tr_b16 a[90:91], v113 offset:6144\n\t"
        "s_waitcnt lgkmcnt(8)\n\t"
        "ds_read_b64_tr_b16 a[80:81], v114 offset:0\n\t"
        "ds_read_b64_tr_b16 a[82:83], v114 offset:2048\n\t"
        "ds_read_b64_tr_b16 a[92:93], v114 offset:4096\n\t"
        "ds_read_b64_tr_b16 a[94:95], v114 offset:6144\n\t"
        "s_waitcnt lgkmcnt(8)\n\t"
        "ds_read_b64_tr_b16 v[56:57], v115 offset:0\n\t"
        "ds_read_b64_tr_b16 v[58:59], v115 offset:2048\n\t"
        "ds_read_b64_tr_b16 v[60:61], v115 offset:4096\n\t"
        "ds_read_b64_tr_b16 v[62:63], v115 offset:6144\n\t"
        "s_add_u32 %[t1], %[ro], 2\n\t"
        "s_and_b32 %[t1], %[t1], 7\n\t"
        "s_lshl_b32 %[t1], %[t1], 18\n\t"
        "s_add_u32 %[t0], %[t1], 131072\n\t"
        "buffer_load_dwordx4 a[120:123], v110, %[so], %[t0] offen\n\t"
        "s_sleep 2\n\t"
        "s_add_u32 %[t0], %[t1], 147456\n\t"
        "buffer_load_dwordx4 a[124:127], v110, %[so], %[t0] offen\n\t"
        "s_sleep 2\n\t"
        "s_add_u32 %[t0], %[t1], 163840\n\t"
        "buffer_load_dwordx4 a[128:131], v110, %[so], %[t0] offen\n\t"
        "s_sleep 2\n\t"
        "s_add_u32 %[t0], %[t1], 180224\n\t"
        "buffer_load_dwordx4 a[132:135], v110, %[so], %[t0] offen\n\t"
        "s_sleep 2\n\t"
        "s_add_u32 %[t0], %[t1], 196608\n\t"
        "buffer_load_dwordx4 a[136:139], v110, %[so], %[t0] offen\n\t"
        "s_sleep 2\n\t"
        "s_add_u32 %[t0], %[t1], 212992\n\t"
        "buffer_load_dwordx4 a[140:143], v110, %[so], %[t0] offen\n\t"
        "s_sleep 2\n\t"
        "s_add_u32 %[t0], %[t1], 229376\n\t"
        "buffer_load_dwordx4 v[32:35], v110, %[so], %[t0] offen\n\t"
        "s_sleep 2\n\t"
        "s_add_u32 %[t0], %[t1], 245760\n\t"
        "buffer_load_dwordx4 v[36:39], v110, %[so], %[t0] offen\n\t"
        "s_sleep 2\n\t"
        "s_waitcnt lgkmcnt(0)\n\t"
        "ds_write_b128 v116, v[56:59] offset:6144\n\t"
        "ds_write_b128 v116, v[60:63] offset:7168\n\t"
        "s_waitcnt vmcnt(8)\n\t"
        "ds_write_b128 v111, a[96:99] offset:0\n\t"
        "ds_write_b128 v111, a[100:103] offset:1024\n\t"
        "ds_write_b128 v111, a[104:107] offset:2048\n\t"
        "ds_write_b128 v111, a[108:111] offset:3072\n\t"
        "ds_write_b128 v111, a[112:115] offset:4096\n\t"
        "ds_write_b128 v111, a[116:119] offset:5120\n\t"
        "ds_write_b128 v111, v[24:27] offset:6144\n\t"
        "ds_write_b128 v111, v[28:31] offset:7168\n\t"
        "ds_read_b64_tr_b16 a[96:97], v112 offset:0\n\t"
        "ds_read_b64_tr_b16 a[98:99], v112 offset:2048\n\t"
        "ds_read_b64_tr_b16 a[108:109], v112 offset:4096\n\t"
        "ds_read_b64_tr_b16 a[110:111], v112 offset:6144\n\t"
        "s_waitcnt lgkmcnt(8)\n\t"
        "ds_read_b64_tr_b16 a[100:101], v113 offset:0\n\t"
        "ds_read_b64_tr_b16 a[102:103], v113 offset:2048\n\t"
        "ds_read_b64_tr_b16 a[112:113], v113 offset:4096\n\t"
        "ds_read_b64_tr_b16 a[114:115], v113 offset:6144\n\t"
        "s_waitcnt lgkmcnt(8)\n\t"
        "ds_read_b64_tr_b16 a[104:105], v114 offset:0\n\t"
        "ds_read_b64_tr_b16 a[106:107], v114 offset:2048\n\t"
        "ds_read_b64_tr_b16 a[116:117], v114 offset:4096\n\t"
        "ds_read_b64_tr_b16 a[118:119], v114 offset:6144\n\t"
        "s_waitcnt lgkmcnt(8)\n\t"
        "ds_read_b64_tr_b16 v[56:57], v115 offset:0\n\t"
        "ds_read_b64_tr_b16 v[58:59], v115 offset:2048\n\t"
        "ds_read_b64_tr_b16 v[60:61], v115 offset:4096\n\t"
        "ds_read_b64_tr_b16 v[62:63], v115 offset:6144\n\t"
        "s_add_u32 %[t1], %[ro], 3\n\t"
        "s_and_b32 %[t1], %[t1], 7\n\t"
        "s_lshl_b32 %[t1], %[t1], 18\n\t"
        "s_add_u32 %[t0], %[t1], 0\n\t"
        "buffer_load_dwordx4 a[144:147], v110, %[so], %[t0] offen\n\t"
        "s_sleep 2\n\t"
        "s_add_u32 %[t0], %[t1], 16384\n\t"
        "buffer_load_dwordx4 a[148:151], v110, %[so], %[t0] offen\n\t"
        "s_sleep 2\n\t"
        "s_add_u32 %[t0], %[t1], 32768\n\t"
        "buffer_load_dwordx4 a[152:155], v110, %[so], %[t0] offen\n\t"
        "s_sleep 2\n\t"
        "s_add_u32 %[t0], %[t1], 49152\n\t"
        "buffer_load_dwordx4 a[156:159], v110, %[so], %[t0] offen\n\t"
        "s_sleep 2\n\t"
        "s_add_u32 %[t0], %[t1], 65536\n\t"
        "buffer_load_dwordx4 a[160:163], v110, %[so], %[t0] offen\n\t"
        "s_sleep 2\n\t"
        "s_add_u32 %[t0], %[t1], 81920\n\t"
        "buffer_load_dwordx4 a[164:167], v110, %[so], %[t0] offen\n\t"
        "s_sleep 2\n\t"
        "s_add_u32 %[t0], %[t1], 98304\n\t"
        "buffer_load_dwordx4 v[40:43], v110, %[so], %[t0] offen\n\t"
        "s_sleep 2\n\t"
        "s_add_u32 %[t0], %[t1], 114688\n\t"
        "buffer_load_dwordx4 v[44:47], v110, %[so], %[t0] offen\n\t"
        "s_sleep 2\n\t"
        "s_waitcnt lgkmcnt(0)\n\t"
        "ds_write_b128 v116, v[56:59] offset:8192\n\t"
        "ds_write_b128 v116, v[60:63] offset:9216\n\t"
        "s_waitcnt vmcnt(8)\n\t"
        "ds_write_b128 v111, a[120:123] offset:0\n\t"
        "ds_write_b128 v111, a[124:127] offset:1024\n\t"
        "ds_write_b128 v111, a[128:131] offset:2048\n\t"
        "ds_write_b128 v111, a[132:135] offset:3072\n\t"
        "ds_write_b128 v111, a[136:139] offset:4096\n\t"
        "ds_write_b128 v111, a[140:143] offset:5120\n\t"
        "ds_write_b128 v111, v[32:35] offset:6144\n\t"
        "ds_write_b128 v111, v[36:39] offset:7168\n\t"
        "ds_read_b64_tr_b16 a[120:121], v112 offset:0\n\t"
        "ds_read_b64_tr_b16 a[122:123], v112 offset:2048\n\t"
        "ds_read_b64_tr_b16 a[132:133], v112 offset:4096\n\t"
        "ds_read_b64_tr_b16 a[134:135], v112 offset:6144\n\t"
        "s_waitcnt lgkmcnt(8)\n\t"
        "ds_read_b64_tr_b16 a[124:125], v113 offset:0\n\t"
        "ds_read_b64_tr_b16 a[126:127], v113 offset:2048\n\t"
        "ds_read_b64_tr_b16 a[136:137], v113 offset:4096\n\t"
        "ds_read_b64_tr_b16 a[138:139], v113 offset:6144\n\t"
        "s_waitcnt lgkmcnt(8)\n\t"
        "ds_read_b64_tr_b16 a[128:129], v114 offset:0\n\t"
        "ds_read_b64_tr_b16 a[130:131], v114 offset:2048\n\t"
        "ds_read_b64_tr_b16 a[140:141], v114 offset:4096\n\t"
        "ds_read_b64_tr_b16 a[142:143], v114 offset:6144\n\t"
        "s_waitcnt lgkmcnt(8)\n\t"
        "ds_read_b64_tr_b16 v[56:57], v115 offset:0\n\t"
        "ds_read_b64_tr_b16 v[58:59], v115 offset:2048\n\t"
        "ds_read_b64_tr_b16 v[60:61], v115 offset:4096\n\t"
        "ds_read_b64_tr_b16 v[62:63], v115 offset:6144\n\t"
        "s_add_u32 %[t1], %[ro], 3\n\t"
        "s_and_b32 %[t1], %[t1], 7\n\t"
        "s_lshl_b32 %[t1], %[t1], 18\n\t"
        "s_add_u32 %[t0], %[t1], 131072\n\t"
        "buffer_load_dwordx4 a[168:171], v110, %[so], %[t0] offen\n\t"
        "s_sleep 2\n\t"
        "s_add_u32 %[t0], %[t1], 147456\n\t"
        "buffer_load_dwordx4 a[172:175], v110, %[so], %[t0] offen\n\t"
        "s_sleep 2\n\t"
        "s_add_u32 %[t0], %[t1], 163840\n\t"
        "buffer_load_dwordx4 a[176:179], v110, %[so], %[t0] offen\n\t"
        "s_sleep 2\n\t"
        "s_add_u32 %[t0], %[t1], 180224\n\t"
        "buffer_load_dwordx4 a[180:183], v110, %[so], %[t0] offen\n\t"
        "s_sleep 2\n\t"
        "s_add_u32 %[t0], %[t1], 196608\n\t"
        "buffer_load_dwordx4 a[184:187], v110, %[so], %[t0] offen\n\t"
        "s_sleep 2\n\t"
        "s_add_u32 %[t0], %[t1], 212992\n\t"
        "buffer_load_dwordx4 a[188:191], v110, %[so], %[t0] offen\n\t"
        "s_sleep 2\n\t"
        "s_add_u32 %[t0], %[t1], 229376\n\t"
        "buffer_load_dwordx4 v[48:51], v110, %[so], %[t0] offen\n\t"
        "s_sleep 2\n\t"
        "s_add_u32 %[t0], %[t1], 245760\n\t"
        "buffer_load_dwordx4 v[52:55], v110, %[so], %[t0] offen\n\t"
        "s_sleep 2\n\t"
        "s_waitcnt lgkmcnt(0)\n\t"
        "ds_write_b128 v116, v[56:59] offset:10240\n\t"
        "ds_write_b128 v116, v[60:63] offset:11264\n\t"
        "s_waitcnt vmcnt(8)\n\t"
        "ds_write_b128 v111, a[144:147] offset:0\n\t"
        "ds_write_b128 v111, a[148:151] offset:1024\n\t"
        "ds_write_b128 v111, a[152:155] offset:2048\n\t"
        "ds_write_b128 v111, a[156:159] offset:3072\n\t"
        "ds_write_b128 v111, a[160:163] offset:4096\n\t"
        "ds_write_b128 v111, a[164:167] offset:5120\n\t"
        "ds_write_b128 v111, v[40:43] offset:6144\n\t"
        "ds_write_b128 v111, v[44:47] offset:7168\n\t"
        "ds_read_b64_tr_b16 a[144:145], v112 offset:0\n\t"
        "ds_read_b64_tr_b16 a[146:147], v112 offset:2048\n\t"
        "ds_read_b64_tr_b16 a[156:157], v112 offset:4096\n\t"
        "ds_read_b64_tr_b16 a[158:159], v112 offset:6144\n\t"
        "s_waitcnt lgkmcnt(8)\n\t"
        "ds_read_b64_tr_b16 a[148:149], v113 offset:0\n\t"
        "ds_read_b64_tr_b16 a[150:151], v113 offset:2048\n\t"
        "ds_read_b64_tr_b16 a[160:161], v113 offset:4096\n\t"
        "ds_read_b64_tr_b16 a[162:163], v113 offset:6144\n\t"
        "s_waitcnt lgkmcnt(8)\n\t"
        "ds_read_b64_tr_b16 a[152:153], v114 offset:0\n\t"
        "ds_read_b64_tr_b16 a[154:155], v114 offset:2048\n\t"
        "ds_read_b64_tr_b16 a[164:165], v114 offset:4096\n\t"
        "ds_read_b64_tr_b16 a[166:167], v114 offset:6144\n\t"
        "s_waitcnt lgkmcnt(8)\n\t"
        "ds_read_b64_tr_b16 v[56:57], v115 offset:0\n\t"
        "ds_read_b64_tr_b16 v[58:59], v115 offset:2048\n\t"
        "ds_read_b64_tr_b16 v[60:61], v115 offset:4096\n\t"
        "ds_read_b64_tr_b16 v[62:63], v115 offset:6144\n\t"
        "s_add_u32 %[t1], %[ro], 4\n\t"
        "s_and_b32 %[t1], %[t1], 7\n\t"
        "s_lshl_b32 %[t1], %[t1], 18\n\t"
        "s_add_u32 %[t0], %[t1], 0\n\t"
        "buffer_load_dwordx4 a[192:195], v110, %[so], %[t0] offen\n\t"
        "s_sleep 2\n\t"
        "s_add_u32 %[t0], %[t1], 16384\n\t"
        "buffer_load_dwordx4 a[196:199], v110, %[so], %[t0] offen\n\t"
        "s_sleep 2\n\t"
        "s_add_u32 %[t0], %[t1], 32768\n\t"
        "buffer_load_dwordx4 a[200:203], v110, %[so], %[t0] offen\n\t"
        "s_sleep 2\n\t"
        "s_add_u32 %[t0], %[t1], 49152\n\t"
        "buffer_load_dwordx4 a[204:207], v110, %[so], %[t0] offen\n\t"
        "s_sleep 2\n\t"
        "s_add_u32 %[t0], %[t1], 65536\n\t"
        "buffer_load_dwordx4 a[208:211], v110, %[so], %[t0] offen\n\t"
        "s_sleep 2\n\t"
        "s_add_u32 %[t0], %[t1], 81920\n\t"
        "buffer_load_dwordx4 a[212:215], v110, %[so], %[t0] offen\n\t"
        "s_sleep 2\n\t"
        "s_add_u32 %[t0], %[t1], 98304\n\t"
        "buffer_load_dwordx4 v[24:27], v110, %[so], %[t0] offen\n\t"
        "s_sleep 2\n\t"
        "s_add_u32 %[t0], %[t1], 114688\n\t"
        "buffer_load_dwordx4 v[28:31], v110, %[so], %[t0] offen\n\t"
        "s_sleep 2\n\t"
        "s_waitcnt lgkmcnt(0)\n\t"
        "ds_write_b128 v116, v[56:59] offset:12288\n\t"
        "ds_write_b128 v116, v[60:63] offset:13312\n\t"
        "s_waitcnt vmcnt(8)\n\t"
        "ds_write_b128 v111, a[168:171] offset:0\n\t"
        "ds_write_b128 v111, a[172:175] offset:1024\n\t"
        "ds_write_b128 v111, a[176:179] offset:2048\n\t"
        "ds_write_b128 v111, a[180:183] offset:3072\n\t"
        "ds_write_b128 v111, a[184:187] offset:4096\n\t"
        "ds_write_b128 v111, a[188:191] offset:5120\n\t"
        "ds_write_b128 v111, v[48:51] offset:6144\n\t"
        "ds_write_b128 v111, v[52:55] offset:7168\n\t"
        "ds_read_b64_tr_b16 a[168:169], v112 offset:0\n\t"
        "ds_read_b64_tr_b16 a[170:171], v112 offset:2048\n\t"
        "ds_read_b64_tr_b16 a[180:181], v112 offset:4096\n\t"
        "ds_read_b64_tr_b16 a[182:183], v112 offset:6144\n\t"
        "s_waitcnt lgkmcnt(8)\n\t"
        "ds_read_b64_tr_b16 a[172:173], v113 offset:0\n\t"
        "ds_read_b64_tr_b16 a[174:175], v113 offset:2048\n\t"
        "ds_read_b64_tr_b16 a[184:185], v113 offset:4096\n\t"
        "ds_read_b64_tr_b16 a[186:187], v113 offset:6144\n\t"
        "s_waitcnt lgkmcnt(8)\n\t"
        "ds_read_b64_tr_b16 a[176:177], v114 offset:0\n\t"
        "ds_read_b64_tr_b16 a[178:179], v114 offset:2048\n\t"
        "ds_read_b64_tr_b16 a[188:189], v114 offset:4096\n\t"
        "ds_read_b64_tr_b16 a[190:191], v114 offset:6144\n\t"
        "s_waitcnt lgkmcnt(8)\n\t"
        "ds_read_b64_tr_b16 v[56:57], v115 offset:0\n\t"
        "ds_read_b64_tr_b16 v[58:59], v115 offset:2048\n\t"
        "ds_read_b64_tr_b16 v[60:61], v115 offset:4096\n\t"
        "ds_read_b64_tr_b16 v[62:63], v115 offset:6144\n\t"
        "s_add_u32 %[t1], %[ro], 4\n\t"
        "s_and_b32 %[t1], %[t1], 7\n\t"
        "s_lshl_b32 %[t1], %[t1], 18\n\t"
        "s_add_u32 %[t0], %[t1], 131072\n\t"
        "buffer_load_dwordx4 a[216:219], v110, %[so], %[t0] offen\n\t"
        "s_sleep 2\n\t"
        "s_add_u32 %[t0], %[t1], 147456\n\t"
        "buffer_load_dwordx4 a[220:223], v110, %[so], %[t0] offen\n\t"
        "s_sleep 2\n\t"
        "s_add_u32 %[t0], %[t1], 163840\n\t"
        "buffer_load_dwordx4 a[224:227], v110, %[so], %[t0] offen\n\t"
        "s_sleep 2\n\t"
        "s_add_u32 %[t0], %[t1], 180224\n\t"
        "buffer_load_dwordx4 a[228:231], v110, %[so], %[t0] offen\n\t"
        "s_sleep 2\n\t"
        "s_add_u32 %[t0], %[t1], 196608\n\t"
        "buffer_load_dwordx4 a[232:235], v110, %[so], %[t0] offen\n\t"
        "s_sleep 2\n\t"
        "s_add_u32 %[t0], %[t1], 212992\n\t"
        "buffer_load_dwordx4 a[236:239], v110, %[so], %[t0] offen\n\t"
        "s_sleep 2\n\t"
        "s_add_u32 %[t0], %[t1], 229376\n\t"
        "buffer_load_dwordx4 v[32:35], v110, %[so], %[t0] offen\n\t"
        "s_sleep 2\n\t"
        "s_add_u32 %[t0], %[t1], 245760\n\t"
        "buffer_load_dwordx4 v[36:39], v110, %[so], %[t0] offen\n\t"
        "s_sleep 2\n\t"
        "s_waitcnt lgkmcnt(0)\n\t"
        "ds_write_b128 v116, v[56:59] offset:14336\n\t"
        "ds_write_b128 v116, v[60:63] offset:15360\n\t"
        "s_waitcnt vmcnt(8)\n\t"
        "ds_write_b128 v111, a[192:195] offset:0\n\t"
        "ds_write_b128 v111, a[196:199] offset:1024\n\t"
        "ds_write_b128 v111, a[200:203] offset:2048\n\t"
        "ds_write_b128 v111, a[204:207] offset:3072\n\t"
        "ds_write_b128 v111, a[208:211] offset:4096\n\t"
        "ds_write_b128 v111, a[212:215] offset:5120\n\t"
        "ds_write_b128 v111, v[24:27] offset:6144\n\t"
        "ds_write_b128 v111, v[28:31] offset:7168\n\t"
        "ds_read_b64_tr_b16 a[192:193], v112 offset:0\n\t"
        "ds_read_b64_tr_b16 a[194:195], v112 offset:2048\n\t"
        "ds_read_b64_tr_b16 a[204:205], v112 offset:4096\n\t"
        "ds_read_b64_tr_b16 a[206:207], v112 offset:6144\n\t"
        "s_waitcnt lgkmcnt(8)\n\t"
        "ds_read_b64_tr_b16 a[196:197], v113 offset:0\n\t"
        "ds_read_b64_tr_b16 a[198:199], v113 offset:2048\n\t"
        "ds_read_b64_tr_b16 a[208:209], v113 offset:4096\n\t"
        "ds_read_b64_tr_b16 a[210:211], v113 offset:6144\n\t"
        "s_waitcnt lgkmcnt(8)\n\t"
        "ds_read_b64_tr_b16 a[200:201], v114 offset:0\n\t"
        "ds_read_b64_tr_b16 a[202:203], v114 offset:2048\n\t"
        "ds_read_b64_tr_b16 a[212:213], v114 offset:4096\n\t"
        "ds_read_b64_tr_b16 a[214:215], v114 offset:6144\n\t"
        "s_waitcnt lgkmcnt(8)\n\t"
        "ds_read_b64_tr_b16 v[56:57], v115 offset:0\n\t"
        "ds_read_b64_tr_b16 v[58:59], v115 offset:2048\n\t"
        "ds_read_b64_tr_b16 v[60:61], v115 offset:4096\n\t"
        "ds_read_b64_tr_b16 v[62:63], v115 offset:6144\n\t"
        "s_add_u32 %[t1], %[ro], 5\n\t"
        "s_and_b32 %[t1], %[t1], 7\n\t"
        "s_lshl_b32 %[t1], %[t1], 18\n\t"
        "s_add_u32 %[t0], %[t1], 0\n\t"
        "buffer_load_dwordx4 a[240:243], v110, %[so], %[t0] offen\n\t"
        "s_sleep 2\n\t"
        "s_add_u32 %[t0], %[t1], 16384\n\t"
        "buffer_load_dwordx4 a[244:247], v110, %[so], %[t0] offen\n\t"
        "s_sleep 2\n\t"
        "s_add_u32 %[t0], %[t1], 32768\n\t"
        "buffer_load_dwordx4 a[248:251], v110, %[so], %[t0] offen\n\t"
        "s_sleep 2\n\t"
        "s_add_u32 %[t0], %[t1], 49152\n\t"
        "buffer_load_dwordx4 a[252:255], v110, %[so], %[t0] offen\n\t"
        "s_sleep 2\n\t"
        "s_add_u32 %[t0], %[t1], 65536\n\t"
        "buffer_load_dwordx4 v[128:131], v110, %[so], %[t0] offen\n\t"
        "s_sleep 2\n\t"
        "s_add_u32 %[t0], %[t1], 81920\n\t"
        "buffer_load_dwordx4 v[132:135], v110, %[so], %[t0] offen\n\t"
        "s_sleep 2\n\t"
        "s_add_u32 %[t0], %[t1], 98304\n\t"
        "buffer_load_dwordx4 v[40:43], v110, %[so], %[t0] offen\n\t"
        "s_sleep 2\n\t"
        "s_add_u32 %[t0], %[t1], 114688\n\t"
        "buffer_load_dwordx4 v[44:47], v110, %[so], %[t0] offen\n\t"
        "s_sleep 2\n\t"
        "s_waitcnt lgkmcnt(0)\n\t"
        "ds_write_b128 v116, v[56:59] offset:16384\n\t"
        "ds_write_b128 v116, v[60:63] offset:17408\n\t"
        "s_waitcnt vmcnt(8)\n\t"
        "ds_write_b128 v111, a[216:219] offset:0\n\t"
        "ds_write_b128 v111, a[220:223] offset:1024\n\t"
        "ds_write_b128 v111, a[224:227] offset:2048\n\t"
        "ds_write_b128 v111, a[228:231] offset:3072\n\t"
        "ds_write_b128 v111, a[232:235] offset:4096\n\t"
        "ds_write_b128 v111, a[236:239] offset:5120\n\t"
        "ds_write_b128 v111, v[32:35] offset:6144\n\t"
        "ds_write_b128 v111, v[36:39] offset:7168\n\t"
        "ds_read_b64_tr_b16 a[216:217], v112 offset:0\n\t"
        "ds_read_b64_tr_b16 a[218:219], v112 offset:2048\n\t"
        "ds_read_b64_tr_b16 a[228:229], v112 offset:4096\n\t"
        "ds_read_b64_tr_b16 a[230:231], v112 offset:6144\n\t"
        "s_waitcnt lgkmcnt(8)\n\t"
        "ds_read_b64_tr_b16 a[220:221], v113 offset:0\n\t"
        "ds_read_b64_tr_b16 a[222:223], v113 offset:2048\n\t"
        "ds_read_b64_tr_b16 a[232:233], v113 offset:4096\n\t"
        "ds_read_b64_tr_b16 a[234:235], v113 offset:6144\n\t"
        "s_waitcnt lgkmcnt(8)\n\t"
        "ds_read_b64_tr_b16 a[224:225], v114 offset:0\n\t"
        "ds_read_b64_tr_b16 a[226:227], v114 offset:2048\n\t"
        "ds_read_b64_tr_b16 a[236:237], v114 offset:4096\n\t"
        "ds_read_b64_tr_b16 a[238:239], v114 offset:6144\n\t"
        "s_waitcnt lgkmcnt(8)\n\t"
        "ds_read_b64_tr_b16 v[56:57], v115 offset:0\n\t"
        "ds_read_b64_tr_b16 v[58:59], v115 offset:2048\n\t"
        "ds_read_b64_tr_b16 v[60:61], v115 offset:4096\n\t"
        "ds_read_b64_tr_b16 v[62:63], v115 offset:6144\n\t"
        "s_add_u32 %[t1], %[ro], 5\n\t"
        "s_and_b32 %[t1], %[t1], 7\n\t"
        "s_lshl_b32 %[t1], %[t1], 18\n\t"
        "s_add_u32 %[t0], %[t1], 131072\n\t"
        "buffer_load_dwordx4 v[136:139], v110, %[so], %[t0] offen\n\t"
        "s_sleep 2\n\t"
        "s_add_u32 %[t0], %[t1], 147456\n\t"
        "buffer_load_dwordx4 v[140:143], v110, %[so], %[t0] offen\n\t"
        "s_sleep 2\n\t"
        "s_add_u32 %[t0], %[t1], 163840\n\t"
        "buffer_load_dwordx4 v[144:147], v110, %[so], %[t0] offen\n\t"
        "s_sleep 2\n\t"
        "s_add_u32 %[t0], %[t1], 180224\n\t"
        "buffer_load_dwordx4 v[148:151], v110, %[so], %[t0] offen\n\t"
        "s_sleep 2\n\t"
        "s_add_u32 %[t0], %[t1], 196608\n\t"
        "buffer_load_dwordx4 v[152:155], v110, %[so], %[t0] offen\n\t"
        "s_sleep 2\n\t"
        "s_add_u32 %[t0], %[t1], 212992\n\t"
        "buffer_load_dwordx4 v[156:159], v110, %[so], %[t0] offen\n\t"
        "s_sleep 2\n\t"
        "s_add_u32 %[t0], %[t1], 229376\n\t"
        "buffer_load_dwordx4 v[48:51], v110, %[so], %[t0] offen\n\t"
        "s_sleep 2\n\t"
        "s_add_u32 %[t0], %[t1], 245760\n\t"
        "buffer_load_dwordx4 v[52:55], v110, %[so], %[t0] offen\n\t"
        "s_sleep 2\n\t"
        "s_waitcnt lgkmcnt(0)\n\t"
        "ds_write_b128 v116, v[56:59] offset:18432\n\t"
        "ds_write_b128 v116, v[60:63] offset:19456\n\t"
        "s_waitcnt vmcnt(8)\n\t"
        "ds_write_b128 v111, a[240:243] offset:0\n\t"
        "ds_write_b128 v111, a[244:247] offset:1024\n\t"
        "ds_write_b128 v111, a[248:251] offset:2048\n\t"
        "ds_write_b128 v111, a[252:255] offset:3072\n\t"
        "ds_write_b128 v111, v[128:131] offset:4096\n\t"
        "ds_write_b128 v111, v[132:135] offset:5120\n\t"
        "ds_write_b128 v111, v[40:43] offset:6144\n\t"
        "ds_write_b128 v111, v[44:47] offset:7168\n\t"
        "ds_read_b64_tr_b16 a[240:241], v112 offset:0\n\t"
        "ds_read_b64_tr_b16 a[242:243], v112 offset:2048\n\t"
        "ds_read_b64_tr_b16 a[252:253], v112 offset:4096\n\t"
        "ds_read_b64_tr_b16 a[254:255], v112 offset:6144\n\t"
        "s_waitcnt lgkmcnt(8)\n\t"
        "ds_read_b64_tr_b16 a[244:245], v113 offset:0\n\t"
        "ds_read_b64_tr_b16 a[246:247], v113 offset:2048\n\t"
        "ds_read_b64_tr_b16 v[128:129], v113 offset:4096\n\t"
        "ds_read_b64_tr_b16 v[130:131], v113 offset:6144\n\t"
        "s_waitcnt lgkmcnt(8)\n\t"
        "ds_read_b64_tr_b16 a[248:249], v114 offset:0\n\t"
        "ds_read_b64_tr_b16 a[250:251], v114 offset:2048\n\t"
        "ds_read_b64_tr_b16 v[132:133], v114 offset:4096\n\t"
        "ds_read_b64_tr_b16 v[134:135], v114 offset:6144\n\t"
        "s_waitcnt lgkmcnt(8)\n\t"
        "ds_read_b64_tr_b16 v[56:57], v115 offset:0\n\t"
        "ds_read_b64_tr_b16 v[58:59], v115 offset:2048\n\t"
        "ds_read_b64_tr_b16 v[60:61], v115 offset:4096\n\t"
        "ds_read_b64_tr_b16 v[62:63], v115 offset:6144\n\t"
        "s_add_u32 %[t1], %[ro], 6\n\t"
        "s_and_b32 %[t1], %[t1], 7\n\t"
        "s_lshl_b32 %[t1], %[t1], 18\n\t"
        "s_add_u32 %[t0], %[t1], 0\n\t"
        "buffer_load_dwordx4 v[160:163], v110, %[so], %[t0] offen\n\t"
        "s_sleep 2\n\t"
        "s_add_u32 %[t0], %[t1], 16384\n\t"
        "buffer_load_dwordx4 v[164:167], v110, %[so], %[t0] offen\n\t"
        "s_sleep 2\n\t"
        "s_add_u32 %[t0], %[t1], 32768\n\t"
        "buffer_load_dwordx4 v[168:171], v110, %[so], %[t0] offen\n\t"
        "s_sleep 2\n\t"
        "s_add_u32 %[t0], %[t1], 49152\n\t"
        "buffer_load_dwordx4 v[172:175], v110, %[so], %[t0] offen\n\t"
        "s_sleep 2\n\t"
        "s_add_u32 %[t0], %[t1], 65536\n\t"
        "buffer_load_dwordx4 v[176:179], v110, %[so], %[t0] offen\n\t"
        "s_sleep 2\n\t"
        "s_add_u32 %[t0], %[t1], 81920\n\t"
        "buffer_load_dwordx4 v[180:183], v110, %[so], %[t0] offen\n\t"
        "s_sleep 2\n\t"
        "s_add_u32 %[t0], %[t1], 98304\n\t"
        "buffer_load_dwordx4 v[24:27], v110, %[so], %[t0] offen\n\t"
        "s_sleep 2\n\t"
        "s_add_u32 %[t0], %[t1], 114688\n\t"
        "buffer_load_dwordx4 v[28:31], v110, %[so], %[t0] offen\n\t"
        "s_sleep 2\n\t"
        "s_waitcnt lgkmcnt(0)\n\t"
        "ds_write_b128 v116, v[56:59] offset:20480\n\t"
        "ds_write_b128 v116, v[60:63] offset:21504\n\t"
        "s_waitcnt vmcnt(8)\n\t"
        "ds_write_b128 v111, v[136:139] offset:0\n\t"
        "ds_write_b128 v111, v[140:143] offset:1024\n\t"
        "ds_write_b128 v111, v[144:147] offset:2048\n\t"
        "ds_write_b128 v111, v[148:151] offset:3072\n\t"
        "ds_write_b128 v111, v[152:155] offset:4096\n\t"
        "ds_write_b128 v111, v[156:159] offset:5120\n\t"
        "ds_write_b128 v111, v[48:51] offset:6144\n\t"
        "ds_write_b128 v111, v[52:55] offset:7168\n\t"
        "ds_read_b64_tr_b16 v[136:137], v112 offset:0\n\t"
        "ds_read_b64_tr_b16 v[138:139], v112 offset:2048\n\t"
        "ds_read_b64_tr_b16 v[148:149], v112 offset:4096\n\t"
        "ds_read_b64_tr_b16 v[150:151], v112 offset:6144\n\t"
        "s_waitcnt lgkmcnt(8)\n\t"
        "ds_read_b64_tr_b16 v[140:141], v113 offset:0\n\t"
        "ds_read_b64_tr_b16 v[142:143], v113 offset:2048\n\t"
        "ds_read_b64_tr_b16 v[152:153], v113 offset:4096\n\t"
        "ds_read_b64_tr_b16 v[154:155], v113 offset:6144\n\t"
        "s_waitcnt lgkmcnt(8)\n\t"
        "ds_read_b64_tr_b16 v[144:145], v114 offset:0\n\t"
        "ds_read_b64_tr_b16 v[146:147], v114 offset:2048\n\t"
        "ds_read_b64_tr_b16 v[156:157], v114 offset:4096\n\t"
        "ds_read_b64_tr_b16 v[158:159], v114 offset:6144\n\t"
        "s_waitcnt lgkmcnt(8)\n\t"
        "ds_read_b64_tr_b16 v[56:57], v115 offset:0\n\t"
        "ds_read_b64_tr_b16 v[58:59], v115 offset:2048\n\t"
        "ds_read_b64_tr_b16 v[60:61], v115 offset:4096\n\t"
        "ds_read_b64_tr_b16 v[62:63], v115 offset:6144\n\t"
        "s_add_u32 %[t1], %[ro], 6\n\t"
        "s_and_b32 %[t1], %[t1], 7\n\t"
        "s_lshl_b32 %[t1], %[t1], 18\n\t"
        "s_add_u32 %[t0], %[t1], 131072\n\t"
        "buffer_load_dwordx4 v[184:187], v110, %[so], %[t0] offen\n\t"
        "s_sleep 2\n\t"
        "s_add_u32 %[t0], %[t1], 147456\n\t"
        "buffer_load_dwordx4 v[188:191], v110, %[so], %[t0] offen\n\t"
        "s_sleep 2\n\t"
        "s_add_u32 %[t0], %[t1], 163840\n\t"
        "buffer_load_dwordx4 v[192:195], v110, %[so], %[t0] offen\n\t"
        "s_sleep 2\n\t"
        "s_add_u32 %[t0], %[t1], 180224\n\t"
        "buffer_load_dwordx4 v[196:199], v110, %[so], %[t0] offen\n\t"
        "s_sleep 2\n\t"
        "s_add_u32 %[t0], %[t1], 196608\n\t"
        "buffer_load_dwordx4 v[200:203], v110, %[so], %[t0] offen\n\t"
        "s_sleep 2\n\t"
        "s_add_u32 %[t0], %[t1], 212992\n\t"
        "buffer_load_dwordx4 v[204:207], v110, %[so], %[t0] offen\n\t"
        "s_sleep 2\n\t"
        "s_add_u32 %[t0], %[t1], 229376\n\t"
        "buffer_load_dwordx4 v[32:35], v110, %[so], %[t0] offen\n\t"
        "s_sleep 2\n\t"
        "s_add_u32 %[t0], %[t1], 245760\n\t"
        "buffer_load_dwordx4 v[36:39], v110, %[so], %[t0] offen\n\t"
        "s_sleep 2\n\t"
        "s_waitcnt lgkmcnt(0)\n\t"
        "ds_write_b128 v116, v[56:59] offset:22528\n\t"
        "ds_write_b128 v116, v[60:63] offset:23552\n\t"
        "s_waitcnt vmcnt(8)\n\t"
        "ds_write_b128 v111, v[160:163] offset:0\n\t"
        "ds_write_b128 v111, v[164:167] offset:1024\n\t"
        "ds_write_b128 v111, v[168:171] offset:2048\n\t"
        "ds_write_b128 v111, v[172:175] offset:3072\n\t"
        "ds_write_b128 v111, v[176:179] offset:4096\n\t"
        "ds_write_b128 v111, v[180:183] offset:5120\n\t"
        "ds_write_b128 v111, v[24:27] offset:6144\n\t"
        "ds_write_b128 v111, v[28:31] offset:7168\n\t"
        "ds_read_b64_tr_b16 v[160:161], v112 offset:0\n\t"
        "ds_read_b64_tr_b16 v[162:163], v112 offset:2048\n\t"
        "ds_read_b64_tr_b16 v[172:173], v112 offset:4096\n\t"
        "ds_read_b64_tr_b16 v[174:175], v112 offset:6144\n\t"
        "s_waitcnt lgkmcnt(8)\n\t"
        "ds_read_b64_tr_b16 v[164:165], v113 offset:0\n\t"
        "ds_read_b64_tr_b16 v[166:167], v113 offset:2048\n\t"
        "ds_read_b64_tr_b16 v[176:177], v113 offset:4096\n\t"
        "ds_read_b64_tr_b16 v[178:179], v113 offset:6144\n\t"
        "s_waitcnt lgkmcnt(8)\n\t"
        "ds_read_b64_tr_b16 v[168:169], v114 offset:0\n\t"
        "ds_read_b64_tr_b16 v[170:171], v114 offset:2048\n\t"
        "ds_read_b64_tr_b16 v[180:181], v114 offset:4096\n\t"
        "ds_read_b64_tr_b16 v[182:183], v114 offset:6144\n\t"
        "s_waitcnt lgkmcnt(8)\n\t"
        "ds_read_b64_tr_b16 v[56:57], v115 offset:0\n\t"
        "ds_read_b64_tr_b16 v[58:59], v115 offset:2048\n\t"
        "ds_read_b64_tr_b16 v[60:61], v115 offset:4096\n\t"
        "ds_read_b64_tr_b16 v[62:63], v115 offset:6144\n\t"
        "s_add_u32 %[t1], %[ro], 7\n\t"
        "s_and_b32 %[t1], %[t1], 7\n\t"
        "s_lshl_b32 %[t1], %[t1], 18\n\t"
        "s_add_u32 %[t0], %[t1], 0\n\t"
        "buffer_load_dwordx4 v[208:211], v110, %[so], %[t0] offen\n\t"
        "s_sleep 2\n\t"
        "s_add_u32 %[t0], %[t1], 16384\n\t"
        "buffer_load_dwordx4 v[212:215], v110, %[so], %[t0] offen\n\t"
        "s_sleep 2\n\t"
        "s_add_u32 %[t0], %[t1], 32768\n\t"
        "buffer_load_dwordx4 v[216:219], v110, %[so], %[t0] offen\n\t"
        "s_sleep 2\n\t"
        "s_add_u32 %[t0], %[t1], 49152\n\t"
        "buffer_load_dwordx4 v[220:223], v110, %[so], %[t0] offen\n\t"
        "s_sleep 2\n\t"
        "s_add_u32 %[t0], %[t1], 65536\n\t"
        "buffer_load_dwordx4 v[224:227], v110, %[so], %[t0] offen\n\t"
        "s_sleep 2\n\t"
        "s_add_u32 %[t0], %[t1], 81920\n\t"
        "buffer_load_dwordx4 v[228:231], v110, %[so], %[t0] offen\n\t"
        "s_sleep 2\n\t"
        "s_add_u32 %[t0], %[t1], 98304\n\t"
        "buffer_load_dwordx4 v[40:43], v110, %[so], %[t0] offen\n\t"
        "s_sleep 2\n\t"
        "s_add_u32 %[t0], %[t1], 114688\n\t"
        "buffer_load_dwordx4 v[44:47], v110, %[so], %[t0] offen\n\t"
        "s_sleep 2\n\t"
        "s_waitcnt lgkmcnt(0)\n\t"
        "ds_write_b128 v116, v[56:59] offset:24576\n\t"
        "ds_write_b128 v116, v[60:63] offset:25600\n\t"
        "s_waitcnt vmcnt(8)\n\t"
        "ds_write_b128 v111, v[184:187] offset:0\n\t"
        "ds_write_b128 v111, v[188:191] offset:1024\n\t"
        "ds_write_b128 v111, v[192:195] offset:2048\n\t"
        "ds_write_b128 v111, v[196:199] offset:3072\n\t"
        "ds_write_b128 v111, v[200:203] offset:4096\n\t"
        "ds_write_b128 v111, v[204:207] offset:5120\n\t"
        "ds_write_b128 v111, v[32:35] offset:6144\n\t"
        "ds_write_b128 v111, v[36:39] offset:7168\n\t"
        "ds_read_b64_tr_b16 v[184:185], v112 offset:0\n\t"
        "ds_read_b64_tr_b16 v[186:187], v112 offset:2048\n\t"
        "ds_read_b64_tr_b16 v[196:197], v112 offset:4096\n\t"
        "ds_read_b64_tr_b16 v[198:199], v112 offset:6144\n\t"
        "s_waitcnt lgkmcnt(8)\n\t"
        "ds_read_b64_tr_b16 v[188:189], v113 offset:0\n\t"
        "ds_read_b64_tr_b16 v[190:191], v113 offset:2048\n\t"
        "ds_read_b64_tr_b16 v[200:201], v113 offset:4096\n\t"
        "ds_read_b64_tr_b16 v[202:203], v113 offset:6144\n\t"
        "s_waitcnt lgkmcnt(8)\n\t"
        "ds_read_b64_tr_b16 v[192:193], v114 offset:0\n\t"
        "ds_read_b64_tr_b16 v[194:195], v114 offset:2048\n\t"
        "ds_read_b64_tr_b16 v[204:205], v114 offset:4096\n\t"
        "ds_read_b64_tr_b16 v[206:207], v114 offset:6144\n\t"
        "s_waitcnt lgkmcnt(8)\n\t"
        "ds_read_b64_tr_b16 v[56:57], v115 offset:0\n\t"
        "ds_read_b64_tr_b16 v[58:59], v115 offset:2048\n\t"
        "ds_read_b64_tr_b16 v[60:61], v115 offset:4096\n\t"
        "ds_read_b64_tr_b16 v[62:63], v115 offset:6144\n\t"
        "s_add_u32 %[t1], %[ro], 7\n\t"
        "s_and_b32 %[t1], %[t1], 7\n\t"
        "s_lshl_b32 %[t1], %[t1], 18\n\t"
        "s_add_u32 %[t0], %[t1], 131072\n\t"
        "buffer_load_dwordx4 v[232:235], v110, %[so], %[t0] offen\n\t"
        "s_sleep 2\n\t"
        "s_add_u32 %[t0], %[t1], 147456\n\t"
        "buffer_load_dwordx4 v[236:239], v110, %[so], %[t0] offen\n\t"
        "s_sleep 2\n\t"
        "s_add_u32 %[t0], %[t1], 163840\n\t"
        "buffer_load_dwordx4 v[240:243], v110, %[so], %[t0] offen\n\t"
        "s_sleep 2\n\t"
        "s_add_u32 %[t0], %[t1], 180224\n\t"
        "buffer_load_dwordx4 v[244:247], v110, %[so], %[t0] offen\n\t"
        "s_sleep 2\n\t"
        "s_add_u32 %[t0], %[t1], 196608\n\t"
        "buffer_load_dwordx4 v[248:251], v110, %[so], %[t0] offen\n\t"
        "s_sleep 2\n\t"
        "s_add_u32 %[t0], %[t1], 212992\n\t"
        "buffer_load_dwordx4 v[252:255], v110, %[so], %[t0] offen\n\t"
        "s_sleep 2\n\t"
        "s_add_u32 %[t0], %[t1], 229376\n\t"
        "buffer_load_dwordx4 v[48:51], v110, %[so], %[t0] offen\n\t"
        "s_sleep 2\n\t"
        "s_add_u32 %[t0], %[t1], 245760\n\t"
        "buffer_load_dwordx4 v[52:55], v110, %[so], %[t0] offen\n\t"
        "s_sleep 2\n\t"
        "s_waitcnt lgkmcnt(0)\n\t"
        "ds_write_b128 v116, v[56:59] offset:26624\n\t"
        "ds_write_b128 v116, v[60:63] offset:27648\n\t"
        "s_waitcnt vmcnt(8)\n\t"
        "ds_write_b128 v111, v[208:211] offset:0\n\t"
        "ds_write_b128 v111, v[212:215] offset:1024\n\t"
        "ds_write_b128 v111, v[216:219] offset:2048\n\t"
        "ds_write_b128 v111, v[220:223] offset:3072\n\t"
        "ds_write_b128 v111, v[224:227] offset:4096\n\t"
        "ds_write_b128 v111, v[228:231] offset:5120\n\t"
        "ds_write_b128 v111, v[40:43] offset:6144\n\t"
        "ds_write_b128 v111, v[44:47] offset:7168\n\t"
        "ds_read_b64_tr_b16 v[208:209], v112 offset:0\n\t"
        "ds_read_b64_tr_b16 v[210:211], v112 offset:2048\n\t"
        "ds_read_b64_tr_b16 v[220:221], v112 offset:4096\n\t"
        "ds_read_b64_tr_b16 v[222:223], v112 offset:6144\n\t"
        "s_waitcnt lgkmcnt(8)\n\t"
        "ds_read_b64_tr_b16 v[212:213], v113 offset:0\n\t"
        "ds_read_b64_tr_b16 v[214:215], v113 offset:2048\n\t"
        "ds_read_b64_tr_b16 v[224:225], v113 offset:4096\n\t"
        "ds_read_b64_tr_b16 v[226:227], v113 offset:6144\n\t"
        "s_waitcnt lgkmcnt(8)\n\t"
        "ds_read_b64_tr_b16 v[216:217], v114 offset:0\n\t"
        "ds_read_b64_tr_b16 v[218:219], v114 offset:2048\n\t"
        "ds_read_b64_tr_b16 v[228:229], v114 offset:4096\n\t"
        "ds_read_b64_tr_b16 v[230:231], v114 offset:6144\n\t"
        "s_waitcnt lgkmcnt(8)\n\t"
        "ds_read_b64_tr_b16 v[56:57], v115 offset:0\n\t"
        "ds_read_b64_tr_b16 v[58:59], v115 offset:2048\n\t"
        "ds_read_b64_tr_b16 v[60:61], v115 offset:4096\n\t"
        "ds_read_b64_tr_b16 v[62:63], v115 offset:6144\n\t"
        "s_waitcnt lgkmcnt(0)\n\t"
        "ds_write_b128 v116, v[56:59] offset:28672\n\t"
        "ds_write_b128 v116, v[60:63] offset:29696\n\t"
        "s_waitcnt vmcnt(0)\n\t"
        "ds_write_b128 v111, v[232:235] offset:0\n\t"
        "ds_write_b128 v111, v[236:239] offset:1024\n\t"
        "ds_write_b128 v111, v[240:243] offset:2048\n\t"
        "ds_write_b128 v111, v[244:247] offset:3072\n\t"
        "ds_write_b128 v111, v[248:251] offset:4096\n\t"
        "ds_write_b128 v111, v[252:255] offset:5120\n\t"
        "ds_write_b128 v111, v[48:51] offset:6144\n\t"
        "ds_write_b128 v111, v[52:55] offset:7168\n\t"
        "ds_read_b64_tr_b16 v[232:233], v112 offset:0\n\t"
        "ds_read_b64_tr_b16 v[234:235], v112 offset:2048\n\t"
        "ds_read_b64_tr_b16 v[244:245], v112 offset:4096\n\t"
        "ds_read_b64_tr_b16 v[246:247], v112 offset:6144\n\t"
        "s_waitcnt lgkmcnt(8)\n\t"
        "ds_read_b64_tr_b16 v[236:237], v113 offset:0\n\t"
        "ds_read_b64_tr_b16 v[238:239], v113 offset:2048\n\t"
        "ds_read_b64_tr_b16 v[248:249], v113 offset:4096\n\t"
        "ds_read_b64_tr_b16 v[250:251], v113 offset:6144\n\t"
        "s_waitcnt lgkmcnt(8)\n\t"
        "ds_read_b64_tr_b16 v[240:241], v114 offset:0\n\t"
        "ds_read_b64_tr_b16 v[242:243], v114 offset:2048\n\t"
        "ds_read_b64_tr_b16 v[252:253], v114 offset:4096\n\t"
        "ds_read_b64_tr_b16 v[254:255], v114 offset:6144\n\t"
        "s_waitcnt lgkmcnt(8)\n\t"
        "ds_read_b64_tr_b16 v[56:57], v115 offset:0\n\t"
        "ds_read_b64_tr_b16 v[58:59], v115 offset:2048\n\t"
        "ds_read_b64_tr_b16 v[60:61], v115 offset:4096\n\t"
        "ds_read_b64_tr_b16 v[62:63], v115 offset:6144\n\t"
        "s_waitcnt lgkmcnt(0)\n\t"
        "ds_write_b128 v116, v[56:59] offset:30720\n\t"
        "ds_write_b128 v116, v[60:63] offset:31744\n\t"
        "ds_read_b128 v[88:91], v116 offset:0\n\t"
        "ds_read_b128 v[92:95], v116 offset:1024\n\t"
        "ds_read_b128 v[96:99], v116 offset:2048\n\t"
        "s_mov_b32 %[sp], 0\n\t"
        "LP_cv%=:\n\t"
        "buffer_load_dword v104, off, %[sw], %[cv] sc1\n\t"
        "s_waitcnt vmcnt(0) lgkmcnt(0)\n\t"
        "v_readfirstlane_b32 %[t0], v104\n\t"
        "s_cmp_ge_u32 %[t0], 8\n\t"
        "s_cbranch_scc1 LP_wait%=\n\t"
        "s_sleep 8\n\t"
        "s_add_u32 %[sp], %[sp], 1\n\t"
        "s_cmp_lt_u32 %[sp], 0x100000\n\t"
        "s_cbranch_scc1 LP_cv%=\n\t"
        "v_mov_b32_e32 v104, 256\n\t"
        "s_mov_b32 %[t0], 33024\n\t"
        "buffer_store_dword v104, off, %[sw], %[t0] sc1\n\t"
        "s_endpgm\n\t"
        "LP_wait%=:\n\t"
        "s_mov_b32 %[sp], 0\n\t"
        "LP_poll%=:\n\t"
        "buffer_load_dword v103, v118, %[sw], 0 offen sc1\n\t"
        "s_lshr_b32 %[t1], %[tl], 1\n\t"
        "s_waitcnt vmcnt(0)\n\t"
        "v_min_u32_dpp v103, v103, v103 quad_perm:[1,0,3,2] row_mask:0xf bank_mask:0xf\n\t"
        "s_nop 1\n\t"
        "v_min_u32_dpp v103, v103, v103 quad_perm:[2,3,0,1] row_mask:0xf bank_mask:0xf\n\t"
        "s_nop 1\n\t"
        "v_min_u32_dpp v103, v103, v103 row_ror:4 row_mask:0xf bank_mask:0xf\n\t"
        "s_nop 0\n\t"
        "v_readfirstlane_b32 %[pg], v103\n\t"
        "s_cmp_gt_u32 %[pg], %[t1]\n\t"
        "s_cbranch_scc1 LP_have%=\n\t"
        "s_cmp_eq_u32 %[pg], %[t1]\n\t"
        "s_cbranch_scc1 LP_near%=\n\t"
        "s_sleep 24\n\t"
        "LP_near%=:\n\t"
        "s_sleep 2\n\t"
        "s_add_u32 %[sp], %[sp], 1\n\t"
        "s_cmp_lt_u32 %[sp], 0x100000\n\t"
        "s_cbranch_scc1 LP_poll%=\n\t"
        "v_mov_b32_e32 v104, 128\n\t"
        "s_mov_b32 %[t0], 33024\n\t"
        "buffer_store_dword v104, off, %[sw], %[t0] sc1\n\t"
        "s_endpgm\n\t"
        "LP_have%=:\n\t"
        "s_lshl_b32 %[nx], %[tl], 17\n\t"
        "s_add_u32 %[t0], %[ro], 0\n\t"
        "s_and_b32 %[t0], %[t0], 7\n\t"
        "s_lshl_b32 %[t0], %[t0], 12\n\t"
        "s_add_u32 %[t0], %[t0], %[nx]\n\t"
        "buffer_load_dwordx4 v[24:27], v117, %[sw], %[t0] offen offset:0 sc1\n\t"
        "buffer_load_dwordx4 v[28:31], v117, %[sw], %[t0] offen offset:1024 sc1\n\t"
        "buffer_load_dwordx4 v[32:35], v117, %[sw], %[t0] offen offset:2048 sc1\n\t"
        "buffer_load_dwordx4 v[36:39], v117, %[sw], %[t0] offen offset:3072 sc1\n\t"
        "s_add_u32 %[t0], %[ro], 1\n\t"
        "s_and_b32 %[t0], %[t0], 7\n\t"
        "s_lshl_b32 %[t0], %[t0], 12\n\t"
        "s_add_u32 %[t0], %[t0], %[nx]\n\t"
        "buffer_load_dwordx4 v[40:43], v117, %[sw], %[t0] offen offset:0 sc1\n\t"
        "buffer_load_dwordx4 v[44:47], v117, %[sw], %[t0] offen offset:1024 sc1\n\t"
        "buffer_load_dwordx4 v[48:51], v117, %[sw], %[t0] offen offset:2048 sc1\n\t"
        "buffer_load_dwordx4 v[52:55], v117, %[sw], %[t0] offen offset:3072 sc1\n\t"
        "s_add_u32 %[t0], %[ro], 2\n\t"
        "s_and_b32 %[t0], %[t0], 7\n\t"
        "s_lshl_b32 %[t0], %[t0], 12\n\t"
        "s_add_u32 %[t0], %[t0], %[nx]\n\t"
        "buffer_load_dwordx4 v[56:59], v117, %[sw], %[t0] offen offset:0 sc1\n\t"
        "buffer_load_dwordx4 v[60:63], v117, %[sw], %[t0] offen offset:1024 sc1\n\t"
        "buffer_load_dwordx4 v[64:67], v117, %[sw], %[t0] offen offset:2048 sc1\n\t"
        "buffer_load_dwordx4 v[68:71], v117, %[sw], %[t0] offen offset:3072 sc1\n\t"
        "s_add_u32 %[t0], %[ro], 3\n\t"
        "s_and_b32 %[t0], %[t0], 7\n\t"
        "s_lshl_b32 %[t0], %[t0], 12\n\t"
        "s_add_u32 %[t0], %[t0], %[nx]\n\t"
        "buffer_load_dwordx4 v[72:75], v117, %[sw], %[t0] offen offset:0 sc1\n\t"
        "buffer_load_dwordx4 v[76:79], v117, %[sw], %[t0] offen offset:1024 sc1\n\t"
        "buffer_load_dwordx4 v[80:83], v117, %[sw], %[t0] offen offset:2048 sc1\n\t"
        "buffer_load_dwordx4 v[84:87], v117, %[sw], %[t0] offen offset:3072 sc1\n\t"
        "buffer_load_dword v103, v118, %[sw], 0 offen sc1\n\t"
        "LP_tile%=:\n\t"
        "s_and_b32 %[t0], %[tl], 1\n\t"
        "s_lshl_b32 %[t0], %[t0], 14\n\t"
        "s_lshr_b32 %[t1], %[tl], 1\n\t"
        "s_lshl_b32 %[t1], %[t1], 6\n\t"
        "s_add_u32 %[t0], %[t0], %[t1]\n\t"
        "buffer_load_dword v100, v119, %[sw], %[t0] offen sc1\n\t"
        "s_add_u32 %[t0], %[t0], 32768\n\t"
        "buffer_load_dword v101, v119, %[sw], %[t0] offen sc1\n\t"
        "s_lshl_b32 %[t0], %[tl], 6\n\t"
        "buffer_load_dword v102, v120, %[sw], %[t0] offen sc1\n\t"
        "buffer_load_dword v103, v118, %[sw], 0 offen sc1\n\t"
        "s_mov_b32 %[cu], %[nx]\n\t"
        "s_waitcnt vmcnt(20) lgkmcnt(2)\n\t"
        "v_mfma_f32_16x16x32_bf16 v[20:23], v[88:91], v[24:27], 0\n\t"
        "ds_read_b128 v[88:91], v116 offset:3072\n\t"
        "v_mfma_f32_16x16x32_bf16 v[8:11], a[0:3], v[24:27], 0\n\t"
        "v_mfma_f32_16x16x32_bf16 v[12:15], a[4:7], v[24:27], 0\n\t"
        "v_mfma_f32_16x16x32_bf16 v[16:19], a[8:11], v[24:27], 0\n\t"
        "s_add_u32 %[t0], %[ro], 4\n\t"
        "s_and_b32 %[t0], %[t0], 7\n\t"
        "s_lshl_b32 %[t0], %[t0], 12\n\t"
        "s_add_u32 %[t0], %[t0], %[cu]\n\t"
        "buffer_load_dwordx4 v[24:27], v117, %[sw], %[t0] offen offset:0 sc1\n\t"
        "s_waitcnt vmcnt(20) lgkmcnt(2)\n\t"
        "v_mfma_f32_16x16x32_bf16 v[20:23], v[92:95], v[28:31], v[20:23]\n\t"
        "ds_read_b128 v[92:95], v116 offset:4096\n\t"
        "v_mfma_f32_16x16x32_bf16 v[8:11], a[12:15], v[28:31], v[8:11]\n\t"
        "v_mfma_f32_16x16x32_bf16 v[12:15], a[16:19], v[28:31], v[12:15]\n\t"
        "v_mfma_f32_16x16x32_bf16 v[16:19], a[20:23], v[28:31], v[16:19]\n\t"
        "buffer_load_dwordx4 v[28:31], v117, %[sw], %[t0] offen offset:1024 sc1\n\t"
        "s_waitcnt vmcnt(20) lgkmcnt(2)\n\t"
        "v_mfma_f32_16x16x32_bf16 v[20:23], v[96:99], v[32:35], v[20:23]\n\t"
        "ds_read_b128 v[96:99], v116 offset:5120\n\t"
        "v_mfma_f32_16x16x32_bf16 v[8:11], a[24:27], v[32:35], v[8:11]\n\t"
        "v_mfma_f32_16x16x32_bf16 v[12:15], a[28:31], v[32:35], v[12:15]\n\t"
        "v_mfma_f32_16x16x32_bf16 v[16:19], a[32:35], v[32:35], v[16:19]\n\t"
        "buffer_load_dwordx4 v[32:35], v117, %[sw], %[t0] offen offset:2048 sc1\n\t"
        "s_waitcnt vmcnt(20) lgkmcnt(2)\n\t"
        "v_mfma_f32_16x16x32_bf16 v[20:23], v[88:91], v[36:39], v[20:23]\n\t"
        "ds_read_b128 v[88:91], v116 offset:6144\n\t"
        "v_mfma_f32_16x16x32_bf16 v[8:11], a[36:39], v[36:39], v[8:11]\n\t"
        "v_mfma_f32_16x16x32_bf16 v[12:15], a[40:43], v[36:39], v[12:15]\n\t"
        "v_mfma_f32_16x16x32_bf16 v[16:19], a[44:47], v[36:39], v[16:19]\n\t"
        "buffer_load_dwordx4 v[36:39], v117, %[sw], %[t0] offen offset:3072 sc1\n\t"
        "s_waitcnt vmcnt(20) lgkmcnt(2)\n\t"
        "v_mfma_f32_16x16x32_bf16 v[20:23], v[92:95], v[40:43], v[20:23]\n\t"
        "ds_read_b128 v[92:95], v116 offset:7168\n\t"
        "v_mfma_f32_16x16x32_bf16 v[8:11], a[48:51], v[40:43], v[8:11]\n\t"
        "v_mfma_f32_16x16x32_bf16 v[12:15], a[52:55], v[40:43], v[12:15]\n\t"
        "v_mfma_f32_16x16x32_bf16 v[16:19], a[56:59], v[40:43], v[16:19]\n\t"
        "s_add_u32 %[t0], %[ro], 5\n\t"
        "s_and_b32 %[t0], %[t0], 7\n\t"
        "s_lshl_b32 %[t0], %[t0], 12\n\t"
        "s_add_u32 %[t0], %[t0], %[cu]\n\t"
        "buffer_load_dwordx4 v[40:43], v117, %[sw], %[t0] offen offset:0 sc1\n\t"
        "s_waitcnt vmcnt(20) lgkmcnt(2)\n\t"
        "v_mfma_f32_16x16x32_bf16 v[20:23], v[96:99], v[44:47], v[20:23]\n\t"
        "ds_read_b128 v[96:99], v116 offset:8192\n\t"
        "v_mfma_f32_16x16x32_bf16 v[8:11], a[60:63], v[44:47], v[8:11]\n\t"
        "v_mfma_f32_16x16x32_bf16 v[12:15], a[64:67], v[44:47], v[12:15]\n\t"
        "v_mfma_f32_16x16x32_bf16 v[16:19], a[68:71], v[44:47], v[16:19]\n\t"
        "buffer_load_dwordx4 v[44:47], v117, %[sw], %[t0] offen offset:1024 sc1\n\t"
        "s_waitcnt vmcnt(20) lgkmcnt(2)\n\t"
        "v_mfma_f32_16x16x32_bf16 v[20:23], v[88:91], v[48:51], v[20:23]\n\t"
        "ds_read_b128 v[88:91], v116 offset:9216\n\t"
        "v_mfma_f32_16x16x32_bf16 v[8:11], a[72:75], v[48:51], v[8:11]\n\t"
        "v_mfma_f32_16x16x32_bf16 v[12:15], a[76:79], v[48:51], v[12:15]\n\t"
        "v_mfma_f32_16x16x32_bf16 v[16:19], a[80:83], v[48:51], v[16:19]\n\t"
        "buffer_load_dwordx4 v[48:51], v117, %[sw], %[t0] offen offset:2048 sc1\n\t"
        "s_waitcnt vmcnt(20) lgkmcnt(2)\n\t"
        "v_mfma_f32_16x16x32_bf16 v[20:23], v[92:95], v[52:55], v[20:23]\n\t"
        "ds_read_b128 v[92:95], v116 offset:10240\n\t"
        "v_mfma_f32_16x16x32_bf16 v[8:11], a[84:87], v[52:55], v[8:11]\n\t"
        "v_mfma_f32_16x16x32_bf16 v[12:15], a[88:91], v[52:55], v[12:15]\n\t"
        "v_mfma_f32_16x16x32_bf16 v[16:19], a[92:95], v[52:55], v[16:19]\n\t"
        "buffer_load_dwordx4 v[52:55], v117, %[sw], %[t0] offen offset:3072 sc1\n\t"
        "s_waitcnt vmcnt(20) lgkmcnt(2)\n\t"
        "s_barrier\n\t"
        "v_mfma_f32_16x16x32_bf16 v[20:23], v[96:99], v[56:59], v[20:23]\n\t"
        "ds_read_b128 v[96:99], v116 offset:11264\n\t"
        "v_mfma_f32_16x16x32_bf16 v[8:11], a[96:99], v[56:59], v[8:11]\n\t"
        "v_mfma_f32_16x16x32_bf16 v[12:15], a[100:103], v[56:59], v[12:15]\n\t"
        "v_mfma_f32_16x16x32_bf16 v[16:19], a[104:107], v[56:59], v[16:19]\n\t"
        "s_add_u32 %[t0], %[ro], 6\n\t"
        "s_and_b32 %[t0], %[t0], 7\n\t"
        "s_lshl_b32 %[t0], %[t0], 12\n\t"
        "s_add_u32 %[t0], %[t0], %[cu]\n\t"
        "buffer_load_dwordx4 v[56:59], v117, %[sw], %[t0] offen offset:0 sc1\n\t"
        "s_waitcnt vmcnt(20) lgkmcnt(2)\n\t"
        "v_mfma_f32_16x16x32_bf16 v[20:23], v[88:91], v[60:63], v[20:23]\n\t"
        "ds_read_b128 v[88:91], v116 offset:12288\n\t"
        "v_mfma_f32_16x16x32_bf16 v[8:11], a[108:111], v[60:63], v[8:11]\n\t"
        "v_mfma_f32_16x16x32_bf16 v[12:15], a[112:115], v[60:63], v[12:15]\n\t"
        "v_mfma_f32_16x16x32_bf16 v[16:19], a[116:119], v[60:63], v[16:19]\n\t"
        "buffer_load_dwordx4 v[60:63], v117, %[sw], %[t0] offen offset:1024 sc1\n\t"
        "s_waitcnt vmcnt(20) lgkmcnt(2)\n\t"
        "v_mfma_f32_16x16x32_bf16 v[20:23], v[92:95], v[64:67], v[20:23]\n\t"
        "ds_read_b128 v[92:95], v116 offset:13312\n\t"
        "v_mfma_f32_16x16x32_bf16 v[8:11], a[120:123], v[64:67], v[8:11]\n\t"
        "v_mfma_f32_16x16x32_bf16 v[12:15], a[124:127], v[64:67], v[12:15]\n\t"
        "v_mfma_f32_16x16x32_bf16 v[16:19], a[128:131], v[64:67], v[16:19]\n\t"
        "buffer_load_dwordx4 v[64:67], v117, %[sw], %[t0] offen offset:2048 sc1\n\t"
        "s_waitcnt vmcnt(20) lgkmcnt(2)\n\t"
        "v_mfma_f32_16x16x32_bf16 v[20:23], v[96:99], v[68:71], v[20:23]\n\t"
        "ds_read_b128 v[96:99], v116 offset:14336\n\t"
        "v_mfma_f32_16x16x32_bf16 v[8:11], a[132:135], v[68:71], v[8:11]\n\t"
        "v_mfma_f32_16x16x32_bf16 v[12:15], a[136:139], v[68:71], v[12:15]\n\t"
        "v_mfma_f32_16x16x32_bf16 v[16:19], a[140:143], v[68:71], v[16:19]\n\t"
        "buffer_load_dwordx4 v[68:71], v117, %[sw], %[t0] offen offset:3072 sc1\n\t"
        "s_waitcnt vmcnt(20) lgkmcnt(2)\n\t"
        "v_mfma_f32_16x16x32_bf16 v[20:23], v[88:91], v[72:75], v[20:23]\n\t"
        "ds_read_b128 v[88:91], v116 offset:15360\n\t"
        "v_mfma_f32_16x16x32_bf16 v[8:11], a[144:147], v[72:75], v[8:11]\n\t"
        "v_mfma_f32_16x16x32_bf16 v[12:15], a[148:151], v[72:75], v[12:15]\n\t"
        "v_mfma_f32_16x16x32_bf16 v[16:19], a[152:155], v[72:75], v[16:19]\n\t"
        "s_add_u32 %[t0], %[ro], 7\n\t"
        "s_and_b32 %[t0], %[t0], 7\n\t"
        "s_lshl_b32 %[t0], %[t0], 12\n\t"
        "s_add_u32 %[t0], %[t0], %[cu]\n\t"
        "buffer_load_dwordx4 v[72:75], v117, %[sw], %[t0] offen offset:0 sc1\n\t"
        "s_waitcnt vmcnt(13) lgkmcnt(2)\n\t"
        "v_mfma_f32_16x16x32_bf16 v[20:23], v[92:95], v[76:79], v[20:23]\n\t"
        "ds_read_b128 v[92:95], v116 offset:16384\n\t"
        "v_min_u32_dpp v103, v103, v103 quad_perm:[1,0,3,2] row_mask:0xf bank_mask:0xf\n\t"
        "s_nop 0\n\t"
        "v_mfma_f32_16x16x32_bf16 v[8:11], a[156:159], v[76:79], v[8:11]\n\t"
        "v_min_u32_dpp v103, v103, v103 quad_perm:[2,3,0,1] row_mask:0xf bank_mask:0xf\n\t"
        "s_nop 0\n\t"
        "v_mfma_f32_16x16x32_bf16 v[12:15], a[160:163], v[76:79], v[12:15]\n\t"
        "v_min_u32_dpp v103, v103, v103 row_ror:4 row_mask:0xf bank_mask:0xf\n\t"
        "s_nop 0\n\t"
        "v_mfma_f32_16x16x32_bf16 v[16:19], a[164:167], v[76:79], v[16:19]\n\t"
        "buffer_load_dwordx4 v[76:79], v117, %[sw], %[t0] offen offset:1024 sc1\n\t"
        "s_waitcnt vmcnt(20) lgkmcnt(2)\n\t"
        "v_mfma_f32_16x16x32_bf16 v[20:23], v[96:99], v[80:83], v[20:23]\n\t"
        "ds_read_b128 v[96:99], v116 offset:17408\n\t"
        "v_readfirstlane_b32 %[pg], v103\n\t"
        "s_add_u32 %[t1], %[tl], 8\n\t"
        "s_lshl_b32 %[nx], %[t1], 17\n\t"
        "s_lshr_b32 %[t1], %[t1], 1\n\t"
        "v_mfma_f32_16x16x32_bf16 v[8:11], a[168:171], v[80:83], v[8:11]\n\t"
        "s_cmp_gt_u32 %[pg], %[t1]\n\t"
        "s_cselect_b32 %[nx], %[nx], 0xf0000000\n\t"
        "v_mfma_f32_16x16x32_bf16 v[12:15], a[172:175], v[80:83], v[12:15]\n\t"
        "v_mfma_f32_16x16x32_bf16 v[16:19], a[176:179], v[80:83], v[16:19]\n\t"
        "buffer_load_dwordx4 v[80:83], v117, %[sw], %[t0] offen offset:2048 sc1\n\t"
        "s_waitcnt vmcnt(20) lgkmcnt(2)\n\t"
        "v_mfma_f32_16x16x32_bf16 v[20:23], v[88:91], v[84:87], v[20:23]\n\t"
        "ds_read_b128 v[88:91], v116 offset:18432\n\t"
        "v_mfma_f32_16x16x32_bf16 v[8:11], a[180:183], v[84:87], v[8:11]\n\t"
        "v_mfma_f32_16x16x32_bf16 v[12:15], a[184:187], v[84:87], v[12:15]\n\t"
        "v_mfma_f32_16x16x32_bf16 v[16:19], a[188:191], v[84:87], v[16:19]\n\t"
        "buffer_load_dwordx4 v[84:87], v117, %[sw], %[t0] offen offset:3072 sc1\n\t"
        "s_waitcnt vmcnt(15) lgkmcnt(2)\n\t"
        "v_mfma_f32_16x16x32_bf16 v[20:23], v[92:95], v[24:27], v[20:23]\n\t"
        "ds_read_b128 v[92:95], v116 offset:19456\n\t"
        "v_mfma_f32_16x16x32_bf16 v[8:11], a[192:195], v[24:27], v[8:11]\n\t"
        "v_mfma_f32_16x16x32_bf16 v[12:15], a[196:199], v[24:27], v[12:15]\n\t"
        "v_mfma_f32_16x16x32_bf16 v[16:19], a[200:203], v[24:27], v[16:19]\n\t"
        "s_add_u32 %[t0], %[ro], 0\n\t"
        "s_and_b32 %[t0], %[t0], 7\n\t"
        "s_lshl_b32 %[t0], %[t0], 12\n\t"
        "s_add_u32 %[t0], %[t0], %[nx]\n\t"
        "buffer_load_dwordx4 v[24:27], v117, %[sw], %[t0] offen offset:0 sc1\n\t"
        "s_waitcnt vmcnt(15) lgkmcnt(2)\n\t"
        "v_mfma_f32_16x16x32_bf16 v[20:23], v[96:99], v[28:31], v[20:23]\n\t"
        "ds_read_b128 v[96:99], v116 offset:20480\n\t"
        "v_mfma_f32_16x16x32_bf16 v[8:11], a[204:207], v[28:31], v[8:11]\n\t"
        "v_mfma_f32_16x16x32_bf16 v[12:15], a[208:211], v[28:31], v[12:15]\n\t"
        "v_sub_f32_e32 v104, 1.0, v102\n\t"
        "v_mfma_f32_16x16x32_bf16 v[16:19], a[212:215], v[28:31], v[16:19]\n\t"
        "buffer_load_dwordx4 v[28:31], v117, %[sw], %[t0] offen offset:1024 sc1\n\t"
        "s_waitcnt vmcnt(15) lgkmcnt(2)\n\t"
        "v_mfma_f32_16x16x32_bf16 v[20:23], v[88:91], v[32:35], v[20:23]\n\t"
        "ds_read_b128 v[88:91], v116 offset:21504\n\t"
        "v_mfma_f32_16x16x32_bf16 v[8:11], a[216:219], v[32:35], v[8:11]\n\t"
        "v_mfma_f32_16x16x32_bf16 v[12:15], a[220:223], v[32:35], v[12:15]\n\t"
        "v_cvt_pk_bf16_f32 v104, 0, v104\n\t"
        "v_mfma_f32_16x16x32_bf16 v[16:19], a[224:227], v[32:35], v[16:19]\n\t"
        "buffer_load_dwordx4 v[32:35], v117, %[sw], %[t0] offen offset:2048 sc1\n\t"
        "s_waitcnt vmcnt(15) lgkmcnt(2)\n\t"
        "v_mfma_f32_16x16x32_bf16 v[20:23], v[92:95], v[36:39], v[20:23]\n\t"
        "ds_read_b128 v[92:95], v116 offset:22528\n\t"
        "v_mfma_f32_16x16x32_bf16 v[8:11], a[228:231], v[36:39], v[8:11]\n\t"
        "v_mfma_f32_16x16x32_bf16 v[12:15], a[232:235], v[36:39], v[12:15]\n\t"
        "v_lshlrev_b32_e32 v106, 16, v100\n\t"
        "v_mfma_f32_16x16x32_bf16 v[16:19], a[236:239], v[36:39], v[16:19]\n\t"
        "buffer_load_dwordx4 v[36:39], v117, %[sw], %[t0] offen offset:3072 sc1\n\t"
        "s_waitcnt vmcnt(15) lgkmcnt(2)\n\t"
        "v_mfma_f32_16x16x32_bf16 v[20:23], v[96:99], v[40:43], v[20:23]\n\t"
        "ds_read_b128 v[96:99], v116 offset:23552\n\t"
        "v_mfma_f32_16x16x32_bf16 v[8:11], a[240:243], v[40:43], v[8:11]\n\t"
        "v_mfma_f32_16x16x32_bf16 v[12:15], a[244:247], v[40:43], v[12:15]\n\t"
        "v_mul_f32_e32 v106, v102, v106\n\t"
        "v_mfma_f32_16x16x32_bf16 v[16:19], a[248:251], v[40:43], v[16:19]\n\t"
        "s_add_u32 %[t0], %[ro], 1\n\t"
        "s_and_b32 %[t0], %[t0], 7\n\t"
        "s_lshl_b32 %[t0], %[t0], 12\n\t"
        "s_add_u32 %[t0], %[t0], %[nx]\n\t"
        "buffer_load_dwordx4 v[40:43], v117, %[sw], %[t0] offen offset:0 sc1\n\t"
        "s_waitcnt vmcnt(15) lgkmcnt(2)\n\t"
        "v_mfma_f32_16x16x32_bf16 v[20:23], v[88:91], v[44:47], v[20:23]\n\t"
        "ds_read_b128 v[88:91], v116 offset:24576\n\t"
        "v_mfma_f32_16x16x32_bf16 v[8:11], a[252:255], v[44:47], v[8:11]\n\t"
        "v_mfma_f32_16x16x32_bf16 v[12:15], v[128:131], v[44:47], v[12:15]\n\t"
        "v_cvt_pk_bf16_f32 v106, 0, v106\n\t"
        "v_mfma_f32_16x16x32_bf16 v[16:19], v[132:135], v[44:47], v[16:19]\n\t"
        "buffer_load_dwordx4 v[44:47], v117, %[sw], %[t0] offen offset:1024 sc1\n\t"
        "s_waitcnt vmcnt(15) lgkmcnt(2)\n\t"
        "v_mfma_f32_16x16x32_bf16 v[20:23], v[92:95], v[48:51], v[20:23]\n\t"
        "ds_read_b128 v[92:95], v116 offset:25600\n\t"
        "v_mfma_f32_16x16x32_bf16 v[8:11], v[136:139], v[48:51], v[8:11]\n\t"
        "v_mfma_f32_16x16x32_bf16 v[12:15], v[140:143], v[48:51], v[12:15]\n\t"
        "v_and_b32_e32 v107, 0xffff0000, v100\n\t"
        "v_mfma_f32_16x16x32_bf16 v[16:19], v[144:147], v[48:51], v[16:19]\n\t"
        "buffer_load_dwordx4 v[48:51], v117, %[sw], %[t0] offen offset:2048 sc1\n\t"
        "s_waitcnt vmcnt(15) lgkmcnt(2)\n\t"
        "v_mfma_f32_16x16x32_bf16 v[20:23], v[96:99], v[52:55], v[20:23]\n\t"
        "ds_read_b128 v[96:99], v116 offset:26624\n\t"
        "v_mfma_f32_16x16x32_bf16 v[8:11], v[148:151], v[52:55], v[8:11]\n\t"
        "v_mfma_f32_16x16x32_bf16 v[12:15], v[152:155], v[52:55], v[12:15]\n\t"
        "v_mul_f32_e32 v107, v102, v107\n\t"
        "v_mfma_f32_16x16x32_bf16 v[16:19], v[156:159], v[52:55], v[16:19]\n\t"
        "buffer_load_dwordx4 v[52:55], v117, %[sw], %[t0] offen offset:3072 sc1\n\t"
        "s_waitcnt vmcnt(15) lgkmcnt(2)\n\t"
        "v_mfma_f32_16x16x32_bf16 v[20:23], v[88:91], v[56:59], v[20:23]\n\t"
        "ds_read_b128 v[88:91], v116 offset:27648\n\t"
        "v_mfma_f32_16x16x32_bf16 v[8:11], v[160:163], v[56:59], v[8:11]\n\t"
        "v_mfma_f32_16x16x32_bf16 v[12:15], v[164:167], v[56:59], v[12:15]\n\t"
        "v_cvt_pk_bf16_f32 v107, 0, v107\n\t"
        "v_mfma_f32_16x16x32_bf16 v[16:19], v[168:171], v[56:59], v[16:19]\n\t"
        "s_add_u32 %[t0], %[ro], 2\n\t"
        "s_and_b32 %[t0], %[t0], 7\n\t"
        "s_lshl_b32 %[t0], %[t0], 12\n\t"
        "s_add_u32 %[t0], %[t0], %[nx]\n\t"
        "buffer_load_dwordx4 v[56:59], v117, %[sw], %[t0] offen offset:0 sc1\n\t"
        "s_waitcnt vmcnt(15) lgkmcnt(2)\n\t"
        "v_mfma_f32_16x16x32_bf16 v[20:23], v[92:95], v[60:63], v[20:23]\n\t"
        "ds_read_b128 v[92:95], v116 offset:28672\n\t"
        "v_mfma_f32_16x16x32_bf16 v[8:11], v[172:175], v[60:63], v[8:11]\n\t"
        "v_mfma_f32_16x16x32_bf16 v[12:15], v[176:179], v[60:63], v[12:15]\n\t"
        "v_lshlrev_b32_e32 v108, 16, v101\n\t"
        "v_mfma_f32_16x16x32_bf16 v[16:19], v[180:183], v[60:63], v[16:19]\n\t"
        "buffer_load_dwordx4 v[60:63], v117, %[sw], %[t0] offen offset:1024 sc1\n\t"
        "s_waitcnt vmcnt(15) lgkmcnt(2)\n\t"
        "v_mfma_f32_16x16x32_bf16 v[20:23], v[96:99], v[64:67], v[20:23]\n\t"
        "ds_read_b128 v[96:99], v116 offset:29696\n\t"
        "v_mfma_f32_16x16x32_bf16 v[8:11], v[184:187], v[64:67], v[8:11]\n\t"
        "v_mfma_f32_16x16x32_bf16 v[12:15], v[188:191], v[64:67], v[12:15]\n\t"
        "v_mul_f32_e32 v108, v102, v108\n\t"
        "v_mfma_f32_16x16x32_bf16 v[16:19], v[192:195], v[64:67], v[16:19]\n\t"
        "buffer_load_dwordx4 v[64:67], v117, %[sw], %[t0] offen offset:2048 sc1\n\t"
        "s_waitcnt vmcnt(15) lgkmcnt(2)\n\t"
        "v_mfma_f32_16x16x32_bf16 v[20:23], v[88:91], v[68:71], v[20:23]\n\t"
        "ds_read_b128 v[88:91], v116 offset:30720\n\t"
        "v_mfma_f32_16x16x32_bf16 v[8:11], v[196:199], v[68:71], v[8:11]\n\t"
        "v_mfma_f32_16x16x32_bf16 v[12:15], v[200:203], v[68:71], v[12:15]\n\t"
        "v_cvt_pk_bf16_f32 v108, 0, v108\n\t"
        "v_mfma_f32_16x16x32_bf16 v[16:19], v[204:207], v[68:71], v[16:19]\n\t"
        "buffer_load_dwordx4 v[68:71], v117, %[sw], %[t0] offen offset:3072 sc1\n\t"
        "s_waitcnt vmcnt(15) lgkmcnt(2)\n\t"
        "v_mfma_f32_16x16x32_bf16 v[20:23], v[92:95], v[72:75], v[20:23]\n\t"
        "ds_read_b128 v[92:95], v116 offset:31744\n\t"
        "v_mfma_f32_16x16x32_bf16 v[8:11], v[208:211], v[72:75], v[8:11]\n\t"
        "v_mfma_f32_16x16x32_bf16 v[12:15], v[212:215], v[72:75], v[12:15]\n\t"
        "v_and_b32_e32 v109, 0xffff0000, v101\n\t"
        "v_mfma_f32_16x16x32_bf16 v[16:19], v[216:219], v[72:75], v[16:19]\n\t"
        "s_add_u32 %[t0], %[ro], 3\n\t"
        "s_and_b32 %[t0], %[t0], 7\n\t"
        "s_lshl_b32 %[t0], %[t0], 12\n\t"
        "s_add_u32 %[t0], %[t0], %[nx]\n\t"
        "buffer_load_dwordx4 v[72:75], v117, %[sw], %[t0] offen offset:0 sc1\n\t"
        "s_waitcnt vmcnt(15) lgkmcnt(2)\n\t"
        "v_mfma_f32_16x16x32_bf16 v[20:23], v[96:99], v[76:79], v[20:23]\n\t"
        "v_mfma_f32_16x16x32_bf16 v[8:11], v[220:223], v[76:79], v[8:11]\n\t"
        "v_mfma_f32_16x16x32_bf16 v[12:15], v[224:227], v[76:79], v[12:15]\n\t"
        "v_mul_f32_e32 v109, v102, v109\n\t"
        "v_mfma_f32_16x16x32_bf16 v[16:19], v[228:231], v[76:79], v[16:19]\n\t"
        "buffer_load_dwordx4 v[76:79], v117, %[sw], %[t0] offen offset:1024 sc1\n\t"
        "s_waitcnt vmcnt(15) lgkmcnt(1)\n\t"
        "v_mfma_f32_16x16x32_bf16 v[20:23], v[88:91], v[80:83], v[20:23]\n\t"
        "ds_read_b128 v[88:91], v116 offset:0\n\t"
        "v_mfma_f32_16x16x32_bf16 v[8:11], v[232:235], v[80:83], v[8:11]\n\t"
        "v_mfma_f32_16x16x32_bf16 v[12:15], v[236:239], v[80:83], v[12:15]\n\t"
        "v_cvt_pk_bf16_f32 v109, 0, v109\n\t"
        "v_mfma_f32_16x16x32_bf16 v[16:19], v[240:243], v[80:83], v[16:19]\n\t"
        "buffer_load_dwordx4 v[80:83], v117, %[sw], %[t0] offen offset:2048 sc1\n\t"
        "s_waitcnt vmcnt(15) lgkmcnt(1)\n\t"
        "v_mfma_f32_16x16x32_bf16 v[20:23], v[92:95], v[84:87], v[20:23]\n\t"
        "ds_read_b128 v[92:95], v116 offset:1024\n\t"
        "v_mfma_f32_16x16x32_bf16 v[8:11], v[244:247], v[84:87], v[8:11]\n\t"
        "ds_read_b128 v[96:99], v116 offset:2048\n\t"
        "v_mfma_f32_16x16x32_bf16 v[12:15], v[248:251], v[84:87], v[12:15]\n\t"
        "v_mfma_f32_16x16x32_bf16 v[16:19], v[252:255], v[84:87], v[16:19]\n\t"
        "buffer_load_dwordx4 v[84:87], v117, %[sw], %[t0] offen offset:3072 sc1\n\t"
        "s_lshl_b32 %[t0], %[tl], 15\n\t"
        "s_nop 0\n\t"
        "ds_write_b128 v122, v[20:23] offset:192\n\t"
        "s_nop 0\n\t"
        "ds_write_b128 v122, v[8:11] offset:0\n\t"
        "s_nop 0\n\t"
        "ds_write_b128 v122, v[12:15] offset:64\n\t"
        "ds_write_b128 v122, v[16:19] offset:128\n\t"
        "s_waitcnt lgkmcnt(0)\n\t"
        "s_barrier\n\t"
        "ds_read_b128 v[8:11], v123 offset:0\n\t"
        "ds_read_b128 v[12:15], v123 offset:4352\n\t"
        "ds_read_b128 v[16:19], v123 offset:8704\n\t"
        "ds_read_b128 v[20:23], v123 offset:13056\n\t"
        "s_waitcnt lgkmcnt(2)\n\t"
        "v_pk_add_f32 v[8:9], v[8:9], v[12:13]\n\t"
        "v_pk_add_f32 v[10:11], v[10:11], v[14:15]\n\t"
        "s_waitcnt lgkmcnt(0)\n\t"
        "v_pk_add_f32 v[16:17], v[16:17], v[20:21]\n\t"
        "v_pk_add_f32 v[18:19], v[18:19], v[22:23]\n\t"
        "v_pk_add_f32 v[8:9], v[8:9], v[16:17]\n\t"
        "v_pk_add_f32 v[10:11], v[10:11], v[18:19]\n\t"
        "v_cvt_pk_bf16_f32 v8, 0, v8\n\t"
        "v_cvt_pk_bf16_f32 v9, 0, v9\n\t"
        "v_cvt_pk_bf16_f32 v10, 0, v10\n\t"
        "v_cvt_pk_bf16_f32 v11, 0, v11\n\t"
        "v_mul_f32_e32 v8, v104, v8\n\t"
        "v_mul_f32_e32 v9, v104, v9\n\t"
        "v_mul_f32_e32 v10, v104, v10\n\t"
        "v_mul_f32_e32 v11, v104, v11\n\t"
        "v_cvt_pk_bf16_f32 v8, 0, v8\n\t"
        "v_cvt_pk_bf16_f32 v9, 0, v9\n\t"
        "v_cvt_pk_bf16_f32 v10, 0, v10\n\t"
        "v_cvt_pk_bf16_f32 v11, 0, v11\n\t"
        "v_add_f32_e32 v8, v106, v8\n\t"
        "v_add_f32_e32 v9, v107, v9\n\t"
        "v_add_f32_e32 v10, v108, v10\n\t"
        "v_add_f32_e32 v11, v109, v11\n\t"
        "v_cvt_pk_bf16_f32 v104, v8, v9\n\t"
        "v_cvt_pk_bf16_f32 v105, v10, v11\n\t"
        "buffer_store_dwordx2 v[104:105], v121, %[su], %[t0] offen sc1\n\t"
        "s_add_u32 %[tl], %[tl], 8\n\t"
        "s_cmp_ge_u32 %[tl], 128\n\t"
        "s_cbranch_scc1 LP_end%=\n\t"
        "s_cmp_lg_u32 %[nx], 0xf0000000\n\t"
        "s_cbranch_scc1 LP_tile%=\n\t"
        "s_branch LP_wait%=\n\t"
        "LP_end%=:\n\t"
        "s_waitcnt vmcnt(0) lgkmcnt(0)\n\t"
        ".Lk_end_P:\n\t"
        : [t0] "=&s"(t0), [t1] "=&s"(t1), [tl] "+s"(tl), [pg] "=&s"(pg), [cu] "=&s"(cu), [nx] "=&s"(nx), [sp] "=&s"(sp), [tu] "=&s"(tu)
        : [so] "s"(so), [sw] "s"(sw), [su] "s"(su), [cv] "s"(F_CVY + nt * 128), [ro] "s"(PROT ? (nt + 2 * rp) & 7 : 0), [db] "s"(W_DBG + (128 + j) * 8192 + 512),
          "{v110}"((w * 1024 + (l >> 3)) * 2048 + nt * 128 + (l & 7) * 16), "{v111}"(stg + ((l * 16) ^ ((l & 48) << 1))), "{v112}"(lr), "{v113}"(lr ^ 32u), "{v114}"(lr ^ 64u), "{v115}"(lr ^ 96u),
          "{v116}"(lb + w * 32768 + l * 16), "{v117}"(W_OX + w * 32768 + l * 16), "{v118}"(F_PROG + (w * 8 + (l & 7)) * 128),
          "{v119}"(W_A + ((nt * 2 + (n >> 3)) * 4 + 2 * (n & 1)) * 32768 + w * 4096 + ((n & 7) >> 1) * 16 + g * 4), "{v120}"(W_GATE + (4 * w + g) * 4), "{v121}"((4 * w + g) * 2048 + nt * 128 + n * 8),
          "{v122}"(lb + 131072 + w * 4352 + n * 272 + g * 16), "{v123}"(lb + 131072 + (4 * w + g) * 272 + n * 16)
        : "memory", "scc", "vcc", "v8", "v9", "v10", "v11", "v12", "v13", "v14", "v15", "v16", "v17", "v18", "v19", "v20", "v21", "v22", "v23", "v24", "v25", "v26", "v27", "v28", "v29", "v30", "v31", "v32", "v33", "v34", "v35", "v36", "v37", "v38", "v39", "v40", "v41", "v42", "v43", "v44", "v45", "v46", "v47", "v48", "v49", "v50", "v51", "v52", "v53", "v54", "v55", "v56", "v57", "v58", "v59", "v60", "v61", "v62", "v63", "v64", "v65", "v66", "v67", "v68", "v69", "v70", "v71", "v72", "v73", "v74", "v75", "v76", "v77", "v78", "v79", "v80", "v81", "v82", "v83", "v84", "v85", "v86", "v87", "v88", "v89", "v90", "v91", "v92", "v93", "v94", "v95", "v96", "v97", "v98", "v99", "v100", "v101", "v102", "v103", "v104", "v105", "v106", "v107", "v108", "v109", "v128", "v129", "v130", "v131", "v132", "v133", "v134", "v135", "v136", "v137", "v138", "v139", "v140", "v141", "v142", "v143", "v144", "v145", "v146", "v147", "v148", "v149", "v150", "v151", "v152", "v153", "v154", "v155", "v156", "v157", "v158", "v159", "v160", "v161", "v162", "v163", "v164", "v165", "v166", "v167", "v168", "v169", "v170", "v171", "v172", "v173", "v174", "v175", "v176", "v177", "v178", "v179", "v180", "v181", "v182", "v183", "v184", "v185", "v186", "v187", "v188", "v189", "v190", "v191", "v192", "v193", "v194", "v195", "v196", "v197", "v198", "v199", "v200", "v201", "v202", "v203", "v204", "v205", "v206", "v207", "v208", "v209", "v210", "v211", "v212", "v213", "v214", "v215", "v216", "v217", "v218", "v219", "v220", "v221", "v222", "v223", "v224", "v225", "v226", "v227", "v228", "v229", "v230", "v231", "v232", "v233", "v234", "v235", "v236", "v237", "v238", "v239", "v240", "v241", "v242", "v243", "v244", "v245", "v246", "v247", "v248", "v249", "v250", "v251", "v252", "v253", "v254", "v255", "a0", "a1", "a2", "a3", "a4", "a5", "a6", "a7", "a8", "a9", "a10", "a11", "a12", "a13", "a14", "a15", "a16", "a17", "a18", "a19", "a20", "a21", "a22", "a23", "a24", "a25", "a26", "a27", "a28", "a29", "a30", "a31", "a32", "a33", "a34", "a35", "a36", "a37", "a38", "a39", "a40", "a41", "a42", "a43", "a44", "a45", "a46", "a47", "a48", "a49", "a50", "a51", "a52", "a53", "a54", "a55", "a56", "a57", "a58", "a59", "a60", "a61", "a62", "a63", "a64", "a65", "a66", "a67", "a68", "a69", "a70", "a71", "a72", "a73", "a74", "a75", "a76", "a77", "a78", "a79", "a80", "a81", "a82", "a83", "a84", "a85", "a86", "a87", "a88", "a89", "a90", "a91", "a92", "a93", "a94", "a95", "a96", "a97", "a98", "a99", "a100", "a101", "a102", "a103", "a104", "a105", "a106", "a107", "a108", "a109", "a110", "a111", "a112", "a113", "a114", "a115", "a116", "a117", "a118", "a119", "a120", "a121", "a122", "a123", "a124", "a125", "a126", "a127", "a128", "a129", "a130", "a131", "a132", "a133", "a134", "a135", "a136", "a137", "a138", "a139", "a140", "a141", "a142", "a143", "a144", "a145", "a146", "a147", "a148", "a149", "a150", "a151", "a152", "a153", "a154", "a155", "a156", "a157", "a158", "a159", "a160", "a161", "a162", "a163", "a164", "a165", "a166", "a167", "a168", "a169", "a170", "a171", "a172", "a173", "a174", "a175", "a176", "a177", "a178", "a179", "a180", "a181", "a182", "a183", "a184", "a185", "a186", "a187", "a188", "a189", "a190", "a191", "a192", "a193", "a194", "a195", "a196", "a197", "a198", "a199", "a200", "a201", "a202", "a203", "a204", "a205", "a206", "a207", "a208", "a209", "a210", "a211", "a212", "a213", "a214", "a215", "a216", "a217", "a218", "a219", "a220", "a221", "a222", "a223", "a224", "a225", "a226", "a227", "a228", "a229", "a230", "a231", "a232", "a233", "a234", "a235", "a236", "a237", "a238", "a239", "a240", "a241", "a242", "a243", "a244", "a245", "a246", "a247", "a248", "a249", "a250", "a251", "a252", "a253", "a254", "a255");
}
__device__ __forceinline__ void bulk_block(const Params& p, rsrc_t r, unsigned char* lds, const int wv, const int j) {
    BSTAMP(0);
    proj_block(p, lds, wv, (j & 6) * 4 + (j >> 4), ((j >> 2) & 2) | (j & 1), j);
    BSTAMP(2);
    if (XG) { const int x = (j & 7) >> 1, tt = (j >> 3) * 2 + (j & 1); gate_block<4>(p, r, j, wv); conv_prep(p, r, wv, tt, x, x + 1); }
    BSTAMP(5);
    if (!conv_core(p, r, lds, wv, j)) return;
    BSTAMP(3);
    if (GDLY) wait_long(rtnow() + GDLY);
    out_block(p, lds, wv, ((j & 7) >> 1) + (j & 1) * 4, j >> 3, j);
    BSTAMP(7);
}

__device__ __forceinline__ void rec_block(const Params& p, rsrc_t r, unsigned char* lds, const int wv, const int b) {
    const int xq = (b & 7) >> 1, t = (b >> 3) * 2 + (b & 1);
    const unsigned lb = __builtin_amdgcn_readfirstlane((unsigned)(size_t)lds);
    int kind, id, rg = 0, rot = 0, bq = 99;
    if (RPL == 0) { kind = t < 8 ? 0 : t < 24 ? 1 : 2; id = t < 8 ? xq * 8 + t : t < 24 ? (xq & 1) * 16 + t - 8 : xq * 8 + t - 24; rg = xq >> 1; }
    else if (RPL == 4) { kind = t < 8 ? 0 : t < 24 ? 1 : 2; id = t < 8 ? xq * 8 + t : t < 24 ? xq * 8 + ((t - 8) >> 1) : xq * 8 + t - 24; rg = t & 1; }
    else if (RPL == 1) { kind = xq == 0 ? 0 : xq < 3 ? 1 : 2; id = t; rg = xq - 1; }
    else if (RPL == 2) { const int na = xq == 2 ? 10 : 11, q = xq * 21 + t - na; kind = xq == 3 ? 2 : t < na ? 0 : 1; id = xq == 3 ? t : t < na ? xq * 11 + t : q >> 1; rg = q & 1; }
    else { const int na = xq == 2 ? 12 : 10, q = t - na; kind = xq == 3 ? 2 : t < na ? 0 : 1; rg = xq < 2 ? xq : q & 1; bq = q;
        const int c2 = q >> 1, cs = xq < 2 ? (q < 6 ? q : q < 20 ? q + 2 : q + 4) : (c2 < 2 ? 6 + c2 : c2 < 4 ? 20 + c2 : 22 + c2);
        id = xq == 3 ? t : t < na ? xq * 10 + t : cs; rot = !ROT ? 0 : xq < 2 ? (cs & 1) * 4 : ((cs & 1) * 2 + rg) * 2; }
    if (kind == 0) rec_a(p, lb, wv, id, xq);
    else if (kind == 1) rec_b(p, lb, wv, id, rg, xq, rot, bq);
    else {
        const int hi = id, j = hi - 32; (void)j;
        BSTAMP(0);
        rec_helper(p, r, lds, wv, hi);
        BSTAMP(6);
        if (XG) return;
        if (XWAIT == 1) { if (!wait_word(r, F_GO + (hi >> 1) * 128, 1u, 1024u)) return; }
        if (XSTG) wait_long(rtnow() + (hi & 7) * XSTG);
        gate_block<16>(p, r, hi, wv);
        BSTAMP(2);
        if (XWAIT == 2) { if (!wait_word(r, F_GO + (hi >> 1) * 128, 1u, 1024u)) return; }
        conv_prep(p, r, wv, hi, 0, 4);
        BSTAMP(3);
    }
}

__global__ void __launch_bounds__(256, 1) k_main(Params p) {
    extern __shared__ __attribute__((aligned(16))) unsigned char lds[];
    rsrc_t r = __builtin_amdgcn_make_buffer_rsrc(p.ws, 0, W_TOTAL, 0x00020000);
    const int wv = __builtin_amdgcn_readfirstlane(threadIdx.x >> 6);
    int role = blockIdx.x;
    unsigned ctouch = 0u;
    if (CODE_T) {
        unsigned long long pc; int ea, eb, eg;
        asm volatile("s_getpc_b64 %0\n.Lk_begin:\n\ts_mov_b32 %1, .Lk_end_A-.Lk_begin\n\ts_mov_b32 %2, .Lk_end_B-.Lk_begin\n\ts_mov_b32 %3, .Lk_end_P-.Lk_begin" : "=s"(pc), "=s"(ea), "=s"(eb), "=s"(eg));
        const int o = ((blockIdx.x >> 3) * 64 + (threadIdx.x >> 2)) * 128;
        if ((threadIdx.x & 3) == 0 && o < max(ea, max(eb, eg)) - 4) asm volatile("global_load_dword %0, %1, off" : "+v"(ctouch) : "v"((const unsigned char*)pc + o) : "memory");
    }
    if (blockIdx.x == 0 && wv == 1) {
        const int l = threadIdx.x & 63; const v4u z = {0u, 0u, 0u, 0u};
#pragma unroll
        for (int i = 0; i < 6; ++i) st16(r, F_CONV + i * 1024 + l * 16, z);
        DRAIN();
        for (unsigned spin = 0; spin < (1u << 20); ++spin) {
            if (l < 8) __hip_atomic_fetch_add((unsigned*)(p.ws + F_BEAT + l * 128), 1u, __ATOMIC_RELAXED, __HIP_MEMORY_SCOPE_AGENT);
            __builtin_amdgcn_s_sleep(BEAT_SLP);
            unsigned c = ld4(r, l < 8 ? F_TKT + l * 64 : OOB);
            c += __shfl_xor(c, 1); c += __shfl_xor(c, 2); c += __shfl_xor(c, 4);
            if (__builtin_amdgcn_readfirstlane(c) >= 256u) break; }
    }
    if (threadIdx.x == 0) {
        const unsigned bo = F_BEAT + (blockIdx.x & 7) * 128, b0 = ld4(r, bo);
        for (unsigned spin = 0; spin < (1u << 22); ++spin) { MEMBAR(); if (ld4(r, bo) != b0) break; }
        MEMBAR();
        unsigned xcc; asm volatile("s_getreg_b32 %0, hwreg(HW_REG_XCC_ID)" : "=s"(xcc)); xcc &= 7u;
        unsigned* tk = (unsigned*)(p.ws + F_TKT);
        unsigned t = __hip_atomic_fetch_add(tk + xcc * 16, 1u, __ATOMIC_RELAXED, __HIP_MEMORY_SCOPE_AGENT);
        if (t >= 32u) {
            unsigned o = __hip_atomic_fetch_add(tk + 128, 1u, __ATOMIC_RELAXED, __HIP_MEMORY_SCOPE_AGENT);
            for (unsigned spin = 0; spin < (1u << 22); ++spin) { unsigned tot = 0u;
                for (int x = 0; x < 8; ++x) tot += __hip_atomic_load(tk + x * 16, __ATOMIC_RELAXED, __HIP_MEMORY_SCOPE_AGENT);
                if (tot >= 256u) break; }
            for (int x = 0; x < 8; ++x) { const unsigned cnt = __hip_atomic_load(tk + x * 16, __ATOMIC_RELAXED, __HIP_MEMORY_SCOPE_AGENT), fr = cnt < 32u ? 32u - cnt : 0u;
                if (o < fr) { xcc = (unsigned)x; t = cnt + o; break; }
                o -= fr; }
        }
        *(volatile int*)lds = (int)((xcc >> 2) * 128u + (t >> 1) * 8u + (xcc & 3u) * 2u + (t & 1u));
    }
    asm volatile("s_waitcnt vmcnt(0)" : "+v"(ctouch) :: "memory");
    __syncthreads();
    role = __builtin_amdgcn_readfirstlane(*(volatile int*)lds) & 255;
    __syncthreads();
    if (role >= 128) bulk_block(p, r, lds, wv, role - 128); else rec_block(p, r, lds, wv, role);
}

extern "C" void kernel_launch(void* const* d_in, const int* in_sizes, int n_in, void* d_out, int out_size, void* d_ws, size_t ws_size, hipStream_t stream) {
    Params p;
    p.Aq = (const bf16_t*)d_in[0]; p.Ak = (const bf16_t*)d_in[1]; p.Av = (const bf16_t*)d_in[2];
    p.shift_k = (const bf16_t*)d_in[3]; p.ssm_k = (const bf16_t*)d_in[4]; p.ssm_D = (const bf16_t*)d_in[5];
    p.mem0 = (const bf16_t*)d_in[7]; p.Wq = (const bf16_t*)d_in[8]; p.Wk = (const bf16_t*)d_in[9];
    p.Wv = (const bf16_t*)d_in[10]; p.Wo = (const bf16_t*)d_in[11]; p.gate_w = (const bf16_t*)d_in[13];
    p.out = (bf16_t*)d_out; p.ws = (unsigned char*)d_ws;
    (void)hipFuncSetAttribute((const void*)k_main, hipFuncAttributeMaxDynamicSharedMemorySize, L_TOTAL);
    k_main<<<256, 256, L_TOTAL, stream>>>(p);
}
```

```cpp
#include <hip/hip_runtime.h>
#include <stdint.h>
#include <type_traits>
typedef unsigned short bf16_t;
using bf16x8 = __attribute__((ext_vector_type(8))) short;
using f32x4 = __attribute__((ext_vector_type(4))) float;
typedef unsigned v4u __attribute__((ext_vector_type(4)));
typedef unsigned v2u __attribute__((ext_vector_type(2)));
typedef __amdgpu_buffer_rsrc_t rsrc_t;

__device__ __forceinline__ float bf2f(bf16_t b) { return __uint_as_float(((unsigned)b) << 16); }
typedef __bf16 hbf2 __attribute__((ext_vector_type(2)));
typedef float f32x2 __attribute__((ext_vector_type(2)));
__device__ __forceinline__ unsigned pack2(float a, float b) { f32x2 v = {a, b}; return __builtin_bit_cast(unsigned, __builtin_convertvector(v, hbf2)); }
__device__ __forceinline__ float rbfh(float f) { return __uint_as_float(pack2(f, 0.f) << 16); }
__device__ __forceinline__ unsigned nosent(unsigned x) {
    typedef unsigned short us2 __attribute__((ext_vector_type(2)));
    const us2 a = __builtin_bit_cast(us2, x), c = {(unsigned short)0xFFFE, (unsigned short)0xFFFE}; return __builtin_bit_cast(unsigned, __builtin_elementwise_min(a, c)); }

constexpr int S = 2048, D = 1024, H = 32, DH = 128, M = 8, SEG = 32, NSEG = 64;

constexpr unsigned W_FLG = 0;
constexpr unsigned F_KVQ = W_FLG;
constexpr unsigned F_CONV = W_FLG + 32768;
constexpr unsigned F_ERR = W_FLG + 32768 + 256;
constexpr unsigned F_PREP = W_FLG + 32768 + 512;
constexpr unsigned F_CAL = W_FLG + 33792;
constexpr unsigned F_TKT = W_FLG + 34816;
constexpr unsigned F_BEAT = W_FLG + 40960;
constexpr unsigned F_PROG = W_FLG + 49152;
constexpr unsigned F_CVU = W_FLG + 53248, F_CVY = W_FLG + 57344;
constexpr unsigned F_OP = W_FLG + 65536;
constexpr unsigned F_ATT = W_FLG + 131072;
constexpr unsigned F_RDY = W_FLG + 147456;
constexpr unsigned FLG_BYTES = 180224;
constexpr unsigned HO_KVM = W_FLG + FLG_BYTES;
constexpr unsigned W_A = HO_KVM + 64 * 32 * 4096;
constexpr unsigned W_GATE = W_A + S * D * 2;
constexpr unsigned W_QX = W_GATE + 8192;
constexpr unsigned W_KX = W_QX + S * 4096 * 2;
constexpr unsigned W_VF = W_KX + S * 4096 * 2;
constexpr unsigned W_OX = W_VF + 64 * 32 * 8192;
constexpr unsigned W_PART = W_OX + S * 4096 * 2;
constexpr unsigned W_Y = W_PART;
constexpr unsigned W_DBG = W_PART + 32 * 4 * 64 * 1024 * 4;
constexpr unsigned W_U = W_DBG + 256 * 64 * 16 * 8;
constexpr unsigned W_TOTAL = W_U + S * D * 2;

__device__ __forceinline__ v4u ld16(rsrc_t r, unsigned off) { return __builtin_amdgcn_raw_buffer_load_b128(r, off, 0, 16); }
__device__ __forceinline__ v2u ld8(rsrc_t r, unsigned off) { return __builtin_amdgcn_raw_buffer_load_b64(r, off, 0, 16); }
__device__ __forceinline__ unsigned ld4(rsrc_t r, unsigned off) { return __builtin_amdgcn_raw_buffer_load_b32(r, off, 0, 16); }
__device__ __forceinline__ void st16(rsrc_t r, unsigned off, v4u v) { __builtin_amdgcn_raw_buffer_store_b128(v, r, off, 0, 16); }
__device__ __forceinline__ void st8(rsrc_t r, unsigned off, v2u v) { __builtin_amdgcn_raw_buffer_store_b64(v, r, off, 0, 16); }
__device__ __forceinline__ void st4(rsrc_t r, unsigned off, unsigned v) { __builtin_amdgcn_raw_buffer_store_b32(v, r, off, 0, 16); }
__device__ __forceinline__ bool okdw(unsigned x) { return ((x & 0xFFFFu) != 0xFFFFu) & ((x >> 16) != 0xFFFFu); }
typedef unsigned short us2_t __attribute__((ext_vector_type(2)));
__device__ __forceinline__ unsigned pkmax(unsigned a, unsigned b) { return __builtin_bit_cast(unsigned, __builtin_elementwise_max(__builtin_bit_cast(us2_t, a), __builtin_bit_cast(us2_t, b))); }
__device__ __forceinline__ unsigned pkmax4(v4u v) { return pkmax(pkmax(v.x, v.y), pkmax(v.z, v.w)); }
__device__ __forceinline__ bool ok16(v4u v) { return okdw(pkmax4(v)); }
__device__ __forceinline__ bool ok8(v2u v) { return okdw(v.x) & okdw(v.y); }
__device__ __forceinline__ bf16x8 as_frag(v4u v) { return __builtin_bit_cast(bf16x8, v); }
#define MFMA(a, b, c) __builtin_amdgcn_mfma_f32_16x16x32_bf16((a), (b), (c), 0, 0, 0)
#define D1 130
#define D2 80
#define D3 76
#define LT_A 1
#define LB0 12
#define LC0 20
#define LA0 24
#define TR_UP 4
#define TR_DN 2
#define L_DEC 3
#define WARM1 0
#define KROT 0
#define STAG 1
#define UP_SH 2
#define EPI_SLP 0
#define SITA 1
#define PAIRX 4
#define DUE_P 1
#define LATE 1
#define EARLY_FROM 56
#define SKEW 0
#define OUT_WARM 1
#define ADAPT 1
#define RPL 0
#define CV_POL 2
#define POLL_SLP 4
#define TOUCH 1
#define ROT 0
#define CODE_T 1
#define CAL_T 450u
#define GTOUCH 0
#define XTOUCH 32
#define ST_P 32
#define CV_NAP 4000u
#define XSLP 2
#define XDLY 0
#define GDLY 0
#define YDLY 0
#define PROT 3
#define XSTG 0
#define XG 0
#define MS_BYTES 65536
#define BEAT_SLP 0
#define T2U 2
#define T2A 0
#define T2F 0
#define XWAIT 1
#define PEN 48
#define MEMBAR() asm volatile("" ::: "memory")
#define FT(i) do { } while (0)
#define ENDPGM() asm volatile("s_endpgm" ::: "memory")
#define DRAIN() asm volatile("s_waitcnt vmcnt(0)" ::: "memory")

__device__ __forceinline__ float xor16_max(float x) { auto r = __builtin_amdgcn_permlane16_swap(__float_as_uint(x), __float_as_uint(x), false, false); return fmaxf(__uint_as_float(r[0]), __uint_as_float(r[1])); }
__device__ __forceinline__ float xor32_max(float x) { auto r = __builtin_amdgcn_permlane32_swap(__float_as_uint(x), __float_as_uint(x), false, false); return fmaxf(__uint_as_float(r[0]), __uint_as_float(r[1])); }
__device__ __forceinline__ float xor16_add(float x) { auto r = __builtin_amdgcn_permlane16_swap(__float_as_uint(x), __float_as_uint(x), false, false); return __uint_as_float(r[0]) + __uint_as_float(r[1]); }
__device__ __forceinline__ float xor32_add(float x) { auto r = __builtin_amdgcn_permlane32_swap(__float_as_uint(x), __float_as_uint(x), false, false); return __uint_as_float(r[0]) + __uint_as_float(r[1]); }
__device__ __forceinline__ v2u tr_read(unsigned lds_addr) { v2u r; asm volatile("ds_read_b64_tr_b16 %0, %1\n\ts_waitcnt lgkmcnt(0)" : "=&v"(r) : "v"(lds_addr) : "memory"); return r; }

typedef short v4i16_t __attribute__((ext_vector_type(4)));
__device__ __forceinline__ v2u trb(unsigned lds_addr) { return __builtin_bit_cast(v2u, __builtin_amdgcn_ds_read_tr16_b64_v4i16((__attribute__((address_space(3))) v4i16_t*)(size_t)lds_addr)); }
__device__ __forceinline__ int otid_(int wv) { int t; asm volatile("v_mbcnt_lo_u32_b32 %0, -1, 0\n\tv_mbcnt_hi_u32_b32 %0, -1, %0" : "=v"(t)); return wv * 64 + t; }
#define otid() otid_(wv)
__device__ __forceinline__ bool wait_word(rsrc_t r, unsigned off, unsigned want, unsigned code) {
    for (unsigned spin = 0;; ++spin) { MEMBAR();
        const unsigned v = __builtin_amdgcn_readfirstlane(ld4(r, off));
        if (v >= want) return true;
        if (spin > (1u << 20)) { st4(r, F_ERR, code); return false; }
        __builtin_amdgcn_s_sleep(8); }
}

__device__ __forceinline__ unsigned rtnow() { return (unsigned)__builtin_amdgcn_s_memrealtime(); }
__device__ __forceinline__ void wait_until(unsigned target) { for (int i = 0; i < 4096; ++i) { if ((int)(rtnow() - target) >= 0) break; } }
__device__ __forceinline__ void wait_long(unsigned target) { for (int i = 0; i < 4096; ++i) { if ((int)(rtnow() - target) >= 0) break; __builtin_amdgcn_s_sleep(8); } }
constexpr int RED_G = 320;
constexpr int RED_T = 4 * RED_G;
constexpr int L_RED = 0;
constexpr int L_KX = 65536;
constexpr int KXP = 272;
constexpr int L_VF = L_KX + 2 * 32 * KXP;
constexpr int L_QKV = L_VF + 16384;
constexpr int L_OFF = L_QKV + 6144;
constexpr int L_STMP = L_OFF + 256;
constexpr int L_TOTAL = 163840;
constexpr unsigned U_A = 0, U_S = 32, U_B = 36, U_INIT = 113, U_C = 128, U_END = 512;
#define PLACE 1
#define CAL_N 5
#define STEP_MIN 190u

struct Params {
    const bf16_t *Aq, *Ak, *Av, *shift_k, *ssm_k, *ssm_D, *mem0, *Wq, *Wk, *Wv, *Wo, *gate_w;
    bf16_t* out; unsigned char* ws;
};
__host__ __device__ __forceinline__ unsigned hx_base(const void* ws) { const size_t a = (size_t)ws; return (unsigned)((((a + W_TOTAL + 0x7FFFFFu) >> 23) << 23) - a); }

#define STAMP(i) do { } while (0)
#define BSTAMP(i) do { } while (0)
#define EV(code) do { } while (0)
#define SPINS(i) do { } while (0)
typedef short v4s __attribute__((ext_vector_type(4)));
typedef unsigned v32u __attribute__((ext_vector_type(32)));
typedef unsigned v16u __attribute__((ext_vector_type(16)));
static_assert(L_VF - L_KX == 16384 + 1024, "fragment read offsets");
#define MFMA16(a, b, c) __builtin_amdgcn_mfma_f32_16x16x16bf16_1k(__builtin_bit_cast(v4s, (a)), __builtin_bit_cast(v4s, (b)), (c), 0, 0, 0)
__device__ __forceinline__ unsigned ror8(unsigned x) { return (unsigned)__builtin_amdgcn_update_dpp(0, (int)x, 0x128, 0xF, 0xF, false); }
__device__ __forceinline__ unsigned qx1(unsigned x) { return (unsigned)__builtin_amdgcn_update_dpp(0, (int)x, 0xB1, 0xF, 0xF, false); }
__device__ __forceinline__ unsigned wave_pkmax(unsigned d) {
    d = pkmax(d, (unsigned)__builtin_amdgcn_update_dpp((int)d, (int)d, 0xB1, 0xF, 0xF, false)); d = pkmax(d, (unsigned)__builtin_amdgcn_update_dpp((int)d, (int)d, 0x4E, 0xF, 0xF, false));
    d = pkmax(d, (unsigned)__builtin_amdgcn_update_dpp((int)d, (int)d, 0x124, 0xF, 0xF, false)); d = pkmax(d, (unsigned)__builtin_amdgcn_update_dpp((int)d, (int)d, 0x128, 0xF, 0xF, false));
    { auto q = __builtin_amdgcn_permlane16_swap(d, d, false, false); d = pkmax(q[0], q[1]); }
    { auto q = __builtin_amdgcn_permlane32_swap(d, d, false, false); d = pkmax(q[0], q[1]); }
    return d; }
__device__ __forceinline__ int wave_max(int d) {
    d = max(d, __builtin_amdgcn_update_dpp(d, d, 0xB1, 0xF, 0xF, false)); d = max(d, __builtin_amdgcn_update_dpp(d, d, 0x4E, 0xF, 0xF, false));
    d = max(d, __builtin_amdgcn_update_dpp(d, d, 0x124, 0xF, 0xF, false)); d = max(d, __builtin_amdgcn_update_dpp(d, d, 0x128, 0xF, 0xF, false));
    { auto q = __builtin_amdgcn_permlane16_swap((unsigned)d, (unsigned)d, false, false); d = max((int)q[0], (int)q[1]); }
    { auto q = __builtin_amdgcn_permlane32_swap((unsigned)d, (unsigned)d, false, false); d = max((int)q[0], (int)q[1]); }
    return d; }

__device__ __forceinline__ v4u mk_srd(const void* p, unsigned bytes) { const size_t a = (size_t)p; v4u d;
    d.x = __builtin_amdgcn_readfirstlane((unsigned)a); d.y = __builtin_amdgcn_readfirstlane((unsigned)(a >> 32) & 0xFFFFu); d.z = __builtin_amdgcn_readfirstlane(bytes); d.w = 0x00020000u; return d; }
__device__ __forceinline__ bf16x8 ldsa(unsigned addr) { return *(const __attribute__((address_space(3))) bf16x8*)(size_t)addr; }
constexpr unsigned HX_BYTES = 2097152, OOB = 0xF0000000u;
constexpr unsigned F_RDY0 = W_FLG + 34304;
constexpr unsigned F_GO = W_FLG + 36864;
constexpr int L_KV = 90112, L_MS = 139264, L_IMG = 155648, IMG_PS = 1056, IMG_MS = 2112, LB_R = 8192, LB_STG = 16384, LB_STGP = 8704;
static_assert(F_KVQ == 0 && F_ERR == 33024 && F_CONV == 32768 && F_CAL == 33792 && F_RDY0 == 34304 && F_GO == 36864 && F_PROG == 49152 && W_QX == 12771328 && W_KX == 29548544 && W_OX == 63102976 && W_VF == 46325760 && W_DBG == 113434624 && L_KV == 90112 && L_MS == 139264 && L_IMG == 155648 && IMG_PS == 1056 && IMG_MS == 2112 && LB_R == 8192 && LB_STG == 16384 && LB_STGP == 8704, "constants of the generated code");
#define REC_S_IN "{s[8:11]}"(sh), "{s[12:15]}"(sw), "{s[16:19]}"(s1), "{s[20:23]}"(s2), "{s29}"(D1), "{s30}"(D2), "{s43}"(w), "{s44}"(id), "{s49}"(rb), "{s50}"(PEN), "{s51}"(LT_A), "{s45}"(F_GO + (cp * 4 + w) * 128)
__device__ __forceinline__ void rec_a(const Params& p, unsigned lb, const int wv, const int id, const int cp) {
    const int w = wv, l = otid() & 63, n = l & 15, g = l >> 4, h = id, rb = id;
    const v4u sh = mk_srd(p.ws + hx_base(p.ws), HX_BYTES - 8192), sw = mk_srd(p.ws, W_TOTAL), s1 = mk_srd(w < 2 ? p.Wq : p.Wk, 1024 * 4096 * 2), s2 = s1;
    const int r8 = l >> 3, pc = l & 7, pp = n >> 3, bb = (n >> 2) & 1, f = (g & 1) | (pp << 1);
    const unsigned stg = lb + 131072 + w * 8192;
    const unsigned g_w = r8 * 8192 + h * 256 + (w & 1) * 128 + pc * 16;
    const unsigned wb0 = stg + r8 * 128 + (((pc >> 1) ^ (r8 >> 2)) << 5) + (pc & 1) * 16, wb1 = wb0 ^ 64u;
    const unsigned rb0 = stg + (16 * (g >> 1) + 8 * pp + 4 * (g & 1) + bb) * 128 + (f << 5) + (n & 3) * 8;
    const unsigned a_mem = cp * 32768 + (w >> 1) * 16384 + (w & 1) * 4096 + l * 16;
    const unsigned l_ms = lb + L_MS + l * 16;
    const unsigned l_iw = lb + L_IMG + (w >> 1) * IMG_MS + (g >> 1) * IMG_PS + (64 * (w & 1) + n) * 8 + (g & 1) * 4;
    const unsigned l_iq = lb + L_IMG + (n & 1) * IMG_PS + (8 * g + (n >> 2)) * 8;
    const unsigned a_vm = (16 + (h >> 2)) * 16384 + (h & 3) * 2048 + w * 512 + (l & 31) * 16;
    const unsigned a_o = n < 8 ? (8 + (n >> 2) * 4 + (h >> 3)) * 16384 + (h & 7) * 1024 + w * 256 + (n & 3) * 64 + g * 16 : OOB;
    const unsigned a_ox = (((w & 1) * 32 + h) * 4 + w) * 1024 + l * 16;
    asm volatile(
        "s_lshl_b32 s34, s43, 2\n\t"
        "s_add_u32 s34, s34, 0\n\t"
        "s_and_b32 s34, s34, 15\n\t"
        "s_lshl_b32 s34, s34, 19\n\t"
        "buffer_load_dwordx4 v[224:227], v96, s[16:19], s34 offen nt\n\t"
        "s_add_u32 s34, s34, 0x10000\n\t"
        "buffer_load_dwordx4 v[228:231], v96, s[16:19], s34 offen nt\n\t"
        "s_add_u32 s34, s34, 0x10000\n\t"
        "buffer_load_dwordx4 v[232:235], v96, s[16:19], s34 offen nt\n\t"
        "s_add_u32 s34, s34, 0x10000\n\t"
        "buffer_load_dwordx4 v[236:239], v96, s[16:19], s34 offen nt\n\t"
        "s_add_u32 s34, s34, 0x10000\n\t"
        "buffer_load_dwordx4 v[240:243], v96, s[16:19], s34 offen nt\n\t"
        "s_add_u32 s34, s34, 0x10000\n\t"
        "buffer_load_dwordx4 v[244:247], v96, s[16:19], s34 offen nt\n\t"
        "s_add_u32 s34, s34, 0x10000\n\t"
        "buffer_load_dwordx4 v[248:251], v96, s[16:19], s34 offen nt\n\t"
        "s_add_u32 s34, s34, 0x10000\n\t"
        "buffer_load_dwordx4 v[252:255], v96, s[16:19], s34 offen nt\n\t"
        "s_lshl_b32 s34, s43, 2\n\t"
        "s_add_u32 s34, s34, 1\n\t"
        "s_and_b32 s34, s34, 15\n\t"
        "s_lshl_b32 s34, s34, 19\n\t"
        "buffer_load_dwordx4 v[0:3], v96, s[16:19], s34 offen nt\n\t"
        "s_add_u32 s34, s34, 0x10000\n\t"
        "buffer_load_dwordx4 v[4:7], v96, s[16:19], s34 offen nt\n\t"
        "s_add_u32 s34, s34, 0x10000\n\t"
        "buffer_load_dwordx4 v[8:11], v96, s[16:19], s34 offen nt\n\t"
        "s_add_u32 s34, s34, 0x10000\n\t"
        "buffer_load_dwordx4 v[12:15], v96, s[16:19], s34 offen nt\n\t"
        "s_add_u32 s34, s34, 0x10000\n\t"
        "buffer_load_dwordx4 v[16:19], v96, s[16:19], s34 offen nt\n\t"
        "s_add_u32 s34, s34, 0x10000\n\t"
        "buffer_load_dwordx4 v[20:23], v96, s[16:19], s34 offen nt\n\t"
        "s_add_u32 s34, s34, 0x10000\n\t"
        "buffer_load_dwordx4 v[24:27], v96, s[16:19], s34 offen nt\n\t"
        "s_add_u32 s34, s34, 0x10000\n\t"
        "buffer_load_dwordx4 v[28:31], v96, s[16:19], s34 offen nt\n\t"
        "s_lshl_b32 s34, s43, 2\n\t"
        "s_add_u32 s34, s34, 2\n\t"
        "s_and_b32 s34, s34, 15\n\t"
        "s_lshl_b32 s34, s34, 19\n\t"
        "buffer_load_dwordx4 v[32:35], v96, s[16:19], s34 offen nt\n\t"
        "s_add_u32 s34, s34, 0x10000\n\t"
        "buffer_load_dwordx4 v[36:39], v96, s[16:19], s34 offen nt\n\t"
        "s_add_u32 s34, s34, 0x10000\n\t"
        "buffer_load_dwordx4 v[40:43], v96, s[16:19], s34 offen nt\n\t"
        "s_add_u32 s34, s34, 0x10000\n\t"
        "buffer_load_dwordx4 v[44:47], v96, s[16:19], s34 offen nt\n\t"
        "s_add_u32 s34, s34, 0x10000\n\t"
        "buffer_load_dwordx4 v[48:51], v96, s[16:19], s34 offen nt\n\t"
        "s_add_u32 s34, s34, 0x10000\n\t"
        "buffer_load_dwordx4 v[52:55], v96, s[16:19], s34 offen nt\n\t"
        "s_add_u32 s34, s34, 0x10000\n\t"
        "buffer_load_dwordx4 v[56:59], v96, s[16:19], s34 offen nt\n\t"
        "s_add_u32 s34, s34, 0x10000\n\t"
        "buffer_load_dwordx4 v[60:63], v96, s[16:19], s34 offen nt\n\t"
        "v_cmp_le_i32_e64 s[60:61], 0, v126\n\t"
        "v_cmp_le_i32_e64 s[62:63], 1, v126\n\t"
        "v_cmp_le_i32_e64 s[64:65], 2, v126\n\t"
        "v_cmp_le_i32_e64 s[66:67], 3, v126\n\t"
        "v_cmp_le_i32_e64 s[68:69], 16, v126\n\t"
        "v_cmp_le_i32_e64 s[70:71], 17, v126\n\t"
        "v_cmp_le_i32_e64 s[72:73], 18, v126\n\t"
        "v_cmp_le_i32_e64 s[74:75], 19, v126\n\t"
        "s_mul_i32 s35, s43, 10240\n\t"
        "v_add_u32_e32 v126, s35, v124\n\t"
        "s_waitcnt vmcnt(16)\n\t"
        "ds_write_b128 v97, v[224:227] offset:0\n\t"
        "ds_write_b128 v98, v[228:231] offset:1024\n\t"
        "ds_write_b128 v97, v[232:235] offset:2048\n\t"
        "ds_write_b128 v98, v[236:239] offset:3072\n\t"
        "ds_write_b128 v97, v[240:243] offset:4096\n\t"
        "ds_write_b128 v98, v[244:247] offset:5120\n\t"
        "ds_write_b128 v97, v[248:251] offset:6144\n\t"
        "ds_write_b128 v98, v[252:255] offset:7168\n\t"
        "ds_read_b64_tr_b16 a[0:1], v99 offset:0\n\t"
        "ds_read_b64_tr_b16 a[2:3], v99 offset:256\n\t"
        "ds_read_b64_tr_b16 a[4:5], v99 offset:4096\n\t"
        "ds_read_b64_tr_b16 a[6:7], v99 offset:4352\n\t"
        "s_waitcnt lgkmcnt(8)\n\t"
        "ds_read_b64_tr_b16 a[8:9], v100 offset:0\n\t"
        "ds_read_b64_tr_b16 a[10:11], v100 offset:256\n\t"
        "ds_read_b64_tr_b16 a[12:13], v100 offset:4096\n\t"
        "ds_read_b64_tr_b16 a[14:15], v100 offset:4352\n\t"
        "s_waitcnt lgkmcnt(8)\n\t"
        "ds_read_b64_tr_b16 a[16:17], v101 offset:0\n\t"
        "ds_read_b64_tr_b16 a[18:19], v101 offset:256\n\t"
        "ds_read_b64_tr_b16 a[20:21], v101 offset:4096\n\t"
        "ds_read_b64_tr_b16 a[22:23], v101 offset:4352\n\t"
        "s_waitcnt lgkmcnt(8)\n\t"
        "ds_read_b64_tr_b16 v[104:105], v102 offset:0\n\t"
        "ds_read_b64_tr_b16 v[106:107], v102 offset:256\n\t"
        "ds_read_b64_tr_b16 v[108:109], v102 offset:4096\n\t"
        "ds_read_b64_tr_b16 v[110:111], v102 offset:4352\n\t"
        "s_lshl_b32 s34, s43, 2\n\t"
        "s_add_u32 s34, s34, 3\n\t"
        "s_and_b32 s34, s34, 15\n\t"
        "s_lshl_b32 s34, s34, 19\n\t"
        "buffer_load_dwordx4 v[64:67], v96, s[16:19], s34 offen nt\n\t"
        "s_add_u32 s34, s34, 0x10000\n\t"
        "buffer_load_dwordx4 v[68:71], v96, s[16:19], s34 offen nt\n\t"
        "s_add_u32 s34, s34, 0x10000\n\t"
        "buffer_load_dwordx4 v[72:75], v96, s[16:19], s34 offen nt\n\t"
        "s_add_u32 s34, s34, 0x10000\n\t"
        "buffer_load_dwordx4 v[76:79], v96, s[16:19], s34 offen nt\n\t"
        "s_add_u32 s34, s34, 0x10000\n\t"
        "buffer_load_dwordx4 v[80:83], v96, s[16:19], s34 offen nt\n\t"
        "s_add_u32 s34, s34, 0x10000\n\t"
        "buffer_load_dwordx4 v[84:87], v96, s[16:19], s34 offen nt\n\t"
        "s_add_u32 s34, s34, 0x10000\n\t"
        "buffer_load_dwordx4 v[88:91], v96, s[16:19], s34 offen nt\n\t"
        "s_add_u32 s34, s34, 0x10000\n\t"
        "buffer_load_dwordx4 v[92:95], v96, s[16:19], s34 offen nt\n\t"
        "s_waitcnt lgkmcnt(0)\n\t"
        "ds_write_b128 v117, v[104:107] offset:0\n\t"
        "ds_write_b128 v117, v[108:111] offset:1024\n\t"
        "s_waitcnt vmcnt(16)\n\t"
        "ds_write_b128 v97, v[0:3] offset:0\n\t"
        "ds_write_b128 v98, v[4:7] offset:1024\n\t"
        "ds_write_b128 v97, v[8:11] offset:2048\n\t"
        "ds_write_b128 v98, v[12:15] offset:3072\n\t"
        "ds_write_b128 v97, v[16:19] offset:4096\n\t"
        "ds_write_b128 v98, v[20:23] offset:5120\n\t"
        "ds_write_b128 v97, v[24:27] offset:6144\n\t"
        "ds_write_b128 v98, v[28:31] offset:7168\n\t"
        "ds_read_b64_tr_b16 a[24:25], v99 offset:0\n\t"
        "ds_read_b64_tr_b16 a[26:27], v99 offset:256\n\t"
        "ds_read_b64_tr_b16 a[28:29], v99 offset:4096\n\t"
        "ds_read_b64_tr_b16 a[30:31], v99 offset:4352\n\t"
        "s_waitcnt lgkmcnt(8)\n\t"
        "ds_read_b64_tr_b16 a[32:33], v100 offset:0\n\t"
        "ds_read_b64_tr_b16 a[34:35], v100 offset:256\n\t"
        "ds_read_b64_tr_b16 a[36:37], v100 offset:4096\n\t"
        "ds_read_b64_tr_b16 a[38:39], v100 offset:4352\n\t"
        "s_waitcnt lgkmcnt(8)\n\t"
        "ds_read_b64_tr_b16 a[40:41], v101 offset:0\n\t"
        "ds_read_b64_tr_b16 a[42:43], v101 offset:256\n\t"
        "ds_read_b64_tr_b16 a[44:45], v101 offset:4096\n\t"
        "ds_read_b64_tr_b16 a[46:47], v101 offset:4352\n\t"
        "s_waitcnt lgkmcnt(8)\n\t"
        "ds_read_b64_tr_b16 v[104:105], v102 offset:0\n\t"
        "ds_read_b64_tr_b16 v[106:107], v102 offset:256\n\t"
        "ds_read_b64_tr_b16 v[108:109], v102 offset:4096\n\t"
        "ds_read_b64_tr_b16 v[110:111], v102 offset:4352\n\t"
        "s_lshl_b32 s34, s43, 2\n\t"
        "s_add_u32 s34, s34, 4\n\t"
        "s_and_b32 s34, s34, 15\n\t"
        "s_lshl_b32 s34, s34, 19\n\t"
        "buffer_load_dwordx4 a[96:99], v96, s[16:19], s34 offen nt\n\t"
        "s_add_u32 s34, s34, 0x10000\n\t"
        "buffer_load_dwordx4 a[100:103], v96, s[16:19], s34 offen nt\n\t"
        "s_add_u32 s34, s34, 0x10000\n\t"
        "buffer_load_dwordx4 a[104:107], v96, s[16:19], s34 offen nt\n\t"
        "s_add_u32 s34, s34, 0x10000\n\t"
        "buffer_load_dwordx4 a[108:111], v96, s[16:19], s34 offen nt\n\t"
        "s_add_u32 s34, s34, 0x10000\n\t"
        "buffer_load_dwordx4 a[112:115], v96, s[16:19], s34 offen nt\n\t"
        "s_add_u32 s34, s34, 0x10000\n\t"
        "buffer_load_dwordx4 a[116:119], v96, s[16:19], s34 offen nt\n\t"
        "s_add_u32 s34, s34, 0x10000\n\t"
        "buffer_load_dwordx4 a[120:123], v96, s[16:19], s34 offen nt\n\t"
        "s_add_u32 s34, s34, 0x10000\n\t"
        "buffer_load_dwordx4 a[124:127], v96, s[16:19], s34 offen nt\n\t"
        "s_waitcnt lgkmcnt(0)\n\t"
        "ds_write_b128 v117, v[104:107] offset:2048\n\t"
        "ds_write_b128 v117, v[108:111] offset:3072\n\t"
        "s_waitcnt vmcnt(16)\n\t"
        "ds_write_b128 v97, v[32:35] offset:0\n\t"
        "ds_write_b128 v98, v[36:39] offset:1024\n\t"
        "ds_write_b128 v97, v[40:43] offset:2048\n\t"
        "ds_write_b128 v98, v[44:47] offset:3072\n\t"
        "ds_write_b128 v97, v[48:51] offset:4096\n\t"
        "ds_write_b128 v98, v[52:55] offset:5120\n\t"
        "ds_write_b128 v97, v[56:59] offset:6144\n\t"
        "ds_write_b128 v98, v[60:63] offset:7168\n\t"
        "ds_read_b64_tr_b16 a[48:49], v99 offset:0\n\t"
        "ds_read_b64_tr_b16 a[50:51], v99 offset:256\n\t"
        "ds_read_b64_tr_b16 a[52:53], v99 offset:4096\n\t"
        "ds_read_b64_tr_b16 a[54:55], v99 offset:4352\n\t"
        "s_waitcnt lgkmcnt(8)\n\t"
        "ds_read_b64_tr_b16 a[56:57], v100 offset:0\n\t"
        "ds_read_b64_tr_b16 a[58:59], v100 offset:256\n\t"
        "ds_read_b64_tr_b16 a[60:61], v100 offset:4096\n\t"
        "ds_read_b64_tr_b16 a[62:63], v100 offset:4352\n\t"
        "s_waitcnt lgkmcnt(8)\n\t"
        "ds_read_b64_tr_b16 a[64:65], v101 offset:0\n\t"
        "ds_read_b64_tr_b16 a[66:67], v101 offset:256\n\t"
        "ds_read_b64_tr_b16 a[68:69], v101 offset:4096\n\t"
        "ds_read_b64_tr_b16 a[70:71], v101 offset:4352\n\t"
        "s_waitcnt lgkmcnt(8)\n\t"
        "ds_read_b64_tr_b16 v[104:105], v102 offset:0\n\t"
        "ds_read_b64_tr_b16 v[106:107], v102 offset:256\n\t"
        "ds_read_b64_tr_b16 v[108:109], v102 offset:4096\n\t"
        "ds_read_b64_tr_b16 v[110:111], v102 offset:4352\n\t"
        "s_lshl_b32 s34, s43, 2\n\t"
        "s_add_u32 s34, s34, 5\n\t"
        "s_and_b32 s34, s34, 15\n\t"
        "s_lshl_b32 s34, s34, 19\n\t"
        "buffer_load_dwordx4 a[128:131], v96, s[16:19], s34 offen nt\n\t"
        "s_add_u32 s34, s34, 0x10000\n\t"
        "buffer_load_dwordx4 a[132:135], v96, s[16:19], s34 offen nt\n\t"
        "s_add_u32 s34, s34, 0x10000\n\t"
        "buffer_load_dwordx4 a[136:139], v96, s[16:19], s34 offen nt\n\t"
        "s_add_u32 s34, s34, 0x10000\n\t"
        "buffer_load_dwordx4 a[140:143], v96, s[16:19], s34 offen nt\n\t"
        "s_add_u32 s34, s34, 0x10000\n\t"
        "buffer_load_dwordx4 a[144:147], v96, s[16:19], s34 offen nt\n\t"
        "s_add_u32 s34, s34, 0x10000\n\t"
        "buffer_load_dwordx4 a[148:151], v96, s[16:19], s34 offen nt\n\t"
        "s_add_u32 s34, s34, 0x10000\n\t"
        "buffer_load_dwordx4 a[152:155], v96, s[16:19], s34 offen nt\n\t"
        "s_add_u32 s34, s34, 0x10000\n\t"
        "buffer_load_dwordx4 a[156:159], v96, s[16:19], s34 offen nt\n\t"
        "s_waitcnt lgkmcnt(0)\n\t"
        "ds_write_b128 v117, v[104:107] offset:4096\n\t"
        "ds_write_b128 v117, v[108:111] offset:5120\n\t"
        "s_waitcnt vmcnt(16)\n\t"
        "ds_write_b128 v97, v[64:67] offset:0\n\t"
        "ds_write_b128 v98, v[68:71] offset:1024\n\t"
        "ds_write_b128 v97, v[72:75] offset:2048\n\t"
        "ds_write_b128 v98, v[76:79] offset:3072\n\t"
        "ds_write_b128 v97, v[80:83] offset:4096\n\t"
        "ds_write_b128 v98, v[84:87] offset:5120\n\t"
        "ds_write_b128 v97, v[88:91] offset:6144\n\t"
        "ds_write_b128 v98, v[92:95] offset:7168\n\t"
        "ds_read_b64_tr_b16 a[72:73], v99 offset:0\n\t"
        "ds_read_b64_tr_b16 a[74:75], v99 offset:256\n\t"
        "ds_read_b64_tr_b16 a[76:77], v99 offset:4096\n\t"
        "ds_read_b64_tr_b16 a[78:79], v99 offset:4352\n\t"
        "s_waitcnt lgkmcnt(8)\n\t"
        "ds_read_b64_tr_b16 a[80:81], v100 offset:0\n\t"
        "ds_read_b64_tr_b16 a[82:83], v100 offset:256\n\t"
        "ds_read_b64_tr_b16 a[84:85], v100 offset:4096\n\t"
        "ds_read_b64_tr_b16 a[86:87], v100 offset:4352\n\t"
        "s_waitcnt lgkmcnt(8)\n\t"
        "ds_read_b64_tr_b16 a[88:89], v101 offset:0\n\t"
        "ds_read_b64_tr_b16 a[90:91], v101 offset:256\n\t"
        "ds_read_b64_tr_b16 a[92:93], v101 offset:4096\n\t"
        "ds_read_b64_tr_b16 a[94:95], v101 offset:4352\n\t"
        "s_waitcnt lgkmcnt(8)\n\t"
        "ds_read_b64_tr_b16 v[104:105], v102 offset:0\n\t"
        "ds_read_b64_tr_b16 v[106:107], v102 offset:256\n\t"
        "ds_read_b64_tr_b16 v[108:109], v102 offset:4096\n\t"
        "ds_read_b64_tr_b16 v[110:111], v102 offset:4352\n\t"
        "s_lshl_b32 s34, s43, 2\n\t"
        "s_add_u32 s34, s34, 6\n\t"
        "s_and_b32 s34, s34, 15\n\t"
        "s_lshl_b32 s34, s34, 19\n\t"
        "buffer_load_dwordx4 a[160:163], v96, s[16:19], s34 offen nt\n\t"
        "s_add_u32 s34, s34, 0x10000\n\t"
        "buffer_load_dwordx4 a[164:167], v96, s[16:19], s34 offen nt\n\t"
        "s_add_u32 s34, s34, 0x10000\n\t"
        "buffer_load_dwordx4 a[168:171], v96, s[16:19], s34 offen nt\n\t"
        "s_add_u32 s34, s34, 0x10000\n\t"
        "buffer_load_dwordx4 a[172:175], v96, s[16:19], s34 offen nt\n\t"
        "s_add_u32 s34, s34, 0x10000\n\t"
        "buffer_load_dwordx4 a[176:179], v96, s[16:19], s34 offen nt\n\t"
        "s_add_u32 s34, s34, 0x10000\n\t"
        "buffer_load_dwordx4 a[180:183], v96, s[16:19], s34 offen nt\n\t"
        "s_add_u32 s34, s34, 0x10000\n\t"
        "buffer_load_dwordx4 a[184:187], v96, s[16:19], s34 offen nt\n\t"
        "s_add_u32 s34, s34, 0x10000\n\t"
        "buffer_load_dwordx4 a[188:191], v96, s[16:19], s34 offen nt\n\t"
        "s_waitcnt lgkmcnt(0)\n\t"
        "ds_write_b128 v117, v[104:107] offset:6144\n\t"
        "ds_write_b128 v117, v[108:111] offset:7168\n\t"
        "s_waitcnt vmcnt(16)\n\t"
        "ds_write_b128 v97, a[96:99] offset:0\n\t"
        "ds_write_b128 v98, a[100:103] offset:1024\n\t"
        "ds_write_b128 v97, a[104:107] offset:2048\n\t"
        "ds_write_b128 v98, a[108:111] offset:3072\n\t"
        "ds_write_b128 v97, a[112:115] offset:4096\n\t"
        "ds_write_b128 v98, a[116:119] offset:5120\n\t"
        "ds_write_b128 v97, a[120:123] offset:6144\n\t"
        "ds_write_b128 v98, a[124:127] offset:7168\n\t"
        "ds_read_b64_tr_b16 a[96:97], v99 offset:0\n\t"
        "ds_read_b64_tr_b16 a[98:99], v99 offset:256\n\t"
        "ds_read_b64_tr_b16 a[100:101], v99 offset:4096\n\t"
        "ds_read_b64_tr_b16 a[102:103], v99 offset:4352\n\t"
        "s_waitcnt lgkmcnt(8)\n\t"
        "ds_read_b64_tr_b16 a[104:105], v100 offset:0\n\t"
        "ds_read_b64_tr_b16 a[106:107], v100 offset:256\n\t"
        "ds_read_b64_tr_b16 a[108:109], v100 offset:4096\n\t"
        "ds_read_b64_tr_b16 a[110:111], v100 offset:4352\n\t"
        "s_waitcnt lgkmcnt(8)\n\t"
        "ds_read_b64_tr_b16 a[112:113], v101 offset:0\n\t"
        "ds_read_b64_tr_b16 a[114:115], v101 offset:256\n\t"
        "ds_read_b64_tr_b16 a[116:117], v101 offset:4096\n\t"
        "ds_read_b64_tr_b16 a[118:119], v101 offset:4352\n\t"
        "s_waitcnt lgkmcnt(8)\n\t"
        "ds_read_b64_tr_b16 v[104:105], v102 offset:0\n\t"
        "ds_read_b64_tr_b16 v[106:107], v102 offset:256\n\t"
        "ds_read_b64_tr_b16 v[108:109], v102 offset:4096\n\t"
        "ds_read_b64_tr_b16 v[110:111], v102 offset:4352\n\t"
        "s_lshl_b32 s34, s43, 2\n\t"
        "s_add_u32 s34, s34, 7\n\t"
        "s_and_b32 s34, s34, 15\n\t"
        "s_lshl_b32 s34, s34, 19\n\t"
        "buffer_load_dwordx4 a[192:195], v96, s[16:19], s34 offen nt\n\t"
        "s_add_u32 s34, s34, 0x10000\n\t"
        "buffer_load_dwordx4 a[196:199], v96, s[16:19], s34 offen nt\n\t"
        "s_add_u32 s34, s34, 0x10000\n\t"
        "buffer_load_dwordx4 a[200:203], v96, s[16:19], s34 offen nt\n\t"
        "s_add_u32 s34, s34, 0x10000\n\t"
        "buffer_load_dwordx4 a[204:207], v96, s[16:19], s34 offen nt\n\t"
        "s_add_u32 s34, s34, 0x10000\n\t"
        "buffer_load_dwordx4 a[208:211], v96, s[16:19], s34 offen nt\n\t"
        "s_add_u32 s34, s34, 0x10000\n\t"
        "buffer_load_dwordx4 a[212:215], v96, s[16:19], s34 offen nt\n\t"
        "s_add_u32 s34, s34, 0x10000\n\t"
        "buffer_load_dwordx4 a[216:219], v96, s[16:19], s34 offen nt\n\t"
        "s_add_u32 s34, s34, 0x10000\n\t"
        "buffer_load_dwordx4 a[220:223], v96, s[16:19], s34 offen nt\n\t"
        "s_waitcnt lgkmcnt(0)\n\t"
        "ds_write_b128 v126, v[104:107] offset:0\n\t"
        "ds_write_b128 v126, v[108:111] offset:1024\n\t"
        "s_waitcnt vmcnt(16)\n\t"
        "ds_write_b128 v97, a[128:131] offset:0\n\t"
        "ds_write_b128 v98, a[132:135] offset:1024\n\t"
        "ds_write_b128 v97, a[136:139] offset:2048\n\t"
        "ds_write_b128 v98, a[140:143] offset:3072\n\t"
        "ds_write_b128 v97, a[144:147] offset:4096\n\t"
        "ds_write_b128 v98, a[148:151] offset:5120\n\t"
        "ds_write_b128 v97, a[152:155] offset:6144\n\t"
        "ds_write_b128 v98, a[156:159] offset:7168\n\t"
        "ds_read_b64_tr_b16 a[120:121], v99 offset:0\n\t"
        "ds_read_b64_tr_b16 a[122:123], v99 offset:256\n\t"
        "ds_read_b64_tr_b16 a[124:125], v99 offset:4096\n\t"
        "ds_read_b64_tr_b16 a[126:127], v99 offset:4352\n\t"
        "s_waitcnt lgkmcnt(8)\n\t"
        "ds_read_b64_tr_b16 a[128:129], v100 offset:0\n\t"
        "ds_read_b64_tr_b16 a[130:131], v100 offset:256\n\t"
        "ds_read_b64_tr_b16 a[132:133], v100 offset:4096\n\t"
        "ds_read_b64_tr_b16 a[134:135], v100 offset:4352\n\t"
        "s_waitcnt lgkmcnt(8)\n\t"
        "ds_read_b64_tr_b16 a[136:137], v101 offset:0\n\t"
        "ds_read_b64_tr_b16 a[138:139], v101 offset:256\n\t"
        "ds_read_b64_tr_b16 a[140:141], v101 offset:4096\n\t"
        "ds_read_b64_tr_b16 a[142:143], v101 offset:4352\n\t"
        "s_waitcnt lgkmcnt(8)\n\t"
        "ds_read_b64_tr_b16 v[104:105], v102 offset:0\n\t"
        "ds_read_b64_tr_b16 v[106:107], v102 offset:256\n\t"
        "ds_read_b64_tr_b16 v[108:109], v102 offset:4096\n\t"
        "ds_read_b64_tr_b16 v[110:111], v102 offset:4352\n\t"
        "s_lshl_b32 s34, s43, 2\n\t"
        "s_add_u32 s34, s34, 8\n\t"
        "s_and_b32 s34, s34, 15\n\t"
        "s_lshl_b32 s34, s34, 19\n\t"
        "buffer_load_dwordx4 a[224:227], v96, s[16:19], s34 offen nt\n\t"
        "s_add_u32 s34, s34, 0x10000\n\t"
        "buffer_load_dwordx4 a[228:231], v96, s[16:19], s34 offen nt\n\t"
        "s_add_u32 s34, s34, 0x10000\n\t"
        "buffer_load_dwordx4 a[232:235], v96, s[16:19], s34 offen nt\n\t"
        "s_add_u32 s34, s34, 0x10000\n\t"
        "buffer_load_dwordx4 a[236:239], v96, s[16:19], s34 offen nt\n\t"
        "s_add_u32 s34, s34, 0x10000\n\t"
        "buffer_load_dwordx4 a[240:243], v96, s[16:19], s34 offen nt\n\t"
        "s_add_u32 s34, s34, 0x10000\n\t"
        "buffer_load_dwordx4 a[244:247], v96, s[16:19], s34 offen nt\n\t"
        "s_add_u32 s34, s34, 0x10000\n\t"
        "buffer_load_dwordx4 a[248:251], v96, s[16:19], s34 offen nt\n\t"
        "s_add_u32 s34, s34, 0x10000\n\t"
        "buffer_load_dwordx4 a[252:255], v96, s[16:19], s34 offen nt\n\t"
        "s_waitcnt lgkmcnt(0)\n\t"
        "ds_write_b128 v126, v[104:107] offset:2048\n\t"
        "ds_write_b128 v126, v[108:111] offset:3072\n\t"
        "s_waitcnt vmcnt(16)\n\t"
        "ds_write_b128 v97, a[160:163] offset:0\n\t"
        "ds_write_b128 v98, a[164:167] offset:1024\n\t"
        "ds_write_b128 v97, a[168:171] offset:2048\n\t"
        "ds_write_b128 v98, a[172:175] offset:3072\n\t"
        "ds_write_b128 v97, a[176:179] offset:4096\n\t"
        "ds_write_b128 v98, a[180:183] offset:5120\n\t"
        "ds_write_b128 v97, a[184:187] offset:6144\n\t"
        "ds_write_b128 v98, a[188:191] offset:7168\n\t"
        "ds_read_b64_tr_b16 a[144:145], v99 offset:0\n\t"
        "ds_read_b64_tr_b16 a[146:147], v99 offset:256\n\t"
        "ds_read_b64_tr_b16 a[148:149], v99 offset:4096\n\t"
        "ds_read_b64_tr_b16 a[150:151], v99 offset:4352\n\t"
        "s_waitcnt lgkmcnt(8)\n\t"
        "ds_read_b64_tr_b16 a[152:153], v100 offset:0\n\t"
        "ds_read_b64_tr_b16 a[154:155], v100 offset:256\n\t"
        "ds_read_b64_tr_b16 a[156:157], v100 offset:4096\n\t"
        "ds_read_b64_tr_b16 a[158:159], v100 offset:4352\n\t"
        "s_waitcnt lgkmcnt(8)\n\t"
        "ds_read_b64_tr_b16 a[160:161], v101 offset:0\n\t"
        "ds_read_b64_tr_b16 a[162:163], v101 offset:256\n\t"
        "ds_read_b64_tr_b16 a[164:165], v101 offset:4096\n\t"
        "ds_read_b64_tr_b16 a[166:167], v101 offset:4352\n\t"
        "s_waitcnt lgkmcnt(8)\n\t"
        "ds_read_b64_tr_b16 v[104:105], v102 offset:0\n\t"
        "ds_read_b64_tr_b16 v[106:107], v102 offset:256\n\t"
        "ds_read_b64_tr_b16 v[108:109], v102 offset:4096\n\t"
        "ds_read_b64_tr_b16 v[110:111], v102 offset:4352\n\t"
        "s_lshl_b32 s34, s43, 2\n\t"
        "s_add_u32 s34, s34, 9\n\t"
        "s_and_b32 s34, s34, 15\n\t"
        "s_lshl_b32 s34, s34, 19\n\t"
        "buffer_load_dwordx4 v[128:131], v96, s[16:19], s34 offen nt\n\t"
        "s_add_u32 s34, s34, 0x10000\n\t"
        "buffer_load_dwordx4 v[132:135], v96, s[16:19], s34 offen nt\n\t"
        "s_add_u32 s34, s34, 0x10000\n\t"
        "buffer_load_dwordx4 v[136:139], v96, s[16:19], s34 offen nt\n\t"
        "s_add_u32 s34, s34, 0x10000\n\t"
        "buffer_load_dwordx4 v[140:143], v96, s[16:19], s34 offen nt\n\t"
        "s_add_u32 s34, s34, 0x10000\n\t"
        "buffer_load_dwordx4 v[144:147], v96, s[16:19], s34 offen nt\n\t"
        "s_add_u32 s34, s34, 0x10000\n\t"
        "buffer_load_dwordx4 v[148:151], v96, s[16:19], s34 offen nt\n\t"
        "s_add_u32 s34, s34, 0x10000\n\t"
        "buffer_load_dwordx4 v[152:155], v96, s[16:19], s34 offen nt\n\t"
        "s_add_u32 s34, s34, 0x10000\n\t"
        "buffer_load_dwordx4 v[156:159], v96, s[16:19], s34 offen nt\n\t"
        "s_waitcnt lgkmcnt(0)\n\t"
        "ds_write_b128 v126, v[104:107] offset:4096\n\t"
        "ds_write_b128 v126, v[108:111] offset:5120\n\t"
        "s_waitcnt vmcnt(16)\n\t"
        "ds_write_b128 v97, a[192:195] offset:0\n\t"
        "ds_write_b128 v98, a[196:199] offset:1024\n\t"
        "ds_write_b128 v97, a[200:203] offset:2048\n\t"
        "ds_write_b128 v98, a[204:207] offset:3072\n\t"
        "ds_write_b128 v97, a[208:211] offset:4096\n\t"
        "ds_write_b128 v98, a[212:215] offset:5120\n\t"
        "ds_write_b128 v97, a[216:219] offset:6144\n\t"
        "ds_write_b128 v98, a[220:223] offset:7168\n\t"
        "ds_read_b64_tr_b16 a[168:169], v99 offset:0\n\t"
        "ds_read_b64_tr_b16 a[170:171], v99 offset:256\n\t"
        "ds_read_b64_tr_b16 a[172:173], v99 offset:4096\n\t"
        "ds_read_b64_tr_b16 a[174:175], v99 offset:4352\n\t"
        "s_waitcnt lgkmcnt(8)\n\t"
        "ds_read_b64_tr_b16 a[176:177], v100 offset:0\n\t"
        "ds_read_b64_tr_b16 a[178:179], v100 offset:256\n\t"
        "ds_read_b64_tr_b16 a[180:181], v100 offset:4096\n\t"
        "ds_read_b64_tr_b16 a[182:183], v100 offset:4352\n\t"
        "s_waitcnt lgkmcnt(8)\n\t"
        "ds_read_b64_tr_b16 a[184:185], v101 offset:0\n\t"
        "ds_read_b64_tr_b16 a[186:187], v101 offset:256\n\t"
        "ds_read_b64_tr_b16 a[188:189], v101 offset:4096\n\t"
        "ds_read_b64_tr_b16 a[190:191], v101 offset:4352\n\t"
        "s_waitcnt lgkmcnt(8)\n\t"
        "ds_read_b64_tr_b16 v[104:105], v102 offset:0\n\t"
        "ds_read_b64_tr_b16 v[106:107], v102 offset:256\n\t"
        "ds_read_b64_tr_b16 v[108:109], v102 offset:4096\n\t"
        "ds_read_b64_tr_b16 v[110:111], v102 offset:4352\n\t"
        "s_lshl_b32 s34, s43, 2\n\t"
        "s_add_u32 s34, s34, 10\n\t"
        "s_and_b32 s34, s34, 15\n\t"
        "s_lshl_b32 s34, s34, 19\n\t"
        "buffer_load_dwordx4 v[160:163], v96, s[16:19], s34 offen nt\n\t"
        "s_add_u32 s34, s34, 0x10000\n\t"
        "buffer_load_dwordx4 v[164:167], v96, s[16:19], s34 offen nt\n\t"
        "s_add_u32 s34, s34, 0x10000\n\t"
        "buffer_load_dwordx4 v[168:171], v96, s[16:19], s34 offen nt\n\t"
        "s_add_u32 s34, s34, 0x10000\n\t"
        "buffer_load_dwordx4 v[172:175], v96, s[16:19], s34 offen nt\n\t"
        "s_add_u32 s34, s34, 0x10000\n\t"
        "buffer_load_dwordx4 v[176:179], v96, s[16:19], s34 offen nt\n\t"
        "s_add_u32 s34, s34, 0x10000\n\t"
        "buffer_load_dwordx4 v[180:183], v96, s[16:19], s34 offen nt\n\t"
        "s_add_u32 s34, s34, 0x10000\n\t"
        "buffer_load_dwordx4 v[184:187], v96, s[16:19], s34 offen nt\n\t"
        "s_add_u32 s34, s34, 0x10000\n\t"
        "buffer_load_dwordx4 v[188:191], v96, s[16:19], s34 offen nt\n\t"
        "s_waitcnt lgkmcnt(0)\n\t"
        "ds_write_b128 v126, v[104:107] offset:6144\n\t"
        "ds_write_b128 v126, v[108:111] offset:7168\n\t"
        "s_waitcnt vmcnt(16)\n\t"
        "ds_write_b128 v97, a[224:227] offset:0\n\t"
        "ds_write_b128 v98, a[228:231] offset:1024\n\t"
        "ds_write_b128 v97, a[232:235] offset:2048\n\t"
        "ds_write_b128 v98, a[236:239] offset:3072\n\t"
        "ds_write_b128 v97, a[240:243] offset:4096\n\t"
        "ds_write_b128 v98, a[244:247] offset:5120\n\t"
        "ds_write_b128 v97, a[248:251] offset:6144\n\t"
        "ds_write_b128 v98, a[252:255] offset:7168\n\t"
        "ds_read_b64_tr_b16 a[192:193], v99 offset:0\n\t"
        "ds_read_b64_tr_b16 a[194:195], v99 offset:256\n\t"
        "ds_read_b64_tr_b16 a[196:197], v99 offset:4096\n\t"
        "ds_read_b64_tr_b16 a[198:199], v99 offset:4352\n\t"
        "s_waitcnt lgkmcnt(8)\n\t"
        "ds_read_b64_tr_b16 a[200:201], v100 offset:0\n\t"
        "ds_read_b64_tr_b16 a[202:203], v100 offset:256\n\t"
        "ds_read_b64_tr_b16 a[204:205], v100 offset:4096\n\t"
        "ds_read_b64_tr_b16 a[206:207], v100 offset:4352\n\t"
        "s_waitcnt lgkmcnt(8)\n\t"
        "ds_read_b64_tr_b16 a[208:209], v101 offset:0\n\t"
        "ds_read_b64_tr_b16 a[210:211], v101 offset:256\n\t"
        "ds_read_b64_tr_b16 a[212:213], v101 offset:4096\n\t"
        "ds_read_b64_tr_b16 a[214:215], v101 offset:4352\n\t"
        "s_waitcnt lgkmcnt(8)\n\t"
        "ds_read_b64_tr_b16 v[104:105], v102 offset:0\n\t"
        "ds_read_b64_tr_b16 v[106:107], v102 offset:256\n\t"
        "ds_read_b64_tr_b16 v[108:109], v102 offset:4096\n\t"
        "ds_read_b64_tr_b16 v[110:111], v102 offset:4352\n\t"
        "s_lshl_b32 s34, s43, 2\n\t"
        "s_add_u32 s34, s34, 11\n\t"
        "s_and_b32 s34, s34, 15\n\t"
        "s_lshl_b32 s34, s34, 19\n\t"
        "buffer_load_dwordx4 v[192:195], v96, s[16:19], s34 offen nt\n\t"
        "s_add_u32 s34, s34, 0x10000\n\t"
        "buffer_load_dwordx4 v[196:199], v96, s[16:19], s34 offen nt\n\t"
        "s_add_u32 s34, s34, 0x10000\n\t"
        "buffer_load_dwordx4 v[200:203], v96, s[16:19], s34 offen nt\n\t"
        "s_add_u32 s34, s34, 0x10000\n\t"
        "buffer_load_dwordx4 v[204:207], v96, s[16:19], s34 offen nt\n\t"
        "s_add_u32 s34, s34, 0x10000\n\t"
        "buffer_load_dwordx4 v[208:211], v96, s[16:19], s34 offen nt\n\t"
        "s_add_u32 s34, s34, 0x10000\n\t"
        "buffer_load_dwordx4 v[212:215], v96, s[16:19], s34 offen nt\n\t"
        "s_add_u32 s34, s34, 0x10000\n\t"
        "buffer_load_dwordx4 v[216:219], v96, s[16:19], s34 offen nt\n\t"
        "s_add_u32 s34, s34, 0x10000\n\t"
        "buffer_load_dwordx4 v[220:223], v96, s[16:19], s34 offen nt\n\t"
        "s_waitcnt lgkmcnt(0)\n\t"
        "ds_write_b128 v117, v[104:107] offset:8192\n\t"
        "ds_write_b128 v117, v[108:111] offset:9216\n\t"
        "s_waitcnt vmcnt(16)\n\t"
        "ds_write_b128 v97, v[128:131] offset:0\n\t"
        "ds_write_b128 v98, v[132:135] offset:1024\n\t"
        "ds_write_b128 v97, v[136:139] offset:2048\n\t"
        "ds_write_b128 v98, v[140:143] offset:3072\n\t"
        "ds_write_b128 v97, v[144:147] offset:4096\n\t"
        "ds_write_b128 v98, v[148:151] offset:5120\n\t"
        "ds_write_b128 v97, v[152:155] offset:6144\n\t"
        "ds_write_b128 v98, v[156:159] offset:7168\n\t"
        "ds_read_b64_tr_b16 a[216:217], v99 offset:0\n\t"
        "ds_read_b64_tr_b16 a[218:219], v99 offset:256\n\t"
        "ds_read_b64_tr_b16 a[220:221], v99 offset:4096\n\t"
        "ds_read_b64_tr_b16 a[222:223], v99 offset:4352\n\t"
        "s_waitcnt lgkmcnt(8)\n\t"
        "ds_read_b64_tr_b16 a[224:225], v100 offset:0\n\t"
        "ds_read_b64_tr_b16 a[226:227], v100 offset:256\n\t"
        "ds_read_b64_tr_b16 a[228:229], v100 offset:4096\n\t"
        "ds_read_b64_tr_b16 a[230:231], v100 offset:4352\n\t"
        "s_waitcnt lgkmcnt(8)\n\t"
        "ds_read_b64_tr_b16 a[232:233], v101 offset:0\n\t"
        "ds_read_b64_tr_b16 a[234:235], v101 offset:256\n\t"
        "ds_read_b64_tr_b16 a[236:237], v101 offset:4096\n\t"
        "ds_read_b64_tr_b16 a[238:239], v101 offset:4352\n\t"
        "s_waitcnt lgkmcnt(8)\n\t"
        "ds_read_b64_tr_b16 v[104:105], v102 offset:0\n\t"
        "ds_read_b64_tr_b16 v[106:107], v102 offset:256\n\t"
        "ds_read_b64_tr_b16 v[108:109], v102 offset:4096\n\t"
        "ds_read_b64_tr_b16 v[110:111], v102 offset:4352\n\t"
        "s_lshl_b32 s34, s43, 2\n\t"
        "s_add_u32 s34, s34, 12\n\t"
        "s_and_b32 s34, s34, 15\n\t"
        "s_lshl_b32 s34, s34, 19\n\t"
        "buffer_load_dwordx4 v[224:227], v96, s[16:19], s34 offen nt\n\t"
        "s_add_u32 s34, s34, 0x10000\n\t"
        "buffer_load_dwordx4 v[228:231], v96, s[16:19], s34 offen nt\n\t"
        "s_add_u32 s34, s34, 0x10000\n\t"
        "buffer_load_dwordx4 v[232:235], v96, s[16:19], s34 offen nt\n\t"
        "s_add_u32 s34, s34, 0x10000\n\t"
        "buffer_load_dwordx4 v[236:239], v96, s[16:19], s34 offen nt\n\t"
        "s_add_u32 s34, s34, 0x10000\n\t"
        "buffer_load_dwordx4 v[240:243], v96, s[16:19], s34 offen nt\n\t"
        "s_add_u32 s34, s34, 0x10000\n\t"
        "buffer_load_dwordx4 v[244:247], v96, s[16:19], s34 offen nt\n\t"
        "s_add_u32 s34, s34, 0x10000\n\t"
        "buffer_load_dwordx4 v[248:251], v96, s[16:19], s34 offen nt\n\t"
        "s_add_u32 s34, s34, 0x10000\n\t"
        "buffer_load_dwordx4 v[252:255], v96, s[16:19], s34 offen nt\n\t"
        "s_waitcnt lgkmcnt(0)\n\t"
        "ds_write_b128 v117, v[104:107] offset:10240\n\t"
        "ds_write_b128 v117, v[108:111] offset:11264\n\t"
        "s_waitcnt vmcnt(16)\n\t"
        "ds_write_b128 v97, v[160:163] offset:0\n\t"
        "ds_write_b128 v98, v[164:167] offset:1024\n\t"
        "ds_write_b128 v97, v[168:171] offset:2048\n\t"
        "ds_write_b128 v98, v[172:175] offset:3072\n\t"
        "ds_write_b128 v97, v[176:179] offset:4096\n\t"
        "ds_write_b128 v98, v[180:183] offset:5120\n\t"
        "ds_write_b128 v97, v[184:187] offset:6144\n\t"
        "ds_write_b128 v98, v[188:191] offset:7168\n\t"
        "ds_read_b64_tr_b16 a[240:241], v99 offset:0\n\t"
        "ds_read_b64_tr_b16 a[242:243], v99 offset:256\n\t"
        "ds_read_b64_tr_b16 a[244:245], v99 offset:4096\n\t"
        "ds_read_b64_tr_b16 a[246:247], v99 offset:4352\n\t"
        "s_waitcnt lgkmcnt(8)\n\t"
        "ds_read_b64_tr_b16 a[248:249], v100 offset:0\n\t"
        "ds_read_b64_tr_b16 a[250:251], v100 offset:256\n\t"
        "ds_read_b64_tr_b16 a[252:253], v100 offset:4096\n\t"
        "ds_read_b64_tr_b16 a[254:255], v100 offset:4352\n\t"
        "s_waitcnt lgkmcnt(8)\n\t"
        "ds_read_b64_tr_b16 v[128:129], v101 offset:0\n\t"
        "ds_read_b64_tr_b16 v[130:131], v101 offset:256\n\t"
        "ds_read_b64_tr_b16 v[132:133], v101 offset:4096\n\t"
        "ds_read_b64_tr_b16 v[134:135], v101 offset:4352\n\t"
        "s_waitcnt lgkmcnt(8)\n\t"
        "ds_read_b64_tr_b16 v[104:105], v102 offset:0\n\t"
        "ds_read_b64_tr_b16 v[106:107], v102 offset:256\n\t"
        "ds_read_b64_tr_b16 v[108:109], v102 offset:4096\n\t"
        "ds_read_b64_tr_b16 v[110:111], v102 offset:4352\n\t"
        "s_lshl_b32 s34, s43, 2\n\t"
        "s_add_u32 s34, s34, 13\n\t"
        "s_and_b32 s34, s34, 15\n\t"
        "s_lshl_b32 s34, s34, 19\n\t"
        "buffer_load_dwordx4 v[0:3], v96, s[16:19], s34 offen nt\n\t"
        "s_add_u32 s34, s34, 0x10000\n\t"
        "buffer_load_dwordx4 v[4:7], v96, s[16:19], s34 offen nt\n\t"
        "s_add_u32 s34, s34, 0x10000\n\t"
        "buffer_load_dwordx4 v[8:11], v96, s[16:19], s34 offen nt\n\t"
        "s_add_u32 s34, s34, 0x10000\n\t"
        "buffer_load_dwordx4 v[12:15], v96, s[16:19], s34 offen nt\n\t"
        "s_add_u32 s34, s34, 0x10000\n\t"
        "buffer_load_dwordx4 v[16:19], v96, s[16:19], s34 offen nt\n\t"
        "s_add_u32 s34, s34, 0x10000\n\t"
        "buffer_load_dwordx4 v[20:23], v96, s[16:19], s34 offen nt\n\t"
        "s_add_u32 s34, s34, 0x10000\n\t"
        "buffer_load_dwordx4 v[24:27], v96, s[16:19], s34 offen nt\n\t"
        "s_add_u32 s34, s34, 0x10000\n\t"
        "buffer_load_dwordx4 v[28:31], v96, s[16:19], s34 offen nt\n\t"
        "s_waitcnt lgkmcnt(0)\n\t"
        "ds_write_b128 v117, v[104:107] offset:12288\n\t"
        "ds_write_b128 v117, v[108:111] offset:13312\n\t"
        "s_waitcnt vmcnt(16)\n\t"
        "ds_write_b128 v97, v[192:195] offset:0\n\t"
        "ds_write_b128 v98, v[196:199] offset:1024\n\t"
        "ds_write_b128 v97, v[200:203] offset:2048\n\t"
        "ds_write_b128 v98, v[204:207] offset:3072\n\t"
        "ds_write_b128 v97, v[208:211] offset:4096\n\t"
        "ds_write_b128 v98, v[212:215] offset:5120\n\t"
        "ds_write_b128 v97, v[216:219] offset:6144\n\t"
        "ds_write_b128 v98, v[220:223] offset:7168\n\t"
        "ds_read_b64_tr_b16 v[136:137], v99 offset:0\n\t"
        "ds_read_b64_tr_b16 v[138:139], v99 offset:256\n\t"
        "ds_read_b64_tr_b16 v[140:141], v99 offset:4096\n\t"
        "ds_read_b64_tr_b16 v[142:143], v99 offset:4352\n\t"
        "s_waitcnt lgkmcnt(8)\n\t"
        "ds_read_b64_tr_b16 v[144:145], v100 offset:0\n\t"
        "ds_read_b64_tr_b16 v[146:147], v100 offset:256\n\t"
        "ds_read_b64_tr_b16 v[148:149], v100 offset:4096\n\t"
        "ds_read_b64_tr_b16 v[150:151], v100 offset:4352\n\t"
        "s_waitcnt lgkmcnt(8)\n\t"
        "ds_read_b64_tr_b16 v[152:153], v101 offset:0\n\t"
        "ds_read_b64_tr_b16 v[154:155], v101 offset:256\n\t"
        "ds_read_b64_tr_b16 v[156:157], v101 offset:4096\n\t"
        "ds_read_b64_tr_b16 v[158:159], v101 offset:4352\n\t"
        "s_waitcnt lgkmcnt(8)\n\t"
        "ds_read_b64_tr_b16 v[104:105], v102 offset:0\n\t"
        "ds_read_b64_tr_b16 v[106:107], v102 offset:256\n\t"
        "ds_read_b64_tr_b16 v[108:109], v102 offset:4096\n\t"
        "ds_read_b64_tr_b16 v[110:111], v102 offset:4352\n\t"
        "s_lshl_b32 s34, s43, 2\n\t"
        "s_add_u32 s34, s34, 14\n\t"
        "s_and_b32 s34, s34, 15\n\t"
        "s_lshl_b32 s34, s34, 19\n\t"
        "buffer_load_dwordx4 v[32:35], v96, s[16:19], s34 offen nt\n\t"
        "s_add_u32 s34, s34, 0x10000\n\t"
        "buffer_load_dwordx4 v[36:39], v96, s[16:19], s34 offen nt\n\t"
        "s_add_u32 s34, s34, 0x10000\n\t"
        "buffer_load_dwordx4 v[40:43], v96, s[16:19], s34 offen nt\n\t"
        "s_add_u32 s34, s34, 0x10000\n\t"
        "buffer_load_dwordx4 v[44:47], v96, s[16:19], s34 offen nt\n\t"
        "s_add_u32 s34, s34, 0x10000\n\t"
        "buffer_load_dwordx4 v[48:51], v96, s[16:19], s34 offen nt\n\t"
        "s_add_u32 s34, s34, 0x10000\n\t"
        "buffer_load_dwordx4 v[52:55], v96, s[16:19], s34 offen nt\n\t"
        "s_add_u32 s34, s34, 0x10000\n\t"
        "buffer_load_dwordx4 v[56:59], v96, s[16:19], s34 offen nt\n\t"
        "s_add_u32 s34, s34, 0x10000\n\t"
        "buffer_load_dwordx4 v[60:63], v96, s[16:19], s34 offen nt\n\t"
        "s_waitcnt lgkmcnt(0)\n\t"
        "ds_write_b128 v117, v[104:107] offset:14336\n\t"
        "ds_write_b128 v117, v[108:111] offset:15360\n\t"
        "s_waitcnt vmcnt(16)\n\t"
        "ds_write_b128 v97, v[224:227] offset:0\n\t"
        "ds_write_b128 v98, v[228:231] offset:1024\n\t"
        "ds_write_b128 v97, v[232:235] offset:2048\n\t"
        "ds_write_b128 v98, v[236:239] offset:3072\n\t"
        "ds_write_b128 v97, v[240:243] offset:4096\n\t"
        "ds_write_b128 v98, v[244:247] offset:5120\n\t"
        "ds_write_b128 v97, v[248:251] offset:6144\n\t"
        "ds_write_b128 v98, v[252:255] offset:7168\n\t"
        "ds_read_b64_tr_b16 v[160:161], v99 offset:0\n\t"
        "ds_read_b64_tr_b16 v[162:163], v99 offset:256\n\t"
        "ds_read_b64_tr_b16 v[164:165], v99 offset:4096\n\t"
        "ds_read_b64_tr_b16 v[166:167], v99 offset:4352\n\t"
        "s_waitcnt lgkmcnt(8)\n\t"
        "ds_read_b64_tr_b16 v[168:169], v100 offset:0\n\t"
        "ds_read_b64_tr_b16 v[170:171], v100 offset:256\n\t"
        "ds_read_b64_tr_b16 v[172:173], v100 offset:4096\n\t"
        "ds_read_b64_tr_b16 v[174:175], v100 offset:4352\n\t"
        "s_waitcnt lgkmcnt(8)\n\t"
        "ds_read_b64_tr_b16 v[176:177], v101 offset:0\n\t"
        "ds_read_b64_tr_b16 v[178:179], v101 offset:256\n\t"
        "ds_read_b64_tr_b16 v[180:181], v101 offset:4096\n\t"
        "ds_read_b64_tr_b16 v[182:183], v101 offset:4352\n\t"
        "s_waitcnt lgkmcnt(8)\n\t"
        "ds_read_b64_tr_b16 v[104:105], v102 offset:0\n\t"
        "ds_read_b64_tr_b16 v[106:107], v102 offset:256\n\t"
        "ds_read_b64_tr_b16 v[108:109], v102 offset:4096\n\t"
        "ds_read_b64_tr_b16 v[110:111], v102 offset:4352\n\t"
        "s_lshl_b32 s34, s43, 2\n\t"
        "s_add_u32 s34, s34, 15\n\t"
        "s_and_b32 s34, s34, 15\n\t"
        "s_lshl_b32 s34, s34, 19\n\t"
        "buffer_load_dwordx4 v[64:67], v96, s[16:19], s34 offen nt\n\t"
        "s_add_u32 s34, s34, 0x10000\n\t"
        "buffer_load_dwordx4 v[68:71], v96, s[16:19], s34 offen nt\n\t"
        "s_add_u32 s34, s34, 0x10000\n\t"
        "buffer_load_dwordx4 v[72:75], v96, s[16:19], s34 offen nt\n\t"
        "s_add_u32 s34, s34, 0x10000\n\t"
        "buffer_load_dwordx4 v[76:79], v96, s[16:19], s34 offen nt\n\t"
        "s_add_u32 s34, s34, 0x10000\n\t"
        "buffer_load_dwordx4 v[80:83], v96, s[16:19], s34 offen nt\n\t"
        "s_add_u32 s34, s34, 0x10000\n\t"
        "buffer_load_dwordx4 v[84:87], v96, s[16:19], s34 offen nt\n\t"
        "s_add_u32 s34, s34, 0x10000\n\t"
        "buffer_load_dwordx4 v[88:91], v96, s[16:19], s34 offen nt\n\t"
        "s_add_u32 s34, s34, 0x10000\n\t"
        "buffer_load_dwordx4 v[92:95], v96, s[16:19], s34 offen nt\n\t"
        "s_waitcnt lgkmcnt(0)\n\t"
        "ds_write_b128 v117, v[104:107] offset:16384\n\t"
        "ds_write_b128 v117, v[108:111] offset:17408\n\t"
        "s_waitcnt vmcnt(16)\n\t"
        "ds_write_b128 v97, v[0:3] offset:0\n\t"
        "ds_write_b128 v98, v[4:7] offset:1024\n\t"
        "ds_write_b128 v97, v[8:11] offset:2048\n\t"
        "ds_write_b128 v98, v[12:15] offset:3072\n\t"
        "ds_write_b128 v97, v[16:19] offset:4096\n\t"
        "ds_write_b128 v98, v[20:23] offset:5120\n\t"
        "ds_write_b128 v97, v[24:27] offset:6144\n\t"
        "ds_write_b128 v98, v[28:31] offset:7168\n\t"
        "ds_read_b64_tr_b16 v[184:185], v99 offset:0\n\t"
        "ds_read_b64_tr_b16 v[186:187], v99 offset:256\n\t"
        "ds_read_b64_tr_b16 v[188:189], v99 offset:4096\n\t"
        "ds_read_b64_tr_b16 v[190:191], v99 offset:4352\n\t"
        "s_waitcnt lgkmcnt(8)\n\t"
        "ds_read_b64_tr_b16 v[192:193], v100 offset:0\n\t"
        "ds_read_b64_tr_b16 v[194:195], v100 offset:256\n\t"
        "ds_read_b64_tr_b16 v[196:197], v100 offset:4096\n\t"
        "ds_read_b64_tr_b16 v[198:199], v100 offset:4352\n\t"
        "s_waitcnt lgkmcnt(8)\n\t"
        "ds_read_b64_tr_b16 v[200:201], v101 offset:0\n\t"
        "ds_read_b64_tr_b16 v[202:203], v101 offset:256\n\t"
        "ds_read_b64_tr_b16 v[204:205], v101 offset:4096\n\t"
        "ds_read_b64_tr_b16 v[206:207], v101 offset:4352\n\t"
        "s_waitcnt lgkmcnt(8)\n\t"
        "ds_read_b64_tr_b16 v[104:105], v102 offset:0\n\t"
        "ds_read_b64_tr_b16 v[106:107], v102 offset:256\n\t"
        "ds_read_b64_tr_b16 v[108:109], v102 offset:4096\n\t"
        "ds_read_b64_tr_b16 v[110:111], v102 offset:4352\n\t"
        "s_waitcnt lgkmcnt(0)\n\t"
        "ds_write_b128 v117, v[104:107] offset:18432\n\t"
        "ds_write_b128 v117, v[108:111] offset:19456\n\t"
        "s_waitcnt vmcnt(8)\n\t"
        "ds_write_b128 v97, v[32:35] offset:0\n\t"
        "ds_write_b128 v98, v[36:39] offset:1024\n\t"
        "ds_write_b128 v97, v[40:43] offset:2048\n\t"
        "ds_write_b128 v98, v[44:47] offset:3072\n\t"
        "ds_write_b128 v97, v[48:51] offset:4096\n\t"
        "ds_write_b128 v98, v[52:55] offset:5120\n\t"
        "ds_write_b128 v97, v[56:59] offset:6144\n\t"
        "ds_write_b128 v98, v[60:63] offset:7168\n\t"
        "ds_read_b64_tr_b16 v[208:209], v99 offset:0\n\t"
        "ds_read_b64_tr_b16 v[210:211], v99 offset:256\n\t"
        "ds_read_b64_tr_b16 v[212:213], v99 offset:4096\n\t"
        "ds_read_b64_tr_b16 v[214:215], v99 offset:4352\n\t"
        "s_waitcnt lgkmcnt(8)\n\t"
        "ds_read_b64_tr_b16 v[216:217], v100 offset:0\n\t"
        "ds_read_b64_tr_b16 v[218:219], v100 offset:256\n\t"
        "ds_read_b64_tr_b16 v[220:221], v100 offset:4096\n\t"
        "ds_read_b64_tr_b16 v[222:223], v100 offset:4352\n\t"
        "s_waitcnt lgkmcnt(8)\n\t"
        "ds_read_b64_tr_b16 v[224:225], v101 offset:0\n\t"
        "ds_read_b64_tr_b16 v[226:227], v101 offset:256\n\t"
        "ds_read_b64_tr_b16 v[228:229], v101 offset:4096\n\t"
        "ds_read_b64_tr_b16 v[230:231], v101 offset:4352\n\t"
        "s_waitcnt lgkmcnt(8)\n\t"
        "ds_read_b64_tr_b16 v[104:105], v102 offset:0\n\t"
        "ds_read_b64_tr_b16 v[106:107], v102 offset:256\n\t"
        "ds_read_b64_tr_b16 v[108:109], v102 offset:4096\n\t"
        "ds_read_b64_tr_b16 v[110:111], v102 offset:4352\n\t"
        "s_mov_b32 s35, 33792\n\t"
        "buffer_load_dword v0, off, s[12:15], s35 sc1\n\t"
        "s_waitcnt lgkmcnt(0)\n\t"
        "ds_write_b128 v117, v[104:107] offset:20480\n\t"
        "ds_write_b128 v117, v[108:111] offset:21504\n\t"
        "s_waitcnt vmcnt(0)\n\t"
        "ds_write_b128 v97, v[64:67] offset:0\n\t"
        "ds_write_b128 v98, v[68:71] offset:1024\n\t"
        "ds_write_b128 v97, v[72:75] offset:2048\n\t"
        "ds_write_b128 v98, v[76:79] offset:3072\n\t"
        "ds_write_b128 v97, v[80:83] offset:4096\n\t"
        "ds_write_b128 v98, v[84:87] offset:5120\n\t"
        "ds_write_b128 v97, v[88:91] offset:6144\n\t"
        "ds_write_b128 v98, v[92:95] offset:7168\n\t"
        "ds_read_b64_tr_b16 v[232:233], v99 offset:0\n\t"
        "ds_read_b64_tr_b16 v[234:235], v99 offset:256\n\t"
        "ds_read_b64_tr_b16 v[236:237], v99 offset:4096\n\t"
        "ds_read_b64_tr_b16 v[238:239], v99 offset:4352\n\t"
        "s_waitcnt lgkmcnt(8)\n\t"
        "ds_read_b64_tr_b16 v[240:241], v100 offset:0\n\t"
        "ds_read_b64_tr_b16 v[242:243], v100 offset:256\n\t"
        "ds_read_b64_tr_b16 v[244:245], v100 offset:4096\n\t"
        "ds_read_b64_tr_b16 v[246:247], v100 offset:4352\n\t"
        "s_waitcnt lgkmcnt(8)\n\t"
        "ds_read_b64_tr_b16 v[248:249], v101 offset:0\n\t"
        "ds_read_b64_tr_b16 v[250:251], v101 offset:256\n\t"
        "ds_read_b64_tr_b16 v[252:253], v101 offset:4096\n\t"
        "ds_read_b64_tr_b16 v[254:255], v101 offset:4352\n\t"
        "s_waitcnt lgkmcnt(8)\n\t"
        "ds_read_b64_tr_b16 v[104:105], v102 offset:0\n\t"
        "ds_read_b64_tr_b16 v[106:107], v102 offset:256\n\t"
        "ds_read_b64_tr_b16 v[108:109], v102 offset:4096\n\t"
        "ds_read_b64_tr_b16 v[110:111], v102 offset:4352\n\t"
        "s_mov_b32 s42, 0\n\t"
        "LA_cal%=:\n\t"
        "v_readfirstlane_b32 s34, v0\n\t"
        "s_nop 0\n\t"
        "s_cmp_lg_u32 s34, 0\n\t"
        "s_cbranch_scc1 LA_calok%=\n\t"
        "s_sleep 4\n\t"
        "s_mov_b32 s35, 33792\n\t"
        "buffer_load_dword v0, off, s[12:15], s35 sc1\n\t"
        "s_waitcnt vmcnt(0)\n\t"
        "s_add_u32 s42, s42, 1\n\t"
        "s_cmp_lt_u32 s42, 0x100000\n\t"
        "s_cbranch_scc1 LA_cal%=\n\t"
        "s_movk_i32 s34, 8\n\t"
        "s_branch LA_die%=\n\t"
        "LA_calok%=:\n\t"
        "s_lshl_b32 s35, s44, 10\n\t"
        "v_lshlrev_b32_e32 v1, 2, v122\n\t"
        "v_and_b32_e32 v1, 0x3c0, v1\n\t"
        "buffer_load_dword v1, v1, s[12:15], s35 offen offset:0 sc1\n\t"
        "s_waitcnt lgkmcnt(0)\n\t"
        "ds_write_b128 v126, v[104:107] offset:8192\n\t"
        "ds_write_b128 v126, v[108:111] offset:9216\n\t"
        "s_waitcnt lgkmcnt(0)\n\t"
        "v_mov_b32_e32 v110, v103\n\t"
        "ds_read_b128 v[64:67], v126 offset:0\n\t"
        "ds_read_b128 v[68:71], v126 offset:1024\n\t"
        "ds_read_b128 v[72:75], v126 offset:2048\n\t"
        "ds_read_b128 v[76:79], v126 offset:3072\n\t"
        "ds_read_b128 v[80:83], v126 offset:4096\n\t"
        "ds_read_b128 v[84:87], v126 offset:5120\n\t"
        "ds_read_b128 v[88:91], v126 offset:6144\n\t"
        "ds_read_b128 v[92:95], v126 offset:7168\n\t"
        "ds_read_b128 v[96:99], v126 offset:8192\n\t"
        "ds_read_b128 v[100:103], v126 offset:9216\n\t"
        "s_waitcnt lgkmcnt(0)\n\t"
        "v_mov_b32_e32 v126, 0\n\t"
        "s_barrier\n\t"
        "s_mov_b32 s48, 0\n\t"
        "s_mov_b32 s54, 0\n\t"
        "s_mov_b32 s0, 0x3f803f80\n\t"
        "s_mov_b32 s1, s0\n\t"
        "s_movk_i32 s56, 24576\n\t"
        "s_and_b32 s58, s43, 1\n\t"
        "s_lshl_b32 s58, s58, 12\n\t"
        "s_lshl_b32 s57, s44, 5\n\t"
        "s_add_u32 s57, s57, s43\n\t"
        "s_lshl_b32 s57, s57, 2\n\t"
        "s_add_u32 s57, s57, 458752\n\t"
        "s_waitcnt vmcnt(0)\n\t"
        "v_mov_b32_e32 v111, v1\n\t"
        "s_lshr_b32 s59, s48, 1\n\t"
        "s_cmp_gt_u32 s54, s59\n\t"
        "s_cbranch_scc1 LA_kv0ok%=\n\t"
        "s_mov_b32 s42, 0\n\t"
        "s_waitcnt vmcnt(0)\n\t"
        "s_branch LA_kv0w%=\n\t"
        "LA_kv0%=:\n\t"
        "s_waitcnt vmcnt(0)\n\t"
        "LA_kv0w%=:\n\t"
        "v_min_u32_dpp v111, v111, v111 quad_perm:[1,0,3,2] row_mask:0xf bank_mask:0xf\n\t"
        "s_nop 1\n\t"
        "v_min_u32_dpp v111, v111, v111 quad_perm:[2,3,0,1] row_mask:0xf bank_mask:0xf\n\t"
        "s_nop 1\n\t"
        "v_min_u32_dpp v111, v111, v111 row_ror:4 row_mask:0xf bank_mask:0xf\n\t"
        "s_nop 1\n\t"
        "v_min_u32_dpp v111, v111, v111 row_ror:8 row_mask:0xf bank_mask:0xf\n\t"
        "s_nop 0\n\t"
        "v_readfirstlane_b32 s54, v111\n\t"
        "s_nop 0\n\t"
        "s_cmp_gt_u32 s54, s59\n\t"
        "s_cbranch_scc1 LA_kv0ok%=\n\t"
        "s_sleep 4\n\t"
        "s_lshl_b32 s35, s44, 10\n\t"
        "v_lshlrev_b32_e32 v111, 2, v122\n\t"
        "v_and_b32_e32 v111, 0x3c0, v111\n\t"
        "buffer_load_dword v111, v111, s[12:15], s35 offen offset:0 sc1\n\t"
        "s_add_u32 s42, s42, 1\n\t"
        "s_cmp_lt_u32 s42, 0x100000\n\t"
        "s_cbranch_scc1 LA_kv0%=\n\t"
        "s_movk_i32 s34, 16\n\t"
        "s_branch LA_die%=\n\t"
        "LA_kv0ok%=:\n\t"
        "s_and_b32 s36, s48, 1\n\t"
        "s_mul_i32 s36, s36, 24576\n\t"
        "s_add_u32 s36, s36, s55\n\t"
        "s_lshl_b32 s37, s48, 18\n\t"
        "s_lshl_b32 s34, s43, 11\n\t"
        "s_add_u32 s37, s37, s34\n\t"
        "s_add_u32 m0, s36, 0\n\t"
        "s_add_u32 s34, s37, 29548544\n\t"
        "buffer_load_dwordx4 v122, s[12:15], s34 offen sc1 lds\n\t"
        "s_add_u32 m0, s36, 1024\n\t"
        "s_add_u32 s34, s37, 29549568\n\t"
        "buffer_load_dwordx4 v122, s[12:15], s34 offen sc1 lds\n\t"
        "s_add_u32 m0, s36, 8192\n\t"
        "s_add_u32 s34, s37, 12771328\n\t"
        "buffer_load_dwordx4 v122, s[12:15], s34 offen sc1 lds\n\t"
        "s_add_u32 m0, s36, 9216\n\t"
        "s_add_u32 s34, s37, 12772352\n\t"
        "buffer_load_dwordx4 v122, s[12:15], s34 offen sc1 lds\n\t"
        "s_add_u32 m0, s36, 16384\n\t"
        "s_add_u32 s34, s37, 46325760\n\t"
        "buffer_load_dwordx4 v122, s[12:15], s34 offen sc1 lds\n\t"
        "s_add_u32 m0, s36, 17408\n\t"
        "s_add_u32 s34, s37, 46326784\n\t"
        "buffer_load_dwordx4 v122, s[12:15], s34 offen sc1 lds\n\t"
        "v_readfirstlane_b32 s34, v0\n\t"
        "s_nop 0\n\t"
        "s_and_b32 s34, s34, 8192\n\t"
        "s_add_u32 s8, s8, s34\n\t"
        "s_addc_u32 s9, s9, 0\n\t"
        "s_barrier\n\t"
        "s_cmp_lg_u32 s43, 0\n\t"
        "s_cbranch_scc1 LA_st%=\n\t"
        "v_mov_b32_e32 v2, 1\n\t"
        "s_mov_b32 s35, 34304\n\t"
        "s_mov_b64 exec, 1\n\t"
        "buffer_atomic_add v2, off, s[12:15], s35 sc0\n\t"
        "s_mov_b64 exec, -1\n\t"
        "s_waitcnt vmcnt(0)\n\t"
        "v_readfirstlane_b32 s34, v2\n\t"
        "s_nop 0\n\t"
        "s_cmp_lg_u32 s34, 95\n\t"
        "s_cbranch_scc1 LA_st%=\n\t"
        "s_memrealtime s[32:33]\n\t"
        "v_mbcnt_lo_u32_b32 v3, -1, 0\n\t"
        "v_lshlrev_b32_e32 v3, 7, v3\n\t"
        "s_waitcnt lgkmcnt(0)\n\t"
        "s_add_u32 s34, s32, 50\n\t"
        "s_or_b32 s34, s34, 1\n\t"
        "v_mov_b32_e32 v2, s34\n\t"
        "s_mov_b32 s35, 36864\n\t"
        "s_mov_b64 exec, 0xffff\n\t"
        "buffer_store_dword v2, v3, s[12:15], s35 offen sc1\n\t"
        "s_mov_b64 exec, -1\n\t"
        "LA_st%=:\n\t"
        "s_mov_b32 s24, 0\n\t"
        "s_mov_b32 s26, 0\n\t"
        "s_mov_b32 s27, 0x80000\n\t"
        "s_mov_b32 s28, 0x180000\n\t"
        "s_mov_b32 s31, 0\n\t"
        "s_mov_b32 s47, 0\n\t"
        "s_mov_b32 s38, 0xff00ff00\n\t"
        "s_mov_b32 s39, 0xff00ff00\n\t"
        "s_mov_b32 s40, 0\n\t"
        "s_mov_b32 s41, -1\n\t"
        "ds_read_b128 v[32:35], v117 offset:0\n\t"
        "ds_read_b128 v[36:39], v117 offset:1024\n\t"
        "ds_read_b128 v[40:43], v117 offset:2048\n\t"
        "ds_read_b128 v[44:47], v117 offset:3072\n\t"
        "ds_read_b128 v[48:51], v117 offset:4096\n\t"
        "ds_read_b128 v[52:55], v117 offset:5120\n\t"
        "ds_read_b128 v[56:59], v117 offset:6144\n\t"
        "ds_read_b128 v[60:63], v117 offset:7168\n\t"
        "v_mov_b64_e32 v[0:1], 0\n\t"
        "v_mov_b64_e32 v[2:3], 0\n\t"
        "v_mov_b64_e32 v[4:5], 0\n\t"
        "v_mov_b64_e32 v[6:7], 0\n\t"
        "v_mov_b64_e32 v[8:9], 0\n\t"
        "v_mov_b64_e32 v[10:11], 0\n\t"
        "v_mov_b64_e32 v[12:13], 0\n\t"
        "v_mov_b64_e32 v[14:15], 0\n\t"
        "s_mov_b32 s42, 0\n\t"
        "LA_gw%=:\n\t"
        "buffer_load_dword v16, off, s[12:15], s45 sc1\n\t"
        "s_waitcnt vmcnt(0)\n\t"
        "v_readfirstlane_b32 s25, v16\n\t"
        "s_nop 0\n\t"
        "s_cmp_lg_u32 s25, 0\n\t"
        "s_cbranch_scc1 LA_loop%=\n\t"
        "s_sleep 2\n\t"
        "s_add_u32 s42, s42, 1\n\t"
        "s_cmp_lt_u32 s42, 0x100000\n\t"
        "s_cbranch_scc1 LA_gw%=\n\t"
        "s_movk_i32 s34, 9\n\t"
        "s_branch LA_die%=\n\t"
        ".p2align 8\n\t"
        "LA_loop%=:\n\t"
        "s_mov_b32 s42, 0\n\t"
        "s_sub_u32 s35, s25, 1\n\t"
        "LA_wa%=:\n\t"
        "s_memrealtime s[32:33]\n\t"
        "s_waitcnt lgkmcnt(0)\n\t"
        "s_sub_u32 s34, s35, s32\n\t"
        "s_cmpk_lt_u32 s34, 0x2000\n\t"
        "s_cbranch_scc1 LA_wa%=\n\t"
        "LA_rd%=:\n\t"
        "buffer_load_dwordx4 v[16:19], v112, s[8:11], s26 offen offset:0 sc1\n\t"
        "s_nop 12\n\t"
        "buffer_load_dwordx4 v[20:23], v112, s[8:11], s26 offen offset:1024 sc1\n\t"
        "s_nop 12\n\t"
        "buffer_load_dwordx4 v[24:27], v112, s[8:11], s26 offen offset:2048 sc1\n\t"
        "s_nop 12\n\t"
        "buffer_load_dwordx4 v[28:31], v112, s[8:11], s26 offen offset:3072 sc1\n\t"
        "LA_go%=:\n\t"
        "s_waitcnt vmcnt(3)\n\t"
        "v_smfmac_f32_16x16x64_bf16 v[0:3], v[16:19], a[0:7], v110\n\t"
        "ds_write_b128 v113, v[16:19] offset:0\n\t"
        "v_smfmac_f32_16x16x64_bf16 v[4:7], v[16:19], a[8:15], v110\n\t"
        "v_max3_u32 v111, v16, v17, v18\n\t"
        "v_max_u32_e32 v111, v19, v111\n\t"
        "v_smfmac_f32_16x16x64_bf16 v[8:11], v[16:19], a[16:23], v110\n\t"
        "v_smfmac_f32_16x16x64_bf16 v[12:15], v[16:19], v[32:39], v110\n\t"
        "s_waitcnt vmcnt(2)\n\t"
        "v_smfmac_f32_16x16x64_bf16 v[0:3], v[20:23], a[24:31], v110\n\t"
        "ds_write_b128 v113, v[20:23] offset:1024\n\t"
        "v_smfmac_f32_16x16x64_bf16 v[4:7], v[20:23], a[32:39], v110\n\t"
        "v_max3_u32 v111, v20, v21, v111\n\t"
        "v_max3_u32 v111, v22, v23, v111\n\t"
        "v_smfmac_f32_16x16x64_bf16 v[8:11], v[20:23], a[40:47], v110\n\t"
        "s_waitcnt vmcnt(1)\n\t"
        "ds_write_b128 v113, v[24:27] offset:2048\n\t"
        "v_smfmac_f32_16x16x64_bf16 v[12:15], v[20:23], v[40:47], v110\n\t"
        "v_max3_u32 v111, v24, v25, v111\n\t"
        "v_max3_u32 v111, v26, v27, v111\n\t"
        "v_smfmac_f32_16x16x64_bf16 v[0:3], v[24:27], a[48:55], v110\n\t"
        "v_smfmac_f32_16x16x64_bf16 v[4:7], v[24:27], a[56:63], v110\n\t"
        "s_waitcnt vmcnt(0)\n\t"
        "ds_write_b128 v113, v[28:31] offset:3072\n\t"
        "v_max3_u32 v111, v28, v29, v111\n\t"
        "v_max3_u32 v111, v30, v31, v111\n\t"
        "v_smfmac_f32_16x16x64_bf16 v[8:11], v[24:27], a[64:71], v110\n\t"
        "v_cmp_eq_u32_e32 vcc, -1, v111\n\t"
        "v_smfmac_f32_16x16x64_bf16 v[12:15], v[24:27], v[48:55], v110\n\t"
        "s_waitcnt lgkmcnt(0)\n\t"
        "s_cbranch_vccnz LA_rdfail%=\n\t"
        "s_barrier\n\t"
        "ds_read_b128 v[16:19], v114 offset:0\n\t"
        "ds_read_b128 v[20:23], v114 offset:1024\n\t"
        "ds_read_b128 v[24:27], v114 offset:2048\n\t"
        "v_smfmac_f32_16x16x64_bf16 v[0:3], v[28:31], a[72:79], v110\n\t"
        "v_smfmac_f32_16x16x64_bf16 v[4:7], v[28:31], a[80:87], v110\n\t"
        "v_smfmac_f32_16x16x64_bf16 v[8:11], v[28:31], a[88:95], v110\n\t"
        "v_smfmac_f32_16x16x64_bf16 v[12:15], v[28:31], v[56:63], v110\n\t"
        "ds_read_b128 v[28:31], v114 offset:3072\n\t"
        "s_waitcnt lgkmcnt(3)\n\t"
        "v_smfmac_f32_16x16x64_bf16 v[12:15], v[16:19], v[64:71], v110\n\t"
        "ds_read_b128 v[32:35], v117 offset:8192\n\t"
        "v_smfmac_f32_16x16x64_bf16 v[0:3], v[16:19], a[96:103], v110\n\t"
        "ds_read_b128 v[36:39], v117 offset:9216\n\t"
        "v_smfmac_f32_16x16x64_bf16 v[4:7], v[16:19], a[104:111], v110\n\t"
        "s_waitcnt lgkmcnt(4)\n\t"
        "v_smfmac_f32_16x16x64_bf16 v[8:11], v[16:19], a[112:119], v110\n\t"
        "ds_read_b128 v[16:19], v115 offset:0\n\t"
        "v_smfmac_f32_16x16x64_bf16 v[12:15], v[20:23], v[72:79], v110\n\t"
        "ds_read_b128 v[40:43], v117 offset:10240\n\t"
        "v_smfmac_f32_16x16x64_bf16 v[0:3], v[20:23], a[120:127], v110\n\t"
        "ds_read_b128 v[44:47], v117 offset:11264\n\t"
        "v_smfmac_f32_16x16x64_bf16 v[4:7], v[20:23], a[128:135], v110\n\t"
        "s_waitcnt lgkmcnt(6)\n\t"
        "v_smfmac_f32_16x16x64_bf16 v[8:11], v[20:23], a[136:143], v110\n\t"
        "ds_read_b128 v[20:23], v115 offset:1024\n\t"
        "v_smfmac_f32_16x16x64_bf16 v[12:15], v[24:27], v[80:87], v110\n\t"
        "ds_read_b128 v[48:51], v117 offset:12288\n\t"
        "v_smfmac_f32_16x16x64_bf16 v[0:3], v[24:27], a[144:151], v110\n\t"
        "ds_read_b128 v[52:55], v117 offset:13312\n\t"
        "v_smfmac_f32_16x16x64_bf16 v[4:7], v[24:27], a[152:159], v110\n\t"
        "s_waitcnt lgkmcnt(8)\n\t"
        "v_smfmac_f32_16x16x64_bf16 v[8:11], v[24:27], a[160:167], v110\n\t"
        "ds_read_b128 v[24:27], v115 offset:2048\n\t"
        "v_smfmac_f32_16x16x64_bf16 v[12:15], v[28:31], v[88:95], v110\n\t"
        "ds_read_b128 v[56:59], v117 offset:14336\n\t"
        "v_smfmac_f32_16x16x64_bf16 v[0:3], v[28:31], a[168:175], v110\n\t"
        "ds_read_b128 v[60:63], v117 offset:15360\n\t"
        "v_smfmac_f32_16x16x64_bf16 v[4:7], v[28:31], a[176:183], v110\n\t"
        "s_waitcnt lgkmcnt(8)\n\t"
        "v_smfmac_f32_16x16x64_bf16 v[8:11], v[28:31], a[184:191], v110\n\t"
        "ds_read_b128 v[28:31], v115 offset:3072\n\t"
        "v_smfmac_f32_16x16x64_bf16 v[12:15], v[16:19], v[32:39], v110\n\t"
        "ds_read_b128 v[32:35], v117 offset:16384\n\t"
        "v_smfmac_f32_16x16x64_bf16 v[0:3], v[16:19], a[192:199], v110\n\t"
        "ds_read_b128 v[36:39], v117 offset:17408\n\t"
        "v_smfmac_f32_16x16x64_bf16 v[4:7], v[16:19], a[200:207], v110\n\t"
        "s_waitcnt lgkmcnt(8)\n\t"
        "v_smfmac_f32_16x16x64_bf16 v[8:11], v[16:19], a[208:215], v110\n\t"
        "ds_read_b128 v[16:19], v116 offset:0\n\t"
        "v_smfmac_f32_16x16x64_bf16 v[12:15], v[20:23], v[40:47], v110\n\t"
        "ds_read_b128 v[40:43], v117 offset:18432\n\t"
        "v_smfmac_f32_16x16x64_bf16 v[0:3], v[20:23], a[216:223], v110\n\t"
        "ds_read_b128 v[44:47], v117 offset:19456\n\t"
        "v_smfmac_f32_16x16x64_bf16 v[4:7], v[20:23], a[224:231], v110\n\t"
        "s_waitcnt lgkmcnt(8)\n\t"
        "v_smfmac_f32_16x16x64_bf16 v[8:11], v[20:23], a[232:239], v110\n\t"
        "ds_read_b128 v[20:23], v116 offset:1024\n\t"
        "v_smfmac_f32_16x16x64_bf16 v[12:15], v[24:27], v[48:55], v110\n\t"
        "ds_read_b128 v[48:51], v117 offset:20480\n\t"
        "v_smfmac_f32_16x16x64_bf16 v[0:3], v[24:27], a[240:247], v110\n\t"
        "ds_read_b128 v[52:55], v117 offset:21504\n\t"
        "v_smfmac_f32_16x16x64_bf16 v[4:7], v[24:27], a[248:255], v110\n\t"
        "s_waitcnt lgkmcnt(8)\n\t"
        "v_smfmac_f32_16x16x64_bf16 v[8:11], v[24:27], v[128:135], v110\n\t"
        "ds_read_b128 v[24:27], v116 offset:2048\n\t"
        "v_smfmac_f32_16x16x64_bf16 v[12:15], v[28:31], v[56:63], v110\n\t"
        "ds_read_b128 v[56:59], v124 offset:3072\n\t"
        "v_smfmac_f32_16x16x64_bf16 v[0:3], v[28:31], v[136:143], v110\n\t"
        "ds_read_b128 v[60:63], v124 offset:7168\n\t"
        "v_smfmac_f32_16x16x64_bf16 v[4:7], v[28:31], v[144:151], v110\n\t"
        "s_waitcnt lgkmcnt(8)\n\t"
        "v_smfmac_f32_16x16x64_bf16 v[8:11], v[28:31], v[152:159], v110\n\t"
        "ds_read_b128 v[28:31], v116 offset:3072\n\t"
        "v_smfmac_f32_16x16x64_bf16 v[12:15], v[16:19], v[32:39], v110\n\t"
        "ds_read_b128 v[32:35], v124 offset:0\n\t"
        "v_smfmac_f32_16x16x64_bf16 v[0:3], v[16:19], v[160:167], v110\n\t"
        "ds_read_b128 v[36:39], v124 offset:4096\n\t"
        "v_smfmac_f32_16x16x64_bf16 v[4:7], v[16:19], v[168:175], v110\n\t"
        "s_waitcnt lgkmcnt(8)\n\t"
        "v_smfmac_f32_16x16x64_bf16 v[8:11], v[16:19], v[176:183], v110\n\t"
        "v_smfmac_f32_16x16x64_bf16 v[12:15], v[20:23], v[40:47], v110\n\t"
        "ds_read_b128 v[40:43], v124 offset:1024\n\t"
        "v_smfmac_f32_16x16x64_bf16 v[8:11], v[20:23], v[200:207], v110\n\t"
        "ds_read_b128 v[44:47], v124 offset:5120\n\t"
        "s_waitcnt lgkmcnt(7)\n\t"
        "v_smfmac_f32_16x16x64_bf16 v[12:15], v[24:27], v[48:55], v110\n\t"
        "ds_read_b128 v[48:51], v124 offset:2048\n\t"
        "v_smfmac_f32_16x16x64_bf16 v[8:11], v[24:27], v[224:231], v110\n\t"
        "ds_read_b128 v[52:55], v124 offset:6144\n\t"
        "s_waitcnt lgkmcnt(6)\n\t"
        "v_smfmac_f32_16x16x64_bf16 v[12:15], v[28:31], v[96:103], v110\n\t"
        "v_smfmac_f32_16x16x64_bf16 v[8:11], v[28:31], v[248:255], v110\n\t"
        "v_smfmac_f32_16x16x64_bf16 v[0:3], v[20:23], v[184:191], v110\n\t"
        "s_lshl_b32 s34, s43, 11\n\t"
        "v_add_u32_e32 v108, s34, v124\n\t"
        "v_smfmac_f32_16x16x64_bf16 v[0:3], v[24:27], v[208:215], v110\n\t"
        "v_add_u32_e32 v109, s58, v124\n\t"
        "v_smfmac_f32_16x16x64_bf16 v[4:7], v[20:23], v[192:199], v110\n\t"
        "v_smfmac_f32_16x16x64_bf16 v[0:3], v[28:31], v[232:239], v110\n\t"
        "v_smfmac_f32_16x16x64_bf16 v[4:7], v[24:27], v[216:223], v110\n\t"
        "buffer_load_dwordx4 v[104:107], v120, s[8:11], s26 offen sc1\n\t"
        "v_smfmac_f32_16x16x64_bf16 v[4:7], v[28:31], v[240:247], v110\n\t"
        "v_add_f32_e32 v12, v12, v13\n\t"
        "v_add_f32_e32 v14, v14, v15\n\t"
        "v_cvt_pk_bf16_f32 v12, v12, v14\n\t"
        "v_add_f32_e32 v8, v8, v9\n\t"
        "v_add_f32_e32 v10, v10, v11\n\t"
        "v_cvt_pk_bf16_f32 v8, v8, v10\n\t"
        "ds_write2_b32 v118, v8, v12 offset0:64 offset1:96\n\t"
        "v_add_f32_e32 v0, v0, v1\n\t"
        "v_add_f32_e32 v2, v2, v3\n\t"
        "v_cvt_pk_bf16_f32 v0, v0, v2\n\t"
        "v_add_f32_e32 v4, v4, v5\n\t"
        "v_add_f32_e32 v6, v6, v7\n\t"
        "v_cvt_pk_bf16_f32 v4, v4, v6\n\t"
        "ds_write2_b32 v118, v0, v4 offset0:0 offset1:32\n\t"
        "s_cmp_eq_u32 s24, 63\n\t"
        "s_waitcnt lgkmcnt(0)\n\t"
        "s_barrier\n\t"
        "s_cbranch_scc1 LA_skip%=\n\t"
        "ds_read_b64_tr_b16 v[16:17], v119 offset:0\n\t"
        "ds_read_b64_tr_b16 v[18:19], v119 offset:32\n\t"
        "ds_read_b64_tr_b16 v[20:21], v119 offset:256\n\t"
        "ds_read_b64_tr_b16 v[22:23], v119 offset:288\n\t"
        "ds_read_b64_tr_b16 v[24:25], v119 offset:512\n\t"
        "ds_read_b64_tr_b16 v[26:27], v119 offset:544\n\t"
        "ds_read_b64_tr_b16 v[28:29], v119 offset:768\n\t"
        "ds_read_b64_tr_b16 v[30:31], v119 offset:800\n\t"
        "buffer_store_dword v126, v127, s[12:15], 0 offen sc1\n\t"
        "s_waitcnt lgkmcnt(6)\n\t"
        "v_mfma_f32_16x16x32_bf16 v[0:3], v[32:35], v[16:19], 0\n\t"
        "ds_read_b64_tr_b16 v[32:33], v119 offset:2112\n\t"
        "ds_read_b64_tr_b16 v[34:35], v119 offset:2144\n\t"
        "v_mfma_f32_16x16x32_bf16 v[4:7], v[36:39], v[16:19], 0\n\t"
        "ds_read_b64_tr_b16 v[36:37], v119 offset:2368\n\t"
        "ds_read_b64_tr_b16 v[38:39], v119 offset:2400\n\t"
        "s_waitcnt lgkmcnt(8)\n\t"
        "v_mfma_f32_16x16x32_bf16 v[0:3], v[40:43], v[20:23], v[0:3]\n\t"
        "ds_read_b64_tr_b16 v[40:41], v119 offset:2624\n\t"
        "ds_read_b64_tr_b16 v[42:43], v119 offset:2656\n\t"
        "v_mfma_f32_16x16x32_bf16 v[4:7], v[44:47], v[20:23], v[4:7]\n\t"
        "ds_read_b64_tr_b16 v[44:45], v119 offset:2880\n\t"
        "ds_read_b64_tr_b16 v[46:47], v119 offset:2912\n\t"
        "s_waitcnt lgkmcnt(10)\n\t"
        "v_mfma_f32_16x16x32_bf16 v[0:3], v[48:51], v[24:27], v[0:3]\n\t"
        "ds_read_b128 v[48:51], v108 offset:16384\n\t"
        "v_mfma_f32_16x16x32_bf16 v[4:7], v[52:55], v[24:27], v[4:7]\n\t"
        "ds_read_b128 v[52:55], v108 offset:17408\n\t"
        "s_waitcnt lgkmcnt(10)\n\t"
        "v_mfma_f32_16x16x32_bf16 v[0:3], v[56:59], v[28:31], v[0:3]\n\t"
        "v_mfma_f32_16x16x32_bf16 v[4:7], v[60:63], v[28:31], v[4:7]\n\t"
        "s_waitcnt lgkmcnt(2)\n\t"
        "v_mfma_f32_16x16x32_bf16 v[8:11], v[32:35], v[16:19], 0\n\t"
        "s_nop 1\n\t"
        "v_mfma_f32_16x16x32_bf16 v[8:11], v[36:39], v[20:23], v[8:11]\n\t"
        "v_mov_b64_e32 v[16:17], s[0:1]\n\t"
        "v_mov_b64_e32 v[18:19], s[0:1]\n\t"
        "v_mfma_f32_16x16x32_bf16 v[8:11], v[40:43], v[24:27], v[8:11]\n\t"
        "v_max3_f32 v59, v0, v1, v2\n\t"
        "v_max3_f32 v59, v59, v3, v4\n\t"
        "v_mfma_f32_16x16x32_bf16 v[8:11], v[44:47], v[28:31], v[8:11]\n\t"
        "v_max3_f32 v59, v59, v5, v6\n\t"
        "v_max_f32_e32 v60, v59, v7\n\t"
        "v_max_f32_e32 v59, v59, v7\n\t"
        "v_cndmask_b32_e64 v0, v0, v4, s[38:39]\n\t"
        "v_cndmask_b32_e64 v1, v1, v5, s[38:39]\n\t"
        "v_permlane32_swap_b32_e32 v59, v60\n\t"
        "v_cndmask_b32_e64 v2, v2, v6, s[38:39]\n\t"
        "v_cndmask_b32_e64 v3, v3, v7, s[38:39]\n\t"
        "v_max3_f32 v59, v59, v60, v8\n\t"
        "v_max3_f32 v59, v59, v9, v10\n\t"
        "v_max_f32_e32 v60, v59, v11\n\t"
        "v_max_f32_e32 v59, v59, v11\n\t"
        "v_cndmask_b32_e64 v8, v8, v9, s[38:39]\n\t"
        "v_cndmask_b32_e64 v10, v10, v11, s[38:39]\n\t"
        "v_permlane16_swap_b32_e32 v59, v60\n\t"
        "v_max_f32_e32 v59, v59, v60\n\t"
        "v_mul_f32_e32 v59, 0xbe02940c, v59\n\t"
        "v_cndmask_b32_e64 v58, v8, v10, s[40:41]\n\t"
        "v_fmamk_f32 v0, v0, 0x3e02940c, v59\n\t"
        "v_exp_f32_e32 v0, v0\n\t"
        "v_fmamk_f32 v1, v1, 0x3e02940c, v59\n\t"
        "v_exp_f32_e32 v1, v1\n\t"
        "v_fmamk_f32 v2, v2, 0x3e02940c, v59\n\t"
        "v_exp_f32_e32 v2, v2\n\t"
        "v_fmamk_f32 v3, v3, 0x3e02940c, v59\n\t"
        "v_exp_f32_e32 v3, v3\n\t"
        "v_fmamk_f32 v58, v58, 0x3e02940c, v59\n\t"
        "v_cvt_pk_bf16_f32 v12, v0, v1\n\t"
        "v_cvt_pk_bf16_f32 v13, v2, v3\n\t"
        "v_exp_f32_e32 v58, v58\n\t"
        "s_waitcnt lgkmcnt(0)\n\t"
        "v_mov_b32_dpp v14, v12 row_ror:8 row_mask:0xf bank_mask:0xf\n\t"
        "v_mov_b32_dpp v15, v13 row_ror:8 row_mask:0xf bank_mask:0xf\n\t"
        "v_mov_b32_dpp v59, v58 row_ror:8 row_mask:0xf bank_mask:0xf\n\t"
        "v_cvt_pk_bf16_f32 v59, v58, v59\n\t"
        "v_mfma_f32_16x16x32_bf16 v[20:23], v[16:19], v[12:15], 0\n\t"
        "v_cndmask_b32_e64 v56, v59, 0, s[40:41]\n\t"
        "v_cndmask_b32_e64 v57, 0, v59, s[40:41]\n\t"
        "v_mfma_f32_16x16x32_bf16 v[4:7], v[48:51], v[12:15], 0\n\t"
        "s_waitcnt vmcnt(1)\n\t"
        "v_max3_u32 v111, v104, v105, v106\n\t"
        "v_mfma_f32_16x16x32_bf16 v[8:11], v[52:55], v[12:15], 0\n\t"
        "v_max_u32_e32 v111, v111, v107\n\t"
        "v_mfma_f32_16x16x16_bf16 v[20:23], v[16:17], v[56:57], v[20:23]\n\t"
        "v_cmp_eq_u32_e32 vcc, -1, v111\n\t"
        "v_mfma_f32_16x16x16_bf16 v[4:7], v[104:105], v[56:57], v[4:7]\n\t"
        "s_nop 1\n\t"
        "v_mfma_f32_16x16x16_bf16 v[8:11], v[106:107], v[56:57], v[8:11]\n\t"
        "s_cbranch_vccnz LA_vmfail%=\n\t"
        "LA_vmok%=:\n\t"
        "s_nop 1\n\t"
        "v_rcp_f32_e32 v59, v20\n\t"
        "s_nop 0\n\t"
        "v_pk_mul_f32 v[4:5], v[4:5], v[58:59] op_sel:[0,1] op_sel_hi:[1,1]\n\t"
        "v_cvt_pk_bf16_f32 v0, v4, v5\n\t"
        "v_pk_mul_f32 v[6:7], v[6:7], v[58:59] op_sel:[0,1] op_sel_hi:[1,1]\n\t"
        "v_cvt_pk_bf16_f32 v1, v6, v7\n\t"
        "v_pk_mul_f32 v[8:9], v[8:9], v[58:59] op_sel:[0,1] op_sel_hi:[1,1]\n\t"
        "v_cvt_pk_bf16_f32 v2, v8, v9\n\t"
        "v_pk_mul_f32 v[10:11], v[10:11], v[58:59] op_sel:[0,1] op_sel_hi:[1,1]\n\t"
        "v_cvt_pk_bf16_f32 v3, v10, v11\n\t"
        "buffer_store_dwordx4 v[0:3], v121, s[8:11], s26 offen sc1\n\t"
        "LA_x%=:\n\t"
        "s_memrealtime s[52:53]\n\t"
        "ds_read_b128 v[16:19], v109 offset:8192\n\t"
        "ds_read_b128 v[20:23], v109 offset:9216\n\t"
        "ds_read_b128 v[24:27], v109 offset:10240\n\t"
        "ds_read_b128 v[28:31], v109 offset:11264\n\t"
        "ds_read_b128 v[56:59], v124 offset:0\n\t"
        "ds_read_b128 v[60:63], v124 offset:4096\n\t"
        "ds_read_b128 v[12:15], v124 offset:1024\n\t"
        "v_xor_b32_e32 v126, 0x800, v108\n\t"
        "s_add_u32 s48, s24, 1\n\t"
        "s_min_u32 s48, s48, 63\n\t"
        "buffer_load_dwordx2 v[108:109], v125, s[8:11], s26 offen sc1\n\t"
        "s_lshl_b32 s35, s44, 10\n\t"
        "v_lshlrev_b32_e32 v111, 2, v122\n\t"
        "v_and_b32_e32 v111, 0x3c0, v111\n\t"
        "buffer_load_dword v111, v111, s[12:15], s35 offen offset:0 sc1\n\t"
        "s_waitcnt lgkmcnt(6)\n\t"
        "v_mfma_f32_16x16x32_bf16 v[8:11], v[32:35], v[16:19], 0\n\t"
        "ds_read_b128 v[32:35], v124 offset:5120\n\t"
        "s_waitcnt lgkmcnt(6)\n\t"
        "v_mfma_f32_16x16x32_bf16 v[8:11], v[36:39], v[20:23], v[8:11]\n\t"
        "ds_read_b128 v[36:39], v124 offset:2048\n\t"
        "s_waitcnt lgkmcnt(6)\n\t"
        "v_mfma_f32_16x16x32_bf16 v[8:11], v[40:43], v[24:27], v[8:11]\n\t"
        "ds_read_b128 v[40:43], v124 offset:6144\n\t"
        "s_waitcnt lgkmcnt(6)\n\t"
        "v_mfma_f32_16x16x32_bf16 v[8:11], v[44:47], v[28:31], v[8:11]\n\t"
        "ds_read_b128 v[44:47], v124 offset:3072\n\t"
        "s_add_u32 s35, s24, 1\n\t"
        "s_waitcnt lgkmcnt(6)\n\t"
        "v_mfma_f32_16x16x32_bf16 v[0:3], v[56:59], v[16:19], 0\n\t"
        "ds_read_b128 v[56:59], v124 offset:7168\n\t"
        "s_waitcnt lgkmcnt(6)\n\t"
        "v_mfma_f32_16x16x32_bf16 v[4:7], v[60:63], v[16:19], 0\n\t"
        "s_and_b32 s36, s35, 1\n\t"
        "s_mul_i32 s36, s36, 24576\n\t"
        "s_waitcnt lgkmcnt(5)\n\t"
        "v_mfma_f32_16x16x32_bf16 v[0:3], v[12:15], v[20:23], v[0:3]\n\t"
        "s_add_u32 s36, s36, s55\n\t"
        "s_lshl_b32 s37, s48, 18\n\t"
        "s_waitcnt lgkmcnt(4)\n\t"
        "v_mfma_f32_16x16x32_bf16 v[4:7], v[32:35], v[20:23], v[4:7]\n\t"
        "v_xor_b32_e32 v32, 0x200, v120\n\t"
        "buffer_load_dwordx4 v[12:15], v32, s[8:11], s26 offen sc1\n\t"
        "s_waitcnt lgkmcnt(3)\n\t"
        "v_mfma_f32_16x16x32_bf16 v[0:3], v[36:39], v[24:27], v[0:3]\n\t"
        "s_lshl_b32 s34, s43, 11\n\t"
        "s_add_u32 s37, s37, s34\n\t"
        "s_waitcnt lgkmcnt(2)\n\t"
        "v_mfma_f32_16x16x32_bf16 v[4:7], v[40:43], v[24:27], v[4:7]\n\t"
        "v_cvt_pk_bf16_f32 v8, 0, v8\n\t"
        "v_cvt_pk_bf16_f32 v9, 0, v9\n\t"
        "v_cvt_pk_bf16_f32 v10, 0, v10\n\t"
        "v_cvt_pk_bf16_f32 v11, 0, v11\n\t"
        "s_waitcnt lgkmcnt(1)\n\t"
        "v_mfma_f32_16x16x32_bf16 v[0:3], v[44:47], v[28:31], v[0:3]\n\t"
        "v_mul_f32_e32 v8, 0x3db504f3, v8\n\t"
        "v_mul_f32_e32 v9, 0x3db504f3, v9\n\t"
        "v_mul_f32_e32 v10, 0x3db504f3, v10\n\t"
        "v_mul_f32_e32 v11, 0x3db504f3, v11\n\t"
        "s_waitcnt lgkmcnt(0)\n\t"
        "v_mfma_f32_16x16x32_bf16 v[4:7], v[56:59], v[28:31], v[4:7]\n\t"
        "v_cvt_pk_bf16_f32 v8, 0, v8\n\t"
        "v_cvt_pk_bf16_f32 v9, 0, v9\n\t"
        "v_cvt_pk_bf16_f32 v10, 0, v10\n\t"
        "v_cvt_pk_bf16_f32 v11, 0, v11\n\t"
        "ds_read_b128 v[56:59], v126 offset:16384\n\t"
        "ds_read_b128 v[60:63], v126 offset:17408\n\t"
        "v_mov_b64_e32 v[36:37], -1\n\t"
        "v_mov_b64_e32 v[38:39], -1\n\t"
        "v_mov_b32_e32 v40, 0xce6e6b28\n\t"
        "v_mov_b32_e32 v46, 0x3db504f3\n\t"
        "v_mov_b32_e32 v47, 0x3fb8aa3b\n\t"
        "v_cvt_pk_bf16_f32 v0, 0, v0\n\t"
        "v_cvt_pk_bf16_f32 v1, 0, v1\n\t"
        "v_cvt_pk_bf16_f32 v2, 0, v2\n\t"
        "v_cvt_pk_bf16_f32 v3, 0, v3\n\t"
        "v_cvt_pk_bf16_f32 v4, 0, v4\n\t"
        "v_cvt_pk_bf16_f32 v5, 0, v5\n\t"
        "v_cvt_pk_bf16_f32 v6, 0, v6\n\t"
        "v_cvt_pk_bf16_f32 v7, 0, v7\n\t"
        "v_pk_mul_f32 v[0:1], v[0:1], v[46:47] op_sel_hi:[1,0]\n\t"
        "v_pk_mul_f32 v[2:3], v[2:3], v[46:47] op_sel_hi:[1,0]\n\t"
        "v_pk_mul_f32 v[4:5], v[4:5], v[46:47] op_sel_hi:[1,0]\n\t"
        "v_pk_mul_f32 v[6:7], v[6:7], v[46:47] op_sel_hi:[1,0]\n\t"
        "s_lshr_b32 s59, s48, 1\n\t"
        "s_cmp_gt_u32 s54, s59\n\t"
        "s_cbranch_scc1 LA_kvok%=\n\t"
        "s_mov_b32 s42, 0\n\t"
        "s_waitcnt vmcnt(1)\n\t"
        "s_branch LA_kvw%=\n\t"
        "LA_kv%=:\n\t"
        "s_waitcnt vmcnt(0)\n\t"
        "LA_kvw%=:\n\t"
        "v_min_u32_dpp v111, v111, v111 quad_perm:[1,0,3,2] row_mask:0xf bank_mask:0xf\n\t"
        "s_nop 1\n\t"
        "v_min_u32_dpp v111, v111, v111 quad_perm:[2,3,0,1] row_mask:0xf bank_mask:0xf\n\t"
        "s_nop 1\n\t"
        "v_min_u32_dpp v111, v111, v111 row_ror:4 row_mask:0xf bank_mask:0xf\n\t"
        "s_nop 1\n\t"
        "v_min_u32_dpp v111, v111, v111 row_ror:8 row_mask:0xf bank_mask:0xf\n\t"
        "s_nop 0\n\t"
        "v_readfirstlane_b32 s54, v111\n\t"
        "s_nop 0\n\t"
        "s_cmp_gt_u32 s54, s59\n\t"
        "s_cbranch_scc1 LA_kvok%=\n\t"
        "s_sleep 4\n\t"
        "s_lshl_b32 s35, s44, 10\n\t"
        "v_lshlrev_b32_e32 v111, 2, v122\n\t"
        "v_and_b32_e32 v111, 0x3c0, v111\n\t"
        "buffer_load_dword v111, v111, s[12:15], s35 offen offset:0 sc1\n\t"
        "s_add_u32 s42, s42, 1\n\t"
        "s_cmp_lt_u32 s42, 0x100000\n\t"
        "s_cbranch_scc1 LA_kv%=\n\t"
        "s_movk_i32 s34, 16\n\t"
        "s_branch LA_die%=\n\t"
        "LA_kvok%=:\n\t"
        "s_add_u32 m0, s36, 0\n\t"
        "s_add_u32 s34, s37, 29548544\n\t"
        "buffer_load_dwordx4 v122, s[12:15], s34 offen sc1 lds\n\t"
        "v_cvt_pk_bf16_f32 v0, 0, v0\n\t"
        "v_cvt_pk_bf16_f32 v1, 0, v1\n\t"
        "v_cvt_pk_bf16_f32 v2, 0, v2\n\t"
        "v_cvt_pk_bf16_f32 v3, 0, v3\n\t"
        "v_cvt_pk_bf16_f32 v4, 0, v4\n\t"
        "v_cvt_pk_bf16_f32 v5, 0, v5\n\t"
        "v_cvt_pk_bf16_f32 v6, 0, v6\n\t"
        "v_cvt_pk_bf16_f32 v7, 0, v7\n\t"
        "s_add_u32 m0, s36, 1024\n\t"
        "s_add_u32 s34, s37, 29549568\n\t"
        "buffer_load_dwordx4 v122, s[12:15], s34 offen sc1 lds\n\t"
        "v_cndmask_b32_e64 v0, v40, v0, s[60:61]\n\t"
        "v_cndmask_b32_e64 v1, v40, v1, s[62:63]\n\t"
        "v_cndmask_b32_e64 v2, v40, v2, s[64:65]\n\t"
        "v_cndmask_b32_e64 v3, v40, v3, s[66:67]\n\t"
        "v_cndmask_b32_e64 v4, v40, v4, s[68:69]\n\t"
        "v_cndmask_b32_e64 v5, v40, v5, s[70:71]\n\t"
        "v_cndmask_b32_e64 v6, v40, v6, s[72:73]\n\t"
        "v_cndmask_b32_e64 v7, v40, v7, s[74:75]\n\t"
        "s_add_u32 m0, s36, 8192\n\t"
        "s_add_u32 s34, s37, 12771328\n\t"
        "buffer_load_dwordx4 v122, s[12:15], s34 offen sc1 lds\n\t"
        "v_max3_f32 v42, v0, v1, v2\n\t"
        "v_max3_f32 v42, v42, v3, v4\n\t"
        "v_max3_f32 v42, v42, v5, v6\n\t"
        "v_max3_f32 v42, v42, v7, v8\n\t"
        "v_max3_f32 v42, v42, v9, v10\n\t"
        "v_mov_b32_e32 v43, v11\n\t"
        "v_max_f32_e32 v44, v42, v43\n\t"
        "v_max_f32_e32 v42, v42, v43\n\t"
        "s_pack_ll_b32_b16 s34, s52, s24\n\t"
        "v_mov_b32_e32 v40, s34\n\t"
        "s_add_u32 s35, s26, s57\n\t"
        "v_permlane16_swap_b32_e32 v42, v44\n\t"
        "v_max_f32_e32 v45, v42, v44\n\t"
        "v_max_f32_e32 v42, v42, v44\n\t"
        "s_mov_b64 exec, 1\n\t"
        "buffer_store_dword v40, off, s[8:11], s35 sc1\n\t"
        "s_mov_b64 exec, -1\n\t"
        "v_permlane32_swap_b32_e32 v42, v45\n\t"
        "v_max_f32_e32 v42, v42, v45\n\t"
        "v_mul_f32_e32 v42, 0xbfb8aa3b, v42\n\t"
        "v_pk_fma_f32 v[0:1], v[0:1], v[46:47], v[42:43] op_sel:[0,1,0] op_sel_hi:[1,1,0]\n\t"
        "s_add_u32 m0, s36, 9216\n\t"
        "s_add_u32 s34, s37, 12772352\n\t"
        "buffer_load_dwordx4 v122, s[12:15], s34 offen sc1 lds\n\t"
        "v_exp_f32_e32 v0, v0\n\t"
        "v_exp_f32_e32 v1, v1\n\t"
        "v_pk_fma_f32 v[2:3], v[2:3], v[46:47], v[42:43] op_sel:[0,1,0] op_sel_hi:[1,1,0]\n\t"
        "v_exp_f32_e32 v2, v2\n\t"
        "v_exp_f32_e32 v3, v3\n\t"
        "v_pk_fma_f32 v[4:5], v[4:5], v[46:47], v[42:43] op_sel:[0,1,0] op_sel_hi:[1,1,0]\n\t"
        "v_exp_f32_e32 v4, v4\n\t"
        "s_add_u32 m0, s36, 16384\n\t"
        "s_add_u32 s34, s37, 46325760\n\t"
        "buffer_load_dwordx4 v122, s[12:15], s34 offen sc1 lds\n\t"
        "v_exp_f32_e32 v5, v5\n\t"
        "v_pk_fma_f32 v[6:7], v[6:7], v[46:47], v[42:43] op_sel:[0,1,0] op_sel_hi:[1,1,0]\n\t"
        "v_exp_f32_e32 v6, v6\n\t"
        "v_exp_f32_e32 v7, v7\n\t"
        "v_pk_fma_f32 v[8:9], v[8:9], v[46:47], v[42:43] op_sel:[0,1,0] op_sel_hi:[1,1,0]\n\t"
        "v_exp_f32_e32 v8, v8\n\t"
        "v_exp_f32_e32 v9, v9\n\t"
        "s_add_u32 m0, s36, 17408\n\t"
        "s_add_u32 s34, s37, 46326784\n\t"
        "buffer_load_dwordx4 v122, s[12:15], s34 offen sc1 lds\n\t"
        "v_pk_fma_f32 v[10:11], v[10:11], v[46:47], v[42:43] op_sel:[0,1,0] op_sel_hi:[1,1,0]\n\t"
        "v_exp_f32_e32 v10, v10\n\t"
        "v_exp_f32_e32 v11, v11\n\t"
        "v_pk_add_f32 v[42:43], v[0:1], v[2:3]\n\t"
        "v_cndmask_b32_e64 v8, v8, 0, s[40:41]\n\t"
        "v_cndmask_b32_e64 v9, v9, 0, s[40:41]\n\t"
        "v_cndmask_b32_e64 v10, v10, 0, s[40:41]\n\t"
        "buffer_store_dwordx4 v[36:39], v121, s[8:11], s28 offen sc1\n\t"
        "v_cndmask_b32_e64 v11, v11, 0, s[40:41]\n\t"
        "v_pk_add_f32 v[42:43], v[42:43], v[4:5]\n\t"
        "v_pk_add_f32 v[42:43], v[42:43], v[6:7]\n\t"
        "v_pk_add_f32 v[42:43], v[42:43], v[8:9]\n\t"
        "v_pk_add_f32 v[42:43], v[42:43], v[10:11]\n\t"
        "v_add_f32_e32 v44, v42, v43\n\t"
        "v_add_f32_e32 v42, v42, v43\n\t"
        "s_lshl_b32 s34, s24, 18\n\t"
        "s_add_u32 s34, s34, 63102976\n\t"
        "v_permlane16_swap_b32_e32 v42, v44\n\t"
        "v_add_f32_e32 v45, v42, v44\n\t"
        "v_add_f32_e32 v42, v42, v44\n\t"
        "s_add_u32 s35, s24, 1\n\t"
        "v_mov_b32_e32 v126, s35\n\t"
        "v_permlane32_swap_b32_e32 v42, v45\n\t"
        "v_add_f32_e32 v42, v42, v45\n\t"
        "v_rcp_f32_e32 v42, v42\n\t"
        "s_waitcnt vmcnt(8) lgkmcnt(0)\n\t"
        "v_max3_u32 v45, v12, v13, v14\n\t"
        "v_max3_u32 v45, v45, v15, v104\n\t"
        "v_max3_u32 v45, v45, v105, v106\n\t"
        "v_max_u32_e32 v45, v45, v107\n\t"
        "v_cmp_eq_u32_e32 vcc, -1, v45\n\t"
        "s_nop 0\n\t"
        "s_cbranch_vccnz LA_v2fail%=\n\t"
        "LA_v2ok%=:\n\t"
        "v_and_b32_e32 v45, 0xff, v109\n\t"
        "v_cmp_ne_u32_e32 vcc, s24, v45\n\t"
        "s_nop 0\n\t"
        "s_cbranch_vccnz LA_stre%=\n\t"
        "LA_stok%=:\n\t"
        "v_pk_mul_f32 v[0:1], v[0:1], v[42:43] op_sel_hi:[1,0]\n\t"
        "v_subrev_u32_e32 v108, s25, v108\n\t"
        "v_min_u32_dpp v111, v111, v111 quad_perm:[1,0,3,2] row_mask:0xf bank_mask:0xf\n\t"
        "v_cvt_pk_bf16_f32 v32, v0, v1\n\t"
        "v_pk_mul_f32 v[2:3], v[2:3], v[42:43] op_sel_hi:[1,0]\n\t"
        "v_cvt_pk_bf16_f32 v33, v2, v3\n\t"
        "v_max_i32_dpp v108, v108, v108 quad_perm:[1,0,3,2] row_mask:0xf bank_mask:0xf\n\t"
        "v_min_u32_dpp v111, v111, v111 quad_perm:[2,3,0,1] row_mask:0xf bank_mask:0xf\n\t"
        "v_pk_mul_f32 v[4:5], v[4:5], v[42:43] op_sel_hi:[1,0]\n\t"
        "v_cvt_pk_bf16_f32 v34, v4, v5\n\t"
        "v_pk_mul_f32 v[6:7], v[6:7], v[42:43] op_sel_hi:[1,0]\n\t"
        "v_max_i32_dpp v108, v108, v108 quad_perm:[2,3,0,1] row_mask:0xf bank_mask:0xf\n\t"
        "v_min_u32_dpp v111, v111, v111 row_ror:4 row_mask:0xf bank_mask:0xf\n\t"
        "v_cvt_pk_bf16_f32 v35, v6, v7\n\t"
        "v_pk_mul_f32 v[8:9], v[8:9], v[42:43] op_sel_hi:[1,0]\n\t"
        "v_cvt_pk_bf16_f32 v36, v8, v9\n\t"
        "v_max_i32_dpp v108, v108, v108 row_ror:4 row_mask:0xf bank_mask:0xf\n\t"
        "v_min_u32_dpp v111, v111, v111 row_ror:8 row_mask:0xf bank_mask:0xf\n\t"
        "v_pk_mul_f32 v[10:11], v[10:11], v[42:43] op_sel_hi:[1,0]\n\t"
        "v_cvt_pk_bf16_f32 v37, v10, v11\n\t"
        "v_mfma_f32_16x16x32_bf16 v[16:19], v[48:51], v[32:35], 0\n\t"
        "v_max_i32_dpp v108, v108, v108 row_ror:8 row_mask:0xf bank_mask:0xf\n\t"
        "v_mfma_f32_16x16x32_bf16 v[20:23], v[52:55], v[32:35], 0\n\t"
        "v_mfma_f32_16x16x32_bf16 v[24:27], v[56:59], v[32:35], 0\n\t"
        "v_mfma_f32_16x16x32_bf16 v[28:31], v[60:63], v[32:35], 0\n\t"
        "v_max_i32_dpp v108, v108, v108 row_bcast:15 row_mask:0xa bank_mask:0xf\n\t"
        "v_xor_b32_e32 v45, 0x400, v123\n\t"
        "v_add_u32_e32 v124, s56, v124\n\t"
        "v_mfma_f32_16x16x16_bf16 v[16:19], v[104:105], v[36:37], v[16:19]\n\t"
        "v_max_i32_dpp v108, v108, v108 row_bcast:31 row_mask:0xc bank_mask:0xf\n\t"
        "v_mfma_f32_16x16x16_bf16 v[20:23], v[106:107], v[36:37], v[20:23]\n\t"
        "v_mfma_f32_16x16x16_bf16 v[24:27], v[12:13], v[36:37], v[24:27]\n\t"
        "v_mfma_f32_16x16x16_bf16 v[28:31], v[14:15], v[36:37], v[28:31]\n\t"
        "ds_read_b128 v[48:51], v117 offset:4096\n\t"
        "ds_read_b128 v[52:55], v117 offset:5120\n\t"
        "ds_read_b128 v[56:59], v117 offset:6144\n\t"
        "ds_read_b128 v[60:63], v117 offset:7168\n\t"
        "s_sub_u32 s56, 0, s56\n\t"
        "v_cvt_pk_bf16_f32 v0, v16, v17\n\t"
        "v_cvt_pk_bf16_f32 v1, v18, v19\n\t"
        "v_cvt_pk_bf16_f32 v2, v20, v21\n\t"
        "v_cvt_pk_bf16_f32 v3, v22, v23\n\t"
        "buffer_store_dwordx4 v[0:3], v123, s[12:15], s34 offen sc1\n\t"
        "v_cvt_pk_bf16_f32 v4, v24, v25\n\t"
        "v_cvt_pk_bf16_f32 v5, v26, v27\n\t"
        "v_cvt_pk_bf16_f32 v6, v28, v29\n\t"
        "v_cvt_pk_bf16_f32 v7, v30, v31\n\t"
        "buffer_store_dwordx4 v[4:7], v45, s[12:15], s34 offen sc1\n\t"
        "s_cmp_eq_u32 s24, 63\n\t"
        "s_cbranch_scc1 LA_last%=\n\t"
        "ds_read_b128 v[32:35], v117 offset:0\n\t"
        "ds_read_b128 v[36:39], v117 offset:1024\n\t"
        "ds_read_b128 v[40:43], v117 offset:2048\n\t"
        "ds_read_b128 v[44:47], v117 offset:3072\n\t"
        "v_mov_b64_e32 v[14:15], 0\n\t"
        "v_mov_b64_e32 v[12:13], 0\n\t"
        "v_mov_b64_e32 v[10:11], 0\n\t"
        "v_mov_b64_e32 v[8:9], 0\n\t"
        "v_mov_b64_e32 v[6:7], 0\n\t"
        "v_mov_b64_e32 v[4:5], 0\n\t"
        "v_mov_b64_e32 v[2:3], 0\n\t"
        "v_mov_b64_e32 v[0:1], 0\n\t"
        "v_readfirstlane_b32 s54, v111\n\t"
        "v_readfirstlane_b32 s35, v109\n\t"
        "v_readlane_b32 s36, v108, 63\n\t"
        "s_bfe_u32 s29, s35, 0xc0008\n\t"
        "s_lshr_b32 s30, s35, 20\n\t"
        "s_add_u32 s35, s31, s50\n\t"
        "s_mul_i32 s37, s50, 3\n\t"
        "s_min_u32 s35, s35, s37\n\t"
        "s_add_i32 s34, s36, s51\n\t"
        "s_cmp_gt_i32 s34, 0\n\t"
        "s_cselect_b32 s31, s35, 0\n\t"
        "s_cselect_b32 s34, s34, 0\n\t"
        "s_add_u32 s34, s34, s31\n\t"
        "s_add_u32 s37, s25, s34\n\t"
        "s_add_u32 s45, s37, s29\n\t"
        "s_add_u32 s25, s45, s30\n\t"
        "s_add_u32 s24, s24, 1\n\t"
        "s_mov_b32 s28, s26\n\t"
        "s_mov_b32 s26, s27\n\t"
        "s_add_u32 s27, s27, 0x80000\n\t"
        "s_and_b32 s27, s27, 0x1fffff\n\t"
        "s_branch LA_loop%=\n\t"
        "LA_last%=:\n\t"
        "s_waitcnt vmcnt(0)\n\t"
        "s_barrier\n\t"
        "buffer_store_dword v126, v127, s[12:15], 0 offen sc1\n\t"
        "s_endpgm\n\t"
        "LA_skip%=:\n\t"
        "buffer_store_dword v126, v127, s[12:15], 0 offen sc1\n\t"
        "ds_read_b64_tr_b16 v[32:33], v119 offset:2112\n\t"
        "ds_read_b64_tr_b16 v[34:35], v119 offset:2144\n\t"
        "ds_read_b64_tr_b16 v[36:37], v119 offset:2368\n\t"
        "ds_read_b64_tr_b16 v[38:39], v119 offset:2400\n\t"
        "ds_read_b64_tr_b16 v[40:41], v119 offset:2624\n\t"
        "ds_read_b64_tr_b16 v[42:43], v119 offset:2656\n\t"
        "ds_read_b64_tr_b16 v[44:45], v119 offset:2880\n\t"
        "ds_read_b64_tr_b16 v[46:47], v119 offset:2912\n\t"
        "ds_read_b128 v[48:51], v108 offset:16384\n\t"
        "ds_read_b128 v[52:55], v108 offset:17408\n\t"
        "s_waitcnt lgkmcnt(0)\n\t"
        "s_branch LA_x%=\n\t"
        "LA_stre%=:\n\t"
        "s_mov_b32 s42, 0\n\t"
        "LA_str2%=:\n\t"
        "buffer_load_dwordx2 v[108:109], v125, s[8:11], s26 offen sc1\n\t"
        "s_waitcnt vmcnt(0)\n\t"
        "v_and_b32_e32 v45, 0xff, v109\n\t"
        "v_cmp_ne_u32_e32 vcc, s24, v45\n\t"
        "s_nop 0\n\t"
        "s_cbranch_vccz LA_stok%=\n\t"
        "s_add_u32 s42, s42, 1\n\t"
        "s_cmp_lt_u32 s42, 0x100000\n\t"
        "s_cbranch_scc1 LA_str2%=\n\t"
        "s_movk_i32 s34, 5\n\t"
        "s_branch LA_die%=\n\t"
        "LA_v2fail%=:\n\t"
        "s_mov_b32 s42, 0\n\t"
        "LA_v2re%=:\n\t"
        "v_xor_b32_e32 v45, 0x200, v120\n\t"
        "buffer_load_dwordx4 v[12:15], v45, s[8:11], s26 offen sc1\n\t"
        "buffer_load_dwordx4 v[104:107], v120, s[8:11], s26 offen sc1\n\t"
        "s_waitcnt vmcnt(0)\n\t"
        "v_max3_u32 v45, v12, v13, v14\n\t"
        "v_max3_u32 v45, v45, v15, v104\n\t"
        "v_max3_u32 v45, v45, v105, v106\n\t"
        "v_max_u32_e32 v45, v45, v107\n\t"
        "v_cmp_eq_u32_e32 vcc, -1, v45\n\t"
        "s_nop 0\n\t"
        "s_cbranch_vccz LA_v2ok%=\n\t"
        "s_add_u32 s42, s42, 1\n\t"
        "s_cmp_lt_u32 s42, 0x100000\n\t"
        "s_cbranch_scc1 LA_v2re%=\n\t"
        "s_movk_i32 s34, 6\n\t"
        "s_branch LA_die%=\n\t"
        "LA_rdfail%=:\n\t"
        "s_or_b32 s47, s47, 1\n\t"
        "buffer_load_dwordx4 v[16:19], v112, s[8:11], s26 offen offset:0 sc1\n\t"
        "buffer_load_dwordx4 v[20:23], v112, s[8:11], s26 offen offset:1024 sc1\n\t"
        "buffer_load_dwordx4 v[24:27], v112, s[8:11], s26 offen offset:2048 sc1\n\t"
        "buffer_load_dwordx4 v[28:31], v112, s[8:11], s26 offen offset:3072 sc1\n\t"
        "s_nop 7\n\t"
        "v_mov_b64_e32 v[0:1], 0\n\t"
        "v_mov_b64_e32 v[2:3], 0\n\t"
        "v_mov_b64_e32 v[4:5], 0\n\t"
        "v_mov_b64_e32 v[6:7], 0\n\t"
        "v_mov_b64_e32 v[8:9], 0\n\t"
        "v_mov_b64_e32 v[10:11], 0\n\t"
        "v_mov_b64_e32 v[12:13], 0\n\t"
        "v_mov_b64_e32 v[14:15], 0\n\t"
        "s_branch LA_rdre%=w\n\t"
        "LA_rdre%=:\n\t"
        "buffer_load_dwordx4 v[16:19], v112, s[8:11], s26 offen offset:0 sc1\n\t"
        "buffer_load_dwordx4 v[20:23], v112, s[8:11], s26 offen offset:1024 sc1\n\t"
        "buffer_load_dwordx4 v[24:27], v112, s[8:11], s26 offen offset:2048 sc1\n\t"
        "buffer_load_dwordx4 v[28:31], v112, s[8:11], s26 offen offset:3072 sc1\n\t"
        "LA_rdre%=w:\n\t"
        "s_waitcnt vmcnt(0) lgkmcnt(0)\n\t"
        "v_max3_u32 v111, v16, v17, v18\n\t"
        "v_max3_u32 v111, v111, v19, v20\n\t"
        "v_max3_u32 v111, v111, v21, v22\n\t"
        "v_max3_u32 v111, v111, v23, v24\n\t"
        "v_max3_u32 v111, v111, v25, v26\n\t"
        "v_max3_u32 v111, v111, v27, v28\n\t"
        "v_max3_u32 v111, v111, v29, v30\n\t"
        "v_max_u32_e32 v111, v111, v31\n\t"
        "v_cmp_eq_u32_e32 vcc, -1, v111\n\t"
        "s_nop 0\n\t"
        "s_cbranch_vccz LA_go%=\n\t"
        "s_add_u32 s42, s42, 1\n\t"
        "s_cmp_lt_u32 s42, 0x100000\n\t"
        "s_cbranch_scc1 LA_rdre%=\n\t"
        "s_movk_i32 s34, 1\n\t"
        "s_branch LA_die%=\n\t"
        "LA_f2%=:\n\t"
        "s_or_b32 s47, s47, 1\n\t"
        "buffer_load_dwordx4 v[24:27], v112, s[8:11], s26 offen offset:2048 sc1\n\t"
        "s_nop 7\n\t"
        "s_branch LA_r2%=w\n\t"
        "LA_r2%=:\n\t"
        "buffer_load_dwordx4 v[24:27], v112, s[8:11], s26 offen offset:2048 sc1\n\t"
        "LA_r2%=w:\n\t"
        "s_waitcnt vmcnt(0) lgkmcnt(0)\n\t"
        "v_max3_u32 v111, v24, v25, v26\n\t"
        "v_max_u32_e32 v111, v111, v27\n\t"
        "v_cmp_eq_u32_e32 vcc, -1, v111\n\t"
        "s_nop 0\n\t"
        "s_cbranch_vccz LA_go%=\n\t"
        "s_add_u32 s42, s42, 1\n\t"
        "s_cmp_lt_u32 s42, 0x100000\n\t"
        "s_cbranch_scc1 LA_r2%=\n\t"
        "s_movk_i32 s34, 1\n\t"
        "s_branch LA_die%=\n\t"
        "LA_f3%=:\n\t"
        "s_or_b32 s47, s47, 1\n\t"
        "buffer_load_dwordx4 v[28:31], v112, s[8:11], s26 offen offset:3072 sc1\n\t"
        "s_nop 7\n\t"
        "s_branch LA_r3%=w\n\t"
        "LA_r3%=:\n\t"
        "buffer_load_dwordx4 v[28:31], v112, s[8:11], s26 offen offset:3072 sc1\n\t"
        "LA_r3%=w:\n\t"
        "s_waitcnt vmcnt(0) lgkmcnt(0)\n\t"
        "v_max3_u32 v111, v28, v29, v30\n\t"
        "v_max_u32_e32 v111, v111, v31\n\t"
        "v_cmp_eq_u32_e32 vcc, -1, v111\n\t"
        "s_nop 0\n\t"
        "s_cbranch_vccz LA_go%=\n\t"
        "s_add_u32 s42, s42, 1\n\t"
        "s_cmp_lt_u32 s42, 0x100000\n\t"
        "s_cbranch_scc1 LA_r3%=\n\t"
        "s_movk_i32 s34, 1\n\t"
        "s_branch LA_die%=\n\t"
        "LA_vmfail%=:\n\t"
        "s_or_b32 s47, s47, 2\n\t"
        "s_mov_b32 s42, 0\n\t"
        "LA_vmre%=:\n\t"
        "buffer_load_dwordx4 v[104:107], v120, s[8:11], s26 offen sc1\n\t"
        "v_mfma_f32_16x16x32_bf16 v[4:7], v[48:51], v[12:15], 0\n\t"
        "v_mfma_f32_16x16x32_bf16 v[8:11], v[52:55], v[12:15], 0\n\t"
        "s_nop 7\n\t"
        "s_waitcnt vmcnt(0)\n\t"
        "v_mfma_f32_16x16x16_bf16 v[4:7], v[104:105], v[56:57], v[4:7]\n\t"
        "v_mfma_f32_16x16x16_bf16 v[8:11], v[106:107], v[56:57], v[8:11]\n\t"
        "v_max3_u32 v111, v104, v105, v106\n\t"
        "v_max_u32_e32 v111, v111, v107\n\t"
        "v_cmp_eq_u32_e32 vcc, -1, v111\n\t"
        "s_nop 7\n\t"
        "s_cbranch_vccz LA_vmok%=\n\t"
        "s_add_u32 s42, s42, 1\n\t"
        "s_cmp_lt_u32 s42, 0x100000\n\t"
        "s_cbranch_scc1 LA_vmre%=\n\t"
        "s_movk_i32 s34, 2\n\t"
        "s_branch LA_die%=\n\t"
        "LA_die%=:\n\t"
        "v_mov_b32_e32 v30, s34\n\t"
        "s_mov_b32 s35, 33024\n\t"
        "s_mov_b64 exec, 1\n\t"
        "buffer_store_dword v30, off, s[12:15], s35 sc1\n\t"
        "s_endpgm\n\t"
        ".Lk_end_A:\n\t"
        :: REC_S_IN, "{v96}"(g_w), "{v97}"(wb0), "{v98}"(wb1), "{v99}"(rb0), "{v100}"(rb0 ^ 32u), "{v101}"(rb0 ^ 64u), "{v102}"(rb0 ^ 96u),
           "{v112}"(a_mem), "{v113}"(l_ms + w * 4096), "{v114}"(l_ms + ((w + 1) & 3) * 4096), "{v115}"(l_ms + ((w + 2) & 3) * 4096), "{v116}"(l_ms + ((w + 3) & 3) * 4096),
           "{v117}"(lb + w * 22528 + l * 16), "{v118}"(l_iw), "{v119}"(l_iq), "{v120}"(a_vm), "{v121}"(a_o), "{v122}"(h * 8192 + l * 16), "{v123}"(a_ox), "{v124}"(lb + L_KV + l * 16), "{s55}"(lb + L_KV + w * 2048),
           "{v125}"((24 + cp) * 16384 + l * ST_P), "{v126}"(16 * (w & 1) + n - 4 * g), "{v103}"((l & 1) ? 0xEEEEEEEEu : 0x44444444u),
           "{v127}"((l == 0 && w == 0) ? F_PROG + h * 128 : OOB)
        : "memory", "vcc", "scc", "v0", "v255", "a0", "a255", "s101");
    __builtin_unreachable();
}
__device__ __forceinline__ void rec_b(const Params& p, unsigned lb, const int wv, const int id, const int rg, const int cp, const int rot, const int bq) {
    const int w = wv, l = otid() & 63, n = l & 15, g = l >> 4, cs = id, rb = 32 + id * 2 + rg;
    const v4u sh = mk_srd(p.ws + hx_base(p.ws), HX_BYTES - 8192), sw = mk_srd(p.ws, W_TOTAL), s1 = mk_srd(p.Wv, 1024 * 4096 * 2), s2 = mk_srd(p.Wo, 1024 * 4096 * 2);
    const int r8 = l >> 3, pc = l & 7, pp = n >> 3, bb = (n >> 2) & 1, f = (g & 1) | (pp << 1);
    const unsigned stg = lb + LB_STG + w * LB_STGP;
    const unsigned g_v = r8 * 8192 + cs * 256 + rg * 128 + pc * 16, g_o = (l >> 2) * 2048 + cs * 64 + (l & 3) * 16;
    const unsigned wb0 = stg + r8 * 128 + (((pc >> 1) ^ (r8 >> 2)) << 5) + (pc & 1) * 16, wb1 = wb0 ^ 64u;
    const unsigned rb0 = stg + (16 * (g >> 1) + 8 * pp + 4 * (g & 1) + bb) * 128 + (f << 5) + (n & 3) * 8;
    const unsigned o_r = stg + (g >> 1) * (2048 + 128) + (g & 1) * (1024 + 32) + (bb + 4 * pp) * 64 + (n & 3) * 8;
    const unsigned a_mem = cp * 32768 + (w >> 1) * 16384 + (w & 1) * 4096 + l * 16;
    const unsigned a_vmw = w < 2 ? (16 + (cs >> 2)) * 16384 + (cs & 3) * 2048 + (2 * rg + w) * 512 + (l & 31) * 16 + (g >> 1) * 8 : OOB;
    const unsigned a_or = (8 + rg * 4 + w) * 16384 + l * 16;
    const unsigned a_mw = (RPL != 0 && RPL != 4 && w == 3) ? OOB : w * 32768 + (cs >> 4) * 16384 + ((cs >> 1) & 7) * 1024 + (cs & 1) * 512 + (l >> 5) * 256 + rg * 128 + (l & 31) * 4;
    const unsigned a_sw = (w == 0 && l < 4) ? (24 + l) * 16384 + (id * 2 + rg) * ST_P : OOB;
    const bf16_t* m0p = p.mem0 + (4 * rg + ((l >> 3) & 3)) * 1024 + cs * 32 + (l >> 5) * 16 + (l & 7) * 2;
    asm volatile(
        "global_load_dword v109, v[108:109], off\n\t"
        "s_add_u32 s35, s68, 0\n\t"
        "s_and_b32 s35, s35, 7\n\t"
        "s_lshl_b32 s69, s35, 10\n\t"
        "s_add_u32 s35, s68, 2\n\t"
        "s_and_b32 s35, s35, 7\n\t"
        "s_lshl_b32 s70, s35, 10\n\t"
        "s_add_u32 s35, s68, 4\n\t"
        "s_and_b32 s35, s35, 7\n\t"
        "s_lshl_b32 s71, s35, 10\n\t"
        "s_add_u32 s35, s68, 6\n\t"
        "s_and_b32 s35, s35, 7\n\t"
        "s_lshl_b32 s72, s35, 10\n\t"
        "s_lshl_b32 s34, s43, 21\n\t"
        "s_add_u32 s34, s34, 0\n\t"
        "buffer_load_dwordx4 v[128:131], v96, s[16:19], s34 offen nt\n\t"
        "s_add_u32 s34, s34, 0x10000\n\t"
        "buffer_load_dwordx4 v[132:135], v96, s[16:19], s34 offen nt\n\t"
        "s_add_u32 s34, s34, 0x10000\n\t"
        "buffer_load_dwordx4 v[136:139], v96, s[16:19], s34 offen nt\n\t"
        "s_add_u32 s34, s34, 0x10000\n\t"
        "buffer_load_dwordx4 v[140:143], v96, s[16:19], s34 offen nt\n\t"
        "s_add_u32 s34, s34, 0x10000\n\t"
        "buffer_load_dwordx4 v[144:147], v96, s[16:19], s34 offen nt\n\t"
        "s_add_u32 s34, s34, 0x10000\n\t"
        "buffer_load_dwordx4 v[148:151], v96, s[16:19], s34 offen nt\n\t"
        "s_add_u32 s34, s34, 0x10000\n\t"
        "buffer_load_dwordx4 v[152:155], v96, s[16:19], s34 offen nt\n\t"
        "s_add_u32 s34, s34, 0x10000\n\t"
        "buffer_load_dwordx4 v[156:159], v96, s[16:19], s34 offen nt\n\t"
        "s_lshl_b32 s34, s43, 21\n\t"
        "s_add_u32 s34, s34, 524288\n\t"
        "buffer_load_dwordx4 v[160:163], v96, s[16:19], s34 offen nt\n\t"
        "s_add_u32 s34, s34, 0x10000\n\t"
        "buffer_load_dwordx4 v[164:167], v96, s[16:19], s34 offen nt\n\t"
        "s_add_u32 s34, s34, 0x10000\n\t"
        "buffer_load_dwordx4 v[168:171], v96, s[16:19], s34 offen nt\n\t"
        "s_add_u32 s34, s34, 0x10000\n\t"
        "buffer_load_dwordx4 v[172:175], v96, s[16:19], s34 offen nt\n\t"
        "s_add_u32 s34, s34, 0x10000\n\t"
        "buffer_load_dwordx4 v[176:179], v96, s[16:19], s34 offen nt\n\t"
        "s_add_u32 s34, s34, 0x10000\n\t"
        "buffer_load_dwordx4 v[180:183], v96, s[16:19], s34 offen nt\n\t"
        "s_add_u32 s34, s34, 0x10000\n\t"
        "buffer_load_dwordx4 v[184:187], v96, s[16:19], s34 offen nt\n\t"
        "s_add_u32 s34, s34, 0x10000\n\t"
        "buffer_load_dwordx4 v[188:191], v96, s[16:19], s34 offen nt\n\t"
        "s_lshl_b32 s34, s43, 21\n\t"
        "s_add_u32 s34, s34, 1048576\n\t"
        "buffer_load_dwordx4 v[192:195], v96, s[16:19], s34 offen nt\n\t"
        "s_add_u32 s34, s34, 0x10000\n\t"
        "buffer_load_dwordx4 v[196:199], v96, s[16:19], s34 offen nt\n\t"
        "s_add_u32 s34, s34, 0x10000\n\t"
        "buffer_load_dwordx4 v[200:203], v96, s[16:19], s34 offen nt\n\t"
        "s_add_u32 s34, s34, 0x10000\n\t"
        "buffer_load_dwordx4 v[204:207], v96, s[16:19], s34 offen nt\n\t"
        "s_add_u32 s34, s34, 0x10000\n\t"
        "buffer_load_dwordx4 v[208:211], v96, s[16:19], s34 offen nt\n\t"
        "s_add_u32 s34, s34, 0x10000\n\t"
        "buffer_load_dwordx4 v[212:215], v96, s[16:19], s34 offen nt\n\t"
        "s_add_u32 s34, s34, 0x10000\n\t"
        "buffer_load_dwordx4 v[216:219], v96, s[16:19], s34 offen nt\n\t"
        "s_add_u32 s34, s34, 0x10000\n\t"
        "buffer_load_dwordx4 v[220:223], v96, s[16:19], s34 offen nt\n\t"
        "s_waitcnt vmcnt(16)\n\t"
        "ds_write_b128 v98, v[128:131] offset:0\n\t"
        "ds_write_b128 v99, v[132:135] offset:1024\n\t"
        "ds_write_b128 v98, v[136:139] offset:2048\n\t"
        "ds_write_b128 v99, v[140:143] offset:3072\n\t"
        "ds_write_b128 v98, v[144:147] offset:4096\n\t"
        "ds_write_b128 v99, v[148:151] offset:5120\n\t"
        "ds_write_b128 v98, v[152:155] offset:6144\n\t"
        "ds_write_b128 v99, v[156:159] offset:7168\n\t"
        "ds_read_b64_tr_b16 v[128:129], v100 offset:0\n\t"
        "ds_read_b64_tr_b16 v[130:131], v100 offset:256\n\t"
        "ds_read_b64_tr_b16 v[132:133], v100 offset:4096\n\t"
        "ds_read_b64_tr_b16 v[134:135], v100 offset:4352\n\t"
        "s_waitcnt lgkmcnt(8)\n\t"
        "ds_read_b64_tr_b16 v[136:137], v101 offset:0\n\t"
        "ds_read_b64_tr_b16 v[138:139], v101 offset:256\n\t"
        "ds_read_b64_tr_b16 v[140:141], v101 offset:4096\n\t"
        "ds_read_b64_tr_b16 v[142:143], v101 offset:4352\n\t"
        "s_waitcnt lgkmcnt(8)\n\t"
        "ds_read_b64_tr_b16 v[144:145], v102 offset:0\n\t"
        "ds_read_b64_tr_b16 v[146:147], v102 offset:256\n\t"
        "ds_read_b64_tr_b16 v[148:149], v102 offset:4096\n\t"
        "ds_read_b64_tr_b16 v[150:151], v102 offset:4352\n\t"
        "s_waitcnt lgkmcnt(8)\n\t"
        "ds_read_b64_tr_b16 v[152:153], v103 offset:0\n\t"
        "ds_read_b64_tr_b16 v[154:155], v103 offset:256\n\t"
        "ds_read_b64_tr_b16 v[156:157], v103 offset:4096\n\t"
        "ds_read_b64_tr_b16 v[158:159], v103 offset:4352\n\t"
        "s_lshl_b32 s34, s43, 21\n\t"
        "s_add_u32 s34, s34, 1572864\n\t"
        "buffer_load_dwordx4 v[224:227], v96, s[16:19], s34 offen nt\n\t"
        "s_add_u32 s34, s34, 0x10000\n\t"
        "buffer_load_dwordx4 v[228:231], v96, s[16:19], s34 offen nt\n\t"
        "s_add_u32 s34, s34, 0x10000\n\t"
        "buffer_load_dwordx4 v[232:235], v96, s[16:19], s34 offen nt\n\t"
        "s_add_u32 s34, s34, 0x10000\n\t"
        "buffer_load_dwordx4 v[236:239], v96, s[16:19], s34 offen nt\n\t"
        "s_add_u32 s34, s34, 0x10000\n\t"
        "buffer_load_dwordx4 v[240:243], v96, s[16:19], s34 offen nt\n\t"
        "s_add_u32 s34, s34, 0x10000\n\t"
        "buffer_load_dwordx4 v[244:247], v96, s[16:19], s34 offen nt\n\t"
        "s_add_u32 s34, s34, 0x10000\n\t"
        "buffer_load_dwordx4 v[248:251], v96, s[16:19], s34 offen nt\n\t"
        "s_add_u32 s34, s34, 0x10000\n\t"
        "buffer_load_dwordx4 v[252:255], v96, s[16:19], s34 offen nt\n\t"
        "s_waitcnt lgkmcnt(0)\n\t"
        "s_waitcnt vmcnt(16)\n\t"
        "ds_write_b128 v98, v[160:163] offset:0\n\t"
        "ds_write_b128 v99, v[164:167] offset:1024\n\t"
        "ds_write_b128 v98, v[168:171] offset:2048\n\t"
        "ds_write_b128 v99, v[172:175] offset:3072\n\t"
        "ds_write_b128 v98, v[176:179] offset:4096\n\t"
        "ds_write_b128 v99, v[180:183] offset:5120\n\t"
        "ds_write_b128 v98, v[184:187] offset:6144\n\t"
        "ds_write_b128 v99, v[188:191] offset:7168\n\t"
        "ds_read_b64_tr_b16 v[160:161], v100 offset:0\n\t"
        "ds_read_b64_tr_b16 v[162:163], v100 offset:256\n\t"
        "ds_read_b64_tr_b16 v[164:165], v100 offset:4096\n\t"
        "ds_read_b64_tr_b16 v[166:167], v100 offset:4352\n\t"
        "s_waitcnt lgkmcnt(8)\n\t"
        "ds_read_b64_tr_b16 v[168:169], v101 offset:0\n\t"
        "ds_read_b64_tr_b16 v[170:171], v101 offset:256\n\t"
        "ds_read_b64_tr_b16 v[172:173], v101 offset:4096\n\t"
        "ds_read_b64_tr_b16 v[174:175], v101 offset:4352\n\t"
        "s_waitcnt lgkmcnt(8)\n\t"
        "ds_read_b64_tr_b16 v[176:177], v102 offset:0\n\t"
        "ds_read_b64_tr_b16 v[178:179], v102 offset:256\n\t"
        "ds_read_b64_tr_b16 v[180:181], v102 offset:4096\n\t"
        "ds_read_b64_tr_b16 v[182:183], v102 offset:4352\n\t"
        "s_waitcnt lgkmcnt(8)\n\t"
        "ds_read_b64_tr_b16 v[184:185], v103 offset:0\n\t"
        "ds_read_b64_tr_b16 v[186:187], v103 offset:256\n\t"
        "ds_read_b64_tr_b16 v[188:189], v103 offset:4096\n\t"
        "ds_read_b64_tr_b16 v[190:191], v103 offset:4352\n\t"
        "s_add_u32 s35, s68, 0\n\t"
        "s_and_b32 s35, s35, 7\n\t"
        "s_lshl_b32 s35, s35, 18\n\t"
        "s_lshl_b32 s34, s43, 21\n\t"
        "s_add_u32 s34, s34, s35\n\t"
        "buffer_load_dwordx4 a[0:3], v97, s[20:23], s34 offen\n\t"
        "s_add_u32 s34, s34, 0x8000\n\t"
        "buffer_load_dwordx4 a[4:7], v97, s[20:23], s34 offen\n\t"
        "s_add_u32 s34, s34, 0x8000\n\t"
        "buffer_load_dwordx4 a[8:11], v97, s[20:23], s34 offen\n\t"
        "s_add_u32 s34, s34, 0x8000\n\t"
        "buffer_load_dwordx4 a[12:15], v97, s[20:23], s34 offen\n\t"
        "s_add_u32 s34, s34, 0x8000\n\t"
        "buffer_load_dwordx4 a[16:19], v97, s[20:23], s34 offen\n\t"
        "s_add_u32 s34, s34, 0x8000\n\t"
        "buffer_load_dwordx4 a[20:23], v97, s[20:23], s34 offen\n\t"
        "s_add_u32 s34, s34, 0x8000\n\t"
        "buffer_load_dwordx4 a[24:27], v97, s[20:23], s34 offen\n\t"
        "s_add_u32 s34, s34, 0x8000\n\t"
        "buffer_load_dwordx4 a[28:31], v97, s[20:23], s34 offen\n\t"
        "s_waitcnt lgkmcnt(0)\n\t"
        "s_waitcnt vmcnt(16)\n\t"
        "ds_write_b128 v98, v[192:195] offset:0\n\t"
        "ds_write_b128 v99, v[196:199] offset:1024\n\t"
        "ds_write_b128 v98, v[200:203] offset:2048\n\t"
        "ds_write_b128 v99, v[204:207] offset:3072\n\t"
        "ds_write_b128 v98, v[208:211] offset:4096\n\t"
        "ds_write_b128 v99, v[212:215] offset:5120\n\t"
        "ds_write_b128 v98, v[216:219] offset:6144\n\t"
        "ds_write_b128 v99, v[220:223] offset:7168\n\t"
        "ds_read_b64_tr_b16 v[192:193], v100 offset:0\n\t"
        "ds_read_b64_tr_b16 v[194:195], v100 offset:256\n\t"
        "ds_read_b64_tr_b16 v[196:197], v100 offset:4096\n\t"
        "ds_read_b64_tr_b16 v[198:199], v100 offset:4352\n\t"
        "s_waitcnt lgkmcnt(8)\n\t"
        "ds_read_b64_tr_b16 v[200:201], v101 offset:0\n\t"
        "ds_read_b64_tr_b16 v[202:203], v101 offset:256\n\t"
        "ds_read_b64_tr_b16 v[204:205], v101 offset:4096\n\t"
        "ds_read_b64_tr_b16 v[206:207], v101 offset:4352\n\t"
        "s_waitcnt lgkmcnt(8)\n\t"
        "ds_read_b64_tr_b16 v[208:209], v102 offset:0\n\t"
        "ds_read_b64_tr_b16 v[210:211], v102 offset:256\n\t"
        "ds_read_b64_tr_b16 v[212:213], v102 offset:4096\n\t"
        "ds_read_b64_tr_b16 v[214:215], v102 offset:4352\n\t"
        "s_waitcnt lgkmcnt(8)\n\t"
        "ds_read_b64_tr_b16 v[216:217], v103 offset:0\n\t"
        "ds_read_b64_tr_b16 v[218:219], v103 offset:256\n\t"
        "ds_read_b64_tr_b16 v[220:221], v103 offset:4096\n\t"
        "ds_read_b64_tr_b16 v[222:223], v103 offset:4352\n\t"
        "s_add_u32 s35, s68, 1\n\t"
        "s_and_b32 s35, s35, 7\n\t"
        "s_lshl_b32 s35, s35, 18\n\t"
        "s_lshl_b32 s34, s43, 21\n\t"
        "s_add_u32 s34, s34, s35\n\t"
        "buffer_load_dwordx4 a[32:35], v97, s[20:23], s34 offen\n\t"
        "s_add_u32 s34, s34, 0x8000\n\t"
        "buffer_load_dwordx4 a[36:39], v97, s[20:23], s34 offen\n\t"
        "s_add_u32 s34, s34, 0x8000\n\t"
        "buffer_load_dwordx4 a[40:43], v97, s[20:23], s34 offen\n\t"
        "s_add_u32 s34, s34, 0x8000\n\t"
        "buffer_load_dwordx4 a[44:47], v97, s[20:23], s34 offen\n\t"
        "s_add_u32 s34, s34, 0x8000\n\t"
        "buffer_load_dwordx4 a[48:51], v97, s[20:23], s34 offen\n\t"
        "s_add_u32 s34, s34, 0x8000\n\t"
        "buffer_load_dwordx4 a[52:55], v97, s[20:23], s34 offen\n\t"
        "s_add_u32 s34, s34, 0x8000\n\t"
        "buffer_load_dwordx4 a[56:59], v97, s[20:23], s34 offen\n\t"
        "s_add_u32 s34, s34, 0x8000\n\t"
        "buffer_load_dwordx4 a[60:63], v97, s[20:23], s34 offen\n\t"
        "s_waitcnt lgkmcnt(0)\n\t"
        "s_waitcnt vmcnt(16)\n\t"
        "ds_write_b128 v98, v[224:227] offset:0\n\t"
        "ds_write_b128 v99, v[228:231] offset:1024\n\t"
        "ds_write_b128 v98, v[232:235] offset:2048\n\t"
        "ds_write_b128 v99, v[236:239] offset:3072\n\t"
        "ds_write_b128 v98, v[240:243] offset:4096\n\t"
        "ds_write_b128 v99, v[244:247] offset:5120\n\t"
        "ds_write_b128 v98, v[248:251] offset:6144\n\t"
        "ds_write_b128 v99, v[252:255] offset:7168\n\t"
        "ds_read_b64_tr_b16 v[224:225], v100 offset:0\n\t"
        "ds_read_b64_tr_b16 v[226:227], v100 offset:256\n\t"
        "ds_read_b64_tr_b16 v[228:229], v100 offset:4096\n\t"
        "ds_read_b64_tr_b16 v[230:231], v100 offset:4352\n\t"
        "s_waitcnt lgkmcnt(8)\n\t"
        "ds_read_b64_tr_b16 v[232:233], v101 offset:0\n\t"
        "ds_read_b64_tr_b16 v[234:235], v101 offset:256\n\t"
        "ds_read_b64_tr_b16 v[236:237], v101 offset:4096\n\t"
        "ds_read_b64_tr_b16 v[238:239], v101 offset:4352\n\t"
        "s_waitcnt lgkmcnt(8)\n\t"
        "ds_read_b64_tr_b16 v[240:241], v102 offset:0\n\t"
        "ds_read_b64_tr_b16 v[242:243], v102 offset:256\n\t"
        "ds_read_b64_tr_b16 v[244:245], v102 offset:4096\n\t"
        "ds_read_b64_tr_b16 v[246:247], v102 offset:4352\n\t"
        "s_waitcnt lgkmcnt(8)\n\t"
        "ds_read_b64_tr_b16 v[248:249], v103 offset:0\n\t"
        "ds_read_b64_tr_b16 v[250:251], v103 offset:256\n\t"
        "ds_read_b64_tr_b16 v[252:253], v103 offset:4096\n\t"
        "ds_read_b64_tr_b16 v[254:255], v103 offset:4352\n\t"
        "s_add_u32 s35, s68, 2\n\t"
        "s_and_b32 s35, s35, 7\n\t"
        "s_lshl_b32 s35, s35, 18\n\t"
        "s_lshl_b32 s34, s43, 21\n\t"
        "s_add_u32 s34, s34, s35\n\t"
        "buffer_load_dwordx4 a[64:67], v97, s[20:23], s34 offen\n\t"
        "s_add_u32 s34, s34, 0x8000\n\t"
        "buffer_load_dwordx4 a[68:71], v97, s[20:23], s34 offen\n\t"
        "s_add_u32 s34, s34, 0x8000\n\t"
        "buffer_load_dwordx4 a[72:75], v97, s[20:23], s34 offen\n\t"
        "s_add_u32 s34, s34, 0x8000\n\t"
        "buffer_load_dwordx4 a[76:79], v97, s[20:23], s34 offen\n\t"
        "s_add_u32 s34, s34, 0x8000\n\t"
        "buffer_load_dwordx4 a[80:83], v97, s[20:23], s34 offen\n\t"
        "s_add_u32 s34, s34, 0x8000\n\t"
        "buffer_load_dwordx4 a[84:87], v97, s[20:23], s34 offen\n\t"
        "s_add_u32 s34, s34, 0x8000\n\t"
        "buffer_load_dwordx4 a[88:91], v97, s[20:23], s34 offen\n\t"
        "s_add_u32 s34, s34, 0x8000\n\t"
        "buffer_load_dwordx4 a[92:95], v97, s[20:23], s34 offen\n\t"
        "s_waitcnt lgkmcnt(0)\n\t"
        "s_waitcnt vmcnt(16)\n\t"
        "ds_write_b128 v122, a[0:3] offset:0\n\t"
        "ds_write_b128 v122, a[4:7] offset:1056\n\t"
        "ds_write_b128 v122, a[8:11] offset:2176\n\t"
        "ds_write_b128 v122, a[12:15] offset:3232\n\t"
        "ds_write_b128 v122, a[16:19] offset:4352\n\t"
        "ds_write_b128 v122, a[20:23] offset:5408\n\t"
        "ds_write_b128 v122, a[24:27] offset:6528\n\t"
        "ds_write_b128 v122, a[28:31] offset:7584\n\t"
        "ds_read_b64_tr_b16 a[0:1], v123 offset:0\n\t"
        "ds_read_b64_tr_b16 a[2:3], v123 offset:128\n\t"
        "ds_read_b64_tr_b16 a[4:5], v123 offset:4352\n\t"
        "ds_read_b64_tr_b16 a[6:7], v123 offset:4480\n\t"
        "s_waitcnt lgkmcnt(8)\n\t"
        "ds_read_b64_tr_b16 a[8:9], v123 offset:32\n\t"
        "ds_read_b64_tr_b16 a[10:11], v123 offset:160\n\t"
        "ds_read_b64_tr_b16 a[12:13], v123 offset:4384\n\t"
        "ds_read_b64_tr_b16 a[14:15], v123 offset:4512\n\t"
        "s_waitcnt lgkmcnt(8)\n\t"
        "ds_read_b64_tr_b16 a[16:17], v123 offset:512\n\t"
        "ds_read_b64_tr_b16 a[18:19], v123 offset:640\n\t"
        "ds_read_b64_tr_b16 a[20:21], v123 offset:4864\n\t"
        "ds_read_b64_tr_b16 a[22:23], v123 offset:4992\n\t"
        "s_waitcnt lgkmcnt(8)\n\t"
        "ds_read_b64_tr_b16 a[24:25], v123 offset:544\n\t"
        "ds_read_b64_tr_b16 a[26:27], v123 offset:672\n\t"
        "ds_read_b64_tr_b16 a[28:29], v123 offset:4896\n\t"
        "ds_read_b64_tr_b16 a[30:31], v123 offset:5024\n\t"
        "s_waitcnt lgkmcnt(8)\n\t"
        "s_add_u32 s35, s68, 3\n\t"
        "s_and_b32 s35, s35, 7\n\t"
        "s_lshl_b32 s35, s35, 18\n\t"
        "s_lshl_b32 s34, s43, 21\n\t"
        "s_add_u32 s34, s34, s35\n\t"
        "buffer_load_dwordx4 a[96:99], v97, s[20:23], s34 offen\n\t"
        "s_add_u32 s34, s34, 0x8000\n\t"
        "buffer_load_dwordx4 a[100:103], v97, s[20:23], s34 offen\n\t"
        "s_add_u32 s34, s34, 0x8000\n\t"
        "buffer_load_dwordx4 a[104:107], v97, s[20:23], s34 offen\n\t"
        "s_add_u32 s34, s34, 0x8000\n\t"
        "buffer_load_dwordx4 a[108:111], v97, s[20:23], s34 offen\n\t"
        "s_add_u32 s34, s34, 0x8000\n\t"
        "buffer_load_dwordx4 a[112:115], v97, s[20:23], s34 offen\n\t"
        "s_add_u32 s34, s34, 0x8000\n\t"
        "buffer_load_dwordx4 a[116:119], v97, s[20:23], s34 offen\n\t"
        "s_add_u32 s34, s34, 0x8000\n\t"
        "buffer_load_dwordx4 a[120:123], v97, s[20:23], s34 offen\n\t"
        "s_add_u32 s34, s34, 0x8000\n\t"
        "buffer_load_dwordx4 a[124:127], v97, s[20:23], s34 offen\n\t"
        "s_waitcnt lgkmcnt(0)\n\t"
        "s_waitcnt vmcnt(16)\n\t"
        "ds_write_b128 v122, a[32:35] offset:0\n\t"
        "ds_write_b128 v122, a[36:39] offset:1056\n\t"
        "ds_write_b128 v122, a[40:43] offset:2176\n\t"
        "ds_write_b128 v122, a[44:47] offset:3232\n\t"
        "ds_write_b128 v122, a[48:51] offset:4352\n\t"
        "ds_write_b128 v122, a[52:55] offset:5408\n\t"
        "ds_write_b128 v122, a[56:59] offset:6528\n\t"
        "ds_write_b128 v122, a[60:63] offset:7584\n\t"
        "ds_read_b64_tr_b16 a[32:33], v123 offset:0\n\t"
        "ds_read_b64_tr_b16 a[34:35], v123 offset:128\n\t"
        "ds_read_b64_tr_b16 a[36:37], v123 offset:4352\n\t"
        "ds_read_b64_tr_b16 a[38:39], v123 offset:4480\n\t"
        "s_waitcnt lgkmcnt(8)\n\t"
        "ds_read_b64_tr_b16 a[40:41], v123 offset:32\n\t"
        "ds_read_b64_tr_b16 a[42:43], v123 offset:160\n\t"
        "ds_read_b64_tr_b16 a[44:45], v123 offset:4384\n\t"
        "ds_read_b64_tr_b16 a[46:47], v123 offset:4512\n\t"
        "s_waitcnt lgkmcnt(8)\n\t"
        "ds_read_b64_tr_b16 a[48:49], v123 offset:512\n\t"
        "ds_read_b64_tr_b16 a[50:51], v123 offset:640\n\t"
        "ds_read_b64_tr_b16 a[52:53], v123 offset:4864\n\t"
        "ds_read_b64_tr_b16 a[54:55], v123 offset:4992\n\t"
        "s_waitcnt lgkmcnt(8)\n\t"
        "ds_read_b64_tr_b16 a[56:57], v123 offset:544\n\t"
        "ds_read_b64_tr_b16 a[58:59], v123 offset:672\n\t"
        "ds_read_b64_tr_b16 a[60:61], v123 offset:4896\n\t"
        "ds_read_b64_tr_b16 a[62:63], v123 offset:5024\n\t"
        "s_waitcnt lgkmcnt(8)\n\t"
        "s_add_u32 s35, s68, 4\n\t"
        "s_and_b32 s35, s35, 7\n\t"
        "s_lshl_b32 s35, s35, 18\n\t"
        "s_lshl_b32 s34, s43, 21\n\t"
        "s_add_u32 s34, s34, s35\n\t"
        "buffer_load_dwordx4 a[128:131], v97, s[20:23], s34 offen\n\t"
        "s_add_u32 s34, s34, 0x8000\n\t"
        "buffer_load_dwordx4 a[132:135], v97, s[20:23], s34 offen\n\t"
        "s_add_u32 s34, s34, 0x8000\n\t"
        "buffer_load_dwordx4 a[136:139], v97, s[20:23], s34 offen\n\t"
        "s_add_u32 s34, s34, 0x8000\n\t"
        "buffer_load_dwordx4 a[140:143], v97, s[20:23], s34 offen\n\t"
        "s_add_u32 s34, s34, 0x8000\n\t"
        "buffer_load_dwordx4 a[144:147], v97, s[20:23], s34 offen\n\t"
        "s_add_u32 s34, s34, 0x8000\n\t"
        "buffer_load_dwordx4 a[148:151], v97, s[20:23], s34 offen\n\t"
        "s_add_u32 s34, s34, 0x8000\n\t"
        "buffer_load_dwordx4 a[152:155], v97, s[20:23], s34 offen\n\t"
        "s_add_u32 s34, s34, 0x8000\n\t"
        "buffer_load_dwordx4 a[156:159], v97, s[20:23], s34 offen\n\t"
        "s_waitcnt lgkmcnt(0)\n\t"
        "s_waitcnt vmcnt(16)\n\t"
        "ds_write_b128 v122, a[64:67] offset:0\n\t"
        "ds_write_b128 v122, a[68:71] offset:1056\n\t"
        "ds_write_b128 v122, a[72:75] offset:2176\n\t"
        "ds_write_b128 v122, a[76:79] offset:3232\n\t"
        "ds_write_b128 v122, a[80:83] offset:4352\n\t"
        "ds_write_b128 v122, a[84:87] offset:5408\n\t"
        "ds_write_b128 v122, a[88:91] offset:6528\n\t"
        "ds_write_b128 v122, a[92:95] offset:7584\n\t"
        "ds_read_b64_tr_b16 a[64:65], v123 offset:0\n\t"
        "ds_read_b64_tr_b16 a[66:67], v123 offset:128\n\t"
        "ds_read_b64_tr_b16 a[68:69], v123 offset:4352\n\t"
        "ds_read_b64_tr_b16 a[70:71], v123 offset:4480\n\t"
        "s_waitcnt lgkmcnt(8)\n\t"
        "ds_read_b64_tr_b16 a[72:73], v123 offset:32\n\t"
        "ds_read_b64_tr_b16 a[74:75], v123 offset:160\n\t"
        "ds_read_b64_tr_b16 a[76:77], v123 offset:4384\n\t"
        "ds_read_b64_tr_b16 a[78:79], v123 offset:4512\n\t"
        "s_waitcnt lgkmcnt(8)\n\t"
        "ds_read_b64_tr_b16 a[80:81], v123 offset:512\n\t"
        "ds_read_b64_tr_b16 a[82:83], v123 offset:640\n\t"
        "ds_read_b64_tr_b16 a[84:85], v123 offset:4864\n\t"
        "ds_read_b64_tr_b16 a[86:87], v123 offset:4992\n\t"
        "s_waitcnt lgkmcnt(8)\n\t"
        "ds_read_b64_tr_b16 a[88:89], v123 offset:544\n\t"
        "ds_read_b64_tr_b16 a[90:91], v123 offset:672\n\t"
        "ds_read_b64_tr_b16 a[92:93], v123 offset:4896\n\t"
        "ds_read_b64_tr_b16 a[94:95], v123 offset:5024\n\t"
        "s_waitcnt lgkmcnt(8)\n\t"
        "s_add_u32 s35, s68, 5\n\t"
        "s_and_b32 s35, s35, 7\n\t"
        "s_lshl_b32 s35, s35, 18\n\t"
        "s_lshl_b32 s34, s43, 21\n\t"
        "s_add_u32 s34, s34, s35\n\t"
        "buffer_load_dwordx4 a[160:163], v97, s[20:23], s34 offen\n\t"
        "s_add_u32 s34, s34, 0x8000\n\t"
        "buffer_load_dwordx4 a[164:167], v97, s[20:23], s34 offen\n\t"
        "s_add_u32 s34, s34, 0x8000\n\t"
        "buffer_load_dwordx4 a[168:171], v97, s[20:23], s34 offen\n\t"
        "s_add_u32 s34, s34, 0x8000\n\t"
        "buffer_load_dwordx4 a[172:175], v97, s[20:23], s34 offen\n\t"
        "s_add_u32 s34, s34, 0x8000\n\t"
        "buffer_load_dwordx4 a[176:179], v97, s[20:23], s34 offen\n\t"
        "s_add_u32 s34, s34, 0x8000\n\t"
        "buffer_load_dwordx4 a[180:183], v97, s[20:23], s34 offen\n\t"
        "s_add_u32 s34, s34, 0x8000\n\t"
        "buffer_load_dwordx4 a[184:187], v97, s[20:23], s34 offen\n\t"
        "s_add_u32 s34, s34, 0x8000\n\t"
        "buffer_load_dwordx4 a[188:191], v97, s[20:23], s34 offen\n\t"
        "s_waitcnt lgkmcnt(0)\n\t"
        "s_waitcnt vmcnt(16)\n\t"
        "ds_write_b128 v122, a[96:99] offset:0\n\t"
        "ds_write_b128 v122, a[100:103] offset:1056\n\t"
        "ds_write_b128 v122, a[104:107] offset:2176\n\t"
        "ds_write_b128 v122, a[108:111] offset:3232\n\t"
        "ds_write_b128 v122, a[112:115] offset:4352\n\t"
        "ds_write_b128 v122, a[116:119] offset:5408\n\t"
        "ds_write_b128 v122, a[120:123] offset:6528\n\t"
        "ds_write_b128 v122, a[124:127] offset:7584\n\t"
        "ds_read_b64_tr_b16 a[96:97], v123 offset:0\n\t"
        "ds_read_b64_tr_b16 a[98:99], v123 offset:128\n\t"
        "ds_read_b64_tr_b16 a[100:101], v123 offset:4352\n\t"
        "ds_read_b64_tr_b16 a[102:103], v123 offset:4480\n\t"
        "s_waitcnt lgkmcnt(8)\n\t"
        "ds_read_b64_tr_b16 a[104:105], v123 offset:32\n\t"
        "ds_read_b64_tr_b16 a[106:107], v123 offset:160\n\t"
        "ds_read_b64_tr_b16 a[108:109], v123 offset:4384\n\t"
        "ds_read_b64_tr_b16 a[110:111], v123 offset:4512\n\t"
        "s_waitcnt lgkmcnt(8)\n\t"
        "ds_read_b64_tr_b16 a[112:113], v123 offset:512\n\t"
        "ds_read_b64_tr_b16 a[114:115], v123 offset:640\n\t"
        "ds_read_b64_tr_b16 a[116:117], v123 offset:4864\n\t"
        "ds_read_b64_tr_b16 a[118:119], v123 offset:4992\n\t"
        "s_waitcnt lgkmcnt(8)\n\t"
        "ds_read_b64_tr_b16 a[120:121], v123 offset:544\n\t"
        "ds_read_b64_tr_b16 a[122:123], v123 offset:672\n\t"
        "ds_read_b64_tr_b16 a[124:125], v123 offset:4896\n\t"
        "ds_read_b64_tr_b16 a[126:127], v123 offset:5024\n\t"
        "s_waitcnt lgkmcnt(8)\n\t"
        "s_add_u32 s35, s68, 6\n\t"
        "s_and_b32 s35, s35, 7\n\t"
        "s_lshl_b32 s35, s35, 18\n\t"
        "s_lshl_b32 s34, s43, 21\n\t"
        "s_add_u32 s34, s34, s35\n\t"
        "buffer_load_dwordx4 a[192:195], v97, s[20:23], s34 offen\n\t"
        "s_add_u32 s34, s34, 0x8000\n\t"
        "buffer_load_dwordx4 a[196:199], v97, s[20:23], s34 offen\n\t"
        "s_add_u32 s34, s34, 0x8000\n\t"
        "buffer_load_dwordx4 a[200:203], v97, s[20:23], s34 offen\n\t"
        "s_add_u32 s34, s34, 0x8000\n\t"
        "buffer_load_dwordx4 a[204:207], v97, s[20:23], s34 offen\n\t"
        "s_add_u32 s34, s34, 0x8000\n\t"
        "buffer_load_dwordx4 a[208:211], v97, s[20:23], s34 offen\n\t"
        "s_add_u32 s34, s34, 0x8000\n\t"
        "buffer_load_dwordx4 a[212:215], v97, s[20:23], s34 offen\n\t"
        "s_add_u32 s34, s34, 0x8000\n\t"
        "buffer_load_dwordx4 a[216:219], v97, s[20:23], s34 offen\n\t"
        "s_add_u32 s34, s34, 0x8000\n\t"
        "buffer_load_dwordx4 a[220:223], v97, s[20:23], s34 offen\n\t"
        "s_waitcnt lgkmcnt(0)\n\t"
        "s_waitcnt vmcnt(16)\n\t"
        "ds_write_b128 v122, a[128:131] offset:0\n\t"
        "ds_write_b128 v122, a[132:135] offset:1056\n\t"
        "ds_write_b128 v122, a[136:139] offset:2176\n\t"
        "ds_write_b128 v122, a[140:143] offset:3232\n\t"
        "ds_write_b128 v122, a[144:147] offset:4352\n\t"
        "ds_write_b128 v122, a[148:151] offset:5408\n\t"
        "ds_write_b128 v122, a[152:155] offset:6528\n\t"
        "ds_write_b128 v122, a[156:159] offset:7584\n\t"
        "ds_read_b64_tr_b16 a[128:129], v123 offset:0\n\t"
        "ds_read_b64_tr_b16 a[130:131], v123 offset:128\n\t"
        "ds_read_b64_tr_b16 a[132:133], v123 offset:4352\n\t"
        "ds_read_b64_tr_b16 a[134:135], v123 offset:4480\n\t"
        "s_waitcnt lgkmcnt(8)\n\t"
        "ds_read_b64_tr_b16 a[136:137], v123 offset:32\n\t"
        "ds_read_b64_tr_b16 a[138:139], v123 offset:160\n\t"
        "ds_read_b64_tr_b16 a[140:141], v123 offset:4384\n\t"
        "ds_read_b64_tr_b16 a[142:143], v123 offset:4512\n\t"
        "s_waitcnt lgkmcnt(8)\n\t"
        "ds_read_b64_tr_b16 a[144:145], v123 offset:512\n\t"
        "ds_read_b64_tr_b16 a[146:147], v123 offset:640\n\t"
        "ds_read_b64_tr_b16 a[148:149], v123 offset:4864\n\t"
        "ds_read_b64_tr_b16 a[150:151], v123 offset:4992\n\t"
        "s_waitcnt lgkmcnt(8)\n\t"
        "ds_read_b64_tr_b16 a[152:153], v123 offset:544\n\t"
        "ds_read_b64_tr_b16 a[154:155], v123 offset:672\n\t"
        "ds_read_b64_tr_b16 a[156:157], v123 offset:4896\n\t"
        "ds_read_b64_tr_b16 a[158:159], v123 offset:5024\n\t"
        "s_waitcnt lgkmcnt(8)\n\t"
        "s_add_u32 s35, s68, 7\n\t"
        "s_and_b32 s35, s35, 7\n\t"
        "s_lshl_b32 s35, s35, 18\n\t"
        "s_lshl_b32 s34, s43, 21\n\t"
        "s_add_u32 s34, s34, s35\n\t"
        "buffer_load_dwordx4 a[224:227], v97, s[20:23], s34 offen\n\t"
        "s_add_u32 s34, s34, 0x8000\n\t"
        "buffer_load_dwordx4 a[228:231], v97, s[20:23], s34 offen\n\t"
        "s_add_u32 s34, s34, 0x8000\n\t"
        "buffer_load_dwordx4 a[232:235], v97, s[20:23], s34 offen\n\t"
        "s_add_u32 s34, s34, 0x8000\n\t"
        "buffer_load_dwordx4 a[236:239], v97, s[20:23], s34 offen\n\t"
        "s_add_u32 s34, s34, 0x8000\n\t"
        "buffer_load_dwordx4 a[240:243], v97, s[20:23], s34 offen\n\t"
        "s_add_u32 s34, s34, 0x8000\n\t"
        "buffer_load_dwordx4 a[244:247], v97, s[20:23], s34 offen\n\t"
        "s_add_u32 s34, s34, 0x8000\n\t"
        "buffer_load_dwordx4 a[248:251], v97, s[20:23], s34 offen\n\t"
        "s_add_u32 s34, s34, 0x8000\n\t"
        "buffer_load_dwordx4 a[252:255], v97, s[20:23], s34 offen\n\t"
        "s_waitcnt lgkmcnt(0)\n\t"
        "s_waitcnt vmcnt(16)\n\t"
        "ds_write_b128 v122, a[160:163] offset:0\n\t"
        "ds_write_b128 v122, a[164:167] offset:1056\n\t"
        "ds_write_b128 v122, a[168:171] offset:2176\n\t"
        "ds_write_b128 v122, a[172:175] offset:3232\n\t"
        "ds_write_b128 v122, a[176:179] offset:4352\n\t"
        "ds_write_b128 v122, a[180:183] offset:5408\n\t"
        "ds_write_b128 v122, a[184:187] offset:6528\n\t"
        "ds_write_b128 v122, a[188:191] offset:7584\n\t"
        "ds_read_b64_tr_b16 a[160:161], v123 offset:0\n\t"
        "ds_read_b64_tr_b16 a[162:163], v123 offset:128\n\t"
        "ds_read_b64_tr_b16 a[164:165], v123 offset:4352\n\t"
        "ds_read_b64_tr_b16 a[166:167], v123 offset:4480\n\t"
        "s_waitcnt lgkmcnt(8)\n\t"
        "ds_read_b64_tr_b16 a[168:169], v123 offset:32\n\t"
        "ds_read_b64_tr_b16 a[170:171], v123 offset:160\n\t"
        "ds_read_b64_tr_b16 a[172:173], v123 offset:4384\n\t"
        "ds_read_b64_tr_b16 a[174:175], v123 offset:4512\n\t"
        "s_waitcnt lgkmcnt(8)\n\t"
        "ds_read_b64_tr_b16 a[176:177], v123 offset:512\n\t"
        "ds_read_b64_tr_b16 a[178:179], v123 offset:640\n\t"
        "ds_read_b64_tr_b16 a[180:181], v123 offset:4864\n\t"
        "ds_read_b64_tr_b16 a[182:183], v123 offset:4992\n\t"
        "s_waitcnt lgkmcnt(8)\n\t"
        "ds_read_b64_tr_b16 a[184:185], v123 offset:544\n\t"
        "ds_read_b64_tr_b16 a[186:187], v123 offset:672\n\t"
        "ds_read_b64_tr_b16 a[188:189], v123 offset:4896\n\t"
        "ds_read_b64_tr_b16 a[190:191], v123 offset:5024\n\t"
        "s_waitcnt lgkmcnt(8)\n\t"
        "s_waitcnt lgkmcnt(0)\n\t"
        "s_waitcnt vmcnt(8)\n\t"
        "ds_write_b128 v122, a[192:195] offset:0\n\t"
        "ds_write_b128 v122, a[196:199] offset:1056\n\t"
        "ds_write_b128 v122, a[200:203] offset:2176\n\t"
        "ds_write_b128 v122, a[204:207] offset:3232\n\t"
        "ds_write_b128 v122, a[208:211] offset:4352\n\t"
        "ds_write_b128 v122, a[212:215] offset:5408\n\t"
        "ds_write_b128 v122, a[216:219] offset:6528\n\t"
        "ds_write_b128 v122, a[220:223] offset:7584\n\t"
        "ds_read_b64_tr_b16 a[192:193], v123 offset:0\n\t"
        "ds_read_b64_tr_b16 a[194:195], v123 offset:128\n\t"
        "ds_read_b64_tr_b16 a[196:197], v123 offset:4352\n\t"
        "ds_read_b64_tr_b16 a[198:199], v123 offset:4480\n\t"
        "s_waitcnt lgkmcnt(8)\n\t"
        "ds_read_b64_tr_b16 a[200:201], v123 offset:32\n\t"
        "ds_read_b64_tr_b16 a[202:203], v123 offset:160\n\t"
        "ds_read_b64_tr_b16 a[204:205], v123 offset:4384\n\t"
        "ds_read_b64_tr_b16 a[206:207], v123 offset:4512\n\t"
        "s_waitcnt lgkmcnt(8)\n\t"
        "ds_read_b64_tr_b16 a[208:209], v123 offset:512\n\t"
        "ds_read_b64_tr_b16 a[210:211], v123 offset:640\n\t"
        "ds_read_b64_tr_b16 a[212:213], v123 offset:4864\n\t"
        "ds_read_b64_tr_b16 a[214:215], v123 offset:4992\n\t"
        "s_waitcnt lgkmcnt(8)\n\t"
        "ds_read_b64_tr_b16 a[216:217], v123 offset:544\n\t"
        "ds_read_b64_tr_b16 a[218:219], v123 offset:672\n\t"
        "ds_read_b64_tr_b16 a[220:221], v123 offset:4896\n\t"
        "ds_read_b64_tr_b16 a[222:223], v123 offset:5024\n\t"
        "s_waitcnt lgkmcnt(8)\n\t"
        "s_waitcnt lgkmcnt(0)\n\t"
        "s_waitcnt vmcnt(0)\n\t"
        "ds_write_b128 v122, a[224:227] offset:0\n\t"
        "ds_write_b128 v122, a[228:231] offset:1056\n\t"
        "ds_write_b128 v122, a[232:235] offset:2176\n\t"
        "ds_write_b128 v122, a[236:239] offset:3232\n\t"
        "ds_write_b128 v122, a[240:243] offset:4352\n\t"
        "ds_write_b128 v122, a[244:247] offset:5408\n\t"
        "ds_write_b128 v122, a[248:251] offset:6528\n\t"
        "ds_write_b128 v122, a[252:255] offset:7584\n\t"
        "ds_read_b64_tr_b16 a[224:225], v123 offset:0\n\t"
        "ds_read_b64_tr_b16 a[226:227], v123 offset:128\n\t"
        "ds_read_b64_tr_b16 a[228:229], v123 offset:4352\n\t"
        "ds_read_b64_tr_b16 a[230:231], v123 offset:4480\n\t"
        "s_waitcnt lgkmcnt(8)\n\t"
        "ds_read_b64_tr_b16 a[232:233], v123 offset:32\n\t"
        "ds_read_b64_tr_b16 a[234:235], v123 offset:160\n\t"
        "ds_read_b64_tr_b16 a[236:237], v123 offset:4384\n\t"
        "ds_read_b64_tr_b16 a[238:239], v123 offset:4512\n\t"
        "s_waitcnt lgkmcnt(8)\n\t"
        "ds_read_b64_tr_b16 a[240:241], v123 offset:512\n\t"
        "ds_read_b64_tr_b16 a[242:243], v123 offset:640\n\t"
        "ds_read_b64_tr_b16 a[244:245], v123 offset:4864\n\t"
        "ds_read_b64_tr_b16 a[246:247], v123 offset:4992\n\t"
        "s_waitcnt lgkmcnt(8)\n\t"
        "ds_read_b64_tr_b16 a[248:249], v123 offset:544\n\t"
        "ds_read_b64_tr_b16 a[250:251], v123 offset:672\n\t"
        "ds_read_b64_tr_b16 a[252:253], v123 offset:4896\n\t"
        "ds_read_b64_tr_b16 a[254:255], v123 offset:5024\n\t"
        "s_waitcnt lgkmcnt(8)\n\t"
        "s_mov_b32 s35, 33792\n\t"
        "buffer_load_dword v0, off, s[12:15], s35 sc1\n\t"
        "s_waitcnt lgkmcnt(0)\n\t"
        "s_waitcnt vmcnt(0)\n\t"
        "s_mov_b32 s42, 0\n\t"
        "LB_cal%=:\n\t"
        "v_readfirstlane_b32 s34, v0\n\t"
        "s_nop 0\n\t"
        "s_cmp_lg_u32 s34, 0\n\t"
        "s_cbranch_scc1 LB_calok%=\n\t"
        "s_sleep 4\n\t"
        "s_mov_b32 s35, 33792\n\t"
        "buffer_load_dword v0, off, s[12:15], s35 sc1\n\t"
        "s_waitcnt vmcnt(0)\n\t"
        "s_add_u32 s42, s42, 1\n\t"
        "s_cmp_lt_u32 s42, 0x100000\n\t"
        "s_cbranch_scc1 LB_cal%=\n\t"
        "s_movk_i32 s34, 8\n\t"
        "s_branch LB_die%=\n\t"
        "LB_calok%=:\n\t"
        "v_readfirstlane_b32 s34, v0\n\t"
        "s_nop 0\n\t"
        "s_and_b32 s34, s34, 8192\n\t"
        "s_add_u32 s8, s8, s34\n\t"
        "s_addc_u32 s9, s9, 0\n\t"
        "v_min_u32_e32 v109, -2, v109\n\t"
        "buffer_store_dword v109, v116, s[8:11], 0 offen sc1\n\t"
        "s_memrealtime s[32:33]\n\t"
        "s_waitcnt lgkmcnt(0)\n\t"
        "v_mov_b32_e32 v110, s32\n\t"
        "s_lshl_b32 s34, s29, 8\n\t"
        "s_lshl_b32 s35, s30, 20\n\t"
        "s_or_b32 s54, s34, s35\n\t"
        "s_add_u32 s34, s54, 12589056\n\t"
        "v_mov_b32_e32 v111, s34\n\t"
        "s_barrier\n\t"
        "s_cmp_lg_u32 s43, 0\n\t"
        "s_cbranch_scc1 LB_st%=\n\t"
        "v_mov_b32_e32 v2, 1\n\t"
        "s_mov_b32 s35, 34304\n\t"
        "s_mov_b64 exec, 1\n\t"
        "buffer_atomic_add v2, off, s[12:15], s35 sc0\n\t"
        "s_mov_b64 exec, -1\n\t"
        "s_waitcnt vmcnt(0)\n\t"
        "v_readfirstlane_b32 s34, v2\n\t"
        "s_nop 0\n\t"
        "s_cmp_lg_u32 s34, 95\n\t"
        "s_cbranch_scc1 LB_st%=\n\t"
        "s_memrealtime s[32:33]\n\t"
        "v_mbcnt_lo_u32_b32 v3, -1, 0\n\t"
        "v_lshlrev_b32_e32 v3, 7, v3\n\t"
        "s_waitcnt lgkmcnt(0)\n\t"
        "s_add_u32 s34, s32, 50\n\t"
        "s_or_b32 s34, s34, 1\n\t"
        "v_mov_b32_e32 v2, s34\n\t"
        "s_mov_b32 s35, 36864\n\t"
        "s_mov_b64 exec, 0xffff\n\t"
        "buffer_store_dword v2, v3, s[12:15], s35 offen sc1\n\t"
        "s_mov_b64 exec, -1\n\t"
        "LB_st%=:\n\t"
        "s_mov_b32 s24, 0\n\t"
        "s_mov_b32 s26, 0\n\t"
        "s_mov_b32 s27, 0x80000\n\t"
        "s_mov_b32 s28, 0x180000\n\t"
        "s_mov_b32 s31, 0\n\t"
        "s_mov_b32 s47, 0\n\t"
        "s_mov_b32 s38, 0xff00ff00\n\t"
        "s_mov_b32 s39, 0xff00ff00\n\t"
        "s_mov_b32 s40, 0\n\t"
        "s_mov_b32 s41, -1\n\t"
        "v_mov_b64_e32 v[0:1], 0\n\t"
        "v_mov_b64_e32 v[2:3], 0\n\t"
        "v_mov_b64_e32 v[4:5], 0\n\t"
        "v_mov_b64_e32 v[6:7], 0\n\t"
        "v_mov_b64_e32 v[8:9], 0\n\t"
        "v_mov_b64_e32 v[10:11], 0\n\t"
        "v_mov_b64_e32 v[12:13], 0\n\t"
        "v_mov_b64_e32 v[14:15], 0\n\t"
        "s_mov_b32 s42, 0\n\t"
        "LB_gw%=:\n\t"
        "buffer_load_dword v16, off, s[12:15], s45 sc1\n\t"
        "s_waitcnt vmcnt(0)\n\t"
        "v_readfirstlane_b32 s25, v16\n\t"
        "s_nop 0\n\t"
        "s_cmp_lg_u32 s25, 0\n\t"
        "s_cbranch_scc1 LB_loop%=\n\t"
        "s_sleep 2\n\t"
        "s_add_u32 s42, s42, 1\n\t"
        "s_cmp_lt_u32 s42, 0x100000\n\t"
        "s_cbranch_scc1 LB_gw%=\n\t"
        "s_movk_i32 s34, 9\n\t"
        "s_branch LB_die%=\n\t"
        ".p2align 8\n\t"
        "LB_loop%=:\n\t"
        "s_mov_b32 s42, 0\n\t"
        "s_sub_u32 s35, s25, 1\n\t"
        "LB_wa%=:\n\t"
        "s_memrealtime s[32:33]\n\t"
        "s_waitcnt lgkmcnt(0)\n\t"
        "s_sub_u32 s34, s35, s32\n\t"
        "s_cmpk_lt_u32 s34, 0x2000\n\t"
        "s_cbranch_scc1 LB_wa%=\n\t"
        "LB_rd%=:\n\t"
        "buffer_load_dwordx4 v[16:19], v112, s[8:11], s26 offen offset:0 sc1\n\t"
        "s_nop 9\n\t"
        "buffer_load_dwordx4 v[20:23], v112, s[8:11], s26 offen offset:1024 sc1\n\t"
        "s_nop 9\n\t"
        "buffer_load_dwordx4 v[24:27], v112, s[8:11], s26 offen offset:2048 sc1\n\t"
        "s_nop 9\n\t"
        "buffer_load_dwordx4 v[28:31], v112, s[8:11], s26 offen offset:3072 sc1\n\t"
        "LB_go%=:\n\t"
        "s_waitcnt vmcnt(3)\n\t"
        "v_smfmac_f32_16x16x64_bf16 v[0:3], v[16:19], v[128:135], v106\n\t"
        "v_max3_u32 v108, v16, v17, v18\n\t"
        "v_max_u32_e32 v108, v19, v108\n\t"
        "v_smfmac_f32_16x16x64_bf16 v[4:7], v[16:19], v[136:143], v106\n\t"
        "v_smfmac_f32_16x16x64_bf16 v[8:11], v[16:19], v[144:151], v106\n\t"
        "v_smfmac_f32_16x16x64_bf16 v[12:15], v[16:19], v[152:159], v106\n\t"
        "s_waitcnt vmcnt(2)\n\t"
        "v_smfmac_f32_16x16x64_bf16 v[0:3], v[20:23], v[160:167], v106\n\t"
        "v_max3_u32 v108, v20, v21, v108\n\t"
        "v_max3_u32 v108, v22, v23, v108\n\t"
        "v_smfmac_f32_16x16x64_bf16 v[4:7], v[20:23], v[168:175], v106\n\t"
        "v_smfmac_f32_16x16x64_bf16 v[8:11], v[20:23], v[176:183], v106\n\t"
        "v_smfmac_f32_16x16x64_bf16 v[12:15], v[20:23], v[184:191], v106\n\t"
        "s_waitcnt vmcnt(1)\n\t"
        "v_smfmac_f32_16x16x64_bf16 v[0:3], v[24:27], v[192:199], v106\n\t"
        "v_max3_u32 v108, v24, v25, v108\n\t"
        "v_max3_u32 v108, v26, v27, v108\n\t"
        "v_smfmac_f32_16x16x64_bf16 v[4:7], v[24:27], v[200:207], v106\n\t"
        "v_smfmac_f32_16x16x64_bf16 v[8:11], v[24:27], v[208:215], v106\n\t"
        "v_smfmac_f32_16x16x64_bf16 v[12:15], v[24:27], v[216:223], v106\n\t"
        "s_waitcnt vmcnt(0)\n\t"
        "v_smfmac_f32_16x16x64_bf16 v[0:3], v[28:31], v[224:231], v106\n\t"
        "v_max3_u32 v108, v28, v29, v108\n\t"
        "v_max3_u32 v108, v30, v31, v108\n\t"
        "v_smfmac_f32_16x16x64_bf16 v[4:7], v[28:31], v[232:239], v106\n\t"
        "v_cmp_eq_u32_e32 vcc, -1, v108\n\t"
        "v_smfmac_f32_16x16x64_bf16 v[8:11], v[28:31], v[240:247], v106\n\t"
        "v_smfmac_f32_16x16x64_bf16 v[12:15], v[28:31], v[248:255], v106\n\t"
        "s_cbranch_vccnz LB_rdfail%=\n\t"
        "s_nop 0\n\t"
        "v_add_f32_e32 v0, v0, v1\n\t"
        "v_add_f32_e32 v1, v2, v3\n\t"
        "ds_write_b64 v118, v[0:1] offset:0\n\t"
        "v_add_f32_e32 v4, v4, v5\n\t"
        "v_add_f32_e32 v5, v6, v7\n\t"
        "ds_write_b64 v118, v[4:5] offset:512\n\t"
        "v_add_f32_e32 v8, v8, v9\n\t"
        "v_add_f32_e32 v9, v10, v11\n\t"
        "ds_write_b64 v118, v[8:9] offset:1024\n\t"
        "v_add_f32_e32 v12, v12, v13\n\t"
        "v_add_f32_e32 v13, v14, v15\n\t"
        "ds_write_b64 v118, v[12:13] offset:1536\n\t"
        "buffer_store_dwordx2 v[110:111], v117, s[8:11], s26 offen sc1\n\t"
        "s_waitcnt lgkmcnt(0)\n\t"
        "s_barrier\n\t"
        "ds_read_b128 v[16:19], v119 offset:0\n\t"
        "ds_read_b128 v[20:23], v119 offset:2048\n\t"
        "ds_read_b128 v[24:27], v119 offset:4096\n\t"
        "ds_read_b128 v[28:31], v119 offset:6144\n\t"
        "buffer_load_dwordx4 v[64:67], v124, s[8:11], s28 offen sc1\n\t"
        "s_waitcnt lgkmcnt(2)\n\t"
        "v_pk_add_f32 v[16:17], v[16:17], v[20:21]\n\t"
        "v_pk_add_f32 v[18:19], v[18:19], v[22:23]\n\t"
        "s_waitcnt lgkmcnt(0)\n\t"
        "v_pk_add_f32 v[24:25], v[24:25], v[28:29]\n\t"
        "v_pk_add_f32 v[26:27], v[26:27], v[30:31]\n\t"
        "v_pk_add_f32 v[16:17], v[16:17], v[24:25]\n\t"
        "v_pk_add_f32 v[18:19], v[18:19], v[26:27]\n\t"
        "v_cvt_pk_bf16_f32 v20, v16, v17\n\t"
        "v_cvt_pk_bf16_f32 v21, v18, v19\n\t"
        "buffer_store_dwordx2 v[20:21], v114, s[8:11], s26 offen sc1\n\t"
        "s_cmp_eq_u32 s24, 63\n\t"
        "s_cbranch_scc1 LB_end%=\n\t"
        "s_memrealtime s[52:53]\n\t"
        "v_mov_b64_e32 v[24:25], -1\n\t"
        "v_mov_b64_e32 v[32:33], 0\n\t"
        "v_mov_b64_e32 v[34:35], 0\n\t"
        "v_mov_b64_e32 v[36:37], 0\n\t"
        "v_mov_b64_e32 v[38:39], 0\n\t"
        "v_mov_b64_e32 v[40:41], 0\n\t"
        "v_mov_b64_e32 v[42:43], 0\n\t"
        "v_mov_b64_e32 v[44:45], 0\n\t"
        "v_mov_b64_e32 v[46:47], 0\n\t"
        "v_mov_b64_e32 v[0:1], 0\n\t"
        "v_mov_b64_e32 v[2:3], 0\n\t"
        "v_mov_b64_e32 v[4:5], 0\n\t"
        "v_mov_b64_e32 v[6:7], 0\n\t"
        "v_mov_b64_e32 v[8:9], 0\n\t"
        "v_mov_b64_e32 v[10:11], 0\n\t"
        "v_mov_b64_e32 v[12:13], 0\n\t"
        "v_mov_b64_e32 v[14:15], 0\n\t"
        "s_add_u32 s46, s26, s69\n\t"
        "s_add_u32 s48, s26, s70\n\t"
        "s_add_u32 s73, s26, s71\n\t"
        "s_add_u32 s74, s26, s72\n\t"
        "s_sleep 1\n\t"
        "buffer_load_dwordx2 v[104:105], v113, s[8:11], s26 offen sc1\n\t"
        "buffer_store_dword v24, v116, s[8:11], s28 offen sc1\n\t"
        "s_xor_b32 s34, s26, 0x100000\n\t"
        "buffer_store_dwordx2 v[24:25], v114, s[8:11], s34 offen sc1\n\t"
        "s_add_u32 s66, s49, s43\n\t"
        "s_cmp_lg_u32 s66, 32\n\t"
        "s_cbranch_scc1 LB_norep%=\n\t"
        "s_waitcnt vmcnt(4)\n\t"
        "s_sub_u32 s66, s24, 1\n\t"
        "v_lshrrev_b32_e32 v68, 16, v64\n\t"
        "v_cmp_eq_u32_e32 vcc, s66, v68\n\t"
        "v_subrev_u32_e32 v64, s59, v64\n\t"
        "v_bfe_i32 v64, v64, 0, 16\n\t"
        "v_cndmask_b32_e32 v64, 0, v64, vcc\n\t"
        "v_lshrrev_b32_e32 v68, 16, v65\n\t"
        "v_cmp_eq_u32_e32 vcc, s66, v68\n\t"
        "v_subrev_u32_e32 v65, s59, v65\n\t"
        "v_bfe_i32 v65, v65, 0, 16\n\t"
        "v_cndmask_b32_e32 v65, 0, v65, vcc\n\t"
        "v_lshrrev_b32_e32 v68, 16, v66\n\t"
        "v_cmp_eq_u32_e32 vcc, s66, v68\n\t"
        "v_subrev_u32_e32 v66, s59, v66\n\t"
        "v_bfe_i32 v66, v66, 0, 16\n\t"
        "v_cndmask_b32_e32 v66, 0, v66, vcc\n\t"
        "v_lshrrev_b32_e32 v68, 16, v67\n\t"
        "v_cmp_eq_u32_e32 vcc, s66, v68\n\t"
        "v_subrev_u32_e32 v67, s59, v67\n\t"
        "v_bfe_i32 v67, v67, 0, 16\n\t"
        "v_cndmask_b32_e32 v67, 0, v67, vcc\n\t"
        "v_max3_i32 v64, v64, v65, v66\n\t"
        "v_max_i32_e32 v64, v64, v67\n\t"
        "s_nop 1\n\t"
        "v_max_i32_dpp v64, v64, v64 quad_perm:[1,0,3,2] row_mask:0xf bank_mask:0xf\n\t"
        "s_nop 1\n\t"
        "v_max_i32_dpp v64, v64, v64 quad_perm:[2,3,0,1] row_mask:0xf bank_mask:0xf\n\t"
        "s_nop 1\n\t"
        "v_max_i32_dpp v64, v64, v64 row_ror:4 row_mask:0xf bank_mask:0xf\n\t"
        "s_nop 1\n\t"
        "v_max_i32_dpp v64, v64, v64 row_ror:8 row_mask:0xf bank_mask:0xf\n\t"
        "v_mov_b32_e32 v65, v64\n\t"
        "s_nop 1\n\t"
        "v_permlane16_swap_b32_e32 v64, v65\n\t"
        "v_max_i32_e32 v64, v64, v65\n\t"
        "v_mov_b32_e32 v65, v64\n\t"
        "s_nop 1\n\t"
        "v_permlane32_swap_b32_e32 v64, v65\n\t"
        "v_max_i32_e32 v64, v64, v65\n\t"
        "s_nop 0\n\t"
        "v_readfirstlane_b32 s64, v64\n\t"
        "LB_norep%=:\n\t"
        "s_waitcnt vmcnt(2) lgkmcnt(0)\n\t"
        "s_mov_b32 s61, s29\n\t"
        "s_mov_b32 s62, s30\n\t"
        "s_mov_b32 s63, s31\n\t"
        "v_and_b32_e32 v30, 0xff, v105\n\t"
        "v_cmp_ne_u32_e32 vcc, s24, v30\n\t"
        "s_nop 0\n\t"
        "s_cbranch_vccnz LB_stre%=\n\t"
        "LB_stok%=:\n\t"
        "v_subrev_u32_e32 v104, s25, v104\n\t"
        "s_nop 1\n\t"
        "v_max_i32_dpp v104, v104, v104 quad_perm:[1,0,3,2] row_mask:0xf bank_mask:0xf\n\t"
        "s_nop 1\n\t"
        "v_max_i32_dpp v104, v104, v104 quad_perm:[2,3,0,1] row_mask:0xf bank_mask:0xf\n\t"
        "s_nop 1\n\t"
        "v_max_i32_dpp v104, v104, v104 row_ror:4 row_mask:0xf bank_mask:0xf\n\t"
        "s_nop 1\n\t"
        "v_max_i32_dpp v104, v104, v104 row_ror:8 row_mask:0xf bank_mask:0xf\n\t"
        "s_nop 1\n\t"
        "v_max_i32_dpp v104, v104, v104 row_bcast:15 row_mask:0xa bank_mask:0xf\n\t"
        "s_nop 1\n\t"
        "v_max_i32_dpp v104, v104, v104 row_bcast:31 row_mask:0xc bank_mask:0xf\n\t"
        "v_readfirstlane_b32 s35, v105\n\t"
        "v_readlane_b32 s36, v104, 63\n\t"
        "s_bfe_u32 s29, s35, 0xc0008\n\t"
        "s_lshr_b32 s30, s35, 20\n\t"
        "s_add_u32 s35, s31, s50\n\t"
        "s_mul_i32 s37, s50, 3\n\t"
        "s_min_u32 s35, s35, s37\n\t"
        "s_add_i32 s34, s36, s51\n\t"
        "s_cmp_gt_i32 s34, 0\n\t"
        "s_cselect_b32 s31, s35, 0\n\t"
        "s_cselect_b32 s34, s34, 0\n\t"
        "s_add_u32 s34, s34, s31\n\t"
        "s_add_u32 s37, s25, s34\n\t"
        "s_add_u32 s45, s37, s29\n\t"
        "s_add_u32 s25, s45, s30\n\t"
        "s_add_u32 s66, s49, s43\n\t"
        "s_cmp_lg_u32 s66, 32\n\t"
        "s_cbranch_scc1 LB_noctl%=\n\t"
        "s_cmp_eq_u32 s24, 0\n\t"
        "s_cbranch_scc1 LB_ctl0%=\n\t"
        "s_cmp_eq_u32 s24, 1\n\t"
        "s_cbranch_scc1 LB_ctl1%=\n\t"
        "s_add_i32 s65, s36, s62\n\t"
        "s_lshl_b32 s66, s65, 1\n\t"
        "s_sub_i32 s66, s66, s56\n\t"
        "s_cmp_gt_i32 s66, 56\n\t"
        "s_cbranch_scc1 LB_c1%=\n\t"
        "s_lshl_b32 s66, s65, 1\n\t"
        "s_add_u32 s34, s56, 8\n\t"
        "s_min_i32 s34, s34, s66\n\t"
        "s_sub_u32 s35, s56, s66\n\t"
        "s_ashr_i32 s35, s35, 2\n\t"
        "s_max_i32 s35, s35, 1\n\t"
        "s_sub_u32 s35, s56, s35\n\t"
        "s_max_i32 s56, s34, s35\n\t"
        "s_branch LB_c2%=\n\t"
        "LB_c1%=:\n\t"
        "s_add_u32 s56, s56, 2\n\t"
        "s_add_i32 s66, s64, s57\n\t"
        "s_cmp_gt_i32 s66, s61\n\t"
        "s_cbranch_scc1 LB_c2%=\n\t"
        "s_add_u32 s57, s57, 4\n\t"
        "s_min_u32 s57, s57, 24\n\t"
        "LB_c2%=:\n\t"
        "s_cmp_lg_u32 s63, 0\n\t"
        "s_cbranch_scc1 LB_c4%=\n\t"
        "s_lshl_b32 s66, s64, 1\n\t"
        "s_sub_i32 s66, s66, s55\n\t"
        "s_cmp_gt_i32 s66, 56\n\t"
        "s_cbranch_scc1 LB_c3%=\n\t"
        "s_lshl_b32 s66, s64, 1\n\t"
        "s_add_u32 s34, s55, 8\n\t"
        "s_min_i32 s34, s34, s66\n\t"
        "s_sub_u32 s35, s55, s66\n\t"
        "s_ashr_i32 s35, s35, 2\n\t"
        "s_max_i32 s35, s35, 1\n\t"
        "s_sub_u32 s35, s55, s35\n\t"
        "s_max_i32 s55, s34, s35\n\t"
        "s_branch LB_c4%=\n\t"
        "LB_c3%=:\n\t"
        "s_add_u32 s55, s55, 2\n\t"
        "s_cmp_lt_i32 s60, s58\n\t"
        "s_cbranch_scc1 LB_c4%=\n\t"
        "s_add_u32 s66, s58, 8\n\t"
        "s_cmp_gt_i32 s60, s66\n\t"
        "s_cbranch_scc1 LB_c4%=\n\t"
        "s_add_u32 s58, s58, 4\n\t"
        "s_min_u32 s58, s58, 21\n\t"
        "LB_c4%=:\n\t"
        "s_and_b32 s66, s24, 3\n\t"
        "s_cmp_eq_u32 s66, 0\n\t"
        "s_cselect_b32 s66, 1, 0\n\t"
        "s_sub_u32 s57, s57, s66\n\t"
        "s_max_u32 s57, s57, 8\n\t"
        "s_sub_u32 s58, s58, s66\n\t"
        "s_max_u32 s58, s58, 5\n\t"
        "s_add_u32 s34, s55, 1\n\t"
        "s_lshr_b32 s34, s34, 1\n\t"
        "s_add_u32 s34, s34, s57\n\t"
        "s_min_u32 s34, s34, 1000\n\t"
        "s_lshl_b32 s34, s34, 8\n\t"
        "s_add_u32 s35, s56, 1\n\t"
        "s_lshr_b32 s35, s35, 1\n\t"
        "s_add_u32 s35, s35, s58\n\t"
        "s_min_u32 s35, s35, 1000\n\t"
        "s_lshl_b32 s35, s35, 20\n\t"
        "s_or_b32 s54, s34, s35\n\t"
        "s_branch LB_ctl1%=\n\t"
        "LB_ctl0%=:\n\t"
        "s_movk_i32 s57, 8\n\t"
        "s_movk_i32 s58, 5\n\t"
        "s_sub_u32 s55, s29, 32\n\t"
        "s_lshl_b32 s55, s55, 1\n\t"
        "s_sub_u32 s56, s30, 17\n\t"
        "s_lshl_b32 s56, s56, 1\n\t"
        "LB_ctl1%=:\n\t"
        "s_mov_b32 s59, s37\n\t"
        "s_sub_u32 s60, 0, s36\n\t"
        "LB_noctl%=:\n\t"
        "s_cmp_eq_u32 s24, 0\n\t"
        "s_cselect_b32 s34, 3073, 1\n\t"
        "s_add_u32 s34, s34, s24\n\t"
        "s_add_u32 s34, s34, s54\n\t"
        "v_mov_b32_e32 v111, s34\n\t"
        "s_mov_b32 s42, 0\n\t"
        "s_sub_u32 s35, s45, 1\n\t"
        "LB_wo%=:\n\t"
        "s_memrealtime s[32:33]\n\t"
        "s_waitcnt lgkmcnt(0)\n\t"
        "s_sub_u32 s34, s35, s32\n\t"
        "s_cmpk_lt_u32 s34, 0x2000\n\t"
        "s_cbranch_scc1 LB_wo%=\n\t"
        "LB_ro%=:\n\t"
        "buffer_load_dwordx4 v[64:67], v115, s[8:11], s46 offen offset:0 sc1\n\t"
        "s_nop 12\n\t"
        "buffer_load_dwordx4 v[68:71], v115, s[8:11], s46 offen offset:1024 sc1\n\t"
        "s_nop 12\n\t"
        "buffer_load_dwordx4 v[72:75], v115, s[8:11], s48 offen offset:0 sc1\n\t"
        "s_nop 12\n\t"
        "buffer_load_dwordx4 v[76:79], v115, s[8:11], s48 offen offset:1024 sc1\n\t"
        "s_nop 12\n\t"
        "buffer_load_dwordx4 v[80:83], v115, s[8:11], s73 offen offset:0 sc1\n\t"
        "s_nop 12\n\t"
        "buffer_load_dwordx4 v[84:87], v115, s[8:11], s73 offen offset:1024 sc1\n\t"
        "s_nop 12\n\t"
        "buffer_load_dwordx4 v[88:91], v115, s[8:11], s74 offen offset:0 sc1\n\t"
        "s_nop 12\n\t"
        "buffer_load_dwordx4 v[92:95], v115, s[8:11], s74 offen offset:1024 sc1\n\t"
        "LB_go2%=:\n\t"
        "s_waitcnt vmcnt(7)\n\t"
        "v_smfmac_f32_16x16x64_bf16 v[32:35], v[64:67], a[0:7], v106\n\t"
        "v_max3_u32 v108, v64, v65, v66\n\t"
        "v_max_u32_e32 v108, v67, v108\n\t"
        "v_smfmac_f32_16x16x64_bf16 v[40:43], v[64:67], a[16:23], v106\n\t"
        "v_smfmac_f32_16x16x64_bf16 v[36:39], v[64:67], a[8:15], v106\n\t"
        "v_smfmac_f32_16x16x64_bf16 v[44:47], v[64:67], a[24:31], v106\n\t"
        "s_waitcnt vmcnt(6)\n\t"
        "v_smfmac_f32_16x16x64_bf16 v[32:35], v[68:71], a[32:39], v106\n\t"
        "v_max3_u32 v108, v68, v69, v108\n\t"
        "v_max3_u32 v108, v70, v71, v108\n\t"
        "v_smfmac_f32_16x16x64_bf16 v[40:43], v[68:71], a[48:55], v106\n\t"
        "v_smfmac_f32_16x16x64_bf16 v[36:39], v[68:71], a[40:47], v106\n\t"
        "v_smfmac_f32_16x16x64_bf16 v[44:47], v[68:71], a[56:63], v106\n\t"
        "s_waitcnt vmcnt(5)\n\t"
        "v_smfmac_f32_16x16x64_bf16 v[32:35], v[72:75], a[64:71], v106\n\t"
        "v_max3_u32 v108, v72, v73, v108\n\t"
        "v_max3_u32 v108, v74, v75, v108\n\t"
        "v_smfmac_f32_16x16x64_bf16 v[40:43], v[72:75], a[80:87], v106\n\t"
        "v_smfmac_f32_16x16x64_bf16 v[36:39], v[72:75], a[72:79], v106\n\t"
        "v_smfmac_f32_16x16x64_bf16 v[44:47], v[72:75], a[88:95], v106\n\t"
        "s_waitcnt vmcnt(4)\n\t"
        "v_smfmac_f32_16x16x64_bf16 v[32:35], v[76:79], a[96:103], v106\n\t"
        "v_max3_u32 v108, v76, v77, v108\n\t"
        "v_max3_u32 v108, v78, v79, v108\n\t"
        "v_smfmac_f32_16x16x64_bf16 v[40:43], v[76:79], a[112:119], v106\n\t"
        "v_smfmac_f32_16x16x64_bf16 v[36:39], v[76:79], a[104:111], v106\n\t"
        "v_smfmac_f32_16x16x64_bf16 v[44:47], v[76:79], a[120:127], v106\n\t"
        "s_waitcnt vmcnt(3)\n\t"
        "v_smfmac_f32_16x16x64_bf16 v[32:35], v[80:83], a[128:135], v106\n\t"
        "v_max3_u32 v108, v80, v81, v108\n\t"
        "v_max3_u32 v108, v82, v83, v108\n\t"
        "v_smfmac_f32_16x16x64_bf16 v[40:43], v[80:83], a[144:151], v106\n\t"
        "v_smfmac_f32_16x16x64_bf16 v[36:39], v[80:83], a[136:143], v106\n\t"
        "v_smfmac_f32_16x16x64_bf16 v[44:47], v[80:83], a[152:159], v106\n\t"
        "s_waitcnt vmcnt(2)\n\t"
        "v_smfmac_f32_16x16x64_bf16 v[32:35], v[84:87], a[160:167], v106\n\t"
        "v_max3_u32 v108, v84, v85, v108\n\t"
        "v_max3_u32 v108, v86, v87, v108\n\t"
        "v_smfmac_f32_16x16x64_bf16 v[40:43], v[84:87], a[176:183], v106\n\t"
        "v_smfmac_f32_16x16x64_bf16 v[36:39], v[84:87], a[168:175], v106\n\t"
        "v_smfmac_f32_16x16x64_bf16 v[44:47], v[84:87], a[184:191], v106\n\t"
        "s_waitcnt vmcnt(1)\n\t"
        "v_smfmac_f32_16x16x64_bf16 v[32:35], v[88:91], a[192:199], v106\n\t"
        "v_max3_u32 v108, v88, v89, v108\n\t"
        "v_max3_u32 v108, v90, v91, v108\n\t"
        "v_smfmac_f32_16x16x64_bf16 v[40:43], v[88:91], a[208:215], v106\n\t"
        "v_smfmac_f32_16x16x64_bf16 v[36:39], v[88:91], a[200:207], v106\n\t"
        "v_smfmac_f32_16x16x64_bf16 v[44:47], v[88:91], a[216:223], v106\n\t"
        "s_waitcnt vmcnt(0)\n\t"
        "v_smfmac_f32_16x16x64_bf16 v[32:35], v[92:95], a[224:231], v106\n\t"
        "v_max3_u32 v108, v92, v93, v108\n\t"
        "v_max3_u32 v108, v94, v95, v108\n\t"
        "v_smfmac_f32_16x16x64_bf16 v[40:43], v[92:95], a[240:247], v106\n\t"
        "v_cmp_eq_u32_e32 vcc, -1, v108\n\t"
        "v_smfmac_f32_16x16x64_bf16 v[36:39], v[92:95], a[232:239], v106\n\t"
        "v_smfmac_f32_16x16x64_bf16 v[44:47], v[92:95], a[248:255], v106\n\t"
        "s_cbranch_vccnz LB_rofail%=\n\t"
        "s_nop 0\n\t"
        "v_add_f32_e32 v32, v32, v33\n\t"
        "s_nop 1\n\t"
        "v_add_f32_e32 v40, v42, v43\n\t"
        "v_add_f32_e32 v32, v32, v40\n\t"
        "v_add_f32_e32 v36, v36, v37\n\t"
        "v_add_f32_e32 v44, v46, v47\n\t"
        "v_add_f32_e32 v36, v36, v44\n\t"
        "ds_write2_b32 v120, v32, v36 offset0:0 offset1:64\n\t"
        "s_waitcnt lgkmcnt(0)\n\t"
        "s_barrier\n\t"
        "ds_read_b64 v[64:65], v121 offset:0\n\t"
        "ds_read_b64 v[66:67], v121 offset:512\n\t"
        "ds_read_b64 v[68:69], v121 offset:1024\n\t"
        "ds_read_b64 v[70:71], v121 offset:1536\n\t"
        "s_waitcnt lgkmcnt(2)\n\t"
        "v_pk_add_f32 v[64:65], v[64:65], v[66:67]\n\t"
        "s_waitcnt lgkmcnt(0)\n\t"
        "v_pk_add_f32 v[68:69], v[68:69], v[70:71]\n\t"
        "v_pk_add_f32 v[64:65], v[64:65], v[68:69]\n\t"
        "v_cvt_pk_bf16_f32 v64, v64, v65\n\t"
        "buffer_store_dword v64, v116, s[8:11], s27 offen sc1\n\t"
        "s_memrealtime s[32:33]\n\t"
        "s_waitcnt lgkmcnt(0)\n\t"
        "v_mov_b32_e32 v110, s32\n\t"
        "s_add_u32 s24, s24, 1\n\t"
        "s_mov_b32 s28, s26\n\t"
        "s_mov_b32 s26, s27\n\t"
        "s_add_u32 s27, s27, 0x80000\n\t"
        "s_and_b32 s27, s27, 0x1fffff\n\t"
        "s_branch LB_loop%=\n\t"
        "LB_end%=:\n\t"
        "s_endpgm\n\t"
        "LB_stre%=:\n\t"
        "s_mov_b32 s42, 0\n\t"
        "LB_str2%=:\n\t"
        "buffer_load_dwordx2 v[104:105], v113, s[8:11], s26 offen sc1\n\t"
        "s_waitcnt vmcnt(0)\n\t"
        "v_and_b32_e32 v30, 0xff, v105\n\t"
        "v_cmp_ne_u32_e32 vcc, s24, v30\n\t"
        "s_nop 0\n\t"
        "s_cbranch_vccz LB_stok%=\n\t"
        "s_add_u32 s42, s42, 1\n\t"
        "s_cmp_lt_u32 s42, 0x100000\n\t"
        "s_cbranch_scc1 LB_str2%=\n\t"
        "s_movk_i32 s34, 5\n\t"
        "s_branch LB_die%=\n\t"
        "LB_rdfail%=:\n\t"
        "s_or_b32 s47, s47, 1\n\t"
        "buffer_load_dwordx4 v[16:19], v112, s[8:11], s26 offen offset:0 sc1\n\t"
        "buffer_load_dwordx4 v[20:23], v112, s[8:11], s26 offen offset:1024 sc1\n\t"
        "buffer_load_dwordx4 v[24:27], v112, s[8:11], s26 offen offset:2048 sc1\n\t"
        "buffer_load_dwordx4 v[28:31], v112, s[8:11], s26 offen offset:3072 sc1\n\t"
        "s_nop 7\n\t"
        "v_mov_b64_e32 v[0:1], 0\n\t"
        "v_mov_b64_e32 v[2:3], 0\n\t"
        "v_mov_b64_e32 v[4:5], 0\n\t"
        "v_mov_b64_e32 v[6:7], 0\n\t"
        "v_mov_b64_e32 v[8:9], 0\n\t"
        "v_mov_b64_e32 v[10:11], 0\n\t"
        "v_mov_b64_e32 v[12:13], 0\n\t"
        "v_mov_b64_e32 v[14:15], 0\n\t"
        "s_branch LB_rdre%=w\n\t"
        "LB_rdre%=:\n\t"
        "buffer_load_dwordx4 v[16:19], v112, s[8:11], s26 offen offset:0 sc1\n\t"
        "buffer_load_dwordx4 v[20:23], v112, s[8:11], s26 offen offset:1024 sc1\n\t"
        "buffer_load_dwordx4 v[24:27], v112, s[8:11], s26 offen offset:2048 sc1\n\t"
        "buffer_load_dwordx4 v[28:31], v112, s[8:11], s26 offen offset:3072 sc1\n\t"
        "LB_rdre%=w:\n\t"
        "s_waitcnt vmcnt(0) lgkmcnt(0)\n\t"
        "v_max3_u32 v108, v16, v17, v18\n\t"
        "v_max3_u32 v108, v108, v19, v20\n\t"
        "v_max3_u32 v108, v108, v21, v22\n\t"
        "v_max3_u32 v108, v108, v23, v24\n\t"
        "v_max3_u32 v108, v108, v25, v26\n\t"
        "v_max3_u32 v108, v108, v27, v28\n\t"
        "v_max3_u32 v108, v108, v29, v30\n\t"
        "v_max_u32_e32 v108, v108, v31\n\t"
        "v_cmp_eq_u32_e32 vcc, -1, v108\n\t"
        "s_nop 0\n\t"
        "s_cbranch_vccz LB_go%=\n\t"
        "s_add_u32 s42, s42, 1\n\t"
        "s_cmp_lt_u32 s42, 0x100000\n\t"
        "s_cbranch_scc1 LB_rdre%=\n\t"
        "s_movk_i32 s34, 3\n\t"
        "s_branch LB_die%=\n\t"
        "LB_rofail%=:\n\t"
        "s_or_b32 s47, s47, 4\n\t"
        "buffer_load_dwordx4 v[64:67], v115, s[8:11], s46 offen offset:0 sc1\n\t"
        "buffer_load_dwordx4 v[68:71], v115, s[8:11], s46 offen offset:1024 sc1\n\t"
        "buffer_load_dwordx4 v[72:75], v115, s[8:11], s48 offen offset:0 sc1\n\t"
        "buffer_load_dwordx4 v[76:79], v115, s[8:11], s48 offen offset:1024 sc1\n\t"
        "buffer_load_dwordx4 v[80:83], v115, s[8:11], s73 offen offset:0 sc1\n\t"
        "buffer_load_dwordx4 v[84:87], v115, s[8:11], s73 offen offset:1024 sc1\n\t"
        "buffer_load_dwordx4 v[88:91], v115, s[8:11], s74 offen offset:0 sc1\n\t"
        "buffer_load_dwordx4 v[92:95], v115, s[8:11], s74 offen offset:1024 sc1\n\t"
        "s_nop 7\n\t"
        "v_mov_b64_e32 v[32:33], 0\n\t"
        "v_mov_b64_e32 v[34:35], 0\n\t"
        "v_mov_b64_e32 v[36:37], 0\n\t"
        "v_mov_b64_e32 v[38:39], 0\n\t"
        "v_mov_b64_e32 v[40:41], 0\n\t"
        "v_mov_b64_e32 v[42:43], 0\n\t"
        "v_mov_b64_e32 v[44:45], 0\n\t"
        "v_mov_b64_e32 v[46:47], 0\n\t"
        "s_branch LB_rore%=w\n\t"
        "LB_rore%=:\n\t"
        "buffer_load_dwordx4 v[64:67], v115, s[8:11], s46 offen offset:0 sc1\n\t"
        "buffer_load_dwordx4 v[68:71], v115, s[8:11], s46 offen offset:1024 sc1\n\t"
        "buffer_load_dwordx4 v[72:75], v115, s[8:11], s48 offen offset:0 sc1\n\t"
        "buffer_load_dwordx4 v[76:79], v115, s[8:11], s48 offen offset:1024 sc1\n\t"
        "buffer_load_dwordx4 v[80:83], v115, s[8:11], s73 offen offset:0 sc1\n\t"
        "buffer_load_dwordx4 v[84:87], v115, s[8:11], s73 offen offset:1024 sc1\n\t"
        "buffer_load_dwordx4 v[88:91], v115, s[8:11], s74 offen offset:0 sc1\n\t"
        "buffer_load_dwordx4 v[92:95], v115, s[8:11], s74 offen offset:1024 sc1\n\t"
        "LB_rore%=w:\n\t"
        "s_waitcnt vmcnt(0) lgkmcnt(0)\n\t"
        "v_max3_u32 v108, v64, v65, v66\n\t"
        "v_max3_u32 v108, v108, v67, v68\n\t"
        "v_max3_u32 v108, v108, v69, v70\n\t"
        "v_max3_u32 v108, v108, v71, v72\n\t"
        "v_max3_u32 v108, v108, v73, v74\n\t"
        "v_max3_u32 v108, v108, v75, v76\n\t"
        "v_max3_u32 v108, v108, v77, v78\n\t"
        "v_max3_u32 v108, v108, v79, v80\n\t"
        "v_max3_u32 v108, v108, v81, v82\n\t"
        "v_max3_u32 v108, v108, v83, v84\n\t"
        "v_max3_u32 v108, v108, v85, v86\n\t"
        "v_max3_u32 v108, v108, v87, v88\n\t"
        "v_max3_u32 v108, v108, v89, v90\n\t"
        "v_max3_u32 v108, v108, v91, v92\n\t"
        "v_max3_u32 v108, v108, v93, v94\n\t"
        "v_max_u32_e32 v108, v108, v95\n\t"
        "v_cmp_eq_u32_e32 vcc, -1, v108\n\t"
        "s_nop 0\n\t"
        "s_cbranch_vccz LB_go2%=\n\t"
        "s_add_u32 s42, s42, 1\n\t"
        "s_cmp_lt_u32 s42, 0x100000\n\t"
        "s_cbranch_scc1 LB_rore%=\n\t"
        "s_movk_i32 s34, 4\n\t"
        "s_branch LB_die%=\n\t"
        "LB_die%=:\n\t"
        "v_mov_b32_e32 v30, s34\n\t"
        "s_mov_b32 s35, 33024\n\t"
        "s_mov_b64 exec, 1\n\t"
        "buffer_store_dword v30, off, s[12:15], s35 sc1\n\t"
        "s_endpgm\n\t"
        ".Lk_end_B:\n\t"
        :: REC_S_IN, "{v96}"(g_v), "{v97}"(g_o), "{v98}"(wb0), "{v99}"(wb1), "{v100}"(rb0), "{v101}"(rb0 ^ 32u), "{v102}"(rb0 ^ 64u), "{v103}"(rb0 ^ 96u),
           "{v106}"((l & 1) ? 0xEEEEEEEEu : 0x44444444u), "{v[108:109]}"(m0p), "{s68}"(rot),
           "{v112}"(a_mem), "{v113}"((24 + cp) * 16384 + l * ST_P), "{v114}"(a_vmw), "{v115}"(a_or), "{v116}"(a_mw), "{v117}"(a_sw),
           "{v118}"(lb + w * 2048 + (g >> 1) * 256 + n * 16 + (g & 1) * 8), "{v119}"(lb + (w & 1) * 1024 + l * 16), "{v120}"(lb + LB_R + w * 512 + l * 4),
           "{v121}"(lb + LB_R + l * 8), "{v122}"(stg + l * 16), "{v123}"(o_r), "{v124}"(rb + w == 32 ? 28 * 16384 + (l & 31) * 128 : OOB)
        : "memory", "vcc", "scc", "v0", "v255", "a0", "a255", "s101");
    __builtin_unreachable();
}
__device__ __forceinline__ void rec_helper(const Params& p, rsrc_t r, unsigned char* lds, const int wv, const int hi) {
    const int tid = otid(), l = tid & 63;
    const rsrc_t rh = __builtin_amdgcn_make_buffer_rsrc(p.ws + hx_base(p.ws), 0, HX_BYTES, 0x00020000);
    const v4u ones = {0xFFFFFFFFu, 0xFFFFFFFFu, 0xFFFFFFFFu, 0xFFFFFFFFu};
    unsigned best0 = ~0u, best1 = ~0u, d[5] = {0u, 0u, 0u, 0u, 0u};
    const bool meas = (hi & 7) < 2;
    if (meas) {
        const unsigned ca = l >= 8 ? OOB : (31 + wv * 32) * 16384 + hi * 256 + l * 16;
        const unsigned tend = rtnow() + CAL_T;
#pragma unroll 1
        for (int i = 0; i < 64 && (i < 3 || (int)(rtnow() - tend) < 0); ++i) {
            const unsigned t0 = (unsigned)__builtin_amdgcn_s_memtime(); __builtin_amdgcn_s_waitcnt(0);
            st16(rh, ca, ones); DRAIN();
            const unsigned t1 = (unsigned)__builtin_amdgcn_s_memtime(); __builtin_amdgcn_s_waitcnt(0);
            st16(rh, ca + 8192, ones); DRAIN();
            const unsigned t2 = (unsigned)__builtin_amdgcn_s_memtime(); __builtin_amdgcn_s_waitcnt(0);
            best0 = min(best0, t1 - t0); best1 = min(best1, t2 - t1); }
        if (l < 2) __hip_atomic_fetch_max((unsigned*)(p.ws + F_CAL + 128) + l, 65535u - min(l ? best1 : best0, 65535u), __ATOMIC_RELAXED, __HIP_MEMORY_SCOPE_AGENT);
    } else if (TOUCH) {
        const int xq = hi >> 3, k = ((hi & 7) - 2) * 4 + wv;
#pragma unroll
        for (int i = 0; i < 5; ++i) { const bf16_t* q;
            if (i & 1) { const int z = (i >> 1) * 24 + k, st = z & 3, c = 4 + (z >> 2);
                q = ((st & 1) ? p.Wk : p.Wq) + (size_t)(((((st & 1) * 2 + 1) * 4 + c) & 15) * 64 + l) * 4096 + (xq * 8 + 1 + (st >> 1) * 4) * 128 + 64; }
            else { const int z = min((i >> 1) * 24 + k, 63);
                q = p.Wo + (size_t)(((z & 3) * 8 + (z >> 3)) * 128 + ((z >> 2) & 1) * 64 + l) * 1024 + (RPL == 4 ? (xq >> 1) : (xq & 1)) * 512 + 192; if (RPL == 4 && (xq & 1)) q = p.gate_w; }
            asm volatile("global_load_dword %0, %1, off" : "+v"(d[i]) : "v"(q) : "memory"); }
    }
    auto more = [&]() {
        const int xq = hi >> 3, k = (hi & 7) * 4 + wv;
#pragma unroll 1
        for (int z = k; z < 56 * T2U; z += 32) { const int s7 = z % 7, rr = z / 7;
            const bf16_t* q = p.Wo + (size_t)(((rr & 3) * 8 + 8 - T2U + (rr >> 3)) * 128 + ((rr >> 2) & 1) * 64 + l) * 1024 + (xq & 1) * 512 + (s7 + (s7 >= 3)) * 64;
            asm volatile("global_load_dword %0, %1, off" : "+v"(d[0]) : "v"(q) : "memory"); }
#pragma unroll 1
        for (int z = k; z < 28 * T2A; z += 32) { const int st = z % 28, c = 16 - T2A + z / 28, s2 = st + (st >= 5) + (st >= 6) + (st >= 19) + (st >= 20);
            const bf16_t* q = ((s2 & 2) ? p.Wk : p.Wq) + (size_t)((((s2 & 3) * 4 + c) & 15) * 64 + l) * 4096 + (xq * 8 + (s2 >> 2)) * 128 + (s2 & 1) * 64;
            asm volatile("global_load_dword %0, %1, off" : "+v"(d[0]) : "v"(q) : "memory"); } };
    if (T2F) more();
    st16(r, tid < 64 ? F_KVQ + hi * 1024 + tid * 16 : tid < 72 ? F_PROG + hi * 128 + (tid - 64) * 16 : tid < 80 ? F_CVY + hi * 128 + (tid - 72) * 16 : OOB, (v4u){0u, 0u, 0u, 0u});
#pragma unroll
    for (int i = 0; i < 2; ++i) st16(r, W_OX + 63 * 262144 + hi * 8192 + i * 4096 + tid * 16, ones);
#pragma unroll
    for (int i = 0; i < 16; ++i) st16(rh, hi * 65536 + i * 4096 + tid * 16, ones);
    asm volatile("s_waitcnt vmcnt(0)" : "+v"(d[0]), "+v"(d[1]), "+v"(d[2]), "+v"(d[3]), "+v"(d[4]) :: "memory");
    __syncthreads();
    if (tid == 0 && __hip_atomic_fetch_add((unsigned*)(p.ws + F_CAL + 256), 1u, __ATOMIC_RELAXED, __HIP_MEMORY_SCOPE_AGENT) == 31u) {
        const v2u c = ld8(r, F_CAL + 128); st4(r, F_CAL, c.x < c.y ? 8193u : 1u); }
    if (!T2F) more();
    asm volatile("s_waitcnt vmcnt(0)" : "+v"(d[0]) :: "memory");
}
using f32x16 = __attribute__((ext_vector_type(16))) float;
constexpr int CV_RP = 80;
constexpr int CV_U = 96 * CV_RP;
constexpr int CV_KP = 4416;
constexpr int CV_K0 = 8 * CV_U;
__device__ __forceinline__ float blo(unsigned x) { return __uint_as_float(x << 16); }
__device__ __forceinline__ float bhi(unsigned x) { return __uint_as_float(x & 0xFFFF0000u); }
#define NAP() do { __builtin_amdgcn_sched_barrier(0); __builtin_amdgcn_s_sleep(XSLP); __builtin_amdgcn_sched_barrier(0); } while (0)
template <int NT>
__device__ __forceinline__ void gate_block(const Params& p, rsrc_t r, const int hi, const int wv) {
    const int l = otid() & 63, t0 = (hi * 4 + wv) * NT;
    v4u xg[NT][2], gw[2];
#pragma unroll
    for (int q = 0; q < 2; ++q) gw[q] = *(const v4u*)(p.gate_w + q * 512 + l * 8);
#pragma unroll
    for (int i = 0; i < NT; ++i)
#pragma unroll
        for (int q = 0; q < 2; ++q) { xg[i][q] = *(const v4u*)(p.Aq + (size_t)(t0 + i) * D + q * 512 + l * 8); NAP(); }
    float mine = 0.f;
#pragma unroll
    for (int i = 0; i < NT; ++i) { float acc = 0.f;
#pragma unroll
        for (int q = 0; q < 2; ++q)
#pragma unroll
            for (int e = 0; e < 4; ++e) { const unsigned pr = pack2(blo(xg[i][q][e]) * blo(gw[q][e]), bhi(xg[i][q][e]) * bhi(gw[q][e])); acc += blo(pr); acc += bhi(pr); }
        for (int o = 32; o > 0; o >>= 1) acc += __shfl_xor(acc, o);
        if (l == i) mine = acc; }
    if (l < NT) { const float z = rbfh(mine); st4(r, W_GATE + (t0 + l) * 4, __float_as_uint(rbfh(1.f / (1.f + expf(-z))))); }
}
__device__ __forceinline__ void conv_prep(const Params& p, rsrc_t r, const int wv, const int hi, const int G0, const int G1) {
    const int tid = otid(), Q = tid >> 2, cg = tid & 3, ch0 = hi * 32 + cg * 8;
    v4u sk[4]; float sv[8][4];
#pragma unroll
    for (int q = 0; q < 4; ++q) sk[q] = *(const v4u*)(p.shift_k + ch0 * 4 + q * 8);
#pragma unroll
    for (int c8 = 0; c8 < 8; ++c8)
#pragma unroll
        for (int tp = 0; tp < 4; ++tp) { const unsigned sw = sk[c8 >> 1][(c8 & 1) * 2 + (tp >> 1)]; sv[c8][tp] = (tp & 1) ? bhi(sw) : blo(sw); }
    const rsrc_t rk = __builtin_amdgcn_make_buffer_rsrc((void*)p.Ak, 0, S * D * 2, 0x00020000), rv = __builtin_amdgcn_make_buffer_rsrc((void*)p.Av, 0, S * D * 2, 0x00020000),
                 rq = __builtin_amdgcn_make_buffer_rsrc((void*)p.Aq, 0, S * D * 2, 0x00020000);
#pragma unroll 1
    for (int G = G0; G < G1; ++G) {
        const unsigned ro = (unsigned)((Q * 32 + G * 8) * D + ch0) * 2u, uo = hi * 131072 + tid * 16;
        v4u kr[11], u[8], qv[8];
#pragma unroll
        for (int i = 0; i < 11; ++i) { kr[i] = __builtin_amdgcn_raw_buffer_load_b128(rk, (i < 3 && Q + G == 0) ? OOB : ro + (i - 3) * 2048, 0, 0); NAP(); }
#pragma unroll
        for (int i = 0; i < 8; ++i) { u[i] = __builtin_amdgcn_raw_buffer_load_b128(rv, ro + i * 2048, 0, 0); NAP(); }
#pragma unroll
        for (int i = 0; i < 8; ++i) { qv[i] = __builtin_amdgcn_raw_buffer_load_b128(rq, ro + i * 2048, 0, 0); NAP(); }
#pragma unroll
        for (int q = 0; q < 4; ++q) {
#pragma unroll
            for (int i = 0; i < 8; ++i) {
                unsigned ks = pack2(blo(kr[i][q]) * sv[2 * q][3], bhi(kr[i][q]) * sv[2 * q + 1][3]), pr;
#pragma unroll
                for (int d = 1; d < 4; ++d) { pr = pack2(blo(kr[i + d][q]) * sv[2 * q][3 - d], bhi(kr[i + d][q]) * sv[2 * q + 1][3 - d]); ks = pack2(blo(ks) + blo(pr), bhi(ks) + bhi(pr)); }
                u[i][q] = pack2(blo(ks) * blo(u[i][q]), bhi(ks) * bhi(u[i][q])); }
            v4u lo, hh;
#pragma unroll
            for (int z = 0; z < 4; ++z) { const unsigned x = u[7 - 2 * z][q], y = u[6 - 2 * z][q]; lo[z] = __builtin_amdgcn_perm(y, x, 0x05040100u); hh[z] = __builtin_amdgcn_perm(y, x, 0x07060302u); }
            st16(r, W_U + uo + q * 32768 + G * 4096, lo); NAP(); st16(r, W_U + uo + q * 32768 + 16384 + G * 4096, hh); NAP();
#pragma unroll
            for (int z = 0; z < 2; ++z) { st16(r, W_Y + uo + q * 32768 + (2 * G + z) * 4096, (v4u){qv[4 * z][q], qv[4 * z + 1][q], qv[4 * z + 2][q], qv[4 * z + 3][q]}); NAP(); }
        }
    }
    DRAIN(); __syncthreads();
    if (tid == 0) __hip_atomic_fetch_add((unsigned*)(p.ws + F_CONV), 1u, __ATOMIC_RELAXED, __HIP_MEMORY_SCOPE_AGENT);
}
__device__ __forceinline__ bool conv_core(const Params& p, rsrc_t r, unsigned char* lds, const int wv, const int un) {
    const int tid = otid(), w = wv, l = tid & 63, Q = tid >> 2, cg = tid & 3;
    const rsrc_t rs = __builtin_amdgcn_make_buffer_rsrc((void*)p.ssm_k, 0, S * D * 2, 0x00020000);
    v4u kraw[4]; v2u kext[4];
    auto kload = [&](int n) { const unsigned o = (unsigned)(((un >> 2) * 32 + w * 8 + (un & 3) * 2 + n) * S + l * 8) * 2u;
#pragma unroll
        for (int i = 0; i < 4; ++i) { kraw[i] = __builtin_amdgcn_raw_buffer_load_b128(rs, o + i * 1024, 0, CV_POL);
            kext[i] = __builtin_amdgcn_raw_buffer_load_b64(rs, (i == 3 && l == 63) ? OOB : o + i * 1024 + 16, 0, 0); } };
    kload(0);
    if (!wait_word(r, F_CONV, XG ? 128u : 32u, 2048u)) return false;
    v4u ur[8], qr[8];
#pragma unroll
    for (int i = 0; i < 8; ++i) ur[i] = ld16(r, W_U + un * 32768 + i * 4096 + tid * 16);
#pragma unroll
    for (int i = 0; i < 8; ++i) qr[i] = ld16(r, W_Y + un * 32768 + i * 4096 + tid * 16);
    const unsigned dv = *(const unsigned*)(p.ssm_D + (un >> 2) * 32 + cg * 8 + (un & 3) * 2);
    for (int i = tid; i < 8 * 160; i += 256) *(v4u*)(lds + (i / 160) * CV_U + (i % 160) * 16) = (v4u){0u, 0u, 0u, 0u};
    unsigned char* kc = lds + CV_K0 + w * 4 * CV_KP;
    if (l < 32) {
#pragma unroll
        for (int q = 0; q < 4; ++q) *(unsigned*)(kc + q * CV_KP + l * 4) = 0u; }
    const int b = l & 31, kg = l >> 5;
    const unsigned kb = (unsigned)(size_t)kc + (b & 3) * CV_KP + ((b & 28) - 8 * kg + 56 - 16) * 2, ub0 = (unsigned)(size_t)lds + (1 + b) * CV_RP + kg * 16;
    unsigned char* const urow = lds + 2 * cg * CV_U + (32 + Q) * CV_RP;
#pragma unroll
    for (int i = 0; i < 8; ++i) *(v4u*)(urow + (i >> 2) * CV_U + (i & 3) * 16) = ur[i];
    __syncthreads();
#pragma unroll 1
        for (int ci = 0; ci < 2; ++ci) {
            const int c8 = 2 * w + ci;
#pragma unroll
            for (int i = 0; i < 4; ++i) { const v4u e = kraw[i]; const v2u f = kext[i]; unsigned char* q = kc + 128 + (i * 64 + l) * 16;
                const unsigned s0 = __builtin_amdgcn_alignbit(e.y, e.x, 16), s1 = __builtin_amdgcn_alignbit(e.z, e.y, 16), s2 = __builtin_amdgcn_alignbit(e.w, e.z, 16),
                               s3 = __builtin_amdgcn_alignbit(f.x, e.w, 16), s4 = __builtin_amdgcn_alignbit(f.y, f.x, 16);
                *(v4u*)q = (v4u){s0, s1, s2, s3}; *(v4u*)(q + CV_KP) = (v4u){e.y, e.z, e.w, f.x}; *(v4u*)(q + 2 * CV_KP) = (v4u){s1, s2, s3, s4}; *(v4u*)(q + 3 * CV_KP) = (v4u){e.z, e.w, f.x, f.y};
                if (i == 0 && l == 0) {
                    *(v2u*)(q - 8) = (v2u){0u, e.x << 16}; *(v2u*)(q + CV_KP - 8) = (v2u){0u, e.x}; *(v2u*)(q + 2 * CV_KP - 8) = (v2u){e.x << 16, s0}; *(v2u*)(q + 3 * CV_KP - 8) = (v2u){e.x, e.y}; } }
            if (ci == 0) kload(1);
            __builtin_amdgcn_wave_barrier();
            f32x16 acc0, acc1;
            asm volatile(
                "ds_read_b64 v[192:193], %[kb] offset:32\n\t"
                "ds_read_b64 v[194:195], %[kb] offset:40\n\t"
                "ds_read_b128 v[196:199], %[ub] offset:5040\n\t"
                "ds_read_b128 v[200:203], %[ub] offset:2480\n\t"
                "ds_read_b64 v[204:205], %[kb] offset:2080\n\t"
                "ds_read_b64 v[206:207], %[kb] offset:2088\n\t"
                "ds_read_b64 v[208:209], %[kb] offset:0\n\t"
                "ds_read_b64 v[210:211], %[kb] offset:8\n\t"
                "ds_read_b128 v[212:215], %[ub] offset:5072\n\t"
                "ds_read_b128 v[216:219], %[ub] offset:2512\n\t"
                "ds_read_b64 v[220:221], %[kb] offset:2048\n\t"
                "ds_read_b64 v[222:223], %[kb] offset:2056\n\t"
                "s_waitcnt lgkmcnt(9)\n\t"
                "v_mfma_f32_32x32x16_bf16 %[a1], v[192:195], v[196:199], 0\n\t"
                "ds_read_b64 v[224:225], %[kb] offset:96\n\t"
                "ds_read_b64 v[226:227], %[kb] offset:104\n\t"
                "ds_read_b128 v[228:231], %[ub] offset:4960\n\t"
                "s_waitcnt lgkmcnt(11)\n\t"
                "v_mfma_f32_32x32x16_bf16 %[a0], v[192:195], v[200:203], 0\n\t"
                "ds_read_b128 v[232:235], %[ub] offset:2400\n\t"
                "s_waitcnt lgkmcnt(10)\n\t"
                "v_mfma_f32_32x32x16_bf16 %[a1], v[204:207], v[200:203], %[a1]\n\t"
                "ds_read_b64 v[236:237], %[kb] offset:2144\n\t"
                "ds_read_b64 v[238:239], %[kb] offset:2152\n\t"
                "s_waitcnt lgkmcnt(9)\n\t"
                "v_mfma_f32_32x32x16_bf16 %[a1], v[208:211], v[212:215], %[a1]\n\t"
                "ds_read_b64 v[240:241], %[kb] offset:64\n\t"
                "ds_read_b64 v[242:243], %[kb] offset:72\n\t"
                "ds_read_b128 v[244:247], %[ub] offset:4992\n\t"
                "s_waitcnt lgkmcnt(11)\n\t"
                "v_mfma_f32_32x32x16_bf16 %[a0], v[208:211], v[216:219], %[a0]\n\t"
                "ds_read_b128 v[248:251], %[ub] offset:2432\n\t"
                "s_waitcnt lgkmcnt(10)\n\t"
                "v_mfma_f32_32x32x16_bf16 %[a1], v[220:223], v[216:219], %[a1]\n\t"
                "ds_read_b64 v[252:253], %[kb] offset:2112\n\t"
                "ds_read_b64 v[254:255], %[kb] offset:2120\n\t"
                "s_waitcnt lgkmcnt(9)\n\t"
                "v_mfma_f32_32x32x16_bf16 %[a1], v[224:227], v[228:231], %[a1]\n\t"
                "ds_read_b64 v[192:193], %[kb] offset:160\n\t"
                "ds_read_b64 v[194:195], %[kb] offset:168\n\t"
                "ds_read_b128 v[196:199], %[ub] offset:4880\n\t"
                "s_waitcnt lgkmcnt(11)\n\t"
                "v_mfma_f32_32x32x16_bf16 %[a0], v[224:227], v[232:235], %[a0]\n\t"
                "ds_read_b128 v[200:203], %[ub] offset:2320\n\t"
                "s_waitcnt lgkmcnt(10)\n\t"
                "v_mfma_f32_32x32x16_bf16 %[a1], v[236:239], v[232:235], %[a1]\n\t"
                "ds_read_b64 v[204:205], %[kb] offset:2208\n\t"
                "ds_read_b64 v[206:207], %[kb] offset:2216\n\t"
                "s_waitcnt lgkmcnt(9)\n\t"
                "v_mfma_f32_32x32x16_bf16 %[a1], v[240:243], v[244:247], %[a1]\n\t"
                "ds_read_b64 v[208:209], %[kb] offset:128\n\t"
                "ds_read_b64 v[210:211], %[kb] offset:136\n\t"
                "ds_read_b128 v[212:215], %[ub] offset:4912\n\t"
                "s_waitcnt lgkmcnt(11)\n\t"
                "v_mfma_f32_32x32x16_bf16 %[a0], v[240:243], v[248:251], %[a0]\n\t"
                "ds_read_b128 v[216:219], %[ub] offset:2352\n\t"
                "s_waitcnt lgkmcnt(10)\n\t"
                "v_mfma_f32_32x32x16_bf16 %[a1], v[252:255], v[248:251], %[a1]\n\t"
                "ds_read_b64 v[220:221], %[kb] offset:2176\n\t"
                "ds_read_b64 v[222:223], %[kb] offset:2184\n\t"
                "s_waitcnt lgkmcnt(9)\n\t"
                "v_mfma_f32_32x32x16_bf16 %[a1], v[192:195], v[196:199], %[a1]\n\t"
                "ds_read_b64 v[224:225], %[kb] offset:224\n\t"
                "ds_read_b64 v[226:227], %[kb] offset:232\n\t"
                "ds_read_b128 v[228:231], %[ub] offset:4800\n\t"
                "s_waitcnt lgkmcnt(11)\n\t"
                "v_mfma_f32_32x32x16_bf16 %[a0], v[192:195], v[200:203], %[a0]\n\t"
                "ds_read_b128 v[232:235], %[ub] offset:2240\n\t"
                "s_waitcnt lgkmcnt(10)\n\t"
                "v_mfma_f32_32x32x16_bf16 %[a1], v[204:207], v[200:203], %[a1]\n\t"
                "ds_read_b64 v[236:237], %[kb] offset:2272\n\t"
                "ds_read_b64 v[238:239], %[kb] offset:2280\n\t"
                "s_waitcnt lgkmcnt(9)\n\t"
                "v_mfma_f32_32x32x16_bf16 %[a1], v[208:211], v[212:215], %[a1]\n\t"
                "ds_read_b64 v[240:241], %[kb] offset:192\n\t"
                "ds_read_b64 v[242:243], %[kb] offset:200\n\t"
                "ds_read_b128 v[244:247], %[ub] offset:4832\n\t"
                "s_waitcnt lgkmcnt(11)\n\t"
                "v_mfma_f32_32x32x16_bf16 %[a0], v[208:211], v[216:219], %[a0]\n\t"
                "ds_read_b128 v[248:251], %[ub] offset:2272\n\t"
                "s_waitcnt lgkmcnt(10)\n\t"
                "v_mfma_f32_32x32x16_bf16 %[a1], v[220:223], v[216:219], %[a1]\n\t"
                "ds_read_b64 v[252:253], %[kb] offset:2240\n\t"
                "ds_read_b64 v[254:255], %[kb] offset:2248\n\t"
                "s_waitcnt lgkmcnt(9)\n\t"
                "v_mfma_f32_32x32x16_bf16 %[a1], v[224:227], v[228:231], %[a1]\n\t"
                "ds_read_b64 v[192:193], %[kb] offset:288\n\t"
                "ds_read_b64 v[194:195], %[kb] offset:296\n\t"
                "ds_read_b128 v[196:199], %[ub] offset:4720\n\t"
                "s_waitcnt lgkmcnt(11)\n\t"
                "v_mfma_f32_32x32x16_bf16 %[a0], v[224:227], v[232:235], %[a0]\n\t"
                "ds_read_b128 v[200:203], %[ub] offset:2160\n\t"
                "s_waitcnt lgkmcnt(10)\n\t"
                "v_mfma_f32_32x32x16_bf16 %[a1], v[236:239], v[232:235], %[a1]\n\t"
                "ds_read_b64 v[204:205], %[kb] offset:2336\n\t"
                "ds_read_b64 v[206:207], %[kb] offset:2344\n\t"
                "s_waitcnt lgkmcnt(9)\n\t"
                "v_mfma_f32_32x32x16_bf16 %[a1], v[240:243], v[244:247], %[a1]\n\t"
                "ds_read_b64 v[208:209], %[kb] offset:256\n\t"
                "ds_read_b64 v[210:211], %[kb] offset:264\n\t"
                "ds_read_b128 v[212:215], %[ub] offset:4752\n\t"
                "s_waitcnt lgkmcnt(11)\n\t"
                "v_mfma_f32_32x32x16_bf16 %[a0], v[240:243], v[248:251], %[a0]\n\t"
                "ds_read_b128 v[216:219], %[ub] offset:2192\n\t"
                "s_waitcnt lgkmcnt(10)\n\t"
                "v_mfma_f32_32x32x16_bf16 %[a1], v[252:255], v[248:251], %[a1]\n\t"
                "ds_read_b64 v[220:221], %[kb] offset:2304\n\t"
                "ds_read_b64 v[222:223], %[kb] offset:2312\n\t"
                "s_waitcnt lgkmcnt(9)\n\t"
                "v_mfma_f32_32x32x16_bf16 %[a1], v[192:195], v[196:199], %[a1]\n\t"
                "ds_read_b64 v[224:225], %[kb] offset:352\n\t"
                "ds_read_b64 v[226:227], %[kb] offset:360\n\t"
                "ds_read_b128 v[228:231], %[ub] offset:4640\n\t"
                "s_waitcnt lgkmcnt(11)\n\t"
                "v_mfma_f32_32x32x16_bf16 %[a0], v[192:195], v[200:203], %[a0]\n\t"
                "ds_read_b128 v[232:235], %[ub] offset:2080\n\t"
                "s_waitcnt lgkmcnt(10)\n\t"
                "v_mfma_f32_32x32x16_bf16 %[a1], v[204:207], v[200:203], %[a1]\n\t"
                "ds_read_b64 v[236:237], %[kb] offset:2400\n\t"
                "ds_read_b64 v[238:239], %[kb] offset:2408\n\t"
                "s_waitcnt lgkmcnt(9)\n\t"
                "v_mfma_f32_32x32x16_bf16 %[a1], v[208:211], v[212:215], %[a1]\n\t"
                "ds_read_b64 v[240:241], %[kb] offset:320\n\t"
                "ds_read_b64 v[242:243], %[kb] offset:328\n\t"
                "ds_read_b128 v[244:247], %[ub] offset:4672\n\t"
                "s_waitcnt lgkmcnt(11)\n\t"
                "v_mfma_f32_32x32x16_bf16 %[a0], v[208:211], v[216:219], %[a0]\n\t"
                "ds_read_b128 v[248:251], %[ub] offset:2112\n\t"
                "s_waitcnt lgkmcnt(10)\n\t"
                "v_mfma_f32_32x32x16_bf16 %[a1], v[220:223], v[216:219], %[a1]\n\t"
                "ds_read_b64 v[252:253], %[kb] offset:2368\n\t"
                "ds_read_b64 v[254:255], %[kb] offset:2376\n\t"
                "s_waitcnt lgkmcnt(9)\n\t"
                "v_mfma_f32_32x32x16_bf16 %[a1], v[224:227], v[228:231], %[a1]\n\t"
                "ds_read_b64 v[192:193], %[kb] offset:416\n\t"
                "ds_read_b64 v[194:195], %[kb] offset:424\n\t"
                "ds_read_b128 v[196:199], %[ub] offset:4560\n\t"
                "s_waitcnt lgkmcnt(11)\n\t"
                "v_mfma_f32_32x32x16_bf16 %[a0], v[224:227], v[232:235], %[a0]\n\t"
                "ds_read_b128 v[200:203], %[ub] offset:2000\n\t"
                "s_waitcnt lgkmcnt(10)\n\t"
                "v_mfma_f32_32x32x16_bf16 %[a1], v[236:239], v[232:235], %[a1]\n\t"
                "ds_read_b64 v[204:205], %[kb] offset:2464\n\t"
                "ds_read_b64 v[206:207], %[kb] offset:2472\n\t"
                "s_waitcnt lgkmcnt(9)\n\t"
                "v_mfma_f32_32x32x16_bf16 %[a1], v[240:243], v[244:247], %[a1]\n\t"
                "ds_read_b64 v[208:209], %[kb] offset:384\n\t"
                "ds_read_b64 v[210:211], %[kb] offset:392\n\t"
                "ds_read_b128 v[212:215], %[ub] offset:4592\n\t"
                "s_waitcnt lgkmcnt(11)\n\t"
                "v_mfma_f32_32x32x16_bf16 %[a0], v[240:243], v[248:251], %[a0]\n\t"
                "ds_read_b128 v[216:219], %[ub] offset:2032\n\t"
                "s_waitcnt lgkmcnt(10)\n\t"
                "v_mfma_f32_32x32x16_bf16 %[a1], v[252:255], v[248:251], %[a1]\n\t"
                "ds_read_b64 v[220:221], %[kb] offset:2432\n\t"
                "ds_read_b64 v[222:223], %[kb] offset:2440\n\t"
                "s_waitcnt lgkmcnt(9)\n\t"
                "v_mfma_f32_32x32x16_bf16 %[a1], v[192:195], v[196:199], %[a1]\n\t"
                "ds_read_b64 v[224:225], %[kb] offset:480\n\t"
                "ds_read_b64 v[226:227], %[kb] offset:488\n\t"
                "ds_read_b128 v[228:231], %[ub] offset:4480\n\t"
                "s_waitcnt lgkmcnt(11)\n\t"
                "v_mfma_f32_32x32x16_bf16 %[a0], v[192:195], v[200:203], %[a0]\n\t"
                "ds_read_b128 v[232:235], %[ub] offset:1920\n\t"
                "s_waitcnt lgkmcnt(10)\n\t"
                "v_mfma_f32_32x32x16_bf16 %[a1], v[204:207], v[200:203], %[a1]\n\t"
                "ds_read_b64 v[236:237], %[kb] offset:2528\n\t"
                "ds_read_b64 v[238:239], %[kb] offset:2536\n\t"
                "s_waitcnt lgkmcnt(9)\n\t"
                "v_mfma_f32_32x32x16_bf16 %[a1], v[208:211], v[212:215], %[a1]\n\t"
                "ds_read_b64 v[240:241], %[kb] offset:448\n\t"
                "ds_read_b64 v[242:243], %[kb] offset:456\n\t"
                "ds_read_b128 v[244:247], %[ub] offset:4512\n\t"
                "s_waitcnt lgkmcnt(11)\n\t"
                "v_mfma_f32_32x32x16_bf16 %[a0], v[208:211], v[216:219], %[a0]\n\t"
                "ds_read_b128 v[248:251], %[ub] offset:1952\n\t"
                "s_waitcnt lgkmcnt(10)\n\t"
                "v_mfma_f32_32x32x16_bf16 %[a1], v[220:223], v[216:219], %[a1]\n\t"
                "ds_read_b64 v[252:253], %[kb] offset:2496\n\t"
                "ds_read_b64 v[254:255], %[kb] offset:2504\n\t"
                "s_waitcnt lgkmcnt(9)\n\t"
                "v_mfma_f32_32x32x16_bf16 %[a1], v[224:227], v[228:231], %[a1]\n\t"
                "ds_read_b64 v[192:193], %[kb] offset:544\n\t"
                "ds_read_b64 v[194:195], %[kb] offset:552\n\t"
                "ds_read_b128 v[196:199], %[ub] offset:4400\n\t"
                "s_waitcnt lgkmcnt(11)\n\t"
                "v_mfma_f32_32x32x16_bf16 %[a0], v[224:227], v[232:235], %[a0]\n\t"
                "ds_read_b128 v[200:203], %[ub] offset:1840\n\t"
                "s_waitcnt lgkmcnt(10)\n\t"
                "v_mfma_f32_32x32x16_bf16 %[a1], v[236:239], v[232:235], %[a1]\n\t"
                "ds_read_b64 v[204:205], %[kb] offset:2592\n\t"
                "ds_read_b64 v[206:207], %[kb] offset:2600\n\t"
                "s_waitcnt lgkmcnt(9)\n\t"
                "v_mfma_f32_32x32x16_bf16 %[a1], v[240:243], v[244:247], %[a1]\n\t"
                "ds_read_b64 v[208:209], %[kb] offset:512\n\t"
                "ds_read_b64 v[210:211], %[kb] offset:520\n\t"
                "ds_read_b128 v[212:215], %[ub] offset:4432\n\t"
                "s_waitcnt lgkmcnt(11)\n\t"
                "v_mfma_f32_32x32x16_bf16 %[a0], v[240:243], v[248:251], %[a0]\n\t"
                "ds_read_b128 v[216:219], %[ub] offset:1872\n\t"
                "s_waitcnt lgkmcnt(10)\n\t"
                "v_mfma_f32_32x32x16_bf16 %[a1], v[252:255], v[248:251], %[a1]\n\t"
                "ds_read_b64 v[220:221], %[kb] offset:2560\n\t"
                "ds_read_b64 v[222:223], %[kb] offset:2568\n\t"
                "s_waitcnt lgkmcnt(9)\n\t"
                "v_mfma_f32_32x32x16_bf16 %[a1], v[192:195], v[196:199], %[a1]\n\t"
                "ds_read_b64 v[224:225], %[kb] offset:608\n\t"
                "ds_read_b64 v[226:227], %[kb] offset:616\n\t"
                "ds_read_b128 v[228:231], %[ub] offset:4320\n\t"
                "s_waitcnt lgkmcnt(11)\n\t"
                "v_mfma_f32_32x32x16_bf16 %[a0], v[192:195], v[200:203], %[a0]\n\t"
                "ds_read_b128 v[232:235], %[ub] offset:1760\n\t"
                "s_waitcnt lgkmcnt(10)\n\t"
                "v_mfma_f32_32x32x16_bf16 %[a1], v[204:207], v[200:203], %[a1]\n\t"
                "ds_read_b64 v[236:237], %[kb] offset:2656\n\t"
                "ds_read_b64 v[238:239], %[kb] offset:2664\n\t"
                "s_waitcnt lgkmcnt(9)\n\t"
                "v_mfma_f32_32x32x16_bf16 %[a1], v[208:211], v[212:215], %[a1]\n\t"
                "ds_read_b64 v[240:241], %[kb] offset:576\n\t"
                "ds_read_b64 v[242:243], %[kb] offset:584\n\t"
                "ds_read_b128 v[244:247], %[ub] offset:4352\n\t"
                "s_waitcnt lgkmcnt(11)\n\t"
                "v_mfma_f32_32x32x16_bf16 %[a0], v[208:211], v[216:219], %[a0]\n\t"
                "ds_read_b128 v[248:251], %[ub] offset:1792\n\t"
                "s_waitcnt lgkmcnt(10)\n\t"
                "v_mfma_f32_32x32x16_bf16 %[a1], v[220:223], v[216:219], %[a1]\n\t"
                "ds_read_b64 v[252:253], %[kb] offset:2624\n\t"
                "ds_read_b64 v[254:255], %[kb] offset:2632\n\t"
                "s_waitcnt lgkmcnt(9)\n\t"
                "v_mfma_f32_32x32x16_bf16 %[a1], v[224:227], v[228:231], %[a1]\n\t"
                "ds_read_b64 v[192:193], %[kb] offset:672\n\t"
                "ds_read_b64 v[194:195], %[kb] offset:680\n\t"
                "ds_read_b128 v[196:199], %[ub] offset:4240\n\t"
                "s_waitcnt lgkmcnt(11)\n\t"
                "v_mfma_f32_32x32x16_bf16 %[a0], v[224:227], v[232:235], %[a0]\n\t"
                "ds_read_b128 v[200:203], %[ub] offset:1680\n\t"
                "s_waitcnt lgkmcnt(10)\n\t"
                "v_mfma_f32_32x32x16_bf16 %[a1], v[236:239], v[232:235], %[a1]\n\t"
                "ds_read_b64 v[204:205], %[kb] offset:2720\n\t"
                "ds_read_b64 v[206:207], %[kb] offset:2728\n\t"
                "s_waitcnt lgkmcnt(9)\n\t"
                "v_mfma_f32_32x32x16_bf16 %[a1], v[240:243], v[244:247], %[a1]\n\t"
                "ds_read_b64 v[208:209], %[kb] offset:640\n\t"
                "ds_read_b64 v[210:211], %[kb] offset:648\n\t"
                "ds_read_b128 v[212:215], %[ub] offset:4272\n\t"
                "s_waitcnt lgkmcnt(11)\n\t"
                "v_mfma_f32_32x32x16_bf16 %[a0], v[240:243], v[248:251], %[a0]\n\t"
                "ds_read_b128 v[216:219], %[ub] offset:1712\n\t"
                "s_waitcnt lgkmcnt(10)\n\t"
                "v_mfma_f32_32x32x16_bf16 %[a1], v[252:255], v[248:251], %[a1]\n\t"
                "ds_read_b64 v[220:221], %[kb] offset:2688\n\t"
                "ds_read_b64 v[222:223], %[kb] offset:2696\n\t"
                "s_waitcnt lgkmcnt(9)\n\t"
                "v_mfma_f32_32x32x16_bf16 %[a1], v[192:195], v[196:199], %[a1]\n\t"
                "ds_read_b64 v[224:225], %[kb] offset:736\n\t"
                "ds_read_b64 v[226:227], %[kb] offset:744\n\t"
                "ds_read_b128 v[228:231], %[ub] offset:4160\n\t"
                "s_waitcnt lgkmcnt(11)\n\t"
                "v_mfma_f32_32x32x16_bf16 %[a0], v[192:195], v[200:203], %[a0]\n\t"
                "ds_read_b128 v[232:235], %[ub] offset:1600\n\t"
                "s_waitcnt lgkmcnt(10)\n\t"
                "v_mfma_f32_32x32x16_bf16 %[a1], v[204:207], v[200:203], %[a1]\n\t"
                "ds_read_b64 v[236:237], %[kb] offset:2784\n\t"
                "ds_read_b64 v[238:239], %[kb] offset:2792\n\t"
                "s_waitcnt lgkmcnt(9)\n\t"
                "v_mfma_f32_32x32x16_bf16 %[a1], v[208:211], v[212:215], %[a1]\n\t"
                "ds_read_b64 v[240:241], %[kb] offset:704\n\t"
                "ds_read_b64 v[242:243], %[kb] offset:712\n\t"
                "ds_read_b128 v[244:247], %[ub] offset:4192\n\t"
                "s_waitcnt lgkmcnt(11)\n\t"
                "v_mfma_f32_32x32x16_bf16 %[a0], v[208:211], v[216:219], %[a0]\n\t"
                "ds_read_b128 v[248:251], %[ub] offset:1632\n\t"
                "s_waitcnt lgkmcnt(10)\n\t"
                "v_mfma_f32_32x32x16_bf16 %[a1], v[220:223], v[216:219], %[a1]\n\t"
                "ds_read_b64 v[252:253], %[kb] offset:2752\n\t"
                "ds_read_b64 v[254:255], %[kb] offset:2760\n\t"
                "s_waitcnt lgkmcnt(9)\n\t"
                "v_mfma_f32_32x32x16_bf16 %[a1], v[224:227], v[228:231], %[a1]\n\t"
                "ds_read_b64 v[192:193], %[kb] offset:800\n\t"
                "ds_read_b64 v[194:195], %[kb] offset:808\n\t"
                "ds_read_b128 v[196:199], %[ub] offset:4080\n\t"
                "s_waitcnt lgkmcnt(11)\n\t"
                "v_mfma_f32_32x32x16_bf16 %[a0], v[224:227], v[232:235], %[a0]\n\t"
                "ds_read_b128 v[200:203], %[ub] offset:1520\n\t"
                "s_waitcnt lgkmcnt(10)\n\t"
                "v_mfma_f32_32x32x16_bf16 %[a1], v[236:239], v[232:235], %[a1]\n\t"
                "ds_read_b64 v[204:205], %[kb] offset:2848\n\t"
                "ds_read_b64 v[206:207], %[kb] offset:2856\n\t"
                "s_waitcnt lgkmcnt(9)\n\t"
                "v_mfma_f32_32x32x16_bf16 %[a1], v[240:243], v[244:247], %[a1]\n\t"
                "ds_read_b64 v[208:209], %[kb] offset:768\n\t"
                "ds_read_b64 v[210:211], %[kb] offset:776\n\t"
                "ds_read_b128 v[212:215], %[ub] offset:4112\n\t"
                "s_waitcnt lgkmcnt(11)\n\t"
                "v_mfma_f32_32x32x16_bf16 %[a0], v[240:243], v[248:251], %[a0]\n\t"
                "ds_read_b128 v[216:219], %[ub] offset:1552\n\t"
                "s_waitcnt lgkmcnt(10)\n\t"
                "v_mfma_f32_32x32x16_bf16 %[a1], v[252:255], v[248:251], %[a1]\n\t"
                "ds_read_b64 v[220:221], %[kb] offset:2816\n\t"
                "ds_read_b64 v[222:223], %[kb] offset:2824\n\t"
                "s_waitcnt lgkmcnt(9)\n\t"
                "v_mfma_f32_32x32x16_bf16 %[a1], v[192:195], v[196:199], %[a1]\n\t"
                "ds_read_b64 v[224:225], %[kb] offset:864\n\t"
                "ds_read_b64 v[226:227], %[kb] offset:872\n\t"
                "ds_read_b128 v[228:231], %[ub] offset:4000\n\t"
                "s_waitcnt lgkmcnt(11)\n\t"
                "v_mfma_f32_32x32x16_bf16 %[a0], v[192:195], v[200:203], %[a0]\n\t"
                "ds_read_b128 v[232:235], %[ub] offset:1440\n\t"
                "s_waitcnt lgkmcnt(10)\n\t"
                "v_mfma_f32_32x32x16_bf16 %[a1], v[204:207], v[200:203], %[a1]\n\t"
                "ds_read_b64 v[236:237], %[kb] offset:2912\n\t"
                "ds_read_b64 v[238:239], %[kb] offset:2920\n\t"
                "s_waitcnt lgkmcnt(9)\n\t"
                "v_mfma_f32_32x32x16_bf16 %[a1], v[208:211], v[212:215], %[a1]\n\t"
                "ds_read_b64 v[240:241], %[kb] offset:832\n\t"
                "ds_read_b64 v[242:243], %[kb] offset:840\n\t"
                "ds_read_b128 v[244:247], %[ub] offset:4032\n\t"
                "s_waitcnt lgkmcnt(11)\n\t"
                "v_mfma_f32_32x32x16_bf16 %[a0], v[208:211], v[216:219], %[a0]\n\t"
                "ds_read_b128 v[248:251], %[ub] offset:1472\n\t"
                "s_waitcnt lgkmcnt(10)\n\t"
                "v_mfma_f32_32x32x16_bf16 %[a1], v[220:223], v[216:219], %[a1]\n\t"
                "ds_read_b64 v[252:253], %[kb] offset:2880\n\t"
                "ds_read_b64 v[254:255], %[kb] offset:2888\n\t"
                "s_waitcnt lgkmcnt(9)\n\t"
                "v_mfma_f32_32x32x16_bf16 %[a1], v[224:227], v[228:231], %[a1]\n\t"
                "ds_read_b64 v[192:193], %[kb] offset:928\n\t"
                "ds_read_b64 v[194:195], %[kb] offset:936\n\t"
                "ds_read_b128 v[196:199], %[ub] offset:3920\n\t"
                "s_waitcnt lgkmcnt(11)\n\t"
                "v_mfma_f32_32x32x16_bf16 %[a0], v[224:227], v[232:235], %[a0]\n\t"
                "ds_read_b128 v[200:203], %[ub] offset:1360\n\t"
                "s_waitcnt lgkmcnt(10)\n\t"
                "v_mfma_f32_32x32x16_bf16 %[a1], v[236:239], v[232:235], %[a1]\n\t"
                "ds_read_b64 v[204:205], %[kb] offset:2976\n\t"
                "ds_read_b64 v[206:207], %[kb] offset:2984\n\t"
                "s_waitcnt lgkmcnt(9)\n\t"
                "v_mfma_f32_32x32x16_bf16 %[a1], v[240:243], v[244:247], %[a1]\n\t"
                "ds_read_b64 v[208:209], %[kb] offset:896\n\t"
                "ds_read_b64 v[210:211], %[kb] offset:904\n\t"
                "ds_read_b128 v[212:215], %[ub] offset:3952\n\t"
                "s_waitcnt lgkmcnt(11)\n\t"
                "v_mfma_f32_32x32x16_bf16 %[a0], v[240:243], v[248:251], %[a0]\n\t"
                "ds_read_b128 v[216:219], %[ub] offset:1392\n\t"
                "s_waitcnt lgkmcnt(10)\n\t"
                "v_mfma_f32_32x32x16_bf16 %[a1], v[252:255], v[248:251], %[a1]\n\t"
                "ds_read_b64 v[220:221], %[kb] offset:2944\n\t"
                "ds_read_b64 v[222:223], %[kb] offset:2952\n\t"
                "s_waitcnt lgkmcnt(9)\n\t"
                "v_mfma_f32_32x32x16_bf16 %[a1], v[192:195], v[196:199], %[a1]\n\t"
                "ds_read_b64 v[224:225], %[kb] offset:992\n\t"
                "ds_read_b64 v[226:227], %[kb] offset:1000\n\t"
                "ds_read_b128 v[228:231], %[ub] offset:3840\n\t"
                "s_waitcnt lgkmcnt(11)\n\t"
                "v_mfma_f32_32x32x16_bf16 %[a0], v[192:195], v[200:203], %[a0]\n\t"
                "ds_read_b128 v[232:235], %[ub] offset:1280\n\t"
                "s_waitcnt lgkmcnt(10)\n\t"
                "v_mfma_f32_32x32x16_bf16 %[a1], v[204:207], v[200:203], %[a1]\n\t"
                "ds_read_b64 v[236:237], %[kb] offset:3040\n\t"
                "ds_read_b64 v[238:239], %[kb] offset:3048\n\t"
                "s_waitcnt lgkmcnt(9)\n\t"
                "v_mfma_f32_32x32x16_bf16 %[a1], v[208:211], v[212:215], %[a1]\n\t"
                "ds_read_b64 v[240:241], %[kb] offset:960\n\t"
                "ds_read_b64 v[242:243], %[kb] offset:968\n\t"
                "ds_read_b128 v[244:247], %[ub] offset:3872\n\t"
                "s_waitcnt lgkmcnt(11)\n\t"
                "v_mfma_f32_32x32x16_bf16 %[a0], v[208:211], v[216:219], %[a0]\n\t"
                "ds_read_b128 v[248:251], %[ub] offset:1312\n\t"
                "s_waitcnt lgkmcnt(10)\n\t"
                "v_mfma_f32_32x32x16_bf16 %[a1], v[220:223], v[216:219], %[a1]\n\t"
                "ds_read_b64 v[252:253], %[kb] offset:3008\n\t"
                "ds_read_b64 v[254:255], %[kb] offset:3016\n\t"
                "s_waitcnt lgkmcnt(9)\n\t"
                "v_mfma_f32_32x32x16_bf16 %[a1], v[224:227], v[228:231], %[a1]\n\t"
                "ds_read_b64 v[192:193], %[kb] offset:1056\n\t"
                "ds_read_b64 v[194:195], %[kb] offset:1064\n\t"
                "ds_read_b128 v[196:199], %[ub] offset:3760\n\t"
                "s_waitcnt lgkmcnt(11)\n\t"
                "v_mfma_f32_32x32x16_bf16 %[a0], v[224:227], v[232:235], %[a0]\n\t"
                "ds_read_b128 v[200:203], %[ub] offset:1200\n\t"
                "s_waitcnt lgkmcnt(10)\n\t"
                "v_mfma_f32_32x32x16_bf16 %[a1], v[236:239], v[232:235], %[a1]\n\t"
                "ds_read_b64 v[204:205], %[kb] offset:3104\n\t"
                "ds_read_b64 v[206:207], %[kb] offset:3112\n\t"
                "s_waitcnt lgkmcnt(9)\n\t"
                "v_mfma_f32_32x32x16_bf16 %[a1], v[240:243], v[244:247], %[a1]\n\t"
                "ds_read_b64 v[208:209], %[kb] offset:1024\n\t"
                "ds_read_b64 v[210:211], %[kb] offset:1032\n\t"
                "ds_read_b128 v[212:215], %[ub] offset:3792\n\t"
                "s_waitcnt lgkmcnt(11)\n\t"
                "v_mfma_f32_32x32x16_bf16 %[a0], v[240:243], v[248:251], %[a0]\n\t"
                "ds_read_b128 v[216:219], %[ub] offset:1232\n\t"
                "s_waitcnt lgkmcnt(10)\n\t"
                "v_mfma_f32_32x32x16_bf16 %[a1], v[252:255], v[248:251], %[a1]\n\t"
                "ds_read_b64 v[220:221], %[kb] offset:3072\n\t"
                "ds_read_b64 v[222:223], %[kb] offset:3080\n\t"
                "s_waitcnt lgkmcnt(9)\n\t"
                "v_mfma_f32_32x32x16_bf16 %[a1], v[192:195], v[196:199], %[a1]\n\t"
                "ds_read_b64 v[224:225], %[kb] offset:1120\n\t"
                "ds_read_b64 v[226:227], %[kb] offset:1128\n\t"
                "ds_read_b128 v[228:231], %[ub] offset:3680\n\t"
                "s_waitcnt lgkmcnt(11)\n\t"
                "v_mfma_f32_32x32x16_bf16 %[a0], v[192:195], v[200:203], %[a0]\n\t"
                "ds_read_b128 v[232:235], %[ub] offset:1120\n\t"
                "s_waitcnt lgkmcnt(10)\n\t"
                "v_mfma_f32_32x32x16_bf16 %[a1], v[204:207], v[200:203], %[a1]\n\t"
                "ds_read_b64 v[236:237], %[kb] offset:3168\n\t"
                "ds_read_b64 v[238:239], %[kb] offset:3176\n\t"
                "s_waitcnt lgkmcnt(9)\n\t"
                "v_mfma_f32_32x32x16_bf16 %[a1], v[208:211], v[212:215], %[a1]\n\t"
                "ds_read_b64 v[240:241], %[kb] offset:1088\n\t"
                "ds_read_b64 v[242:243], %[kb] offset:1096\n\t"
                "ds_read_b128 v[244:247], %[ub] offset:3712\n\t"
                "s_waitcnt lgkmcnt(11)\n\t"
                "v_mfma_f32_32x32x16_bf16 %[a0], v[208:211], v[216:219], %[a0]\n\t"
                "ds_read_b128 v[248:251], %[ub] offset:1152\n\t"
                "s_waitcnt lgkmcnt(10)\n\t"
                "v_mfma_f32_32x32x16_bf16 %[a1], v[220:223], v[216:219], %[a1]\n\t"
                "ds_read_b64 v[252:253], %[kb] offset:3136\n\t"
                "ds_read_b64 v[254:255], %[kb] offset:3144\n\t"
                "s_waitcnt lgkmcnt(9)\n\t"
                "v_mfma_f32_32x32x16_bf16 %[a1], v[224:227], v[228:231], %[a1]\n\t"
                "ds_read_b64 v[192:193], %[kb] offset:1184\n\t"
                "ds_read_b64 v[194:195], %[kb] offset:1192\n\t"
                "ds_read_b128 v[196:199], %[ub] offset:3600\n\t"
                "s_waitcnt lgkmcnt(11)\n\t"
                "v_mfma_f32_32x32x16_bf16 %[a0], v[224:227], v[232:235], %[a0]\n\t"
                "ds_read_b128 v[200:203], %[ub] offset:1040\n\t"
                "s_waitcnt lgkmcnt(10)\n\t"
                "v_mfma_f32_32x32x16_bf16 %[a1], v[236:239], v[232:235], %[a1]\n\t"
                "ds_read_b64 v[204:205], %[kb] offset:3232\n\t"
                "ds_read_b64 v[206:207], %[kb] offset:3240\n\t"
                "s_waitcnt lgkmcnt(9)\n\t"
                "v_mfma_f32_32x32x16_bf16 %[a1], v[240:243], v[244:247], %[a1]\n\t"
                "ds_read_b64 v[208:209], %[kb] offset:1152\n\t"
                "ds_read_b64 v[210:211], %[kb] offset:1160\n\t"
                "ds_read_b128 v[212:215], %[ub] offset:3632\n\t"
                "s_waitcnt lgkmcnt(11)\n\t"
                "v_mfma_f32_32x32x16_bf16 %[a0], v[240:243], v[248:251], %[a0]\n\t"
                "ds_read_b128 v[216:219], %[ub] offset:1072\n\t"
                "s_waitcnt lgkmcnt(10)\n\t"
                "v_mfma_f32_32x32x16_bf16 %[a1], v[252:255], v[248:251], %[a1]\n\t"
                "ds_read_b64 v[220:221], %[kb] offset:3200\n\t"
                "ds_read_b64 v[222:223], %[kb] offset:3208\n\t"
                "s_waitcnt lgkmcnt(9)\n\t"
                "v_mfma_f32_32x32x16_bf16 %[a1], v[192:195], v[196:199], %[a1]\n\t"
                "ds_read_b64 v[224:225], %[kb] offset:1248\n\t"
                "ds_read_b64 v[226:227], %[kb] offset:1256\n\t"
                "ds_read_b128 v[228:231], %[ub] offset:3520\n\t"
                "s_waitcnt lgkmcnt(11)\n\t"
                "v_mfma_f32_32x32x16_bf16 %[a0], v[192:195], v[200:203], %[a0]\n\t"
                "ds_read_b128 v[232:235], %[ub] offset:960\n\t"
                "s_waitcnt lgkmcnt(10)\n\t"
                "v_mfma_f32_32x32x16_bf16 %[a1], v[204:207], v[200:203], %[a1]\n\t"
                "ds_read_b64 v[236:237], %[kb] offset:3296\n\t"
                "ds_read_b64 v[238:239], %[kb] offset:3304\n\t"
                "s_waitcnt lgkmcnt(9)\n\t"
                "v_mfma_f32_32x32x16_bf16 %[a1], v[208:211], v[212:215], %[a1]\n\t"
                "ds_read_b64 v[240:241], %[kb] offset:1216\n\t"
                "ds_read_b64 v[242:243], %[kb] offset:1224\n\t"
                "ds_read_b128 v[244:247], %[ub] offset:3552\n\t"
                "s_waitcnt lgkmcnt(11)\n\t"
                "v_mfma_f32_32x32x16_bf16 %[a0], v[208:211], v[216:219], %[a0]\n\t"
                "ds_read_b128 v[248:251], %[ub] offset:992\n\t"
                "s_waitcnt lgkmcnt(10)\n\t"
                "v_mfma_f32_32x32x16_bf16 %[a1], v[220:223], v[216:219], %[a1]\n\t"
                "ds_read_b64 v[252:253], %[kb] offset:3264\n\t"
                "ds_read_b64 v[254:255], %[kb] offset:3272\n\t"
                "s_waitcnt lgkmcnt(9)\n\t"
                "v_mfma_f32_32x32x16_bf16 %[a1], v[224:227], v[228:231], %[a1]\n\t"
                "ds_read_b64 v[192:193], %[kb] offset:1312\n\t"
                "ds_read_b64 v[194:195], %[kb] offset:1320\n\t"
                "ds_read_b128 v[196:199], %[ub] offset:3440\n\t"
                "s_waitcnt lgkmcnt(11)\n\t"
                "v_mfma_f32_32x32x16_bf16 %[a0], v[224:227], v[232:235], %[a0]\n\t"
                "ds_read_b128 v[200:203], %[ub] offset:880\n\t"
                "s_waitcnt lgkmcnt(10)\n\t"
                "v_mfma_f32_32x32x16_bf16 %[a1], v[236:239], v[232:235], %[a1]\n\t"
                "ds_read_b64 v[204:205], %[kb] offset:3360\n\t"
                "ds_read_b64 v[206:207], %[kb] offset:3368\n\t"
                "s_waitcnt lgkmcnt(9)\n\t"
                "v_mfma_f32_32x32x16_bf16 %[a1], v[240:243], v[244:247], %[a1]\n\t"
                "ds_read_b64 v[208:209], %[kb] offset:1280\n\t"
                "ds_read_b64 v[210:211], %[kb] offset:1288\n\t"
                "ds_read_b128 v[212:215], %[ub] offset:3472\n\t"
                "s_waitcnt lgkmcnt(11)\n\t"
                "v_mfma_f32_32x32x16_bf16 %[a0], v[240:243], v[248:251], %[a0]\n\t"
                "ds_read_b128 v[216:219], %[ub] offset:912\n\t"
                "s_waitcnt lgkmcnt(10)\n\t"
                "v_mfma_f32_32x32x16_bf16 %[a1], v[252:255], v[248:251], %[a1]\n\t"
                "ds_read_b64 v[220:221], %[kb] offset:3328\n\t"
                "ds_read_b64 v[222:223], %[kb] offset:3336\n\t"
                "s_waitcnt lgkmcnt(9)\n\t"
                "v_mfma_f32_32x32x16_bf16 %[a1], v[192:195], v[196:199], %[a1]\n\t"
                "ds_read_b64 v[224:225], %[kb] offset:1376\n\t"
                "ds_read_b64 v[226:227], %[kb] offset:1384\n\t"
                "ds_read_b128 v[228:231], %[ub] offset:3360\n\t"
                "s_waitcnt lgkmcnt(11)\n\t"
                "v_mfma_f32_32x32x16_bf16 %[a0], v[192:195], v[200:203], %[a0]\n\t"
                "ds_read_b128 v[232:235], %[ub] offset:800\n\t"
                "s_waitcnt lgkmcnt(10)\n\t"
                "v_mfma_f32_32x32x16_bf16 %[a1], v[204:207], v[200:203], %[a1]\n\t"
                "ds_read_b64 v[236:237], %[kb] offset:3424\n\t"
                "ds_read_b64 v[238:239], %[kb] offset:3432\n\t"
                "s_waitcnt lgkmcnt(9)\n\t"
                "v_mfma_f32_32x32x16_bf16 %[a1], v[208:211], v[212:215], %[a1]\n\t"
                "ds_read_b64 v[240:241], %[kb] offset:1344\n\t"
                "ds_read_b64 v[242:243], %[kb] offset:1352\n\t"
                "ds_read_b128 v[244:247], %[ub] offset:3392\n\t"
                "s_waitcnt lgkmcnt(11)\n\t"
                "v_mfma_f32_32x32x16_bf16 %[a0], v[208:211], v[216:219], %[a0]\n\t"
                "ds_read_b128 v[248:251], %[ub] offset:832\n\t"
                "s_waitcnt lgkmcnt(10)\n\t"
                "v_mfma_f32_32x32x16_bf16 %[a1], v[220:223], v[216:219], %[a1]\n\t"
                "ds_read_b64 v[252:253], %[kb] offset:3392\n\t"
                "ds_read_b64 v[254:255], %[kb] offset:3400\n\t"
                "s_waitcnt lgkmcnt(9)\n\t"
                "v_mfma_f32_32x32x16_bf16 %[a1], v[224:227], v[228:231], %[a1]\n\t"
                "ds_read_b64 v[192:193], %[kb] offset:1440\n\t"
                "ds_read_b64 v[194:195], %[kb] offset:1448\n\t"
                "ds_read_b128 v[196:199], %[ub] offset:3280\n\t"
                "s_waitcnt lgkmcnt(11)\n\t"
                "v_mfma_f32_32x32x16_bf16 %[a0], v[224:227], v[232:235], %[a0]\n\t"
                "ds_read_b128 v[200:203], %[ub] offset:720\n\t"
                "s_waitcnt lgkmcnt(10)\n\t"
                "v_mfma_f32_32x32x16_bf16 %[a1], v[236:239], v[232:235], %[a1]\n\t"
                "ds_read_b64 v[204:205], %[kb] offset:3488\n\t"
                "ds_read_b64 v[206:207], %[kb] offset:3496\n\t"
                "s_waitcnt lgkmcnt(9)\n\t"
                "v_mfma_f32_32x32x16_bf16 %[a1], v[240:243], v[244:247], %[a1]\n\t"
                "ds_read_b64 v[208:209], %[kb] offset:1408\n\t"
                "ds_read_b64 v[210:211], %[kb] offset:1416\n\t"
                "ds_read_b128 v[212:215], %[ub] offset:3312\n\t"
                "s_waitcnt lgkmcnt(11)\n\t"
                "v_mfma_f32_32x32x16_bf16 %[a0], v[240:243], v[248:251], %[a0]\n\t"
                "ds_read_b128 v[216:219], %[ub] offset:752\n\t"
                "s_waitcnt lgkmcnt(10)\n\t"
                "v_mfma_f32_32x32x16_bf16 %[a1], v[252:255], v[248:251], %[a1]\n\t"
                "ds_read_b64 v[220:221], %[kb] offset:3456\n\t"
                "ds_read_b64 v[222:223], %[kb] offset:3464\n\t"
                "s_waitcnt lgkmcnt(9)\n\t"
                "v_mfma_f32_32x32x16_bf16 %[a1], v[192:195], v[196:199], %[a1]\n\t"
                "ds_read_b64 v[224:225], %[kb] offset:1504\n\t"
                "ds_read_b64 v[226:227], %[kb] offset:1512\n\t"
                "ds_read_b128 v[228:231], %[ub] offset:3200\n\t"
                "s_waitcnt lgkmcnt(11)\n\t"
                "v_mfma_f32_32x32x16_bf16 %[a0], v[192:195], v[200:203], %[a0]\n\t"
                "ds_read_b128 v[232:235], %[ub] offset:640\n\t"
                "s_waitcnt lgkmcnt(10)\n\t"
                "v_mfma_f32_32x32x16_bf16 %[a1], v[204:207], v[200:203], %[a1]\n\t"
                "ds_read_b64 v[236:237], %[kb] offset:3552\n\t"
                "ds_read_b64 v[238:239], %[kb] offset:3560\n\t"
                "s_waitcnt lgkmcnt(9)\n\t"
                "v_mfma_f32_32x32x16_bf16 %[a1], v[208:211], v[212:215], %[a1]\n\t"
                "ds_read_b64 v[240:241], %[kb] offset:1472\n\t"
                "ds_read_b64 v[242:243], %[kb] offset:1480\n\t"
                "ds_read_b128 v[244:247], %[ub] offset:3232\n\t"
                "s_waitcnt lgkmcnt(11)\n\t"
                "v_mfma_f32_32x32x16_bf16 %[a0], v[208:211], v[216:219], %[a0]\n\t"
                "ds_read_b128 v[248:251], %[ub] offset:672\n\t"
                "s_waitcnt lgkmcnt(10)\n\t"
                "v_mfma_f32_32x32x16_bf16 %[a1], v[220:223], v[216:219], %[a1]\n\t"
                "ds_read_b64 v[252:253], %[kb] offset:3520\n\t"
                "ds_read_b64 v[254:255], %[kb] offset:3528\n\t"
                "s_waitcnt lgkmcnt(9)\n\t"
                "v_mfma_f32_32x32x16_bf16 %[a1], v[224:227], v[228:231], %[a1]\n\t"
                "ds_read_b64 v[192:193], %[kb] offset:1568\n\t"
                "ds_read_b64 v[194:195], %[kb] offset:1576\n\t"
                "ds_read_b128 v[196:199], %[ub] offset:3120\n\t"
                "s_waitcnt lgkmcnt(11)\n\t"
                "v_mfma_f32_32x32x16_bf16 %[a0], v[224:227], v[232:235], %[a0]\n\t"
                "ds_read_b128 v[200:203], %[ub] offset:560\n\t"
                "s_waitcnt lgkmcnt(10)\n\t"
                "v_mfma_f32_32x32x16_bf16 %[a1], v[236:239], v[232:235], %[a1]\n\t"
                "ds_read_b64 v[204:205], %[kb] offset:3616\n\t"
                "ds_read_b64 v[206:207], %[kb] offset:3624\n\t"
                "s_waitcnt lgkmcnt(9)\n\t"
                "v_mfma_f32_32x32x16_bf16 %[a1], v[240:243], v[244:247], %[a1]\n\t"
                "ds_read_b64 v[208:209], %[kb] offset:1536\n\t"
                "ds_read_b64 v[210:211], %[kb] offset:1544\n\t"
                "ds_read_b128 v[212:215], %[ub] offset:3152\n\t"
                "s_waitcnt lgkmcnt(11)\n\t"
                "v_mfma_f32_32x32x16_bf16 %[a0], v[240:243], v[248:251], %[a0]\n\t"
                "ds_read_b128 v[216:219], %[ub] offset:592\n\t"
                "s_waitcnt lgkmcnt(10)\n\t"
                "v_mfma_f32_32x32x16_bf16 %[a1], v[252:255], v[248:251], %[a1]\n\t"
                "ds_read_b64 v[220:221], %[kb] offset:3584\n\t"
                "ds_read_b64 v[222:223], %[kb] offset:3592\n\t"
                "s_waitcnt lgkmcnt(9)\n\t"
                "v_mfma_f32_32x32x16_bf16 %[a1], v[192:195], v[196:199], %[a1]\n\t"
                "ds_read_b64 v[224:225], %[kb] offset:1632\n\t"
                "ds_read_b64 v[226:227], %[kb] offset:1640\n\t"
                "ds_read_b128 v[228:231], %[ub] offset:3040\n\t"
                "s_waitcnt lgkmcnt(11)\n\t"
                "v_mfma_f32_32x32x16_bf16 %[a0], v[192:195], v[200:203], %[a0]\n\t"
                "ds_read_b128 v[232:235], %[ub] offset:480\n\t"
                "s_waitcnt lgkmcnt(10)\n\t"
                "v_mfma_f32_32x32x16_bf16 %[a1], v[204:207], v[200:203], %[a1]\n\t"
                "ds_read_b64 v[236:237], %[kb] offset:3680\n\t"
                "ds_read_b64 v[238:239], %[kb] offset:3688\n\t"
                "s_waitcnt lgkmcnt(9)\n\t"
                "v_mfma_f32_32x32x16_bf16 %[a1], v[208:211], v[212:215], %[a1]\n\t"
                "ds_read_b64 v[240:241], %[kb] offset:1600\n\t"
                "ds_read_b64 v[242:243], %[kb] offset:1608\n\t"
                "ds_read_b128 v[244:247], %[ub] offset:3072\n\t"
                "s_waitcnt lgkmcnt(11)\n\t"
                "v_mfma_f32_32x32x16_bf16 %[a0], v[208:211], v[216:219], %[a0]\n\t"
                "ds_read_b128 v[248:251], %[ub] offset:512\n\t"
                "s_waitcnt lgkmcnt(10)\n\t"
                "v_mfma_f32_32x32x16_bf16 %[a1], v[220:223], v[216:219], %[a1]\n\t"
                "ds_read_b64 v[252:253], %[kb] offset:3648\n\t"
                "ds_read_b64 v[254:255], %[kb] offset:3656\n\t"
                "s_waitcnt lgkmcnt(9)\n\t"
                "v_mfma_f32_32x32x16_bf16 %[a1], v[224:227], v[228:231], %[a1]\n\t"
                "ds_read_b64 v[192:193], %[kb] offset:1696\n\t"
                "ds_read_b64 v[194:195], %[kb] offset:1704\n\t"
                "ds_read_b128 v[196:199], %[ub] offset:2960\n\t"
                "s_waitcnt lgkmcnt(11)\n\t"
                "v_mfma_f32_32x32x16_bf16 %[a0], v[224:227], v[232:235], %[a0]\n\t"
                "ds_read_b128 v[200:203], %[ub] offset:400\n\t"
                "s_waitcnt lgkmcnt(10)\n\t"
                "v_mfma_f32_32x32x16_bf16 %[a1], v[236:239], v[232:235], %[a1]\n\t"
                "ds_read_b64 v[204:205], %[kb] offset:3744\n\t"
                "ds_read_b64 v[206:207], %[kb] offset:3752\n\t"
                "s_waitcnt lgkmcnt(9)\n\t"
                "v_mfma_f32_32x32x16_bf16 %[a1], v[240:243], v[244:247], %[a1]\n\t"
                "ds_read_b64 v[208:209], %[kb] offset:1664\n\t"
                "ds_read_b64 v[210:211], %[kb] offset:1672\n\t"
                "ds_read_b128 v[212:215], %[ub] offset:2992\n\t"
                "s_waitcnt lgkmcnt(11)\n\t"
                "v_mfma_f32_32x32x16_bf16 %[a0], v[240:243], v[248:251], %[a0]\n\t"
                "ds_read_b128 v[216:219], %[ub] offset:432\n\t"
                "s_waitcnt lgkmcnt(10)\n\t"
                "v_mfma_f32_32x32x16_bf16 %[a1], v[252:255], v[248:251], %[a1]\n\t"
                "ds_read_b64 v[220:221], %[kb] offset:3712\n\t"
                "ds_read_b64 v[222:223], %[kb] offset:3720\n\t"
                "s_waitcnt lgkmcnt(9)\n\t"
                "v_mfma_f32_32x32x16_bf16 %[a1], v[192:195], v[196:199], %[a1]\n\t"
                "ds_read_b64 v[224:225], %[kb] offset:1760\n\t"
                "ds_read_b64 v[226:227], %[kb] offset:1768\n\t"
                "ds_read_b128 v[228:231], %[ub] offset:2880\n\t"
                "s_waitcnt lgkmcnt(11)\n\t"
                "v_mfma_f32_32x32x16_bf16 %[a0], v[192:195], v[200:203], %[a0]\n\t"
                "ds_read_b128 v[232:235], %[ub] offset:320\n\t"
                "s_waitcnt lgkmcnt(10)\n\t"
                "v_mfma_f32_32x32x16_bf16 %[a1], v[204:207], v[200:203], %[a1]\n\t"
                "ds_read_b64 v[236:237], %[kb] offset:3808\n\t"
                "ds_read_b64 v[238:239], %[kb] offset:3816\n\t"
                "s_waitcnt lgkmcnt(9)\n\t"
                "v_mfma_f32_32x32x16_bf16 %[a1], v[208:211], v[212:215], %[a1]\n\t"
                "ds_read_b64 v[240:241], %[kb] offset:1728\n\t"
                "ds_read_b64 v[242:243], %[kb] offset:1736\n\t"
                "ds_read_b128 v[244:247], %[ub] offset:2912\n\t"
                "s_waitcnt lgkmcnt(11)\n\t"
                "v_mfma_f32_32x32x16_bf16 %[a0], v[208:211], v[216:219], %[a0]\n\t"
                "ds_read_b128 v[248:251], %[ub] offset:352\n\t"
                "s_waitcnt lgkmcnt(10)\n\t"
                "v_mfma_f32_32x32x16_bf16 %[a1], v[220:223], v[216:219], %[a1]\n\t"
                "ds_read_b64 v[252:253], %[kb] offset:3776\n\t"
                "ds_read_b64 v[254:255], %[kb] offset:3784\n\t"
                "s_waitcnt lgkmcnt(9)\n\t"
                "v_mfma_f32_32x32x16_bf16 %[a1], v[224:227], v[228:231], %[a1]\n\t"
                "ds_read_b64 v[192:193], %[kb] offset:1824\n\t"
                "ds_read_b64 v[194:195], %[kb] offset:1832\n\t"
                "ds_read_b128 v[196:199], %[ub] offset:2800\n\t"
                "s_waitcnt lgkmcnt(11)\n\t"
                "v_mfma_f32_32x32x16_bf16 %[a0], v[224:227], v[232:235], %[a0]\n\t"
                "ds_read_b128 v[200:203], %[ub] offset:240\n\t"
                "s_waitcnt lgkmcnt(10)\n\t"
                "v_mfma_f32_32x32x16_bf16 %[a1], v[236:239], v[232:235], %[a1]\n\t"
                "ds_read_b64 v[204:205], %[kb] offset:3872\n\t"
                "ds_read_b64 v[206:207], %[kb] offset:3880\n\t"
                "s_waitcnt lgkmcnt(9)\n\t"
                "v_mfma_f32_32x32x16_bf16 %[a1], v[240:243], v[244:247], %[a1]\n\t"
                "ds_read_b64 v[208:209], %[kb] offset:1792\n\t"
                "ds_read_b64 v[210:211], %[kb] offset:1800\n\t"
                "ds_read_b128 v[212:215], %[ub] offset:2832\n\t"
                "s_waitcnt lgkmcnt(11)\n\t"
                "v_mfma_f32_32x32x16_bf16 %[a0], v[240:243], v[248:251], %[a0]\n\t"
                "ds_read_b128 v[216:219], %[ub] offset:272\n\t"
                "s_waitcnt lgkmcnt(10)\n\t"
                "v_mfma_f32_32x32x16_bf16 %[a1], v[252:255], v[248:251], %[a1]\n\t"
                "ds_read_b64 v[220:221], %[kb] offset:3840\n\t"
                "ds_read_b64 v[222:223], %[kb] offset:3848\n\t"
                "s_waitcnt lgkmcnt(9)\n\t"
                "v_mfma_f32_32x32x16_bf16 %[a1], v[192:195], v[196:199], %[a1]\n\t"
                "ds_read_b64 v[224:225], %[kb] offset:1888\n\t"
                "ds_read_b64 v[226:227], %[kb] offset:1896\n\t"
                "ds_read_b128 v[228:231], %[ub] offset:2720\n\t"
                "s_waitcnt lgkmcnt(11)\n\t"
                "v_mfma_f32_32x32x16_bf16 %[a0], v[192:195], v[200:203], %[a0]\n\t"
                "ds_read_b128 v[232:235], %[ub] offset:160\n\t"
                "s_waitcnt lgkmcnt(10)\n\t"
                "v_mfma_f32_32x32x16_bf16 %[a1], v[204:207], v[200:203], %[a1]\n\t"
                "ds_read_b64 v[236:237], %[kb] offset:3936\n\t"
                "ds_read_b64 v[238:239], %[kb] offset:3944\n\t"
                "s_waitcnt lgkmcnt(9)\n\t"
                "v_mfma_f32_32x32x16_bf16 %[a1], v[208:211], v[212:215], %[a1]\n\t"
                "ds_read_b64 v[240:241], %[kb] offset:1856\n\t"
                "ds_read_b64 v[242:243], %[kb] offset:1864\n\t"
                "ds_read_b128 v[244:247], %[ub] offset:2752\n\t"
                "s_waitcnt lgkmcnt(11)\n\t"
                "v_mfma_f32_32x32x16_bf16 %[a0], v[208:211], v[216:219], %[a0]\n\t"
                "ds_read_b128 v[248:251], %[ub] offset:192\n\t"
                "s_waitcnt lgkmcnt(10)\n\t"
                "v_mfma_f32_32x32x16_bf16 %[a1], v[220:223], v[216:219], %[a1]\n\t"
                "ds_read_b64 v[252:253], %[kb] offset:3904\n\t"
                "ds_read_b64 v[254:255], %[kb] offset:3912\n\t"
                "s_waitcnt lgkmcnt(9)\n\t"
                "v_mfma_f32_32x32x16_bf16 %[a1], v[224:227], v[228:231], %[a1]\n\t"
                "ds_read_b64 v[192:193], %[kb] offset:1952\n\t"
                "ds_read_b64 v[194:195], %[kb] offset:1960\n\t"
                "ds_read_b128 v[196:199], %[ub] offset:2640\n\t"
                "s_waitcnt lgkmcnt(11)\n\t"
                "v_mfma_f32_32x32x16_bf16 %[a0], v[224:227], v[232:235], %[a0]\n\t"
                "ds_read_b128 v[200:203], %[ub] offset:80\n\t"
                "s_waitcnt lgkmcnt(10)\n\t"
                "v_mfma_f32_32x32x16_bf16 %[a1], v[236:239], v[232:235], %[a1]\n\t"
                "ds_read_b64 v[204:205], %[kb] offset:4000\n\t"
                "ds_read_b64 v[206:207], %[kb] offset:4008\n\t"
                "s_waitcnt lgkmcnt(9)\n\t"
                "v_mfma_f32_32x32x16_bf16 %[a1], v[240:243], v[244:247], %[a1]\n\t"
                "ds_read_b64 v[208:209], %[kb] offset:1920\n\t"
                "ds_read_b64 v[210:211], %[kb] offset:1928\n\t"
                "ds_read_b128 v[212:215], %[ub] offset:2672\n\t"
                "s_waitcnt lgkmcnt(11)\n\t"
                "v_mfma_f32_32x32x16_bf16 %[a0], v[240:243], v[248:251], %[a0]\n\t"
                "ds_read_b128 v[216:219], %[ub] offset:112\n\t"
                "s_waitcnt lgkmcnt(10)\n\t"
                "v_mfma_f32_32x32x16_bf16 %[a1], v[252:255], v[248:251], %[a1]\n\t"
                "ds_read_b64 v[220:221], %[kb] offset:3968\n\t"
                "ds_read_b64 v[222:223], %[kb] offset:3976\n\t"
                "s_waitcnt lgkmcnt(9)\n\t"
                "v_mfma_f32_32x32x16_bf16 %[a1], v[192:195], v[196:199], %[a1]\n\t"
                "ds_read_b64 v[224:225], %[kb] offset:2016\n\t"
                "ds_read_b64 v[226:227], %[kb] offset:2024\n\t"
                "ds_read_b128 v[228:231], %[ub] offset:2560\n\t"
                "s_waitcnt lgkmcnt(11)\n\t"
                "v_mfma_f32_32x32x16_bf16 %[a0], v[192:195], v[200:203], %[a0]\n\t"
                "ds_read_b128 v[232:235], %[ub] offset:0\n\t"
                "s_waitcnt lgkmcnt(10)\n\t"
                "v_mfma_f32_32x32x16_bf16 %[a1], v[204:207], v[200:203], %[a1]\n\t"
                "ds_read_b64 v[236:237], %[kb] offset:4064\n\t"
                "ds_read_b64 v[238:239], %[kb] offset:4072\n\t"
                "s_waitcnt lgkmcnt(9)\n\t"
                "v_mfma_f32_32x32x16_bf16 %[a1], v[208:211], v[212:215], %[a1]\n\t"
                "ds_read_b64 v[240:241], %[kb] offset:1984\n\t"
                "ds_read_b64 v[242:243], %[kb] offset:1992\n\t"
                "ds_read_b128 v[244:247], %[ub] offset:2592\n\t"
                "s_waitcnt lgkmcnt(11)\n\t"
                "v_mfma_f32_32x32x16_bf16 %[a0], v[208:211], v[216:219], %[a0]\n\t"
                "ds_read_b128 v[248:251], %[ub] offset:32\n\t"
                "s_waitcnt lgkmcnt(10)\n\t"
                "v_mfma_f32_32x32x16_bf16 %[a1], v[220:223], v[216:219], %[a1]\n\t"
                "ds_read_b64 v[252:253], %[kb] offset:4032\n\t"
                "ds_read_b64 v[254:255], %[kb] offset:4040\n\t"
                "s_waitcnt lgkmcnt(9)\n\t"
                "v_mfma_f32_32x32x16_bf16 %[a1], v[224:227], v[228:231], %[a1]\n\t"
                "s_waitcnt lgkmcnt(8)\n\t"
                "v_mfma_f32_32x32x16_bf16 %[a0], v[224:227], v[232:235], %[a0]\n\t"
                "s_waitcnt lgkmcnt(6)\n\t"
                "v_mfma_f32_32x32x16_bf16 %[a1], v[236:239], v[232:235], %[a1]\n\t"
                "s_waitcnt lgkmcnt(3)\n\t"
                "v_mfma_f32_32x32x16_bf16 %[a1], v[240:243], v[244:247], %[a1]\n\t"
                "s_waitcnt lgkmcnt(2)\n\t"
                "v_mfma_f32_32x32x16_bf16 %[a0], v[240:243], v[248:251], %[a0]\n\t"
                "s_waitcnt lgkmcnt(0)\n\t"
                "v_mfma_f32_32x32x16_bf16 %[a1], v[252:255], v[248:251], %[a1]\n\t"
                "s_nop 7\n\t"
                "s_nop 3\n\t"
                : [a0] "=&v"(acc0), [a1] "=&v"(acc1) : [kb] "v"(kb), [ub] "v"(ub0 + c8 * CV_U) : "memory", "v192", "v193", "v194", "v195", "v196", "v197", "v198", "v199", "v200", "v201", "v202", "v203", "v204", "v205", "v206", "v207", "v208", "v209", "v210", "v211", "v212", "v213", "v214", "v215", "v216", "v217", "v218", "v219", "v220", "v221", "v222", "v223", "v224", "v225", "v226", "v227", "v228", "v229", "v230", "v231", "v232", "v233", "v234", "v235", "v236", "v237", "v238", "v239", "v240", "v241", "v242", "v243", "v244", "v245", "v246", "v247", "v248", "v249", "v250", "v251", "v252", "v253", "v254", "v255");
            unsigned char* uc = lds + c8 * CV_U + (32 + b) * CV_RP + kg * 8;
#pragma unroll
            for (int q = 0; q < 4; ++q) {
                *(v2u*)(uc + q * 16) = (v2u){pack2(acc0[4 * q], acc0[4 * q + 1]), pack2(acc0[4 * q + 2], acc0[4 * q + 3])};
                *(v2u*)(uc + 32 * CV_RP + q * 16) = (v2u){pack2(acc1[4 * q], acc1[4 * q + 1]), pack2(acc1[4 * q + 2], acc1[4 * q + 3])}; }
            __builtin_amdgcn_wave_barrier();
        }
    __syncthreads();
#pragma unroll
    for (int G = 0; G < 4; ++G) { const v4u lo = *(const v4u*)(urow + G * 16), hh = *(const v4u*)(urow + CV_U + G * 16); v4u o[2];
#pragma unroll
        for (int i = 0; i < 8; ++i) { const unsigned u0 = ur[G][(7 - i) >> 1], u1 = ur[4 + G][(7 - i) >> 1], qq = qr[2 * G + (i >> 2)][i & 3];
            const unsigned ud = pack2(((i & 1) ? blo(u0) : bhi(u0)) * blo(dv), ((i & 1) ? blo(u1) : bhi(u1)) * bhi(dv));
            const unsigned y = pack2(((i & 1) ? bhi(lo[i >> 1]) : blo(lo[i >> 1])) + blo(ud), ((i & 1) ? bhi(hh[i >> 1]) : blo(hh[i >> 1])) + bhi(ud));
            o[i >> 2][i & 3] = pack2(blo(qq) * blo(y), bhi(qq) * bhi(y)); }
        st16(r, W_A + un * 32768 + 2 * G * 4096 + tid * 16, o[0]); st16(r, W_A + un * 32768 + (2 * G + 1) * 4096 + tid * 16, o[1]); }
    DRAIN(); __syncthreads();
    if (tid == 0) __hip_atomic_fetch_add((unsigned*)(p.ws + F_CVY + (un >> 3) * 128), 1u, __ATOMIC_RELAXED, __HIP_MEMORY_SCOPE_AGENT);
    return true;
}

__device__ __forceinline__ void proj_block(const Params& p, unsigned char* lds, const int wv, const int h, const int c, const int j) {
    const int w = wv, l = otid() & 63, n = l & 15, g = l >> 4, tm = w >> 1, tn = w & 1;
    const unsigned lb = __builtin_amdgcn_readfirstlane((unsigned)(size_t)lds);
    const v4u sx = mk_srd(p.Aq, S * D * 2), sq = mk_srd(c < 2 ? p.Wq : p.Wk, 1024 * 4096 * 2), sv = mk_srd(p.Wv, 1024 * 4096 * 2), sw = mk_srd(p.ws, W_TOTAL);
    const unsigned lx = lb + (32 * tm + n) * 128 + ((g ^ (n & 7)) << 4), lr = lb + 131072, rr = 8 * g + (n >> 2);
    const int cv = ((c & 1) ^ 1) * 2 + (c >> 1);
    const unsigned aq = (c < 2 ? W_QX : W_KX) + (tm * 32 + h) * 8192 + (2 * (c & 1) + tn) * 1024 + l * 16;
    const int hq = h & 7, tk = ((hq - (hq > 1) - (hq > 5)) * 4 + c) * 4 + w, ts6 = tk % 6, tch = 15 - tk / 6, tx = tk - 6 * GTOUCH;
    const bf16_t* tp = (h & 3) == 1 ? p.gate_w : tx < 0 ? (ts6 % 3 == 0 ? p.Wq : ts6 % 3 == 1 ? p.Wk : p.Wv) + (size_t)(tch * 64 + l) * 4096 + ((h & 24) + 1 + (ts6 / 3) * 4) * 128 + 64
                     : tx < XTOUCH ? p.Aq + (tx * 64 + l) * 64 : p.gate_w;
    unsigned t0, xo, so, cn; unsigned long long ts, ta, tb, tc, td;
    asm volatile(
        "s_mov_b32 %[xo], 0\n\t"
        "s_mov_b32 %[so], 0\n\t"
        "s_mov_b32 %[cn], 1\n\t"
        "global_load_dword v88, v[114:115], off\n\t"
        "s_add_u32 m0, %[ld], 0\n\t"
        "s_add_u32 %[t0], %[xo], 0\n\t"
        "buffer_load_dwordx4 v100, %[sx], %[t0] offen lds\n\t"
        "s_add_u32 m0, %[ld], 1024\n\t"
        "s_add_u32 %[t0], %[xo], 16384\n\t"
        "buffer_load_dwordx4 v100, %[sx], %[t0] offen lds\n\t"
        "s_add_u32 m0, %[ld], 8192\n\t"
        "s_add_u32 %[t0], %[xo], 128\n\t"
        "buffer_load_dwordx4 v100, %[sx], %[t0] offen lds\n\t"
        "s_add_u32 m0, %[ld], 9216\n\t"
        "s_add_u32 %[t0], %[xo], 16512\n\t"
        "buffer_load_dwordx4 v100, %[sx], %[t0] offen lds\n\t"
        "s_add_u32 m0, %[ld], 16384\n\t"
        "s_add_u32 %[t0], %[xo], 256\n\t"
        "buffer_load_dwordx4 v100, %[sx], %[t0] offen lds\n\t"
        "s_add_u32 m0, %[ld], 17408\n\t"
        "s_add_u32 %[t0], %[xo], 16640\n\t"
        "buffer_load_dwordx4 v100, %[sx], %[t0] offen lds\n\t"
        "s_add_u32 m0, %[ld], 24576\n\t"
        "s_add_u32 %[t0], %[xo], 384\n\t"
        "buffer_load_dwordx4 v100, %[sx], %[t0] offen lds\n\t"
        "s_add_u32 m0, %[ld], 25600\n\t"
        "s_add_u32 %[t0], %[xo], 16768\n\t"
        "buffer_load_dwordx4 v100, %[sx], %[t0] offen lds\n\t"
        "s_mov_b32 %[t0], 0\n\t"
        "buffer_load_dwordx4 a[192:195], v101, %[sq], %[t0] offen\n\t"
        "buffer_load_dwordx4 a[200:203], v112, %[sv], %[t0] offen\n\t"
        "s_mov_b32 %[t0], 262144\n\t"
        "buffer_load_dwordx4 a[196:199], v101, %[sq], %[t0] offen\n\t"
        "s_mov_b32 %[t0], 524288\n\t"
        "buffer_load_dwordx4 a[204:207], v101, %[sq], %[t0] offen\n\t"
        "buffer_load_dwordx4 a[212:215], v112, %[sv], %[t0] offen\n\t"
        "s_mov_b32 %[t0], 786432\n\t"
        "buffer_load_dwordx4 a[208:211], v101, %[sq], %[t0] offen\n\t"
        "s_mov_b32 %[t0], 1048576\n\t"
        "buffer_load_dwordx4 a[216:219], v101, %[sq], %[t0] offen\n\t"
        "buffer_load_dwordx4 a[224:227], v112, %[sv], %[t0] offen\n\t"
        "s_mov_b32 %[t0], 1310720\n\t"
        "buffer_load_dwordx4 a[220:223], v101, %[sq], %[t0] offen\n\t"
        "s_mov_b32 %[t0], 1572864\n\t"
        "buffer_load_dwordx4 a[228:231], v101, %[sq], %[t0] offen\n\t"
        "buffer_load_dwordx4 a[236:239], v112, %[sv], %[t0] offen\n\t"
        "s_mov_b32 %[t0], 1835008\n\t"
        "buffer_load_dwordx4 a[232:235], v101, %[sq], %[t0] offen\n\t"
        "s_waitcnt vmcnt(9)\n\t"
        "ds_write_b128 v102, a[192:195] offset:0\n\t"
        "ds_write_b128 v102, a[196:199] offset:4096\n\t"
        "ds_write_b128 v116, a[200:203] offset:8192\n\t"
        "s_waitcnt lgkmcnt(0)\n\t"
        "s_barrier\n\t"
        "ds_read_b64_tr_b16 a[0:1], v103 offset:0\n\t"
        "ds_read_b64_tr_b16 a[2:3], v103 offset:512\n\t"
        "ds_read_b64_tr_b16 a[4:5], v103 offset:8\n\t"
        "ds_read_b64_tr_b16 a[6:7], v103 offset:520\n\t"
        "ds_read_b64_tr_b16 a[8:9], v104 offset:0\n\t"
        "ds_read_b64_tr_b16 a[10:11], v104 offset:256\n\t"
        "s_waitcnt lgkmcnt(5)\n\t"
        "ds_read_b64_tr_b16 a[12:13], v103 offset:4096\n\t"
        "ds_read_b64_tr_b16 a[14:15], v103 offset:4608\n\t"
        "ds_read_b64_tr_b16 a[16:17], v103 offset:4104\n\t"
        "ds_read_b64_tr_b16 a[18:19], v103 offset:4616\n\t"
        "ds_read_b64_tr_b16 a[20:21], v104 offset:2048\n\t"
        "ds_read_b64_tr_b16 a[22:23], v104 offset:2304\n\t"
        "ds_read_b128 v[56:59], v105 offset:0\n\t"
        "ds_read_b128 v[60:63], v105 offset:2048\n\t"
        "ds_read_b128 v[64:67], v106 offset:0\n\t"
        "ds_read_b128 v[68:71], v106 offset:2048\n\t"
        "s_add_u32 m0, %[ld], 32768\n\t"
        "s_add_u32 %[t0], %[xo], 512\n\t"
        "buffer_load_dwordx4 v100, %[sx], %[t0] offen lds\n\t"
        "s_add_u32 m0, %[ld], 33792\n\t"
        "s_add_u32 %[t0], %[xo], 16896\n\t"
        "buffer_load_dwordx4 v100, %[sx], %[t0] offen lds\n\t"
        "s_add_u32 m0, %[ld], 40960\n\t"
        "s_add_u32 %[t0], %[xo], 640\n\t"
        "buffer_load_dwordx4 v100, %[sx], %[t0] offen lds\n\t"
        "s_add_u32 m0, %[ld], 41984\n\t"
        "s_add_u32 %[t0], %[xo], 17024\n\t"
        "buffer_load_dwordx4 v100, %[sx], %[t0] offen lds\n\t"
        "s_add_u32 m0, %[ld], 49152\n\t"
        "s_add_u32 %[t0], %[xo], 768\n\t"
        "buffer_load_dwordx4 v100, %[sx], %[t0] offen lds\n\t"
        "s_add_u32 m0, %[ld], 50176\n\t"
        "s_add_u32 %[t0], %[xo], 17152\n\t"
        "buffer_load_dwordx4 v100, %[sx], %[t0] offen lds\n\t"
        "s_add_u32 m0, %[ld], 57344\n\t"
        "s_add_u32 %[t0], %[xo], 896\n\t"
        "buffer_load_dwordx4 v100, %[sx], %[t0] offen lds\n\t"
        "s_add_u32 m0, %[ld], 58368\n\t"
        "s_add_u32 %[t0], %[xo], 17280\n\t"
        "buffer_load_dwordx4 v100, %[sx], %[t0] offen lds\n\t"
        "s_mov_b32 %[t0], 2097152\n\t"
        "buffer_load_dwordx4 a[240:243], v101, %[sq], %[t0] offen\n\t"
        "buffer_load_dwordx4 a[248:251], v112, %[sv], %[t0] offen\n\t"
        "s_mov_b32 %[t0], 2359296\n\t"
        "buffer_load_dwordx4 a[244:247], v101, %[sq], %[t0] offen\n\t"
        "s_waitcnt vmcnt(17)\n\t"
        "ds_write_b128 v102, a[204:207] offset:16384\n\t"
        "ds_write_b128 v102, a[208:211] offset:20480\n\t"
        "ds_write_b128 v116, a[212:215] offset:24576\n\t"
        "s_waitcnt lgkmcnt(0)\n\t"
        "s_barrier\n\t"
        "v_mfma_f32_16x16x32_bf16 v[8:11], a[0:3], v[56:59], 0\n\t"
        "ds_read_b64_tr_b16 a[24:25], v103 offset:16384\n\t"
        "ds_read_b64_tr_b16 a[26:27], v103 offset:16896\n\t"
        "v_mfma_f32_16x16x32_bf16 v[12:15], a[4:7], v[56:59], 0\n\t"
        "ds_read_b64_tr_b16 a[28:29], v103 offset:16392\n\t"
        "ds_read_b64_tr_b16 a[30:31], v103 offset:16904\n\t"
        "v_mfma_f32_16x16x32_bf16 v[16:19], v[56:59], a[8:11], 0\n\t"
        "ds_read_b64_tr_b16 a[32:33], v104 offset:16384\n\t"
        "ds_read_b64_tr_b16 a[34:35], v104 offset:16640\n\t"
        "v_mfma_f32_16x16x32_bf16 v[20:23], a[0:3], v[60:63], 0\n\t"
        "s_waitcnt lgkmcnt(5)\n\t"
        "ds_read_b64_tr_b16 a[36:37], v103 offset:20480\n\t"
        "v_mfma_f32_16x16x32_bf16 v[24:27], a[4:7], v[60:63], 0\n\t"
        "ds_read_b64_tr_b16 a[38:39], v103 offset:20992\n\t"
        "ds_read_b64_tr_b16 a[40:41], v103 offset:20488\n\t"
        "v_mfma_f32_16x16x32_bf16 v[28:31], v[60:63], a[8:11], 0\n\t"
        "ds_read_b64_tr_b16 a[42:43], v103 offset:21000\n\t"
        "ds_read_b64_tr_b16 a[44:45], v104 offset:18432\n\t"
        "v_mfma_f32_16x16x32_bf16 v[8:11], a[12:15], v[64:67], v[8:11]\n\t"
        "ds_read_b64_tr_b16 a[46:47], v104 offset:18688\n\t"
        "ds_read_b128 v[72:75], v105 offset:8192\n\t"
        "v_mfma_f32_16x16x32_bf16 v[12:15], a[16:19], v[64:67], v[12:15]\n\t"
        "ds_read_b128 v[76:79], v105 offset:10240\n\t"
        "ds_read_b128 v[80:83], v106 offset:8192\n\t"
        "v_mfma_f32_16x16x32_bf16 v[16:19], v[64:67], a[20:23], v[16:19]\n\t"
        "ds_read_b128 v[84:87], v106 offset:10240\n\t"
        "v_mfma_f32_16x16x32_bf16 v[20:23], a[12:15], v[68:71], v[20:23]\n\t"
        "v_mfma_f32_16x16x32_bf16 v[24:27], a[16:19], v[68:71], v[24:27]\n\t"
        "v_mfma_f32_16x16x32_bf16 v[28:31], v[68:71], a[20:23], v[28:31]\n\t"
        "s_mov_b32 %[t0], 2621440\n\t"
        "buffer_load_dwordx4 a[252:255], v101, %[sq], %[t0] offen\n\t"
        "buffer_load_dwordx4 v[132:135], v112, %[sv], %[t0] offen\n\t"
        "s_mov_b32 %[t0], 2883584\n\t"
        "buffer_load_dwordx4 v[128:131], v101, %[sq], %[t0] offen\n\t"
        "s_waitcnt vmcnt(17)\n\t"
        "ds_write_b128 v102, a[216:219] offset:0\n\t"
        "ds_write_b128 v102, a[220:223] offset:4096\n\t"
        "ds_write_b128 v116, a[224:227] offset:8192\n\t"
        "s_waitcnt lgkmcnt(0)\n\t"
        "s_barrier\n\t"
        "v_mfma_f32_16x16x32_bf16 v[8:11], a[24:27], v[72:75], v[8:11]\n\t"
        "ds_read_b64_tr_b16 a[48:49], v103 offset:0\n\t"
        "ds_read_b64_tr_b16 a[50:51], v103 offset:512\n\t"
        "v_mfma_f32_16x16x32_bf16 v[12:15], a[28:31], v[72:75], v[12:15]\n\t"
        "ds_read_b64_tr_b16 a[52:53], v103 offset:8\n\t"
        "ds_read_b64_tr_b16 a[54:55], v103 offset:520\n\t"
        "v_mfma_f32_16x16x32_bf16 v[16:19], v[72:75], a[32:35], v[16:19]\n\t"
        "ds_read_b64_tr_b16 a[56:57], v104 offset:0\n\t"
        "ds_read_b64_tr_b16 a[58:59], v104 offset:256\n\t"
        "v_mfma_f32_16x16x32_bf16 v[20:23], a[24:27], v[76:79], v[20:23]\n\t"
        "s_waitcnt lgkmcnt(5)\n\t"
        "ds_read_b64_tr_b16 a[60:61], v103 offset:4096\n\t"
        "v_mfma_f32_16x16x32_bf16 v[24:27], a[28:31], v[76:79], v[24:27]\n\t"
        "ds_read_b64_tr_b16 a[62:63], v103 offset:4608\n\t"
        "ds_read_b64_tr_b16 a[64:65], v103 offset:4104\n\t"
        "v_mfma_f32_16x16x32_bf16 v[28:31], v[76:79], a[32:35], v[28:31]\n\t"
        "ds_read_b64_tr_b16 a[66:67], v103 offset:4616\n\t"
        "ds_read_b64_tr_b16 a[68:69], v104 offset:2048\n\t"
        "v_mfma_f32_16x16x32_bf16 v[8:11], a[36:39], v[80:83], v[8:11]\n\t"
        "ds_read_b64_tr_b16 a[70:71], v104 offset:2304\n\t"
        "ds_read_b128 v[56:59], v105 offset:16384\n\t"
        "v_mfma_f32_16x16x32_bf16 v[12:15], a[40:43], v[80:83], v[12:15]\n\t"
        "ds_read_b128 v[60:63], v105 offset:18432\n\t"
        "ds_read_b128 v[64:67], v106 offset:16384\n\t"
        "v_mfma_f32_16x16x32_bf16 v[16:19], v[80:83], a[44:47], v[16:19]\n\t"
        "ds_read_b128 v[68:71], v106 offset:18432\n\t"
        "v_mfma_f32_16x16x32_bf16 v[20:23], a[36:39], v[84:87], v[20:23]\n\t"
        "v_mfma_f32_16x16x32_bf16 v[24:27], a[40:43], v[84:87], v[24:27]\n\t"
        "v_mfma_f32_16x16x32_bf16 v[28:31], v[84:87], a[44:47], v[28:31]\n\t"
        "s_mov_b32 %[t0], 3145728\n\t"
        "buffer_load_dwordx4 v[136:139], v101, %[sq], %[t0] offen\n\t"
        "buffer_load_dwordx4 v[144:147], v112, %[sv], %[t0] offen\n\t"
        "s_mov_b32 %[t0], 3407872\n\t"
        "buffer_load_dwordx4 v[140:143], v101, %[sq], %[t0] offen\n\t"
        "s_waitcnt vmcnt(17)\n\t"
        "ds_write_b128 v102, a[228:231] offset:16384\n\t"
        "ds_write_b128 v102, a[232:235] offset:20480\n\t"
        "ds_write_b128 v116, a[236:239] offset:24576\n\t"
        "s_waitcnt lgkmcnt(0)\n\t"
        "s_barrier\n\t"
        "v_mfma_f32_16x16x32_bf16 v[8:11], a[48:51], v[56:59], v[8:11]\n\t"
        "ds_read_b64_tr_b16 a[72:73], v103 offset:16384\n\t"
        "ds_read_b64_tr_b16 a[74:75], v103 offset:16896\n\t"
        "v_mfma_f32_16x16x32_bf16 v[12:15], a[52:55], v[56:59], v[12:15]\n\t"
        "ds_read_b64_tr_b16 a[76:77], v103 offset:16392\n\t"
        "ds_read_b64_tr_b16 a[78:79], v103 offset:16904\n\t"
        "v_mfma_f32_16x16x32_bf16 v[16:19], v[56:59], a[56:59], v[16:19]\n\t"
        "ds_read_b64_tr_b16 a[80:81], v104 offset:16384\n\t"
        "ds_read_b64_tr_b16 a[82:83], v104 offset:16640\n\t"
        "v_mfma_f32_16x16x32_bf16 v[20:23], a[48:51], v[60:63], v[20:23]\n\t"
        "s_waitcnt lgkmcnt(5)\n\t"
        "ds_read_b64_tr_b16 a[84:85], v103 offset:20480\n\t"
        "v_mfma_f32_16x16x32_bf16 v[24:27], a[52:55], v[60:63], v[24:27]\n\t"
        "ds_read_b64_tr_b16 a[86:87], v103 offset:20992\n\t"
        "ds_read_b64_tr_b16 a[88:89], v103 offset:20488\n\t"
        "v_mfma_f32_16x16x32_bf16 v[28:31], v[60:63], a[56:59], v[28:31]\n\t"
        "ds_read_b64_tr_b16 a[90:91], v103 offset:21000\n\t"
        "ds_read_b64_tr_b16 a[92:93], v104 offset:18432\n\t"
        "v_mfma_f32_16x16x32_bf16 v[8:11], a[60:63], v[64:67], v[8:11]\n\t"
        "ds_read_b64_tr_b16 a[94:95], v104 offset:18688\n\t"
        "ds_read_b128 v[72:75], v105 offset:24576\n\t"
        "v_mfma_f32_16x16x32_bf16 v[12:15], a[64:67], v[64:67], v[12:15]\n\t"
        "ds_read_b128 v[76:79], v105 offset:26624\n\t"
        "ds_read_b128 v[80:83], v106 offset:24576\n\t"
        "v_mfma_f32_16x16x32_bf16 v[16:19], v[64:67], a[68:71], v[16:19]\n\t"
        "ds_read_b128 v[84:87], v106 offset:26624\n\t"
        "v_mfma_f32_16x16x32_bf16 v[20:23], a[60:63], v[68:71], v[20:23]\n\t"
        "v_mfma_f32_16x16x32_bf16 v[24:27], a[64:67], v[68:71], v[24:27]\n\t"
        "v_mfma_f32_16x16x32_bf16 v[28:31], v[68:71], a[68:71], v[28:31]\n\t"
        "s_mov_b32 %[t0], 3670016\n\t"
        "buffer_load_dwordx4 v[148:151], v101, %[sq], %[t0] offen\n\t"
        "buffer_load_dwordx4 v[156:159], v112, %[sv], %[t0] offen\n\t"
        "s_mov_b32 %[t0], 3932160\n\t"
        "buffer_load_dwordx4 v[152:155], v101, %[sq], %[t0] offen\n\t"
        "s_waitcnt vmcnt(9)\n\t"
        "ds_write_b128 v102, a[240:243] offset:0\n\t"
        "ds_write_b128 v102, a[244:247] offset:4096\n\t"
        "ds_write_b128 v116, a[248:251] offset:8192\n\t"
        "s_waitcnt lgkmcnt(0)\n\t"
        "s_barrier\n\t"
        "v_mfma_f32_16x16x32_bf16 v[8:11], a[72:75], v[72:75], v[8:11]\n\t"
        "ds_read_b64_tr_b16 a[96:97], v103 offset:0\n\t"
        "ds_read_b64_tr_b16 a[98:99], v103 offset:512\n\t"
        "v_mfma_f32_16x16x32_bf16 v[12:15], a[76:79], v[72:75], v[12:15]\n\t"
        "ds_read_b64_tr_b16 a[100:101], v103 offset:8\n\t"
        "ds_read_b64_tr_b16 a[102:103], v103 offset:520\n\t"
        "v_mfma_f32_16x16x32_bf16 v[16:19], v[72:75], a[80:83], v[16:19]\n\t"
        "ds_read_b64_tr_b16 a[104:105], v104 offset:0\n\t"
        "ds_read_b64_tr_b16 a[106:107], v104 offset:256\n\t"
        "v_mfma_f32_16x16x32_bf16 v[20:23], a[72:75], v[76:79], v[20:23]\n\t"
        "s_waitcnt lgkmcnt(5)\n\t"
        "ds_read_b64_tr_b16 a[108:109], v103 offset:4096\n\t"
        "v_mfma_f32_16x16x32_bf16 v[24:27], a[76:79], v[76:79], v[24:27]\n\t"
        "ds_read_b64_tr_b16 a[110:111], v103 offset:4608\n\t"
        "ds_read_b64_tr_b16 a[112:113], v103 offset:4104\n\t"
        "v_mfma_f32_16x16x32_bf16 v[28:31], v[76:79], a[80:83], v[28:31]\n\t"
        "ds_read_b64_tr_b16 a[114:115], v103 offset:4616\n\t"
        "ds_read_b64_tr_b16 a[116:117], v104 offset:2048\n\t"
        "v_mfma_f32_16x16x32_bf16 v[8:11], a[84:87], v[80:83], v[8:11]\n\t"
        "ds_read_b64_tr_b16 a[118:119], v104 offset:2304\n\t"
        "ds_read_b128 v[56:59], v105 offset:32768\n\t"
        "v_mfma_f32_16x16x32_bf16 v[12:15], a[88:91], v[80:83], v[12:15]\n\t"
        "ds_read_b128 v[60:63], v105 offset:34816\n\t"
        "ds_read_b128 v[64:67], v106 offset:32768\n\t"
        "v_mfma_f32_16x16x32_bf16 v[16:19], v[80:83], a[92:95], v[16:19]\n\t"
        "ds_read_b128 v[68:71], v106 offset:34816\n\t"
        "v_mfma_f32_16x16x32_bf16 v[20:23], a[84:87], v[84:87], v[20:23]\n\t"
        "v_mfma_f32_16x16x32_bf16 v[24:27], a[88:91], v[84:87], v[24:27]\n\t"
        "v_mfma_f32_16x16x32_bf16 v[28:31], v[84:87], a[92:95], v[28:31]\n\t"
        "s_add_u32 m0, %[ld], 65536\n\t"
        "s_add_u32 %[t0], %[xo], 1024\n\t"
        "buffer_load_dwordx4 v100, %[sx], %[t0] offen lds\n\t"
        "s_add_u32 m0, %[ld], 66560\n\t"
        "s_add_u32 %[t0], %[xo], 17408\n\t"
        "buffer_load_dwordx4 v100, %[sx], %[t0] offen lds\n\t"
        "s_add_u32 m0, %[ld], 73728\n\t"
        "s_add_u32 %[t0], %[xo], 1152\n\t"
        "buffer_load_dwordx4 v100, %[sx], %[t0] offen lds\n\t"
        "s_add_u32 m0, %[ld], 74752\n\t"
        "s_add_u32 %[t0], %[xo], 17536\n\t"
        "buffer_load_dwordx4 v100, %[sx], %[t0] offen lds\n\t"
        "s_add_u32 m0, %[ld], 81920\n\t"
        "s_add_u32 %[t0], %[xo], 1280\n\t"
        "buffer_load_dwordx4 v100, %[sx], %[t0] offen lds\n\t"
        "s_add_u32 m0, %[ld], 82944\n\t"
        "s_add_u32 %[t0], %[xo], 17664\n\t"
        "buffer_load_dwordx4 v100, %[sx], %[t0] offen lds\n\t"
        "s_add_u32 m0, %[ld], 90112\n\t"
        "s_add_u32 %[t0], %[xo], 1408\n\t"
        "buffer_load_dwordx4 v100, %[sx], %[t0] offen lds\n\t"
        "s_add_u32 m0, %[ld], 91136\n\t"
        "s_add_u32 %[t0], %[xo], 17792\n\t"
        "buffer_load_dwordx4 v100, %[sx], %[t0] offen lds\n\t"
        "s_mov_b32 %[t0], 4194304\n\t"
        "buffer_load_dwordx4 v[160:163], v101, %[sq], %[t0] offen\n\t"
        "buffer_load_dwordx4 v[168:171], v112, %[sv], %[t0] offen\n\t"
        "s_mov_b32 %[t0], 4456448\n\t"
        "buffer_load_dwordx4 v[164:167], v101, %[sq], %[t0] offen\n\t"
        "s_waitcnt vmcnt(17)\n\t"
        "ds_write_b128 v102, a[252:255] offset:16384\n\t"
        "ds_write_b128 v102, v[128:131] offset:20480\n\t"
        "ds_write_b128 v116, v[132:135] offset:24576\n\t"
        "s_waitcnt lgkmcnt(0)\n\t"
        "s_barrier\n\t"
        "v_mfma_f32_16x16x32_bf16 v[8:11], a[96:99], v[56:59], v[8:11]\n\t"
        "ds_read_b64_tr_b16 a[120:121], v103 offset:16384\n\t"
        "ds_read_b64_tr_b16 a[122:123], v103 offset:16896\n\t"
        "v_mfma_f32_16x16x32_bf16 v[12:15], a[100:103], v[56:59], v[12:15]\n\t"
        "ds_read_b64_tr_b16 a[124:125], v103 offset:16392\n\t"
        "ds_read_b64_tr_b16 a[126:127], v103 offset:16904\n\t"
        "v_mfma_f32_16x16x32_bf16 v[16:19], v[56:59], a[104:107], v[16:19]\n\t"
        "ds_read_b64_tr_b16 a[128:129], v104 offset:16384\n\t"
        "ds_read_b64_tr_b16 a[130:131], v104 offset:16640\n\t"
        "v_mfma_f32_16x16x32_bf16 v[20:23], a[96:99], v[60:63], v[20:23]\n\t"
        "s_waitcnt lgkmcnt(5)\n\t"
        "ds_read_b64_tr_b16 a[132:133], v103 offset:20480\n\t"
        "v_mfma_f32_16x16x32_bf16 v[24:27], a[100:103], v[60:63], v[24:27]\n\t"
        "ds_read_b64_tr_b16 a[134:135], v103 offset:20992\n\t"
        "ds_read_b64_tr_b16 a[136:137], v103 offset:20488\n\t"
        "v_mfma_f32_16x16x32_bf16 v[28:31], v[60:63], a[104:107], v[28:31]\n\t"
        "ds_read_b64_tr_b16 a[138:139], v103 offset:21000\n\t"
        "ds_read_b64_tr_b16 a[140:141], v104 offset:18432\n\t"
        "v_mfma_f32_16x16x32_bf16 v[8:11], a[108:111], v[64:67], v[8:11]\n\t"
        "ds_read_b64_tr_b16 a[142:143], v104 offset:18688\n\t"
        "ds_read_b128 v[72:75], v105 offset:40960\n\t"
        "v_mfma_f32_16x16x32_bf16 v[12:15], a[112:115], v[64:67], v[12:15]\n\t"
        "ds_read_b128 v[76:79], v105 offset:43008\n\t"
        "ds_read_b128 v[80:83], v106 offset:40960\n\t"
        "v_mfma_f32_16x16x32_bf16 v[16:19], v[64:67], a[116:119], v[16:19]\n\t"
        "ds_read_b128 v[84:87], v106 offset:43008\n\t"
        "v_mfma_f32_16x16x32_bf16 v[20:23], a[108:111], v[68:71], v[20:23]\n\t"
        "v_mfma_f32_16x16x32_bf16 v[24:27], a[112:115], v[68:71], v[24:27]\n\t"
        "v_mfma_f32_16x16x32_bf16 v[28:31], v[68:71], a[116:119], v[28:31]\n\t"
        "s_mov_b32 %[t0], 4718592\n\t"
        "buffer_load_dwordx4 v[172:175], v101, %[sq], %[t0] offen\n\t"
        "buffer_load_dwordx4 v[180:183], v112, %[sv], %[t0] offen\n\t"
        "s_mov_b32 %[t0], 4980736\n\t"
        "buffer_load_dwordx4 v[176:179], v101, %[sq], %[t0] offen\n\t"
        "s_waitcnt vmcnt(17)\n\t"
        "ds_write_b128 v102, v[136:139] offset:0\n\t"
        "ds_write_b128 v102, v[140:143] offset:4096\n\t"
        "ds_write_b128 v116, v[144:147] offset:8192\n\t"
        "s_waitcnt lgkmcnt(0)\n\t"
        "s_barrier\n\t"
        "v_mfma_f32_16x16x32_bf16 v[8:11], a[120:123], v[72:75], v[8:11]\n\t"
        "ds_read_b64_tr_b16 a[144:145], v103 offset:0\n\t"
        "ds_read_b64_tr_b16 a[146:147], v103 offset:512\n\t"
        "v_mfma_f32_16x16x32_bf16 v[12:15], a[124:127], v[72:75], v[12:15]\n\t"
        "ds_read_b64_tr_b16 a[148:149], v103 offset:8\n\t"
        "ds_read_b64_tr_b16 a[150:151], v103 offset:520\n\t"
        "v_mfma_f32_16x16x32_bf16 v[16:19], v[72:75], a[128:131], v[16:19]\n\t"
        "ds_read_b64_tr_b16 a[152:153], v104 offset:0\n\t"
        "ds_read_b64_tr_b16 a[154:155], v104 offset:256\n\t"
        "v_mfma_f32_16x16x32_bf16 v[20:23], a[120:123], v[76:79], v[20:23]\n\t"
        "s_waitcnt lgkmcnt(5)\n\t"
        "ds_read_b64_tr_b16 a[156:157], v103 offset:4096\n\t"
        "v_mfma_f32_16x16x32_bf16 v[24:27], a[124:127], v[76:79], v[24:27]\n\t"
        "ds_read_b64_tr_b16 a[158:159], v103 offset:4608\n\t"
        "ds_read_b64_tr_b16 a[160:161], v103 offset:4104\n\t"
        "v_mfma_f32_16x16x32_bf16 v[28:31], v[76:79], a[128:131], v[28:31]\n\t"
        "ds_read_b64_tr_b16 a[162:163], v103 offset:4616\n\t"
        "ds_read_b64_tr_b16 a[164:165], v104 offset:2048\n\t"
        "v_mfma_f32_16x16x32_bf16 v[8:11], a[132:135], v[80:83], v[8:11]\n\t"
        "ds_read_b64_tr_b16 a[166:167], v104 offset:2304\n\t"
        "ds_read_b128 v[56:59], v105 offset:49152\n\t"
        "v_mfma_f32_16x16x32_bf16 v[12:15], a[136:139], v[80:83], v[12:15]\n\t"
        "ds_read_b128 v[60:63], v105 offset:51200\n\t"
        "ds_read_b128 v[64:67], v106 offset:49152\n\t"
        "v_mfma_f32_16x16x32_bf16 v[16:19], v[80:83], a[140:143], v[16:19]\n\t"
        "ds_read_b128 v[68:71], v106 offset:51200\n\t"
        "v_mfma_f32_16x16x32_bf16 v[20:23], a[132:135], v[84:87], v[20:23]\n\t"
        "v_mfma_f32_16x16x32_bf16 v[24:27], a[136:139], v[84:87], v[24:27]\n\t"
        "v_mfma_f32_16x16x32_bf16 v[28:31], v[84:87], a[140:143], v[28:31]\n\t"
        "s_mov_b32 %[t0], 5242880\n\t"
        "buffer_load_dwordx4 v[184:187], v101, %[sq], %[t0] offen\n\t"
        "buffer_load_dwordx4 v[192:195], v112, %[sv], %[t0] offen\n\t"
        "s_mov_b32 %[t0], 5505024\n\t"
        "buffer_load_dwordx4 v[188:191], v101, %[sq], %[t0] offen\n\t"
        "s_waitcnt vmcnt(17)\n\t"
        "ds_write_b128 v102, v[148:151] offset:16384\n\t"
        "ds_write_b128 v102, v[152:155] offset:20480\n\t"
        "ds_write_b128 v116, v[156:159] offset:24576\n\t"
        "s_waitcnt lgkmcnt(0)\n\t"
        "s_barrier\n\t"
        "v_mfma_f32_16x16x32_bf16 v[8:11], a[144:147], v[56:59], v[8:11]\n\t"
        "ds_read_b64_tr_b16 a[168:169], v103 offset:16384\n\t"
        "ds_read_b64_tr_b16 a[170:171], v103 offset:16896\n\t"
        "v_mfma_f32_16x16x32_bf16 v[12:15], a[148:151], v[56:59], v[12:15]\n\t"
        "ds_read_b64_tr_b16 a[172:173], v103 offset:16392\n\t"
        "ds_read_b64_tr_b16 a[174:175], v103 offset:16904\n\t"
        "v_mfma_f32_16x16x32_bf16 v[16:19], v[56:59], a[152:155], v[16:19]\n\t"
        "ds_read_b64_tr_b16 a[176:177], v104 offset:16384\n\t"
        "ds_read_b64_tr_b16 a[178:179], v104 offset:16640\n\t"
        "v_mfma_f32_16x16x32_bf16 v[20:23], a[144:147], v[60:63], v[20:23]\n\t"
        "s_waitcnt lgkmcnt(5)\n\t"
        "ds_read_b64_tr_b16 a[180:181], v103 offset:20480\n\t"
        "v_mfma_f32_16x16x32_bf16 v[24:27], a[148:151], v[60:63], v[24:27]\n\t"
        "ds_read_b64_tr_b16 a[182:183], v103 offset:20992\n\t"
        "ds_read_b64_tr_b16 a[184:185], v103 offset:20488\n\t"
        "v_mfma_f32_16x16x32_bf16 v[28:31], v[60:63], a[152:155], v[28:31]\n\t"
        "ds_read_b64_tr_b16 a[186:187], v103 offset:21000\n\t"
        "ds_read_b64_tr_b16 a[188:189], v104 offset:18432\n\t"
        "v_mfma_f32_16x16x32_bf16 v[8:11], a[156:159], v[64:67], v[8:11]\n\t"
        "ds_read_b64_tr_b16 a[190:191], v104 offset:18688\n\t"
        "ds_read_b128 v[72:75], v105 offset:57344\n\t"
        "v_mfma_f32_16x16x32_bf16 v[12:15], a[160:163], v[64:67], v[12:15]\n\t"
        "ds_read_b128 v[76:79], v105 offset:59392\n\t"
        "ds_read_b128 v[80:83], v106 offset:57344\n\t"
        "v_mfma_f32_16x16x32_bf16 v[16:19], v[64:67], a[164:167], v[16:19]\n\t"
        "ds_read_b128 v[84:87], v106 offset:59392\n\t"
        "v_mfma_f32_16x16x32_bf16 v[20:23], a[156:159], v[68:71], v[20:23]\n\t"
        "v_mfma_f32_16x16x32_bf16 v[24:27], a[160:163], v[68:71], v[24:27]\n\t"
        "v_mfma_f32_16x16x32_bf16 v[28:31], v[68:71], a[164:167], v[28:31]\n\t"
        "s_mov_b32 %[t0], 5767168\n\t"
        "buffer_load_dwordx4 v[196:199], v101, %[sq], %[t0] offen\n\t"
        "buffer_load_dwordx4 v[204:207], v112, %[sv], %[t0] offen\n\t"
        "s_mov_b32 %[t0], 6029312\n\t"
        "buffer_load_dwordx4 v[200:203], v101, %[sq], %[t0] offen\n\t"
        "s_waitcnt vmcnt(9)\n\t"
        "ds_write_b128 v102, v[160:163] offset:0\n\t"
        "ds_write_b128 v102, v[164:167] offset:4096\n\t"
        "ds_write_b128 v116, v[168:171] offset:8192\n\t"
        "s_waitcnt lgkmcnt(0)\n\t"
        "s_barrier\n\t"
        "v_mfma_f32_16x16x32_bf16 v[8:11], a[168:171], v[72:75], v[8:11]\n\t"
        "ds_read_b64_tr_b16 a[192:193], v103 offset:0\n\t"
        "ds_read_b64_tr_b16 a[194:195], v103 offset:512\n\t"
        "v_mfma_f32_16x16x32_bf16 v[12:15], a[172:175], v[72:75], v[12:15]\n\t"
        "ds_read_b64_tr_b16 a[196:197], v103 offset:8\n\t"
        "ds_read_b64_tr_b16 a[198:199], v103 offset:520\n\t"
        "v_mfma_f32_16x16x32_bf16 v[16:19], v[72:75], a[176:179], v[16:19]\n\t"
        "ds_read_b64_tr_b16 a[200:201], v104 offset:0\n\t"
        "ds_read_b64_tr_b16 a[202:203], v104 offset:256\n\t"
        "v_mfma_f32_16x16x32_bf16 v[20:23], a[168:171], v[76:79], v[20:23]\n\t"
        "s_waitcnt lgkmcnt(5)\n\t"
        "ds_read_b64_tr_b16 a[204:205], v103 offset:4096\n\t"
        "v_mfma_f32_16x16x32_bf16 v[24:27], a[172:175], v[76:79], v[24:27]\n\t"
        "ds_read_b64_tr_b16 a[206:207], v103 offset:4608\n\t"
        "ds_read_b64_tr_b16 a[208:209], v103 offset:4104\n\t"
        "v_mfma_f32_16x16x32_bf16 v[28:31], v[76:79], a[176:179], v[28:31]\n\t"
        "ds_read_b64_tr_b16 a[210:211], v103 offset:4616\n\t"
        "ds_read_b64_tr_b16 a[212:213], v104 offset:2048\n\t"
        "v_mfma_f32_16x16x32_bf16 v[8:11], a[180:183], v[80:83], v[8:11]\n\t"
        "ds_read_b64_tr_b16 a[214:215], v104 offset:2304\n\t"
        "ds_read_b128 v[56:59], v107 offset:0\n\t"
        "v_mfma_f32_16x16x32_bf16 v[12:15], a[184:187], v[80:83], v[12:15]\n\t"
        "ds_read_b128 v[60:63], v107 offset:2048\n\t"
        "ds_read_b128 v[64:67], v108 offset:0\n\t"
        "v_mfma_f32_16x16x32_bf16 v[16:19], v[80:83], a[188:191], v[16:19]\n\t"
        "ds_read_b128 v[68:71], v108 offset:2048\n\t"
        "v_mfma_f32_16x16x32_bf16 v[20:23], a[180:183], v[84:87], v[20:23]\n\t"
        "v_mfma_f32_16x16x32_bf16 v[24:27], a[184:187], v[84:87], v[24:27]\n\t"
        "v_mfma_f32_16x16x32_bf16 v[28:31], v[84:87], a[188:191], v[28:31]\n\t"
        "s_add_u32 m0, %[ld], 98304\n\t"
        "s_add_u32 %[t0], %[xo], 1536\n\t"
        "buffer_load_dwordx4 v100, %[sx], %[t0] offen lds\n\t"
        "s_add_u32 m0, %[ld], 99328\n\t"
        "s_add_u32 %[t0], %[xo], 17920\n\t"
        "buffer_load_dwordx4 v100, %[sx], %[t0] offen lds\n\t"
        "s_add_u32 m0, %[ld], 106496\n\t"
        "s_add_u32 %[t0], %[xo], 1664\n\t"
        "buffer_load_dwordx4 v100, %[sx], %[t0] offen lds\n\t"
        "s_add_u32 m0, %[ld], 107520\n\t"
        "s_add_u32 %[t0], %[xo], 18048\n\t"
        "buffer_load_dwordx4 v100, %[sx], %[t0] offen lds\n\t"
        "s_add_u32 m0, %[ld], 114688\n\t"
        "s_add_u32 %[t0], %[xo], 1792\n\t"
        "buffer_load_dwordx4 v100, %[sx], %[t0] offen lds\n\t"
        "s_add_u32 m0, %[ld], 115712\n\t"
        "s_add_u32 %[t0], %[xo], 18176\n\t"
        "buffer_load_dwordx4 v100, %[sx], %[t0] offen lds\n\t"
        "s_add_u32 m0, %[ld], 122880\n\t"
        "s_add_u32 %[t0], %[xo], 1920\n\t"
        "buffer_load_dwordx4 v100, %[sx], %[t0] offen lds\n\t"
        "s_add_u32 m0, %[ld], 123904\n\t"
        "s_add_u32 %[t0], %[xo], 18304\n\t"
        "buffer_load_dwordx4 v100, %[sx], %[t0] offen lds\n\t"
        "s_mov_b32 %[t0], 6291456\n\t"
        "buffer_load_dwordx4 v[208:211], v101, %[sq], %[t0] offen\n\t"
        "buffer_load_dwordx4 v[216:219], v112, %[sv], %[t0] offen\n\t"
        "s_mov_b32 %[t0], 6553600\n\t"
        "buffer_load_dwordx4 v[212:215], v101, %[sq], %[t0] offen\n\t"
        "s_waitcnt vmcnt(17)\n\t"
        "ds_write_b128 v102, v[172:175] offset:16384\n\t"
        "ds_write_b128 v102, v[176:179] offset:20480\n\t"
        "ds_write_b128 v116, v[180:183] offset:24576\n\t"
        "s_waitcnt lgkmcnt(0)\n\t"
        "s_barrier\n\t"
        "v_mfma_f32_16x16x32_bf16 v[8:11], a[192:195], v[56:59], v[8:11]\n\t"
        "ds_read_b64_tr_b16 a[216:217], v103 offset:16384\n\t"
        "ds_read_b64_tr_b16 a[218:219], v103 offset:16896\n\t"
        "v_mfma_f32_16x16x32_bf16 v[12:15], a[196:199], v[56:59], v[12:15]\n\t"
        "ds_read_b64_tr_b16 a[220:221], v103 offset:16392\n\t"
        "ds_read_b64_tr_b16 a[222:223], v103 offset:16904\n\t"
        "v_mfma_f32_16x16x32_bf16 v[16:19], v[56:59], a[200:203], v[16:19]\n\t"
        "ds_read_b64_tr_b16 a[224:225], v104 offset:16384\n\t"
        "ds_read_b64_tr_b16 a[226:227], v104 offset:16640\n\t"
        "v_mfma_f32_16x16x32_bf16 v[20:23], a[192:195], v[60:63], v[20:23]\n\t"
        "s_waitcnt lgkmcnt(5)\n\t"
        "ds_read_b64_tr_b16 a[228:229], v103 offset:20480\n\t"
        "v_mfma_f32_16x16x32_bf16 v[24:27], a[196:199], v[60:63], v[24:27]\n\t"
        "ds_read_b64_tr_b16 a[230:231], v103 offset:20992\n\t"
        "ds_read_b64_tr_b16 a[232:233], v103 offset:20488\n\t"
        "v_mfma_f32_16x16x32_bf16 v[28:31], v[60:63], a[200:203], v[28:31]\n\t"
        "ds_read_b64_tr_b16 a[234:235], v103 offset:21000\n\t"
        "ds_read_b64_tr_b16 a[236:237], v104 offset:18432\n\t"
        "v_mfma_f32_16x16x32_bf16 v[8:11], a[204:207], v[64:67], v[8:11]\n\t"
        "ds_read_b64_tr_b16 a[238:239], v104 offset:18688\n\t"
        "ds_read_b128 v[72:75], v107 offset:8192\n\t"
        "v_mfma_f32_16x16x32_bf16 v[12:15], a[208:211], v[64:67], v[12:15]\n\t"
        "ds_read_b128 v[76:79], v107 offset:10240\n\t"
        "ds_read_b128 v[80:83], v108 offset:8192\n\t"
        "v_mfma_f32_16x16x32_bf16 v[16:19], v[64:67], a[212:215], v[16:19]\n\t"
        "ds_read_b128 v[84:87], v108 offset:10240\n\t"
        "v_mfma_f32_16x16x32_bf16 v[20:23], a[204:207], v[68:71], v[20:23]\n\t"
        "v_mfma_f32_16x16x32_bf16 v[24:27], a[208:211], v[68:71], v[24:27]\n\t"
        "v_mfma_f32_16x16x32_bf16 v[28:31], v[68:71], a[212:215], v[28:31]\n\t"
        "s_mov_b32 %[t0], 6815744\n\t"
        "buffer_load_dwordx4 v[220:223], v101, %[sq], %[t0] offen\n\t"
        "buffer_load_dwordx4 v[228:231], v112, %[sv], %[t0] offen\n\t"
        "s_mov_b32 %[t0], 7077888\n\t"
        "buffer_load_dwordx4 v[224:227], v101, %[sq], %[t0] offen\n\t"
        "s_waitcnt vmcnt(17)\n\t"
        "ds_write_b128 v102, v[184:187] offset:0\n\t"
        "ds_write_b128 v102, v[188:191] offset:4096\n\t"
        "ds_write_b128 v116, v[192:195] offset:8192\n\t"
        "s_waitcnt lgkmcnt(0)\n\t"
        "s_barrier\n\t"
        "v_mfma_f32_16x16x32_bf16 v[8:11], a[216:219], v[72:75], v[8:11]\n\t"
        "ds_read_b64_tr_b16 a[240:241], v103 offset:0\n\t"
        "ds_read_b64_tr_b16 a[242:243], v103 offset:512\n\t"
        "v_mfma_f32_16x16x32_bf16 v[12:15], a[220:223], v[72:75], v[12:15]\n\t"
        "ds_read_b64_tr_b16 a[244:245], v103 offset:8\n\t"
        "ds_read_b64_tr_b16 a[246:247], v103 offset:520\n\t"
        "v_mfma_f32_16x16x32_bf16 v[16:19], v[72:75], a[224:227], v[16:19]\n\t"
        "ds_read_b64_tr_b16 a[248:249], v104 offset:0\n\t"
        "ds_read_b64_tr_b16 a[250:251], v104 offset:256\n\t"
        "v_mfma_f32_16x16x32_bf16 v[20:23], a[216:219], v[76:79], v[20:23]\n\t"
        "s_waitcnt lgkmcnt(5)\n\t"
        "ds_read_b64_tr_b16 a[252:253], v103 offset:4096\n\t"
        "v_mfma_f32_16x16x32_bf16 v[24:27], a[220:223], v[76:79], v[24:27]\n\t"
        "ds_read_b64_tr_b16 a[254:255], v103 offset:4608\n\t"
        "ds_read_b64_tr_b16 v[128:129], v103 offset:4104\n\t"
        "v_mfma_f32_16x16x32_bf16 v[28:31], v[76:79], a[224:227], v[28:31]\n\t"
        "ds_read_b64_tr_b16 v[130:131], v103 offset:4616\n\t"
        "ds_read_b64_tr_b16 v[132:133], v104 offset:2048\n\t"
        "v_mfma_f32_16x16x32_bf16 v[8:11], a[228:231], v[80:83], v[8:11]\n\t"
        "ds_read_b64_tr_b16 v[134:135], v104 offset:2304\n\t"
        "ds_read_b128 v[56:59], v107 offset:16384\n\t"
        "v_mfma_f32_16x16x32_bf16 v[12:15], a[232:235], v[80:83], v[12:15]\n\t"
        "ds_read_b128 v[60:63], v107 offset:18432\n\t"
        "ds_read_b128 v[64:67], v108 offset:16384\n\t"
        "v_mfma_f32_16x16x32_bf16 v[16:19], v[80:83], a[236:239], v[16:19]\n\t"
        "ds_read_b128 v[68:71], v108 offset:18432\n\t"
        "v_mfma_f32_16x16x32_bf16 v[20:23], a[228:231], v[84:87], v[20:23]\n\t"
        "v_mfma_f32_16x16x32_bf16 v[24:27], a[232:235], v[84:87], v[24:27]\n\t"
        "v_mfma_f32_16x16x32_bf16 v[28:31], v[84:87], a[236:239], v[28:31]\n\t"
        "s_mov_b32 %[t0], 7340032\n\t"
        "buffer_load_dwordx4 v[232:235], v101, %[sq], %[t0] offen\n\t"
        "buffer_load_dwordx4 v[240:243], v112, %[sv], %[t0] offen\n\t"
        "s_mov_b32 %[t0], 7602176\n\t"
        "buffer_load_dwordx4 v[236:239], v101, %[sq], %[t0] offen\n\t"
        "s_waitcnt vmcnt(17)\n\t"
        "ds_write_b128 v102, v[196:199] offset:16384\n\t"
        "ds_write_b128 v102, v[200:203] offset:20480\n\t"
        "ds_write_b128 v116, v[204:207] offset:24576\n\t"
        "s_waitcnt lgkmcnt(0)\n\t"
        "s_barrier\n\t"
        "v_mfma_f32_16x16x32_bf16 v[8:11], a[240:243], v[56:59], v[8:11]\n\t"
        "ds_read_b64_tr_b16 v[136:137], v103 offset:16384\n\t"
        "ds_read_b64_tr_b16 v[138:139], v103 offset:16896\n\t"
        "v_mfma_f32_16x16x32_bf16 v[12:15], a[244:247], v[56:59], v[12:15]\n\t"
        "ds_read_b64_tr_b16 v[140:141], v103 offset:16392\n\t"
        "ds_read_b64_tr_b16 v[142:143], v103 offset:16904\n\t"
        "v_mfma_f32_16x16x32_bf16 v[16:19], v[56:59], a[248:251], v[16:19]\n\t"
        "ds_read_b64_tr_b16 v[144:145], v104 offset:16384\n\t"
        "ds_read_b64_tr_b16 v[146:147], v104 offset:16640\n\t"
        "v_mfma_f32_16x16x32_bf16 v[20:23], a[240:243], v[60:63], v[20:23]\n\t"
        "s_waitcnt lgkmcnt(5)\n\t"
        "ds_read_b64_tr_b16 v[148:149], v103 offset:20480\n\t"
        "v_mfma_f32_16x16x32_bf16 v[24:27], a[244:247], v[60:63], v[24:27]\n\t"
        "ds_read_b64_tr_b16 v[150:151], v103 offset:20992\n\t"
        "ds_read_b64_tr_b16 v[152:153], v103 offset:20488\n\t"
        "v_mfma_f32_16x16x32_bf16 v[28:31], v[60:63], a[248:251], v[28:31]\n\t"
        "ds_read_b64_tr_b16 v[154:155], v103 offset:21000\n\t"
        "ds_read_b64_tr_b16 v[156:157], v104 offset:18432\n\t"
        "v_mfma_f32_16x16x32_bf16 v[8:11], a[252:255], v[64:67], v[8:11]\n\t"
        "ds_read_b64_tr_b16 v[158:159], v104 offset:18688\n\t"
        "ds_read_b128 v[72:75], v107 offset:24576\n\t"
        "v_mfma_f32_16x16x32_bf16 v[12:15], v[128:131], v[64:67], v[12:15]\n\t"
        "ds_read_b128 v[76:79], v107 offset:26624\n\t"
        "ds_read_b128 v[80:83], v108 offset:24576\n\t"
        "v_mfma_f32_16x16x32_bf16 v[16:19], v[64:67], v[132:135], v[16:19]\n\t"
        "ds_read_b128 v[84:87], v108 offset:26624\n\t"
        "v_mfma_f32_16x16x32_bf16 v[20:23], a[252:255], v[68:71], v[20:23]\n\t"
        "v_mfma_f32_16x16x32_bf16 v[24:27], v[128:131], v[68:71], v[24:27]\n\t"
        "v_mfma_f32_16x16x32_bf16 v[28:31], v[68:71], v[132:135], v[28:31]\n\t"
        "s_mov_b32 %[t0], 7864320\n\t"
        "buffer_load_dwordx4 v[244:247], v101, %[sq], %[t0] offen\n\t"
        "buffer_load_dwordx4 v[252:255], v112, %[sv], %[t0] offen\n\t"
        "s_mov_b32 %[t0], 8126464\n\t"
        "buffer_load_dwordx4 v[248:251], v101, %[sq], %[t0] offen\n\t"
        "s_waitcnt vmcnt(9)\n\t"
        "ds_write_b128 v102, v[208:211] offset:0\n\t"
        "ds_write_b128 v102, v[212:215] offset:4096\n\t"
        "ds_write_b128 v116, v[216:219] offset:8192\n\t"
        "s_waitcnt lgkmcnt(0)\n\t"
        "s_barrier\n\t"
        "v_mfma_f32_16x16x32_bf16 v[8:11], v[136:139], v[72:75], v[8:11]\n\t"
        "ds_read_b64_tr_b16 v[160:161], v103 offset:0\n\t"
        "ds_read_b64_tr_b16 v[162:163], v103 offset:512\n\t"
        "v_mfma_f32_16x16x32_bf16 v[12:15], v[140:143], v[72:75], v[12:15]\n\t"
        "ds_read_b64_tr_b16 v[164:165], v103 offset:8\n\t"
        "ds_read_b64_tr_b16 v[166:167], v103 offset:520\n\t"
        "v_mfma_f32_16x16x32_bf16 v[16:19], v[72:75], v[144:147], v[16:19]\n\t"
        "ds_read_b64_tr_b16 v[168:169], v104 offset:0\n\t"
        "ds_read_b64_tr_b16 v[170:171], v104 offset:256\n\t"
        "v_mfma_f32_16x16x32_bf16 v[20:23], v[136:139], v[76:79], v[20:23]\n\t"
        "s_waitcnt lgkmcnt(5)\n\t"
        "ds_read_b64_tr_b16 v[172:173], v103 offset:4096\n\t"
        "v_mfma_f32_16x16x32_bf16 v[24:27], v[140:143], v[76:79], v[24:27]\n\t"
        "ds_read_b64_tr_b16 v[174:175], v103 offset:4608\n\t"
        "ds_read_b64_tr_b16 v[176:177], v103 offset:4104\n\t"
        "v_mfma_f32_16x16x32_bf16 v[28:31], v[76:79], v[144:147], v[28:31]\n\t"
        "ds_read_b64_tr_b16 v[178:179], v103 offset:4616\n\t"
        "ds_read_b64_tr_b16 v[180:181], v104 offset:2048\n\t"
        "v_mfma_f32_16x16x32_bf16 v[8:11], v[148:151], v[80:83], v[8:11]\n\t"
        "ds_read_b64_tr_b16 v[182:183], v104 offset:2304\n\t"
        "ds_read_b128 v[56:59], v107 offset:32768\n\t"
        "v_mfma_f32_16x16x32_bf16 v[12:15], v[152:155], v[80:83], v[12:15]\n\t"
        "ds_read_b128 v[60:63], v107 offset:34816\n\t"
        "ds_read_b128 v[64:67], v108 offset:32768\n\t"
        "v_mfma_f32_16x16x32_bf16 v[16:19], v[80:83], v[156:159], v[16:19]\n\t"
        "ds_read_b128 v[68:71], v108 offset:34816\n\t"
        "v_mfma_f32_16x16x32_bf16 v[20:23], v[148:151], v[84:87], v[20:23]\n\t"
        "v_mfma_f32_16x16x32_bf16 v[24:27], v[152:155], v[84:87], v[24:27]\n\t"
        "v_mfma_f32_16x16x32_bf16 v[28:31], v[84:87], v[156:159], v[28:31]\n\t"
        "s_mov_b32 %[xo], 0x20000\n\t"
        "s_add_u32 m0, %[ld], 0\n\t"
        "s_add_u32 %[t0], %[xo], 0\n\t"
        "buffer_load_dwordx4 v100, %[sx], %[t0] offen lds\n\t"
        "s_add_u32 m0, %[ld], 1024\n\t"
        "s_add_u32 %[t0], %[xo], 16384\n\t"
        "buffer_load_dwordx4 v100, %[sx], %[t0] offen lds\n\t"
        "s_add_u32 m0, %[ld], 8192\n\t"
        "s_add_u32 %[t0], %[xo], 128\n\t"
        "buffer_load_dwordx4 v100, %[sx], %[t0] offen lds\n\t"
        "s_add_u32 m0, %[ld], 9216\n\t"
        "s_add_u32 %[t0], %[xo], 16512\n\t"
        "buffer_load_dwordx4 v100, %[sx], %[t0] offen lds\n\t"
        "s_add_u32 m0, %[ld], 16384\n\t"
        "s_add_u32 %[t0], %[xo], 256\n\t"
        "buffer_load_dwordx4 v100, %[sx], %[t0] offen lds\n\t"
        "s_add_u32 m0, %[ld], 17408\n\t"
        "s_add_u32 %[t0], %[xo], 16640\n\t"
        "buffer_load_dwordx4 v100, %[sx], %[t0] offen lds\n\t"
        "s_add_u32 m0, %[ld], 24576\n\t"
        "s_add_u32 %[t0], %[xo], 384\n\t"
        "buffer_load_dwordx4 v100, %[sx], %[t0] offen lds\n\t"
        "s_add_u32 m0, %[ld], 25600\n\t"
        "s_add_u32 %[t0], %[xo], 16768\n\t"
        "buffer_load_dwordx4 v100, %[sx], %[t0] offen lds\n\t"
        "s_mov_b32 %[xo], 0\n\t"
        "s_waitcnt vmcnt(14)\n\t"
        "ds_write_b128 v102, v[220:223] offset:16384\n\t"
        "ds_write_b128 v102, v[224:227] offset:20480\n\t"
        "ds_write_b128 v116, v[228:231] offset:24576\n\t"
        "s_waitcnt lgkmcnt(0)\n\t"
        "s_barrier\n\t"
        "v_mfma_f32_16x16x32_bf16 v[8:11], v[160:163], v[56:59], v[8:11]\n\t"
        "ds_read_b64_tr_b16 v[184:185], v103 offset:16384\n\t"
        "ds_read_b64_tr_b16 v[186:187], v103 offset:16896\n\t"
        "v_mfma_f32_16x16x32_bf16 v[12:15], v[164:167], v[56:59], v[12:15]\n\t"
        "ds_read_b64_tr_b16 v[188:189], v103 offset:16392\n\t"
        "ds_read_b64_tr_b16 v[190:191], v103 offset:16904\n\t"
        "v_mfma_f32_16x16x32_bf16 v[16:19], v[56:59], v[168:171], v[16:19]\n\t"
        "ds_read_b64_tr_b16 v[192:193], v104 offset:16384\n\t"
        "ds_read_b64_tr_b16 v[194:195], v104 offset:16640\n\t"
        "v_mfma_f32_16x16x32_bf16 v[20:23], v[160:163], v[60:63], v[20:23]\n\t"
        "s_waitcnt lgkmcnt(5)\n\t"
        "ds_read_b64_tr_b16 v[196:197], v103 offset:20480\n\t"
        "v_mfma_f32_16x16x32_bf16 v[24:27], v[164:167], v[60:63], v[24:27]\n\t"
        "ds_read_b64_tr_b16 v[198:199], v103 offset:20992\n\t"
        "ds_read_b64_tr_b16 v[200:201], v103 offset:20488\n\t"
        "v_mfma_f32_16x16x32_bf16 v[28:31], v[60:63], v[168:171], v[28:31]\n\t"
        "ds_read_b64_tr_b16 v[202:203], v103 offset:21000\n\t"
        "ds_read_b64_tr_b16 v[204:205], v104 offset:18432\n\t"
        "v_mfma_f32_16x16x32_bf16 v[8:11], v[172:175], v[64:67], v[8:11]\n\t"
        "ds_read_b64_tr_b16 v[206:207], v104 offset:18688\n\t"
        "ds_read_b128 v[72:75], v107 offset:40960\n\t"
        "v_mfma_f32_16x16x32_bf16 v[12:15], v[176:179], v[64:67], v[12:15]\n\t"
        "ds_read_b128 v[76:79], v107 offset:43008\n\t"
        "ds_read_b128 v[80:83], v108 offset:40960\n\t"
        "v_mfma_f32_16x16x32_bf16 v[16:19], v[64:67], v[180:183], v[16:19]\n\t"
        "ds_read_b128 v[84:87], v108 offset:43008\n\t"
        "v_mfma_f32_16x16x32_bf16 v[20:23], v[172:175], v[68:71], v[20:23]\n\t"
        "v_mfma_f32_16x16x32_bf16 v[24:27], v[176:179], v[68:71], v[24:27]\n\t"
        "v_mfma_f32_16x16x32_bf16 v[28:31], v[68:71], v[180:183], v[28:31]\n\t"
        "s_waitcnt vmcnt(11)\n\t"
        "ds_write_b128 v102, v[232:235] offset:0\n\t"
        "ds_write_b128 v102, v[236:239] offset:4096\n\t"
        "ds_write_b128 v116, v[240:243] offset:8192\n\t"
        "s_waitcnt lgkmcnt(0)\n\t"
        "s_barrier\n\t"
        "v_mfma_f32_16x16x32_bf16 v[8:11], v[184:187], v[72:75], v[8:11]\n\t"
        "ds_read_b64_tr_b16 v[208:209], v103 offset:0\n\t"
        "ds_read_b64_tr_b16 v[210:211], v103 offset:512\n\t"
        "v_mfma_f32_16x16x32_bf16 v[12:15], v[188:191], v[72:75], v[12:15]\n\t"
        "ds_read_b64_tr_b16 v[212:213], v103 offset:8\n\t"
        "ds_read_b64_tr_b16 v[214:215], v103 offset:520\n\t"
        "v_mfma_f32_16x16x32_bf16 v[16:19], v[72:75], v[192:195], v[16:19]\n\t"
        "ds_read_b64_tr_b16 v[216:217], v104 offset:0\n\t"
        "ds_read_b64_tr_b16 v[218:219], v104 offset:256\n\t"
        "v_mfma_f32_16x16x32_bf16 v[20:23], v[184:187], v[76:79], v[20:23]\n\t"
        "s_waitcnt lgkmcnt(5)\n\t"
        "ds_read_b64_tr_b16 v[220:221], v103 offset:4096\n\t"
        "v_mfma_f32_16x16x32_bf16 v[24:27], v[188:191], v[76:79], v[24:27]\n\t"
        "ds_read_b64_tr_b16 v[222:223], v103 offset:4608\n\t"
        "ds_read_b64_tr_b16 v[224:225], v103 offset:4104\n\t"
        "v_mfma_f32_16x16x32_bf16 v[28:31], v[76:79], v[192:195], v[28:31]\n\t"
        "ds_read_b64_tr_b16 v[226:227], v103 offset:4616\n\t"
        "ds_read_b64_tr_b16 v[228:229], v104 offset:2048\n\t"
        "v_mfma_f32_16x16x32_bf16 v[8:11], v[196:199], v[80:83], v[8:11]\n\t"
        "ds_read_b64_tr_b16 v[230:231], v104 offset:2304\n\t"
        "ds_read_b128 v[56:59], v107 offset:49152\n\t"
        "v_mfma_f32_16x16x32_bf16 v[12:15], v[200:203], v[80:83], v[12:15]\n\t"
        "ds_read_b128 v[60:63], v107 offset:51200\n\t"
        "ds_read_b128 v[64:67], v108 offset:49152\n\t"
        "v_mfma_f32_16x16x32_bf16 v[16:19], v[80:83], v[204:207], v[16:19]\n\t"
        "ds_read_b128 v[68:71], v108 offset:51200\n\t"
        "v_mfma_f32_16x16x32_bf16 v[20:23], v[196:199], v[84:87], v[20:23]\n\t"
        "v_mfma_f32_16x16x32_bf16 v[24:27], v[200:203], v[84:87], v[24:27]\n\t"
        "v_mfma_f32_16x16x32_bf16 v[28:31], v[84:87], v[204:207], v[28:31]\n\t"
        "s_waitcnt vmcnt(8)\n\t"
        "ds_write_b128 v102, v[244:247] offset:16384\n\t"
        "ds_write_b128 v102, v[248:251] offset:20480\n\t"
        "ds_write_b128 v116, v[252:255] offset:24576\n\t"
        "s_waitcnt lgkmcnt(0)\n\t"
        "s_barrier\n\t"
        "v_mfma_f32_16x16x32_bf16 v[8:11], v[208:211], v[56:59], v[8:11]\n\t"
        "ds_read_b64_tr_b16 v[232:233], v103 offset:16384\n\t"
        "ds_read_b64_tr_b16 v[234:235], v103 offset:16896\n\t"
        "v_mfma_f32_16x16x32_bf16 v[12:15], v[212:215], v[56:59], v[12:15]\n\t"
        "ds_read_b64_tr_b16 v[236:237], v103 offset:16392\n\t"
        "ds_read_b64_tr_b16 v[238:239], v103 offset:16904\n\t"
        "v_mfma_f32_16x16x32_bf16 v[16:19], v[56:59], v[216:219], v[16:19]\n\t"
        "ds_read_b64_tr_b16 v[240:241], v104 offset:16384\n\t"
        "ds_read_b64_tr_b16 v[242:243], v104 offset:16640\n\t"
        "v_mfma_f32_16x16x32_bf16 v[20:23], v[208:211], v[60:63], v[20:23]\n\t"
        "s_waitcnt lgkmcnt(5)\n\t"
        "ds_read_b64_tr_b16 v[244:245], v103 offset:20480\n\t"
        "v_mfma_f32_16x16x32_bf16 v[24:27], v[212:215], v[60:63], v[24:27]\n\t"
        "ds_read_b64_tr_b16 v[246:247], v103 offset:20992\n\t"
        "ds_read_b64_tr_b16 v[248:249], v103 offset:20488\n\t"
        "v_mfma_f32_16x16x32_bf16 v[28:31], v[60:63], v[216:219], v[28:31]\n\t"
        "ds_read_b64_tr_b16 v[250:251], v103 offset:21000\n\t"
        "ds_read_b64_tr_b16 v[252:253], v104 offset:18432\n\t"
        "v_mfma_f32_16x16x32_bf16 v[8:11], v[220:223], v[64:67], v[8:11]\n\t"
        "ds_read_b64_tr_b16 v[254:255], v104 offset:18688\n\t"
        "ds_read_b128 v[72:75], v107 offset:57344\n\t"
        "v_mfma_f32_16x16x32_bf16 v[12:15], v[224:227], v[64:67], v[12:15]\n\t"
        "ds_read_b128 v[76:79], v107 offset:59392\n\t"
        "ds_read_b128 v[80:83], v108 offset:57344\n\t"
        "v_mfma_f32_16x16x32_bf16 v[16:19], v[64:67], v[228:231], v[16:19]\n\t"
        "ds_read_b128 v[84:87], v108 offset:59392\n\t"
        "v_mfma_f32_16x16x32_bf16 v[20:23], v[220:223], v[68:71], v[20:23]\n\t"
        "v_mfma_f32_16x16x32_bf16 v[24:27], v[224:227], v[68:71], v[24:27]\n\t"
        "v_mfma_f32_16x16x32_bf16 v[28:31], v[68:71], v[228:231], v[28:31]\n\t"
        "s_waitcnt vmcnt(0) lgkmcnt(0)\n\t"
        "s_barrier\n\t"
        "v_mfma_f32_16x16x32_bf16 v[8:11], v[232:235], v[72:75], v[8:11]\n\t"
        "v_mfma_f32_16x16x32_bf16 v[12:15], v[236:239], v[72:75], v[12:15]\n\t"
        "v_mfma_f32_16x16x32_bf16 v[16:19], v[72:75], v[240:243], v[16:19]\n\t"
        "v_mfma_f32_16x16x32_bf16 v[20:23], v[232:235], v[76:79], v[20:23]\n\t"
        "v_mfma_f32_16x16x32_bf16 v[24:27], v[236:239], v[76:79], v[24:27]\n\t"
        "v_mfma_f32_16x16x32_bf16 v[28:31], v[76:79], v[240:243], v[28:31]\n\t"
        "v_mfma_f32_16x16x32_bf16 v[8:11], v[244:247], v[80:83], v[8:11]\n\t"
        "v_mfma_f32_16x16x32_bf16 v[12:15], v[248:251], v[80:83], v[12:15]\n\t"
        "v_mfma_f32_16x16x32_bf16 v[16:19], v[80:83], v[252:255], v[16:19]\n\t"
        "v_mfma_f32_16x16x32_bf16 v[20:23], v[244:247], v[84:87], v[20:23]\n\t"
        "v_mfma_f32_16x16x32_bf16 v[24:27], v[248:251], v[84:87], v[24:27]\n\t"
        "v_mfma_f32_16x16x32_bf16 v[28:31], v[84:87], v[252:255], v[28:31]\n\t"
        "s_mov_b32 %[xo], 0x20000\n\t"
        "ds_read_b128 v[56:59], v105 offset:0\n\t"
        "ds_read_b128 v[60:63], v105 offset:2048\n\t"
        "ds_read_b128 v[64:67], v106 offset:0\n\t"
        "ds_read_b128 v[68:71], v106 offset:2048\n\t"
        "ds_read_b128 v[72:75], v105 offset:8192\n\t"
        "ds_read_b128 v[76:79], v105 offset:10240\n\t"
        "ds_read_b128 v[80:83], v106 offset:8192\n\t"
        "v_cvt_pk_bf16_f32 v88, v8, v9\n\t"
        "v_cvt_pk_bf16_f32 v89, v10, v11\n\t"
        "v_cvt_pk_bf16_f32 v90, v12, v13\n\t"
        "v_cvt_pk_bf16_f32 v91, v14, v15\n\t"
        "buffer_store_dwordx4 v[88:91], v109, %[sw], %[so] offen sc1\n\t"
        "v_cvt_pk_bf16_f32 v92, v20, v21\n\t"
        "v_cvt_pk_bf16_f32 v93, v22, v23\n\t"
        "v_cvt_pk_bf16_f32 v94, v24, v25\n\t"
        "v_cvt_pk_bf16_f32 v95, v26, v27\n\t"
        "buffer_store_dwordx4 v[92:95], v110, %[sw], %[so] offen sc1\n\t"
        "v_cvt_pk_bf16_f32 v96, v16, v17\n\t"
        "v_cvt_pk_bf16_f32 v97, v18, v19\n\t"
        "v_cvt_pk_bf16_f32 v98, v28, v29\n\t"
        "v_cvt_pk_bf16_f32 v99, v30, v31\n\t"
        "buffer_store_dwordx4 v[96:99], v111, %[sw], %[so] offen sc1\n\t"
        "s_mov_b32 %[so], 0xf0000000\n\t"
        "s_mov_b32 %[xo], 33792\n\t"
        "s_mov_b32 %[cn], 0\n\t"
        "LG_cal%=:\n\t"
        "buffer_load_dword v118, off, %[sw], %[xo] sc1\n\t"
        "s_waitcnt vmcnt(0)\n\t"
        "v_readfirstlane_b32 %[t0], v118\n\t"
        "s_cmp_lg_u32 %[t0], 0\n\t"
        "s_cbranch_scc1 LG_calok%=\n\t"
        "s_sleep 4\n\t"
        "s_add_u32 %[cn], %[cn], 1\n\t"
        "s_cmp_lt_u32 %[cn], 0x100000\n\t"
        "s_cbranch_scc1 LG_cal%=\n\t"
        "s_endpgm\n\t"
        "LG_calok%=:\n\t"
        "s_mov_b32 %[xo], 0x20000\n\t"
        "s_mov_b32 %[cn], 1\n\t"
        "v_mov_b32_e32 v117, %[cn]\n\t"
        "buffer_store_dword v117, v113, %[sw], 0 offen sc1\n\t"
        "s_add_u32 m0, %[ld], 32768\n\t"
        "s_add_u32 %[t0], %[xo], 512\n\t"
        "buffer_load_dwordx4 v100, %[sx], %[t0] offen lds\n\t"
        "s_add_u32 m0, %[ld], 33792\n\t"
        "s_add_u32 %[t0], %[xo], 16896\n\t"
        "buffer_load_dwordx4 v100, %[sx], %[t0] offen lds\n\t"
        "s_add_u32 m0, %[ld], 40960\n\t"
        "s_add_u32 %[t0], %[xo], 640\n\t"
        "buffer_load_dwordx4 v100, %[sx], %[t0] offen lds\n\t"
        "s_add_u32 m0, %[ld], 41984\n\t"
        "s_add_u32 %[t0], %[xo], 17024\n\t"
        "buffer_load_dwordx4 v100, %[sx], %[t0] offen lds\n\t"
        "s_add_u32 m0, %[ld], 49152\n\t"
        "s_add_u32 %[t0], %[xo], 768\n\t"
        "buffer_load_dwordx4 v100, %[sx], %[t0] offen lds\n\t"
        "s_add_u32 m0, %[ld], 50176\n\t"
        "s_add_u32 %[t0], %[xo], 17152\n\t"
        "buffer_load_dwordx4 v100, %[sx], %[t0] offen lds\n\t"
        "s_add_u32 m0, %[ld], 57344\n\t"
        "s_add_u32 %[t0], %[xo], 896\n\t"
        "buffer_load_dwordx4 v100, %[sx], %[t0] offen lds\n\t"
        "s_add_u32 m0, %[ld], 58368\n\t"
        "s_add_u32 %[t0], %[xo], 17280\n\t"
        "buffer_load_dwordx4 v100, %[sx], %[t0] offen lds\n\t"
        "s_add_u32 m0, %[ld], 65536\n\t"
        "s_add_u32 %[t0], %[xo], 1024\n\t"
        "buffer_load_dwordx4 v100, %[sx], %[t0] offen lds\n\t"
        "s_add_u32 m0, %[ld], 66560\n\t"
        "s_add_u32 %[t0], %[xo], 17408\n\t"
        "buffer_load_dwordx4 v100, %[sx], %[t0] offen lds\n\t"
        "s_add_u32 m0, %[ld], 73728\n\t"
        "s_add_u32 %[t0], %[xo], 1152\n\t"
        "buffer_load_dwordx4 v100, %[sx], %[t0] offen lds\n\t"
        "s_add_u32 m0, %[ld], 74752\n\t"
        "s_add_u32 %[t0], %[xo], 17536\n\t"
        "buffer_load_dwordx4 v100, %[sx], %[t0] offen lds\n\t"
        "s_add_u32 m0, %[ld], 81920\n\t"
        "s_add_u32 %[t0], %[xo], 1280\n\t"
        "buffer_load_dwordx4 v100, %[sx], %[t0] offen lds\n\t"
        "s_add_u32 m0, %[ld], 82944\n\t"
        "s_add_u32 %[t0], %[xo], 17664\n\t"
        "buffer_load_dwordx4 v100, %[sx], %[t0] offen lds\n\t"
        "s_add_u32 m0, %[ld], 90112\n\t"
        "s_add_u32 %[t0], %[xo], 1408\n\t"
        "buffer_load_dwordx4 v100, %[sx], %[t0] offen lds\n\t"
        "s_add_u32 m0, %[ld], 91136\n\t"
        "s_add_u32 %[t0], %[xo], 17792\n\t"
        "buffer_load_dwordx4 v100, %[sx], %[t0] offen lds\n\t"
        "s_add_u32 m0, %[ld], 98304\n\t"
        "s_add_u32 %[t0], %[xo], 1536\n\t"
        "buffer_load_dwordx4 v100, %[sx], %[t0] offen lds\n\t"
        "s_add_u32 m0, %[ld], 99328\n\t"
        "s_add_u32 %[t0], %[xo], 17920\n\t"
        "buffer_load_dwordx4 v100, %[sx], %[t0] offen lds\n\t"
        "s_add_u32 m0, %[ld], 106496\n\t"
        "s_add_u32 %[t0], %[xo], 1664\n\t"
        "buffer_load_dwordx4 v100, %[sx], %[t0] offen lds\n\t"
        "s_branch LG_mid%=\n\t"
        "LG_loop%=:\n\t"
        "s_waitcnt lgkmcnt(6)\n\t"
        "v_mfma_f32_16x16x32_bf16 v[8:11], a[0:3], v[56:59], 0\n\t"
        "v_mfma_f32_16x16x32_bf16 v[12:15], a[4:7], v[56:59], 0\n\t"
        "ds_read_b128 v[84:87], v106 offset:10240\n\t"
        "v_mfma_f32_16x16x32_bf16 v[16:19], v[56:59], a[8:11], 0\n\t"
        "s_add_u32 m0, %[ld], 107520\n\t"
        "s_add_u32 %[t0], %[xo], 18048\n\t"
        "s_waitcnt lgkmcnt(6)\n\t"
        "v_mfma_f32_16x16x32_bf16 v[20:23], a[0:3], v[60:63], 0\n\t"
        "buffer_load_dwordx4 v100, %[sx], %[t0] offen lds\n\t"
        "v_mfma_f32_16x16x32_bf16 v[24:27], a[4:7], v[60:63], 0\n\t"
        "ds_read_b128 v[56:59], v105 offset:16384\n\t"
        "v_mfma_f32_16x16x32_bf16 v[28:31], v[60:63], a[8:11], 0\n\t"
        "s_waitcnt lgkmcnt(6)\n\t"
        "v_mfma_f32_16x16x32_bf16 v[8:11], a[12:15], v[64:67], v[8:11]\n\t"
        "v_cvt_pk_bf16_f32 v88, v32, v33\n\t"
        "v_cvt_pk_bf16_f32 v89, v34, v35\n\t"
        "v_mfma_f32_16x16x32_bf16 v[12:15], a[16:19], v[64:67], v[12:15]\n\t"
        "ds_read_b128 v[60:63], v105 offset:18432\n\t"
        "v_cvt_pk_bf16_f32 v90, v36, v37\n\t"
        "v_mfma_f32_16x16x32_bf16 v[16:19], v[64:67], a[20:23], v[16:19]\n\t"
        "s_add_u32 m0, %[ld], 114688\n\t"
        "s_add_u32 %[t0], %[xo], 1792\n\t"
        "s_waitcnt lgkmcnt(6)\n\t"
        "v_mfma_f32_16x16x32_bf16 v[20:23], a[12:15], v[68:71], v[20:23]\n\t"
        "buffer_load_dwordx4 v100, %[sx], %[t0] offen lds\n\t"
        "v_mfma_f32_16x16x32_bf16 v[24:27], a[16:19], v[68:71], v[24:27]\n\t"
        "ds_read_b128 v[64:67], v106 offset:16384\n\t"
        "v_cvt_pk_bf16_f32 v91, v38, v39\n\t"
        "v_mfma_f32_16x16x32_bf16 v[28:31], v[68:71], a[20:23], v[28:31]\n\t"
        "buffer_store_dwordx4 v[88:91], v109, %[sw], %[so] offen sc1\n\t"
        "s_waitcnt lgkmcnt(6)\n\t"
        "v_mfma_f32_16x16x32_bf16 v[8:11], a[24:27], v[72:75], v[8:11]\n\t"
        "v_cvt_pk_bf16_f32 v92, v44, v45\n\t"
        "v_cvt_pk_bf16_f32 v93, v46, v47\n\t"
        "v_mfma_f32_16x16x32_bf16 v[12:15], a[28:31], v[72:75], v[12:15]\n\t"
        "ds_read_b128 v[68:71], v106 offset:18432\n\t"
        "v_cvt_pk_bf16_f32 v94, v48, v49\n\t"
        "v_mfma_f32_16x16x32_bf16 v[16:19], v[72:75], a[32:35], v[16:19]\n\t"
        "s_add_u32 m0, %[ld], 115712\n\t"
        "s_add_u32 %[t0], %[xo], 18176\n\t"
        "s_waitcnt lgkmcnt(6)\n\t"
        "v_mfma_f32_16x16x32_bf16 v[20:23], a[24:27], v[76:79], v[20:23]\n\t"
        "buffer_load_dwordx4 v100, %[sx], %[t0] offen lds\n\t"
        "v_mfma_f32_16x16x32_bf16 v[24:27], a[28:31], v[76:79], v[24:27]\n\t"
        "ds_read_b128 v[72:75], v105 offset:24576\n\t"
        "v_cvt_pk_bf16_f32 v95, v50, v51\n\t"
        "v_mfma_f32_16x16x32_bf16 v[28:31], v[76:79], a[32:35], v[28:31]\n\t"
        "buffer_store_dwordx4 v[92:95], v110, %[sw], %[so] offen sc1\n\t"
        "s_waitcnt lgkmcnt(6)\n\t"
        "v_mfma_f32_16x16x32_bf16 v[8:11], a[36:39], v[80:83], v[8:11]\n\t"
        "v_cvt_pk_bf16_f32 v96, v40, v41\n\t"
        "v_cvt_pk_bf16_f32 v97, v42, v43\n\t"
        "v_mfma_f32_16x16x32_bf16 v[12:15], a[40:43], v[80:83], v[12:15]\n\t"
        "ds_read_b128 v[76:79], v105 offset:26624\n\t"
        "v_cvt_pk_bf16_f32 v98, v52, v53\n\t"
        "v_mfma_f32_16x16x32_bf16 v[16:19], v[80:83], a[44:47], v[16:19]\n\t"
        "s_add_u32 m0, %[ld], 122880\n\t"
        "s_add_u32 %[t0], %[xo], 1920\n\t"
        "s_waitcnt lgkmcnt(6)\n\t"
        "v_mfma_f32_16x16x32_bf16 v[20:23], a[36:39], v[84:87], v[20:23]\n\t"
        "buffer_load_dwordx4 v100, %[sx], %[t0] offen lds\n\t"
        "v_mfma_f32_16x16x32_bf16 v[24:27], a[40:43], v[84:87], v[24:27]\n\t"
        "ds_read_b128 v[80:83], v106 offset:24576\n\t"
        "v_cvt_pk_bf16_f32 v99, v54, v55\n\t"
        "v_mfma_f32_16x16x32_bf16 v[28:31], v[84:87], a[44:47], v[28:31]\n\t"
        "buffer_store_dwordx4 v[96:99], v111, %[sw], %[so] offen sc1\n\t"
        "s_waitcnt lgkmcnt(6)\n\t"
        "v_mfma_f32_16x16x32_bf16 v[8:11], a[48:51], v[56:59], v[8:11]\n\t"
        "s_lshl_b32 %[so], %[cn], 19\n\t"
        "v_mfma_f32_16x16x32_bf16 v[12:15], a[52:55], v[56:59], v[12:15]\n\t"
        "ds_read_b128 v[84:87], v106 offset:26624\n\t"
        "v_mfma_f32_16x16x32_bf16 v[16:19], v[56:59], a[56:59], v[16:19]\n\t"
        "s_add_u32 m0, %[ld], 123904\n\t"
        "s_add_u32 %[t0], %[xo], 18304\n\t"
        "s_waitcnt lgkmcnt(6)\n\t"
        "v_mfma_f32_16x16x32_bf16 v[20:23], a[48:51], v[60:63], v[20:23]\n\t"
        "buffer_load_dwordx4 v100, %[sx], %[t0] offen lds\n\t"
        "v_mfma_f32_16x16x32_bf16 v[24:27], a[52:55], v[60:63], v[24:27]\n\t"
        "v_mfma_f32_16x16x32_bf16 v[28:31], v[60:63], a[56:59], v[28:31]\n\t"
        "s_waitcnt lgkmcnt(5)\n\t"
        "v_mfma_f32_16x16x32_bf16 v[8:11], a[60:63], v[64:67], v[8:11]\n\t"
        "s_waitcnt vmcnt(19) lgkmcnt(0)\n\t"
        "s_barrier\n\t"
        "s_add_u32 %[xo], %[xo], 0x20000\n\t"
        "v_mfma_f32_16x16x32_bf16 v[12:15], a[64:67], v[64:67], v[12:15]\n\t"
        "ds_read_b128 v[56:59], v105 offset:32768\n\t"
        "v_mfma_f32_16x16x32_bf16 v[16:19], v[64:67], a[68:71], v[16:19]\n\t"
        "ds_read_b128 v[60:63], v105 offset:34816\n\t"
        "s_add_u32 m0, %[ld], 0\n\t"
        "s_add_u32 %[t0], %[xo], 0\n\t"
        "v_mfma_f32_16x16x32_bf16 v[20:23], a[60:63], v[68:71], v[20:23]\n\t"
        "buffer_load_dwordx4 v100, %[sx], %[t0] offen lds\n\t"
        "v_mfma_f32_16x16x32_bf16 v[24:27], a[64:67], v[68:71], v[24:27]\n\t"
        "ds_read_b128 v[64:67], v106 offset:32768\n\t"
        "v_mfma_f32_16x16x32_bf16 v[28:31], v[68:71], a[68:71], v[28:31]\n\t"
        "v_mfma_f32_16x16x32_bf16 v[8:11], a[72:75], v[72:75], v[8:11]\n\t"
        "v_mfma_f32_16x16x32_bf16 v[12:15], a[76:79], v[72:75], v[12:15]\n\t"
        "ds_read_b128 v[68:71], v106 offset:34816\n\t"
        "v_mfma_f32_16x16x32_bf16 v[16:19], v[72:75], a[80:83], v[16:19]\n\t"
        "s_add_u32 m0, %[ld], 1024\n\t"
        "s_add_u32 %[t0], %[xo], 16384\n\t"
        "v_mfma_f32_16x16x32_bf16 v[20:23], a[72:75], v[76:79], v[20:23]\n\t"
        "buffer_load_dwordx4 v100, %[sx], %[t0] offen lds\n\t"
        "v_mfma_f32_16x16x32_bf16 v[24:27], a[76:79], v[76:79], v[24:27]\n\t"
        "ds_read_b128 v[72:75], v105 offset:40960\n\t"
        "v_mfma_f32_16x16x32_bf16 v[28:31], v[76:79], a[80:83], v[28:31]\n\t"
        "v_mfma_f32_16x16x32_bf16 v[8:11], a[84:87], v[80:83], v[8:11]\n\t"
        "v_mfma_f32_16x16x32_bf16 v[12:15], a[88:91], v[80:83], v[12:15]\n\t"
        "ds_read_b128 v[76:79], v105 offset:43008\n\t"
        "v_mfma_f32_16x16x32_bf16 v[16:19], v[80:83], a[92:95], v[16:19]\n\t"
        "s_add_u32 m0, %[ld], 8192\n\t"
        "s_add_u32 %[t0], %[xo], 128\n\t"
        "v_mfma_f32_16x16x32_bf16 v[20:23], a[84:87], v[84:87], v[20:23]\n\t"
        "buffer_load_dwordx4 v100, %[sx], %[t0] offen lds\n\t"
        "v_mfma_f32_16x16x32_bf16 v[24:27], a[88:91], v[84:87], v[24:27]\n\t"
        "ds_read_b128 v[80:83], v106 offset:40960\n\t"
        "v_mfma_f32_16x16x32_bf16 v[28:31], v[84:87], a[92:95], v[28:31]\n\t"
        "s_waitcnt lgkmcnt(6)\n\t"
        "v_mfma_f32_16x16x32_bf16 v[8:11], a[96:99], v[56:59], v[8:11]\n\t"
        "v_mfma_f32_16x16x32_bf16 v[12:15], a[100:103], v[56:59], v[12:15]\n\t"
        "ds_read_b128 v[84:87], v106 offset:43008\n\t"
        "v_mfma_f32_16x16x32_bf16 v[16:19], v[56:59], a[104:107], v[16:19]\n\t"
        "s_add_u32 m0, %[ld], 9216\n\t"
        "s_add_u32 %[t0], %[xo], 16512\n\t"
        "s_waitcnt lgkmcnt(6)\n\t"
        "v_mfma_f32_16x16x32_bf16 v[20:23], a[96:99], v[60:63], v[20:23]\n\t"
        "buffer_load_dwordx4 v100, %[sx], %[t0] offen lds\n\t"
        "v_mfma_f32_16x16x32_bf16 v[24:27], a[100:103], v[60:63], v[24:27]\n\t"
        "ds_read_b128 v[56:59], v105 offset:49152\n\t"
        "v_mfma_f32_16x16x32_bf16 v[28:31], v[60:63], a[104:107], v[28:31]\n\t"
        "s_waitcnt lgkmcnt(6)\n\t"
        "v_mfma_f32_16x16x32_bf16 v[8:11], a[108:111], v[64:67], v[8:11]\n\t"
        "v_mfma_f32_16x16x32_bf16 v[12:15], a[112:115], v[64:67], v[12:15]\n\t"
        "ds_read_b128 v[60:63], v105 offset:51200\n\t"
        "v_mfma_f32_16x16x32_bf16 v[16:19], v[64:67], a[116:119], v[16:19]\n\t"
        "s_add_u32 m0, %[ld], 16384\n\t"
        "s_add_u32 %[t0], %[xo], 256\n\t"
        "s_waitcnt lgkmcnt(6)\n\t"
        "v_mfma_f32_16x16x32_bf16 v[20:23], a[108:111], v[68:71], v[20:23]\n\t"
        "buffer_load_dwordx4 v100, %[sx], %[t0] offen lds\n\t"
        "v_mfma_f32_16x16x32_bf16 v[24:27], a[112:115], v[68:71], v[24:27]\n\t"
        "ds_read_b128 v[64:67], v106 offset:49152\n\t"
        "v_mfma_f32_16x16x32_bf16 v[28:31], v[68:71], a[116:119], v[28:31]\n\t"
        "s_waitcnt lgkmcnt(6)\n\t"
        "v_mfma_f32_16x16x32_bf16 v[8:11], a[120:123], v[72:75], v[8:11]\n\t"
        "v_mfma_f32_16x16x32_bf16 v[12:15], a[124:127], v[72:75], v[12:15]\n\t"
        "ds_read_b128 v[68:71], v106 offset:51200\n\t"
        "v_mfma_f32_16x16x32_bf16 v[16:19], v[72:75], a[128:131], v[16:19]\n\t"
        "s_add_u32 m0, %[ld], 17408\n\t"
        "s_add_u32 %[t0], %[xo], 16640\n\t"
        "s_waitcnt lgkmcnt(6)\n\t"
        "v_mfma_f32_16x16x32_bf16 v[20:23], a[120:123], v[76:79], v[20:23]\n\t"
        "buffer_load_dwordx4 v100, %[sx], %[t0] offen lds\n\t"
        "v_mfma_f32_16x16x32_bf16 v[24:27], a[124:127], v[76:79], v[24:27]\n\t"
        "ds_read_b128 v[72:75], v105 offset:57344\n\t"
        "v_mfma_f32_16x16x32_bf16 v[28:31], v[76:79], a[128:131], v[28:31]\n\t"
        "s_waitcnt lgkmcnt(6)\n\t"
        "v_mfma_f32_16x16x32_bf16 v[8:11], a[132:135], v[80:83], v[8:11]\n\t"
        "v_mfma_f32_16x16x32_bf16 v[12:15], a[136:139], v[80:83], v[12:15]\n\t"
        "ds_read_b128 v[76:79], v105 offset:59392\n\t"
        "v_mfma_f32_16x16x32_bf16 v[16:19], v[80:83], a[140:143], v[16:19]\n\t"
        "s_add_u32 m0, %[ld], 24576\n\t"
        "s_add_u32 %[t0], %[xo], 384\n\t"
        "s_waitcnt lgkmcnt(6)\n\t"
        "v_mfma_f32_16x16x32_bf16 v[20:23], a[132:135], v[84:87], v[20:23]\n\t"
        "buffer_load_dwordx4 v100, %[sx], %[t0] offen lds\n\t"
        "v_mfma_f32_16x16x32_bf16 v[24:27], a[136:139], v[84:87], v[24:27]\n\t"
        "ds_read_b128 v[80:83], v106 offset:57344\n\t"
        "v_mfma_f32_16x16x32_bf16 v[28:31], v[84:87], a[140:143], v[28:31]\n\t"
        "s_waitcnt lgkmcnt(6)\n\t"
        "v_mfma_f32_16x16x32_bf16 v[8:11], a[144:147], v[56:59], v[8:11]\n\t"
        "v_mfma_f32_16x16x32_bf16 v[12:15], a[148:151], v[56:59], v[12:15]\n\t"
        "ds_read_b128 v[84:87], v106 offset:59392\n\t"
        "v_mfma_f32_16x16x32_bf16 v[16:19], v[56:59], a[152:155], v[16:19]\n\t"
        "s_add_u32 m0, %[ld], 25600\n\t"
        "s_add_u32 %[t0], %[xo], 16768\n\t"
        "s_waitcnt lgkmcnt(6)\n\t"
        "v_mfma_f32_16x16x32_bf16 v[20:23], a[144:147], v[60:63], v[20:23]\n\t"
        "buffer_load_dwordx4 v100, %[sx], %[t0] offen lds\n\t"
        "v_mfma_f32_16x16x32_bf16 v[24:27], a[148:151], v[60:63], v[24:27]\n\t"
        "v_mfma_f32_16x16x32_bf16 v[28:31], v[60:63], a[152:155], v[28:31]\n\t"
        "s_waitcnt lgkmcnt(5)\n\t"
        "v_mfma_f32_16x16x32_bf16 v[8:11], a[156:159], v[64:67], v[8:11]\n\t"
        "s_waitcnt vmcnt(9) lgkmcnt(0)\n\t"
        "s_barrier\n\t"
        "v_mfma_f32_16x16x32_bf16 v[12:15], a[160:163], v[64:67], v[12:15]\n\t"
        "ds_read_b128 v[56:59], v107 offset:0\n\t"
        "v_mfma_f32_16x16x32_bf16 v[16:19], v[64:67], a[164:167], v[16:19]\n\t"
        "ds_read_b128 v[60:63], v107 offset:2048\n\t"
        "s_add_u32 m0, %[ld], 32768\n\t"
        "s_add_u32 %[t0], %[xo], 512\n\t"
        "v_mfma_f32_16x16x32_bf16 v[20:23], a[156:159], v[68:71], v[20:23]\n\t"
        "buffer_load_dwordx4 v100, %[sx], %[t0] offen lds\n\t"
        "v_mfma_f32_16x16x32_bf16 v[24:27], a[160:163], v[68:71], v[24:27]\n\t"
        "ds_read_b128 v[64:67], v108 offset:0\n\t"
        "v_mfma_f32_16x16x32_bf16 v[28:31], v[68:71], a[164:167], v[28:31]\n\t"
        "v_mfma_f32_16x16x32_bf16 v[8:11], a[168:171], v[72:75], v[8:11]\n\t"
        "v_mfma_f32_16x16x32_bf16 v[12:15], a[172:175], v[72:75], v[12:15]\n\t"
        "ds_read_b128 v[68:71], v108 offset:2048\n\t"
        "v_mov_b32_e32 v117, %[cn]\n\t"
        "buffer_store_dword v117, v113, %[sw], 0 offen sc1\n\t"
        "s_add_u32 %[cn], %[cn], 1\n\t"
        "v_mfma_f32_16x16x32_bf16 v[16:19], v[72:75], a[176:179], v[16:19]\n\t"
        "s_add_u32 m0, %[ld], 33792\n\t"
        "s_add_u32 %[t0], %[xo], 16896\n\t"
        "v_mfma_f32_16x16x32_bf16 v[20:23], a[168:171], v[76:79], v[20:23]\n\t"
        "buffer_load_dwordx4 v100, %[sx], %[t0] offen lds\n\t"
        "v_mfma_f32_16x16x32_bf16 v[24:27], a[172:175], v[76:79], v[24:27]\n\t"
        "ds_read_b128 v[72:75], v107 offset:8192\n\t"
        "v_mfma_f32_16x16x32_bf16 v[28:31], v[76:79], a[176:179], v[28:31]\n\t"
        "v_mfma_f32_16x16x32_bf16 v[8:11], a[180:183], v[80:83], v[8:11]\n\t"
        "v_mfma_f32_16x16x32_bf16 v[12:15], a[184:187], v[80:83], v[12:15]\n\t"
        "ds_read_b128 v[76:79], v107 offset:10240\n\t"
        "v_mfma_f32_16x16x32_bf16 v[16:19], v[80:83], a[188:191], v[16:19]\n\t"
        "s_add_u32 m0, %[ld], 40960\n\t"
        "s_add_u32 %[t0], %[xo], 640\n\t"
        "v_mfma_f32_16x16x32_bf16 v[20:23], a[180:183], v[84:87], v[20:23]\n\t"
        "buffer_load_dwordx4 v100, %[sx], %[t0] offen lds\n\t"
        "v_mfma_f32_16x16x32_bf16 v[24:27], a[184:187], v[84:87], v[24:27]\n\t"
        "ds_read_b128 v[80:83], v108 offset:8192\n\t"
        "v_mfma_f32_16x16x32_bf16 v[28:31], v[84:87], a[188:191], v[28:31]\n\t"
        "s_waitcnt lgkmcnt(6)\n\t"
        "v_mfma_f32_16x16x32_bf16 v[8:11], a[192:195], v[56:59], v[8:11]\n\t"
        "v_mfma_f32_16x16x32_bf16 v[12:15], a[196:199], v[56:59], v[12:15]\n\t"
        "ds_read_b128 v[84:87], v108 offset:10240\n\t"
        "v_mfma_f32_16x16x32_bf16 v[16:19], v[56:59], a[200:203], v[16:19]\n\t"
        "s_add_u32 m0, %[ld], 41984\n\t"
        "s_add_u32 %[t0], %[xo], 17024\n\t"
        "s_waitcnt lgkmcnt(6)\n\t"
        "v_mfma_f32_16x16x32_bf16 v[20:23], a[192:195], v[60:63], v[20:23]\n\t"
        "buffer_load_dwordx4 v100, %[sx], %[t0] offen lds\n\t"
        "v_mfma_f32_16x16x32_bf16 v[24:27], a[196:199], v[60:63], v[24:27]\n\t"
        "ds_read_b128 v[56:59], v107 offset:16384\n\t"
        "v_mfma_f32_16x16x32_bf16 v[28:31], v[60:63], a[200:203], v[28:31]\n\t"
        "s_waitcnt lgkmcnt(6)\n\t"
        "v_mfma_f32_16x16x32_bf16 v[8:11], a[204:207], v[64:67], v[8:11]\n\t"
        "v_mfma_f32_16x16x32_bf16 v[12:15], a[208:211], v[64:67], v[12:15]\n\t"
        "ds_read_b128 v[60:63], v107 offset:18432\n\t"
        "v_mfma_f32_16x16x32_bf16 v[16:19], v[64:67], a[212:215], v[16:19]\n\t"
        "s_add_u32 m0, %[ld], 49152\n\t"
        "s_add_u32 %[t0], %[xo], 768\n\t"
        "s_waitcnt lgkmcnt(6)\n\t"
        "v_mfma_f32_16x16x32_bf16 v[20:23], a[204:207], v[68:71], v[20:23]\n\t"
        "buffer_load_dwordx4 v100, %[sx], %[t0] offen lds\n\t"
        "v_mfma_f32_16x16x32_bf16 v[24:27], a[208:211], v[68:71], v[24:27]\n\t"
        "ds_read_b128 v[64:67], v108 offset:16384\n\t"
        "v_mfma_f32_16x16x32_bf16 v[28:31], v[68:71], a[212:215], v[28:31]\n\t"
        "s_waitcnt lgkmcnt(6)\n\t"
        "v_mfma_f32_16x16x32_bf16 v[8:11], a[216:219], v[72:75], v[8:11]\n\t"
        "v_mfma_f32_16x16x32_bf16 v[12:15], a[220:223], v[72:75], v[12:15]\n\t"
        "ds_read_b128 v[68:71], v108 offset:18432\n\t"
        "v_mfma_f32_16x16x32_bf16 v[16:19], v[72:75], a[224:227], v[16:19]\n\t"
        "s_add_u32 m0, %[ld], 50176\n\t"
        "s_add_u32 %[t0], %[xo], 17152\n\t"
        "s_waitcnt lgkmcnt(6)\n\t"
        "v_mfma_f32_16x16x32_bf16 v[20:23], a[216:219], v[76:79], v[20:23]\n\t"
        "buffer_load_dwordx4 v100, %[sx], %[t0] offen lds\n\t"
        "v_mfma_f32_16x16x32_bf16 v[24:27], a[220:223], v[76:79], v[24:27]\n\t"
        "ds_read_b128 v[72:75], v107 offset:24576\n\t"
        "v_mfma_f32_16x16x32_bf16 v[28:31], v[76:79], a[224:227], v[28:31]\n\t"
        "s_waitcnt lgkmcnt(6)\n\t"
        "v_mfma_f32_16x16x32_bf16 v[8:11], a[228:231], v[80:83], v[8:11]\n\t"
        "v_mfma_f32_16x16x32_bf16 v[12:15], a[232:235], v[80:83], v[12:15]\n\t"
        "ds_read_b128 v[76:79], v107 offset:26624\n\t"
        "v_mfma_f32_16x16x32_bf16 v[16:19], v[80:83], a[236:239], v[16:19]\n\t"
        "s_add_u32 m0, %[ld], 57344\n\t"
        "s_add_u32 %[t0], %[xo], 896\n\t"
        "s_waitcnt lgkmcnt(6)\n\t"
        "v_mfma_f32_16x16x32_bf16 v[20:23], a[228:231], v[84:87], v[20:23]\n\t"
        "buffer_load_dwordx4 v100, %[sx], %[t0] offen lds\n\t"
        "v_mfma_f32_16x16x32_bf16 v[24:27], a[232:235], v[84:87], v[24:27]\n\t"
        "ds_read_b128 v[80:83], v108 offset:24576\n\t"
        "v_mfma_f32_16x16x32_bf16 v[28:31], v[84:87], a[236:239], v[28:31]\n\t"
        "s_waitcnt lgkmcnt(6)\n\t"
        "v_mfma_f32_16x16x32_bf16 v[8:11], a[240:243], v[56:59], v[8:11]\n\t"
        "v_mfma_f32_16x16x32_bf16 v[12:15], a[244:247], v[56:59], v[12:15]\n\t"
        "ds_read_b128 v[84:87], v108 offset:26624\n\t"
        "v_mfma_f32_16x16x32_bf16 v[16:19], v[56:59], a[248:251], v[16:19]\n\t"
        "s_add_u32 m0, %[ld], 58368\n\t"
        "s_add_u32 %[t0], %[xo], 17280\n\t"
        "s_waitcnt lgkmcnt(6)\n\t"
        "v_mfma_f32_16x16x32_bf16 v[20:23], a[240:243], v[60:63], v[20:23]\n\t"
        "buffer_load_dwordx4 v100, %[sx], %[t0] offen lds\n\t"
        "v_mfma_f32_16x16x32_bf16 v[24:27], a[244:247], v[60:63], v[24:27]\n\t"
        "v_mfma_f32_16x16x32_bf16 v[28:31], v[60:63], a[248:251], v[28:31]\n\t"
        "s_waitcnt lgkmcnt(5)\n\t"
        "v_mfma_f32_16x16x32_bf16 v[8:11], a[252:255], v[64:67], v[8:11]\n\t"
        "s_waitcnt vmcnt(17) lgkmcnt(0)\n\t"
        "s_barrier\n\t"
        "v_mfma_f32_16x16x32_bf16 v[12:15], v[128:131], v[64:67], v[12:15]\n\t"
        "ds_read_b128 v[56:59], v107 offset:32768\n\t"
        "v_mfma_f32_16x16x32_bf16 v[16:19], v[64:67], v[132:135], v[16:19]\n\t"
        "ds_read_b128 v[60:63], v107 offset:34816\n\t"
        "s_add_u32 m0, %[ld], 65536\n\t"
        "s_add_u32 %[t0], %[xo], 1024\n\t"
        "v_mfma_f32_16x16x32_bf16 v[20:23], a[252:255], v[68:71], v[20:23]\n\t"
        "buffer_load_dwordx4 v100, %[sx], %[t0] offen lds\n\t"
        "v_mfma_f32_16x16x32_bf16 v[24:27], v[128:131], v[68:71], v[24:27]\n\t"
        "ds_read_b128 v[64:67], v108 offset:32768\n\t"
        "v_mfma_f32_16x16x32_bf16 v[28:31], v[68:71], v[132:135], v[28:31]\n\t"
        "v_mfma_f32_16x16x32_bf16 v[8:11], v[136:139], v[72:75], v[8:11]\n\t"
        "v_mfma_f32_16x16x32_bf16 v[12:15], v[140:143], v[72:75], v[12:15]\n\t"
        "ds_read_b128 v[68:71], v108 offset:34816\n\t"
        "v_mfma_f32_16x16x32_bf16 v[16:19], v[72:75], v[144:147], v[16:19]\n\t"
        "s_add_u32 m0, %[ld], 66560\n\t"
        "s_add_u32 %[t0], %[xo], 17408\n\t"
        "v_mfma_f32_16x16x32_bf16 v[20:23], v[136:139], v[76:79], v[20:23]\n\t"
        "buffer_load_dwordx4 v100, %[sx], %[t0] offen lds\n\t"
        "v_mfma_f32_16x16x32_bf16 v[24:27], v[140:143], v[76:79], v[24:27]\n\t"
        "ds_read_b128 v[72:75], v107 offset:40960\n\t"
        "v_mfma_f32_16x16x32_bf16 v[28:31], v[76:79], v[144:147], v[28:31]\n\t"
        "v_mfma_f32_16x16x32_bf16 v[8:11], v[148:151], v[80:83], v[8:11]\n\t"
        "v_mfma_f32_16x16x32_bf16 v[12:15], v[152:155], v[80:83], v[12:15]\n\t"
        "ds_read_b128 v[76:79], v107 offset:43008\n\t"
        "v_mfma_f32_16x16x32_bf16 v[16:19], v[80:83], v[156:159], v[16:19]\n\t"
        "s_add_u32 m0, %[ld], 73728\n\t"
        "s_add_u32 %[t0], %[xo], 1152\n\t"
        "v_mfma_f32_16x16x32_bf16 v[20:23], v[148:151], v[84:87], v[20:23]\n\t"
        "buffer_load_dwordx4 v100, %[sx], %[t0] offen lds\n\t"
        "v_mfma_f32_16x16x32_bf16 v[24:27], v[152:155], v[84:87], v[24:27]\n\t"
        "ds_read_b128 v[80:83], v108 offset:40960\n\t"
        "v_mfma_f32_16x16x32_bf16 v[28:31], v[84:87], v[156:159], v[28:31]\n\t"
        "s_waitcnt lgkmcnt(6)\n\t"
        "v_mfma_f32_16x16x32_bf16 v[8:11], v[160:163], v[56:59], v[8:11]\n\t"
        "v_mfma_f32_16x16x32_bf16 v[12:15], v[164:167], v[56:59], v[12:15]\n\t"
        "ds_read_b128 v[84:87], v108 offset:43008\n\t"
        "v_mfma_f32_16x16x32_bf16 v[16:19], v[56:59], v[168:171], v[16:19]\n\t"
        "s_add_u32 m0, %[ld], 74752\n\t"
        "s_add_u32 %[t0], %[xo], 17536\n\t"
        "s_waitcnt lgkmcnt(6)\n\t"
        "v_mfma_f32_16x16x32_bf16 v[20:23], v[160:163], v[60:63], v[20:23]\n\t"
        "buffer_load_dwordx4 v100, %[sx], %[t0] offen lds\n\t"
        "v_mfma_f32_16x16x32_bf16 v[24:27], v[164:167], v[60:63], v[24:27]\n\t"
        "ds_read_b128 v[56:59], v107 offset:49152\n\t"
        "v_mfma_f32_16x16x32_bf16 v[28:31], v[60:63], v[168:171], v[28:31]\n\t"
        "s_waitcnt lgkmcnt(6)\n\t"
        "v_mfma_f32_16x16x32_bf16 v[8:11], v[172:175], v[64:67], v[8:11]\n\t"
        "v_mfma_f32_16x16x32_bf16 v[12:15], v[176:179], v[64:67], v[12:15]\n\t"
        "ds_read_b128 v[60:63], v107 offset:51200\n\t"
        "v_mfma_f32_16x16x32_bf16 v[16:19], v[64:67], v[180:183], v[16:19]\n\t"
        "s_add_u32 m0, %[ld], 81920\n\t"
        "s_add_u32 %[t0], %[xo], 1280\n\t"
        "s_waitcnt lgkmcnt(6)\n\t"
        "v_mfma_f32_16x16x32_bf16 v[20:23], v[172:175], v[68:71], v[20:23]\n\t"
        "buffer_load_dwordx4 v100, %[sx], %[t0] offen lds\n\t"
        "v_mfma_f32_16x16x32_bf16 v[24:27], v[176:179], v[68:71], v[24:27]\n\t"
        "ds_read_b128 v[64:67], v108 offset:49152\n\t"
        "v_mfma_f32_16x16x32_bf16 v[28:31], v[68:71], v[180:183], v[28:31]\n\t"
        "s_waitcnt lgkmcnt(6)\n\t"
        "v_mfma_f32_16x16x32_bf16 v[8:11], v[184:187], v[72:75], v[8:11]\n\t"
        "v_mfma_f32_16x16x32_bf16 v[12:15], v[188:191], v[72:75], v[12:15]\n\t"
        "ds_read_b128 v[68:71], v108 offset:51200\n\t"
        "v_mfma_f32_16x16x32_bf16 v[16:19], v[72:75], v[192:195], v[16:19]\n\t"
        "s_add_u32 m0, %[ld], 82944\n\t"
        "s_add_u32 %[t0], %[xo], 17664\n\t"
        "s_waitcnt lgkmcnt(6)\n\t"
        "v_mfma_f32_16x16x32_bf16 v[20:23], v[184:187], v[76:79], v[20:23]\n\t"
        "buffer_load_dwordx4 v100, %[sx], %[t0] offen lds\n\t"
        "v_mfma_f32_16x16x32_bf16 v[24:27], v[188:191], v[76:79], v[24:27]\n\t"
        "ds_read_b128 v[72:75], v107 offset:57344\n\t"
        "v_mfma_f32_16x16x32_bf16 v[28:31], v[76:79], v[192:195], v[28:31]\n\t"
        "s_waitcnt lgkmcnt(6)\n\t"
        "v_mfma_f32_16x16x32_bf16 v[8:11], v[196:199], v[80:83], v[8:11]\n\t"
        "v_mfma_f32_16x16x32_bf16 v[12:15], v[200:203], v[80:83], v[12:15]\n\t"
        "ds_read_b128 v[76:79], v107 offset:59392\n\t"
        "v_mfma_f32_16x16x32_bf16 v[16:19], v[80:83], v[204:207], v[16:19]\n\t"
        "s_add_u32 m0, %[ld], 90112\n\t"
        "s_add_u32 %[t0], %[xo], 1408\n\t"
        "s_waitcnt lgkmcnt(6)\n\t"
        "v_mfma_f32_16x16x32_bf16 v[20:23], v[196:199], v[84:87], v[20:23]\n\t"
        "buffer_load_dwordx4 v100, %[sx], %[t0] offen lds\n\t"
        "v_mfma_f32_16x16x32_bf16 v[24:27], v[200:203], v[84:87], v[24:27]\n\t"
        "ds_read_b128 v[80:83], v108 offset:57344\n\t"
        "v_mfma_f32_16x16x32_bf16 v[28:31], v[84:87], v[204:207], v[28:31]\n\t"
        "s_waitcnt lgkmcnt(6)\n\t"
        "v_mfma_f32_16x16x32_bf16 v[8:11], v[208:211], v[56:59], v[8:11]\n\t"
        "v_mfma_f32_16x16x32_bf16 v[12:15], v[212:215], v[56:59], v[12:15]\n\t"
        "ds_read_b128 v[84:87], v108 offset:59392\n\t"
        "v_mfma_f32_16x16x32_bf16 v[16:19], v[56:59], v[216:219], v[16:19]\n\t"
        "s_add_u32 m0, %[ld], 91136\n\t"
        "s_add_u32 %[t0], %[xo], 17792\n\t"
        "s_waitcnt lgkmcnt(6)\n\t"
        "v_mfma_f32_16x16x32_bf16 v[20:23], v[208:211], v[60:63], v[20:23]\n\t"
        "buffer_load_dwordx4 v100, %[sx], %[t0] offen lds\n\t"
        "v_mfma_f32_16x16x32_bf16 v[24:27], v[212:215], v[60:63], v[24:27]\n\t"
        "v_mfma_f32_16x16x32_bf16 v[28:31], v[60:63], v[216:219], v[28:31]\n\t"
        "s_waitcnt lgkmcnt(5)\n\t"
        "v_mfma_f32_16x16x32_bf16 v[8:11], v[220:223], v[64:67], v[8:11]\n\t"
        "s_waitcnt vmcnt(17) lgkmcnt(0)\n\t"
        "s_barrier\n\t"
        "v_mfma_f32_16x16x32_bf16 v[12:15], v[224:227], v[64:67], v[12:15]\n\t"
        "ds_read_b128 v[56:59], v105 offset:0\n\t"
        "v_mfma_f32_16x16x32_bf16 v[16:19], v[64:67], v[228:231], v[16:19]\n\t"
        "ds_read_b128 v[60:63], v105 offset:2048\n\t"
        "s_add_u32 m0, %[ld], 98304\n\t"
        "s_add_u32 %[t0], %[xo], 1536\n\t"
        "v_mfma_f32_16x16x32_bf16 v[20:23], v[220:223], v[68:71], v[20:23]\n\t"
        "buffer_load_dwordx4 v100, %[sx], %[t0] offen lds\n\t"
        "v_mfma_f32_16x16x32_bf16 v[24:27], v[224:227], v[68:71], v[24:27]\n\t"
        "ds_read_b128 v[64:67], v106 offset:0\n\t"
        "v_mfma_f32_16x16x32_bf16 v[28:31], v[68:71], v[228:231], v[28:31]\n\t"
        "v_mfma_f32_16x16x32_bf16 v[8:11], v[232:235], v[72:75], v[8:11]\n\t"
        "v_mfma_f32_16x16x32_bf16 v[12:15], v[236:239], v[72:75], v[12:15]\n\t"
        "ds_read_b128 v[68:71], v106 offset:2048\n\t"
        "v_mfma_f32_16x16x32_bf16 v[16:19], v[72:75], v[240:243], v[16:19]\n\t"
        "s_add_u32 m0, %[ld], 99328\n\t"
        "s_add_u32 %[t0], %[xo], 17920\n\t"
        "v_mfma_f32_16x16x32_bf16 v[20:23], v[232:235], v[76:79], v[20:23]\n\t"
        "buffer_load_dwordx4 v100, %[sx], %[t0] offen lds\n\t"
        "v_mfma_f32_16x16x32_bf16 v[24:27], v[236:239], v[76:79], v[24:27]\n\t"
        "ds_read_b128 v[72:75], v105 offset:8192\n\t"
        "v_mfma_f32_16x16x32_bf16 v[28:31], v[76:79], v[240:243], v[28:31]\n\t"
        "v_mfma_f32_16x16x32_bf16 v[8:11], v[244:247], v[80:83], v[8:11]\n\t"
        "v_mfma_f32_16x16x32_bf16 v[12:15], v[248:251], v[80:83], v[12:15]\n\t"
        "ds_read_b128 v[76:79], v105 offset:10240\n\t"
        "v_mfma_f32_16x16x32_bf16 v[16:19], v[80:83], v[252:255], v[16:19]\n\t"
        "s_add_u32 m0, %[ld], 106496\n\t"
        "s_add_u32 %[t0], %[xo], 1664\n\t"
        "v_mfma_f32_16x16x32_bf16 v[20:23], v[244:247], v[84:87], v[20:23]\n\t"
        "buffer_load_dwordx4 v100, %[sx], %[t0] offen lds\n\t"
        "v_mfma_f32_16x16x32_bf16 v[24:27], v[248:251], v[84:87], v[24:27]\n\t"
        "ds_read_b128 v[80:83], v106 offset:8192\n\t"
        "v_mfma_f32_16x16x32_bf16 v[28:31], v[84:87], v[252:255], v[28:31]\n\t"
        "LG_mid%=:\n\t"
        "s_waitcnt lgkmcnt(6)\n\t"
        "v_mfma_f32_16x16x32_bf16 v[32:35], a[0:3], v[56:59], 0\n\t"
        "v_mfma_f32_16x16x32_bf16 v[36:39], a[4:7], v[56:59], 0\n\t"
        "ds_read_b128 v[84:87], v106 offset:10240\n\t"
        "v_mfma_f32_16x16x32_bf16 v[40:43], v[56:59], a[8:11], 0\n\t"
        "s_add_u32 m0, %[ld], 107520\n\t"
        "s_add_u32 %[t0], %[xo], 18048\n\t"
        "s_waitcnt lgkmcnt(6)\n\t"
        "v_mfma_f32_16x16x32_bf16 v[44:47], a[0:3], v[60:63], 0\n\t"
        "buffer_load_dwordx4 v100, %[sx], %[t0] offen lds\n\t"
        "v_mfma_f32_16x16x32_bf16 v[48:51], a[4:7], v[60:63], 0\n\t"
        "ds_read_b128 v[56:59], v105 offset:16384\n\t"
        "v_mfma_f32_16x16x32_bf16 v[52:55], v[60:63], a[8:11], 0\n\t"
        "s_waitcnt lgkmcnt(6)\n\t"
        "v_mfma_f32_16x16x32_bf16 v[32:35], a[12:15], v[64:67], v[32:35]\n\t"
        "v_cvt_pk_bf16_f32 v88, v8, v9\n\t"
        "v_cvt_pk_bf16_f32 v89, v10, v11\n\t"
        "v_mfma_f32_16x16x32_bf16 v[36:39], a[16:19], v[64:67], v[36:39]\n\t"
        "ds_read_b128 v[60:63], v105 offset:18432\n\t"
        "v_cvt_pk_bf16_f32 v90, v12, v13\n\t"
        "v_mfma_f32_16x16x32_bf16 v[40:43], v[64:67], a[20:23], v[40:43]\n\t"
        "s_add_u32 m0, %[ld], 114688\n\t"
        "s_add_u32 %[t0], %[xo], 1792\n\t"
        "s_waitcnt lgkmcnt(6)\n\t"
        "v_mfma_f32_16x16x32_bf16 v[44:47], a[12:15], v[68:71], v[44:47]\n\t"
        "buffer_load_dwordx4 v100, %[sx], %[t0] offen lds\n\t"
        "v_mfma_f32_16x16x32_bf16 v[48:51], a[16:19], v[68:71], v[48:51]\n\t"
        "ds_read_b128 v[64:67], v106 offset:16384\n\t"
        "v_cvt_pk_bf16_f32 v91, v14, v15\n\t"
        "v_mfma_f32_16x16x32_bf16 v[52:55], v[68:71], a[20:23], v[52:55]\n\t"
        "buffer_store_dwordx4 v[88:91], v109, %[sw], %[so] offen sc1\n\t"
        "s_waitcnt lgkmcnt(6)\n\t"
        "v_mfma_f32_16x16x32_bf16 v[32:35], a[24:27], v[72:75], v[32:35]\n\t"
        "v_cvt_pk_bf16_f32 v92, v20, v21\n\t"
        "v_cvt_pk_bf16_f32 v93, v22, v23\n\t"
        "v_mfma_f32_16x16x32_bf16 v[36:39], a[28:31], v[72:75], v[36:39]\n\t"
        "ds_read_b128 v[68:71], v106 offset:18432\n\t"
        "v_cvt_pk_bf16_f32 v94, v24, v25\n\t"
        "v_mfma_f32_16x16x32_bf16 v[40:43], v[72:75], a[32:35], v[40:43]\n\t"
        "s_add_u32 m0, %[ld], 115712\n\t"
        "s_add_u32 %[t0], %[xo], 18176\n\t"
        "s_waitcnt lgkmcnt(6)\n\t"
        "v_mfma_f32_16x16x32_bf16 v[44:47], a[24:27], v[76:79], v[44:47]\n\t"
        "buffer_load_dwordx4 v100, %[sx], %[t0] offen lds\n\t"
        "v_mfma_f32_16x16x32_bf16 v[48:51], a[28:31], v[76:79], v[48:51]\n\t"
        "ds_read_b128 v[72:75], v105 offset:24576\n\t"
        "v_cvt_pk_bf16_f32 v95, v26, v27\n\t"
        "v_mfma_f32_16x16x32_bf16 v[52:55], v[76:79], a[32:35], v[52:55]\n\t"
        "buffer_store_dwordx4 v[92:95], v110, %[sw], %[so] offen sc1\n\t"
        "s_waitcnt lgkmcnt(6)\n\t"
        "v_mfma_f32_16x16x32_bf16 v[32:35], a[36:39], v[80:83], v[32:35]\n\t"
        "v_cvt_pk_bf16_f32 v96, v16, v17\n\t"
        "v_cvt_pk_bf16_f32 v97, v18, v19\n\t"
        "v_mfma_f32_16x16x32_bf16 v[36:39], a[40:43], v[80:83], v[36:39]\n\t"
        "ds_read_b128 v[76:79], v105 offset:26624\n\t"
        "v_cvt_pk_bf16_f32 v98, v28, v29\n\t"
        "v_mfma_f32_16x16x32_bf16 v[40:43], v[80:83], a[44:47], v[40:43]\n\t"
        "s_add_u32 m0, %[ld], 122880\n\t"
        "s_add_u32 %[t0], %[xo], 1920\n\t"
        "s_waitcnt lgkmcnt(6)\n\t"
        "v_mfma_f32_16x16x32_bf16 v[44:47], a[36:39], v[84:87], v[44:47]\n\t"
        "buffer_load_dwordx4 v100, %[sx], %[t0] offen lds\n\t"
        "v_mfma_f32_16x16x32_bf16 v[48:51], a[40:43], v[84:87], v[48:51]\n\t"
        "ds_read_b128 v[80:83], v106 offset:24576\n\t"
        "v_cvt_pk_bf16_f32 v99, v30, v31\n\t"
        "v_mfma_f32_16x16x32_bf16 v[52:55], v[84:87], a[44:47], v[52:55]\n\t"
        "buffer_store_dwordx4 v[96:99], v111, %[sw], %[so] offen sc1\n\t"
        "s_waitcnt lgkmcnt(6)\n\t"
        "v_mfma_f32_16x16x32_bf16 v[32:35], a[48:51], v[56:59], v[32:35]\n\t"
        "s_lshl_b32 %[so], %[cn], 19\n\t"
        "v_mfma_f32_16x16x32_bf16 v[36:39], a[52:55], v[56:59], v[36:39]\n\t"
        "ds_read_b128 v[84:87], v106 offset:26624\n\t"
        "v_mfma_f32_16x16x32_bf16 v[40:43], v[56:59], a[56:59], v[40:43]\n\t"
        "s_add_u32 m0, %[ld], 123904\n\t"
        "s_add_u32 %[t0], %[xo], 18304\n\t"
        "s_waitcnt lgkmcnt(6)\n\t"
        "v_mfma_f32_16x16x32_bf16 v[44:47], a[48:51], v[60:63], v[44:47]\n\t"
        "buffer_load_dwordx4 v100, %[sx], %[t0] offen lds\n\t"
        "v_mfma_f32_16x16x32_bf16 v[48:51], a[52:55], v[60:63], v[48:51]\n\t"
        "v_mfma_f32_16x16x32_bf16 v[52:55], v[60:63], a[56:59], v[52:55]\n\t"
        "s_waitcnt lgkmcnt(5)\n\t"
        "v_mfma_f32_16x16x32_bf16 v[32:35], a[60:63], v[64:67], v[32:35]\n\t"
        "s_waitcnt vmcnt(19) lgkmcnt(0)\n\t"
        "s_barrier\n\t"
        "s_add_u32 %[xo], %[xo], 0x20000\n\t"
        "v_mfma_f32_16x16x32_bf16 v[36:39], a[64:67], v[64:67], v[36:39]\n\t"
        "ds_read_b128 v[56:59], v105 offset:32768\n\t"
        "v_mfma_f32_16x16x32_bf16 v[40:43], v[64:67], a[68:71], v[40:43]\n\t"
        "ds_read_b128 v[60:63], v105 offset:34816\n\t"
        "s_add_u32 m0, %[ld], 0\n\t"
        "s_add_u32 %[t0], %[xo], 0\n\t"
        "v_mfma_f32_16x16x32_bf16 v[44:47], a[60:63], v[68:71], v[44:47]\n\t"
        "buffer_load_dwordx4 v100, %[sx], %[t0] offen lds\n\t"
        "v_mfma_f32_16x16x32_bf16 v[48:51], a[64:67], v[68:71], v[48:51]\n\t"
        "ds_read_b128 v[64:67], v106 offset:32768\n\t"
        "v_mfma_f32_16x16x32_bf16 v[52:55], v[68:71], a[68:71], v[52:55]\n\t"
        "v_mfma_f32_16x16x32_bf16 v[32:35], a[72:75], v[72:75], v[32:35]\n\t"
        "v_mfma_f32_16x16x32_bf16 v[36:39], a[76:79], v[72:75], v[36:39]\n\t"
        "ds_read_b128 v[68:71], v106 offset:34816\n\t"
        "v_mfma_f32_16x16x32_bf16 v[40:43], v[72:75], a[80:83], v[40:43]\n\t"
        "s_add_u32 m0, %[ld], 1024\n\t"
        "s_add_u32 %[t0], %[xo], 16384\n\t"
        "v_mfma_f32_16x16x32_bf16 v[44:47], a[72:75], v[76:79], v[44:47]\n\t"
        "buffer_load_dwordx4 v100, %[sx], %[t0] offen lds\n\t"
        "v_mfma_f32_16x16x32_bf16 v[48:51], a[76:79], v[76:79], v[48:51]\n\t"
        "ds_read_b128 v[72:75], v105 offset:40960\n\t"
        "v_mfma_f32_16x16x32_bf16 v[52:55], v[76:79], a[80:83], v[52:55]\n\t"
        "v_mfma_f32_16x16x32_bf16 v[32:35], a[84:87], v[80:83], v[32:35]\n\t"
        "v_mfma_f32_16x16x32_bf16 v[36:39], a[88:91], v[80:83], v[36:39]\n\t"
        "ds_read_b128 v[76:79], v105 offset:43008\n\t"
        "v_mfma_f32_16x16x32_bf16 v[40:43], v[80:83], a[92:95], v[40:43]\n\t"
        "s_add_u32 m0, %[ld], 8192\n\t"
        "s_add_u32 %[t0], %[xo], 128\n\t"
        "v_mfma_f32_16x16x32_bf16 v[44:47], a[84:87], v[84:87], v[44:47]\n\t"
        "buffer_load_dwordx4 v100, %[sx], %[t0] offen lds\n\t"
        "v_mfma_f32_16x16x32_bf16 v[48:51], a[88:91], v[84:87], v[48:51]\n\t"
        "ds_read_b128 v[80:83], v106 offset:40960\n\t"
        "v_mfma_f32_16x16x32_bf16 v[52:55], v[84:87], a[92:95], v[52:55]\n\t"
        "s_waitcnt lgkmcnt(6)\n\t"
        "v_mfma_f32_16x16x32_bf16 v[32:35], a[96:99], v[56:59], v[32:35]\n\t"
        "v_mfma_f32_16x16x32_bf16 v[36:39], a[100:103], v[56:59], v[36:39]\n\t"
        "ds_read_b128 v[84:87], v106 offset:43008\n\t"
        "v_mfma_f32_16x16x32_bf16 v[40:43], v[56:59], a[104:107], v[40:43]\n\t"
        "s_add_u32 m0, %[ld], 9216\n\t"
        "s_add_u32 %[t0], %[xo], 16512\n\t"
        "s_waitcnt lgkmcnt(6)\n\t"
        "v_mfma_f32_16x16x32_bf16 v[44:47], a[96:99], v[60:63], v[44:47]\n\t"
        "buffer_load_dwordx4 v100, %[sx], %[t0] offen lds\n\t"
        "v_mfma_f32_16x16x32_bf16 v[48:51], a[100:103], v[60:63], v[48:51]\n\t"
        "ds_read_b128 v[56:59], v105 offset:49152\n\t"
        "v_mfma_f32_16x16x32_bf16 v[52:55], v[60:63], a[104:107], v[52:55]\n\t"
        "s_waitcnt lgkmcnt(6)\n\t"
        "v_mfma_f32_16x16x32_bf16 v[32:35], a[108:111], v[64:67], v[32:35]\n\t"
        "v_mfma_f32_16x16x32_bf16 v[36:39], a[112:115], v[64:67], v[36:39]\n\t"
        "ds_read_b128 v[60:63], v105 offset:51200\n\t"
        "v_mfma_f32_16x16x32_bf16 v[40:43], v[64:67], a[116:119], v[40:43]\n\t"
        "s_add_u32 m0, %[ld], 16384\n\t"
        "s_add_u32 %[t0], %[xo], 256\n\t"
        "s_waitcnt lgkmcnt(6)\n\t"
        "v_mfma_f32_16x16x32_bf16 v[44:47], a[108:111], v[68:71], v[44:47]\n\t"
        "buffer_load_dwordx4 v100, %[sx], %[t0] offen lds\n\t"
        "v_mfma_f32_16x16x32_bf16 v[48:51], a[112:115], v[68:71], v[48:51]\n\t"
        "ds_read_b128 v[64:67], v106 offset:49152\n\t"
        "v_mfma_f32_16x16x32_bf16 v[52:55], v[68:71], a[116:119], v[52:55]\n\t"
        "s_waitcnt lgkmcnt(6)\n\t"
        "v_mfma_f32_16x16x32_bf16 v[32:35], a[120:123], v[72:75], v[32:35]\n\t"
        "v_mfma_f32_16x16x32_bf16 v[36:39], a[124:127], v[72:75], v[36:39]\n\t"
        "ds_read_b128 v[68:71], v106 offset:51200\n\t"
        "v_mfma_f32_16x16x32_bf16 v[40:43], v[72:75], a[128:131], v[40:43]\n\t"
        "s_add_u32 m0, %[ld], 17408\n\t"
        "s_add_u32 %[t0], %[xo], 16640\n\t"
        "s_waitcnt lgkmcnt(6)\n\t"
        "v_mfma_f32_16x16x32_bf16 v[44:47], a[120:123], v[76:79], v[44:47]\n\t"
        "buffer_load_dwordx4 v100, %[sx], %[t0] offen lds\n\t"
        "v_mfma_f32_16x16x32_bf16 v[48:51], a[124:127], v[76:79], v[48:51]\n\t"
        "ds_read_b128 v[72:75], v105 offset:57344\n\t"
        "v_mfma_f32_16x16x32_bf16 v[52:55], v[76:79], a[128:131], v[52:55]\n\t"
        "s_waitcnt lgkmcnt(6)\n\t"
        "v_mfma_f32_16x16x32_bf16 v[32:35], a[132:135], v[80:83], v[32:35]\n\t"
        "v_mfma_f32_16x16x32_bf16 v[36:39], a[136:139], v[80:83], v[36:39]\n\t"
        "ds_read_b128 v[76:79], v105 offset:59392\n\t"
        "v_mfma_f32_16x16x32_bf16 v[40:43], v[80:83], a[140:143], v[40:43]\n\t"
        "s_add_u32 m0, %[ld], 24576\n\t"
        "s_add_u32 %[t0], %[xo], 384\n\t"
        "s_waitcnt lgkmcnt(6)\n\t"
        "v_mfma_f32_16x16x32_bf16 v[44:47], a[132:135], v[84:87], v[44:47]\n\t"
        "buffer_load_dwordx4 v100, %[sx], %[t0] offen lds\n\t"
        "v_mfma_f32_16x16x32_bf16 v[48:51], a[136:139], v[84:87], v[48:51]\n\t"
        "ds_read_b128 v[80:83], v106 offset:57344\n\t"
        "v_mfma_f32_16x16x32_bf16 v[52:55], v[84:87], a[140:143], v[52:55]\n\t"
        "s_waitcnt lgkmcnt(6)\n\t"
        "v_mfma_f32_16x16x32_bf16 v[32:35], a[144:147], v[56:59], v[32:35]\n\t"
        "v_mfma_f32_16x16x32_bf16 v[36:39], a[148:151], v[56:59], v[36:39]\n\t"
        "ds_read_b128 v[84:87], v106 offset:59392\n\t"
        "v_mfma_f32_16x16x32_bf16 v[40:43], v[56:59], a[152:155], v[40:43]\n\t"
        "s_add_u32 m0, %[ld], 25600\n\t"
        "s_add_u32 %[t0], %[xo], 16768\n\t"
        "s_waitcnt lgkmcnt(6)\n\t"
        "v_mfma_f32_16x16x32_bf16 v[44:47], a[144:147], v[60:63], v[44:47]\n\t"
        "buffer_load_dwordx4 v100, %[sx], %[t0] offen lds\n\t"
        "v_mfma_f32_16x16x32_bf16 v[48:51], a[148:151], v[60:63], v[48:51]\n\t"
        "v_mfma_f32_16x16x32_bf16 v[52:55], v[60:63], a[152:155], v[52:55]\n\t"
        "s_waitcnt lgkmcnt(5)\n\t"
        "v_mfma_f32_16x16x32_bf16 v[32:35], a[156:159], v[64:67], v[32:35]\n\t"
        "s_waitcnt vmcnt(9) lgkmcnt(0)\n\t"
        "s_barrier\n\t"
        "v_mfma_f32_16x16x32_bf16 v[36:39], a[160:163], v[64:67], v[36:39]\n\t"
        "ds_read_b128 v[56:59], v107 offset:0\n\t"
        "v_mfma_f32_16x16x32_bf16 v[40:43], v[64:67], a[164:167], v[40:43]\n\t"
        "ds_read_b128 v[60:63], v107 offset:2048\n\t"
        "s_add_u32 m0, %[ld], 32768\n\t"
        "s_add_u32 %[t0], %[xo], 512\n\t"
        "v_mfma_f32_16x16x32_bf16 v[44:47], a[156:159], v[68:71], v[44:47]\n\t"
        "buffer_load_dwordx4 v100, %[sx], %[t0] offen lds\n\t"
        "v_mfma_f32_16x16x32_bf16 v[48:51], a[160:163], v[68:71], v[48:51]\n\t"
        "ds_read_b128 v[64:67], v108 offset:0\n\t"
        "v_mfma_f32_16x16x32_bf16 v[52:55], v[68:71], a[164:167], v[52:55]\n\t"
        "v_mfma_f32_16x16x32_bf16 v[32:35], a[168:171], v[72:75], v[32:35]\n\t"
        "v_mfma_f32_16x16x32_bf16 v[36:39], a[172:175], v[72:75], v[36:39]\n\t"
        "ds_read_b128 v[68:71], v108 offset:2048\n\t"
        "v_mov_b32_e32 v117, %[cn]\n\t"
        "buffer_store_dword v117, v113, %[sw], 0 offen sc1\n\t"
        "s_add_u32 %[cn], %[cn], 1\n\t"
        "v_mfma_f32_16x16x32_bf16 v[40:43], v[72:75], a[176:179], v[40:43]\n\t"
        "s_add_u32 m0, %[ld], 33792\n\t"
        "s_add_u32 %[t0], %[xo], 16896\n\t"
        "v_mfma_f32_16x16x32_bf16 v[44:47], a[168:171], v[76:79], v[44:47]\n\t"
        "buffer_load_dwordx4 v100, %[sx], %[t0] offen lds\n\t"
        "v_mfma_f32_16x16x32_bf16 v[48:51], a[172:175], v[76:79], v[48:51]\n\t"
        "ds_read_b128 v[72:75], v107 offset:8192\n\t"
        "v_mfma_f32_16x16x32_bf16 v[52:55], v[76:79], a[176:179], v[52:55]\n\t"
        "v_mfma_f32_16x16x32_bf16 v[32:35], a[180:183], v[80:83], v[32:35]\n\t"
        "v_mfma_f32_16x16x32_bf16 v[36:39], a[184:187], v[80:83], v[36:39]\n\t"
        "ds_read_b128 v[76:79], v107 offset:10240\n\t"
        "v_mfma_f32_16x16x32_bf16 v[40:43], v[80:83], a[188:191], v[40:43]\n\t"
        "s_add_u32 m0, %[ld], 40960\n\t"
        "s_add_u32 %[t0], %[xo], 640\n\t"
        "v_mfma_f32_16x16x32_bf16 v[44:47], a[180:183], v[84:87], v[44:47]\n\t"
        "buffer_load_dwordx4 v100, %[sx], %[t0] offen lds\n\t"
        "v_mfma_f32_16x16x32_bf16 v[48:51], a[184:187], v[84:87], v[48:51]\n\t"
        "ds_read_b128 v[80:83], v108 offset:8192\n\t"
        "v_mfma_f32_16x16x32_bf16 v[52:55], v[84:87], a[188:191], v[52:55]\n\t"
        "s_waitcnt lgkmcnt(6)\n\t"
        "v_mfma_f32_16x16x32_bf16 v[32:35], a[192:195], v[56:59], v[32:35]\n\t"
        "v_mfma_f32_16x16x32_bf16 v[36:39], a[196:199], v[56:59], v[36:39]\n\t"
        "ds_read_b128 v[84:87], v108 offset:10240\n\t"
        "v_mfma_f32_16x16x32_bf16 v[40:43], v[56:59], a[200:203], v[40:43]\n\t"
        "s_add_u32 m0, %[ld], 41984\n\t"
        "s_add_u32 %[t0], %[xo], 17024\n\t"
        "s_waitcnt lgkmcnt(6)\n\t"
        "v_mfma_f32_16x16x32_bf16 v[44:47], a[192:195], v[60:63], v[44:47]\n\t"
        "buffer_load_dwordx4 v100, %[sx], %[t0] offen lds\n\t"
        "v_mfma_f32_16x16x32_bf16 v[48:51], a[196:199], v[60:63], v[48:51]\n\t"
        "ds_read_b128 v[56:59], v107 offset:16384\n\t"
        "v_mfma_f32_16x16x32_bf16 v[52:55], v[60:63], a[200:203], v[52:55]\n\t"
        "s_waitcnt lgkmcnt(6)\n\t"
        "v_mfma_f32_16x16x32_bf16 v[32:35], a[204:207], v[64:67], v[32:35]\n\t"
        "v_mfma_f32_16x16x32_bf16 v[36:39], a[208:211], v[64:67], v[36:39]\n\t"
        "ds_read_b128 v[60:63], v107 offset:18432\n\t"
        "v_mfma_f32_16x16x32_bf16 v[40:43], v[64:67], a[212:215], v[40:43]\n\t"
        "s_add_u32 m0, %[ld], 49152\n\t"
        "s_add_u32 %[t0], %[xo], 768\n\t"
        "s_waitcnt lgkmcnt(6)\n\t"
        "v_mfma_f32_16x16x32_bf16 v[44:47], a[204:207], v[68:71], v[44:47]\n\t"
        "buffer_load_dwordx4 v100, %[sx], %[t0] offen lds\n\t"
        "v_mfma_f32_16x16x32_bf16 v[48:51], a[208:211], v[68:71], v[48:51]\n\t"
        "ds_read_b128 v[64:67], v108 offset:16384\n\t"
        "v_mfma_f32_16x16x32_bf16 v[52:55], v[68:71], a[212:215], v[52:55]\n\t"
        "s_waitcnt lgkmcnt(6)\n\t"
        "v_mfma_f32_16x16x32_bf16 v[32:35], a[216:219], v[72:75], v[32:35]\n\t"
        "v_mfma_f32_16x16x32_bf16 v[36:39], a[220:223], v[72:75], v[36:39]\n\t"
        "ds_read_b128 v[68:71], v108 offset:18432\n\t"
        "v_mfma_f32_16x16x32_bf16 v[40:43], v[72:75], a[224:227], v[40:43]\n\t"
        "s_add_u32 m0, %[ld], 50176\n\t"
        "s_add_u32 %[t0], %[xo], 17152\n\t"
        "s_waitcnt lgkmcnt(6)\n\t"
        "v_mfma_f32_16x16x32_bf16 v[44:47], a[216:219], v[76:79], v[44:47]\n\t"
        "buffer_load_dwordx4 v100, %[sx], %[t0] offen lds\n\t"
        "v_mfma_f32_16x16x32_bf16 v[48:51], a[220:223], v[76:79], v[48:51]\n\t"
        "ds_read_b128 v[72:75], v107 offset:24576\n\t"
        "v_mfma_f32_16x16x32_bf16 v[52:55], v[76:79], a[224:227], v[52:55]\n\t"
        "s_waitcnt lgkmcnt(6)\n\t"
        "v_mfma_f32_16x16x32_bf16 v[32:35], a[228:231], v[80:83], v[32:35]\n\t"
        "v_mfma_f32_16x16x32_bf16 v[36:39], a[232:235], v[80:83], v[36:39]\n\t"
        "ds_read_b128 v[76:79], v107 offset:26624\n\t"
        "v_mfma_f32_16x16x32_bf16 v[40:43], v[80:83], a[236:239], v[40:43]\n\t"
        "s_add_u32 m0, %[ld], 57344\n\t"
        "s_add_u32 %[t0], %[xo], 896\n\t"
        "s_waitcnt lgkmcnt(6)\n\t"
        "v_mfma_f32_16x16x32_bf16 v[44:47], a[228:231], v[84:87], v[44:47]\n\t"
        "buffer_load_dwordx4 v100, %[sx], %[t0] offen lds\n\t"
        "v_mfma_f32_16x16x32_bf16 v[48:51], a[232:235], v[84:87], v[48:51]\n\t"
        "ds_read_b128 v[80:83], v108 offset:24576\n\t"
        "v_mfma_f32_16x16x32_bf16 v[52:55], v[84:87], a[236:239], v[52:55]\n\t"
        "s_waitcnt lgkmcnt(6)\n\t"
        "v_mfma_f32_16x16x32_bf16 v[32:35], a[240:243], v[56:59], v[32:35]\n\t"
        "v_mfma_f32_16x16x32_bf16 v[36:39], a[244:247], v[56:59], v[36:39]\n\t"
        "ds_read_b128 v[84:87], v108 offset:26624\n\t"
        "v_mfma_f32_16x16x32_bf16 v[40:43], v[56:59], a[248:251], v[40:43]\n\t"
        "s_add_u32 m0, %[ld], 58368\n\t"
        "s_add_u32 %[t0], %[xo], 17280\n\t"
        "s_waitcnt lgkmcnt(6)\n\t"
        "v_mfma_f32_16x16x32_bf16 v[44:47], a[240:243], v[60:63], v[44:47]\n\t"
        "buffer_load_dwordx4 v100, %[sx], %[t0] offen lds\n\t"
        "v_mfma_f32_16x16x32_bf16 v[48:51], a[244:247], v[60:63], v[48:51]\n\t"
        "v_mfma_f32_16x16x32_bf16 v[52:55], v[60:63], a[248:251], v[52:55]\n\t"
        "s_waitcnt lgkmcnt(5)\n\t"
        "v_mfma_f32_16x16x32_bf16 v[32:35], a[252:255], v[64:67], v[32:35]\n\t"
        "s_waitcnt vmcnt(17) lgkmcnt(0)\n\t"
        "s_barrier\n\t"
        "v_mfma_f32_16x16x32_bf16 v[36:39], v[128:131], v[64:67], v[36:39]\n\t"
        "ds_read_b128 v[56:59], v107 offset:32768\n\t"
        "v_mfma_f32_16x16x32_bf16 v[40:43], v[64:67], v[132:135], v[40:43]\n\t"
        "ds_read_b128 v[60:63], v107 offset:34816\n\t"
        "s_add_u32 m0, %[ld], 65536\n\t"
        "s_add_u32 %[t0], %[xo], 1024\n\t"
        "v_mfma_f32_16x16x32_bf16 v[44:47], a[252:255], v[68:71], v[44:47]\n\t"
        "buffer_load_dwordx4 v100, %[sx], %[t0] offen lds\n\t"
        "v_mfma_f32_16x16x32_bf16 v[48:51], v[128:131], v[68:71], v[48:51]\n\t"
        "ds_read_b128 v[64:67], v108 offset:32768\n\t"
        "v_mfma_f32_16x16x32_bf16 v[52:55], v[68:71], v[132:135], v[52:55]\n\t"
        "v_mfma_f32_16x16x32_bf16 v[32:35], v[136:139], v[72:75], v[32:35]\n\t"
        "v_mfma_f32_16x16x32_bf16 v[36:39], v[140:143], v[72:75], v[36:39]\n\t"
        "ds_read_b128 v[68:71], v108 offset:34816\n\t"
        "v_mfma_f32_16x16x32_bf16 v[40:43], v[72:75], v[144:147], v[40:43]\n\t"
        "s_add_u32 m0, %[ld], 66560\n\t"
        "s_add_u32 %[t0], %[xo], 17408\n\t"
        "v_mfma_f32_16x16x32_bf16 v[44:47], v[136:139], v[76:79], v[44:47]\n\t"
        "buffer_load_dwordx4 v100, %[sx], %[t0] offen lds\n\t"
        "v_mfma_f32_16x16x32_bf16 v[48:51], v[140:143], v[76:79], v[48:51]\n\t"
        "ds_read_b128 v[72:75], v107 offset:40960\n\t"
        "v_mfma_f32_16x16x32_bf16 v[52:55], v[76:79], v[144:147], v[52:55]\n\t"
        "v_mfma_f32_16x16x32_bf16 v[32:35], v[148:151], v[80:83], v[32:35]\n\t"
        "v_mfma_f32_16x16x32_bf16 v[36:39], v[152:155], v[80:83], v[36:39]\n\t"
        "ds_read_b128 v[76:79], v107 offset:43008\n\t"
        "v_mfma_f32_16x16x32_bf16 v[40:43], v[80:83], v[156:159], v[40:43]\n\t"
        "s_add_u32 m0, %[ld], 73728\n\t"
        "s_add_u32 %[t0], %[xo], 1152\n\t"
        "v_mfma_f32_16x16x32_bf16 v[44:47], v[148:151], v[84:87], v[44:47]\n\t"
        "buffer_load_dwordx4 v100, %[sx], %[t0] offen lds\n\t"
        "v_mfma_f32_16x16x32_bf16 v[48:51], v[152:155], v[84:87], v[48:51]\n\t"
        "ds_read_b128 v[80:83], v108 offset:40960\n\t"
        "v_mfma_f32_16x16x32_bf16 v[52:55], v[84:87], v[156:159], v[52:55]\n\t"
        "s_waitcnt lgkmcnt(6)\n\t"
        "v_mfma_f32_16x16x32_bf16 v[32:35], v[160:163], v[56:59], v[32:35]\n\t"
        "v_mfma_f32_16x16x32_bf16 v[36:39], v[164:167], v[56:59], v[36:39]\n\t"
        "ds_read_b128 v[84:87], v108 offset:43008\n\t"
        "v_mfma_f32_16x16x32_bf16 v[40:43], v[56:59], v[168:171], v[40:43]\n\t"
        "s_add_u32 m0, %[ld], 74752\n\t"
        "s_add_u32 %[t0], %[xo], 17536\n\t"
        "s_waitcnt lgkmcnt(6)\n\t"
        "v_mfma_f32_16x16x32_bf16 v[44:47], v[160:163], v[60:63], v[44:47]\n\t"
        "buffer_load_dwordx4 v100, %[sx], %[t0] offen lds\n\t"
        "v_mfma_f32_16x16x32_bf16 v[48:51], v[164:167], v[60:63], v[48:51]\n\t"
        "ds_read_b128 v[56:59], v107 offset:49152\n\t"
        "v_mfma_f32_16x16x32_bf16 v[52:55], v[60:63], v[168:171], v[52:55]\n\t"
        "s_waitcnt lgkmcnt(6)\n\t"
        "v_mfma_f32_16x16x32_bf16 v[32:35], v[172:175], v[64:67], v[32:35]\n\t"
        "v_mfma_f32_16x16x32_bf16 v[36:39], v[176:179], v[64:67], v[36:39]\n\t"
        "ds_read_b128 v[60:63], v107 offset:51200\n\t"
        "v_mfma_f32_16x16x32_bf16 v[40:43], v[64:67], v[180:183], v[40:43]\n\t"
        "s_add_u32 m0, %[ld], 81920\n\t"
        "s_add_u32 %[t0], %[xo], 1280\n\t"
        "s_waitcnt lgkmcnt(6)\n\t"
        "v_mfma_f32_16x16x32_bf16 v[44:47], v[172:175], v[68:71], v[44:47]\n\t"
        "buffer_load_dwordx4 v100, %[sx], %[t0] offen lds\n\t"
        "v_mfma_f32_16x16x32_bf16 v[48:51], v[176:179], v[68:71], v[48:51]\n\t"
        "ds_read_b128 v[64:67], v108 offset:49152\n\t"
        "v_mfma_f32_16x16x32_bf16 v[52:55], v[68:71], v[180:183], v[52:55]\n\t"
        "s_waitcnt lgkmcnt(6)\n\t"
        "v_mfma_f32_16x16x32_bf16 v[32:35], v[184:187], v[72:75], v[32:35]\n\t"
        "v_mfma_f32_16x16x32_bf16 v[36:39], v[188:191], v[72:75], v[36:39]\n\t"
        "ds_read_b128 v[68:71], v108 offset:51200\n\t"
        "v_mfma_f32_16x16x32_bf16 v[40:43], v[72:75], v[192:195], v[40:43]\n\t"
        "s_add_u32 m0, %[ld], 82944\n\t"
        "s_add_u32 %[t0], %[xo], 17664\n\t"
        "s_waitcnt lgkmcnt(6)\n\t"
        "v_mfma_f32_16x16x32_bf16 v[44:47], v[184:187], v[76:79], v[44:47]\n\t"
        "buffer_load_dwordx4 v100, %[sx], %[t0] offen lds\n\t"
        "v_mfma_f32_16x16x32_bf16 v[48:51], v[188:191], v[76:79], v[48:51]\n\t"
        "ds_read_b128 v[72:75], v107 offset:57344\n\t"
        "v_mfma_f32_16x16x32_bf16 v[52:55], v[76:79], v[192:195], v[52:55]\n\t"
        "s_waitcnt lgkmcnt(6)\n\t"
        "v_mfma_f32_16x16x32_bf16 v[32:35], v[196:199], v[80:83], v[32:35]\n\t"
        "v_mfma_f32_16x16x32_bf16 v[36:39], v[200:203], v[80:83], v[36:39]\n\t"
        "ds_read_b128 v[76:79], v107 offset:59392\n\t"
        "v_mfma_f32_16x16x32_bf16 v[40:43], v[80:83], v[204:207], v[40:43]\n\t"
        "s_add_u32 m0, %[ld], 90112\n\t"
        "s_add_u32 %[t0], %[xo], 1408\n\t"
        "s_waitcnt lgkmcnt(6)\n\t"
        "v_mfma_f32_16x16x32_bf16 v[44:47], v[196:199], v[84:87], v[44:47]\n\t"
        "buffer_load_dwordx4 v100, %[sx], %[t0] offen lds\n\t"
        "v_mfma_f32_16x16x32_bf16 v[48:51], v[200:203], v[84:87], v[48:51]\n\t"
        "ds_read_b128 v[80:83], v108 offset:57344\n\t"
        "v_mfma_f32_16x16x32_bf16 v[52:55], v[84:87], v[204:207], v[52:55]\n\t"
        "s_waitcnt lgkmcnt(6)\n\t"
        "v_mfma_f32_16x16x32_bf16 v[32:35], v[208:211], v[56:59], v[32:35]\n\t"
        "v_mfma_f32_16x16x32_bf16 v[36:39], v[212:215], v[56:59], v[36:39]\n\t"
        "ds_read_b128 v[84:87], v108 offset:59392\n\t"
        "v_mfma_f32_16x16x32_bf16 v[40:43], v[56:59], v[216:219], v[40:43]\n\t"
        "s_add_u32 m0, %[ld], 91136\n\t"
        "s_add_u32 %[t0], %[xo], 17792\n\t"
        "s_waitcnt lgkmcnt(6)\n\t"
        "v_mfma_f32_16x16x32_bf16 v[44:47], v[208:211], v[60:63], v[44:47]\n\t"
        "buffer_load_dwordx4 v100, %[sx], %[t0] offen lds\n\t"
        "v_mfma_f32_16x16x32_bf16 v[48:51], v[212:215], v[60:63], v[48:51]\n\t"
        "v_mfma_f32_16x16x32_bf16 v[52:55], v[60:63], v[216:219], v[52:55]\n\t"
        "s_waitcnt lgkmcnt(5)\n\t"
        "v_mfma_f32_16x16x32_bf16 v[32:35], v[220:223], v[64:67], v[32:35]\n\t"
        "s_waitcnt vmcnt(17) lgkmcnt(0)\n\t"
        "s_barrier\n\t"
        "v_mfma_f32_16x16x32_bf16 v[36:39], v[224:227], v[64:67], v[36:39]\n\t"
        "ds_read_b128 v[56:59], v105 offset:0\n\t"
        "v_mfma_f32_16x16x32_bf16 v[40:43], v[64:67], v[228:231], v[40:43]\n\t"
        "ds_read_b128 v[60:63], v105 offset:2048\n\t"
        "s_add_u32 m0, %[ld], 98304\n\t"
        "s_add_u32 %[t0], %[xo], 1536\n\t"
        "v_mfma_f32_16x16x32_bf16 v[44:47], v[220:223], v[68:71], v[44:47]\n\t"
        "buffer_load_dwordx4 v100, %[sx], %[t0] offen lds\n\t"
        "v_mfma_f32_16x16x32_bf16 v[48:51], v[224:227], v[68:71], v[48:51]\n\t"
        "ds_read_b128 v[64:67], v106 offset:0\n\t"
        "v_mfma_f32_16x16x32_bf16 v[52:55], v[68:71], v[228:231], v[52:55]\n\t"
        "v_mfma_f32_16x16x32_bf16 v[32:35], v[232:235], v[72:75], v[32:35]\n\t"
        "v_mfma_f32_16x16x32_bf16 v[36:39], v[236:239], v[72:75], v[36:39]\n\t"
        "ds_read_b128 v[68:71], v106 offset:2048\n\t"
        "v_mfma_f32_16x16x32_bf16 v[40:43], v[72:75], v[240:243], v[40:43]\n\t"
        "s_add_u32 m0, %[ld], 99328\n\t"
        "s_add_u32 %[t0], %[xo], 17920\n\t"
        "v_mfma_f32_16x16x32_bf16 v[44:47], v[232:235], v[76:79], v[44:47]\n\t"
        "buffer_load_dwordx4 v100, %[sx], %[t0] offen lds\n\t"
        "v_mfma_f32_16x16x32_bf16 v[48:51], v[236:239], v[76:79], v[48:51]\n\t"
        "ds_read_b128 v[72:75], v105 offset:8192\n\t"
        "v_mfma_f32_16x16x32_bf16 v[52:55], v[76:79], v[240:243], v[52:55]\n\t"
        "v_mfma_f32_16x16x32_bf16 v[32:35], v[244:247], v[80:83], v[32:35]\n\t"
        "v_mfma_f32_16x16x32_bf16 v[36:39], v[248:251], v[80:83], v[36:39]\n\t"
        "ds_read_b128 v[76:79], v105 offset:10240\n\t"
        "v_mfma_f32_16x16x32_bf16 v[40:43], v[80:83], v[252:255], v[40:43]\n\t"
        "s_add_u32 m0, %[ld], 106496\n\t"
        "s_add_u32 %[t0], %[xo], 1664\n\t"
        "v_mfma_f32_16x16x32_bf16 v[44:47], v[244:247], v[84:87], v[44:47]\n\t"
        "buffer_load_dwordx4 v100, %[sx], %[t0] offen lds\n\t"
        "v_mfma_f32_16x16x32_bf16 v[48:51], v[248:251], v[84:87], v[48:51]\n\t"
        "ds_read_b128 v[80:83], v106 offset:8192\n\t"
        "v_mfma_f32_16x16x32_bf16 v[52:55], v[84:87], v[252:255], v[52:55]\n\t"
        "s_cmp_lt_u32 %[cn], 32\n\t"
        "s_cbranch_scc1 LG_loop%=\n\t"
        "s_nop 7\n\t"
        "v_cvt_pk_bf16_f32 v88, v32, v33\n\t"
        "v_cvt_pk_bf16_f32 v89, v34, v35\n\t"
        "v_cvt_pk_bf16_f32 v90, v36, v37\n\t"
        "v_cvt_pk_bf16_f32 v91, v38, v39\n\t"
        "buffer_store_dwordx4 v[88:91], v109, %[sw], %[so] offen sc1\n\t"
        "v_cvt_pk_bf16_f32 v92, v44, v45\n\t"
        "v_cvt_pk_bf16_f32 v93, v46, v47\n\t"
        "v_cvt_pk_bf16_f32 v94, v48, v49\n\t"
        "v_cvt_pk_bf16_f32 v95, v50, v51\n\t"
        "buffer_store_dwordx4 v[92:95], v110, %[sw], %[so] offen sc1\n\t"
        "v_cvt_pk_bf16_f32 v96, v40, v41\n\t"
        "v_cvt_pk_bf16_f32 v97, v42, v43\n\t"
        "v_cvt_pk_bf16_f32 v98, v52, v53\n\t"
        "v_cvt_pk_bf16_f32 v99, v54, v55\n\t"
        "buffer_store_dwordx4 v[96:99], v111, %[sw], %[so] offen sc1\n\t"
        "s_waitcnt vmcnt(0) lgkmcnt(0)\n\t"
        "v_mov_b32_e32 v117, %[cn]\n\t"
        "buffer_store_dword v117, v113, %[sw], 0 offen sc1\n\t"
        "s_barrier\n\t"
        "LG_end%=:\n\t"
        ".Lk_end_G:\n\t"
        : [t0] "=&s"(t0), [xo] "=&s"(xo), [so] "=&s"(so), [cn] "=&s"(cn), [ts] "=&s"(ts), [ta] "=&s"(ta), [tb] "=&s"(tb), [tc] "=&s"(tc), [td] "=&s"(td)
        : [sx] "s"(sx), [sq] "s"(sq), [sv] "s"(sv), [sw] "s"(sw), [ld] "s"(lb + w * 2048),
          "{v100}"((16 * w + (l >> 3)) * 2048 + (((l & 7) ^ (l >> 3)) << 4)), "{v101}"((8 * w + (l >> 3)) * 8192 + h * 256 + (c & 1) * 128 + (l & 7) * 16),
          "{v112}"((16 * w + (l >> 2)) * 8192 + h * 256 + cv * 64 + (l & 3) * 16), "{v102}"(lb + 131072 + w * 1024 + ((l * 16) ^ ((l & 16) << 2))), "{v116}"(lb + 131072 + w * 1024 + ((l * 16) ^ (l & 32))),
          "{v103}"(lr + rr * 128 + ((tn ^ (n >> 3)) * 64) + (n & 3) * 16), "{v104}"(lr + 8192 + rr * 64 + ((tn ^ (g & 1)) * 32) + (n & 3) * 8), "{v105}"(lx), "{v106}"(lx ^ 64u), "{v107}"(lx + 65536), "{v108}"((lx ^ 64u) + 65536),
          "{v109}"(aq), "{v110}"(aq + 4096), "{v111}"(W_VF + (tm * 32 + h) * 8192 + (2 * cv + tn) * 1024 + l * 16), "{v113}"(l == 0 ? F_KVQ + h * 1024 + (c * 4 + w) * 64 : OOB), "{v[114:115]}"(tp)
        : "memory", "scc", "m0", "v8", "v9", "v10", "v11", "v12", "v13", "v14", "v15", "v16", "v17", "v18", "v19", "v20", "v21", "v22", "v23", "v24", "v25", "v26", "v27", "v28", "v29", "v30", "v31", "v32", "v33", "v34", "v35", "v36", "v37", "v38", "v39", "v40", "v41", "v42", "v43", "v44", "v45", "v46", "v47", "v48", "v49", "v50", "v51", "v52", "v53", "v54", "v55", "v56", "v57", "v58", "v59", "v60", "v61", "v62", "v63", "v64", "v65", "v66", "v67", "v68", "v69", "v70", "v71", "v72", "v73", "v74", "v75", "v76", "v77", "v78", "v79", "v80", "v81", "v82", "v83", "v84", "v85", "v86", "v87", "v88", "v89", "v90", "v91", "v92", "v93", "v94", "v95", "v96", "v97", "v98", "v99", "v117", "v118", "v119", "v120", "v121", "v122", "v123", "v124", "v125", "v126", "v127", "v128", "v129", "v130", "v131", "v132", "v133", "v134", "v135", "v136", "v137", "v138", "v139", "v140", "v141", "v142", "v143", "v144", "v145", "v146", "v147", "v148", "v149", "v150", "v151", "v152", "v153", "v154", "v155", "v156", "v157", "v158", "v159", "v160", "v161", "v162", "v163", "v164", "v165", "v166", "v167", "v168", "v169", "v170", "v171", "v172", "v173", "v174", "v175", "v176", "v177", "v178", "v179", "v180", "v181", "v182", "v183", "v184", "v185", "v186", "v187", "v188", "v189", "v190", "v191", "v192", "v193", "v194", "v195", "v196", "v197", "v198", "v199", "v200", "v201", "v202", "v203", "v204", "v205", "v206", "v207", "v208", "v209", "v210", "v211", "v212", "v213", "v214", "v215", "v216", "v217", "v218", "v219", "v220", "v221", "v222", "v223", "v224", "v225", "v226", "v227", "v228", "v229", "v230", "v231", "v232", "v233", "v234", "v235", "v236", "v237", "v238", "v239", "v240", "v241", "v242", "v243", "v244", "v245", "v246", "v247", "v248", "v249", "v250", "v251", "v252", "v253", "v254", "v255", "a0", "a1", "a2", "a3", "a4", "a5", "a6", "a7", "a8", "a9", "a10", "a11", "a12", "a13", "a14", "a15", "a16", "a17", "a18", "a19", "a20", "a21", "a22", "a23", "a24", "a25", "a26", "a27", "a28", "a29", "a30", "a31", "a32", "a33", "a34", "a35", "a36", "a37", "a38", "a39", "a40", "a41", "a42", "a43", "a44", "a45", "a46", "a47", "a48", "a49", "a50", "a51", "a52", "a53", "a54", "a55", "a56", "a57", "a58", "a59", "a60", "a61", "a62", "a63", "a64", "a65", "a66", "a67", "a68", "a69", "a70", "a71", "a72", "a73", "a74", "a75", "a76", "a77", "a78", "a79", "a80", "a81", "a82", "a83", "a84", "a85", "a86", "a87", "a88", "a89", "a90", "a91", "a92", "a93", "a94", "a95", "a96", "a97", "a98", "a99", "a100", "a101", "a102", "a103", "a104", "a105", "a106", "a107", "a108", "a109", "a110", "a111", "a112", "a113", "a114", "a115", "a116", "a117", "a118", "a119", "a120", "a121", "a122", "a123", "a124", "a125", "a126", "a127", "a128", "a129", "a130", "a131", "a132", "a133", "a134", "a135", "a136", "a137", "a138", "a139", "a140", "a141", "a142", "a143", "a144", "a145", "a146", "a147", "a148", "a149", "a150", "a151", "a152", "a153", "a154", "a155", "a156", "a157", "a158", "a159", "a160", "a161", "a162", "a163", "a164", "a165", "a166", "a167", "a168", "a169", "a170", "a171", "a172", "a173", "a174", "a175", "a176", "a177", "a178", "a179", "a180", "a181", "a182", "a183", "a184", "a185", "a186", "a187", "a188", "a189", "a190", "a191", "a192", "a193", "a194", "a195", "a196", "a197", "a198", "a199", "a200", "a201", "a202", "a203", "a204", "a205", "a206", "a207", "a208", "a209", "a210", "a211", "a212", "a213", "a214", "a215", "a216", "a217", "a218", "a219", "a220", "a221", "a222", "a223", "a224", "a225", "a226", "a227", "a228", "a229", "a230", "a231", "a232", "a233", "a234", "a235", "a236", "a237", "a238", "a239", "a240", "a241", "a242", "a243", "a244", "a245", "a246", "a247", "a248", "a249", "a250", "a251", "a252", "a253", "a254", "a255");
}

__device__ __forceinline__ void out_block(const Params& p, unsigned char* lds, const int wv, const int rp, const int nt, const int j) {
    const int w = wv, l = otid() & 63, n = l & 15, g = l >> 4;
    const unsigned lb = __builtin_amdgcn_readfirstlane((unsigned)(size_t)lds), stg = lb + 131072 + w * 8192;
    const v4u so = mk_srd(p.Wo, 1024 * 4096 * 2), sw = mk_srd(p.ws, W_TOTAL), su = mk_srd(p.out, S * D * 2);
    const unsigned lr = stg + (4 * g + (n >> 2)) * 128 + ((2 * (g & 1) + (n >> 3)) << 5) + (n & 3) * 8;
    unsigned t0, t1, tl = rp, pg, cu, nx, sp, re; unsigned long long tu;
    asm volatile(
        "s_add_u32 %[t1], %[ro], 0\n\t"
        "s_and_b32 %[t1], %[t1], 7\n\t"
        "s_lshl_b32 %[t1], %[t1], 18\n\t"
        "s_add_u32 %[t0], %[t1], 0\n\t"
        "buffer_load_dwordx4 a[0:3], v110, %[so], %[t0] offen\n\t"
        "s_sleep 2\n\t"
        "s_add_u32 %[t0], %[t1], 16384\n\t"
        "buffer_load_dwordx4 a[4:7], v110, %[so], %[t0] offen\n\t"
        "s_sleep 2\n\t"
        "s_add_u32 %[t0], %[t1], 32768\n\t"
        "buffer_load_dwordx4 a[8:11], v110, %[so], %[t0] offen\n\t"
        "s_sleep 2\n\t"
        "s_add_u32 %[t0], %[t1], 49152\n\t"
        "buffer_load_dwordx4 a[12:15], v110, %[so], %[t0] offen\n\t"
        "s_sleep 2\n\t"
        "s_add_u32 %[t0], %[t1], 65536\n\t"
        "buffer_load_dwordx4 a[16:19], v110, %[so], %[t0] offen\n\t"
        "s_sleep 2\n\t"
        "s_add_u32 %[t0], %[t1], 81920\n\t"
        "buffer_load_dwordx4 a[20:23], v110, %[so], %[t0] offen\n\t"
        "s_sleep 2\n\t"
        "s_add_u32 %[t0], %[t1], 98304\n\t"
        "buffer_load_dwordx4 v[24:27], v110, %[so], %[t0] offen\n\t"
        "s_sleep 2\n\t"
        "s_add_u32 %[t0], %[t1], 114688\n\t"
        "buffer_load_dwordx4 v[28:31], v110, %[so], %[t0] offen\n\t"
        "s_sleep 2\n\t"
        "s_add_u32 %[t1], %[ro], 0\n\t"
        "s_and_b32 %[t1], %[t1], 7\n\t"
        "s_lshl_b32 %[t1], %[t1], 18\n\t"
        "s_add_u32 %[t0], %[t1], 131072\n\t"
        "buffer_load_dwordx4 a[24:27], v110, %[so], %[t0] offen\n\t"
        "s_sleep 2\n\t"
        "s_add_u32 %[t0], %[t1], 147456\n\t"
        "buffer_load_dwordx4 a[28:31], v110, %[so], %[t0] offen\n\t"
        "s_sleep 2\n\t"
        "s_add_u32 %[t0], %[t1], 163840\n\t"
        "buffer_load_dwordx4 a[32:35], v110, %[so], %[t0] offen\n\t"
        "s_sleep 2\n\t"
        "s_add_u32 %[t0], %[t1], 180224\n\t"
        "buffer_load_dwordx4 a[36:39], v110, %[so], %[t0] offen\n\t"
        "s_sleep 2\n\t"
        "s_add_u32 %[t0], %[t1], 196608\n\t"
        "buffer_load_dwordx4 a[40:43], v110, %[so], %[t0] offen\n\t"
        "s_sleep 2\n\t"
        "s_add_u32 %[t0], %[t1], 212992\n\t"
        "buffer_load_dwordx4 a[44:47], v110, %[so], %[t0] offen\n\t"
        "s_sleep 2\n\t"
        "s_add_u32 %[t0], %[t1], 229376\n\t"
        "buffer_load_dwordx4 v[32:35], v110, %[so], %[t0] offen\n\t"
        "s_sleep 2\n\t"
        "s_add_u32 %[t0], %[t1], 245760\n\t"
        "buffer_load_dwordx4 v[36:39], v110, %[so], %[t0] offen\n\t"
        "s_sleep 2\n\t"
        "s_waitcnt vmcnt(8)\n\t"
        "ds_write_b128 v111, a[0:3] offset:0\n\t"
        "ds_write_b128 v111, a[4:7] offset:1024\n\t"
        "ds_write_b128 v111, a[8:11] offset:2048\n\t"
        "ds_write_b128 v111, a[12:15] offset:3072\n\t"
        "ds_write_b128 v111, a[16:19] offset:4096\n\t"
        "ds_write_b128 v111, a[20:23] offset:5120\n\t"
        "ds_write_b128 v111, v[24:27] offset:6144\n\t"
        "ds_write_b128 v111, v[28:31] offset:7168\n\t"
        "ds_read_b64_tr_b16 a[0:1], v112 offset:0\n\t"
        "ds_read_b64_tr_b16 a[2:3], v112 offset:2048\n\t"
        "ds_read_b64_tr_b16 a[12:13], v112 offset:4096\n\t"
        "ds_read_b64_tr_b16 a[14:15], v112 offset:6144\n\t"
        "s_waitcnt lgkmcnt(8)\n\t"
        "ds_read_b64_tr_b16 a[4:5], v113 offset:0\n\t"
        "ds_read_b64_tr_b16 a[6:7], v113 offset:2048\n\t"
        "ds_read_b64_tr_b16 a[16:17], v113 offset:4096\n\t"
        "ds_read_b64_tr_b16 a[18:19], v113 offset:6144\n\t"
        "s_waitcnt lgkmcnt(8)\n\t"
        "ds_read_b64_tr_b16 a[8:9], v114 offset:0\n\t"
        "ds_read_b64_tr_b16 a[10:11], v114 offset:2048\n\t"
        "ds_read_b64_tr_b16 a[20:21], v114 offset:4096\n\t"
        "ds_read_b64_tr_b16 a[22:23], v114 offset:6144\n\t"
        "s_waitcnt lgkmcnt(8)\n\t"
        "ds_read_b64_tr_b16 v[56:57], v115 offset:0\n\t"
        "ds_read_b64_tr_b16 v[58:59], v115 offset:2048\n\t"
        "ds_read_b64_tr_b16 v[60:61], v115 offset:4096\n\t"
        "ds_read_b64_tr_b16 v[62:63], v115 offset:6144\n\t"
        "s_add_u32 %[t1], %[ro], 1\n\t"
        "s_and_b32 %[t1], %[t1], 7\n\t"
        "s_lshl_b32 %[t1], %[t1], 18\n\t"
        "s_add_u32 %[t0], %[t1], 0\n\t"
        "buffer_load_dwordx4 a[48:51], v110, %[so], %[t0] offen\n\t"
        "s_sleep 2\n\t"
        "s_add_u32 %[t0], %[t1], 16384\n\t"
        "buffer_load_dwordx4 a[52:55], v110, %[so], %[t0] offen\n\t"
        "s_sleep 2\n\t"
        "s_add_u32 %[t0], %[t1], 32768\n\t"
        "buffer_load_dwordx4 a[56:59], v110, %[so], %[t0] offen\n\t"
        "s_sleep 2\n\t"
        "s_add_u32 %[t0], %[t1], 49152\n\t"
        "buffer_load_dwordx4 a[60:63], v110, %[so], %[t0] offen\n\t"
        "s_sleep 2\n\t"
        "s_add_u32 %[t0], %[t1], 65536\n\t"
        "buffer_load_dwordx4 a[64:67], v110, %[so], %[t0] offen\n\t"
        "s_sleep 2\n\t"
        "s_add_u32 %[t0], %[t1], 81920\n\t"
        "buffer_load_dwordx4 a[68:71], v110, %[so], %[t0] offen\n\t"
        "s_sleep 2\n\t"
        "s_add_u32 %[t0], %[t1], 98304\n\t"
        "buffer_load_dwordx4 v[40:43], v110, %[so], %[t0] offen\n\t"
        "s_sleep 2\n\t"
        "s_add_u32 %[t0], %[t1], 114688\n\t"
        "buffer_load_dwordx4 v[44:47], v110, %[so], %[t0] offen\n\t"
        "s_sleep 2\n\t"
        "s_waitcnt lgkmcnt(0)\n\t"
        "ds_write_b128 v116, v[56:59] offset:0\n\t"
        "ds_write_b128 v116, v[60:63] offset:1024\n\t"
        "s_waitcnt vmcnt(8)\n\t"
        "ds_write_b128 v111, a[24:27] offset:0\n\t"
        "ds_write_b128 v111, a[28:31] offset:1024\n\t"
        "ds_write_b128 v111, a[32:35] offset:2048\n\t"
        "ds_write_b128 v111, a[36:39] offset:3072\n\t"
        "ds_write_b128 v111, a[40:43] offset:4096\n\t"
        "ds_write_b128 v111, a[44:47] offset:5120\n\t"
        "ds_write_b128 v111, v[32:35] offset:6144\n\t"
        "ds_write_b128 v111, v[36:39] offset:7168\n\t"
        "ds_read_b64_tr_b16 a[24:25], v112 offset:0\n\t"
        "ds_read_b64_tr_b16 a[26:27], v112 offset:2048\n\t"
        "ds_read_b64_tr_b16 a[36:37], v112 offset:4096\n\t"
        "ds_read_b64_tr_b16 a[38:39], v112 offset:6144\n\t"
        "s_waitcnt lgkmcnt(8)\n\t"
        "ds_read_b64_tr_b16 a[28:29], v113 offset:0\n\t"
        "ds_read_b64_tr_b16 a[30:31], v113 offset:2048\n\t"
        "ds_read_b64_tr_b16 a[40:41], v113 offset:4096\n\t"
        "ds_read_b64_tr_b16 a[42:43], v113 offset:6144\n\t"
        "s_waitcnt lgkmcnt(8)\n\t"
        "ds_read_b64_tr_b16 a[32:33], v114 offset:0\n\t"
        "ds_read_b64_tr_b16 a[34:35], v114 offset:2048\n\t"
        "ds_read_b64_tr_b16 a[44:45], v114 offset:4096\n\t"
        "ds_read_b64_tr_b16 a[46:47], v114 offset:6144\n\t"
        "s_waitcnt lgkmcnt(8)\n\t"
        "ds_read_b64_tr_b16 v[56:57], v115 offset:0\n\t"
        "ds_read_b64_tr_b16 v[58:59], v115 offset:2048\n\t"
        "ds_read_b64_tr_b16 v[60:61], v115 offset:4096\n\t"
        "ds_read_b64_tr_b16 v[62:63], v115 offset:6144\n\t"
        "s_add_u32 %[t1], %[ro], 1\n\t"
        "s_and_b32 %[t1], %[t1], 7\n\t"
        "s_lshl_b32 %[t1], %[t1], 18\n\t"
        "s_add_u32 %[t0], %[t1], 131072\n\t"
        "buffer_load_dwordx4 a[72:75], v110, %[so], %[t0] offen\n\t"
        "s_sleep 2\n\t"
        "s_add_u32 %[t0], %[t1], 147456\n\t"
        "buffer_load_dwordx4 a[76:79], v110, %[so], %[t0] offen\n\t"
        "s_sleep 2\n\t"
        "s_add_u32 %[t0], %[t1], 163840\n\t"
        "buffer_load_dwordx4 a[80:83], v110, %[so], %[t0] offen\n\t"
        "s_sleep 2\n\t"
        "s_add_u32 %[t0], %[t1], 180224\n\t"
        "buffer_load_dwordx4 a[84:87], v110, %[so], %[t0] offen\n\t"
        "s_sleep 2\n\t"
        "s_add_u32 %[t0], %[t1], 196608\n\t"
        "buffer_load_dwordx4 a[88:91], v110, %[so], %[t0] offen\n\t"
        "s_sleep 2\n\t"
        "s_add_u32 %[t0], %[t1], 212992\n\t"
        "buffer_load_dwordx4 a[92:95], v110, %[so], %[t0] offen\n\t"
        "s_sleep 2\n\t"
        "s_add_u32 %[t0], %[t1], 229376\n\t"
        "buffer_load_dwordx4 v[48:51], v110, %[so], %[t0] offen\n\t"
        "s_sleep 2\n\t"
        "s_add_u32 %[t0], %[t1], 245760\n\t"
        "buffer_load_dwordx4 v[52:55], v110, %[so], %[t0] offen\n\t"
        "s_sleep 2\n\t"
        "s_waitcnt lgkmcnt(0)\n\t"
        "ds_write_b128 v116, v[56:59] offset:2048\n\t"
        "ds_write_b128 v116, v[60:63] offset:3072\n\t"
        "s_waitcnt vmcnt(8)\n\t"
        "ds_write_b128 v111, a[48:51] offset:0\n\t"
        "ds_write_b128 v111, a[52:55] offset:1024\n\t"
        "ds_write_b128 v111, a[56:59] offset:2048\n\t"
        "ds_write_b128 v111, a[60:63] offset:3072\n\t"
        "ds_write_b128 v111, a[64:67] offset:4096\n\t"
        "ds_write_b128 v111, a[68:71] offset:5120\n\t"
        "ds_write_b128 v111, v[40:43] offset:6144\n\t"
        "ds_write_b128 v111, v[44:47] offset:7168\n\t"
        "ds_read_b64_tr_b16 a[48:49], v112 offset:0\n\t"
        "ds_read_b64_tr_b16 a[50:51], v112 offset:2048\n\t"
        "ds_read_b64_tr_b16 a[60:61], v112 offset:4096\n\t"
        "ds_read_b64_tr_b16 a[62:63], v112 offset:6144\n\t"
        "s_waitcnt lgkmcnt(8)\n\t"
        "ds_read_b64_tr_b16 a[52:53], v113 offset:0\n\t"
        "ds_read_b64_tr_b16 a[54:55], v113 offset:2048\n\t"
        "ds_read_b64_tr_b16 a[64:65], v113 offset:4096\n\t"
        "ds_read_b64_tr_b16 a[66:67], v113 offset:6144\n\t"
        "s_waitcnt lgkmcnt(8)\n\t"
        "ds_read_b64_tr_b16 a[56:57], v114 offset:0\n\t"
        "ds_read_b64_tr_b16 a[58:59], v114 offset:2048\n\t"
        "ds_read_b64_tr_b16 a[68:69], v114 offset:4096\n\t"
        "ds_read_b64_tr_b16 a[70:71], v114 offset:6144\n\t"
        "s_waitcnt lgkmcnt(8)\n\t"
        "ds_read_b64_tr_b16 v[56:57], v115 offset:0\n\t"
        "ds_read_b64_tr_b16 v[58:59], v115 offset:2048\n\t"
        "ds_read_b64_tr_b16 v[60:61], v115 offset:4096\n\t"
        "ds_read_b64_tr_b16 v[62:63], v115 offset:6144\n\t"
        "s_add_u32 %[t1], %[ro], 2\n\t"
        "s_and_b32 %[t1], %[t1], 7\n\t"
        "s_lshl_b32 %[t1], %[t1], 18\n\t"
        "s_add_u32 %[t0], %[t1], 0\n\t"
        "buffer_load_dwordx4 a[96:99], v110, %[so], %[t0] offen\n\t"
        "s_sleep 2\n\t"
        "s_add_u32 %[t0], %[t1], 16384\n\t"
        "buffer_load_dwordx4 a[100:103], v110, %[so], %[t0] offen\n\t"
        "s_sleep 2\n\t"
        "s_add_u32 %[t0], %[t1], 32768\n\t"
        "buffer_load_dwordx4 a[104:107], v110, %[so], %[t0] offen\n\t"
        "s_sleep 2\n\t"
        "s_add_u32 %[t0], %[t1], 49152\n\t"
        "buffer_load_dwordx4 a[108:111], v110, %[so], %[t0] offen\n\t"
        "s_sleep 2\n\t"
        "s_add_u32 %[t0], %[t1], 65536\n\t"
        "buffer_load_dwordx4 a[112:115], v110, %[so], %[t0] offen\n\t"
        "s_sleep 2\n\t"
        "s_add_u32 %[t0], %[t1], 81920\n\t"
        "buffer_load_dwordx4 a[116:119], v110, %[so], %[t0] offen\n\t"
        "s_sleep 2\n\t"
        "s_add_u32 %[t0], %[t1], 98304\n\t"
        "buffer_load_dwordx4 v[24:27], v110, %[so], %[t0] offen\n\t"
        "s_sleep 2\n\t"
        "s_add_u32 %[t0], %[t1], 114688\n\t"
        "buffer_load_dwordx4 v[28:31], v110, %[so], %[t0] offen\n\t"
        "s_sleep 2\n\t"
        "s_waitcnt lgkmcnt(0)\n\t"
        "ds_write_b128 v116, v[56:59] offset:4096\n\t"
        "ds_write_b128 v116, v[60:63] offset:5120\n\t"
        "s_waitcnt vmcnt(8)\n\t"
        "ds_write_b128 v111, a[72:75] offset:0\n\t"
        "ds_write_b128 v111, a[76:79] offset:1024\n\t"
        "ds_write_b128 v111, a[80:83] offset:2048\n\t"
        "ds_write_b128 v111, a[84:87] offset:3072\n\t"
        "ds_write_b128 v111, a[88:91] offset:4096\n\t"
        "ds_write_b128 v111, a[92:95] offset:5120\n\t"
        "ds_write_b128 v111, v[48:51] offset:6144\n\t"
        "ds_write_b128 v111, v[52:55] offset:7168\n\t"
        "ds_read_b64_tr_b16 a[72:73], v112 offset:0\n\t"
        "ds_read_b64_tr_b16 a[74:75], v112 offset:2048\n\t"
        "ds_read_b64_tr_b16 a[84:85], v112 offset:4096\n\t"
        "ds_read_b64_tr_b16 a[86:87], v112 offset:6144\n\t"
        "s_waitcnt lgkmcnt(8)\n\t"
        "ds_read_b64_tr_b16 a[76:77], v113 offset:0\n\t"
        "ds_read_b64_tr_b16 a[78:79], v113 offset:2048\n\t"
        "ds_read_b64_tr_b16 a[88:89], v113 offset:4096\n\t"
        "ds_read_b64_tr_b16 a[90:91], v113 offset:6144\n\t"
        "s_waitcnt lgkmcnt(8)\n\t"
        "ds_read_b64_tr_b16 a[80:81], v114 offset:0\n\t"
        "ds_read_b64_tr_b16 a[82:83], v114 offset:2048\n\t"
        "ds_read_b64_tr_b16 a[92:93], v114 offset:4096\n\t"
        "ds_read_b64_tr_b16 a[94:95], v114 offset:6144\n\t"
        "s_waitcnt lgkmcnt(8)\n\t"
        "ds_read_b64_tr_b16 v[56:57], v115 offset:0\n\t"
        "ds_read_b64_tr_b16 v[58:59], v115 offset:2048\n\t"
        "ds_read_b64_tr_b16 v[60:61], v115 offset:4096\n\t"
        "ds_read_b64_tr_b16 v[62:63], v115 offset:6144\n\t"
        "s_add_u32 %[t1], %[ro], 2\n\t"
        "s_and_b32 %[t1], %[t1], 7\n\t"
        "s_lshl_b32 %[t1], %[t1], 18\n\t"
        "s_add_u32 %[t0], %[t1], 131072\n\t"
        "buffer_load_dwordx4 a[120:123], v110, %[so], %[t0] offen\n\t"
        "s_sleep 2\n\t"
        "s_add_u32 %[t0], %[t1], 147456\n\t"
        "buffer_load_dwordx4 a[124:127], v110, %[so], %[t0] offen\n\t"
        "s_sleep 2\n\t"
        "s_add_u32 %[t0], %[t1], 163840\n\t"
        "buffer_load_dwordx4 a[128:131], v110, %[so], %[t0] offen\n\t"
        "s_sleep 2\n\t"
        "s_add_u32 %[t0], %[t1], 180224\n\t"
        "buffer_load_dwordx4 a[132:135], v110, %[so], %[t0] offen\n\t"
        "s_sleep 2\n\t"
        "s_add_u32 %[t0], %[t1], 196608\n\t"
        "buffer_load_dwordx4 a[136:139], v110, %[so], %[t0] offen\n\t"
        "s_sleep 2\n\t"
        "s_add_u32 %[t0], %[t1], 212992\n\t"
        "buffer_load_dwordx4 a[140:143], v110, %[so], %[t0] offen\n\t"
        "s_sleep 2\n\t"
        "s_add_u32 %[t0], %[t1], 229376\n\t"
        "buffer_load_dwordx4 v[32:35], v110, %[so], %[t0] offen\n\t"
        "s_sleep 2\n\t"
        "s_add_u32 %[t0], %[t1], 245760\n\t"
        "buffer_load_dwordx4 v[36:39], v110, %[so], %[t0] offen\n\t"
        "s_sleep 2\n\t"
        "s_waitcnt lgkmcnt(0)\n\t"
        "ds_write_b128 v116, v[56:59] offset:6144\n\t"
        "ds_write_b128 v116, v[60:63] offset:7168\n\t"
        "s_waitcnt vmcnt(8)\n\t"
        "ds_write_b128 v111, a[96:99] offset:0\n\t"
        "ds_write_b128 v111, a[100:103] offset:1024\n\t"
        "ds_write_b128 v111, a[104:107] offset:2048\n\t"
        "ds_write_b128 v111, a[108:111] offset:3072\n\t"
        "ds_write_b128 v111, a[112:115] offset:4096\n\t"
        "ds_write_b128 v111, a[116:119] offset:5120\n\t"
        "ds_write_b128 v111, v[24:27] offset:6144\n\t"
        "ds_write_b128 v111, v[28:31] offset:7168\n\t"
        "ds_read_b64_tr_b16 a[96:97], v112 offset:0\n\t"
        "ds_read_b64_tr_b16 a[98:99], v112 offset:2048\n\t"
        "ds_read_b64_tr_b16 a[108:109], v112 offset:4096\n\t"
        "ds_read_b64_tr_b16 a[110:111], v112 offset:6144\n\t"
        "s_waitcnt lgkmcnt(8)\n\t"
        "ds_read_b64_tr_b16 a[100:101], v113 offset:0\n\t"
        "ds_read_b64_tr_b16 a[102:103], v113 offset:2048\n\t"
        "ds_read_b64_tr_b16 a[112:113], v113 offset:4096\n\t"
        "ds_read_b64_tr_b16 a[114:115], v113 offset:6144\n\t"
        "s_waitcnt lgkmcnt(8)\n\t"
        "ds_read_b64_tr_b16 a[104:105], v114 offset:0\n\t"
        "ds_read_b64_tr_b16 a[106:107], v114 offset:2048\n\t"
        "ds_read_b64_tr_b16 a[116:117], v114 offset:4096\n\t"
        "ds_read_b64_tr_b16 a[118:119], v114 offset:6144\n\t"
        "s_waitcnt lgkmcnt(8)\n\t"
        "ds_read_b64_tr_b16 v[56:57], v115 offset:0\n\t"
        "ds_read_b64_tr_b16 v[58:59], v115 offset:2048\n\t"
        "ds_read_b64_tr_b16 v[60:61], v115 offset:4096\n\t"
        "ds_read_b64_tr_b16 v[62:63], v115 offset:6144\n\t"
        "s_add_u32 %[t1], %[ro], 3\n\t"
        "s_and_b32 %[t1], %[t1], 7\n\t"
        "s_lshl_b32 %[t1], %[t1], 18\n\t"
        "s_add_u32 %[t0], %[t1], 0\n\t"
        "buffer_load_dwordx4 a[144:147], v110, %[so], %[t0] offen\n\t"
        "s_sleep 2\n\t"
        "s_add_u32 %[t0], %[t1], 16384\n\t"
        "buffer_load_dwordx4 a[148:151], v110, %[so], %[t0] offen\n\t"
        "s_sleep 2\n\t"
        "s_add_u32 %[t0], %[t1], 32768\n\t"
        "buffer_load_dwordx4 a[152:155], v110, %[so], %[t0] offen\n\t"
        "s_sleep 2\n\t"
        "s_add_u32 %[t0], %[t1], 49152\n\t"
        "buffer_load_dwordx4 a[156:159], v110, %[so], %[t0] offen\n\t"
        "s_sleep 2\n\t"
        "s_add_u32 %[t0], %[t1], 65536\n\t"
        "buffer_load_dwordx4 a[160:163], v110, %[so], %[t0] offen\n\t"
        "s_sleep 2\n\t"
        "s_add_u32 %[t0], %[t1], 81920\n\t"
        "buffer_load_dwordx4 a[164:167], v110, %[so], %[t0] offen\n\t"
        "s_sleep 2\n\t"
        "s_add_u32 %[t0], %[t1], 98304\n\t"
        "buffer_load_dwordx4 v[40:43], v110, %[so], %[t0] offen\n\t"
        "s_sleep 2\n\t"
        "s_add_u32 %[t0], %[t1], 114688\n\t"
        "buffer_load_dwordx4 v[44:47], v110, %[so], %[t0] offen\n\t"
        "s_sleep 2\n\t"
        "s_waitcnt lgkmcnt(0)\n\t"
        "ds_write_b128 v116, v[56:59] offset:8192\n\t"
        "ds_write_b128 v116, v[60:63] offset:9216\n\t"
        "s_waitcnt vmcnt(8)\n\t"
        "ds_write_b128 v111, a[120:123] offset:0\n\t"
        "ds_write_b128 v111, a[124:127] offset:1024\n\t"
        "ds_write_b128 v111, a[128:131] offset:2048\n\t"
        "ds_write_b128 v111, a[132:135] offset:3072\n\t"
        "ds_write_b128 v111, a[136:139] offset:4096\n\t"
        "ds_write_b128 v111, a[140:143] offset:5120\n\t"
        "ds_write_b128 v111, v[32:35] offset:6144\n\t"
        "ds_write_b128 v111, v[36:39] offset:7168\n\t"
        "ds_read_b64_tr_b16 a[120:121], v112 offset:0\n\t"
        "ds_read_b64_tr_b16 a[122:123], v112 offset:2048\n\t"
        "ds_read_b64_tr_b16 a[132:133], v112 offset:4096\n\t"
        "ds_read_b64_tr_b16 a[134:135], v112 offset:6144\n\t"
        "s_waitcnt lgkmcnt(8)\n\t"
        "ds_read_b64_tr_b16 a[124:125], v113 offset:0\n\t"
        "ds_read_b64_tr_b16 a[126:127], v113 offset:2048\n\t"
        "ds_read_b64_tr_b16 a[136:137], v113 offset:4096\n\t"
        "ds_read_b64_tr_b16 a[138:139], v113 offset:6144\n\t"
        "s_waitcnt lgkmcnt(8)\n\t"
        "ds_read_b64_tr_b16 a[128:129], v114 offset:0\n\t"
        "ds_read_b64_tr_b16 a[130:131], v114 offset:2048\n\t"
        "ds_read_b64_tr_b16 a[140:141], v114 offset:4096\n\t"
        "ds_read_b64_tr_b16 a[142:143], v114 offset:6144\n\t"
        "s_waitcnt lgkmcnt(8)\n\t"
        "ds_read_b64_tr_b16 v[56:57], v115 offset:0\n\t"
        "ds_read_b64_tr_b16 v[58:59], v115 offset:2048\n\t"
        "ds_read_b64_tr_b16 v[60:61], v115 offset:4096\n\t"
        "ds_read_b64_tr_b16 v[62:63], v115 offset:6144\n\t"
        "s_add_u32 %[t1], %[ro], 3\n\t"
        "s_and_b32 %[t1], %[t1], 7\n\t"
        "s_lshl_b32 %[t1], %[t1], 18\n\t"
        "s_add_u32 %[t0], %[t1], 131072\n\t"
        "buffer_load_dwordx4 a[168:171], v110, %[so], %[t0] offen\n\t"
        "s_sleep 2\n\t"
        "s_add_u32 %[t0], %[t1], 147456\n\t"
        "buffer_load_dwordx4 a[172:175], v110, %[so], %[t0] offen\n\t"
        "s_sleep 2\n\t"
        "s_add_u32 %[t0], %[t1], 163840\n\t"
        "buffer_load_dwordx4 a[176:179], v110, %[so], %[t0] offen\n\t"
        "s_sleep 2\n\t"
        "s_add_u32 %[t0], %[t1], 180224\n\t"
        "buffer_load_dwordx4 a[180:183], v110, %[so], %[t0] offen\n\t"
        "s_sleep 2\n\t"
        "s_add_u32 %[t0], %[t1], 196608\n\t"
        "buffer_load_dwordx4 a[184:187], v110, %[so], %[t0] offen\n\t"
        "s_sleep 2\n\t"
        "s_add_u32 %[t0], %[t1], 212992\n\t"
        "buffer_load_dwordx4 a[188:191], v110, %[so], %[t0] offen\n\t"
        "s_sleep 2\n\t"
        "s_add_u32 %[t0], %[t1], 229376\n\t"
        "buffer_load_dwordx4 v[48:51], v110, %[so], %[t0] offen\n\t"
        "s_sleep 2\n\t"
        "s_add_u32 %[t0], %[t1], 245760\n\t"
        "buffer_load_dwordx4 v[52:55], v110, %[so], %[t0] offen\n\t"
        "s_sleep 2\n\t"
        "s_waitcnt lgkmcnt(0)\n\t"
        "ds_write_b128 v116, v[56:59] offset:10240\n\t"
        "ds_write_b128 v116, v[60:63] offset:11264\n\t"
        "s_waitcnt vmcnt(8)\n\t"
        "ds_write_b128 v111, a[144:147] offset:0\n\t"
        "ds_write_b128 v111, a[148:151] offset:1024\n\t"
        "ds_write_b128 v111, a[152:155] offset:2048\n\t"
        "ds_write_b128 v111, a[156:159] offset:3072\n\t"
        "ds_write_b128 v111, a[160:163] offset:4096\n\t"
        "ds_write_b128 v111, a[164:167] offset:5120\n\t"
        "ds_write_b128 v111, v[40:43] offset:6144\n\t"
        "ds_write_b128 v111, v[44:47] offset:7168\n\t"
        "ds_read_b64_tr_b16 a[144:145], v112 offset:0\n\t"
        "ds_read_b64_tr_b16 a[146:147], v112 offset:2048\n\t"
        "ds_read_b64_tr_b16 a[156:157], v112 offset:4096\n\t"
        "ds_read_b64_tr_b16 a[158:159], v112 offset:6144\n\t"
        "s_waitcnt lgkmcnt(8)\n\t"
        "ds_read_b64_tr_b16 a[148:149], v113 offset:0\n\t"
        "ds_read_b64_tr_b16 a[150:151], v113 offset:2048\n\t"
        "ds_read_b64_tr_b16 a[160:161], v113 offset:4096\n\t"
        "ds_read_b64_tr_b16 a[162:163], v113 offset:6144\n\t"
        "s_waitcnt lgkmcnt(8)\n\t"
        "ds_read_b64_tr_b16 a[152:153], v114 offset:0\n\t"
        "ds_read_b64_tr_b16 a[154:155], v114 offset:2048\n\t"
        "ds_read_b64_tr_b16 a[164:165], v114 offset:4096\n\t"
        "ds_read_b64_tr_b16 a[166:167], v114 offset:6144\n\t"
        "s_waitcnt lgkmcnt(8)\n\t"
        "ds_read_b64_tr_b16 v[56:57], v115 offset:0\n\t"
        "ds_read_b64_tr_b16 v[58:59], v115 offset:2048\n\t"
        "ds_read_b64_tr_b16 v[60:61], v115 offset:4096\n\t"
        "ds_read_b64_tr_b16 v[62:63], v115 offset:6144\n\t"
        "s_add_u32 %[t1], %[ro], 4\n\t"
        "s_and_b32 %[t1], %[t1], 7\n\t"
        "s_lshl_b32 %[t1], %[t1], 18\n\t"
        "s_add_u32 %[t0], %[t1], 0\n\t"
        "buffer_load_dwordx4 a[192:195], v110, %[so], %[t0] offen\n\t"
        "s_sleep 2\n\t"
        "s_add_u32 %[t0], %[t1], 16384\n\t"
        "buffer_load_dwordx4 a[196:199], v110, %[so], %[t0] offen\n\t"
        "s_sleep 2\n\t"
        "s_add_u32 %[t0], %[t1], 32768\n\t"
        "buffer_load_dwordx4 a[200:203], v110, %[so], %[t0] offen\n\t"
        "s_sleep 2\n\t"
        "s_add_u32 %[t0], %[t1], 49152\n\t"
        "buffer_load_dwordx4 a[204:207], v110, %[so], %[t0] offen\n\t"
        "s_sleep 2\n\t"
        "s_add_u32 %[t0], %[t1], 65536\n\t"
        "buffer_load_dwordx4 a[208:211], v110, %[so], %[t0] offen\n\t"
        "s_sleep 2\n\t"
        "s_add_u32 %[t0], %[t1], 81920\n\t"
        "buffer_load_dwordx4 a[212:215], v110, %[so], %[t0] offen\n\t"
        "s_sleep 2\n\t"
        "s_add_u32 %[t0], %[t1], 98304\n\t"
        "buffer_load_dwordx4 v[24:27], v110, %[so], %[t0] offen\n\t"
        "s_sleep 2\n\t"
        "s_add_u32 %[t0], %[t1], 114688\n\t"
        "buffer_load_dwordx4 v[28:31], v110, %[so], %[t0] offen\n\t"
        "s_sleep 2\n\t"
        "s_waitcnt lgkmcnt(0)\n\t"
        "ds_write_b128 v116, v[56:59] offset:12288\n\t"
        "ds_write_b128 v116, v[60:63] offset:13312\n\t"
        "s_waitcnt vmcnt(8)\n\t"
        "ds_write_b128 v111, a[168:171] offset:0\n\t"
        "ds_write_b128 v111, a[172:175] offset:1024\n\t"
        "ds_write_b128 v111, a[176:179] offset:2048\n\t"
        "ds_write_b128 v111, a[180:183] offset:3072\n\t"
        "ds_write_b128 v111, a[184:187] offset:4096\n\t"
        "ds_write_b128 v111, a[188:191] offset:5120\n\t"
        "ds_write_b128 v111, v[48:51] offset:6144\n\t"
        "ds_write_b128 v111, v[52:55] offset:7168\n\t"
        "ds_read_b64_tr_b16 a[168:169], v112 offset:0\n\t"
        "ds_read_b64_tr_b16 a[170:171], v112 offset:2048\n\t"
        "ds_read_b64_tr_b16 a[180:181], v112 offset:4096\n\t"
        "ds_read_b64_tr_b16 a[182:183], v112 offset:6144\n\t"
        "s_waitcnt lgkmcnt(8)\n\t"
        "ds_read_b64_tr_b16 a[172:173], v113 offset:0\n\t"
        "ds_read_b64_tr_b16 a[174:175], v113 offset:2048\n\t"
        "ds_read_b64_tr_b16 a[184:185], v113 offset:4096\n\t"
        "ds_read_b64_tr_b16 a[186:187], v113 offset:6144\n\t"
        "s_waitcnt lgkmcnt(8)\n\t"
        "ds_read_b64_tr_b16 a[176:177], v114 offset:0\n\t"
        "ds_read_b64_tr_b16 a[178:179], v114 offset:2048\n\t"
        "ds_read_b64_tr_b16 a[188:189], v114 offset:4096\n\t"
        "ds_read_b64_tr_b16 a[190:191], v114 offset:6144\n\t"
        "s_waitcnt lgkmcnt(8)\n\t"
        "ds_read_b64_tr_b16 v[56:57], v115 offset:0\n\t"
        "ds_read_b64_tr_b16 v[58:59], v115 offset:2048\n\t"
        "ds_read_b64_tr_b16 v[60:61], v115 offset:4096\n\t"
        "ds_read_b64_tr_b16 v[62:63], v115 offset:6144\n\t"
        "s_add_u32 %[t1], %[ro], 4\n\t"
        "s_and_b32 %[t1], %[t1], 7\n\t"
        "s_lshl_b32 %[t1], %[t1], 18\n\t"
        "s_add_u32 %[t0], %[t1], 131072\n\t"
        "buffer_load_dwordx4 a[216:219], v110, %[so], %[t0] offen\n\t"
        "s_sleep 2\n\t"
        "s_add_u32 %[t0], %[t1], 147456\n\t"
        "buffer_load_dwordx4 a[220:223], v110, %[so], %[t0] offen\n\t"
        "s_sleep 2\n\t"
        "s_add_u32 %[t0], %[t1], 163840\n\t"
        "buffer_load_dwordx4 a[224:227], v110, %[so], %[t0] offen\n\t"
        "s_sleep 2\n\t"
        "s_add_u32 %[t0], %[t1], 180224\n\t"
        "buffer_load_dwordx4 a[228:231], v110, %[so], %[t0] offen\n\t"
        "s_sleep 2\n\t"
        "s_add_u32 %[t0], %[t1], 196608\n\t"
        "buffer_load_dwordx4 a[232:235], v110, %[so], %[t0] offen\n\t"
        "s_sleep 2\n\t"
        "s_add_u32 %[t0], %[t1], 212992\n\t"
        "buffer_load_dwordx4 a[236:239], v110, %[so], %[t0] offen\n\t"
        "s_sleep 2\n\t"
        "s_add_u32 %[t0], %[t1], 229376\n\t"
        "buffer_load_dwordx4 v[32:35], v110, %[so], %[t0] offen\n\t"
        "s_sleep 2\n\t"
        "s_add_u32 %[t0], %[t1], 245760\n\t"
        "buffer_load_dwordx4 v[36:39], v110, %[so], %[t0] offen\n\t"
        "s_sleep 2\n\t"
        "s_waitcnt lgkmcnt(0)\n\t"
        "ds_write_b128 v116, v[56:59] offset:14336\n\t"
        "ds_write_b128 v116, v[60:63] offset:15360\n\t"
        "s_waitcnt vmcnt(8)\n\t"
        "ds_write_b128 v111, a[192:195] offset:0\n\t"
        "ds_write_b128 v111, a[196:199] offset:1024\n\t"
        "ds_write_b128 v111, a[200:203] offset:2048\n\t"
        "ds_write_b128 v111, a[204:207] offset:3072\n\t"
        "ds_write_b128 v111, a[208:211] offset:4096\n\t"
        "ds_write_b128 v111, a[212:215] offset:5120\n\t"
        "ds_write_b128 v111, v[24:27] offset:6144\n\t"
        "ds_write_b128 v111, v[28:31] offset:7168\n\t"
        "ds_read_b64_tr_b16 a[192:193], v112 offset:0\n\t"
        "ds_read_b64_tr_b16 a[194:195], v112 offset:2048\n\t"
        "ds_read_b64_tr_b16 a[204:205], v112 offset:4096\n\t"
        "ds_read_b64_tr_b16 a[206:207], v112 offset:6144\n\t"
        "s_waitcnt lgkmcnt(8)\n\t"
        "ds_read_b64_tr_b16 a[196:197], v113 offset:0\n\t"
        "ds_read_b64_tr_b16 a[198:199], v113 offset:2048\n\t"
        "ds_read_b64_tr_b16 a[208:209], v113 offset:4096\n\t"
        "ds_read_b64_tr_b16 a[210:211], v113 offset:6144\n\t"
        "s_waitcnt lgkmcnt(8)\n\t"
        "ds_read_b64_tr_b16 a[200:201], v114 offset:0\n\t"
        "ds_read_b64_tr_b16 a[202:203], v114 offset:2048\n\t"
        "ds_read_b64_tr_b16 a[212:213], v114 offset:4096\n\t"
        "ds_read_b64_tr_b16 a[214:215], v114 offset:6144\n\t"
        "s_waitcnt lgkmcnt(8)\n\t"
        "ds_read_b64_tr_b16 v[56:57], v115 offset:0\n\t"
        "ds_read_b64_tr_b16 v[58:59], v115 offset:2048\n\t"
        "ds_read_b64_tr_b16 v[60:61], v115 offset:4096\n\t"
        "ds_read_b64_tr_b16 v[62:63], v115 offset:6144\n\t"
        "s_add_u32 %[t1], %[ro], 5\n\t"
        "s_and_b32 %[t1], %[t1], 7\n\t"
        "s_lshl_b32 %[t1], %[t1], 18\n\t"
        "s_add_u32 %[t0], %[t1], 0\n\t"
        "buffer_load_dwordx4 a[240:243], v110, %[so], %[t0] offen\n\t"
        "s_sleep 2\n\t"
        "s_add_u32 %[t0], %[t1], 16384\n\t"
        "buffer_load_dwordx4 a[244:247], v110, %[so], %[t0] offen\n\t"
        "s_sleep 2\n\t"
        "s_add_u32 %[t0], %[t1], 32768\n\t"
        "buffer_load_dwordx4 a[248:251], v110, %[so], %[t0] offen\n\t"
        "s_sleep 2\n\t"
        "s_add_u32 %[t0], %[t1], 49152\n\t"
        "buffer_load_dwordx4 a[252:255], v110, %[so], %[t0] offen\n\t"
        "s_sleep 2\n\t"
        "s_add_u32 %[t0], %[t1], 65536\n\t"
        "buffer_load_dwordx4 v[128:131], v110, %[so], %[t0] offen\n\t"
        "s_sleep 2\n\t"
        "s_add_u32 %[t0], %[t1], 81920\n\t"
        "buffer_load_dwordx4 v[132:135], v110, %[so], %[t0] offen\n\t"
        "s_sleep 2\n\t"
        "s_add_u32 %[t0], %[t1], 98304\n\t"
        "buffer_load_dwordx4 v[40:43], v110, %[so], %[t0] offen\n\t"
        "s_sleep 2\n\t"
        "s_add_u32 %[t0], %[t1], 114688\n\t"
        "buffer_load_dwordx4 v[44:47], v110, %[so], %[t0] offen\n\t"
        "s_sleep 2\n\t"
        "s_waitcnt lgkmcnt(0)\n\t"
        "ds_write_b128 v116, v[56:59] offset:16384\n\t"
        "ds_write_b128 v116, v[60:63] offset:17408\n\t"
        "s_waitcnt vmcnt(8)\n\t"
        "ds_write_b128 v111, a[216:219] offset:0\n\t"
        "ds_write_b128 v111, a[220:223] offset:1024\n\t"
        "ds_write_b128 v111, a[224:227] offset:2048\n\t"
        "ds_write_b128 v111, a[228:231] offset:3072\n\t"
        "ds_write_b128 v111, a[232:235] offset:4096\n\t"
        "ds_write_b128 v111, a[236:239] offset:5120\n\t"
        "ds_write_b128 v111, v[32:35] offset:6144\n\t"
        "ds_write_b128 v111, v[36:39] offset:7168\n\t"
        "ds_read_b64_tr_b16 a[216:217], v112 offset:0\n\t"
        "ds_read_b64_tr_b16 a[218:219], v112 offset:2048\n\t"
        "ds_read_b64_tr_b16 a[228:229], v112 offset:4096\n\t"
        "ds_read_b64_tr_b16 a[230:231], v112 offset:6144\n\t"
        "s_waitcnt lgkmcnt(8)\n\t"
        "ds_read_b64_tr_b16 a[220:221], v113 offset:0\n\t"
        "ds_read_b64_tr_b16 a[222:223], v113 offset:2048\n\t"
        "ds_read_b64_tr_b16 a[232:233], v113 offset:4096\n\t"
        "ds_read_b64_tr_b16 a[234:235], v113 offset:6144\n\t"
        "s_waitcnt lgkmcnt(8)\n\t"
        "ds_read_b64_tr_b16 a[224:225], v114 offset:0\n\t"
        "ds_read_b64_tr_b16 a[226:227], v114 offset:2048\n\t"
        "ds_read_b64_tr_b16 a[236:237], v114 offset:4096\n\t"
        "ds_read_b64_tr_b16 a[238:239], v114 offset:6144\n\t"
        "s_waitcnt lgkmcnt(8)\n\t"
        "ds_read_b64_tr_b16 v[56:57], v115 offset:0\n\t"
        "ds_read_b64_tr_b16 v[58:59], v115 offset:2048\n\t"
        "ds_read_b64_tr_b16 v[60:61], v115 offset:4096\n\t"
        "ds_read_b64_tr_b16 v[62:63], v115 offset:6144\n\t"
        "s_add_u32 %[t1], %[ro], 5\n\t"
        "s_and_b32 %[t1], %[t1], 7\n\t"
        "s_lshl_b32 %[t1], %[t1], 18\n\t"
        "s_add_u32 %[t0], %[t1], 131072\n\t"
        "buffer_load_dwordx4 v[136:139], v110, %[so], %[t0] offen\n\t"
        "s_sleep 2\n\t"
        "s_add_u32 %[t0], %[t1], 147456\n\t"
        "buffer_load_dwordx4 v[140:143], v110, %[so], %[t0] offen\n\t"
        "s_sleep 2\n\t"
        "s_add_u32 %[t0], %[t1], 163840\n\t"
        "buffer_load_dwordx4 v[144:147], v110, %[so], %[t0] offen\n\t"
        "s_sleep 2\n\t"
        "s_add_u32 %[t0], %[t1], 180224\n\t"
        "buffer_load_dwordx4 v[148:151], v110, %[so], %[t0] offen\n\t"
        "s_sleep 2\n\t"
        "s_add_u32 %[t0], %[t1], 196608\n\t"
        "buffer_load_dwordx4 v[152:155], v110, %[so], %[t0] offen\n\t"
        "s_sleep 2\n\t"
        "s_add_u32 %[t0], %[t1], 212992\n\t"
        "buffer_load_dwordx4 v[156:159], v110, %[so], %[t0] offen\n\t"
        "s_sleep 2\n\t"
        "s_add_u32 %[t0], %[t1], 229376\n\t"
        "buffer_load_dwordx4 v[48:51], v110, %[so], %[t0] offen\n\t"
        "s_sleep 2\n\t"
        "s_add_u32 %[t0], %[t1], 245760\n\t"
        "buffer_load_dwordx4 v[52:55], v110, %[so], %[t0] offen\n\t"
        "s_sleep 2\n\t"
        "s_waitcnt lgkmcnt(0)\n\t"
        "ds_write_b128 v116, v[56:59] offset:18432\n\t"
        "ds_write_b128 v116, v[60:63] offset:19456\n\t"
        "s_waitcnt vmcnt(8)\n\t"
        "ds_write_b128 v111, a[240:243] offset:0\n\t"
        "ds_write_b128 v111, a[244:247] offset:1024\n\t"
        "ds_write_b128 v111, a[248:251] offset:2048\n\t"
        "ds_write_b128 v111, a[252:255] offset:3072\n\t"
        "ds_write_b128 v111, v[128:131] offset:4096\n\t"
        "ds_write_b128 v111, v[132:135] offset:5120\n\t"
        "ds_write_b128 v111, v[40:43] offset:6144\n\t"
        "ds_write_b128 v111, v[44:47] offset:7168\n\t"
        "ds_read_b64_tr_b16 a[240:241], v112 offset:0\n\t"
        "ds_read_b64_tr_b16 a[242:243], v112 offset:2048\n\t"
        "ds_read_b64_tr_b16 a[252:253], v112 offset:4096\n\t"
        "ds_read_b64_tr_b16 a[254:255], v112 offset:6144\n\t"
        "s_waitcnt lgkmcnt(8)\n\t"
        "ds_read_b64_tr_b16 a[244:245], v113 offset:0\n\t"
        "ds_read_b64_tr_b16 a[246:247], v113 offset:2048\n\t"
        "ds_read_b64_tr_b16 v[128:129], v113 offset:4096\n\t"
        "ds_read_b64_tr_b16 v[130:131], v113 offset:6144\n\t"
        "s_waitcnt lgkmcnt(8)\n\t"
        "ds_read_b64_tr_b16 a[248:249], v114 offset:0\n\t"
        "ds_read_b64_tr_b16 a[250:251], v114 offset:2048\n\t"
        "ds_read_b64_tr_b16 v[132:133], v114 offset:4096\n\t"
        "ds_read_b64_tr_b16 v[134:135], v114 offset:6144\n\t"
        "s_waitcnt lgkmcnt(8)\n\t"
        "ds_read_b64_tr_b16 v[56:57], v115 offset:0\n\t"
        "ds_read_b64_tr_b16 v[58:59], v115 offset:2048\n\t"
        "ds_read_b64_tr_b16 v[60:61], v115 offset:4096\n\t"
        "ds_read_b64_tr_b16 v[62:63], v115 offset:6144\n\t"
        "s_add_u32 %[t1], %[ro], 6\n\t"
        "s_and_b32 %[t1], %[t1], 7\n\t"
        "s_lshl_b32 %[t1], %[t1], 18\n\t"
        "s_add_u32 %[t0], %[t1], 0\n\t"
        "buffer_load_dwordx4 v[160:163], v110, %[so], %[t0] offen\n\t"
        "s_sleep 2\n\t"
        "s_add_u32 %[t0], %[t1], 16384\n\t"
        "buffer_load_dwordx4 v[164:167], v110, %[so], %[t0] offen\n\t"
        "s_sleep 2\n\t"
        "s_add_u32 %[t0], %[t1], 32768\n\t"
        "buffer_load_dwordx4 v[168:171], v110, %[so], %[t0] offen\n\t"
        "s_sleep 2\n\t"
        "s_add_u32 %[t0], %[t1], 49152\n\t"
        "buffer_load_dwordx4 v[172:175], v110, %[so], %[t0] offen\n\t"
        "s_sleep 2\n\t"
        "s_add_u32 %[t0], %[t1], 65536\n\t"
        "buffer_load_dwordx4 v[176:179], v110, %[so], %[t0] offen\n\t"
        "s_sleep 2\n\t"
        "s_add_u32 %[t0], %[t1], 81920\n\t"
        "buffer_load_dwordx4 v[180:183], v110, %[so], %[t0] offen\n\t"
        "s_sleep 2\n\t"
        "s_add_u32 %[t0], %[t1], 98304\n\t"
        "buffer_load_dwordx4 v[24:27], v110, %[so], %[t0] offen\n\t"
        "s_sleep 2\n\t"
        "s_add_u32 %[t0], %[t1], 114688\n\t"
        "buffer_load_dwordx4 v[28:31], v110, %[so], %[t0] offen\n\t"
        "s_sleep 2\n\t"
        "s_waitcnt lgkmcnt(0)\n\t"
        "ds_write_b128 v116, v[56:59] offset:20480\n\t"
        "ds_write_b128 v116, v[60:63] offset:21504\n\t"
        "s_waitcnt vmcnt(8)\n\t"
        "ds_write_b128 v111, v[136:139] offset:0\n\t"
        "ds_write_b128 v111, v[140:143] offset:1024\n\t"
        "ds_write_b128 v111, v[144:147] offset:2048\n\t"
        "ds_write_b128 v111, v[148:151] offset:3072\n\t"
        "ds_write_b128 v111, v[152:155] offset:4096\n\t"
        "ds_write_b128 v111, v[156:159] offset:5120\n\t"
        "ds_write_b128 v111, v[48:51] offset:6144\n\t"
        "ds_write_b128 v111, v[52:55] offset:7168\n\t"
        "ds_read_b64_tr_b16 v[136:137], v112 offset:0\n\t"
        "ds_read_b64_tr_b16 v[138:139], v112 offset:2048\n\t"
        "ds_read_b64_tr_b16 v[148:149], v112 offset:4096\n\t"
        "ds_read_b64_tr_b16 v[150:151], v112 offset:6144\n\t"
        "s_waitcnt lgkmcnt(8)\n\t"
        "ds_read_b64_tr_b16 v[140:141], v113 offset:0\n\t"
        "ds_read_b64_tr_b16 v[142:143], v113 offset:2048\n\t"
        "ds_read_b64_tr_b16 v[152:153], v113 offset:4096\n\t"
        "ds_read_b64_tr_b16 v[154:155], v113 offset:6144\n\t"
        "s_waitcnt lgkmcnt(8)\n\t"
        "ds_read_b64_tr_b16 v[144:145], v114 offset:0\n\t"
        "ds_read_b64_tr_b16 v[146:147], v114 offset:2048\n\t"
        "ds_read_b64_tr_b16 v[156:157], v114 offset:4096\n\t"
        "ds_read_b64_tr_b16 v[158:159], v114 offset:6144\n\t"
        "s_waitcnt lgkmcnt(8)\n\t"
        "ds_read_b64_tr_b16 v[56:57], v115 offset:0\n\t"
        "ds_read_b64_tr_b16 v[58:59], v115 offset:2048\n\t"
        "ds_read_b64_tr_b16 v[60:61], v115 offset:4096\n\t"
        "ds_read_b64_tr_b16 v[62:63], v115 offset:6144\n\t"
        "s_add_u32 %[t1], %[ro], 6\n\t"
        "s_and_b32 %[t1], %[t1], 7\n\t"
        "s_lshl_b32 %[t1], %[t1], 18\n\t"
        "s_add_u32 %[t0], %[t1], 131072\n\t"
        "buffer_load_dwordx4 v[184:187], v110, %[so], %[t0] offen\n\t"
        "s_sleep 2\n\t"
        "s_add_u32 %[t0], %[t1], 147456\n\t"
        "buffer_load_dwordx4 v[188:191], v110, %[so], %[t0] offen\n\t"
        "s_sleep 2\n\t"
        "s_add_u32 %[t0], %[t1], 163840\n\t"
        "buffer_load_dwordx4 v[192:195], v110, %[so], %[t0] offen\n\t"
        "s_sleep 2\n\t"
        "s_add_u32 %[t0], %[t1], 180224\n\t"
        "buffer_load_dwordx4 v[196:199], v110, %[so], %[t0] offen\n\t"
        "s_sleep 2\n\t"
        "s_add_u32 %[t0], %[t1], 196608\n\t"
        "buffer_load_dwordx4 v[200:203], v110, %[so], %[t0] offen\n\t"
        "s_sleep 2\n\t"
        "s_add_u32 %[t0], %[t1], 212992\n\t"
        "buffer_load_dwordx4 v[204:207], v110, %[so], %[t0] offen\n\t"
        "s_sleep 2\n\t"
        "s_add_u32 %[t0], %[t1], 229376\n\t"
        "buffer_load_dwordx4 v[32:35], v110, %[so], %[t0] offen\n\t"
        "s_sleep 2\n\t"
        "s_add_u32 %[t0], %[t1], 245760\n\t"
        "buffer_load_dwordx4 v[36:39], v110, %[so], %[t0] offen\n\t"
        "s_sleep 2\n\t"
        "s_waitcnt lgkmcnt(0)\n\t"
        "ds_write_b128 v116, v[56:59] offset:22528\n\t"
        "ds_write_b128 v116, v[60:63] offset:23552\n\t"
        "s_waitcnt vmcnt(8)\n\t"
        "ds_write_b128 v111, v[160:163] offset:0\n\t"
        "ds_write_b128 v111, v[164:167] offset:1024\n\t"
        "ds_write_b128 v111, v[168:171] offset:2048\n\t"
        "ds_write_b128 v111, v[172:175] offset:3072\n\t"
        "ds_write_b128 v111, v[176:179] offset:4096\n\t"
        "ds_write_b128 v111, v[180:183] offset:5120\n\t"
        "ds_write_b128 v111, v[24:27] offset:6144\n\t"
        "ds_write_b128 v111, v[28:31] offset:7168\n\t"
        "ds_read_b64_tr_b16 v[160:161], v112 offset:0\n\t"
        "ds_read_b64_tr_b16 v[162:163], v112 offset:2048\n\t"
        "ds_read_b64_tr_b16 v[172:173], v112 offset:4096\n\t"
        "ds_read_b64_tr_b16 v[174:175], v112 offset:6144\n\t"
        "s_waitcnt lgkmcnt(8)\n\t"
        "ds_read_b64_tr_b16 v[164:165], v113 offset:0\n\t"
        "ds_read_b64_tr_b16 v[166:167], v113 offset:2048\n\t"
        "ds_read_b64_tr_b16 v[176:177], v113 offset:4096\n\t"
        "ds_read_b64_tr_b16 v[178:179], v113 offset:6144\n\t"
        "s_waitcnt lgkmcnt(8)\n\t"
        "ds_read_b64_tr_b16 v[168:169], v114 offset:0\n\t"
        "ds_read_b64_tr_b16 v[170:171], v114 offset:2048\n\t"
        "ds_read_b64_tr_b16 v[180:181], v114 offset:4096\n\t"
        "ds_read_b64_tr_b16 v[182:183], v114 offset:6144\n\t"
        "s_waitcnt lgkmcnt(8)\n\t"
        "ds_read_b64_tr_b16 v[56:57], v115 offset:0\n\t"
        "ds_read_b64_tr_b16 v[58:59], v115 offset:2048\n\t"
        "ds_read_b64_tr_b16 v[60:61], v115 offset:4096\n\t"
        "ds_read_b64_tr_b16 v[62:63], v115 offset:6144\n\t"
        "s_add_u32 %[t1], %[ro], 7\n\t"
        "s_and_b32 %[t1], %[t1], 7\n\t"
        "s_lshl_b32 %[t1], %[t1], 18\n\t"
        "s_add_u32 %[t0], %[t1], 0\n\t"
        "buffer_load_dwordx4 v[208:211], v110, %[so], %[t0] offen\n\t"
        "s_sleep 2\n\t"
        "s_add_u32 %[t0], %[t1], 16384\n\t"
        "buffer_load_dwordx4 v[212:215], v110, %[so], %[t0] offen\n\t"
        "s_sleep 2\n\t"
        "s_add_u32 %[t0], %[t1], 32768\n\t"
        "buffer_load_dwordx4 v[216:219], v110, %[so], %[t0] offen\n\t"
        "s_sleep 2\n\t"
        "s_add_u32 %[t0], %[t1], 49152\n\t"
        "buffer_load_dwordx4 v[220:223], v110, %[so], %[t0] offen\n\t"
        "s_sleep 2\n\t"
        "s_add_u32 %[t0], %[t1], 65536\n\t"
        "buffer_load_dwordx4 v[224:227], v110, %[so], %[t0] offen\n\t"
        "s_sleep 2\n\t"
        "s_add_u32 %[t0], %[t1], 81920\n\t"
        "buffer_load_dwordx4 v[228:231], v110, %[so], %[t0] offen\n\t"
        "s_sleep 2\n\t"
        "s_add_u32 %[t0], %[t1], 98304\n\t"
        "buffer_load_dwordx4 v[40:43], v110, %[so], %[t0] offen\n\t"
        "s_sleep 2\n\t"
        "s_add_u32 %[t0], %[t1], 114688\n\t"
        "buffer_load_dwordx4 v[44:47], v110, %[so], %[t0] offen\n\t"
        "s_sleep 2\n\t"
        "s_waitcnt lgkmcnt(0)\n\t"
        "ds_write_b128 v116, v[56:59] offset:24576\n\t"
        "ds_write_b128 v116, v[60:63] offset:25600\n\t"
        "s_waitcnt vmcnt(8)\n\t"
        "ds_write_b128 v111, v[184:187] offset:0\n\t"
        "ds_write_b128 v111, v[188:191] offset:1024\n\t"
        "ds_write_b128 v111, v[192:195] offset:2048\n\t"
        "ds_write_b128 v111, v[196:199] offset:3072\n\t"
        "ds_write_b128 v111, v[200:203] offset:4096\n\t"
        "ds_write_b128 v111, v[204:207] offset:5120\n\t"
        "ds_write_b128 v111, v[32:35] offset:6144\n\t"
        "ds_write_b128 v111, v[36:39] offset:7168\n\t"
        "ds_read_b64_tr_b16 v[184:185], v112 offset:0\n\t"
        "ds_read_b64_tr_b16 v[186:187], v112 offset:2048\n\t"
        "ds_read_b64_tr_b16 v[196:197], v112 offset:4096\n\t"
        "ds_read_b64_tr_b16 v[198:199], v112 offset:6144\n\t"
        "s_waitcnt lgkmcnt(8)\n\t"
        "ds_read_b64_tr_b16 v[188:189], v113 offset:0\n\t"
        "ds_read_b64_tr_b16 v[190:191], v113 offset:2048\n\t"
        "ds_read_b64_tr_b16 v[200:201], v113 offset:4096\n\t"
        "ds_read_b64_tr_b16 v[202:203], v113 offset:6144\n\t"
        "s_waitcnt lgkmcnt(8)\n\t"
        "ds_read_b64_tr_b16 v[192:193], v114 offset:0\n\t"
        "ds_read_b64_tr_b16 v[194:195], v114 offset:2048\n\t"
        "ds_read_b64_tr_b16 v[204:205], v114 offset:4096\n\t"
        "ds_read_b64_tr_b16 v[206:207], v114 offset:6144\n\t"
        "s_waitcnt lgkmcnt(8)\n\t"
        "ds_read_b64_tr_b16 v[56:57], v115 offset:0\n\t"
        "ds_read_b64_tr_b16 v[58:59], v115 offset:2048\n\t"
        "ds_read_b64_tr_b16 v[60:61], v115 offset:4096\n\t"
        "ds_read_b64_tr_b16 v[62:63], v115 offset:6144\n\t"
        "s_add_u32 %[t1], %[ro], 7\n\t"
        "s_and_b32 %[t1], %[t1], 7\n\t"
        "s_lshl_b32 %[t1], %[t1], 18\n\t"
        "s_add_u32 %[t0], %[t1], 131072\n\t"
        "buffer_load_dwordx4 v[232:235], v110, %[so], %[t0] offen\n\t"
        "s_sleep 2\n\t"
        "s_add_u32 %[t0], %[t1], 147456\n\t"
        "buffer_load_dwordx4 v[236:239], v110, %[so], %[t0] offen\n\t"
        "s_sleep 2\n\t"
        "s_add_u32 %[t0], %[t1], 163840\n\t"
        "buffer_load_dwordx4 v[240:243], v110, %[so], %[t0] offen\n\t"
        "s_sleep 2\n\t"
        "s_add_u32 %[t0], %[t1], 180224\n\t"
        "buffer_load_dwordx4 v[244:247], v110, %[so], %[t0] offen\n\t"
        "s_sleep 2\n\t"
        "s_add_u32 %[t0], %[t1], 196608\n\t"
        "buffer_load_dwordx4 v[248:251], v110, %[so], %[t0] offen\n\t"
        "s_sleep 2\n\t"
        "s_add_u32 %[t0], %[t1], 212992\n\t"
        "buffer_load_dwordx4 v[252:255], v110, %[so], %[t0] offen\n\t"
        "s_sleep 2\n\t"
        "s_add_u32 %[t0], %[t1], 229376\n\t"
        "buffer_load_dwordx4 v[48:51], v110, %[so], %[t0] offen\n\t"
        "s_sleep 2\n\t"
        "s_add_u32 %[t0], %[t1], 245760\n\t"
        "buffer_load_dwordx4 v[52:55], v110, %[so], %[t0] offen\n\t"
        "s_sleep 2\n\t"
        "s_waitcnt lgkmcnt(0)\n\t"
        "ds_write_b128 v116, v[56:59] offset:26624\n\t"
        "ds_write_b128 v116, v[60:63] offset:27648\n\t"
        "s_waitcnt vmcnt(8)\n\t"
        "ds_write_b128 v111, v[208:211] offset:0\n\t"
        "ds_write_b128 v111, v[212:215] offset:1024\n\t"
        "ds_write_b128 v111, v[216:219] offset:2048\n\t"
        "ds_write_b128 v111, v[220:223] offset:3072\n\t"
        "ds_write_b128 v111, v[224:227] offset:4096\n\t"
        "ds_write_b128 v111, v[228:231] offset:5120\n\t"
        "ds_write_b128 v111, v[40:43] offset:6144\n\t"
        "ds_write_b128 v111, v[44:47] offset:7168\n\t"
        "ds_read_b64_tr_b16 v[208:209], v112 offset:0\n\t"
        "ds_read_b64_tr_b16 v[210:211], v112 offset:2048\n\t"
        "ds_read_b64_tr_b16 v[220:221], v112 offset:4096\n\t"
        "ds_read_b64_tr_b16 v[222:223], v112 offset:6144\n\t"
        "s_waitcnt lgkmcnt(8)\n\t"
        "ds_read_b64_tr_b16 v[212:213], v113 offset:0\n\t"
        "ds_read_b64_tr_b16 v[214:215], v113 offset:2048\n\t"
        "ds_read_b64_tr_b16 v[224:225], v113 offset:4096\n\t"
        "ds_read_b64_tr_b16 v[226:227], v113 offset:6144\n\t"
        "s_waitcnt lgkmcnt(8)\n\t"
        "ds_read_b64_tr_b16 v[216:217], v114 offset:0\n\t"
        "ds_read_b64_tr_b16 v[218:219], v114 offset:2048\n\t"
        "ds_read_b64_tr_b16 v[228:229], v114 offset:4096\n\t"
        "ds_read_b64_tr_b16 v[230:231], v114 offset:6144\n\t"
        "s_waitcnt lgkmcnt(8)\n\t"
        "ds_read_b64_tr_b16 v[56:57], v115 offset:0\n\t"
        "ds_read_b64_tr_b16 v[58:59], v115 offset:2048\n\t"
        "ds_read_b64_tr_b16 v[60:61], v115 offset:4096\n\t"
        "ds_read_b64_tr_b16 v[62:63], v115 offset:6144\n\t"
        "s_waitcnt lgkmcnt(0)\n\t"
        "ds_write_b128 v116, v[56:59] offset:28672\n\t"
        "ds_write_b128 v116, v[60:63] offset:29696\n\t"
        "s_waitcnt vmcnt(0)\n\t"
        "ds_write_b128 v111, v[232:235] offset:0\n\t"
        "ds_write_b128 v111, v[236:239] offset:1024\n\t"
        "ds_write_b128 v111, v[240:243] offset:2048\n\t"
        "ds_write_b128 v111, v[244:247] offset:3072\n\t"
        "ds_write_b128 v111, v[248:251] offset:4096\n\t"
        "ds_write_b128 v111, v[252:255] offset:5120\n\t"
        "ds_write_b128 v111, v[48:51] offset:6144\n\t"
        "ds_write_b128 v111, v[52:55] offset:7168\n\t"
        "ds_read_b64_tr_b16 v[232:233], v112 offset:0\n\t"
        "ds_read_b64_tr_b16 v[234:235], v112 offset:2048\n\t"
        "ds_read_b64_tr_b16 v[244:245], v112 offset:4096\n\t"
        "ds_read_b64_tr_b16 v[246:247], v112 offset:6144\n\t"
        "s_waitcnt lgkmcnt(8)\n\t"
        "ds_read_b64_tr_b16 v[236:237], v113 offset:0\n\t"
        "ds_read_b64_tr_b16 v[238:239], v113 offset:2048\n\t"
        "ds_read_b64_tr_b16 v[248:249], v113 offset:4096\n\t"
        "ds_read_b64_tr_b16 v[250:251], v113 offset:6144\n\t"
        "s_waitcnt lgkmcnt(8)\n\t"
        "ds_read_b64_tr_b16 v[240:241], v114 offset:0\n\t"
        "ds_read_b64_tr_b16 v[242:243], v114 offset:2048\n\t"
        "ds_read_b64_tr_b16 v[252:253], v114 offset:4096\n\t"
        "ds_read_b64_tr_b16 v[254:255], v114 offset:6144\n\t"
        "s_waitcnt lgkmcnt(8)\n\t"
        "ds_read_b64_tr_b16 v[56:57], v115 offset:0\n\t"
        "ds_read_b64_tr_b16 v[58:59], v115 offset:2048\n\t"
        "ds_read_b64_tr_b16 v[60:61], v115 offset:4096\n\t"
        "ds_read_b64_tr_b16 v[62:63], v115 offset:6144\n\t"
        "s_waitcnt lgkmcnt(0)\n\t"
        "ds_write_b128 v116, v[56:59] offset:30720\n\t"
        "ds_write_b128 v116, v[60:63] offset:31744\n\t"
        "ds_read_b128 v[88:91], v116 offset:0\n\t"
        "ds_read_b128 v[92:95], v116 offset:1024\n\t"
        "ds_read_b128 v[96:99], v116 offset:2048\n\t"
        "s_mov_b32 %[sp], 0\n\t"
        "LP_cv%=:\n\t"
        "buffer_load_dword v104, off, %[sw], %[cv] sc1\n\t"
        "s_waitcnt vmcnt(0) lgkmcnt(0)\n\t"
        "v_readfirstlane_b32 %[t0], v104\n\t"
        "s_cmp_ge_u32 %[t0], 8\n\t"
        "s_cbranch_scc1 LP_wait%=\n\t"
        "s_sleep 8\n\t"
        "s_add_u32 %[sp], %[sp], 1\n\t"
        "s_cmp_lt_u32 %[sp], 0x100000\n\t"
        "s_cbranch_scc1 LP_cv%=\n\t"
        "v_mov_b32_e32 v104, 256\n\t"
        "s_mov_b32 %[t0], 33024\n\t"
        "buffer_store_dword v104, off, %[sw], %[t0] sc1\n\t"
        "s_endpgm\n\t"
        "LP_wait%=:\n\t"
        "s_mov_b32 %[re], 0\n\t"
        "LP_again%=:\n\t"
        "s_mov_b32 %[sp], 0\n\t"
        "LP_poll%=:\n\t"
        "buffer_load_dword v103, v118, %[sw], 0 offen sc1\n\t"
        "s_lshr_b32 %[t1], %[tl], 1\n\t"
        "s_waitcnt vmcnt(0)\n\t"
        "v_min_u32_dpp v103, v103, v103 quad_perm:[1,0,3,2] row_mask:0xf bank_mask:0xf\n\t"
        "s_nop 1\n\t"
        "v_min_u32_dpp v103, v103, v103 quad_perm:[2,3,0,1] row_mask:0xf bank_mask:0xf\n\t"
        "s_nop 1\n\t"
        "v_min_u32_dpp v103, v103, v103 row_ror:4 row_mask:0xf bank_mask:0xf\n\t"
        "s_nop 0\n\t"
        "v_readfirstlane_b32 %[pg], v103\n\t"
        "s_cmp_gt_u32 %[pg], %[t1]\n\t"
        "s_cbranch_scc1 LP_have%=\n\t"
        "s_add_u32 %[t0], %[pg], %[re]\n\t"
        "s_cmp_eq_u32 %[t0], 63\n\t"
        "s_cbranch_scc0 LP_no%=\n\t"
        "s_sleep 16\n\t"
        "s_branch LP_have%=\n\t"
        "LP_no%=:\n\t"
        "s_min_u32 %[t1], %[t1], 62\n\t"
        "s_cmp_ge_u32 %[pg], %[t1]\n\t"
        "s_cbranch_scc1 LP_near%=\n\t"
        "s_sleep 24\n\t"
        "LP_near%=:\n\t"
        "s_sleep 2\n\t"
        "s_add_u32 %[sp], %[sp], 1\n\t"
        "s_cmp_lt_u32 %[sp], 0x100000\n\t"
        "s_cbranch_scc1 LP_poll%=\n\t"
        "v_mov_b32_e32 v104, 128\n\t"
        "s_mov_b32 %[t0], 33024\n\t"
        "buffer_store_dword v104, off, %[sw], %[t0] sc1\n\t"
        "s_endpgm\n\t"
        "LP_have%=:\n\t"
        "s_lshl_b32 %[nx], %[tl], 17\n\t"
        "s_add_u32 %[t1], %[t1], 4\n\t"
        "s_cmp_gt_u32 %[pg], %[t1]\n\t"
        "s_cbranch_scc1 LP_run%=\n\t"
        "s_add_u32 %[t0], %[ro], 0\n\t"
        "s_and_b32 %[t0], %[t0], 7\n\t"
        "s_lshl_b32 %[t0], %[t0], 12\n\t"
        "s_add_u32 %[t0], %[t0], %[nx]\n\t"
        "buffer_load_dwordx4 v[24:27], v117, %[sw], %[t0] offen offset:0 sc1\n\t"
        "buffer_load_dwordx4 v[28:31], v117, %[sw], %[t0] offen offset:1024 sc1\n\t"
        "buffer_load_dwordx4 v[32:35], v117, %[sw], %[t0] offen offset:2048 sc1\n\t"
        "buffer_load_dwordx4 v[36:39], v117, %[sw], %[t0] offen offset:3072 sc1\n\t"
        "s_add_u32 %[t0], %[ro], 1\n\t"
        "s_and_b32 %[t0], %[t0], 7\n\t"
        "s_lshl_b32 %[t0], %[t0], 12\n\t"
        "s_add_u32 %[t0], %[t0], %[nx]\n\t"
        "buffer_load_dwordx4 v[40:43], v117, %[sw], %[t0] offen offset:0 sc1\n\t"
        "buffer_load_dwordx4 v[44:47], v117, %[sw], %[t0] offen offset:1024 sc1\n\t"
        "s_waitcnt vmcnt(5) lgkmcnt(2)\n\t"
        "v_mfma_f32_16x16x32_bf16 v[20:23], v[88:91], v[24:27], 0\n\t"
        "ds_read_b128 v[88:91], v116 offset:3072\n\t"
        "v_mfma_f32_16x16x32_bf16 v[8:11], a[0:3], v[24:27], 0\n\t"
        "v_max3_u32 v125, v24, v25, v26\n\t"
        "buffer_load_dwordx4 v[48:51], v117, %[sw], %[t0] offen offset:2048 sc1\n\t"
        "v_mfma_f32_16x16x32_bf16 v[12:15], a[4:7], v[24:27], 0\n\t"
        "v_max_u32_e32 v125, v27, v125\n\t"
        "v_mfma_f32_16x16x32_bf16 v[16:19], a[8:11], v[24:27], 0\n\t"
        "buffer_load_dwordx4 v[52:55], v117, %[sw], %[t0] offen offset:3072 sc1\n\t"
        "s_waitcnt vmcnt(6) lgkmcnt(2)\n\t"
        "v_mfma_f32_16x16x32_bf16 v[20:23], v[92:95], v[28:31], v[20:23]\n\t"
        "ds_read_b128 v[92:95], v116 offset:4096\n\t"
        "v_mfma_f32_16x16x32_bf16 v[8:11], a[12:15], v[28:31], v[8:11]\n\t"
        "v_max3_u32 v125, v28, v29, v125\n\t"
        "s_add_u32 %[t0], %[ro], 2\n\t"
        "s_and_b32 %[t0], %[t0], 7\n\t"
        "s_lshl_b32 %[t0], %[t0], 12\n\t"
        "s_add_u32 %[t0], %[t0], %[nx]\n\t"
        "buffer_load_dwordx4 v[56:59], v117, %[sw], %[t0] offen offset:0 sc1\n\t"
        "v_mfma_f32_16x16x32_bf16 v[12:15], a[16:19], v[28:31], v[12:15]\n\t"
        "v_max3_u32 v125, v30, v31, v125\n\t"
        "v_mfma_f32_16x16x32_bf16 v[16:19], a[20:23], v[28:31], v[16:19]\n\t"
        "buffer_load_dwordx4 v[60:63], v117, %[sw], %[t0] offen offset:1024 sc1\n\t"
        "s_and_b32 %[sp], %[tl], 1\n\t"
        "s_lshl_b32 %[sp], %[sp], 14\n\t"
        "s_lshr_b32 %[t1], %[tl], 1\n\t"
        "s_lshl_b32 %[t1], %[t1], 6\n\t"
        "s_add_u32 %[t1], %[sp], %[t1]\n\t"
        "buffer_load_dword v100, v119, %[sw], %[t1] offen sc1\n\t"
        "s_add_u32 %[t1], %[t1], 32768\n\t"
        "buffer_load_dword v101, v119, %[sw], %[t1] offen sc1\n\t"
        "s_lshl_b32 %[t1], %[tl], 6\n\t"
        "buffer_load_dword v102, v120, %[sw], %[t1] offen sc1\n\t"
        "buffer_load_dword v103, v118, %[sw], 0 offen sc1\n\t"
        "s_waitcnt vmcnt(11) lgkmcnt(2)\n\t"
        "v_mfma_f32_16x16x32_bf16 v[20:23], v[96:99], v[32:35], v[20:23]\n\t"
        "ds_read_b128 v[96:99], v116 offset:5120\n\t"
        "v_mfma_f32_16x16x32_bf16 v[8:11], a[24:27], v[32:35], v[8:11]\n\t"
        "v_max3_u32 v125, v32, v33, v125\n\t"
        "buffer_load_dwordx4 v[64:67], v117, %[sw], %[t0] offen offset:2048 sc1\n\t"
        "v_mfma_f32_16x16x32_bf16 v[12:15], a[28:31], v[32:35], v[12:15]\n\t"
        "v_max3_u32 v125, v34, v35, v125\n\t"
        "v_mfma_f32_16x16x32_bf16 v[16:19], a[32:35], v[32:35], v[16:19]\n\t"
        "buffer_load_dwordx4 v[68:71], v117, %[sw], %[t0] offen offset:3072 sc1\n\t"
        "s_waitcnt vmcnt(12) lgkmcnt(2)\n\t"
        "v_mfma_f32_16x16x32_bf16 v[20:23], v[88:91], v[36:39], v[20:23]\n\t"
        "ds_read_b128 v[88:91], v116 offset:6144\n\t"
        "v_mfma_f32_16x16x32_bf16 v[8:11], a[36:39], v[36:39], v[8:11]\n\t"
        "v_max3_u32 v125, v36, v37, v125\n\t"
        "s_add_u32 %[t0], %[ro], 3\n\t"
        "s_and_b32 %[t0], %[t0], 7\n\t"
        "s_lshl_b32 %[t0], %[t0], 12\n\t"
        "s_add_u32 %[t0], %[t0], %[nx]\n\t"
        "buffer_load_dwordx4 v[72:75], v117, %[sw], %[t0] offen offset:0 sc1\n\t"
        "v_mfma_f32_16x16x32_bf16 v[12:15], a[40:43], v[36:39], v[12:15]\n\t"
        "v_max3_u32 v125, v38, v39, v125\n\t"
        "v_mfma_f32_16x16x32_bf16 v[16:19], a[44:47], v[36:39], v[16:19]\n\t"
        "buffer_load_dwordx4 v[76:79], v117, %[sw], %[t0] offen offset:1024 sc1\n\t"
        "s_waitcnt vmcnt(13) lgkmcnt(2)\n\t"
        "v_mfma_f32_16x16x32_bf16 v[20:23], v[92:95], v[40:43], v[20:23]\n\t"
        "ds_read_b128 v[92:95], v116 offset:7168\n\t"
        "v_mfma_f32_16x16x32_bf16 v[8:11], a[48:51], v[40:43], v[8:11]\n\t"
        "v_max3_u32 v125, v40, v41, v125\n\t"
        "buffer_load_dwordx4 v[80:83], v117, %[sw], %[t0] offen offset:2048 sc1\n\t"
        "v_mfma_f32_16x16x32_bf16 v[12:15], a[52:55], v[40:43], v[12:15]\n\t"
        "v_max3_u32 v125, v42, v43, v125\n\t"
        "v_mfma_f32_16x16x32_bf16 v[16:19], a[56:59], v[40:43], v[16:19]\n\t"
        "buffer_load_dwordx4 v[84:87], v117, %[sw], %[t0] offen offset:3072 sc1\n\t"
        "s_waitcnt vmcnt(14) lgkmcnt(2)\n\t"
        "v_mfma_f32_16x16x32_bf16 v[20:23], v[96:99], v[44:47], v[20:23]\n\t"
        "ds_read_b128 v[96:99], v116 offset:8192\n\t"
        "v_mfma_f32_16x16x32_bf16 v[8:11], a[60:63], v[44:47], v[8:11]\n\t"
        "v_max3_u32 v125, v44, v45, v125\n\t"
        "s_add_u32 %[t0], %[ro], 4\n\t"
        "s_and_b32 %[t0], %[t0], 7\n\t"
        "s_lshl_b32 %[t0], %[t0], 12\n\t"
        "s_add_u32 %[t0], %[t0], %[nx]\n\t"
        "buffer_load_dwordx4 v[24:27], v117, %[sw], %[t0] offen offset:0 sc1\n\t"
        "v_mfma_f32_16x16x32_bf16 v[12:15], a[64:67], v[44:47], v[12:15]\n\t"
        "v_max3_u32 v125, v46, v47, v125\n\t"
        "v_mfma_f32_16x16x32_bf16 v[16:19], a[68:71], v[44:47], v[16:19]\n\t"
        "buffer_load_dwordx4 v[28:31], v117, %[sw], %[t0] offen offset:1024 sc1\n\t"
        "s_waitcnt vmcnt(15) lgkmcnt(2)\n\t"
        "v_mfma_f32_16x16x32_bf16 v[20:23], v[88:91], v[48:51], v[20:23]\n\t"
        "ds_read_b128 v[88:91], v116 offset:9216\n\t"
        "v_mfma_f32_16x16x32_bf16 v[8:11], a[72:75], v[48:51], v[8:11]\n\t"
        "v_max3_u32 v125, v48, v49, v125\n\t"
        "v_mfma_f32_16x16x32_bf16 v[12:15], a[76:79], v[48:51], v[12:15]\n\t"
        "v_max3_u32 v125, v50, v51, v125\n\t"
        "v_mfma_f32_16x16x32_bf16 v[16:19], a[80:83], v[48:51], v[16:19]\n\t"
        "buffer_load_dwordx4 v[32:35], v117, %[sw], %[t0] offen offset:2048 sc1\n\t"
        "s_waitcnt vmcnt(15) lgkmcnt(2)\n\t"
        "v_mfma_f32_16x16x32_bf16 v[20:23], v[92:95], v[52:55], v[20:23]\n\t"
        "ds_read_b128 v[92:95], v116 offset:10240\n\t"
        "v_mfma_f32_16x16x32_bf16 v[8:11], a[84:87], v[52:55], v[8:11]\n\t"
        "v_max3_u32 v125, v52, v53, v125\n\t"
        "v_mfma_f32_16x16x32_bf16 v[12:15], a[88:91], v[52:55], v[12:15]\n\t"
        "v_max3_u32 v125, v54, v55, v125\n\t"
        "v_mfma_f32_16x16x32_bf16 v[16:19], a[92:95], v[52:55], v[16:19]\n\t"
        "buffer_load_dwordx4 v[36:39], v117, %[sw], %[t0] offen offset:3072 sc1\n\t"
        "s_waitcnt vmcnt(15) lgkmcnt(2)\n\t"
        "s_cmp_lg_u32 %[re], 0\n\t"
        "s_cbranch_scc1 LP_nb%=\n\t"
        "s_barrier\n\t"
        "LP_nb%=:\n\t"
        "v_mfma_f32_16x16x32_bf16 v[20:23], v[96:99], v[56:59], v[20:23]\n\t"
        "ds_read_b128 v[96:99], v116 offset:11264\n\t"
        "v_mfma_f32_16x16x32_bf16 v[8:11], a[96:99], v[56:59], v[8:11]\n\t"
        "v_max3_u32 v125, v56, v57, v125\n\t"
        "v_mfma_f32_16x16x32_bf16 v[12:15], a[100:103], v[56:59], v[12:15]\n\t"
        "v_max3_u32 v125, v58, v59, v125\n\t"
        "v_mfma_f32_16x16x32_bf16 v[16:19], a[104:107], v[56:59], v[16:19]\n\t"
        "s_add_u32 %[t0], %[ro], 5\n\t"
        "s_and_b32 %[t0], %[t0], 7\n\t"
        "s_lshl_b32 %[t0], %[t0], 12\n\t"
        "s_add_u32 %[t0], %[t0], %[nx]\n\t"
        "buffer_load_dwordx4 v[40:43], v117, %[sw], %[t0] offen offset:0 sc1\n\t"
        "s_waitcnt vmcnt(15) lgkmcnt(2)\n\t"
        "v_mfma_f32_16x16x32_bf16 v[20:23], v[88:91], v[60:63], v[20:23]\n\t"
        "ds_read_b128 v[88:91], v116 offset:12288\n\t"
        "v_mfma_f32_16x16x32_bf16 v[8:11], a[108:111], v[60:63], v[8:11]\n\t"
        "v_max3_u32 v125, v60, v61, v125\n\t"
        "v_mfma_f32_16x16x32_bf16 v[12:15], a[112:115], v[60:63], v[12:15]\n\t"
        "v_max3_u32 v125, v62, v63, v125\n\t"
        "v_mfma_f32_16x16x32_bf16 v[16:19], a[116:119], v[60:63], v[16:19]\n\t"
        "buffer_load_dwordx4 v[44:47], v117, %[sw], %[t0] offen offset:1024 sc1\n\t"
        "s_waitcnt vmcnt(11) lgkmcnt(2)\n\t"
        "v_mfma_f32_16x16x32_bf16 v[20:23], v[92:95], v[64:67], v[20:23]\n\t"
        "ds_read_b128 v[92:95], v116 offset:13312\n\t"
        "v_mfma_f32_16x16x32_bf16 v[8:11], a[120:123], v[64:67], v[8:11]\n\t"
        "v_max3_u32 v125, v64, v65, v125\n\t"
        "v_mfma_f32_16x16x32_bf16 v[12:15], a[124:127], v[64:67], v[12:15]\n\t"
        "v_max3_u32 v125, v66, v67, v125\n\t"
        "v_mfma_f32_16x16x32_bf16 v[16:19], a[128:131], v[64:67], v[16:19]\n\t"
        "buffer_load_dwordx4 v[48:51], v117, %[sw], %[t0] offen offset:2048 sc1\n\t"
        "s_waitcnt vmcnt(11) lgkmcnt(2)\n\t"
        "v_mfma_f32_16x16x32_bf16 v[20:23], v[96:99], v[68:71], v[20:23]\n\t"
        "ds_read_b128 v[96:99], v116 offset:14336\n\t"
        "v_mfma_f32_16x16x32_bf16 v[8:11], a[132:135], v[68:71], v[8:11]\n\t"
        "v_max3_u32 v125, v68, v69, v125\n\t"
        "v_mfma_f32_16x16x32_bf16 v[12:15], a[136:139], v[68:71], v[12:15]\n\t"
        "v_max3_u32 v125, v70, v71, v125\n\t"
        "v_mfma_f32_16x16x32_bf16 v[16:19], a[140:143], v[68:71], v[16:19]\n\t"
        "buffer_load_dwordx4 v[52:55], v117, %[sw], %[t0] offen offset:3072 sc1\n\t"
        "s_waitcnt vmcnt(11) lgkmcnt(2)\n\t"
        "v_mfma_f32_16x16x32_bf16 v[20:23], v[88:91], v[72:75], v[20:23]\n\t"
        "ds_read_b128 v[88:91], v116 offset:15360\n\t"
        "v_mfma_f32_16x16x32_bf16 v[8:11], a[144:147], v[72:75], v[8:11]\n\t"
        "v_max3_u32 v125, v72, v73, v125\n\t"
        "v_mfma_f32_16x16x32_bf16 v[12:15], a[148:151], v[72:75], v[12:15]\n\t"
        "v_max3_u32 v125, v74, v75, v125\n\t"
        "v_mfma_f32_16x16x32_bf16 v[16:19], a[152:155], v[72:75], v[16:19]\n\t"
        "s_add_u32 %[t0], %[ro], 6\n\t"
        "s_and_b32 %[t0], %[t0], 7\n\t"
        "s_lshl_b32 %[t0], %[t0], 12\n\t"
        "s_add_u32 %[t0], %[t0], %[nx]\n\t"
        "buffer_load_dwordx4 v[56:59], v117, %[sw], %[t0] offen offset:0 sc1\n\t"
        "s_waitcnt vmcnt(11) lgkmcnt(2)\n\t"
        "v_mfma_f32_16x16x32_bf16 v[20:23], v[92:95], v[76:79], v[20:23]\n\t"
        "ds_read_b128 v[92:95], v116 offset:16384\n\t"
        "v_mfma_f32_16x16x32_bf16 v[8:11], a[156:159], v[76:79], v[8:11]\n\t"
        "v_max3_u32 v125, v76, v77, v125\n\t"
        "v_mfma_f32_16x16x32_bf16 v[12:15], a[160:163], v[76:79], v[12:15]\n\t"
        "v_max3_u32 v125, v78, v79, v125\n\t"
        "v_mfma_f32_16x16x32_bf16 v[16:19], a[164:167], v[76:79], v[16:19]\n\t"
        "buffer_load_dwordx4 v[60:63], v117, %[sw], %[t0] offen offset:1024 sc1\n\t"
        "s_waitcnt vmcnt(11) lgkmcnt(2)\n\t"
        "v_mfma_f32_16x16x32_bf16 v[20:23], v[96:99], v[80:83], v[20:23]\n\t"
        "ds_read_b128 v[96:99], v116 offset:17408\n\t"
        "v_mfma_f32_16x16x32_bf16 v[8:11], a[168:171], v[80:83], v[8:11]\n\t"
        "v_max3_u32 v125, v80, v81, v125\n\t"
        "v_mfma_f32_16x16x32_bf16 v[12:15], a[172:175], v[80:83], v[12:15]\n\t"
        "v_max3_u32 v125, v82, v83, v125\n\t"
        "v_mfma_f32_16x16x32_bf16 v[16:19], a[176:179], v[80:83], v[16:19]\n\t"
        "buffer_load_dwordx4 v[64:67], v117, %[sw], %[t0] offen offset:2048 sc1\n\t"
        "s_waitcnt vmcnt(11) lgkmcnt(2)\n\t"
        "v_mfma_f32_16x16x32_bf16 v[20:23], v[88:91], v[84:87], v[20:23]\n\t"
        "ds_read_b128 v[88:91], v116 offset:18432\n\t"
        "v_mfma_f32_16x16x32_bf16 v[8:11], a[180:183], v[84:87], v[8:11]\n\t"
        "v_max3_u32 v125, v84, v85, v125\n\t"
        "v_mfma_f32_16x16x32_bf16 v[12:15], a[184:187], v[84:87], v[12:15]\n\t"
        "v_max3_u32 v125, v86, v87, v125\n\t"
        "v_mfma_f32_16x16x32_bf16 v[16:19], a[188:191], v[84:87], v[16:19]\n\t"
        "buffer_load_dwordx4 v[68:71], v117, %[sw], %[t0] offen offset:3072 sc1\n\t"
        "s_waitcnt vmcnt(11) lgkmcnt(2)\n\t"
        "v_mfma_f32_16x16x32_bf16 v[20:23], v[92:95], v[24:27], v[20:23]\n\t"
        "ds_read_b128 v[92:95], v116 offset:19456\n\t"
        "v_mfma_f32_16x16x32_bf16 v[8:11], a[192:195], v[24:27], v[8:11]\n\t"
        "v_max3_u32 v125, v24, v25, v125\n\t"
        "v_mfma_f32_16x16x32_bf16 v[12:15], a[196:199], v[24:27], v[12:15]\n\t"
        "v_max3_u32 v125, v26, v27, v125\n\t"
        "v_mfma_f32_16x16x32_bf16 v[16:19], a[200:203], v[24:27], v[16:19]\n\t"
        "s_add_u32 %[t0], %[ro], 7\n\t"
        "s_and_b32 %[t0], %[t0], 7\n\t"
        "s_lshl_b32 %[t0], %[t0], 12\n\t"
        "s_add_u32 %[t0], %[t0], %[nx]\n\t"
        "buffer_load_dwordx4 v[72:75], v117, %[sw], %[t0] offen offset:0 sc1\n\t"
        "s_waitcnt vmcnt(11) lgkmcnt(2)\n\t"
        "v_mfma_f32_16x16x32_bf16 v[20:23], v[96:99], v[28:31], v[20:23]\n\t"
        "ds_read_b128 v[96:99], v116 offset:20480\n\t"
        "v_mfma_f32_16x16x32_bf16 v[8:11], a[204:207], v[28:31], v[8:11]\n\t"
        "v_max3_u32 v125, v28, v29, v125\n\t"
        "v_mfma_f32_16x16x32_bf16 v[12:15], a[208:211], v[28:31], v[12:15]\n\t"
        "v_max3_u32 v125, v30, v31, v125\n\t"
        "v_sub_f32_e32 v104, 1.0, v102\n\t"
        "v_mfma_f32_16x16x32_bf16 v[16:19], a[212:215], v[28:31], v[16:19]\n\t"
        "buffer_load_dwordx4 v[76:79], v117, %[sw], %[t0] offen offset:1024 sc1\n\t"
        "s_waitcnt vmcnt(11) lgkmcnt(2)\n\t"
        "v_mfma_f32_16x16x32_bf16 v[20:23], v[88:91], v[32:35], v[20:23]\n\t"
        "ds_read_b128 v[88:91], v116 offset:21504\n\t"
        "v_mfma_f32_16x16x32_bf16 v[8:11], a[216:219], v[32:35], v[8:11]\n\t"
        "v_max3_u32 v125, v32, v33, v125\n\t"
        "v_mfma_f32_16x16x32_bf16 v[12:15], a[220:223], v[32:35], v[12:15]\n\t"
        "v_max3_u32 v125, v34, v35, v125\n\t"
        "v_cvt_pk_bf16_f32 v104, 0, v104\n\t"
        "v_mfma_f32_16x16x32_bf16 v[16:19], a[224:227], v[32:35], v[16:19]\n\t"
        "buffer_load_dwordx4 v[80:83], v117, %[sw], %[t0] offen offset:2048 sc1\n\t"
        "s_waitcnt vmcnt(11) lgkmcnt(2)\n\t"
        "v_mfma_f32_16x16x32_bf16 v[20:23], v[92:95], v[36:39], v[20:23]\n\t"
        "ds_read_b128 v[92:95], v116 offset:22528\n\t"
        "v_mfma_f32_16x16x32_bf16 v[8:11], a[228:231], v[36:39], v[8:11]\n\t"
        "v_max3_u32 v125, v36, v37, v125\n\t"
        "v_mfma_f32_16x16x32_bf16 v[12:15], a[232:235], v[36:39], v[12:15]\n\t"
        "v_max3_u32 v125, v38, v39, v125\n\t"
        "v_lshlrev_b32_e32 v106, 16, v100\n\t"
        "v_mfma_f32_16x16x32_bf16 v[16:19], a[236:239], v[36:39], v[16:19]\n\t"
        "buffer_load_dwordx4 v[84:87], v117, %[sw], %[t0] offen offset:3072 sc1\n\t"
        "s_waitcnt vmcnt(11) lgkmcnt(2)\n\t"
        "v_mfma_f32_16x16x32_bf16 v[20:23], v[96:99], v[40:43], v[20:23]\n\t"
        "ds_read_b128 v[96:99], v116 offset:23552\n\t"
        "v_mfma_f32_16x16x32_bf16 v[8:11], a[240:243], v[40:43], v[8:11]\n\t"
        "v_max3_u32 v125, v40, v41, v125\n\t"
        "v_mfma_f32_16x16x32_bf16 v[12:15], a[244:247], v[40:43], v[12:15]\n\t"
        "v_max3_u32 v125, v42, v43, v125\n\t"
        "v_mul_f32_e32 v106, v102, v106\n\t"
        "v_mfma_f32_16x16x32_bf16 v[16:19], a[248:251], v[40:43], v[16:19]\n\t"
        "s_waitcnt vmcnt(10) lgkmcnt(2)\n\t"
        "v_mfma_f32_16x16x32_bf16 v[20:23], v[88:91], v[44:47], v[20:23]\n\t"
        "ds_read_b128 v[88:91], v116 offset:24576\n\t"
        "v_mfma_f32_16x16x32_bf16 v[8:11], a[252:255], v[44:47], v[8:11]\n\t"
        "v_max3_u32 v125, v44, v45, v125\n\t"
        "v_mfma_f32_16x16x32_bf16 v[12:15], v[128:131], v[44:47], v[12:15]\n\t"
        "v_max3_u32 v125, v46, v47, v125\n\t"
        "v_cvt_pk_bf16_f32 v106, 0, v106\n\t"
        "v_mfma_f32_16x16x32_bf16 v[16:19], v[132:135], v[44:47], v[16:19]\n\t"
        "s_waitcnt vmcnt(9) lgkmcnt(2)\n\t"
        "v_mfma_f32_16x16x32_bf16 v[20:23], v[92:95], v[48:51], v[20:23]\n\t"
        "ds_read_b128 v[92:95], v116 offset:25600\n\t"
        "v_mfma_f32_16x16x32_bf16 v[8:11], v[136:139], v[48:51], v[8:11]\n\t"
        "v_max3_u32 v125, v48, v49, v125\n\t"
        "v_mfma_f32_16x16x32_bf16 v[12:15], v[140:143], v[48:51], v[12:15]\n\t"
        "v_max3_u32 v125, v50, v51, v125\n\t"
        "v_and_b32_e32 v107, 0xffff0000, v100\n\t"
        "v_mfma_f32_16x16x32_bf16 v[16:19], v[144:147], v[48:51], v[16:19]\n\t"
        "s_waitcnt vmcnt(8) lgkmcnt(2)\n\t"
        "v_mfma_f32_16x16x32_bf16 v[20:23], v[96:99], v[52:55], v[20:23]\n\t"
        "ds_read_b128 v[96:99], v116 offset:26624\n\t"
        "v_mfma_f32_16x16x32_bf16 v[8:11], v[148:151], v[52:55], v[8:11]\n\t"
        "v_max3_u32 v125, v52, v53, v125\n\t"
        "v_mfma_f32_16x16x32_bf16 v[12:15], v[152:155], v[52:55], v[12:15]\n\t"
        "v_max3_u32 v125, v54, v55, v125\n\t"
        "v_mul_f32_e32 v107, v102, v107\n\t"
        "v_mfma_f32_16x16x32_bf16 v[16:19], v[156:159], v[52:55], v[16:19]\n\t"
        "s_waitcnt vmcnt(7) lgkmcnt(2)\n\t"
        "v_mfma_f32_16x16x32_bf16 v[20:23], v[88:91], v[56:59], v[20:23]\n\t"
        "ds_read_b128 v[88:91], v116 offset:27648\n\t"
        "v_mfma_f32_16x16x32_bf16 v[8:11], v[160:163], v[56:59], v[8:11]\n\t"
        "v_max3_u32 v125, v56, v57, v125\n\t"
        "v_mfma_f32_16x16x32_bf16 v[12:15], v[164:167], v[56:59], v[12:15]\n\t"
        "v_max3_u32 v125, v58, v59, v125\n\t"
        "v_cvt_pk_bf16_f32 v107, 0, v107\n\t"
        "v_mfma_f32_16x16x32_bf16 v[16:19], v[168:171], v[56:59], v[16:19]\n\t"
        "s_waitcnt vmcnt(6) lgkmcnt(2)\n\t"
        "v_mfma_f32_16x16x32_bf16 v[20:23], v[92:95], v[60:63], v[20:23]\n\t"
        "ds_read_b128 v[92:95], v116 offset:28672\n\t"
        "v_mfma_f32_16x16x32_bf16 v[8:11], v[172:175], v[60:63], v[8:11]\n\t"
        "v_max3_u32 v125, v60, v61, v125\n\t"
        "v_mfma_f32_16x16x32_bf16 v[12:15], v[176:179], v[60:63], v[12:15]\n\t"
        "v_max3_u32 v125, v62, v63, v125\n\t"
        "v_lshlrev_b32_e32 v108, 16, v101\n\t"
        "v_mfma_f32_16x16x32_bf16 v[16:19], v[180:183], v[60:63], v[16:19]\n\t"
        "s_waitcnt vmcnt(5) lgkmcnt(2)\n\t"
        "v_mfma_f32_16x16x32_bf16 v[20:23], v[96:99], v[64:67], v[20:23]\n\t"
        "ds_read_b128 v[96:99], v116 offset:29696\n\t"
        "v_mfma_f32_16x16x32_bf16 v[8:11], v[184:187], v[64:67], v[8:11]\n\t"
        "v_max3_u32 v125, v64, v65, v125\n\t"
        "v_mfma_f32_16x16x32_bf16 v[12:15], v[188:191], v[64:67], v[12:15]\n\t"
        "v_max3_u32 v125, v66, v67, v125\n\t"
        "v_mul_f32_e32 v108, v102, v108\n\t"
        "v_mfma_f32_16x16x32_bf16 v[16:19], v[192:195], v[64:67], v[16:19]\n\t"
        "s_waitcnt vmcnt(4) lgkmcnt(2)\n\t"
        "v_mfma_f32_16x16x32_bf16 v[20:23], v[88:91], v[68:71], v[20:23]\n\t"
        "ds_read_b128 v[88:91], v116 offset:30720\n\t"
        "v_mfma_f32_16x16x32_bf16 v[8:11], v[196:199], v[68:71], v[8:11]\n\t"
        "v_max3_u32 v125, v68, v69, v125\n\t"
        "v_mfma_f32_16x16x32_bf16 v[12:15], v[200:203], v[68:71], v[12:15]\n\t"
        "v_max3_u32 v125, v70, v71, v125\n\t"
        "v_cvt_pk_bf16_f32 v108, 0, v108\n\t"
        "v_mfma_f32_16x16x32_bf16 v[16:19], v[204:207], v[68:71], v[16:19]\n\t"
        "s_waitcnt vmcnt(3) lgkmcnt(2)\n\t"
        "v_mfma_f32_16x16x32_bf16 v[20:23], v[92:95], v[72:75], v[20:23]\n\t"
        "ds_read_b128 v[92:95], v116 offset:31744\n\t"
        "v_mfma_f32_16x16x32_bf16 v[8:11], v[208:211], v[72:75], v[8:11]\n\t"
        "v_max3_u32 v125, v72, v73, v125\n\t"
        "v_mfma_f32_16x16x32_bf16 v[12:15], v[212:215], v[72:75], v[12:15]\n\t"
        "v_max3_u32 v125, v74, v75, v125\n\t"
        "v_and_b32_e32 v109, 0xffff0000, v101\n\t"
        "v_mfma_f32_16x16x32_bf16 v[16:19], v[216:219], v[72:75], v[16:19]\n\t"
        "s_waitcnt vmcnt(2) lgkmcnt(2)\n\t"
        "v_mfma_f32_16x16x32_bf16 v[20:23], v[96:99], v[76:79], v[20:23]\n\t"
        "v_mfma_f32_16x16x32_bf16 v[8:11], v[220:223], v[76:79], v[8:11]\n\t"
        "v_max3_u32 v125, v76, v77, v125\n\t"
        "v_mfma_f32_16x16x32_bf16 v[12:15], v[224:227], v[76:79], v[12:15]\n\t"
        "v_max3_u32 v125, v78, v79, v125\n\t"
        "v_mul_f32_e32 v109, v102, v109\n\t"
        "v_mfma_f32_16x16x32_bf16 v[16:19], v[228:231], v[76:79], v[16:19]\n\t"
        "s_waitcnt vmcnt(1) lgkmcnt(1)\n\t"
        "v_mfma_f32_16x16x32_bf16 v[20:23], v[88:91], v[80:83], v[20:23]\n\t"
        "ds_read_b128 v[88:91], v116 offset:0\n\t"
        "v_mfma_f32_16x16x32_bf16 v[8:11], v[232:235], v[80:83], v[8:11]\n\t"
        "v_max3_u32 v125, v80, v81, v125\n\t"
        "v_mfma_f32_16x16x32_bf16 v[12:15], v[236:239], v[80:83], v[12:15]\n\t"
        "v_max3_u32 v125, v82, v83, v125\n\t"
        "v_cvt_pk_bf16_f32 v109, 0, v109\n\t"
        "v_mfma_f32_16x16x32_bf16 v[16:19], v[240:243], v[80:83], v[16:19]\n\t"
        "s_waitcnt vmcnt(0) lgkmcnt(1)\n\t"
        "v_mfma_f32_16x16x32_bf16 v[20:23], v[92:95], v[84:87], v[20:23]\n\t"
        "ds_read_b128 v[92:95], v116 offset:1024\n\t"
        "v_mfma_f32_16x16x32_bf16 v[8:11], v[244:247], v[84:87], v[8:11]\n\t"
        "ds_read_b128 v[96:99], v116 offset:2048\n\t"
        "v_max3_u32 v125, v84, v85, v125\n\t"
        "v_mfma_f32_16x16x32_bf16 v[12:15], v[248:251], v[84:87], v[12:15]\n\t"
        "v_max3_u32 v125, v86, v87, v125\n\t"
        "v_mfma_f32_16x16x32_bf16 v[16:19], v[252:255], v[84:87], v[16:19]\n\t"
        "v_cmp_eq_u32_e32 vcc, -1, v125\n\t"
        "s_mov_b32 %[nx], 0xf0000000\n\t"
        "s_mov_b32 %[re], 64\n\t"
        "s_cbranch_vccz LP_epi%=\n\t"
        "s_waitcnt lgkmcnt(0)\n\t"
        "s_branch LP_again%=\n\t"
        "LP_run%=:\n\t"
        "s_add_u32 %[t0], %[ro], 0\n\t"
        "s_and_b32 %[t0], %[t0], 7\n\t"
        "s_lshl_b32 %[t0], %[t0], 12\n\t"
        "s_add_u32 %[t0], %[t0], %[nx]\n\t"
        "buffer_load_dwordx4 v[24:27], v117, %[sw], %[t0] offen offset:0 sc1\n\t"
        "buffer_load_dwordx4 v[28:31], v117, %[sw], %[t0] offen offset:1024 sc1\n\t"
        "buffer_load_dwordx4 v[32:35], v117, %[sw], %[t0] offen offset:2048 sc1\n\t"
        "buffer_load_dwordx4 v[36:39], v117, %[sw], %[t0] offen offset:3072 sc1\n\t"
        "s_add_u32 %[t0], %[ro], 1\n\t"
        "s_and_b32 %[t0], %[t0], 7\n\t"
        "s_lshl_b32 %[t0], %[t0], 12\n\t"
        "s_add_u32 %[t0], %[t0], %[nx]\n\t"
        "buffer_load_dwordx4 v[40:43], v117, %[sw], %[t0] offen offset:0 sc1\n\t"
        "buffer_load_dwordx4 v[44:47], v117, %[sw], %[t0] offen offset:1024 sc1\n\t"
        "buffer_load_dwordx4 v[48:51], v117, %[sw], %[t0] offen offset:2048 sc1\n\t"
        "buffer_load_dwordx4 v[52:55], v117, %[sw], %[t0] offen offset:3072 sc1\n\t"
        "s_add_u32 %[t0], %[ro], 2\n\t"
        "s_and_b32 %[t0], %[t0], 7\n\t"
        "s_lshl_b32 %[t0], %[t0], 12\n\t"
        "s_add_u32 %[t0], %[t0], %[nx]\n\t"
        "buffer_load_dwordx4 v[56:59], v117, %[sw], %[t0] offen offset:0 sc1\n\t"
        "buffer_load_dwordx4 v[60:63], v117, %[sw], %[t0] offen offset:1024 sc1\n\t"
        "buffer_load_dwordx4 v[64:67], v117, %[sw], %[t0] offen offset:2048 sc1\n\t"
        "buffer_load_dwordx4 v[68:71], v117, %[sw], %[t0] offen offset:3072 sc1\n\t"
        "s_add_u32 %[t0], %[ro], 3\n\t"
        "s_and_b32 %[t0], %[t0], 7\n\t"
        "s_lshl_b32 %[t0], %[t0], 12\n\t"
        "s_add_u32 %[t0], %[t0], %[nx]\n\t"
        "buffer_load_dwordx4 v[72:75], v117, %[sw], %[t0] offen offset:0 sc1\n\t"
        "buffer_load_dwordx4 v[76:79], v117, %[sw], %[t0] offen offset:1024 sc1\n\t"
        "buffer_load_dwordx4 v[80:83], v117, %[sw], %[t0] offen offset:2048 sc1\n\t"
        "buffer_load_dwordx4 v[84:87], v117, %[sw], %[t0] offen offset:3072 sc1\n\t"
        "buffer_load_dword v103, v118, %[sw], 0 offen sc1\n\t"
        "LP_tile%=:\n\t"
        "s_and_b32 %[sp], %[tl], 1\n\t"
        "s_lshl_b32 %[sp], %[sp], 14\n\t"
        "s_lshr_b32 %[t1], %[tl], 1\n\t"
        "s_lshl_b32 %[t1], %[t1], 6\n\t"
        "s_add_u32 %[t1], %[sp], %[t1]\n\t"
        "buffer_load_dword v100, v119, %[sw], %[t1] offen sc1\n\t"
        "s_add_u32 %[t1], %[t1], 32768\n\t"
        "buffer_load_dword v101, v119, %[sw], %[t1] offen sc1\n\t"
        "s_lshl_b32 %[t1], %[tl], 6\n\t"
        "buffer_load_dword v102, v120, %[sw], %[t1] offen sc1\n\t"
        "buffer_load_dword v103, v118, %[sw], 0 offen sc1\n\t"
        "s_mov_b32 %[cu], %[nx]\n\t"
        "s_waitcnt vmcnt(20) lgkmcnt(2)\n\t"
        "v_mfma_f32_16x16x32_bf16 v[20:23], v[88:91], v[24:27], 0\n\t"
        "ds_read_b128 v[88:91], v116 offset:3072\n\t"
        "v_mfma_f32_16x16x32_bf16 v[8:11], a[0:3], v[24:27], 0\n\t"
        "v_mfma_f32_16x16x32_bf16 v[12:15], a[4:7], v[24:27], 0\n\t"
        "v_mfma_f32_16x16x32_bf16 v[16:19], a[8:11], v[24:27], 0\n\t"
        "s_add_u32 %[t0], %[ro], 4\n\t"
        "s_and_b32 %[t0], %[t0], 7\n\t"
        "s_lshl_b32 %[t0], %[t0], 12\n\t"
        "s_add_u32 %[t0], %[t0], %[cu]\n\t"
        "buffer_load_dwordx4 v[24:27], v117, %[sw], %[t0] offen offset:0 sc1\n\t"
        "s_waitcnt vmcnt(20) lgkmcnt(2)\n\t"
        "v_mfma_f32_16x16x32_bf16 v[20:23], v[92:95], v[28:31], v[20:23]\n\t"
        "ds_read_b128 v[92:95], v116 offset:4096\n\t"
        "v_mfma_f32_16x16x32_bf16 v[8:11], a[12:15], v[28:31], v[8:11]\n\t"
        "v_mfma_f32_16x16x32_bf16 v[12:15], a[16:19], v[28:31], v[12:15]\n\t"
        "v_mfma_f32_16x16x32_bf16 v[16:19], a[20:23], v[28:31], v[16:19]\n\t"
        "buffer_load_dwordx4 v[28:31], v117, %[sw], %[t0] offen offset:1024 sc1\n\t"
        "s_waitcnt vmcnt(20) lgkmcnt(2)\n\t"
        "v_mfma_f32_16x16x32_bf16 v[20:23], v[96:99], v[32:35], v[20:23]\n\t"
        "ds_read_b128 v[96:99], v116 offset:5120\n\t"
        "v_mfma_f32_16x16x32_bf16 v[8:11], a[24:27], v[32:35], v[8:11]\n\t"
        "v_mfma_f32_16x16x32_bf16 v[12:15], a[28:31], v[32:35], v[12:15]\n\t"
        "v_mfma_f32_16x16x32_bf16 v[16:19], a[32:35], v[32:35], v[16:19]\n\t"
        "buffer_load_dwordx4 v[32:35], v117, %[sw], %[t0] offen offset:2048 sc1\n\t"
        "s_waitcnt vmcnt(20) lgkmcnt(2)\n\t"
        "v_mfma_f32_16x16x32_bf16 v[20:23], v[88:91], v[36:39], v[20:23]\n\t"
        "ds_read_b128 v[88:91], v116 offset:6144\n\t"
        "v_mfma_f32_16x16x32_bf16 v[8:11], a[36:39], v[36:39], v[8:11]\n\t"
        "v_mfma_f32_16x16x32_bf16 v[12:15], a[40:43], v[36:39], v[12:15]\n\t"
        "v_mfma_f32_16x16x32_bf16 v[16:19], a[44:47], v[36:39], v[16:19]\n\t"
        "buffer_load_dwordx4 v[36:39], v117, %[sw], %[t0] offen offset:3072 sc1\n\t"
        "s_waitcnt vmcnt(20) lgkmcnt(2)\n\t"
        "v_mfma_f32_16x16x32_bf16 v[20:23], v[92:95], v[40:43], v[20:23]\n\t"
        "ds_read_b128 v[92:95], v116 offset:7168\n\t"
        "v_mfma_f32_16x16x32_bf16 v[8:11], a[48:51], v[40:43], v[8:11]\n\t"
        "v_mfma_f32_16x16x32_bf16 v[12:15], a[52:55], v[40:43], v[12:15]\n\t"
        "v_mfma_f32_16x16x32_bf16 v[16:19], a[56:59], v[40:43], v[16:19]\n\t"
        "s_add_u32 %[t0], %[ro], 5\n\t"
        "s_and_b32 %[t0], %[t0], 7\n\t"
        "s_lshl_b32 %[t0], %[t0], 12\n\t"
        "s_add_u32 %[t0], %[t0], %[cu]\n\t"
        "buffer_load_dwordx4 v[40:43], v117, %[sw], %[t0] offen offset:0 sc1\n\t"
        "s_waitcnt vmcnt(20) lgkmcnt(2)\n\t"
        "v_mfma_f32_16x16x32_bf16 v[20:23], v[96:99], v[44:47], v[20:23]\n\t"
        "ds_read_b128 v[96:99], v116 offset:8192\n\t"
        "v_mfma_f32_16x16x32_bf16 v[8:11], a[60:63], v[44:47], v[8:11]\n\t"
        "v_mfma_f32_16x16x32_bf16 v[12:15], a[64:67], v[44:47], v[12:15]\n\t"
        "v_mfma_f32_16x16x32_bf16 v[16:19], a[68:71], v[44:47], v[16:19]\n\t"
        "buffer_load_dwordx4 v[44:47], v117, %[sw], %[t0] offen offset:1024 sc1\n\t"
        "s_waitcnt vmcnt(20) lgkmcnt(2)\n\t"
        "v_mfma_f32_16x16x32_bf16 v[20:23], v[88:91], v[48:51], v[20:23]\n\t"
        "ds_read_b128 v[88:91], v116 offset:9216\n\t"
        "v_mfma_f32_16x16x32_bf16 v[8:11], a[72:75], v[48:51], v[8:11]\n\t"
        "v_mfma_f32_16x16x32_bf16 v[12:15], a[76:79], v[48:51], v[12:15]\n\t"
        "v_mfma_f32_16x16x32_bf16 v[16:19], a[80:83], v[48:51], v[16:19]\n\t"
        "buffer_load_dwordx4 v[48:51], v117, %[sw], %[t0] offen offset:2048 sc1\n\t"
        "s_waitcnt vmcnt(20) lgkmcnt(2)\n\t"
        "v_mfma_f32_16x16x32_bf16 v[20:23], v[92:95], v[52:55], v[20:23]\n\t"
        "ds_read_b128 v[92:95], v116 offset:10240\n\t"
        "v_mfma_f32_16x16x32_bf16 v[8:11], a[84:87], v[52:55], v[8:11]\n\t"
        "v_mfma_f32_16x16x32_bf16 v[12:15], a[88:91], v[52:55], v[12:15]\n\t"
        "v_mfma_f32_16x16x32_bf16 v[16:19], a[92:95], v[52:55], v[16:19]\n\t"
        "buffer_load_dwordx4 v[52:55], v117, %[sw], %[t0] offen offset:3072 sc1\n\t"
        "s_waitcnt vmcnt(20) lgkmcnt(2)\n\t"
        "s_barrier\n\t"
        "v_mfma_f32_16x16x32_bf16 v[20:23], v[96:99], v[56:59], v[20:23]\n\t"
        "ds_read_b128 v[96:99], v116 offset:11264\n\t"
        "v_mfma_f32_16x16x32_bf16 v[8:11], a[96:99], v[56:59], v[8:11]\n\t"
        "v_mfma_f32_16x16x32_bf16 v[12:15], a[100:103], v[56:59], v[12:15]\n\t"
        "v_mfma_f32_16x16x32_bf16 v[16:19], a[104:107], v[56:59], v[16:19]\n\t"
        "s_add_u32 %[t0], %[ro], 6\n\t"
        "s_and_b32 %[t0], %[t0], 7\n\t"
        "s_lshl_b32 %[t0], %[t0], 12\n\t"
        "s_add_u32 %[t0], %[t0], %[cu]\n\t"
        "buffer_load_dwordx4 v[56:59], v117, %[sw], %[t0] offen offset:0 sc1\n\t"
        "s_waitcnt vmcnt(20) lgkmcnt(2)\n\t"
        "v_mfma_f32_16x16x32_bf16 v[20:23], v[88:91], v[60:63], v[20:23]\n\t"
        "ds_read_b128 v[88:91], v116 offset:12288\n\t"
        "v_mfma_f32_16x16x32_bf16 v[8:11], a[108:111], v[60:63], v[8:11]\n\t"
        "v_mfma_f32_16x16x32_bf16 v[12:15], a[112:115], v[60:63], v[12:15]\n\t"
        "v_mfma_f32_16x16x32_bf16 v[16:19], a[116:119], v[60:63], v[16:19]\n\t"
        "buffer_load_dwordx4 v[60:63], v117, %[sw], %[t0] offen offset:1024 sc1\n\t"
        "s_waitcnt vmcnt(20) lgkmcnt(2)\n\t"
        "v_mfma_f32_16x16x32_bf16 v[20:23], v[92:95], v[64:67], v[20:23]\n\t"
        "ds_read_b128 v[92:95], v116 offset:13312\n\t"
        "v_mfma_f32_16x16x32_bf16 v[8:11], a[120:123], v[64:67], v[8:11]\n\t"
        "v_mfma_f32_16x16x32_bf16 v[12:15], a[124:127], v[64:67], v[12:15]\n\t"
        "v_mfma_f32_16x16x32_bf16 v[16:19], a[128:131], v[64:67], v[16:19]\n\t"
        "buffer_load_dwordx4 v[64:67], v117, %[sw], %[t0] offen offset:2048 sc1\n\t"
        "s_waitcnt vmcnt(20) lgkmcnt(2)\n\t"
        "v_mfma_f32_16x16x32_bf16 v[20:23], v[96:99], v[68:71], v[20:23]\n\t"
        "ds_read_b128 v[96:99], v116 offset:14336\n\t"
        "v_mfma_f32_16x16x32_bf16 v[8:11], a[132:135], v[68:71], v[8:11]\n\t"
        "v_mfma_f32_16x16x32_bf16 v[12:15], a[136:139], v[68:71], v[12:15]\n\t"
        "v_mfma_f32_16x16x32_bf16 v[16:19], a[140:143], v[68:71], v[16:19]\n\t"
        "buffer_load_dwordx4 v[68:71], v117, %[sw], %[t0] offen offset:3072 sc1\n\t"
        "s_waitcnt vmcnt(20) lgkmcnt(2)\n\t"
        "v_mfma_f32_16x16x32_bf16 v[20:23], v[88:91], v[72:75], v[20:23]\n\t"
        "ds_read_b128 v[88:91], v116 offset:15360\n\t"
        "v_mfma_f32_16x16x32_bf16 v[8:11], a[144:147], v[72:75], v[8:11]\n\t"
        "v_mfma_f32_16x16x32_bf16 v[12:15], a[148:151], v[72:75], v[12:15]\n\t"
        "v_mfma_f32_16x16x32_bf16 v[16:19], a[152:155], v[72:75], v[16:19]\n\t"
        "s_add_u32 %[t0], %[ro], 7\n\t"
        "s_and_b32 %[t0], %[t0], 7\n\t"
        "s_lshl_b32 %[t0], %[t0], 12\n\t"
        "s_add_u32 %[t0], %[t0], %[cu]\n\t"
        "buffer_load_dwordx4 v[72:75], v117, %[sw], %[t0] offen offset:0 sc1\n\t"
        "s_waitcnt vmcnt(13) lgkmcnt(2)\n\t"
        "v_mfma_f32_16x16x32_bf16 v[20:23], v[92:95], v[76:79], v[20:23]\n\t"
        "ds_read_b128 v[92:95], v116 offset:16384\n\t"
        "v_min_u32_dpp v103, v103, v103 quad_perm:[1,0,3,2] row_mask:0xf bank_mask:0xf\n\t"
        "s_nop 0\n\t"
        "v_mfma_f32_16x16x32_bf16 v[8:11], a[156:159], v[76:79], v[8:11]\n\t"
        "v_min_u32_dpp v103, v103, v103 quad_perm:[2,3,0,1] row_mask:0xf bank_mask:0xf\n\t"
        "s_nop 0\n\t"
        "v_mfma_f32_16x16x32_bf16 v[12:15], a[160:163], v[76:79], v[12:15]\n\t"
        "v_min_u32_dpp v103, v103, v103 row_ror:4 row_mask:0xf bank_mask:0xf\n\t"
        "s_nop 0\n\t"
        "v_mfma_f32_16x16x32_bf16 v[16:19], a[164:167], v[76:79], v[16:19]\n\t"
        "buffer_load_dwordx4 v[76:79], v117, %[sw], %[t0] offen offset:1024 sc1\n\t"
        "s_waitcnt vmcnt(20) lgkmcnt(2)\n\t"
        "v_mfma_f32_16x16x32_bf16 v[20:23], v[96:99], v[80:83], v[20:23]\n\t"
        "ds_read_b128 v[96:99], v116 offset:17408\n\t"
        "v_readfirstlane_b32 %[pg], v103\n\t"
        "s_add_u32 %[t1], %[tl], 8\n\t"
        "s_lshl_b32 %[nx], %[t1], 17\n\t"
        "s_lshr_b32 %[t1], %[t1], 1\n\t"
        "v_mfma_f32_16x16x32_bf16 v[8:11], a[168:171], v[80:83], v[8:11]\n\t"
        "s_cmp_gt_u32 %[pg], %[t1]\n\t"
        "s_cselect_b32 %[nx], %[nx], 0xf0000000\n\t"
        "v_mfma_f32_16x16x32_bf16 v[12:15], a[172:175], v[80:83], v[12:15]\n\t"
        "v_mfma_f32_16x16x32_bf16 v[16:19], a[176:179], v[80:83], v[16:19]\n\t"
        "buffer_load_dwordx4 v[80:83], v117, %[sw], %[t0] offen offset:2048 sc1\n\t"
        "s_waitcnt vmcnt(20) lgkmcnt(2)\n\t"
        "v_mfma_f32_16x16x32_bf16 v[20:23], v[88:91], v[84:87], v[20:23]\n\t"
        "ds_read_b128 v[88:91], v116 offset:18432\n\t"
        "v_mfma_f32_16x16x32_bf16 v[8:11], a[180:183], v[84:87], v[8:11]\n\t"
        "v_mfma_f32_16x16x32_bf16 v[12:15], a[184:187], v[84:87], v[12:15]\n\t"
        "v_mfma_f32_16x16x32_bf16 v[16:19], a[188:191], v[84:87], v[16:19]\n\t"
        "buffer_load_dwordx4 v[84:87], v117, %[sw], %[t0] offen offset:3072 sc1\n\t"
        "s_waitcnt vmcnt(15) lgkmcnt(2)\n\t"
        "v_mfma_f32_16x16x32_bf16 v[20:23], v[92:95], v[24:27], v[20:23]\n\t"
        "ds_read_b128 v[92:95], v116 offset:19456\n\t"
        "v_mfma_f32_16x16x32_bf16 v[8:11], a[192:195], v[24:27], v[8:11]\n\t"
        "v_mfma_f32_16x16x32_bf16 v[12:15], a[196:199], v[24:27], v[12:15]\n\t"
        "v_mfma_f32_16x16x32_bf16 v[16:19], a[200:203], v[24:27], v[16:19]\n\t"
        "s_add_u32 %[t0], %[ro], 0\n\t"
        "s_and_b32 %[t0], %[t0], 7\n\t"
        "s_lshl_b32 %[t0], %[t0], 12\n\t"
        "s_add_u32 %[t0], %[t0], %[nx]\n\t"
        "buffer_load_dwordx4 v[24:27], v117, %[sw], %[t0] offen offset:0 sc1\n\t"
        "s_waitcnt vmcnt(15) lgkmcnt(2)\n\t"
        "v_mfma_f32_16x16x32_bf16 v[20:23], v[96:99], v[28:31], v[20:23]\n\t"
        "ds_read_b128 v[96:99], v116 offset:20480\n\t"
        "v_mfma_f32_16x16x32_bf16 v[8:11], a[204:207], v[28:31], v[8:11]\n\t"
        "v_mfma_f32_16x16x32_bf16 v[12:15], a[208:211], v[28:31], v[12:15]\n\t"
        "v_sub_f32_e32 v104, 1.0, v102\n\t"
        "v_mfma_f32_16x16x32_bf16 v[16:19], a[212:215], v[28:31], v[16:19]\n\t"
        "buffer_load_dwordx4 v[28:31], v117, %[sw], %[t0] offen offset:1024 sc1\n\t"
        "s_waitcnt vmcnt(15) lgkmcnt(2)\n\t"
        "v_mfma_f32_16x16x32_bf16 v[20:23], v[88:91], v[32:35], v[20:23]\n\t"
        "ds_read_b128 v[88:91], v116 offset:21504\n\t"
        "v_mfma_f32_16x16x32_bf16 v[8:11], a[216:219], v[32:35], v[8:11]\n\t"
        "v_mfma_f32_16x16x32_bf16 v[12:15], a[220:223], v[32:35], v[12:15]\n\t"
        "v_cvt_pk_bf16_f32 v104, 0, v104\n\t"
        "v_mfma_f32_16x16x32_bf16 v[16:19], a[224:227], v[32:35], v[16:19]\n\t"
        "buffer_load_dwordx4 v[32:35], v117, %[sw], %[t0] offen offset:2048 sc1\n\t"
        "s_waitcnt vmcnt(15) lgkmcnt(2)\n\t"
        "v_mfma_f32_16x16x32_bf16 v[20:23], v[92:95], v[36:39], v[20:23]\n\t"
        "ds_read_b128 v[92:95], v116 offset:22528\n\t"
        "v_mfma_f32_16x16x32_bf16 v[8:11], a[228:231], v[36:39], v[8:11]\n\t"
        "v_mfma_f32_16x16x32_bf16 v[12:15], a[232:235], v[36:39], v[12:15]\n\t"
        "v_lshlrev_b32_e32 v106, 16, v100\n\t"
        "v_mfma_f32_16x16x32_bf16 v[16:19], a[236:239], v[36:39], v[16:19]\n\t"
        "buffer_load_dwordx4 v[36:39], v117, %[sw], %[t0] offen offset:3072 sc1\n\t"
        "s_waitcnt vmcnt(15) lgkmcnt(2)\n\t"
        "v_mfma_f32_16x16x32_bf16 v[20:23], v[96:99], v[40:43], v[20:23]\n\t"
        "ds_read_b128 v[96:99], v116 offset:23552\n\t"
        "v_mfma_f32_16x16x32_bf16 v[8:11], a[240:243], v[40:43], v[8:11]\n\t"
        "v_mfma_f32_16x16x32_bf16 v[12:15], a[244:247], v[40:43], v[12:15]\n\t"
        "v_mul_f32_e32 v106, v102, v106\n\t"
        "v_mfma_f32_16x16x32_bf16 v[16:19], a[248:251], v[40:43], v[16:19]\n\t"
        "s_add_u32 %[t0], %[ro], 1\n\t"
        "s_and_b32 %[t0], %[t0], 7\n\t"
        "s_lshl_b32 %[t0], %[t0], 12\n\t"
        "s_add_u32 %[t0], %[t0], %[nx]\n\t"
        "buffer_load_dwordx4 v[40:43], v117, %[sw], %[t0] offen offset:0 sc1\n\t"
        "s_waitcnt vmcnt(15) lgkmcnt(2)\n\t"
        "v_mfma_f32_16x16x32_bf16 v[20:23], v[88:91], v[44:47], v[20:23]\n\t"
        "ds_read_b128 v[88:91], v116 offset:24576\n\t"
        "v_mfma_f32_16x16x32_bf16 v[8:11], a[252:255], v[44:47], v[8:11]\n\t"
        "v_mfma_f32_16x16x32_bf16 v[12:15], v[128:131], v[44:47], v[12:15]\n\t"
        "v_cvt_pk_bf16_f32 v106, 0, v106\n\t"
        "v_mfma_f32_16x16x32_bf16 v[16:19], v[132:135], v[44:47], v[16:19]\n\t"
        "buffer_load_dwordx4 v[44:47], v117, %[sw], %[t0] offen offset:1024 sc1\n\t"
        "s_waitcnt vmcnt(15) lgkmcnt(2)\n\t"
        "v_mfma_f32_16x16x32_bf16 v[20:23], v[92:95], v[48:51], v[20:23]\n\t"
        "ds_read_b128 v[92:95], v116 offset:25600\n\t"
        "v_mfma_f32_16x16x32_bf16 v[8:11], v[136:139], v[48:51], v[8:11]\n\t"
        "v_mfma_f32_16x16x32_bf16 v[12:15], v[140:143], v[48:51], v[12:15]\n\t"
        "v_and_b32_e32 v107, 0xffff0000, v100\n\t"
        "v_mfma_f32_16x16x32_bf16 v[16:19], v[144:147], v[48:51], v[16:19]\n\t"
        "buffer_load_dwordx4 v[48:51], v117, %[sw], %[t0] offen offset:2048 sc1\n\t"
        "s_waitcnt vmcnt(15) lgkmcnt(2)\n\t"
        "v_mfma_f32_16x16x32_bf16 v[20:23], v[96:99], v[52:55], v[20:23]\n\t"
        "ds_read_b128 v[96:99], v116 offset:26624\n\t"
        "v_mfma_f32_16x16x32_bf16 v[8:11], v[148:151], v[52:55], v[8:11]\n\t"
        "v_mfma_f32_16x16x32_bf16 v[12:15], v[152:155], v[52:55], v[12:15]\n\t"
        "v_mul_f32_e32 v107, v102, v107\n\t"
        "v_mfma_f32_16x16x32_bf16 v[16:19], v[156:159], v[52:55], v[16:19]\n\t"
        "buffer_load_dwordx4 v[52:55], v117, %[sw], %[t0] offen offset:3072 sc1\n\t"
        "s_waitcnt vmcnt(15) lgkmcnt(2)\n\t"
        "v_mfma_f32_16x16x32_bf16 v[20:23], v[88:91], v[56:59], v[20:23]\n\t"
        "ds_read_b128 v[88:91], v116 offset:27648\n\t"
        "v_mfma_f32_16x16x32_bf16 v[8:11], v[160:163], v[56:59], v[8:11]\n\t"
        "v_mfma_f32_16x16x32_bf16 v[12:15], v[164:167], v[56:59], v[12:15]\n\t"
        "v_cvt_pk_bf16_f32 v107, 0, v107\n\t"
        "v_mfma_f32_16x16x32_bf16 v[16:19], v[168:171], v[56:59], v[16:19]\n\t"
        "s_add_u32 %[t0], %[ro], 2\n\t"
        "s_and_b32 %[t0], %[t0], 7\n\t"
        "s_lshl_b32 %[t0], %[t0], 12\n\t"
        "s_add_u32 %[t0], %[t0], %[nx]\n\t"
        "buffer_load_dwordx4 v[56:59], v117, %[sw], %[t0] offen offset:0 sc1\n\t"
        "s_waitcnt vmcnt(15) lgkmcnt(2)\n\t"
        "v_mfma_f32_16x16x32_bf16 v[20:23], v[92:95], v[60:63], v[20:23]\n\t"
        "ds_read_b128 v[92:95], v116 offset:28672\n\t"
        "v_mfma_f32_16x16x32_bf16 v[8:11], v[172:175], v[60:63], v[8:11]\n\t"
        "v_mfma_f32_16x16x32_bf16 v[12:15], v[176:179], v[60:63], v[12:15]\n\t"
        "v_lshlrev_b32_e32 v108, 16, v101\n\t"
        "v_mfma_f32_16x16x32_bf16 v[16:19], v[180:183], v[60:63], v[16:19]\n\t"
        "buffer_load_dwordx4 v[60:63], v117, %[sw], %[t0] offen offset:1024 sc1\n\t"
        "s_waitcnt vmcnt(15) lgkmcnt(2)\n\t"
        "v_mfma_f32_16x16x32_bf16 v[20:23], v[96:99], v[64:67], v[20:23]\n\t"
        "ds_read_b128 v[96:99], v116 offset:29696\n\t"
        "v_mfma_f32_16x16x32_bf16 v[8:11], v[184:187], v[64:67], v[8:11]\n\t"
        "v_mfma_f32_16x16x32_bf16 v[12:15], v[188:191], v[64:67], v[12:15]\n\t"
        "v_mul_f32_e32 v108, v102, v108\n\t"
        "v_mfma_f32_16x16x32_bf16 v[16:19], v[192:195], v[64:67], v[16:19]\n\t"
        "buffer_load_dwordx4 v[64:67], v117, %[sw], %[t0] offen offset:2048 sc1\n\t"
        "s_waitcnt vmcnt(15) lgkmcnt(2)\n\t"
        "v_mfma_f32_16x16x32_bf16 v[20:23], v[88:91], v[68:71], v[20:23]\n\t"
        "ds_read_b128 v[88:91], v116 offset:30720\n\t"
        "v_mfma_f32_16x16x32_bf16 v[8:11], v[196:199], v[68:71], v[8:11]\n\t"
        "v_mfma_f32_16x16x32_bf16 v[12:15], v[200:203], v[68:71], v[12:15]\n\t"
        "v_cvt_pk_bf16_f32 v108, 0, v108\n\t"
        "v_mfma_f32_16x16x32_bf16 v[16:19], v[204:207], v[68:71], v[16:19]\n\t"
        "buffer_load_dwordx4 v[68:71], v117, %[sw], %[t0] offen offset:3072 sc1\n\t"
        "s_waitcnt vmcnt(15) lgkmcnt(2)\n\t"
        "v_mfma_f32_16x16x32_bf16 v[20:23], v[92:95], v[72:75], v[20:23]\n\t"
        "ds_read_b128 v[92:95], v116 offset:31744\n\t"
        "v_mfma_f32_16x16x32_bf16 v[8:11], v[208:211], v[72:75], v[8:11]\n\t"
        "v_mfma_f32_16x16x32_bf16 v[12:15], v[212:215], v[72:75], v[12:15]\n\t"
        "v_and_b32_e32 v109, 0xffff0000, v101\n\t"
        "v_mfma_f32_16x16x32_bf16 v[16:19], v[216:219], v[72:75], v[16:19]\n\t"
        "s_add_u32 %[t0], %[ro], 3\n\t"
        "s_and_b32 %[t0], %[t0], 7\n\t"
        "s_lshl_b32 %[t0], %[t0], 12\n\t"
        "s_add_u32 %[t0], %[t0], %[nx]\n\t"
        "buffer_load_dwordx4 v[72:75], v117, %[sw], %[t0] offen offset:0 sc1\n\t"
        "s_waitcnt vmcnt(15) lgkmcnt(2)\n\t"
        "v_mfma_f32_16x16x32_bf16 v[20:23], v[96:99], v[76:79], v[20:23]\n\t"
        "v_mfma_f32_16x16x32_bf16 v[8:11], v[220:223], v[76:79], v[8:11]\n\t"
        "v_mfma_f32_16x16x32_bf16 v[12:15], v[224:227], v[76:79], v[12:15]\n\t"
        "v_mul_f32_e32 v109, v102, v109\n\t"
        "v_mfma_f32_16x16x32_bf16 v[16:19], v[228:231], v[76:79], v[16:19]\n\t"
        "buffer_load_dwordx4 v[76:79], v117, %[sw], %[t0] offen offset:1024 sc1\n\t"
        "s_waitcnt vmcnt(15) lgkmcnt(1)\n\t"
        "v_mfma_f32_16x16x32_bf16 v[20:23], v[88:91], v[80:83], v[20:23]\n\t"
        "ds_read_b128 v[88:91], v116 offset:0\n\t"
        "v_mfma_f32_16x16x32_bf16 v[8:11], v[232:235], v[80:83], v[8:11]\n\t"
        "v_mfma_f32_16x16x32_bf16 v[12:15], v[236:239], v[80:83], v[12:15]\n\t"
        "v_cvt_pk_bf16_f32 v109, 0, v109\n\t"
        "v_mfma_f32_16x16x32_bf16 v[16:19], v[240:243], v[80:83], v[16:19]\n\t"
        "buffer_load_dwordx4 v[80:83], v117, %[sw], %[t0] offen offset:2048 sc1\n\t"
        "s_waitcnt vmcnt(15) lgkmcnt(1)\n\t"
        "v_mfma_f32_16x16x32_bf16 v[20:23], v[92:95], v[84:87], v[20:23]\n\t"
        "ds_read_b128 v[92:95], v116 offset:1024\n\t"
        "v_mfma_f32_16x16x32_bf16 v[8:11], v[244:247], v[84:87], v[8:11]\n\t"
        "ds_read_b128 v[96:99], v116 offset:2048\n\t"
        "v_mfma_f32_16x16x32_bf16 v[12:15], v[248:251], v[84:87], v[12:15]\n\t"
        "v_mfma_f32_16x16x32_bf16 v[16:19], v[252:255], v[84:87], v[16:19]\n\t"
        "buffer_load_dwordx4 v[84:87], v117, %[sw], %[t0] offen offset:3072 sc1\n\t"
        "LP_epi%=:\n\t"
        "s_lshl_b32 %[t0], %[tl], 15\n\t"
        "s_nop 0\n\t"
        "ds_write_b128 v122, v[20:23] offset:192\n\t"
        "s_nop 0\n\t"
        "ds_write_b128 v122, v[8:11] offset:0\n\t"
        "s_nop 0\n\t"
        "ds_write_b128 v122, v[12:15] offset:64\n\t"
        "ds_write_b128 v122, v[16:19] offset:128\n\t"
        "s_waitcnt lgkmcnt(0)\n\t"
        "s_barrier\n\t"
        "ds_read_b128 v[8:11], v123 offset:0\n\t"
        "ds_read_b128 v[12:15], v123 offset:4352\n\t"
        "ds_read_b128 v[16:19], v123 offset:8704\n\t"
        "ds_read_b128 v[20:23], v123 offset:13056\n\t"
        "s_waitcnt lgkmcnt(2)\n\t"
        "v_pk_add_f32 v[8:9], v[8:9], v[12:13]\n\t"
        "v_pk_add_f32 v[10:11], v[10:11], v[14:15]\n\t"
        "s_waitcnt lgkmcnt(0)\n\t"
        "v_pk_add_f32 v[16:17], v[16:17], v[20:21]\n\t"
        "v_pk_add_f32 v[18:19], v[18:19], v[22:23]\n\t"
        "v_pk_add_f32 v[8:9], v[8:9], v[16:17]\n\t"
        "v_pk_add_f32 v[10:11], v[10:11], v[18:19]\n\t"
        "v_cvt_pk_bf16_f32 v8, 0, v8\n\t"
        "v_cvt_pk_bf16_f32 v9, 0, v9\n\t"
        "v_cvt_pk_bf16_f32 v10, 0, v10\n\t"
        "v_cvt_pk_bf16_f32 v11, 0, v11\n\t"
        "v_mul_f32_e32 v8, v104, v8\n\t"
        "v_mul_f32_e32 v9, v104, v9\n\t"
        "v_mul_f32_e32 v10, v104, v10\n\t"
        "v_mul_f32_e32 v11, v104, v11\n\t"
        "v_cvt_pk_bf16_f32 v8, 0, v8\n\t"
        "v_cvt_pk_bf16_f32 v9, 0, v9\n\t"
        "v_cvt_pk_bf16_f32 v10, 0, v10\n\t"
        "v_cvt_pk_bf16_f32 v11, 0, v11\n\t"
        "v_add_f32_e32 v8, v106, v8\n\t"
        "v_add_f32_e32 v9, v107, v9\n\t"
        "v_add_f32_e32 v10, v108, v10\n\t"
        "v_add_f32_e32 v11, v109, v11\n\t"
        "v_cvt_pk_bf16_f32 v104, v8, v9\n\t"
        "v_cvt_pk_bf16_f32 v105, v10, v11\n\t"
        "buffer_store_dwordx2 v[104:105], v121, %[su], %[t0] offen sc1\n\t"
        "s_add_u32 %[tl], %[tl], 8\n\t"
        "s_cmp_ge_u32 %[tl], 128\n\t"
        "s_cbranch_scc1 LP_end%=\n\t"
        "s_cmp_lg_u32 %[nx], 0xf0000000\n\t"
        "s_cbranch_scc1 LP_tile%=\n\t"
        "s_branch LP_wait%=\n\t"
        "LP_end%=:\n\t"
        "s_waitcnt vmcnt(0) lgkmcnt(0)\n\t"
        ".Lk_end_P:\n\t"
        : [t0] "=&s"(t0), [t1] "=&s"(t1), [tl] "+s"(tl), [pg] "=&s"(pg), [cu] "=&s"(cu), [nx] "=&s"(nx), [sp] "=&s"(sp), [re] "=&s"(re), [tu] "=&s"(tu)
        : [so] "s"(so), [sw] "s"(sw), [su] "s"(su), [cv] "s"(F_CVY + nt * 128), [ro] "s"(PROT ? (nt + 2 * rp) & 7 : 0), [db] "s"(W_DBG + (128 + j) * 8192 + 512),
          "{v110}"((w * 1024 + (l >> 3)) * 2048 + nt * 128 + (l & 7) * 16), "{v111}"(stg + ((l * 16) ^ ((l & 48) << 1))), "{v112}"(lr), "{v113}"(lr ^ 32u), "{v114}"(lr ^ 64u), "{v115}"(lr ^ 96u),
          "{v116}"(lb + w * 32768 + l * 16), "{v117}"(W_OX + w * 32768 + l * 16), "{v118}"(F_PROG + (w * 8 + (l & 7)) * 128),
          "{v119}"(W_A + ((nt * 2 + (n >> 3)) * 4 + 2 * (n & 1)) * 32768 + w * 4096 + ((n & 7) >> 1) * 16 + g * 4), "{v120}"(W_GATE + (4 * w + g) * 4), "{v121}"((4 * w + g) * 2048 + nt * 128 + n * 8),
          "{v122}"(lb + 131072 + w * 4352 + n * 272 + g * 16), "{v123}"(lb + 131072 + (4 * w + g) * 272 + n * 16)
        : "memory", "scc", "vcc", "v8", "v9", "v10", "v11", "v12", "v13", "v14", "v15", "v16", "v17", "v18", "v19", "v20", "v21", "v22", "v23", "v24", "v25", "v26", "v27", "v28", "v29", "v30", "v31", "v32", "v33", "v34", "v35", "v36", "v37", "v38", "v39", "v40", "v41", "v42", "v43", "v44", "v45", "v46", "v47", "v48", "v49", "v50", "v51", "v52", "v53", "v54", "v55", "v56", "v57", "v58", "v59", "v60", "v61", "v62", "v63", "v64", "v65", "v66", "v67", "v68", "v69", "v70", "v71", "v72", "v73", "v74", "v75", "v76", "v77", "v78", "v79", "v80", "v81", "v82", "v83", "v84", "v85", "v86", "v87", "v88", "v89", "v90", "v91", "v92", "v93", "v94", "v95", "v96", "v97", "v98", "v99", "v100", "v101", "v102", "v103", "v104", "v105", "v106", "v107", "v108", "v109", "v124", "v125", "v126", "v127", "v128", "v129", "v130", "v131", "v132", "v133", "v134", "v135", "v136", "v137", "v138", "v139", "v140", "v141", "v142", "v143", "v144", "v145", "v146", "v147", "v148", "v149", "v150", "v151", "v152", "v153", "v154", "v155", "v156", "v157", "v158", "v159", "v160", "v161", "v162", "v163", "v164", "v165", "v166", "v167", "v168", "v169", "v170", "v171", "v172", "v173", "v174", "v175", "v176", "v177", "v178", "v179", "v180", "v181", "v182", "v183", "v184", "v185", "v186", "v187", "v188", "v189", "v190", "v191", "v192", "v193", "v194", "v195", "v196", "v197", "v198", "v199", "v200", "v201", "v202", "v203", "v204", "v205", "v206", "v207", "v208", "v209", "v210", "v211", "v212", "v213", "v214", "v215", "v216", "v217", "v218", "v219", "v220", "v221", "v222", "v223", "v224", "v225", "v226", "v227", "v228", "v229", "v230", "v231", "v232", "v233", "v234", "v235", "v236", "v237", "v238", "v239", "v240", "v241", "v242", "v243", "v244", "v245", "v246", "v247", "v248", "v249", "v250", "v251", "v252", "v253", "v254", "v255", "a0", "a1", "a2", "a3", "a4", "a5", "a6", "a7", "a8", "a9", "a10", "a11", "a12", "a13", "a14", "a15", "a16", "a17", "a18", "a19", "a20", "a21", "a22", "a23", "a24", "a25", "a26", "a27", "a28", "a29", "a30", "a31", "a32", "a33", "a34", "a35", "a36", "a37", "a38", "a39", "a40", "a41", "a42", "a43", "a44", "a45", "a46", "a47", "a48", "a49", "a50", "a51", "a52", "a53", "a54", "a55", "a56", "a57", "a58", "a59", "a60", "a61", "a62", "a63", "a64", "a65", "a66", "a67", "a68", "a69", "a70", "a71", "a72", "a73", "a74", "a75", "a76", "a77", "a78", "a79", "a80", "a81", "a82", "a83", "a84", "a85", "a86", "a87", "a88", "a89", "a90", "a91", "a92", "a93", "a94", "a95", "a96", "a97", "a98", "a99", "a100", "a101", "a102", "a103", "a104", "a105", "a106", "a107", "a108", "a109", "a110", "a111", "a112", "a113", "a114", "a115", "a116", "a117", "a118", "a119", "a120", "a121", "a122", "a123", "a124", "a125", "a126", "a127", "a128", "a129", "a130", "a131", "a132", "a133", "a134", "a135", "a136", "a137", "a138", "a139", "a140", "a141", "a142", "a143", "a144", "a145", "a146", "a147", "a148", "a149", "a150", "a151", "a152", "a153", "a154", "a155", "a156", "a157", "a158", "a159", "a160", "a161", "a162", "a163", "a164", "a165", "a166", "a167", "a168", "a169", "a170", "a171", "a172", "a173", "a174", "a175", "a176", "a177", "a178", "a179", "a180", "a181", "a182", "a183", "a184", "a185", "a186", "a187", "a188", "a189", "a190", "a191", "a192", "a193", "a194", "a195", "a196", "a197", "a198", "a199", "a200", "a201", "a202", "a203", "a204", "a205", "a206", "a207", "a208", "a209", "a210", "a211", "a212", "a213", "a214", "a215", "a216", "a217", "a218", "a219", "a220", "a221", "a222", "a223", "a224", "a225", "a226", "a227", "a228", "a229", "a230", "a231", "a232", "a233", "a234", "a235", "a236", "a237", "a238", "a239", "a240", "a241", "a242", "a243", "a244", "a245", "a246", "a247", "a248", "a249", "a250", "a251", "a252", "a253", "a254", "a255");
}
__device__ __forceinline__ void bulk_block(const Params& p, rsrc_t r, unsigned char* lds, const int wv, const int j) {
    BSTAMP(0);
    proj_block(p, lds, wv, (j & 6) * 4 + (j >> 4), ((j >> 2) & 2) | (j & 1), j);
    BSTAMP(2);
    if (XG) { const int x = (j & 7) >> 1, tt = (j >> 3) * 2 + (j & 1); gate_block<4>(p, r, j, wv); conv_prep(p, r, wv, tt, x, x + 1); }
    BSTAMP(5);
    if (!conv_core(p, r, lds, wv, j)) return;
    BSTAMP(3);
    if (GDLY) wait_long(rtnow() + GDLY);
    out_block(p, lds, wv, ((j & 7) >> 1) + (j & 1) * 4, j >> 3, j);
    BSTAMP(7);
}

__device__ __forceinline__ void rec_block(const Params& p, rsrc_t r, unsigned char* lds, const int wv, const int b) {
    const int xq = (b & 7) >> 1, t = (b >> 3) * 2 + (b & 1);
    const unsigned lb = __builtin_amdgcn_readfirstlane((unsigned)(size_t)lds);
    int kind, id, rg = 0, rot = 0, bq = 99;
    if (RPL == 0) { kind = t < 8 ? 0 : t < 24 ? 1 : 2; id = t < 8 ? xq * 8 + t : t < 24 ? (xq & 1) * 16 + t - 8 : xq * 8 + t - 24; rg = xq >> 1; }
    else if (RPL == 4) { kind = t < 8 ? 0 : t < 24 ? 1 : 2; id = t < 8 ? xq * 8 + t : t < 24 ? xq * 8 + ((t - 8) >> 1) : xq * 8 + t - 24; rg = t & 1; }
    else if (RPL == 1) { kind = xq == 0 ? 0 : xq < 3 ? 1 : 2; id = t; rg = xq - 1; }
    else if (RPL == 2) { const int na = xq == 2 ? 10 : 11, q = xq * 21 + t - na; kind = xq == 3 ? 2 : t < na ? 0 : 1; id = xq == 3 ? t : t < na ? xq * 11 + t : q >> 1; rg = q & 1; }
    else { const int na = xq == 2 ? 12 : 10, q = t - na; kind = xq == 3 ? 2 : t < na ? 0 : 1; rg = xq < 2 ? xq : q & 1; bq = q;
        const int c2 = q >> 1, cs = xq < 2 ? (q < 6 ? q : q < 20 ? q + 2 : q + 4) : (c2 < 2 ? 6 + c2 : c2 < 4 ? 20 + c2 : 22 + c2);
        id = xq == 3 ? t : t < na ? xq * 10 + t : cs; rot = !ROT ? 0 : xq < 2 ? (cs & 1) * 4 : ((cs & 1) * 2 + rg) * 2; }
    if (kind == 0) rec_a(p, lb, wv, id, xq);
    else if (kind == 1) rec_b(p, lb, wv, id, rg, xq, rot, bq);
    else {
        const int hi = id, j = hi - 32; (void)j;
        BSTAMP(0);
        rec_helper(p, r, lds, wv, hi);
        BSTAMP(6);
        if (XG) return;
        if (XWAIT == 1) { if (!wait_word(r, F_GO + (hi >> 1) * 128, 1u, 1024u)) return; }
        if (XSTG) wait_long(rtnow() + (hi & 7) * XSTG);
        gate_block<16>(p, r, hi, wv);
        BSTAMP(2);
        if (XWAIT == 2) { if (!wait_word(r, F_GO + (hi >> 1) * 128, 1u, 1024u)) return; }
        conv_prep(p, r, wv, hi, 0, 4);
        BSTAMP(3);
    }
}

__global__ void __launch_bounds__(256, 1) k_main(Params p) {
    extern __shared__ __attribute__((aligned(16))) unsigned char lds[];
    rsrc_t r = __builtin_amdgcn_make_buffer_rsrc(p.ws, 0, W_TOTAL, 0x00020000);
    const int wv = __builtin_amdgcn_readfirstlane(threadIdx.x >> 6);
    int role = blockIdx.x;
    unsigned ctouch = 0u;
    if (CODE_T) {
        unsigned long long pc; int ea, eb, eg;
        asm volatile("s_getpc_b64 %0\n.Lk_begin:\n\ts_mov_b32 %1, .Lk_end_A-.Lk_begin\n\ts_mov_b32 %2, .Lk_end_B-.Lk_begin\n\ts_mov_b32 %3, .Lk_end_P-.Lk_begin" : "=s"(pc), "=s"(ea), "=s"(eb), "=s"(eg));
        const int o = ((blockIdx.x >> 3) * 64 + (threadIdx.x >> 2)) * 128;
        if ((threadIdx.x & 3) == 0 && o < max(ea, max(eb, eg)) - 4) asm volatile("global_load_dword %0, %1, off" : "+v"(ctouch) : "v"((const unsigned char*)pc + o) : "memory");
    }
    if (blockIdx.x == 0 && wv == 1) {
        const int l = threadIdx.x & 63; const v4u z = {0u, 0u, 0u, 0u};
#pragma unroll
        for (int i = 0; i < 6; ++i) st16(r, F_CONV + i * 1024 + l * 16, z);
        DRAIN();
        for (unsigned spin = 0; spin < (1u << 20); ++spin) {
            if (l < 8) __hip_atomic_fetch_add((unsigned*)(p.ws + F_BEAT + l * 128), 1u, __ATOMIC_RELAXED, __HIP_MEMORY_SCOPE_AGENT);
            __builtin_amdgcn_s_sleep(BEAT_SLP);
            unsigned c = ld4(r, l < 8 ? F_TKT + l * 64 : OOB);
            c += __shfl_xor(c, 1); c += __shfl_xor(c, 2); c += __shfl_xor(c, 4);
            if (__builtin_amdgcn_readfirstlane(c) >= 256u) break; }
    }
    if (threadIdx.x == 0) {
        const unsigned bo = F_BEAT + (blockIdx.x & 7) * 128, b0 = ld4(r, bo);
        for (unsigned spin = 0; spin < (1u << 22); ++spin) { MEMBAR(); if (ld4(r, bo) != b0) break; }
        MEMBAR();
        unsigned xcc; asm volatile("s_getreg_b32 %0, hwreg(HW_REG_XCC_ID)" : "=s"(xcc)); xcc &= 7u;
        unsigned* tk = (unsigned*)(p.ws + F_TKT);
        unsigned t = __hip_atomic_fetch_add(tk + xcc * 16, 1u, __ATOMIC_RELAXED, __HIP_MEMORY_SCOPE_AGENT);
        if (t >= 32u) {
            unsigned o = __hip_atomic_fetch_add(tk + 128, 1u, __ATOMIC_RELAXED, __HIP_MEMORY_SCOPE_AGENT);
            for (unsigned spin = 0; spin < (1u << 22); ++spin) { unsigned tot = 0u;
                for (int x = 0; x < 8; ++x) tot += __hip_atomic_load(tk + x * 16, __ATOMIC_RELAXED, __HIP_MEMORY_SCOPE_AGENT);
                if (tot >= 256u) break; }
            for (int x = 0; x < 8; ++x) { const unsigned cnt = __hip_atomic_load(tk + x * 16, __ATOMIC_RELAXED, __HIP_MEMORY_SCOPE_AGENT), fr = cnt < 32u ? 32u - cnt : 0u;
                if (o < fr) { xcc = (unsigned)x; t = cnt + o; break; }
                o -= fr; }
        }
        *(volatile int*)lds = (int)((xcc >> 2) * 128u + (t >> 1) * 8u + (xcc & 3u) * 2u + (t & 1u));
    }
    asm volatile("s_waitcnt vmcnt(0)" : "+v"(ctouch) :: "memory");
    __syncthreads();
    role = __builtin_amdgcn_readfirstlane(*(volatile int*)lds) & 255;
    __syncthreads();
    if (role >= 128) bulk_block(p, r, lds, wv, role - 128); else rec_block(p, r, lds, wv, role);
}

extern "C" void kernel_launch(void* const* d_in, const int* in_sizes, int n_in, void* d_out, int out_size, void* d_ws, size_t ws_size, hipStream_t stream) {
    Params p;
    p.Aq = (const bf16_t*)d_in[0]; p.Ak = (const bf16_t*)d_in[1]; p.Av = (const bf16_t*)d_in[2];
    p.shift_k = (const bf16_t*)d_in[3]; p.ssm_k = (const bf16_t*)d_in[4]; p.ssm_D = (const bf16_t*)d_in[5];
    p.mem0 = (const bf16_t*)d_in[7]; p.Wq = (const bf16_t*)d_in[8]; p.Wk = (const bf16_t*)d_in[9];
    p.Wv = (const bf16_t*)d_in[10]; p.Wo = (const bf16_t*)d_in[11]; p.gate_w = (const bf16_t*)d_in[13];
    p.out = (bf16_t*)d_out; p.ws = (unsigned char*)d_ws;
    (void)hipFuncSetAttribute((const void*)k_main, hipFuncAttributeMaxDynamicSharedMemorySize, L_TOTAL);
    k_main<<<256, 256, L_TOTAL, stream>>>(p);
}
```

```cpp
#include <hip/hip_runtime.h>
#include <stdint.h>
typedef unsigned short bf16_t;
using f32x16 = __attribute__((ext_vector_type(16))) float;
typedef unsigned v4u __attribute__((ext_vector_type(4)));
typedef unsigned v2u __attribute__((ext_vector_type(2)));
typedef __amdgpu_buffer_rsrc_t rsrc_t;
typedef __bf16 hbf2 __attribute__((ext_vector_type(2)));
typedef float f32x2 __attribute__((ext_vector_type(2)));
__device__ __forceinline__ unsigned pack2(float a, float b) { f32x2 v = {a, b}; return __builtin_bit_cast(unsigned, __builtin_convertvector(v, hbf2)); }
__device__ __forceinline__ float rbfh(float f) { return __uint_as_float(pack2(f, 0.f) << 16); }
__device__ __forceinline__ float blo(unsigned x) { return __uint_as_float(x << 16); }
__device__ __forceinline__ float bhi(unsigned x) { return __uint_as_float(x & 0xFFFF0000u); }

constexpr int S = 2048, D = 1024;

constexpr unsigned F_KVQ = 0;
constexpr unsigned F_CONV = 32768;
constexpr unsigned F_ERR = 33024;
constexpr unsigned F_CAL = 33792;
constexpr unsigned F_RDY0 = 34304;
constexpr unsigned F_TKT = 34816;
constexpr unsigned F_GO = 36864;
constexpr unsigned F_BEAT = 40960;
constexpr unsigned F_PROG = 49152;
constexpr unsigned F_CVY = 57344;
constexpr unsigned W_A = 8568832;
constexpr unsigned W_GATE = W_A + S * D * 2;
constexpr unsigned W_QX = W_GATE + 8192;
constexpr unsigned W_KX = W_QX + S * 4096 * 2;
constexpr unsigned W_VF = W_KX + S * 4096 * 2;
constexpr unsigned W_OX = W_VF + S * 4096 * 2;
constexpr unsigned W_Y = W_OX + S * 4096 * 2;
constexpr unsigned W_U = 115531776;
constexpr unsigned W_TOTAL = W_U + S * D * 2;
constexpr unsigned OOB = 0xF0000000u;
constexpr int L_TOTAL = 163840;

__device__ __forceinline__ v4u ld16(rsrc_t r, unsigned off) { return __builtin_amdgcn_raw_buffer_load_b128(r, off, 0, 16); }
__device__ __forceinline__ v2u ld8(rsrc_t r, unsigned off) { return __builtin_amdgcn_raw_buffer_load_b64(r, off, 0, 16); }
__device__ __forceinline__ unsigned ld4(rsrc_t r, unsigned off) { return __builtin_amdgcn_raw_buffer_load_b32(r, off, 0, 16); }
__device__ __forceinline__ void st16(rsrc_t r, unsigned off, v4u v) { __builtin_amdgcn_raw_buffer_store_b128(v, r, off, 0, 16); }
__device__ __forceinline__ void st4(rsrc_t r, unsigned off, unsigned v) { __builtin_amdgcn_raw_buffer_store_b32(v, r, off, 0, 16); }
#define D1 128
#define D2 77
#define LT_A 1
#define PEN 48
#define CAL_T 450u
#define XSLP 3
#define T2U 2
#define TPRE 48
#define MEMBAR() asm volatile("" ::: "memory")
#define DRAIN() asm volatile("s_waitcnt vmcnt(0)" ::: "memory")
__device__ __forceinline__ int otid_(int wv) { int t; asm volatile("v_mbcnt_lo_u32_b32 %0, -1, 0\n\tv_mbcnt_hi_u32_b32 %0, -1, %0" : "=v"(t)); return wv * 64 + t; }
#define otid() otid_(wv)
__device__ __forceinline__ bool wait_word(rsrc_t r, unsigned off, unsigned want, unsigned code) {
    for (unsigned spin = 0;; ++spin) { MEMBAR();
        const unsigned v = __builtin_amdgcn_readfirstlane(ld4(r, off));
        if (v >= want) { MEMBAR(); return true; }
        if (spin > (1u << 20)) { st4(r, F_ERR, code); return false; }
        __builtin_amdgcn_s_sleep(8); }
}
__device__ __forceinline__ void touch_l2(const void* q, unsigned ldsdst) { asm volatile("s_mov_b32 m0, %1\n\ts_nop 0\n\tglobal_load_lds_dword %0, off" :: "v"(q), "s"(ldsdst) : "m0", "memory"); }
__device__ __forceinline__ unsigned rtnow() { return (unsigned)__builtin_amdgcn_s_memrealtime(); }

struct Params {
    const bf16_t *Aq, *Ak, *Av, *shift_k, *ssm_k, *ssm_D, *mem0, *Wq, *Wk, *Wv, *Wo, *gate_w;
    bf16_t* out; unsigned char* ws;
};
__device__ __forceinline__ unsigned ho_base(const void* ws) { const size_t a = (size_t)ws; return (unsigned)((((a + W_TOTAL + 0x7FFFFFu) >> 23) << 23) - a); }
__device__ __forceinline__ v4u mk_srd(const void* p, unsigned bytes) { const size_t a = (size_t)p; v4u d;
    d.x = __builtin_amdgcn_readfirstlane((unsigned)a); d.y = __builtin_amdgcn_readfirstlane((unsigned)(a >> 32) & 0xFFFFu); d.z = __builtin_amdgcn_readfirstlane(bytes); d.w = 0x00020000u; return d; }
constexpr unsigned HO_BYTES = 2097152;
constexpr int L_KV = 90112, L_MS = 139264, L_IMG = 155648, IMG_PS = 1056, IMG_MS = 2112, LB_R = 8192, LB_STG = 16384, LB_STGP = 8704;
static_assert(F_KVQ == 0 && F_ERR == 33024 && F_CONV == 32768 && F_CAL == 33792 && F_RDY0 == 34304 && F_GO == 36864 && F_PROG == 49152 && W_QX == 12771328 && W_KX == 29548544 && W_OX == 63102976 && W_VF == 46325760 && L_KV == 90112 && L_MS == 139264 && L_IMG == 155648 && IMG_PS == 1056 && IMG_MS == 2112 && LB_R == 8192 && LB_STG == 16384 && LB_STGP == 8704, "constants of the generated code");
#define REC_S_IN "{s[8:11]}"(sh), "{s[12:15]}"(sw), "{s[16:19]}"(s1), "{s[20:23]}"(s2), "{s29}"(D1), "{s30}"(D2), "{s43}"(w), "{s44}"(id), "{s49}"(rb), "{s50}"(PEN), "{s51}"(LT_A), "{s45}"(F_GO + (cp * 4 + w) * 128)
__device__ __forceinline__ void rec_a(const Params& p, unsigned lb, const int wv, const int id, const int cp) {
    const int w = wv, l = otid() & 63, n = l & 15, g = l >> 4, h = id, rb = id;
    const v4u sh = mk_srd(p.ws + ho_base(p.ws), HO_BYTES - 8192), sw = mk_srd(p.ws, W_TOTAL), s1 = mk_srd(w < 2 ? p.Wq : p.Wk, 1024 * 4096 * 2), s2 = s1;
    const int r8 = l >> 3, pc = l & 7, pp = n >> 3, bb = (n >> 2) & 1, f = (g & 1) | (pp << 1);
    const unsigned stg = lb + 131072 + w * 8192;
    const unsigned g_w = r8 * 8192 + h * 256 + (w & 1) * 128 + pc * 16;
    const unsigned wb0 = stg + r8 * 128 + (((pc >> 1) ^ (r8 >> 2)) << 5) + (pc & 1) * 16, wb1 = wb0 ^ 64u;
    const unsigned rb0 = stg + (16 * (g >> 1) + 8 * pp + 4 * (g & 1) + bb) * 128 + (f << 5) + (n & 3) * 8;
    const unsigned a_mem = cp * 32768 + (w >> 1) * 16384 + (w & 1) * 4096 + l * 16;
    const unsigned l_ms = lb + L_MS + l * 16;
    const unsigned l_iw = lb + L_IMG + (w >> 1) * IMG_MS + (g >> 1) * IMG_PS + (64 * (w & 1) + n) * 8 + (g & 1) * 4;
    const unsigned l_iq = lb + L_IMG + (n & 1) * IMG_PS + (8 * g + (n >> 2)) * 8;
    const unsigned a_vm = (16 + (h >> 2)) * 16384 + (h & 3) * 2048 + w * 512 + (l & 31) * 16;
    const unsigned a_o = n < 8 ? (8 + (n >> 2) * 4 + (h >> 3)) * 16384 + (h & 7) * 1024 + w * 256 + (n & 3) * 64 + g * 16 : OOB;
    const unsigned a_ox = (((w & 1) * 32 + h) * 4 + w) * 1024 + l * 16;
    asm volatile(
        "s_nop 4\n\t"
        "s_lshl_b32 s34, s43, 2\n\t"
        "s_add_u32 s34, s34, 0\n\t"
        "s_and_b32 s34, s34, 15\n\t"
        "s_lshl_b32 s34, s34, 19\n\t"
        "buffer_load_dwordx4 v[224:227], v96, s[16:19], s34 offen nt\n\t"
        "s_add_u32 s34, s34, 0x10000\n\t"
        "buffer_load_dwordx4 v[228:231], v96, s[16:19], s34 offen nt\n\t"
        "s_add_u32 s34, s34, 0x10000\n\t"
        "buffer_load_dwordx4 v[232:235], v96, s[16:19], s34 offen nt\n\t"
        "s_add_u32 s34, s34, 0x10000\n\t"
        "buffer_load_dwordx4 v[236:239], v96, s[16:19], s34 offen nt\n\t"
        "s_add_u32 s34, s34, 0x10000\n\t"
        "buffer_load_dwordx4 v[240:243], v96, s[16:19], s34 offen nt\n\t"
        "s_add_u32 s34, s34, 0x10000\n\t"
        "buffer_load_dwordx4 v[244:247], v96, s[16:19], s34 offen nt\n\t"
        "s_add_u32 s34, s34, 0x10000\n\t"
        "buffer_load_dwordx4 v[248:251], v96, s[16:19], s34 offen nt\n\t"
        "s_add_u32 s34, s34, 0x10000\n\t"
        "buffer_load_dwordx4 v[252:255], v96, s[16:19], s34 offen nt\n\t"
        "s_lshl_b32 s34, s43, 2\n\t"
        "s_add_u32 s34, s34, 1\n\t"
        "s_and_b32 s34, s34, 15\n\t"
        "s_lshl_b32 s34, s34, 19\n\t"
        "buffer_load_dwordx4 v[0:3], v96, s[16:19], s34 offen nt\n\t"
        "s_add_u32 s34, s34, 0x10000\n\t"
        "buffer_load_dwordx4 v[4:7], v96, s[16:19], s34 offen nt\n\t"
        "s_add_u32 s34, s34, 0x10000\n\t"
        "buffer_load_dwordx4 v[8:11], v96, s[16:19], s34 offen nt\n\t"
        "s_add_u32 s34, s34, 0x10000\n\t"
        "buffer_load_dwordx4 v[12:15], v96, s[16:19], s34 offen nt\n\t"
        "s_add_u32 s34, s34, 0x10000\n\t"
        "buffer_load_dwordx4 v[16:19], v96, s[16:19], s34 offen nt\n\t"
        "s_add_u32 s34, s34, 0x10000\n\t"
        "buffer_load_dwordx4 v[20:23], v96, s[16:19], s34 offen nt\n\t"
        "s_add_u32 s34, s34, 0x10000\n\t"
        "buffer_load_dwordx4 v[24:27], v96, s[16:19], s34 offen nt\n\t"
        "s_add_u32 s34, s34, 0x10000\n\t"
        "buffer_load_dwordx4 v[28:31], v96, s[16:19], s34 offen nt\n\t"
        "s_lshl_b32 s34, s43, 2\n\t"
        "s_add_u32 s34, s34, 2\n\t"
        "s_and_b32 s34, s34, 15\n\t"
        "s_lshl_b32 s34, s34, 19\n\t"
        "buffer_load_dwordx4 v[32:35], v96, s[16:19], s34 offen nt\n\t"
        "s_add_u32 s34, s34, 0x10000\n\t"
        "buffer_load_dwordx4 v[36:39], v96, s[16:19], s34 offen nt\n\t"
        "s_add_u32 s34, s34, 0x10000\n\t"
        "buffer_load_dwordx4 v[40:43], v96, s[16:19], s34 offen nt\n\t"
        "s_add_u32 s34, s34, 0x10000\n\t"
        "buffer_load_dwordx4 v[44:47], v96, s[16:19], s34 offen nt\n\t"
        "s_add_u32 s34, s34, 0x10000\n\t"
        "buffer_load_dwordx4 v[48:51], v96, s[16:19], s34 offen nt\n\t"
        "s_add_u32 s34, s34, 0x10000\n\t"
        "buffer_load_dwordx4 v[52:55], v96, s[16:19], s34 offen nt\n\t"
        "s_add_u32 s34, s34, 0x10000\n\t"
        "buffer_load_dwordx4 v[56:59], v96, s[16:19], s34 offen nt\n\t"
        "s_add_u32 s34, s34, 0x10000\n\t"
        "buffer_load_dwordx4 v[60:63], v96, s[16:19], s34 offen nt\n\t"
        "v_cmp_le_i32_e64 s[60:61], 0, v126\n\t"
        "v_cmp_le_i32_e64 s[62:63], 1, v126\n\t"
        "v_cmp_le_i32_e64 s[64:65], 2, v126\n\t"
        "v_cmp_le_i32_e64 s[66:67], 3, v126\n\t"
        "v_cmp_le_i32_e64 s[68:69], 16, v126\n\t"
        "v_cmp_le_i32_e64 s[70:71], 17, v126\n\t"
        "v_cmp_le_i32_e64 s[72:73], 18, v126\n\t"
        "v_cmp_le_i32_e64 s[74:75], 19, v126\n\t"
        "s_mul_i32 s35, s43, 10240\n\t"
        "v_add_u32_e32 v126, s35, v124\n\t"
        "s_waitcnt vmcnt(16)\n\t"
        "ds_write_b128 v97, v[224:227] offset:0\n\t"
        "ds_write_b128 v98, v[228:231] offset:1024\n\t"
        "ds_write_b128 v97, v[232:235] offset:2048\n\t"
        "ds_write_b128 v98, v[236:239] offset:3072\n\t"
        "ds_write_b128 v97, v[240:243] offset:4096\n\t"
        "ds_write_b128 v98, v[244:247] offset:5120\n\t"
        "ds_write_b128 v97, v[248:251] offset:6144\n\t"
        "ds_write_b128 v98, v[252:255] offset:7168\n\t"
        "ds_read_b64_tr_b16 a[0:1], v99 offset:0\n\t"
        "ds_read_b64_tr_b16 a[2:3], v99 offset:256\n\t"
        "ds_read_b64_tr_b16 a[4:5], v99 offset:4096\n\t"
        "ds_read_b64_tr_b16 a[6:7], v99 offset:4352\n\t"
        "s_waitcnt lgkmcnt(8)\n\t"
        "ds_read_b64_tr_b16 a[8:9], v100 offset:0\n\t"
        "ds_read_b64_tr_b16 a[10:11], v100 offset:256\n\t"
        "ds_read_b64_tr_b16 a[12:13], v100 offset:4096\n\t"
        "ds_read_b64_tr_b16 a[14:15], v100 offset:4352\n\t"
        "s_waitcnt lgkmcnt(8)\n\t"
        "ds_read_b64_tr_b16 a[16:17], v101 offset:0\n\t"
        "ds_read_b64_tr_b16 a[18:19], v101 offset:256\n\t"
        "ds_read_b64_tr_b16 a[20:21], v101 offset:4096\n\t"
        "ds_read_b64_tr_b16 a[22:23], v101 offset:4352\n\t"
        "s_waitcnt lgkmcnt(8)\n\t"
        "ds_read_b64_tr_b16 v[104:105], v102 offset:0\n\t"
        "ds_read_b64_tr_b16 v[106:107], v102 offset:256\n\t"
        "ds_read_b64_tr_b16 v[108:109], v102 offset:4096\n\t"
        "ds_read_b64_tr_b16 v[110:111], v102 offset:4352\n\t"
        "s_lshl_b32 s34, s43, 2\n\t"
        "s_add_u32 s34, s34, 3\n\t"
        "s_and_b32 s34, s34, 15\n\t"
        "s_lshl_b32 s34, s34, 19\n\t"
        "buffer_load_dwordx4 v[64:67], v96, s[16:19], s34 offen nt\n\t"
        "s_add_u32 s34, s34, 0x10000\n\t"
        "buffer_load_dwordx4 v[68:71], v96, s[16:19], s34 offen nt\n\t"
        "s_add_u32 s34, s34, 0x10000\n\t"
        "buffer_load_dwordx4 v[72:75], v96, s[16:19], s34 offen nt\n\t"
        "s_add_u32 s34, s34, 0x10000\n\t"
        "buffer_load_dwordx4 v[76:79], v96, s[16:19], s34 offen nt\n\t"
        "s_add_u32 s34, s34, 0x10000\n\t"
        "buffer_load_dwordx4 v[80:83], v96, s[16:19], s34 offen nt\n\t"
        "s_add_u32 s34, s34, 0x10000\n\t"
        "buffer_load_dwordx4 v[84:87], v96, s[16:19], s34 offen nt\n\t"
        "s_add_u32 s34, s34, 0x10000\n\t"
        "buffer_load_dwordx4 v[88:91], v96, s[16:19], s34 offen nt\n\t"
        "s_add_u32 s34, s34, 0x10000\n\t"
        "buffer_load_dwordx4 v[92:95], v96, s[16:19], s34 offen nt\n\t"
        "s_waitcnt lgkmcnt(0)\n\t"
        "ds_write_b128 v117, v[104:107] offset:0\n\t"
        "ds_write_b128 v117, v[108:111] offset:1024\n\t"
        "s_waitcnt vmcnt(16)\n\t"
        "ds_write_b128 v97, v[0:3] offset:0\n\t"
        "ds_write_b128 v98, v[4:7] offset:1024\n\t"
        "ds_write_b128 v97, v[8:11] offset:2048\n\t"
        "ds_write_b128 v98, v[12:15] offset:3072\n\t"
        "ds_write_b128 v97, v[16:19] offset:4096\n\t"
        "ds_write_b128 v98, v[20:23] offset:5120\n\t"
        "ds_write_b128 v97, v[24:27] offset:6144\n\t"
        "ds_write_b128 v98, v[28:31] offset:7168\n\t"
        "ds_read_b64_tr_b16 a[24:25], v99 offset:0\n\t"
        "ds_read_b64_tr_b16 a[26:27], v99 offset:256\n\t"
        "ds_read_b64_tr_b16 a[28:29], v99 offset:4096\n\t"
        "ds_read_b64_tr_b16 a[30:31], v99 offset:4352\n\t"
        "s_waitcnt lgkmcnt(8)\n\t"
        "ds_read_b64_tr_b16 a[32:33], v100 offset:0\n\t"
        "ds_read_b64_tr_b16 a[34:35], v100 offset:256\n\t"
        "ds_read_b64_tr_b16 a[36:37], v100 offset:4096\n\t"
        "ds_read_b64_tr_b16 a[38:39], v100 offset:4352\n\t"
        "s_waitcnt lgkmcnt(8)\n\t"
        "ds_read_b64_tr_b16 a[40:41], v101 offset:0\n\t"
        "ds_read_b64_tr_b16 a[42:43], v101 offset:256\n\t"
        "ds_read_b64_tr_b16 a[44:45], v101 offset:4096\n\t"
        "ds_read_b64_tr_b16 a[46:47], v101 offset:4352\n\t"
        "s_waitcnt lgkmcnt(8)\n\t"
        "ds_read_b64_tr_b16 v[104:105], v102 offset:0\n\t"
        "ds_read_b64_tr_b16 v[106:107], v102 offset:256\n\t"
        "ds_read_b64_tr_b16 v[108:109], v102 offset:4096\n\t"
        "ds_read_b64_tr_b16 v[110:111], v102 offset:4352\n\t"
        "s_lshl_b32 s34, s43, 2\n\t"
        "s_add_u32 s34, s34, 4\n\t"
        "s_and_b32 s34, s34, 15\n\t"
        "s_lshl_b32 s34, s34, 19\n\t"
        "buffer_load_dwordx4 a[96:99], v96, s[16:19], s34 offen nt\n\t"
        "s_add_u32 s34, s34, 0x10000\n\t"
        "buffer_load_dwordx4 a[100:103], v96, s[16:19], s34 offen nt\n\t"
        "s_add_u32 s34, s34, 0x10000\n\t"
        "buffer_load_dwordx4 a[104:107], v96, s[16:19], s34 offen nt\n\t"
        "s_add_u32 s34, s34, 0x10000\n\t"
        "buffer_load_dwordx4 a[108:111], v96, s[16:19], s34 offen nt\n\t"
        "s_add_u32 s34, s34, 0x10000\n\t"
        "buffer_load_dwordx4 a[112:115], v96, s[16:19], s34 offen nt\n\t"
        "s_add_u32 s34, s34, 0x10000\n\t"
        "buffer_load_dwordx4 a[116:119], v96, s[16:19], s34 offen nt\n\t"
        "s_add_u32 s34, s34, 0x10000\n\t"
        "buffer_load_dwordx4 a[120:123], v96, s[16:19], s34 offen nt\n\t"
        "s_add_u32 s34, s34, 0x10000\n\t"
        "buffer_load_dwordx4 a[124:127], v96, s[16:19], s34 offen nt\n\t"
        "s_waitcnt lgkmcnt(0)\n\t"
        "ds_write_b128 v117, v[104:107] offset:2048\n\t"
        "ds_write_b128 v117, v[108:111] offset:3072\n\t"
        "s_waitcnt vmcnt(16)\n\t"
        "ds_write_b128 v97, v[32:35] offset:0\n\t"
        "ds_write_b128 v98, v[36:39] offset:1024\n\t"
        "ds_write_b128 v97, v[40:43] offset:2048\n\t"
        "ds_write_b128 v98, v[44:47] offset:3072\n\t"
        "ds_write_b128 v97, v[48:51] offset:4096\n\t"
        "ds_write_b128 v98, v[52:55] offset:5120\n\t"
        "ds_write_b128 v97, v[56:59] offset:6144\n\t"
        "ds_write_b128 v98, v[60:63] offset:7168\n\t"
        "ds_read_b64_tr_b16 a[48:49], v99 offset:0\n\t"
        "ds_read_b64_tr_b16 a[50:51], v99 offset:256\n\t"
        "ds_read_b64_tr_b16 a[52:53], v99 offset:4096\n\t"
        "ds_read_b64_tr_b16 a[54:55], v99 offset:4352\n\t"
        "s_waitcnt lgkmcnt(8)\n\t"
        "ds_read_b64_tr_b16 a[56:57], v100 offset:0\n\t"
        "ds_read_b64_tr_b16 a[58:59], v100 offset:256\n\t"
        "ds_read_b64_tr_b16 a[60:61], v100 offset:4096\n\t"
        "ds_read_b64_tr_b16 a[62:63], v100 offset:4352\n\t"
        "s_waitcnt lgkmcnt(8)\n\t"
        "ds_read_b64_tr_b16 a[64:65], v101 offset:0\n\t"
        "ds_read_b64_tr_b16 a[66:67], v101 offset:256\n\t"
        "ds_read_b64_tr_b16 a[68:69], v101 offset:4096\n\t"
        "ds_read_b64_tr_b16 a[70:71], v101 offset:4352\n\t"
        "s_waitcnt lgkmcnt(8)\n\t"
        "ds_read_b64_tr_b16 v[104:105], v102 offset:0\n\t"
        "ds_read_b64_tr_b16 v[106:107], v102 offset:256\n\t"
        "ds_read_b64_tr_b16 v[108:109], v102 offset:4096\n\t"
        "ds_read_b64_tr_b16 v[110:111], v102 offset:4352\n\t"
        "s_lshl_b32 s34, s43, 2\n\t"
        "s_add_u32 s34, s34, 5\n\t"
        "s_and_b32 s34, s34, 15\n\t"
        "s_lshl_b32 s34, s34, 19\n\t"
        "buffer_load_dwordx4 a[128:131], v96, s[16:19], s34 offen nt\n\t"
        "s_add_u32 s34, s34, 0x10000\n\t"
        "buffer_load_dwordx4 a[132:135], v96, s[16:19], s34 offen nt\n\t"
        "s_add_u32 s34, s34, 0x10000\n\t"
        "buffer_load_dwordx4 a[136:139], v96, s[16:19], s34 offen nt\n\t"
        "s_add_u32 s34, s34, 0x10000\n\t"
        "buffer_load_dwordx4 a[140:143], v96, s[16:19], s34 offen nt\n\t"
        "s_add_u32 s34, s34, 0x10000\n\t"
        "buffer_load_dwordx4 a[144:147], v96, s[16:19], s34 offen nt\n\t"
        "s_add_u32 s34, s34, 0x10000\n\t"
        "buffer_load_dwordx4 a[148:151], v96, s[16:19], s34 offen nt\n\t"
        "s_add_u32 s34, s34, 0x10000\n\t"
        "buffer_load_dwordx4 a[152:155], v96, s[16:19], s34 offen nt\n\t"
        "s_add_u32 s34, s34, 0x10000\n\t"
        "buffer_load_dwordx4 a[156:159], v96, s[16:19], s34 offen nt\n\t"
        "s_waitcnt lgkmcnt(0)\n\t"
        "ds_write_b128 v117, v[104:107] offset:4096\n\t"
        "ds_write_b128 v117, v[108:111] offset:5120\n\t"
        "s_waitcnt vmcnt(16)\n\t"
        "ds_write_b128 v97, v[64:67] offset:0\n\t"
        "ds_write_b128 v98, v[68:71] offset:1024\n\t"
        "ds_write_b128 v97, v[72:75] offset:2048\n\t"
        "ds_write_b128 v98, v[76:79] offset:3072\n\t"
        "ds_write_b128 v97, v[80:83] offset:4096\n\t"
        "ds_write_b128 v98, v[84:87] offset:5120\n\t"
        "ds_write_b128 v97, v[88:91] offset:6144\n\t"
        "ds_write_b128 v98, v[92:95] offset:7168\n\t"
        "ds_read_b64_tr_b16 a[72:73], v99 offset:0\n\t"
        "ds_read_b64_tr_b16 a[74:75], v99 offset:256\n\t"
        "ds_read_b64_tr_b16 a[76:77], v99 offset:4096\n\t"
        "ds_read_b64_tr_b16 a[78:79], v99 offset:4352\n\t"
        "s_waitcnt lgkmcnt(8)\n\t"
        "ds_read_b64_tr_b16 a[80:81], v100 offset:0\n\t"
        "ds_read_b64_tr_b16 a[82:83], v100 offset:256\n\t"
        "ds_read_b64_tr_b16 a[84:85], v100 offset:4096\n\t"
        "ds_read_b64_tr_b16 a[86:87], v100 offset:4352\n\t"
        "s_waitcnt lgkmcnt(8)\n\t"
        "ds_read_b64_tr_b16 a[88:89], v101 offset:0\n\t"
        "ds_read_b64_tr_b16 a[90:91], v101 offset:256\n\t"
        "ds_read_b64_tr_b16 a[92:93], v101 offset:4096\n\t"
        "ds_read_b64_tr_b16 a[94:95], v101 offset:4352\n\t"
        "s_waitcnt lgkmcnt(8)\n\t"
        "ds_read_b64_tr_b16 v[104:105], v102 offset:0\n\t"
        "ds_read_b64_tr_b16 v[106:107], v102 offset:256\n\t"
        "ds_read_b64_tr_b16 v[108:109], v102 offset:4096\n\t"
        "ds_read_b64_tr_b16 v[110:111], v102 offset:4352\n\t"
        "s_lshl_b32 s34, s43, 2\n\t"
        "s_add_u32 s34, s34, 6\n\t"
        "s_and_b32 s34, s34, 15\n\t"
        "s_lshl_b32 s34, s34, 19\n\t"
        "buffer_load_dwordx4 a[160:163], v96, s[16:19], s34 offen nt\n\t"
        "s_add_u32 s34, s34, 0x10000\n\t"
        "buffer_load_dwordx4 a[164:167], v96, s[16:19], s34 offen nt\n\t"
        "s_add_u32 s34, s34, 0x10000\n\t"
        "buffer_load_dwordx4 a[168:171], v96, s[16:19], s34 offen nt\n\t"
        "s_add_u32 s34, s34, 0x10000\n\t"
        "buffer_load_dwordx4 a[172:175], v96, s[16:19], s34 offen nt\n\t"
        "s_add_u32 s34, s34, 0x10000\n\t"
        "buffer_load_dwordx4 a[176:179], v96, s[16:19], s34 offen nt\n\t"
        "s_add_u32 s34, s34, 0x10000\n\t"
        "buffer_load_dwordx4 a[180:183], v96, s[16:19], s34 offen nt\n\t"
        "s_add_u32 s34, s34, 0x10000\n\t"
        "buffer_load_dwordx4 a[184:187], v96, s[16:19], s34 offen nt\n\t"
        "s_add_u32 s34, s34, 0x10000\n\t"
        "buffer_load_dwordx4 a[188:191], v96, s[16:19], s34 offen nt\n\t"
        "s_waitcnt lgkmcnt(0)\n\t"
        "ds_write_b128 v117, v[104:107] offset:6144\n\t"
        "ds_write_b128 v117, v[108:111] offset:7168\n\t"
        "s_waitcnt vmcnt(16)\n\t"
        "ds_write_b128 v97, a[96:99] offset:0\n\t"
        "ds_write_b128 v98, a[100:103] offset:1024\n\t"
        "ds_write_b128 v97, a[104:107] offset:2048\n\t"
        "ds_write_b128 v98, a[108:111] offset:3072\n\t"
        "ds_write_b128 v97, a[112:115] offset:4096\n\t"
        "ds_write_b128 v98, a[116:119] offset:5120\n\t"
        "ds_write_b128 v97, a[120:123] offset:6144\n\t"
        "ds_write_b128 v98, a[124:127] offset:7168\n\t"
        "ds_read_b64_tr_b16 a[96:97], v99 offset:0\n\t"
        "ds_read_b64_tr_b16 a[98:99], v99 offset:256\n\t"
        "ds_read_b64_tr_b16 a[100:101], v99 offset:4096\n\t"
        "ds_read_b64_tr_b16 a[102:103], v99 offset:4352\n\t"
        "s_waitcnt lgkmcnt(8)\n\t"
        "ds_read_b64_tr_b16 a[104:105], v100 offset:0\n\t"
        "ds_read_b64_tr_b16 a[106:107], v100 offset:256\n\t"
        "ds_read_b64_tr_b16 a[108:109], v100 offset:4096\n\t"
        "ds_read_b64_tr_b16 a[110:111], v100 offset:4352\n\t"
        "s_waitcnt lgkmcnt(8)\n\t"
        "ds_read_b64_tr_b16 a[112:113], v101 offset:0\n\t"
        "ds_read_b64_tr_b16 a[114:115], v101 offset:256\n\t"
        "ds_read_b64_tr_b16 a[116:117], v101 offset:4096\n\t"
        "ds_read_b64_tr_b16 a[118:119], v101 offset:4352\n\t"
        "s_waitcnt lgkmcnt(8)\n\t"
        "ds_read_b64_tr_b16 v[104:105], v102 offset:0\n\t"
        "ds_read_b64_tr_b16 v[106:107], v102 offset:256\n\t"
        "ds_read_b64_tr_b16 v[108:109], v102 offset:4096\n\t"
        "ds_read_b64_tr_b16 v[110:111], v102 offset:4352\n\t"
        "s_lshl_b32 s34, s43, 2\n\t"
        "s_add_u32 s34, s34, 7\n\t"
        "s_and_b32 s34, s34, 15\n\t"
        "s_lshl_b32 s34, s34, 19\n\t"
        "buffer_load_dwordx4 a[192:195], v96, s[16:19], s34 offen nt\n\t"
        "s_add_u32 s34, s34, 0x10000\n\t"
        "buffer_load_dwordx4 a[196:199], v96, s[16:19], s34 offen nt\n\t"
        "s_add_u32 s34, s34, 0x10000\n\t"
        "buffer_load_dwordx4 a[200:203], v96, s[16:19], s34 offen nt\n\t"
        "s_add_u32 s34, s34, 0x10000\n\t"
        "buffer_load_dwordx4 a[204:207], v96, s[16:19], s34 offen nt\n\t"
        "s_add_u32 s34, s34, 0x10000\n\t"
        "buffer_load_dwordx4 a[208:211], v96, s[16:19], s34 offen nt\n\t"
        "s_add_u32 s34, s34, 0x10000\n\t"
        "buffer_load_dwordx4 a[212:215], v96, s[16:19], s34 offen nt\n\t"
        "s_add_u32 s34, s34, 0x10000\n\t"
        "buffer_load_dwordx4 a[216:219], v96, s[16:19], s34 offen nt\n\t"
        "s_add_u32 s34, s34, 0x10000\n\t"
        "buffer_load_dwordx4 a[220:223], v96, s[16:19], s34 offen nt\n\t"
        "s_waitcnt lgkmcnt(0)\n\t"
        "ds_write_b128 v126, v[104:107] offset:0\n\t"
        "ds_write_b128 v126, v[108:111] offset:1024\n\t"
        "s_waitcnt vmcnt(16)\n\t"
        "ds_write_b128 v97, a[128:131] offset:0\n\t"
        "ds_write_b128 v98, a[132:135] offset:1024\n\t"
        "ds_write_b128 v97, a[136:139] offset:2048\n\t"
        "ds_write_b128 v98, a[140:143] offset:3072\n\t"
        "ds_write_b128 v97, a[144:147] offset:4096\n\t"
        "ds_write_b128 v98, a[148:151] offset:5120\n\t"
        "ds_write_b128 v97, a[152:155] offset:6144\n\t"
        "ds_write_b128 v98, a[156:159] offset:7168\n\t"
        "ds_read_b64_tr_b16 a[120:121], v99 offset:0\n\t"
        "ds_read_b64_tr_b16 a[122:123], v99 offset:256\n\t"
        "ds_read_b64_tr_b16 a[124:125], v99 offset:4096\n\t"
        "ds_read_b64_tr_b16 a[126:127], v99 offset:4352\n\t"
        "s_waitcnt lgkmcnt(8)\n\t"
        "ds_read_b64_tr_b16 a[128:129], v100 offset:0\n\t"
        "ds_read_b64_tr_b16 a[130:131], v100 offset:256\n\t"
        "ds_read_b64_tr_b16 a[132:133], v100 offset:4096\n\t"
        "ds_read_b64_tr_b16 a[134:135], v100 offset:4352\n\t"
        "s_waitcnt lgkmcnt(8)\n\t"
        "ds_read_b64_tr_b16 a[136:137], v101 offset:0\n\t"
        "ds_read_b64_tr_b16 a[138:139], v101 offset:256\n\t"
        "ds_read_b64_tr_b16 a[140:141], v101 offset:4096\n\t"
        "ds_read_b64_tr_b16 a[142:143], v101 offset:4352\n\t"
        "s_waitcnt lgkmcnt(8)\n\t"
        "ds_read_b64_tr_b16 v[104:105], v102 offset:0\n\t"
        "ds_read_b64_tr_b16 v[106:107], v102 offset:256\n\t"
        "ds_read_b64_tr_b16 v[108:109], v102 offset:4096\n\t"
        "ds_read_b64_tr_b16 v[110:111], v102 offset:4352\n\t"
        "s_lshl_b32 s34, s43, 2\n\t"
        "s_add_u32 s34, s34, 8\n\t"
        "s_and_b32 s34, s34, 15\n\t"
        "s_lshl_b32 s34, s34, 19\n\t"
        "buffer_load_dwordx4 a[224:227], v96, s[16:19], s34 offen nt\n\t"
        "s_add_u32 s34, s34, 0x10000\n\t"
        "buffer_load_dwordx4 a[228:231], v96, s[16:19], s34 offen nt\n\t"
        "s_add_u32 s34, s34, 0x10000\n\t"
        "buffer_load_dwordx4 a[232:235], v96, s[16:19], s34 offen nt\n\t"
        "s_add_u32 s34, s34, 0x10000\n\t"
        "buffer_load_dwordx4 a[236:239], v96, s[16:19], s34 offen nt\n\t"
        "s_add_u32 s34, s34, 0x10000\n\t"
        "buffer_load_dwordx4 a[240:243], v96, s[16:19], s34 offen nt\n\t"
        "s_add_u32 s34, s34, 0x10000\n\t"
        "buffer_load_dwordx4 a[244:247], v96, s[16:19], s34 offen nt\n\t"
        "s_add_u32 s34, s34, 0x10000\n\t"
        "buffer_load_dwordx4 a[248:251], v96, s[16:19], s34 offen nt\n\t"
        "s_add_u32 s34, s34, 0x10000\n\t"
        "buffer_load_dwordx4 a[252:255], v96, s[16:19], s34 offen nt\n\t"
        "s_waitcnt lgkmcnt(0)\n\t"
        "ds_write_b128 v126, v[104:107] offset:2048\n\t"
        "ds_write_b128 v126, v[108:111] offset:3072\n\t"
        "s_waitcnt vmcnt(16)\n\t"
        "ds_write_b128 v97, a[160:163] offset:0\n\t"
        "ds_write_b128 v98, a[164:167] offset:1024\n\t"
        "ds_write_b128 v97, a[168:171] offset:2048\n\t"
        "ds_write_b128 v98, a[172:175] offset:3072\n\t"
        "ds_write_b128 v97, a[176:179] offset:4096\n\t"
        "ds_write_b128 v98, a[180:183] offset:5120\n\t"
        "ds_write_b128 v97, a[184:187] offset:6144\n\t"
        "ds_write_b128 v98, a[188:191] offset:7168\n\t"
        "ds_read_b64_tr_b16 a[144:145], v99 offset:0\n\t"
        "ds_read_b64_tr_b16 a[146:147], v99 offset:256\n\t"
        "ds_read_b64_tr_b16 a[148:149], v99 offset:4096\n\t"
        "ds_read_b64_tr_b16 a[150:151], v99 offset:4352\n\t"
        "s_waitcnt lgkmcnt(8)\n\t"
        "ds_read_b64_tr_b16 a[152:153], v100 offset:0\n\t"
        "ds_read_b64_tr_b16 a[154:155], v100 offset:256\n\t"
        "ds_read_b64_tr_b16 a[156:157], v100 offset:4096\n\t"
        "ds_read_b64_tr_b16 a[158:159], v100 offset:4352\n\t"
        "s_waitcnt lgkmcnt(8)\n\t"
        "ds_read_b64_tr_b16 a[160:161], v101 offset:0\n\t"
        "ds_read_b64_tr_b16 a[162:163], v101 offset:256\n\t"
        "ds_read_b64_tr_b16 a[164:165], v101 offset:4096\n\t"
        "ds_read_b64_tr_b16 a[166:167], v101 offset:4352\n\t"
        "s_waitcnt lgkmcnt(8)\n\t"
        "ds_read_b64_tr_b16 v[104:105], v102 offset:0\n\t"
        "ds_read_b64_tr_b16 v[106:107], v102 offset:256\n\t"
        "ds_read_b64_tr_b16 v[108:109], v102 offset:4096\n\t"
        "ds_read_b64_tr_b16 v[110:111], v102 offset:4352\n\t"
        "s_lshl_b32 s34, s43, 2\n\t"
        "s_add_u32 s34, s34, 9\n\t"
        "s_and_b32 s34, s34, 15\n\t"
        "s_lshl_b32 s34, s34, 19\n\t"
        "buffer_load_dwordx4 v[128:131], v96, s[16:19], s34 offen nt\n\t"
        "s_add_u32 s34, s34, 0x10000\n\t"
        "buffer_load_dwordx4 v[132:135], v96, s[16:19], s34 offen nt\n\t"
        "s_add_u32 s34, s34, 0x10000\n\t"
        "buffer_load_dwordx4 v[136:139], v96, s[16:19], s34 offen nt\n\t"
        "s_add_u32 s34, s34, 0x10000\n\t"
        "buffer_load_dwordx4 v[140:143], v96, s[16:19], s34 offen nt\n\t"
        "s_add_u32 s34, s34, 0x10000\n\t"
        "buffer_load_dwordx4 v[144:147], v96, s[16:19], s34 offen nt\n\t"
        "s_add_u32 s34, s34, 0x10000\n\t"
        "buffer_load_dwordx4 v[148:151], v96, s[16:19], s34 offen nt\n\t"
        "s_add_u32 s34, s34, 0x10000\n\t"
        "buffer_load_dwordx4 v[152:155], v96, s[16:19], s34 offen nt\n\t"
        "s_add_u32 s34, s34, 0x10000\n\t"
        "buffer_load_dwordx4 v[156:159], v96, s[16:19], s34 offen nt\n\t"
        "s_waitcnt lgkmcnt(0)\n\t"
        "ds_write_b128 v126, v[104:107] offset:4096\n\t"
        "ds_write_b128 v126, v[108:111] offset:5120\n\t"
        "s_waitcnt vmcnt(16)\n\t"
        "ds_write_b128 v97, a[192:195] offset:0\n\t"
        "ds_write_b128 v98, a[196:199] offset:1024\n\t"
        "ds_write_b128 v97, a[200:203] offset:2048\n\t"
        "ds_write_b128 v98, a[204:207] offset:3072\n\t"
        "ds_write_b128 v97, a[208:211] offset:4096\n\t"
        "ds_write_b128 v98, a[212:215] offset:5120\n\t"
        "ds_write_b128 v97, a[216:219] offset:6144\n\t"
        "ds_write_b128 v98, a[220:223] offset:7168\n\t"
        "ds_read_b64_tr_b16 a[168:169], v99 offset:0\n\t"
        "ds_read_b64_tr_b16 a[170:171], v99 offset:256\n\t"
        "ds_read_b64_tr_b16 a[172:173], v99 offset:4096\n\t"
        "ds_read_b64_tr_b16 a[174:175], v99 offset:4352\n\t"
        "s_waitcnt lgkmcnt(8)\n\t"
        "ds_read_b64_tr_b16 a[176:177], v100 offset:0\n\t"
        "ds_read_b64_tr_b16 a[178:179], v100 offset:256\n\t"
        "ds_read_b64_tr_b16 a[180:181], v100 offset:4096\n\t"
        "ds_read_b64_tr_b16 a[182:183], v100 offset:4352\n\t"
        "s_waitcnt lgkmcnt(8)\n\t"
        "ds_read_b64_tr_b16 a[184:185], v101 offset:0\n\t"
        "ds_read_b64_tr_b16 a[186:187], v101 offset:256\n\t"
        "ds_read_b64_tr_b16 a[188:189], v101 offset:4096\n\t"
        "ds_read_b64_tr_b16 a[190:191], v101 offset:4352\n\t"
        "s_waitcnt lgkmcnt(8)\n\t"
        "ds_read_b64_tr_b16 v[104:105], v102 offset:0\n\t"
        "ds_read_b64_tr_b16 v[106:107], v102 offset:256\n\t"
        "ds_read_b64_tr_b16 v[108:109], v102 offset:4096\n\t"
        "ds_read_b64_tr_b16 v[110:111], v102 offset:4352\n\t"
        "s_lshl_b32 s34, s43, 2\n\t"
        "s_add_u32 s34, s34, 10\n\t"
        "s_and_b32 s34, s34, 15\n\t"
        "s_lshl_b32 s34, s34, 19\n\t"
        "buffer_load_dwordx4 v[160:163], v96, s[16:19], s34 offen nt\n\t"
        "s_add_u32 s34, s34, 0x10000\n\t"
        "buffer_load_dwordx4 v[164:167], v96, s[16:19], s34 offen nt\n\t"
        "s_add_u32 s34, s34, 0x10000\n\t"
        "buffer_load_dwordx4 v[168:171], v96, s[16:19], s34 offen nt\n\t"
        "s_add_u32 s34, s34, 0x10000\n\t"
        "buffer_load_dwordx4 v[172:175], v96, s[16:19], s34 offen nt\n\t"
        "s_add_u32 s34, s34, 0x10000\n\t"
        "buffer_load_dwordx4 v[176:179], v96, s[16:19], s34 offen nt\n\t"
        "s_add_u32 s34, s34, 0x10000\n\t"
        "buffer_load_dwordx4 v[180:183], v96, s[16:19], s34 offen nt\n\t"
        "s_add_u32 s34, s34, 0x10000\n\t"
        "buffer_load_dwordx4 v[184:187], v96, s[16:19], s34 offen nt\n\t"
        "s_add_u32 s34, s34, 0x10000\n\t"
        "buffer_load_dwordx4 v[188:191], v96, s[16:19], s34 offen nt\n\t"
        "s_waitcnt lgkmcnt(0)\n\t"
        "ds_write_b128 v126, v[104:107] offset:6144\n\t"
        "ds_write_b128 v126, v[108:111] offset:7168\n\t"
        "s_waitcnt vmcnt(16)\n\t"
        "ds_write_b128 v97, a[224:227] offset:0\n\t"
        "ds_write_b128 v98, a[228:231] offset:1024\n\t"
        "ds_write_b128 v97, a[232:235] offset:2048\n\t"
        "ds_write_b128 v98, a[236:239] offset:3072\n\t"
        "ds_write_b128 v97, a[240:243] offset:4096\n\t"
        "ds_write_b128 v98, a[244:247] offset:5120\n\t"
        "ds_write_b128 v97, a[248:251] offset:6144\n\t"
        "ds_write_b128 v98, a[252:255] offset:7168\n\t"
        "ds_read_b64_tr_b16 a[192:193], v99 offset:0\n\t"
        "ds_read_b64_tr_b16 a[194:195], v99 offset:256\n\t"
        "ds_read_b64_tr_b16 a[196:197], v99 offset:4096\n\t"
        "ds_read_b64_tr_b16 a[198:199], v99 offset:4352\n\t"
        "s_waitcnt lgkmcnt(8)\n\t"
        "ds_read_b64_tr_b16 a[200:201], v100 offset:0\n\t"
        "ds_read_b64_tr_b16 a[202:203], v100 offset:256\n\t"
        "ds_read_b64_tr_b16 a[204:205], v100 offset:4096\n\t"
        "ds_read_b64_tr_b16 a[206:207], v100 offset:4352\n\t"
        "s_waitcnt lgkmcnt(8)\n\t"
        "ds_read_b64_tr_b16 a[208:209], v101 offset:0\n\t"
        "ds_read_b64_tr_b16 a[210:211], v101 offset:256\n\t"
        "ds_read_b64_tr_b16 a[212:213], v101 offset:4096\n\t"
        "ds_read_b64_tr_b16 a[214:215], v101 offset:4352\n\t"
        "s_waitcnt lgkmcnt(8)\n\t"
        "ds_read_b64_tr_b16 v[104:105], v102 offset:0\n\t"
        "ds_read_b64_tr_b16 v[106:107], v102 offset:256\n\t"
        "ds_read_b64_tr_b16 v[108:109], v102 offset:4096\n\t"
        "ds_read_b64_tr_b16 v[110:111], v102 offset:4352\n\t"
        "s_lshl_b32 s34, s43, 2\n\t"
        "s_add_u32 s34, s34, 11\n\t"
        "s_and_b32 s34, s34, 15\n\t"
        "s_lshl_b32 s34, s34, 19\n\t"
        "buffer_load_dwordx4 v[192:195], v96, s[16:19], s34 offen nt\n\t"
        "s_add_u32 s34, s34, 0x10000\n\t"
        "buffer_load_dwordx4 v[196:199], v96, s[16:19], s34 offen nt\n\t"
        "s_add_u32 s34, s34, 0x10000\n\t"
        "buffer_load_dwordx4 v[200:203], v96, s[16:19], s34 offen nt\n\t"
        "s_add_u32 s34, s34, 0x10000\n\t"
        "buffer_load_dwordx4 v[204:207], v96, s[16:19], s34 offen nt\n\t"
        "s_add_u32 s34, s34, 0x10000\n\t"
        "buffer_load_dwordx4 v[208:211], v96, s[16:19], s34 offen nt\n\t"
        "s_add_u32 s34, s34, 0x10000\n\t"
        "buffer_load_dwordx4 v[212:215], v96, s[16:19], s34 offen nt\n\t"
        "s_add_u32 s34, s34, 0x10000\n\t"
        "buffer_load_dwordx4 v[216:219], v96, s[16:19], s34 offen nt\n\t"
        "s_add_u32 s34, s34, 0x10000\n\t"
        "buffer_load_dwordx4 v[220:223], v96, s[16:19], s34 offen nt\n\t"
        "s_waitcnt lgkmcnt(0)\n\t"
        "ds_write_b128 v117, v[104:107] offset:8192\n\t"
        "ds_write_b128 v117, v[108:111] offset:9216\n\t"
        "s_waitcnt vmcnt(16)\n\t"
        "ds_write_b128 v97, v[128:131] offset:0\n\t"
        "ds_write_b128 v98, v[132:135] offset:1024\n\t"
        "ds_write_b128 v97, v[136:139] offset:2048\n\t"
        "ds_write_b128 v98, v[140:143] offset:3072\n\t"
        "ds_write_b128 v97, v[144:147] offset:4096\n\t"
        "ds_write_b128 v98, v[148:151] offset:5120\n\t"
        "ds_write_b128 v97, v[152:155] offset:6144\n\t"
        "ds_write_b128 v98, v[156:159] offset:7168\n\t"
        "ds_read_b64_tr_b16 a[216:217], v99 offset:0\n\t"
        "ds_read_b64_tr_b16 a[218:219], v99 offset:256\n\t"
        "ds_read_b64_tr_b16 a[220:221], v99 offset:4096\n\t"
        "ds_read_b64_tr_b16 a[222:223], v99 offset:4352\n\t"
        "s_waitcnt lgkmcnt(8)\n\t"
        "ds_read_b64_tr_b16 a[224:225], v100 offset:0\n\t"
        "ds_read_b64_tr_b16 a[226:227], v100 offset:256\n\t"
        "ds_read_b64_tr_b16 a[228:229], v100 offset:4096\n\t"
        "ds_read_b64_tr_b16 a[230:231], v100 offset:4352\n\t"
        "s_waitcnt lgkmcnt(8)\n\t"
        "ds_read_b64_tr_b16 a[232:233], v101 offset:0\n\t"
        "ds_read_b64_tr_b16 a[234:235], v101 offset:256\n\t"
        "ds_read_b64_tr_b16 a[236:237], v101 offset:4096\n\t"
        "ds_read_b64_tr_b16 a[238:239], v101 offset:4352\n\t"
        "s_waitcnt lgkmcnt(8)\n\t"
        "ds_read_b64_tr_b16 v[104:105], v102 offset:0\n\t"
        "ds_read_b64_tr_b16 v[106:107], v102 offset:256\n\t"
        "ds_read_b64_tr_b16 v[108:109], v102 offset:4096\n\t"
        "ds_read_b64_tr_b16 v[110:111], v102 offset:4352\n\t"
        "s_lshl_b32 s34, s43, 2\n\t"
        "s_add_u32 s34, s34, 12\n\t"
        "s_and_b32 s34, s34, 15\n\t"
        "s_lshl_b32 s34, s34, 19\n\t"
        "buffer_load_dwordx4 v[224:227], v96, s[16:19], s34 offen nt\n\t"
        "s_add_u32 s34, s34, 0x10000\n\t"
        "buffer_load_dwordx4 v[228:231], v96, s[16:19], s34 offen nt\n\t"
        "s_add_u32 s34, s34, 0x10000\n\t"
        "buffer_load_dwordx4 v[232:235], v96, s[16:19], s34 offen nt\n\t"
        "s_add_u32 s34, s34, 0x10000\n\t"
        "buffer_load_dwordx4 v[236:239], v96, s[16:19], s34 offen nt\n\t"
        "s_add_u32 s34, s34, 0x10000\n\t"
        "buffer_load_dwordx4 v[240:243], v96, s[16:19], s34 offen nt\n\t"
        "s_add_u32 s34, s34, 0x10000\n\t"
        "buffer_load_dwordx4 v[244:247], v96, s[16:19], s34 offen nt\n\t"
        "s_add_u32 s34, s34, 0x10000\n\t"
        "buffer_load_dwordx4 v[248:251], v96, s[16:19], s34 offen nt\n\t"
        "s_add_u32 s34, s34, 0x10000\n\t"
        "buffer_load_dwordx4 v[252:255], v96, s[16:19], s34 offen nt\n\t"
        "s_waitcnt lgkmcnt(0)\n\t"
        "ds_write_b128 v117, v[104:107] offset:10240\n\t"
        "ds_write_b128 v117, v[108:111] offset:11264\n\t"
        "s_waitcnt vmcnt(16)\n\t"
        "ds_write_b128 v97, v[160:163] offset:0\n\t"
        "ds_write_b128 v98, v[164:167] offset:1024\n\t"
        "ds_write_b128 v97, v[168:171] offset:2048\n\t"
        "ds_write_b128 v98, v[172:175] offset:3072\n\t"
        "ds_write_b128 v97, v[176:179] offset:4096\n\t"
        "ds_write_b128 v98, v[180:183] offset:5120\n\t"
        "ds_write_b128 v97, v[184:187] offset:6144\n\t"
        "ds_write_b128 v98, v[188:191] offset:7168\n\t"
        "ds_read_b64_tr_b16 a[240:241], v99 offset:0\n\t"
        "ds_read_b64_tr_b16 a[242:243], v99 offset:256\n\t"
        "ds_read_b64_tr_b16 a[244:245], v99 offset:4096\n\t"
        "ds_read_b64_tr_b16 a[246:247], v99 offset:4352\n\t"
        "s_waitcnt lgkmcnt(8)\n\t"
        "ds_read_b64_tr_b16 a[248:249], v100 offset:0\n\t"
        "ds_read_b64_tr_b16 a[250:251], v100 offset:256\n\t"
        "ds_read_b64_tr_b16 a[252:253], v100 offset:4096\n\t"
        "ds_read_b64_tr_b16 a[254:255], v100 offset:4352\n\t"
        "s_waitcnt lgkmcnt(8)\n\t"
        "ds_read_b64_tr_b16 v[128:129], v101 offset:0\n\t"
        "ds_read_b64_tr_b16 v[130:131], v101 offset:256\n\t"
        "ds_read_b64_tr_b16 v[132:133], v101 offset:4096\n\t"
        "ds_read_b64_tr_b16 v[134:135], v101 offset:4352\n\t"
        "s_waitcnt lgkmcnt(8)\n\t"
        "ds_read_b64_tr_b16 v[104:105], v102 offset:0\n\t"
        "ds_read_b64_tr_b16 v[106:107], v102 offset:256\n\t"
        "ds_read_b64_tr_b16 v[108:109], v102 offset:4096\n\t"
        "ds_read_b64_tr_b16 v[110:111], v102 offset:4352\n\t"
        "s_lshl_b32 s34, s43, 2\n\t"
        "s_add_u32 s34, s34, 13\n\t"
        "s_and_b32 s34, s34, 15\n\t"
        "s_lshl_b32 s34, s34, 19\n\t"
        "buffer_load_dwordx4 v[0:3], v96, s[16:19], s34 offen nt\n\t"
        "s_add_u32 s34, s34, 0x10000\n\t"
        "buffer_load_dwordx4 v[4:7], v96, s[16:19], s34 offen nt\n\t"
        "s_add_u32 s34, s34, 0x10000\n\t"
        "buffer_load_dwordx4 v[8:11], v96, s[16:19], s34 offen nt\n\t"
        "s_add_u32 s34, s34, 0x10000\n\t"
        "buffer_load_dwordx4 v[12:15], v96, s[16:19], s34 offen nt\n\t"
        "s_add_u32 s34, s34, 0x10000\n\t"
        "buffer_load_dwordx4 v[16:19], v96, s[16:19], s34 offen nt\n\t"
        "s_add_u32 s34, s34, 0x10000\n\t"
        "buffer_load_dwordx4 v[20:23], v96, s[16:19], s34 offen nt\n\t"
        "s_add_u32 s34, s34, 0x10000\n\t"
        "buffer_load_dwordx4 v[24:27], v96, s[16:19], s34 offen nt\n\t"
        "s_add_u32 s34, s34, 0x10000\n\t"
        "buffer_load_dwordx4 v[28:31], v96, s[16:19], s34 offen nt\n\t"
        "s_waitcnt lgkmcnt(0)\n\t"
        "ds_write_b128 v117, v[104:107] offset:12288\n\t"
        "ds_write_b128 v117, v[108:111] offset:13312\n\t"
        "s_waitcnt vmcnt(16)\n\t"
        "ds_write_b128 v97, v[192:195] offset:0\n\t"
        "ds_write_b128 v98, v[196:199] offset:1024\n\t"
        "ds_write_b128 v97, v[200:203] offset:2048\n\t"
        "ds_write_b128 v98, v[204:207] offset:3072\n\t"
        "ds_write_b128 v97, v[208:211] offset:4096\n\t"
        "ds_write_b128 v98, v[212:215] offset:5120\n\t"
        "ds_write_b128 v97, v[216:219] offset:6144\n\t"
        "ds_write_b128 v98, v[220:223] offset:7168\n\t"
        "ds_read_b64_tr_b16 v[136:137], v99 offset:0\n\t"
        "ds_read_b64_tr_b16 v[138:139], v99 offset:256\n\t"
        "ds_read_b64_tr_b16 v[140:141], v99 offset:4096\n\t"
        "ds_read_b64_tr_b16 v[142:143], v99 offset:4352\n\t"
        "s_waitcnt lgkmcnt(8)\n\t"
        "ds_read_b64_tr_b16 v[144:145], v100 offset:0\n\t"
        "ds_read_b64_tr_b16 v[146:147], v100 offset:256\n\t"
        "ds_read_b64_tr_b16 v[148:149], v100 offset:4096\n\t"
        "ds_read_b64_tr_b16 v[150:151], v100 offset:4352\n\t"
        "s_waitcnt lgkmcnt(8)\n\t"
        "ds_read_b64_tr_b16 v[152:153], v101 offset:0\n\t"
        "ds_read_b64_tr_b16 v[154:155], v101 offset:256\n\t"
        "ds_read_b64_tr_b16 v[156:157], v101 offset:4096\n\t"
        "ds_read_b64_tr_b16 v[158:159], v101 offset:4352\n\t"
        "s_waitcnt lgkmcnt(8)\n\t"
        "ds_read_b64_tr_b16 v[104:105], v102 offset:0\n\t"
        "ds_read_b64_tr_b16 v[106:107], v102 offset:256\n\t"
        "ds_read_b64_tr_b16 v[108:109], v102 offset:4096\n\t"
        "ds_read_b64_tr_b16 v[110:111], v102 offset:4352\n\t"
        "s_lshl_b32 s34, s43, 2\n\t"
        "s_add_u32 s34, s34, 14\n\t"
        "s_and_b32 s34, s34, 15\n\t"
        "s_lshl_b32 s34, s34, 19\n\t"
        "buffer_load_dwordx4 v[32:35], v96, s[16:19], s34 offen nt\n\t"
        "s_add_u32 s34, s34, 0x10000\n\t"
        "buffer_load_dwordx4 v[36:39], v96, s[16:19], s34 offen nt\n\t"
        "s_add_u32 s34, s34, 0x10000\n\t"
        "buffer_load_dwordx4 v[40:43], v96, s[16:19], s34 offen nt\n\t"
        "s_add_u32 s34, s34, 0x10000\n\t"
        "buffer_load_dwordx4 v[44:47], v96, s[16:19], s34 offen nt\n\t"
        "s_add_u32 s34, s34, 0x10000\n\t"
        "buffer_load_dwordx4 v[48:51], v96, s[16:19], s34 offen nt\n\t"
        "s_add_u32 s34, s34, 0x10000\n\t"
        "buffer_load_dwordx4 v[52:55], v96, s[16:19], s34 offen nt\n\t"
        "s_add_u32 s34, s34, 0x10000\n\t"
        "buffer_load_dwordx4 v[56:59], v96, s[16:19], s34 offen nt\n\t"
        "s_add_u32 s34, s34, 0x10000\n\t"
        "buffer_load_dwordx4 v[60:63], v96, s[16:19], s34 offen nt\n\t"
        "s_waitcnt lgkmcnt(0)\n\t"
        "ds_write_b128 v117, v[104:107] offset:14336\n\t"
        "ds_write_b128 v117, v[108:111] offset:15360\n\t"
        "s_waitcnt vmcnt(16)\n\t"
        "ds_write_b128 v97, v[224:227] offset:0\n\t"
        "ds_write_b128 v98, v[228:231] offset:1024\n\t"
        "ds_write_b128 v97, v[232:235] offset:2048\n\t"
        "ds_write_b128 v98, v[236:239] offset:3072\n\t"
        "ds_write_b128 v97, v[240:243] offset:4096\n\t"
        "ds_write_b128 v98, v[244:247] offset:5120\n\t"
        "ds_write_b128 v97, v[248:251] offset:6144\n\t"
        "ds_write_b128 v98, v[252:255] offset:7168\n\t"
        "ds_read_b64_tr_b16 v[160:161], v99 offset:0\n\t"
        "ds_read_b64_tr_b16 v[162:163], v99 offset:256\n\t"
        "ds_read_b64_tr_b16 v[164:165], v99 offset:4096\n\t"
        "ds_read_b64_tr_b16 v[166:167], v99 offset:4352\n\t"
        "s_waitcnt lgkmcnt(8)\n\t"
        "ds_read_b64_tr_b16 v[168:169], v100 offset:0\n\t"
        "ds_read_b64_tr_b16 v[170:171], v100 offset:256\n\t"
        "ds_read_b64_tr_b16 v[172:173], v100 offset:4096\n\t"
        "ds_read_b64_tr_b16 v[174:175], v100 offset:4352\n\t"
        "s_waitcnt lgkmcnt(8)\n\t"
        "ds_read_b64_tr_b16 v[176:177], v101 offset:0\n\t"
        "ds_read_b64_tr_b16 v[178:179], v101 offset:256\n\t"
        "ds_read_b64_tr_b16 v[180:181], v101 offset:4096\n\t"
        "ds_read_b64_tr_b16 v[182:183], v101 offset:4352\n\t"
        "s_waitcnt lgkmcnt(8)\n\t"
        "ds_read_b64_tr_b16 v[104:105], v102 offset:0\n\t"
        "ds_read_b64_tr_b16 v[106:107], v102 offset:256\n\t"
        "ds_read_b64_tr_b16 v[108:109], v102 offset:4096\n\t"
        "ds_read_b64_tr_b16 v[110:111], v102 offset:4352\n\t"
        "s_lshl_b32 s34, s43, 2\n\t"
        "s_add_u32 s34, s34, 15\n\t"
        "s_and_b32 s34, s34, 15\n\t"
        "s_lshl_b32 s34, s34, 19\n\t"
        "buffer_load_dwordx4 v[64:67], v96, s[16:19], s34 offen nt\n\t"
        "s_add_u32 s34, s34, 0x10000\n\t"
        "buffer_load_dwordx4 v[68:71], v96, s[16:19], s34 offen nt\n\t"
        "s_add_u32 s34, s34, 0x10000\n\t"
        "buffer_load_dwordx4 v[72:75], v96, s[16:19], s34 offen nt\n\t"
        "s_add_u32 s34, s34, 0x10000\n\t"
        "buffer_load_dwordx4 v[76:79], v96, s[16:19], s34 offen nt\n\t"
        "s_add_u32 s34, s34, 0x10000\n\t"
        "buffer_load_dwordx4 v[80:83], v96, s[16:19], s34 offen nt\n\t"
        "s_add_u32 s34, s34, 0x10000\n\t"
        "buffer_load_dwordx4 v[84:87], v96, s[16:19], s34 offen nt\n\t"
        "s_add_u32 s34, s34, 0x10000\n\t"
        "buffer_load_dwordx4 v[88:91], v96, s[16:19], s34 offen nt\n\t"
        "s_add_u32 s34, s34, 0x10000\n\t"
        "buffer_load_dwordx4 v[92:95], v96, s[16:19], s34 offen nt\n\t"
        "s_waitcnt lgkmcnt(0)\n\t"
        "ds_write_b128 v117, v[104:107] offset:16384\n\t"
        "ds_write_b128 v117, v[108:111] offset:17408\n\t"
        "s_waitcnt vmcnt(16)\n\t"
        "ds_write_b128 v97, v[0:3] offset:0\n\t"
        "ds_write_b128 v98, v[4:7] offset:1024\n\t"
        "ds_write_b128 v97, v[8:11] offset:2048\n\t"
        "ds_write_b128 v98, v[12:15] offset:3072\n\t"
        "ds_write_b128 v97, v[16:19] offset:4096\n\t"
        "ds_write_b128 v98, v[20:23] offset:5120\n\t"
        "ds_write_b128 v97, v[24:27] offset:6144\n\t"
        "ds_write_b128 v98, v[28:31] offset:7168\n\t"
        "ds_read_b64_tr_b16 v[184:185], v99 offset:0\n\t"
        "ds_read_b64_tr_b16 v[186:187], v99 offset:256\n\t"
        "ds_read_b64_tr_b16 v[188:189], v99 offset:4096\n\t"
        "ds_read_b64_tr_b16 v[190:191], v99 offset:4352\n\t"
        "s_waitcnt lgkmcnt(8)\n\t"
        "ds_read_b64_tr_b16 v[192:193], v100 offset:0\n\t"
        "ds_read_b64_tr_b16 v[194:195], v100 offset:256\n\t"
        "ds_read_b64_tr_b16 v[196:197], v100 offset:4096\n\t"
        "ds_read_b64_tr_b16 v[198:199], v100 offset:4352\n\t"
        "s_waitcnt lgkmcnt(8)\n\t"
        "ds_read_b64_tr_b16 v[200:201], v101 offset:0\n\t"
        "ds_read_b64_tr_b16 v[202:203], v101 offset:256\n\t"
        "ds_read_b64_tr_b16 v[204:205], v101 offset:4096\n\t"
        "ds_read_b64_tr_b16 v[206:207], v101 offset:4352\n\t"
        "s_waitcnt lgkmcnt(8)\n\t"
        "ds_read_b64_tr_b16 v[104:105], v102 offset:0\n\t"
        "ds_read_b64_tr_b16 v[106:107], v102 offset:256\n\t"
        "ds_read_b64_tr_b16 v[108:109], v102 offset:4096\n\t"
        "ds_read_b64_tr_b16 v[110:111], v102 offset:4352\n\t"
        "s_waitcnt lgkmcnt(0)\n\t"
        "ds_write_b128 v117, v[104:107] offset:18432\n\t"
        "ds_write_b128 v117, v[108:111] offset:19456\n\t"
        "s_waitcnt vmcnt(8)\n\t"
        "ds_write_b128 v97, v[32:35] offset:0\n\t"
        "ds_write_b128 v98, v[36:39] offset:1024\n\t"
        "ds_write_b128 v97, v[40:43] offset:2048\n\t"
        "ds_write_b128 v98, v[44:47] offset:3072\n\t"
        "ds_write_b128 v97, v[48:51] offset:4096\n\t"
        "ds_write_b128 v98, v[52:55] offset:5120\n\t"
        "ds_write_b128 v97, v[56:59] offset:6144\n\t"
        "ds_write_b128 v98, v[60:63] offset:7168\n\t"
        "ds_read_b64_tr_b16 v[208:209], v99 offset:0\n\t"
        "ds_read_b64_tr_b16 v[210:211], v99 offset:256\n\t"
        "ds_read_b64_tr_b16 v[212:213], v99 offset:4096\n\t"
        "ds_read_b64_tr_b16 v[214:215], v99 offset:4352\n\t"
        "s_waitcnt lgkmcnt(8)\n\t"
        "ds_read_b64_tr_b16 v[216:217], v100 offset:0\n\t"
        "ds_read_b64_tr_b16 v[218:219], v100 offset:256\n\t"
        "ds_read_b64_tr_b16 v[220:221], v100 offset:4096\n\t"
        "ds_read_b64_tr_b16 v[222:223], v100 offset:4352\n\t"
        "s_waitcnt lgkmcnt(8)\n\t"
        "ds_read_b64_tr_b16 v[224:225], v101 offset:0\n\t"
        "ds_read_b64_tr_b16 v[226:227], v101 offset:256\n\t"
        "ds_read_b64_tr_b16 v[228:229], v101 offset:4096\n\t"
        "ds_read_b64_tr_b16 v[230:231], v101 offset:4352\n\t"
        "s_waitcnt lgkmcnt(8)\n\t"
        "ds_read_b64_tr_b16 v[104:105], v102 offset:0\n\t"
        "ds_read_b64_tr_b16 v[106:107], v102 offset:256\n\t"
        "ds_read_b64_tr_b16 v[108:109], v102 offset:4096\n\t"
        "ds_read_b64_tr_b16 v[110:111], v102 offset:4352\n\t"
        "s_mov_b32 s35, 33792\n\t"
        "buffer_load_dword v0, off, s[12:15], s35 sc1\n\t"
        "s_waitcnt lgkmcnt(0)\n\t"
        "ds_write_b128 v117, v[104:107] offset:20480\n\t"
        "ds_write_b128 v117, v[108:111] offset:21504\n\t"
        "s_waitcnt vmcnt(0)\n\t"
        "ds_write_b128 v97, v[64:67] offset:0\n\t"
        "ds_write_b128 v98, v[68:71] offset:1024\n\t"
        "ds_write_b128 v97, v[72:75] offset:2048\n\t"
        "ds_write_b128 v98, v[76:79] offset:3072\n\t"
        "ds_write_b128 v97, v[80:83] offset:4096\n\t"
        "ds_write_b128 v98, v[84:87] offset:5120\n\t"
        "ds_write_b128 v97, v[88:91] offset:6144\n\t"
        "ds_write_b128 v98, v[92:95] offset:7168\n\t"
        "ds_read_b64_tr_b16 v[232:233], v99 offset:0\n\t"
        "ds_read_b64_tr_b16 v[234:235], v99 offset:256\n\t"
        "ds_read_b64_tr_b16 v[236:237], v99 offset:4096\n\t"
        "ds_read_b64_tr_b16 v[238:239], v99 offset:4352\n\t"
        "s_waitcnt lgkmcnt(8)\n\t"
        "ds_read_b64_tr_b16 v[240:241], v100 offset:0\n\t"
        "ds_read_b64_tr_b16 v[242:243], v100 offset:256\n\t"
        "ds_read_b64_tr_b16 v[244:245], v100 offset:4096\n\t"
        "ds_read_b64_tr_b16 v[246:247], v100 offset:4352\n\t"
        "s_waitcnt lgkmcnt(8)\n\t"
        "ds_read_b64_tr_b16 v[248:249], v101 offset:0\n\t"
        "ds_read_b64_tr_b16 v[250:251], v101 offset:256\n\t"
        "ds_read_b64_tr_b16 v[252:253], v101 offset:4096\n\t"
        "ds_read_b64_tr_b16 v[254:255], v101 offset:4352\n\t"
        "s_waitcnt lgkmcnt(8)\n\t"
        "ds_read_b64_tr_b16 v[104:105], v102 offset:0\n\t"
        "ds_read_b64_tr_b16 v[106:107], v102 offset:256\n\t"
        "ds_read_b64_tr_b16 v[108:109], v102 offset:4096\n\t"
        "ds_read_b64_tr_b16 v[110:111], v102 offset:4352\n\t"
        "s_mov_b32 s42, 0\n\t"
        "LA_cal%=:\n\t"
        "v_readfirstlane_b32 s34, v0\n\t"
        "s_nop 0\n\t"
        "s_cmp_lg_u32 s34, 0\n\t"
        "s_cbranch_scc1 LA_calok%=\n\t"
        "s_sleep 4\n\t"
        "s_mov_b32 s35, 33792\n\t"
        "buffer_load_dword v0, off, s[12:15], s35 sc1\n\t"
        "s_waitcnt vmcnt(0)\n\t"
        "s_add_u32 s42, s42, 1\n\t"
        "s_cmp_lt_u32 s42, 0x100000\n\t"
        "s_cbranch_scc1 LA_cal%=\n\t"
        "s_movk_i32 s34, 8\n\t"
        "s_branch LA_die%=\n\t"
        "LA_calok%=:\n\t"
        "s_lshl_b32 s35, s44, 10\n\t"
        "v_lshlrev_b32_e32 v1, 2, v122\n\t"
        "v_and_b32_e32 v1, 0x3c0, v1\n\t"
        "buffer_load_dword v1, v1, s[12:15], s35 offen offset:0 sc1\n\t"
        "s_waitcnt lgkmcnt(0)\n\t"
        "ds_write_b128 v126, v[104:107] offset:8192\n\t"
        "ds_write_b128 v126, v[108:111] offset:9216\n\t"
        "s_waitcnt lgkmcnt(0)\n\t"
        "v_mov_b32_e32 v110, v103\n\t"
        "ds_read_b128 v[64:67], v126 offset:0\n\t"
        "ds_read_b128 v[68:71], v126 offset:1024\n\t"
        "ds_read_b128 v[72:75], v126 offset:2048\n\t"
        "ds_read_b128 v[76:79], v126 offset:3072\n\t"
        "ds_read_b128 v[80:83], v126 offset:4096\n\t"
        "ds_read_b128 v[84:87], v126 offset:5120\n\t"
        "ds_read_b128 v[88:91], v126 offset:6144\n\t"
        "ds_read_b128 v[92:95], v126 offset:7168\n\t"
        "ds_read_b128 v[96:99], v126 offset:8192\n\t"
        "ds_read_b128 v[100:103], v126 offset:9216\n\t"
        "s_waitcnt lgkmcnt(0)\n\t"
        "v_mov_b32_e32 v126, 0\n\t"
        "s_barrier\n\t"
        "s_mov_b32 s48, 0\n\t"
        "s_mov_b32 s54, 0\n\t"
        "s_mov_b32 s0, 0x3f803f80\n\t"
        "s_mov_b32 s1, s0\n\t"
        "s_movk_i32 s56, 24576\n\t"
        "s_and_b32 s58, s43, 1\n\t"
        "s_lshl_b32 s58, s58, 12\n\t"
        "s_lshl_b32 s57, s44, 5\n\t"
        "s_add_u32 s57, s57, s43\n\t"
        "s_lshl_b32 s57, s57, 2\n\t"
        "s_add_u32 s57, s57, 458752\n\t"
        "s_waitcnt vmcnt(0)\n\t"
        "v_mov_b32_e32 v111, v1\n\t"
        "v_readfirstlane_b32 s34, v0\n\t"
        "s_nop 0\n\t"
        "s_and_b32 s34, s34, 8192\n\t"
        "s_add_u32 s8, s8, s34\n\t"
        "s_addc_u32 s9, s9, 0\n\t"
        "s_barrier\n\t"
        "s_cmp_lg_u32 s43, 0\n\t"
        "s_cbranch_scc1 LA_st%=\n\t"
        "v_mov_b32_e32 v2, 1\n\t"
        "s_mov_b32 s35, 34304\n\t"
        "s_mov_b64 exec, 1\n\t"
        "buffer_atomic_add v2, off, s[12:15], s35 sc0\n\t"
        "s_mov_b64 exec, -1\n\t"
        "s_waitcnt vmcnt(0)\n\t"
        "v_readfirstlane_b32 s34, v2\n\t"
        "s_nop 0\n\t"
        "s_cmp_lg_u32 s34, 95\n\t"
        "s_cbranch_scc1 LA_st%=\n\t"
        "s_memrealtime s[32:33]\n\t"
        "v_mbcnt_lo_u32_b32 v3, -1, 0\n\t"
        "v_lshlrev_b32_e32 v3, 7, v3\n\t"
        "s_waitcnt lgkmcnt(0)\n\t"
        "s_add_u32 s34, s32, 50\n\t"
        "s_or_b32 s34, s34, 1\n\t"
        "v_mov_b32_e32 v2, s34\n\t"
        "s_mov_b32 s35, 36864\n\t"
        "s_mov_b64 exec, 0xffff\n\t"
        "buffer_store_dword v2, v3, s[12:15], s35 offen sc1\n\t"
        "s_mov_b64 exec, -1\n\t"
        "LA_st%=:\n\t"
        "s_mov_b32 s24, 0\n\t"
        "s_mov_b32 s26, 0\n\t"
        "s_mov_b32 s27, 0x80000\n\t"
        "s_mov_b32 s28, 0x180000\n\t"
        "s_mov_b32 s31, 0\n\t"
        "s_mov_b32 s47, 0\n\t"
        "s_mov_b32 s38, 0xff00ff00\n\t"
        "s_mov_b32 s39, 0xff00ff00\n\t"
        "s_mov_b32 s40, 0\n\t"
        "s_mov_b32 s41, -1\n\t"
        "ds_read_b128 v[32:35], v117 offset:0\n\t"
        "ds_read_b128 v[36:39], v117 offset:1024\n\t"
        "ds_read_b128 v[40:43], v117 offset:2048\n\t"
        "ds_read_b128 v[44:47], v117 offset:3072\n\t"
        "ds_read_b128 v[48:51], v117 offset:4096\n\t"
        "ds_read_b128 v[52:55], v117 offset:5120\n\t"
        "ds_read_b128 v[56:59], v117 offset:6144\n\t"
        "ds_read_b128 v[60:63], v117 offset:7168\n\t"
        "v_mov_b64_e32 v[0:1], 0\n\t"
        "v_mov_b64_e32 v[2:3], 0\n\t"
        "v_mov_b64_e32 v[4:5], 0\n\t"
        "v_mov_b64_e32 v[6:7], 0\n\t"
        "v_mov_b64_e32 v[8:9], 0\n\t"
        "v_mov_b64_e32 v[10:11], 0\n\t"
        "v_mov_b64_e32 v[12:13], 0\n\t"
        "v_mov_b64_e32 v[14:15], 0\n\t"
        "s_lshr_b32 s59, s48, 1\n\t"
        "s_cmp_gt_u32 s54, s59\n\t"
        "s_cbranch_scc1 LA_kv0ok%=\n\t"
        "s_mov_b32 s42, 0\n\t"
        "s_waitcnt vmcnt(0)\n\t"
        "s_branch LA_kv0w%=\n\t"
        "LA_kv0%=:\n\t"
        "s_waitcnt vmcnt(0)\n\t"
        "LA_kv0w%=:\n\t"
        "v_min_u32_dpp v111, v111, v111 quad_perm:[1,0,3,2] row_mask:0xf bank_mask:0xf\n\t"
        "s_nop 1\n\t"
        "v_min_u32_dpp v111, v111, v111 quad_perm:[2,3,0,1] row_mask:0xf bank_mask:0xf\n\t"
        "s_nop 1\n\t"
        "v_min_u32_dpp v111, v111, v111 row_ror:4 row_mask:0xf bank_mask:0xf\n\t"
        "s_nop 1\n\t"
        "v_min_u32_dpp v111, v111, v111 row_ror:8 row_mask:0xf bank_mask:0xf\n\t"
        "s_nop 0\n\t"
        "v_readfirstlane_b32 s54, v111\n\t"
        "s_nop 0\n\t"
        "s_cmp_gt_u32 s54, s59\n\t"
        "s_cbranch_scc1 LA_kv0ok%=\n\t"
        "s_sleep 4\n\t"
        "s_lshl_b32 s35, s44, 10\n\t"
        "v_lshlrev_b32_e32 v111, 2, v122\n\t"
        "v_and_b32_e32 v111, 0x3c0, v111\n\t"
        "buffer_load_dword v111, v111, s[12:15], s35 offen offset:0 sc1\n\t"
        "s_add_u32 s42, s42, 1\n\t"
        "s_cmp_lt_u32 s42, 0x100000\n\t"
        "s_cbranch_scc1 LA_kv0%=\n\t"
        "s_movk_i32 s34, 16\n\t"
        "s_branch LA_die%=\n\t"
        "LA_kv0ok%=:\n\t"
        "s_and_b32 s36, s48, 1\n\t"
        "s_mul_i32 s36, s36, 24576\n\t"
        "s_add_u32 s36, s36, s55\n\t"
        "s_lshl_b32 s37, s48, 18\n\t"
        "s_lshl_b32 s34, s43, 11\n\t"
        "s_add_u32 s37, s37, s34\n\t"
        "s_add_u32 m0, s36, 0\n\t"
        "s_add_u32 s34, s37, 29548544\n\t"
        "buffer_load_dwordx4 v122, s[12:15], s34 offen sc1 lds\n\t"
        "s_add_u32 m0, s36, 1024\n\t"
        "s_add_u32 s34, s37, 29549568\n\t"
        "buffer_load_dwordx4 v122, s[12:15], s34 offen sc1 lds\n\t"
        "s_add_u32 m0, s36, 8192\n\t"
        "s_add_u32 s34, s37, 12771328\n\t"
        "buffer_load_dwordx4 v122, s[12:15], s34 offen sc1 lds\n\t"
        "s_add_u32 m0, s36, 9216\n\t"
        "s_add_u32 s34, s37, 12772352\n\t"
        "buffer_load_dwordx4 v122, s[12:15], s34 offen sc1 lds\n\t"
        "s_add_u32 m0, s36, 16384\n\t"
        "s_add_u32 s34, s37, 46325760\n\t"
        "buffer_load_dwordx4 v122, s[12:15], s34 offen sc1 lds\n\t"
        "s_add_u32 m0, s36, 17408\n\t"
        "s_add_u32 s34, s37, 46326784\n\t"
        "buffer_load_dwordx4 v122, s[12:15], s34 offen sc1 lds\n\t"
        "s_mov_b32 s42, 0\n\t"
        "LA_gw%=:\n\t"
        "buffer_load_dword v16, off, s[12:15], s45 sc1\n\t"
        "s_waitcnt vmcnt(0)\n\t"
        "v_readfirstlane_b32 s25, v16\n\t"
        "s_nop 0\n\t"
        "s_cmp_lg_u32 s25, 0\n\t"
        "s_cbranch_scc1 LA_loop%=\n\t"
        "s_sleep 2\n\t"
        "s_add_u32 s42, s42, 1\n\t"
        "s_cmp_lt_u32 s42, 0x100000\n\t"
        "s_cbranch_scc1 LA_gw%=\n\t"
        "s_movk_i32 s34, 9\n\t"
        "s_branch LA_die%=\n\t"
        ".p2align 8\n\t"
        "LA_loop%=:\n\t"
        "s_mov_b32 s42, 0\n\t"
        "s_sub_u32 s35, s25, 8\n\t"
        "s_waitcnt lgkmcnt(0)\n\t"
        "s_memrealtime s[2:3]\n\t"
        "s_memrealtime s[4:5]\n\t"
        "s_memrealtime s[6:7]\n\t"
        "LA_wa%=:\n\t"
        "s_memrealtime s[32:33]\n\t"
        "s_waitcnt lgkmcnt(6)\n\t"
        "s_sub_u32 s34, s35, s2\n\t"
        "s_cmpk_lt_u32 s34, 0x2000\n\t"
        "s_cbranch_scc0 LA_wa%=x\n\t"
        "s_memrealtime s[2:3]\n\t"
        "s_waitcnt lgkmcnt(6)\n\t"
        "s_sub_u32 s34, s35, s4\n\t"
        "s_cmpk_lt_u32 s34, 0x2000\n\t"
        "s_cbranch_scc0 LA_wa%=x\n\t"
        "s_memrealtime s[4:5]\n\t"
        "s_waitcnt lgkmcnt(6)\n\t"
        "s_sub_u32 s34, s35, s6\n\t"
        "s_cmpk_lt_u32 s34, 0x2000\n\t"
        "s_cbranch_scc0 LA_wa%=x\n\t"
        "s_memrealtime s[6:7]\n\t"
        "s_waitcnt lgkmcnt(6)\n\t"
        "s_sub_u32 s34, s35, s32\n\t"
        "s_cmpk_lt_u32 s34, 0x2000\n\t"
        "s_cbranch_scc1 LA_wa%=\n\t"
        "s_nop 3\n\t"
        "LA_wa%=x:\n\t"
        "s_add_i32 s34, s34, 4\n\t"
        "s_max_i32 s34, s34, 0\n\t"
        "s_bitcmp1_b32 s34, 1\n\t"
        "s_cbranch_scc0 LA_wa%=y\n\t"
        "s_nop 15\n\t"
        "LA_wa%=y:\n\t"
        "s_bitcmp1_b32 s34, 0\n\t"
        "s_cbranch_scc0 LA_wa%=z\n\t"
        "s_nop 9\n\t"
        "LA_wa%=z:\n\t"
        "LA_rd%=:\n\t"
        "buffer_load_dwordx4 v[16:19], v112, s[8:11], s26 offen offset:0 sc1\n\t"
        "s_nop 12\n\t"
        "buffer_load_dwordx4 v[20:23], v112, s[8:11], s26 offen offset:1024 sc1\n\t"
        "s_nop 12\n\t"
        "buffer_load_dwordx4 v[24:27], v112, s[8:11], s26 offen offset:2048 sc1\n\t"
        "s_nop 12\n\t"
        "buffer_load_dwordx4 v[28:31], v112, s[8:11], s26 offen offset:3072 sc1\n\t"
        "LA_go%=:\n\t"
        "s_waitcnt vmcnt(3)\n\t"
        "v_smfmac_f32_16x16x64_bf16 v[0:3], v[16:19], a[0:7], v110\n\t"
        "ds_write_b128 v113, v[16:19] offset:0\n\t"
        "v_smfmac_f32_16x16x64_bf16 v[4:7], v[16:19], a[8:15], v110\n\t"
        "v_max3_u32 v111, v16, v17, v18\n\t"
        "v_max_u32_e32 v111, v19, v111\n\t"
        "v_smfmac_f32_16x16x64_bf16 v[8:11], v[16:19], a[16:23], v110\n\t"
        "v_smfmac_f32_16x16x64_bf16 v[12:15], v[16:19], v[32:39], v110\n\t"
        "s_waitcnt vmcnt(2)\n\t"
        "v_smfmac_f32_16x16x64_bf16 v[0:3], v[20:23], a[24:31], v110\n\t"
        "ds_write_b128 v113, v[20:23] offset:1024\n\t"
        "v_smfmac_f32_16x16x64_bf16 v[4:7], v[20:23], a[32:39], v110\n\t"
        "v_max3_u32 v111, v20, v21, v111\n\t"
        "v_max3_u32 v111, v22, v23, v111\n\t"
        "v_smfmac_f32_16x16x64_bf16 v[8:11], v[20:23], a[40:47], v110\n\t"
        "s_waitcnt vmcnt(1)\n\t"
        "ds_write_b128 v113, v[24:27] offset:2048\n\t"
        "v_smfmac_f32_16x16x64_bf16 v[12:15], v[20:23], v[40:47], v110\n\t"
        "v_max3_u32 v111, v24, v25, v111\n\t"
        "v_max3_u32 v111, v26, v27, v111\n\t"
        "v_smfmac_f32_16x16x64_bf16 v[0:3], v[24:27], a[48:55], v110\n\t"
        "v_smfmac_f32_16x16x64_bf16 v[4:7], v[24:27], a[56:63], v110\n\t"
        "s_waitcnt vmcnt(0)\n\t"
        "ds_write_b128 v113, v[28:31] offset:3072\n\t"
        "v_max3_u32 v111, v28, v29, v111\n\t"
        "v_max3_u32 v111, v30, v31, v111\n\t"
        "v_smfmac_f32_16x16x64_bf16 v[8:11], v[24:27], a[64:71], v110\n\t"
        "v_cmp_eq_u32_e32 vcc, -1, v111\n\t"
        "v_smfmac_f32_16x16x64_bf16 v[12:15], v[24:27], v[48:55], v110\n\t"
        "s_waitcnt lgkmcnt(0)\n\t"
        "s_cbranch_vccnz LA_rdfail%=\n\t"
        "s_barrier\n\t"
        "ds_read_b128 v[16:19], v114 offset:0\n\t"
        "ds_read_b128 v[20:23], v114 offset:1024\n\t"
        "ds_read_b128 v[24:27], v114 offset:2048\n\t"
        "v_smfmac_f32_16x16x64_bf16 v[0:3], v[28:31], a[72:79], v110\n\t"
        "v_smfmac_f32_16x16x64_bf16 v[4:7], v[28:31], a[80:87], v110\n\t"
        "v_smfmac_f32_16x16x64_bf16 v[8:11], v[28:31], a[88:95], v110\n\t"
        "v_smfmac_f32_16x16x64_bf16 v[12:15], v[28:31], v[56:63], v110\n\t"
        "ds_read_b128 v[28:31], v114 offset:3072\n\t"
        "s_waitcnt lgkmcnt(3)\n\t"
        "v_smfmac_f32_16x16x64_bf16 v[12:15], v[16:19], v[64:71], v110\n\t"
        "ds_read_b128 v[32:35], v117 offset:8192\n\t"
        "v_smfmac_f32_16x16x64_bf16 v[0:3], v[16:19], a[96:103], v110\n\t"
        "ds_read_b128 v[36:39], v117 offset:9216\n\t"
        "v_smfmac_f32_16x16x64_bf16 v[4:7], v[16:19], a[104:111], v110\n\t"
        "s_waitcnt lgkmcnt(4)\n\t"
        "v_smfmac_f32_16x16x64_bf16 v[8:11], v[16:19], a[112:119], v110\n\t"
        "ds_read_b128 v[16:19], v115 offset:0\n\t"
        "v_smfmac_f32_16x16x64_bf16 v[12:15], v[20:23], v[72:79], v110\n\t"
        "ds_read_b128 v[40:43], v117 offset:10240\n\t"
        "v_smfmac_f32_16x16x64_bf16 v[0:3], v[20:23], a[120:127], v110\n\t"
        "ds_read_b128 v[44:47], v117 offset:11264\n\t"
        "v_smfmac_f32_16x16x64_bf16 v[4:7], v[20:23], a[128:135], v110\n\t"
        "s_waitcnt lgkmcnt(6)\n\t"
        "v_smfmac_f32_16x16x64_bf16 v[8:11], v[20:23], a[136:143], v110\n\t"
        "ds_read_b128 v[20:23], v115 offset:1024\n\t"
        "v_smfmac_f32_16x16x64_bf16 v[12:15], v[24:27], v[80:87], v110\n\t"
        "ds_read_b128 v[48:51], v117 offset:12288\n\t"
        "v_smfmac_f32_16x16x64_bf16 v[0:3], v[24:27], a[144:151], v110\n\t"
        "ds_read_b128 v[52:55], v117 offset:13312\n\t"
        "v_smfmac_f32_16x16x64_bf16 v[4:7], v[24:27], a[152:159], v110\n\t"
        "s_waitcnt lgkmcnt(8)\n\t"
        "v_smfmac_f32_16x16x64_bf16 v[8:11], v[24:27], a[160:167], v110\n\t"
        "ds_read_b128 v[24:27], v115 offset:2048\n\t"
        "v_smfmac_f32_16x16x64_bf16 v[12:15], v[28:31], v[88:95], v110\n\t"
        "ds_read_b128 v[56:59], v117 offset:14336\n\t"
        "v_smfmac_f32_16x16x64_bf16 v[0:3], v[28:31], a[168:175], v110\n\t"
        "ds_read_b128 v[60:63], v117 offset:15360\n\t"
        "v_smfmac_f32_16x16x64_bf16 v[4:7], v[28:31], a[176:183], v110\n\t"
        "s_waitcnt lgkmcnt(8)\n\t"
        "v_smfmac_f32_16x16x64_bf16 v[8:11], v[28:31], a[184:191], v110\n\t"
        "ds_read_b128 v[28:31], v115 offset:3072\n\t"
        "v_smfmac_f32_16x16x64_bf16 v[12:15], v[16:19], v[32:39], v110\n\t"
        "ds_read_b128 v[32:35], v117 offset:16384\n\t"
        "v_smfmac_f32_16x16x64_bf16 v[0:3], v[16:19], a[192:199], v110\n\t"
        "ds_read_b128 v[36:39], v117 offset:17408\n\t"
        "v_smfmac_f32_16x16x64_bf16 v[4:7], v[16:19], a[200:207], v110\n\t"
        "s_waitcnt lgkmcnt(8)\n\t"
        "v_smfmac_f32_16x16x64_bf16 v[8:11], v[16:19], a[208:215], v110\n\t"
        "ds_read_b128 v[16:19], v116 offset:0\n\t"
        "v_smfmac_f32_16x16x64_bf16 v[12:15], v[20:23], v[40:47], v110\n\t"
        "ds_read_b128 v[40:43], v117 offset:18432\n\t"
        "v_smfmac_f32_16x16x64_bf16 v[0:3], v[20:23], a[216:223], v110\n\t"
        "ds_read_b128 v[44:47], v117 offset:19456\n\t"
        "v_smfmac_f32_16x16x64_bf16 v[4:7], v[20:23], a[224:231], v110\n\t"
        "s_waitcnt lgkmcnt(8)\n\t"
        "v_smfmac_f32_16x16x64_bf16 v[8:11], v[20:23], a[232:239], v110\n\t"
        "ds_read_b128 v[20:23], v116 offset:1024\n\t"
        "v_smfmac_f32_16x16x64_bf16 v[12:15], v[24:27], v[48:55], v110\n\t"
        "ds_read_b128 v[48:51], v117 offset:20480\n\t"
        "v_smfmac_f32_16x16x64_bf16 v[0:3], v[24:27], a[240:247], v110\n\t"
        "ds_read_b128 v[52:55], v117 offset:21504\n\t"
        "v_smfmac_f32_16x16x64_bf16 v[4:7], v[24:27], a[248:255], v110\n\t"
        "s_waitcnt lgkmcnt(8)\n\t"
        "v_smfmac_f32_16x16x64_bf16 v[8:11], v[24:27], v[128:135], v110\n\t"
        "ds_read_b128 v[24:27], v116 offset:2048\n\t"
        "v_smfmac_f32_16x16x64_bf16 v[12:15], v[28:31], v[56:63], v110\n\t"
        "ds_read_b128 v[56:59], v124 offset:3072\n\t"
        "v_smfmac_f32_16x16x64_bf16 v[0:3], v[28:31], v[136:143], v110\n\t"
        "ds_read_b128 v[60:63], v124 offset:7168\n\t"
        "v_smfmac_f32_16x16x64_bf16 v[4:7], v[28:31], v[144:151], v110\n\t"
        "s_waitcnt lgkmcnt(8)\n\t"
        "v_smfmac_f32_16x16x64_bf16 v[8:11], v[28:31], v[152:159], v110\n\t"
        "ds_read_b128 v[28:31], v116 offset:3072\n\t"
        "v_smfmac_f32_16x16x64_bf16 v[12:15], v[16:19], v[32:39], v110\n\t"
        "ds_read_b128 v[32:35], v124 offset:0\n\t"
        "v_smfmac_f32_16x16x64_bf16 v[0:3], v[16:19], v[160:167], v110\n\t"
        "ds_read_b128 v[36:39], v124 offset:4096\n\t"
        "v_smfmac_f32_16x16x64_bf16 v[4:7], v[16:19], v[168:175], v110\n\t"
        "s_waitcnt lgkmcnt(8)\n\t"
        "v_smfmac_f32_16x16x64_bf16 v[8:11], v[16:19], v[176:183], v110\n\t"
        "v_smfmac_f32_16x16x64_bf16 v[12:15], v[20:23], v[40:47], v110\n\t"
        "ds_read_b128 v[40:43], v124 offset:1024\n\t"
        "v_smfmac_f32_16x16x64_bf16 v[8:11], v[20:23], v[200:207], v110\n\t"
        "ds_read_b128 v[44:47], v124 offset:5120\n\t"
        "s_waitcnt lgkmcnt(7)\n\t"
        "v_smfmac_f32_16x16x64_bf16 v[12:15], v[24:27], v[48:55], v110\n\t"
        "ds_read_b128 v[48:51], v124 offset:2048\n\t"
        "v_smfmac_f32_16x16x64_bf16 v[8:11], v[24:27], v[224:231], v110\n\t"
        "ds_read_b128 v[52:55], v124 offset:6144\n\t"
        "s_waitcnt lgkmcnt(6)\n\t"
        "v_smfmac_f32_16x16x64_bf16 v[12:15], v[28:31], v[96:103], v110\n\t"
        "v_smfmac_f32_16x16x64_bf16 v[8:11], v[28:31], v[248:255], v110\n\t"
        "v_smfmac_f32_16x16x64_bf16 v[0:3], v[20:23], v[184:191], v110\n\t"
        "s_lshl_b32 s34, s43, 11\n\t"
        "v_add_u32_e32 v108, s34, v124\n\t"
        "v_smfmac_f32_16x16x64_bf16 v[0:3], v[24:27], v[208:215], v110\n\t"
        "v_add_u32_e32 v109, s58, v124\n\t"
        "v_smfmac_f32_16x16x64_bf16 v[4:7], v[20:23], v[192:199], v110\n\t"
        "v_smfmac_f32_16x16x64_bf16 v[0:3], v[28:31], v[232:239], v110\n\t"
        "v_smfmac_f32_16x16x64_bf16 v[4:7], v[24:27], v[216:223], v110\n\t"
        "buffer_load_dwordx4 v[104:107], v120, s[8:11], s26 offen sc1\n\t"
        "v_smfmac_f32_16x16x64_bf16 v[4:7], v[28:31], v[240:247], v110\n\t"
        "v_add_f32_e32 v12, v12, v13\n\t"
        "v_add_f32_e32 v14, v14, v15\n\t"
        "v_cvt_pk_bf16_f32 v12, v12, v14\n\t"
        "v_add_f32_e32 v8, v8, v9\n\t"
        "v_add_f32_e32 v10, v10, v11\n\t"
        "v_cvt_pk_bf16_f32 v8, v8, v10\n\t"
        "ds_write2_b32 v118, v8, v12 offset0:64 offset1:96\n\t"
        "v_add_f32_e32 v0, v0, v1\n\t"
        "v_add_f32_e32 v2, v2, v3\n\t"
        "v_cvt_pk_bf16_f32 v0, v0, v2\n\t"
        "v_add_f32_e32 v4, v4, v5\n\t"
        "v_add_f32_e32 v6, v6, v7\n\t"
        "v_cvt_pk_bf16_f32 v4, v4, v6\n\t"
        "ds_write2_b32 v118, v0, v4 offset0:0 offset1:32\n\t"
        "s_cmp_eq_u32 s24, 63\n\t"
        "s_waitcnt lgkmcnt(0)\n\t"
        "s_barrier\n\t"
        "s_cbranch_scc1 LA_skip%=\n\t"
        "ds_read_b64_tr_b16 v[16:17], v119 offset:0\n\t"
        "ds_read_b64_tr_b16 v[18:19], v119 offset:32\n\t"
        "ds_read_b64_tr_b16 v[20:21], v119 offset:256\n\t"
        "ds_read_b64_tr_b16 v[22:23], v119 offset:288\n\t"
        "ds_read_b64_tr_b16 v[24:25], v119 offset:512\n\t"
        "ds_read_b64_tr_b16 v[26:27], v119 offset:544\n\t"
        "ds_read_b64_tr_b16 v[28:29], v119 offset:768\n\t"
        "ds_read_b64_tr_b16 v[30:31], v119 offset:800\n\t"
        "buffer_store_dword v126, v127, s[12:15], 0 offen sc1\n\t"
        "s_waitcnt lgkmcnt(6)\n\t"
        "v_mfma_f32_16x16x32_bf16 v[0:3], v[32:35], v[16:19], 0\n\t"
        "ds_read_b64_tr_b16 v[32:33], v119 offset:2112\n\t"
        "ds_read_b64_tr_b16 v[34:35], v119 offset:2144\n\t"
        "v_mfma_f32_16x16x32_bf16 v[4:7], v[36:39], v[16:19], 0\n\t"
        "ds_read_b64_tr_b16 v[36:37], v119 offset:2368\n\t"
        "ds_read_b64_tr_b16 v[38:39], v119 offset:2400\n\t"
        "s_waitcnt lgkmcnt(8)\n\t"
        "v_mfma_f32_16x16x32_bf16 v[0:3], v[40:43], v[20:23], v[0:3]\n\t"
        "ds_read_b64_tr_b16 v[40:41], v119 offset:2624\n\t"
        "ds_read_b64_tr_b16 v[42:43], v119 offset:2656\n\t"
        "v_mfma_f32_16x16x32_bf16 v[4:7], v[44:47], v[20:23], v[4:7]\n\t"
        "ds_read_b64_tr_b16 v[44:45], v119 offset:2880\n\t"
        "ds_read_b64_tr_b16 v[46:47], v119 offset:2912\n\t"
        "s_waitcnt lgkmcnt(10)\n\t"
        "v_mfma_f32_16x16x32_bf16 v[0:3], v[48:51], v[24:27], v[0:3]\n\t"
        "ds_read_b128 v[48:51], v108 offset:16384\n\t"
        "v_mfma_f32_16x16x32_bf16 v[4:7], v[52:55], v[24:27], v[4:7]\n\t"
        "ds_read_b128 v[52:55], v108 offset:17408\n\t"
        "s_waitcnt lgkmcnt(10)\n\t"
        "v_mfma_f32_16x16x32_bf16 v[0:3], v[56:59], v[28:31], v[0:3]\n\t"
        "v_mfma_f32_16x16x32_bf16 v[4:7], v[60:63], v[28:31], v[4:7]\n\t"
        "s_waitcnt lgkmcnt(2)\n\t"
        "v_mfma_f32_16x16x32_bf16 v[8:11], v[32:35], v[16:19], 0\n\t"
        "s_nop 1\n\t"
        "v_mfma_f32_16x16x32_bf16 v[8:11], v[36:39], v[20:23], v[8:11]\n\t"
        "v_mov_b64_e32 v[16:17], s[0:1]\n\t"
        "v_mov_b64_e32 v[18:19], s[0:1]\n\t"
        "v_mfma_f32_16x16x32_bf16 v[8:11], v[40:43], v[24:27], v[8:11]\n\t"
        "v_max3_f32 v59, v0, v1, v2\n\t"
        "v_max3_f32 v59, v59, v3, v4\n\t"
        "v_mfma_f32_16x16x32_bf16 v[8:11], v[44:47], v[28:31], v[8:11]\n\t"
        "v_max3_f32 v59, v59, v5, v6\n\t"
        "v_max_f32_e32 v60, v59, v7\n\t"
        "v_max_f32_e32 v59, v59, v7\n\t"
        "v_cndmask_b32_e64 v0, v0, v4, s[38:39]\n\t"
        "v_cndmask_b32_e64 v1, v1, v5, s[38:39]\n\t"
        "v_permlane32_swap_b32_e32 v59, v60\n\t"
        "v_cndmask_b32_e64 v2, v2, v6, s[38:39]\n\t"
        "v_cndmask_b32_e64 v3, v3, v7, s[38:39]\n\t"
        "v_max3_f32 v59, v59, v60, v8\n\t"
        "v_max3_f32 v59, v59, v9, v10\n\t"
        "v_max_f32_e32 v60, v59, v11\n\t"
        "v_max_f32_e32 v59, v59, v11\n\t"
        "v_cndmask_b32_e64 v8, v8, v9, s[38:39]\n\t"
        "v_cndmask_b32_e64 v10, v10, v11, s[38:39]\n\t"
        "v_permlane16_swap_b32_e32 v59, v60\n\t"
        "v_max_f32_e32 v59, v59, v60\n\t"
        "v_mul_f32_e32 v59, 0xbe02940c, v59\n\t"
        "v_cndmask_b32_e64 v58, v8, v10, s[40:41]\n\t"
        "v_fmamk_f32 v0, v0, 0x3e02940c, v59\n\t"
        "v_exp_f32_e32 v0, v0\n\t"
        "v_fmamk_f32 v1, v1, 0x3e02940c, v59\n\t"
        "v_exp_f32_e32 v1, v1\n\t"
        "v_fmamk_f32 v2, v2, 0x3e02940c, v59\n\t"
        "v_exp_f32_e32 v2, v2\n\t"
        "v_fmamk_f32 v3, v3, 0x3e02940c, v59\n\t"
        "v_exp_f32_e32 v3, v3\n\t"
        "v_fmamk_f32 v58, v58, 0x3e02940c, v59\n\t"
        "v_cvt_pk_bf16_f32 v12, v0, v1\n\t"
        "v_cvt_pk_bf16_f32 v13, v2, v3\n\t"
        "v_exp_f32_e32 v58, v58\n\t"
        "s_waitcnt lgkmcnt(0)\n\t"
        "v_mov_b32_dpp v14, v12 row_ror:8 row_mask:0xf bank_mask:0xf\n\t"
        "v_mov_b32_dpp v15, v13 row_ror:8 row_mask:0xf bank_mask:0xf\n\t"
        "v_mov_b32_dpp v59, v58 row_ror:8 row_mask:0xf bank_mask:0xf\n\t"
        "v_cvt_pk_bf16_f32 v59, v58, v59\n\t"
        "v_mfma_f32_16x16x32_bf16 v[20:23], v[16:19], v[12:15], 0\n\t"
        "v_cndmask_b32_e64 v56, v59, 0, s[40:41]\n\t"
        "v_cndmask_b32_e64 v57, 0, v59, s[40:41]\n\t"
        "v_mfma_f32_16x16x32_bf16 v[4:7], v[48:51], v[12:15], 0\n\t"
        "s_waitcnt vmcnt(1)\n\t"
        "v_max3_u32 v111, v104, v105, v106\n\t"
        "v_mfma_f32_16x16x32_bf16 v[8:11], v[52:55], v[12:15], 0\n\t"
        "v_max_u32_e32 v111, v111, v107\n\t"
        "v_mfma_f32_16x16x16_bf16 v[20:23], v[16:17], v[56:57], v[20:23]\n\t"
        "v_cmp_eq_u32_e32 vcc, -1, v111\n\t"
        "v_mfma_f32_16x16x16_bf16 v[4:7], v[104:105], v[56:57], v[4:7]\n\t"
        "s_nop 1\n\t"
        "v_mfma_f32_16x16x16_bf16 v[8:11], v[106:107], v[56:57], v[8:11]\n\t"
        "s_cbranch_vccnz LA_vmfail%=\n\t"
        "LA_vmok%=:\n\t"
        "s_nop 1\n\t"
        "v_rcp_f32_e32 v59, v20\n\t"
        "s_nop 0\n\t"
        "v_pk_mul_f32 v[4:5], v[4:5], v[58:59] op_sel:[0,1] op_sel_hi:[1,1]\n\t"
        "v_cvt_pk_bf16_f32 v0, v4, v5\n\t"
        "v_pk_mul_f32 v[6:7], v[6:7], v[58:59] op_sel:[0,1] op_sel_hi:[1,1]\n\t"
        "v_cvt_pk_bf16_f32 v1, v6, v7\n\t"
        "v_pk_mul_f32 v[8:9], v[8:9], v[58:59] op_sel:[0,1] op_sel_hi:[1,1]\n\t"
        "v_cvt_pk_bf16_f32 v2, v8, v9\n\t"
        "v_pk_mul_f32 v[10:11], v[10:11], v[58:59] op_sel:[0,1] op_sel_hi:[1,1]\n\t"
        "v_cvt_pk_bf16_f32 v3, v10, v11\n\t"
        "buffer_store_dwordx4 v[0:3], v121, s[8:11], s26 offen sc1\n\t"
        "LA_x%=:\n\t"
        "s_memrealtime s[52:53]\n\t"
        "ds_read_b128 v[16:19], v109 offset:8192\n\t"
        "ds_read_b128 v[20:23], v109 offset:9216\n\t"
        "ds_read_b128 v[24:27], v109 offset:10240\n\t"
        "ds_read_b128 v[28:31], v109 offset:11264\n\t"
        "ds_read_b128 v[56:59], v124 offset:0\n\t"
        "ds_read_b128 v[60:63], v124 offset:4096\n\t"
        "ds_read_b128 v[12:15], v124 offset:1024\n\t"
        "v_xor_b32_e32 v126, 0x800, v108\n\t"
        "s_add_u32 s48, s24, 1\n\t"
        "s_min_u32 s48, s48, 63\n\t"
        "buffer_load_dwordx2 v[108:109], v125, s[8:11], s26 offen sc1\n\t"
        "s_lshl_b32 s35, s44, 10\n\t"
        "s_lshr_b32 s34, s48, 1\n\t"
        "s_add_u32 s34, s34, 1\n\t"
        "s_min_u32 s34, s34, 31\n\t"
        "s_cmp_gt_u32 s54, s34\n\t"
        "s_cselect_b32 s35, 0xf0000000, s35\n\t"
        "v_lshlrev_b32_e32 v111, 2, v122\n\t"
        "v_and_b32_e32 v111, 0x3c0, v111\n\t"
        "buffer_load_dword v111, v111, s[12:15], s35 offen offset:0 sc1\n\t"
        "s_waitcnt lgkmcnt(6)\n\t"
        "v_mfma_f32_16x16x32_bf16 v[8:11], v[32:35], v[16:19], 0\n\t"
        "ds_read_b128 v[32:35], v124 offset:5120\n\t"
        "s_waitcnt lgkmcnt(6)\n\t"
        "v_mfma_f32_16x16x32_bf16 v[8:11], v[36:39], v[20:23], v[8:11]\n\t"
        "ds_read_b128 v[36:39], v124 offset:2048\n\t"
        "s_waitcnt lgkmcnt(6)\n\t"
        "v_mfma_f32_16x16x32_bf16 v[8:11], v[40:43], v[24:27], v[8:11]\n\t"
        "ds_read_b128 v[40:43], v124 offset:6144\n\t"
        "s_waitcnt lgkmcnt(6)\n\t"
        "v_mfma_f32_16x16x32_bf16 v[8:11], v[44:47], v[28:31], v[8:11]\n\t"
        "ds_read_b128 v[44:47], v124 offset:3072\n\t"
        "s_add_u32 s35, s24, 1\n\t"
        "s_waitcnt lgkmcnt(6)\n\t"
        "v_mfma_f32_16x16x32_bf16 v[0:3], v[56:59], v[16:19], 0\n\t"
        "ds_read_b128 v[56:59], v124 offset:7168\n\t"
        "s_waitcnt lgkmcnt(6)\n\t"
        "v_mfma_f32_16x16x32_bf16 v[4:7], v[60:63], v[16:19], 0\n\t"
        "s_and_b32 s36, s35, 1\n\t"
        "s_mul_i32 s36, s36, 24576\n\t"
        "s_waitcnt lgkmcnt(5)\n\t"
        "v_mfma_f32_16x16x32_bf16 v[0:3], v[12:15], v[20:23], v[0:3]\n\t"
        "s_add_u32 s36, s36, s55\n\t"
        "s_lshl_b32 s37, s48, 18\n\t"
        "s_waitcnt lgkmcnt(4)\n\t"
        "v_mfma_f32_16x16x32_bf16 v[4:7], v[32:35], v[20:23], v[4:7]\n\t"
        "v_xor_b32_e32 v32, 0x200, v120\n\t"
        "buffer_load_dwordx4 v[12:15], v32, s[8:11], s26 offen sc1\n\t"
        "s_waitcnt lgkmcnt(3)\n\t"
        "v_mfma_f32_16x16x32_bf16 v[0:3], v[36:39], v[24:27], v[0:3]\n\t"
        "s_lshl_b32 s34, s43, 11\n\t"
        "s_add_u32 s37, s37, s34\n\t"
        "s_waitcnt lgkmcnt(2)\n\t"
        "v_mfma_f32_16x16x32_bf16 v[4:7], v[40:43], v[24:27], v[4:7]\n\t"
        "v_cvt_pk_bf16_f32 v8, 0, v8\n\t"
        "v_cvt_pk_bf16_f32 v9, 0, v9\n\t"
        "v_cvt_pk_bf16_f32 v10, 0, v10\n\t"
        "v_cvt_pk_bf16_f32 v11, 0, v11\n\t"
        "s_waitcnt lgkmcnt(1)\n\t"
        "v_mfma_f32_16x16x32_bf16 v[0:3], v[44:47], v[28:31], v[0:3]\n\t"
        "v_mul_f32_e32 v8, 0x3db504f3, v8\n\t"
        "v_mul_f32_e32 v9, 0x3db504f3, v9\n\t"
        "v_mul_f32_e32 v10, 0x3db504f3, v10\n\t"
        "v_mul_f32_e32 v11, 0x3db504f3, v11\n\t"
        "s_waitcnt lgkmcnt(0)\n\t"
        "v_mfma_f32_16x16x32_bf16 v[4:7], v[56:59], v[28:31], v[4:7]\n\t"
        "v_cvt_pk_bf16_f32 v8, 0, v8\n\t"
        "v_cvt_pk_bf16_f32 v9, 0, v9\n\t"
        "v_cvt_pk_bf16_f32 v10, 0, v10\n\t"
        "v_cvt_pk_bf16_f32 v11, 0, v11\n\t"
        "ds_read_b128 v[56:59], v126 offset:16384\n\t"
        "ds_read_b128 v[60:63], v126 offset:17408\n\t"
        "v_mov_b64_e32 v[36:37], -1\n\t"
        "v_mov_b64_e32 v[38:39], -1\n\t"
        "v_mov_b32_e32 v40, 0xce6e6b28\n\t"
        "v_mov_b32_e32 v46, 0x3db504f3\n\t"
        "v_mov_b32_e32 v47, 0x3fb8aa3b\n\t"
        "v_cvt_pk_bf16_f32 v0, 0, v0\n\t"
        "v_cvt_pk_bf16_f32 v1, 0, v1\n\t"
        "v_cvt_pk_bf16_f32 v2, 0, v2\n\t"
        "v_cvt_pk_bf16_f32 v3, 0, v3\n\t"
        "v_cvt_pk_bf16_f32 v4, 0, v4\n\t"
        "v_cvt_pk_bf16_f32 v5, 0, v5\n\t"
        "v_cvt_pk_bf16_f32 v6, 0, v6\n\t"
        "v_cvt_pk_bf16_f32 v7, 0, v7\n\t"
        "v_pk_mul_f32 v[0:1], v[0:1], v[46:47] op_sel_hi:[1,0]\n\t"
        "v_pk_mul_f32 v[2:3], v[2:3], v[46:47] op_sel_hi:[1,0]\n\t"
        "v_pk_mul_f32 v[4:5], v[4:5], v[46:47] op_sel_hi:[1,0]\n\t"
        "v_pk_mul_f32 v[6:7], v[6:7], v[46:47] op_sel_hi:[1,0]\n\t"
        "s_lshr_b32 s59, s48, 1\n\t"
        "s_cmp_gt_u32 s54, s59\n\t"
        "s_cbranch_scc1 LA_kvok%=\n\t"
        "s_mov_b32 s42, 0\n\t"
        "s_waitcnt vmcnt(1)\n\t"
        "s_branch LA_kvw%=\n\t"
        "LA_kv%=:\n\t"
        "s_waitcnt vmcnt(0)\n\t"
        "LA_kvw%=:\n\t"
        "v_min_u32_dpp v111, v111, v111 quad_perm:[1,0,3,2] row_mask:0xf bank_mask:0xf\n\t"
        "s_nop 1\n\t"
        "v_min_u32_dpp v111, v111, v111 quad_perm:[2,3,0,1] row_mask:0xf bank_mask:0xf\n\t"
        "s_nop 1\n\t"
        "v_min_u32_dpp v111, v111, v111 row_ror:4 row_mask:0xf bank_mask:0xf\n\t"
        "s_nop 1\n\t"
        "v_min_u32_dpp v111, v111, v111 row_ror:8 row_mask:0xf bank_mask:0xf\n\t"
        "s_nop 0\n\t"
        "v_readfirstlane_b32 s54, v111\n\t"
        "s_nop 0\n\t"
        "s_cmp_gt_u32 s54, s59\n\t"
        "s_cbranch_scc1 LA_kvok%=\n\t"
        "s_sleep 4\n\t"
        "s_lshl_b32 s35, s44, 10\n\t"
        "v_lshlrev_b32_e32 v111, 2, v122\n\t"
        "v_and_b32_e32 v111, 0x3c0, v111\n\t"
        "buffer_load_dword v111, v111, s[12:15], s35 offen offset:0 sc1\n\t"
        "s_add_u32 s42, s42, 1\n\t"
        "s_cmp_lt_u32 s42, 0x100000\n\t"
        "s_cbranch_scc1 LA_kv%=\n\t"
        "s_movk_i32 s34, 16\n\t"
        "s_branch LA_die%=\n\t"
        "LA_kvok%=:\n\t"
        "s_add_u32 m0, s36, 0\n\t"
        "s_add_u32 s34, s37, 29548544\n\t"
        "buffer_load_dwordx4 v122, s[12:15], s34 offen sc1 lds\n\t"
        "v_cvt_pk_bf16_f32 v0, 0, v0\n\t"
        "v_cvt_pk_bf16_f32 v1, 0, v1\n\t"
        "v_cvt_pk_bf16_f32 v2, 0, v2\n\t"
        "v_cvt_pk_bf16_f32 v3, 0, v3\n\t"
        "v_cvt_pk_bf16_f32 v4, 0, v4\n\t"
        "v_cvt_pk_bf16_f32 v5, 0, v5\n\t"
        "v_cvt_pk_bf16_f32 v6, 0, v6\n\t"
        "v_cvt_pk_bf16_f32 v7, 0, v7\n\t"
        "s_add_u32 m0, s36, 1024\n\t"
        "s_add_u32 s34, s37, 29549568\n\t"
        "buffer_load_dwordx4 v122, s[12:15], s34 offen sc1 lds\n\t"
        "v_cndmask_b32_e64 v0, v40, v0, s[60:61]\n\t"
        "v_cndmask_b32_e64 v1, v40, v1, s[62:63]\n\t"
        "v_cndmask_b32_e64 v2, v40, v2, s[64:65]\n\t"
        "v_cndmask_b32_e64 v3, v40, v3, s[66:67]\n\t"
        "v_cndmask_b32_e64 v4, v40, v4, s[68:69]\n\t"
        "v_cndmask_b32_e64 v5, v40, v5, s[70:71]\n\t"
        "v_cndmask_b32_e64 v6, v40, v6, s[72:73]\n\t"
        "v_cndmask_b32_e64 v7, v40, v7, s[74:75]\n\t"
        "s_add_u32 m0, s36, 8192\n\t"
        "s_add_u32 s34, s37, 12771328\n\t"
        "buffer_load_dwordx4 v122, s[12:15], s34 offen sc1 lds\n\t"
        "v_max3_f32 v42, v0, v1, v2\n\t"
        "v_max3_f32 v42, v42, v3, v4\n\t"
        "v_max3_f32 v42, v42, v5, v6\n\t"
        "v_max3_f32 v42, v42, v7, v8\n\t"
        "v_max3_f32 v42, v42, v9, v10\n\t"
        "v_mov_b32_e32 v43, v11\n\t"
        "v_max_f32_e32 v44, v42, v43\n\t"
        "v_max_f32_e32 v42, v42, v43\n\t"
        "s_pack_ll_b32_b16 s34, s52, s24\n\t"
        "v_mov_b32_e32 v40, s34\n\t"
        "s_add_u32 s35, s26, s57\n\t"
        "v_permlane16_swap_b32_e32 v42, v44\n\t"
        "v_max_f32_e32 v45, v42, v44\n\t"
        "v_max_f32_e32 v42, v42, v44\n\t"
        "s_mov_b64 exec, 1\n\t"
        "buffer_store_dword v40, off, s[8:11], s35 sc1\n\t"
        "s_mov_b64 exec, -1\n\t"
        "v_permlane32_swap_b32_e32 v42, v45\n\t"
        "v_max_f32_e32 v42, v42, v45\n\t"
        "v_mul_f32_e32 v42, 0xbfb8aa3b, v42\n\t"
        "v_pk_fma_f32 v[0:1], v[0:1], v[46:47], v[42:43] op_sel:[0,1,0] op_sel_hi:[1,1,0]\n\t"
        "s_add_u32 m0, s36, 9216\n\t"
        "s_add_u32 s34, s37, 12772352\n\t"
        "buffer_load_dwordx4 v122, s[12:15], s34 offen sc1 lds\n\t"
        "v_exp_f32_e32 v0, v0\n\t"
        "v_exp_f32_e32 v1, v1\n\t"
        "v_pk_fma_f32 v[2:3], v[2:3], v[46:47], v[42:43] op_sel:[0,1,0] op_sel_hi:[1,1,0]\n\t"
        "v_exp_f32_e32 v2, v2\n\t"
        "v_exp_f32_e32 v3, v3\n\t"
        "v_pk_fma_f32 v[4:5], v[4:5], v[46:47], v[42:43] op_sel:[0,1,0] op_sel_hi:[1,1,0]\n\t"
        "v_exp_f32_e32 v4, v4\n\t"
        "s_add_u32 m0, s36, 16384\n\t"
        "s_add_u32 s34, s37, 46325760\n\t"
        "buffer_load_dwordx4 v122, s[12:15], s34 offen sc1 lds\n\t"
        "v_exp_f32_e32 v5, v5\n\t"
        "v_pk_fma_f32 v[6:7], v[6:7], v[46:47], v[42:43] op_sel:[0,1,0] op_sel_hi:[1,1,0]\n\t"
        "v_exp_f32_e32 v6, v6\n\t"
        "v_exp_f32_e32 v7, v7\n\t"
        "v_pk_fma_f32 v[8:9], v[8:9], v[46:47], v[42:43] op_sel:[0,1,0] op_sel_hi:[1,1,0]\n\t"
        "v_exp_f32_e32 v8, v8\n\t"
        "v_exp_f32_e32 v9, v9\n\t"
        "s_add_u32 m0, s36, 17408\n\t"
        "s_add_u32 s34, s37, 46326784\n\t"
        "buffer_load_dwordx4 v122, s[12:15], s34 offen sc1 lds\n\t"
        "v_pk_fma_f32 v[10:11], v[10:11], v[46:47], v[42:43] op_sel:[0,1,0] op_sel_hi:[1,1,0]\n\t"
        "v_exp_f32_e32 v10, v10\n\t"
        "v_exp_f32_e32 v11, v11\n\t"
        "v_pk_add_f32 v[42:43], v[0:1], v[2:3]\n\t"
        "v_cndmask_b32_e64 v8, v8, 0, s[40:41]\n\t"
        "v_cndmask_b32_e64 v9, v9, 0, s[40:41]\n\t"
        "v_cndmask_b32_e64 v10, v10, 0, s[40:41]\n\t"
        "buffer_store_dwordx4 v[36:39], v121, s[8:11], s28 offen sc1\n\t"
        "v_cndmask_b32_e64 v11, v11, 0, s[40:41]\n\t"
        "v_pk_add_f32 v[42:43], v[42:43], v[4:5]\n\t"
        "v_pk_add_f32 v[42:43], v[42:43], v[6:7]\n\t"
        "v_pk_add_f32 v[42:43], v[42:43], v[8:9]\n\t"
        "v_pk_add_f32 v[42:43], v[42:43], v[10:11]\n\t"
        "v_add_f32_e32 v44, v42, v43\n\t"
        "v_add_f32_e32 v42, v42, v43\n\t"
        "s_lshl_b32 s34, s24, 18\n\t"
        "s_add_u32 s34, s34, 63102976\n\t"
        "v_permlane16_swap_b32_e32 v42, v44\n\t"
        "v_add_f32_e32 v45, v42, v44\n\t"
        "v_add_f32_e32 v42, v42, v44\n\t"
        "s_add_u32 s35, s24, 1\n\t"
        "v_mov_b32_e32 v126, s35\n\t"
        "v_permlane32_swap_b32_e32 v42, v45\n\t"
        "v_add_f32_e32 v42, v42, v45\n\t"
        "v_rcp_f32_e32 v42, v42\n\t"
        "s_waitcnt vmcnt(8) lgkmcnt(0)\n\t"
        "v_max3_u32 v45, v12, v13, v14\n\t"
        "v_max3_u32 v45, v45, v15, v104\n\t"
        "v_max3_u32 v45, v45, v105, v106\n\t"
        "v_max_u32_e32 v45, v45, v107\n\t"
        "v_cmp_eq_u32_e32 vcc, -1, v45\n\t"
        "s_nop 0\n\t"
        "s_cbranch_vccnz LA_v2fail%=\n\t"
        "LA_v2ok%=:\n\t"
        "v_and_b32_e32 v45, 0xff, v109\n\t"
        "v_cmp_ne_u32_e32 vcc, s24, v45\n\t"
        "s_nop 0\n\t"
        "s_cbranch_vccnz LA_stre%=\n\t"
        "LA_stok%=:\n\t"
        "v_pk_mul_f32 v[0:1], v[0:1], v[42:43] op_sel_hi:[1,0]\n\t"
        "v_subrev_u32_e32 v108, s25, v108\n\t"
        "v_min_u32_dpp v111, v111, v111 quad_perm:[1,0,3,2] row_mask:0xf bank_mask:0xf\n\t"
        "v_cvt_pk_bf16_f32 v32, v0, v1\n\t"
        "v_pk_mul_f32 v[2:3], v[2:3], v[42:43] op_sel_hi:[1,0]\n\t"
        "v_cvt_pk_bf16_f32 v33, v2, v3\n\t"
        "v_max_i32_dpp v108, v108, v108 quad_perm:[1,0,3,2] row_mask:0xf bank_mask:0xf\n\t"
        "v_min_u32_dpp v111, v111, v111 quad_perm:[2,3,0,1] row_mask:0xf bank_mask:0xf\n\t"
        "v_pk_mul_f32 v[4:5], v[4:5], v[42:43] op_sel_hi:[1,0]\n\t"
        "v_cvt_pk_bf16_f32 v34, v4, v5\n\t"
        "v_pk_mul_f32 v[6:7], v[6:7], v[42:43] op_sel_hi:[1,0]\n\t"
        "v_max_i32_dpp v108, v108, v108 quad_perm:[2,3,0,1] row_mask:0xf bank_mask:0xf\n\t"
        "v_min_u32_dpp v111, v111, v111 row_ror:4 row_mask:0xf bank_mask:0xf\n\t"
        "v_cvt_pk_bf16_f32 v35, v6, v7\n\t"
        "v_pk_mul_f32 v[8:9], v[8:9], v[42:43] op_sel_hi:[1,0]\n\t"
        "v_cvt_pk_bf16_f32 v36, v8, v9\n\t"
        "v_max_i32_dpp v108, v108, v108 row_ror:4 row_mask:0xf bank_mask:0xf\n\t"
        "v_min_u32_dpp v111, v111, v111 row_ror:8 row_mask:0xf bank_mask:0xf\n\t"
        "v_pk_mul_f32 v[10:11], v[10:11], v[42:43] op_sel_hi:[1,0]\n\t"
        "v_cvt_pk_bf16_f32 v37, v10, v11\n\t"
        "v_mfma_f32_16x16x32_bf16 v[16:19], v[48:51], v[32:35], 0\n\t"
        "v_max_i32_dpp v108, v108, v108 row_ror:8 row_mask:0xf bank_mask:0xf\n\t"
        "v_mfma_f32_16x16x32_bf16 v[20:23], v[52:55], v[32:35], 0\n\t"
        "v_mfma_f32_16x16x32_bf16 v[24:27], v[56:59], v[32:35], 0\n\t"
        "v_mfma_f32_16x16x32_bf16 v[28:31], v[60:63], v[32:35], 0\n\t"
        "v_max_i32_dpp v108, v108, v108 row_bcast:15 row_mask:0xa bank_mask:0xf\n\t"
        "v_xor_b32_e32 v45, 0x400, v123\n\t"
        "v_add_u32_e32 v124, s56, v124\n\t"
        "v_mfma_f32_16x16x16_bf16 v[16:19], v[104:105], v[36:37], v[16:19]\n\t"
        "v_max_i32_dpp v108, v108, v108 row_bcast:31 row_mask:0xc bank_mask:0xf\n\t"
        "v_mfma_f32_16x16x16_bf16 v[20:23], v[106:107], v[36:37], v[20:23]\n\t"
        "v_mfma_f32_16x16x16_bf16 v[24:27], v[12:13], v[36:37], v[24:27]\n\t"
        "v_mfma_f32_16x16x16_bf16 v[28:31], v[14:15], v[36:37], v[28:31]\n\t"
        "ds_read_b128 v[48:51], v117 offset:4096\n\t"
        "ds_read_b128 v[52:55], v117 offset:5120\n\t"
        "ds_read_b128 v[56:59], v117 offset:6144\n\t"
        "ds_read_b128 v[60:63], v117 offset:7168\n\t"
        "s_sub_u32 s56, 0, s56\n\t"
        "v_cvt_pk_bf16_f32 v0, v16, v17\n\t"
        "v_cvt_pk_bf16_f32 v1, v18, v19\n\t"
        "v_cvt_pk_bf16_f32 v2, v20, v21\n\t"
        "v_cvt_pk_bf16_f32 v3, v22, v23\n\t"
        "buffer_store_dwordx4 v[0:3], v123, s[12:15], s34 offen sc1\n\t"
        "v_cvt_pk_bf16_f32 v4, v24, v25\n\t"
        "v_cvt_pk_bf16_f32 v5, v26, v27\n\t"
        "v_cvt_pk_bf16_f32 v6, v28, v29\n\t"
        "v_cvt_pk_bf16_f32 v7, v30, v31\n\t"
        "buffer_store_dwordx4 v[4:7], v45, s[12:15], s34 offen sc1\n\t"
        "s_cmp_eq_u32 s24, 63\n\t"
        "s_cbranch_scc1 LA_last%=\n\t"
        "ds_read_b128 v[32:35], v117 offset:0\n\t"
        "ds_read_b128 v[36:39], v117 offset:1024\n\t"
        "ds_read_b128 v[40:43], v117 offset:2048\n\t"
        "ds_read_b128 v[44:47], v117 offset:3072\n\t"
        "v_mov_b64_e32 v[14:15], 0\n\t"
        "v_mov_b64_e32 v[12:13], 0\n\t"
        "v_mov_b64_e32 v[10:11], 0\n\t"
        "v_mov_b64_e32 v[8:9], 0\n\t"
        "v_mov_b64_e32 v[6:7], 0\n\t"
        "v_mov_b64_e32 v[4:5], 0\n\t"
        "v_mov_b64_e32 v[2:3], 0\n\t"
        "v_mov_b64_e32 v[0:1], 0\n\t"
        "v_readfirstlane_b32 s34, v111\n\t"
        "s_nop 0\n\t"
        "s_max_u32 s54, s54, s34\n\t"
        "v_readfirstlane_b32 s35, v109\n\t"
        "v_readlane_b32 s36, v108, 63\n\t"
        "s_bfe_u32 s29, s35, 0xc0008\n\t"
        "s_lshr_b32 s30, s35, 20\n\t"
        "s_add_u32 s35, s31, s50\n\t"
        "s_mul_i32 s37, s50, 3\n\t"
        "s_min_u32 s35, s35, s37\n\t"
        "s_add_i32 s34, s36, s51\n\t"
        "s_cmp_gt_i32 s34, 0\n\t"
        "s_cselect_b32 s31, s35, 0\n\t"
        "s_cselect_b32 s34, s34, 0\n\t"
        "s_add_u32 s34, s34, s31\n\t"
        "s_add_u32 s37, s25, s34\n\t"
        "s_add_u32 s45, s37, s29\n\t"
        "s_add_u32 s25, s45, s30\n\t"
        "s_add_u32 s24, s24, 1\n\t"
        "s_mov_b32 s28, s26\n\t"
        "s_mov_b32 s26, s27\n\t"
        "s_add_u32 s27, s27, 0x80000\n\t"
        "s_and_b32 s27, s27, 0x1fffff\n\t"
        "s_branch LA_loop%=\n\t"
        "LA_last%=:\n\t"
        "s_waitcnt vmcnt(0)\n\t"
        "s_barrier\n\t"
        "buffer_store_dword v126, v127, s[12:15], 0 offen sc1\n\t"
        "s_endpgm\n\t"
        "LA_skip%=:\n\t"
        "buffer_store_dword v126, v127, s[12:15], 0 offen sc1\n\t"
        "ds_read_b64_tr_b16 v[32:33], v119 offset:2112\n\t"
        "ds_read_b64_tr_b16 v[34:35], v119 offset:2144\n\t"
        "ds_read_b64_tr_b16 v[36:37], v119 offset:2368\n\t"
        "ds_read_b64_tr_b16 v[38:39], v119 offset:2400\n\t"
        "ds_read_b64_tr_b16 v[40:41], v119 offset:2624\n\t"
        "ds_read_b64_tr_b16 v[42:43], v119 offset:2656\n\t"
        "ds_read_b64_tr_b16 v[44:45], v119 offset:2880\n\t"
        "ds_read_b64_tr_b16 v[46:47], v119 offset:2912\n\t"
        "ds_read_b128 v[48:51], v108 offset:16384\n\t"
        "ds_read_b128 v[52:55], v108 offset:17408\n\t"
        "s_waitcnt lgkmcnt(0)\n\t"
        "s_branch LA_x%=\n\t"
        "LA_stre%=:\n\t"
        "s_mov_b32 s42, 0\n\t"
        "LA_str2%=:\n\t"
        "buffer_load_dwordx2 v[108:109], v125, s[8:11], s26 offen sc1\n\t"
        "s_waitcnt vmcnt(0)\n\t"
        "v_and_b32_e32 v45, 0xff, v109\n\t"
        "v_cmp_ne_u32_e32 vcc, s24, v45\n\t"
        "s_nop 0\n\t"
        "s_cbranch_vccz LA_stok%=\n\t"
        "s_add_u32 s42, s42, 1\n\t"
        "s_cmp_lt_u32 s42, 0x100000\n\t"
        "s_cbranch_scc1 LA_str2%=\n\t"
        "s_movk_i32 s34, 5\n\t"
        "s_branch LA_die%=\n\t"
        "LA_v2fail%=:\n\t"
        "s_mov_b32 s42, 0\n\t"
        "LA_v2re%=:\n\t"
        "v_xor_b32_e32 v45, 0x200, v120\n\t"
        "buffer_load_dwordx4 v[12:15], v45, s[8:11], s26 offen sc1\n\t"
        "buffer_load_dwordx4 v[104:107], v120, s[8:11], s26 offen sc1\n\t"
        "s_waitcnt vmcnt(0)\n\t"
        "v_max3_u32 v45, v12, v13, v14\n\t"
        "v_max3_u32 v45, v45, v15, v104\n\t"
        "v_max3_u32 v45, v45, v105, v106\n\t"
        "v_max_u32_e32 v45, v45, v107\n\t"
        "v_cmp_eq_u32_e32 vcc, -1, v45\n\t"
        "s_nop 0\n\t"
        "s_cbranch_vccz LA_v2ok%=\n\t"
        "s_add_u32 s42, s42, 1\n\t"
        "s_cmp_lt_u32 s42, 0x100000\n\t"
        "s_cbranch_scc1 LA_v2re%=\n\t"
        "s_movk_i32 s34, 6\n\t"
        "s_branch LA_die%=\n\t"
        "LA_rdfail%=:\n\t"
        "s_or_b32 s47, s47, 1\n\t"
        "buffer_load_dwordx4 v[16:19], v112, s[8:11], s26 offen offset:0 sc1\n\t"
        "buffer_load_dwordx4 v[20:23], v112, s[8:11], s26 offen offset:1024 sc1\n\t"
        "buffer_load_dwordx4 v[24:27], v112, s[8:11], s26 offen offset:2048 sc1\n\t"
        "buffer_load_dwordx4 v[28:31], v112, s[8:11], s26 offen offset:3072 sc1\n\t"
        "s_nop 7\n\t"
        "v_mov_b64_e32 v[0:1], 0\n\t"
        "v_mov_b64_e32 v[2:3], 0\n\t"
        "v_mov_b64_e32 v[4:5], 0\n\t"
        "v_mov_b64_e32 v[6:7], 0\n\t"
        "v_mov_b64_e32 v[8:9], 0\n\t"
        "v_mov_b64_e32 v[10:11], 0\n\t"
        "v_mov_b64_e32 v[12:13], 0\n\t"
        "v_mov_b64_e32 v[14:15], 0\n\t"
        "s_branch LA_rdre%=w\n\t"
        "LA_rdre%=:\n\t"
        "buffer_load_dwordx4 v[16:19], v112, s[8:11], s26 offen offset:0 sc1\n\t"
        "buffer_load_dwordx4 v[20:23], v112, s[8:11], s26 offen offset:1024 sc1\n\t"
        "buffer_load_dwordx4 v[24:27], v112, s[8:11], s26 offen offset:2048 sc1\n\t"
        "buffer_load_dwordx4 v[28:31], v112, s[8:11], s26 offen offset:3072 sc1\n\t"
        "LA_rdre%=w:\n\t"
        "s_waitcnt vmcnt(0) lgkmcnt(0)\n\t"
        "v_max3_u32 v111, v16, v17, v18\n\t"
        "v_max3_u32 v111, v111, v19, v20\n\t"
        "v_max3_u32 v111, v111, v21, v22\n\t"
        "v_max3_u32 v111, v111, v23, v24\n\t"
        "v_max3_u32 v111, v111, v25, v26\n\t"
        "v_max3_u32 v111, v111, v27, v28\n\t"
        "v_max3_u32 v111, v111, v29, v30\n\t"
        "v_max_u32_e32 v111, v111, v31\n\t"
        "v_cmp_eq_u32_e32 vcc, -1, v111\n\t"
        "s_nop 0\n\t"
        "s_cbranch_vccz LA_go%=\n\t"
        "s_add_u32 s42, s42, 1\n\t"
        "s_cmp_lt_u32 s42, 0x100000\n\t"
        "s_cbranch_scc1 LA_rdre%=\n\t"
        "s_movk_i32 s34, 1\n\t"
        "s_branch LA_die%=\n\t"
        "LA_vmfail%=:\n\t"
        "s_or_b32 s47, s47, 2\n\t"
        "s_mov_b32 s42, 0\n\t"
        "LA_vmre%=:\n\t"
        "buffer_load_dwordx4 v[104:107], v120, s[8:11], s26 offen sc1\n\t"
        "v_mfma_f32_16x16x32_bf16 v[4:7], v[48:51], v[12:15], 0\n\t"
        "v_mfma_f32_16x16x32_bf16 v[8:11], v[52:55], v[12:15], 0\n\t"
        "s_nop 7\n\t"
        "s_waitcnt vmcnt(0)\n\t"
        "v_mfma_f32_16x16x16_bf16 v[4:7], v[104:105], v[56:57], v[4:7]\n\t"
        "v_mfma_f32_16x16x16_bf16 v[8:11], v[106:107], v[56:57], v[8:11]\n\t"
        "v_max3_u32 v111, v104, v105, v106\n\t"
        "v_max_u32_e32 v111, v111, v107\n\t"
        "v_cmp_eq_u32_e32 vcc, -1, v111\n\t"
        "s_nop 7\n\t"
        "s_cbranch_vccz LA_vmok%=\n\t"
        "s_add_u32 s42, s42, 1\n\t"
        "s_cmp_lt_u32 s42, 0x100000\n\t"
        "s_cbranch_scc1 LA_vmre%=\n\t"
        "s_movk_i32 s34, 2\n\t"
        "s_branch LA_die%=\n\t"
        "LA_die%=:\n\t"
        "v_mov_b32_e32 v30, s34\n\t"
        "s_mov_b32 s35, 33024\n\t"
        "s_mov_b64 exec, 1\n\t"
        "buffer_store_dword v30, off, s[12:15], s35 sc1\n\t"
        "s_endpgm\n\t"
        ".Lk_end_A:\n\t"
        :: REC_S_IN, "{v96}"(g_w), "{v97}"(wb0), "{v98}"(wb1), "{v99}"(rb0), "{v100}"(rb0 ^ 32u), "{v101}"(rb0 ^ 64u), "{v102}"(rb0 ^ 96u),
           "{v112}"(a_mem), "{v113}"(l_ms + w * 4096), "{v114}"(l_ms + ((w + 1) & 3) * 4096), "{v115}"(l_ms + ((w + 2) & 3) * 4096), "{v116}"(l_ms + ((w + 3) & 3) * 4096),
           "{v117}"(lb + w * 22528 + l * 16), "{v118}"(l_iw), "{v119}"(l_iq), "{v120}"(a_vm), "{v121}"(a_o), "{v122}"(h * 8192 + l * 16), "{v123}"(a_ox), "{v124}"(lb + L_KV + l * 16), "{s55}"(lb + L_KV + w * 2048),
           "{v125}"((24 + cp) * 16384 + l * 32), "{v126}"(16 * (w & 1) + n - 4 * g), "{v103}"((l & 1) ? 0xEEEEEEEEu : 0x44444444u),
           "{v127}"((l == 0 && w == 0) ? F_PROG + h * 128 : OOB)
        : "memory", "vcc", "scc", "v0", "v255", "a0", "a255", "s101");
    __builtin_unreachable();
}
__device__ __forceinline__ void rec_b(const Params& p, unsigned lb, const int wv, const int id, const int rg, const int cp) {
    const int w = wv, l = otid() & 63, n = l & 15, g = l >> 4, cs = id, rb = 32 + id * 2 + rg;
    const v4u sh = mk_srd(p.ws + ho_base(p.ws), HO_BYTES - 8192), sw = mk_srd(p.ws, W_TOTAL), s1 = mk_srd(p.Wv, 1024 * 4096 * 2), s2 = mk_srd(p.Wo, 1024 * 4096 * 2);
    const int r8 = l >> 3, pc = l & 7, pp = n >> 3, bb = (n >> 2) & 1, f = (g & 1) | (pp << 1);
    const unsigned stg = lb + LB_STG + w * LB_STGP;
    const unsigned g_v = r8 * 8192 + cs * 256 + rg * 128 + pc * 16, g_o = (l >> 2) * 2048 + cs * 64 + (l & 3) * 16;
    const unsigned wb0 = stg + r8 * 128 + (((pc >> 1) ^ (r8 >> 2)) << 5) + (pc & 1) * 16, wb1 = wb0 ^ 64u;
    const unsigned rb0 = stg + (16 * (g >> 1) + 8 * pp + 4 * (g & 1) + bb) * 128 + (f << 5) + (n & 3) * 8;
    const unsigned o_r = stg + (g >> 1) * (2048 + 128) + (g & 1) * (1024 + 32) + (bb + 4 * pp) * 64 + (n & 3) * 8;
    const unsigned a_mem = cp * 32768 + (w >> 1) * 16384 + (w & 1) * 4096 + l * 16;
    const unsigned a_vmw = w < 2 ? (16 + (cs >> 2)) * 16384 + (cs & 3) * 2048 + (2 * rg + w) * 512 + (l & 31) * 16 + (g >> 1) * 8 : OOB;
    const unsigned a_or = (8 + rg * 4 + w) * 16384 + l * 16;
    const unsigned a_mw = w * 32768 + (cs >> 4) * 16384 + ((cs >> 1) & 7) * 1024 + (cs & 1) * 512 + (l >> 5) * 256 + rg * 128 + (l & 31) * 4;
    const unsigned a_sw = (w == 0 && l < 4) ? (24 + l) * 16384 + (id * 2 + rg) * 32 : OOB;
    const bf16_t* m0p = p.mem0 + (4 * rg + ((l >> 3) & 3)) * 1024 + cs * 32 + (l >> 5) * 16 + (l & 7) * 2;
    asm volatile(
        "s_nop 4\n\t"
        "global_load_dword v109, v[108:109], off\n\t"
        "s_lshl_b32 s34, s43, 21\n\t"
        "s_add_u32 s34, s34, 0\n\t"
        "buffer_load_dwordx4 v[128:131], v96, s[16:19], s34 offen nt\n\t"
        "s_add_u32 s34, s34, 0x10000\n\t"
        "buffer_load_dwordx4 v[132:135], v96, s[16:19], s34 offen nt\n\t"
        "s_add_u32 s34, s34, 0x10000\n\t"
        "buffer_load_dwordx4 v[136:139], v96, s[16:19], s34 offen nt\n\t"
        "s_add_u32 s34, s34, 0x10000\n\t"
        "buffer_load_dwordx4 v[140:143], v96, s[16:19], s34 offen nt\n\t"
        "s_add_u32 s34, s34, 0x10000\n\t"
        "buffer_load_dwordx4 v[144:147], v96, s[16:19], s34 offen nt\n\t"
        "s_add_u32 s34, s34, 0x10000\n\t"
        "buffer_load_dwordx4 v[148:151], v96, s[16:19], s34 offen nt\n\t"
        "s_add_u32 s34, s34, 0x10000\n\t"
        "buffer_load_dwordx4 v[152:155], v96, s[16:19], s34 offen nt\n\t"
        "s_add_u32 s34, s34, 0x10000\n\t"
        "buffer_load_dwordx4 v[156:159], v96, s[16:19], s34 offen nt\n\t"
        "s_lshl_b32 s34, s43, 21\n\t"
        "s_add_u32 s34, s34, 524288\n\t"
        "buffer_load_dwordx4 v[160:163], v96, s[16:19], s34 offen nt\n\t"
        "s_add_u32 s34, s34, 0x10000\n\t"
        "buffer_load_dwordx4 v[164:167], v96, s[16:19], s34 offen nt\n\t"
        "s_add_u32 s34, s34, 0x10000\n\t"
        "buffer_load_dwordx4 v[168:171], v96, s[16:19], s34 offen nt\n\t"
        "s_add_u32 s34, s34, 0x10000\n\t"
        "buffer_load_dwordx4 v[172:175], v96, s[16:19], s34 offen nt\n\t"
        "s_add_u32 s34, s34, 0x10000\n\t"
        "buffer_load_dwordx4 v[176:179], v96, s[16:19], s34 offen nt\n\t"
        "s_add_u32 s34, s34, 0x10000\n\t"
        "buffer_load_dwordx4 v[180:183], v96, s[16:19], s34 offen nt\n\t"
        "s_add_u32 s34, s34, 0x10000\n\t"
        "buffer_load_dwordx4 v[184:187], v96, s[16:19], s34 offen nt\n\t"
        "s_add_u32 s34, s34, 0x10000\n\t"
        "buffer_load_dwordx4 v[188:191], v96, s[16:19], s34 offen nt\n\t"
        "s_lshl_b32 s34, s43, 21\n\t"
        "s_add_u32 s34, s34, 1048576\n\t"
        "buffer_load_dwordx4 v[192:195], v96, s[16:19], s34 offen nt\n\t"
        "s_add_u32 s34, s34, 0x10000\n\t"
        "buffer_load_dwordx4 v[196:199], v96, s[16:19], s34 offen nt\n\t"
        "s_add_u32 s34, s34, 0x10000\n\t"
        "buffer_load_dwordx4 v[200:203], v96, s[16:19], s34 offen nt\n\t"
        "s_add_u32 s34, s34, 0x10000\n\t"
        "buffer_load_dwordx4 v[204:207], v96, s[16:19], s34 offen nt\n\t"
        "s_add_u32 s34, s34, 0x10000\n\t"
        "buffer_load_dwordx4 v[208:211], v96, s[16:19], s34 offen nt\n\t"
        "s_add_u32 s34, s34, 0x10000\n\t"
        "buffer_load_dwordx4 v[212:215], v96, s[16:19], s34 offen nt\n\t"
        "s_add_u32 s34, s34, 0x10000\n\t"
        "buffer_load_dwordx4 v[216:219], v96, s[16:19], s34 offen nt\n\t"
        "s_add_u32 s34, s34, 0x10000\n\t"
        "buffer_load_dwordx4 v[220:223], v96, s[16:19], s34 offen nt\n\t"
        "s_waitcnt vmcnt(16)\n\t"
        "ds_write_b128 v98, v[128:131] offset:0\n\t"
        "ds_write_b128 v99, v[132:135] offset:1024\n\t"
        "ds_write_b128 v98, v[136:139] offset:2048\n\t"
        "ds_write_b128 v99, v[140:143] offset:3072\n\t"
        "ds_write_b128 v98, v[144:147] offset:4096\n\t"
        "ds_write_b128 v99, v[148:151] offset:5120\n\t"
        "ds_write_b128 v98, v[152:155] offset:6144\n\t"
        "ds_write_b128 v99, v[156:159] offset:7168\n\t"
        "ds_read_b64_tr_b16 v[128:129], v100 offset:0\n\t"
        "ds_read_b64_tr_b16 v[130:131], v100 offset:256\n\t"
        "ds_read_b64_tr_b16 v[132:133], v100 offset:4096\n\t"
        "ds_read_b64_tr_b16 v[134:135], v100 offset:4352\n\t"
        "s_waitcnt lgkmcnt(8)\n\t"
        "ds_read_b64_tr_b16 v[136:137], v101 offset:0\n\t"
        "ds_read_b64_tr_b16 v[138:139], v101 offset:256\n\t"
        "ds_read_b64_tr_b16 v[140:141], v101 offset:4096\n\t"
        "ds_read_b64_tr_b16 v[142:143], v101 offset:4352\n\t"
        "s_waitcnt lgkmcnt(8)\n\t"
        "ds_read_b64_tr_b16 v[144:145], v102 offset:0\n\t"
        "ds_read_b64_tr_b16 v[146:147], v102 offset:256\n\t"
        "ds_read_b64_tr_b16 v[148:149], v102 offset:4096\n\t"
        "ds_read_b64_tr_b16 v[150:151], v102 offset:4352\n\t"
        "s_waitcnt lgkmcnt(8)\n\t"
        "ds_read_b64_tr_b16 v[152:153], v103 offset:0\n\t"
        "ds_read_b64_tr_b16 v[154:155], v103 offset:256\n\t"
        "ds_read_b64_tr_b16 v[156:157], v103 offset:4096\n\t"
        "ds_read_b64_tr_b16 v[158:159], v103 offset:4352\n\t"
        "s_lshl_b32 s34, s43, 21\n\t"
        "s_add_u32 s34, s34, 1572864\n\t"
        "buffer_load_dwordx4 v[224:227], v96, s[16:19], s34 offen nt\n\t"
        "s_add_u32 s34, s34, 0x10000\n\t"
        "buffer_load_dwordx4 v[228:231], v96, s[16:19], s34 offen nt\n\t"
        "s_add_u32 s34, s34, 0x10000\n\t"
        "buffer_load_dwordx4 v[232:235], v96, s[16:19], s34 offen nt\n\t"
        "s_add_u32 s34, s34, 0x10000\n\t"
        "buffer_load_dwordx4 v[236:239], v96, s[16:19], s34 offen nt\n\t"
        "s_add_u32 s34, s34, 0x10000\n\t"
        "buffer_load_dwordx4 v[240:243], v96, s[16:19], s34 offen nt\n\t"
        "s_add_u32 s34, s34, 0x10000\n\t"
        "buffer_load_dwordx4 v[244:247], v96, s[16:19], s34 offen nt\n\t"
        "s_add_u32 s34, s34, 0x10000\n\t"
        "buffer_load_dwordx4 v[248:251], v96, s[16:19], s34 offen nt\n\t"
        "s_add_u32 s34, s34, 0x10000\n\t"
        "buffer_load_dwordx4 v[252:255], v96, s[16:19], s34 offen nt\n\t"
        "s_waitcnt lgkmcnt(0)\n\t"
        "s_waitcnt vmcnt(16)\n\t"
        "ds_write_b128 v98, v[160:163] offset:0\n\t"
        "ds_write_b128 v99, v[164:167] offset:1024\n\t"
        "ds_write_b128 v98, v[168:171] offset:2048\n\t"
        "ds_write_b128 v99, v[172:175] offset:3072\n\t"
        "ds_write_b128 v98, v[176:179] offset:4096\n\t"
        "ds_write_b128 v99, v[180:183] offset:5120\n\t"
        "ds_write_b128 v98, v[184:187] offset:6144\n\t"
        "ds_write_b128 v99, v[188:191] offset:7168\n\t"
        "ds_read_b64_tr_b16 v[160:161], v100 offset:0\n\t"
        "ds_read_b64_tr_b16 v[162:163], v100 offset:256\n\t"
        "ds_read_b64_tr_b16 v[164:165], v100 offset:4096\n\t"
        "ds_read_b64_tr_b16 v[166:167], v100 offset:4352\n\t"
        "s_waitcnt lgkmcnt(8)\n\t"
        "ds_read_b64_tr_b16 v[168:169], v101 offset:0\n\t"
        "ds_read_b64_tr_b16 v[170:171], v101 offset:256\n\t"
        "ds_read_b64_tr_b16 v[172:173], v101 offset:4096\n\t"
        "ds_read_b64_tr_b16 v[174:175], v101 offset:4352\n\t"
        "s_waitcnt lgkmcnt(8)\n\t"
        "ds_read_b64_tr_b16 v[176:177], v102 offset:0\n\t"
        "ds_read_b64_tr_b16 v[178:179], v102 offset:256\n\t"
        "ds_read_b64_tr_b16 v[180:181], v102 offset:4096\n\t"
        "ds_read_b64_tr_b16 v[182:183], v102 offset:4352\n\t"
        "s_waitcnt lgkmcnt(8)\n\t"
        "ds_read_b64_tr_b16 v[184:185], v103 offset:0\n\t"
        "ds_read_b64_tr_b16 v[186:187], v103 offset:256\n\t"
        "ds_read_b64_tr_b16 v[188:189], v103 offset:4096\n\t"
        "ds_read_b64_tr_b16 v[190:191], v103 offset:4352\n\t"
        "s_lshl_b32 s34, s43, 21\n\t"
        "s_add_u32 s34, s34, 0\n\t"
        "buffer_load_dwordx4 a[0:3], v97, s[20:23], s34 offen\n\t"
        "s_add_u32 s34, s34, 0x8000\n\t"
        "buffer_load_dwordx4 a[4:7], v97, s[20:23], s34 offen\n\t"
        "s_add_u32 s34, s34, 0x8000\n\t"
        "buffer_load_dwordx4 a[8:11], v97, s[20:23], s34 offen\n\t"
        "s_add_u32 s34, s34, 0x8000\n\t"
        "buffer_load_dwordx4 a[12:15], v97, s[20:23], s34 offen\n\t"
        "s_add_u32 s34, s34, 0x8000\n\t"
        "buffer_load_dwordx4 a[16:19], v97, s[20:23], s34 offen\n\t"
        "s_add_u32 s34, s34, 0x8000\n\t"
        "buffer_load_dwordx4 a[20:23], v97, s[20:23], s34 offen\n\t"
        "s_add_u32 s34, s34, 0x8000\n\t"
        "buffer_load_dwordx4 a[24:27], v97, s[20:23], s34 offen\n\t"
        "s_add_u32 s34, s34, 0x8000\n\t"
        "buffer_load_dwordx4 a[28:31], v97, s[20:23], s34 offen\n\t"
        "s_waitcnt lgkmcnt(0)\n\t"
        "s_waitcnt vmcnt(16)\n\t"
        "ds_write_b128 v98, v[192:195] offset:0\n\t"
        "ds_write_b128 v99, v[196:199] offset:1024\n\t"
        "ds_write_b128 v98, v[200:203] offset:2048\n\t"
        "ds_write_b128 v99, v[204:207] offset:3072\n\t"
        "ds_write_b128 v98, v[208:211] offset:4096\n\t"
        "ds_write_b128 v99, v[212:215] offset:5120\n\t"
        "ds_write_b128 v98, v[216:219] offset:6144\n\t"
        "ds_write_b128 v99, v[220:223] offset:7168\n\t"
        "ds_read_b64_tr_b16 v[192:193], v100 offset:0\n\t"
        "ds_read_b64_tr_b16 v[194:195], v100 offset:256\n\t"
        "ds_read_b64_tr_b16 v[196:197], v100 offset:4096\n\t"
        "ds_read_b64_tr_b16 v[198:199], v100 offset:4352\n\t"
        "s_waitcnt lgkmcnt(8)\n\t"
        "ds_read_b64_tr_b16 v[200:201], v101 offset:0\n\t"
        "ds_read_b64_tr_b16 v[202:203], v101 offset:256\n\t"
        "ds_read_b64_tr_b16 v[204:205], v101 offset:4096\n\t"
        "ds_read_b64_tr_b16 v[206:207], v101 offset:4352\n\t"
        "s_waitcnt lgkmcnt(8)\n\t"
        "ds_read_b64_tr_b16 v[208:209], v102 offset:0\n\t"
        "ds_read_b64_tr_b16 v[210:211], v102 offset:256\n\t"
        "ds_read_b64_tr_b16 v[212:213], v102 offset:4096\n\t"
        "ds_read_b64_tr_b16 v[214:215], v102 offset:4352\n\t"
        "s_waitcnt lgkmcnt(8)\n\t"
        "ds_read_b64_tr_b16 v[216:217], v103 offset:0\n\t"
        "ds_read_b64_tr_b16 v[218:219], v103 offset:256\n\t"
        "ds_read_b64_tr_b16 v[220:221], v103 offset:4096\n\t"
        "ds_read_b64_tr_b16 v[222:223], v103 offset:4352\n\t"
        "s_lshl_b32 s34, s43, 21\n\t"
        "s_add_u32 s34, s34, 262144\n\t"
        "buffer_load_dwordx4 a[32:35], v97, s[20:23], s34 offen\n\t"
        "s_add_u32 s34, s34, 0x8000\n\t"
        "buffer_load_dwordx4 a[36:39], v97, s[20:23], s34 offen\n\t"
        "s_add_u32 s34, s34, 0x8000\n\t"
        "buffer_load_dwordx4 a[40:43], v97, s[20:23], s34 offen\n\t"
        "s_add_u32 s34, s34, 0x8000\n\t"
        "buffer_load_dwordx4 a[44:47], v97, s[20:23], s34 offen\n\t"
        "s_add_u32 s34, s34, 0x8000\n\t"
        "buffer_load_dwordx4 a[48:51], v97, s[20:23], s34 offen\n\t"
        "s_add_u32 s34, s34, 0x8000\n\t"
        "buffer_load_dwordx4 a[52:55], v97, s[20:23], s34 offen\n\t"
        "s_add_u32 s34, s34, 0x8000\n\t"
        "buffer_load_dwordx4 a[56:59], v97, s[20:23], s34 offen\n\t"
        "s_add_u32 s34, s34, 0x8000\n\t"
        "buffer_load_dwordx4 a[60:63], v97, s[20:23], s34 offen\n\t"
        "s_waitcnt lgkmcnt(0)\n\t"
        "s_waitcnt vmcnt(16)\n\t"
        "ds_write_b128 v98, v[224:227] offset:0\n\t"
        "ds_write_b128 v99, v[228:231] offset:1024\n\t"
        "ds_write_b128 v98, v[232:235] offset:2048\n\t"
        "ds_write_b128 v99, v[236:239] offset:3072\n\t"
        "ds_write_b128 v98, v[240:243] offset:4096\n\t"
        "ds_write_b128 v99, v[244:247] offset:5120\n\t"
        "ds_write_b128 v98, v[248:251] offset:6144\n\t"
        "ds_write_b128 v99, v[252:255] offset:7168\n\t"
        "ds_read_b64_tr_b16 v[224:225], v100 offset:0\n\t"
        "ds_read_b64_tr_b16 v[226:227], v100 offset:256\n\t"
        "ds_read_b64_tr_b16 v[228:229], v100 offset:4096\n\t"
        "ds_read_b64_tr_b16 v[230:231], v100 offset:4352\n\t"
        "s_waitcnt lgkmcnt(8)\n\t"
        "ds_read_b64_tr_b16 v[232:233], v101 offset:0\n\t"
        "ds_read_b64_tr_b16 v[234:235], v101 offset:256\n\t"
        "ds_read_b64_tr_b16 v[236:237], v101 offset:4096\n\t"
        "ds_read_b64_tr_b16 v[238:239], v101 offset:4352\n\t"
        "s_waitcnt lgkmcnt(8)\n\t"
        "ds_read_b64_tr_b16 v[240:241], v102 offset:0\n\t"
        "ds_read_b64_tr_b16 v[242:243], v102 offset:256\n\t"
        "ds_read_b64_tr_b16 v[244:245], v102 offset:4096\n\t"
        "ds_read_b64_tr_b16 v[246:247], v102 offset:4352\n\t"
        "s_waitcnt lgkmcnt(8)\n\t"
        "ds_read_b64_tr_b16 v[248:249], v103 offset:0\n\t"
        "ds_read_b64_tr_b16 v[250:251], v103 offset:256\n\t"
        "ds_read_b64_tr_b16 v[252:253], v103 offset:4096\n\t"
        "ds_read_b64_tr_b16 v[254:255], v103 offset:4352\n\t"
        "s_lshl_b32 s34, s43, 21\n\t"
        "s_add_u32 s34, s34, 524288\n\t"
        "buffer_load_dwordx4 a[64:67], v97, s[20:23], s34 offen\n\t"
        "s_add_u32 s34, s34, 0x8000\n\t"
        "buffer_load_dwordx4 a[68:71], v97, s[20:23], s34 offen\n\t"
        "s_add_u32 s34, s34, 0x8000\n\t"
        "buffer_load_dwordx4 a[72:75], v97, s[20:23], s34 offen\n\t"
        "s_add_u32 s34, s34, 0x8000\n\t"
        "buffer_load_dwordx4 a[76:79], v97, s[20:23], s34 offen\n\t"
        "s_add_u32 s34, s34, 0x8000\n\t"
        "buffer_load_dwordx4 a[80:83], v97, s[20:23], s34 offen\n\t"
        "s_add_u32 s34, s34, 0x8000\n\t"
        "buffer_load_dwordx4 a[84:87], v97, s[20:23], s34 offen\n\t"
        "s_add_u32 s34, s34, 0x8000\n\t"
        "buffer_load_dwordx4 a[88:91], v97, s[20:23], s34 offen\n\t"
        "s_add_u32 s34, s34, 0x8000\n\t"
        "buffer_load_dwordx4 a[92:95], v97, s[20:23], s34 offen\n\t"
        "s_waitcnt lgkmcnt(0)\n\t"
        "s_waitcnt vmcnt(16)\n\t"
        "ds_write_b128 v122, a[0:3] offset:0\n\t"
        "ds_write_b128 v122, a[4:7] offset:1056\n\t"
        "ds_write_b128 v122, a[8:11] offset:2176\n\t"
        "ds_write_b128 v122, a[12:15] offset:3232\n\t"
        "ds_write_b128 v122, a[16:19] offset:4352\n\t"
        "ds_write_b128 v122, a[20:23] offset:5408\n\t"
        "ds_write_b128 v122, a[24:27] offset:6528\n\t"
        "ds_write_b128 v122, a[28:31] offset:7584\n\t"
        "ds_read_b64_tr_b16 a[0:1], v123 offset:0\n\t"
        "ds_read_b64_tr_b16 a[2:3], v123 offset:128\n\t"
        "ds_read_b64_tr_b16 a[4:5], v123 offset:4352\n\t"
        "ds_read_b64_tr_b16 a[6:7], v123 offset:4480\n\t"
        "s_waitcnt lgkmcnt(8)\n\t"
        "ds_read_b64_tr_b16 a[8:9], v123 offset:32\n\t"
        "ds_read_b64_tr_b16 a[10:11], v123 offset:160\n\t"
        "ds_read_b64_tr_b16 a[12:13], v123 offset:4384\n\t"
        "ds_read_b64_tr_b16 a[14:15], v123 offset:4512\n\t"
        "s_waitcnt lgkmcnt(8)\n\t"
        "ds_read_b64_tr_b16 a[16:17], v123 offset:512\n\t"
        "ds_read_b64_tr_b16 a[18:19], v123 offset:640\n\t"
        "ds_read_b64_tr_b16 a[20:21], v123 offset:4864\n\t"
        "ds_read_b64_tr_b16 a[22:23], v123 offset:4992\n\t"
        "s_waitcnt lgkmcnt(8)\n\t"
        "ds_read_b64_tr_b16 a[24:25], v123 offset:544\n\t"
        "ds_read_b64_tr_b16 a[26:27], v123 offset:672\n\t"
        "ds_read_b64_tr_b16 a[28:29], v123 offset:4896\n\t"
        "ds_read_b64_tr_b16 a[30:31], v123 offset:5024\n\t"
        "s_waitcnt lgkmcnt(8)\n\t"
        "s_lshl_b32 s34, s43, 21\n\t"
        "s_add_u32 s34, s34, 786432\n\t"
        "buffer_load_dwordx4 a[96:99], v97, s[20:23], s34 offen\n\t"
        "s_add_u32 s34, s34, 0x8000\n\t"
        "buffer_load_dwordx4 a[100:103], v97, s[20:23], s34 offen\n\t"
        "s_add_u32 s34, s34, 0x8000\n\t"
        "buffer_load_dwordx4 a[104:107], v97, s[20:23], s34 offen\n\t"
        "s_add_u32 s34, s34, 0x8000\n\t"
        "buffer_load_dwordx4 a[108:111], v97, s[20:23], s34 offen\n\t"
        "s_add_u32 s34, s34, 0x8000\n\t"
        "buffer_load_dwordx4 a[112:115], v97, s[20:23], s34 offen\n\t"
        "s_add_u32 s34, s34, 0x8000\n\t"
        "buffer_load_dwordx4 a[116:119], v97, s[20:23], s34 offen\n\t"
        "s_add_u32 s34, s34, 0x8000\n\t"
        "buffer_load_dwordx4 a[120:123], v97, s[20:23], s34 offen\n\t"
        "s_add_u32 s34, s34, 0x8000\n\t"
        "buffer_load_dwordx4 a[124:127], v97, s[20:23], s34 offen\n\t"
        "s_waitcnt lgkmcnt(0)\n\t"
        "s_waitcnt vmcnt(16)\n\t"
        "ds_write_b128 v122, a[32:35] offset:0\n\t"
        "ds_write_b128 v122, a[36:39] offset:1056\n\t"
        "ds_write_b128 v122, a[40:43] offset:2176\n\t"
        "ds_write_b128 v122, a[44:47] offset:3232\n\t"
        "ds_write_b128 v122, a[48:51] offset:4352\n\t"
        "ds_write_b128 v122, a[52:55] offset:5408\n\t"
        "ds_write_b128 v122, a[56:59] offset:6528\n\t"
        "ds_write_b128 v122, a[60:63] offset:7584\n\t"
        "ds_read_b64_tr_b16 a[32:33], v123 offset:0\n\t"
        "ds_read_b64_tr_b16 a[34:35], v123 offset:128\n\t"
        "ds_read_b64_tr_b16 a[36:37], v123 offset:4352\n\t"
        "ds_read_b64_tr_b16 a[38:39], v123 offset:4480\n\t"
        "s_waitcnt lgkmcnt(8)\n\t"
        "ds_read_b64_tr_b16 a[40:41], v123 offset:32\n\t"
        "ds_read_b64_tr_b16 a[42:43], v123 offset:160\n\t"
        "ds_read_b64_tr_b16 a[44:45], v123 offset:4384\n\t"
        "ds_read_b64_tr_b16 a[46:47], v123 offset:4512\n\t"
        "s_waitcnt lgkmcnt(8)\n\t"
        "ds_read_b64_tr_b16 a[48:49], v123 offset:512\n\t"
        "ds_read_b64_tr_b16 a[50:51], v123 offset:640\n\t"
        "ds_read_b64_tr_b16 a[52:53], v123 offset:4864\n\t"
        "ds_read_b64_tr_b16 a[54:55], v123 offset:4992\n\t"
        "s_waitcnt lgkmcnt(8)\n\t"
        "ds_read_b64_tr_b16 a[56:57], v123 offset:544\n\t"
        "ds_read_b64_tr_b16 a[58:59], v123 offset:672\n\t"
        "ds_read_b64_tr_b16 a[60:61], v123 offset:4896\n\t"
        "ds_read_b64_tr_b16 a[62:63], v123 offset:5024\n\t"
        "s_waitcnt lgkmcnt(8)\n\t"
        "s_lshl_b32 s34, s43, 21\n\t"
        "s_add_u32 s34, s34, 1048576\n\t"
        "buffer_load_dwordx4 a[128:131], v97, s[20:23], s34 offen\n\t"
        "s_add_u32 s34, s34, 0x8000\n\t"
        "buffer_load_dwordx4 a[132:135], v97, s[20:23], s34 offen\n\t"
        "s_add_u32 s34, s34, 0x8000\n\t"
        "buffer_load_dwordx4 a[136:139], v97, s[20:23], s34 offen\n\t"
        "s_add_u32 s34, s34, 0x8000\n\t"
        "buffer_load_dwordx4 a[140:143], v97, s[20:23], s34 offen\n\t"
        "s_add_u32 s34, s34, 0x8000\n\t"
        "buffer_load_dwordx4 a[144:147], v97, s[20:23], s34 offen\n\t"
        "s_add_u32 s34, s34, 0x8000\n\t"
        "buffer_load_dwordx4 a[148:151], v97, s[20:23], s34 offen\n\t"
        "s_add_u32 s34, s34, 0x8000\n\t"
        "buffer_load_dwordx4 a[152:155], v97, s[20:23], s34 offen\n\t"
        "s_add_u32 s34, s34, 0x8000\n\t"
        "buffer_load_dwordx4 a[156:159], v97, s[20:23], s34 offen\n\t"
        "s_waitcnt lgkmcnt(0)\n\t"
        "s_waitcnt vmcnt(16)\n\t"
        "ds_write_b128 v122, a[64:67] offset:0\n\t"
        "ds_write_b128 v122, a[68:71] offset:1056\n\t"
        "ds_write_b128 v122, a[72:75] offset:2176\n\t"
        "ds_write_b128 v122, a[76:79] offset:3232\n\t"
        "ds_write_b128 v122, a[80:83] offset:4352\n\t"
        "ds_write_b128 v122, a[84:87] offset:5408\n\t"
        "ds_write_b128 v122, a[88:91] offset:6528\n\t"
        "ds_write_b128 v122, a[92:95] offset:7584\n\t"
        "ds_read_b64_tr_b16 a[64:65], v123 offset:0\n\t"
        "ds_read_b64_tr_b16 a[66:67], v123 offset:128\n\t"
        "ds_read_b64_tr_b16 a[68:69], v123 offset:4352\n\t"
        "ds_read_b64_tr_b16 a[70:71], v123 offset:4480\n\t"
        "s_waitcnt lgkmcnt(8)\n\t"
        "ds_read_b64_tr_b16 a[72:73], v123 offset:32\n\t"
        "ds_read_b64_tr_b16 a[74:75], v123 offset:160\n\t"
        "ds_read_b64_tr_b16 a[76:77], v123 offset:4384\n\t"
        "ds_read_b64_tr_b16 a[78:79], v123 offset:4512\n\t"
        "s_waitcnt lgkmcnt(8)\n\t"
        "ds_read_b64_tr_b16 a[80:81], v123 offset:512\n\t"
        "ds_read_b64_tr_b16 a[82:83], v123 offset:640\n\t"
        "ds_read_b64_tr_b16 a[84:85], v123 offset:4864\n\t"
        "ds_read_b64_tr_b16 a[86:87], v123 offset:4992\n\t"
        "s_waitcnt lgkmcnt(8)\n\t"
        "ds_read_b64_tr_b16 a[88:89], v123 offset:544\n\t"
        "ds_read_b64_tr_b16 a[90:91], v123 offset:672\n\t"
        "ds_read_b64_tr_b16 a[92:93], v123 offset:4896\n\t"
        "ds_read_b64_tr_b16 a[94:95], v123 offset:5024\n\t"
        "s_waitcnt lgkmcnt(8)\n\t"
        "s_lshl_b32 s34, s43, 21\n\t"
        "s_add_u32 s34, s34, 1310720\n\t"
        "buffer_load_dwordx4 a[160:163], v97, s[20:23], s34 offen\n\t"
        "s_add_u32 s34, s34, 0x8000\n\t"
        "buffer_load_dwordx4 a[164:167], v97, s[20:23], s34 offen\n\t"
        "s_add_u32 s34, s34, 0x8000\n\t"
        "buffer_load_dwordx4 a[168:171], v97, s[20:23], s34 offen\n\t"
        "s_add_u32 s34, s34, 0x8000\n\t"
        "buffer_load_dwordx4 a[172:175], v97, s[20:23], s34 offen\n\t"
        "s_add_u32 s34, s34, 0x8000\n\t"
        "buffer_load_dwordx4 a[176:179], v97, s[20:23], s34 offen\n\t"
        "s_add_u32 s34, s34, 0x8000\n\t"
        "buffer_load_dwordx4 a[180:183], v97, s[20:23], s34 offen\n\t"
        "s_add_u32 s34, s34, 0x8000\n\t"
        "buffer_load_dwordx4 a[184:187], v97, s[20:23], s34 offen\n\t"
        "s_add_u32 s34, s34, 0x8000\n\t"
        "buffer_load_dwordx4 a[188:191], v97, s[20:23], s34 offen\n\t"
        "s_waitcnt lgkmcnt(0)\n\t"
        "s_waitcnt vmcnt(16)\n\t"
        "ds_write_b128 v122, a[96:99] offset:0\n\t"
        "ds_write_b128 v122, a[100:103] offset:1056\n\t"
        "ds_write_b128 v122, a[104:107] offset:2176\n\t"
        "ds_write_b128 v122, a[108:111] offset:3232\n\t"
        "ds_write_b128 v122, a[112:115] offset:4352\n\t"
        "ds_write_b128 v122, a[116:119] offset:5408\n\t"
        "ds_write_b128 v122, a[120:123] offset:6528\n\t"
        "ds_write_b128 v122, a[124:127] offset:7584\n\t"
        "ds_read_b64_tr_b16 a[96:97], v123 offset:0\n\t"
        "ds_read_b64_tr_b16 a[98:99], v123 offset:128\n\t"
        "ds_read_b64_tr_b16 a[100:101], v123 offset:4352\n\t"
        "ds_read_b64_tr_b16 a[102:103], v123 offset:4480\n\t"
        "s_waitcnt lgkmcnt(8)\n\t"
        "ds_read_b64_tr_b16 a[104:105], v123 offset:32\n\t"
        "ds_read_b64_tr_b16 a[106:107], v123 offset:160\n\t"
        "ds_read_b64_tr_b16 a[108:109], v123 offset:4384\n\t"
        "ds_read_b64_tr_b16 a[110:111], v123 offset:4512\n\t"
        "s_waitcnt lgkmcnt(8)\n\t"
        "ds_read_b64_tr_b16 a[112:113], v123 offset:512\n\t"
        "ds_read_b64_tr_b16 a[114:115], v123 offset:640\n\t"
        "ds_read_b64_tr_b16 a[116:117], v123 offset:4864\n\t"
        "ds_read_b64_tr_b16 a[118:119], v123 offset:4992\n\t"
        "s_waitcnt lgkmcnt(8)\n\t"
        "ds_read_b64_tr_b16 a[120:121], v123 offset:544\n\t"
        "ds_read_b64_tr_b16 a[122:123], v123 offset:672\n\t"
        "ds_read_b64_tr_b16 a[124:125], v123 offset:4896\n\t"
        "ds_read_b64_tr_b16 a[126:127], v123 offset:5024\n\t"
        "s_waitcnt lgkmcnt(8)\n\t"
        "s_lshl_b32 s34, s43, 21\n\t"
        "s_add_u32 s34, s34, 1572864\n\t"
        "buffer_load_dwordx4 a[192:195], v97, s[20:23], s34 offen\n\t"
        "s_add_u32 s34, s34, 0x8000\n\t"
        "buffer_load_dwordx4 a[196:199], v97, s[20:23], s34 offen\n\t"
        "s_add_u32 s34, s34, 0x8000\n\t"
        "buffer_load_dwordx4 a[200:203], v97, s[20:23], s34 offen\n\t"
        "s_add_u32 s34, s34, 0x8000\n\t"
        "buffer_load_dwordx4 a[204:207], v97, s[20:23], s34 offen\n\t"
        "s_add_u32 s34, s34, 0x8000\n\t"
        "buffer_load_dwordx4 a[208:211], v97, s[20:23], s34 offen\n\t"
        "s_add_u32 s34, s34, 0x8000\n\t"
        "buffer_load_dwordx4 a[212:215], v97, s[20:23], s34 offen\n\t"
        "s_add_u32 s34, s34, 0x8000\n\t"
        "buffer_load_dwordx4 a[216:219], v97, s[20:23], s34 offen\n\t"
        "s_add_u32 s34, s34, 0x8000\n\t"
        "buffer_load_dwordx4 a[220:223], v97, s[20:23], s34 offen\n\t"
        "s_waitcnt lgkmcnt(0)\n\t"
        "s_waitcnt vmcnt(16)\n\t"
        "ds_write_b128 v122, a[128:131] offset:0\n\t"
        "ds_write_b128 v122, a[132:135] offset:1056\n\t"
        "ds_write_b128 v122, a[136:139] offset:2176\n\t"
        "ds_write_b128 v122, a[140:143] offset:3232\n\t"
        "ds_write_b128 v122, a[144:147] offset:4352\n\t"
        "ds_write_b128 v122, a[148:151] offset:5408\n\t"
        "ds_write_b128 v122, a[152:155] offset:6528\n\t"
        "ds_write_b128 v122, a[156:159] offset:7584\n\t"
        "ds_read_b64_tr_b16 a[128:129], v123 offset:0\n\t"
        "ds_read_b64_tr_b16 a[130:131], v123 offset:128\n\t"
        "ds_read_b64_tr_b16 a[132:133], v123 offset:4352\n\t"
        "ds_read_b64_tr_b16 a[134:135], v123 offset:4480\n\t"
        "s_waitcnt lgkmcnt(8)\n\t"
        "ds_read_b64_tr_b16 a[136:137], v123 offset:32\n\t"
        "ds_read_b64_tr_b16 a[138:139], v123 offset:160\n\t"
        "ds_read_b64_tr_b16 a[140:141], v123 offset:4384\n\t"
        "ds_read_b64_tr_b16 a[142:143], v123 offset:4512\n\t"
        "s_waitcnt lgkmcnt(8)\n\t"
        "ds_read_b64_tr_b16 a[144:145], v123 offset:512\n\t"
        "ds_read_b64_tr_b16 a[146:147], v123 offset:640\n\t"
        "ds_read_b64_tr_b16 a[148:149], v123 offset:4864\n\t"
        "ds_read_b64_tr_b16 a[150:151], v123 offset:4992\n\t"
        "s_waitcnt lgkmcnt(8)\n\t"
        "ds_read_b64_tr_b16 a[152:153], v123 offset:544\n\t"
        "ds_read_b64_tr_b16 a[154:155], v123 offset:672\n\t"
        "ds_read_b64_tr_b16 a[156:157], v123 offset:4896\n\t"
        "ds_read_b64_tr_b16 a[158:159], v123 offset:5024\n\t"
        "s_waitcnt lgkmcnt(8)\n\t"
        "s_lshl_b32 s34, s43, 21\n\t"
        "s_add_u32 s34, s34, 1835008\n\t"
        "buffer_load_dwordx4 a[224:227], v97, s[20:23], s34 offen\n\t"
        "s_add_u32 s34, s34, 0x8000\n\t"
        "buffer_load_dwordx4 a[228:231], v97, s[20:23], s34 offen\n\t"
        "s_add_u32 s34, s34, 0x8000\n\t"
        "buffer_load_dwordx4 a[232:235], v97, s[20:23], s34 offen\n\t"
        "s_add_u32 s34, s34, 0x8000\n\t"
        "buffer_load_dwordx4 a[236:239], v97, s[20:23], s34 offen\n\t"
        "s_add_u32 s34, s34, 0x8000\n\t"
        "buffer_load_dwordx4 a[240:243], v97, s[20:23], s34 offen\n\t"
        "s_add_u32 s34, s34, 0x8000\n\t"
        "buffer_load_dwordx4 a[244:247], v97, s[20:23], s34 offen\n\t"
        "s_add_u32 s34, s34, 0x8000\n\t"
        "buffer_load_dwordx4 a[248:251], v97, s[20:23], s34 offen\n\t"
        "s_add_u32 s34, s34, 0x8000\n\t"
        "buffer_load_dwordx4 a[252:255], v97, s[20:23], s34 offen\n\t"
        "s_waitcnt lgkmcnt(0)\n\t"
        "s_waitcnt vmcnt(16)\n\t"
        "ds_write_b128 v122, a[160:163] offset:0\n\t"
        "ds_write_b128 v122, a[164:167] offset:1056\n\t"
        "ds_write_b128 v122, a[168:171] offset:2176\n\t"
        "ds_write_b128 v122, a[172:175] offset:3232\n\t"
        "ds_write_b128 v122, a[176:179] offset:4352\n\t"
        "ds_write_b128 v122, a[180:183] offset:5408\n\t"
        "ds_write_b128 v122, a[184:187] offset:6528\n\t"
        "ds_write_b128 v122, a[188:191] offset:7584\n\t"
        "ds_read_b64_tr_b16 a[160:161], v123 offset:0\n\t"
        "ds_read_b64_tr_b16 a[162:163], v123 offset:128\n\t"
        "ds_read_b64_tr_b16 a[164:165], v123 offset:4352\n\t"
        "ds_read_b64_tr_b16 a[166:167], v123 offset:4480\n\t"
        "s_waitcnt lgkmcnt(8)\n\t"
        "ds_read_b64_tr_b16 a[168:169], v123 offset:32\n\t"
        "ds_read_b64_tr_b16 a[170:171], v123 offset:160\n\t"
        "ds_read_b64_tr_b16 a[172:173], v123 offset:4384\n\t"
        "ds_read_b64_tr_b16 a[174:175], v123 offset:4512\n\t"
        "s_waitcnt lgkmcnt(8)\n\t"
        "ds_read_b64_tr_b16 a[176:177], v123 offset:512\n\t"
        "ds_read_b64_tr_b16 a[178:179], v123 offset:640\n\t"
        "ds_read_b64_tr_b16 a[180:181], v123 offset:4864\n\t"
        "ds_read_b64_tr_b16 a[182:183], v123 offset:4992\n\t"
        "s_waitcnt lgkmcnt(8)\n\t"
        "ds_read_b64_tr_b16 a[184:185], v123 offset:544\n\t"
        "ds_read_b64_tr_b16 a[186:187], v123 offset:672\n\t"
        "ds_read_b64_tr_b16 a[188:189], v123 offset:4896\n\t"
        "ds_read_b64_tr_b16 a[190:191], v123 offset:5024\n\t"
        "s_waitcnt lgkmcnt(8)\n\t"
        "s_waitcnt lgkmcnt(0)\n\t"
        "s_waitcnt vmcnt(8)\n\t"
        "ds_write_b128 v122, a[192:195] offset:0\n\t"
        "ds_write_b128 v122, a[196:199] offset:1056\n\t"
        "ds_write_b128 v122, a[200:203] offset:2176\n\t"
        "ds_write_b128 v122, a[204:207] offset:3232\n\t"
        "ds_write_b128 v122, a[208:211] offset:4352\n\t"
        "ds_write_b128 v122, a[212:215] offset:5408\n\t"
        "ds_write_b128 v122, a[216:219] offset:6528\n\t"
        "ds_write_b128 v122, a[220:223] offset:7584\n\t"
        "ds_read_b64_tr_b16 a[192:193], v123 offset:0\n\t"
        "ds_read_b64_tr_b16 a[194:195], v123 offset:128\n\t"
        "ds_read_b64_tr_b16 a[196:197], v123 offset:4352\n\t"
        "ds_read_b64_tr_b16 a[198:199], v123 offset:4480\n\t"
        "s_waitcnt lgkmcnt(8)\n\t"
        "ds_read_b64_tr_b16 a[200:201], v123 offset:32\n\t"
        "ds_read_b64_tr_b16 a[202:203], v123 offset:160\n\t"
        "ds_read_b64_tr_b16 a[204:205], v123 offset:4384\n\t"
        "ds_read_b64_tr_b16 a[206:207], v123 offset:4512\n\t"
        "s_waitcnt lgkmcnt(8)\n\t"
        "ds_read_b64_tr_b16 a[208:209], v123 offset:512\n\t"
        "ds_read_b64_tr_b16 a[210:211], v123 offset:640\n\t"
        "ds_read_b64_tr_b16 a[212:213], v123 offset:4864\n\t"
        "ds_read_b64_tr_b16 a[214:215], v123 offset:4992\n\t"
        "s_waitcnt lgkmcnt(8)\n\t"
        "ds_read_b64_tr_b16 a[216:217], v123 offset:544\n\t"
        "ds_read_b64_tr_b16 a[218:219], v123 offset:672\n\t"
        "ds_read_b64_tr_b16 a[220:221], v123 offset:4896\n\t"
        "ds_read_b64_tr_b16 a[222:223], v123 offset:5024\n\t"
        "s_waitcnt lgkmcnt(8)\n\t"
        "s_waitcnt lgkmcnt(0)\n\t"
        "s_waitcnt vmcnt(0)\n\t"
        "ds_write_b128 v122, a[224:227] offset:0\n\t"
        "ds_write_b128 v122, a[228:231] offset:1056\n\t"
        "ds_write_b128 v122, a[232:235] offset:2176\n\t"
        "ds_write_b128 v122, a[236:239] offset:3232\n\t"
        "ds_write_b128 v122, a[240:243] offset:4352\n\t"
        "ds_write_b128 v122, a[244:247] offset:5408\n\t"
        "ds_write_b128 v122, a[248:251] offset:6528\n\t"
        "ds_write_b128 v122, a[252:255] offset:7584\n\t"
        "ds_read_b64_tr_b16 a[224:225], v123 offset:0\n\t"
        "ds_read_b64_tr_b16 a[226:227], v123 offset:128\n\t"
        "ds_read_b64_tr_b16 a[228:229], v123 offset:4352\n\t"
        "ds_read_b64_tr_b16 a[230:231], v123 offset:4480\n\t"
        "s_waitcnt lgkmcnt(8)\n\t"
        "ds_read_b64_tr_b16 a[232:233], v123 offset:32\n\t"
        "ds_read_b64_tr_b16 a[234:235], v123 offset:160\n\t"
        "ds_read_b64_tr_b16 a[236:237], v123 offset:4384\n\t"
        "ds_read_b64_tr_b16 a[238:239], v123 offset:4512\n\t"
        "s_waitcnt lgkmcnt(8)\n\t"
        "ds_read_b64_tr_b16 a[240:241], v123 offset:512\n\t"
        "ds_read_b64_tr_b16 a[242:243], v123 offset:640\n\t"
        "ds_read_b64_tr_b16 a[244:245], v123 offset:4864\n\t"
        "ds_read_b64_tr_b16 a[246:247], v123 offset:4992\n\t"
        "s_waitcnt lgkmcnt(8)\n\t"
        "ds_read_b64_tr_b16 a[248:249], v123 offset:544\n\t"
        "ds_read_b64_tr_b16 a[250:251], v123 offset:672\n\t"
        "ds_read_b64_tr_b16 a[252:253], v123 offset:4896\n\t"
        "ds_read_b64_tr_b16 a[254:255], v123 offset:5024\n\t"
        "s_waitcnt lgkmcnt(8)\n\t"
        "s_mov_b32 s35, 33792\n\t"
        "buffer_load_dword v0, off, s[12:15], s35 sc1\n\t"
        "s_waitcnt lgkmcnt(0)\n\t"
        "s_waitcnt vmcnt(0)\n\t"
        "s_mov_b32 s42, 0\n\t"
        "LB_cal%=:\n\t"
        "v_readfirstlane_b32 s34, v0\n\t"
        "s_nop 0\n\t"
        "s_cmp_lg_u32 s34, 0\n\t"
        "s_cbranch_scc1 LB_calok%=\n\t"
        "s_sleep 4\n\t"
        "s_mov_b32 s35, 33792\n\t"
        "buffer_load_dword v0, off, s[12:15], s35 sc1\n\t"
        "s_waitcnt vmcnt(0)\n\t"
        "s_add_u32 s42, s42, 1\n\t"
        "s_cmp_lt_u32 s42, 0x100000\n\t"
        "s_cbranch_scc1 LB_cal%=\n\t"
        "s_movk_i32 s34, 8\n\t"
        "s_branch LB_die%=\n\t"
        "LB_calok%=:\n\t"
        "v_readfirstlane_b32 s34, v0\n\t"
        "s_nop 0\n\t"
        "s_and_b32 s34, s34, 8192\n\t"
        "s_add_u32 s8, s8, s34\n\t"
        "s_addc_u32 s9, s9, 0\n\t"
        "v_min_u32_e32 v109, -2, v109\n\t"
        "buffer_store_dword v109, v116, s[8:11], 0 offen sc1\n\t"
        "s_memrealtime s[32:33]\n\t"
        "s_waitcnt lgkmcnt(0)\n\t"
        "v_mov_b32_e32 v110, s32\n\t"
        "s_lshl_b32 s34, s29, 8\n\t"
        "s_lshl_b32 s35, s30, 20\n\t"
        "s_or_b32 s54, s34, s35\n\t"
        "s_add_u32 s34, s54, 12589056\n\t"
        "v_mov_b32_e32 v111, s34\n\t"
        "s_barrier\n\t"
        "s_cmp_lg_u32 s43, 0\n\t"
        "s_cbranch_scc1 LB_st%=\n\t"
        "v_mov_b32_e32 v2, 1\n\t"
        "s_mov_b32 s35, 34304\n\t"
        "s_mov_b64 exec, 1\n\t"
        "buffer_atomic_add v2, off, s[12:15], s35 sc0\n\t"
        "s_mov_b64 exec, -1\n\t"
        "s_waitcnt vmcnt(0)\n\t"
        "v_readfirstlane_b32 s34, v2\n\t"
        "s_nop 0\n\t"
        "s_cmp_lg_u32 s34, 95\n\t"
        "s_cbranch_scc1 LB_st%=\n\t"
        "s_memrealtime s[32:33]\n\t"
        "v_mbcnt_lo_u32_b32 v3, -1, 0\n\t"
        "v_lshlrev_b32_e32 v3, 7, v3\n\t"
        "s_waitcnt lgkmcnt(0)\n\t"
        "s_add_u32 s34, s32, 50\n\t"
        "s_or_b32 s34, s34, 1\n\t"
        "v_mov_b32_e32 v2, s34\n\t"
        "s_mov_b32 s35, 36864\n\t"
        "s_mov_b64 exec, 0xffff\n\t"
        "buffer_store_dword v2, v3, s[12:15], s35 offen sc1\n\t"
        "s_mov_b64 exec, -1\n\t"
        "LB_st%=:\n\t"
        "s_mov_b32 s24, 0\n\t"
        "s_mov_b32 s26, 0\n\t"
        "s_mov_b32 s27, 0x80000\n\t"
        "s_mov_b32 s28, 0x180000\n\t"
        "s_mov_b32 s31, 0\n\t"
        "s_mov_b32 s47, 0\n\t"
        "s_mov_b32 s38, 0xff00ff00\n\t"
        "s_mov_b32 s39, 0xff00ff00\n\t"
        "s_mov_b32 s40, 0\n\t"
        "s_mov_b32 s41, -1\n\t"
        "v_mov_b64_e32 v[0:1], 0\n\t"
        "v_mov_b64_e32 v[2:3], 0\n\t"
        "v_mov_b64_e32 v[4:5], 0\n\t"
        "v_mov_b64_e32 v[6:7], 0\n\t"
        "v_mov_b64_e32 v[8:9], 0\n\t"
        "v_mov_b64_e32 v[10:11], 0\n\t"
        "v_mov_b64_e32 v[12:13], 0\n\t"
        "v_mov_b64_e32 v[14:15], 0\n\t"
        "s_mov_b32 s42, 0\n\t"
        "LB_gw%=:\n\t"
        "buffer_load_dword v16, off, s[12:15], s45 sc1\n\t"
        "s_waitcnt vmcnt(0)\n\t"
        "v_readfirstlane_b32 s25, v16\n\t"
        "s_nop 0\n\t"
        "s_cmp_lg_u32 s25, 0\n\t"
        "s_cbranch_scc1 LB_loop%=\n\t"
        "s_sleep 2\n\t"
        "s_add_u32 s42, s42, 1\n\t"
        "s_cmp_lt_u32 s42, 0x100000\n\t"
        "s_cbranch_scc1 LB_gw%=\n\t"
        "s_movk_i32 s34, 9\n\t"
        "s_branch LB_die%=\n\t"
        ".p2align 8\n\t"
        "LB_loop%=:\n\t"
        "s_mov_b32 s42, 0\n\t"
        "s_sub_u32 s35, s25, 8\n\t"
        "s_waitcnt lgkmcnt(0)\n\t"
        "s_memrealtime s[2:3]\n\t"
        "s_memrealtime s[4:5]\n\t"
        "s_memrealtime s[6:7]\n\t"
        "LB_wa%=:\n\t"
        "s_memrealtime s[32:33]\n\t"
        "s_waitcnt lgkmcnt(6)\n\t"
        "s_sub_u32 s34, s35, s2\n\t"
        "s_cmpk_lt_u32 s34, 0x2000\n\t"
        "s_cbranch_scc0 LB_wa%=x\n\t"
        "s_memrealtime s[2:3]\n\t"
        "s_waitcnt lgkmcnt(6)\n\t"
        "s_sub_u32 s34, s35, s4\n\t"
        "s_cmpk_lt_u32 s34, 0x2000\n\t"
        "s_cbranch_scc0 LB_wa%=x\n\t"
        "s_memrealtime s[4:5]\n\t"
        "s_waitcnt lgkmcnt(6)\n\t"
        "s_sub_u32 s34, s35, s6\n\t"
        "s_cmpk_lt_u32 s34, 0x2000\n\t"
        "s_cbranch_scc0 LB_wa%=x\n\t"
        "s_memrealtime s[6:7]\n\t"
        "s_waitcnt lgkmcnt(6)\n\t"
        "s_sub_u32 s34, s35, s32\n\t"
        "s_cmpk_lt_u32 s34, 0x2000\n\t"
        "s_cbranch_scc1 LB_wa%=\n\t"
        "s_nop 3\n\t"
        "LB_wa%=x:\n\t"
        "s_add_i32 s34, s34, 4\n\t"
        "s_max_i32 s34, s34, 0\n\t"
        "s_bitcmp1_b32 s34, 1\n\t"
        "s_cbranch_scc0 LB_wa%=y\n\t"
        "s_nop 15\n\t"
        "LB_wa%=y:\n\t"
        "s_bitcmp1_b32 s34, 0\n\t"
        "s_cbranch_scc0 LB_wa%=z\n\t"
        "s_nop 9\n\t"
        "LB_wa%=z:\n\t"
        "LB_rd%=:\n\t"
        "buffer_load_dwordx4 v[16:19], v112, s[8:11], s26 offen offset:0 sc1\n\t"
        "s_nop 9\n\t"
        "buffer_load_dwordx4 v[20:23], v112, s[8:11], s26 offen offset:1024 sc1\n\t"
        "s_nop 9\n\t"
        "buffer_load_dwordx4 v[24:27], v112, s[8:11], s26 offen offset:2048 sc1\n\t"
        "s_nop 9\n\t"
        "buffer_load_dwordx4 v[28:31], v112, s[8:11], s26 offen offset:3072 sc1\n\t"
        "LB_go%=:\n\t"
        "s_waitcnt vmcnt(3)\n\t"
        "v_smfmac_f32_16x16x64_bf16 v[0:3], v[16:19], v[128:135], v106\n\t"
        "v_max3_u32 v108, v16, v17, v18\n\t"
        "v_max_u32_e32 v108, v19, v108\n\t"
        "v_smfmac_f32_16x16x64_bf16 v[4:7], v[16:19], v[136:143], v106\n\t"
        "v_smfmac_f32_16x16x64_bf16 v[8:11], v[16:19], v[144:151], v106\n\t"
        "v_smfmac_f32_16x16x64_bf16 v[12:15], v[16:19], v[152:159], v106\n\t"
        "s_waitcnt vmcnt(2)\n\t"
        "v_smfmac_f32_16x16x64_bf16 v[0:3], v[20:23], v[160:167], v106\n\t"
        "v_max3_u32 v108, v20, v21, v108\n\t"
        "v_max3_u32 v108, v22, v23, v108\n\t"
        "v_smfmac_f32_16x16x64_bf16 v[4:7], v[20:23], v[168:175], v106\n\t"
        "v_smfmac_f32_16x16x64_bf16 v[8:11], v[20:23], v[176:183], v106\n\t"
        "v_smfmac_f32_16x16x64_bf16 v[12:15], v[20:23], v[184:191], v106\n\t"
        "s_waitcnt vmcnt(1)\n\t"
        "v_smfmac_f32_16x16x64_bf16 v[0:3], v[24:27], v[192:199], v106\n\t"
        "v_max3_u32 v108, v24, v25, v108\n\t"
        "v_max3_u32 v108, v26, v27, v108\n\t"
        "v_smfmac_f32_16x16x64_bf16 v[4:7], v[24:27], v[200:207], v106\n\t"
        "v_smfmac_f32_16x16x64_bf16 v[8:11], v[24:27], v[208:215], v106\n\t"
        "v_smfmac_f32_16x16x64_bf16 v[12:15], v[24:27], v[216:223], v106\n\t"
        "s_waitcnt vmcnt(0)\n\t"
        "v_smfmac_f32_16x16x64_bf16 v[0:3], v[28:31], v[224:231], v106\n\t"
        "v_max3_u32 v108, v28, v29, v108\n\t"
        "v_max3_u32 v108, v30, v31, v108\n\t"
        "v_smfmac_f32_16x16x64_bf16 v[4:7], v[28:31], v[232:239], v106\n\t"
        "v_cmp_eq_u32_e32 vcc, -1, v108\n\t"
        "v_smfmac_f32_16x16x64_bf16 v[8:11], v[28:31], v[240:247], v106\n\t"
        "v_smfmac_f32_16x16x64_bf16 v[12:15], v[28:31], v[248:255], v106\n\t"
        "s_cbranch_vccnz LB_rdfail%=\n\t"
        "s_nop 0\n\t"
        "v_add_f32_e32 v0, v0, v1\n\t"
        "v_add_f32_e32 v1, v2, v3\n\t"
        "ds_write_b64 v118, v[0:1] offset:0\n\t"
        "v_add_f32_e32 v4, v4, v5\n\t"
        "v_add_f32_e32 v5, v6, v7\n\t"
        "ds_write_b64 v118, v[4:5] offset:512\n\t"
        "v_add_f32_e32 v8, v8, v9\n\t"
        "v_add_f32_e32 v9, v10, v11\n\t"
        "ds_write_b64 v118, v[8:9] offset:1024\n\t"
        "v_add_f32_e32 v12, v12, v13\n\t"
        "v_add_f32_e32 v13, v14, v15\n\t"
        "ds_write_b64 v118, v[12:13] offset:1536\n\t"
        "buffer_store_dwordx2 v[110:111], v117, s[8:11], s26 offen sc1\n\t"
        "s_waitcnt lgkmcnt(0)\n\t"
        "s_barrier\n\t"
        "ds_read_b128 v[16:19], v119 offset:0\n\t"
        "ds_read_b128 v[20:23], v119 offset:2048\n\t"
        "ds_read_b128 v[24:27], v119 offset:4096\n\t"
        "ds_read_b128 v[28:31], v119 offset:6144\n\t"
        "buffer_load_dwordx4 v[64:67], v124, s[8:11], s28 offen sc1\n\t"
        "s_waitcnt lgkmcnt(2)\n\t"
        "v_pk_add_f32 v[16:17], v[16:17], v[20:21]\n\t"
        "v_pk_add_f32 v[18:19], v[18:19], v[22:23]\n\t"
        "s_waitcnt lgkmcnt(0)\n\t"
        "v_pk_add_f32 v[24:25], v[24:25], v[28:29]\n\t"
        "v_pk_add_f32 v[26:27], v[26:27], v[30:31]\n\t"
        "v_pk_add_f32 v[16:17], v[16:17], v[24:25]\n\t"
        "v_pk_add_f32 v[18:19], v[18:19], v[26:27]\n\t"
        "v_cvt_pk_bf16_f32 v20, v16, v17\n\t"
        "v_cvt_pk_bf16_f32 v21, v18, v19\n\t"
        "buffer_store_dwordx2 v[20:21], v114, s[8:11], s26 offen sc1\n\t"
        "s_cmp_eq_u32 s24, 63\n\t"
        "s_cbranch_scc1 LB_end%=\n\t"
        "s_memrealtime s[52:53]\n\t"
        "v_mov_b64_e32 v[24:25], -1\n\t"
        "v_mov_b64_e32 v[32:33], 0\n\t"
        "v_mov_b64_e32 v[34:35], 0\n\t"
        "v_mov_b64_e32 v[36:37], 0\n\t"
        "v_mov_b64_e32 v[38:39], 0\n\t"
        "v_mov_b64_e32 v[40:41], 0\n\t"
        "v_mov_b64_e32 v[42:43], 0\n\t"
        "v_mov_b64_e32 v[44:45], 0\n\t"
        "v_mov_b64_e32 v[46:47], 0\n\t"
        "v_mov_b64_e32 v[0:1], 0\n\t"
        "v_mov_b64_e32 v[2:3], 0\n\t"
        "v_mov_b64_e32 v[4:5], 0\n\t"
        "v_mov_b64_e32 v[6:7], 0\n\t"
        "v_mov_b64_e32 v[8:9], 0\n\t"
        "v_mov_b64_e32 v[10:11], 0\n\t"
        "v_mov_b64_e32 v[12:13], 0\n\t"
        "v_mov_b64_e32 v[14:15], 0\n\t"
        "s_add_u32 s46, s26, 4096\n\t"
        "s_sleep 1\n\t"
        "buffer_load_dwordx2 v[104:105], v113, s[8:11], s26 offen sc1\n\t"
        "buffer_store_dword v24, v116, s[8:11], s28 offen sc1\n\t"
        "s_xor_b32 s34, s26, 0x100000\n\t"
        "buffer_store_dwordx2 v[24:25], v114, s[8:11], s34 offen sc1\n\t"
        "s_add_u32 s66, s49, s43\n\t"
        "s_cmp_lg_u32 s66, 32\n\t"
        "s_cbranch_scc1 LB_norep%=\n\t"
        "s_waitcnt vmcnt(4)\n\t"
        "s_sub_u32 s66, s24, 1\n\t"
        "v_lshrrev_b32_e32 v68, 16, v64\n\t"
        "v_cmp_eq_u32_e32 vcc, s66, v68\n\t"
        "v_subrev_u32_e32 v64, s59, v64\n\t"
        "v_bfe_i32 v64, v64, 0, 16\n\t"
        "v_cndmask_b32_e32 v64, 0, v64, vcc\n\t"
        "v_lshrrev_b32_e32 v68, 16, v65\n\t"
        "v_cmp_eq_u32_e32 vcc, s66, v68\n\t"
        "v_subrev_u32_e32 v65, s59, v65\n\t"
        "v_bfe_i32 v65, v65, 0, 16\n\t"
        "v_cndmask_b32_e32 v65, 0, v65, vcc\n\t"
        "v_lshrrev_b32_e32 v68, 16, v66\n\t"
        "v_cmp_eq_u32_e32 vcc, s66, v68\n\t"
        "v_subrev_u32_e32 v66, s59, v66\n\t"
        "v_bfe_i32 v66, v66, 0, 16\n\t"
        "v_cndmask_b32_e32 v66, 0, v66, vcc\n\t"
        "v_lshrrev_b32_e32 v68, 16, v67\n\t"
        "v_cmp_eq_u32_e32 vcc, s66, v68\n\t"
        "v_subrev_u32_e32 v67, s59, v67\n\t"
        "v_bfe_i32 v67, v67, 0, 16\n\t"
        "v_cndmask_b32_e32 v67, 0, v67, vcc\n\t"
        "v_max3_i32 v64, v64, v65, v66\n\t"
        "v_max_i32_e32 v64, v64, v67\n\t"
        "s_nop 1\n\t"
        "v_max_i32_dpp v64, v64, v64 quad_perm:[1,0,3,2] row_mask:0xf bank_mask:0xf\n\t"
        "s_nop 1\n\t"
        "v_max_i32_dpp v64, v64, v64 quad_perm:[2,3,0,1] row_mask:0xf bank_mask:0xf\n\t"
        "s_nop 1\n\t"
        "v_max_i32_dpp v64, v64, v64 row_ror:4 row_mask:0xf bank_mask:0xf\n\t"
        "s_nop 1\n\t"
        "v_max_i32_dpp v64, v64, v64 row_ror:8 row_mask:0xf bank_mask:0xf\n\t"
        "v_mov_b32_e32 v65, v64\n\t"
        "s_nop 1\n\t"
        "v_permlane16_swap_b32_e32 v64, v65\n\t"
        "v_max_i32_e32 v64, v64, v65\n\t"
        "v_mov_b32_e32 v65, v64\n\t"
        "s_nop 1\n\t"
        "v_permlane32_swap_b32_e32 v64, v65\n\t"
        "v_max_i32_e32 v64, v64, v65\n\t"
        "s_nop 0\n\t"
        "v_readfirstlane_b32 s64, v64\n\t"
        "LB_norep%=:\n\t"
        "s_waitcnt vmcnt(2) lgkmcnt(0)\n\t"
        "s_mov_b32 s61, s29\n\t"
        "s_mov_b32 s62, s30\n\t"
        "s_mov_b32 s63, s31\n\t"
        "v_and_b32_e32 v30, 0xff, v105\n\t"
        "v_cmp_ne_u32_e32 vcc, s24, v30\n\t"
        "s_nop 0\n\t"
        "s_cbranch_vccnz LB_stre%=\n\t"
        "LB_stok%=:\n\t"
        "v_subrev_u32_e32 v104, s25, v104\n\t"
        "s_nop 1\n\t"
        "v_max_i32_dpp v104, v104, v104 quad_perm:[1,0,3,2] row_mask:0xf bank_mask:0xf\n\t"
        "s_nop 1\n\t"
        "v_max_i32_dpp v104, v104, v104 quad_perm:[2,3,0,1] row_mask:0xf bank_mask:0xf\n\t"
        "s_nop 1\n\t"
        "v_max_i32_dpp v104, v104, v104 row_ror:4 row_mask:0xf bank_mask:0xf\n\t"
        "s_nop 1\n\t"
        "v_max_i32_dpp v104, v104, v104 row_ror:8 row_mask:0xf bank_mask:0xf\n\t"
        "s_nop 1\n\t"
        "v_max_i32_dpp v104, v104, v104 row_bcast:15 row_mask:0xa bank_mask:0xf\n\t"
        "s_nop 1\n\t"
        "v_max_i32_dpp v104, v104, v104 row_bcast:31 row_mask:0xc bank_mask:0xf\n\t"
        "v_readfirstlane_b32 s35, v105\n\t"
        "v_readlane_b32 s36, v104, 63\n\t"
        "s_bfe_u32 s29, s35, 0xc0008\n\t"
        "s_lshr_b32 s30, s35, 20\n\t"
        "s_add_u32 s35, s31, s50\n\t"
        "s_mul_i32 s37, s50, 3\n\t"
        "s_min_u32 s35, s35, s37\n\t"
        "s_add_i32 s34, s36, s51\n\t"
        "s_cmp_gt_i32 s34, 0\n\t"
        "s_cselect_b32 s31, s35, 0\n\t"
        "s_cselect_b32 s34, s34, 0\n\t"
        "s_add_u32 s34, s34, s31\n\t"
        "s_add_u32 s37, s25, s34\n\t"
        "s_add_u32 s45, s37, s29\n\t"
        "s_add_u32 s25, s45, s30\n\t"
        "s_add_u32 s66, s49, s43\n\t"
        "s_cmp_lg_u32 s66, 32\n\t"
        "s_cbranch_scc1 LB_noctl%=\n\t"
        "s_cmp_eq_u32 s24, 0\n\t"
        "s_cbranch_scc1 LB_ctl0%=\n\t"
        "s_cmp_lt_u32 s24, 6\n\t"
        "s_cselect_b32 s68, 32, 8\n\t"
        "s_add_i32 s65, s36, s62\n\t"
        "s_cmp_eq_u32 s24, 1\n\t"
        "s_cselect_b32 s34, 6, 0\n\t"
        "s_cselect_b32 s35, 3, 0\n\t"
        "s_sub_u32 s64, s64, s34\n\t"
        "s_sub_u32 s65, s65, s35\n\t"
        "s_lshl_b32 s66, s65, 1\n\t"
        "s_sub_i32 s66, s66, s56\n\t"
        "s_cmp_gt_i32 s66, 56\n\t"
        "s_cbranch_scc1 LB_c1%=\n\t"
        "s_lshl_b32 s66, s65, 1\n\t"
        "s_add_u32 s34, s56, s68\n\t"
        "s_min_i32 s34, s34, s66\n\t"
        "s_sub_u32 s35, s56, s66\n\t"
        "s_ashr_i32 s35, s35, 1\n\t"
        "s_max_i32 s35, s35, 1\n\t"
        "s_sub_u32 s35, s56, s35\n\t"
        "s_max_i32 s56, s34, s35\n\t"
        "s_branch LB_c2%=\n\t"
        "LB_c1%=:\n\t"
        "s_add_u32 s56, s56, 2\n\t"
        "s_add_i32 s66, s64, s57\n\t"
        "s_cmp_gt_i32 s66, s61\n\t"
        "s_cbranch_scc1 LB_c2%=\n\t"
        "s_add_u32 s57, s57, 4\n\t"
        "s_min_u32 s57, s57, 24\n\t"
        "LB_c2%=:\n\t"
        "s_cmp_lg_u32 s63, 0\n\t"
        "s_cbranch_scc1 LB_c4%=\n\t"
        "s_lshl_b32 s66, s64, 1\n\t"
        "s_sub_i32 s66, s66, s55\n\t"
        "s_cmp_gt_i32 s66, 56\n\t"
        "s_cbranch_scc1 LB_c3%=\n\t"
        "s_lshl_b32 s66, s64, 1\n\t"
        "s_add_u32 s34, s55, s68\n\t"
        "s_min_i32 s34, s34, s66\n\t"
        "s_sub_u32 s35, s55, s66\n\t"
        "s_ashr_i32 s35, s35, 1\n\t"
        "s_max_i32 s35, s35, 1\n\t"
        "s_sub_u32 s35, s55, s35\n\t"
        "s_max_i32 s55, s34, s35\n\t"
        "s_branch LB_c4%=\n\t"
        "LB_c3%=:\n\t"
        "s_add_u32 s55, s55, 2\n\t"
        "s_cmp_lt_i32 s60, s58\n\t"
        "s_cbranch_scc1 LB_c4%=\n\t"
        "s_add_u32 s66, s58, 8\n\t"
        "s_cmp_gt_i32 s60, s66\n\t"
        "s_cbranch_scc1 LB_c4%=\n\t"
        "s_add_u32 s58, s58, 4\n\t"
        "s_min_u32 s58, s58, 22\n\t"
        "LB_c4%=:\n\t"
        "s_and_b32 s66, s24, 3\n\t"
        "s_cmp_eq_u32 s66, 0\n\t"
        "s_cselect_b32 s66, 1, 0\n\t"
        "s_cmp_lt_u32 s24, 30\n\t"
        "s_cselect_b32 s34, 8, 8\n\t"
        "s_cselect_b32 s35, 6, 6\n\t"
        "s_sub_u32 s57, s57, s66\n\t"
        "s_max_i32 s57, s57, s34\n\t"
        "s_sub_u32 s58, s58, s66\n\t"
        "s_max_i32 s58, s58, s35\n\t"
        "s_add_u32 s34, s55, 1\n\t"
        "s_lshr_b32 s34, s34, 1\n\t"
        "s_add_u32 s34, s34, s57\n\t"
        "s_min_u32 s34, s34, 1000\n\t"
        "s_lshl_b32 s34, s34, 8\n\t"
        "s_add_u32 s35, s56, 1\n\t"
        "s_lshr_b32 s35, s35, 1\n\t"
        "s_add_u32 s35, s35, s58\n\t"
        "s_min_u32 s35, s35, 1000\n\t"
        "s_lshl_b32 s35, s35, 20\n\t"
        "s_or_b32 s54, s34, s35\n\t"
        "s_branch LB_ctl1%=\n\t"
        "LB_ctl0%=:\n\t"
        "s_movk_i32 s57, 8\n\t"
        "s_movk_i32 s58, 6\n\t"
        "s_sub_u32 s55, s29, 32\n\t"
        "s_lshl_b32 s55, s55, 1\n\t"
        "s_sub_u32 s56, s30, 18\n\t"
        "s_lshl_b32 s56, s56, 1\n\t"
        "LB_ctl1%=:\n\t"
        "s_mov_b32 s59, s37\n\t"
        "s_sub_u32 s60, 0, s36\n\t"
        "LB_noctl%=:\n\t"
        "s_cmp_eq_u32 s24, 0\n\t"
        "s_cselect_b32 s34, 1537, 1\n\t"
        "s_add_u32 s34, s34, s24\n\t"
        "s_add_u32 s34, s34, s54\n\t"
        "v_mov_b32_e32 v111, s34\n\t"
        "s_mov_b32 s42, 0\n\t"
        "s_sub_u32 s35, s45, 8\n\t"
        "s_waitcnt lgkmcnt(0)\n\t"
        "s_memrealtime s[2:3]\n\t"
        "s_memrealtime s[4:5]\n\t"
        "s_memrealtime s[6:7]\n\t"
        "LB_wo%=:\n\t"
        "s_memrealtime s[32:33]\n\t"
        "s_waitcnt lgkmcnt(6)\n\t"
        "s_sub_u32 s34, s35, s2\n\t"
        "s_cmpk_lt_u32 s34, 0x2000\n\t"
        "s_cbranch_scc0 LB_wo%=x\n\t"
        "s_memrealtime s[2:3]\n\t"
        "s_waitcnt lgkmcnt(6)\n\t"
        "s_sub_u32 s34, s35, s4\n\t"
        "s_cmpk_lt_u32 s34, 0x2000\n\t"
        "s_cbranch_scc0 LB_wo%=x\n\t"
        "s_memrealtime s[4:5]\n\t"
        "s_waitcnt lgkmcnt(6)\n\t"
        "s_sub_u32 s34, s35, s6\n\t"
        "s_cmpk_lt_u32 s34, 0x2000\n\t"
        "s_cbranch_scc0 LB_wo%=x\n\t"
        "s_memrealtime s[6:7]\n\t"
        "s_waitcnt lgkmcnt(6)\n\t"
        "s_sub_u32 s34, s35, s32\n\t"
        "s_cmpk_lt_u32 s34, 0x2000\n\t"
        "s_cbranch_scc1 LB_wo%=\n\t"
        "s_nop 3\n\t"
        "LB_wo%=x:\n\t"
        "s_add_i32 s34, s34, 4\n\t"
        "s_max_i32 s34, s34, 0\n\t"
        "s_bitcmp1_b32 s34, 1\n\t"
        "s_cbranch_scc0 LB_wo%=y\n\t"
        "s_nop 15\n\t"
        "LB_wo%=y:\n\t"
        "s_bitcmp1_b32 s34, 0\n\t"
        "s_cbranch_scc0 LB_wo%=z\n\t"
        "s_nop 9\n\t"
        "LB_wo%=z:\n\t"
        "LB_ro%=:\n\t"
        "buffer_load_dwordx4 v[64:67], v115, s[8:11], s26 offen offset:0 sc1\n\t"
        "s_nop 12\n\t"
        "buffer_load_dwordx4 v[68:71], v115, s[8:11], s26 offen offset:1024 sc1\n\t"
        "s_nop 12\n\t"
        "buffer_load_dwordx4 v[72:75], v115, s[8:11], s26 offen offset:2048 sc1\n\t"
        "s_nop 12\n\t"
        "buffer_load_dwordx4 v[76:79], v115, s[8:11], s26 offen offset:3072 sc1\n\t"
        "s_nop 12\n\t"
        "buffer_load_dwordx4 v[80:83], v115, s[8:11], s46 offen offset:0 sc1\n\t"
        "s_nop 12\n\t"
        "buffer_load_dwordx4 v[84:87], v115, s[8:11], s46 offen offset:1024 sc1\n\t"
        "s_nop 12\n\t"
        "buffer_load_dwordx4 v[88:91], v115, s[8:11], s46 offen offset:2048 sc1\n\t"
        "s_nop 12\n\t"
        "buffer_load_dwordx4 v[92:95], v115, s[8:11], s46 offen offset:3072 sc1\n\t"
        "LB_go2%=:\n\t"
        "s_waitcnt vmcnt(7)\n\t"
        "v_smfmac_f32_16x16x64_bf16 v[32:35], v[64:67], a[0:7], v106\n\t"
        "v_max3_u32 v108, v64, v65, v66\n\t"
        "v_max_u32_e32 v108, v67, v108\n\t"
        "v_smfmac_f32_16x16x64_bf16 v[40:43], v[64:67], a[16:23], v106\n\t"
        "v_smfmac_f32_16x16x64_bf16 v[36:39], v[64:67], a[8:15], v106\n\t"
        "v_smfmac_f32_16x16x64_bf16 v[44:47], v[64:67], a[24:31], v106\n\t"
        "s_waitcnt vmcnt(6)\n\t"
        "v_smfmac_f32_16x16x64_bf16 v[32:35], v[68:71], a[32:39], v106\n\t"
        "v_max3_u32 v108, v68, v69, v108\n\t"
        "v_max3_u32 v108, v70, v71, v108\n\t"
        "v_smfmac_f32_16x16x64_bf16 v[40:43], v[68:71], a[48:55], v106\n\t"
        "v_smfmac_f32_16x16x64_bf16 v[36:39], v[68:71], a[40:47], v106\n\t"
        "v_smfmac_f32_16x16x64_bf16 v[44:47], v[68:71], a[56:63], v106\n\t"
        "s_waitcnt vmcnt(5)\n\t"
        "v_smfmac_f32_16x16x64_bf16 v[32:35], v[72:75], a[64:71], v106\n\t"
        "v_max3_u32 v108, v72, v73, v108\n\t"
        "v_max3_u32 v108, v74, v75, v108\n\t"
        "v_smfmac_f32_16x16x64_bf16 v[40:43], v[72:75], a[80:87], v106\n\t"
        "v_smfmac_f32_16x16x64_bf16 v[36:39], v[72:75], a[72:79], v106\n\t"
        "v_smfmac_f32_16x16x64_bf16 v[44:47], v[72:75], a[88:95], v106\n\t"
        "s_waitcnt vmcnt(4)\n\t"
        "v_smfmac_f32_16x16x64_bf16 v[32:35], v[76:79], a[96:103], v106\n\t"
        "v_max3_u32 v108, v76, v77, v108\n\t"
        "v_max3_u32 v108, v78, v79, v108\n\t"
        "v_smfmac_f32_16x16x64_bf16 v[40:43], v[76:79], a[112:119], v106\n\t"
        "v_smfmac_f32_16x16x64_bf16 v[36:39], v[76:79], a[104:111], v106\n\t"
        "v_smfmac_f32_16x16x64_bf16 v[44:47], v[76:79], a[120:127], v106\n\t"
        "s_waitcnt vmcnt(3)\n\t"
        "v_smfmac_f32_16x16x64_bf16 v[32:35], v[80:83], a[128:135], v106\n\t"
        "v_max3_u32 v108, v80, v81, v108\n\t"
        "v_max3_u32 v108, v82, v83, v108\n\t"
        "v_smfmac_f32_16x16x64_bf16 v[40:43], v[80:83], a[144:151], v106\n\t"
        "v_smfmac_f32_16x16x64_bf16 v[36:39], v[80:83], a[136:143], v106\n\t"
        "v_smfmac_f32_16x16x64_bf16 v[44:47], v[80:83], a[152:159], v106\n\t"
        "s_waitcnt vmcnt(2)\n\t"
        "v_smfmac_f32_16x16x64_bf16 v[32:35], v[84:87], a[160:167], v106\n\t"
        "v_max3_u32 v108, v84, v85, v108\n\t"
        "v_max3_u32 v108, v86, v87, v108\n\t"
        "v_smfmac_f32_16x16x64_bf16 v[40:43], v[84:87], a[176:183], v106\n\t"
        "v_smfmac_f32_16x16x64_bf16 v[36:39], v[84:87], a[168:175], v106\n\t"
        "v_smfmac_f32_16x16x64_bf16 v[44:47], v[84:87], a[184:191], v106\n\t"
        "s_waitcnt vmcnt(1)\n\t"
        "v_smfmac_f32_16x16x64_bf16 v[32:35], v[88:91], a[192:199], v106\n\t"
        "v_max3_u32 v108, v88, v89, v108\n\t"
        "v_max3_u32 v108, v90, v91, v108\n\t"
        "v_smfmac_f32_16x16x64_bf16 v[40:43], v[88:91], a[208:215], v106\n\t"
        "v_smfmac_f32_16x16x64_bf16 v[36:39], v[88:91], a[200:207], v106\n\t"
        "v_smfmac_f32_16x16x64_bf16 v[44:47], v[88:91], a[216:223], v106\n\t"
        "s_waitcnt vmcnt(0)\n\t"
        "v_smfmac_f32_16x16x64_bf16 v[32:35], v[92:95], a[224:231], v106\n\t"
        "v_max3_u32 v108, v92, v93, v108\n\t"
        "v_max3_u32 v108, v94, v95, v108\n\t"
        "v_smfmac_f32_16x16x64_bf16 v[40:43], v[92:95], a[240:247], v106\n\t"
        "v_cmp_eq_u32_e32 vcc, -1, v108\n\t"
        "v_smfmac_f32_16x16x64_bf16 v[36:39], v[92:95], a[232:239], v106\n\t"
        "v_smfmac_f32_16x16x64_bf16 v[44:47], v[92:95], a[248:255], v106\n\t"
        "s_cbranch_vccnz LB_rofail%=\n\t"
        "s_nop 0\n\t"
        "v_add_f32_e32 v32, v32, v33\n\t"
        "s_nop 1\n\t"
        "v_add_f32_e32 v40, v42, v43\n\t"
        "v_add_f32_e32 v32, v32, v40\n\t"
        "v_add_f32_e32 v36, v36, v37\n\t"
        "v_add_f32_e32 v44, v46, v47\n\t"
        "v_add_f32_e32 v36, v36, v44\n\t"
        "ds_write2_b32 v120, v32, v36 offset0:0 offset1:64\n\t"
        "s_waitcnt lgkmcnt(0)\n\t"
        "s_barrier\n\t"
        "ds_read_b64 v[64:65], v121 offset:0\n\t"
        "ds_read_b64 v[66:67], v121 offset:512\n\t"
        "ds_read_b64 v[68:69], v121 offset:1024\n\t"
        "ds_read_b64 v[70:71], v121 offset:1536\n\t"
        "s_waitcnt lgkmcnt(2)\n\t"
        "v_pk_add_f32 v[64:65], v[64:65], v[66:67]\n\t"
        "s_waitcnt lgkmcnt(0)\n\t"
        "v_pk_add_f32 v[68:69], v[68:69], v[70:71]\n\t"
        "v_pk_add_f32 v[64:65], v[64:65], v[68:69]\n\t"
        "v_cvt_pk_bf16_f32 v64, v64, v65\n\t"
        "buffer_store_dword v64, v116, s[8:11], s27 offen sc1\n\t"
        "s_memrealtime s[32:33]\n\t"
        "s_waitcnt lgkmcnt(0)\n\t"
        "v_mov_b32_e32 v110, s32\n\t"
        "s_add_u32 s24, s24, 1\n\t"
        "s_mov_b32 s28, s26\n\t"
        "s_mov_b32 s26, s27\n\t"
        "s_add_u32 s27, s27, 0x80000\n\t"
        "s_and_b32 s27, s27, 0x1fffff\n\t"
        "s_branch LB_loop%=\n\t"
        "LB_end%=:\n\t"
        "s_endpgm\n\t"
        "LB_stre%=:\n\t"
        "s_mov_b32 s42, 0\n\t"
        "LB_str2%=:\n\t"
        "buffer_load_dwordx2 v[104:105], v113, s[8:11], s26 offen sc1\n\t"
        "s_waitcnt vmcnt(0)\n\t"
        "v_and_b32_e32 v30, 0xff, v105\n\t"
        "v_cmp_ne_u32_e32 vcc, s24, v30\n\t"
        "s_nop 0\n\t"
        "s_cbranch_vccz LB_stok%=\n\t"
        "s_add_u32 s42, s42, 1\n\t"
        "s_cmp_lt_u32 s42, 0x100000\n\t"
        "s_cbranch_scc1 LB_str2%=\n\t"
        "s_movk_i32 s34, 5\n\t"
        "s_branch LB_die%=\n\t"
        "LB_rdfail%=:\n\t"
        "s_or_b32 s47, s47, 1\n\t"
        "buffer_load_dwordx4 v[16:19], v112, s[8:11], s26 offen offset:0 sc1\n\t"
        "buffer_load_dwordx4 v[20:23], v112, s[8:11], s26 offen offset:1024 sc1\n\t"
        "buffer_load_dwordx4 v[24:27], v112, s[8:11], s26 offen offset:2048 sc1\n\t"
        "buffer_load_dwordx4 v[28:31], v112, s[8:11], s26 offen offset:3072 sc1\n\t"
        "s_nop 7\n\t"
        "v_mov_b64_e32 v[0:1], 0\n\t"
        "v_mov_b64_e32 v[2:3], 0\n\t"
        "v_mov_b64_e32 v[4:5], 0\n\t"
        "v_mov_b64_e32 v[6:7], 0\n\t"
        "v_mov_b64_e32 v[8:9], 0\n\t"
        "v_mov_b64_e32 v[10:11], 0\n\t"
        "v_mov_b64_e32 v[12:13], 0\n\t"
        "v_mov_b64_e32 v[14:15], 0\n\t"
        "s_branch LB_rdre%=w\n\t"
        "LB_rdre%=:\n\t"
        "buffer_load_dwordx4 v[16:19], v112, s[8:11], s26 offen offset:0 sc1\n\t"
        "buffer_load_dwordx4 v[20:23], v112, s[8:11], s26 offen offset:1024 sc1\n\t"
        "buffer_load_dwordx4 v[24:27], v112, s[8:11], s26 offen offset:2048 sc1\n\t"
        "buffer_load_dwordx4 v[28:31], v112, s[8:11], s26 offen offset:3072 sc1\n\t"
        "LB_rdre%=w:\n\t"
        "s_waitcnt vmcnt(0) lgkmcnt(0)\n\t"
        "v_max3_u32 v108, v16, v17, v18\n\t"
        "v_max3_u32 v108, v108, v19, v20\n\t"
        "v_max3_u32 v108, v108, v21, v22\n\t"
        "v_max3_u32 v108, v108, v23, v24\n\t"
        "v_max3_u32 v108, v108, v25, v26\n\t"
        "v_max3_u32 v108, v108, v27, v28\n\t"
        "v_max3_u32 v108, v108, v29, v30\n\t"
        "v_max_u32_e32 v108, v108, v31\n\t"
        "v_cmp_eq_u32_e32 vcc, -1, v108\n\t"
        "s_nop 0\n\t"
        "s_cbranch_vccz LB_go%=\n\t"
        "s_add_u32 s42, s42, 1\n\t"
        "s_cmp_lt_u32 s42, 0x100000\n\t"
        "s_cbranch_scc1 LB_rdre%=\n\t"
        "s_movk_i32 s34, 3\n\t"
        "s_branch LB_die%=\n\t"
        "LB_rofail%=:\n\t"
        "s_or_b32 s47, s47, 4\n\t"
        "buffer_load_dwordx4 v[64:67], v115, s[8:11], s26 offen offset:0 sc1\n\t"
        "buffer_load_dwordx4 v[68:71], v115, s[8:11], s26 offen offset:1024 sc1\n\t"
        "buffer_load_dwordx4 v[72:75], v115, s[8:11], s26 offen offset:2048 sc1\n\t"
        "buffer_load_dwordx4 v[76:79], v115, s[8:11], s26 offen offset:3072 sc1\n\t"
        "buffer_load_dwordx4 v[80:83], v115, s[8:11], s46 offen offset:0 sc1\n\t"
        "buffer_load_dwordx4 v[84:87], v115, s[8:11], s46 offen offset:1024 sc1\n\t"
        "buffer_load_dwordx4 v[88:91], v115, s[8:11], s46 offen offset:2048 sc1\n\t"
        "buffer_load_dwordx4 v[92:95], v115, s[8:11], s46 offen offset:3072 sc1\n\t"
        "s_nop 7\n\t"
        "v_mov_b64_e32 v[32:33], 0\n\t"
        "v_mov_b64_e32 v[34:35], 0\n\t"
        "v_mov_b64_e32 v[36:37], 0\n\t"
        "v_mov_b64_e32 v[38:39], 0\n\t"
        "v_mov_b64_e32 v[40:41], 0\n\t"
        "v_mov_b64_e32 v[42:43], 0\n\t"
        "v_mov_b64_e32 v[44:45], 0\n\t"
        "v_mov_b64_e32 v[46:47], 0\n\t"
        "s_branch LB_rore%=w\n\t"
        "LB_rore%=:\n\t"
        "buffer_load_dwordx4 v[64:67], v115, s[8:11], s26 offen offset:0 sc1\n\t"
        "buffer_load_dwordx4 v[68:71], v115, s[8:11], s26 offen offset:1024 sc1\n\t"
        "buffer_load_dwordx4 v[72:75], v115, s[8:11], s26 offen offset:2048 sc1\n\t"
        "buffer_load_dwordx4 v[76:79], v115, s[8:11], s26 offen offset:3072 sc1\n\t"
        "buffer_load_dwordx4 v[80:83], v115, s[8:11], s46 offen offset:0 sc1\n\t"
        "buffer_load_dwordx4 v[84:87], v115, s[8:11], s46 offen offset:1024 sc1\n\t"
        "buffer_load_dwordx4 v[88:91], v115, s[8:11], s46 offen offset:2048 sc1\n\t"
        "buffer_load_dwordx4 v[92:95], v115, s[8:11], s46 offen offset:3072 sc1\n\t"
        "LB_rore%=w:\n\t"
        "s_waitcnt vmcnt(0) lgkmcnt(0)\n\t"
        "v_max3_u32 v108, v64, v65, v66\n\t"
        "v_max3_u32 v108, v108, v67, v68\n\t"
        "v_max3_u32 v108, v108, v69, v70\n\t"
        "v_max3_u32 v108, v108, v71, v72\n\t"
        "v_max3_u32 v108, v108, v73, v74\n\t"
        "v_max3_u32 v108, v108, v75, v76\n\t"
        "v_max3_u32 v108, v108, v77, v78\n\t"
        "v_max3_u32 v108, v108, v79, v80\n\t"
        "v_max3_u32 v108, v108, v81, v82\n\t"
        "v_max3_u32 v108, v108, v83, v84\n\t"
        "v_max3_u32 v108, v108, v85, v86\n\t"
        "v_max3_u32 v108, v108, v87, v88\n\t"
        "v_max3_u32 v108, v108, v89, v90\n\t"
        "v_max3_u32 v108, v108, v91, v92\n\t"
        "v_max3_u32 v108, v108, v93, v94\n\t"
        "v_max_u32_e32 v108, v108, v95\n\t"
        "v_cmp_eq_u32_e32 vcc, -1, v108\n\t"
        "s_nop 0\n\t"
        "s_cbranch_vccz LB_go2%=\n\t"
        "s_add_u32 s42, s42, 1\n\t"
        "s_cmp_lt_u32 s42, 0x100000\n\t"
        "s_cbranch_scc1 LB_rore%=\n\t"
        "s_movk_i32 s34, 4\n\t"
        "s_branch LB_die%=\n\t"
        "LB_die%=:\n\t"
        "v_mov_b32_e32 v30, s34\n\t"
        "s_mov_b32 s35, 33024\n\t"
        "s_mov_b64 exec, 1\n\t"
        "buffer_store_dword v30, off, s[12:15], s35 sc1\n\t"
        "s_endpgm\n\t"
        ".Lk_end_B:\n\t"
        :: REC_S_IN, "{v96}"(g_v), "{v97}"(g_o), "{v98}"(wb0), "{v99}"(wb1), "{v100}"(rb0), "{v101}"(rb0 ^ 32u), "{v102}"(rb0 ^ 64u), "{v103}"(rb0 ^ 96u),
           "{v106}"((l & 1) ? 0xEEEEEEEEu : 0x44444444u), "{v[108:109]}"(m0p),
           "{v112}"(a_mem), "{v113}"((24 + cp) * 16384 + l * 32), "{v114}"(a_vmw), "{v115}"(a_or), "{v116}"(a_mw), "{v117}"(a_sw),
           "{v118}"(lb + w * 2048 + (g >> 1) * 256 + n * 16 + (g & 1) * 8), "{v119}"(lb + (w & 1) * 1024 + l * 16), "{v120}"(lb + LB_R + w * 512 + l * 4),
           "{v121}"(lb + LB_R + l * 8), "{v122}"(stg + l * 16), "{v123}"(o_r), "{v124}"(rb + w == 32 ? 28 * 16384 + (l & 31) * 128 : OOB)
        : "memory", "vcc", "scc", "v0", "v255", "a0", "a255", "s101");
    __builtin_unreachable();
}
__device__ __forceinline__ void rec_helper(const Params& p, rsrc_t r, unsigned lb, const int wv, const int hi) {
    const int tid = otid(), l = tid & 63, xq = hi >> 3;
    const rsrc_t rh = __builtin_amdgcn_make_buffer_rsrc(p.ws + ho_base(p.ws), 0, HO_BYTES, 0x00020000);
    const v4u ones = {0xFFFFFFFFu, 0xFFFFFFFFu, 0xFFFFFFFFu, 0xFFFFFFFFu};
    const bool meas = (hi & 7) < 2;
    if (meas) {
        const unsigned ca = l >= 8 ? OOB : (31 + wv * 32) * 16384 + hi * 256 + l * 16, tend = rtnow() + CAL_T;
        unsigned best0 = ~0u, best1 = ~0u;
#pragma unroll 1
        for (int i = 0; i < 64 && (i < 3 || (int)(rtnow() - tend) < 0); ++i) {
            const unsigned t0 = (unsigned)__builtin_amdgcn_s_memtime(); __builtin_amdgcn_s_waitcnt(0);
            st16(rh, ca, ones); DRAIN();
            const unsigned t1 = (unsigned)__builtin_amdgcn_s_memtime(); __builtin_amdgcn_s_waitcnt(0);
            st16(rh, ca + 8192, ones); DRAIN();
            const unsigned t2 = (unsigned)__builtin_amdgcn_s_memtime(); __builtin_amdgcn_s_waitcnt(0);
            best0 = min(best0, t1 - t0); best1 = min(best1, t2 - t1); }
        if (l < 2) __hip_atomic_fetch_max((unsigned*)(p.ws + F_CAL + 128) + l, 65535u - min(l ? best1 : best0, 65535u), __ATOMIC_RELAXED, __HIP_MEMORY_SCOPE_AGENT);
    }
    auto touch = [&](int z0, int z1) {
#pragma unroll 1
        for (int z = z0 + ((hi & 7) - 2) * 4 + wv; z < z1; z += 24) { const bf16_t* q;
            if (z < 48) q = ((z & 1) ? p.Wk : p.Wq) + (size_t)(((((z & 1) * 2 + 1) * 4 + 4 + (z >> 2)) & 15) * 64 + l) * 4096 + (xq * 8 + 1 + (z & 2) * 2) * 128 + 64;
            else if (z < 112) { const int y = z - 48; q = p.Wo + (size_t)(((y & 3) * 8 + (y >> 3)) * 128 + ((y >> 2) & 1) * 64 + l) * 1024 + (xq & 1) * 512 + 192; }
            else { const int s7 = (z - 112) % 7, y = (z - 112) / 7; q = p.Wo + (size_t)(((y & 3) * 8 + 8 - T2U + (y >> 3)) * 128 + ((y >> 2) & 1) * 64 + l) * 1024 + (xq & 1) * 512 + (s7 + (s7 >= 3)) * 64; }
            touch_l2(q, lb + 1024); } };
    if (!meas) touch(0, TPRE);
    st16(r, tid < 64 ? F_KVQ + hi * 1024 + tid * 16 : tid < 72 ? F_PROG + hi * 128 + (tid - 64) * 16 : tid < 80 ? F_CVY + hi * 128 + (tid - 72) * 16 : OOB, (v4u){0u, 0u, 0u, 0u});
#pragma unroll
    for (int i = 0; i < 2; ++i) st16(r, W_OX + 63 * 262144 + hi * 8192 + i * 4096 + tid * 16, ones);
#pragma unroll
    for (int i = 0; i < 16; ++i) st16(rh, hi * 65536 + i * 4096 + tid * 16, ones);
    DRAIN();
    __syncthreads();
    if (tid == 0 && __hip_atomic_fetch_add((unsigned*)(p.ws + F_CAL + 256), 1u, __ATOMIC_RELAXED, __HIP_MEMORY_SCOPE_AGENT) == 31u) {
        const v2u c = ld8(r, F_CAL + 128); st4(r, F_CAL, c.x < c.y ? 8193u : 1u); }
    if (!meas) touch(TPRE, 112 + 56 * T2U);
    DRAIN();
}
constexpr int CV_RP = 80;
constexpr int CV_U = 96 * CV_RP;
constexpr int CV_KP = 4416;
constexpr int CV_K0 = 8 * CV_U;
#define NAP() do { __builtin_amdgcn_sched_barrier(0); __builtin_amdgcn_s_sleep(XSLP); __builtin_amdgcn_sched_barrier(0); } while (0)
__device__ __forceinline__ void gate_block(const Params& p, rsrc_t r, const int hi, const int wv) {
    constexpr int NT = 4;
    const int l = otid() & 63, t0 = (hi * 4 + wv) * NT;
    v4u xg[NT][2], gw[2];
#pragma unroll
    for (int q = 0; q < 2; ++q) gw[q] = *(const v4u*)(p.gate_w + q * 512 + l * 8);
#pragma unroll
    for (int i = 0; i < NT; ++i)
#pragma unroll
        for (int q = 0; q < 2; ++q) { xg[i][q] = *(const v4u*)(p.Aq + (size_t)(t0 + i) * D + q * 512 + l * 8); NAP(); }
    float mine = 0.f;
#pragma unroll
    for (int i = 0; i < NT; ++i) { float acc = 0.f;
#pragma unroll
        for (int q = 0; q < 2; ++q)
#pragma unroll
            for (int e = 0; e < 4; ++e) { const unsigned pr = pack2(blo(xg[i][q][e]) * blo(gw[q][e]), bhi(xg[i][q][e]) * bhi(gw[q][e])); acc += blo(pr); acc += bhi(pr); }
        for (int o = 32; o > 0; o >>= 1) acc += __shfl_xor(acc, o);
        if (l == i) mine = acc; }
    if (l < NT) { const float z = rbfh(mine); st4(r, W_GATE + (t0 + l) * 4, __float_as_uint(rbfh(1.f / (1.f + expf(-z))))); }
}
__device__ __forceinline__ void conv_prep(const Params& p, rsrc_t r, const int wv, const int hi, const int G) {
    const int tid = otid(), Q = tid >> 2, cg = tid & 3, ch0 = hi * 32 + cg * 8;
    v4u sk[4]; float sv[8][4];
#pragma unroll
    for (int q = 0; q < 4; ++q) sk[q] = *(const v4u*)(p.shift_k + ch0 * 4 + q * 8);
#pragma unroll
    for (int c8 = 0; c8 < 8; ++c8)
#pragma unroll
        for (int tp = 0; tp < 4; ++tp) { const unsigned sw = sk[c8 >> 1][(c8 & 1) * 2 + (tp >> 1)]; sv[c8][tp] = (tp & 1) ? bhi(sw) : blo(sw); }
    const rsrc_t rk = __builtin_amdgcn_make_buffer_rsrc((void*)p.Ak, 0, S * D * 2, 0x00020000), rv = __builtin_amdgcn_make_buffer_rsrc((void*)p.Av, 0, S * D * 2, 0x00020000),
                 rq = __builtin_amdgcn_make_buffer_rsrc((void*)p.Aq, 0, S * D * 2, 0x00020000);
    {
        const unsigned ro = (unsigned)((Q * 32 + G * 8) * D + ch0) * 2u, uo = hi * 131072 + tid * 16;
        v4u kr[11], u[8], qv[8];
#pragma unroll
        for (int i = 0; i < 11; ++i) { kr[i] = __builtin_amdgcn_raw_buffer_load_b128(rk, (i < 3 && Q + G == 0) ? OOB : ro + (i - 3) * 2048, 0, 0); NAP(); }
#pragma unroll
        for (int i = 0; i < 8; ++i) { u[i] = __builtin_amdgcn_raw_buffer_load_b128(rv, ro + i * 2048, 0, 0); NAP(); }
#pragma unroll
        for (int i = 0; i < 8; ++i) { qv[i] = __builtin_amdgcn_raw_buffer_load_b128(rq, ro + i * 2048, 0, 0); NAP(); }
#pragma unroll
        for (int q = 0; q < 4; ++q) {
#pragma unroll
            for (int i = 0; i < 8; ++i) {
                unsigned ks = pack2(blo(kr[i][q]) * sv[2 * q][3], bhi(kr[i][q]) * sv[2 * q + 1][3]), pr;
#pragma unroll
                for (int d = 1; d < 4; ++d) { pr = pack2(blo(kr[i + d][q]) * sv[2 * q][3 - d], bhi(kr[i + d][q]) * sv[2 * q + 1][3 - d]); ks = pack2(blo(ks) + blo(pr), bhi(ks) + bhi(pr)); }
                u[i][q] = pack2(blo(ks) * blo(u[i][q]), bhi(ks) * bhi(u[i][q])); }
            v4u lo, hh;
#pragma unroll
            for (int z = 0; z < 4; ++z) { const unsigned x = u[7 - 2 * z][q], y = u[6 - 2 * z][q]; lo[z] = __builtin_amdgcn_perm(y, x, 0x05040100u); hh[z] = __builtin_amdgcn_perm(y, x, 0x07060302u); }
            st16(r, W_U + uo + q * 32768 + G * 4096, lo); NAP(); st16(r, W_U + uo + q * 32768 + 16384 + G * 4096, hh); NAP();
#pragma unroll
            for (int z = 0; z < 2; ++z) { st16(r, W_Y + uo + q * 32768 + (2 * G + z) * 4096, (v4u){qv[4 * z][q], qv[4 * z + 1][q], qv[4 * z + 2][q], qv[4 * z + 3][q]}); NAP(); }
        }
    }
    DRAIN(); __syncthreads();
    if (tid == 0) __hip_atomic_fetch_add((unsigned*)(p.ws + F_CONV), 1u, __ATOMIC_RELAXED, __HIP_MEMORY_SCOPE_AGENT);
    if (tid == 0) __hip_atomic_fetch_add((unsigned*)(p.ws + F_CVY + hi * 128 + 64), 1u, __ATOMIC_RELAXED, __HIP_MEMORY_SCOPE_AGENT);
}
__device__ __forceinline__ bool conv_core(const Params& p, rsrc_t r, unsigned char* lds, const int wv, const int un) {
    const int tid = otid(), w = wv, l = tid & 63, Q = tid >> 2, cg = tid & 3;
    const rsrc_t rs = __builtin_amdgcn_make_buffer_rsrc((void*)p.ssm_k, 0, S * D * 2, 0x00020000);
    v4u kraw[4]; v2u kext[4];
    auto kload = [&](int n) { const unsigned o = (unsigned)(((un >> 2) * 32 + w * 8 + (un & 3) * 2 + n) * S + l * 8) * 2u;
#pragma unroll
        for (int i = 0; i < 4; ++i) { kraw[i] = __builtin_amdgcn_raw_buffer_load_b128(rs, o + i * 1024, 0, 2);
            kext[i] = __builtin_amdgcn_raw_buffer_load_b64(rs, (i == 3 && l == 63) ? OOB : o + i * 1024 + 16, 0, 0); } };
    kload(0);
    if (!wait_word(r, F_CVY + (un >> 2) * 128 + 64, 4u, 2048u)) return false;
    v4u ur[8], qr[8];
#pragma unroll
    for (int i = 0; i < 8; ++i) ur[i] = ld16(r, W_U + un * 32768 + i * 4096 + tid * 16);
#pragma unroll
    for (int i = 0; i < 8; ++i) qr[i] = ld16(r, W_Y + un * 32768 + i * 4096 + tid * 16);
    const unsigned dv = *(const unsigned*)(p.ssm_D + (un >> 2) * 32 + cg * 8 + (un & 3) * 2);
    for (int i = tid; i < 8 * 160; i += 256) *(v4u*)(lds + (i / 160) * CV_U + (i % 160) * 16) = (v4u){0u, 0u, 0u, 0u};
    unsigned char* kc = lds + CV_K0 + w * 4 * CV_KP;
    if (l < 32) {
#pragma unroll
        for (int q = 0; q < 4; ++q) *(unsigned*)(kc + q * CV_KP + l * 4) = 0u; }
    const int b = l & 31, kg = l >> 5;
    const unsigned kb = (unsigned)(size_t)kc + (b & 3) * CV_KP + ((b & 28) - 8 * kg + 56 - 16) * 2, ub0 = (unsigned)(size_t)lds + (1 + b) * CV_RP + kg * 16;
    unsigned char* const urow = lds + 2 * cg * CV_U + (32 + Q) * CV_RP;
#pragma unroll
    for (int i = 0; i < 8; ++i) *(v4u*)(urow + (i >> 2) * CV_U + (i & 3) * 16) = ur[i];
    __syncthreads();
#pragma unroll 1
        for (int ci = 0; ci < 2; ++ci) {
            const int c8 = 2 * w + ci;
#pragma unroll
            for (int i = 0; i < 4; ++i) { const v4u e = kraw[i]; const v2u f = kext[i]; unsigned char* q = kc + 128 + (i * 64 + l) * 16;
                const unsigned s0 = __builtin_amdgcn_alignbit(e.y, e.x, 16), s1 = __builtin_amdgcn_alignbit(e.z, e.y, 16), s2 = __builtin_amdgcn_alignbit(e.w, e.z, 16),
                               s3 = __builtin_amdgcn_alignbit(f.x, e.w, 16), s4 = __builtin_amdgcn_alignbit(f.y, f.x, 16);
                *(v4u*)q = (v4u){s0, s1, s2, s3}; *(v4u*)(q + CV_KP) = (v4u){e.y, e.z, e.w, f.x}; *(v4u*)(q + 2 * CV_KP) = (v4u){s1, s2, s3, s4}; *(v4u*)(q + 3 * CV_KP) = (v4u){e.z, e.w, f.x, f.y};
                if (i == 0 && l == 0) {
                    *(v2u*)(q - 8) = (v2u){0u, e.x << 16}; *(v2u*)(q + CV_KP - 8) = (v2u){0u, e.x}; *(v2u*)(q + 2 * CV_KP - 8) = (v2u){e.x << 16, s0}; *(v2u*)(q + 3 * CV_KP - 8) = (v2u){e.x, e.y}; } }
            if (ci == 0) kload(1);
            __builtin_amdgcn_wave_barrier();
            f32x16 acc0, acc1;
            asm volatile(
                "ds_read_b64 v[192:193], %[kb] offset:32\n\t"
                "ds_read_b64 v[194:195], %[kb] offset:40\n\t"
                "ds_read_b128 v[196:199], %[ub] offset:5040\n\t"
                "ds_read_b128 v[200:203], %[ub] offset:2480\n\t"
                "ds_read_b64 v[204:205], %[kb] offset:2080\n\t"
                "ds_read_b64 v[206:207], %[kb] offset:2088\n\t"
                "ds_read_b64 v[208:209], %[kb] offset:0\n\t"
                "ds_read_b64 v[210:211], %[kb] offset:8\n\t"
                "ds_read_b128 v[212:215], %[ub] offset:5072\n\t"
                "ds_read_b128 v[216:219], %[ub] offset:2512\n\t"
                "ds_read_b64 v[220:221], %[kb] offset:2048\n\t"
                "ds_read_b64 v[222:223], %[kb] offset:2056\n\t"
                "s_waitcnt lgkmcnt(9)\n\t"
                "v_mfma_f32_32x32x16_bf16 %[a1], v[192:195], v[196:199], 0\n\t"
                "ds_read_b64 v[224:225], %[kb] offset:96\n\t"
                "ds_read_b64 v[226:227], %[kb] offset:104\n\t"
                "ds_read_b128 v[228:231], %[ub] offset:4960\n\t"
                "s_waitcnt lgkmcnt(11)\n\t"
                "v_mfma_f32_32x32x16_bf16 %[a0], v[192:195], v[200:203], 0\n\t"
                "ds_read_b128 v[232:235], %[ub] offset:2400\n\t"
                "s_waitcnt lgkmcnt(10)\n\t"
                "v_mfma_f32_32x32x16_bf16 %[a1], v[204:207], v[200:203], %[a1]\n\t"
                "ds_read_b64 v[236:237], %[kb] offset:2144\n\t"
                "ds_read_b64 v[238:239], %[kb] offset:2152\n\t"
                "s_waitcnt lgkmcnt(9)\n\t"
                "v_mfma_f32_32x32x16_bf16 %[a1], v[208:211], v[212:215], %[a1]\n\t"
                "ds_read_b64 v[240:241], %[kb] offset:64\n\t"
                "ds_read_b64 v[242:243], %[kb] offset:72\n\t"
                "ds_read_b128 v[244:247], %[ub] offset:4992\n\t"
                "s_waitcnt lgkmcnt(11)\n\t"
                "v_mfma_f32_32x32x16_bf16 %[a0], v[208:211], v[216:219], %[a0]\n\t"
                "ds_read_b128 v[248:251], %[ub] offset:2432\n\t"
                "s_waitcnt lgkmcnt(10)\n\t"
                "v_mfma_f32_32x32x16_bf16 %[a1], v[220:223], v[216:219], %[a1]\n\t"
                "ds_read_b64 v[252:253], %[kb] offset:2112\n\t"
                "ds_read_b64 v[254:255], %[kb] offset:2120\n\t"
                "s_waitcnt lgkmcnt(9)\n\t"
                "v_mfma_f32_32x32x16_bf16 %[a1], v[224:227], v[228:231], %[a1]\n\t"
                "ds_read_b64 v[192:193], %[kb] offset:160\n\t"
                "ds_read_b64 v[194:195], %[kb] offset:168\n\t"
                "ds_read_b128 v[196:199], %[ub] offset:4880\n\t"
                "s_waitcnt lgkmcnt(11)\n\t"
                "v_mfma_f32_32x32x16_bf16 %[a0], v[224:227], v[232:235], %[a0]\n\t"
                "ds_read_b128 v[200:203], %[ub] offset:2320\n\t"
                "s_waitcnt lgkmcnt(10)\n\t"
                "v_mfma_f32_32x32x16_bf16 %[a1], v[236:239], v[232:235], %[a1]\n\t"
                "ds_read_b64 v[204:205], %[kb] offset:2208\n\t"
                "ds_read_b64 v[206:207], %[kb] offset:2216\n\t"
                "s_waitcnt lgkmcnt(9)\n\t"
                "v_mfma_f32_32x32x16_bf16 %[a1], v[240:243], v[244:247], %[a1]\n\t"
                "ds_read_b64 v[208:209], %[kb] offset:128\n\t"
                "ds_read_b64 v[210:211], %[kb] offset:136\n\t"
                "ds_read_b128 v[212:215], %[ub] offset:4912\n\t"
                "s_waitcnt lgkmcnt(11)\n\t"
                "v_mfma_f32_32x32x16_bf16 %[a0], v[240:243], v[248:251], %[a0]\n\t"
                "ds_read_b128 v[216:219], %[ub] offset:2352\n\t"
                "s_waitcnt lgkmcnt(10)\n\t"
                "v_mfma_f32_32x32x16_bf16 %[a1], v[252:255], v[248:251], %[a1]\n\t"
                "ds_read_b64 v[220:221], %[kb] offset:2176\n\t"
                "ds_read_b64 v[222:223], %[kb] offset:2184\n\t"
                "s_waitcnt lgkmcnt(9)\n\t"
                "v_mfma_f32_32x32x16_bf16 %[a1], v[192:195], v[196:199], %[a1]\n\t"
                "ds_read_b64 v[224:225], %[kb] offset:224\n\t"
                "ds_read_b64 v[226:227], %[kb] offset:232\n\t"
                "ds_read_b128 v[228:231], %[ub] offset:4800\n\t"
                "s_waitcnt lgkmcnt(11)\n\t"
                "v_mfma_f32_32x32x16_bf16 %[a0], v[192:195], v[200:203], %[a0]\n\t"
                "ds_read_b128 v[232:235], %[ub] offset:2240\n\t"
                "s_waitcnt lgkmcnt(10)\n\t"
                "v_mfma_f32_32x32x16_bf16 %[a1], v[204:207], v[200:203], %[a1]\n\t"
                "ds_read_b64 v[236:237], %[kb] offset:2272\n\t"
                "ds_read_b64 v[238:239], %[kb] offset:2280\n\t"
                "s_waitcnt lgkmcnt(9)\n\t"
                "v_mfma_f32_32x32x16_bf16 %[a1], v[208:211], v[212:215], %[a1]\n\t"
                "ds_read_b64 v[240:241], %[kb] offset:192\n\t"
                "ds_read_b64 v[242:243], %[kb] offset:200\n\t"
                "ds_read_b128 v[244:247], %[ub] offset:4832\n\t"
                "s_waitcnt lgkmcnt(11)\n\t"
                "v_mfma_f32_32x32x16_bf16 %[a0], v[208:211], v[216:219], %[a0]\n\t"
                "ds_read_b128 v[248:251], %[ub] offset:2272\n\t"
                "s_waitcnt lgkmcnt(10)\n\t"
                "v_mfma_f32_32x32x16_bf16 %[a1], v[220:223], v[216:219], %[a1]\n\t"
                "ds_read_b64 v[252:253], %[kb] offset:2240\n\t"
                "ds_read_b64 v[254:255], %[kb] offset:2248\n\t"
                "s_waitcnt lgkmcnt(9)\n\t"
                "v_mfma_f32_32x32x16_bf16 %[a1], v[224:227], v[228:231], %[a1]\n\t"
                "ds_read_b64 v[192:193], %[kb] offset:288\n\t"
                "ds_read_b64 v[194:195], %[kb] offset:296\n\t"
                "ds_read_b128 v[196:199], %[ub] offset:4720\n\t"
                "s_waitcnt lgkmcnt(11)\n\t"
                "v_mfma_f32_32x32x16_bf16 %[a0], v[224:227], v[232:235], %[a0]\n\t"
                "ds_read_b128 v[200:203], %[ub] offset:2160\n\t"
                "s_waitcnt lgkmcnt(10)\n\t"
                "v_mfma_f32_32x32x16_bf16 %[a1], v[236:239], v[232:235], %[a1]\n\t"
                "ds_read_b64 v[204:205], %[kb] offset:2336\n\t"
                "ds_read_b64 v[206:207], %[kb] offset:2344\n\t"
                "s_waitcnt lgkmcnt(9)\n\t"
                "v_mfma_f32_32x32x16_bf16 %[a1], v[240:243], v[244:247], %[a1]\n\t"
                "ds_read_b64 v[208:209], %[kb] offset:256\n\t"
                "ds_read_b64 v[210:211], %[kb] offset:264\n\t"
                "ds_read_b128 v[212:215], %[ub] offset:4752\n\t"
                "s_waitcnt lgkmcnt(11)\n\t"
                "v_mfma_f32_32x32x16_bf16 %[a0], v[240:243], v[248:251], %[a0]\n\t"
                "ds_read_b128 v[216:219], %[ub] offset:2192\n\t"
                "s_waitcnt lgkmcnt(10)\n\t"
                "v_mfma_f32_32x32x16_bf16 %[a1], v[252:255], v[248:251], %[a1]\n\t"
                "ds_read_b64 v[220:221], %[kb] offset:2304\n\t"
                "ds_read_b64 v[222:223], %[kb] offset:2312\n\t"
                "s_waitcnt lgkmcnt(9)\n\t"
                "v_mfma_f32_32x32x16_bf16 %[a1], v[192:195], v[196:199], %[a1]\n\t"
                "ds_read_b64 v[224:225], %[kb] offset:352\n\t"
                "ds_read_b64 v[226:227], %[kb] offset:360\n\t"
                "ds_read_b128 v[228:231], %[ub] offset:4640\n\t"
                "s_waitcnt lgkmcnt(11)\n\t"
                "v_mfma_f32_32x32x16_bf16 %[a0], v[192:195], v[200:203], %[a0]\n\t"
                "ds_read_b128 v[232:235], %[ub] offset:2080\n\t"
                "s_waitcnt lgkmcnt(10)\n\t"
                "v_mfma_f32_32x32x16_bf16 %[a1], v[204:207], v[200:203], %[a1]\n\t"
                "ds_read_b64 v[236:237], %[kb] offset:2400\n\t"
                "ds_read_b64 v[238:239], %[kb] offset:2408\n\t"
                "s_waitcnt lgkmcnt(9)\n\t"
                "v_mfma_f32_32x32x16_bf16 %[a1], v[208:211], v[212:215], %[a1]\n\t"
                "ds_read_b64 v[240:241], %[kb] offset:320\n\t"
                "ds_read_b64 v[242:243], %[kb] offset:328\n\t"
                "ds_read_b128 v[244:247], %[ub] offset:4672\n\t"
                "s_waitcnt lgkmcnt(11)\n\t"
                "v_mfma_f32_32x32x16_bf16 %[a0], v[208:211], v[216:219], %[a0]\n\t"
                "ds_read_b128 v[248:251], %[ub] offset:2112\n\t"
                "s_waitcnt lgkmcnt(10)\n\t"
                "v_mfma_f32_32x32x16_bf16 %[a1], v[220:223], v[216:219], %[a1]\n\t"
                "ds_read_b64 v[252:253], %[kb] offset:2368\n\t"
                "ds_read_b64 v[254:255], %[kb] offset:2376\n\t"
                "s_waitcnt lgkmcnt(9)\n\t"
                "v_mfma_f32_32x32x16_bf16 %[a1], v[224:227], v[228:231], %[a1]\n\t"
                "ds_read_b64 v[192:193], %[kb] offset:416\n\t"
                "ds_read_b64 v[194:195], %[kb] offset:424\n\t"
                "ds_read_b128 v[196:199], %[ub] offset:4560\n\t"
                "s_waitcnt lgkmcnt(11)\n\t"
                "v_mfma_f32_32x32x16_bf16 %[a0], v[224:227], v[232:235], %[a0]\n\t"
                "ds_read_b128 v[200:203], %[ub] offset:2000\n\t"
                "s_waitcnt lgkmcnt(10)\n\t"
                "v_mfma_f32_32x32x16_bf16 %[a1], v[236:239], v[232:235], %[a1]\n\t"
                "ds_read_b64 v[204:205], %[kb] offset:2464\n\t"
                "ds_read_b64 v[206:207], %[kb] offset:2472\n\t"
                "s_waitcnt lgkmcnt(9)\n\t"
                "v_mfma_f32_32x32x16_bf16 %[a1], v[240:243], v[244:247], %[a1]\n\t"
                "ds_read_b64 v[208:209], %[kb] offset:384\n\t"
                "ds_read_b64 v[210:211], %[kb] offset:392\n\t"
                "ds_read_b128 v[212:215], %[ub] offset:4592\n\t"
                "s_waitcnt lgkmcnt(11)\n\t"
                "v_mfma_f32_32x32x16_bf16 %[a0], v[240:243], v[248:251], %[a0]\n\t"
                "ds_read_b128 v[216:219], %[ub] offset:2032\n\t"
                "s_waitcnt lgkmcnt(10)\n\t"
                "v_mfma_f32_32x32x16_bf16 %[a1], v[252:255], v[248:251], %[a1]\n\t"
                "ds_read_b64 v[220:221], %[kb] offset:2432\n\t"
                "ds_read_b64 v[222:223], %[kb] offset:2440\n\t"
                "s_waitcnt lgkmcnt(9)\n\t"
                "v_mfma_f32_32x32x16_bf16 %[a1], v[192:195], v[196:199], %[a1]\n\t"
                "ds_read_b64 v[224:225], %[kb] offset:480\n\t"
                "ds_read_b64 v[226:227], %[kb] offset:488\n\t"
                "ds_read_b128 v[228:231], %[ub] offset:4480\n\t"
                "s_waitcnt lgkmcnt(11)\n\t"
                "v_mfma_f32_32x32x16_bf16 %[a0], v[192:195], v[200:203], %[a0]\n\t"
                "ds_read_b128 v[232:235], %[ub] offset:1920\n\t"
                "s_waitcnt lgkmcnt(10)\n\t"
                "v_mfma_f32_32x32x16_bf16 %[a1], v[204:207], v[200:203], %[a1]\n\t"
                "ds_read_b64 v[236:237], %[kb] offset:2528\n\t"
                "ds_read_b64 v[238:239], %[kb] offset:2536\n\t"
                "s_waitcnt lgkmcnt(9)\n\t"
                "v_mfma_f32_32x32x16_bf16 %[a1], v[208:211], v[212:215], %[a1]\n\t"
                "ds_read_b64 v[240:241], %[kb] offset:448\n\t"
                "ds_read_b64 v[242:243], %[kb] offset:456\n\t"
                "ds_read_b128 v[244:247], %[ub] offset:4512\n\t"
                "s_waitcnt lgkmcnt(11)\n\t"
                "v_mfma_f32_32x32x16_bf16 %[a0], v[208:211], v[216:219], %[a0]\n\t"
                "ds_read_b128 v[248:251], %[ub] offset:1952\n\t"
                "s_waitcnt lgkmcnt(10)\n\t"
                "v_mfma_f32_32x32x16_bf16 %[a1], v[220:223], v[216:219], %[a1]\n\t"
                "ds_read_b64 v[252:253], %[kb] offset:2496\n\t"
                "ds_read_b64 v[254:255], %[kb] offset:2504\n\t"
                "s_waitcnt lgkmcnt(9)\n\t"
                "v_mfma_f32_32x32x16_bf16 %[a1], v[224:227], v[228:231], %[a1]\n\t"
                "ds_read_b64 v[192:193], %[kb] offset:544\n\t"
                "ds_read_b64 v[194:195], %[kb] offset:552\n\t"
                "ds_read_b128 v[196:199], %[ub] offset:4400\n\t"
                "s_waitcnt lgkmcnt(11)\n\t"
                "v_mfma_f32_32x32x16_bf16 %[a0], v[224:227], v[232:235], %[a0]\n\t"
                "ds_read_b128 v[200:203], %[ub] offset:1840\n\t"
                "s_waitcnt lgkmcnt(10)\n\t"
                "v_mfma_f32_32x32x16_bf16 %[a1], v[236:239], v[232:235], %[a1]\n\t"
                "ds_read_b64 v[204:205], %[kb] offset:2592\n\t"
                "ds_read_b64 v[206:207], %[kb] offset:2600\n\t"
                "s_waitcnt lgkmcnt(9)\n\t"
                "v_mfma_f32_32x32x16_bf16 %[a1], v[240:243], v[244:247], %[a1]\n\t"
                "ds_read_b64 v[208:209], %[kb] offset:512\n\t"
                "ds_read_b64 v[210:211], %[kb] offset:520\n\t"
                "ds_read_b128 v[212:215], %[ub] offset:4432\n\t"
                "s_waitcnt lgkmcnt(11)\n\t"
                "v_mfma_f32_32x32x16_bf16 %[a0], v[240:243], v[248:251], %[a0]\n\t"
                "ds_read_b128 v[216:219], %[ub] offset:1872\n\t"
                "s_waitcnt lgkmcnt(10)\n\t"
                "v_mfma_f32_32x32x16_bf16 %[a1], v[252:255], v[248:251], %[a1]\n\t"
                "ds_read_b64 v[220:221], %[kb] offset:2560\n\t"
                "ds_read_b64 v[222:223], %[kb] offset:2568\n\t"
                "s_waitcnt lgkmcnt(9)\n\t"
                "v_mfma_f32_32x32x16_bf16 %[a1], v[192:195], v[196:199], %[a1]\n\t"
                "ds_read_b64 v[224:225], %[kb] offset:608\n\t"
                "ds_read_b64 v[226:227], %[kb] offset:616\n\t"
                "ds_read_b128 v[228:231], %[ub] offset:4320\n\t"
                "s_waitcnt lgkmcnt(11)\n\t"
                "v_mfma_f32_32x32x16_bf16 %[a0], v[192:195], v[200:203], %[a0]\n\t"
                "ds_read_b128 v[232:235], %[ub] offset:1760\n\t"
                "s_waitcnt lgkmcnt(10)\n\t"
                "v_mfma_f32_32x32x16_bf16 %[a1], v[204:207], v[200:203], %[a1]\n\t"
                "ds_read_b64 v[236:237], %[kb] offset:2656\n\t"
                "ds_read_b64 v[238:239], %[kb] offset:2664\n\t"
                "s_waitcnt lgkmcnt(9)\n\t"
                "v_mfma_f32_32x32x16_bf16 %[a1], v[208:211], v[212:215], %[a1]\n\t"
                "ds_read_b64 v[240:241], %[kb] offset:576\n\t"
                "ds_read_b64 v[242:243], %[kb] offset:584\n\t"
                "ds_read_b128 v[244:247], %[ub] offset:4352\n\t"
                "s_waitcnt lgkmcnt(11)\n\t"
                "v_mfma_f32_32x32x16_bf16 %[a0], v[208:211], v[216:219], %[a0]\n\t"
                "ds_read_b128 v[248:251], %[ub] offset:1792\n\t"
                "s_waitcnt lgkmcnt(10)\n\t"
                "v_mfma_f32_32x32x16_bf16 %[a1], v[220:223], v[216:219], %[a1]\n\t"
                "ds_read_b64 v[252:253], %[kb] offset:2624\n\t"
                "ds_read_b64 v[254:255], %[kb] offset:2632\n\t"
                "s_waitcnt lgkmcnt(9)\n\t"
                "v_mfma_f32_32x32x16_bf16 %[a1], v[224:227], v[228:231], %[a1]\n\t"
                "ds_read_b64 v[192:193], %[kb] offset:672\n\t"
                "ds_read_b64 v[194:195], %[kb] offset:680\n\t"
                "ds_read_b128 v[196:199], %[ub] offset:4240\n\t"
                "s_waitcnt lgkmcnt(11)\n\t"
                "v_mfma_f32_32x32x16_bf16 %[a0], v[224:227], v[232:235], %[a0]\n\t"
                "ds_read_b128 v[200:203], %[ub] offset:1680\n\t"
                "s_waitcnt lgkmcnt(10)\n\t"
                "v_mfma_f32_32x32x16_bf16 %[a1], v[236:239], v[232:235], %[a1]\n\t"
                "ds_read_b64 v[204:205], %[kb] offset:2720\n\t"
                "ds_read_b64 v[206:207], %[kb] offset:2728\n\t"
                "s_waitcnt lgkmcnt(9)\n\t"
                "v_mfma_f32_32x32x16_bf16 %[a1], v[240:243], v[244:247], %[a1]\n\t"
                "ds_read_b64 v[208:209], %[kb] offset:640\n\t"
                "ds_read_b64 v[210:211], %[kb] offset:648\n\t"
                "ds_read_b128 v[212:215], %[ub] offset:4272\n\t"
                "s_waitcnt lgkmcnt(11)\n\t"
                "v_mfma_f32_32x32x16_bf16 %[a0], v[240:243], v[248:251], %[a0]\n\t"
                "ds_read_b128 v[216:219], %[ub] offset:1712\n\t"
                "s_waitcnt lgkmcnt(10)\n\t"
                "v_mfma_f32_32x32x16_bf16 %[a1], v[252:255], v[248:251], %[a1]\n\t"
                "ds_read_b64 v[220:221], %[kb] offset:2688\n\t"
                "ds_read_b64 v[222:223], %[kb] offset:2696\n\t"
                "s_waitcnt lgkmcnt(9)\n\t"
                "v_mfma_f32_32x32x16_bf16 %[a1], v[192:195], v[196:199], %[a1]\n\t"
                "ds_read_b64 v[224:225], %[kb] offset:736\n\t"
                "ds_read_b64 v[226:227], %[kb] offset:744\n\t"
                "ds_read_b128 v[228:231], %[ub] offset:4160\n\t"
                "s_waitcnt lgkmcnt(11)\n\t"
                "v_mfma_f32_32x32x16_bf16 %[a0], v[192:195], v[200:203], %[a0]\n\t"
                "ds_read_b128 v[232:235], %[ub] offset:1600\n\t"
                "s_waitcnt lgkmcnt(10)\n\t"
                "v_mfma_f32_32x32x16_bf16 %[a1], v[204:207], v[200:203], %[a1]\n\t"
                "ds_read_b64 v[236:237], %[kb] offset:2784\n\t"
                "ds_read_b64 v[238:239], %[kb] offset:2792\n\t"
                "s_waitcnt lgkmcnt(9)\n\t"
                "v_mfma_f32_32x32x16_bf16 %[a1], v[208:211], v[212:215], %[a1]\n\t"
                "ds_read_b64 v[240:241], %[kb] offset:704\n\t"
                "ds_read_b64 v[242:243], %[kb] offset:712\n\t"
                "ds_read_b128 v[244:247], %[ub] offset:4192\n\t"
                "s_waitcnt lgkmcnt(11)\n\t"
                "v_mfma_f32_32x32x16_bf16 %[a0], v[208:211], v[216:219], %[a0]\n\t"
                "ds_read_b128 v[248:251], %[ub] offset:1632\n\t"
                "s_waitcnt lgkmcnt(10)\n\t"
                "v_mfma_f32_32x32x16_bf16 %[a1], v[220:223], v[216:219], %[a1]\n\t"
                "ds_read_b64 v[252:253], %[kb] offset:2752\n\t"
                "ds_read_b64 v[254:255], %[kb] offset:2760\n\t"
                "s_waitcnt lgkmcnt(9)\n\t"
                "v_mfma_f32_32x32x16_bf16 %[a1], v[224:227], v[228:231], %[a1]\n\t"
                "ds_read_b64 v[192:193], %[kb] offset:800\n\t"
                "ds_read_b64 v[194:195], %[kb] offset:808\n\t"
                "ds_read_b128 v[196:199], %[ub] offset:4080\n\t"
                "s_waitcnt lgkmcnt(11)\n\t"
                "v_mfma_f32_32x32x16_bf16 %[a0], v[224:227], v[232:235], %[a0]\n\t"
                "ds_read_b128 v[200:203], %[ub] offset:1520\n\t"
                "s_waitcnt lgkmcnt(10)\n\t"
                "v_mfma_f32_32x32x16_bf16 %[a1], v[236:239], v[232:235], %[a1]\n\t"
                "ds_read_b64 v[204:205], %[kb] offset:2848\n\t"
                "ds_read_b64 v[206:207], %[kb] offset:2856\n\t"
                "s_waitcnt lgkmcnt(9)\n\t"
                "v_mfma_f32_32x32x16_bf16 %[a1], v[240:243], v[244:247], %[a1]\n\t"
                "ds_read_b64 v[208:209], %[kb] offset:768\n\t"
                "ds_read_b64 v[210:211], %[kb] offset:776\n\t"
                "ds_read_b128 v[212:215], %[ub] offset:4112\n\t"
                "s_waitcnt lgkmcnt(11)\n\t"
                "v_mfma_f32_32x32x16_bf16 %[a0], v[240:243], v[248:251], %[a0]\n\t"
                "ds_read_b128 v[216:219], %[ub] offset:1552\n\t"
                "s_waitcnt lgkmcnt(10)\n\t"
                "v_mfma_f32_32x32x16_bf16 %[a1], v[252:255], v[248:251], %[a1]\n\t"
                "ds_read_b64 v[220:221], %[kb] offset:2816\n\t"
                "ds_read_b64 v[222:223], %[kb] offset:2824\n\t"
                "s_waitcnt lgkmcnt(9)\n\t"
                "v_mfma_f32_32x32x16_bf16 %[a1], v[192:195], v[196:199], %[a1]\n\t"
                "ds_read_b64 v[224:225], %[kb] offset:864\n\t"
                "ds_read_b64 v[226:227], %[kb] offset:872\n\t"
                "ds_read_b128 v[228:231], %[ub] offset:4000\n\t"
                "s_waitcnt lgkmcnt(11)\n\t"
                "v_mfma_f32_32x32x16_bf16 %[a0], v[192:195], v[200:203], %[a0]\n\t"
                "ds_read_b128 v[232:235], %[ub] offset:1440\n\t"
                "s_waitcnt lgkmcnt(10)\n\t"
                "v_mfma_f32_32x32x16_bf16 %[a1], v[204:207], v[200:203], %[a1]\n\t"
                "ds_read_b64 v[236:237], %[kb] offset:2912\n\t"
                "ds_read_b64 v[238:239], %[kb] offset:2920\n\t"
                "s_waitcnt lgkmcnt(9)\n\t"
                "v_mfma_f32_32x32x16_bf16 %[a1], v[208:211], v[212:215], %[a1]\n\t"
                "ds_read_b64 v[240:241], %[kb] offset:832\n\t"
                "ds_read_b64 v[242:243], %[kb] offset:840\n\t"
                "ds_read_b128 v[244:247], %[ub] offset:4032\n\t"
                "s_waitcnt lgkmcnt(11)\n\t"
                "v_mfma_f32_32x32x16_bf16 %[a0], v[208:211], v[216:219], %[a0]\n\t"
                "ds_read_b128 v[248:251], %[ub] offset:1472\n\t"
                "s_waitcnt lgkmcnt(10)\n\t"
                "v_mfma_f32_32x32x16_bf16 %[a1], v[220:223], v[216:219], %[a1]\n\t"
                "ds_read_b64 v[252:253], %[kb] offset:2880\n\t"
                "ds_read_b64 v[254:255], %[kb] offset:2888\n\t"
                "s_waitcnt lgkmcnt(9)\n\t"
                "v_mfma_f32_32x32x16_bf16 %[a1], v[224:227], v[228:231], %[a1]\n\t"
                "ds_read_b64 v[192:193], %[kb] offset:928\n\t"
                "ds_read_b64 v[194:195], %[kb] offset:936\n\t"
                "ds_read_b128 v[196:199], %[ub] offset:3920\n\t"
                "s_waitcnt lgkmcnt(11)\n\t"
                "v_mfma_f32_32x32x16_bf16 %[a0], v[224:227], v[232:235], %[a0]\n\t"
                "ds_read_b128 v[200:203], %[ub] offset:1360\n\t"
                "s_waitcnt lgkmcnt(10)\n\t"
                "v_mfma_f32_32x32x16_bf16 %[a1], v[236:239], v[232:235], %[a1]\n\t"
                "ds_read_b64 v[204:205], %[kb] offset:2976\n\t"
                "ds_read_b64 v[206:207], %[kb] offset:2984\n\t"
                "s_waitcnt lgkmcnt(9)\n\t"
                "v_mfma_f32_32x32x16_bf16 %[a1], v[240:243], v[244:247], %[a1]\n\t"
                "ds_read_b64 v[208:209], %[kb] offset:896\n\t"
                "ds_read_b64 v[210:211], %[kb] offset:904\n\t"
                "ds_read_b128 v[212:215], %[ub] offset:3952\n\t"
                "s_waitcnt lgkmcnt(11)\n\t"
                "v_mfma_f32_32x32x16_bf16 %[a0], v[240:243], v[248:251], %[a0]\n\t"
                "ds_read_b128 v[216:219], %[ub] offset:1392\n\t"
                "s_waitcnt lgkmcnt(10)\n\t"
                "v_mfma_f32_32x32x16_bf16 %[a1], v[252:255], v[248:251], %[a1]\n\t"
                "ds_read_b64 v[220:221], %[kb] offset:2944\n\t"
                "ds_read_b64 v[222:223], %[kb] offset:2952\n\t"
                "s_waitcnt lgkmcnt(9)\n\t"
                "v_mfma_f32_32x32x16_bf16 %[a1], v[192:195], v[196:199], %[a1]\n\t"
                "ds_read_b64 v[224:225], %[kb] offset:992\n\t"
                "ds_read_b64 v[226:227], %[kb] offset:1000\n\t"
                "ds_read_b128 v[228:231], %[ub] offset:3840\n\t"
                "s_waitcnt lgkmcnt(11)\n\t"
                "v_mfma_f32_32x32x16_bf16 %[a0], v[192:195], v[200:203], %[a0]\n\t"
                "ds_read_b128 v[232:235], %[ub] offset:1280\n\t"
                "s_waitcnt lgkmcnt(10)\n\t"
                "v_mfma_f32_32x32x16_bf16 %[a1], v[204:207], v[200:203], %[a1]\n\t"
                "ds_read_b64 v[236:237], %[kb] offset:3040\n\t"
                "ds_read_b64 v[238:239], %[kb] offset:3048\n\t"
                "s_waitcnt lgkmcnt(9)\n\t"
                "v_mfma_f32_32x32x16_bf16 %[a1], v[208:211], v[212:215], %[a1]\n\t"
                "ds_read_b64 v[240:241], %[kb] offset:960\n\t"
                "ds_read_b64 v[242:243], %[kb] offset:968\n\t"
                "ds_read_b128 v[244:247], %[ub] offset:3872\n\t"
                "s_waitcnt lgkmcnt(11)\n\t"
                "v_mfma_f32_32x32x16_bf16 %[a0], v[208:211], v[216:219], %[a0]\n\t"
                "ds_read_b128 v[248:251], %[ub] offset:1312\n\t"
                "s_waitcnt lgkmcnt(10)\n\t"
                "v_mfma_f32_32x32x16_bf16 %[a1], v[220:223], v[216:219], %[a1]\n\t"
                "ds_read_b64 v[252:253], %[kb] offset:3008\n\t"
                "ds_read_b64 v[254:255], %[kb] offset:3016\n\t"
                "s_waitcnt lgkmcnt(9)\n\t"
                "v_mfma_f32_32x32x16_bf16 %[a1], v[224:227], v[228:231], %[a1]\n\t"
                "ds_read_b64 v[192:193], %[kb] offset:1056\n\t"
                "ds_read_b64 v[194:195], %[kb] offset:1064\n\t"
                "ds_read_b128 v[196:199], %[ub] offset:3760\n\t"
                "s_waitcnt lgkmcnt(11)\n\t"
                "v_mfma_f32_32x32x16_bf16 %[a0], v[224:227], v[232:235], %[a0]\n\t"
                "ds_read_b128 v[200:203], %[ub] offset:1200\n\t"
                "s_waitcnt lgkmcnt(10)\n\t"
                "v_mfma_f32_32x32x16_bf16 %[a1], v[236:239], v[232:235], %[a1]\n\t"
                "ds_read_b64 v[204:205], %[kb] offset:3104\n\t"
                "ds_read_b64 v[206:207], %[kb] offset:3112\n\t"
                "s_waitcnt lgkmcnt(9)\n\t"
                "v_mfma_f32_32x32x16_bf16 %[a1], v[240:243], v[244:247], %[a1]\n\t"
                "ds_read_b64 v[208:209], %[kb] offset:1024\n\t"
                "ds_read_b64 v[210:211], %[kb] offset:1032\n\t"
                "ds_read_b128 v[212:215], %[ub] offset:3792\n\t"
                "s_waitcnt lgkmcnt(11)\n\t"
                "v_mfma_f32_32x32x16_bf16 %[a0], v[240:243], v[248:251], %[a0]\n\t"
                "ds_read_b128 v[216:219], %[ub] offset:1232\n\t"
                "s_waitcnt lgkmcnt(10)\n\t"
                "v_mfma_f32_32x32x16_bf16 %[a1], v[252:255], v[248:251], %[a1]\n\t"
                "ds_read_b64 v[220:221], %[kb] offset:3072\n\t"
                "ds_read_b64 v[222:223], %[kb] offset:3080\n\t"
                "s_waitcnt lgkmcnt(9)\n\t"
                "v_mfma_f32_32x32x16_bf16 %[a1], v[192:195], v[196:199], %[a1]\n\t"
                "ds_read_b64 v[224:225], %[kb] offset:1120\n\t"
                "ds_read_b64 v[226:227], %[kb] offset:1128\n\t"
                "ds_read_b128 v[228:231], %[ub] offset:3680\n\t"
                "s_waitcnt lgkmcnt(11)\n\t"
                "v_mfma_f32_32x32x16_bf16 %[a0], v[192:195], v[200:203], %[a0]\n\t"
                "ds_read_b128 v[232:235], %[ub] offset:1120\n\t"
                "s_waitcnt lgkmcnt(10)\n\t"
                "v_mfma_f32_32x32x16_bf16 %[a1], v[204:207], v[200:203], %[a1]\n\t"
                "ds_read_b64 v[236:237], %[kb] offset:3168\n\t"
                "ds_read_b64 v[238:239], %[kb] offset:3176\n\t"
                "s_waitcnt lgkmcnt(9)\n\t"
                "v_mfma_f32_32x32x16_bf16 %[a1], v[208:211], v[212:215], %[a1]\n\t"
                "ds_read_b64 v[240:241], %[kb] offset:1088\n\t"
                "ds_read_b64 v[242:243], %[kb] offset:1096\n\t"
                "ds_read_b128 v[244:247], %[ub] offset:3712\n\t"
                "s_waitcnt lgkmcnt(11)\n\t"
                "v_mfma_f32_32x32x16_bf16 %[a0], v[208:211], v[216:219], %[a0]\n\t"
                "ds_read_b128 v[248:251], %[ub] offset:1152\n\t"
                "s_waitcnt lgkmcnt(10)\n\t"
                "v_mfma_f32_32x32x16_bf16 %[a1], v[220:223], v[216:219], %[a1]\n\t"
                "ds_read_b64 v[252:253], %[kb] offset:3136\n\t"
                "ds_read_b64 v[254:255], %[kb] offset:3144\n\t"
                "s_waitcnt lgkmcnt(9)\n\t"
                "v_mfma_f32_32x32x16_bf16 %[a1], v[224:227], v[228:231], %[a1]\n\t"
                "ds_read_b64 v[192:193], %[kb] offset:1184\n\t"
                "ds_read_b64 v[194:195], %[kb] offset:1192\n\t"
                "ds_read_b128 v[196:199], %[ub] offset:3600\n\t"
                "s_waitcnt lgkmcnt(11)\n\t"
                "v_mfma_f32_32x32x16_bf16 %[a0], v[224:227], v[232:235], %[a0]\n\t"
                "ds_read_b128 v[200:203], %[ub] offset:1040\n\t"
                "s_waitcnt lgkmcnt(10)\n\t"
                "v_mfma_f32_32x32x16_bf16 %[a1], v[236:239], v[232:235], %[a1]\n\t"
                "ds_read_b64 v[204:205], %[kb] offset:3232\n\t"
                "ds_read_b64 v[206:207], %[kb] offset:3240\n\t"
                "s_waitcnt lgkmcnt(9)\n\t"
                "v_mfma_f32_32x32x16_bf16 %[a1], v[240:243], v[244:247], %[a1]\n\t"
                "ds_read_b64 v[208:209], %[kb] offset:1152\n\t"
                "ds_read_b64 v[210:211], %[kb] offset:1160\n\t"
                "ds_read_b128 v[212:215], %[ub] offset:3632\n\t"
                "s_waitcnt lgkmcnt(11)\n\t"
                "v_mfma_f32_32x32x16_bf16 %[a0], v[240:243], v[248:251], %[a0]\n\t"
                "ds_read_b128 v[216:219], %[ub] offset:1072\n\t"
                "s_waitcnt lgkmcnt(10)\n\t"
                "v_mfma_f32_32x32x16_bf16 %[a1], v[252:255], v[248:251], %[a1]\n\t"
                "ds_read_b64 v[220:221], %[kb] offset:3200\n\t"
                "ds_read_b64 v[222:223], %[kb] offset:3208\n\t"
                "s_waitcnt lgkmcnt(9)\n\t"
                "v_mfma_f32_32x32x16_bf16 %[a1], v[192:195], v[196:199], %[a1]\n\t"
                "ds_read_b64 v[224:225], %[kb] offset:1248\n\t"
                "ds_read_b64 v[226:227], %[kb] offset:1256\n\t"
                "ds_read_b128 v[228:231], %[ub] offset:3520\n\t"
                "s_waitcnt lgkmcnt(11)\n\t"
                "v_mfma_f32_32x32x16_bf16 %[a0], v[192:195], v[200:203], %[a0]\n\t"
                "ds_read_b128 v[232:235], %[ub] offset:960\n\t"
                "s_waitcnt lgkmcnt(10)\n\t"
                "v_mfma_f32_32x32x16_bf16 %[a1], v[204:207], v[200:203], %[a1]\n\t"
                "ds_read_b64 v[236:237], %[kb] offset:3296\n\t"
                "ds_read_b64 v[238:239], %[kb] offset:3304\n\t"
                "s_waitcnt lgkmcnt(9)\n\t"
                "v_mfma_f32_32x32x16_bf16 %[a1], v[208:211], v[212:215], %[a1]\n\t"
                "ds_read_b64 v[240:241], %[kb] offset:1216\n\t"
                "ds_read_b64 v[242:243], %[kb] offset:1224\n\t"
                "ds_read_b128 v[244:247], %[ub] offset:3552\n\t"
                "s_waitcnt lgkmcnt(11)\n\t"
                "v_mfma_f32_32x32x16_bf16 %[a0], v[208:211], v[216:219], %[a0]\n\t"
                "ds_read_b128 v[248:251], %[ub] offset:992\n\t"
                "s_waitcnt lgkmcnt(10)\n\t"
                "v_mfma_f32_32x32x16_bf16 %[a1], v[220:223], v[216:219], %[a1]\n\t"
                "ds_read_b64 v[252:253], %[kb] offset:3264\n\t"
                "ds_read_b64 v[254:255], %[kb] offset:3272\n\t"
                "s_waitcnt lgkmcnt(9)\n\t"
                "v_mfma_f32_32x32x16_bf16 %[a1], v[224:227], v[228:231], %[a1]\n\t"
                "ds_read_b64 v[192:193], %[kb] offset:1312\n\t"
                "ds_read_b64 v[194:195], %[kb] offset:1320\n\t"
                "ds_read_b128 v[196:199], %[ub] offset:3440\n\t"
                "s_waitcnt lgkmcnt(11)\n\t"
                "v_mfma_f32_32x32x16_bf16 %[a0], v[224:227], v[232:235], %[a0]\n\t"
                "ds_read_b128 v[200:203], %[ub] offset:880\n\t"
                "s_waitcnt lgkmcnt(10)\n\t"
                "v_mfma_f32_32x32x16_bf16 %[a1], v[236:239], v[232:235], %[a1]\n\t"
                "ds_read_b64 v[204:205], %[kb] offset:3360\n\t"
                "ds_read_b64 v[206:207], %[kb] offset:3368\n\t"
                "s_waitcnt lgkmcnt(9)\n\t"
                "v_mfma_f32_32x32x16_bf16 %[a1], v[240:243], v[244:247], %[a1]\n\t"
                "ds_read_b64 v[208:209], %[kb] offset:1280\n\t"
                "ds_read_b64 v[210:211], %[kb] offset:1288\n\t"
                "ds_read_b128 v[212:215], %[ub] offset:3472\n\t"
                "s_waitcnt lgkmcnt(11)\n\t"
                "v_mfma_f32_32x32x16_bf16 %[a0], v[240:243], v[248:251], %[a0]\n\t"
                "ds_read_b128 v[216:219], %[ub] offset:912\n\t"
                "s_waitcnt lgkmcnt(10)\n\t"
                "v_mfma_f32_32x32x16_bf16 %[a1], v[252:255], v[248:251], %[a1]\n\t"
                "ds_read_b64 v[220:221], %[kb] offset:3328\n\t"
                "ds_read_b64 v[222:223], %[kb] offset:3336\n\t"
                "s_waitcnt lgkmcnt(9)\n\t"
                "v_mfma_f32_32x32x16_bf16 %[a1], v[192:195], v[196:199], %[a1]\n\t"
                "ds_read_b64 v[224:225], %[kb] offset:1376\n\t"
                "ds_read_b64 v[226:227], %[kb] offset:1384\n\t"
                "ds_read_b128 v[228:231], %[ub] offset:3360\n\t"
                "s_waitcnt lgkmcnt(11)\n\t"
                "v_mfma_f32_32x32x16_bf16 %[a0], v[192:195], v[200:203], %[a0]\n\t"
                "ds_read_b128 v[232:235], %[ub] offset:800\n\t"
                "s_waitcnt lgkmcnt(10)\n\t"
                "v_mfma_f32_32x32x16_bf16 %[a1], v[204:207], v[200:203], %[a1]\n\t"
                "ds_read_b64 v[236:237], %[kb] offset:3424\n\t"
                "ds_read_b64 v[238:239], %[kb] offset:3432\n\t"
                "s_waitcnt lgkmcnt(9)\n\t"
                "v_mfma_f32_32x32x16_bf16 %[a1], v[208:211], v[212:215], %[a1]\n\t"
                "ds_read_b64 v[240:241], %[kb] offset:1344\n\t"
                "ds_read_b64 v[242:243], %[kb] offset:1352\n\t"
                "ds_read_b128 v[244:247], %[ub] offset:3392\n\t"
                "s_waitcnt lgkmcnt(11)\n\t"
                "v_mfma_f32_32x32x16_bf16 %[a0], v[208:211], v[216:219], %[a0]\n\t"
                "ds_read_b128 v[248:251], %[ub] offset:832\n\t"
                "s_waitcnt lgkmcnt(10)\n\t"
                "v_mfma_f32_32x32x16_bf16 %[a1], v[220:223], v[216:219], %[a1]\n\t"
                "ds_read_b64 v[252:253], %[kb] offset:3392\n\t"
                "ds_read_b64 v[254:255], %[kb] offset:3400\n\t"
                "s_waitcnt lgkmcnt(9)\n\t"
                "v_mfma_f32_32x32x16_bf16 %[a1], v[224:227], v[228:231], %[a1]\n\t"
                "ds_read_b64 v[192:193], %[kb] offset:1440\n\t"
                "ds_read_b64 v[194:195], %[kb] offset:1448\n\t"
                "ds_read_b128 v[196:199], %[ub] offset:3280\n\t"
                "s_waitcnt lgkmcnt(11)\n\t"
                "v_mfma_f32_32x32x16_bf16 %[a0], v[224:227], v[232:235], %[a0]\n\t"
                "ds_read_b128 v[200:203], %[ub] offset:720\n\t"
                "s_waitcnt lgkmcnt(10)\n\t"
                "v_mfma_f32_32x32x16_bf16 %[a1], v[236:239], v[232:235], %[a1]\n\t"
                "ds_read_b64 v[204:205], %[kb] offset:3488\n\t"
                "ds_read_b64 v[206:207], %[kb] offset:3496\n\t"
                "s_waitcnt lgkmcnt(9)\n\t"
                "v_mfma_f32_32x32x16_bf16 %[a1], v[240:243], v[244:247], %[a1]\n\t"
                "ds_read_b64 v[208:209], %[kb] offset:1408\n\t"
                "ds_read_b64 v[210:211], %[kb] offset:1416\n\t"
                "ds_read_b128 v[212:215], %[ub] offset:3312\n\t"
                "s_waitcnt lgkmcnt(11)\n\t"
                "v_mfma_f32_32x32x16_bf16 %[a0], v[240:243], v[248:251], %[a0]\n\t"
                "ds_read_b128 v[216:219], %[ub] offset:752\n\t"
                "s_waitcnt lgkmcnt(10)\n\t"
                "v_mfma_f32_32x32x16_bf16 %[a1], v[252:255], v[248:251], %[a1]\n\t"
                "ds_read_b64 v[220:221], %[kb] offset:3456\n\t"
                "ds_read_b64 v[222:223], %[kb] offset:3464\n\t"
                "s_waitcnt lgkmcnt(9)\n\t"
                "v_mfma_f32_32x32x16_bf16 %[a1], v[192:195], v[196:199], %[a1]\n\t"
                "ds_read_b64 v[224:225], %[kb] offset:1504\n\t"
                "ds_read_b64 v[226:227], %[kb] offset:1512\n\t"
                "ds_read_b128 v[228:231], %[ub] offset:3200\n\t"
                "s_waitcnt lgkmcnt(11)\n\t"
                "v_mfma_f32_32x32x16_bf16 %[a0], v[192:195], v[200:203], %[a0]\n\t"
                "ds_read_b128 v[232:235], %[ub] offset:640\n\t"
                "s_waitcnt lgkmcnt(10)\n\t"
                "v_mfma_f32_32x32x16_bf16 %[a1], v[204:207], v[200:203], %[a1]\n\t"
                "ds_read_b64 v[236:237], %[kb] offset:3552\n\t"
                "ds_read_b64 v[238:239], %[kb] offset:3560\n\t"
                "s_waitcnt lgkmcnt(9)\n\t"
                "v_mfma_f32_32x32x16_bf16 %[a1], v[208:211], v[212:215], %[a1]\n\t"
                "ds_read_b64 v[240:241], %[kb] offset:1472\n\t"
                "ds_read_b64 v[242:243], %[kb] offset:1480\n\t"
                "ds_read_b128 v[244:247], %[ub] offset:3232\n\t"
                "s_waitcnt lgkmcnt(11)\n\t"
                "v_mfma_f32_32x32x16_bf16 %[a0], v[208:211], v[216:219], %[a0]\n\t"
                "ds_read_b128 v[248:251], %[ub] offset:672\n\t"
                "s_waitcnt lgkmcnt(10)\n\t"
                "v_mfma_f32_32x32x16_bf16 %[a1], v[220:223], v[216:219], %[a1]\n\t"
                "ds_read_b64 v[252:253], %[kb] offset:3520\n\t"
                "ds_read_b64 v[254:255], %[kb] offset:3528\n\t"
                "s_waitcnt lgkmcnt(9)\n\t"
                "v_mfma_f32_32x32x16_bf16 %[a1], v[224:227], v[228:231], %[a1]\n\t"
                "ds_read_b64 v[192:193], %[kb] offset:1568\n\t"
                "ds_read_b64 v[194:195], %[kb] offset:1576\n\t"
                "ds_read_b128 v[196:199], %[ub] offset:3120\n\t"
                "s_waitcnt lgkmcnt(11)\n\t"
                "v_mfma_f32_32x32x16_bf16 %[a0], v[224:227], v[232:235], %[a0]\n\t"
                "ds_read_b128 v[200:203], %[ub] offset:560\n\t"
                "s_waitcnt lgkmcnt(10)\n\t"
                "v_mfma_f32_32x32x16_bf16 %[a1], v[236:239], v[232:235], %[a1]\n\t"
                "ds_read_b64 v[204:205], %[kb] offset:3616\n\t"
                "ds_read_b64 v[206:207], %[kb] offset:3624\n\t"
                "s_waitcnt lgkmcnt(9)\n\t"
                "v_mfma_f32_32x32x16_bf16 %[a1], v[240:243], v[244:247], %[a1]\n\t"
                "ds_read_b64 v[208:209], %[kb] offset:1536\n\t"
                "ds_read_b64 v[210:211], %[kb] offset:1544\n\t"
                "ds_read_b128 v[212:215], %[ub] offset:3152\n\t"
                "s_waitcnt lgkmcnt(11)\n\t"
                "v_mfma_f32_32x32x16_bf16 %[a0], v[240:243], v[248:251], %[a0]\n\t"
                "ds_read_b128 v[216:219], %[ub] offset:592\n\t"
                "s_waitcnt lgkmcnt(10)\n\t"
                "v_mfma_f32_32x32x16_bf16 %[a1], v[252:255], v[248:251], %[a1]\n\t"
                "ds_read_b64 v[220:221], %[kb] offset:3584\n\t"
                "ds_read_b64 v[222:223], %[kb] offset:3592\n\t"
                "s_waitcnt lgkmcnt(9)\n\t"
                "v_mfma_f32_32x32x16_bf16 %[a1], v[192:195], v[196:199], %[a1]\n\t"
                "ds_read_b64 v[224:225], %[kb] offset:1632\n\t"
                "ds_read_b64 v[226:227], %[kb] offset:1640\n\t"
                "ds_read_b128 v[228:231], %[ub] offset:3040\n\t"
                "s_waitcnt lgkmcnt(11)\n\t"
                "v_mfma_f32_32x32x16_bf16 %[a0], v[192:195], v[200:203], %[a0]\n\t"
                "ds_read_b128 v[232:235], %[ub] offset:480\n\t"
                "s_waitcnt lgkmcnt(10)\n\t"
                "v_mfma_f32_32x32x16_bf16 %[a1], v[204:207], v[200:203], %[a1]\n\t"
                "ds_read_b64 v[236:237], %[kb] offset:3680\n\t"
                "ds_read_b64 v[238:239], %[kb] offset:3688\n\t"
                "s_waitcnt lgkmcnt(9)\n\t"
                "v_mfma_f32_32x32x16_bf16 %[a1], v[208:211], v[212:215], %[a1]\n\t"
                "ds_read_b64 v[240:241], %[kb] offset:1600\n\t"
                "ds_read_b64 v[242:243], %[kb] offset:1608\n\t"
                "ds_read_b128 v[244:247], %[ub] offset:3072\n\t"
                "s_waitcnt lgkmcnt(11)\n\t"
                "v_mfma_f32_32x32x16_bf16 %[a0], v[208:211], v[216:219], %[a0]\n\t"
                "ds_read_b128 v[248:251], %[ub] offset:512\n\t"
                "s_waitcnt lgkmcnt(10)\n\t"
                "v_mfma_f32_32x32x16_bf16 %[a1], v[220:223], v[216:219], %[a1]\n\t"
                "ds_read_b64 v[252:253], %[kb] offset:3648\n\t"
                "ds_read_b64 v[254:255], %[kb] offset:3656\n\t"
                "s_waitcnt lgkmcnt(9)\n\t"
                "v_mfma_f32_32x32x16_bf16 %[a1], v[224:227], v[228:231], %[a1]\n\t"
                "ds_read_b64 v[192:193], %[kb] offset:1696\n\t"
                "ds_read_b64 v[194:195], %[kb] offset:1704\n\t"
                "ds_read_b128 v[196:199], %[ub] offset:2960\n\t"
                "s_waitcnt lgkmcnt(11)\n\t"
                "v_mfma_f32_32x32x16_bf16 %[a0], v[224:227], v[232:235], %[a0]\n\t"
                "ds_read_b128 v[200:203], %[ub] offset:400\n\t"
                "s_waitcnt lgkmcnt(10)\n\t"
                "v_mfma_f32_32x32x16_bf16 %[a1], v[236:239], v[232:235], %[a1]\n\t"
                "ds_read_b64 v[204:205], %[kb] offset:3744\n\t"
                "ds_read_b64 v[206:207], %[kb] offset:3752\n\t"
                "s_waitcnt lgkmcnt(9)\n\t"
                "v_mfma_f32_32x32x16_bf16 %[a1], v[240:243], v[244:247], %[a1]\n\t"
                "ds_read_b64 v[208:209], %[kb] offset:1664\n\t"
                "ds_read_b64 v[210:211], %[kb] offset:1672\n\t"
                "ds_read_b128 v[212:215], %[ub] offset:2992\n\t"
                "s_waitcnt lgkmcnt(11)\n\t"
                "v_mfma_f32_32x32x16_bf16 %[a0], v[240:243], v[248:251], %[a0]\n\t"
                "ds_read_b128 v[216:219], %[ub] offset:432\n\t"
                "s_waitcnt lgkmcnt(10)\n\t"
                "v_mfma_f32_32x32x16_bf16 %[a1], v[252:255], v[248:251], %[a1]\n\t"
                "ds_read_b64 v[220:221], %[kb] offset:3712\n\t"
                "ds_read_b64 v[222:223], %[kb] offset:3720\n\t"
                "s_waitcnt lgkmcnt(9)\n\t"
                "v_mfma_f32_32x32x16_bf16 %[a1], v[192:195], v[196:199], %[a1]\n\t"
                "ds_read_b64 v[224:225], %[kb] offset:1760\n\t"
                "ds_read_b64 v[226:227], %[kb] offset:1768\n\t"
                "ds_read_b128 v[228:231], %[ub] offset:2880\n\t"
                "s_waitcnt lgkmcnt(11)\n\t"
                "v_mfma_f32_32x32x16_bf16 %[a0], v[192:195], v[200:203], %[a0]\n\t"
                "ds_read_b128 v[232:235], %[ub] offset:320\n\t"
                "s_waitcnt lgkmcnt(10)\n\t"
                "v_mfma_f32_32x32x16_bf16 %[a1], v[204:207], v[200:203], %[a1]\n\t"
                "ds_read_b64 v[236:237], %[kb] offset:3808\n\t"
                "ds_read_b64 v[238:239], %[kb] offset:3816\n\t"
                "s_waitcnt lgkmcnt(9)\n\t"
                "v_mfma_f32_32x32x16_bf16 %[a1], v[208:211], v[212:215], %[a1]\n\t"
                "ds_read_b64 v[240:241], %[kb] offset:1728\n\t"
                "ds_read_b64 v[242:243], %[kb] offset:1736\n\t"
                "ds_read_b128 v[244:247], %[ub] offset:2912\n\t"
                "s_waitcnt lgkmcnt(11)\n\t"
                "v_mfma_f32_32x32x16_bf16 %[a0], v[208:211], v[216:219], %[a0]\n\t"
                "ds_read_b128 v[248:251], %[ub] offset:352\n\t"
                "s_waitcnt lgkmcnt(10)\n\t"
                "v_mfma_f32_32x32x16_bf16 %[a1], v[220:223], v[216:219], %[a1]\n\t"
                "ds_read_b64 v[252:253], %[kb] offset:3776\n\t"
                "ds_read_b64 v[254:255], %[kb] offset:3784\n\t"
                "s_waitcnt lgkmcnt(9)\n\t"
                "v_mfma_f32_32x32x16_bf16 %[a1], v[224:227], v[228:231], %[a1]\n\t"
                "ds_read_b64 v[192:193], %[kb] offset:1824\n\t"
                "ds_read_b64 v[194:195], %[kb] offset:1832\n\t"
                "ds_read_b128 v[196:199], %[ub] offset:2800\n\t"
                "s_waitcnt lgkmcnt(11)\n\t"
                "v_mfma_f32_32x32x16_bf16 %[a0], v[224:227], v[232:235], %[a0]\n\t"
                "ds_read_b128 v[200:203], %[ub] offset:240\n\t"
                "s_waitcnt lgkmcnt(10)\n\t"
                "v_mfma_f32_32x32x16_bf16 %[a1], v[236:239], v[232:235], %[a1]\n\t"
                "ds_read_b64 v[204:205], %[kb] offset:3872\n\t"
                "ds_read_b64 v[206:207], %[kb] offset:3880\n\t"
                "s_waitcnt lgkmcnt(9)\n\t"
                "v_mfma_f32_32x32x16_bf16 %[a1], v[240:243], v[244:247], %[a1]\n\t"
                "ds_read_b64 v[208:209], %[kb] offset:1792\n\t"
                "ds_read_b64 v[210:211], %[kb] offset:1800\n\t"
                "ds_read_b128 v[212:215], %[ub] offset:2832\n\t"
                "s_waitcnt lgkmcnt(11)\n\t"
                "v_mfma_f32_32x32x16_bf16 %[a0], v[240:243], v[248:251], %[a0]\n\t"
                "ds_read_b128 v[216:219], %[ub] offset:272\n\t"
                "s_waitcnt lgkmcnt(10)\n\t"
                "v_mfma_f32_32x32x16_bf16 %[a1], v[252:255], v[248:251], %[a1]\n\t"
                "ds_read_b64 v[220:221], %[kb] offset:3840\n\t"
                "ds_read_b64 v[222:223], %[kb] offset:3848\n\t"
                "s_waitcnt lgkmcnt(9)\n\t"
                "v_mfma_f32_32x32x16_bf16 %[a1], v[192:195], v[196:199], %[a1]\n\t"
                "ds_read_b64 v[224:225], %[kb] offset:1888\n\t"
                "ds_read_b64 v[226:227], %[kb] offset:1896\n\t"
                "ds_read_b128 v[228:231], %[ub] offset:2720\n\t"
                "s_waitcnt lgkmcnt(11)\n\t"
                "v_mfma_f32_32x32x16_bf16 %[a0], v[192:195], v[200:203], %[a0]\n\t"
                "ds_read_b128 v[232:235], %[ub] offset:160\n\t"
                "s_waitcnt lgkmcnt(10)\n\t"
                "v_mfma_f32_32x32x16_bf16 %[a1], v[204:207], v[200:203], %[a1]\n\t"
                "ds_read_b64 v[236:237], %[kb] offset:3936\n\t"
                "ds_read_b64 v[238:239], %[kb] offset:3944\n\t"
                "s_waitcnt lgkmcnt(9)\n\t"
                "v_mfma_f32_32x32x16_bf16 %[a1], v[208:211], v[212:215], %[a1]\n\t"
                "ds_read_b64 v[240:241], %[kb] offset:1856\n\t"
                "ds_read_b64 v[242:243], %[kb] offset:1864\n\t"
                "ds_read_b128 v[244:247], %[ub] offset:2752\n\t"
                "s_waitcnt lgkmcnt(11)\n\t"
                "v_mfma_f32_32x32x16_bf16 %[a0], v[208:211], v[216:219], %[a0]\n\t"
                "ds_read_b128 v[248:251], %[ub] offset:192\n\t"
                "s_waitcnt lgkmcnt(10)\n\t"
                "v_mfma_f32_32x32x16_bf16 %[a1], v[220:223], v[216:219], %[a1]\n\t"
                "ds_read_b64 v[252:253], %[kb] offset:3904\n\t"
                "ds_read_b64 v[254:255], %[kb] offset:3912\n\t"
                "s_waitcnt lgkmcnt(9)\n\t"
                "v_mfma_f32_32x32x16_bf16 %[a1], v[224:227], v[228:231], %[a1]\n\t"
                "ds_read_b64 v[192:193], %[kb] offset:1952\n\t"
                "ds_read_b64 v[194:195], %[kb] offset:1960\n\t"
                "ds_read_b128 v[196:199], %[ub] offset:2640\n\t"
                "s_waitcnt lgkmcnt(11)\n\t"
                "v_mfma_f32_32x32x16_bf16 %[a0], v[224:227], v[232:235], %[a0]\n\t"
                "ds_read_b128 v[200:203], %[ub] offset:80\n\t"
                "s_waitcnt lgkmcnt(10)\n\t"
                "v_mfma_f32_32x32x16_bf16 %[a1], v[236:239], v[232:235], %[a1]\n\t"
                "ds_read_b64 v[204:205], %[kb] offset:4000\n\t"
                "ds_read_b64 v[206:207], %[kb] offset:4008\n\t"
                "s_waitcnt lgkmcnt(9)\n\t"
                "v_mfma_f32_32x32x16_bf16 %[a1], v[240:243], v[244:247], %[a1]\n\t"
                "ds_read_b64 v[208:209], %[kb] offset:1920\n\t"
                "ds_read_b64 v[210:211], %[kb] offset:1928\n\t"
                "ds_read_b128 v[212:215], %[ub] offset:2672\n\t"
                "s_waitcnt lgkmcnt(11)\n\t"
                "v_mfma_f32_32x32x16_bf16 %[a0], v[240:243], v[248:251], %[a0]\n\t"
                "ds_read_b128 v[216:219], %[ub] offset:112\n\t"
                "s_waitcnt lgkmcnt(10)\n\t"
                "v_mfma_f32_32x32x16_bf16 %[a1], v[252:255], v[248:251], %[a1]\n\t"
                "ds_read_b64 v[220:221], %[kb] offset:3968\n\t"
                "ds_read_b64 v[222:223], %[kb] offset:3976\n\t"
                "s_waitcnt lgkmcnt(9)\n\t"
                "v_mfma_f32_32x32x16_bf16 %[a1], v[192:195], v[196:199], %[a1]\n\t"
                "ds_read_b64 v[224:225], %[kb] offset:2016\n\t"
                "ds_read_b64 v[226:227], %[kb] offset:2024\n\t"
                "ds_read_b128 v[228:231], %[ub] offset:2560\n\t"
                "s_waitcnt lgkmcnt(11)\n\t"
                "v_mfma_f32_32x32x16_bf16 %[a0], v[192:195], v[200:203], %[a0]\n\t"
                "ds_read_b128 v[232:235], %[ub] offset:0\n\t"
                "s_waitcnt lgkmcnt(10)\n\t"
                "v_mfma_f32_32x32x16_bf16 %[a1], v[204:207], v[200:203], %[a1]\n\t"
                "ds_read_b64 v[236:237], %[kb] offset:4064\n\t"
                "ds_read_b64 v[238:239], %[kb] offset:4072\n\t"
                "s_waitcnt lgkmcnt(9)\n\t"
                "v_mfma_f32_32x32x16_bf16 %[a1], v[208:211], v[212:215], %[a1]\n\t"
                "ds_read_b64 v[240:241], %[kb] offset:1984\n\t"
                "ds_read_b64 v[242:243], %[kb] offset:1992\n\t"
                "ds_read_b128 v[244:247], %[ub] offset:2592\n\t"
                "s_waitcnt lgkmcnt(11)\n\t"
                "v_mfma_f32_32x32x16_bf16 %[a0], v[208:211], v[216:219], %[a0]\n\t"
                "ds_read_b128 v[248:251], %[ub] offset:32\n\t"
                "s_waitcnt lgkmcnt(10)\n\t"
                "v_mfma_f32_32x32x16_bf16 %[a1], v[220:223], v[216:219], %[a1]\n\t"
                "ds_read_b64 v[252:253], %[kb] offset:4032\n\t"
                "ds_read_b64 v[254:255], %[kb] offset:4040\n\t"
                "s_waitcnt lgkmcnt(9)\n\t"
                "v_mfma_f32_32x32x16_bf16 %[a1], v[224:227], v[228:231], %[a1]\n\t"
                "s_waitcnt lgkmcnt(8)\n\t"
                "v_mfma_f32_32x32x16_bf16 %[a0], v[224:227], v[232:235], %[a0]\n\t"
                "s_waitcnt lgkmcnt(6)\n\t"
                "v_mfma_f32_32x32x16_bf16 %[a1], v[236:239], v[232:235], %[a1]\n\t"
                "s_waitcnt lgkmcnt(3)\n\t"
                "v_mfma_f32_32x32x16_bf16 %[a1], v[240:243], v[244:247], %[a1]\n\t"
                "s_waitcnt lgkmcnt(2)\n\t"
                "v_mfma_f32_32x32x16_bf16 %[a0], v[240:243], v[248:251], %[a0]\n\t"
                "s_waitcnt lgkmcnt(0)\n\t"
                "v_mfma_f32_32x32x16_bf16 %[a1], v[252:255], v[248:251], %[a1]\n\t"
                "s_nop 7\n\t"
                "s_nop 3\n\t"
                : [a0] "=&v"(acc0), [a1] "=&v"(acc1) : [kb] "v"(kb), [ub] "v"(ub0 + c8 * CV_U) : "memory", "v192", "v193", "v194", "v195", "v196", "v197", "v198", "v199", "v200", "v201", "v202", "v203", "v204", "v205", "v206", "v207", "v208", "v209", "v210", "v211", "v212", "v213", "v214", "v215", "v216", "v217", "v218", "v219", "v220", "v221", "v222", "v223", "v224", "v225", "v226", "v227", "v228", "v229", "v230", "v231", "v232", "v233", "v234", "v235", "v236", "v237", "v238", "v239", "v240", "v241", "v242", "v243", "v244", "v245", "v246", "v247", "v248", "v249", "v250", "v251", "v252", "v253", "v254", "v255");
            unsigned char* uc = lds + c8 * CV_U + (32 + b) * CV_RP + kg * 8;
#pragma unroll
            for (int q = 0; q < 4; ++q) {
                *(v2u*)(uc + q * 16) = (v2u){pack2(acc0[4 * q], acc0[4 * q + 1]), pack2(acc0[4 * q + 2], acc0[4 * q + 3])};
                *(v2u*)(uc + 32 * CV_RP + q * 16) = (v2u){pack2(acc1[4 * q], acc1[4 * q + 1]), pack2(acc1[4 * q + 2], acc1[4 * q + 3])}; }
            __builtin_amdgcn_wave_barrier();
        }
    __syncthreads();
#pragma unroll
    for (int G = 0; G < 4; ++G) { const v4u lo = *(const v4u*)(urow + G * 16), hh = *(const v4u*)(urow + CV_U + G * 16); v4u o[2];
#pragma unroll
        for (int i = 0; i < 8; ++i) { const unsigned u0 = ur[G][(7 - i) >> 1], u1 = ur[4 + G][(7 - i) >> 1], qq = qr[2 * G + (i >> 2)][i & 3];
            const unsigned ud = pack2(((i & 1) ? blo(u0) : bhi(u0)) * blo(dv), ((i & 1) ? blo(u1) : bhi(u1)) * bhi(dv));
            const unsigned y = pack2(((i & 1) ? bhi(lo[i >> 1]) : blo(lo[i >> 1])) + blo(ud), ((i & 1) ? bhi(hh[i >> 1]) : blo(hh[i >> 1])) + bhi(ud));
            o[i >> 2][i & 3] = pack2(blo(qq) * blo(y), bhi(qq) * bhi(y)); }
        st16(r, W_A + un * 32768 + 2 * G * 4096 + tid * 16, o[0]); st16(r, W_A + un * 32768 + (2 * G + 1) * 4096 + tid * 16, o[1]); }
    DRAIN(); __syncthreads();
    if (tid == 0) __hip_atomic_fetch_add((unsigned*)(p.ws + F_CVY + (un >> 3) * 128), 1u, __ATOMIC_RELAXED, __HIP_MEMORY_SCOPE_AGENT);
    return true;
}

__device__ __forceinline__ void proj_block(const Params& p, unsigned char* lds, const int wv, const int h, const int c) {
    const int w = wv, l = otid() & 63, n = l & 15, g = l >> 4, tm = w >> 1, tn = w & 1;
    const unsigned lb = __builtin_amdgcn_readfirstlane((unsigned)(size_t)lds);
    const v4u sx = mk_srd(p.Aq, S * D * 2), sq = mk_srd(c < 2 ? p.Wq : p.Wk, 1024 * 4096 * 2), sv = mk_srd(p.Wv, 1024 * 4096 * 2), sw = mk_srd(p.ws, W_TOTAL);
    const unsigned lx = lb + (32 * tm + n) * 128 + ((g ^ (n & 7)) << 4), lr = lb + 131072, rr = 8 * g + (n >> 2);
    const int cv = ((c & 1) ^ 1) * 2 + (c >> 1);
    const unsigned aq = (c < 2 ? W_QX : W_KX) + (tm * 32 + h) * 8192 + (2 * (c & 1) + tn) * 1024 + l * 16;
    const int hq = h & 7, tk = ((hq - (hq > 1)) * 4 + c) * 4 + w;
    const bf16_t* tp = (hq != 1 && tk < 32) ? p.Aq + (tk * 64 + l) * 64 : p.gate_w;
    unsigned t0, xo, so, cn;
    asm volatile(
        "s_nop 4\n\t"
        "s_mov_b32 %[xo], 0\n\t"
        "s_mov_b32 %[so], 0\n\t"
        "s_mov_b32 %[cn], 1\n\t"
        "global_load_dword v88, v[114:115], off\n\t"
        "s_add_u32 m0, %[ld], 0\n\t"
        "s_add_u32 %[t0], %[xo], 0\n\t"
        "buffer_load_dwordx4 v100, %[sx], %[t0] offen lds\n\t"
        "s_add_u32 m0, %[ld], 1024\n\t"
        "s_add_u32 %[t0], %[xo], 16384\n\t"
        "buffer_load_dwordx4 v100, %[sx], %[t0] offen lds\n\t"
        "s_add_u32 m0, %[ld], 8192\n\t"
        "s_add_u32 %[t0], %[xo], 128\n\t"
        "buffer_load_dwordx4 v100, %[sx], %[t0] offen lds\n\t"
        "s_add_u32 m0, %[ld], 9216\n\t"
        "s_add_u32 %[t0], %[xo], 16512\n\t"
        "buffer_load_dwordx4 v100, %[sx], %[t0] offen lds\n\t"
        "s_add_u32 m0, %[ld], 16384\n\t"
        "s_add_u32 %[t0], %[xo], 256\n\t"
        "buffer_load_dwordx4 v100, %[sx], %[t0] offen lds\n\t"
        "s_add_u32 m0, %[ld], 17408\n\t"
        "s_add_u32 %[t0], %[xo], 16640\n\t"
        "buffer_load_dwordx4 v100, %[sx], %[t0] offen lds\n\t"
        "s_add_u32 m0, %[ld], 24576\n\t"
        "s_add_u32 %[t0], %[xo], 384\n\t"
        "buffer_load_dwordx4 v100, %[sx], %[t0] offen lds\n\t"
        "s_add_u32 m0, %[ld], 25600\n\t"
        "s_add_u32 %[t0], %[xo], 16768\n\t"
        "buffer_load_dwordx4 v100, %[sx], %[t0] offen lds\n\t"
        "s_mov_b32 %[t0], 0\n\t"
        "buffer_load_dwordx4 a[192:195], v101, %[sq], %[t0] offen\n\t"
        "buffer_load_dwordx4 a[200:203], v112, %[sv], %[t0] offen\n\t"
        "s_mov_b32 %[t0], 262144\n\t"
        "buffer_load_dwordx4 a[196:199], v101, %[sq], %[t0] offen\n\t"
        "s_mov_b32 %[t0], 524288\n\t"
        "buffer_load_dwordx4 a[204:207], v101, %[sq], %[t0] offen\n\t"
        "buffer_load_dwordx4 a[212:215], v112, %[sv], %[t0] offen\n\t"
        "s_mov_b32 %[t0], 786432\n\t"
        "buffer_load_dwordx4 a[208:211], v101, %[sq], %[t0] offen\n\t"
        "s_mov_b32 %[t0], 1048576\n\t"
        "buffer_load_dwordx4 a[216:219], v101, %[sq], %[t0] offen\n\t"
        "buffer_load_dwordx4 a[224:227], v112, %[sv], %[t0] offen\n\t"
        "s_mov_b32 %[t0], 1310720\n\t"
        "buffer_load_dwordx4 a[220:223], v101, %[sq], %[t0] offen\n\t"
        "s_mov_b32 %[t0], 1572864\n\t"
        "buffer_load_dwordx4 a[228:231], v101, %[sq], %[t0] offen\n\t"
        "buffer_load_dwordx4 a[236:239], v112, %[sv], %[t0] offen\n\t"
        "s_mov_b32 %[t0], 1835008\n\t"
        "buffer_load_dwordx4 a[232:235], v101, %[sq], %[t0] offen\n\t"
        "s_waitcnt vmcnt(9)\n\t"
        "ds_write_b128 v102, a[192:195] offset:0\n\t"
        "ds_write_b128 v102, a[196:199] offset:4096\n\t"
        "ds_write_b128 v116, a[200:203] offset:8192\n\t"
        "s_waitcnt lgkmcnt(0)\n\t"
        "s_barrier\n\t"
        "ds_read_b64_tr_b16 a[0:1], v103 offset:0\n\t"
        "ds_read_b64_tr_b16 a[2:3], v103 offset:512\n\t"
        "ds_read_b64_tr_b16 a[4:5], v103 offset:8\n\t"
        "ds_read_b64_tr_b16 a[6:7], v103 offset:520\n\t"
        "ds_read_b64_tr_b16 a[8:9], v104 offset:0\n\t"
        "ds_read_b64_tr_b16 a[10:11], v104 offset:256\n\t"
        "s_waitcnt lgkmcnt(5)\n\t"
        "ds_read_b64_tr_b16 a[12:13], v103 offset:4096\n\t"
        "ds_read_b64_tr_b16 a[14:15], v103 offset:4608\n\t"
        "ds_read_b64_tr_b16 a[16:17], v103 offset:4104\n\t"
        "ds_read_b64_tr_b16 a[18:19], v103 offset:4616\n\t"
        "ds_read_b64_tr_b16 a[20:21], v104 offset:2048\n\t"
        "ds_read_b64_tr_b16 a[22:23], v104 offset:2304\n\t"
        "ds_read_b128 v[56:59], v105 offset:0\n\t"
        "ds_read_b128 v[60:63], v105 offset:2048\n\t"
        "ds_read_b128 v[64:67], v106 offset:0\n\t"
        "ds_read_b128 v[68:71], v106 offset:2048\n\t"
        "s_add_u32 m0, %[ld], 32768\n\t"
        "s_add_u32 %[t0], %[xo], 512\n\t"
        "buffer_load_dwordx4 v100, %[sx], %[t0] offen lds\n\t"
        "s_add_u32 m0, %[ld], 33792\n\t"
        "s_add_u32 %[t0], %[xo], 16896\n\t"
        "buffer_load_dwordx4 v100, %[sx], %[t0] offen lds\n\t"
        "s_add_u32 m0, %[ld], 40960\n\t"
        "s_add_u32 %[t0], %[xo], 640\n\t"
        "buffer_load_dwordx4 v100, %[sx], %[t0] offen lds\n\t"
        "s_add_u32 m0, %[ld], 41984\n\t"
        "s_add_u32 %[t0], %[xo], 17024\n\t"
        "buffer_load_dwordx4 v100, %[sx], %[t0] offen lds\n\t"
        "s_add_u32 m0, %[ld], 49152\n\t"
        "s_add_u32 %[t0], %[xo], 768\n\t"
        "buffer_load_dwordx4 v100, %[sx], %[t0] offen lds\n\t"
        "s_add_u32 m0, %[ld], 50176\n\t"
        "s_add_u32 %[t0], %[xo], 17152\n\t"
        "buffer_load_dwordx4 v100, %[sx], %[t0] offen lds\n\t"
        "s_add_u32 m0, %[ld], 57344\n\t"
        "s_add_u32 %[t0], %[xo], 896\n\t"
        "buffer_load_dwordx4 v100, %[sx], %[t0] offen lds\n\t"
        "s_add_u32 m0, %[ld], 58368\n\t"
        "s_add_u32 %[t0], %[xo], 17280\n\t"
        "buffer_load_dwordx4 v100, %[sx], %[t0] offen lds\n\t"
        "s_mov_b32 %[t0], 2097152\n\t"
        "buffer_load_dwordx4 a[240:243], v101, %[sq], %[t0] offen\n\t"
        "buffer_load_dwordx4 a[248:251], v112, %[sv], %[t0] offen\n\t"
        "s_mov_b32 %[t0], 2359296\n\t"
        "buffer_load_dwordx4 a[244:247], v101, %[sq], %[t0] offen\n\t"
        "s_waitcnt vmcnt(17)\n\t"
        "ds_write_b128 v102, a[204:207] offset:16384\n\t"
        "ds_write_b128 v102, a[208:211] offset:20480\n\t"
        "ds_write_b128 v116, a[212:215] offset:24576\n\t"
        "s_waitcnt lgkmcnt(0)\n\t"
        "s_barrier\n\t"
        "v_mfma_f32_16x16x32_bf16 v[8:11], a[0:3], v[56:59], 0\n\t"
        "ds_read_b64_tr_b16 a[24:25], v103 offset:16384\n\t"
        "ds_read_b64_tr_b16 a[26:27], v103 offset:16896\n\t"
        "v_mfma_f32_16x16x32_bf16 v[12:15], a[4:7], v[56:59], 0\n\t"
        "ds_read_b64_tr_b16 a[28:29], v103 offset:16392\n\t"
        "ds_read_b64_tr_b16 a[30:31], v103 offset:16904\n\t"
        "v_mfma_f32_16x16x32_bf16 v[16:19], v[56:59], a[8:11], 0\n\t"
        "ds_read_b64_tr_b16 a[32:33], v104 offset:16384\n\t"
        "ds_read_b64_tr_b16 a[34:35], v104 offset:16640\n\t"
        "v_mfma_f32_16x16x32_bf16 v[20:23], a[0:3], v[60:63], 0\n\t"
        "s_waitcnt lgkmcnt(5)\n\t"
        "ds_read_b64_tr_b16 a[36:37], v103 offset:20480\n\t"
        "v_mfma_f32_16x16x32_bf16 v[24:27], a[4:7], v[60:63], 0\n\t"
        "ds_read_b64_tr_b16 a[38:39], v103 offset:20992\n\t"
        "ds_read_b64_tr_b16 a[40:41], v103 offset:20488\n\t"
        "v_mfma_f32_16x16x32_bf16 v[28:31], v[60:63], a[8:11], 0\n\t"
        "ds_read_b64_tr_b16 a[42:43], v103 offset:21000\n\t"
        "ds_read_b64_tr_b16 a[44:45], v104 offset:18432\n\t"
        "v_mfma_f32_16x16x32_bf16 v[8:11], a[12:15], v[64:67], v[8:11]\n\t"
        "ds_read_b64_tr_b16 a[46:47], v104 offset:18688\n\t"
        "ds_read_b128 v[72:75], v105 offset:8192\n\t"
        "v_mfma_f32_16x16x32_bf16 v[12:15], a[16:19], v[64:67], v[12:15]\n\t"
        "ds_read_b128 v[76:79], v105 offset:10240\n\t"
        "ds_read_b128 v[80:83], v106 offset:8192\n\t"
        "v_mfma_f32_16x16x32_bf16 v[16:19], v[64:67], a[20:23], v[16:19]\n\t"
        "ds_read_b128 v[84:87], v106 offset:10240\n\t"
        "v_mfma_f32_16x16x32_bf16 v[20:23], a[12:15], v[68:71], v[20:23]\n\t"
        "v_mfma_f32_16x16x32_bf16 v[24:27], a[16:19], v[68:71], v[24:27]\n\t"
        "v_mfma_f32_16x16x32_bf16 v[28:31], v[68:71], a[20:23], v[28:31]\n\t"
        "s_mov_b32 %[t0], 2621440\n\t"
        "buffer_load_dwordx4 a[252:255], v101, %[sq], %[t0] offen\n\t"
        "buffer_load_dwordx4 v[132:135], v112, %[sv], %[t0] offen\n\t"
        "s_mov_b32 %[t0], 2883584\n\t"
        "buffer_load_dwordx4 v[128:131], v101, %[sq], %[t0] offen\n\t"
        "s_waitcnt vmcnt(17)\n\t"
        "ds_write_b128 v102, a[216:219] offset:0\n\t"
        "ds_write_b128 v102, a[220:223] offset:4096\n\t"
        "ds_write_b128 v116, a[224:227] offset:8192\n\t"
        "s_waitcnt lgkmcnt(0)\n\t"
        "s_barrier\n\t"
        "v_mfma_f32_16x16x32_bf16 v[8:11], a[24:27], v[72:75], v[8:11]\n\t"
        "ds_read_b64_tr_b16 a[48:49], v103 offset:0\n\t"
        "ds_read_b64_tr_b16 a[50:51], v103 offset:512\n\t"
        "v_mfma_f32_16x16x32_bf16 v[12:15], a[28:31], v[72:75], v[12:15]\n\t"
        "ds_read_b64_tr_b16 a[52:53], v103 offset:8\n\t"
        "ds_read_b64_tr_b16 a[54:55], v103 offset:520\n\t"
        "v_mfma_f32_16x16x32_bf16 v[16:19], v[72:75], a[32:35], v[16:19]\n\t"
        "ds_read_b64_tr_b16 a[56:57], v104 offset:0\n\t"
        "ds_read_b64_tr_b16 a[58:59], v104 offset:256\n\t"
        "v_mfma_f32_16x16x32_bf16 v[20:23], a[24:27], v[76:79], v[20:23]\n\t"
        "s_waitcnt lgkmcnt(5)\n\t"
        "ds_read_b64_tr_b16 a[60:61], v103 offset:4096\n\t"
        "v_mfma_f32_16x16x32_bf16 v[24:27], a[28:31], v[76:79], v[24:27]\n\t"
        "ds_read_b64_tr_b16 a[62:63], v103 offset:4608\n\t"
        "ds_read_b64_tr_b16 a[64:65], v103 offset:4104\n\t"
        "v_mfma_f32_16x16x32_bf16 v[28:31], v[76:79], a[32:35], v[28:31]\n\t"
        "ds_read_b64_tr_b16 a[66:67], v103 offset:4616\n\t"
        "ds_read_b64_tr_b16 a[68:69], v104 offset:2048\n\t"
        "v_mfma_f32_16x16x32_bf16 v[8:11], a[36:39], v[80:83], v[8:11]\n\t"
        "ds_read_b64_tr_b16 a[70:71], v104 offset:2304\n\t"
        "ds_read_b128 v[56:59], v105 offset:16384\n\t"
        "v_mfma_f32_16x16x32_bf16 v[12:15], a[40:43], v[80:83], v[12:15]\n\t"
        "ds_read_b128 v[60:63], v105 offset:18432\n\t"
        "ds_read_b128 v[64:67], v106 offset:16384\n\t"
        "v_mfma_f32_16x16x32_bf16 v[16:19], v[80:83], a[44:47], v[16:19]\n\t"
        "ds_read_b128 v[68:71], v106 offset:18432\n\t"
        "v_mfma_f32_16x16x32_bf16 v[20:23], a[36:39], v[84:87], v[20:23]\n\t"
        "v_mfma_f32_16x16x32_bf16 v[24:27], a[40:43], v[84:87], v[24:27]\n\t"
        "v_mfma_f32_16x16x32_bf16 v[28:31], v[84:87], a[44:47], v[28:31]\n\t"
        "s_mov_b32 %[t0], 3145728\n\t"
        "buffer_load_dwordx4 v[136:139], v101, %[sq], %[t0] offen\n\t"
        "buffer_load_dwordx4 v[144:147], v112, %[sv], %[t0] offen\n\t"
        "s_mov_b32 %[t0], 3407872\n\t"
        "buffer_load_dwordx4 v[140:143], v101, %[sq], %[t0] offen\n\t"
        "s_waitcnt vmcnt(17)\n\t"
        "ds_write_b128 v102, a[228:231] offset:16384\n\t"
        "ds_write_b128 v102, a[232:235] offset:20480\n\t"
        "ds_write_b128 v116, a[236:239] offset:24576\n\t"
        "s_waitcnt lgkmcnt(0)\n\t"
        "s_barrier\n\t"
        "v_mfma_f32_16x16x32_bf16 v[8:11], a[48:51], v[56:59], v[8:11]\n\t"
        "ds_read_b64_tr_b16 a[72:73], v103 offset:16384\n\t"
        "ds_read_b64_tr_b16 a[74:75], v103 offset:16896\n\t"
        "v_mfma_f32_16x16x32_bf16 v[12:15], a[52:55], v[56:59], v[12:15]\n\t"
        "ds_read_b64_tr_b16 a[76:77], v103 offset:16392\n\t"
        "ds_read_b64_tr_b16 a[78:79], v103 offset:16904\n\t"
        "v_mfma_f32_16x16x32_bf16 v[16:19], v[56:59], a[56:59], v[16:19]\n\t"
        "ds_read_b64_tr_b16 a[80:81], v104 offset:16384\n\t"
        "ds_read_b64_tr_b16 a[82:83], v104 offset:16640\n\t"
        "v_mfma_f32_16x16x32_bf16 v[20:23], a[48:51], v[60:63], v[20:23]\n\t"
        "s_waitcnt lgkmcnt(5)\n\t"
        "ds_read_b64_tr_b16 a[84:85], v103 offset:20480\n\t"
        "v_mfma_f32_16x16x32_bf16 v[24:27], a[52:55], v[60:63], v[24:27]\n\t"
        "ds_read_b64_tr_b16 a[86:87], v103 offset:20992\n\t"
        "ds_read_b64_tr_b16 a[88:89], v103 offset:20488\n\t"
        "v_mfma_f32_16x16x32_bf16 v[28:31], v[60:63], a[56:59], v[28:31]\n\t"
        "ds_read_b64_tr_b16 a[90:91], v103 offset:21000\n\t"
        "ds_read_b64_tr_b16 a[92:93], v104 offset:18432\n\t"
        "v_mfma_f32_16x16x32_bf16 v[8:11], a[60:63], v[64:67], v[8:11]\n\t"
        "ds_read_b64_tr_b16 a[94:95], v104 offset:18688\n\t"
        "ds_read_b128 v[72:75], v105 offset:24576\n\t"
        "v_mfma_f32_16x16x32_bf16 v[12:15], a[64:67], v[64:67], v[12:15]\n\t"
        "ds_read_b128 v[76:79], v105 offset:26624\n\t"
        "ds_read_b128 v[80:83], v106 offset:24576\n\t"
        "v_mfma_f32_16x16x32_bf16 v[16:19], v[64:67], a[68:71], v[16:19]\n\t"
        "ds_read_b128 v[84:87], v106 offset:26624\n\t"
        "v_mfma_f32_16x16x32_bf16 v[20:23], a[60:63], v[68:71], v[20:23]\n\t"
        "v_mfma_f32_16x16x32_bf16 v[24:27], a[64:67], v[68:71], v[24:27]\n\t"
        "v_mfma_f32_16x16x32_bf16 v[28:31], v[68:71], a[68:71], v[28:31]\n\t"
        "s_mov_b32 %[t0], 3670016\n\t"
        "buffer_load_dwordx4 v[148:151], v101, %[sq], %[t0] offen\n\t"
        "buffer_load_dwordx4 v[156:159], v112, %[sv], %[t0] offen\n\t"
        "s_mov_b32 %[t0], 3932160\n\t"
        "buffer_load_dwordx4 v[152:155], v101, %[sq], %[t0] offen\n\t"
        "s_waitcnt vmcnt(9)\n\t"
        "ds_write_b128 v102, a[240:243] offset:0\n\t"
        "ds_write_b128 v102, a[244:247] offset:4096\n\t"
        "ds_write_b128 v116, a[248:251] offset:8192\n\t"
        "s_waitcnt lgkmcnt(0)\n\t"
        "s_barrier\n\t"
        "v_mfma_f32_16x16x32_bf16 v[8:11], a[72:75], v[72:75], v[8:11]\n\t"
        "ds_read_b64_tr_b16 a[96:97], v103 offset:0\n\t"
        "ds_read_b64_tr_b16 a[98:99], v103 offset:512\n\t"
        "v_mfma_f32_16x16x32_bf16 v[12:15], a[76:79], v[72:75], v[12:15]\n\t"
        "ds_read_b64_tr_b16 a[100:101], v103 offset:8\n\t"
        "ds_read_b64_tr_b16 a[102:103], v103 offset:520\n\t"
        "v_mfma_f32_16x16x32_bf16 v[16:19], v[72:75], a[80:83], v[16:19]\n\t"
        "ds_read_b64_tr_b16 a[104:105], v104 offset:0\n\t"
        "ds_read_b64_tr_b16 a[106:107], v104 offset:256\n\t"
        "v_mfma_f32_16x16x32_bf16 v[20:23], a[72:75], v[76:79], v[20:23]\n\t"
        "s_waitcnt lgkmcnt(5)\n\t"
        "ds_read_b64_tr_b16 a[108:109], v103 offset:4096\n\t"
        "v_mfma_f32_16x16x32_bf16 v[24:27], a[76:79], v[76:79], v[24:27]\n\t"
        "ds_read_b64_tr_b16 a[110:111], v103 offset:4608\n\t"
        "ds_read_b64_tr_b16 a[112:113], v103 offset:4104\n\t"
        "v_mfma_f32_16x16x32_bf16 v[28:31], v[76:79], a[80:83], v[28:31]\n\t"
        "ds_read_b64_tr_b16 a[114:115], v103 offset:4616\n\t"
        "ds_read_b64_tr_b16 a[116:117], v104 offset:2048\n\t"
        "v_mfma_f32_16x16x32_bf16 v[8:11], a[84:87], v[80:83], v[8:11]\n\t"
        "ds_read_b64_tr_b16 a[118:119], v104 offset:2304\n\t"
        "ds_read_b128 v[56:59], v105 offset:32768\n\t"
        "v_mfma_f32_16x16x32_bf16 v[12:15], a[88:91], v[80:83], v[12:15]\n\t"
        "ds_read_b128 v[60:63], v105 offset:34816\n\t"
        "ds_read_b128 v[64:67], v106 offset:32768\n\t"
        "v_mfma_f32_16x16x32_bf16 v[16:19], v[80:83], a[92:95], v[16:19]\n\t"
        "ds_read_b128 v[68:71], v106 offset:34816\n\t"
        "v_mfma_f32_16x16x32_bf16 v[20:23], a[84:87], v[84:87], v[20:23]\n\t"
        "v_mfma_f32_16x16x32_bf16 v[24:27], a[88:91], v[84:87], v[24:27]\n\t"
        "v_mfma_f32_16x16x32_bf16 v[28:31], v[84:87], a[92:95], v[28:31]\n\t"
        "s_add_u32 m0, %[ld], 65536\n\t"
        "s_add_u32 %[t0], %[xo], 1024\n\t"
        "buffer_load_dwordx4 v100, %[sx], %[t0] offen lds\n\t"
        "s_add_u32 m0, %[ld], 66560\n\t"
        "s_add_u32 %[t0], %[xo], 17408\n\t"
        "buffer_load_dwordx4 v100, %[sx], %[t0] offen lds\n\t"
        "s_add_u32 m0, %[ld], 73728\n\t"
        "s_add_u32 %[t0], %[xo], 1152\n\t"
        "buffer_load_dwordx4 v100, %[sx], %[t0] offen lds\n\t"
        "s_add_u32 m0, %[ld], 74752\n\t"
        "s_add_u32 %[t0], %[xo], 17536\n\t"
        "buffer_load_dwordx4 v100, %[sx], %[t0] offen lds\n\t"
        "s_add_u32 m0, %[ld], 81920\n\t"
        "s_add_u32 %[t0], %[xo], 1280\n\t"
        "buffer_load_dwordx4 v100, %[sx], %[t0] offen lds\n\t"
        "s_add_u32 m0, %[ld], 82944\n\t"
        "s_add_u32 %[t0], %[xo], 17664\n\t"
        "buffer_load_dwordx4 v100, %[sx], %[t0] offen lds\n\t"
        "s_add_u32 m0, %[ld], 90112\n\t"
        "s_add_u32 %[t0], %[xo], 1408\n\t"
        "buffer_load_dwordx4 v100, %[sx], %[t0] offen lds\n\t"
        "s_add_u32 m0, %[ld], 91136\n\t"
        "s_add_u32 %[t0], %[xo], 17792\n\t"
        "buffer_load_dwordx4 v100, %[sx], %[t0] offen lds\n\t"
        "s_mov_b32 %[t0], 4194304\n\t"
        "buffer_load_dwordx4 v[160:163], v101, %[sq], %[t0] offen\n\t"
        "buffer_load_dwordx4 v[168:171], v112, %[sv], %[t0] offen\n\t"
        "s_mov_b32 %[t0], 4456448\n\t"
        "buffer_load_dwordx4 v[164:167], v101, %[sq], %[t0] offen\n\t"
        "s_waitcnt vmcnt(17)\n\t"
        "ds_write_b128 v102, a[252:255] offset:16384\n\t"
        "ds_write_b128 v102, v[128:131] offset:20480\n\t"
        "ds_write_b128 v116, v[132:135] offset:24576\n\t"
        "s_waitcnt lgkmcnt(0)\n\t"
        "s_barrier\n\t"
        "v_mfma_f32_16x16x32_bf16 v[8:11], a[96:99], v[56:59], v[8:11]\n\t"
        "ds_read_b64_tr_b16 a[120:121], v103 offset:16384\n\t"
        "ds_read_b64_tr_b16 a[122:123], v103 offset:16896\n\t"
        "v_mfma_f32_16x16x32_bf16 v[12:15], a[100:103], v[56:59], v[12:15]\n\t"
        "ds_read_b64_tr_b16 a[124:125], v103 offset:16392\n\t"
        "ds_read_b64_tr_b16 a[126:127], v103 offset:16904\n\t"
        "v_mfma_f32_16x16x32_bf16 v[16:19], v[56:59], a[104:107], v[16:19]\n\t"
        "ds_read_b64_tr_b16 a[128:129], v104 offset:16384\n\t"
        "ds_read_b64_tr_b16 a[130:131], v104 offset:16640\n\t"
        "v_mfma_f32_16x16x32_bf16 v[20:23], a[96:99], v[60:63], v[20:23]\n\t"
        "s_waitcnt lgkmcnt(5)\n\t"
        "ds_read_b64_tr_b16 a[132:133], v103 offset:20480\n\t"
        "v_mfma_f32_16x16x32_bf16 v[24:27], a[100:103], v[60:63], v[24:27]\n\t"
        "ds_read_b64_tr_b16 a[134:135], v103 offset:20992\n\t"
        "ds_read_b64_tr_b16 a[136:137], v103 offset:20488\n\t"
        "v_mfma_f32_16x16x32_bf16 v[28:31], v[60:63], a[104:107], v[28:31]\n\t"
        "ds_read_b64_tr_b16 a[138:139], v103 offset:21000\n\t"
        "ds_read_b64_tr_b16 a[140:141], v104 offset:18432\n\t"
        "v_mfma_f32_16x16x32_bf16 v[8:11], a[108:111], v[64:67], v[8:11]\n\t"
        "ds_read_b64_tr_b16 a[142:143], v104 offset:18688\n\t"
        "ds_read_b128 v[72:75], v105 offset:40960\n\t"
        "v_mfma_f32_16x16x32_bf16 v[12:15], a[112:115], v[64:67], v[12:15]\n\t"
        "ds_read_b128 v[76:79], v105 offset:43008\n\t"
        "ds_read_b128 v[80:83], v106 offset:40960\n\t"
        "v_mfma_f32_16x16x32_bf16 v[16:19], v[64:67], a[116:119], v[16:19]\n\t"
        "ds_read_b128 v[84:87], v106 offset:43008\n\t"
        "v_mfma_f32_16x16x32_bf16 v[20:23], a[108:111], v[68:71], v[20:23]\n\t"
        "v_mfma_f32_16x16x32_bf16 v[24:27], a[112:115], v[68:71], v[24:27]\n\t"
        "v_mfma_f32_16x16x32_bf16 v[28:31], v[68:71], a[116:119], v[28:31]\n\t"
        "s_mov_b32 %[t0], 4718592\n\t"
        "buffer_load_dwordx4 v[172:175], v101, %[sq], %[t0] offen\n\t"
        "buffer_load_dwordx4 v[180:183], v112, %[sv], %[t0] offen\n\t"
        "s_mov_b32 %[t0], 4980736\n\t"
        "buffer_load_dwordx4 v[176:179], v101, %[sq], %[t0] offen\n\t"
        "s_waitcnt vmcnt(17)\n\t"
        "ds_write_b128 v102, v[136:139] offset:0\n\t"
        "ds_write_b128 v102, v[140:143] offset:4096\n\t"
        "ds_write_b128 v116, v[144:147] offset:8192\n\t"
        "s_waitcnt lgkmcnt(0)\n\t"
        "s_barrier\n\t"
        "v_mfma_f32_16x16x32_bf16 v[8:11], a[120:123], v[72:75], v[8:11]\n\t"
        "ds_read_b64_tr_b16 a[144:145], v103 offset:0\n\t"
        "ds_read_b64_tr_b16 a[146:147], v103 offset:512\n\t"
        "v_mfma_f32_16x16x32_bf16 v[12:15], a[124:127], v[72:75], v[12:15]\n\t"
        "ds_read_b64_tr_b16 a[148:149], v103 offset:8\n\t"
        "ds_read_b64_tr_b16 a[150:151], v103 offset:520\n\t"
        "v_mfma_f32_16x16x32_bf16 v[16:19], v[72:75], a[128:131], v[16:19]\n\t"
        "ds_read_b64_tr_b16 a[152:153], v104 offset:0\n\t"
        "ds_read_b64_tr_b16 a[154:155], v104 offset:256\n\t"
        "v_mfma_f32_16x16x32_bf16 v[20:23], a[120:123], v[76:79], v[20:23]\n\t"
        "s_waitcnt lgkmcnt(5)\n\t"
        "ds_read_b64_tr_b16 a[156:157], v103 offset:4096\n\t"
        "v_mfma_f32_16x16x32_bf16 v[24:27], a[124:127], v[76:79], v[24:27]\n\t"
        "ds_read_b64_tr_b16 a[158:159], v103 offset:4608\n\t"
        "ds_read_b64_tr_b16 a[160:161], v103 offset:4104\n\t"
        "v_mfma_f32_16x16x32_bf16 v[28:31], v[76:79], a[128:131], v[28:31]\n\t"
        "ds_read_b64_tr_b16 a[162:163], v103 offset:4616\n\t"
        "ds_read_b64_tr_b16 a[164:165], v104 offset:2048\n\t"
        "v_mfma_f32_16x16x32_bf16 v[8:11], a[132:135], v[80:83], v[8:11]\n\t"
        "ds_read_b64_tr_b16 a[166:167], v104 offset:2304\n\t"
        "ds_read_b128 v[56:59], v105 offset:49152\n\t"
        "v_mfma_f32_16x16x32_bf16 v[12:15], a[136:139], v[80:83], v[12:15]\n\t"
        "ds_read_b128 v[60:63], v105 offset:51200\n\t"
        "ds_read_b128 v[64:67], v106 offset:49152\n\t"
        "v_mfma_f32_16x16x32_bf16 v[16:19], v[80:83], a[140:143], v[16:19]\n\t"
        "ds_read_b128 v[68:71], v106 offset:51200\n\t"
        "v_mfma_f32_16x16x32_bf16 v[20:23], a[132:135], v[84:87], v[20:23]\n\t"
        "v_mfma_f32_16x16x32_bf16 v[24:27], a[136:139], v[84:87], v[24:27]\n\t"
        "v_mfma_f32_16x16x32_bf16 v[28:31], v[84:87], a[140:143], v[28:31]\n\t"
        "s_mov_b32 %[t0], 5242880\n\t"
        "buffer_load_dwordx4 v[184:187], v101, %[sq], %[t0] offen\n\t"
        "buffer_load_dwordx4 v[192:195], v112, %[sv], %[t0] offen\n\t"
        "s_mov_b32 %[t0], 5505024\n\t"
        "buffer_load_dwordx4 v[188:191], v101, %[sq], %[t0] offen\n\t"
        "s_waitcnt vmcnt(17)\n\t"
        "ds_write_b128 v102, v[148:151] offset:16384\n\t"
        "ds_write_b128 v102, v[152:155] offset:20480\n\t"
        "ds_write_b128 v116, v[156:159] offset:24576\n\t"
        "s_waitcnt lgkmcnt(0)\n\t"
        "s_barrier\n\t"
        "v_mfma_f32_16x16x32_bf16 v[8:11], a[144:147], v[56:59], v[8:11]\n\t"
        "ds_read_b64_tr_b16 a[168:169], v103 offset:16384\n\t"
        "ds_read_b64_tr_b16 a[170:171], v103 offset:16896\n\t"
        "v_mfma_f32_16x16x32_bf16 v[12:15], a[148:151], v[56:59], v[12:15]\n\t"
        "ds_read_b64_tr_b16 a[172:173], v103 offset:16392\n\t"
        "ds_read_b64_tr_b16 a[174:175], v103 offset:16904\n\t"
        "v_mfma_f32_16x16x32_bf16 v[16:19], v[56:59], a[152:155], v[16:19]\n\t"
        "ds_read_b64_tr_b16 a[176:177], v104 offset:16384\n\t"
        "ds_read_b64_tr_b16 a[178:179], v104 offset:16640\n\t"
        "v_mfma_f32_16x16x32_bf16 v[20:23], a[144:147], v[60:63], v[20:23]\n\t"
        "s_waitcnt lgkmcnt(5)\n\t"
        "ds_read_b64_tr_b16 a[180:181], v103 offset:20480\n\t"
        "v_mfma_f32_16x16x32_bf16 v[24:27], a[148:151], v[60:63], v[24:27]\n\t"
        "ds_read_b64_tr_b16 a[182:183], v103 offset:20992\n\t"
        "ds_read_b64_tr_b16 a[184:185], v103 offset:20488\n\t"
        "v_mfma_f32_16x16x32_bf16 v[28:31], v[60:63], a[152:155], v[28:31]\n\t"
        "ds_read_b64_tr_b16 a[186:187], v103 offset:21000\n\t"
        "ds_read_b64_tr_b16 a[188:189], v104 offset:18432\n\t"
        "v_mfma_f32_16x16x32_bf16 v[8:11], a[156:159], v[64:67], v[8:11]\n\t"
        "ds_read_b64_tr_b16 a[190:191], v104 offset:18688\n\t"
        "ds_read_b128 v[72:75], v105 offset:57344\n\t"
        "v_mfma_f32_16x16x32_bf16 v[12:15], a[160:163], v[64:67], v[12:15]\n\t"
        "ds_read_b128 v[76:79], v105 offset:59392\n\t"
        "ds_read_b128 v[80:83], v106 offset:57344\n\t"
        "v_mfma_f32_16x16x32_bf16 v[16:19], v[64:67], a[164:167], v[16:19]\n\t"
        "ds_read_b128 v[84:87], v106 offset:59392\n\t"
        "v_mfma_f32_16x16x32_bf16 v[20:23], a[156:159], v[68:71], v[20:23]\n\t"
        "v_mfma_f32_16x16x32_bf16 v[24:27], a[160:163], v[68:71], v[24:27]\n\t"
        "v_mfma_f32_16x16x32_bf16 v[28:31], v[68:71], a[164:167], v[28:31]\n\t"
        "s_mov_b32 %[t0], 5767168\n\t"
        "buffer_load_dwordx4 v[196:199], v101, %[sq], %[t0] offen\n\t"
        "buffer_load_dwordx4 v[204:207], v112, %[sv], %[t0] offen\n\t"
        "s_mov_b32 %[t0], 6029312\n\t"
        "buffer_load_dwordx4 v[200:203], v101, %[sq], %[t0] offen\n\t"
        "s_waitcnt vmcnt(9)\n\t"
        "ds_write_b128 v102, v[160:163] offset:0\n\t"
        "ds_write_b128 v102, v[164:167] offset:4096\n\t"
        "ds_write_b128 v116, v[168:171] offset:8192\n\t"
        "s_waitcnt lgkmcnt(0)\n\t"
        "s_barrier\n\t"
        "v_mfma_f32_16x16x32_bf16 v[8:11], a[168:171], v[72:75], v[8:11]\n\t"
        "ds_read_b64_tr_b16 a[192:193], v103 offset:0\n\t"
        "ds_read_b64_tr_b16 a[194:195], v103 offset:512\n\t"
        "v_mfma_f32_16x16x32_bf16 v[12:15], a[172:175], v[72:75], v[12:15]\n\t"
        "ds_read_b64_tr_b16 a[196:197], v103 offset:8\n\t"
        "ds_read_b64_tr_b16 a[198:199], v103 offset:520\n\t"
        "v_mfma_f32_16x16x32_bf16 v[16:19], v[72:75], a[176:179], v[16:19]\n\t"
        "ds_read_b64_tr_b16 a[200:201], v104 offset:0\n\t"
        "ds_read_b64_tr_b16 a[202:203], v104 offset:256\n\t"
        "v_mfma_f32_16x16x32_bf16 v[20:23], a[168:171], v[76:79], v[20:23]\n\t"
        "s_waitcnt lgkmcnt(5)\n\t"
        "ds_read_b64_tr_b16 a[204:205], v103 offset:4096\n\t"
        "v_mfma_f32_16x16x32_bf16 v[24:27], a[172:175], v[76:79], v[24:27]\n\t"
        "ds_read_b64_tr_b16 a[206:207], v103 offset:4608\n\t"
        "ds_read_b64_tr_b16 a[208:209], v103 offset:4104\n\t"
        "v_mfma_f32_16x16x32_bf16 v[28:31], v[76:79], a[176:179], v[28:31]\n\t"
        "ds_read_b64_tr_b16 a[210:211], v103 offset:4616\n\t"
        "ds_read_b64_tr_b16 a[212:213], v104 offset:2048\n\t"
        "v_mfma_f32_16x16x32_bf16 v[8:11], a[180:183], v[80:83], v[8:11]\n\t"
        "ds_read_b64_tr_b16 a[214:215], v104 offset:2304\n\t"
        "ds_read_b128 v[56:59], v107 offset:0\n\t"
        "v_mfma_f32_16x16x32_bf16 v[12:15], a[184:187], v[80:83], v[12:15]\n\t"
        "ds_read_b128 v[60:63], v107 offset:2048\n\t"
        "ds_read_b128 v[64:67], v108 offset:0\n\t"
        "v_mfma_f32_16x16x32_bf16 v[16:19], v[80:83], a[188:191], v[16:19]\n\t"
        "ds_read_b128 v[68:71], v108 offset:2048\n\t"
        "v_mfma_f32_16x16x32_bf16 v[20:23], a[180:183], v[84:87], v[20:23]\n\t"
        "v_mfma_f32_16x16x32_bf16 v[24:27], a[184:187], v[84:87], v[24:27]\n\t"
        "v_mfma_f32_16x16x32_bf16 v[28:31], v[84:87], a[188:191], v[28:31]\n\t"
        "s_add_u32 m0, %[ld], 98304\n\t"
        "s_add_u32 %[t0], %[xo], 1536\n\t"
        "buffer_load_dwordx4 v100, %[sx], %[t0] offen lds\n\t"
        "s_add_u32 m0, %[ld], 99328\n\t"
        "s_add_u32 %[t0], %[xo], 17920\n\t"
        "buffer_load_dwordx4 v100, %[sx], %[t0] offen lds\n\t"
        "s_add_u32 m0, %[ld], 106496\n\t"
        "s_add_u32 %[t0], %[xo], 1664\n\t"
        "buffer_load_dwordx4 v100, %[sx], %[t0] offen lds\n\t"
        "s_add_u32 m0, %[ld], 107520\n\t"
        "s_add_u32 %[t0], %[xo], 18048\n\t"
        "buffer_load_dwordx4 v100, %[sx], %[t0] offen lds\n\t"
        "s_add_u32 m0, %[ld], 114688\n\t"
        "s_add_u32 %[t0], %[xo], 1792\n\t"
        "buffer_load_dwordx4 v100, %[sx], %[t0] offen lds\n\t"
        "s_add_u32 m0, %[ld], 115712\n\t"
        "s_add_u32 %[t0], %[xo], 18176\n\t"
        "buffer_load_dwordx4 v100, %[sx], %[t0] offen lds\n\t"
        "s_add_u32 m0, %[ld], 122880\n\t"
        "s_add_u32 %[t0], %[xo], 1920\n\t"
        "buffer_load_dwordx4 v100, %[sx], %[t0] offen lds\n\t"
        "s_add_u32 m0, %[ld], 123904\n\t"
        "s_add_u32 %[t0], %[xo], 18304\n\t"
        "buffer_load_dwordx4 v100, %[sx], %[t0] offen lds\n\t"
        "s_mov_b32 %[t0], 6291456\n\t"
        "buffer_load_dwordx4 v[208:211], v101, %[sq], %[t0] offen\n\t"
        "buffer_load_dwordx4 v[216:219], v112, %[sv], %[t0] offen\n\t"
        "s_mov_b32 %[t0], 6553600\n\t"
        "buffer_load_dwordx4 v[212:215], v101, %[sq], %[t0] offen\n\t"
        "s_waitcnt vmcnt(17)\n\t"
        "ds_write_b128 v102, v[172:175] offset:16384\n\t"
        "ds_write_b128 v102, v[176:179] offset:20480\n\t"
        "ds_write_b128 v116, v[180:183] offset:24576\n\t"
        "s_waitcnt lgkmcnt(0)\n\t"
        "s_barrier\n\t"
        "v_mfma_f32_16x16x32_bf16 v[8:11], a[192:195], v[56:59], v[8:11]\n\t"
        "ds_read_b64_tr_b16 a[216:217], v103 offset:16384\n\t"
        "ds_read_b64_tr_b16 a[218:219], v103 offset:16896\n\t"
        "v_mfma_f32_16x16x32_bf16 v[12:15], a[196:199], v[56:59], v[12:15]\n\t"
        "ds_read_b64_tr_b16 a[220:221], v103 offset:16392\n\t"
        "ds_read_b64_tr_b16 a[222:223], v103 offset:16904\n\t"
        "v_mfma_f32_16x16x32_bf16 v[16:19], v[56:59], a[200:203], v[16:19]\n\t"
        "ds_read_b64_tr_b16 a[224:225], v104 offset:16384\n\t"
        "ds_read_b64_tr_b16 a[226:227], v104 offset:16640\n\t"
        "v_mfma_f32_16x16x32_bf16 v[20:23], a[192:195], v[60:63], v[20:23]\n\t"
        "s_waitcnt lgkmcnt(5)\n\t"
        "ds_read_b64_tr_b16 a[228:229], v103 offset:20480\n\t"
        "v_mfma_f32_16x16x32_bf16 v[24:27], a[196:199], v[60:63], v[24:27]\n\t"
        "ds_read_b64_tr_b16 a[230:231], v103 offset:20992\n\t"
        "ds_read_b64_tr_b16 a[232:233], v103 offset:20488\n\t"
        "v_mfma_f32_16x16x32_bf16 v[28:31], v[60:63], a[200:203], v[28:31]\n\t"
        "ds_read_b64_tr_b16 a[234:235], v103 offset:21000\n\t"
        "ds_read_b64_tr_b16 a[236:237], v104 offset:18432\n\t"
        "v_mfma_f32_16x16x32_bf16 v[8:11], a[204:207], v[64:67], v[8:11]\n\t"
        "ds_read_b64_tr_b16 a[238:239], v104 offset:18688\n\t"
        "ds_read_b128 v[72:75], v107 offset:8192\n\t"
        "v_mfma_f32_16x16x32_bf16 v[12:15], a[208:211], v[64:67], v[12:15]\n\t"
        "ds_read_b128 v[76:79], v107 offset:10240\n\t"
        "ds_read_b128 v[80:83], v108 offset:8192\n\t"
        "v_mfma_f32_16x16x32_bf16 v[16:19], v[64:67], a[212:215], v[16:19]\n\t"
        "ds_read_b128 v[84:87], v108 offset:10240\n\t"
        "v_mfma_f32_16x16x32_bf16 v[20:23], a[204:207], v[68:71], v[20:23]\n\t"
        "v_mfma_f32_16x16x32_bf16 v[24:27], a[208:211], v[68:71], v[24:27]\n\t"
        "v_mfma_f32_16x16x32_bf16 v[28:31], v[68:71], a[212:215], v[28:31]\n\t"
        "s_mov_b32 %[t0], 6815744\n\t"
        "buffer_load_dwordx4 v[220:223], v101, %[sq], %[t0] offen\n\t"
        "buffer_load_dwordx4 v[228:231], v112, %[sv], %[t0] offen\n\t"
        "s_mov_b32 %[t0], 7077888\n\t"
        "buffer_load_dwordx4 v[224:227], v101, %[sq], %[t0] offen\n\t"
        "s_waitcnt vmcnt(17)\n\t"
        "ds_write_b128 v102, v[184:187] offset:0\n\t"
        "ds_write_b128 v102, v[188:191] offset:4096\n\t"
        "ds_write_b128 v116, v[192:195] offset:8192\n\t"
        "s_waitcnt lgkmcnt(0)\n\t"
        "s_barrier\n\t"
        "v_mfma_f32_16x16x32_bf16 v[8:11], a[216:219], v[72:75], v[8:11]\n\t"
        "ds_read_b64_tr_b16 a[240:241], v103 offset:0\n\t"
        "ds_read_b64_tr_b16 a[242:243], v103 offset:512\n\t"
        "v_mfma_f32_16x16x32_bf16 v[12:15], a[220:223], v[72:75], v[12:15]\n\t"
        "ds_read_b64_tr_b16 a[244:245], v103 offset:8\n\t"
        "ds_read_b64_tr_b16 a[246:247], v103 offset:520\n\t"
        "v_mfma_f32_16x16x32_bf16 v[16:19], v[72:75], a[224:227], v[16:19]\n\t"
        "ds_read_b64_tr_b16 a[248:249], v104 offset:0\n\t"
        "ds_read_b64_tr_b16 a[250:251], v104 offset:256\n\t"
        "v_mfma_f32_16x16x32_bf16 v[20:23], a[216:219], v[76:79], v[20:23]\n\t"
        "s_waitcnt lgkmcnt(5)\n\t"
        "ds_read_b64_tr_b16 a[252:253], v103 offset:4096\n\t"
        "v_mfma_f32_16x16x32_bf16 v[24:27], a[220:223], v[76:79], v[24:27]\n\t"
        "ds_read_b64_tr_b16 a[254:255], v103 offset:4608\n\t"
        "ds_read_b64_tr_b16 v[128:129], v103 offset:4104\n\t"
        "v_mfma_f32_16x16x32_bf16 v[28:31], v[76:79], a[224:227], v[28:31]\n\t"
        "ds_read_b64_tr_b16 v[130:131], v103 offset:4616\n\t"
        "ds_read_b64_tr_b16 v[132:133], v104 offset:2048\n\t"
        "v_mfma_f32_16x16x32_bf16 v[8:11], a[228:231], v[80:83], v[8:11]\n\t"
        "ds_read_b64_tr_b16 v[134:135], v104 offset:2304\n\t"
        "ds_read_b128 v[56:59], v107 offset:16384\n\t"
        "v_mfma_f32_16x16x32_bf16 v[12:15], a[232:235], v[80:83], v[12:15]\n\t"
        "ds_read_b128 v[60:63], v107 offset:18432\n\t"
        "ds_read_b128 v[64:67], v108 offset:16384\n\t"
        "v_mfma_f32_16x16x32_bf16 v[16:19], v[80:83], a[236:239], v[16:19]\n\t"
        "ds_read_b128 v[68:71], v108 offset:18432\n\t"
        "v_mfma_f32_16x16x32_bf16 v[20:23], a[228:231], v[84:87], v[20:23]\n\t"
        "v_mfma_f32_16x16x32_bf16 v[24:27], a[232:235], v[84:87], v[24:27]\n\t"
        "v_mfma_f32_16x16x32_bf16 v[28:31], v[84:87], a[236:239], v[28:31]\n\t"
        "s_mov_b32 %[t0], 7340032\n\t"
        "buffer_load_dwordx4 v[232:235], v101, %[sq], %[t0] offen\n\t"
        "buffer_load_dwordx4 v[240:243], v112, %[sv], %[t0] offen\n\t"
        "s_mov_b32 %[t0], 7602176\n\t"
        "buffer_load_dwordx4 v[236:239], v101, %[sq], %[t0] offen\n\t"
        "s_waitcnt vmcnt(17)\n\t"
        "ds_write_b128 v102, v[196:199] offset:16384\n\t"
        "ds_write_b128 v102, v[200:203] offset:20480\n\t"
        "ds_write_b128 v116, v[204:207] offset:24576\n\t"
        "s_waitcnt lgkmcnt(0)\n\t"
        "s_barrier\n\t"
        "v_mfma_f32_16x16x32_bf16 v[8:11], a[240:243], v[56:59], v[8:11]\n\t"
        "ds_read_b64_tr_b16 v[136:137], v103 offset:16384\n\t"
        "ds_read_b64_tr_b16 v[138:139], v103 offset:16896\n\t"
        "v_mfma_f32_16x16x32_bf16 v[12:15], a[244:247], v[56:59], v[12:15]\n\t"
        "ds_read_b64_tr_b16 v[140:141], v103 offset:16392\n\t"
        "ds_read_b64_tr_b16 v[142:143], v103 offset:16904\n\t"
        "v_mfma_f32_16x16x32_bf16 v[16:19], v[56:59], a[248:251], v[16:19]\n\t"
        "ds_read_b64_tr_b16 v[144:145], v104 offset:16384\n\t"
        "ds_read_b64_tr_b16 v[146:147], v104 offset:16640\n\t"
        "v_mfma_f32_16x16x32_bf16 v[20:23], a[240:243], v[60:63], v[20:23]\n\t"
        "s_waitcnt lgkmcnt(5)\n\t"
        "ds_read_b64_tr_b16 v[148:149], v103 offset:20480\n\t"
        "v_mfma_f32_16x16x32_bf16 v[24:27], a[244:247], v[60:63], v[24:27]\n\t"
        "ds_read_b64_tr_b16 v[150:151], v103 offset:20992\n\t"
        "ds_read_b64_tr_b16 v[152:153], v103 offset:20488\n\t"
        "v_mfma_f32_16x16x32_bf16 v[28:31], v[60:63], a[248:251], v[28:31]\n\t"
        "ds_read_b64_tr_b16 v[154:155], v103 offset:21000\n\t"
        "ds_read_b64_tr_b16 v[156:157], v104 offset:18432\n\t"
        "v_mfma_f32_16x16x32_bf16 v[8:11], a[252:255], v[64:67], v[8:11]\n\t"
        "ds_read_b64_tr_b16 v[158:159], v104 offset:18688\n\t"
        "ds_read_b128 v[72:75], v107 offset:24576\n\t"
        "v_mfma_f32_16x16x32_bf16 v[12:15], v[128:131], v[64:67], v[12:15]\n\t"
        "ds_read_b128 v[76:79], v107 offset:26624\n\t"
        "ds_read_b128 v[80:83], v108 offset:24576\n\t"
        "v_mfma_f32_16x16x32_bf16 v[16:19], v[64:67], v[132:135], v[16:19]\n\t"
        "ds_read_b128 v[84:87], v108 offset:26624\n\t"
        "v_mfma_f32_16x16x32_bf16 v[20:23], a[252:255], v[68:71], v[20:23]\n\t"
        "v_mfma_f32_16x16x32_bf16 v[24:27], v[128:131], v[68:71], v[24:27]\n\t"
        "v_mfma_f32_16x16x32_bf16 v[28:31], v[68:71], v[132:135], v[28:31]\n\t"
        "s_mov_b32 %[t0], 7864320\n\t"
        "buffer_load_dwordx4 v[244:247], v101, %[sq], %[t0] offen\n\t"
        "buffer_load_dwordx4 v[252:255], v112, %[sv], %[t0] offen\n\t"
        "s_mov_b32 %[t0], 8126464\n\t"
        "buffer_load_dwordx4 v[248:251], v101, %[sq], %[t0] offen\n\t"
        "s_waitcnt vmcnt(9)\n\t"
        "ds_write_b128 v102, v[208:211] offset:0\n\t"
        "ds_write_b128 v102, v[212:215] offset:4096\n\t"
        "ds_write_b128 v116, v[216:219] offset:8192\n\t"
        "s_waitcnt lgkmcnt(0)\n\t"
        "s_barrier\n\t"
        "v_mfma_f32_16x16x32_bf16 v[8:11], v[136:139], v[72:75], v[8:11]\n\t"
        "ds_read_b64_tr_b16 v[160:161], v103 offset:0\n\t"
        "ds_read_b64_tr_b16 v[162:163], v103 offset:512\n\t"
        "v_mfma_f32_16x16x32_bf16 v[12:15], v[140:143], v[72:75], v[12:15]\n\t"
        "ds_read_b64_tr_b16 v[164:165], v103 offset:8\n\t"
        "ds_read_b64_tr_b16 v[166:167], v103 offset:520\n\t"
        "v_mfma_f32_16x16x32_bf16 v[16:19], v[72:75], v[144:147], v[16:19]\n\t"
        "ds_read_b64_tr_b16 v[168:169], v104 offset:0\n\t"
        "ds_read_b64_tr_b16 v[170:171], v104 offset:256\n\t"
        "v_mfma_f32_16x16x32_bf16 v[20:23], v[136:139], v[76:79], v[20:23]\n\t"
        "s_waitcnt lgkmcnt(5)\n\t"
        "ds_read_b64_tr_b16 v[172:173], v103 offset:4096\n\t"
        "v_mfma_f32_16x16x32_bf16 v[24:27], v[140:143], v[76:79], v[24:27]\n\t"
        "ds_read_b64_tr_b16 v[174:175], v103 offset:4608\n\t"
        "ds_read_b64_tr_b16 v[176:177], v103 offset:4104\n\t"
        "v_mfma_f32_16x16x32_bf16 v[28:31], v[76:79], v[144:147], v[28:31]\n\t"
        "ds_read_b64_tr_b16 v[178:179], v103 offset:4616\n\t"
        "ds_read_b64_tr_b16 v[180:181], v104 offset:2048\n\t"
        "v_mfma_f32_16x16x32_bf16 v[8:11], v[148:151], v[80:83], v[8:11]\n\t"
        "ds_read_b64_tr_b16 v[182:183], v104 offset:2304\n\t"
        "ds_read_b128 v[56:59], v107 offset:32768\n\t"
        "v_mfma_f32_16x16x32_bf16 v[12:15], v[152:155], v[80:83], v[12:15]\n\t"
        "ds_read_b128 v[60:63], v107 offset:34816\n\t"
        "ds_read_b128 v[64:67], v108 offset:32768\n\t"
        "v_mfma_f32_16x16x32_bf16 v[16:19], v[80:83], v[156:159], v[16:19]\n\t"
        "ds_read_b128 v[68:71], v108 offset:34816\n\t"
        "v_mfma_f32_16x16x32_bf16 v[20:23], v[148:151], v[84:87], v[20:23]\n\t"
        "v_mfma_f32_16x16x32_bf16 v[24:27], v[152:155], v[84:87], v[24:27]\n\t"
        "v_mfma_f32_16x16x32_bf16 v[28:31], v[84:87], v[156:159], v[28:31]\n\t"
        "s_mov_b32 %[xo], 0x20000\n\t"
        "s_add_u32 m0, %[ld], 0\n\t"
        "s_add_u32 %[t0], %[xo], 0\n\t"
        "buffer_load_dwordx4 v100, %[sx], %[t0] offen lds\n\t"
        "s_add_u32 m0, %[ld], 1024\n\t"
        "s_add_u32 %[t0], %[xo], 16384\n\t"
        "buffer_load_dwordx4 v100, %[sx], %[t0] offen lds\n\t"
        "s_add_u32 m0, %[ld], 8192\n\t"
        "s_add_u32 %[t0], %[xo], 128\n\t"
        "buffer_load_dwordx4 v100, %[sx], %[t0] offen lds\n\t"
        "s_add_u32 m0, %[ld], 9216\n\t"
        "s_add_u32 %[t0], %[xo], 16512\n\t"
        "buffer_load_dwordx4 v100, %[sx], %[t0] offen lds\n\t"
        "s_add_u32 m0, %[ld], 16384\n\t"
        "s_add_u32 %[t0], %[xo], 256\n\t"
        "buffer_load_dwordx4 v100, %[sx], %[t0] offen lds\n\t"
        "s_add_u32 m0, %[ld], 17408\n\t"
        "s_add_u32 %[t0], %[xo], 16640\n\t"
        "buffer_load_dwordx4 v100, %[sx], %[t0] offen lds\n\t"
        "s_add_u32 m0, %[ld], 24576\n\t"
        "s_add_u32 %[t0], %[xo], 384\n\t"
        "buffer_load_dwordx4 v100, %[sx], %[t0] offen lds\n\t"
        "s_add_u32 m0, %[ld], 25600\n\t"
        "s_add_u32 %[t0], %[xo], 16768\n\t"
        "buffer_load_dwordx4 v100, %[sx], %[t0] offen lds\n\t"
        "s_mov_b32 %[xo], 0\n\t"
        "s_waitcnt vmcnt(14)\n\t"
        "ds_write_b128 v102, v[220:223] offset:16384\n\t"
        "ds_write_b128 v102, v[224:227] offset:20480\n\t"
        "ds_write_b128 v116, v[228:231] offset:24576\n\t"
        "s_waitcnt lgkmcnt(0)\n\t"
        "s_barrier\n\t"
        "v_mfma_f32_16x16x32_bf16 v[8:11], v[160:163], v[56:59], v[8:11]\n\t"
        "ds_read_b64_tr_b16 v[184:185], v103 offset:16384\n\t"
        "ds_read_b64_tr_b16 v[186:187], v103 offset:16896\n\t"
        "v_mfma_f32_16x16x32_bf16 v[12:15], v[164:167], v[56:59], v[12:15]\n\t"
        "ds_read_b64_tr_b16 v[188:189], v103 offset:16392\n\t"
        "ds_read_b64_tr_b16 v[190:191], v103 offset:16904\n\t"
        "v_mfma_f32_16x16x32_bf16 v[16:19], v[56:59], v[168:171], v[16:19]\n\t"
        "ds_read_b64_tr_b16 v[192:193], v104 offset:16384\n\t"
        "ds_read_b64_tr_b16 v[194:195], v104 offset:16640\n\t"
        "v_mfma_f32_16x16x32_bf16 v[20:23], v[160:163], v[60:63], v[20:23]\n\t"
        "s_waitcnt lgkmcnt(5)\n\t"
        "ds_read_b64_tr_b16 v[196:197], v103 offset:20480\n\t"
        "v_mfma_f32_16x16x32_bf16 v[24:27], v[164:167], v[60:63], v[24:27]\n\t"
        "ds_read_b64_tr_b16 v[198:199], v103 offset:20992\n\t"
        "ds_read_b64_tr_b16 v[200:201], v103 offset:20488\n\t"
        "v_mfma_f32_16x16x32_bf16 v[28:31], v[60:63], v[168:171], v[28:31]\n\t"
        "ds_read_b64_tr_b16 v[202:203], v103 offset:21000\n\t"
        "ds_read_b64_tr_b16 v[204:205], v104 offset:18432\n\t"
        "v_mfma_f32_16x16x32_bf16 v[8:11], v[172:175], v[64:67], v[8:11]\n\t"
        "ds_read_b64_tr_b16 v[206:207], v104 offset:18688\n\t"
        "ds_read_b128 v[72:75], v107 offset:40960\n\t"
        "v_mfma_f32_16x16x32_bf16 v[12:15], v[176:179], v[64:67], v[12:15]\n\t"
        "ds_read_b128 v[76:79], v107 offset:43008\n\t"
        "ds_read_b128 v[80:83], v108 offset:40960\n\t"
        "v_mfma_f32_16x16x32_bf16 v[16:19], v[64:67], v[180:183], v[16:19]\n\t"
        "ds_read_b128 v[84:87], v108 offset:43008\n\t"
        "v_mfma_f32_16x16x32_bf16 v[20:23], v[172:175], v[68:71], v[20:23]\n\t"
        "v_mfma_f32_16x16x32_bf16 v[24:27], v[176:179], v[68:71], v[24:27]\n\t"
        "v_mfma_f32_16x16x32_bf16 v[28:31], v[68:71], v[180:183], v[28:31]\n\t"
        "s_waitcnt vmcnt(11)\n\t"
        "ds_write_b128 v102, v[232:235] offset:0\n\t"
        "ds_write_b128 v102, v[236:239] offset:4096\n\t"
        "ds_write_b128 v116, v[240:243] offset:8192\n\t"
        "s_waitcnt lgkmcnt(0)\n\t"
        "s_barrier\n\t"
        "v_mfma_f32_16x16x32_bf16 v[8:11], v[184:187], v[72:75], v[8:11]\n\t"
        "ds_read_b64_tr_b16 v[208:209], v103 offset:0\n\t"
        "ds_read_b64_tr_b16 v[210:211], v103 offset:512\n\t"
        "v_mfma_f32_16x16x32_bf16 v[12:15], v[188:191], v[72:75], v[12:15]\n\t"
        "ds_read_b64_tr_b16 v[212:213], v103 offset:8\n\t"
        "ds_read_b64_tr_b16 v[214:215], v103 offset:520\n\t"
        "v_mfma_f32_16x16x32_bf16 v[16:19], v[72:75], v[192:195], v[16:19]\n\t"
        "ds_read_b64_tr_b16 v[216:217], v104 offset:0\n\t"
        "ds_read_b64_tr_b16 v[218:219], v104 offset:256\n\t"
        "v_mfma_f32_16x16x32_bf16 v[20:23], v[184:187], v[76:79], v[20:23]\n\t"
        "s_waitcnt lgkmcnt(5)\n\t"
        "ds_read_b64_tr_b16 v[220:221], v103 offset:4096\n\t"
        "v_mfma_f32_16x16x32_bf16 v[24:27], v[188:191], v[76:79], v[24:27]\n\t"
        "ds_read_b64_tr_b16 v[222:223], v103 offset:4608\n\t"
        "ds_read_b64_tr_b16 v[224:225], v103 offset:4104\n\t"
        "v_mfma_f32_16x16x32_bf16 v[28:31], v[76:79], v[192:195], v[28:31]\n\t"
        "ds_read_b64_tr_b16 v[226:227], v103 offset:4616\n\t"
        "ds_read_b64_tr_b16 v[228:229], v104 offset:2048\n\t"
        "v_mfma_f32_16x16x32_bf16 v[8:11], v[196:199], v[80:83], v[8:11]\n\t"
        "ds_read_b64_tr_b16 v[230:231], v104 offset:2304\n\t"
        "ds_read_b128 v[56:59], v107 offset:49152\n\t"
        "v_mfma_f32_16x16x32_bf16 v[12:15], v[200:203], v[80:83], v[12:15]\n\t"
        "ds_read_b128 v[60:63], v107 offset:51200\n\t"
        "ds_read_b128 v[64:67], v108 offset:49152\n\t"
        "v_mfma_f32_16x16x32_bf16 v[16:19], v[80:83], v[204:207], v[16:19]\n\t"
        "ds_read_b128 v[68:71], v108 offset:51200\n\t"
        "v_mfma_f32_16x16x32_bf16 v[20:23], v[196:199], v[84:87], v[20:23]\n\t"
        "v_mfma_f32_16x16x32_bf16 v[24:27], v[200:203], v[84:87], v[24:27]\n\t"
        "v_mfma_f32_16x16x32_bf16 v[28:31], v[84:87], v[204:207], v[28:31]\n\t"
        "s_waitcnt vmcnt(8)\n\t"
        "ds_write_b128 v102, v[244:247] offset:16384\n\t"
        "ds_write_b128 v102, v[248:251] offset:20480\n\t"
        "ds_write_b128 v116, v[252:255] offset:24576\n\t"
        "s_waitcnt lgkmcnt(0)\n\t"
        "s_barrier\n\t"
        "v_mfma_f32_16x16x32_bf16 v[8:11], v[208:211], v[56:59], v[8:11]\n\t"
        "ds_read_b64_tr_b16 v[232:233], v103 offset:16384\n\t"
        "ds_read_b64_tr_b16 v[234:235], v103 offset:16896\n\t"
        "v_mfma_f32_16x16x32_bf16 v[12:15], v[212:215], v[56:59], v[12:15]\n\t"
        "ds_read_b64_tr_b16 v[236:237], v103 offset:16392\n\t"
        "ds_read_b64_tr_b16 v[238:239], v103 offset:16904\n\t"
        "v_mfma_f32_16x16x32_bf16 v[16:19], v[56:59], v[216:219], v[16:19]\n\t"
        "ds_read_b64_tr_b16 v[240:241], v104 offset:16384\n\t"
        "ds_read_b64_tr_b16 v[242:243], v104 offset:16640\n\t"
        "v_mfma_f32_16x16x32_bf16 v[20:23], v[208:211], v[60:63], v[20:23]\n\t"
        "s_waitcnt lgkmcnt(5)\n\t"
        "ds_read_b64_tr_b16 v[244:245], v103 offset:20480\n\t"
        "v_mfma_f32_16x16x32_bf16 v[24:27], v[212:215], v[60:63], v[24:27]\n\t"
        "ds_read_b64_tr_b16 v[246:247], v103 offset:20992\n\t"
        "ds_read_b64_tr_b16 v[248:249], v103 offset:20488\n\t"
        "v_mfma_f32_16x16x32_bf16 v[28:31], v[60:63], v[216:219], v[28:31]\n\t"
        "ds_read_b64_tr_b16 v[250:251], v103 offset:21000\n\t"
        "ds_read_b64_tr_b16 v[252:253], v104 offset:18432\n\t"
        "v_mfma_f32_16x16x32_bf16 v[8:11], v[220:223], v[64:67], v[8:11]\n\t"
        "ds_read_b64_tr_b16 v[254:255], v104 offset:18688\n\t"
        "ds_read_b128 v[72:75], v107 offset:57344\n\t"
        "v_mfma_f32_16x16x32_bf16 v[12:15], v[224:227], v[64:67], v[12:15]\n\t"
        "ds_read_b128 v[76:79], v107 offset:59392\n\t"
        "ds_read_b128 v[80:83], v108 offset:57344\n\t"
        "v_mfma_f32_16x16x32_bf16 v[16:19], v[64:67], v[228:231], v[16:19]\n\t"
        "ds_read_b128 v[84:87], v108 offset:59392\n\t"
        "v_mfma_f32_16x16x32_bf16 v[20:23], v[220:223], v[68:71], v[20:23]\n\t"
        "v_mfma_f32_16x16x32_bf16 v[24:27], v[224:227], v[68:71], v[24:27]\n\t"
        "v_mfma_f32_16x16x32_bf16 v[28:31], v[68:71], v[228:231], v[28:31]\n\t"
        "s_waitcnt vmcnt(0) lgkmcnt(0)\n\t"
        "s_barrier\n\t"
        "v_mfma_f32_16x16x32_bf16 v[8:11], v[232:235], v[72:75], v[8:11]\n\t"
        "v_mfma_f32_16x16x32_bf16 v[12:15], v[236:239], v[72:75], v[12:15]\n\t"
        "v_mfma_f32_16x16x32_bf16 v[16:19], v[72:75], v[240:243], v[16:19]\n\t"
        "v_mfma_f32_16x16x32_bf16 v[20:23], v[232:235], v[76:79], v[20:23]\n\t"
        "v_mfma_f32_16x16x32_bf16 v[24:27], v[236:239], v[76:79], v[24:27]\n\t"
        "v_mfma_f32_16x16x32_bf16 v[28:31], v[76:79], v[240:243], v[28:31]\n\t"
        "v_mfma_f32_16x16x32_bf16 v[8:11], v[244:247], v[80:83], v[8:11]\n\t"
        "v_mfma_f32_16x16x32_bf16 v[12:15], v[248:251], v[80:83], v[12:15]\n\t"
        "v_mfma_f32_16x16x32_bf16 v[16:19], v[80:83], v[252:255], v[16:19]\n\t"
        "v_mfma_f32_16x16x32_bf16 v[20:23], v[244:247], v[84:87], v[20:23]\n\t"
        "v_mfma_f32_16x16x32_bf16 v[24:27], v[248:251], v[84:87], v[24:27]\n\t"
        "v_mfma_f32_16x16x32_bf16 v[28:31], v[84:87], v[252:255], v[28:31]\n\t"
        "s_mov_b32 %[xo], 0x20000\n\t"
        "ds_read_b128 v[56:59], v105 offset:0\n\t"
        "ds_read_b128 v[60:63], v105 offset:2048\n\t"
        "ds_read_b128 v[64:67], v106 offset:0\n\t"
        "ds_read_b128 v[68:71], v106 offset:2048\n\t"
        "ds_read_b128 v[72:75], v105 offset:8192\n\t"
        "ds_read_b128 v[76:79], v105 offset:10240\n\t"
        "ds_read_b128 v[80:83], v106 offset:8192\n\t"
        "v_cvt_pk_bf16_f32 v88, v8, v9\n\t"
        "v_cvt_pk_bf16_f32 v89, v10, v11\n\t"
        "v_cvt_pk_bf16_f32 v90, v12, v13\n\t"
        "v_cvt_pk_bf16_f32 v91, v14, v15\n\t"
        "buffer_store_dwordx4 v[88:91], v109, %[sw], %[so] offen sc1\n\t"
        "v_cvt_pk_bf16_f32 v92, v20, v21\n\t"
        "v_cvt_pk_bf16_f32 v93, v22, v23\n\t"
        "v_cvt_pk_bf16_f32 v94, v24, v25\n\t"
        "v_cvt_pk_bf16_f32 v95, v26, v27\n\t"
        "buffer_store_dwordx4 v[92:95], v110, %[sw], %[so] offen sc1\n\t"
        "v_cvt_pk_bf16_f32 v96, v16, v17\n\t"
        "v_cvt_pk_bf16_f32 v97, v18, v19\n\t"
        "v_cvt_pk_bf16_f32 v98, v28, v29\n\t"
        "v_cvt_pk_bf16_f32 v99, v30, v31\n\t"
        "buffer_store_dwordx4 v[96:99], v111, %[sw], %[so] offen sc1\n\t"
        "s_mov_b32 %[so], 0xf0000000\n\t"
        "s_mov_b32 %[xo], 33792\n\t"
        "s_mov_b32 %[cn], 0\n\t"
        "LG_cal%=:\n\t"
        "buffer_load_dword v118, off, %[sw], %[xo] sc1\n\t"
        "s_waitcnt vmcnt(0)\n\t"
        "v_readfirstlane_b32 %[t0], v118\n\t"
        "s_cmp_lg_u32 %[t0], 0\n\t"
        "s_cbranch_scc1 LG_calok%=\n\t"
        "s_sleep 4\n\t"
        "s_add_u32 %[cn], %[cn], 1\n\t"
        "s_cmp_lt_u32 %[cn], 0x100000\n\t"
        "s_cbranch_scc1 LG_cal%=\n\t"
        "s_endpgm\n\t"
        "LG_calok%=:\n\t"
        "s_mov_b32 %[xo], 0x20000\n\t"
        "s_mov_b32 %[cn], 1\n\t"
        "v_mov_b32_e32 v117, %[cn]\n\t"
        "buffer_store_dword v117, v113, %[sw], 0 offen sc1\n\t"
        "s_add_u32 m0, %[ld], 32768\n\t"
        "s_add_u32 %[t0], %[xo], 512\n\t"
        "buffer_load_dwordx4 v100, %[sx], %[t0] offen lds\n\t"
        "s_add_u32 m0, %[ld], 33792\n\t"
        "s_add_u32 %[t0], %[xo], 16896\n\t"
        "buffer_load_dwordx4 v100, %[sx], %[t0] offen lds\n\t"
        "s_add_u32 m0, %[ld], 40960\n\t"
        "s_add_u32 %[t0], %[xo], 640\n\t"
        "buffer_load_dwordx4 v100, %[sx], %[t0] offen lds\n\t"
        "s_add_u32 m0, %[ld], 41984\n\t"
        "s_add_u32 %[t0], %[xo], 17024\n\t"
        "buffer_load_dwordx4 v100, %[sx], %[t0] offen lds\n\t"
        "s_add_u32 m0, %[ld], 49152\n\t"
        "s_add_u32 %[t0], %[xo], 768\n\t"
        "buffer_load_dwordx4 v100, %[sx], %[t0] offen lds\n\t"
        "s_add_u32 m0, %[ld], 50176\n\t"
        "s_add_u32 %[t0], %[xo], 17152\n\t"
        "buffer_load_dwordx4 v100, %[sx], %[t0] offen lds\n\t"
        "s_add_u32 m0, %[ld], 57344\n\t"
        "s_add_u32 %[t0], %[xo], 896\n\t"
        "buffer_load_dwordx4 v100, %[sx], %[t0] offen lds\n\t"
        "s_add_u32 m0, %[ld], 58368\n\t"
        "s_add_u32 %[t0], %[xo], 17280\n\t"
        "buffer_load_dwordx4 v100, %[sx], %[t0] offen lds\n\t"
        "s_add_u32 m0, %[ld], 65536\n\t"
        "s_add_u32 %[t0], %[xo], 1024\n\t"
        "buffer_load_dwordx4 v100, %[sx], %[t0] offen lds\n\t"
        "s_add_u32 m0, %[ld], 66560\n\t"
        "s_add_u32 %[t0], %[xo], 17408\n\t"
        "buffer_load_dwordx4 v100, %[sx], %[t0] offen lds\n\t"
        "s_add_u32 m0, %[ld], 73728\n\t"
        "s_add_u32 %[t0], %[xo], 1152\n\t"
        "buffer_load_dwordx4 v100, %[sx], %[t0] offen lds\n\t"
        "s_add_u32 m0, %[ld], 74752\n\t"
        "s_add_u32 %[t0], %[xo], 17536\n\t"
        "buffer_load_dwordx4 v100, %[sx], %[t0] offen lds\n\t"
        "s_add_u32 m0, %[ld], 81920\n\t"
        "s_add_u32 %[t0], %[xo], 1280\n\t"
        "buffer_load_dwordx4 v100, %[sx], %[t0] offen lds\n\t"
        "s_add_u32 m0, %[ld], 82944\n\t"
        "s_add_u32 %[t0], %[xo], 17664\n\t"
        "buffer_load_dwordx4 v100, %[sx], %[t0] offen lds\n\t"
        "s_add_u32 m0, %[ld], 90112\n\t"
        "s_add_u32 %[t0], %[xo], 1408\n\t"
        "buffer_load_dwordx4 v100, %[sx], %[t0] offen lds\n\t"
        "s_add_u32 m0, %[ld], 91136\n\t"
        "s_add_u32 %[t0], %[xo], 17792\n\t"
        "buffer_load_dwordx4 v100, %[sx], %[t0] offen lds\n\t"
        "s_add_u32 m0, %[ld], 98304\n\t"
        "s_add_u32 %[t0], %[xo], 1536\n\t"
        "buffer_load_dwordx4 v100, %[sx], %[t0] offen lds\n\t"
        "s_add_u32 m0, %[ld], 99328\n\t"
        "s_add_u32 %[t0], %[xo], 17920\n\t"
        "buffer_load_dwordx4 v100, %[sx], %[t0] offen lds\n\t"
        "s_add_u32 m0, %[ld], 106496\n\t"
        "s_add_u32 %[t0], %[xo], 1664\n\t"
        "buffer_load_dwordx4 v100, %[sx], %[t0] offen lds\n\t"
        "s_branch LG_mid%=\n\t"
        "LG_loop%=:\n\t"
        "s_waitcnt lgkmcnt(6)\n\t"
        "v_mfma_f32_16x16x32_bf16 v[8:11], a[0:3], v[56:59], 0\n\t"
        "v_mfma_f32_16x16x32_bf16 v[12:15], a[4:7], v[56:59], 0\n\t"
        "ds_read_b128 v[84:87], v106 offset:10240\n\t"
        "v_mfma_f32_16x16x32_bf16 v[16:19], v[56:59], a[8:11], 0\n\t"
        "s_add_u32 m0, %[ld], 107520\n\t"
        "s_add_u32 %[t0], %[xo], 18048\n\t"
        "s_waitcnt lgkmcnt(6)\n\t"
        "v_mfma_f32_16x16x32_bf16 v[20:23], a[0:3], v[60:63], 0\n\t"
        "buffer_load_dwordx4 v100, %[sx], %[t0] offen lds\n\t"
        "v_mfma_f32_16x16x32_bf16 v[24:27], a[4:7], v[60:63], 0\n\t"
        "ds_read_b128 v[56:59], v105 offset:16384\n\t"
        "v_mfma_f32_16x16x32_bf16 v[28:31], v[60:63], a[8:11], 0\n\t"
        "s_waitcnt lgkmcnt(6)\n\t"
        "v_mfma_f32_16x16x32_bf16 v[8:11], a[12:15], v[64:67], v[8:11]\n\t"
        "v_cvt_pk_bf16_f32 v88, v32, v33\n\t"
        "v_cvt_pk_bf16_f32 v89, v34, v35\n\t"
        "v_mfma_f32_16x16x32_bf16 v[12:15], a[16:19], v[64:67], v[12:15]\n\t"
        "ds_read_b128 v[60:63], v105 offset:18432\n\t"
        "v_cvt_pk_bf16_f32 v90, v36, v37\n\t"
        "v_mfma_f32_16x16x32_bf16 v[16:19], v[64:67], a[20:23], v[16:19]\n\t"
        "s_add_u32 m0, %[ld], 114688\n\t"
        "s_add_u32 %[t0], %[xo], 1792\n\t"
        "s_waitcnt lgkmcnt(6)\n\t"
        "v_mfma_f32_16x16x32_bf16 v[20:23], a[12:15], v[68:71], v[20:23]\n\t"
        "buffer_load_dwordx4 v100, %[sx], %[t0] offen lds\n\t"
        "v_mfma_f32_16x16x32_bf16 v[24:27], a[16:19], v[68:71], v[24:27]\n\t"
        "ds_read_b128 v[64:67], v106 offset:16384\n\t"
        "v_cvt_pk_bf16_f32 v91, v38, v39\n\t"
        "v_mfma_f32_16x16x32_bf16 v[28:31], v[68:71], a[20:23], v[28:31]\n\t"
        "buffer_store_dwordx4 v[88:91], v109, %[sw], %[so] offen sc1\n\t"
        "s_waitcnt lgkmcnt(6)\n\t"
        "v_mfma_f32_16x16x32_bf16 v[8:11], a[24:27], v[72:75], v[8:11]\n\t"
        "v_cvt_pk_bf16_f32 v92, v44, v45\n\t"
        "v_cvt_pk_bf16_f32 v93, v46, v47\n\t"
        "v_mfma_f32_16x16x32_bf16 v[12:15], a[28:31], v[72:75], v[12:15]\n\t"
        "ds_read_b128 v[68:71], v106 offset:18432\n\t"
        "v_cvt_pk_bf16_f32 v94, v48, v49\n\t"
        "v_mfma_f32_16x16x32_bf16 v[16:19], v[72:75], a[32:35], v[16:19]\n\t"
        "s_add_u32 m0, %[ld], 115712\n\t"
        "s_add_u32 %[t0], %[xo], 18176\n\t"
        "s_waitcnt lgkmcnt(6)\n\t"
        "v_mfma_f32_16x16x32_bf16 v[20:23], a[24:27], v[76:79], v[20:23]\n\t"
        "buffer_load_dwordx4 v100, %[sx], %[t0] offen lds\n\t"
        "v_mfma_f32_16x16x32_bf16 v[24:27], a[28:31], v[76:79], v[24:27]\n\t"
        "ds_read_b128 v[72:75], v105 offset:24576\n\t"
        "v_cvt_pk_bf16_f32 v95, v50, v51\n\t"
        "v_mfma_f32_16x16x32_bf16 v[28:31], v[76:79], a[32:35], v[28:31]\n\t"
        "buffer_store_dwordx4 v[92:95], v110, %[sw], %[so] offen sc1\n\t"
        "s_waitcnt lgkmcnt(6)\n\t"
        "v_mfma_f32_16x16x32_bf16 v[8:11], a[36:39], v[80:83], v[8:11]\n\t"
        "v_cvt_pk_bf16_f32 v96, v40, v41\n\t"
        "v_cvt_pk_bf16_f32 v97, v42, v43\n\t"
        "v_mfma_f32_16x16x32_bf16 v[12:15], a[40:43], v[80:83], v[12:15]\n\t"
        "ds_read_b128 v[76:79], v105 offset:26624\n\t"
        "v_cvt_pk_bf16_f32 v98, v52, v53\n\t"
        "v_mfma_f32_16x16x32_bf16 v[16:19], v[80:83], a[44:47], v[16:19]\n\t"
        "s_add_u32 m0, %[ld], 122880\n\t"
        "s_add_u32 %[t0], %[xo], 1920\n\t"
        "s_waitcnt lgkmcnt(6)\n\t"
        "v_mfma_f32_16x16x32_bf16 v[20:23], a[36:39], v[84:87], v[20:23]\n\t"
        "buffer_load_dwordx4 v100, %[sx], %[t0] offen lds\n\t"
        "v_mfma_f32_16x16x32_bf16 v[24:27], a[40:43], v[84:87], v[24:27]\n\t"
        "ds_read_b128 v[80:83], v106 offset:24576\n\t"
        "v_cvt_pk_bf16_f32 v99, v54, v55\n\t"
        "v_mfma_f32_16x16x32_bf16 v[28:31], v[84:87], a[44:47], v[28:31]\n\t"
        "buffer_store_dwordx4 v[96:99], v111, %[sw], %[so] offen sc1\n\t"
        "s_waitcnt lgkmcnt(6)\n\t"
        "v_mfma_f32_16x16x32_bf16 v[8:11], a[48:51], v[56:59], v[8:11]\n\t"
        "s_lshl_b32 %[so], %[cn], 19\n\t"
        "s_cmp_eq_u32 %[cn], 2\n\t"
        "s_cbranch_scc1 LG_first%=\n\t"
        "LG_back%=:\n\t"
        "v_mfma_f32_16x16x32_bf16 v[12:15], a[52:55], v[56:59], v[12:15]\n\t"
        "ds_read_b128 v[84:87], v106 offset:26624\n\t"
        "v_mfma_f32_16x16x32_bf16 v[16:19], v[56:59], a[56:59], v[16:19]\n\t"
        "s_add_u32 m0, %[ld], 123904\n\t"
        "s_add_u32 %[t0], %[xo], 18304\n\t"
        "s_waitcnt lgkmcnt(6)\n\t"
        "v_mfma_f32_16x16x32_bf16 v[20:23], a[48:51], v[60:63], v[20:23]\n\t"
        "buffer_load_dwordx4 v100, %[sx], %[t0] offen lds\n\t"
        "v_mfma_f32_16x16x32_bf16 v[24:27], a[52:55], v[60:63], v[24:27]\n\t"
        "v_mfma_f32_16x16x32_bf16 v[28:31], v[60:63], a[56:59], v[28:31]\n\t"
        "s_waitcnt lgkmcnt(5)\n\t"
        "v_mfma_f32_16x16x32_bf16 v[8:11], a[60:63], v[64:67], v[8:11]\n\t"
        "s_waitcnt vmcnt(19) lgkmcnt(0)\n\t"
        "s_barrier\n\t"
        "s_add_u32 %[xo], %[xo], 0x20000\n\t"
        "v_mfma_f32_16x16x32_bf16 v[12:15], a[64:67], v[64:67], v[12:15]\n\t"
        "ds_read_b128 v[56:59], v105 offset:32768\n\t"
        "v_mfma_f32_16x16x32_bf16 v[16:19], v[64:67], a[68:71], v[16:19]\n\t"
        "ds_read_b128 v[60:63], v105 offset:34816\n\t"
        "s_add_u32 m0, %[ld], 0\n\t"
        "s_add_u32 %[t0], %[xo], 0\n\t"
        "v_mfma_f32_16x16x32_bf16 v[20:23], a[60:63], v[68:71], v[20:23]\n\t"
        "buffer_load_dwordx4 v100, %[sx], %[t0] offen lds\n\t"
        "v_mfma_f32_16x16x32_bf16 v[24:27], a[64:67], v[68:71], v[24:27]\n\t"
        "ds_read_b128 v[64:67], v106 offset:32768\n\t"
        "v_mfma_f32_16x16x32_bf16 v[28:31], v[68:71], a[68:71], v[28:31]\n\t"
        "v_mfma_f32_16x16x32_bf16 v[8:11], a[72:75], v[72:75], v[8:11]\n\t"
        "v_mfma_f32_16x16x32_bf16 v[12:15], a[76:79], v[72:75], v[12:15]\n\t"
        "ds_read_b128 v[68:71], v106 offset:34816\n\t"
        "v_mfma_f32_16x16x32_bf16 v[16:19], v[72:75], a[80:83], v[16:19]\n\t"
        "s_add_u32 m0, %[ld], 1024\n\t"
        "s_add_u32 %[t0], %[xo], 16384\n\t"
        "v_mfma_f32_16x16x32_bf16 v[20:23], a[72:75], v[76:79], v[20:23]\n\t"
        "buffer_load_dwordx4 v100, %[sx], %[t0] offen lds\n\t"
        "v_mfma_f32_16x16x32_bf16 v[24:27], a[76:79], v[76:79], v[24:27]\n\t"
        "ds_read_b128 v[72:75], v105 offset:40960\n\t"
        "v_mfma_f32_16x16x32_bf16 v[28:31], v[76:79], a[80:83], v[28:31]\n\t"
        "v_mfma_f32_16x16x32_bf16 v[8:11], a[84:87], v[80:83], v[8:11]\n\t"
        "v_mfma_f32_16x16x32_bf16 v[12:15], a[88:91], v[80:83], v[12:15]\n\t"
        "ds_read_b128 v[76:79], v105 offset:43008\n\t"
        "v_mfma_f32_16x16x32_bf16 v[16:19], v[80:83], a[92:95], v[16:19]\n\t"
        "s_add_u32 m0, %[ld], 8192\n\t"
        "s_add_u32 %[t0], %[xo], 128\n\t"
        "v_mfma_f32_16x16x32_bf16 v[20:23], a[84:87], v[84:87], v[20:23]\n\t"
        "buffer_load_dwordx4 v100, %[sx], %[t0] offen lds\n\t"
        "v_mfma_f32_16x16x32_bf16 v[24:27], a[88:91], v[84:87], v[24:27]\n\t"
        "ds_read_b128 v[80:83], v106 offset:40960\n\t"
        "v_mfma_f32_16x16x32_bf16 v[28:31], v[84:87], a[92:95], v[28:31]\n\t"
        "s_waitcnt lgkmcnt(6)\n\t"
        "v_mfma_f32_16x16x32_bf16 v[8:11], a[96:99], v[56:59], v[8:11]\n\t"
        "v_mfma_f32_16x16x32_bf16 v[12:15], a[100:103], v[56:59], v[12:15]\n\t"
        "ds_read_b128 v[84:87], v106 offset:43008\n\t"
        "v_mfma_f32_16x16x32_bf16 v[16:19], v[56:59], a[104:107], v[16:19]\n\t"
        "s_add_u32 m0, %[ld], 9216\n\t"
        "s_add_u32 %[t0], %[xo], 16512\n\t"
        "s_waitcnt lgkmcnt(6)\n\t"
        "v_mfma_f32_16x16x32_bf16 v[20:23], a[96:99], v[60:63], v[20:23]\n\t"
        "buffer_load_dwordx4 v100, %[sx], %[t0] offen lds\n\t"
        "v_mfma_f32_16x16x32_bf16 v[24:27], a[100:103], v[60:63], v[24:27]\n\t"
        "ds_read_b128 v[56:59], v105 offset:49152\n\t"
        "v_mfma_f32_16x16x32_bf16 v[28:31], v[60:63], a[104:107], v[28:31]\n\t"
        "s_waitcnt lgkmcnt(6)\n\t"
        "v_mfma_f32_16x16x32_bf16 v[8:11], a[108:111], v[64:67], v[8:11]\n\t"
        "v_mfma_f32_16x16x32_bf16 v[12:15], a[112:115], v[64:67], v[12:15]\n\t"
        "ds_read_b128 v[60:63], v105 offset:51200\n\t"
        "v_mfma_f32_16x16x32_bf16 v[16:19], v[64:67], a[116:119], v[16:19]\n\t"
        "s_add_u32 m0, %[ld], 16384\n\t"
        "s_add_u32 %[t0], %[xo], 256\n\t"
        "s_waitcnt lgkmcnt(6)\n\t"
        "v_mfma_f32_16x16x32_bf16 v[20:23], a[108:111], v[68:71], v[20:23]\n\t"
        "buffer_load_dwordx4 v100, %[sx], %[t0] offen lds\n\t"
        "v_mfma_f32_16x16x32_bf16 v[24:27], a[112:115], v[68:71], v[24:27]\n\t"
        "ds_read_b128 v[64:67], v106 offset:49152\n\t"
        "v_mfma_f32_16x16x32_bf16 v[28:31], v[68:71], a[116:119], v[28:31]\n\t"
        "s_waitcnt lgkmcnt(6)\n\t"
        "v_mfma_f32_16x16x32_bf16 v[8:11], a[120:123], v[72:75], v[8:11]\n\t"
        "v_mfma_f32_16x16x32_bf16 v[12:15], a[124:127], v[72:75], v[12:15]\n\t"
        "ds_read_b128 v[68:71], v106 offset:51200\n\t"
        "v_mfma_f32_16x16x32_bf16 v[16:19], v[72:75], a[128:131], v[16:19]\n\t"
        "s_add_u32 m0, %[ld], 17408\n\t"
        "s_add_u32 %[t0], %[xo], 16640\n\t"
        "s_waitcnt lgkmcnt(6)\n\t"
        "v_mfma_f32_16x16x32_bf16 v[20:23], a[120:123], v[76:79], v[20:23]\n\t"
        "buffer_load_dwordx4 v100, %[sx], %[t0] offen lds\n\t"
        "v_mfma_f32_16x16x32_bf16 v[24:27], a[124:127], v[76:79], v[24:27]\n\t"
        "ds_read_b128 v[72:75], v105 offset:57344\n\t"
        "v_mfma_f32_16x16x32_bf16 v[28:31], v[76:79], a[128:131], v[28:31]\n\t"
        "s_waitcnt lgkmcnt(6)\n\t"
        "v_mfma_f32_16x16x32_bf16 v[8:11], a[132:135], v[80:83], v[8:11]\n\t"
        "v_mfma_f32_16x16x32_bf16 v[12:15], a[136:139], v[80:83], v[12:15]\n\t"
        "ds_read_b128 v[76:79], v105 offset:59392\n\t"
        "v_mfma_f32_16x16x32_bf16 v[16:19], v[80:83], a[140:143], v[16:19]\n\t"
        "s_add_u32 m0, %[ld], 24576\n\t"
        "s_add_u32 %[t0], %[xo], 384\n\t"
        "s_waitcnt lgkmcnt(6)\n\t"
        "v_mfma_f32_16x16x32_bf16 v[20:23], a[132:135], v[84:87], v[20:23]\n\t"
        "buffer_load_dwordx4 v100, %[sx], %[t0] offen lds\n\t"
        "v_mfma_f32_16x16x32_bf16 v[24:27], a[136:139], v[84:87], v[24:27]\n\t"
        "ds_read_b128 v[80:83], v106 offset:57344\n\t"
        "v_mfma_f32_16x16x32_bf16 v[28:31], v[84:87], a[140:143], v[28:31]\n\t"
        "s_waitcnt lgkmcnt(6)\n\t"
        "v_mfma_f32_16x16x32_bf16 v[8:11], a[144:147], v[56:59], v[8:11]\n\t"
        "v_mfma_f32_16x16x32_bf16 v[12:15], a[148:151], v[56:59], v[12:15]\n\t"
        "ds_read_b128 v[84:87], v106 offset:59392\n\t"
        "v_mfma_f32_16x16x32_bf16 v[16:19], v[56:59], a[152:155], v[16:19]\n\t"
        "s_add_u32 m0, %[ld], 25600\n\t"
        "s_add_u32 %[t0], %[xo], 16768\n\t"
        "s_waitcnt lgkmcnt(6)\n\t"
        "v_mfma_f32_16x16x32_bf16 v[20:23], a[144:147], v[60:63], v[20:23]\n\t"
        "buffer_load_dwordx4 v100, %[sx], %[t0] offen lds\n\t"
        "v_mfma_f32_16x16x32_bf16 v[24:27], a[148:151], v[60:63], v[24:27]\n\t"
        "v_mfma_f32_16x16x32_bf16 v[28:31], v[60:63], a[152:155], v[28:31]\n\t"
        "s_waitcnt lgkmcnt(5)\n\t"
        "v_mfma_f32_16x16x32_bf16 v[8:11], a[156:159], v[64:67], v[8:11]\n\t"
        "s_waitcnt vmcnt(9) lgkmcnt(0)\n\t"
        "s_barrier\n\t"
        "v_mfma_f32_16x16x32_bf16 v[12:15], a[160:163], v[64:67], v[12:15]\n\t"
        "ds_read_b128 v[56:59], v107 offset:0\n\t"
        "v_mfma_f32_16x16x32_bf16 v[16:19], v[64:67], a[164:167], v[16:19]\n\t"
        "ds_read_b128 v[60:63], v107 offset:2048\n\t"
        "s_add_u32 m0, %[ld], 32768\n\t"
        "s_add_u32 %[t0], %[xo], 512\n\t"
        "v_mfma_f32_16x16x32_bf16 v[20:23], a[156:159], v[68:71], v[20:23]\n\t"
        "buffer_load_dwordx4 v100, %[sx], %[t0] offen lds\n\t"
        "v_mfma_f32_16x16x32_bf16 v[24:27], a[160:163], v[68:71], v[24:27]\n\t"
        "ds_read_b128 v[64:67], v108 offset:0\n\t"
        "v_mfma_f32_16x16x32_bf16 v[28:31], v[68:71], a[164:167], v[28:31]\n\t"
        "v_mfma_f32_16x16x32_bf16 v[8:11], a[168:171], v[72:75], v[8:11]\n\t"
        "v_mfma_f32_16x16x32_bf16 v[12:15], a[172:175], v[72:75], v[12:15]\n\t"
        "ds_read_b128 v[68:71], v108 offset:2048\n\t"
        "v_mov_b32_e32 v117, %[cn]\n\t"
        "buffer_store_dword v117, v113, %[sw], 0 offen sc1\n\t"
        "s_add_u32 %[cn], %[cn], 1\n\t"
        "v_mfma_f32_16x16x32_bf16 v[16:19], v[72:75], a[176:179], v[16:19]\n\t"
        "s_add_u32 m0, %[ld], 33792\n\t"
        "s_add_u32 %[t0], %[xo], 16896\n\t"
        "v_mfma_f32_16x16x32_bf16 v[20:23], a[168:171], v[76:79], v[20:23]\n\t"
        "buffer_load_dwordx4 v100, %[sx], %[t0] offen lds\n\t"
        "v_mfma_f32_16x16x32_bf16 v[24:27], a[172:175], v[76:79], v[24:27]\n\t"
        "ds_read_b128 v[72:75], v107 offset:8192\n\t"
        "v_mfma_f32_16x16x32_bf16 v[28:31], v[76:79], a[176:179], v[28:31]\n\t"
        "v_mfma_f32_16x16x32_bf16 v[8:11], a[180:183], v[80:83], v[8:11]\n\t"
        "v_mfma_f32_16x16x32_bf16 v[12:15], a[184:187], v[80:83], v[12:15]\n\t"
        "ds_read_b128 v[76:79], v107 offset:10240\n\t"
        "v_mfma_f32_16x16x32_bf16 v[16:19], v[80:83], a[188:191], v[16:19]\n\t"
        "s_add_u32 m0, %[ld], 40960\n\t"
        "s_add_u32 %[t0], %[xo], 640\n\t"
        "v_mfma_f32_16x16x32_bf16 v[20:23], a[180:183], v[84:87], v[20:23]\n\t"
        "buffer_load_dwordx4 v100, %[sx], %[t0] offen lds\n\t"
        "v_mfma_f32_16x16x32_bf16 v[24:27], a[184:187], v[84:87], v[24:27]\n\t"
        "ds_read_b128 v[80:83], v108 offset:8192\n\t"
        "v_mfma_f32_16x16x32_bf16 v[28:31], v[84:87], a[188:191], v[28:31]\n\t"
        "s_waitcnt lgkmcnt(6)\n\t"
        "v_mfma_f32_16x16x32_bf16 v[8:11], a[192:195], v[56:59], v[8:11]\n\t"
        "v_mfma_f32_16x16x32_bf16 v[12:15], a[196:199], v[56:59], v[12:15]\n\t"
        "ds_read_b128 v[84:87], v108 offset:10240\n\t"
        "v_mfma_f32_16x16x32_bf16 v[16:19], v[56:59], a[200:203], v[16:19]\n\t"
        "s_add_u32 m0, %[ld], 41984\n\t"
        "s_add_u32 %[t0], %[xo], 17024\n\t"
        "s_waitcnt lgkmcnt(6)\n\t"
        "v_mfma_f32_16x16x32_bf16 v[20:23], a[192:195], v[60:63], v[20:23]\n\t"
        "buffer_load_dwordx4 v100, %[sx], %[t0] offen lds\n\t"
        "v_mfma_f32_16x16x32_bf16 v[24:27], a[196:199], v[60:63], v[24:27]\n\t"
        "ds_read_b128 v[56:59], v107 offset:16384\n\t"
        "v_mfma_f32_16x16x32_bf16 v[28:31], v[60:63], a[200:203], v[28:31]\n\t"
        "s_waitcnt lgkmcnt(6)\n\t"
        "v_mfma_f32_16x16x32_bf16 v[8:11], a[204:207], v[64:67], v[8:11]\n\t"
        "v_mfma_f32_16x16x32_bf16 v[12:15], a[208:211], v[64:67], v[12:15]\n\t"
        "ds_read_b128 v[60:63], v107 offset:18432\n\t"
        "v_mfma_f32_16x16x32_bf16 v[16:19], v[64:67], a[212:215], v[16:19]\n\t"
        "s_add_u32 m0, %[ld], 49152\n\t"
        "s_add_u32 %[t0], %[xo], 768\n\t"
        "s_waitcnt lgkmcnt(6)\n\t"
        "v_mfma_f32_16x16x32_bf16 v[20:23], a[204:207], v[68:71], v[20:23]\n\t"
        "buffer_load_dwordx4 v100, %[sx], %[t0] offen lds\n\t"
        "v_mfma_f32_16x16x32_bf16 v[24:27], a[208:211], v[68:71], v[24:27]\n\t"
        "ds_read_b128 v[64:67], v108 offset:16384\n\t"
        "v_mfma_f32_16x16x32_bf16 v[28:31], v[68:71], a[212:215], v[28:31]\n\t"
        "s_waitcnt lgkmcnt(6)\n\t"
        "v_mfma_f32_16x16x32_bf16 v[8:11], a[216:219], v[72:75], v[8:11]\n\t"
        "v_mfma_f32_16x16x32_bf16 v[12:15], a[220:223], v[72:75], v[12:15]\n\t"
        "ds_read_b128 v[68:71], v108 offset:18432\n\t"
        "v_mfma_f32_16x16x32_bf16 v[16:19], v[72:75], a[224:227], v[16:19]\n\t"
        "s_add_u32 m0, %[ld], 50176\n\t"
        "s_add_u32 %[t0], %[xo], 17152\n\t"
        "s_waitcnt lgkmcnt(6)\n\t"
        "v_mfma_f32_16x16x32_bf16 v[20:23], a[216:219], v[76:79], v[20:23]\n\t"
        "buffer_load_dwordx4 v100, %[sx], %[t0] offen lds\n\t"
        "v_mfma_f32_16x16x32_bf16 v[24:27], a[220:223], v[76:79], v[24:27]\n\t"
        "ds_read_b128 v[72:75], v107 offset:24576\n\t"
        "v_mfma_f32_16x16x32_bf16 v[28:31], v[76:79], a[224:227], v[28:31]\n\t"
        "s_waitcnt lgkmcnt(6)\n\t"
        "v_mfma_f32_16x16x32_bf16 v[8:11], a[228:231], v[80:83], v[8:11]\n\t"
        "v_mfma_f32_16x16x32_bf16 v[12:15], a[232:235], v[80:83], v[12:15]\n\t"
        "ds_read_b128 v[76:79], v107 offset:26624\n\t"
        "v_mfma_f32_16x16x32_bf16 v[16:19], v[80:83], a[236:239], v[16:19]\n\t"
        "s_add_u32 m0, %[ld], 57344\n\t"
        "s_add_u32 %[t0], %[xo], 896\n\t"
        "s_waitcnt lgkmcnt(6)\n\t"
        "v_mfma_f32_16x16x32_bf16 v[20:23], a[228:231], v[84:87], v[20:23]\n\t"
        "buffer_load_dwordx4 v100, %[sx], %[t0] offen lds\n\t"
        "v_mfma_f32_16x16x32_bf16 v[24:27], a[232:235], v[84:87], v[24:27]\n\t"
        "ds_read_b128 v[80:83], v108 offset:24576\n\t"
        "v_mfma_f32_16x16x32_bf16 v[28:31], v[84:87], a[236:239], v[28:31]\n\t"
        "s_waitcnt lgkmcnt(6)\n\t"
        "v_mfma_f32_16x16x32_bf16 v[8:11], a[240:243], v[56:59], v[8:11]\n\t"
        "v_mfma_f32_16x16x32_bf16 v[12:15], a[244:247], v[56:59], v[12:15]\n\t"
        "ds_read_b128 v[84:87], v108 offset:26624\n\t"
        "v_mfma_f32_16x16x32_bf16 v[16:19], v[56:59], a[248:251], v[16:19]\n\t"
        "s_add_u32 m0, %[ld], 58368\n\t"
        "s_add_u32 %[t0], %[xo], 17280\n\t"
        "s_waitcnt lgkmcnt(6)\n\t"
        "v_mfma_f32_16x16x32_bf16 v[20:23], a[240:243], v[60:63], v[20:23]\n\t"
        "buffer_load_dwordx4 v100, %[sx], %[t0] offen lds\n\t"
        "v_mfma_f32_16x16x32_bf16 v[24:27], a[244:247], v[60:63], v[24:27]\n\t"
        "v_mfma_f32_16x16x32_bf16 v[28:31], v[60:63], a[248:251], v[28:31]\n\t"
        "s_waitcnt lgkmcnt(5)\n\t"
        "v_mfma_f32_16x16x32_bf16 v[8:11], a[252:255], v[64:67], v[8:11]\n\t"
        "s_waitcnt vmcnt(17) lgkmcnt(0)\n\t"
        "s_barrier\n\t"
        "v_mfma_f32_16x16x32_bf16 v[12:15], v[128:131], v[64:67], v[12:15]\n\t"
        "ds_read_b128 v[56:59], v107 offset:32768\n\t"
        "v_mfma_f32_16x16x32_bf16 v[16:19], v[64:67], v[132:135], v[16:19]\n\t"
        "ds_read_b128 v[60:63], v107 offset:34816\n\t"
        "s_add_u32 m0, %[ld], 65536\n\t"
        "s_add_u32 %[t0], %[xo], 1024\n\t"
        "v_mfma_f32_16x16x32_bf16 v[20:23], a[252:255], v[68:71], v[20:23]\n\t"
        "buffer_load_dwordx4 v100, %[sx], %[t0] offen lds\n\t"
        "v_mfma_f32_16x16x32_bf16 v[24:27], v[128:131], v[68:71], v[24:27]\n\t"
        "ds_read_b128 v[64:67], v108 offset:32768\n\t"
        "v_mfma_f32_16x16x32_bf16 v[28:31], v[68:71], v[132:135], v[28:31]\n\t"
        "v_mfma_f32_16x16x32_bf16 v[8:11], v[136:139], v[72:75], v[8:11]\n\t"
        "v_mfma_f32_16x16x32_bf16 v[12:15], v[140:143], v[72:75], v[12:15]\n\t"
        "ds_read_b128 v[68:71], v108 offset:34816\n\t"
        "v_mfma_f32_16x16x32_bf16 v[16:19], v[72:75], v[144:147], v[16:19]\n\t"
        "s_add_u32 m0, %[ld], 66560\n\t"
        "s_add_u32 %[t0], %[xo], 17408\n\t"
        "v_mfma_f32_16x16x32_bf16 v[20:23], v[136:139], v[76:79], v[20:23]\n\t"
        "buffer_load_dwordx4 v100, %[sx], %[t0] offen lds\n\t"
        "v_mfma_f32_16x16x32_bf16 v[24:27], v[140:143], v[76:79], v[24:27]\n\t"
        "ds_read_b128 v[72:75], v107 offset:40960\n\t"
        "v_mfma_f32_16x16x32_bf16 v[28:31], v[76:79], v[144:147], v[28:31]\n\t"
        "v_mfma_f32_16x16x32_bf16 v[8:11], v[148:151], v[80:83], v[8:11]\n\t"
        "v_mfma_f32_16x16x32_bf16 v[12:15], v[152:155], v[80:83], v[12:15]\n\t"
        "ds_read_b128 v[76:79], v107 offset:43008\n\t"
        "v_mfma_f32_16x16x32_bf16 v[16:19], v[80:83], v[156:159], v[16:19]\n\t"
        "s_add_u32 m0, %[ld], 73728\n\t"
        "s_add_u32 %[t0], %[xo], 1152\n\t"
        "v_mfma_f32_16x16x32_bf16 v[20:23], v[148:151], v[84:87], v[20:23]\n\t"
        "buffer_load_dwordx4 v100, %[sx], %[t0] offen lds\n\t"
        "v_mfma_f32_16x16x32_bf16 v[24:27], v[152:155], v[84:87], v[24:27]\n\t"
        "ds_read_b128 v[80:83], v108 offset:40960\n\t"
        "v_mfma_f32_16x16x32_bf16 v[28:31], v[84:87], v[156:159], v[28:31]\n\t"
        "s_waitcnt lgkmcnt(6)\n\t"
        "v_mfma_f32_16x16x32_bf16 v[8:11], v[160:163], v[56:59], v[8:11]\n\t"
        "v_mfma_f32_16x16x32_bf16 v[12:15], v[164:167], v[56:59], v[12:15]\n\t"
        "ds_read_b128 v[84:87], v108 offset:43008\n\t"
        "v_mfma_f32_16x16x32_bf16 v[16:19], v[56:59], v[168:171], v[16:19]\n\t"
        "s_add_u32 m0, %[ld], 74752\n\t"
        "s_add_u32 %[t0], %[xo], 17536\n\t"
        "s_waitcnt lgkmcnt(6)\n\t"
        "v_mfma_f32_16x16x32_bf16 v[20:23], v[160:163], v[60:63], v[20:23]\n\t"
        "buffer_load_dwordx4 v100, %[sx], %[t0] offen lds\n\t"
        "v_mfma_f32_16x16x32_bf16 v[24:27], v[164:167], v[60:63], v[24:27]\n\t"
        "ds_read_b128 v[56:59], v107 offset:49152\n\t"
        "v_mfma_f32_16x16x32_bf16 v[28:31], v[60:63], v[168:171], v[28:31]\n\t"
        "s_waitcnt lgkmcnt(6)\n\t"
        "v_mfma_f32_16x16x32_bf16 v[8:11], v[172:175], v[64:67], v[8:11]\n\t"
        "v_mfma_f32_16x16x32_bf16 v[12:15], v[176:179], v[64:67], v[12:15]\n\t"
        "ds_read_b128 v[60:63], v107 offset:51200\n\t"
        "v_mfma_f32_16x16x32_bf16 v[16:19], v[64:67], v[180:183], v[16:19]\n\t"
        "s_add_u32 m0, %[ld], 81920\n\t"
        "s_add_u32 %[t0], %[xo], 1280\n\t"
        "s_waitcnt lgkmcnt(6)\n\t"
        "v_mfma_f32_16x16x32_bf16 v[20:23], v[172:175], v[68:71], v[20:23]\n\t"
        "buffer_load_dwordx4 v100, %[sx], %[t0] offen lds\n\t"
        "v_mfma_f32_16x16x32_bf16 v[24:27], v[176:179], v[68:71], v[24:27]\n\t"
        "ds_read_b128 v[64:67], v108 offset:49152\n\t"
        "v_mfma_f32_16x16x32_bf16 v[28:31], v[68:71], v[180:183], v[28:31]\n\t"
        "s_waitcnt lgkmcnt(6)\n\t"
        "v_mfma_f32_16x16x32_bf16 v[8:11], v[184:187], v[72:75], v[8:11]\n\t"
        "v_mfma_f32_16x16x32_bf16 v[12:15], v[188:191], v[72:75], v[12:15]\n\t"
        "ds_read_b128 v[68:71], v108 offset:51200\n\t"
        "v_mfma_f32_16x16x32_bf16 v[16:19], v[72:75], v[192:195], v[16:19]\n\t"
        "s_add_u32 m0, %[ld], 82944\n\t"
        "s_add_u32 %[t0], %[xo], 17664\n\t"
        "s_waitcnt lgkmcnt(6)\n\t"
        "v_mfma_f32_16x16x32_bf16 v[20:23], v[184:187], v[76:79], v[20:23]\n\t"
        "buffer_load_dwordx4 v100, %[sx], %[t0] offen lds\n\t"
        "v_mfma_f32_16x16x32_bf16 v[24:27], v[188:191], v[76:79], v[24:27]\n\t"
        "ds_read_b128 v[72:75], v107 offset:57344\n\t"
        "v_mfma_f32_16x16x32_bf16 v[28:31], v[76:79], v[192:195], v[28:31]\n\t"
        "s_waitcnt lgkmcnt(6)\n\t"
        "v_mfma_f32_16x16x32_bf16 v[8:11], v[196:199], v[80:83], v[8:11]\n\t"
        "v_mfma_f32_16x16x32_bf16 v[12:15], v[200:203], v[80:83], v[12:15]\n\t"
        "ds_read_b128 v[76:79], v107 offset:59392\n\t"
        "v_mfma_f32_16x16x32_bf16 v[16:19], v[80:83], v[204:207], v[16:19]\n\t"
        "s_add_u32 m0, %[ld], 90112\n\t"
        "s_add_u32 %[t0], %[xo], 1408\n\t"
        "s_waitcnt lgkmcnt(6)\n\t"
        "v_mfma_f32_16x16x32_bf16 v[20:23], v[196:199], v[84:87], v[20:23]\n\t"
        "buffer_load_dwordx4 v100, %[sx], %[t0] offen lds\n\t"
        "v_mfma_f32_16x16x32_bf16 v[24:27], v[200:203], v[84:87], v[24:27]\n\t"
        "ds_read_b128 v[80:83], v108 offset:57344\n\t"
        "v_mfma_f32_16x16x32_bf16 v[28:31], v[84:87], v[204:207], v[28:31]\n\t"
        "s_waitcnt lgkmcnt(6)\n\t"
        "v_mfma_f32_16x16x32_bf16 v[8:11], v[208:211], v[56:59], v[8:11]\n\t"
        "v_mfma_f32_16x16x32_bf16 v[12:15], v[212:215], v[56:59], v[12:15]\n\t"
        "ds_read_b128 v[84:87], v108 offset:59392\n\t"
        "v_mfma_f32_16x16x32_bf16 v[16:19], v[56:59], v[216:219], v[16:19]\n\t"
        "s_add_u32 m0, %[ld], 91136\n\t"
        "s_add_u32 %[t0], %[xo], 17792\n\t"
        "s_waitcnt lgkmcnt(6)\n\t"
        "v_mfma_f32_16x16x32_bf16 v[20:23], v[208:211], v[60:63], v[20:23]\n\t"
        "buffer_load_dwordx4 v100, %[sx], %[t0] offen lds\n\t"
        "v_mfma_f32_16x16x32_bf16 v[24:27], v[212:215], v[60:63], v[24:27]\n\t"
        "v_mfma_f32_16x16x32_bf16 v[28:31], v[60:63], v[216:219], v[28:31]\n\t"
        "s_waitcnt lgkmcnt(5)\n\t"
        "v_mfma_f32_16x16x32_bf16 v[8:11], v[220:223], v[64:67], v[8:11]\n\t"
        "s_waitcnt vmcnt(17) lgkmcnt(0)\n\t"
        "s_barrier\n\t"
        "v_mfma_f32_16x16x32_bf16 v[12:15], v[224:227], v[64:67], v[12:15]\n\t"
        "ds_read_b128 v[56:59], v105 offset:0\n\t"
        "v_mfma_f32_16x16x32_bf16 v[16:19], v[64:67], v[228:231], v[16:19]\n\t"
        "ds_read_b128 v[60:63], v105 offset:2048\n\t"
        "s_add_u32 m0, %[ld], 98304\n\t"
        "s_add_u32 %[t0], %[xo], 1536\n\t"
        "v_mfma_f32_16x16x32_bf16 v[20:23], v[220:223], v[68:71], v[20:23]\n\t"
        "buffer_load_dwordx4 v100, %[sx], %[t0] offen lds\n\t"
        "v_mfma_f32_16x16x32_bf16 v[24:27], v[224:227], v[68:71], v[24:27]\n\t"
        "ds_read_b128 v[64:67], v106 offset:0\n\t"
        "v_mfma_f32_16x16x32_bf16 v[28:31], v[68:71], v[228:231], v[28:31]\n\t"
        "v_mfma_f32_16x16x32_bf16 v[8:11], v[232:235], v[72:75], v[8:11]\n\t"
        "v_mfma_f32_16x16x32_bf16 v[12:15], v[236:239], v[72:75], v[12:15]\n\t"
        "ds_read_b128 v[68:71], v106 offset:2048\n\t"
        "v_mfma_f32_16x16x32_bf16 v[16:19], v[72:75], v[240:243], v[16:19]\n\t"
        "s_add_u32 m0, %[ld], 99328\n\t"
        "s_add_u32 %[t0], %[xo], 17920\n\t"
        "v_mfma_f32_16x16x32_bf16 v[20:23], v[232:235], v[76:79], v[20:23]\n\t"
        "buffer_load_dwordx4 v100, %[sx], %[t0] offen lds\n\t"
        "v_mfma_f32_16x16x32_bf16 v[24:27], v[236:239], v[76:79], v[24:27]\n\t"
        "ds_read_b128 v[72:75], v105 offset:8192\n\t"
        "v_mfma_f32_16x16x32_bf16 v[28:31], v[76:79], v[240:243], v[28:31]\n\t"
        "v_mfma_f32_16x16x32_bf16 v[8:11], v[244:247], v[80:83], v[8:11]\n\t"
        "v_mfma_f32_16x16x32_bf16 v[12:15], v[248:251], v[80:83], v[12:15]\n\t"
        "ds_read_b128 v[76:79], v105 offset:10240\n\t"
        "v_mfma_f32_16x16x32_bf16 v[16:19], v[80:83], v[252:255], v[16:19]\n\t"
        "s_add_u32 m0, %[ld], 106496\n\t"
        "s_add_u32 %[t0], %[xo], 1664\n\t"
        "v_mfma_f32_16x16x32_bf16 v[20:23], v[244:247], v[84:87], v[20:23]\n\t"
        "buffer_load_dwordx4 v100, %[sx], %[t0] offen lds\n\t"
        "v_mfma_f32_16x16x32_bf16 v[24:27], v[248:251], v[84:87], v[24:27]\n\t"
        "ds_read_b128 v[80:83], v106 offset:8192\n\t"
        "v_mfma_f32_16x16x32_bf16 v[28:31], v[84:87], v[252:255], v[28:31]\n\t"
        "LG_mid%=:\n\t"
        "s_waitcnt lgkmcnt(6)\n\t"
        "v_mfma_f32_16x16x32_bf16 v[32:35], a[0:3], v[56:59], 0\n\t"
        "v_mfma_f32_16x16x32_bf16 v[36:39], a[4:7], v[56:59], 0\n\t"
        "ds_read_b128 v[84:87], v106 offset:10240\n\t"
        "v_mfma_f32_16x16x32_bf16 v[40:43], v[56:59], a[8:11], 0\n\t"
        "s_add_u32 m0, %[ld], 107520\n\t"
        "s_add_u32 %[t0], %[xo], 18048\n\t"
        "s_waitcnt lgkmcnt(6)\n\t"
        "v_mfma_f32_16x16x32_bf16 v[44:47], a[0:3], v[60:63], 0\n\t"
        "buffer_load_dwordx4 v100, %[sx], %[t0] offen lds\n\t"
        "v_mfma_f32_16x16x32_bf16 v[48:51], a[4:7], v[60:63], 0\n\t"
        "ds_read_b128 v[56:59], v105 offset:16384\n\t"
        "v_mfma_f32_16x16x32_bf16 v[52:55], v[60:63], a[8:11], 0\n\t"
        "s_waitcnt lgkmcnt(6)\n\t"
        "v_mfma_f32_16x16x32_bf16 v[32:35], a[12:15], v[64:67], v[32:35]\n\t"
        "v_cvt_pk_bf16_f32 v88, v8, v9\n\t"
        "v_cvt_pk_bf16_f32 v89, v10, v11\n\t"
        "v_mfma_f32_16x16x32_bf16 v[36:39], a[16:19], v[64:67], v[36:39]\n\t"
        "ds_read_b128 v[60:63], v105 offset:18432\n\t"
        "v_cvt_pk_bf16_f32 v90, v12, v13\n\t"
        "v_mfma_f32_16x16x32_bf16 v[40:43], v[64:67], a[20:23], v[40:43]\n\t"
        "s_add_u32 m0, %[ld], 114688\n\t"
        "s_add_u32 %[t0], %[xo], 1792\n\t"
        "s_waitcnt lgkmcnt(6)\n\t"
        "v_mfma_f32_16x16x32_bf16 v[44:47], a[12:15], v[68:71], v[44:47]\n\t"
        "buffer_load_dwordx4 v100, %[sx], %[t0] offen lds\n\t"
        "v_mfma_f32_16x16x32_bf16 v[48:51], a[16:19], v[68:71], v[48:51]\n\t"
        "ds_read_b128 v[64:67], v106 offset:16384\n\t"
        "v_cvt_pk_bf16_f32 v91, v14, v15\n\t"
        "v_mfma_f32_16x16x32_bf16 v[52:55], v[68:71], a[20:23], v[52:55]\n\t"
        "buffer_store_dwordx4 v[88:91], v109, %[sw], %[so] offen sc1\n\t"
        "s_waitcnt lgkmcnt(6)\n\t"
        "v_mfma_f32_16x16x32_bf16 v[32:35], a[24:27], v[72:75], v[32:35]\n\t"
        "v_cvt_pk_bf16_f32 v92, v20, v21\n\t"
        "v_cvt_pk_bf16_f32 v93, v22, v23\n\t"
        "v_mfma_f32_16x16x32_bf16 v[36:39], a[28:31], v[72:75], v[36:39]\n\t"
        "ds_read_b128 v[68:71], v106 offset:18432\n\t"
        "v_cvt_pk_bf16_f32 v94, v24, v25\n\t"
        "v_mfma_f32_16x16x32_bf16 v[40:43], v[72:75], a[32:35], v[40:43]\n\t"
        "s_add_u32 m0, %[ld], 115712\n\t"
        "s_add_u32 %[t0], %[xo], 18176\n\t"
        "s_waitcnt lgkmcnt(6)\n\t"
        "v_mfma_f32_16x16x32_bf16 v[44:47], a[24:27], v[76:79], v[44:47]\n\t"
        "buffer_load_dwordx4 v100, %[sx], %[t0] offen lds\n\t"
        "v_mfma_f32_16x16x32_bf16 v[48:51], a[28:31], v[76:79], v[48:51]\n\t"
        "ds_read_b128 v[72:75], v105 offset:24576\n\t"
        "v_cvt_pk_bf16_f32 v95, v26, v27\n\t"
        "v_mfma_f32_16x16x32_bf16 v[52:55], v[76:79], a[32:35], v[52:55]\n\t"
        "buffer_store_dwordx4 v[92:95], v110, %[sw], %[so] offen sc1\n\t"
        "s_waitcnt lgkmcnt(6)\n\t"
        "v_mfma_f32_16x16x32_bf16 v[32:35], a[36:39], v[80:83], v[32:35]\n\t"
        "v_cvt_pk_bf16_f32 v96, v16, v17\n\t"
        "v_cvt_pk_bf16_f32 v97, v18, v19\n\t"
        "v_mfma_f32_16x16x32_bf16 v[36:39], a[40:43], v[80:83], v[36:39]\n\t"
        "ds_read_b128 v[76:79], v105 offset:26624\n\t"
        "v_cvt_pk_bf16_f32 v98, v28, v29\n\t"
        "v_mfma_f32_16x16x32_bf16 v[40:43], v[80:83], a[44:47], v[40:43]\n\t"
        "s_add_u32 m0, %[ld], 122880\n\t"
        "s_add_u32 %[t0], %[xo], 1920\n\t"
        "s_waitcnt lgkmcnt(6)\n\t"
        "v_mfma_f32_16x16x32_bf16 v[44:47], a[36:39], v[84:87], v[44:47]\n\t"
        "buffer_load_dwordx4 v100, %[sx], %[t0] offen lds\n\t"
        "v_mfma_f32_16x16x32_bf16 v[48:51], a[40:43], v[84:87], v[48:51]\n\t"
        "ds_read_b128 v[80:83], v106 offset:24576\n\t"
        "v_cvt_pk_bf16_f32 v99, v30, v31\n\t"
        "v_mfma_f32_16x16x32_bf16 v[52:55], v[84:87], a[44:47], v[52:55]\n\t"
        "buffer_store_dwordx4 v[96:99], v111, %[sw], %[so] offen sc1\n\t"
        "s_waitcnt lgkmcnt(6)\n\t"
        "v_mfma_f32_16x16x32_bf16 v[32:35], a[48:51], v[56:59], v[32:35]\n\t"
        "s_lshl_b32 %[so], %[cn], 19\n\t"
        "v_mfma_f32_16x16x32_bf16 v[36:39], a[52:55], v[56:59], v[36:39]\n\t"
        "ds_read_b128 v[84:87], v106 offset:26624\n\t"
        "v_mfma_f32_16x16x32_bf16 v[40:43], v[56:59], a[56:59], v[40:43]\n\t"
        "s_add_u32 m0, %[ld], 123904\n\t"
        "s_add_u32 %[t0], %[xo], 18304\n\t"
        "s_waitcnt lgkmcnt(6)\n\t"
        "v_mfma_f32_16x16x32_bf16 v[44:47], a[48:51], v[60:63], v[44:47]\n\t"
        "buffer_load_dwordx4 v100, %[sx], %[t0] offen lds\n\t"
        "v_mfma_f32_16x16x32_bf16 v[48:51], a[52:55], v[60:63], v[48:51]\n\t"
        "v_mfma_f32_16x16x32_bf16 v[52:55], v[60:63], a[56:59], v[52:55]\n\t"
        "s_waitcnt lgkmcnt(5)\n\t"
        "v_mfma_f32_16x16x32_bf16 v[32:35], a[60:63], v[64:67], v[32:35]\n\t"
        "s_waitcnt vmcnt(19) lgkmcnt(0)\n\t"
        "s_barrier\n\t"
        "s_add_u32 %[xo], %[xo], 0x20000\n\t"
        "v_mfma_f32_16x16x32_bf16 v[36:39], a[64:67], v[64:67], v[36:39]\n\t"
        "ds_read_b128 v[56:59], v105 offset:32768\n\t"
        "v_mfma_f32_16x16x32_bf16 v[40:43], v[64:67], a[68:71], v[40:43]\n\t"
        "ds_read_b128 v[60:63], v105 offset:34816\n\t"
        "s_add_u32 m0, %[ld], 0\n\t"
        "s_add_u32 %[t0], %[xo], 0\n\t"
        "v_mfma_f32_16x16x32_bf16 v[44:47], a[60:63], v[68:71], v[44:47]\n\t"
        "buffer_load_dwordx4 v100, %[sx], %[t0] offen lds\n\t"
        "v_mfma_f32_16x16x32_bf16 v[48:51], a[64:67], v[68:71], v[48:51]\n\t"
        "ds_read_b128 v[64:67], v106 offset:32768\n\t"
        "v_mfma_f32_16x16x32_bf16 v[52:55], v[68:71], a[68:71], v[52:55]\n\t"
        "v_mfma_f32_16x16x32_bf16 v[32:35], a[72:75], v[72:75], v[32:35]\n\t"
        "v_mfma_f32_16x16x32_bf16 v[36:39], a[76:79], v[72:75], v[36:39]\n\t"
        "ds_read_b128 v[68:71], v106 offset:34816\n\t"
        "v_mfma_f32_16x16x32_bf16 v[40:43], v[72:75], a[80:83], v[40:43]\n\t"
        "s_add_u32 m0, %[ld], 1024\n\t"
        "s_add_u32 %[t0], %[xo], 16384\n\t"
        "v_mfma_f32_16x16x32_bf16 v[44:47], a[72:75], v[76:79], v[44:47]\n\t"
        "buffer_load_dwordx4 v100, %[sx], %[t0] offen lds\n\t"
        "v_mfma_f32_16x16x32_bf16 v[48:51], a[76:79], v[76:79], v[48:51]\n\t"
        "ds_read_b128 v[72:75], v105 offset:40960\n\t"
        "v_mfma_f32_16x16x32_bf16 v[52:55], v[76:79], a[80:83], v[52:55]\n\t"
        "v_mfma_f32_16x16x32_bf16 v[32:35], a[84:87], v[80:83], v[32:35]\n\t"
        "v_mfma_f32_16x16x32_bf16 v[36:39], a[88:91], v[80:83], v[36:39]\n\t"
        "ds_read_b128 v[76:79], v105 offset:43008\n\t"
        "v_mfma_f32_16x16x32_bf16 v[40:43], v[80:83], a[92:95], v[40:43]\n\t"
        "s_add_u32 m0, %[ld], 8192\n\t"
        "s_add_u32 %[t0], %[xo], 128\n\t"
        "v_mfma_f32_16x16x32_bf16 v[44:47], a[84:87], v[84:87], v[44:47]\n\t"
        "buffer_load_dwordx4 v100, %[sx], %[t0] offen lds\n\t"
        "v_mfma_f32_16x16x32_bf16 v[48:51], a[88:91], v[84:87], v[48:51]\n\t"
        "ds_read_b128 v[80:83], v106 offset:40960\n\t"
        "v_mfma_f32_16x16x32_bf16 v[52:55], v[84:87], a[92:95], v[52:55]\n\t"
        "s_waitcnt lgkmcnt(6)\n\t"
        "v_mfma_f32_16x16x32_bf16 v[32:35], a[96:99], v[56:59], v[32:35]\n\t"
        "v_mfma_f32_16x16x32_bf16 v[36:39], a[100:103], v[56:59], v[36:39]\n\t"
        "ds_read_b128 v[84:87], v106 offset:43008\n\t"
        "v_mfma_f32_16x16x32_bf16 v[40:43], v[56:59], a[104:107], v[40:43]\n\t"
        "s_add_u32 m0, %[ld], 9216\n\t"
        "s_add_u32 %[t0], %[xo], 16512\n\t"
        "s_waitcnt lgkmcnt(6)\n\t"
        "v_mfma_f32_16x16x32_bf16 v[44:47], a[96:99], v[60:63], v[44:47]\n\t"
        "buffer_load_dwordx4 v100, %[sx], %[t0] offen lds\n\t"
        "v_mfma_f32_16x16x32_bf16 v[48:51], a[100:103], v[60:63], v[48:51]\n\t"
        "ds_read_b128 v[56:59], v105 offset:49152\n\t"
        "v_mfma_f32_16x16x32_bf16 v[52:55], v[60:63], a[104:107], v[52:55]\n\t"
        "s_waitcnt lgkmcnt(6)\n\t"
        "v_mfma_f32_16x16x32_bf16 v[32:35], a[108:111], v[64:67], v[32:35]\n\t"
        "v_mfma_f32_16x16x32_bf16 v[36:39], a[112:115], v[64:67], v[36:39]\n\t"
        "ds_read_b128 v[60:63], v105 offset:51200\n\t"
        "v_mfma_f32_16x16x32_bf16 v[40:43], v[64:67], a[116:119], v[40:43]\n\t"
        "s_add_u32 m0, %[ld], 16384\n\t"
        "s_add_u32 %[t0], %[xo], 256\n\t"
        "s_waitcnt lgkmcnt(6)\n\t"
        "v_mfma_f32_16x16x32_bf16 v[44:47], a[108:111], v[68:71], v[44:47]\n\t"
        "buffer_load_dwordx4 v100, %[sx], %[t0] offen lds\n\t"
        "v_mfma_f32_16x16x32_bf16 v[48:51], a[112:115], v[68:71], v[48:51]\n\t"
        "ds_read_b128 v[64:67], v106 offset:49152\n\t"
        "v_mfma_f32_16x16x32_bf16 v[52:55], v[68:71], a[116:119], v[52:55]\n\t"
        "s_waitcnt lgkmcnt(6)\n\t"
        "v_mfma_f32_16x16x32_bf16 v[32:35], a[120:123], v[72:75], v[32:35]\n\t"
        "v_mfma_f32_16x16x32_bf16 v[36:39], a[124:127], v[72:75], v[36:39]\n\t"
        "ds_read_b128 v[68:71], v106 offset:51200\n\t"
        "v_mfma_f32_16x16x32_bf16 v[40:43], v[72:75], a[128:131], v[40:43]\n\t"
        "s_add_u32 m0, %[ld], 17408\n\t"
        "s_add_u32 %[t0], %[xo], 16640\n\t"
        "s_waitcnt lgkmcnt(6)\n\t"
        "v_mfma_f32_16x16x32_bf16 v[44:47], a[120:123], v[76:79], v[44:47]\n\t"
        "buffer_load_dwordx4 v100, %[sx], %[t0] offen lds\n\t"
        "v_mfma_f32_16x16x32_bf16 v[48:51], a[124:127], v[76:79], v[48:51]\n\t"
        "ds_read_b128 v[72:75], v105 offset:57344\n\t"
        "v_mfma_f32_16x16x32_bf16 v[52:55], v[76:79], a[128:131], v[52:55]\n\t"
        "s_waitcnt lgkmcnt(6)\n\t"
        "v_mfma_f32_16x16x32_bf16 v[32:35], a[132:135], v[80:83], v[32:35]\n\t"
        "v_mfma_f32_16x16x32_bf16 v[36:39], a[136:139], v[80:83], v[36:39]\n\t"
        "ds_read_b128 v[76:79], v105 offset:59392\n\t"
        "v_mfma_f32_16x16x32_bf16 v[40:43], v[80:83], a[140:143], v[40:43]\n\t"
        "s_add_u32 m0, %[ld], 24576\n\t"
        "s_add_u32 %[t0], %[xo], 384\n\t"
        "s_waitcnt lgkmcnt(6)\n\t"
        "v_mfma_f32_16x16x32_bf16 v[44:47], a[132:135], v[84:87], v[44:47]\n\t"
        "buffer_load_dwordx4 v100, %[sx], %[t0] offen lds\n\t"
        "v_mfma_f32_16x16x32_bf16 v[48:51], a[136:139], v[84:87], v[48:51]\n\t"
        "ds_read_b128 v[80:83], v106 offset:57344\n\t"
        "v_mfma_f32_16x16x32_bf16 v[52:55], v[84:87], a[140:143], v[52:55]\n\t"
        "s_waitcnt lgkmcnt(6)\n\t"
        "v_mfma_f32_16x16x32_bf16 v[32:35], a[144:147], v[56:59], v[32:35]\n\t"
        "v_mfma_f32_16x16x32_bf16 v[36:39], a[148:151], v[56:59], v[36:39]\n\t"
        "ds_read_b128 v[84:87], v106 offset:59392\n\t"
        "v_mfma_f32_16x16x32_bf16 v[40:43], v[56:59], a[152:155], v[40:43]\n\t"
        "s_add_u32 m0, %[ld], 25600\n\t"
        "s_add_u32 %[t0], %[xo], 16768\n\t"
        "s_waitcnt lgkmcnt(6)\n\t"
        "v_mfma_f32_16x16x32_bf16 v[44:47], a[144:147], v[60:63], v[44:47]\n\t"
        "buffer_load_dwordx4 v100, %[sx], %[t0] offen lds\n\t"
        "v_mfma_f32_16x16x32_bf16 v[48:51], a[148:151], v[60:63], v[48:51]\n\t"
        "v_mfma_f32_16x16x32_bf16 v[52:55], v[60:63], a[152:155], v[52:55]\n\t"
        "s_waitcnt lgkmcnt(5)\n\t"
        "v_mfma_f32_16x16x32_bf16 v[32:35], a[156:159], v[64:67], v[32:35]\n\t"
        "s_waitcnt vmcnt(9) lgkmcnt(0)\n\t"
        "s_barrier\n\t"
        "v_mfma_f32_16x16x32_bf16 v[36:39], a[160:163], v[64:67], v[36:39]\n\t"
        "ds_read_b128 v[56:59], v107 offset:0\n\t"
        "v_mfma_f32_16x16x32_bf16 v[40:43], v[64:67], a[164:167], v[40:43]\n\t"
        "ds_read_b128 v[60:63], v107 offset:2048\n\t"
        "s_add_u32 m0, %[ld], 32768\n\t"
        "s_add_u32 %[t0], %[xo], 512\n\t"
        "v_mfma_f32_16x16x32_bf16 v[44:47], a[156:159], v[68:71], v[44:47]\n\t"
        "buffer_load_dwordx4 v100, %[sx], %[t0] offen lds\n\t"
        "v_mfma_f32_16x16x32_bf16 v[48:51], a[160:163], v[68:71], v[48:51]\n\t"
        "ds_read_b128 v[64:67], v108 offset:0\n\t"
        "v_mfma_f32_16x16x32_bf16 v[52:55], v[68:71], a[164:167], v[52:55]\n\t"
        "v_mfma_f32_16x16x32_bf16 v[32:35], a[168:171], v[72:75], v[32:35]\n\t"
        "v_mfma_f32_16x16x32_bf16 v[36:39], a[172:175], v[72:75], v[36:39]\n\t"
        "ds_read_b128 v[68:71], v108 offset:2048\n\t"
        "v_mov_b32_e32 v117, %[cn]\n\t"
        "buffer_store_dword v117, v113, %[sw], 0 offen sc1\n\t"
        "s_add_u32 %[cn], %[cn], 1\n\t"
        "v_mfma_f32_16x16x32_bf16 v[40:43], v[72:75], a[176:179], v[40:43]\n\t"
        "s_add_u32 m0, %[ld], 33792\n\t"
        "s_add_u32 %[t0], %[xo], 16896\n\t"
        "v_mfma_f32_16x16x32_bf16 v[44:47], a[168:171], v[76:79], v[44:47]\n\t"
        "buffer_load_dwordx4 v100, %[sx], %[t0] offen lds\n\t"
        "v_mfma_f32_16x16x32_bf16 v[48:51], a[172:175], v[76:79], v[48:51]\n\t"
        "ds_read_b128 v[72:75], v107 offset:8192\n\t"
        "v_mfma_f32_16x16x32_bf16 v[52:55], v[76:79], a[176:179], v[52:55]\n\t"
        "v_mfma_f32_16x16x32_bf16 v[32:35], a[180:183], v[80:83], v[32:35]\n\t"
        "v_mfma_f32_16x16x32_bf16 v[36:39], a[184:187], v[80:83], v[36:39]\n\t"
        "ds_read_b128 v[76:79], v107 offset:10240\n\t"
        "v_mfma_f32_16x16x32_bf16 v[40:43], v[80:83], a[188:191], v[40:43]\n\t"
        "s_add_u32 m0, %[ld], 40960\n\t"
        "s_add_u32 %[t0], %[xo], 640\n\t"
        "v_mfma_f32_16x16x32_bf16 v[44:47], a[180:183], v[84:87], v[44:47]\n\t"
        "buffer_load_dwordx4 v100, %[sx], %[t0] offen lds\n\t"
        "v_mfma_f32_16x16x32_bf16 v[48:51], a[184:187], v[84:87], v[48:51]\n\t"
        "ds_read_b128 v[80:83], v108 offset:8192\n\t"
        "v_mfma_f32_16x16x32_bf16 v[52:55], v[84:87], a[188:191], v[52:55]\n\t"
        "s_waitcnt lgkmcnt(6)\n\t"
        "v_mfma_f32_16x16x32_bf16 v[32:35], a[192:195], v[56:59], v[32:35]\n\t"
        "v_mfma_f32_16x16x32_bf16 v[36:39], a[196:199], v[56:59], v[36:39]\n\t"
        "ds_read_b128 v[84:87], v108 offset:10240\n\t"
        "v_mfma_f32_16x16x32_bf16 v[40:43], v[56:59], a[200:203], v[40:43]\n\t"
        "s_add_u32 m0, %[ld], 41984\n\t"
        "s_add_u32 %[t0], %[xo], 17024\n\t"
        "s_waitcnt lgkmcnt(6)\n\t"
        "v_mfma_f32_16x16x32_bf16 v[44:47], a[192:195], v[60:63], v[44:47]\n\t"
        "buffer_load_dwordx4 v100, %[sx], %[t0] offen lds\n\t"
        "v_mfma_f32_16x16x32_bf16 v[48:51], a[196:199], v[60:63], v[48:51]\n\t"
        "ds_read_b128 v[56:59], v107 offset:16384\n\t"
        "v_mfma_f32_16x16x32_bf16 v[52:55], v[60:63], a[200:203], v[52:55]\n\t"
        "s_waitcnt lgkmcnt(6)\n\t"
        "v_mfma_f32_16x16x32_bf16 v[32:35], a[204:207], v[64:67], v[32:35]\n\t"
        "v_mfma_f32_16x16x32_bf16 v[36:39], a[208:211], v[64:67], v[36:39]\n\t"
        "ds_read_b128 v[60:63], v107 offset:18432\n\t"
        "v_mfma_f32_16x16x32_bf16 v[40:43], v[64:67], a[212:215], v[40:43]\n\t"
        "s_add_u32 m0, %[ld], 49152\n\t"
        "s_add_u32 %[t0], %[xo], 768\n\t"
        "s_waitcnt lgkmcnt(6)\n\t"
        "v_mfma_f32_16x16x32_bf16 v[44:47], a[204:207], v[68:71], v[44:47]\n\t"
        "buffer_load_dwordx4 v100, %[sx], %[t0] offen lds\n\t"
        "v_mfma_f32_16x16x32_bf16 v[48:51], a[208:211], v[68:71], v[48:51]\n\t"
        "ds_read_b128 v[64:67], v108 offset:16384\n\t"
        "v_mfma_f32_16x16x32_bf16 v[52:55], v[68:71], a[212:215], v[52:55]\n\t"
        "s_waitcnt lgkmcnt(6)\n\t"
        "v_mfma_f32_16x16x32_bf16 v[32:35], a[216:219], v[72:75], v[32:35]\n\t"
        "v_mfma_f32_16x16x32_bf16 v[36:39], a[220:223], v[72:75], v[36:39]\n\t"
        "ds_read_b128 v[68:71], v108 offset:18432\n\t"
        "v_mfma_f32_16x16x32_bf16 v[40:43], v[72:75], a[224:227], v[40:43]\n\t"
        "s_add_u32 m0, %[ld], 50176\n\t"
        "s_add_u32 %[t0], %[xo], 17152\n\t"
        "s_waitcnt lgkmcnt(6)\n\t"
        "v_mfma_f32_16x16x32_bf16 v[44:47], a[216:219], v[76:79], v[44:47]\n\t"
        "buffer_load_dwordx4 v100, %[sx], %[t0] offen lds\n\t"
        "v_mfma_f32_16x16x32_bf16 v[48:51], a[220:223], v[76:79], v[48:51]\n\t"
        "ds_read_b128 v[72:75], v107 offset:24576\n\t"
        "v_mfma_f32_16x16x32_bf16 v[52:55], v[76:79], a[224:227], v[52:55]\n\t"
        "s_waitcnt lgkmcnt(6)\n\t"
        "v_mfma_f32_16x16x32_bf16 v[32:35], a[228:231], v[80:83], v[32:35]\n\t"
        "v_mfma_f32_16x16x32_bf16 v[36:39], a[232:235], v[80:83], v[36:39]\n\t"
        "ds_read_b128 v[76:79], v107 offset:26624\n\t"
        "v_mfma_f32_16x16x32_bf16 v[40:43], v[80:83], a[236:239], v[40:43]\n\t"
        "s_add_u32 m0, %[ld], 57344\n\t"
        "s_add_u32 %[t0], %[xo], 896\n\t"
        "s_waitcnt lgkmcnt(6)\n\t"
        "v_mfma_f32_16x16x32_bf16 v[44:47], a[228:231], v[84:87], v[44:47]\n\t"
        "buffer_load_dwordx4 v100, %[sx], %[t0] offen lds\n\t"
        "v_mfma_f32_16x16x32_bf16 v[48:51], a[232:235], v[84:87], v[48:51]\n\t"
        "ds_read_b128 v[80:83], v108 offset:24576\n\t"
        "v_mfma_f32_16x16x32_bf16 v[52:55], v[84:87], a[236:239], v[52:55]\n\t"
        "s_waitcnt lgkmcnt(6)\n\t"
        "v_mfma_f32_16x16x32_bf16 v[32:35], a[240:243], v[56:59], v[32:35]\n\t"
        "v_mfma_f32_16x16x32_bf16 v[36:39], a[244:247], v[56:59], v[36:39]\n\t"
        "ds_read_b128 v[84:87], v108 offset:26624\n\t"
        "v_mfma_f32_16x16x32_bf16 v[40:43], v[56:59], a[248:251], v[40:43]\n\t"
        "s_add_u32 m0, %[ld], 58368\n\t"
        "s_add_u32 %[t0], %[xo], 17280\n\t"
        "s_waitcnt lgkmcnt(6)\n\t"
        "v_mfma_f32_16x16x32_bf16 v[44:47], a[240:243], v[60:63], v[44:47]\n\t"
        "buffer_load_dwordx4 v100, %[sx], %[t0] offen lds\n\t"
        "v_mfma_f32_16x16x32_bf16 v[48:51], a[244:247], v[60:63], v[48:51]\n\t"
        "v_mfma_f32_16x16x32_bf16 v[52:55], v[60:63], a[248:251], v[52:55]\n\t"
        "s_waitcnt lgkmcnt(5)\n\t"
        "v_mfma_f32_16x16x32_bf16 v[32:35], a[252:255], v[64:67], v[32:35]\n\t"
        "s_waitcnt vmcnt(17) lgkmcnt(0)\n\t"
        "s_barrier\n\t"
        "v_mfma_f32_16x16x32_bf16 v[36:39], v[128:131], v[64:67], v[36:39]\n\t"
        "ds_read_b128 v[56:59], v107 offset:32768\n\t"
        "v_mfma_f32_16x16x32_bf16 v[40:43], v[64:67], v[132:135], v[40:43]\n\t"
        "ds_read_b128 v[60:63], v107 offset:34816\n\t"
        "s_add_u32 m0, %[ld], 65536\n\t"
        "s_add_u32 %[t0], %[xo], 1024\n\t"
        "v_mfma_f32_16x16x32_bf16 v[44:47], a[252:255], v[68:71], v[44:47]\n\t"
        "buffer_load_dwordx4 v100, %[sx], %[t0] offen lds\n\t"
        "v_mfma_f32_16x16x32_bf16 v[48:51], v[128:131], v[68:71], v[48:51]\n\t"
        "ds_read_b128 v[64:67], v108 offset:32768\n\t"
        "v_mfma_f32_16x16x32_bf16 v[52:55], v[68:71], v[132:135], v[52:55]\n\t"
        "v_mfma_f32_16x16x32_bf16 v[32:35], v[136:139], v[72:75], v[32:35]\n\t"
        "v_mfma_f32_16x16x32_bf16 v[36:39], v[140:143], v[72:75], v[36:39]\n\t"
        "ds_read_b128 v[68:71], v108 offset:34816\n\t"
        "v_mfma_f32_16x16x32_bf16 v[40:43], v[72:75], v[144:147], v[40:43]\n\t"
        "s_add_u32 m0, %[ld], 66560\n\t"
        "s_add_u32 %[t0], %[xo], 17408\n\t"
        "v_mfma_f32_16x16x32_bf16 v[44:47], v[136:139], v[76:79], v[44:47]\n\t"
        "buffer_load_dwordx4 v100, %[sx], %[t0] offen lds\n\t"
        "v_mfma_f32_16x16x32_bf16 v[48:51], v[140:143], v[76:79], v[48:51]\n\t"
        "ds_read_b128 v[72:75], v107 offset:40960\n\t"
        "v_mfma_f32_16x16x32_bf16 v[52:55], v[76:79], v[144:147], v[52:55]\n\t"
        "v_mfma_f32_16x16x32_bf16 v[32:35], v[148:151], v[80:83], v[32:35]\n\t"
        "v_mfma_f32_16x16x32_bf16 v[36:39], v[152:155], v[80:83], v[36:39]\n\t"
        "ds_read_b128 v[76:79], v107 offset:43008\n\t"
        "v_mfma_f32_16x16x32_bf16 v[40:43], v[80:83], v[156:159], v[40:43]\n\t"
        "s_add_u32 m0, %[ld], 73728\n\t"
        "s_add_u32 %[t0], %[xo], 1152\n\t"
        "v_mfma_f32_16x16x32_bf16 v[44:47], v[148:151], v[84:87], v[44:47]\n\t"
        "buffer_load_dwordx4 v100, %[sx], %[t0] offen lds\n\t"
        "v_mfma_f32_16x16x32_bf16 v[48:51], v[152:155], v[84:87], v[48:51]\n\t"
        "ds_read_b128 v[80:83], v108 offset:40960\n\t"
        "v_mfma_f32_16x16x32_bf16 v[52:55], v[84:87], v[156:159], v[52:55]\n\t"
        "s_waitcnt lgkmcnt(6)\n\t"
        "v_mfma_f32_16x16x32_bf16 v[32:35], v[160:163], v[56:59], v[32:35]\n\t"
        "v_mfma_f32_16x16x32_bf16 v[36:39], v[164:167], v[56:59], v[36:39]\n\t"
        "ds_read_b128 v[84:87], v108 offset:43008\n\t"
        "v_mfma_f32_16x16x32_bf16 v[40:43], v[56:59], v[168:171], v[40:43]\n\t"
        "s_add_u32 m0, %[ld], 74752\n\t"
        "s_add_u32 %[t0], %[xo], 17536\n\t"
        "s_waitcnt lgkmcnt(6)\n\t"
        "v_mfma_f32_16x16x32_bf16 v[44:47], v[160:163], v[60:63], v[44:47]\n\t"
        "buffer_load_dwordx4 v100, %[sx], %[t0] offen lds\n\t"
        "v_mfma_f32_16x16x32_bf16 v[48:51], v[164:167], v[60:63], v[48:51]\n\t"
        "ds_read_b128 v[56:59], v107 offset:49152\n\t"
        "v_mfma_f32_16x16x32_bf16 v[52:55], v[60:63], v[168:171], v[52:55]\n\t"
        "s_waitcnt lgkmcnt(6)\n\t"
        "v_mfma_f32_16x16x32_bf16 v[32:35], v[172:175], v[64:67], v[32:35]\n\t"
        "v_mfma_f32_16x16x32_bf16 v[36:39], v[176:179], v[64:67], v[36:39]\n\t"
        "ds_read_b128 v[60:63], v107 offset:51200\n\t"
        "v_mfma_f32_16x16x32_bf16 v[40:43], v[64:67], v[180:183], v[40:43]\n\t"
        "s_add_u32 m0, %[ld], 81920\n\t"
        "s_add_u32 %[t0], %[xo], 1280\n\t"
        "s_waitcnt lgkmcnt(6)\n\t"
        "v_mfma_f32_16x16x32_bf16 v[44:47], v[172:175], v[68:71], v[44:47]\n\t"
        "buffer_load_dwordx4 v100, %[sx], %[t0] offen lds\n\t"
        "v_mfma_f32_16x16x32_bf16 v[48:51], v[176:179], v[68:71], v[48:51]\n\t"
        "ds_read_b128 v[64:67], v108 offset:49152\n\t"
        "v_mfma_f32_16x16x32_bf16 v[52:55], v[68:71], v[180:183], v[52:55]\n\t"
        "s_waitcnt lgkmcnt(6)\n\t"
        "v_mfma_f32_16x16x32_bf16 v[32:35], v[184:187], v[72:75], v[32:35]\n\t"
        "v_mfma_f32_16x16x32_bf16 v[36:39], v[188:191], v[72:75], v[36:39]\n\t"
        "ds_read_b128 v[68:71], v108 offset:51200\n\t"
        "v_mfma_f32_16x16x32_bf16 v[40:43], v[72:75], v[192:195], v[40:43]\n\t"
        "s_add_u32 m0, %[ld], 82944\n\t"
        "s_add_u32 %[t0], %[xo], 17664\n\t"
        "s_waitcnt lgkmcnt(6)\n\t"
        "v_mfma_f32_16x16x32_bf16 v[44:47], v[184:187], v[76:79], v[44:47]\n\t"
        "buffer_load_dwordx4 v100, %[sx], %[t0] offen lds\n\t"
        "v_mfma_f32_16x16x32_bf16 v[48:51], v[188:191], v[76:79], v[48:51]\n\t"
        "ds_read_b128 v[72:75], v107 offset:57344\n\t"
        "v_mfma_f32_16x16x32_bf16 v[52:55], v[76:79], v[192:195], v[52:55]\n\t"
        "s_waitcnt lgkmcnt(6)\n\t"
        "v_mfma_f32_16x16x32_bf16 v[32:35], v[196:199], v[80:83], v[32:35]\n\t"
        "v_mfma_f32_16x16x32_bf16 v[36:39], v[200:203], v[80:83], v[36:39]\n\t"
        "ds_read_b128 v[76:79], v107 offset:59392\n\t"
        "v_mfma_f32_16x16x32_bf16 v[40:43], v[80:83], v[204:207], v[40:43]\n\t"
        "s_add_u32 m0, %[ld], 90112\n\t"
        "s_add_u32 %[t0], %[xo], 1408\n\t"
        "s_waitcnt lgkmcnt(6)\n\t"
        "v_mfma_f32_16x16x32_bf16 v[44:47], v[196:199], v[84:87], v[44:47]\n\t"
        "buffer_load_dwordx4 v100, %[sx], %[t0] offen lds\n\t"
        "v_mfma_f32_16x16x32_bf16 v[48:51], v[200:203], v[84:87], v[48:51]\n\t"
        "ds_read_b128 v[80:83], v108 offset:57344\n\t"
        "v_mfma_f32_16x16x32_bf16 v[52:55], v[84:87], v[204:207], v[52:55]\n\t"
        "s_waitcnt lgkmcnt(6)\n\t"
        "v_mfma_f32_16x16x32_bf16 v[32:35], v[208:211], v[56:59], v[32:35]\n\t"
        "v_mfma_f32_16x16x32_bf16 v[36:39], v[212:215], v[56:59], v[36:39]\n\t"
        "ds_read_b128 v[84:87], v108 offset:59392\n\t"
        "v_mfma_f32_16x16x32_bf16 v[40:43], v[56:59], v[216:219], v[40:43]\n\t"
        "s_add_u32 m0, %[ld], 91136\n\t"
        "s_add_u32 %[t0], %[xo], 17792\n\t"
        "s_waitcnt lgkmcnt(6)\n\t"
        "v_mfma_f32_16x16x32_bf16 v[44:47], v[208:211], v[60:63], v[44:47]\n\t"
        "buffer_load_dwordx4 v100, %[sx], %[t0] offen lds\n\t"
        "v_mfma_f32_16x16x32_bf16 v[48:51], v[212:215], v[60:63], v[48:51]\n\t"
        "v_mfma_f32_16x16x32_bf16 v[52:55], v[60:63], v[216:219], v[52:55]\n\t"
        "s_waitcnt lgkmcnt(5)\n\t"
        "v_mfma_f32_16x16x32_bf16 v[32:35], v[220:223], v[64:67], v[32:35]\n\t"
        "s_waitcnt vmcnt(17) lgkmcnt(0)\n\t"
        "s_barrier\n\t"
        "v_mfma_f32_16x16x32_bf16 v[36:39], v[224:227], v[64:67], v[36:39]\n\t"
        "ds_read_b128 v[56:59], v105 offset:0\n\t"
        "v_mfma_f32_16x16x32_bf16 v[40:43], v[64:67], v[228:231], v[40:43]\n\t"
        "ds_read_b128 v[60:63], v105 offset:2048\n\t"
        "s_add_u32 m0, %[ld], 98304\n\t"
        "s_add_u32 %[t0], %[xo], 1536\n\t"
        "v_mfma_f32_16x16x32_bf16 v[44:47], v[220:223], v[68:71], v[44:47]\n\t"
        "buffer_load_dwordx4 v100, %[sx], %[t0] offen lds\n\t"
        "v_mfma_f32_16x16x32_bf16 v[48:51], v[224:227], v[68:71], v[48:51]\n\t"
        "ds_read_b128 v[64:67], v106 offset:0\n\t"
        "v_mfma_f32_16x16x32_bf16 v[52:55], v[68:71], v[228:231], v[52:55]\n\t"
        "v_mfma_f32_16x16x32_bf16 v[32:35], v[232:235], v[72:75], v[32:35]\n\t"
        "v_mfma_f32_16x16x32_bf16 v[36:39], v[236:239], v[72:75], v[36:39]\n\t"
        "ds_read_b128 v[68:71], v106 offset:2048\n\t"
        "v_mfma_f32_16x16x32_bf16 v[40:43], v[72:75], v[240:243], v[40:43]\n\t"
        "s_add_u32 m0, %[ld], 99328\n\t"
        "s_add_u32 %[t0], %[xo], 17920\n\t"
        "v_mfma_f32_16x16x32_bf16 v[44:47], v[232:235], v[76:79], v[44:47]\n\t"
        "buffer_load_dwordx4 v100, %[sx], %[t0] offen lds\n\t"
        "v_mfma_f32_16x16x32_bf16 v[48:51], v[236:239], v[76:79], v[48:51]\n\t"
        "ds_read_b128 v[72:75], v105 offset:8192\n\t"
        "v_mfma_f32_16x16x32_bf16 v[52:55], v[76:79], v[240:243], v[52:55]\n\t"
        "v_mfma_f32_16x16x32_bf16 v[32:35], v[244:247], v[80:83], v[32:35]\n\t"
        "v_mfma_f32_16x16x32_bf16 v[36:39], v[248:251], v[80:83], v[36:39]\n\t"
        "ds_read_b128 v[76:79], v105 offset:10240\n\t"
        "v_mfma_f32_16x16x32_bf16 v[40:43], v[80:83], v[252:255], v[40:43]\n\t"
        "s_add_u32 m0, %[ld], 106496\n\t"
        "s_add_u32 %[t0], %[xo], 1664\n\t"
        "v_mfma_f32_16x16x32_bf16 v[44:47], v[244:247], v[84:87], v[44:47]\n\t"
        "buffer_load_dwordx4 v100, %[sx], %[t0] offen lds\n\t"
        "v_mfma_f32_16x16x32_bf16 v[48:51], v[248:251], v[84:87], v[48:51]\n\t"
        "ds_read_b128 v[80:83], v106 offset:8192\n\t"
        "v_mfma_f32_16x16x32_bf16 v[52:55], v[84:87], v[252:255], v[52:55]\n\t"
        "s_cmp_lt_u32 %[cn], 32\n\t"
        "s_cbranch_scc1 LG_loop%=\n\t"
        "s_nop 7\n\t"
        "v_cvt_pk_bf16_f32 v88, v32, v33\n\t"
        "v_cvt_pk_bf16_f32 v89, v34, v35\n\t"
        "v_cvt_pk_bf16_f32 v90, v36, v37\n\t"
        "v_cvt_pk_bf16_f32 v91, v38, v39\n\t"
        "buffer_store_dwordx4 v[88:91], v109, %[sw], %[so] offen sc1\n\t"
        "v_cvt_pk_bf16_f32 v92, v44, v45\n\t"
        "v_cvt_pk_bf16_f32 v93, v46, v47\n\t"
        "v_cvt_pk_bf16_f32 v94, v48, v49\n\t"
        "v_cvt_pk_bf16_f32 v95, v50, v51\n\t"
        "buffer_store_dwordx4 v[92:95], v110, %[sw], %[so] offen sc1\n\t"
        "v_cvt_pk_bf16_f32 v96, v40, v41\n\t"
        "v_cvt_pk_bf16_f32 v97, v42, v43\n\t"
        "v_cvt_pk_bf16_f32 v98, v52, v53\n\t"
        "v_cvt_pk_bf16_f32 v99, v54, v55\n\t"
        "buffer_store_dwordx4 v[96:99], v111, %[sw], %[so] offen sc1\n\t"
        "s_waitcnt vmcnt(0) lgkmcnt(0)\n\t"
        "v_mov_b32_e32 v117, %[cn]\n\t"
        "buffer_store_dword v117, v113, %[sw], 0 offen sc1\n\t"
        "s_barrier\n\t"
        "s_branch LG_end%=\n\t"
        "LG_first%=:\n\t"
        "s_waitcnt vmcnt(0)\n\t"
        "v_mov_b32_e32 v117, %[cn]\n\t"
        "buffer_store_dword v117, v113, %[sw], 0 offen sc1\n\t"
        "s_branch LG_back%=\n\t"
        "LG_end%=:\n\t"
        ".Lk_end_G:\n\t"
        : [t0] "=&s"(t0), [xo] "=&s"(xo), [so] "=&s"(so), [cn] "=&s"(cn)
        : [sx] "s"(sx), [sq] "s"(sq), [sv] "s"(sv), [sw] "s"(sw), [ld] "s"(lb + w * 2048),
          "{v100}"((16 * w + (l >> 3)) * 2048 + (((l & 7) ^ (l >> 3)) << 4)), "{v101}"((8 * w + (l >> 3)) * 8192 + h * 256 + (c & 1) * 128 + (l & 7) * 16),
          "{v112}"((16 * w + (l >> 2)) * 8192 + h * 256 + cv * 64 + (l & 3) * 16), "{v102}"(lb + 131072 + w * 1024 + ((l * 16) ^ ((l & 16) << 2))), "{v116}"(lb + 131072 + w * 1024 + ((l * 16) ^ (l & 32))),
          "{v103}"(lr + rr * 128 + ((tn ^ (n >> 3)) * 64) + (n & 3) * 16), "{v104}"(lr + 8192 + rr * 64 + ((tn ^ (g & 1)) * 32) + (n & 3) * 8), "{v105}"(lx), "{v106}"(lx ^ 64u), "{v107}"(lx + 65536), "{v108}"((lx ^ 64u) + 65536),
          "{v109}"(aq), "{v110}"(aq + 4096), "{v111}"(W_VF + (tm * 32 + h) * 8192 + (2 * cv + tn) * 1024 + l * 16), "{v113}"(l == 0 ? F_KVQ + h * 1024 + (c * 4 + w) * 64 : OOB), "{v[114:115]}"(tp)
        : "memory", "scc", "m0", "v8", "v9", "v10", "v11", "v12", "v13", "v14", "v15", "v16", "v17", "v18", "v19", "v20", "v21", "v22", "v23", "v24", "v25", "v26", "v27", "v28", "v29", "v30", "v31", "v32", "v33", "v34", "v35", "v36", "v37", "v38", "v39", "v40", "v41", "v42", "v43", "v44", "v45", "v46", "v47", "v48", "v49", "v50", "v51", "v52", "v53", "v54", "v55", "v56", "v57", "v58", "v59", "v60", "v61", "v62", "v63", "v64", "v65", "v66", "v67", "v68", "v69", "v70", "v71", "v72", "v73", "v74", "v75", "v76", "v77", "v78", "v79", "v80", "v81", "v82", "v83", "v84", "v85", "v86", "v87", "v88", "v89", "v90", "v91", "v92", "v93", "v94", "v95", "v96", "v97", "v98", "v99", "v117", "v118", "v119", "v120", "v121", "v122", "v123", "v124", "v125", "v126", "v127", "v128", "v129", "v130", "v131", "v132", "v133", "v134", "v135", "v136", "v137", "v138", "v139", "v140", "v141", "v142", "v143", "v144", "v145", "v146", "v147", "v148", "v149", "v150", "v151", "v152", "v153", "v154", "v155", "v156", "v157", "v158", "v159", "v160", "v161", "v162", "v163", "v164", "v165", "v166", "v167", "v168", "v169", "v170", "v171", "v172", "v173", "v174", "v175", "v176", "v177", "v178", "v179", "v180", "v181", "v182", "v183", "v184", "v185", "v186", "v187", "v188", "v189", "v190", "v191", "v192", "v193", "v194", "v195", "v196", "v197", "v198", "v199", "v200", "v201", "v202", "v203", "v204", "v205", "v206", "v207", "v208", "v209", "v210", "v211", "v212", "v213", "v214", "v215", "v216", "v217", "v218", "v219", "v220", "v221", "v222", "v223", "v224", "v225", "v226", "v227", "v228", "v229", "v230", "v231", "v232", "v233", "v234", "v235", "v236", "v237", "v238", "v239", "v240", "v241", "v242", "v243", "v244", "v245", "v246", "v247", "v248", "v249", "v250", "v251", "v252", "v253", "v254", "v255", "a0", "a1", "a2", "a3", "a4", "a5", "a6", "a7", "a8", "a9", "a10", "a11", "a12", "a13", "a14", "a15", "a16", "a17", "a18", "a19", "a20", "a21", "a22", "a23", "a24", "a25", "a26", "a27", "a28", "a29", "a30", "a31", "a32", "a33", "a34", "a35", "a36", "a37", "a38", "a39", "a40", "a41", "a42", "a43", "a44", "a45", "a46", "a47", "a48", "a49", "a50", "a51", "a52", "a53", "a54", "a55", "a56", "a57", "a58", "a59", "a60", "a61", "a62", "a63", "a64", "a65", "a66", "a67", "a68", "a69", "a70", "a71", "a72", "a73", "a74", "a75", "a76", "a77", "a78", "a79", "a80", "a81", "a82", "a83", "a84", "a85", "a86", "a87", "a88", "a89", "a90", "a91", "a92", "a93", "a94", "a95", "a96", "a97", "a98", "a99", "a100", "a101", "a102", "a103", "a104", "a105", "a106", "a107", "a108", "a109", "a110", "a111", "a112", "a113", "a114", "a115", "a116", "a117", "a118", "a119", "a120", "a121", "a122", "a123", "a124", "a125", "a126", "a127", "a128", "a129", "a130", "a131", "a132", "a133", "a134", "a135", "a136", "a137", "a138", "a139", "a140", "a141", "a142", "a143", "a144", "a145", "a146", "a147", "a148", "a149", "a150", "a151", "a152", "a153", "a154", "a155", "a156", "a157", "a158", "a159", "a160", "a161", "a162", "a163", "a164", "a165", "a166", "a167", "a168", "a169", "a170", "a171", "a172", "a173", "a174", "a175", "a176", "a177", "a178", "a179", "a180", "a181", "a182", "a183", "a184", "a185", "a186", "a187", "a188", "a189", "a190", "a191", "a192", "a193", "a194", "a195", "a196", "a197", "a198", "a199", "a200", "a201", "a202", "a203", "a204", "a205", "a206", "a207", "a208", "a209", "a210", "a211", "a212", "a213", "a214", "a215", "a216", "a217", "a218", "a219", "a220", "a221", "a222", "a223", "a224", "a225", "a226", "a227", "a228", "a229", "a230", "a231", "a232", "a233", "a234", "a235", "a236", "a237", "a238", "a239", "a240", "a241", "a242", "a243", "a244", "a245", "a246", "a247", "a248", "a249", "a250", "a251", "a252", "a253", "a254", "a255");
}

__device__ __forceinline__ void out_block(const Params& p, unsigned char* lds, const int wv, const int rp, const int nt) {
    const int w = wv, l = otid() & 63, n = l & 15, g = l >> 4;
    const unsigned lb = __builtin_amdgcn_readfirstlane((unsigned)(size_t)lds), stg = lb + 131072 + w * 8192;
    const v4u so = mk_srd(p.Wo, 1024 * 4096 * 2), sw = mk_srd(p.ws, W_TOTAL), su = mk_srd(p.out, S * D * 2);
    const unsigned lr = stg + (4 * g + (n >> 2)) * 128 + ((2 * (g & 1) + (n >> 3)) << 5) + (n & 3) * 8;
    unsigned t0, t1, tl = rp, pg, cu, nx, sp, re;
    asm volatile(
        "s_nop 4\n\t"
        "s_add_u32 %[t1], %[ro], 0\n\t"
        "s_and_b32 %[t1], %[t1], 7\n\t"
        "s_lshl_b32 %[t1], %[t1], 18\n\t"
        "s_add_u32 %[t0], %[t1], 0\n\t"
        "buffer_load_dwordx4 a[0:3], v110, %[so], %[t0] offen\n\t"
        "s_sleep 2\n\t"
        "s_add_u32 %[t0], %[t1], 16384\n\t"
        "buffer_load_dwordx4 a[4:7], v110, %[so], %[t0] offen\n\t"
        "s_sleep 2\n\t"
        "s_add_u32 %[t0], %[t1], 32768\n\t"
        "buffer_load_dwordx4 a[8:11], v110, %[so], %[t0] offen\n\t"
        "s_sleep 2\n\t"
        "s_add_u32 %[t0], %[t1], 49152\n\t"
        "buffer_load_dwordx4 a[12:15], v110, %[so], %[t0] offen\n\t"
        "s_sleep 2\n\t"
        "s_add_u32 %[t0], %[t1], 65536\n\t"
        "buffer_load_dwordx4 a[16:19], v110, %[so], %[t0] offen\n\t"
        "s_sleep 2\n\t"
        "s_add_u32 %[t0], %[t1], 81920\n\t"
        "buffer_load_dwordx4 a[20:23], v110, %[so], %[t0] offen\n\t"
        "s_sleep 2\n\t"
        "s_add_u32 %[t0], %[t1], 98304\n\t"
        "buffer_load_dwordx4 v[24:27], v110, %[so], %[t0] offen\n\t"
        "s_sleep 2\n\t"
        "s_add_u32 %[t0], %[t1], 114688\n\t"
        "buffer_load_dwordx4 v[28:31], v110, %[so], %[t0] offen\n\t"
        "s_sleep 2\n\t"
        "s_add_u32 %[t1], %[ro], 0\n\t"
        "s_and_b32 %[t1], %[t1], 7\n\t"
        "s_lshl_b32 %[t1], %[t1], 18\n\t"
        "s_add_u32 %[t0], %[t1], 131072\n\t"
        "buffer_load_dwordx4 a[24:27], v110, %[so], %[t0] offen\n\t"
        "s_sleep 2\n\t"
        "s_add_u32 %[t0], %[t1], 147456\n\t"
        "buffer_load_dwordx4 a[28:31], v110, %[so], %[t0] offen\n\t"
        "s_sleep 2\n\t"
        "s_add_u32 %[t0], %[t1], 163840\n\t"
        "buffer_load_dwordx4 a[32:35], v110, %[so], %[t0] offen\n\t"
        "s_sleep 2\n\t"
        "s_add_u32 %[t0], %[t1], 180224\n\t"
        "buffer_load_dwordx4 a[36:39], v110, %[so], %[t0] offen\n\t"
        "s_sleep 2\n\t"
        "s_add_u32 %[t0], %[t1], 196608\n\t"
        "buffer_load_dwordx4 a[40:43], v110, %[so], %[t0] offen\n\t"
        "s_sleep 2\n\t"
        "s_add_u32 %[t0], %[t1], 212992\n\t"
        "buffer_load_dwordx4 a[44:47], v110, %[so], %[t0] offen\n\t"
        "s_sleep 2\n\t"
        "s_add_u32 %[t0], %[t1], 229376\n\t"
        "buffer_load_dwordx4 v[32:35], v110, %[so], %[t0] offen\n\t"
        "s_sleep 2\n\t"
        "s_add_u32 %[t0], %[t1], 245760\n\t"
        "buffer_load_dwordx4 v[36:39], v110, %[so], %[t0] offen\n\t"
        "s_sleep 2\n\t"
        "s_waitcnt vmcnt(8)\n\t"
        "ds_write_b128 v111, a[0:3] offset:0\n\t"
        "ds_write_b128 v111, a[4:7] offset:1024\n\t"
        "ds_write_b128 v111, a[8:11] offset:2048\n\t"
        "ds_write_b128 v111, a[12:15] offset:3072\n\t"
        "ds_write_b128 v111, a[16:19] offset:4096\n\t"
        "ds_write_b128 v111, a[20:23] offset:5120\n\t"
        "ds_write_b128 v111, v[24:27] offset:6144\n\t"
        "ds_write_b128 v111, v[28:31] offset:7168\n\t"
        "ds_read_b64_tr_b16 a[0:1], v112 offset:0\n\t"
        "ds_read_b64_tr_b16 a[2:3], v112 offset:2048\n\t"
        "ds_read_b64_tr_b16 a[12:13], v112 offset:4096\n\t"
        "ds_read_b64_tr_b16 a[14:15], v112 offset:6144\n\t"
        "s_waitcnt lgkmcnt(8)\n\t"
        "ds_read_b64_tr_b16 a[4:5], v113 offset:0\n\t"
        "ds_read_b64_tr_b16 a[6:7], v113 offset:2048\n\t"
        "ds_read_b64_tr_b16 a[16:17], v113 offset:4096\n\t"
        "ds_read_b64_tr_b16 a[18:19], v113 offset:6144\n\t"
        "s_waitcnt lgkmcnt(8)\n\t"
        "ds_read_b64_tr_b16 a[8:9], v114 offset:0\n\t"
        "ds_read_b64_tr_b16 a[10:11], v114 offset:2048\n\t"
        "ds_read_b64_tr_b16 a[20:21], v114 offset:4096\n\t"
        "ds_read_b64_tr_b16 a[22:23], v114 offset:6144\n\t"
        "s_waitcnt lgkmcnt(8)\n\t"
        "ds_read_b64_tr_b16 v[56:57], v115 offset:0\n\t"
        "ds_read_b64_tr_b16 v[58:59], v115 offset:2048\n\t"
        "ds_read_b64_tr_b16 v[60:61], v115 offset:4096\n\t"
        "ds_read_b64_tr_b16 v[62:63], v115 offset:6144\n\t"
        "s_add_u32 %[t1], %[ro], 1\n\t"
        "s_and_b32 %[t1], %[t1], 7\n\t"
        "s_lshl_b32 %[t1], %[t1], 18\n\t"
        "s_add_u32 %[t0], %[t1], 0\n\t"
        "buffer_load_dwordx4 a[48:51], v110, %[so], %[t0] offen\n\t"
        "s_sleep 2\n\t"
        "s_add_u32 %[t0], %[t1], 16384\n\t"
        "buffer_load_dwordx4 a[52:55], v110, %[so], %[t0] offen\n\t"
        "s_sleep 2\n\t"
        "s_add_u32 %[t0], %[t1], 32768\n\t"
        "buffer_load_dwordx4 a[56:59], v110, %[so], %[t0] offen\n\t"
        "s_sleep 2\n\t"
        "s_add_u32 %[t0], %[t1], 49152\n\t"
        "buffer_load_dwordx4 a[60:63], v110, %[so], %[t0] offen\n\t"
        "s_sleep 2\n\t"
        "s_add_u32 %[t0], %[t1], 65536\n\t"
        "buffer_load_dwordx4 a[64:67], v110, %[so], %[t0] offen\n\t"
        "s_sleep 2\n\t"
        "s_add_u32 %[t0], %[t1], 81920\n\t"
        "buffer_load_dwordx4 a[68:71], v110, %[so], %[t0] offen\n\t"
        "s_sleep 2\n\t"
        "s_add_u32 %[t0], %[t1], 98304\n\t"
        "buffer_load_dwordx4 v[40:43], v110, %[so], %[t0] offen\n\t"
        "s_sleep 2\n\t"
        "s_add_u32 %[t0], %[t1], 114688\n\t"
        "buffer_load_dwordx4 v[44:47], v110, %[so], %[t0] offen\n\t"
        "s_sleep 2\n\t"
        "s_waitcnt lgkmcnt(0)\n\t"
        "ds_write_b128 v116, v[56:59] offset:0\n\t"
        "ds_write_b128 v116, v[60:63] offset:1024\n\t"
        "s_waitcnt vmcnt(8)\n\t"
        "ds_write_b128 v111, a[24:27] offset:0\n\t"
        "ds_write_b128 v111, a[28:31] offset:1024\n\t"
        "ds_write_b128 v111, a[32:35] offset:2048\n\t"
        "ds_write_b128 v111, a[36:39] offset:3072\n\t"
        "ds_write_b128 v111, a[40:43] offset:4096\n\t"
        "ds_write_b128 v111, a[44:47] offset:5120\n\t"
        "ds_write_b128 v111, v[32:35] offset:6144\n\t"
        "ds_write_b128 v111, v[36:39] offset:7168\n\t"
        "ds_read_b64_tr_b16 a[24:25], v112 offset:0\n\t"
        "ds_read_b64_tr_b16 a[26:27], v112 offset:2048\n\t"
        "ds_read_b64_tr_b16 a[36:37], v112 offset:4096\n\t"
        "ds_read_b64_tr_b16 a[38:39], v112 offset:6144\n\t"
        "s_waitcnt lgkmcnt(8)\n\t"
        "ds_read_b64_tr_b16 a[28:29], v113 offset:0\n\t"
        "ds_read_b64_tr_b16 a[30:31], v113 offset:2048\n\t"
        "ds_read_b64_tr_b16 a[40:41], v113 offset:4096\n\t"
        "ds_read_b64_tr_b16 a[42:43], v113 offset:6144\n\t"
        "s_waitcnt lgkmcnt(8)\n\t"
        "ds_read_b64_tr_b16 a[32:33], v114 offset:0\n\t"
        "ds_read_b64_tr_b16 a[34:35], v114 offset:2048\n\t"
        "ds_read_b64_tr_b16 a[44:45], v114 offset:4096\n\t"
        "ds_read_b64_tr_b16 a[46:47], v114 offset:6144\n\t"
        "s_waitcnt lgkmcnt(8)\n\t"
        "ds_read_b64_tr_b16 v[56:57], v115 offset:0\n\t"
        "ds_read_b64_tr_b16 v[58:59], v115 offset:2048\n\t"
        "ds_read_b64_tr_b16 v[60:61], v115 offset:4096\n\t"
        "ds_read_b64_tr_b16 v[62:63], v115 offset:6144\n\t"
        "s_add_u32 %[t1], %[ro], 1\n\t"
        "s_and_b32 %[t1], %[t1], 7\n\t"
        "s_lshl_b32 %[t1], %[t1], 18\n\t"
        "s_add_u32 %[t0], %[t1], 131072\n\t"
        "buffer_load_dwordx4 a[72:75], v110, %[so], %[t0] offen\n\t"
        "s_sleep 2\n\t"
        "s_add_u32 %[t0], %[t1], 147456\n\t"
        "buffer_load_dwordx4 a[76:79], v110, %[so], %[t0] offen\n\t"
        "s_sleep 2\n\t"
        "s_add_u32 %[t0], %[t1], 163840\n\t"
        "buffer_load_dwordx4 a[80:83], v110, %[so], %[t0] offen\n\t"
        "s_sleep 2\n\t"
        "s_add_u32 %[t0], %[t1], 180224\n\t"
        "buffer_load_dwordx4 a[84:87], v110, %[so], %[t0] offen\n\t"
        "s_sleep 2\n\t"
        "s_add_u32 %[t0], %[t1], 196608\n\t"
        "buffer_load_dwordx4 a[88:91], v110, %[so], %[t0] offen\n\t"
        "s_sleep 2\n\t"
        "s_add_u32 %[t0], %[t1], 212992\n\t"
        "buffer_load_dwordx4 a[92:95], v110, %[so], %[t0] offen\n\t"
        "s_sleep 2\n\t"
        "s_add_u32 %[t0], %[t1], 229376\n\t"
        "buffer_load_dwordx4 v[48:51], v110, %[so], %[t0] offen\n\t"
        "s_sleep 2\n\t"
        "s_add_u32 %[t0], %[t1], 245760\n\t"
        "buffer_load_dwordx4 v[52:55], v110, %[so], %[t0] offen\n\t"
        "s_sleep 2\n\t"
        "s_waitcnt lgkmcnt(0)\n\t"
        "ds_write_b128 v116, v[56:59] offset:2048\n\t"
        "ds_write_b128 v116, v[60:63] offset:3072\n\t"
        "s_waitcnt vmcnt(8)\n\t"
        "ds_write_b128 v111, a[48:51] offset:0\n\t"
        "ds_write_b128 v111, a[52:55] offset:1024\n\t"
        "ds_write_b128 v111, a[56:59] offset:2048\n\t"
        "ds_write_b128 v111, a[60:63] offset:3072\n\t"
        "ds_write_b128 v111, a[64:67] offset:4096\n\t"
        "ds_write_b128 v111, a[68:71] offset:5120\n\t"
        "ds_write_b128 v111, v[40:43] offset:6144\n\t"
        "ds_write_b128 v111, v[44:47] offset:7168\n\t"
        "ds_read_b64_tr_b16 a[48:49], v112 offset:0\n\t"
        "ds_read_b64_tr_b16 a[50:51], v112 offset:2048\n\t"
        "ds_read_b64_tr_b16 a[60:61], v112 offset:4096\n\t"
        "ds_read_b64_tr_b16 a[62:63], v112 offset:6144\n\t"
        "s_waitcnt lgkmcnt(8)\n\t"
        "ds_read_b64_tr_b16 a[52:53], v113 offset:0\n\t"
        "ds_read_b64_tr_b16 a[54:55], v113 offset:2048\n\t"
        "ds_read_b64_tr_b16 a[64:65], v113 offset:4096\n\t"
        "ds_read_b64_tr_b16 a[66:67], v113 offset:6144\n\t"
        "s_waitcnt lgkmcnt(8)\n\t"
        "ds_read_b64_tr_b16 a[56:57], v114 offset:0\n\t"
        "ds_read_b64_tr_b16 a[58:59], v114 offset:2048\n\t"
        "ds_read_b64_tr_b16 a[68:69], v114 offset:4096\n\t"
        "ds_read_b64_tr_b16 a[70:71], v114 offset:6144\n\t"
        "s_waitcnt lgkmcnt(8)\n\t"
        "ds_read_b64_tr_b16 v[56:57], v115 offset:0\n\t"
        "ds_read_b64_tr_b16 v[58:59], v115 offset:2048\n\t"
        "ds_read_b64_tr_b16 v[60:61], v115 offset:4096\n\t"
        "ds_read_b64_tr_b16 v[62:63], v115 offset:6144\n\t"
        "s_add_u32 %[t1], %[ro], 2\n\t"
        "s_and_b32 %[t1], %[t1], 7\n\t"
        "s_lshl_b32 %[t1], %[t1], 18\n\t"
        "s_add_u32 %[t0], %[t1], 0\n\t"
        "buffer_load_dwordx4 a[96:99], v110, %[so], %[t0] offen\n\t"
        "s_sleep 2\n\t"
        "s_add_u32 %[t0], %[t1], 16384\n\t"
        "buffer_load_dwordx4 a[100:103], v110, %[so], %[t0] offen\n\t"
        "s_sleep 2\n\t"
        "s_add_u32 %[t0], %[t1], 32768\n\t"
        "buffer_load_dwordx4 a[104:107], v110, %[so], %[t0] offen\n\t"
        "s_sleep 2\n\t"
        "s_add_u32 %[t0], %[t1], 49152\n\t"
        "buffer_load_dwordx4 a[108:111], v110, %[so], %[t0] offen\n\t"
        "s_sleep 2\n\t"
        "s_add_u32 %[t0], %[t1], 65536\n\t"
        "buffer_load_dwordx4 a[112:115], v110, %[so], %[t0] offen\n\t"
        "s_sleep 2\n\t"
        "s_add_u32 %[t0], %[t1], 81920\n\t"
        "buffer_load_dwordx4 a[116:119], v110, %[so], %[t0] offen\n\t"
        "s_sleep 2\n\t"
        "s_add_u32 %[t0], %[t1], 98304\n\t"
        "buffer_load_dwordx4 v[24:27], v110, %[so], %[t0] offen\n\t"
        "s_sleep 2\n\t"
        "s_add_u32 %[t0], %[t1], 114688\n\t"
        "buffer_load_dwordx4 v[28:31], v110, %[so], %[t0] offen\n\t"
        "s_sleep 2\n\t"
        "s_waitcnt lgkmcnt(0)\n\t"
        "ds_write_b128 v116, v[56:59] offset:4096\n\t"
        "ds_write_b128 v116, v[60:63] offset:5120\n\t"
        "s_waitcnt vmcnt(8)\n\t"
        "ds_write_b128 v111, a[72:75] offset:0\n\t"
        "ds_write_b128 v111, a[76:79] offset:1024\n\t"
        "ds_write_b128 v111, a[80:83] offset:2048\n\t"
        "ds_write_b128 v111, a[84:87] offset:3072\n\t"
        "ds_write_b128 v111, a[88:91] offset:4096\n\t"
        "ds_write_b128 v111, a[92:95] offset:5120\n\t"
        "ds_write_b128 v111, v[48:51] offset:6144\n\t"
        "ds_write_b128 v111, v[52:55] offset:7168\n\t"
        "ds_read_b64_tr_b16 a[72:73], v112 offset:0\n\t"
        "ds_read_b64_tr_b16 a[74:75], v112 offset:2048\n\t"
        "ds_read_b64_tr_b16 a[84:85], v112 offset:4096\n\t"
        "ds_read_b64_tr_b16 a[86:87], v112 offset:6144\n\t"
        "s_waitcnt lgkmcnt(8)\n\t"
        "ds_read_b64_tr_b16 a[76:77], v113 offset:0\n\t"
        "ds_read_b64_tr_b16 a[78:79], v113 offset:2048\n\t"
        "ds_read_b64_tr_b16 a[88:89], v113 offset:4096\n\t"
        "ds_read_b64_tr_b16 a[90:91], v113 offset:6144\n\t"
        "s_waitcnt lgkmcnt(8)\n\t"
        "ds_read_b64_tr_b16 a[80:81], v114 offset:0\n\t"
        "ds_read_b64_tr_b16 a[82:83], v114 offset:2048\n\t"
        "ds_read_b64_tr_b16 a[92:93], v114 offset:4096\n\t"
        "ds_read_b64_tr_b16 a[94:95], v114 offset:6144\n\t"
        "s_waitcnt lgkmcnt(8)\n\t"
        "ds_read_b64_tr_b16 v[56:57], v115 offset:0\n\t"
        "ds_read_b64_tr_b16 v[58:59], v115 offset:2048\n\t"
        "ds_read_b64_tr_b16 v[60:61], v115 offset:4096\n\t"
        "ds_read_b64_tr_b16 v[62:63], v115 offset:6144\n\t"
        "s_add_u32 %[t1], %[ro], 2\n\t"
        "s_and_b32 %[t1], %[t1], 7\n\t"
        "s_lshl_b32 %[t1], %[t1], 18\n\t"
        "s_add_u32 %[t0], %[t1], 131072\n\t"
        "buffer_load_dwordx4 a[120:123], v110, %[so], %[t0] offen\n\t"
        "s_sleep 2\n\t"
        "s_add_u32 %[t0], %[t1], 147456\n\t"
        "buffer_load_dwordx4 a[124:127], v110, %[so], %[t0] offen\n\t"
        "s_sleep 2\n\t"
        "s_add_u32 %[t0], %[t1], 163840\n\t"
        "buffer_load_dwordx4 a[128:131], v110, %[so], %[t0] offen\n\t"
        "s_sleep 2\n\t"
        "s_add_u32 %[t0], %[t1], 180224\n\t"
        "buffer_load_dwordx4 a[132:135], v110, %[so], %[t0] offen\n\t"
        "s_sleep 2\n\t"
        "s_add_u32 %[t0], %[t1], 196608\n\t"
        "buffer_load_dwordx4 a[136:139], v110, %[so], %[t0] offen\n\t"
        "s_sleep 2\n\t"
        "s_add_u32 %[t0], %[t1], 212992\n\t"
        "buffer_load_dwordx4 a[140:143], v110, %[so], %[t0] offen\n\t"
        "s_sleep 2\n\t"
        "s_add_u32 %[t0], %[t1], 229376\n\t"
        "buffer_load_dwordx4 v[32:35], v110, %[so], %[t0] offen\n\t"
        "s_sleep 2\n\t"
        "s_add_u32 %[t0], %[t1], 245760\n\t"
        "buffer_load_dwordx4 v[36:39], v110, %[so], %[t0] offen\n\t"
        "s_sleep 2\n\t"
        "s_waitcnt lgkmcnt(0)\n\t"
        "ds_write_b128 v116, v[56:59] offset:6144\n\t"
        "ds_write_b128 v116, v[60:63] offset:7168\n\t"
        "s_waitcnt vmcnt(8)\n\t"
        "ds_write_b128 v111, a[96:99] offset:0\n\t"
        "ds_write_b128 v111, a[100:103] offset:1024\n\t"
        "ds_write_b128 v111, a[104:107] offset:2048\n\t"
        "ds_write_b128 v111, a[108:111] offset:3072\n\t"
        "ds_write_b128 v111, a[112:115] offset:4096\n\t"
        "ds_write_b128 v111, a[116:119] offset:5120\n\t"
        "ds_write_b128 v111, v[24:27] offset:6144\n\t"
        "ds_write_b128 v111, v[28:31] offset:7168\n\t"
        "ds_read_b64_tr_b16 a[96:97], v112 offset:0\n\t"
        "ds_read_b64_tr_b16 a[98:99], v112 offset:2048\n\t"
        "ds_read_b64_tr_b16 a[108:109], v112 offset:4096\n\t"
        "ds_read_b64_tr_b16 a[110:111], v112 offset:6144\n\t"
        "s_waitcnt lgkmcnt(8)\n\t"
        "ds_read_b64_tr_b16 a[100:101], v113 offset:0\n\t"
        "ds_read_b64_tr_b16 a[102:103], v113 offset:2048\n\t"
        "ds_read_b64_tr_b16 a[112:113], v113 offset:4096\n\t"
        "ds_read_b64_tr_b16 a[114:115], v113 offset:6144\n\t"
        "s_waitcnt lgkmcnt(8)\n\t"
        "ds_read_b64_tr_b16 a[104:105], v114 offset:0\n\t"
        "ds_read_b64_tr_b16 a[106:107], v114 offset:2048\n\t"
        "ds_read_b64_tr_b16 a[116:117], v114 offset:4096\n\t"
        "ds_read_b64_tr_b16 a[118:119], v114 offset:6144\n\t"
        "s_waitcnt lgkmcnt(8)\n\t"
        "ds_read_b64_tr_b16 v[56:57], v115 offset:0\n\t"
        "ds_read_b64_tr_b16 v[58:59], v115 offset:2048\n\t"
        "ds_read_b64_tr_b16 v[60:61], v115 offset:4096\n\t"
        "ds_read_b64_tr_b16 v[62:63], v115 offset:6144\n\t"
        "s_add_u32 %[t1], %[ro], 3\n\t"
        "s_and_b32 %[t1], %[t1], 7\n\t"
        "s_lshl_b32 %[t1], %[t1], 18\n\t"
        "s_add_u32 %[t0], %[t1], 0\n\t"
        "buffer_load_dwordx4 a[144:147], v110, %[so], %[t0] offen\n\t"
        "s_sleep 2\n\t"
        "s_add_u32 %[t0], %[t1], 16384\n\t"
        "buffer_load_dwordx4 a[148:151], v110, %[so], %[t0] offen\n\t"
        "s_sleep 2\n\t"
        "s_add_u32 %[t0], %[t1], 32768\n\t"
        "buffer_load_dwordx4 a[152:155], v110, %[so], %[t0] offen\n\t"
        "s_sleep 2\n\t"
        "s_add_u32 %[t0], %[t1], 49152\n\t"
        "buffer_load_dwordx4 a[156:159], v110, %[so], %[t0] offen\n\t"
        "s_sleep 2\n\t"
        "s_add_u32 %[t0], %[t1], 65536\n\t"
        "buffer_load_dwordx4 a[160:163], v110, %[so], %[t0] offen\n\t"
        "s_sleep 2\n\t"
        "s_add_u32 %[t0], %[t1], 81920\n\t"
        "buffer_load_dwordx4 a[164:167], v110, %[so], %[t0] offen\n\t"
        "s_sleep 2\n\t"
        "s_add_u32 %[t0], %[t1], 98304\n\t"
        "buffer_load_dwordx4 v[40:43], v110, %[so], %[t0] offen\n\t"
        "s_sleep 2\n\t"
        "s_add_u32 %[t0], %[t1], 114688\n\t"
        "buffer_load_dwordx4 v[44:47], v110, %[so], %[t0] offen\n\t"
        "s_sleep 2\n\t"
        "s_waitcnt lgkmcnt(0)\n\t"
        "ds_write_b128 v116, v[56:59] offset:8192\n\t"
        "ds_write_b128 v116, v[60:63] offset:9216\n\t"
        "s_waitcnt vmcnt(8)\n\t"
        "ds_write_b128 v111, a[120:123] offset:0\n\t"
        "ds_write_b128 v111, a[124:127] offset:1024\n\t"
        "ds_write_b128 v111, a[128:131] offset:2048\n\t"
        "ds_write_b128 v111, a[132:135] offset:3072\n\t"
        "ds_write_b128 v111, a[136:139] offset:4096\n\t"
        "ds_write_b128 v111, a[140:143] offset:5120\n\t"
        "ds_write_b128 v111, v[32:35] offset:6144\n\t"
        "ds_write_b128 v111, v[36:39] offset:7168\n\t"
        "ds_read_b64_tr_b16 a[120:121], v112 offset:0\n\t"
        "ds_read_b64_tr_b16 a[122:123], v112 offset:2048\n\t"
        "ds_read_b64_tr_b16 a[132:133], v112 offset:4096\n\t"
        "ds_read_b64_tr_b16 a[134:135], v112 offset:6144\n\t"
        "s_waitcnt lgkmcnt(8)\n\t"
        "ds_read_b64_tr_b16 a[124:125], v113 offset:0\n\t"
        "ds_read_b64_tr_b16 a[126:127], v113 offset:2048\n\t"
        "ds_read_b64_tr_b16 a[136:137], v113 offset:4096\n\t"
        "ds_read_b64_tr_b16 a[138:139], v113 offset:6144\n\t"
        "s_waitcnt lgkmcnt(8)\n\t"
        "ds_read_b64_tr_b16 a[128:129], v114 offset:0\n\t"
        "ds_read_b64_tr_b16 a[130:131], v114 offset:2048\n\t"
        "ds_read_b64_tr_b16 a[140:141], v114 offset:4096\n\t"
        "ds_read_b64_tr_b16 a[142:143], v114 offset:6144\n\t"
        "s_waitcnt lgkmcnt(8)\n\t"
        "ds_read_b64_tr_b16 v[56:57], v115 offset:0\n\t"
        "ds_read_b64_tr_b16 v[58:59], v115 offset:2048\n\t"
        "ds_read_b64_tr_b16 v[60:61], v115 offset:4096\n\t"
        "ds_read_b64_tr_b16 v[62:63], v115 offset:6144\n\t"
        "s_add_u32 %[t1], %[ro], 3\n\t"
        "s_and_b32 %[t1], %[t1], 7\n\t"
        "s_lshl_b32 %[t1], %[t1], 18\n\t"
        "s_add_u32 %[t0], %[t1], 131072\n\t"
        "buffer_load_dwordx4 a[168:171], v110, %[so], %[t0] offen\n\t"
        "s_sleep 2\n\t"
        "s_add_u32 %[t0], %[t1], 147456\n\t"
        "buffer_load_dwordx4 a[172:175], v110, %[so], %[t0] offen\n\t"
        "s_sleep 2\n\t"
        "s_add_u32 %[t0], %[t1], 163840\n\t"
        "buffer_load_dwordx4 a[176:179], v110, %[so], %[t0] offen\n\t"
        "s_sleep 2\n\t"
        "s_add_u32 %[t0], %[t1], 180224\n\t"
        "buffer_load_dwordx4 a[180:183], v110, %[so], %[t0] offen\n\t"
        "s_sleep 2\n\t"
        "s_add_u32 %[t0], %[t1], 196608\n\t"
        "buffer_load_dwordx4 a[184:187], v110, %[so], %[t0] offen\n\t"
        "s_sleep 2\n\t"
        "s_add_u32 %[t0], %[t1], 212992\n\t"
        "buffer_load_dwordx4 a[188:191], v110, %[so], %[t0] offen\n\t"
        "s_sleep 2\n\t"
        "s_add_u32 %[t0], %[t1], 229376\n\t"
        "buffer_load_dwordx4 v[48:51], v110, %[so], %[t0] offen\n\t"
        "s_sleep 2\n\t"
        "s_add_u32 %[t0], %[t1], 245760\n\t"
        "buffer_load_dwordx4 v[52:55], v110, %[so], %[t0] offen\n\t"
        "s_sleep 2\n\t"
        "s_waitcnt lgkmcnt(0)\n\t"
        "ds_write_b128 v116, v[56:59] offset:10240\n\t"
        "ds_write_b128 v116, v[60:63] offset:11264\n\t"
        "s_waitcnt vmcnt(8)\n\t"
        "ds_write_b128 v111, a[144:147] offset:0\n\t"
        "ds_write_b128 v111, a[148:151] offset:1024\n\t"
        "ds_write_b128 v111, a[152:155] offset:2048\n\t"
        "ds_write_b128 v111, a[156:159] offset:3072\n\t"
        "ds_write_b128 v111, a[160:163] offset:4096\n\t"
        "ds_write_b128 v111, a[164:167] offset:5120\n\t"
        "ds_write_b128 v111, v[40:43] offset:6144\n\t"
        "ds_write_b128 v111, v[44:47] offset:7168\n\t"
        "ds_read_b64_tr_b16 a[144:145], v112 offset:0\n\t"
        "ds_read_b64_tr_b16 a[146:147], v112 offset:2048\n\t"
        "ds_read_b64_tr_b16 a[156:157], v112 offset:4096\n\t"
        "ds_read_b64_tr_b16 a[158:159], v112 offset:6144\n\t"
        "s_waitcnt lgkmcnt(8)\n\t"
        "ds_read_b64_tr_b16 a[148:149], v113 offset:0\n\t"
        "ds_read_b64_tr_b16 a[150:151], v113 offset:2048\n\t"
        "ds_read_b64_tr_b16 a[160:161], v113 offset:4096\n\t"
        "ds_read_b64_tr_b16 a[162:163], v113 offset:6144\n\t"
        "s_waitcnt lgkmcnt(8)\n\t"
        "ds_read_b64_tr_b16 a[152:153], v114 offset:0\n\t"
        "ds_read_b64_tr_b16 a[154:155], v114 offset:2048\n\t"
        "ds_read_b64_tr_b16 a[164:165], v114 offset:4096\n\t"
        "ds_read_b64_tr_b16 a[166:167], v114 offset:6144\n\t"
        "s_waitcnt lgkmcnt(8)\n\t"
        "ds_read_b64_tr_b16 v[56:57], v115 offset:0\n\t"
        "ds_read_b64_tr_b16 v[58:59], v115 offset:2048\n\t"
        "ds_read_b64_tr_b16 v[60:61], v115 offset:4096\n\t"
        "ds_read_b64_tr_b16 v[62:63], v115 offset:6144\n\t"
        "s_add_u32 %[t1], %[ro], 4\n\t"
        "s_and_b32 %[t1], %[t1], 7\n\t"
        "s_lshl_b32 %[t1], %[t1], 18\n\t"
        "s_add_u32 %[t0], %[t1], 0\n\t"
        "buffer_load_dwordx4 a[192:195], v110, %[so], %[t0] offen\n\t"
        "s_sleep 2\n\t"
        "s_add_u32 %[t0], %[t1], 16384\n\t"
        "buffer_load_dwordx4 a[196:199], v110, %[so], %[t0] offen\n\t"
        "s_sleep 2\n\t"
        "s_add_u32 %[t0], %[t1], 32768\n\t"
        "buffer_load_dwordx4 a[200:203], v110, %[so], %[t0] offen\n\t"
        "s_sleep 2\n\t"
        "s_add_u32 %[t0], %[t1], 49152\n\t"
        "buffer_load_dwordx4 a[204:207], v110, %[so], %[t0] offen\n\t"
        "s_sleep 2\n\t"
        "s_add_u32 %[t0], %[t1], 65536\n\t"
        "buffer_load_dwordx4 a[208:211], v110, %[so], %[t0] offen\n\t"
        "s_sleep 2\n\t"
        "s_add_u32 %[t0], %[t1], 81920\n\t"
        "buffer_load_dwordx4 a[212:215], v110, %[so], %[t0] offen\n\t"
        "s_sleep 2\n\t"
        "s_add_u32 %[t0], %[t1], 98304\n\t"
        "buffer_load_dwordx4 v[24:27], v110, %[so], %[t0] offen\n\t"
        "s_sleep 2\n\t"
        "s_add_u32 %[t0], %[t1], 114688\n\t"
        "buffer_load_dwordx4 v[28:31], v110, %[so], %[t0] offen\n\t"
        "s_sleep 2\n\t"
        "s_waitcnt lgkmcnt(0)\n\t"
        "ds_write_b128 v116, v[56:59] offset:12288\n\t"
        "ds_write_b128 v116, v[60:63] offset:13312\n\t"
        "s_waitcnt vmcnt(8)\n\t"
        "ds_write_b128 v111, a[168:171] offset:0\n\t"
        "ds_write_b128 v111, a[172:175] offset:1024\n\t"
        "ds_write_b128 v111, a[176:179] offset:2048\n\t"
        "ds_write_b128 v111, a[180:183] offset:3072\n\t"
        "ds_write_b128 v111, a[184:187] offset:4096\n\t"
        "ds_write_b128 v111, a[188:191] offset:5120\n\t"
        "ds_write_b128 v111, v[48:51] offset:6144\n\t"
        "ds_write_b128 v111, v[52:55] offset:7168\n\t"
        "ds_read_b64_tr_b16 a[168:169], v112 offset:0\n\t"
        "ds_read_b64_tr_b16 a[170:171], v112 offset:2048\n\t"
        "ds_read_b64_tr_b16 a[180:181], v112 offset:4096\n\t"
        "ds_read_b64_tr_b16 a[182:183], v112 offset:6144\n\t"
        "s_waitcnt lgkmcnt(8)\n\t"
        "ds_read_b64_tr_b16 a[172:173], v113 offset:0\n\t"
        "ds_read_b64_tr_b16 a[174:175], v113 offset:2048\n\t"
        "ds_read_b64_tr_b16 a[184:185], v113 offset:4096\n\t"
        "ds_read_b64_tr_b16 a[186:187], v113 offset:6144\n\t"
        "s_waitcnt lgkmcnt(8)\n\t"
        "ds_read_b64_tr_b16 a[176:177], v114 offset:0\n\t"
        "ds_read_b64_tr_b16 a[178:179], v114 offset:2048\n\t"
        "ds_read_b64_tr_b16 a[188:189], v114 offset:4096\n\t"
        "ds_read_b64_tr_b16 a[190:191], v114 offset:6144\n\t"
        "s_waitcnt lgkmcnt(8)\n\t"
        "ds_read_b64_tr_b16 v[56:57], v115 offset:0\n\t"
        "ds_read_b64_tr_b16 v[58:59], v115 offset:2048\n\t"
        "ds_read_b64_tr_b16 v[60:61], v115 offset:4096\n\t"
        "ds_read_b64_tr_b16 v[62:63], v115 offset:6144\n\t"
        "s_add_u32 %[t1], %[ro], 4\n\t"
        "s_and_b32 %[t1], %[t1], 7\n\t"
        "s_lshl_b32 %[t1], %[t1], 18\n\t"
        "s_add_u32 %[t0], %[t1], 131072\n\t"
        "buffer_load_dwordx4 a[216:219], v110, %[so], %[t0] offen\n\t"
        "s_sleep 2\n\t"
        "s_add_u32 %[t0], %[t1], 147456\n\t"
        "buffer_load_dwordx4 a[220:223], v110, %[so], %[t0] offen\n\t"
        "s_sleep 2\n\t"
        "s_add_u32 %[t0], %[t1], 163840\n\t"
        "buffer_load_dwordx4 a[224:227], v110, %[so], %[t0] offen\n\t"
        "s_sleep 2\n\t"
        "s_add_u32 %[t0], %[t1], 180224\n\t"
        "buffer_load_dwordx4 a[228:231], v110, %[so], %[t0] offen\n\t"
        "s_sleep 2\n\t"
        "s_add_u32 %[t0], %[t1], 196608\n\t"
        "buffer_load_dwordx4 a[232:235], v110, %[so], %[t0] offen\n\t"
        "s_sleep 2\n\t"
        "s_add_u32 %[t0], %[t1], 212992\n\t"
        "buffer_load_dwordx4 a[236:239], v110, %[so], %[t0] offen\n\t"
        "s_sleep 2\n\t"
        "s_add_u32 %[t0], %[t1], 229376\n\t"
        "buffer_load_dwordx4 v[32:35], v110, %[so], %[t0] offen\n\t"
        "s_sleep 2\n\t"
        "s_add_u32 %[t0], %[t1], 245760\n\t"
        "buffer_load_dwordx4 v[36:39], v110, %[so], %[t0] offen\n\t"
        "s_sleep 2\n\t"
        "s_waitcnt lgkmcnt(0)\n\t"
        "ds_write_b128 v116, v[56:59] offset:14336\n\t"
        "ds_write_b128 v116, v[60:63] offset:15360\n\t"
        "s_waitcnt vmcnt(8)\n\t"
        "ds_write_b128 v111, a[192:195] offset:0\n\t"
        "ds_write_b128 v111, a[196:199] offset:1024\n\t"
        "ds_write_b128 v111, a[200:203] offset:2048\n\t"
        "ds_write_b128 v111, a[204:207] offset:3072\n\t"
        "ds_write_b128 v111, a[208:211] offset:4096\n\t"
        "ds_write_b128 v111, a[212:215] offset:5120\n\t"
        "ds_write_b128 v111, v[24:27] offset:6144\n\t"
        "ds_write_b128 v111, v[28:31] offset:7168\n\t"
        "ds_read_b64_tr_b16 a[192:193], v112 offset:0\n\t"
        "ds_read_b64_tr_b16 a[194:195], v112 offset:2048\n\t"
        "ds_read_b64_tr_b16 a[204:205], v112 offset:4096\n\t"
        "ds_read_b64_tr_b16 a[206:207], v112 offset:6144\n\t"
        "s_waitcnt lgkmcnt(8)\n\t"
        "ds_read_b64_tr_b16 a[196:197], v113 offset:0\n\t"
        "ds_read_b64_tr_b16 a[198:199], v113 offset:2048\n\t"
        "ds_read_b64_tr_b16 a[208:209], v113 offset:4096\n\t"
        "ds_read_b64_tr_b16 a[210:211], v113 offset:6144\n\t"
        "s_waitcnt lgkmcnt(8)\n\t"
        "ds_read_b64_tr_b16 a[200:201], v114 offset:0\n\t"
        "ds_read_b64_tr_b16 a[202:203], v114 offset:2048\n\t"
        "ds_read_b64_tr_b16 a[212:213], v114 offset:4096\n\t"
        "ds_read_b64_tr_b16 a[214:215], v114 offset:6144\n\t"
        "s_waitcnt lgkmcnt(8)\n\t"
        "ds_read_b64_tr_b16 v[56:57], v115 offset:0\n\t"
        "ds_read_b64_tr_b16 v[58:59], v115 offset:2048\n\t"
        "ds_read_b64_tr_b16 v[60:61], v115 offset:4096\n\t"
        "ds_read_b64_tr_b16 v[62:63], v115 offset:6144\n\t"
        "s_add_u32 %[t1], %[ro], 5\n\t"
        "s_and_b32 %[t1], %[t1], 7\n\t"
        "s_lshl_b32 %[t1], %[t1], 18\n\t"
        "s_add_u32 %[t0], %[t1], 0\n\t"
        "buffer_load_dwordx4 a[240:243], v110, %[so], %[t0] offen\n\t"
        "s_sleep 2\n\t"
        "s_add_u32 %[t0], %[t1], 16384\n\t"
        "buffer_load_dwordx4 a[244:247], v110, %[so], %[t0] offen\n\t"
        "s_sleep 2\n\t"
        "s_add_u32 %[t0], %[t1], 32768\n\t"
        "buffer_load_dwordx4 a[248:251], v110, %[so], %[t0] offen\n\t"
        "s_sleep 2\n\t"
        "s_add_u32 %[t0], %[t1], 49152\n\t"
        "buffer_load_dwordx4 a[252:255], v110, %[so], %[t0] offen\n\t"
        "s_sleep 2\n\t"
        "s_add_u32 %[t0], %[t1], 65536\n\t"
        "buffer_load_dwordx4 v[128:131], v110, %[so], %[t0] offen\n\t"
        "s_sleep 2\n\t"
        "s_add_u32 %[t0], %[t1], 81920\n\t"
        "buffer_load_dwordx4 v[132:135], v110, %[so], %[t0] offen\n\t"
        "s_sleep 2\n\t"
        "s_add_u32 %[t0], %[t1], 98304\n\t"
        "buffer_load_dwordx4 v[40:43], v110, %[so], %[t0] offen\n\t"
        "s_sleep 2\n\t"
        "s_add_u32 %[t0], %[t1], 114688\n\t"
        "buffer_load_dwordx4 v[44:47], v110, %[so], %[t0] offen\n\t"
        "s_sleep 2\n\t"
        "s_waitcnt lgkmcnt(0)\n\t"
        "ds_write_b128 v116, v[56:59] offset:16384\n\t"
        "ds_write_b128 v116, v[60:63] offset:17408\n\t"
        "s_waitcnt vmcnt(8)\n\t"
        "ds_write_b128 v111, a[216:219] offset:0\n\t"
        "ds_write_b128 v111, a[220:223] offset:1024\n\t"
        "ds_write_b128 v111, a[224:227] offset:2048\n\t"
        "ds_write_b128 v111, a[228:231] offset:3072\n\t"
        "ds_write_b128 v111, a[232:235] offset:4096\n\t"
        "ds_write_b128 v111, a[236:239] offset:5120\n\t"
        "ds_write_b128 v111, v[32:35] offset:6144\n\t"
        "ds_write_b128 v111, v[36:39] offset:7168\n\t"
        "ds_read_b64_tr_b16 a[216:217], v112 offset:0\n\t"
        "ds_read_b64_tr_b16 a[218:219], v112 offset:2048\n\t"
        "ds_read_b64_tr_b16 a[228:229], v112 offset:4096\n\t"
        "ds_read_b64_tr_b16 a[230:231], v112 offset:6144\n\t"
        "s_waitcnt lgkmcnt(8)\n\t"
        "ds_read_b64_tr_b16 a[220:221], v113 offset:0\n\t"
        "ds_read_b64_tr_b16 a[222:223], v113 offset:2048\n\t"
        "ds_read_b64_tr_b16 a[232:233], v113 offset:4096\n\t"
        "ds_read_b64_tr_b16 a[234:235], v113 offset:6144\n\t"
        "s_waitcnt lgkmcnt(8)\n\t"
        "ds_read_b64_tr_b16 a[224:225], v114 offset:0\n\t"
        "ds_read_b64_tr_b16 a[226:227], v114 offset:2048\n\t"
        "ds_read_b64_tr_b16 a[236:237], v114 offset:4096\n\t"
        "ds_read_b64_tr_b16 a[238:239], v114 offset:6144\n\t"
        "s_waitcnt lgkmcnt(8)\n\t"
        "ds_read_b64_tr_b16 v[56:57], v115 offset:0\n\t"
        "ds_read_b64_tr_b16 v[58:59], v115 offset:2048\n\t"
        "ds_read_b64_tr_b16 v[60:61], v115 offset:4096\n\t"
        "ds_read_b64_tr_b16 v[62:63], v115 offset:6144\n\t"
        "s_add_u32 %[t1], %[ro], 5\n\t"
        "s_and_b32 %[t1], %[t1], 7\n\t"
        "s_lshl_b32 %[t1], %[t1], 18\n\t"
        "s_add_u32 %[t0], %[t1], 131072\n\t"
        "buffer_load_dwordx4 v[136:139], v110, %[so], %[t0] offen\n\t"
        "s_sleep 2\n\t"
        "s_add_u32 %[t0], %[t1], 147456\n\t"
        "buffer_load_dwordx4 v[140:143], v110, %[so], %[t0] offen\n\t"
        "s_sleep 2\n\t"
        "s_add_u32 %[t0], %[t1], 163840\n\t"
        "buffer_load_dwordx4 v[144:147], v110, %[so], %[t0] offen\n\t"
        "s_sleep 2\n\t"
        "s_add_u32 %[t0], %[t1], 180224\n\t"
        "buffer_load_dwordx4 v[148:151], v110, %[so], %[t0] offen\n\t"
        "s_sleep 2\n\t"
        "s_add_u32 %[t0], %[t1], 196608\n\t"
        "buffer_load_dwordx4 v[152:155], v110, %[so], %[t0] offen\n\t"
        "s_sleep 2\n\t"
        "s_add_u32 %[t0], %[t1], 212992\n\t"
        "buffer_load_dwordx4 v[156:159], v110, %[so], %[t0] offen\n\t"
        "s_sleep 2\n\t"
        "s_add_u32 %[t0], %[t1], 229376\n\t"
        "buffer_load_dwordx4 v[48:51], v110, %[so], %[t0] offen\n\t"
        "s_sleep 2\n\t"
        "s_add_u32 %[t0], %[t1], 245760\n\t"
        "buffer_load_dwordx4 v[52:55], v110, %[so], %[t0] offen\n\t"
        "s_sleep 2\n\t"
        "s_waitcnt lgkmcnt(0)\n\t"
        "ds_write_b128 v116, v[56:59] offset:18432\n\t"
        "ds_write_b128 v116, v[60:63] offset:19456\n\t"
        "s_waitcnt vmcnt(8)\n\t"
        "ds_write_b128 v111, a[240:243] offset:0\n\t"
        "ds_write_b128 v111, a[244:247] offset:1024\n\t"
        "ds_write_b128 v111, a[248:251] offset:2048\n\t"
        "ds_write_b128 v111, a[252:255] offset:3072\n\t"
        "ds_write_b128 v111, v[128:131] offset:4096\n\t"
        "ds_write_b128 v111, v[132:135] offset:5120\n\t"
        "ds_write_b128 v111, v[40:43] offset:6144\n\t"
        "ds_write_b128 v111, v[44:47] offset:7168\n\t"
        "ds_read_b64_tr_b16 a[240:241], v112 offset:0\n\t"
        "ds_read_b64_tr_b16 a[242:243], v112 offset:2048\n\t"
        "ds_read_b64_tr_b16 a[252:253], v112 offset:4096\n\t"
        "ds_read_b64_tr_b16 a[254:255], v112 offset:6144\n\t"
        "s_waitcnt lgkmcnt(8)\n\t"
        "ds_read_b64_tr_b16 a[244:245], v113 offset:0\n\t"
        "ds_read_b64_tr_b16 a[246:247], v113 offset:2048\n\t"
        "ds_read_b64_tr_b16 v[128:129], v113 offset:4096\n\t"
        "ds_read_b64_tr_b16 v[130:131], v113 offset:6144\n\t"
        "s_waitcnt lgkmcnt(8)\n\t"
        "ds_read_b64_tr_b16 a[248:249], v114 offset:0\n\t"
        "ds_read_b64_tr_b16 a[250:251], v114 offset:2048\n\t"
        "ds_read_b64_tr_b16 v[132:133], v114 offset:4096\n\t"
        "ds_read_b64_tr_b16 v[134:135], v114 offset:6144\n\t"
        "s_waitcnt lgkmcnt(8)\n\t"
        "ds_read_b64_tr_b16 v[56:57], v115 offset:0\n\t"
        "ds_read_b64_tr_b16 v[58:59], v115 offset:2048\n\t"
        "ds_read_b64_tr_b16 v[60:61], v115 offset:4096\n\t"
        "ds_read_b64_tr_b16 v[62:63], v115 offset:6144\n\t"
        "s_add_u32 %[t1], %[ro], 6\n\t"
        "s_and_b32 %[t1], %[t1], 7\n\t"
        "s_lshl_b32 %[t1], %[t1], 18\n\t"
        "s_add_u32 %[t0], %[t1], 0\n\t"
        "buffer_load_dwordx4 v[160:163], v110, %[so], %[t0] offen\n\t"
        "s_sleep 2\n\t"
        "s_add_u32 %[t0], %[t1], 16384\n\t"
        "buffer_load_dwordx4 v[164:167], v110, %[so], %[t0] offen\n\t"
        "s_sleep 2\n\t"
        "s_add_u32 %[t0], %[t1], 32768\n\t"
        "buffer_load_dwordx4 v[168:171], v110, %[so], %[t0] offen\n\t"
        "s_sleep 2\n\t"
        "s_add_u32 %[t0], %[t1], 49152\n\t"
        "buffer_load_dwordx4 v[172:175], v110, %[so], %[t0] offen\n\t"
        "s_sleep 2\n\t"
        "s_add_u32 %[t0], %[t1], 65536\n\t"
        "buffer_load_dwordx4 v[176:179], v110, %[so], %[t0] offen\n\t"
        "s_sleep 2\n\t"
        "s_add_u32 %[t0], %[t1], 81920\n\t"
        "buffer_load_dwordx4 v[180:183], v110, %[so], %[t0] offen\n\t"
        "s_sleep 2\n\t"
        "s_add_u32 %[t0], %[t1], 98304\n\t"
        "buffer_load_dwordx4 v[24:27], v110, %[so], %[t0] offen\n\t"
        "s_sleep 2\n\t"
        "s_add_u32 %[t0], %[t1], 114688\n\t"
        "buffer_load_dwordx4 v[28:31], v110, %[so], %[t0] offen\n\t"
        "s_sleep 2\n\t"
        "s_waitcnt lgkmcnt(0)\n\t"
        "ds_write_b128 v116, v[56:59] offset:20480\n\t"
        "ds_write_b128 v116, v[60:63] offset:21504\n\t"
        "s_waitcnt vmcnt(8)\n\t"
        "ds_write_b128 v111, v[136:139] offset:0\n\t"
        "ds_write_b128 v111, v[140:143] offset:1024\n\t"
        "ds_write_b128 v111, v[144:147] offset:2048\n\t"
        "ds_write_b128 v111, v[148:151] offset:3072\n\t"
        "ds_write_b128 v111, v[152:155] offset:4096\n\t"
        "ds_write_b128 v111, v[156:159] offset:5120\n\t"
        "ds_write_b128 v111, v[48:51] offset:6144\n\t"
        "ds_write_b128 v111, v[52:55] offset:7168\n\t"
        "ds_read_b64_tr_b16 v[136:137], v112 offset:0\n\t"
        "ds_read_b64_tr_b16 v[138:139], v112 offset:2048\n\t"
        "ds_read_b64_tr_b16 v[148:149], v112 offset:4096\n\t"
        "ds_read_b64_tr_b16 v[150:151], v112 offset:6144\n\t"
        "s_waitcnt lgkmcnt(8)\n\t"
        "ds_read_b64_tr_b16 v[140:141], v113 offset:0\n\t"
        "ds_read_b64_tr_b16 v[142:143], v113 offset:2048\n\t"
        "ds_read_b64_tr_b16 v[152:153], v113 offset:4096\n\t"
        "ds_read_b64_tr_b16 v[154:155], v113 offset:6144\n\t"
        "s_waitcnt lgkmcnt(8)\n\t"
        "ds_read_b64_tr_b16 v[144:145], v114 offset:0\n\t"
        "ds_read_b64_tr_b16 v[146:147], v114 offset:2048\n\t"
        "ds_read_b64_tr_b16 v[156:157], v114 offset:4096\n\t"
        "ds_read_b64_tr_b16 v[158:159], v114 offset:6144\n\t"
        "s_waitcnt lgkmcnt(8)\n\t"
        "ds_read_b64_tr_b16 v[56:57], v115 offset:0\n\t"
        "ds_read_b64_tr_b16 v[58:59], v115 offset:2048\n\t"
        "ds_read_b64_tr_b16 v[60:61], v115 offset:4096\n\t"
        "ds_read_b64_tr_b16 v[62:63], v115 offset:6144\n\t"
        "s_add_u32 %[t1], %[ro], 6\n\t"
        "s_and_b32 %[t1], %[t1], 7\n\t"
        "s_lshl_b32 %[t1], %[t1], 18\n\t"
        "s_add_u32 %[t0], %[t1], 131072\n\t"
        "buffer_load_dwordx4 v[184:187], v110, %[so], %[t0] offen\n\t"
        "s_sleep 2\n\t"
        "s_add_u32 %[t0], %[t1], 147456\n\t"
        "buffer_load_dwordx4 v[188:191], v110, %[so], %[t0] offen\n\t"
        "s_sleep 2\n\t"
        "s_add_u32 %[t0], %[t1], 163840\n\t"
        "buffer_load_dwordx4 v[192:195], v110, %[so], %[t0] offen\n\t"
        "s_sleep 2\n\t"
        "s_add_u32 %[t0], %[t1], 180224\n\t"
        "buffer_load_dwordx4 v[196:199], v110, %[so], %[t0] offen\n\t"
        "s_sleep 2\n\t"
        "s_add_u32 %[t0], %[t1], 196608\n\t"
        "buffer_load_dwordx4 v[200:203], v110, %[so], %[t0] offen\n\t"
        "s_sleep 2\n\t"
        "s_add_u32 %[t0], %[t1], 212992\n\t"
        "buffer_load_dwordx4 v[204:207], v110, %[so], %[t0] offen\n\t"
        "s_sleep 2\n\t"
        "s_add_u32 %[t0], %[t1], 229376\n\t"
        "buffer_load_dwordx4 v[32:35], v110, %[so], %[t0] offen\n\t"
        "s_sleep 2\n\t"
        "s_add_u32 %[t0], %[t1], 245760\n\t"
        "buffer_load_dwordx4 v[36:39], v110, %[so], %[t0] offen\n\t"
        "s_sleep 2\n\t"
        "s_waitcnt lgkmcnt(0)\n\t"
        "ds_write_b128 v116, v[56:59] offset:22528\n\t"
        "ds_write_b128 v116, v[60:63] offset:23552\n\t"
        "s_waitcnt vmcnt(8)\n\t"
        "ds_write_b128 v111, v[160:163] offset:0\n\t"
        "ds_write_b128 v111, v[164:167] offset:1024\n\t"
        "ds_write_b128 v111, v[168:171] offset:2048\n\t"
        "ds_write_b128 v111, v[172:175] offset:3072\n\t"
        "ds_write_b128 v111, v[176:179] offset:4096\n\t"
        "ds_write_b128 v111, v[180:183] offset:5120\n\t"
        "ds_write_b128 v111, v[24:27] offset:6144\n\t"
        "ds_write_b128 v111, v[28:31] offset:7168\n\t"
        "ds_read_b64_tr_b16 v[160:161], v112 offset:0\n\t"
        "ds_read_b64_tr_b16 v[162:163], v112 offset:2048\n\t"
        "ds_read_b64_tr_b16 v[172:173], v112 offset:4096\n\t"
        "ds_read_b64_tr_b16 v[174:175], v112 offset:6144\n\t"
        "s_waitcnt lgkmcnt(8)\n\t"
        "ds_read_b64_tr_b16 v[164:165], v113 offset:0\n\t"
        "ds_read_b64_tr_b16 v[166:167], v113 offset:2048\n\t"
        "ds_read_b64_tr_b16 v[176:177], v113 offset:4096\n\t"
        "ds_read_b64_tr_b16 v[178:179], v113 offset:6144\n\t"
        "s_waitcnt lgkmcnt(8)\n\t"
        "ds_read_b64_tr_b16 v[168:169], v114 offset:0\n\t"
        "ds_read_b64_tr_b16 v[170:171], v114 offset:2048\n\t"
        "ds_read_b64_tr_b16 v[180:181], v114 offset:4096\n\t"
        "ds_read_b64_tr_b16 v[182:183], v114 offset:6144\n\t"
        "s_waitcnt lgkmcnt(8)\n\t"
        "ds_read_b64_tr_b16 v[56:57], v115 offset:0\n\t"
        "ds_read_b64_tr_b16 v[58:59], v115 offset:2048\n\t"
        "ds_read_b64_tr_b16 v[60:61], v115 offset:4096\n\t"
        "ds_read_b64_tr_b16 v[62:63], v115 offset:6144\n\t"
        "s_add_u32 %[t1], %[ro], 7\n\t"
        "s_and_b32 %[t1], %[t1], 7\n\t"
        "s_lshl_b32 %[t1], %[t1], 18\n\t"
        "s_add_u32 %[t0], %[t1], 0\n\t"
        "buffer_load_dwordx4 v[208:211], v110, %[so], %[t0] offen\n\t"
        "s_sleep 2\n\t"
        "s_add_u32 %[t0], %[t1], 16384\n\t"
        "buffer_load_dwordx4 v[212:215], v110, %[so], %[t0] offen\n\t"
        "s_sleep 2\n\t"
        "s_add_u32 %[t0], %[t1], 32768\n\t"
        "buffer_load_dwordx4 v[216:219], v110, %[so], %[t0] offen\n\t"
        "s_sleep 2\n\t"
        "s_add_u32 %[t0], %[t1], 49152\n\t"
        "buffer_load_dwordx4 v[220:223], v110, %[so], %[t0] offen\n\t"
        "s_sleep 2\n\t"
        "s_add_u32 %[t0], %[t1], 65536\n\t"
        "buffer_load_dwordx4 v[224:227], v110, %[so], %[t0] offen\n\t"
        "s_sleep 2\n\t"
        "s_add_u32 %[t0], %[t1], 81920\n\t"
        "buffer_load_dwordx4 v[228:231], v110, %[so], %[t0] offen\n\t"
        "s_sleep 2\n\t"
        "s_add_u32 %[t0], %[t1], 98304\n\t"
        "buffer_load_dwordx4 v[40:43], v110, %[so], %[t0] offen\n\t"
        "s_sleep 2\n\t"
        "s_add_u32 %[t0], %[t1], 114688\n\t"
        "buffer_load_dwordx4 v[44:47], v110, %[so], %[t0] offen\n\t"
        "s_sleep 2\n\t"
        "s_waitcnt lgkmcnt(0)\n\t"
        "ds_write_b128 v116, v[56:59] offset:24576\n\t"
        "ds_write_b128 v116, v[60:63] offset:25600\n\t"
        "s_waitcnt vmcnt(8)\n\t"
        "ds_write_b128 v111, v[184:187] offset:0\n\t"
        "ds_write_b128 v111, v[188:191] offset:1024\n\t"
        "ds_write_b128 v111, v[192:195] offset:2048\n\t"
        "ds_write_b128 v111, v[196:199] offset:3072\n\t"
        "ds_write_b128 v111, v[200:203] offset:4096\n\t"
        "ds_write_b128 v111, v[204:207] offset:5120\n\t"
        "ds_write_b128 v111, v[32:35] offset:6144\n\t"
        "ds_write_b128 v111, v[36:39] offset:7168\n\t"
        "ds_read_b64_tr_b16 v[184:185], v112 offset:0\n\t"
        "ds_read_b64_tr_b16 v[186:187], v112 offset:2048\n\t"
        "ds_read_b64_tr_b16 v[196:197], v112 offset:4096\n\t"
        "ds_read_b64_tr_b16 v[198:199], v112 offset:6144\n\t"
        "s_waitcnt lgkmcnt(8)\n\t"
        "ds_read_b64_tr_b16 v[188:189], v113 offset:0\n\t"
        "ds_read_b64_tr_b16 v[190:191], v113 offset:2048\n\t"
        "ds_read_b64_tr_b16 v[200:201], v113 offset:4096\n\t"
        "ds_read_b64_tr_b16 v[202:203], v113 offset:6144\n\t"
        "s_waitcnt lgkmcnt(8)\n\t"
        "ds_read_b64_tr_b16 v[192:193], v114 offset:0\n\t"
        "ds_read_b64_tr_b16 v[194:195], v114 offset:2048\n\t"
        "ds_read_b64_tr_b16 v[204:205], v114 offset:4096\n\t"
        "ds_read_b64_tr_b16 v[206:207], v114 offset:6144\n\t"
        "s_waitcnt lgkmcnt(8)\n\t"
        "ds_read_b64_tr_b16 v[56:57], v115 offset:0\n\t"
        "ds_read_b64_tr_b16 v[58:59], v115 offset:2048\n\t"
        "ds_read_b64_tr_b16 v[60:61], v115 offset:4096\n\t"
        "ds_read_b64_tr_b16 v[62:63], v115 offset:6144\n\t"
        "s_add_u32 %[t1], %[ro], 7\n\t"
        "s_and_b32 %[t1], %[t1], 7\n\t"
        "s_lshl_b32 %[t1], %[t1], 18\n\t"
        "s_add_u32 %[t0], %[t1], 131072\n\t"
        "buffer_load_dwordx4 v[232:235], v110, %[so], %[t0] offen\n\t"
        "s_sleep 2\n\t"
        "s_add_u32 %[t0], %[t1], 147456\n\t"
        "buffer_load_dwordx4 v[236:239], v110, %[so], %[t0] offen\n\t"
        "s_sleep 2\n\t"
        "s_add_u32 %[t0], %[t1], 163840\n\t"
        "buffer_load_dwordx4 v[240:243], v110, %[so], %[t0] offen\n\t"
        "s_sleep 2\n\t"
        "s_add_u32 %[t0], %[t1], 180224\n\t"
        "buffer_load_dwordx4 v[244:247], v110, %[so], %[t0] offen\n\t"
        "s_sleep 2\n\t"
        "s_add_u32 %[t0], %[t1], 196608\n\t"
        "buffer_load_dwordx4 v[248:251], v110, %[so], %[t0] offen\n\t"
        "s_sleep 2\n\t"
        "s_add_u32 %[t0], %[t1], 212992\n\t"
        "buffer_load_dwordx4 v[252:255], v110, %[so], %[t0] offen\n\t"
        "s_sleep 2\n\t"
        "s_add_u32 %[t0], %[t1], 229376\n\t"
        "buffer_load_dwordx4 v[48:51], v110, %[so], %[t0] offen\n\t"
        "s_sleep 2\n\t"
        "s_add_u32 %[t0], %[t1], 245760\n\t"
        "buffer_load_dwordx4 v[52:55], v110, %[so], %[t0] offen\n\t"
        "s_sleep 2\n\t"
        "s_waitcnt lgkmcnt(0)\n\t"
        "ds_write_b128 v116, v[56:59] offset:26624\n\t"
        "ds_write_b128 v116, v[60:63] offset:27648\n\t"
        "s_waitcnt vmcnt(8)\n\t"
        "ds_write_b128 v111, v[208:211] offset:0\n\t"
        "ds_write_b128 v111, v[212:215] offset:1024\n\t"
        "ds_write_b128 v111, v[216:219] offset:2048\n\t"
        "ds_write_b128 v111, v[220:223] offset:3072\n\t"
        "ds_write_b128 v111, v[224:227] offset:4096\n\t"
        "ds_write_b128 v111, v[228:231] offset:5120\n\t"
        "ds_write_b128 v111, v[40:43] offset:6144\n\t"
        "ds_write_b128 v111, v[44:47] offset:7168\n\t"
        "ds_read_b64_tr_b16 v[208:209], v112 offset:0\n\t"
        "ds_read_b64_tr_b16 v[210:211], v112 offset:2048\n\t"
        "ds_read_b64_tr_b16 v[220:221], v112 offset:4096\n\t"
        "ds_read_b64_tr_b16 v[222:223], v112 offset:6144\n\t"
        "s_waitcnt lgkmcnt(8)\n\t"
        "ds_read_b64_tr_b16 v[212:213], v113 offset:0\n\t"
        "ds_read_b64_tr_b16 v[214:215], v113 offset:2048\n\t"
        "ds_read_b64_tr_b16 v[224:225], v113 offset:4096\n\t"
        "ds_read_b64_tr_b16 v[226:227], v113 offset:6144\n\t"
        "s_waitcnt lgkmcnt(8)\n\t"
        "ds_read_b64_tr_b16 v[216:217], v114 offset:0\n\t"
        "ds_read_b64_tr_b16 v[218:219], v114 offset:2048\n\t"
        "ds_read_b64_tr_b16 v[228:229], v114 offset:4096\n\t"
        "ds_read_b64_tr_b16 v[230:231], v114 offset:6144\n\t"
        "s_waitcnt lgkmcnt(8)\n\t"
        "ds_read_b64_tr_b16 v[56:57], v115 offset:0\n\t"
        "ds_read_b64_tr_b16 v[58:59], v115 offset:2048\n\t"
        "ds_read_b64_tr_b16 v[60:61], v115 offset:4096\n\t"
        "ds_read_b64_tr_b16 v[62:63], v115 offset:6144\n\t"
        "s_waitcnt lgkmcnt(0)\n\t"
        "ds_write_b128 v116, v[56:59] offset:28672\n\t"
        "ds_write_b128 v116, v[60:63] offset:29696\n\t"
        "s_waitcnt vmcnt(0)\n\t"
        "ds_write_b128 v111, v[232:235] offset:0\n\t"
        "ds_write_b128 v111, v[236:239] offset:1024\n\t"
        "ds_write_b128 v111, v[240:243] offset:2048\n\t"
        "ds_write_b128 v111, v[244:247] offset:3072\n\t"
        "ds_write_b128 v111, v[248:251] offset:4096\n\t"
        "ds_write_b128 v111, v[252:255] offset:5120\n\t"
        "ds_write_b128 v111, v[48:51] offset:6144\n\t"
        "ds_write_b128 v111, v[52:55] offset:7168\n\t"
        "ds_read_b64_tr_b16 v[232:233], v112 offset:0\n\t"
        "ds_read_b64_tr_b16 v[234:235], v112 offset:2048\n\t"
        "ds_read_b64_tr_b16 v[244:245], v112 offset:4096\n\t"
        "ds_read_b64_tr_b16 v[246:247], v112 offset:6144\n\t"
        "s_waitcnt lgkmcnt(8)\n\t"
        "ds_read_b64_tr_b16 v[236:237], v113 offset:0\n\t"
        "ds_read_b64_tr_b16 v[238:239], v113 offset:2048\n\t"
        "ds_read_b64_tr_b16 v[248:249], v113 offset:4096\n\t"
        "ds_read_b64_tr_b16 v[250:251], v113 offset:6144\n\t"
        "s_waitcnt lgkmcnt(8)\n\t"
        "ds_read_b64_tr_b16 v[240:241], v114 offset:0\n\t"
        "ds_read_b64_tr_b16 v[242:243], v114 offset:2048\n\t"
        "ds_read_b64_tr_b16 v[252:253], v114 offset:4096\n\t"
        "ds_read_b64_tr_b16 v[254:255], v114 offset:6144\n\t"
        "s_waitcnt lgkmcnt(8)\n\t"
        "ds_read_b64_tr_b16 v[56:57], v115 offset:0\n\t"
        "ds_read_b64_tr_b16 v[58:59], v115 offset:2048\n\t"
        "ds_read_b64_tr_b16 v[60:61], v115 offset:4096\n\t"
        "ds_read_b64_tr_b16 v[62:63], v115 offset:6144\n\t"
        "s_waitcnt lgkmcnt(0)\n\t"
        "ds_write_b128 v116, v[56:59] offset:30720\n\t"
        "ds_write_b128 v116, v[60:63] offset:31744\n\t"
        "ds_read_b128 v[88:91], v116 offset:0\n\t"
        "ds_read_b128 v[92:95], v116 offset:1024\n\t"
        "ds_read_b128 v[96:99], v116 offset:2048\n\t"
        "s_mov_b32 %[sp], 0\n\t"
        "LP_cv%=:\n\t"
        "buffer_load_dword v104, off, %[sw], %[cv] sc1\n\t"
        "s_waitcnt vmcnt(0) lgkmcnt(0)\n\t"
        "v_readfirstlane_b32 %[t0], v104\n\t"
        "s_cmp_ge_u32 %[t0], 8\n\t"
        "s_cbranch_scc1 LP_wait%=\n\t"
        "s_sleep 8\n\t"
        "s_add_u32 %[sp], %[sp], 1\n\t"
        "s_cmp_lt_u32 %[sp], 0x100000\n\t"
        "s_cbranch_scc1 LP_cv%=\n\t"
        "v_mov_b32_e32 v104, 256\n\t"
        "s_mov_b32 %[t0], 33024\n\t"
        "buffer_store_dword v104, off, %[sw], %[t0] sc1\n\t"
        "s_endpgm\n\t"
        "LP_wait%=:\n\t"
        "s_mov_b32 %[re], 0\n\t"
        "LP_again%=:\n\t"
        "s_mov_b32 %[sp], 0\n\t"
        "LP_poll%=:\n\t"
        "buffer_load_dword v103, v118, %[sw], 0 offen sc1\n\t"
        "s_lshr_b32 %[t1], %[tl], 1\n\t"
        "s_waitcnt vmcnt(0)\n\t"
        "v_min_u32_dpp v103, v103, v103 quad_perm:[1,0,3,2] row_mask:0xf bank_mask:0xf\n\t"
        "s_nop 1\n\t"
        "v_min_u32_dpp v103, v103, v103 quad_perm:[2,3,0,1] row_mask:0xf bank_mask:0xf\n\t"
        "s_nop 1\n\t"
        "v_min_u32_dpp v103, v103, v103 row_ror:4 row_mask:0xf bank_mask:0xf\n\t"
        "s_nop 0\n\t"
        "v_readfirstlane_b32 %[pg], v103\n\t"
        "s_cmp_gt_u32 %[pg], %[t1]\n\t"
        "s_cbranch_scc1 LP_have%=\n\t"
        "s_add_u32 %[t0], %[pg], %[re]\n\t"
        "s_cmp_eq_u32 %[t0], 63\n\t"
        "s_cbranch_scc0 LP_no%=\n\t"
        "s_sleep 16\n\t"
        "s_branch LP_have%=\n\t"
        "LP_no%=:\n\t"
        "s_min_u32 %[t1], %[t1], 62\n\t"
        "s_cmp_ge_u32 %[pg], %[t1]\n\t"
        "s_cbranch_scc1 LP_near%=\n\t"
        "s_sleep 24\n\t"
        "LP_near%=:\n\t"
        "s_sleep 2\n\t"
        "s_add_u32 %[sp], %[sp], 1\n\t"
        "s_cmp_lt_u32 %[sp], 0x100000\n\t"
        "s_cbranch_scc1 LP_poll%=\n\t"
        "v_mov_b32_e32 v104, 128\n\t"
        "s_mov_b32 %[t0], 33024\n\t"
        "buffer_store_dword v104, off, %[sw], %[t0] sc1\n\t"
        "s_endpgm\n\t"
        "LP_have%=:\n\t"
        "s_lshl_b32 %[nx], %[tl], 17\n\t"
        "s_add_u32 %[t1], %[t1], 4\n\t"
        "s_cmp_gt_u32 %[pg], %[t1]\n\t"
        "s_cbranch_scc1 LP_run%=\n\t"
        "s_add_u32 %[t0], %[ro], 0\n\t"
        "s_and_b32 %[t0], %[t0], 7\n\t"
        "s_lshl_b32 %[t0], %[t0], 12\n\t"
        "s_add_u32 %[t0], %[t0], %[nx]\n\t"
        "buffer_load_dwordx4 v[24:27], v117, %[sw], %[t0] offen offset:0 sc1\n\t"
        "buffer_load_dwordx4 v[28:31], v117, %[sw], %[t0] offen offset:1024 sc1\n\t"
        "buffer_load_dwordx4 v[32:35], v117, %[sw], %[t0] offen offset:2048 sc1\n\t"
        "buffer_load_dwordx4 v[36:39], v117, %[sw], %[t0] offen offset:3072 sc1\n\t"
        "s_add_u32 %[t0], %[ro], 1\n\t"
        "s_and_b32 %[t0], %[t0], 7\n\t"
        "s_lshl_b32 %[t0], %[t0], 12\n\t"
        "s_add_u32 %[t0], %[t0], %[nx]\n\t"
        "buffer_load_dwordx4 v[40:43], v117, %[sw], %[t0] offen offset:0 sc1\n\t"
        "buffer_load_dwordx4 v[44:47], v117, %[sw], %[t0] offen offset:1024 sc1\n\t"
        "s_waitcnt vmcnt(5) lgkmcnt(2)\n\t"
        "v_mfma_f32_16x16x32_bf16 v[20:23], v[88:91], v[24:27], 0\n\t"
        "ds_read_b128 v[88:91], v116 offset:3072\n\t"
        "v_mfma_f32_16x16x32_bf16 v[8:11], a[0:3], v[24:27], 0\n\t"
        "v_max3_u32 v125, v24, v25, v26\n\t"
        "buffer_load_dwordx4 v[48:51], v117, %[sw], %[t0] offen offset:2048 sc1\n\t"
        "v_mfma_f32_16x16x32_bf16 v[12:15], a[4:7], v[24:27], 0\n\t"
        "v_max_u32_e32 v125, v27, v125\n\t"
        "v_mfma_f32_16x16x32_bf16 v[16:19], a[8:11], v[24:27], 0\n\t"
        "buffer_load_dwordx4 v[52:55], v117, %[sw], %[t0] offen offset:3072 sc1\n\t"
        "s_waitcnt vmcnt(6) lgkmcnt(2)\n\t"
        "v_mfma_f32_16x16x32_bf16 v[20:23], v[92:95], v[28:31], v[20:23]\n\t"
        "ds_read_b128 v[92:95], v116 offset:4096\n\t"
        "v_mfma_f32_16x16x32_bf16 v[8:11], a[12:15], v[28:31], v[8:11]\n\t"
        "v_max3_u32 v125, v28, v29, v125\n\t"
        "s_add_u32 %[t0], %[ro], 2\n\t"
        "s_and_b32 %[t0], %[t0], 7\n\t"
        "s_lshl_b32 %[t0], %[t0], 12\n\t"
        "s_add_u32 %[t0], %[t0], %[nx]\n\t"
        "buffer_load_dwordx4 v[56:59], v117, %[sw], %[t0] offen offset:0 sc1\n\t"
        "v_mfma_f32_16x16x32_bf16 v[12:15], a[16:19], v[28:31], v[12:15]\n\t"
        "v_max3_u32 v125, v30, v31, v125\n\t"
        "v_mfma_f32_16x16x32_bf16 v[16:19], a[20:23], v[28:31], v[16:19]\n\t"
        "buffer_load_dwordx4 v[60:63], v117, %[sw], %[t0] offen offset:1024 sc1\n\t"
        "s_and_b32 %[sp], %[tl], 1\n\t"
        "s_lshl_b32 %[sp], %[sp], 14\n\t"
        "s_lshr_b32 %[t1], %[tl], 1\n\t"
        "s_lshl_b32 %[t1], %[t1], 6\n\t"
        "s_add_u32 %[t1], %[sp], %[t1]\n\t"
        "buffer_load_dword v100, v119, %[sw], %[t1] offen sc1\n\t"
        "s_add_u32 %[t1], %[t1], 32768\n\t"
        "buffer_load_dword v101, v119, %[sw], %[t1] offen sc1\n\t"
        "s_lshl_b32 %[t1], %[tl], 6\n\t"
        "buffer_load_dword v102, v120, %[sw], %[t1] offen sc1\n\t"
        "buffer_load_dword v103, v118, %[sw], 0 offen sc1\n\t"
        "s_waitcnt vmcnt(11) lgkmcnt(2)\n\t"
        "v_mfma_f32_16x16x32_bf16 v[20:23], v[96:99], v[32:35], v[20:23]\n\t"
        "ds_read_b128 v[96:99], v116 offset:5120\n\t"
        "v_mfma_f32_16x16x32_bf16 v[8:11], a[24:27], v[32:35], v[8:11]\n\t"
        "v_max3_u32 v125, v32, v33, v125\n\t"
        "buffer_load_dwordx4 v[64:67], v117, %[sw], %[t0] offen offset:2048 sc1\n\t"
        "v_mfma_f32_16x16x32_bf16 v[12:15], a[28:31], v[32:35], v[12:15]\n\t"
        "v_max3_u32 v125, v34, v35, v125\n\t"
        "v_mfma_f32_16x16x32_bf16 v[16:19], a[32:35], v[32:35], v[16:19]\n\t"
        "buffer_load_dwordx4 v[68:71], v117, %[sw], %[t0] offen offset:3072 sc1\n\t"
        "s_waitcnt vmcnt(12) lgkmcnt(2)\n\t"
        "v_mfma_f32_16x16x32_bf16 v[20:23], v[88:91], v[36:39], v[20:23]\n\t"
        "ds_read_b128 v[88:91], v116 offset:6144\n\t"
        "v_mfma_f32_16x16x32_bf16 v[8:11], a[36:39], v[36:39], v[8:11]\n\t"
        "v_max3_u32 v125, v36, v37, v125\n\t"
        "s_add_u32 %[t0], %[ro], 3\n\t"
        "s_and_b32 %[t0], %[t0], 7\n\t"
        "s_lshl_b32 %[t0], %[t0], 12\n\t"
        "s_add_u32 %[t0], %[t0], %[nx]\n\t"
        "buffer_load_dwordx4 v[72:75], v117, %[sw], %[t0] offen offset:0 sc1\n\t"
        "v_mfma_f32_16x16x32_bf16 v[12:15], a[40:43], v[36:39], v[12:15]\n\t"
        "v_max3_u32 v125, v38, v39, v125\n\t"
        "v_mfma_f32_16x16x32_bf16 v[16:19], a[44:47], v[36:39], v[16:19]\n\t"
        "buffer_load_dwordx4 v[76:79], v117, %[sw], %[t0] offen offset:1024 sc1\n\t"
        "s_waitcnt vmcnt(13) lgkmcnt(2)\n\t"
        "v_mfma_f32_16x16x32_bf16 v[20:23], v[92:95], v[40:43], v[20:23]\n\t"
        "ds_read_b128 v[92:95], v116 offset:7168\n\t"
        "v_mfma_f32_16x16x32_bf16 v[8:11], a[48:51], v[40:43], v[8:11]\n\t"
        "v_max3_u32 v125, v40, v41, v125\n\t"
        "buffer_load_dwordx4 v[80:83], v117, %[sw], %[t0] offen offset:2048 sc1\n\t"
        "v_mfma_f32_16x16x32_bf16 v[12:15], a[52:55], v[40:43], v[12:15]\n\t"
        "v_max3_u32 v125, v42, v43, v125\n\t"
        "v_mfma_f32_16x16x32_bf16 v[16:19], a[56:59], v[40:43], v[16:19]\n\t"
        "buffer_load_dwordx4 v[84:87], v117, %[sw], %[t0] offen offset:3072 sc1\n\t"
        "s_waitcnt vmcnt(14) lgkmcnt(2)\n\t"
        "v_mfma_f32_16x16x32_bf16 v[20:23], v[96:99], v[44:47], v[20:23]\n\t"
        "ds_read_b128 v[96:99], v116 offset:8192\n\t"
        "v_mfma_f32_16x16x32_bf16 v[8:11], a[60:63], v[44:47], v[8:11]\n\t"
        "v_max3_u32 v125, v44, v45, v125\n\t"
        "s_add_u32 %[t0], %[ro], 4\n\t"
        "s_and_b32 %[t0], %[t0], 7\n\t"
        "s_lshl_b32 %[t0], %[t0], 12\n\t"
        "s_add_u32 %[t0], %[t0], %[nx]\n\t"
        "buffer_load_dwordx4 v[24:27], v117, %[sw], %[t0] offen offset:0 sc1\n\t"
        "v_mfma_f32_16x16x32_bf16 v[12:15], a[64:67], v[44:47], v[12:15]\n\t"
        "v_max3_u32 v125, v46, v47, v125\n\t"
        "v_mfma_f32_16x16x32_bf16 v[16:19], a[68:71], v[44:47], v[16:19]\n\t"
        "buffer_load_dwordx4 v[28:31], v117, %[sw], %[t0] offen offset:1024 sc1\n\t"
        "s_waitcnt vmcnt(15) lgkmcnt(2)\n\t"
        "v_mfma_f32_16x16x32_bf16 v[20:23], v[88:91], v[48:51], v[20:23]\n\t"
        "ds_read_b128 v[88:91], v116 offset:9216\n\t"
        "v_mfma_f32_16x16x32_bf16 v[8:11], a[72:75], v[48:51], v[8:11]\n\t"
        "v_max3_u32 v125, v48, v49, v125\n\t"
        "v_mfma_f32_16x16x32_bf16 v[12:15], a[76:79], v[48:51], v[12:15]\n\t"
        "v_max3_u32 v125, v50, v51, v125\n\t"
        "v_mfma_f32_16x16x32_bf16 v[16:19], a[80:83], v[48:51], v[16:19]\n\t"
        "buffer_load_dwordx4 v[32:35], v117, %[sw], %[t0] offen offset:2048 sc1\n\t"
        "s_waitcnt vmcnt(15) lgkmcnt(2)\n\t"
        "v_mfma_f32_16x16x32_bf16 v[20:23], v[92:95], v[52:55], v[20:23]\n\t"
        "ds_read_b128 v[92:95], v116 offset:10240\n\t"
        "v_mfma_f32_16x16x32_bf16 v[8:11], a[84:87], v[52:55], v[8:11]\n\t"
        "v_max3_u32 v125, v52, v53, v125\n\t"
        "v_mfma_f32_16x16x32_bf16 v[12:15], a[88:91], v[52:55], v[12:15]\n\t"
        "v_max3_u32 v125, v54, v55, v125\n\t"
        "v_mfma_f32_16x16x32_bf16 v[16:19], a[92:95], v[52:55], v[16:19]\n\t"
        "buffer_load_dwordx4 v[36:39], v117, %[sw], %[t0] offen offset:3072 sc1\n\t"
        "s_waitcnt vmcnt(15) lgkmcnt(2)\n\t"
        "s_cmp_lg_u32 %[re], 0\n\t"
        "s_cbranch_scc1 LP_nb%=\n\t"
        "s_barrier\n\t"
        "LP_nb%=:\n\t"
        "v_mfma_f32_16x16x32_bf16 v[20:23], v[96:99], v[56:59], v[20:23]\n\t"
        "ds_read_b128 v[96:99], v116 offset:11264\n\t"
        "v_mfma_f32_16x16x32_bf16 v[8:11], a[96:99], v[56:59], v[8:11]\n\t"
        "v_max3_u32 v125, v56, v57, v125\n\t"
        "v_mfma_f32_16x16x32_bf16 v[12:15], a[100:103], v[56:59], v[12:15]\n\t"
        "v_max3_u32 v125, v58, v59, v125\n\t"
        "v_mfma_f32_16x16x32_bf16 v[16:19], a[104:107], v[56:59], v[16:19]\n\t"
        "s_add_u32 %[t0], %[ro], 5\n\t"
        "s_and_b32 %[t0], %[t0], 7\n\t"
        "s_lshl_b32 %[t0], %[t0], 12\n\t"
        "s_add_u32 %[t0], %[t0], %[nx]\n\t"
        "buffer_load_dwordx4 v[40:43], v117, %[sw], %[t0] offen offset:0 sc1\n\t"
        "s_waitcnt vmcnt(15) lgkmcnt(2)\n\t"
        "v_mfma_f32_16x16x32_bf16 v[20:23], v[88:91], v[60:63], v[20:23]\n\t"
        "ds_read_b128 v[88:91], v116 offset:12288\n\t"
        "v_mfma_f32_16x16x32_bf16 v[8:11], a[108:111], v[60:63], v[8:11]\n\t"
        "v_max3_u32 v125, v60, v61, v125\n\t"
        "v_mfma_f32_16x16x32_bf16 v[12:15], a[112:115], v[60:63], v[12:15]\n\t"
        "v_max3_u32 v125, v62, v63, v125\n\t"
        "v_mfma_f32_16x16x32_bf16 v[16:19], a[116:119], v[60:63], v[16:19]\n\t"
        "buffer_load_dwordx4 v[44:47], v117, %[sw], %[t0] offen offset:1024 sc1\n\t"
        "s_waitcnt vmcnt(11) lgkmcnt(2)\n\t"
        "v_mfma_f32_16x16x32_bf16 v[20:23], v[92:95], v[64:67], v[20:23]\n\t"
        "ds_read_b128 v[92:95], v116 offset:13312\n\t"
        "v_mfma_f32_16x16x32_bf16 v[8:11], a[120:123], v[64:67], v[8:11]\n\t"
        "v_max3_u32 v125, v64, v65, v125\n\t"
        "v_mfma_f32_16x16x32_bf16 v[12:15], a[124:127], v[64:67], v[12:15]\n\t"
        "v_max3_u32 v125, v66, v67, v125\n\t"
        "v_mfma_f32_16x16x32_bf16 v[16:19], a[128:131], v[64:67], v[16:19]\n\t"
        "buffer_load_dwordx4 v[48:51], v117, %[sw], %[t0] offen offset:2048 sc1\n\t"
        "s_waitcnt vmcnt(11) lgkmcnt(2)\n\t"
        "v_mfma_f32_16x16x32_bf16 v[20:23], v[96:99], v[68:71], v[20:23]\n\t"
        "ds_read_b128 v[96:99], v116 offset:14336\n\t"
        "v_mfma_f32_16x16x32_bf16 v[8:11], a[132:135], v[68:71], v[8:11]\n\t"
        "v_max3_u32 v125, v68, v69, v125\n\t"
        "v_mfma_f32_16x16x32_bf16 v[12:15], a[136:139], v[68:71], v[12:15]\n\t"
        "v_max3_u32 v125, v70, v71, v125\n\t"
        "v_mfma_f32_16x16x32_bf16 v[16:19], a[140:143], v[68:71], v[16:19]\n\t"
        "buffer_load_dwordx4 v[52:55], v117, %[sw], %[t0] offen offset:3072 sc1\n\t"
        "s_waitcnt vmcnt(11) lgkmcnt(2)\n\t"
        "v_mfma_f32_16x16x32_bf16 v[20:23], v[88:91], v[72:75], v[20:23]\n\t"
        "ds_read_b128 v[88:91], v116 offset:15360\n\t"
        "v_mfma_f32_16x16x32_bf16 v[8:11], a[144:147], v[72:75], v[8:11]\n\t"
        "v_max3_u32 v125, v72, v73, v125\n\t"
        "v_mfma_f32_16x16x32_bf16 v[12:15], a[148:151], v[72:75], v[12:15]\n\t"
        "v_max3_u32 v125, v74, v75, v125\n\t"
        "v_mfma_f32_16x16x32_bf16 v[16:19], a[152:155], v[72:75], v[16:19]\n\t"
        "s_add_u32 %[t0], %[ro], 6\n\t"
        "s_and_b32 %[t0], %[t0], 7\n\t"
        "s_lshl_b32 %[t0], %[t0], 12\n\t"
        "s_add_u32 %[t0], %[t0], %[nx]\n\t"
        "buffer_load_dwordx4 v[56:59], v117, %[sw], %[t0] offen offset:0 sc1\n\t"
        "s_waitcnt vmcnt(11) lgkmcnt(2)\n\t"
        "v_mfma_f32_16x16x32_bf16 v[20:23], v[92:95], v[76:79], v[20:23]\n\t"
        "ds_read_b128 v[92:95], v116 offset:16384\n\t"
        "v_mfma_f32_16x16x32_bf16 v[8:11], a[156:159], v[76:79], v[8:11]\n\t"
        "v_max3_u32 v125, v76, v77, v125\n\t"
        "v_mfma_f32_16x16x32_bf16 v[12:15], a[160:163], v[76:79], v[12:15]\n\t"
        "v_max3_u32 v125, v78, v79, v125\n\t"
        "v_mfma_f32_16x16x32_bf16 v[16:19], a[164:167], v[76:79], v[16:19]\n\t"
        "buffer_load_dwordx4 v[60:63], v117, %[sw], %[t0] offen offset:1024 sc1\n\t"
        "s_waitcnt vmcnt(11) lgkmcnt(2)\n\t"
        "v_mfma_f32_16x16x32_bf16 v[20:23], v[96:99], v[80:83], v[20:23]\n\t"
        "ds_read_b128 v[96:99], v116 offset:17408\n\t"
        "v_mfma_f32_16x16x32_bf16 v[8:11], a[168:171], v[80:83], v[8:11]\n\t"
        "v_max3_u32 v125, v80, v81, v125\n\t"
        "v_mfma_f32_16x16x32_bf16 v[12:15], a[172:175], v[80:83], v[12:15]\n\t"
        "v_max3_u32 v125, v82, v83, v125\n\t"
        "v_mfma_f32_16x16x32_bf16 v[16:19], a[176:179], v[80:83], v[16:19]\n\t"
        "buffer_load_dwordx4 v[64:67], v117, %[sw], %[t0] offen offset:2048 sc1\n\t"
        "s_waitcnt vmcnt(11) lgkmcnt(2)\n\t"
        "v_mfma_f32_16x16x32_bf16 v[20:23], v[88:91], v[84:87], v[20:23]\n\t"
        "ds_read_b128 v[88:91], v116 offset:18432\n\t"
        "v_mfma_f32_16x16x32_bf16 v[8:11], a[180:183], v[84:87], v[8:11]\n\t"
        "v_max3_u32 v125, v84, v85, v125\n\t"
        "v_mfma_f32_16x16x32_bf16 v[12:15], a[184:187], v[84:87], v[12:15]\n\t"
        "v_max3_u32 v125, v86, v87, v125\n\t"
        "v_mfma_f32_16x16x32_bf16 v[16:19], a[188:191], v[84:87], v[16:19]\n\t"
        "buffer_load_dwordx4 v[68:71], v117, %[sw], %[t0] offen offset:3072 sc1\n\t"
        "s_waitcnt vmcnt(11) lgkmcnt(2)\n\t"
        "v_mfma_f32_16x16x32_bf16 v[20:23], v[92:95], v[24:27], v[20:23]\n\t"
        "ds_read_b128 v[92:95], v116 offset:19456\n\t"
        "v_mfma_f32_16x16x32_bf16 v[8:11], a[192:195], v[24:27], v[8:11]\n\t"
        "v_max3_u32 v125, v24, v25, v125\n\t"
        "v_mfma_f32_16x16x32_bf16 v[12:15], a[196:199], v[24:27], v[12:15]\n\t"
        "v_max3_u32 v125, v26, v27, v125\n\t"
        "v_mfma_f32_16x16x32_bf16 v[16:19], a[200:203], v[24:27], v[16:19]\n\t"
        "s_add_u32 %[t0], %[ro], 7\n\t"
        "s_and_b32 %[t0], %[t0], 7\n\t"
        "s_lshl_b32 %[t0], %[t0], 12\n\t"
        "s_add_u32 %[t0], %[t0], %[nx]\n\t"
        "buffer_load_dwordx4 v[72:75], v117, %[sw], %[t0] offen offset:0 sc1\n\t"
        "s_waitcnt vmcnt(11) lgkmcnt(2)\n\t"
        "v_mfma_f32_16x16x32_bf16 v[20:23], v[96:99], v[28:31], v[20:23]\n\t"
        "ds_read_b128 v[96:99], v116 offset:20480\n\t"
        "v_mfma_f32_16x16x32_bf16 v[8:11], a[204:207], v[28:31], v[8:11]\n\t"
        "v_max3_u32 v125, v28, v29, v125\n\t"
        "v_mfma_f32_16x16x32_bf16 v[12:15], a[208:211], v[28:31], v[12:15]\n\t"
        "v_max3_u32 v125, v30, v31, v125\n\t"
        "v_sub_f32_e32 v104, 1.0, v102\n\t"
        "v_mfma_f32_16x16x32_bf16 v[16:19], a[212:215], v[28:31], v[16:19]\n\t"
        "buffer_load_dwordx4 v[76:79], v117, %[sw], %[t0] offen offset:1024 sc1\n\t"
        "s_waitcnt vmcnt(11) lgkmcnt(2)\n\t"
        "v_mfma_f32_16x16x32_bf16 v[20:23], v[88:91], v[32:35], v[20:23]\n\t"
        "ds_read_b128 v[88:91], v116 offset:21504\n\t"
        "v_mfma_f32_16x16x32_bf16 v[8:11], a[216:219], v[32:35], v[8:11]\n\t"
        "v_max3_u32 v125, v32, v33, v125\n\t"
        "v_mfma_f32_16x16x32_bf16 v[12:15], a[220:223], v[32:35], v[12:15]\n\t"
        "v_max3_u32 v125, v34, v35, v125\n\t"
        "v_cvt_pk_bf16_f32 v104, 0, v104\n\t"
        "v_mfma_f32_16x16x32_bf16 v[16:19], a[224:227], v[32:35], v[16:19]\n\t"
        "buffer_load_dwordx4 v[80:83], v117, %[sw], %[t0] offen offset:2048 sc1\n\t"
        "s_waitcnt vmcnt(11) lgkmcnt(2)\n\t"
        "v_mfma_f32_16x16x32_bf16 v[20:23], v[92:95], v[36:39], v[20:23]\n\t"
        "ds_read_b128 v[92:95], v116 offset:22528\n\t"
        "v_mfma_f32_16x16x32_bf16 v[8:11], a[228:231], v[36:39], v[8:11]\n\t"
        "v_max3_u32 v125, v36, v37, v125\n\t"
        "v_mfma_f32_16x16x32_bf16 v[12:15], a[232:235], v[36:39], v[12:15]\n\t"
        "v_max3_u32 v125, v38, v39, v125\n\t"
        "v_lshlrev_b32_e32 v106, 16, v100\n\t"
        "v_mfma_f32_16x16x32_bf16 v[16:19], a[236:239], v[36:39], v[16:19]\n\t"
        "buffer_load_dwordx4 v[84:87], v117, %[sw], %[t0] offen offset:3072 sc1\n\t"
        "s_waitcnt vmcnt(11) lgkmcnt(2)\n\t"
        "v_mfma_f32_16x16x32_bf16 v[20:23], v[96:99], v[40:43], v[20:23]\n\t"
        "ds_read_b128 v[96:99], v116 offset:23552\n\t"
        "v_mfma_f32_16x16x32_bf16 v[8:11], a[240:243], v[40:43], v[8:11]\n\t"
        "v_max3_u32 v125, v40, v41, v125\n\t"
        "v_mfma_f32_16x16x32_bf16 v[12:15], a[244:247], v[40:43], v[12:15]\n\t"
        "v_max3_u32 v125, v42, v43, v125\n\t"
        "v_mul_f32_e32 v106, v102, v106\n\t"
        "v_mfma_f32_16x16x32_bf16 v[16:19], a[248:251], v[40:43], v[16:19]\n\t"
        "s_waitcnt vmcnt(10) lgkmcnt(2)\n\t"
        "v_mfma_f32_16x16x32_bf16 v[20:23], v[88:91], v[44:47], v[20:23]\n\t"
        "ds_read_b128 v[88:91], v116 offset:24576\n\t"
        "v_mfma_f32_16x16x32_bf16 v[8:11], a[252:255], v[44:47], v[8:11]\n\t"
        "v_max3_u32 v125, v44, v45, v125\n\t"
        "v_mfma_f32_16x16x32_bf16 v[12:15], v[128:131], v[44:47], v[12:15]\n\t"
        "v_max3_u32 v125, v46, v47, v125\n\t"
        "v_cvt_pk_bf16_f32 v106, 0, v106\n\t"
        "v_mfma_f32_16x16x32_bf16 v[16:19], v[132:135], v[44:47], v[16:19]\n\t"
        "s_waitcnt vmcnt(9) lgkmcnt(2)\n\t"
        "v_mfma_f32_16x16x32_bf16 v[20:23], v[92:95], v[48:51], v[20:23]\n\t"
        "ds_read_b128 v[92:95], v116 offset:25600\n\t"
        "v_mfma_f32_16x16x32_bf16 v[8:11], v[136:139], v[48:51], v[8:11]\n\t"
        "v_max3_u32 v125, v48, v49, v125\n\t"
        "v_mfma_f32_16x16x32_bf16 v[12:15], v[140:143], v[48:51], v[12:15]\n\t"
        "v_max3_u32 v125, v50, v51, v125\n\t"
        "v_and_b32_e32 v107, 0xffff0000, v100\n\t"
        "v_mfma_f32_16x16x32_bf16 v[16:19], v[144:147], v[48:51], v[16:19]\n\t"
        "s_waitcnt vmcnt(8) lgkmcnt(2)\n\t"
        "v_mfma_f32_16x16x32_bf16 v[20:23], v[96:99], v[52:55], v[20:23]\n\t"
        "ds_read_b128 v[96:99], v116 offset:26624\n\t"
        "v_mfma_f32_16x16x32_bf16 v[8:11], v[148:151], v[52:55], v[8:11]\n\t"
        "v_max3_u32 v125, v52, v53, v125\n\t"
        "v_mfma_f32_16x16x32_bf16 v[12:15], v[152:155], v[52:55], v[12:15]\n\t"
        "v_max3_u32 v125, v54, v55, v125\n\t"
        "v_mul_f32_e32 v107, v102, v107\n\t"
        "v_mfma_f32_16x16x32_bf16 v[16:19], v[156:159], v[52:55], v[16:19]\n\t"
        "s_waitcnt vmcnt(7) lgkmcnt(2)\n\t"
        "v_mfma_f32_16x16x32_bf16 v[20:23], v[88:91], v[56:59], v[20:23]\n\t"
        "ds_read_b128 v[88:91], v116 offset:27648\n\t"
        "v_mfma_f32_16x16x32_bf16 v[8:11], v[160:163], v[56:59], v[8:11]\n\t"
        "v_max3_u32 v125, v56, v57, v125\n\t"
        "v_mfma_f32_16x16x32_bf16 v[12:15], v[164:167], v[56:59], v[12:15]\n\t"
        "v_max3_u32 v125, v58, v59, v125\n\t"
        "v_cvt_pk_bf16_f32 v107, 0, v107\n\t"
        "v_mfma_f32_16x16x32_bf16 v[16:19], v[168:171], v[56:59], v[16:19]\n\t"
        "s_waitcnt vmcnt(6) lgkmcnt(2)\n\t"
        "v_mfma_f32_16x16x32_bf16 v[20:23], v[92:95], v[60:63], v[20:23]\n\t"
        "ds_read_b128 v[92:95], v116 offset:28672\n\t"
        "v_mfma_f32_16x16x32_bf16 v[8:11], v[172:175], v[60:63], v[8:11]\n\t"
        "v_max3_u32 v125, v60, v61, v125\n\t"
        "v_mfma_f32_16x16x32_bf16 v[12:15], v[176:179], v[60:63], v[12:15]\n\t"
        "v_max3_u32 v125, v62, v63, v125\n\t"
        "v_lshlrev_b32_e32 v108, 16, v101\n\t"
        "v_mfma_f32_16x16x32_bf16 v[16:19], v[180:183], v[60:63], v[16:19]\n\t"
        "s_waitcnt vmcnt(5) lgkmcnt(2)\n\t"
        "v_mfma_f32_16x16x32_bf16 v[20:23], v[96:99], v[64:67], v[20:23]\n\t"
        "ds_read_b128 v[96:99], v116 offset:29696\n\t"
        "v_mfma_f32_16x16x32_bf16 v[8:11], v[184:187], v[64:67], v[8:11]\n\t"
        "v_max3_u32 v125, v64, v65, v125\n\t"
        "v_mfma_f32_16x16x32_bf16 v[12:15], v[188:191], v[64:67], v[12:15]\n\t"
        "v_max3_u32 v125, v66, v67, v125\n\t"
        "v_mul_f32_e32 v108, v102, v108\n\t"
        "v_mfma_f32_16x16x32_bf16 v[16:19], v[192:195], v[64:67], v[16:19]\n\t"
        "s_waitcnt vmcnt(4) lgkmcnt(2)\n\t"
        "v_mfma_f32_16x16x32_bf16 v[20:23], v[88:91], v[68:71], v[20:23]\n\t"
        "ds_read_b128 v[88:91], v116 offset:30720\n\t"
        "v_mfma_f32_16x16x32_bf16 v[8:11], v[196:199], v[68:71], v[8:11]\n\t"
        "v_max3_u32 v125, v68, v69, v125\n\t"
        "v_mfma_f32_16x16x32_bf16 v[12:15], v[200:203], v[68:71], v[12:15]\n\t"
        "v_max3_u32 v125, v70, v71, v125\n\t"
        "v_cvt_pk_bf16_f32 v108, 0, v108\n\t"
        "v_mfma_f32_16x16x32_bf16 v[16:19], v[204:207], v[68:71], v[16:19]\n\t"
        "s_waitcnt vmcnt(3) lgkmcnt(2)\n\t"
        "v_mfma_f32_16x16x32_bf16 v[20:23], v[92:95], v[72:75], v[20:23]\n\t"
        "ds_read_b128 v[92:95], v116 offset:31744\n\t"
        "v_mfma_f32_16x16x32_bf16 v[8:11], v[208:211], v[72:75], v[8:11]\n\t"
        "v_max3_u32 v125, v72, v73, v125\n\t"
        "v_mfma_f32_16x16x32_bf16 v[12:15], v[212:215], v[72:75], v[12:15]\n\t"
        "v_max3_u32 v125, v74, v75, v125\n\t"
        "v_and_b32_e32 v109, 0xffff0000, v101\n\t"
        "v_mfma_f32_16x16x32_bf16 v[16:19], v[216:219], v[72:75], v[16:19]\n\t"
        "s_waitcnt vmcnt(2) lgkmcnt(2)\n\t"
        "v_mfma_f32_16x16x32_bf16 v[20:23], v[96:99], v[76:79], v[20:23]\n\t"
        "v_mfma_f32_16x16x32_bf16 v[8:11], v[220:223], v[76:79], v[8:11]\n\t"
        "v_max3_u32 v125, v76, v77, v125\n\t"
        "v_mfma_f32_16x16x32_bf16 v[12:15], v[224:227], v[76:79], v[12:15]\n\t"
        "v_max3_u32 v125, v78, v79, v125\n\t"
        "v_mul_f32_e32 v109, v102, v109\n\t"
        "v_mfma_f32_16x16x32_bf16 v[16:19], v[228:231], v[76:79], v[16:19]\n\t"
        "s_waitcnt vmcnt(1) lgkmcnt(1)\n\t"
        "v_mfma_f32_16x16x32_bf16 v[20:23], v[88:91], v[80:83], v[20:23]\n\t"
        "ds_read_b128 v[88:91], v116 offset:0\n\t"
        "v_mfma_f32_16x16x32_bf16 v[8:11], v[232:235], v[80:83], v[8:11]\n\t"
        "v_max3_u32 v125, v80, v81, v125\n\t"
        "v_mfma_f32_16x16x32_bf16 v[12:15], v[236:239], v[80:83], v[12:15]\n\t"
        "v_max3_u32 v125, v82, v83, v125\n\t"
        "v_cvt_pk_bf16_f32 v109, 0, v109\n\t"
        "v_mfma_f32_16x16x32_bf16 v[16:19], v[240:243], v[80:83], v[16:19]\n\t"
        "s_waitcnt vmcnt(0) lgkmcnt(1)\n\t"
        "v_mfma_f32_16x16x32_bf16 v[20:23], v[92:95], v[84:87], v[20:23]\n\t"
        "ds_read_b128 v[92:95], v116 offset:1024\n\t"
        "v_mfma_f32_16x16x32_bf16 v[8:11], v[244:247], v[84:87], v[8:11]\n\t"
        "ds_read_b128 v[96:99], v116 offset:2048\n\t"
        "v_max3_u32 v125, v84, v85, v125\n\t"
        "v_mfma_f32_16x16x32_bf16 v[12:15], v[248:251], v[84:87], v[12:15]\n\t"
        "v_max3_u32 v125, v86, v87, v125\n\t"
        "v_mfma_f32_16x16x32_bf16 v[16:19], v[252:255], v[84:87], v[16:19]\n\t"
        "v_cmp_eq_u32_e32 vcc, -1, v125\n\t"
        "s_mov_b32 %[nx], 0xf0000000\n\t"
        "s_mov_b32 %[re], 64\n\t"
        "s_cbranch_vccz LP_epi%=\n\t"
        "s_waitcnt lgkmcnt(0)\n\t"
        "s_branch LP_again%=\n\t"
        "LP_run%=:\n\t"
        "s_add_u32 %[t0], %[ro], 0\n\t"
        "s_and_b32 %[t0], %[t0], 7\n\t"
        "s_lshl_b32 %[t0], %[t0], 12\n\t"
        "s_add_u32 %[t0], %[t0], %[nx]\n\t"
        "buffer_load_dwordx4 v[24:27], v117, %[sw], %[t0] offen offset:0 sc1\n\t"
        "buffer_load_dwordx4 v[28:31], v117, %[sw], %[t0] offen offset:1024 sc1\n\t"
        "buffer_load_dwordx4 v[32:35], v117, %[sw], %[t0] offen offset:2048 sc1\n\t"
        "buffer_load_dwordx4 v[36:39], v117, %[sw], %[t0] offen offset:3072 sc1\n\t"
        "s_add_u32 %[t0], %[ro], 1\n\t"
        "s_and_b32 %[t0], %[t0], 7\n\t"
        "s_lshl_b32 %[t0], %[t0], 12\n\t"
        "s_add_u32 %[t0], %[t0], %[nx]\n\t"
        "buffer_load_dwordx4 v[40:43], v117, %[sw], %[t0] offen offset:0 sc1\n\t"
        "buffer_load_dwordx4 v[44:47], v117, %[sw], %[t0] offen offset:1024 sc1\n\t"
        "buffer_load_dwordx4 v[48:51], v117, %[sw], %[t0] offen offset:2048 sc1\n\t"
        "buffer_load_dwordx4 v[52:55], v117, %[sw], %[t0] offen offset:3072 sc1\n\t"
        "s_add_u32 %[t0], %[ro], 2\n\t"
        "s_and_b32 %[t0], %[t0], 7\n\t"
        "s_lshl_b32 %[t0], %[t0], 12\n\t"
        "s_add_u32 %[t0], %[t0], %[nx]\n\t"
        "buffer_load_dwordx4 v[56:59], v117, %[sw], %[t0] offen offset:0 sc1\n\t"
        "buffer_load_dwordx4 v[60:63], v117, %[sw], %[t0] offen offset:1024 sc1\n\t"
        "buffer_load_dwordx4 v[64:67], v117, %[sw], %[t0] offen offset:2048 sc1\n\t"
        "buffer_load_dwordx4 v[68:71], v117, %[sw], %[t0] offen offset:3072 sc1\n\t"
        "s_add_u32 %[t0], %[ro], 3\n\t"
        "s_and_b32 %[t0], %[t0], 7\n\t"
        "s_lshl_b32 %[t0], %[t0], 12\n\t"
        "s_add_u32 %[t0], %[t0], %[nx]\n\t"
        "buffer_load_dwordx4 v[72:75], v117, %[sw], %[t0] offen offset:0 sc1\n\t"
        "buffer_load_dwordx4 v[76:79], v117, %[sw], %[t0] offen offset:1024 sc1\n\t"
        "buffer_load_dwordx4 v[80:83], v117, %[sw], %[t0] offen offset:2048 sc1\n\t"
        "buffer_load_dwordx4 v[84:87], v117, %[sw], %[t0] offen offset:3072 sc1\n\t"
        "buffer_load_dword v103, v118, %[sw], 0 offen sc1\n\t"
        "LP_tile%=:\n\t"
        "s_and_b32 %[sp], %[tl], 1\n\t"
        "s_lshl_b32 %[sp], %[sp], 14\n\t"
        "s_lshr_b32 %[t1], %[tl], 1\n\t"
        "s_lshl_b32 %[t1], %[t1], 6\n\t"
        "s_add_u32 %[t1], %[sp], %[t1]\n\t"
        "buffer_load_dword v100, v119, %[sw], %[t1] offen sc1\n\t"
        "s_add_u32 %[t1], %[t1], 32768\n\t"
        "buffer_load_dword v101, v119, %[sw], %[t1] offen sc1\n\t"
        "s_lshl_b32 %[t1], %[tl], 6\n\t"
        "buffer_load_dword v102, v120, %[sw], %[t1] offen sc1\n\t"
        "buffer_load_dword v103, v118, %[sw], 0 offen sc1\n\t"
        "s_mov_b32 %[cu], %[nx]\n\t"
        "s_waitcnt vmcnt(20) lgkmcnt(2)\n\t"
        "v_mfma_f32_16x16x32_bf16 v[20:23], v[88:91], v[24:27], 0\n\t"
        "ds_read_b128 v[88:91], v116 offset:3072\n\t"
        "v_mfma_f32_16x16x32_bf16 v[8:11], a[0:3], v[24:27], 0\n\t"
        "v_mfma_f32_16x16x32_bf16 v[12:15], a[4:7], v[24:27], 0\n\t"
        "v_mfma_f32_16x16x32_bf16 v[16:19], a[8:11], v[24:27], 0\n\t"
        "s_add_u32 %[t0], %[ro], 4\n\t"
        "s_and_b32 %[t0], %[t0], 7\n\t"
        "s_lshl_b32 %[t0], %[t0], 12\n\t"
        "s_add_u32 %[t0], %[t0], %[cu]\n\t"
        "buffer_load_dwordx4 v[24:27], v117, %[sw], %[t0] offen offset:0 sc1\n\t"
        "s_waitcnt vmcnt(20) lgkmcnt(2)\n\t"
        "v_mfma_f32_16x16x32_bf16 v[20:23], v[92:95], v[28:31], v[20:23]\n\t"
        "ds_read_b128 v[92:95], v116 offset:4096\n\t"
        "v_mfma_f32_16x16x32_bf16 v[8:11], a[12:15], v[28:31], v[8:11]\n\t"
        "v_mfma_f32_16x16x32_bf16 v[12:15], a[16:19], v[28:31], v[12:15]\n\t"
        "v_mfma_f32_16x16x32_bf16 v[16:19], a[20:23], v[28:31], v[16:19]\n\t"
        "buffer_load_dwordx4 v[28:31], v117, %[sw], %[t0] offen offset:1024 sc1\n\t"
        "s_waitcnt vmcnt(20) lgkmcnt(2)\n\t"
        "v_mfma_f32_16x16x32_bf16 v[20:23], v[96:99], v[32:35], v[20:23]\n\t"
        "ds_read_b128 v[96:99], v116 offset:5120\n\t"
        "v_mfma_f32_16x16x32_bf16 v[8:11], a[24:27], v[32:35], v[8:11]\n\t"
        "v_mfma_f32_16x16x32_bf16 v[12:15], a[28:31], v[32:35], v[12:15]\n\t"
        "v_mfma_f32_16x16x32_bf16 v[16:19], a[32:35], v[32:35], v[16:19]\n\t"
        "buffer_load_dwordx4 v[32:35], v117, %[sw], %[t0] offen offset:2048 sc1\n\t"
        "s_waitcnt vmcnt(20) lgkmcnt(2)\n\t"
        "v_mfma_f32_16x16x32_bf16 v[20:23], v[88:91], v[36:39], v[20:23]\n\t"
        "ds_read_b128 v[88:91], v116 offset:6144\n\t"
        "v_mfma_f32_16x16x32_bf16 v[8:11], a[36:39], v[36:39], v[8:11]\n\t"
        "v_mfma_f32_16x16x32_bf16 v[12:15], a[40:43], v[36:39], v[12:15]\n\t"
        "v_mfma_f32_16x16x32_bf16 v[16:19], a[44:47], v[36:39], v[16:19]\n\t"
        "buffer_load_dwordx4 v[36:39], v117, %[sw], %[t0] offen offset:3072 sc1\n\t"
        "s_waitcnt vmcnt(20) lgkmcnt(2)\n\t"
        "v_mfma_f32_16x16x32_bf16 v[20:23], v[92:95], v[40:43], v[20:23]\n\t"
        "ds_read_b128 v[92:95], v116 offset:7168\n\t"
        "v_mfma_f32_16x16x32_bf16 v[8:11], a[48:51], v[40:43], v[8:11]\n\t"
        "v_mfma_f32_16x16x32_bf16 v[12:15], a[52:55], v[40:43], v[12:15]\n\t"
        "v_mfma_f32_16x16x32_bf16 v[16:19], a[56:59], v[40:43], v[16:19]\n\t"
        "s_add_u32 %[t0], %[ro], 5\n\t"
        "s_and_b32 %[t0], %[t0], 7\n\t"
        "s_lshl_b32 %[t0], %[t0], 12\n\t"
        "s_add_u32 %[t0], %[t0], %[cu]\n\t"
        "buffer_load_dwordx4 v[40:43], v117, %[sw], %[t0] offen offset:0 sc1\n\t"
        "s_waitcnt vmcnt(20) lgkmcnt(2)\n\t"
        "v_mfma_f32_16x16x32_bf16 v[20:23], v[96:99], v[44:47], v[20:23]\n\t"
        "ds_read_b128 v[96:99], v116 offset:8192\n\t"
        "v_mfma_f32_16x16x32_bf16 v[8:11], a[60:63], v[44:47], v[8:11]\n\t"
        "v_mfma_f32_16x16x32_bf16 v[12:15], a[64:67], v[44:47], v[12:15]\n\t"
        "v_mfma_f32_16x16x32_bf16 v[16:19], a[68:71], v[44:47], v[16:19]\n\t"
        "buffer_load_dwordx4 v[44:47], v117, %[sw], %[t0] offen offset:1024 sc1\n\t"
        "s_waitcnt vmcnt(20) lgkmcnt(2)\n\t"
        "v_mfma_f32_16x16x32_bf16 v[20:23], v[88:91], v[48:51], v[20:23]\n\t"
        "ds_read_b128 v[88:91], v116 offset:9216\n\t"
        "v_mfma_f32_16x16x32_bf16 v[8:11], a[72:75], v[48:51], v[8:11]\n\t"
        "v_mfma_f32_16x16x32_bf16 v[12:15], a[76:79], v[48:51], v[12:15]\n\t"
        "v_mfma_f32_16x16x32_bf16 v[16:19], a[80:83], v[48:51], v[16:19]\n\t"
        "buffer_load_dwordx4 v[48:51], v117, %[sw], %[t0] offen offset:2048 sc1\n\t"
        "s_waitcnt vmcnt(20) lgkmcnt(2)\n\t"
        "v_mfma_f32_16x16x32_bf16 v[20:23], v[92:95], v[52:55], v[20:23]\n\t"
        "ds_read_b128 v[92:95], v116 offset:10240\n\t"
        "v_mfma_f32_16x16x32_bf16 v[8:11], a[84:87], v[52:55], v[8:11]\n\t"
        "v_mfma_f32_16x16x32_bf16 v[12:15], a[88:91], v[52:55], v[12:15]\n\t"
        "v_mfma_f32_16x16x32_bf16 v[16:19], a[92:95], v[52:55], v[16:19]\n\t"
        "buffer_load_dwordx4 v[52:55], v117, %[sw], %[t0] offen offset:3072 sc1\n\t"
        "s_waitcnt vmcnt(20) lgkmcnt(2)\n\t"
        "s_barrier\n\t"
        "v_mfma_f32_16x16x32_bf16 v[20:23], v[96:99], v[56:59], v[20:23]\n\t"
        "ds_read_b128 v[96:99], v116 offset:11264\n\t"
        "v_mfma_f32_16x16x32_bf16 v[8:11], a[96:99], v[56:59], v[8:11]\n\t"
        "v_mfma_f32_16x16x32_bf16 v[12:15], a[100:103], v[56:59], v[12:15]\n\t"
        "v_mfma_f32_16x16x32_bf16 v[16:19], a[104:107], v[56:59], v[16:19]\n\t"
        "s_add_u32 %[t0], %[ro], 6\n\t"
        "s_and_b32 %[t0], %[t0], 7\n\t"
        "s_lshl_b32 %[t0], %[t0], 12\n\t"
        "s_add_u32 %[t0], %[t0], %[cu]\n\t"
        "buffer_load_dwordx4 v[56:59], v117, %[sw], %[t0] offen offset:0 sc1\n\t"
        "s_waitcnt vmcnt(20) lgkmcnt(2)\n\t"
        "v_mfma_f32_16x16x32_bf16 v[20:23], v[88:91], v[60:63], v[20:23]\n\t"
        "ds_read_b128 v[88:91], v116 offset:12288\n\t"
        "v_mfma_f32_16x16x32_bf16 v[8:11], a[108:111], v[60:63], v[8:11]\n\t"
        "v_mfma_f32_16x16x32_bf16 v[12:15], a[112:115], v[60:63], v[12:15]\n\t"
        "v_mfma_f32_16x16x32_bf16 v[16:19], a[116:119], v[60:63], v[16:19]\n\t"
        "buffer_load_dwordx4 v[60:63], v117, %[sw], %[t0] offen offset:1024 sc1\n\t"
        "s_waitcnt vmcnt(20) lgkmcnt(2)\n\t"
        "v_mfma_f32_16x16x32_bf16 v[20:23], v[92:95], v[64:67], v[20:23]\n\t"
        "ds_read_b128 v[92:95], v116 offset:13312\n\t"
        "v_mfma_f32_16x16x32_bf16 v[8:11], a[120:123], v[64:67], v[8:11]\n\t"
        "v_mfma_f32_16x16x32_bf16 v[12:15], a[124:127], v[64:67], v[12:15]\n\t"
        "v_mfma_f32_16x16x32_bf16 v[16:19], a[128:131], v[64:67], v[16:19]\n\t"
        "buffer_load_dwordx4 v[64:67], v117, %[sw], %[t0] offen offset:2048 sc1\n\t"
        "s_waitcnt vmcnt(20) lgkmcnt(2)\n\t"
        "v_mfma_f32_16x16x32_bf16 v[20:23], v[96:99], v[68:71], v[20:23]\n\t"
        "ds_read_b128 v[96:99], v116 offset:14336\n\t"
        "v_mfma_f32_16x16x32_bf16 v[8:11], a[132:135], v[68:71], v[8:11]\n\t"
        "v_mfma_f32_16x16x32_bf16 v[12:15], a[136:139], v[68:71], v[12:15]\n\t"
        "v_mfma_f32_16x16x32_bf16 v[16:19], a[140:143], v[68:71], v[16:19]\n\t"
        "buffer_load_dwordx4 v[68:71], v117, %[sw], %[t0] offen offset:3072 sc1\n\t"
        "s_waitcnt vmcnt(20) lgkmcnt(2)\n\t"
        "v_mfma_f32_16x16x32_bf16 v[20:23], v[88:91], v[72:75], v[20:23]\n\t"
        "ds_read_b128 v[88:91], v116 offset:15360\n\t"
        "v_mfma_f32_16x16x32_bf16 v[8:11], a[144:147], v[72:75], v[8:11]\n\t"
        "v_mfma_f32_16x16x32_bf16 v[12:15], a[148:151], v[72:75], v[12:15]\n\t"
        "v_mfma_f32_16x16x32_bf16 v[16:19], a[152:155], v[72:75], v[16:19]\n\t"
        "s_add_u32 %[t0], %[ro], 7\n\t"
        "s_and_b32 %[t0], %[t0], 7\n\t"
        "s_lshl_b32 %[t0], %[t0], 12\n\t"
        "s_add_u32 %[t0], %[t0], %[cu]\n\t"
        "buffer_load_dwordx4 v[72:75], v117, %[sw], %[t0] offen offset:0 sc1\n\t"
        "s_waitcnt vmcnt(13) lgkmcnt(2)\n\t"
        "v_mfma_f32_16x16x32_bf16 v[20:23], v[92:95], v[76:79], v[20:23]\n\t"
        "ds_read_b128 v[92:95], v116 offset:16384\n\t"
        "v_min_u32_dpp v103, v103, v103 quad_perm:[1,0,3,2] row_mask:0xf bank_mask:0xf\n\t"
        "s_nop 0\n\t"
        "v_mfma_f32_16x16x32_bf16 v[8:11], a[156:159], v[76:79], v[8:11]\n\t"
        "v_min_u32_dpp v103, v103, v103 quad_perm:[2,3,0,1] row_mask:0xf bank_mask:0xf\n\t"
        "s_nop 0\n\t"
        "v_mfma_f32_16x16x32_bf16 v[12:15], a[160:163], v[76:79], v[12:15]\n\t"
        "v_min_u32_dpp v103, v103, v103 row_ror:4 row_mask:0xf bank_mask:0xf\n\t"
        "s_nop 0\n\t"
        "v_mfma_f32_16x16x32_bf16 v[16:19], a[164:167], v[76:79], v[16:19]\n\t"
        "buffer_load_dwordx4 v[76:79], v117, %[sw], %[t0] offen offset:1024 sc1\n\t"
        "s_waitcnt vmcnt(20) lgkmcnt(2)\n\t"
        "v_mfma_f32_16x16x32_bf16 v[20:23], v[96:99], v[80:83], v[20:23]\n\t"
        "ds_read_b128 v[96:99], v116 offset:17408\n\t"
        "v_readfirstlane_b32 %[pg], v103\n\t"
        "s_add_u32 %[t1], %[tl], 8\n\t"
        "s_lshl_b32 %[nx], %[t1], 17\n\t"
        "s_lshr_b32 %[t1], %[t1], 1\n\t"
        "v_mfma_f32_16x16x32_bf16 v[8:11], a[168:171], v[80:83], v[8:11]\n\t"
        "s_cmp_gt_u32 %[pg], %[t1]\n\t"
        "s_cselect_b32 %[nx], %[nx], 0xf0000000\n\t"
        "v_mfma_f32_16x16x32_bf16 v[12:15], a[172:175], v[80:83], v[12:15]\n\t"
        "v_mfma_f32_16x16x32_bf16 v[16:19], a[176:179], v[80:83], v[16:19]\n\t"
        "buffer_load_dwordx4 v[80:83], v117, %[sw], %[t0] offen offset:2048 sc1\n\t"
        "s_waitcnt vmcnt(20) lgkmcnt(2)\n\t"
        "v_mfma_f32_16x16x32_bf16 v[20:23], v[88:91], v[84:87], v[20:23]\n\t"
        "ds_read_b128 v[88:91], v116 offset:18432\n\t"
        "v_mfma_f32_16x16x32_bf16 v[8:11], a[180:183], v[84:87], v[8:11]\n\t"
        "v_mfma_f32_16x16x32_bf16 v[12:15], a[184:187], v[84:87], v[12:15]\n\t"
        "v_mfma_f32_16x16x32_bf16 v[16:19], a[188:191], v[84:87], v[16:19]\n\t"
        "buffer_load_dwordx4 v[84:87], v117, %[sw], %[t0] offen offset:3072 sc1\n\t"
        "s_waitcnt vmcnt(15) lgkmcnt(2)\n\t"
        "v_mfma_f32_16x16x32_bf16 v[20:23], v[92:95], v[24:27], v[20:23]\n\t"
        "ds_read_b128 v[92:95], v116 offset:19456\n\t"
        "v_mfma_f32_16x16x32_bf16 v[8:11], a[192:195], v[24:27], v[8:11]\n\t"
        "v_mfma_f32_16x16x32_bf16 v[12:15], a[196:199], v[24:27], v[12:15]\n\t"
        "v_mfma_f32_16x16x32_bf16 v[16:19], a[200:203], v[24:27], v[16:19]\n\t"
        "s_add_u32 %[t0], %[ro], 0\n\t"
        "s_and_b32 %[t0], %[t0], 7\n\t"
        "s_lshl_b32 %[t0], %[t0], 12\n\t"
        "s_add_u32 %[t0], %[t0], %[nx]\n\t"
        "buffer_load_dwordx4 v[24:27], v117, %[sw], %[t0] offen offset:0 sc1\n\t"
        "s_waitcnt vmcnt(15) lgkmcnt(2)\n\t"
        "v_mfma_f32_16x16x32_bf16 v[20:23], v[96:99], v[28:31], v[20:23]\n\t"
        "ds_read_b128 v[96:99], v116 offset:20480\n\t"
        "v_mfma_f32_16x16x32_bf16 v[8:11], a[204:207], v[28:31], v[8:11]\n\t"
        "v_mfma_f32_16x16x32_bf16 v[12:15], a[208:211], v[28:31], v[12:15]\n\t"
        "v_sub_f32_e32 v104, 1.0, v102\n\t"
        "v_mfma_f32_16x16x32_bf16 v[16:19], a[212:215], v[28:31], v[16:19]\n\t"
        "buffer_load_dwordx4 v[28:31], v117, %[sw], %[t0] offen offset:1024 sc1\n\t"
        "s_waitcnt vmcnt(15) lgkmcnt(2)\n\t"
        "v_mfma_f32_16x16x32_bf16 v[20:23], v[88:91], v[32:35], v[20:23]\n\t"
        "ds_read_b128 v[88:91], v116 offset:21504\n\t"
        "v_mfma_f32_16x16x32_bf16 v[8:11], a[216:219], v[32:35], v[8:11]\n\t"
        "v_mfma_f32_16x16x32_bf16 v[12:15], a[220:223], v[32:35], v[12:15]\n\t"
        "v_cvt_pk_bf16_f32 v104, 0, v104\n\t"
        "v_mfma_f32_16x16x32_bf16 v[16:19], a[224:227], v[32:35], v[16:19]\n\t"
        "buffer_load_dwordx4 v[32:35], v117, %[sw], %[t0] offen offset:2048 sc1\n\t"
        "s_waitcnt vmcnt(15) lgkmcnt(2)\n\t"
        "v_mfma_f32_16x16x32_bf16 v[20:23], v[92:95], v[36:39], v[20:23]\n\t"
        "ds_read_b128 v[92:95], v116 offset:22528\n\t"
        "v_mfma_f32_16x16x32_bf16 v[8:11], a[228:231], v[36:39], v[8:11]\n\t"
        "v_mfma_f32_16x16x32_bf16 v[12:15], a[232:235], v[36:39], v[12:15]\n\t"
        "v_lshlrev_b32_e32 v106, 16, v100\n\t"
        "v_mfma_f32_16x16x32_bf16 v[16:19], a[236:239], v[36:39], v[16:19]\n\t"
        "buffer_load_dwordx4 v[36:39], v117, %[sw], %[t0] offen offset:3072 sc1\n\t"
        "s_waitcnt vmcnt(15) lgkmcnt(2)\n\t"
        "v_mfma_f32_16x16x32_bf16 v[20:23], v[96:99], v[40:43], v[20:23]\n\t"
        "ds_read_b128 v[96:99], v116 offset:23552\n\t"
        "v_mfma_f32_16x16x32_bf16 v[8:11], a[240:243], v[40:43], v[8:11]\n\t"
        "v_mfma_f32_16x16x32_bf16 v[12:15], a[244:247], v[40:43], v[12:15]\n\t"
        "v_mul_f32_e32 v106, v102, v106\n\t"
        "v_mfma_f32_16x16x32_bf16 v[16:19], a[248:251], v[40:43], v[16:19]\n\t"
        "s_add_u32 %[t0], %[ro], 1\n\t"
        "s_and_b32 %[t0], %[t0], 7\n\t"
        "s_lshl_b32 %[t0], %[t0], 12\n\t"
        "s_add_u32 %[t0], %[t0], %[nx]\n\t"
        "buffer_load_dwordx4 v[40:43], v117, %[sw], %[t0] offen offset:0 sc1\n\t"
        "s_waitcnt vmcnt(15) lgkmcnt(2)\n\t"
        "v_mfma_f32_16x16x32_bf16 v[20:23], v[88:91], v[44:47], v[20:23]\n\t"
        "ds_read_b128 v[88:91], v116 offset:24576\n\t"
        "v_mfma_f32_16x16x32_bf16 v[8:11], a[252:255], v[44:47], v[8:11]\n\t"
        "v_mfma_f32_16x16x32_bf16 v[12:15], v[128:131], v[44:47], v[12:15]\n\t"
        "v_cvt_pk_bf16_f32 v106, 0, v106\n\t"
        "v_mfma_f32_16x16x32_bf16 v[16:19], v[132:135], v[44:47], v[16:19]\n\t"
        "buffer_load_dwordx4 v[44:47], v117, %[sw], %[t0] offen offset:1024 sc1\n\t"
        "s_waitcnt vmcnt(15) lgkmcnt(2)\n\t"
        "v_mfma_f32_16x16x32_bf16 v[20:23], v[92:95], v[48:51], v[20:23]\n\t"
        "ds_read_b128 v[92:95], v116 offset:25600\n\t"
        "v_mfma_f32_16x16x32_bf16 v[8:11], v[136:139], v[48:51], v[8:11]\n\t"
        "v_mfma_f32_16x16x32_bf16 v[12:15], v[140:143], v[48:51], v[12:15]\n\t"
        "v_and_b32_e32 v107, 0xffff0000, v100\n\t"
        "v_mfma_f32_16x16x32_bf16 v[16:19], v[144:147], v[48:51], v[16:19]\n\t"
        "buffer_load_dwordx4 v[48:51], v117, %[sw], %[t0] offen offset:2048 sc1\n\t"
        "s_waitcnt vmcnt(15) lgkmcnt(2)\n\t"
        "v_mfma_f32_16x16x32_bf16 v[20:23], v[96:99], v[52:55], v[20:23]\n\t"
        "ds_read_b128 v[96:99], v116 offset:26624\n\t"
        "v_mfma_f32_16x16x32_bf16 v[8:11], v[148:151], v[52:55], v[8:11]\n\t"
        "v_mfma_f32_16x16x32_bf16 v[12:15], v[152:155], v[52:55], v[12:15]\n\t"
        "v_mul_f32_e32 v107, v102, v107\n\t"
        "v_mfma_f32_16x16x32_bf16 v[16:19], v[156:159], v[52:55], v[16:19]\n\t"
        "buffer_load_dwordx4 v[52:55], v117, %[sw], %[t0] offen offset:3072 sc1\n\t"
        "s_waitcnt vmcnt(15) lgkmcnt(2)\n\t"
        "v_mfma_f32_16x16x32_bf16 v[20:23], v[88:91], v[56:59], v[20:23]\n\t"
        "ds_read_b128 v[88:91], v116 offset:27648\n\t"
        "v_mfma_f32_16x16x32_bf16 v[8:11], v[160:163], v[56:59], v[8:11]\n\t"
        "v_mfma_f32_16x16x32_bf16 v[12:15], v[164:167], v[56:59], v[12:15]\n\t"
        "v_cvt_pk_bf16_f32 v107, 0, v107\n\t"
        "v_mfma_f32_16x16x32_bf16 v[16:19], v[168:171], v[56:59], v[16:19]\n\t"
        "s_add_u32 %[t0], %[ro], 2\n\t"
        "s_and_b32 %[t0], %[t0], 7\n\t"
        "s_lshl_b32 %[t0], %[t0], 12\n\t"
        "s_add_u32 %[t0], %[t0], %[nx]\n\t"
        "buffer_load_dwordx4 v[56:59], v117, %[sw], %[t0] offen offset:0 sc1\n\t"
        "s_waitcnt vmcnt(15) lgkmcnt(2)\n\t"
        "v_mfma_f32_16x16x32_bf16 v[20:23], v[92:95], v[60:63], v[20:23]\n\t"
        "ds_read_b128 v[92:95], v116 offset:28672\n\t"
        "v_mfma_f32_16x16x32_bf16 v[8:11], v[172:175], v[60:63], v[8:11]\n\t"
        "v_mfma_f32_16x16x32_bf16 v[12:15], v[176:179], v[60:63], v[12:15]\n\t"
        "v_lshlrev_b32_e32 v108, 16, v101\n\t"
        "v_mfma_f32_16x16x32_bf16 v[16:19], v[180:183], v[60:63], v[16:19]\n\t"
        "buffer_load_dwordx4 v[60:63], v117, %[sw], %[t0] offen offset:1024 sc1\n\t"
        "s_waitcnt vmcnt(15) lgkmcnt(2)\n\t"
        "v_mfma_f32_16x16x32_bf16 v[20:23], v[96:99], v[64:67], v[20:23]\n\t"
        "ds_read_b128 v[96:99], v116 offset:29696\n\t"
        "v_mfma_f32_16x16x32_bf16 v[8:11], v[184:187], v[64:67], v[8:11]\n\t"
        "v_mfma_f32_16x16x32_bf16 v[12:15], v[188:191], v[64:67], v[12:15]\n\t"
        "v_mul_f32_e32 v108, v102, v108\n\t"
        "v_mfma_f32_16x16x32_bf16 v[16:19], v[192:195], v[64:67], v[16:19]\n\t"
        "buffer_load_dwordx4 v[64:67], v117, %[sw], %[t0] offen offset:2048 sc1\n\t"
        "s_waitcnt vmcnt(15) lgkmcnt(2)\n\t"
        "v_mfma_f32_16x16x32_bf16 v[20:23], v[88:91], v[68:71], v[20:23]\n\t"
        "ds_read_b128 v[88:91], v116 offset:30720\n\t"
        "v_mfma_f32_16x16x32_bf16 v[8:11], v[196:199], v[68:71], v[8:11]\n\t"
        "v_mfma_f32_16x16x32_bf16 v[12:15], v[200:203], v[68:71], v[12:15]\n\t"
        "v_cvt_pk_bf16_f32 v108, 0, v108\n\t"
        "v_mfma_f32_16x16x32_bf16 v[16:19], v[204:207], v[68:71], v[16:19]\n\t"
        "buffer_load_dwordx4 v[68:71], v117, %[sw], %[t0] offen offset:3072 sc1\n\t"
        "s_waitcnt vmcnt(15) lgkmcnt(2)\n\t"
        "v_mfma_f32_16x16x32_bf16 v[20:23], v[92:95], v[72:75], v[20:23]\n\t"
        "ds_read_b128 v[92:95], v116 offset:31744\n\t"
        "v_mfma_f32_16x16x32_bf16 v[8:11], v[208:211], v[72:75], v[8:11]\n\t"
        "v_mfma_f32_16x16x32_bf16 v[12:15], v[212:215], v[72:75], v[12:15]\n\t"
        "v_and_b32_e32 v109, 0xffff0000, v101\n\t"
        "v_mfma_f32_16x16x32_bf16 v[16:19], v[216:219], v[72:75], v[16:19]\n\t"
        "s_add_u32 %[t0], %[ro], 3\n\t"
        "s_and_b32 %[t0], %[t0], 7\n\t"
        "s_lshl_b32 %[t0], %[t0], 12\n\t"
        "s_add_u32 %[t0], %[t0], %[nx]\n\t"
        "buffer_load_dwordx4 v[72:75], v117, %[sw], %[t0] offen offset:0 sc1\n\t"
        "s_waitcnt vmcnt(15) lgkmcnt(2)\n\t"
        "v_mfma_f32_16x16x32_bf16 v[20:23], v[96:99], v[76:79], v[20:23]\n\t"
        "v_mfma_f32_16x16x32_bf16 v[8:11], v[220:223], v[76:79], v[8:11]\n\t"
        "v_mfma_f32_16x16x32_bf16 v[12:15], v[224:227], v[76:79], v[12:15]\n\t"
        "v_mul_f32_e32 v109, v102, v109\n\t"
        "v_mfma_f32_16x16x32_bf16 v[16:19], v[228:231], v[76:79], v[16:19]\n\t"
        "buffer_load_dwordx4 v[76:79], v117, %[sw], %[t0] offen offset:1024 sc1\n\t"
        "s_waitcnt vmcnt(15) lgkmcnt(1)\n\t"
        "v_mfma_f32_16x16x32_bf16 v[20:23], v[88:91], v[80:83], v[20:23]\n\t"
        "ds_read_b128 v[88:91], v116 offset:0\n\t"
        "v_mfma_f32_16x16x32_bf16 v[8:11], v[232:235], v[80:83], v[8:11]\n\t"
        "v_mfma_f32_16x16x32_bf16 v[12:15], v[236:239], v[80:83], v[12:15]\n\t"
        "v_cvt_pk_bf16_f32 v109, 0, v109\n\t"
        "v_mfma_f32_16x16x32_bf16 v[16:19], v[240:243], v[80:83], v[16:19]\n\t"
        "buffer_load_dwordx4 v[80:83], v117, %[sw], %[t0] offen offset:2048 sc1\n\t"
        "s_waitcnt vmcnt(15) lgkmcnt(1)\n\t"
        "v_mfma_f32_16x16x32_bf16 v[20:23], v[92:95], v[84:87], v[20:23]\n\t"
        "ds_read_b128 v[92:95], v116 offset:1024\n\t"
        "v_mfma_f32_16x16x32_bf16 v[8:11], v[244:247], v[84:87], v[8:11]\n\t"
        "ds_read_b128 v[96:99], v116 offset:2048\n\t"
        "v_mfma_f32_16x16x32_bf16 v[12:15], v[248:251], v[84:87], v[12:15]\n\t"
        "v_mfma_f32_16x16x32_bf16 v[16:19], v[252:255], v[84:87], v[16:19]\n\t"
        "buffer_load_dwordx4 v[84:87], v117, %[sw], %[t0] offen offset:3072 sc1\n\t"
        "LP_epi%=:\n\t"
        "s_lshl_b32 %[t0], %[tl], 15\n\t"
        "s_nop 0\n\t"
        "ds_write_b128 v122, v[20:23] offset:192\n\t"
        "s_nop 0\n\t"
        "ds_write_b128 v122, v[8:11] offset:0\n\t"
        "s_nop 0\n\t"
        "ds_write_b128 v122, v[12:15] offset:64\n\t"
        "ds_write_b128 v122, v[16:19] offset:128\n\t"
        "s_waitcnt lgkmcnt(0)\n\t"
        "s_barrier\n\t"
        "ds_read_b128 v[8:11], v123 offset:0\n\t"
        "ds_read_b128 v[12:15], v123 offset:4352\n\t"
        "ds_read_b128 v[16:19], v123 offset:8704\n\t"
        "ds_read_b128 v[20:23], v123 offset:13056\n\t"
        "s_waitcnt lgkmcnt(2)\n\t"
        "v_pk_add_f32 v[8:9], v[8:9], v[12:13]\n\t"
        "v_pk_add_f32 v[10:11], v[10:11], v[14:15]\n\t"
        "s_waitcnt lgkmcnt(0)\n\t"
        "v_pk_add_f32 v[16:17], v[16:17], v[20:21]\n\t"
        "v_pk_add_f32 v[18:19], v[18:19], v[22:23]\n\t"
        "v_pk_add_f32 v[8:9], v[8:9], v[16:17]\n\t"
        "v_pk_add_f32 v[10:11], v[10:11], v[18:19]\n\t"
        "v_cvt_pk_bf16_f32 v8, 0, v8\n\t"
        "v_cvt_pk_bf16_f32 v9, 0, v9\n\t"
        "v_cvt_pk_bf16_f32 v10, 0, v10\n\t"
        "v_cvt_pk_bf16_f32 v11, 0, v11\n\t"
        "v_mul_f32_e32 v8, v104, v8\n\t"
        "v_mul_f32_e32 v9, v104, v9\n\t"
        "v_mul_f32_e32 v10, v104, v10\n\t"
        "v_mul_f32_e32 v11, v104, v11\n\t"
        "v_cvt_pk_bf16_f32 v8, 0, v8\n\t"
        "v_cvt_pk_bf16_f32 v9, 0, v9\n\t"
        "v_cvt_pk_bf16_f32 v10, 0, v10\n\t"
        "v_cvt_pk_bf16_f32 v11, 0, v11\n\t"
        "v_add_f32_e32 v8, v106, v8\n\t"
        "v_add_f32_e32 v9, v107, v9\n\t"
        "v_add_f32_e32 v10, v108, v10\n\t"
        "v_add_f32_e32 v11, v109, v11\n\t"
        "v_cvt_pk_bf16_f32 v104, v8, v9\n\t"
        "v_cvt_pk_bf16_f32 v105, v10, v11\n\t"
        "buffer_store_dwordx2 v[104:105], v121, %[su], %[t0] offen sc1\n\t"
        "s_add_u32 %[tl], %[tl], 8\n\t"
        "s_cmp_ge_u32 %[tl], 128\n\t"
        "s_cbranch_scc1 LP_end%=\n\t"
        "s_cmp_lg_u32 %[nx], 0xf0000000\n\t"
        "s_cbranch_scc1 LP_tile%=\n\t"
        "s_branch LP_wait%=\n\t"
        "LP_end%=:\n\t"
        "s_waitcnt vmcnt(0) lgkmcnt(0)\n\t"
        ".Lk_end_P:\n\t"
        : [t0] "=&s"(t0), [t1] "=&s"(t1), [tl] "+s"(tl), [pg] "=&s"(pg), [cu] "=&s"(cu), [nx] "=&s"(nx), [sp] "=&s"(sp), [re] "=&s"(re)
        : [so] "s"(so), [sw] "s"(sw), [su] "s"(su), [cv] "s"(F_CVY + nt * 128), [ro] "s"((nt + 2 * rp) & 7),
          "{v110}"((w * 1024 + (l >> 3)) * 2048 + nt * 128 + (l & 7) * 16), "{v111}"(stg + ((l * 16) ^ ((l & 48) << 1))), "{v112}"(lr), "{v113}"(lr ^ 32u), "{v114}"(lr ^ 64u), "{v115}"(lr ^ 96u),
          "{v116}"(lb + w * 32768 + l * 16), "{v117}"(W_OX + w * 32768 + l * 16), "{v118}"(F_PROG + (w * 8 + (l & 7)) * 128),
          "{v119}"(W_A + ((nt * 2 + (n >> 3)) * 4 + 2 * (n & 1)) * 32768 + w * 4096 + ((n & 7) >> 1) * 16 + g * 4), "{v120}"(W_GATE + (4 * w + g) * 4), "{v121}"((4 * w + g) * 2048 + nt * 128 + n * 8),
          "{v122}"(lb + 131072 + w * 4352 + n * 272 + g * 16), "{v123}"(lb + 131072 + (4 * w + g) * 272 + n * 16)
        : "memory", "scc", "vcc", "v8", "v9", "v10", "v11", "v12", "v13", "v14", "v15", "v16", "v17", "v18", "v19", "v20", "v21", "v22", "v23", "v24", "v25", "v26", "v27", "v28", "v29", "v30", "v31", "v32", "v33", "v34", "v35", "v36", "v37", "v38", "v39", "v40", "v41", "v42", "v43", "v44", "v45", "v46", "v47", "v48", "v49", "v50", "v51", "v52", "v53", "v54", "v55", "v56", "v57", "v58", "v59", "v60", "v61", "v62", "v63", "v64", "v65", "v66", "v67", "v68", "v69", "v70", "v71", "v72", "v73", "v74", "v75", "v76", "v77", "v78", "v79", "v80", "v81", "v82", "v83", "v84", "v85", "v86", "v87", "v88", "v89", "v90", "v91", "v92", "v93", "v94", "v95", "v96", "v97", "v98", "v99", "v100", "v101", "v102", "v103", "v104", "v105", "v106", "v107", "v108", "v109", "v124", "v125", "v126", "v127", "v128", "v129", "v130", "v131", "v132", "v133", "v134", "v135", "v136", "v137", "v138", "v139", "v140", "v141", "v142", "v143", "v144", "v145", "v146", "v147", "v148", "v149", "v150", "v151", "v152", "v153", "v154", "v155", "v156", "v157", "v158", "v159", "v160", "v161", "v162", "v163", "v164", "v165", "v166", "v167", "v168", "v169", "v170", "v171", "v172", "v173", "v174", "v175", "v176", "v177", "v178", "v179", "v180", "v181", "v182", "v183", "v184", "v185", "v186", "v187", "v188", "v189", "v190", "v191", "v192", "v193", "v194", "v195", "v196", "v197", "v198", "v199", "v200", "v201", "v202", "v203", "v204", "v205", "v206", "v207", "v208", "v209", "v210", "v211", "v212", "v213", "v214", "v215", "v216", "v217", "v218", "v219", "v220", "v221", "v222", "v223", "v224", "v225", "v226", "v227", "v228", "v229", "v230", "v231", "v232", "v233", "v234", "v235", "v236", "v237", "v238", "v239", "v240", "v241", "v242", "v243", "v244", "v245", "v246", "v247", "v248", "v249", "v250", "v251", "v252", "v253", "v254", "v255", "a0", "a1", "a2", "a3", "a4", "a5", "a6", "a7", "a8", "a9", "a10", "a11", "a12", "a13", "a14", "a15", "a16", "a17", "a18", "a19", "a20", "a21", "a22", "a23", "a24", "a25", "a26", "a27", "a28", "a29", "a30", "a31", "a32", "a33", "a34", "a35", "a36", "a37", "a38", "a39", "a40", "a41", "a42", "a43", "a44", "a45", "a46", "a47", "a48", "a49", "a50", "a51", "a52", "a53", "a54", "a55", "a56", "a57", "a58", "a59", "a60", "a61", "a62", "a63", "a64", "a65", "a66", "a67", "a68", "a69", "a70", "a71", "a72", "a73", "a74", "a75", "a76", "a77", "a78", "a79", "a80", "a81", "a82", "a83", "a84", "a85", "a86", "a87", "a88", "a89", "a90", "a91", "a92", "a93", "a94", "a95", "a96", "a97", "a98", "a99", "a100", "a101", "a102", "a103", "a104", "a105", "a106", "a107", "a108", "a109", "a110", "a111", "a112", "a113", "a114", "a115", "a116", "a117", "a118", "a119", "a120", "a121", "a122", "a123", "a124", "a125", "a126", "a127", "a128", "a129", "a130", "a131", "a132", "a133", "a134", "a135", "a136", "a137", "a138", "a139", "a140", "a141", "a142", "a143", "a144", "a145", "a146", "a147", "a148", "a149", "a150", "a151", "a152", "a153", "a154", "a155", "a156", "a157", "a158", "a159", "a160", "a161", "a162", "a163", "a164", "a165", "a166", "a167", "a168", "a169", "a170", "a171", "a172", "a173", "a174", "a175", "a176", "a177", "a178", "a179", "a180", "a181", "a182", "a183", "a184", "a185", "a186", "a187", "a188", "a189", "a190", "a191", "a192", "a193", "a194", "a195", "a196", "a197", "a198", "a199", "a200", "a201", "a202", "a203", "a204", "a205", "a206", "a207", "a208", "a209", "a210", "a211", "a212", "a213", "a214", "a215", "a216", "a217", "a218", "a219", "a220", "a221", "a222", "a223", "a224", "a225", "a226", "a227", "a228", "a229", "a230", "a231", "a232", "a233", "a234", "a235", "a236", "a237", "a238", "a239", "a240", "a241", "a242", "a243", "a244", "a245", "a246", "a247", "a248", "a249", "a250", "a251", "a252", "a253", "a254", "a255");
}
__device__ __forceinline__ void bulk_block(const Params& p, rsrc_t r, unsigned char* lds, const int wv, const int j) {
    proj_block(p, lds, wv, (j & 6) * 4 + (j >> 4), ((j >> 2) & 2) | (j & 1));
    const int un = ((j & 7) >> 1) * 32 + (j >> 3) * 2 + (j & 1);
    gate_block(p, r, j, wv);
    conv_prep(p, r, wv, un >> 2, un & 3);
    if (!conv_core(p, r, lds, wv, un)) return;
    if (!wait_word(r, F_CONV, 128u, 4096u)) return;
    out_block(p, lds, wv, ((j & 7) >> 1) + (j & 1) * 4, j >> 3);
}

__device__ __forceinline__ void rec_block(const Params& p, rsrc_t r, unsigned char* lds, const int wv, const int b) {
    const int xq = (b & 7) >> 1, t = (b >> 3) * 2 + (b & 1);
    const unsigned lb = __builtin_amdgcn_readfirstlane((unsigned)(size_t)lds);
    if (t < 8) rec_a(p, lb, wv, xq * 8 + t, xq);
    else if (t < 24) rec_b(p, lb, wv, (xq & 1) * 16 + t - 8, xq >> 1, xq);
    else {
        const int hi = xq * 8 + t - 24;
        rec_helper(p, r, lb, wv, hi);
    }
}

__global__ void __launch_bounds__(256, 1) k_main(Params p) {
    extern __shared__ __attribute__((aligned(16))) unsigned char lds[];
    rsrc_t r = __builtin_amdgcn_make_buffer_rsrc(p.ws, 0, W_TOTAL, 0x00020000);
    const int wv = __builtin_amdgcn_readfirstlane(threadIdx.x >> 6);
    {
        unsigned long long pc; int ea, eb, eg;
        asm volatile("s_getpc_b64 %0\n.Lk_begin:\n\ts_mov_b32 %1, .Lk_end_A-.Lk_begin\n\ts_mov_b32 %2, .Lk_end_B-.Lk_begin\n\ts_mov_b32 %3, .Lk_end_P-.Lk_begin" : "=s"(pc), "=s"(ea), "=s"(eb), "=s"(eg));
        const int o = ((blockIdx.x >> 3) * 64 + (threadIdx.x >> 2)) * 128;
        if ((threadIdx.x & 3) == 0 && o < max(ea, max(eb, eg)) - 4) touch_l2((const unsigned char*)pc + o, __builtin_amdgcn_readfirstlane((unsigned)(size_t)lds) + 1024);
    }
    if (blockIdx.x == 0 && wv == 1) {
        const int l = threadIdx.x & 63; const v4u z = {0u, 0u, 0u, 0u};
#pragma unroll
        for (int i = 0; i < 6; ++i) st16(r, F_CONV + i * 1024 + l * 16, z);
        DRAIN();
        for (unsigned spin = 0; spin < (1u << 20); ++spin) {
            if (l < 8) __hip_atomic_fetch_add((unsigned*)(p.ws + F_BEAT + l * 128), 1u, __ATOMIC_RELAXED, __HIP_MEMORY_SCOPE_AGENT);
            unsigned c = ld4(r, l < 8 ? F_TKT + l * 64 : OOB);
            c += __shfl_xor(c, 1); c += __shfl_xor(c, 2); c += __shfl_xor(c, 4);
            if (__builtin_amdgcn_readfirstlane(c) >= 256u) break; }
    }
    if (threadIdx.x == 0) {
        const unsigned bo = F_BEAT + (blockIdx.x & 7) * 128, b0 = ld4(r, bo);
        for (unsigned spin = 0; spin < (1u << 22); ++spin) { MEMBAR(); if (ld4(r, bo) != b0) break; }
        MEMBAR();
        unsigned xcc; asm volatile("s_getreg_b32 %0, hwreg(HW_REG_XCC_ID)" : "=s"(xcc)); xcc &= 7u;
        unsigned* tk = (unsigned*)(p.ws + F_TKT);
        unsigned t = __hip_atomic_fetch_add(tk + xcc * 16, 1u, __ATOMIC_RELAXED, __HIP_MEMORY_SCOPE_AGENT);
        if (t >= 32u) {
            unsigned o = __hip_atomic_fetch_add(tk + 128, 1u, __ATOMIC_RELAXED, __HIP_MEMORY_SCOPE_AGENT);
            for (unsigned spin = 0; spin < (1u << 22); ++spin) { unsigned tot = 0u;
                for (int x = 0; x < 8; ++x) tot += __hip_atomic_load(tk + x * 16, __ATOMIC_RELAXED, __HIP_MEMORY_SCOPE_AGENT);
                if (tot >= 256u) break; }
            for (int x = 0; x < 8; ++x) { const unsigned cnt = __hip_atomic_load(tk + x * 16, __ATOMIC_RELAXED, __HIP_MEMORY_SCOPE_AGENT), fr = cnt < 32u ? 32u - cnt : 0u;
                if (o < fr) { xcc = (unsigned)x; t = cnt + o; break; }
                o -= fr; }
        }
        *(volatile int*)lds = (int)((xcc >> 2) * 128u + (t >> 1) * 8u + (xcc & 3u) * 2u + (t & 1u));
    }
    DRAIN();
    __syncthreads();
    const int role = __builtin_amdgcn_readfirstlane(*(volatile int*)lds) & 255;
    __syncthreads();
    if (role >= 128) bulk_block(p, r, lds, wv, role - 128); else rec_block(p, r, lds, wv, role);
}

extern "C" void kernel_launch(void* const* d_in, const int* in_sizes, int n_in, void* d_out, int out_size, void* d_ws, size_t ws_size, hipStream_t stream) {
    Params p;
    p.Aq = (const bf16_t*)d_in[0]; p.Ak = (const bf16_t*)d_in[1]; p.Av = (const bf16_t*)d_in[2];
    p.shift_k = (const bf16_t*)d_in[3]; p.ssm_k = (const bf16_t*)d_in[4]; p.ssm_D = (const bf16_t*)d_in[5];
    p.mem0 = (const bf16_t*)d_in[7]; p.Wq = (const bf16_t*)d_in[8]; p.Wk = (const bf16_t*)d_in[9];
    p.Wv = (const bf16_t*)d_in[10]; p.Wo = (const bf16_t*)d_in[11]; p.gate_w = (const bf16_t*)d_in[13];
    p.out = (bf16_t*)d_out; p.ws = (unsigned char*)d_ws;
    (void)hipFuncSetAttribute((const void*)k_main, hipFuncAttributeMaxDynamicSharedMemorySize, L_TOTAL);
    k_main<<<256, 256, L_TOTAL, stream>>>(p);
}
```

```cpp
#include <hip/hip_runtime.h>
#include <stdint.h>
typedef unsigned short bf16_t;
using f32x16 = __attribute__((ext_vector_type(16))) float;
typedef unsigned v4u __attribute__((ext_vector_type(4)));
typedef unsigned v2u __attribute__((ext_vector_type(2)));
typedef __amdgpu_buffer_rsrc_t rsrc_t;
typedef __bf16 hbf2 __attribute__((ext_vector_type(2)));
typedef float f32x2 __attribute__((ext_vector_type(2)));
__device__ __forceinline__ unsigned pack2(float a, float b) { f32x2 v = {a, b}; return __builtin_bit_cast(unsigned, __builtin_convertvector(v, hbf2)); }
__device__ __forceinline__ float rbfh(float f) { return __uint_as_float(pack2(f, 0.f) << 16); }
__device__ __forceinline__ float blo(unsigned x) { return __uint_as_float(x << 16); }
__device__ __forceinline__ float bhi(unsigned x) { return __uint_as_float(x & 0xFFFF0000u); }

constexpr int S = 2048, D = 1024;

constexpr unsigned F_KVQ = 0;
constexpr unsigned F_CONV = 32768;
constexpr unsigned F_ERR = 33024;
constexpr unsigned F_CAL = 33792;
constexpr unsigned F_RDY0 = 34304;
constexpr unsigned F_TKT = 34816;
constexpr unsigned F_GO = 36864;
constexpr unsigned F_BEAT = 40960;
constexpr unsigned F_PROG = 49152;
constexpr unsigned F_CVY = 57344;
constexpr unsigned W_A = 8568832;
constexpr unsigned W_GATE = 65536;
constexpr unsigned W_QX = W_A + S * D * 2 + 8192;
constexpr unsigned W_KX = W_QX + S * 4096 * 2;
constexpr unsigned W_VF = W_KX + S * 4096 * 2;
constexpr unsigned W_OX = W_VF + S * 4096 * 2;
constexpr unsigned W_Y = W_OX + S * 4096 * 2;
constexpr unsigned W_U = 115531776;
constexpr unsigned W_TOTAL = W_U + S * D * 2;
constexpr unsigned OOB = 0xF0000000u;
constexpr int L_TOTAL = 163840;

__device__ __forceinline__ v4u ld16(rsrc_t r, unsigned off) { return __builtin_amdgcn_raw_buffer_load_b128(r, off, 0, 16); }
__device__ __forceinline__ v2u ld8(rsrc_t r, unsigned off) { return __builtin_amdgcn_raw_buffer_load_b64(r, off, 0, 16); }
__device__ __forceinline__ unsigned ld4(rsrc_t r, unsigned off) { return __builtin_amdgcn_raw_buffer_load_b32(r, off, 0, 16); }
__device__ __forceinline__ void st16(rsrc_t r, unsigned off, v4u v) { __builtin_amdgcn_raw_buffer_store_b128(v, r, off, 0, 16); }
__device__ __forceinline__ void st4(rsrc_t r, unsigned off, unsigned v) { __builtin_amdgcn_raw_buffer_store_b32(v, r, off, 0, 16); }
#define D1 128
#define D2 77
#define LT_A 1
#define PEN 48
#define CAL_T 450u
#define XSLP 3
#define T2U 2
#define TPRE 48
#define MEMBAR() asm volatile("" ::: "memory")
#define DRAIN() asm volatile("s_waitcnt vmcnt(0)" ::: "memory")
__device__ __forceinline__ int otid_(int wv) { int t; asm volatile("v_mbcnt_lo_u32_b32 %0, -1, 0\n\tv_mbcnt_hi_u32_b32 %0, -1, %0" : "=v"(t)); return wv * 64 + t; }
#define otid() otid_(wv)
__device__ __forceinline__ bool wait_word(rsrc_t r, unsigned off, unsigned want, unsigned code) {
    for (unsigned spin = 0;; ++spin) { MEMBAR();
        const unsigned v = __builtin_amdgcn_readfirstlane(ld4(r, off));
        if (v >= want) { MEMBAR(); return true; }
        if (spin > (1u << 20)) { st4(r, F_ERR, code); return false; }
        __builtin_amdgcn_s_sleep(8); }
}
__device__ __forceinline__ void touch_l2(const void* q, unsigned ldsdst) { asm volatile("s_mov_b32 m0, %1\n\ts_nop 0\n\tglobal_load_lds_dword %0, off" :: "v"(q), "s"(ldsdst) : "m0", "memory"); }
__device__ __forceinline__ unsigned rtnow() { return (unsigned)__builtin_amdgcn_s_memrealtime(); }

struct Params {
    const bf16_t *Aq, *Ak, *Av, *shift_k, *ssm_k, *ssm_D, *mem0, *Wq, *Wk, *Wv, *Wo, *gate_w;
    bf16_t* out; unsigned char* ws;
};
__device__ __forceinline__ unsigned ho_base(const void* ws) { const size_t a = (size_t)ws; return (unsigned)((((a + W_TOTAL + 0x7FFFFFu) >> 23) << 23) - a); }
__device__ __forceinline__ v4u mk_srd(const void* p, unsigned bytes) { const size_t a = (size_t)p; v4u d;
    d.x = __builtin_amdgcn_readfirstlane((unsigned)a); d.y = __builtin_amdgcn_readfirstlane((unsigned)(a >> 32) & 0xFFFFu); d.z = __builtin_amdgcn_readfirstlane(bytes); d.w = 0x00020000u; return d; }
constexpr unsigned HO_BYTES = 2097152;
constexpr int L_KV = 90112, L_MS = 139264, L_IMG = 155648, IMG_PS = 1056, IMG_MS = 2112, LB_R = 8192, LB_STG = 16384, LB_STGP = 8704;
static_assert(F_KVQ == 0 && F_ERR == 33024 && F_CONV == 32768 && F_CAL == 33792 && F_RDY0 == 34304 && F_GO == 36864 && F_PROG == 49152 && W_QX == 12771328 && W_KX == 29548544 && W_OX == 63102976 && W_VF == 46325760 && L_KV == 90112 && L_MS == 139264 && L_IMG == 155648 && IMG_PS == 1056 && IMG_MS == 2112 && LB_R == 8192 && LB_STG == 16384 && LB_STGP == 8704, "constants of the generated code");
#define REC_S_IN "{s[8:11]}"(sh), "{s[12:15]}"(sw), "{s[16:19]}"(s1), "{s[20:23]}"(s2), "{s29}"(D1), "{s30}"(D2), "{s43}"(w), "{s44}"(id), "{s49}"(rb), "{s50}"(PEN), "{s51}"(LT_A), "{s45}"(F_GO + (cp * 4 + w) * 128)
__device__ __forceinline__ void rec_a(const Params& p, unsigned lb, const int wv, const int id, const int cp) {
    const int w = wv, l = otid() & 63, n = l & 15, g = l >> 4, h = id, rb = id;
    const v4u sh = mk_srd(p.ws + ho_base(p.ws), HO_BYTES - 8192), sw = mk_srd(p.ws, W_TOTAL), s1 = mk_srd(w < 2 ? p.Wq : p.Wk, 1024 * 4096 * 2), s2 = s1;
    const int r8 = l >> 3, pc = l & 7, pp = n >> 3, bb = (n >> 2) & 1, f = (g & 1) | (pp << 1);
    const unsigned stg = lb + 131072 + w * 8192;
    const unsigned g_w = r8 * 8192 + h * 256 + (w & 1) * 128 + pc * 16;
    const unsigned wb0 = stg + r8 * 128 + (((pc >> 1) ^ (r8 >> 2)) << 5) + (pc & 1) * 16, wb1 = wb0 ^ 64u;
    const unsigned rb0 = stg + (16 * (g >> 1) + 8 * pp + 4 * (g & 1) + bb) * 128 + (f << 5) + (n & 3) * 8;
    const unsigned a_mem = cp * 32768 + (w >> 1) * 16384 + (w & 1) * 4096 + l * 16;
    const unsigned l_ms = lb + L_MS + l * 16;
    const unsigned l_iw = lb + L_IMG + (w >> 1) * IMG_MS + (g >> 1) * IMG_PS + (64 * (w & 1) + n) * 8 + (g & 1) * 4;
    const unsigned l_iq = lb + L_IMG + (n & 1) * IMG_PS + (8 * g + (n >> 2)) * 8;
    const unsigned a_vm = (16 + (h >> 2)) * 16384 + (h & 3) * 2048 + w * 512 + (l & 31) * 16;
    const unsigned a_o = n < 8 ? (8 + (n >> 2) * 4 + (h >> 3)) * 16384 + (h & 7) * 1024 + w * 256 + (n & 3) * 64 + g * 16 : OOB;
    const unsigned a_ox = (((w & 1) * 32 + h) * 4 + w) * 1024 + l * 16;
    asm volatile(
        "s_nop 4\n\t"
        "s_lshl_b32 s34, s43, 2\n\t"
        "s_add_u32 s34, s34, 0\n\t"
        "s_and_b32 s34, s34, 15\n\t"
        "s_lshl_b32 s34, s34, 19\n\t"
        "buffer_load_dwordx4 v[224:227], v96, s[16:19], s34 offen nt\n\t"
        "s_add_u32 s34, s34, 0x10000\n\t"
        "buffer_load_dwordx4 v[228:231], v96, s[16:19], s34 offen nt\n\t"
        "s_add_u32 s34, s34, 0x10000\n\t"
        "buffer_load_dwordx4 v[232:235], v96, s[16:19], s34 offen nt\n\t"
        "s_add_u32 s34, s34, 0x10000\n\t"
        "buffer_load_dwordx4 v[236:239], v96, s[16:19], s34 offen nt\n\t"
        "s_add_u32 s34, s34, 0x10000\n\t"
        "buffer_load_dwordx4 v[240:243], v96, s[16:19], s34 offen nt\n\t"
        "s_add_u32 s34, s34, 0x10000\n\t"
        "buffer_load_dwordx4 v[244:247], v96, s[16:19], s34 offen nt\n\t"
        "s_add_u32 s34, s34, 0x10000\n\t"
        "buffer_load_dwordx4 v[248:251], v96, s[16:19], s34 offen nt\n\t"
        "s_add_u32 s34, s34, 0x10000\n\t"
        "buffer_load_dwordx4 v[252:255], v96, s[16:19], s34 offen nt\n\t"
        "s_lshl_b32 s34, s43, 2\n\t"
        "s_add_u32 s34, s34, 1\n\t"
        "s_and_b32 s34, s34, 15\n\t"
        "s_lshl_b32 s34, s34, 19\n\t"
        "buffer_load_dwordx4 v[0:3], v96, s[16:19], s34 offen nt\n\t"
        "s_add_u32 s34, s34, 0x10000\n\t"
        "buffer_load_dwordx4 v[4:7], v96, s[16:19], s34 offen nt\n\t"
        "s_add_u32 s34, s34, 0x10000\n\t"
        "buffer_load_dwordx4 v[8:11], v96, s[16:19], s34 offen nt\n\t"
        "s_add_u32 s34, s34, 0x10000\n\t"
        "buffer_load_dwordx4 v[12:15], v96, s[16:19], s34 offen nt\n\t"
        "s_add_u32 s34, s34, 0x10000\n\t"
        "buffer_load_dwordx4 v[16:19], v96, s[16:19], s34 offen nt\n\t"
        "s_add_u32 s34, s34, 0x10000\n\t"
        "buffer_load_dwordx4 v[20:23], v96, s[16:19], s34 offen nt\n\t"
        "s_add_u32 s34, s34, 0x10000\n\t"
        "buffer_load_dwordx4 v[24:27], v96, s[16:19], s34 offen nt\n\t"
        "s_add_u32 s34, s34, 0x10000\n\t"
        "buffer_load_dwordx4 v[28:31], v96, s[16:19], s34 offen nt\n\t"
        "s_lshl_b32 s34, s43, 2\n\t"
        "s_add_u32 s34, s34, 2\n\t"
        "s_and_b32 s34, s34, 15\n\t"
        "s_lshl_b32 s34, s34, 19\n\t"
        "buffer_load_dwordx4 v[32:35], v96, s[16:19], s34 offen nt\n\t"
        "s_add_u32 s34, s34, 0x10000\n\t"
        "buffer_load_dwordx4 v[36:39], v96, s[16:19], s34 offen nt\n\t"
        "s_add_u32 s34, s34, 0x10000\n\t"
        "buffer_load_dwordx4 v[40:43], v96, s[16:19], s34 offen nt\n\t"
        "s_add_u32 s34, s34, 0x10000\n\t"
        "buffer_load_dwordx4 v[44:47], v96, s[16:19], s34 offen nt\n\t"
        "s_add_u32 s34, s34, 0x10000\n\t"
        "buffer_load_dwordx4 v[48:51], v96, s[16:19], s34 offen nt\n\t"
        "s_add_u32 s34, s34, 0x10000\n\t"
        "buffer_load_dwordx4 v[52:55], v96, s[16:19], s34 offen nt\n\t"
        "s_add_u32 s34, s34, 0x10000\n\t"
        "buffer_load_dwordx4 v[56:59], v96, s[16:19], s34 offen nt\n\t"
        "s_add_u32 s34, s34, 0x10000\n\t"
        "buffer_load_dwordx4 v[60:63], v96, s[16:19], s34 offen nt\n\t"
        "v_cmp_le_i32_e64 s[60:61], 0, v126\n\t"
        "v_cmp_le_i32_e64 s[62:63], 1, v126\n\t"
        "v_cmp_le_i32_e64 s[64:65], 2, v126\n\t"
        "v_cmp_le_i32_e64 s[66:67], 3, v126\n\t"
        "v_cmp_le_i32_e64 s[68:69], 16, v126\n\t"
        "v_cmp_le_i32_e64 s[70:71], 17, v126\n\t"
        "v_cmp_le_i32_e64 s[72:73], 18, v126\n\t"
        "v_cmp_le_i32_e64 s[74:75], 19, v126\n\t"
        "s_mul_i32 s35, s43, 10240\n\t"
        "v_add_u32_e32 v126, s35, v124\n\t"
        "s_waitcnt vmcnt(16)\n\t"
        "ds_write_b128 v97, v[224:227] offset:0\n\t"
        "ds_write_b128 v98, v[228:231] offset:1024\n\t"
        "ds_write_b128 v97, v[232:235] offset:2048\n\t"
        "ds_write_b128 v98, v[236:239] offset:3072\n\t"
        "ds_write_b128 v97, v[240:243] offset:4096\n\t"
        "ds_write_b128 v98, v[244:247] offset:5120\n\t"
        "ds_write_b128 v97, v[248:251] offset:6144\n\t"
        "ds_write_b128 v98, v[252:255] offset:7168\n\t"
        "ds_read_b64_tr_b16 a[0:1], v99 offset:0\n\t"
        "ds_read_b64_tr_b16 a[2:3], v99 offset:256\n\t"
        "ds_read_b64_tr_b16 a[4:5], v99 offset:4096\n\t"
        "ds_read_b64_tr_b16 a[6:7], v99 offset:4352\n\t"
        "s_waitcnt lgkmcnt(8)\n\t"
        "ds_read_b64_tr_b16 a[8:9], v100 offset:0\n\t"
        "ds_read_b64_tr_b16 a[10:11], v100 offset:256\n\t"
        "ds_read_b64_tr_b16 a[12:13], v100 offset:4096\n\t"
        "ds_read_b64_tr_b16 a[14:15], v100 offset:4352\n\t"
        "s_waitcnt lgkmcnt(8)\n\t"
        "ds_read_b64_tr_b16 a[16:17], v101 offset:0\n\t"
        "ds_read_b64_tr_b16 a[18:19], v101 offset:256\n\t"
        "ds_read_b64_tr_b16 a[20:21], v101 offset:4096\n\t"
        "ds_read_b64_tr_b16 a[22:23], v101 offset:4352\n\t"
        "s_waitcnt lgkmcnt(8)\n\t"
        "ds_read_b64_tr_b16 v[104:105], v102 offset:0\n\t"
        "ds_read_b64_tr_b16 v[106:107], v102 offset:256\n\t"
        "ds_read_b64_tr_b16 v[108:109], v102 offset:4096\n\t"
        "ds_read_b64_tr_b16 v[110:111], v102 offset:4352\n\t"
        "s_lshl_b32 s34, s43, 2\n\t"
        "s_add_u32 s34, s34, 3\n\t"
        "s_and_b32 s34, s34, 15\n\t"
        "s_lshl_b32 s34, s34, 19\n\t"
        "buffer_load_dwordx4 v[64:67], v96, s[16:19], s34 offen nt\n\t"
        "s_add_u32 s34, s34, 0x10000\n\t"
        "buffer_load_dwordx4 v[68:71], v96, s[16:19], s34 offen nt\n\t"
        "s_add_u32 s34, s34, 0x10000\n\t"
        "buffer_load_dwordx4 v[72:75], v96, s[16:19], s34 offen nt\n\t"
        "s_add_u32 s34, s34, 0x10000\n\t"
        "buffer_load_dwordx4 v[76:79], v96, s[16:19], s34 offen nt\n\t"
        "s_add_u32 s34, s34, 0x10000\n\t"
        "buffer_load_dwordx4 v[80:83], v96, s[16:19], s34 offen nt\n\t"
        "s_add_u32 s34, s34, 0x10000\n\t"
        "buffer_load_dwordx4 v[84:87], v96, s[16:19], s34 offen nt\n\t"
        "s_add_u32 s34, s34, 0x10000\n\t"
        "buffer_load_dwordx4 v[88:91], v96, s[16:19], s34 offen nt\n\t"
        "s_add_u32 s34, s34, 0x10000\n\t"
        "buffer_load_dwordx4 v[92:95], v96, s[16:19], s34 offen nt\n\t"
        "s_waitcnt lgkmcnt(0)\n\t"
        "ds_write_b128 v117, v[104:107] offset:0\n\t"
        "ds_write_b128 v117, v[108:111] offset:1024\n\t"
        "s_waitcnt vmcnt(16)\n\t"
        "ds_write_b128 v97, v[0:3] offset:0\n\t"
        "ds_write_b128 v98, v[4:7] offset:1024\n\t"
        "ds_write_b128 v97, v[8:11] offset:2048\n\t"
        "ds_write_b128 v98, v[12:15] offset:3072\n\t"
        "ds_write_b128 v97, v[16:19] offset:4096\n\t"
        "ds_write_b128 v98, v[20:23] offset:5120\n\t"
        "ds_write_b128 v97, v[24:27] offset:6144\n\t"
        "ds_write_b128 v98, v[28:31] offset:7168\n\t"
        "ds_read_b64_tr_b16 a[24:25], v99 offset:0\n\t"
        "ds_read_b64_tr_b16 a[26:27], v99 offset:256\n\t"
        "ds_read_b64_tr_b16 a[28:29], v99 offset:4096\n\t"
        "ds_read_b64_tr_b16 a[30:31], v99 offset:4352\n\t"
        "s_waitcnt lgkmcnt(8)\n\t"
        "ds_read_b64_tr_b16 a[32:33], v100 offset:0\n\t"
        "ds_read_b64_tr_b16 a[34:35], v100 offset:256\n\t"
        "ds_read_b64_tr_b16 a[36:37], v100 offset:4096\n\t"
        "ds_read_b64_tr_b16 a[38:39], v100 offset:4352\n\t"
        "s_waitcnt lgkmcnt(8)\n\t"
        "ds_read_b64_tr_b16 a[40:41], v101 offset:0\n\t"
        "ds_read_b64_tr_b16 a[42:43], v101 offset:256\n\t"
        "ds_read_b64_tr_b16 a[44:45], v101 offset:4096\n\t"
        "ds_read_b64_tr_b16 a[46:47], v101 offset:4352\n\t"
        "s_waitcnt lgkmcnt(8)\n\t"
        "ds_read_b64_tr_b16 v[104:105], v102 offset:0\n\t"
        "ds_read_b64_tr_b16 v[106:107], v102 offset:256\n\t"
        "ds_read_b64_tr_b16 v[108:109], v102 offset:4096\n\t"
        "ds_read_b64_tr_b16 v[110:111], v102 offset:4352\n\t"
        "s_lshl_b32 s34, s43, 2\n\t"
        "s_add_u32 s34, s34, 4\n\t"
        "s_and_b32 s34, s34, 15\n\t"
        "s_lshl_b32 s34, s34, 19\n\t"
        "buffer_load_dwordx4 a[96:99], v96, s[16:19], s34 offen nt\n\t"
        "s_add_u32 s34, s34, 0x10000\n\t"
        "buffer_load_dwordx4 a[100:103], v96, s[16:19], s34 offen nt\n\t"
        "s_add_u32 s34, s34, 0x10000\n\t"
        "buffer_load_dwordx4 a[104:107], v96, s[16:19], s34 offen nt\n\t"
        "s_add_u32 s34, s34, 0x10000\n\t"
        "buffer_load_dwordx4 a[108:111], v96, s[16:19], s34 offen nt\n\t"
        "s_add_u32 s34, s34, 0x10000\n\t"
        "buffer_load_dwordx4 a[112:115], v96, s[16:19], s34 offen nt\n\t"
        "s_add_u32 s34, s34, 0x10000\n\t"
        "buffer_load_dwordx4 a[116:119], v96, s[16:19], s34 offen nt\n\t"
        "s_add_u32 s34, s34, 0x10000\n\t"
        "buffer_load_dwordx4 a[120:123], v96, s[16:19], s34 offen nt\n\t"
        "s_add_u32 s34, s34, 0x10000\n\t"
        "buffer_load_dwordx4 a[124:127], v96, s[16:19], s34 offen nt\n\t"
        "s_waitcnt lgkmcnt(0)\n\t"
        "ds_write_b128 v117, v[104:107] offset:2048\n\t"
        "ds_write_b128 v117, v[108:111] offset:3072\n\t"
        "s_waitcnt vmcnt(16)\n\t"
        "ds_write_b128 v97, v[32:35] offset:0\n\t"
        "ds_write_b128 v98, v[36:39] offset:1024\n\t"
        "ds_write_b128 v97, v[40:43] offset:2048\n\t"
        "ds_write_b128 v98, v[44:47] offset:3072\n\t"
        "ds_write_b128 v97, v[48:51] offset:4096\n\t"
        "ds_write_b128 v98, v[52:55] offset:5120\n\t"
        "ds_write_b128 v97, v[56:59] offset:6144\n\t"
        "ds_write_b128 v98, v[60:63] offset:7168\n\t"
        "ds_read_b64_tr_b16 a[48:49], v99 offset:0\n\t"
        "ds_read_b64_tr_b16 a[50:51], v99 offset:256\n\t"
        "ds_read_b64_tr_b16 a[52:53], v99 offset:4096\n\t"
        "ds_read_b64_tr_b16 a[54:55], v99 offset:4352\n\t"
        "s_waitcnt lgkmcnt(8)\n\t"
        "ds_read_b64_tr_b16 a[56:57], v100 offset:0\n\t"
        "ds_read_b64_tr_b16 a[58:59], v100 offset:256\n\t"
        "ds_read_b64_tr_b16 a[60:61], v100 offset:4096\n\t"
        "ds_read_b64_tr_b16 a[62:63], v100 offset:4352\n\t"
        "s_waitcnt lgkmcnt(8)\n\t"
        "ds_read_b64_tr_b16 a[64:65], v101 offset:0\n\t"
        "ds_read_b64_tr_b16 a[66:67], v101 offset:256\n\t"
        "ds_read_b64_tr_b16 a[68:69], v101 offset:4096\n\t"
        "ds_read_b64_tr_b16 a[70:71], v101 offset:4352\n\t"
        "s_waitcnt lgkmcnt(8)\n\t"
        "ds_read_b64_tr_b16 v[104:105], v102 offset:0\n\t"
        "ds_read_b64_tr_b16 v[106:107], v102 offset:256\n\t"
        "ds_read_b64_tr_b16 v[108:109], v102 offset:4096\n\t"
        "ds_read_b64_tr_b16 v[110:111], v102 offset:4352\n\t"
        "s_lshl_b32 s34, s43, 2\n\t"
        "s_add_u32 s34, s34, 5\n\t"
        "s_and_b32 s34, s34, 15\n\t"
        "s_lshl_b32 s34, s34, 19\n\t"
        "buffer_load_dwordx4 a[128:131], v96, s[16:19], s34 offen nt\n\t"
        "s_add_u32 s34, s34, 0x10000\n\t"
        "buffer_load_dwordx4 a[132:135], v96, s[16:19], s34 offen nt\n\t"
        "s_add_u32 s34, s34, 0x10000\n\t"
        "buffer_load_dwordx4 a[136:139], v96, s[16:19], s34 offen nt\n\t"
        "s_add_u32 s34, s34, 0x10000\n\t"
        "buffer_load_dwordx4 a[140:143], v96, s[16:19], s34 offen nt\n\t"
        "s_add_u32 s34, s34, 0x10000\n\t"
        "buffer_load_dwordx4 a[144:147], v96, s[16:19], s34 offen nt\n\t"
        "s_add_u32 s34, s34, 0x10000\n\t"
        "buffer_load_dwordx4 a[148:151], v96, s[16:19], s34 offen nt\n\t"
        "s_add_u32 s34, s34, 0x10000\n\t"
        "buffer_load_dwordx4 a[152:155], v96, s[16:19], s34 offen nt\n\t"
        "s_add_u32 s34, s34, 0x10000\n\t"
        "buffer_load_dwordx4 a[156:159], v96, s[16:19], s34 offen nt\n\t"
        "s_waitcnt lgkmcnt(0)\n\t"
        "ds_write_b128 v117, v[104:107] offset:4096\n\t"
        "ds_write_b128 v117, v[108:111] offset:5120\n\t"
        "s_waitcnt vmcnt(16)\n\t"
        "ds_write_b128 v97, v[64:67] offset:0\n\t"
        "ds_write_b128 v98, v[68:71] offset:1024\n\t"
        "ds_write_b128 v97, v[72:75] offset:2048\n\t"
        "ds_write_b128 v98, v[76:79] offset:3072\n\t"
        "ds_write_b128 v97, v[80:83] offset:4096\n\t"
        "ds_write_b128 v98, v[84:87] offset:5120\n\t"
        "ds_write_b128 v97, v[88:91] offset:6144\n\t"
        "ds_write_b128 v98, v[92:95] offset:7168\n\t"
        "ds_read_b64_tr_b16 a[72:73], v99 offset:0\n\t"
        "ds_read_b64_tr_b16 a[74:75], v99 offset:256\n\t"
        "ds_read_b64_tr_b16 a[76:77], v99 offset:4096\n\t"
        "ds_read_b64_tr_b16 a[78:79], v99 offset:4352\n\t"
        "s_waitcnt lgkmcnt(8)\n\t"
        "ds_read_b64_tr_b16 a[80:81], v100 offset:0\n\t"
        "ds_read_b64_tr_b16 a[82:83], v100 offset:256\n\t"
        "ds_read_b64_tr_b16 a[84:85], v100 offset:4096\n\t"
        "ds_read_b64_tr_b16 a[86:87], v100 offset:4352\n\t"
        "s_waitcnt lgkmcnt(8)\n\t"
        "ds_read_b64_tr_b16 a[88:89], v101 offset:0\n\t"
        "ds_read_b64_tr_b16 a[90:91], v101 offset:256\n\t"
        "ds_read_b64_tr_b16 a[92:93], v101 offset:4096\n\t"
        "ds_read_b64_tr_b16 a[94:95], v101 offset:4352\n\t"
        "s_waitcnt lgkmcnt(8)\n\t"
        "ds_read_b64_tr_b16 v[104:105], v102 offset:0\n\t"
        "ds_read_b64_tr_b16 v[106:107], v102 offset:256\n\t"
        "ds_read_b64_tr_b16 v[108:109], v102 offset:4096\n\t"
        "ds_read_b64_tr_b16 v[110:111], v102 offset:4352\n\t"
        "s_lshl_b32 s34, s43, 2\n\t"
        "s_add_u32 s34, s34, 6\n\t"
        "s_and_b32 s34, s34, 15\n\t"
        "s_lshl_b32 s34, s34, 19\n\t"
        "buffer_load_dwordx4 a[160:163], v96, s[16:19], s34 offen nt\n\t"
        "s_add_u32 s34, s34, 0x10000\n\t"
        "buffer_load_dwordx4 a[164:167], v96, s[16:19], s34 offen nt\n\t"
        "s_add_u32 s34, s34, 0x10000\n\t"
        "buffer_load_dwordx4 a[168:171], v96, s[16:19], s34 offen nt\n\t"
        "s_add_u32 s34, s34, 0x10000\n\t"
        "buffer_load_dwordx4 a[172:175], v96, s[16:19], s34 offen nt\n\t"
        "s_add_u32 s34, s34, 0x10000\n\t"
        "buffer_load_dwordx4 a[176:179], v96, s[16:19], s34 offen nt\n\t"
        "s_add_u32 s34, s34, 0x10000\n\t"
        "buffer_load_dwordx4 a[180:183], v96, s[16:19], s34 offen nt\n\t"
        "s_add_u32 s34, s34, 0x10000\n\t"
        "buffer_load_dwordx4 a[184:187], v96, s[16:19], s34 offen nt\n\t"
        "s_add_u32 s34, s34, 0x10000\n\t"
        "buffer_load_dwordx4 a[188:191], v96, s[16:19], s34 offen nt\n\t"
        "s_waitcnt lgkmcnt(0)\n\t"
        "ds_write_b128 v117, v[104:107] offset:6144\n\t"
        "ds_write_b128 v117, v[108:111] offset:7168\n\t"
        "s_waitcnt vmcnt(16)\n\t"
        "ds_write_b128 v97, a[96:99] offset:0\n\t"
        "ds_write_b128 v98, a[100:103] offset:1024\n\t"
        "ds_write_b128 v97, a[104:107] offset:2048\n\t"
        "ds_write_b128 v98, a[108:111] offset:3072\n\t"
        "ds_write_b128 v97, a[112:115] offset:4096\n\t"
        "ds_write_b128 v98, a[116:119] offset:5120\n\t"
        "ds_write_b128 v97, a[120:123] offset:6144\n\t"
        "ds_write_b128 v98, a[124:127] offset:7168\n\t"
        "ds_read_b64_tr_b16 a[96:97], v99 offset:0\n\t"
        "ds_read_b64_tr_b16 a[98:99], v99 offset:256\n\t"
        "ds_read_b64_tr_b16 a[100:101], v99 offset:4096\n\t"
        "ds_read_b64_tr_b16 a[102:103], v99 offset:4352\n\t"
        "s_waitcnt lgkmcnt(8)\n\t"
        "ds_read_b64_tr_b16 a[104:105], v100 offset:0\n\t"
        "ds_read_b64_tr_b16 a[106:107], v100 offset:256\n\t"
        "ds_read_b64_tr_b16 a[108:109], v100 offset:4096\n\t"
        "ds_read_b64_tr_b16 a[110:111], v100 offset:4352\n\t"
        "s_waitcnt lgkmcnt(8)\n\t"
        "ds_read_b64_tr_b16 a[112:113], v101 offset:0\n\t"
        "ds_read_b64_tr_b16 a[114:115], v101 offset:256\n\t"
        "ds_read_b64_tr_b16 a[116:117], v101 offset:4096\n\t"
        "ds_read_b64_tr_b16 a[118:119], v101 offset:4352\n\t"
        "s_waitcnt lgkmcnt(8)\n\t"
        "ds_read_b64_tr_b16 v[104:105], v102 offset:0\n\t"
        "ds_read_b64_tr_b16 v[106:107], v102 offset:256\n\t"
        "ds_read_b64_tr_b16 v[108:109], v102 offset:4096\n\t"
        "ds_read_b64_tr_b16 v[110:111], v102 offset:4352\n\t"
        "s_lshl_b32 s34, s43, 2\n\t"
        "s_add_u32 s34, s34, 7\n\t"
        "s_and_b32 s34, s34, 15\n\t"
        "s_lshl_b32 s34, s34, 19\n\t"
        "buffer_load_dwordx4 a[192:195], v96, s[16:19], s34 offen nt\n\t"
        "s_add_u32 s34, s34, 0x10000\n\t"
        "buffer_load_dwordx4 a[196:199], v96, s[16:19], s34 offen nt\n\t"
        "s_add_u32 s34, s34, 0x10000\n\t"
        "buffer_load_dwordx4 a[200:203], v96, s[16:19], s34 offen nt\n\t"
        "s_add_u32 s34, s34, 0x10000\n\t"
        "buffer_load_dwordx4 a[204:207], v96, s[16:19], s34 offen nt\n\t"
        "s_add_u32 s34, s34, 0x10000\n\t"
        "buffer_load_dwordx4 a[208:211], v96, s[16:19], s34 offen nt\n\t"
        "s_add_u32 s34, s34, 0x10000\n\t"
        "buffer_load_dwordx4 a[212:215], v96, s[16:19], s34 offen nt\n\t"
        "s_add_u32 s34, s34, 0x10000\n\t"
        "buffer_load_dwordx4 a[216:219], v96, s[16:19], s34 offen nt\n\t"
        "s_add_u32 s34, s34, 0x10000\n\t"
        "buffer_load_dwordx4 a[220:223], v96, s[16:19], s34 offen nt\n\t"
        "s_waitcnt lgkmcnt(0)\n\t"
        "ds_write_b128 v126, v[104:107] offset:0\n\t"
        "ds_write_b128 v126, v[108:111] offset:1024\n\t"
        "s_waitcnt vmcnt(16)\n\t"
        "ds_write_b128 v97, a[128:131] offset:0\n\t"
        "ds_write_b128 v98, a[132:135] offset:1024\n\t"
        "ds_write_b128 v97, a[136:139] offset:2048\n\t"
        "ds_write_b128 v98, a[140:143] offset:3072\n\t"
        "ds_write_b128 v97, a[144:147] offset:4096\n\t"
        "ds_write_b128 v98, a[148:151] offset:5120\n\t"
        "ds_write_b128 v97, a[152:155] offset:6144\n\t"
        "ds_write_b128 v98, a[156:159] offset:7168\n\t"
        "ds_read_b64_tr_b16 a[120:121], v99 offset:0\n\t"
        "ds_read_b64_tr_b16 a[122:123], v99 offset:256\n\t"
        "ds_read_b64_tr_b16 a[124:125], v99 offset:4096\n\t"
        "ds_read_b64_tr_b16 a[126:127], v99 offset:4352\n\t"
        "s_waitcnt lgkmcnt(8)\n\t"
        "ds_read_b64_tr_b16 a[128:129], v100 offset:0\n\t"
        "ds_read_b64_tr_b16 a[130:131], v100 offset:256\n\t"
        "ds_read_b64_tr_b16 a[132:133], v100 offset:4096\n\t"
        "ds_read_b64_tr_b16 a[134:135], v100 offset:4352\n\t"
        "s_waitcnt lgkmcnt(8)\n\t"
        "ds_read_b64_tr_b16 a[136:137], v101 offset:0\n\t"
        "ds_read_b64_tr_b16 a[138:139], v101 offset:256\n\t"
        "ds_read_b64_tr_b16 a[140:141], v101 offset:4096\n\t"
        "ds_read_b64_tr_b16 a[142:143], v101 offset:4352\n\t"
        "s_waitcnt lgkmcnt(8)\n\t"
        "ds_read_b64_tr_b16 v[104:105], v102 offset:0\n\t"
        "ds_read_b64_tr_b16 v[106:107], v102 offset:256\n\t"
        "ds_read_b64_tr_b16 v[108:109], v102 offset:4096\n\t"
        "ds_read_b64_tr_b16 v[110:111], v102 offset:4352\n\t"
        "s_lshl_b32 s34, s43, 2\n\t"
        "s_add_u32 s34, s34, 8\n\t"
        "s_and_b32 s34, s34, 15\n\t"
        "s_lshl_b32 s34, s34, 19\n\t"
        "buffer_load_dwordx4 a[224:227], v96, s[16:19], s34 offen nt\n\t"
        "s_add_u32 s34, s34, 0x10000\n\t"
        "buffer_load_dwordx4 a[228:231], v96, s[16:19], s34 offen nt\n\t"
        "s_add_u32 s34, s34, 0x10000\n\t"
        "buffer_load_dwordx4 a[232:235], v96, s[16:19], s34 offen nt\n\t"
        "s_add_u32 s34, s34, 0x10000\n\t"
        "buffer_load_dwordx4 a[236:239], v96, s[16:19], s34 offen nt\n\t"
        "s_add_u32 s34, s34, 0x10000\n\t"
        "buffer_load_dwordx4 a[240:243], v96, s[16:19], s34 offen nt\n\t"
        "s_add_u32 s34, s34, 0x10000\n\t"
        "buffer_load_dwordx4 a[244:247], v96, s[16:19], s34 offen nt\n\t"
        "s_add_u32 s34, s34, 0x10000\n\t"
        "buffer_load_dwordx4 a[248:251], v96, s[16:19], s34 offen nt\n\t"
        "s_add_u32 s34, s34, 0x10000\n\t"
        "buffer_load_dwordx4 a[252:255], v96, s[16:19], s34 offen nt\n\t"
        "s_waitcnt lgkmcnt(0)\n\t"
        "ds_write_b128 v126, v[104:107] offset:2048\n\t"
        "ds_write_b128 v126, v[108:111] offset:3072\n\t"
        "s_waitcnt vmcnt(16)\n\t"
        "ds_write_b128 v97, a[160:163] offset:0\n\t"
        "ds_write_b128 v98, a[164:167] offset:1024\n\t"
        "ds_write_b128 v97, a[168:171] offset:2048\n\t"
        "ds_write_b128 v98, a[172:175] offset:3072\n\t"
        "ds_write_b128 v97, a[176:179] offset:4096\n\t"
        "ds_write_b128 v98, a[180:183] offset:5120\n\t"
        "ds_write_b128 v97, a[184:187] offset:6144\n\t"
        "ds_write_b128 v98, a[188:191] offset:7168\n\t"
        "ds_read_b64_tr_b16 a[144:145], v99 offset:0\n\t"
        "ds_read_b64_tr_b16 a[146:147], v99 offset:256\n\t"
        "ds_read_b64_tr_b16 a[148:149], v99 offset:4096\n\t"
        "ds_read_b64_tr_b16 a[150:151], v99 offset:4352\n\t"
        "s_waitcnt lgkmcnt(8)\n\t"
        "ds_read_b64_tr_b16 a[152:153], v100 offset:0\n\t"
        "ds_read_b64_tr_b16 a[154:155], v100 offset:256\n\t"
        "ds_read_b64_tr_b16 a[156:157], v100 offset:4096\n\t"
        "ds_read_b64_tr_b16 a[158:159], v100 offset:4352\n\t"
        "s_waitcnt lgkmcnt(8)\n\t"
        "ds_read_b64_tr_b16 a[160:161], v101 offset:0\n\t"
        "ds_read_b64_tr_b16 a[162:163], v101 offset:256\n\t"
        "ds_read_b64_tr_b16 a[164:165], v101 offset:4096\n\t"
        "ds_read_b64_tr_b16 a[166:167], v101 offset:4352\n\t"
        "s_waitcnt lgkmcnt(8)\n\t"
        "ds_read_b64_tr_b16 v[104:105], v102 offset:0\n\t"
        "ds_read_b64_tr_b16 v[106:107], v102 offset:256\n\t"
        "ds_read_b64_tr_b16 v[108:109], v102 offset:4096\n\t"
        "ds_read_b64_tr_b16 v[110:111], v102 offset:4352\n\t"
        "s_lshl_b32 s34, s43, 2\n\t"
        "s_add_u32 s34, s34, 9\n\t"
        "s_and_b32 s34, s34, 15\n\t"
        "s_lshl_b32 s34, s34, 19\n\t"
        "buffer_load_dwordx4 v[128:131], v96, s[16:19], s34 offen nt\n\t"
        "s_add_u32 s34, s34, 0x10000\n\t"
        "buffer_load_dwordx4 v[132:135], v96, s[16:19], s34 offen nt\n\t"
        "s_add_u32 s34, s34, 0x10000\n\t"
        "buffer_load_dwordx4 v[136:139], v96, s[16:19], s34 offen nt\n\t"
        "s_add_u32 s34, s34, 0x10000\n\t"
        "buffer_load_dwordx4 v[140:143], v96, s[16:19], s34 offen nt\n\t"
        "s_add_u32 s34, s34, 0x10000\n\t"
        "buffer_load_dwordx4 v[144:147], v96, s[16:19], s34 offen nt\n\t"
        "s_add_u32 s34, s34, 0x10000\n\t"
        "buffer_load_dwordx4 v[148:151], v96, s[16:19], s34 offen nt\n\t"
        "s_add_u32 s34, s34, 0x10000\n\t"
        "buffer_load_dwordx4 v[152:155], v96, s[16:19], s34 offen nt\n\t"
        "s_add_u32 s34, s34, 0x10000\n\t"
        "buffer_load_dwordx4 v[156:159], v96, s[16:19], s34 offen nt\n\t"
        "s_waitcnt lgkmcnt(0)\n\t"
        "ds_write_b128 v126, v[104:107] offset:4096\n\t"
        "ds_write_b128 v126, v[108:111] offset:5120\n\t"
        "s_waitcnt vmcnt(16)\n\t"
        "ds_write_b128 v97, a[192:195] offset:0\n\t"
        "ds_write_b128 v98, a[196:199] offset:1024\n\t"
        "ds_write_b128 v97, a[200:203] offset:2048\n\t"
        "ds_write_b128 v98, a[204:207] offset:3072\n\t"
        "ds_write_b128 v97, a[208:211] offset:4096\n\t"
        "ds_write_b128 v98, a[212:215] offset:5120\n\t"
        "ds_write_b128 v97, a[216:219] offset:6144\n\t"
        "ds_write_b128 v98, a[220:223] offset:7168\n\t"
        "ds_read_b64_tr_b16 a[168:169], v99 offset:0\n\t"
        "ds_read_b64_tr_b16 a[170:171], v99 offset:256\n\t"
        "ds_read_b64_tr_b16 a[172:173], v99 offset:4096\n\t"
        "ds_read_b64_tr_b16 a[174:175], v99 offset:4352\n\t"
        "s_waitcnt lgkmcnt(8)\n\t"
        "ds_read_b64_tr_b16 a[176:177], v100 offset:0\n\t"
        "ds_read_b64_tr_b16 a[178:179], v100 offset:256\n\t"
        "ds_read_b64_tr_b16 a[180:181], v100 offset:4096\n\t"
        "ds_read_b64_tr_b16 a[182:183], v100 offset:4352\n\t"
        "s_waitcnt lgkmcnt(8)\n\t"
        "ds_read_b64_tr_b16 a[184:185], v101 offset:0\n\t"
        "ds_read_b64_tr_b16 a[186:187], v101 offset:256\n\t"
        "ds_read_b64_tr_b16 a[188:189], v101 offset:4096\n\t"
        "ds_read_b64_tr_b16 a[190:191], v101 offset:4352\n\t"
        "s_waitcnt lgkmcnt(8)\n\t"
        "ds_read_b64_tr_b16 v[104:105], v102 offset:0\n\t"
        "ds_read_b64_tr_b16 v[106:107], v102 offset:256\n\t"
        "ds_read_b64_tr_b16 v[108:109], v102 offset:4096\n\t"
        "ds_read_b64_tr_b16 v[110:111], v102 offset:4352\n\t"
        "s_lshl_b32 s34, s43, 2\n\t"
        "s_add_u32 s34, s34, 10\n\t"
        "s_and_b32 s34, s34, 15\n\t"
        "s_lshl_b32 s34, s34, 19\n\t"
        "buffer_load_dwordx4 v[160:163], v96, s[16:19], s34 offen nt\n\t"
        "s_add_u32 s34, s34, 0x10000\n\t"
        "buffer_load_dwordx4 v[164:167], v96, s[16:19], s34 offen nt\n\t"
        "s_add_u32 s34, s34, 0x10000\n\t"
        "buffer_load_dwordx4 v[168:171], v96, s[16:19], s34 offen nt\n\t"
        "s_add_u32 s34, s34, 0x10000\n\t"
        "buffer_load_dwordx4 v[172:175], v96, s[16:19], s34 offen nt\n\t"
        "s_add_u32 s34, s34, 0x10000\n\t"
        "buffer_load_dwordx4 v[176:179], v96, s[16:19], s34 offen nt\n\t"
        "s_add_u32 s34, s34, 0x10000\n\t"
        "buffer_load_dwordx4 v[180:183], v96, s[16:19], s34 offen nt\n\t"
        "s_add_u32 s34, s34, 0x10000\n\t"
        "buffer_load_dwordx4 v[184:187], v96, s[16:19], s34 offen nt\n\t"
        "s_add_u32 s34, s34, 0x10000\n\t"
        "buffer_load_dwordx4 v[188:191], v96, s[16:19], s34 offen nt\n\t"
        "s_waitcnt lgkmcnt(0)\n\t"
        "ds_write_b128 v126, v[104:107] offset:6144\n\t"
        "ds_write_b128 v126, v[108:111] offset:7168\n\t"
        "s_waitcnt vmcnt(16)\n\t"
        "ds_write_b128 v97, a[224:227] offset:0\n\t"
        "ds_write_b128 v98, a[228:231] offset:1024\n\t"
        "ds_write_b128 v97, a[232:235] offset:2048\n\t"
        "ds_write_b128 v98, a[236:239] offset:3072\n\t"
        "ds_write_b128 v97, a[240:243] offset:4096\n\t"
        "ds_write_b128 v98, a[244:247] offset:5120\n\t"
        "ds_write_b128 v97, a[248:251] offset:6144\n\t"
        "ds_write_b128 v98, a[252:255] offset:7168\n\t"
        "ds_read_b64_tr_b16 a[192:193], v99 offset:0\n\t"
        "ds_read_b64_tr_b16 a[194:195], v99 offset:256\n\t"
        "ds_read_b64_tr_b16 a[196:197], v99 offset:4096\n\t"
        "ds_read_b64_tr_b16 a[198:199], v99 offset:4352\n\t"
        "s_waitcnt lgkmcnt(8)\n\t"
        "ds_read_b64_tr_b16 a[200:201], v100 offset:0\n\t"
        "ds_read_b64_tr_b16 a[202:203], v100 offset:256\n\t"
        "ds_read_b64_tr_b16 a[204:205], v100 offset:4096\n\t"
        "ds_read_b64_tr_b16 a[206:207], v100 offset:4352\n\t"
        "s_waitcnt lgkmcnt(8)\n\t"
        "ds_read_b64_tr_b16 a[208:209], v101 offset:0\n\t"
        "ds_read_b64_tr_b16 a[210:211], v101 offset:256\n\t"
        "ds_read_b64_tr_b16 a[212:213], v101 offset:4096\n\t"
        "ds_read_b64_tr_b16 a[214:215], v101 offset:4352\n\t"
        "s_waitcnt lgkmcnt(8)\n\t"
        "ds_read_b64_tr_b16 v[104:105], v102 offset:0\n\t"
        "ds_read_b64_tr_b16 v[106:107], v102 offset:256\n\t"
        "ds_read_b64_tr_b16 v[108:109], v102 offset:4096\n\t"
        "ds_read_b64_tr_b16 v[110:111], v102 offset:4352\n\t"
        "s_lshl_b32 s34, s43, 2\n\t"
        "s_add_u32 s34, s34, 11\n\t"
        "s_and_b32 s34, s34, 15\n\t"
        "s_lshl_b32 s34, s34, 19\n\t"
        "buffer_load_dwordx4 v[192:195], v96, s[16:19], s34 offen nt\n\t"
        "s_add_u32 s34, s34, 0x10000\n\t"
        "buffer_load_dwordx4 v[196:199], v96, s[16:19], s34 offen nt\n\t"
        "s_add_u32 s34, s34, 0x10000\n\t"
        "buffer_load_dwordx4 v[200:203], v96, s[16:19], s34 offen nt\n\t"
        "s_add_u32 s34, s34, 0x10000\n\t"
        "buffer_load_dwordx4 v[204:207], v96, s[16:19], s34 offen nt\n\t"
        "s_add_u32 s34, s34, 0x10000\n\t"
        "buffer_load_dwordx4 v[208:211], v96, s[16:19], s34 offen nt\n\t"
        "s_add_u32 s34, s34, 0x10000\n\t"
        "buffer_load_dwordx4 v[212:215], v96, s[16:19], s34 offen nt\n\t"
        "s_add_u32 s34, s34, 0x10000\n\t"
        "buffer_load_dwordx4 v[216:219], v96, s[16:19], s34 offen nt\n\t"
        "s_add_u32 s34, s34, 0x10000\n\t"
        "buffer_load_dwordx4 v[220:223], v96, s[16:19], s34 offen nt\n\t"
        "s_waitcnt lgkmcnt(0)\n\t"
        "ds_write_b128 v117, v[104:107] offset:8192\n\t"
        "ds_write_b128 v117, v[108:111] offset:9216\n\t"
        "s_waitcnt vmcnt(16)\n\t"
        "ds_write_b128 v97, v[128:131] offset:0\n\t"
        "ds_write_b128 v98, v[132:135] offset:1024\n\t"
        "ds_write_b128 v97, v[136:139] offset:2048\n\t"
        "ds_write_b128 v98, v[140:143] offset:3072\n\t"
        "ds_write_b128 v97, v[144:147] offset:4096\n\t"
        "ds_write_b128 v98, v[148:151] offset:5120\n\t"
        "ds_write_b128 v97, v[152:155] offset:6144\n\t"
        "ds_write_b128 v98, v[156:159] offset:7168\n\t"
        "ds_read_b64_tr_b16 a[216:217], v99 offset:0\n\t"
        "ds_read_b64_tr_b16 a[218:219], v99 offset:256\n\t"
        "ds_read_b64_tr_b16 a[220:221], v99 offset:4096\n\t"
        "ds_read_b64_tr_b16 a[222:223], v99 offset:4352\n\t"
        "s_waitcnt lgkmcnt(8)\n\t"
        "ds_read_b64_tr_b16 a[224:225], v100 offset:0\n\t"
        "ds_read_b64_tr_b16 a[226:227], v100 offset:256\n\t"
        "ds_read_b64_tr_b16 a[228:229], v100 offset:4096\n\t"
        "ds_read_b64_tr_b16 a[230:231], v100 offset:4352\n\t"
        "s_waitcnt lgkmcnt(8)\n\t"
        "ds_read_b64_tr_b16 a[232:233], v101 offset:0\n\t"
        "ds_read_b64_tr_b16 a[234:235], v101 offset:256\n\t"
        "ds_read_b64_tr_b16 a[236:237], v101 offset:4096\n\t"
        "ds_read_b64_tr_b16 a[238:239], v101 offset:4352\n\t"
        "s_waitcnt lgkmcnt(8)\n\t"
        "ds_read_b64_tr_b16 v[104:105], v102 offset:0\n\t"
        "ds_read_b64_tr_b16 v[106:107], v102 offset:256\n\t"
        "ds_read_b64_tr_b16 v[108:109], v102 offset:4096\n\t"
        "ds_read_b64_tr_b16 v[110:111], v102 offset:4352\n\t"
        "s_lshl_b32 s34, s43, 2\n\t"
        "s_add_u32 s34, s34, 12\n\t"
        "s_and_b32 s34, s34, 15\n\t"
        "s_lshl_b32 s34, s34, 19\n\t"
        "buffer_load_dwordx4 v[224:227], v96, s[16:19], s34 offen nt\n\t"
        "s_add_u32 s34, s34, 0x10000\n\t"
        "buffer_load_dwordx4 v[228:231], v96, s[16:19], s34 offen nt\n\t"
        "s_add_u32 s34, s34, 0x10000\n\t"
        "buffer_load_dwordx4 v[232:235], v96, s[16:19], s34 offen nt\n\t"
        "s_add_u32 s34, s34, 0x10000\n\t"
        "buffer_load_dwordx4 v[236:239], v96, s[16:19], s34 offen nt\n\t"
        "s_add_u32 s34, s34, 0x10000\n\t"
        "buffer_load_dwordx4 v[240:243], v96, s[16:19], s34 offen nt\n\t"
        "s_add_u32 s34, s34, 0x10000\n\t"
        "buffer_load_dwordx4 v[244:247], v96, s[16:19], s34 offen nt\n\t"
        "s_add_u32 s34, s34, 0x10000\n\t"
        "buffer_load_dwordx4 v[248:251], v96, s[16:19], s34 offen nt\n\t"
        "s_add_u32 s34, s34, 0x10000\n\t"
        "buffer_load_dwordx4 v[252:255], v96, s[16:19], s34 offen nt\n\t"
        "s_waitcnt lgkmcnt(0)\n\t"
        "ds_write_b128 v117, v[104:107] offset:10240\n\t"
        "ds_write_b128 v117, v[108:111] offset:11264\n\t"
        "s_waitcnt vmcnt(16)\n\t"
        "ds_write_b128 v97, v[160:163] offset:0\n\t"
        "ds_write_b128 v98, v[164:167] offset:1024\n\t"
        "ds_write_b128 v97, v[168:171] offset:2048\n\t"
        "ds_write_b128 v98, v[172:175] offset:3072\n\t"
        "ds_write_b128 v97, v[176:179] offset:4096\n\t"
        "ds_write_b128 v98, v[180:183] offset:5120\n\t"
        "ds_write_b128 v97, v[184:187] offset:6144\n\t"
        "ds_write_b128 v98, v[188:191] offset:7168\n\t"
        "ds_read_b64_tr_b16 a[240:241], v99 offset:0\n\t"
        "ds_read_b64_tr_b16 a[242:243], v99 offset:256\n\t"
        "ds_read_b64_tr_b16 a[244:245], v99 offset:4096\n\t"
        "ds_read_b64_tr_b16 a[246:247], v99 offset:4352\n\t"
        "s_waitcnt lgkmcnt(8)\n\t"
        "ds_read_b64_tr_b16 a[248:249], v100 offset:0\n\t"
        "ds_read_b64_tr_b16 a[250:251], v100 offset:256\n\t"
        "ds_read_b64_tr_b16 a[252:253], v100 offset:4096\n\t"
        "ds_read_b64_tr_b16 a[254:255], v100 offset:4352\n\t"
        "s_waitcnt lgkmcnt(8)\n\t"
        "ds_read_b64_tr_b16 v[128:129], v101 offset:0\n\t"
        "ds_read_b64_tr_b16 v[130:131], v101 offset:256\n\t"
        "ds_read_b64_tr_b16 v[132:133], v101 offset:4096\n\t"
        "ds_read_b64_tr_b16 v[134:135], v101 offset:4352\n\t"
        "s_waitcnt lgkmcnt(8)\n\t"
        "ds_read_b64_tr_b16 v[104:105], v102 offset:0\n\t"
        "ds_read_b64_tr_b16 v[106:107], v102 offset:256\n\t"
        "ds_read_b64_tr_b16 v[108:109], v102 offset:4096\n\t"
        "ds_read_b64_tr_b16 v[110:111], v102 offset:4352\n\t"
        "s_lshl_b32 s34, s43, 2\n\t"
        "s_add_u32 s34, s34, 13\n\t"
        "s_and_b32 s34, s34, 15\n\t"
        "s_lshl_b32 s34, s34, 19\n\t"
        "buffer_load_dwordx4 v[0:3], v96, s[16:19], s34 offen nt\n\t"
        "s_add_u32 s34, s34, 0x10000\n\t"
        "buffer_load_dwordx4 v[4:7], v96, s[16:19], s34 offen nt\n\t"
        "s_add_u32 s34, s34, 0x10000\n\t"
        "buffer_load_dwordx4 v[8:11], v96, s[16:19], s34 offen nt\n\t"
        "s_add_u32 s34, s34, 0x10000\n\t"
        "buffer_load_dwordx4 v[12:15], v96, s[16:19], s34 offen nt\n\t"
        "s_add_u32 s34, s34, 0x10000\n\t"
        "buffer_load_dwordx4 v[16:19], v96, s[16:19], s34 offen nt\n\t"
        "s_add_u32 s34, s34, 0x10000\n\t"
        "buffer_load_dwordx4 v[20:23], v96, s[16:19], s34 offen nt\n\t"
        "s_add_u32 s34, s34, 0x10000\n\t"
        "buffer_load_dwordx4 v[24:27], v96, s[16:19], s34 offen nt\n\t"
        "s_add_u32 s34, s34, 0x10000\n\t"
        "buffer_load_dwordx4 v[28:31], v96, s[16:19], s34 offen nt\n\t"
        "s_waitcnt lgkmcnt(0)\n\t"
        "ds_write_b128 v117, v[104:107] offset:12288\n\t"
        "ds_write_b128 v117, v[108:111] offset:13312\n\t"
        "s_waitcnt vmcnt(16)\n\t"
        "ds_write_b128 v97, v[192:195] offset:0\n\t"
        "ds_write_b128 v98, v[196:199] offset:1024\n\t"
        "ds_write_b128 v97, v[200:203] offset:2048\n\t"
        "ds_write_b128 v98, v[204:207] offset:3072\n\t"
        "ds_write_b128 v97, v[208:211] offset:4096\n\t"
        "ds_write_b128 v98, v[212:215] offset:5120\n\t"
        "ds_write_b128 v97, v[216:219] offset:6144\n\t"
        "ds_write_b128 v98, v[220:223] offset:7168\n\t"
        "ds_read_b64_tr_b16 v[136:137], v99 offset:0\n\t"
        "ds_read_b64_tr_b16 v[138:139], v99 offset:256\n\t"
        "ds_read_b64_tr_b16 v[140:141], v99 offset:4096\n\t"
        "ds_read_b64_tr_b16 v[142:143], v99 offset:4352\n\t"
        "s_waitcnt lgkmcnt(8)\n\t"
        "ds_read_b64_tr_b16 v[144:145], v100 offset:0\n\t"
        "ds_read_b64_tr_b16 v[146:147], v100 offset:256\n\t"
        "ds_read_b64_tr_b16 v[148:149], v100 offset:4096\n\t"
        "ds_read_b64_tr_b16 v[150:151], v100 offset:4352\n\t"
        "s_waitcnt lgkmcnt(8)\n\t"
        "ds_read_b64_tr_b16 v[152:153], v101 offset:0\n\t"
        "ds_read_b64_tr_b16 v[154:155], v101 offset:256\n\t"
        "ds_read_b64_tr_b16 v[156:157], v101 offset:4096\n\t"
        "ds_read_b64_tr_b16 v[158:159], v101 offset:4352\n\t"
        "s_waitcnt lgkmcnt(8)\n\t"
        "ds_read_b64_tr_b16 v[104:105], v102 offset:0\n\t"
        "ds_read_b64_tr_b16 v[106:107], v102 offset:256\n\t"
        "ds_read_b64_tr_b16 v[108:109], v102 offset:4096\n\t"
        "ds_read_b64_tr_b16 v[110:111], v102 offset:4352\n\t"
        "s_lshl_b32 s34, s43, 2\n\t"
        "s_add_u32 s34, s34, 14\n\t"
        "s_and_b32 s34, s34, 15\n\t"
        "s_lshl_b32 s34, s34, 19\n\t"
        "buffer_load_dwordx4 v[32:35], v96, s[16:19], s34 offen nt\n\t"
        "s_add_u32 s34, s34, 0x10000\n\t"
        "buffer_load_dwordx4 v[36:39], v96, s[16:19], s34 offen nt\n\t"
        "s_add_u32 s34, s34, 0x10000\n\t"
        "buffer_load_dwordx4 v[40:43], v96, s[16:19], s34 offen nt\n\t"
        "s_add_u32 s34, s34, 0x10000\n\t"
        "buffer_load_dwordx4 v[44:47], v96, s[16:19], s34 offen nt\n\t"
        "s_add_u32 s34, s34, 0x10000\n\t"
        "buffer_load_dwordx4 v[48:51], v96, s[16:19], s34 offen nt\n\t"
        "s_add_u32 s34, s34, 0x10000\n\t"
        "buffer_load_dwordx4 v[52:55], v96, s[16:19], s34 offen nt\n\t"
        "s_add_u32 s34, s34, 0x10000\n\t"
        "buffer_load_dwordx4 v[56:59], v96, s[16:19], s34 offen nt\n\t"
        "s_add_u32 s34, s34, 0x10000\n\t"
        "buffer_load_dwordx4 v[60:63], v96, s[16:19], s34 offen nt\n\t"
        "s_waitcnt lgkmcnt(0)\n\t"
        "ds_write_b128 v117, v[104:107] offset:14336\n\t"
        "ds_write_b128 v117, v[108:111] offset:15360\n\t"
        "s_waitcnt vmcnt(16)\n\t"
        "ds_write_b128 v97, v[224:227] offset:0\n\t"
        "ds_write_b128 v98, v[228:231] offset:1024\n\t"
        "ds_write_b128 v97, v[232:235] offset:2048\n\t"
        "ds_write_b128 v98, v[236:239] offset:3072\n\t"
        "ds_write_b128 v97, v[240:243] offset:4096\n\t"
        "ds_write_b128 v98, v[244:247] offset:5120\n\t"
        "ds_write_b128 v97, v[248:251] offset:6144\n\t"
        "ds_write_b128 v98, v[252:255] offset:7168\n\t"
        "ds_read_b64_tr_b16 v[160:161], v99 offset:0\n\t"
        "ds_read_b64_tr_b16 v[162:163], v99 offset:256\n\t"
        "ds_read_b64_tr_b16 v[164:165], v99 offset:4096\n\t"
        "ds_read_b64_tr_b16 v[166:167], v99 offset:4352\n\t"
        "s_waitcnt lgkmcnt(8)\n\t"
        "ds_read_b64_tr_b16 v[168:169], v100 offset:0\n\t"
        "ds_read_b64_tr_b16 v[170:171], v100 offset:256\n\t"
        "ds_read_b64_tr_b16 v[172:173], v100 offset:4096\n\t"
        "ds_read_b64_tr_b16 v[174:175], v100 offset:4352\n\t"
        "s_waitcnt lgkmcnt(8)\n\t"
        "ds_read_b64_tr_b16 v[176:177], v101 offset:0\n\t"
        "ds_read_b64_tr_b16 v[178:179], v101 offset:256\n\t"
        "ds_read_b64_tr_b16 v[180:181], v101 offset:4096\n\t"
        "ds_read_b64_tr_b16 v[182:183], v101 offset:4352\n\t"
        "s_waitcnt lgkmcnt(8)\n\t"
        "ds_read_b64_tr_b16 v[104:105], v102 offset:0\n\t"
        "ds_read_b64_tr_b16 v[106:107], v102 offset:256\n\t"
        "ds_read_b64_tr_b16 v[108:109], v102 offset:4096\n\t"
        "ds_read_b64_tr_b16 v[110:111], v102 offset:4352\n\t"
        "s_lshl_b32 s34, s43, 2\n\t"
        "s_add_u32 s34, s34, 15\n\t"
        "s_and_b32 s34, s34, 15\n\t"
        "s_lshl_b32 s34, s34, 19\n\t"
        "buffer_load_dwordx4 v[64:67], v96, s[16:19], s34 offen nt\n\t"
        "s_add_u32 s34, s34, 0x10000\n\t"
        "buffer_load_dwordx4 v[68:71], v96, s[16:19], s34 offen nt\n\t"
        "s_add_u32 s34, s34, 0x10000\n\t"
        "buffer_load_dwordx4 v[72:75], v96, s[16:19], s34 offen nt\n\t"
        "s_add_u32 s34, s34, 0x10000\n\t"
        "buffer_load_dwordx4 v[76:79], v96, s[16:19], s34 offen nt\n\t"
        "s_add_u32 s34, s34, 0x10000\n\t"
        "buffer_load_dwordx4 v[80:83], v96, s[16:19], s34 offen nt\n\t"
        "s_add_u32 s34, s34, 0x10000\n\t"
        "buffer_load_dwordx4 v[84:87], v96, s[16:19], s34 offen nt\n\t"
        "s_add_u32 s34, s34, 0x10000\n\t"
        "buffer_load_dwordx4 v[88:91], v96, s[16:19], s34 offen nt\n\t"
        "s_add_u32 s34, s34, 0x10000\n\t"
        "buffer_load_dwordx4 v[92:95], v96, s[16:19], s34 offen nt\n\t"
        "s_waitcnt lgkmcnt(0)\n\t"
        "ds_write_b128 v117, v[104:107] offset:16384\n\t"
        "ds_write_b128 v117, v[108:111] offset:17408\n\t"
        "s_waitcnt vmcnt(16)\n\t"
        "ds_write_b128 v97, v[0:3] offset:0\n\t"
        "ds_write_b128 v98, v[4:7] offset:1024\n\t"
        "ds_write_b128 v97, v[8:11] offset:2048\n\t"
        "ds_write_b128 v98, v[12:15] offset:3072\n\t"
        "ds_write_b128 v97, v[16:19] offset:4096\n\t"
        "ds_write_b128 v98, v[20:23] offset:5120\n\t"
        "ds_write_b128 v97, v[24:27] offset:6144\n\t"
        "ds_write_b128 v98, v[28:31] offset:7168\n\t"
        "ds_read_b64_tr_b16 v[184:185], v99 offset:0\n\t"
        "ds_read_b64_tr_b16 v[186:187], v99 offset:256\n\t"
        "ds_read_b64_tr_b16 v[188:189], v99 offset:4096\n\t"
        "ds_read_b64_tr_b16 v[190:191], v99 offset:4352\n\t"
        "s_waitcnt lgkmcnt(8)\n\t"
        "ds_read_b64_tr_b16 v[192:193], v100 offset:0\n\t"
        "ds_read_b64_tr_b16 v[194:195], v100 offset:256\n\t"
        "ds_read_b64_tr_b16 v[196:197], v100 offset:4096\n\t"
        "ds_read_b64_tr_b16 v[198:199], v100 offset:4352\n\t"
        "s_waitcnt lgkmcnt(8)\n\t"
        "ds_read_b64_tr_b16 v[200:201], v101 offset:0\n\t"
        "ds_read_b64_tr_b16 v[202:203], v101 offset:256\n\t"
        "ds_read_b64_tr_b16 v[204:205], v101 offset:4096\n\t"
        "ds_read_b64_tr_b16 v[206:207], v101 offset:4352\n\t"
        "s_waitcnt lgkmcnt(8)\n\t"
        "ds_read_b64_tr_b16 v[104:105], v102 offset:0\n\t"
        "ds_read_b64_tr_b16 v[106:107], v102 offset:256\n\t"
        "ds_read_b64_tr_b16 v[108:109], v102 offset:4096\n\t"
        "ds_read_b64_tr_b16 v[110:111], v102 offset:4352\n\t"
        "s_waitcnt lgkmcnt(0)\n\t"
        "ds_write_b128 v117, v[104:107] offset:18432\n\t"
        "ds_write_b128 v117, v[108:111] offset:19456\n\t"
        "s_waitcnt vmcnt(8)\n\t"
        "ds_write_b128 v97, v[32:35] offset:0\n\t"
        "ds_write_b128 v98, v[36:39] offset:1024\n\t"
        "ds_write_b128 v97, v[40:43] offset:2048\n\t"
        "ds_write_b128 v98, v[44:47] offset:3072\n\t"
        "ds_write_b128 v97, v[48:51] offset:4096\n\t"
        "ds_write_b128 v98, v[52:55] offset:5120\n\t"
        "ds_write_b128 v97, v[56:59] offset:6144\n\t"
        "ds_write_b128 v98, v[60:63] offset:7168\n\t"
        "ds_read_b64_tr_b16 v[208:209], v99 offset:0\n\t"
        "ds_read_b64_tr_b16 v[210:211], v99 offset:256\n\t"
        "ds_read_b64_tr_b16 v[212:213], v99 offset:4096\n\t"
        "ds_read_b64_tr_b16 v[214:215], v99 offset:4352\n\t"
        "s_waitcnt lgkmcnt(8)\n\t"
        "ds_read_b64_tr_b16 v[216:217], v100 offset:0\n\t"
        "ds_read_b64_tr_b16 v[218:219], v100 offset:256\n\t"
        "ds_read_b64_tr_b16 v[220:221], v100 offset:4096\n\t"
        "ds_read_b64_tr_b16 v[222:223], v100 offset:4352\n\t"
        "s_waitcnt lgkmcnt(8)\n\t"
        "ds_read_b64_tr_b16 v[224:225], v101 offset:0\n\t"
        "ds_read_b64_tr_b16 v[226:227], v101 offset:256\n\t"
        "ds_read_b64_tr_b16 v[228:229], v101 offset:4096\n\t"
        "ds_read_b64_tr_b16 v[230:231], v101 offset:4352\n\t"
        "s_waitcnt lgkmcnt(8)\n\t"
        "ds_read_b64_tr_b16 v[104:105], v102 offset:0\n\t"
        "ds_read_b64_tr_b16 v[106:107], v102 offset:256\n\t"
        "ds_read_b64_tr_b16 v[108:109], v102 offset:4096\n\t"
        "ds_read_b64_tr_b16 v[110:111], v102 offset:4352\n\t"
        "s_mov_b32 s35, 33792\n\t"
        "buffer_load_dword v0, off, s[12:15], s35 sc1\n\t"
        "s_waitcnt lgkmcnt(0)\n\t"
        "ds_write_b128 v117, v[104:107] offset:20480\n\t"
        "ds_write_b128 v117, v[108:111] offset:21504\n\t"
        "s_waitcnt vmcnt(0)\n\t"
        "ds_write_b128 v97, v[64:67] offset:0\n\t"
        "ds_write_b128 v98, v[68:71] offset:1024\n\t"
        "ds_write_b128 v97, v[72:75] offset:2048\n\t"
        "ds_write_b128 v98, v[76:79] offset:3072\n\t"
        "ds_write_b128 v97, v[80:83] offset:4096\n\t"
        "ds_write_b128 v98, v[84:87] offset:5120\n\t"
        "ds_write_b128 v97, v[88:91] offset:6144\n\t"
        "ds_write_b128 v98, v[92:95] offset:7168\n\t"
        "ds_read_b64_tr_b16 v[232:233], v99 offset:0\n\t"
        "ds_read_b64_tr_b16 v[234:235], v99 offset:256\n\t"
        "ds_read_b64_tr_b16 v[236:237], v99 offset:4096\n\t"
        "ds_read_b64_tr_b16 v[238:239], v99 offset:4352\n\t"
        "s_waitcnt lgkmcnt(8)\n\t"
        "ds_read_b64_tr_b16 v[240:241], v100 offset:0\n\t"
        "ds_read_b64_tr_b16 v[242:243], v100 offset:256\n\t"
        "ds_read_b64_tr_b16 v[244:245], v100 offset:4096\n\t"
        "ds_read_b64_tr_b16 v[246:247], v100 offset:4352\n\t"
        "s_waitcnt lgkmcnt(8)\n\t"
        "ds_read_b64_tr_b16 v[248:249], v101 offset:0\n\t"
        "ds_read_b64_tr_b16 v[250:251], v101 offset:256\n\t"
        "ds_read_b64_tr_b16 v[252:253], v101 offset:4096\n\t"
        "ds_read_b64_tr_b16 v[254:255], v101 offset:4352\n\t"
        "s_waitcnt lgkmcnt(8)\n\t"
        "ds_read_b64_tr_b16 v[104:105], v102 offset:0\n\t"
        "ds_read_b64_tr_b16 v[106:107], v102 offset:256\n\t"
        "ds_read_b64_tr_b16 v[108:109], v102 offset:4096\n\t"
        "ds_read_b64_tr_b16 v[110:111], v102 offset:4352\n\t"
        "s_mov_b32 s42, 0\n\t"
        "LA_cal%=:\n\t"
        "v_readfirstlane_b32 s34, v0\n\t"
        "s_nop 0\n\t"
        "s_cmp_lg_u32 s34, 0\n\t"
        "s_cbranch_scc1 LA_calok%=\n\t"
        "s_sleep 4\n\t"
        "s_mov_b32 s35, 33792\n\t"
        "buffer_load_dword v0, off, s[12:15], s35 sc1\n\t"
        "s_waitcnt vmcnt(0)\n\t"
        "s_add_u32 s42, s42, 1\n\t"
        "s_cmp_lt_u32 s42, 0x100000\n\t"
        "s_cbranch_scc1 LA_cal%=\n\t"
        "s_movk_i32 s34, 8\n\t"
        "s_branch LA_die%=\n\t"
        "LA_calok%=:\n\t"
        "s_lshl_b32 s35, s44, 10\n\t"
        "v_lshlrev_b32_e32 v1, 2, v122\n\t"
        "v_and_b32_e32 v1, 0x3c0, v1\n\t"
        "buffer_load_dword v1, v1, s[12:15], s35 offen offset:0 sc1\n\t"
        "s_waitcnt lgkmcnt(0)\n\t"
        "ds_write_b128 v126, v[104:107] offset:8192\n\t"
        "ds_write_b128 v126, v[108:111] offset:9216\n\t"
        "s_waitcnt lgkmcnt(0)\n\t"
        "v_mov_b32_e32 v110, v103\n\t"
        "ds_read_b128 v[64:67], v126 offset:0\n\t"
        "ds_read_b128 v[68:71], v126 offset:1024\n\t"
        "ds_read_b128 v[72:75], v126 offset:2048\n\t"
        "ds_read_b128 v[76:79], v126 offset:3072\n\t"
        "ds_read_b128 v[80:83], v126 offset:4096\n\t"
        "ds_read_b128 v[84:87], v126 offset:5120\n\t"
        "ds_read_b128 v[88:91], v126 offset:6144\n\t"
        "ds_read_b128 v[92:95], v126 offset:7168\n\t"
        "ds_read_b128 v[96:99], v126 offset:8192\n\t"
        "ds_read_b128 v[100:103], v126 offset:9216\n\t"
        "s_waitcnt lgkmcnt(0)\n\t"
        "v_mov_b32_e32 v126, 0\n\t"
        "s_barrier\n\t"
        "s_mov_b32 s48, 0\n\t"
        "s_mov_b32 s54, 0\n\t"
        "s_mov_b32 s0, 0x3f803f80\n\t"
        "s_mov_b32 s1, s0\n\t"
        "s_movk_i32 s56, 24576\n\t"
        "s_and_b32 s58, s43, 1\n\t"
        "s_lshl_b32 s58, s58, 12\n\t"
        "s_lshl_b32 s57, s44, 5\n\t"
        "s_add_u32 s57, s57, s43\n\t"
        "s_lshl_b32 s57, s57, 2\n\t"
        "s_add_u32 s57, s57, 458752\n\t"
        "s_waitcnt vmcnt(0)\n\t"
        "v_mov_b32_e32 v111, v1\n\t"
        "v_readfirstlane_b32 s34, v0\n\t"
        "s_nop 0\n\t"
        "s_and_b32 s34, s34, 8192\n\t"
        "s_add_u32 s8, s8, s34\n\t"
        "s_addc_u32 s9, s9, 0\n\t"
        "s_barrier\n\t"
        "s_cmp_lg_u32 s43, 0\n\t"
        "s_cbranch_scc1 LA_st%=\n\t"
        "v_mov_b32_e32 v2, 1\n\t"
        "s_mov_b32 s35, 34304\n\t"
        "s_mov_b64 exec, 1\n\t"
        "buffer_atomic_add v2, off, s[12:15], s35 sc0\n\t"
        "s_mov_b64 exec, -1\n\t"
        "s_waitcnt vmcnt(0)\n\t"
        "v_readfirstlane_b32 s34, v2\n\t"
        "s_nop 0\n\t"
        "s_cmp_lg_u32 s34, 95\n\t"
        "s_cbranch_scc1 LA_st%=\n\t"
        "s_memrealtime s[32:33]\n\t"
        "v_mbcnt_lo_u32_b32 v3, -1, 0\n\t"
        "v_lshlrev_b32_e32 v3, 7, v3\n\t"
        "s_waitcnt lgkmcnt(0)\n\t"
        "s_add_u32 s34, s32, 50\n\t"
        "s_or_b32 s34, s34, 1\n\t"
        "v_mov_b32_e32 v2, s34\n\t"
        "s_mov_b32 s35, 36864\n\t"
        "s_mov_b64 exec, 0xffff\n\t"
        "buffer_store_dword v2, v3, s[12:15], s35 offen sc1\n\t"
        "s_mov_b64 exec, -1\n\t"
        "LA_st%=:\n\t"
        "s_mov_b32 s24, 0\n\t"
        "s_mov_b32 s26, 0\n\t"
        "s_mov_b32 s27, 0x80000\n\t"
        "s_mov_b32 s28, 0x180000\n\t"
        "s_mov_b32 s31, 0\n\t"
        "s_mov_b32 s47, 0\n\t"
        "s_mov_b32 s38, 0xff00ff00\n\t"
        "s_mov_b32 s39, 0xff00ff00\n\t"
        "s_mov_b32 s40, 0\n\t"
        "s_mov_b32 s41, -1\n\t"
        "ds_read_b128 v[32:35], v117 offset:0\n\t"
        "ds_read_b128 v[36:39], v117 offset:1024\n\t"
        "ds_read_b128 v[40:43], v117 offset:2048\n\t"
        "ds_read_b128 v[44:47], v117 offset:3072\n\t"
        "ds_read_b128 v[48:51], v117 offset:4096\n\t"
        "ds_read_b128 v[52:55], v117 offset:5120\n\t"
        "ds_read_b128 v[56:59], v117 offset:6144\n\t"
        "ds_read_b128 v[60:63], v117 offset:7168\n\t"
        "v_mov_b64_e32 v[0:1], 0\n\t"
        "v_mov_b64_e32 v[2:3], 0\n\t"
        "v_mov_b64_e32 v[4:5], 0\n\t"
        "v_mov_b64_e32 v[6:7], 0\n\t"
        "v_mov_b64_e32 v[8:9], 0\n\t"
        "v_mov_b64_e32 v[10:11], 0\n\t"
        "v_mov_b64_e32 v[12:13], 0\n\t"
        "v_mov_b64_e32 v[14:15], 0\n\t"
        "s_lshr_b32 s59, s48, 1\n\t"
        "s_cmp_gt_u32 s54, s59\n\t"
        "s_cbranch_scc1 LA_kv0ok%=\n\t"
        "s_mov_b32 s42, 0\n\t"
        "s_waitcnt vmcnt(0)\n\t"
        "s_branch LA_kv0w%=\n\t"
        "LA_kv0%=:\n\t"
        "s_waitcnt vmcnt(0)\n\t"
        "LA_kv0w%=:\n\t"
        "v_min_u32_dpp v111, v111, v111 quad_perm:[1,0,3,2] row_mask:0xf bank_mask:0xf\n\t"
        "s_nop 1\n\t"
        "v_min_u32_dpp v111, v111, v111 quad_perm:[2,3,0,1] row_mask:0xf bank_mask:0xf\n\t"
        "s_nop 1\n\t"
        "v_min_u32_dpp v111, v111, v111 row_ror:4 row_mask:0xf bank_mask:0xf\n\t"
        "s_nop 1\n\t"
        "v_min_u32_dpp v111, v111, v111 row_ror:8 row_mask:0xf bank_mask:0xf\n\t"
        "s_nop 0\n\t"
        "v_readfirstlane_b32 s54, v111\n\t"
        "s_nop 0\n\t"
        "s_cmp_gt_u32 s54, s59\n\t"
        "s_cbranch_scc1 LA_kv0ok%=\n\t"
        "s_sleep 4\n\t"
        "s_lshl_b32 s35, s44, 10\n\t"
        "v_lshlrev_b32_e32 v111, 2, v122\n\t"
        "v_and_b32_e32 v111, 0x3c0, v111\n\t"
        "buffer_load_dword v111, v111, s[12:15], s35 offen offset:0 sc1\n\t"
        "s_add_u32 s42, s42, 1\n\t"
        "s_cmp_lt_u32 s42, 0x100000\n\t"
        "s_cbranch_scc1 LA_kv0%=\n\t"
        "s_movk_i32 s34, 16\n\t"
        "s_branch LA_die%=\n\t"
        "LA_kv0ok%=:\n\t"
        "s_and_b32 s36, s48, 1\n\t"
        "s_mul_i32 s36, s36, 24576\n\t"
        "s_add_u32 s36, s36, s55\n\t"
        "s_lshl_b32 s37, s48, 18\n\t"
        "s_lshl_b32 s34, s43, 11\n\t"
        "s_add_u32 s37, s37, s34\n\t"
        "s_add_u32 m0, s36, 0\n\t"
        "s_add_u32 s34, s37, 29548544\n\t"
        "buffer_load_dwordx4 v122, s[12:15], s34 offen sc1 lds\n\t"
        "s_add_u32 m0, s36, 1024\n\t"
        "s_add_u32 s34, s37, 29549568\n\t"
        "buffer_load_dwordx4 v122, s[12:15], s34 offen sc1 lds\n\t"
        "s_add_u32 m0, s36, 8192\n\t"
        "s_add_u32 s34, s37, 12771328\n\t"
        "buffer_load_dwordx4 v122, s[12:15], s34 offen sc1 lds\n\t"
        "s_add_u32 m0, s36, 9216\n\t"
        "s_add_u32 s34, s37, 12772352\n\t"
        "buffer_load_dwordx4 v122, s[12:15], s34 offen sc1 lds\n\t"
        "s_add_u32 m0, s36, 16384\n\t"
        "s_add_u32 s34, s37, 46325760\n\t"
        "buffer_load_dwordx4 v122, s[12:15], s34 offen sc1 lds\n\t"
        "s_add_u32 m0, s36, 17408\n\t"
        "s_add_u32 s34, s37, 46326784\n\t"
        "buffer_load_dwordx4 v122, s[12:15], s34 offen sc1 lds\n\t"
        "s_mov_b32 s42, 0\n\t"
        "LA_gw%=:\n\t"
        "buffer_load_dword v16, off, s[12:15], s45 sc1\n\t"
        "s_waitcnt vmcnt(0)\n\t"
        "v_readfirstlane_b32 s25, v16\n\t"
        "s_nop 0\n\t"
        "s_cmp_lg_u32 s25, 0\n\t"
        "s_cbranch_scc1 LA_loop%=\n\t"
        "s_sleep 2\n\t"
        "s_add_u32 s42, s42, 1\n\t"
        "s_cmp_lt_u32 s42, 0x100000\n\t"
        "s_cbranch_scc1 LA_gw%=\n\t"
        "s_movk_i32 s34, 9\n\t"
        "s_branch LA_die%=\n\t"
        ".p2align 8\n\t"
        "LA_loop%=:\n\t"
        "s_mov_b32 s42, 0\n\t"
        "s_sub_u32 s35, s25, 8\n\t"
        "s_waitcnt lgkmcnt(0)\n\t"
        "s_memrealtime s[2:3]\n\t"
        "s_memrealtime s[4:5]\n\t"
        "s_memrealtime s[6:7]\n\t"
        "LA_wa%=:\n\t"
        "s_memrealtime s[32:33]\n\t"
        "s_waitcnt lgkmcnt(6)\n\t"
        "s_sub_u32 s34, s35, s2\n\t"
        "s_cmpk_lt_u32 s34, 0x2000\n\t"
        "s_cbranch_scc0 LA_wa%=x\n\t"
        "s_memrealtime s[2:3]\n\t"
        "s_waitcnt lgkmcnt(6)\n\t"
        "s_sub_u32 s34, s35, s4\n\t"
        "s_cmpk_lt_u32 s34, 0x2000\n\t"
        "s_cbranch_scc0 LA_wa%=x\n\t"
        "s_memrealtime s[4:5]\n\t"
        "s_waitcnt lgkmcnt(6)\n\t"
        "s_sub_u32 s34, s35, s6\n\t"
        "s_cmpk_lt_u32 s34, 0x2000\n\t"
        "s_cbranch_scc0 LA_wa%=x\n\t"
        "s_memrealtime s[6:7]\n\t"
        "s_waitcnt lgkmcnt(6)\n\t"
        "s_sub_u32 s34, s35, s32\n\t"
        "s_cmpk_lt_u32 s34, 0x2000\n\t"
        "s_cbranch_scc1 LA_wa%=\n\t"
        "s_nop 3\n\t"
        "LA_wa%=x:\n\t"
        "s_add_i32 s34, s34, 4\n\t"
        "s_max_i32 s34, s34, 0\n\t"
        "s_bitcmp1_b32 s34, 1\n\t"
        "s_cbranch_scc0 LA_wa%=y\n\t"
        "s_nop 15\n\t"
        "LA_wa%=y:\n\t"
        "s_bitcmp1_b32 s34, 0\n\t"
        "s_cbranch_scc0 LA_wa%=z\n\t"
        "s_nop 9\n\t"
        "LA_wa%=z:\n\t"
        "LA_rd%=:\n\t"
        "buffer_load_dwordx4 v[16:19], v112, s[8:11], s26 offen offset:0 sc1\n\t"
        "s_nop 12\n\t"
        "buffer_load_dwordx4 v[20:23], v112, s[8:11], s26 offen offset:1024 sc1\n\t"
        "s_nop 12\n\t"
        "buffer_load_dwordx4 v[24:27], v112, s[8:11], s26 offen offset:2048 sc1\n\t"
        "s_nop 12\n\t"
        "buffer_load_dwordx4 v[28:31], v112, s[8:11], s26 offen offset:3072 sc1\n\t"
        "LA_go%=:\n\t"
        "s_waitcnt vmcnt(3)\n\t"
        "v_smfmac_f32_16x16x64_bf16 v[0:3], v[16:19], a[0:7], v110\n\t"
        "ds_write_b128 v113, v[16:19] offset:0\n\t"
        "v_smfmac_f32_16x16x64_bf16 v[4:7], v[16:19], a[8:15], v110\n\t"
        "v_max3_u32 v111, v16, v17, v18\n\t"
        "v_max_u32_e32 v111, v19, v111\n\t"
        "v_smfmac_f32_16x16x64_bf16 v[8:11], v[16:19], a[16:23], v110\n\t"
        "v_smfmac_f32_16x16x64_bf16 v[12:15], v[16:19], v[32:39], v110\n\t"
        "s_waitcnt vmcnt(2)\n\t"
        "v_smfmac_f32_16x16x64_bf16 v[0:3], v[20:23], a[24:31], v110\n\t"
        "ds_write_b128 v113, v[20:23] offset:1024\n\t"
        "v_smfmac_f32_16x16x64_bf16 v[4:7], v[20:23], a[32:39], v110\n\t"
        "v_max3_u32 v111, v20, v21, v111\n\t"
        "v_max3_u32 v111, v22, v23, v111\n\t"
        "v_smfmac_f32_16x16x64_bf16 v[8:11], v[20:23], a[40:47], v110\n\t"
        "s_waitcnt vmcnt(1)\n\t"
        "ds_write_b128 v113, v[24:27] offset:2048\n\t"
        "v_smfmac_f32_16x16x64_bf16 v[12:15], v[20:23], v[40:47], v110\n\t"
        "v_max3_u32 v111, v24, v25, v111\n\t"
        "v_max3_u32 v111, v26, v27, v111\n\t"
        "v_smfmac_f32_16x16x64_bf16 v[0:3], v[24:27], a[48:55], v110\n\t"
        "v_smfmac_f32_16x16x64_bf16 v[4:7], v[24:27], a[56:63], v110\n\t"
        "s_waitcnt vmcnt(0)\n\t"
        "ds_write_b128 v113, v[28:31] offset:3072\n\t"
        "v_max3_u32 v111, v28, v29, v111\n\t"
        "v_max3_u32 v111, v30, v31, v111\n\t"
        "v_smfmac_f32_16x16x64_bf16 v[8:11], v[24:27], a[64:71], v110\n\t"
        "v_cmp_eq_u32_e32 vcc, -1, v111\n\t"
        "v_smfmac_f32_16x16x64_bf16 v[12:15], v[24:27], v[48:55], v110\n\t"
        "s_waitcnt lgkmcnt(0)\n\t"
        "s_cbranch_vccnz LA_rdfail%=\n\t"
        "s_barrier\n\t"
        "ds_read_b128 v[16:19], v114 offset:0\n\t"
        "ds_read_b128 v[20:23], v114 offset:1024\n\t"
        "ds_read_b128 v[24:27], v114 offset:2048\n\t"
        "v_smfmac_f32_16x16x64_bf16 v[0:3], v[28:31], a[72:79], v110\n\t"
        "v_smfmac_f32_16x16x64_bf16 v[4:7], v[28:31], a[80:87], v110\n\t"
        "v_smfmac_f32_16x16x64_bf16 v[8:11], v[28:31], a[88:95], v110\n\t"
        "v_smfmac_f32_16x16x64_bf16 v[12:15], v[28:31], v[56:63], v110\n\t"
        "ds_read_b128 v[28:31], v114 offset:3072\n\t"
        "s_waitcnt lgkmcnt(3)\n\t"
        "v_smfmac_f32_16x16x64_bf16 v[12:15], v[16:19], v[64:71], v110\n\t"
        "ds_read_b128 v[32:35], v117 offset:8192\n\t"
        "v_smfmac_f32_16x16x64_bf16 v[0:3], v[16:19], a[96:103], v110\n\t"
        "ds_read_b128 v[36:39], v117 offset:9216\n\t"
        "v_smfmac_f32_16x16x64_bf16 v[4:7], v[16:19], a[104:111], v110\n\t"
        "s_waitcnt lgkmcnt(4)\n\t"
        "v_smfmac_f32_16x16x64_bf16 v[8:11], v[16:19], a[112:119], v110\n\t"
        "ds_read_b128 v[16:19], v115 offset:0\n\t"
        "v_smfmac_f32_16x16x64_bf16 v[12:15], v[20:23], v[72:79], v110\n\t"
        "ds_read_b128 v[40:43], v117 offset:10240\n\t"
        "v_smfmac_f32_16x16x64_bf16 v[0:3], v[20:23], a[120:127], v110\n\t"
        "ds_read_b128 v[44:47], v117 offset:11264\n\t"
        "v_smfmac_f32_16x16x64_bf16 v[4:7], v[20:23], a[128:135], v110\n\t"
        "s_waitcnt lgkmcnt(6)\n\t"
        "v_smfmac_f32_16x16x64_bf16 v[8:11], v[20:23], a[136:143], v110\n\t"
        "ds_read_b128 v[20:23], v115 offset:1024\n\t"
        "v_smfmac_f32_16x16x64_bf16 v[12:15], v[24:27], v[80:87], v110\n\t"
        "ds_read_b128 v[48:51], v117 offset:12288\n\t"
        "v_smfmac_f32_16x16x64_bf16 v[0:3], v[24:27], a[144:151], v110\n\t"
        "ds_read_b128 v[52:55], v117 offset:13312\n\t"
        "v_smfmac_f32_16x16x64_bf16 v[4:7], v[24:27], a[152:159], v110\n\t"
        "s_waitcnt lgkmcnt(8)\n\t"
        "v_smfmac_f32_16x16x64_bf16 v[8:11], v[24:27], a[160:167], v110\n\t"
        "ds_read_b128 v[24:27], v115 offset:2048\n\t"
        "v_smfmac_f32_16x16x64_bf16 v[12:15], v[28:31], v[88:95], v110\n\t"
        "ds_read_b128 v[56:59], v117 offset:14336\n\t"
        "v_smfmac_f32_16x16x64_bf16 v[0:3], v[28:31], a[168:175], v110\n\t"
        "ds_read_b128 v[60:63], v117 offset:15360\n\t"
        "v_smfmac_f32_16x16x64_bf16 v[4:7], v[28:31], a[176:183], v110\n\t"
        "s_waitcnt lgkmcnt(8)\n\t"
        "v_smfmac_f32_16x16x64_bf16 v[8:11], v[28:31], a[184:191], v110\n\t"
        "ds_read_b128 v[28:31], v115 offset:3072\n\t"
        "v_smfmac_f32_16x16x64_bf16 v[12:15], v[16:19], v[32:39], v110\n\t"
        "ds_read_b128 v[32:35], v117 offset:16384\n\t"
        "v_smfmac_f32_16x16x64_bf16 v[0:3], v[16:19], a[192:199], v110\n\t"
        "ds_read_b128 v[36:39], v117 offset:17408\n\t"
        "v_smfmac_f32_16x16x64_bf16 v[4:7], v[16:19], a[200:207], v110\n\t"
        "s_waitcnt lgkmcnt(8)\n\t"
        "v_smfmac_f32_16x16x64_bf16 v[8:11], v[16:19], a[208:215], v110\n\t"
        "ds_read_b128 v[16:19], v116 offset:0\n\t"
        "v_smfmac_f32_16x16x64_bf16 v[12:15], v[20:23], v[40:47], v110\n\t"
        "ds_read_b128 v[40:43], v117 offset:18432\n\t"
        "v_smfmac_f32_16x16x64_bf16 v[0:3], v[20:23], a[216:223], v110\n\t"
        "ds_read_b128 v[44:47], v117 offset:19456\n\t"
        "v_smfmac_f32_16x16x64_bf16 v[4:7], v[20:23], a[224:231], v110\n\t"
        "s_waitcnt lgkmcnt(8)\n\t"
        "v_smfmac_f32_16x16x64_bf16 v[8:11], v[20:23], a[232:239], v110\n\t"
        "ds_read_b128 v[20:23], v116 offset:1024\n\t"
        "v_smfmac_f32_16x16x64_bf16 v[12:15], v[24:27], v[48:55], v110\n\t"
        "ds_read_b128 v[48:51], v117 offset:20480\n\t"
        "v_smfmac_f32_16x16x64_bf16 v[0:3], v[24:27], a[240:247], v110\n\t"
        "ds_read_b128 v[52:55], v117 offset:21504\n\t"
        "v_smfmac_f32_16x16x64_bf16 v[4:7], v[24:27], a[248:255], v110\n\t"
        "s_waitcnt lgkmcnt(8)\n\t"
        "v_smfmac_f32_16x16x64_bf16 v[8:11], v[24:27], v[128:135], v110\n\t"
        "ds_read_b128 v[24:27], v116 offset:2048\n\t"
        "v_smfmac_f32_16x16x64_bf16 v[12:15], v[28:31], v[56:63], v110\n\t"
        "ds_read_b128 v[56:59], v124 offset:3072\n\t"
        "v_smfmac_f32_16x16x64_bf16 v[0:3], v[28:31], v[136:143], v110\n\t"
        "ds_read_b128 v[60:63], v124 offset:7168\n\t"
        "v_smfmac_f32_16x16x64_bf16 v[4:7], v[28:31], v[144:151], v110\n\t"
        "s_waitcnt lgkmcnt(8)\n\t"
        "v_smfmac_f32_16x16x64_bf16 v[8:11], v[28:31], v[152:159], v110\n\t"
        "ds_read_b128 v[28:31], v116 offset:3072\n\t"
        "v_smfmac_f32_16x16x64_bf16 v[12:15], v[16:19], v[32:39], v110\n\t"
        "ds_read_b128 v[32:35], v124 offset:0\n\t"
        "v_smfmac_f32_16x16x64_bf16 v[0:3], v[16:19], v[160:167], v110\n\t"
        "ds_read_b128 v[36:39], v124 offset:4096\n\t"
        "v_smfmac_f32_16x16x64_bf16 v[4:7], v[16:19], v[168:175], v110\n\t"
        "s_waitcnt lgkmcnt(8)\n\t"
        "v_smfmac_f32_16x16x64_bf16 v[8:11], v[16:19], v[176:183], v110\n\t"
        "v_smfmac_f32_16x16x64_bf16 v[12:15], v[20:23], v[40:47], v110\n\t"
        "ds_read_b128 v[40:43], v124 offset:1024\n\t"
        "v_smfmac_f32_16x16x64_bf16 v[8:11], v[20:23], v[200:207], v110\n\t"
        "ds_read_b128 v[44:47], v124 offset:5120\n\t"
        "s_waitcnt lgkmcnt(7)\n\t"
        "v_smfmac_f32_16x16x64_bf16 v[12:15], v[24:27], v[48:55], v110\n\t"
        "ds_read_b128 v[48:51], v124 offset:2048\n\t"
        "v_smfmac_f32_16x16x64_bf16 v[8:11], v[24:27], v[224:231], v110\n\t"
        "ds_read_b128 v[52:55], v124 offset:6144\n\t"
        "s_waitcnt lgkmcnt(6)\n\t"
        "v_smfmac_f32_16x16x64_bf16 v[12:15], v[28:31], v[96:103], v110\n\t"
        "v_smfmac_f32_16x16x64_bf16 v[8:11], v[28:31], v[248:255], v110\n\t"
        "v_smfmac_f32_16x16x64_bf16 v[0:3], v[20:23], v[184:191], v110\n\t"
        "s_lshl_b32 s34, s43, 11\n\t"
        "v_add_u32_e32 v108, s34, v124\n\t"
        "v_smfmac_f32_16x16x64_bf16 v[0:3], v[24:27], v[208:215], v110\n\t"
        "v_add_u32_e32 v109, s58, v124\n\t"
        "v_smfmac_f32_16x16x64_bf16 v[4:7], v[20:23], v[192:199], v110\n\t"
        "v_smfmac_f32_16x16x64_bf16 v[0:3], v[28:31], v[232:239], v110\n\t"
        "v_smfmac_f32_16x16x64_bf16 v[4:7], v[24:27], v[216:223], v110\n\t"
        "buffer_load_dwordx4 v[104:107], v120, s[8:11], s26 offen sc1\n\t"
        "v_smfmac_f32_16x16x64_bf16 v[4:7], v[28:31], v[240:247], v110\n\t"
        "v_add_f32_e32 v12, v12, v13\n\t"
        "v_add_f32_e32 v14, v14, v15\n\t"
        "v_cvt_pk_bf16_f32 v12, v12, v14\n\t"
        "v_add_f32_e32 v8, v8, v9\n\t"
        "v_add_f32_e32 v10, v10, v11\n\t"
        "v_cvt_pk_bf16_f32 v8, v8, v10\n\t"
        "ds_write2_b32 v118, v8, v12 offset0:64 offset1:96\n\t"
        "v_add_f32_e32 v0, v0, v1\n\t"
        "v_add_f32_e32 v2, v2, v3\n\t"
        "v_cvt_pk_bf16_f32 v0, v0, v2\n\t"
        "v_add_f32_e32 v4, v4, v5\n\t"
        "v_add_f32_e32 v6, v6, v7\n\t"
        "v_cvt_pk_bf16_f32 v4, v4, v6\n\t"
        "ds_write2_b32 v118, v0, v4 offset0:0 offset1:32\n\t"
        "s_cmp_eq_u32 s24, 63\n\t"
        "s_waitcnt lgkmcnt(0)\n\t"
        "s_barrier\n\t"
        "s_cbranch_scc1 LA_skip%=\n\t"
        "ds_read_b64_tr_b16 v[16:17], v119 offset:0\n\t"
        "ds_read_b64_tr_b16 v[18:19], v119 offset:32\n\t"
        "ds_read_b64_tr_b16 v[20:21], v119 offset:256\n\t"
        "ds_read_b64_tr_b16 v[22:23], v119 offset:288\n\t"
        "ds_read_b64_tr_b16 v[24:25], v119 offset:512\n\t"
        "ds_read_b64_tr_b16 v[26:27], v119 offset:544\n\t"
        "ds_read_b64_tr_b16 v[28:29], v119 offset:768\n\t"
        "ds_read_b64_tr_b16 v[30:31], v119 offset:800\n\t"
        "buffer_store_dword v126, v127, s[12:15], 0 offen sc1\n\t"
        "s_waitcnt lgkmcnt(6)\n\t"
        "v_mfma_f32_16x16x32_bf16 v[0:3], v[32:35], v[16:19], 0\n\t"
        "ds_read_b64_tr_b16 v[32:33], v119 offset:2112\n\t"
        "ds_read_b64_tr_b16 v[34:35], v119 offset:2144\n\t"
        "v_mfma_f32_16x16x32_bf16 v[4:7], v[36:39], v[16:19], 0\n\t"
        "ds_read_b64_tr_b16 v[36:37], v119 offset:2368\n\t"
        "ds_read_b64_tr_b16 v[38:39], v119 offset:2400\n\t"
        "s_waitcnt lgkmcnt(8)\n\t"
        "v_mfma_f32_16x16x32_bf16 v[0:3], v[40:43], v[20:23], v[0:3]\n\t"
        "ds_read_b64_tr_b16 v[40:41], v119 offset:2624\n\t"
        "ds_read_b64_tr_b16 v[42:43], v119 offset:2656\n\t"
        "v_mfma_f32_16x16x32_bf16 v[4:7], v[44:47], v[20:23], v[4:7]\n\t"
        "ds_read_b64_tr_b16 v[44:45], v119 offset:2880\n\t"
        "ds_read_b64_tr_b16 v[46:47], v119 offset:2912\n\t"
        "s_waitcnt lgkmcnt(10)\n\t"
        "v_mfma_f32_16x16x32_bf16 v[0:3], v[48:51], v[24:27], v[0:3]\n\t"
        "ds_read_b128 v[48:51], v108 offset:16384\n\t"
        "v_mfma_f32_16x16x32_bf16 v[4:7], v[52:55], v[24:27], v[4:7]\n\t"
        "ds_read_b128 v[52:55], v108 offset:17408\n\t"
        "s_waitcnt lgkmcnt(10)\n\t"
        "v_mfma_f32_16x16x32_bf16 v[0:3], v[56:59], v[28:31], v[0:3]\n\t"
        "v_mfma_f32_16x16x32_bf16 v[4:7], v[60:63], v[28:31], v[4:7]\n\t"
        "s_waitcnt lgkmcnt(2)\n\t"
        "v_mfma_f32_16x16x32_bf16 v[8:11], v[32:35], v[16:19], 0\n\t"
        "s_nop 1\n\t"
        "v_mfma_f32_16x16x32_bf16 v[8:11], v[36:39], v[20:23], v[8:11]\n\t"
        "v_mov_b64_e32 v[16:17], s[0:1]\n\t"
        "v_mov_b64_e32 v[18:19], s[0:1]\n\t"
        "v_mfma_f32_16x16x32_bf16 v[8:11], v[40:43], v[24:27], v[8:11]\n\t"
        "v_max3_f32 v59, v0, v1, v2\n\t"
        "v_max3_f32 v59, v59, v3, v4\n\t"
        "v_mfma_f32_16x16x32_bf16 v[8:11], v[44:47], v[28:31], v[8:11]\n\t"
        "v_max3_f32 v59, v59, v5, v6\n\t"
        "v_max_f32_e32 v60, v59, v7\n\t"
        "v_max_f32_e32 v59, v59, v7\n\t"
        "v_cndmask_b32_e64 v0, v0, v4, s[38:39]\n\t"
        "v_cndmask_b32_e64 v1, v1, v5, s[38:39]\n\t"
        "v_permlane32_swap_b32_e32 v59, v60\n\t"
        "v_cndmask_b32_e64 v2, v2, v6, s[38:39]\n\t"
        "v_cndmask_b32_e64 v3, v3, v7, s[38:39]\n\t"
        "v_max3_f32 v59, v59, v60, v8\n\t"
        "v_max3_f32 v59, v59, v9, v10\n\t"
        "v_max_f32_e32 v60, v59, v11\n\t"
        "v_max_f32_e32 v59, v59, v11\n\t"
        "v_cndmask_b32_e64 v8, v8, v9, s[38:39]\n\t"
        "v_cndmask_b32_e64 v10, v10, v11, s[38:39]\n\t"
        "v_permlane16_swap_b32_e32 v59, v60\n\t"
        "v_max_f32_e32 v59, v59, v60\n\t"
        "v_mul_f32_e32 v59, 0xbe02940c, v59\n\t"
        "v_cndmask_b32_e64 v58, v8, v10, s[40:41]\n\t"
        "v_fmamk_f32 v0, v0, 0x3e02940c, v59\n\t"
        "v_exp_f32_e32 v0, v0\n\t"
        "v_fmamk_f32 v1, v1, 0x3e02940c, v59\n\t"
        "v_exp_f32_e32 v1, v1\n\t"
        "v_fmamk_f32 v2, v2, 0x3e02940c, v59\n\t"
        "v_exp_f32_e32 v2, v2\n\t"
        "v_fmamk_f32 v3, v3, 0x3e02940c, v59\n\t"
        "v_exp_f32_e32 v3, v3\n\t"
        "v_fmamk_f32 v58, v58, 0x3e02940c, v59\n\t"
        "v_cvt_pk_bf16_f32 v12, v0, v1\n\t"
        "v_cvt_pk_bf16_f32 v13, v2, v3\n\t"
        "v_exp_f32_e32 v58, v58\n\t"
        "s_waitcnt lgkmcnt(0)\n\t"
        "v_mov_b32_dpp v14, v12 row_ror:8 row_mask:0xf bank_mask:0xf\n\t"
        "v_mov_b32_dpp v15, v13 row_ror:8 row_mask:0xf bank_mask:0xf\n\t"
        "v_mov_b32_dpp v59, v58 row_ror:8 row_mask:0xf bank_mask:0xf\n\t"
        "v_cvt_pk_bf16_f32 v59, v58, v59\n\t"
        "v_mfma_f32_16x16x32_bf16 v[20:23], v[16:19], v[12:15], 0\n\t"
        "v_cndmask_b32_e64 v56, v59, 0, s[40:41]\n\t"
        "v_cndmask_b32_e64 v57, 0, v59, s[40:41]\n\t"
        "v_mfma_f32_16x16x32_bf16 v[4:7], v[48:51], v[12:15], 0\n\t"
        "s_waitcnt vmcnt(1)\n\t"
        "v_max3_u32 v111, v104, v105, v106\n\t"
        "v_mfma_f32_16x16x32_bf16 v[8:11], v[52:55], v[12:15], 0\n\t"
        "v_max_u32_e32 v111, v111, v107\n\t"
        "v_mfma_f32_16x16x16_bf16 v[20:23], v[16:17], v[56:57], v[20:23]\n\t"
        "v_cmp_eq_u32_e32 vcc, -1, v111\n\t"
        "v_mfma_f32_16x16x16_bf16 v[4:7], v[104:105], v[56:57], v[4:7]\n\t"
        "s_nop 1\n\t"
        "v_mfma_f32_16x16x16_bf16 v[8:11], v[106:107], v[56:57], v[8:11]\n\t"
        "s_cbranch_vccnz LA_vmfail%=\n\t"
        "LA_vmok%=:\n\t"
        "s_nop 1\n\t"
        "v_rcp_f32_e32 v59, v20\n\t"
        "s_nop 0\n\t"
        "v_pk_mul_f32 v[4:5], v[4:5], v[58:59] op_sel:[0,1] op_sel_hi:[1,1]\n\t"
        "v_cvt_pk_bf16_f32 v0, v4, v5\n\t"
        "v_pk_mul_f32 v[6:7], v[6:7], v[58:59] op_sel:[0,1] op_sel_hi:[1,1]\n\t"
        "v_cvt_pk_bf16_f32 v1, v6, v7\n\t"
        "v_pk_mul_f32 v[8:9], v[8:9], v[58:59] op_sel:[0,1] op_sel_hi:[1,1]\n\t"
        "v_cvt_pk_bf16_f32 v2, v8, v9\n\t"
        "v_pk_mul_f32 v[10:11], v[10:11], v[58:59] op_sel:[0,1] op_sel_hi:[1,1]\n\t"
        "v_cvt_pk_bf16_f32 v3, v10, v11\n\t"
        "buffer_store_dwordx4 v[0:3], v121, s[8:11], s26 offen sc1\n\t"
        "LA_x%=:\n\t"
        "s_memrealtime s[52:53]\n\t"
        "ds_read_b128 v[16:19], v109 offset:8192\n\t"
        "ds_read_b128 v[20:23], v109 offset:9216\n\t"
        "ds_read_b128 v[24:27], v109 offset:10240\n\t"
        "ds_read_b128 v[28:31], v109 offset:11264\n\t"
        "ds_read_b128 v[56:59], v124 offset:0\n\t"
        "ds_read_b128 v[60:63], v124 offset:4096\n\t"
        "ds_read_b128 v[12:15], v124 offset:1024\n\t"
        "v_xor_b32_e32 v126, 0x800, v108\n\t"
        "s_add_u32 s48, s24, 1\n\t"
        "s_min_u32 s48, s48, 63\n\t"
        "buffer_load_dwordx2 v[108:109], v125, s[8:11], s26 offen sc1\n\t"
        "s_lshl_b32 s35, s44, 10\n\t"
        "s_lshr_b32 s34, s48, 1\n\t"
        "s_add_u32 s34, s34, 1\n\t"
        "s_min_u32 s34, s34, 31\n\t"
        "s_cmp_gt_u32 s54, s34\n\t"
        "s_cselect_b32 s35, 0xf0000000, s35\n\t"
        "v_lshlrev_b32_e32 v111, 2, v122\n\t"
        "v_and_b32_e32 v111, 0x3c0, v111\n\t"
        "buffer_load_dword v111, v111, s[12:15], s35 offen offset:0 sc1\n\t"
        "s_waitcnt lgkmcnt(6)\n\t"
        "v_mfma_f32_16x16x32_bf16 v[8:11], v[32:35], v[16:19], 0\n\t"
        "ds_read_b128 v[32:35], v124 offset:5120\n\t"
        "s_waitcnt lgkmcnt(6)\n\t"
        "v_mfma_f32_16x16x32_bf16 v[8:11], v[36:39], v[20:23], v[8:11]\n\t"
        "ds_read_b128 v[36:39], v124 offset:2048\n\t"
        "s_waitcnt lgkmcnt(6)\n\t"
        "v_mfma_f32_16x16x32_bf16 v[8:11], v[40:43], v[24:27], v[8:11]\n\t"
        "ds_read_b128 v[40:43], v124 offset:6144\n\t"
        "s_waitcnt lgkmcnt(6)\n\t"
        "v_mfma_f32_16x16x32_bf16 v[8:11], v[44:47], v[28:31], v[8:11]\n\t"
        "ds_read_b128 v[44:47], v124 offset:3072\n\t"
        "s_add_u32 s35, s24, 1\n\t"
        "s_waitcnt lgkmcnt(6)\n\t"
        "v_mfma_f32_16x16x32_bf16 v[0:3], v[56:59], v[16:19], 0\n\t"
        "ds_read_b128 v[56:59], v124 offset:7168\n\t"
        "s_waitcnt lgkmcnt(6)\n\t"
        "v_mfma_f32_16x16x32_bf16 v[4:7], v[60:63], v[16:19], 0\n\t"
        "s_and_b32 s36, s35, 1\n\t"
        "s_mul_i32 s36, s36, 24576\n\t"
        "s_waitcnt lgkmcnt(5)\n\t"
        "v_mfma_f32_16x16x32_bf16 v[0:3], v[12:15], v[20:23], v[0:3]\n\t"
        "s_add_u32 s36, s36, s55\n\t"
        "s_lshl_b32 s37, s48, 18\n\t"
        "s_waitcnt lgkmcnt(4)\n\t"
        "v_mfma_f32_16x16x32_bf16 v[4:7], v[32:35], v[20:23], v[4:7]\n\t"
        "v_xor_b32_e32 v32, 0x200, v120\n\t"
        "buffer_load_dwordx4 v[12:15], v32, s[8:11], s26 offen sc1\n\t"
        "s_waitcnt lgkmcnt(3)\n\t"
        "v_mfma_f32_16x16x32_bf16 v[0:3], v[36:39], v[24:27], v[0:3]\n\t"
        "s_lshl_b32 s34, s43, 11\n\t"
        "s_add_u32 s37, s37, s34\n\t"
        "s_waitcnt lgkmcnt(2)\n\t"
        "v_mfma_f32_16x16x32_bf16 v[4:7], v[40:43], v[24:27], v[4:7]\n\t"
        "v_cvt_pk_bf16_f32 v8, 0, v8\n\t"
        "v_cvt_pk_bf16_f32 v9, 0, v9\n\t"
        "v_cvt_pk_bf16_f32 v10, 0, v10\n\t"
        "v_cvt_pk_bf16_f32 v11, 0, v11\n\t"
        "s_waitcnt lgkmcnt(1)\n\t"
        "v_mfma_f32_16x16x32_bf16 v[0:3], v[44:47], v[28:31], v[0:3]\n\t"
        "v_mul_f32_e32 v8, 0x3db504f3, v8\n\t"
        "v_mul_f32_e32 v9, 0x3db504f3, v9\n\t"
        "v_mul_f32_e32 v10, 0x3db504f3, v10\n\t"
        "v_mul_f32_e32 v11, 0x3db504f3, v11\n\t"
        "s_waitcnt lgkmcnt(0)\n\t"
        "v_mfma_f32_16x16x32_bf16 v[4:7], v[56:59], v[28:31], v[4:7]\n\t"
        "v_cvt_pk_bf16_f32 v8, 0, v8\n\t"
        "v_cvt_pk_bf16_f32 v9, 0, v9\n\t"
        "v_cvt_pk_bf16_f32 v10, 0, v10\n\t"
        "v_cvt_pk_bf16_f32 v11, 0, v11\n\t"
        "ds_read_b128 v[56:59], v126 offset:16384\n\t"
        "ds_read_b128 v[60:63], v126 offset:17408\n\t"
        "v_mov_b64_e32 v[36:37], -1\n\t"
        "v_mov_b64_e32 v[38:39], -1\n\t"
        "v_mov_b32_e32 v40, 0xce6e6b28\n\t"
        "v_mov_b32_e32 v46, 0x3db504f3\n\t"
        "v_mov_b32_e32 v47, 0x3fb8aa3b\n\t"
        "v_cvt_pk_bf16_f32 v0, 0, v0\n\t"
        "v_cvt_pk_bf16_f32 v1, 0, v1\n\t"
        "v_cvt_pk_bf16_f32 v2, 0, v2\n\t"
        "v_cvt_pk_bf16_f32 v3, 0, v3\n\t"
        "v_cvt_pk_bf16_f32 v4, 0, v4\n\t"
        "v_cvt_pk_bf16_f32 v5, 0, v5\n\t"
        "v_cvt_pk_bf16_f32 v6, 0, v6\n\t"
        "v_cvt_pk_bf16_f32 v7, 0, v7\n\t"
        "v_pk_mul_f32 v[0:1], v[0:1], v[46:47] op_sel_hi:[1,0]\n\t"
        "v_pk_mul_f32 v[2:3], v[2:3], v[46:47] op_sel_hi:[1,0]\n\t"
        "v_pk_mul_f32 v[4:5], v[4:5], v[46:47] op_sel_hi:[1,0]\n\t"
        "v_pk_mul_f32 v[6:7], v[6:7], v[46:47] op_sel_hi:[1,0]\n\t"
        "s_lshr_b32 s59, s48, 1\n\t"
        "s_cmp_gt_u32 s54, s59\n\t"
        "s_cbranch_scc1 LA_kvok%=\n\t"
        "s_mov_b32 s42, 0\n\t"
        "s_waitcnt vmcnt(1)\n\t"
        "s_branch LA_kvw%=\n\t"
        "LA_kv%=:\n\t"
        "s_waitcnt vmcnt(0)\n\t"
        "LA_kvw%=:\n\t"
        "v_min_u32_dpp v111, v111, v111 quad_perm:[1,0,3,2] row_mask:0xf bank_mask:0xf\n\t"
        "s_nop 1\n\t"
        "v_min_u32_dpp v111, v111, v111 quad_perm:[2,3,0,1] row_mask:0xf bank_mask:0xf\n\t"
        "s_nop 1\n\t"
        "v_min_u32_dpp v111, v111, v111 row_ror:4 row_mask:0xf bank_mask:0xf\n\t"
        "s_nop 1\n\t"
        "v_min_u32_dpp v111, v111, v111 row_ror:8 row_mask:0xf bank_mask:0xf\n\t"
        "s_nop 0\n\t"
        "v_readfirstlane_b32 s54, v111\n\t"
        "s_nop 0\n\t"
        "s_cmp_gt_u32 s54, s59\n\t"
        "s_cbranch_scc1 LA_kvok%=\n\t"
        "s_sleep 4\n\t"
        "s_lshl_b32 s35, s44, 10\n\t"
        "v_lshlrev_b32_e32 v111, 2, v122\n\t"
        "v_and_b32_e32 v111, 0x3c0, v111\n\t"
        "buffer_load_dword v111, v111, s[12:15], s35 offen offset:0 sc1\n\t"
        "s_add_u32 s42, s42, 1\n\t"
        "s_cmp_lt_u32 s42, 0x100000\n\t"
        "s_cbranch_scc1 LA_kv%=\n\t"
        "s_movk_i32 s34, 16\n\t"
        "s_branch LA_die%=\n\t"
        "LA_kvok%=:\n\t"
        "s_add_u32 m0, s36, 0\n\t"
        "s_add_u32 s34, s37, 29548544\n\t"
        "buffer_load_dwordx4 v122, s[12:15], s34 offen sc1 lds\n\t"
        "v_cvt_pk_bf16_f32 v0, 0, v0\n\t"
        "v_cvt_pk_bf16_f32 v1, 0, v1\n\t"
        "v_cvt_pk_bf16_f32 v2, 0, v2\n\t"
        "v_cvt_pk_bf16_f32 v3, 0, v3\n\t"
        "v_cvt_pk_bf16_f32 v4, 0, v4\n\t"
        "v_cvt_pk_bf16_f32 v5, 0, v5\n\t"
        "v_cvt_pk_bf16_f32 v6, 0, v6\n\t"
        "v_cvt_pk_bf16_f32 v7, 0, v7\n\t"
        "s_add_u32 m0, s36, 1024\n\t"
        "s_add_u32 s34, s37, 29549568\n\t"
        "buffer_load_dwordx4 v122, s[12:15], s34 offen sc1 lds\n\t"
        "v_cndmask_b32_e64 v0, v40, v0, s[60:61]\n\t"
        "v_cndmask_b32_e64 v1, v40, v1, s[62:63]\n\t"
        "v_cndmask_b32_e64 v2, v40, v2, s[64:65]\n\t"
        "v_cndmask_b32_e64 v3, v40, v3, s[66:67]\n\t"
        "v_cndmask_b32_e64 v4, v40, v4, s[68:69]\n\t"
        "v_cndmask_b32_e64 v5, v40, v5, s[70:71]\n\t"
        "v_cndmask_b32_e64 v6, v40, v6, s[72:73]\n\t"
        "v_cndmask_b32_e64 v7, v40, v7, s[74:75]\n\t"
        "s_add_u32 m0, s36, 8192\n\t"
        "s_add_u32 s34, s37, 12771328\n\t"
        "buffer_load_dwordx4 v122, s[12:15], s34 offen sc1 lds\n\t"
        "v_max3_f32 v42, v0, v1, v2\n\t"
        "v_max3_f32 v42, v42, v3, v4\n\t"
        "v_max3_f32 v42, v42, v5, v6\n\t"
        "v_max3_f32 v42, v42, v7, v8\n\t"
        "v_max3_f32 v42, v42, v9, v10\n\t"
        "v_mov_b32_e32 v43, v11\n\t"
        "v_max_f32_e32 v44, v42, v43\n\t"
        "v_max_f32_e32 v42, v42, v43\n\t"
        "s_pack_ll_b32_b16 s34, s52, s24\n\t"
        "v_mov_b32_e32 v40, s34\n\t"
        "s_add_u32 s35, s26, s57\n\t"
        "v_permlane16_swap_b32_e32 v42, v44\n\t"
        "v_max_f32_e32 v45, v42, v44\n\t"
        "v_max_f32_e32 v42, v42, v44\n\t"
        "s_mov_b64 exec, 1\n\t"
        "buffer_store_dword v40, off, s[8:11], s35 sc1\n\t"
        "s_mov_b64 exec, -1\n\t"
        "v_permlane32_swap_b32_e32 v42, v45\n\t"
        "v_max_f32_e32 v42, v42, v45\n\t"
        "v_mul_f32_e32 v42, 0xbfb8aa3b, v42\n\t"
        "v_pk_fma_f32 v[0:1], v[0:1], v[46:47], v[42:43] op_sel:[0,1,0] op_sel_hi:[1,1,0]\n\t"
        "s_add_u32 m0, s36, 9216\n\t"
        "s_add_u32 s34, s37, 12772352\n\t"
        "buffer_load_dwordx4 v122, s[12:15], s34 offen sc1 lds\n\t"
        "v_exp_f32_e32 v0, v0\n\t"
        "v_exp_f32_e32 v1, v1\n\t"
        "v_pk_fma_f32 v[2:3], v[2:3], v[46:47], v[42:43] op_sel:[0,1,0] op_sel_hi:[1,1,0]\n\t"
        "v_exp_f32_e32 v2, v2\n\t"
        "v_exp_f32_e32 v3, v3\n\t"
        "v_pk_fma_f32 v[4:5], v[4:5], v[46:47], v[42:43] op_sel:[0,1,0] op_sel_hi:[1,1,0]\n\t"
        "v_exp_f32_e32 v4, v4\n\t"
        "s_add_u32 m0, s36, 16384\n\t"
        "s_add_u32 s34, s37, 46325760\n\t"
        "buffer_load_dwordx4 v122, s[12:15], s34 offen sc1 lds\n\t"
        "v_exp_f32_e32 v5, v5\n\t"
        "v_pk_fma_f32 v[6:7], v[6:7], v[46:47], v[42:43] op_sel:[0,1,0] op_sel_hi:[1,1,0]\n\t"
        "v_exp_f32_e32 v6, v6\n\t"
        "v_exp_f32_e32 v7, v7\n\t"
        "v_pk_fma_f32 v[8:9], v[8:9], v[46:47], v[42:43] op_sel:[0,1,0] op_sel_hi:[1,1,0]\n\t"
        "v_exp_f32_e32 v8, v8\n\t"
        "v_exp_f32_e32 v9, v9\n\t"
        "s_add_u32 m0, s36, 17408\n\t"
        "s_add_u32 s34, s37, 46326784\n\t"
        "buffer_load_dwordx4 v122, s[12:15], s34 offen sc1 lds\n\t"
        "v_pk_fma_f32 v[10:11], v[10:11], v[46:47], v[42:43] op_sel:[0,1,0] op_sel_hi:[1,1,0]\n\t"
        "v_exp_f32_e32 v10, v10\n\t"
        "v_exp_f32_e32 v11, v11\n\t"
        "v_pk_add_f32 v[42:43], v[0:1], v[2:3]\n\t"
        "v_cndmask_b32_e64 v8, v8, 0, s[40:41]\n\t"
        "v_cndmask_b32_e64 v9, v9, 0, s[40:41]\n\t"
        "v_cndmask_b32_e64 v10, v10, 0, s[40:41]\n\t"
        "buffer_store_dwordx4 v[36:39], v121, s[8:11], s28 offen sc1\n\t"
        "v_cndmask_b32_e64 v11, v11, 0, s[40:41]\n\t"
        "v_pk_add_f32 v[42:43], v[42:43], v[4:5]\n\t"
        "v_pk_add_f32 v[42:43], v[42:43], v[6:7]\n\t"
        "v_pk_add_f32 v[42:43], v[42:43], v[8:9]\n\t"
        "v_pk_add_f32 v[42:43], v[42:43], v[10:11]\n\t"
        "v_add_f32_e32 v44, v42, v43\n\t"
        "v_add_f32_e32 v42, v42, v43\n\t"
        "s_lshl_b32 s34, s24, 18\n\t"
        "s_add_u32 s34, s34, 63102976\n\t"
        "v_permlane16_swap_b32_e32 v42, v44\n\t"
        "v_add_f32_e32 v45, v42, v44\n\t"
        "v_add_f32_e32 v42, v42, v44\n\t"
        "s_add_u32 s35, s24, 1\n\t"
        "v_mov_b32_e32 v126, s35\n\t"
        "v_permlane32_swap_b32_e32 v42, v45\n\t"
        "v_add_f32_e32 v42, v42, v45\n\t"
        "v_rcp_f32_e32 v42, v42\n\t"
        "s_waitcnt vmcnt(8) lgkmcnt(0)\n\t"
        "v_max3_u32 v45, v12, v13, v14\n\t"
        "v_max3_u32 v45, v45, v15, v104\n\t"
        "v_max3_u32 v45, v45, v105, v106\n\t"
        "v_max_u32_e32 v45, v45, v107\n\t"
        "v_cmp_eq_u32_e32 vcc, -1, v45\n\t"
        "s_nop 0\n\t"
        "s_cbranch_vccnz LA_v2fail%=\n\t"
        "LA_v2ok%=:\n\t"
        "v_and_b32_e32 v45, 0xff, v109\n\t"
        "v_cmp_ne_u32_e32 vcc, s24, v45\n\t"
        "s_nop 0\n\t"
        "s_cbranch_vccnz LA_stre%=\n\t"
        "LA_stok%=:\n\t"
        "v_pk_mul_f32 v[0:1], v[0:1], v[42:43] op_sel_hi:[1,0]\n\t"
        "v_subrev_u32_e32 v108, s25, v108\n\t"
        "v_min_u32_dpp v111, v111, v111 quad_perm:[1,0,3,2] row_mask:0xf bank_mask:0xf\n\t"
        "v_cvt_pk_bf16_f32 v32, v0, v1\n\t"
        "v_pk_mul_f32 v[2:3], v[2:3], v[42:43] op_sel_hi:[1,0]\n\t"
        "v_cvt_pk_bf16_f32 v33, v2, v3\n\t"
        "v_max_i32_dpp v108, v108, v108 quad_perm:[1,0,3,2] row_mask:0xf bank_mask:0xf\n\t"
        "v_min_u32_dpp v111, v111, v111 quad_perm:[2,3,0,1] row_mask:0xf bank_mask:0xf\n\t"
        "v_pk_mul_f32 v[4:5], v[4:5], v[42:43] op_sel_hi:[1,0]\n\t"
        "v_cvt_pk_bf16_f32 v34, v4, v5\n\t"
        "v_pk_mul_f32 v[6:7], v[6:7], v[42:43] op_sel_hi:[1,0]\n\t"
        "v_max_i32_dpp v108, v108, v108 quad_perm:[2,3,0,1] row_mask:0xf bank_mask:0xf\n\t"
        "v_min_u32_dpp v111, v111, v111 row_ror:4 row_mask:0xf bank_mask:0xf\n\t"
        "v_cvt_pk_bf16_f32 v35, v6, v7\n\t"
        "v_pk_mul_f32 v[8:9], v[8:9], v[42:43] op_sel_hi:[1,0]\n\t"
        "v_cvt_pk_bf16_f32 v36, v8, v9\n\t"
        "v_max_i32_dpp v108, v108, v108 row_ror:4 row_mask:0xf bank_mask:0xf\n\t"
        "v_min_u32_dpp v111, v111, v111 row_ror:8 row_mask:0xf bank_mask:0xf\n\t"
        "v_pk_mul_f32 v[10:11], v[10:11], v[42:43] op_sel_hi:[1,0]\n\t"
        "v_cvt_pk_bf16_f32 v37, v10, v11\n\t"
        "v_mfma_f32_16x16x32_bf16 v[16:19], v[48:51], v[32:35], 0\n\t"
        "v_max_i32_dpp v108, v108, v108 row_ror:8 row_mask:0xf bank_mask:0xf\n\t"
        "v_mfma_f32_16x16x32_bf16 v[20:23], v[52:55], v[32:35], 0\n\t"
        "v_mfma_f32_16x16x32_bf16 v[24:27], v[56:59], v[32:35], 0\n\t"
        "v_mfma_f32_16x16x32_bf16 v[28:31], v[60:63], v[32:35], 0\n\t"
        "v_max_i32_dpp v108, v108, v108 row_bcast:15 row_mask:0xa bank_mask:0xf\n\t"
        "v_xor_b32_e32 v45, 0x400, v123\n\t"
        "v_add_u32_e32 v124, s56, v124\n\t"
        "v_mfma_f32_16x16x16_bf16 v[16:19], v[104:105], v[36:37], v[16:19]\n\t"
        "v_max_i32_dpp v108, v108, v108 row_bcast:31 row_mask:0xc bank_mask:0xf\n\t"
        "v_mfma_f32_16x16x16_bf16 v[20:23], v[106:107], v[36:37], v[20:23]\n\t"
        "v_mfma_f32_16x16x16_bf16 v[24:27], v[12:13], v[36:37], v[24:27]\n\t"
        "v_mfma_f32_16x16x16_bf16 v[28:31], v[14:15], v[36:37], v[28:31]\n\t"
        "ds_read_b128 v[48:51], v117 offset:4096\n\t"
        "ds_read_b128 v[52:55], v117 offset:5120\n\t"
        "ds_read_b128 v[56:59], v117 offset:6144\n\t"
        "ds_read_b128 v[60:63], v117 offset:7168\n\t"
        "s_sub_u32 s56, 0, s56\n\t"
        "v_cvt_pk_bf16_f32 v0, v16, v17\n\t"
        "v_cvt_pk_bf16_f32 v1, v18, v19\n\t"
        "v_cvt_pk_bf16_f32 v2, v20, v21\n\t"
        "v_cvt_pk_bf16_f32 v3, v22, v23\n\t"
        "buffer_store_dwordx4 v[0:3], v123, s[12:15], s34 offen sc1\n\t"
        "v_cvt_pk_bf16_f32 v4, v24, v25\n\t"
        "v_cvt_pk_bf16_f32 v5, v26, v27\n\t"
        "v_cvt_pk_bf16_f32 v6, v28, v29\n\t"
        "v_cvt_pk_bf16_f32 v7, v30, v31\n\t"
        "buffer_store_dwordx4 v[4:7], v45, s[12:15], s34 offen sc1\n\t"
        "s_cmp_eq_u32 s24, 63\n\t"
        "s_cbranch_scc1 LA_last%=\n\t"
        "ds_read_b128 v[32:35], v117 offset:0\n\t"
        "ds_read_b128 v[36:39], v117 offset:1024\n\t"
        "ds_read_b128 v[40:43], v117 offset:2048\n\t"
        "ds_read_b128 v[44:47], v117 offset:3072\n\t"
        "v_mov_b64_e32 v[14:15], 0\n\t"
        "v_mov_b64_e32 v[12:13], 0\n\t"
        "v_mov_b64_e32 v[10:11], 0\n\t"
        "v_mov_b64_e32 v[8:9], 0\n\t"
        "v_mov_b64_e32 v[6:7], 0\n\t"
        "v_mov_b64_e32 v[4:5], 0\n\t"
        "v_mov_b64_e32 v[2:3], 0\n\t"
        "v_mov_b64_e32 v[0:1], 0\n\t"
        "v_readfirstlane_b32 s34, v111\n\t"
        "s_nop 0\n\t"
        "s_max_u32 s54, s54, s34\n\t"
        "v_readfirstlane_b32 s35, v109\n\t"
        "v_readlane_b32 s36, v108, 63\n\t"
        "s_bfe_u32 s29, s35, 0xc0008\n\t"
        "s_lshr_b32 s30, s35, 20\n\t"
        "s_add_u32 s35, s31, s50\n\t"
        "s_mul_i32 s37, s50, 3\n\t"
        "s_min_u32 s35, s35, s37\n\t"
        "s_add_i32 s34, s36, s51\n\t"
        "s_cmp_gt_i32 s34, 0\n\t"
        "s_cselect_b32 s31, s35, 0\n\t"
        "s_cselect_b32 s34, s34, 0\n\t"
        "s_add_u32 s34, s34, s31\n\t"
        "s_add_u32 s37, s25, s34\n\t"
        "s_add_u32 s45, s37, s29\n\t"
        "s_add_u32 s25, s45, s30\n\t"
        "s_add_u32 s24, s24, 1\n\t"
        "s_mov_b32 s28, s26\n\t"
        "s_mov_b32 s26, s27\n\t"
        "s_add_u32 s27, s27, 0x80000\n\t"
        "s_and_b32 s27, s27, 0x1fffff\n\t"
        "s_branch LA_loop%=\n\t"
        "LA_last%=:\n\t"
        "s_waitcnt vmcnt(0)\n\t"
        "s_barrier\n\t"
        "buffer_store_dword v126, v127, s[12:15], 0 offen sc1\n\t"
        "s_endpgm\n\t"
        "LA_skip%=:\n\t"
        "buffer_store_dword v126, v127, s[12:15], 0 offen sc1\n\t"
        "ds_read_b64_tr_b16 v[32:33], v119 offset:2112\n\t"
        "ds_read_b64_tr_b16 v[34:35], v119 offset:2144\n\t"
        "ds_read_b64_tr_b16 v[36:37], v119 offset:2368\n\t"
        "ds_read_b64_tr_b16 v[38:39], v119 offset:2400\n\t"
        "ds_read_b64_tr_b16 v[40:41], v119 offset:2624\n\t"
        "ds_read_b64_tr_b16 v[42:43], v119 offset:2656\n\t"
        "ds_read_b64_tr_b16 v[44:45], v119 offset:2880\n\t"
        "ds_read_b64_tr_b16 v[46:47], v119 offset:2912\n\t"
        "ds_read_b128 v[48:51], v108 offset:16384\n\t"
        "ds_read_b128 v[52:55], v108 offset:17408\n\t"
        "s_waitcnt lgkmcnt(0)\n\t"
        "s_branch LA_x%=\n\t"
        "LA_stre%=:\n\t"
        "s_mov_b32 s42, 0\n\t"
        "LA_str2%=:\n\t"
        "buffer_load_dwordx2 v[108:109], v125, s[8:11], s26 offen sc1\n\t"
        "s_waitcnt vmcnt(0)\n\t"
        "v_and_b32_e32 v45, 0xff, v109\n\t"
        "v_cmp_ne_u32_e32 vcc, s24, v45\n\t"
        "s_nop 0\n\t"
        "s_cbranch_vccz LA_stok%=\n\t"
        "s_add_u32 s42, s42, 1\n\t"
        "s_cmp_lt_u32 s42, 0x100000\n\t"
        "s_cbranch_scc1 LA_str2%=\n\t"
        "s_movk_i32 s34, 5\n\t"
        "s_branch LA_die%=\n\t"
        "LA_v2fail%=:\n\t"
        "s_mov_b32 s42, 0\n\t"
        "LA_v2re%=:\n\t"
        "v_xor_b32_e32 v45, 0x200, v120\n\t"
        "buffer_load_dwordx4 v[12:15], v45, s[8:11], s26 offen sc1\n\t"
        "buffer_load_dwordx4 v[104:107], v120, s[8:11], s26 offen sc1\n\t"
        "s_waitcnt vmcnt(0)\n\t"
        "v_max3_u32 v45, v12, v13, v14\n\t"
        "v_max3_u32 v45, v45, v15, v104\n\t"
        "v_max3_u32 v45, v45, v105, v106\n\t"
        "v_max_u32_e32 v45, v45, v107\n\t"
        "v_cmp_eq_u32_e32 vcc, -1, v45\n\t"
        "s_nop 0\n\t"
        "s_cbranch_vccz LA_v2ok%=\n\t"
        "s_add_u32 s42, s42, 1\n\t"
        "s_cmp_lt_u32 s42, 0x100000\n\t"
        "s_cbranch_scc1 LA_v2re%=\n\t"
        "s_movk_i32 s34, 6\n\t"
        "s_branch LA_die%=\n\t"
        "LA_rdfail%=:\n\t"
        "s_or_b32 s47, s47, 1\n\t"
        "buffer_load_dwordx4 v[16:19], v112, s[8:11], s26 offen offset:0 sc1\n\t"
        "buffer_load_dwordx4 v[20:23], v112, s[8:11], s26 offen offset:1024 sc1\n\t"
        "buffer_load_dwordx4 v[24:27], v112, s[8:11], s26 offen offset:2048 sc1\n\t"
        "buffer_load_dwordx4 v[28:31], v112, s[8:11], s26 offen offset:3072 sc1\n\t"
        "s_nop 7\n\t"
        "v_mov_b64_e32 v[0:1], 0\n\t"
        "v_mov_b64_e32 v[2:3], 0\n\t"
        "v_mov_b64_e32 v[4:5], 0\n\t"
        "v_mov_b64_e32 v[6:7], 0\n\t"
        "v_mov_b64_e32 v[8:9], 0\n\t"
        "v_mov_b64_e32 v[10:11], 0\n\t"
        "v_mov_b64_e32 v[12:13], 0\n\t"
        "v_mov_b64_e32 v[14:15], 0\n\t"
        "s_branch LA_rdre%=w\n\t"
        "LA_rdre%=:\n\t"
        "buffer_load_dwordx4 v[16:19], v112, s[8:11], s26 offen offset:0 sc1\n\t"
        "buffer_load_dwordx4 v[20:23], v112, s[8:11], s26 offen offset:1024 sc1\n\t"
        "buffer_load_dwordx4 v[24:27], v112, s[8:11], s26 offen offset:2048 sc1\n\t"
        "buffer_load_dwordx4 v[28:31], v112, s[8:11], s26 offen offset:3072 sc1\n\t"
        "LA_rdre%=w:\n\t"
        "s_waitcnt vmcnt(0) lgkmcnt(0)\n\t"
        "v_max3_u32 v111, v16, v17, v18\n\t"
        "v_max3_u32 v111, v111, v19, v20\n\t"
        "v_max3_u32 v111, v111, v21, v22\n\t"
        "v_max3_u32 v111, v111, v23, v24\n\t"
        "v_max3_u32 v111, v111, v25, v26\n\t"
        "v_max3_u32 v111, v111, v27, v28\n\t"
        "v_max3_u32 v111, v111, v29, v30\n\t"
        "v_max_u32_e32 v111, v111, v31\n\t"
        "v_cmp_eq_u32_e32 vcc, -1, v111\n\t"
        "s_nop 0\n\t"
        "s_cbranch_vccz LA_go%=\n\t"
        "s_add_u32 s42, s42, 1\n\t"
        "s_cmp_lt_u32 s42, 0x100000\n\t"
        "s_cbranch_scc1 LA_rdre%=\n\t"
        "s_movk_i32 s34, 1\n\t"
        "s_branch LA_die%=\n\t"
        "LA_vmfail%=:\n\t"
        "s_or_b32 s47, s47, 2\n\t"
        "s_mov_b32 s42, 0\n\t"
        "LA_vmre%=:\n\t"
        "buffer_load_dwordx4 v[104:107], v120, s[8:11], s26 offen sc1\n\t"
        "v_mfma_f32_16x16x32_bf16 v[4:7], v[48:51], v[12:15], 0\n\t"
        "v_mfma_f32_16x16x32_bf16 v[8:11], v[52:55], v[12:15], 0\n\t"
        "s_nop 7\n\t"
        "s_waitcnt vmcnt(0)\n\t"
        "v_mfma_f32_16x16x16_bf16 v[4:7], v[104:105], v[56:57], v[4:7]\n\t"
        "v_mfma_f32_16x16x16_bf16 v[8:11], v[106:107], v[56:57], v[8:11]\n\t"
        "v_max3_u32 v111, v104, v105, v106\n\t"
        "v_max_u32_e32 v111, v111, v107\n\t"
        "v_cmp_eq_u32_e32 vcc, -1, v111\n\t"
        "s_nop 7\n\t"
        "s_cbranch_vccz LA_vmok%=\n\t"
        "s_add_u32 s42, s42, 1\n\t"
        "s_cmp_lt_u32 s42, 0x100000\n\t"
        "s_cbranch_scc1 LA_vmre%=\n\t"
        "s_movk_i32 s34, 2\n\t"
        "s_branch LA_die%=\n\t"
        "LA_die%=:\n\t"
        "v_mov_b32_e32 v30, s34\n\t"
        "s_mov_b32 s35, 33024\n\t"
        "s_mov_b64 exec, 1\n\t"
        "buffer_store_dword v30, off, s[12:15], s35 sc1\n\t"
        "s_endpgm\n\t"
        ".Lk_end_A:\n\t"
        :: REC_S_IN, "{v96}"(g_w), "{v97}"(wb0), "{v98}"(wb1), "{v99}"(rb0), "{v100}"(rb0 ^ 32u), "{v101}"(rb0 ^ 64u), "{v102}"(rb0 ^ 96u),
           "{v112}"(a_mem), "{v113}"(l_ms + w * 4096), "{v114}"(l_ms + ((w + 1) & 3) * 4096), "{v115}"(l_ms + ((w + 2) & 3) * 4096), "{v116}"(l_ms + ((w + 3) & 3) * 4096),
           "{v117}"(lb + w * 22528 + l * 16), "{v118}"(l_iw), "{v119}"(l_iq), "{v120}"(a_vm), "{v121}"(a_o), "{v122}"(h * 8192 + l * 16), "{v123}"(a_ox), "{v124}"(lb + L_KV + l * 16), "{s55}"(lb + L_KV + w * 2048),
           "{v125}"((24 + cp) * 16384 + l * 32), "{v126}"(16 * (w & 1) + n - 4 * g), "{v103}"((l & 1) ? 0xEEEEEEEEu : 0x44444444u),
           "{v127}"((l == 0 && w == 0) ? F_PROG + h * 128 : OOB)
        : "memory", "vcc", "scc", "v0", "v255", "a0", "a255", "s101");
    __builtin_unreachable();
}
__device__ __forceinline__ void rec_b(const Params& p, unsigned lb, const int wv, const int id, const int rg, const int cp) {
    const int w = wv, l = otid() & 63, n = l & 15, g = l >> 4, cs = id, rb = 32 + id * 2 + rg;
    const v4u sh = mk_srd(p.ws + ho_base(p.ws), HO_BYTES - 8192), sw = mk_srd(p.ws, W_TOTAL), s1 = mk_srd(p.Wv, 1024 * 4096 * 2), s2 = mk_srd(p.Wo, 1024 * 4096 * 2);
    const int r8 = l >> 3, pc = l & 7, pp = n >> 3, bb = (n >> 2) & 1, f = (g & 1) | (pp << 1);
    const unsigned stg = lb + LB_STG + w * LB_STGP;
    const unsigned g_v = r8 * 8192 + cs * 256 + rg * 128 + pc * 16, g_o = (l >> 2) * 2048 + cs * 64 + (l & 3) * 16;
    const unsigned wb0 = stg + r8 * 128 + (((pc >> 1) ^ (r8 >> 2)) << 5) + (pc & 1) * 16, wb1 = wb0 ^ 64u;
    const unsigned rb0 = stg + (16 * (g >> 1) + 8 * pp + 4 * (g & 1) + bb) * 128 + (f << 5) + (n & 3) * 8;
    const unsigned o_r = stg + (g >> 1) * (2048 + 128) + (g & 1) * (1024 + 32) + (bb + 4 * pp) * 64 + (n & 3) * 8;
    const unsigned a_mem = cp * 32768 + (w >> 1) * 16384 + (w & 1) * 4096 + l * 16;
    const unsigned a_vmw = w < 2 ? (16 + (cs >> 2)) * 16384 + (cs & 3) * 2048 + (2 * rg + w) * 512 + (l & 31) * 16 + (g >> 1) * 8 : OOB;
    const unsigned a_or = (8 + rg * 4 + w) * 16384 + l * 16;
    const unsigned a_mw = w * 32768 + (cs >> 4) * 16384 + ((cs >> 1) & 7) * 1024 + (cs & 1) * 512 + (l >> 5) * 256 + rg * 128 + (l & 31) * 4;
    const unsigned a_sw = (w == 0 && l < 4) ? (24 + l) * 16384 + (id * 2 + rg) * 32 : OOB;
    const bf16_t* m0p = p.mem0 + (4 * rg + ((l >> 3) & 3)) * 1024 + cs * 32 + (l >> 5) * 16 + (l & 7) * 2;
    asm volatile(
        "s_nop 4\n\t"
        "global_load_dword v109, v[108:109], off\n\t"
        "s_lshl_b32 s34, s43, 21\n\t"
        "s_add_u32 s34, s34, 0\n\t"
        "buffer_load_dwordx4 v[128:131], v96, s[16:19], s34 offen nt\n\t"
        "s_add_u32 s34, s34, 0x10000\n\t"
        "buffer_load_dwordx4 v[132:135], v96, s[16:19], s34 offen nt\n\t"
        "s_add_u32 s34, s34, 0x10000\n\t"
        "buffer_load_dwordx4 v[136:139], v96, s[16:19], s34 offen nt\n\t"
        "s_add_u32 s34, s34, 0x10000\n\t"
        "buffer_load_dwordx4 v[140:143], v96, s[16:19], s34 offen nt\n\t"
        "s_add_u32 s34, s34, 0x10000\n\t"
        "buffer_load_dwordx4 v[144:147], v96, s[16:19], s34 offen nt\n\t"
        "s_add_u32 s34, s34, 0x10000\n\t"
        "buffer_load_dwordx4 v[148:151], v96, s[16:19], s34 offen nt\n\t"
        "s_add_u32 s34, s34, 0x10000\n\t"
        "buffer_load_dwordx4 v[152:155], v96, s[16:19], s34 offen nt\n\t"
        "s_add_u32 s34, s34, 0x10000\n\t"
        "buffer_load_dwordx4 v[156:159], v96, s[16:19], s34 offen nt\n\t"
        "s_lshl_b32 s34, s43, 21\n\t"
        "s_add_u32 s34, s34, 524288\n\t"
        "buffer_load_dwordx4 v[160:163], v96, s[16:19], s34 offen nt\n\t"
        "s_add_u32 s34, s34, 0x10000\n\t"
        "buffer_load_dwordx4 v[164:167], v96, s[16:19], s34 offen nt\n\t"
        "s_add_u32 s34, s34, 0x10000\n\t"
        "buffer_load_dwordx4 v[168:171], v96, s[16:19], s34 offen nt\n\t"
        "s_add_u32 s34, s34, 0x10000\n\t"
        "buffer_load_dwordx4 v[172:175], v96, s[16:19], s34 offen nt\n\t"
        "s_add_u32 s34, s34, 0x10000\n\t"
        "buffer_load_dwordx4 v[176:179], v96, s[16:19], s34 offen nt\n\t"
        "s_add_u32 s34, s34, 0x10000\n\t"
        "buffer_load_dwordx4 v[180:183], v96, s[16:19], s34 offen nt\n\t"
        "s_add_u32 s34, s34, 0x10000\n\t"
        "buffer_load_dwordx4 v[184:187], v96, s[16:19], s34 offen nt\n\t"
        "s_add_u32 s34, s34, 0x10000\n\t"
        "buffer_load_dwordx4 v[188:191], v96, s[16:19], s34 offen nt\n\t"
        "s_lshl_b32 s34, s43, 21\n\t"
        "s_add_u32 s34, s34, 1048576\n\t"
        "buffer_load_dwordx4 v[192:195], v96, s[16:19], s34 offen nt\n\t"
        "s_add_u32 s34, s34, 0x10000\n\t"
        "buffer_load_dwordx4 v[196:199], v96, s[16:19], s34 offen nt\n\t"
        "s_add_u32 s34, s34, 0x10000\n\t"
        "buffer_load_dwordx4 v[200:203], v96, s[16:19], s34 offen nt\n\t"
        "s_add_u32 s34, s34, 0x10000\n\t"
        "buffer_load_dwordx4 v[204:207], v96, s[16:19], s34 offen nt\n\t"
        "s_add_u32 s34, s34, 0x10000\n\t"
        "buffer_load_dwordx4 v[208:211], v96, s[16:19], s34 offen nt\n\t"
        "s_add_u32 s34, s34, 0x10000\n\t"
        "buffer_load_dwordx4 v[212:215], v96, s[16:19], s34 offen nt\n\t"
        "s_add_u32 s34, s34, 0x10000\n\t"
        "buffer_load_dwordx4 v[216:219], v96, s[16:19], s34 offen nt\n\t"
        "s_add_u32 s34, s34, 0x10000\n\t"
        "buffer_load_dwordx4 v[220:223], v96, s[16:19], s34 offen nt\n\t"
        "s_waitcnt vmcnt(16)\n\t"
        "ds_write_b128 v98, v[128:131] offset:0\n\t"
        "ds_write_b128 v99, v[132:135] offset:1024\n\t"
        "ds_write_b128 v98, v[136:139] offset:2048\n\t"
        "ds_write_b128 v99, v[140:143] offset:3072\n\t"
        "ds_write_b128 v98, v[144:147] offset:4096\n\t"
        "ds_write_b128 v99, v[148:151] offset:5120\n\t"
        "ds_write_b128 v98, v[152:155] offset:6144\n\t"
        "ds_write_b128 v99, v[156:159] offset:7168\n\t"
        "ds_read_b64_tr_b16 v[128:129], v100 offset:0\n\t"
        "ds_read_b64_tr_b16 v[130:131], v100 offset:256\n\t"
        "ds_read_b64_tr_b16 v[132:133], v100 offset:4096\n\t"
        "ds_read_b64_tr_b16 v[134:135], v100 offset:4352\n\t"
        "s_waitcnt lgkmcnt(8)\n\t"
        "ds_read_b64_tr_b16 v[136:137], v101 offset:0\n\t"
        "ds_read_b64_tr_b16 v[138:139], v101 offset:256\n\t"
        "ds_read_b64_tr_b16 v[140:141], v101 offset:4096\n\t"
        "ds_read_b64_tr_b16 v[142:143], v101 offset:4352\n\t"
        "s_waitcnt lgkmcnt(8)\n\t"
        "ds_read_b64_tr_b16 v[144:145], v102 offset:0\n\t"
        "ds_read_b64_tr_b16 v[146:147], v102 offset:256\n\t"
        "ds_read_b64_tr_b16 v[148:149], v102 offset:4096\n\t"
        "ds_read_b64_tr_b16 v[150:151], v102 offset:4352\n\t"
        "s_waitcnt lgkmcnt(8)\n\t"
        "ds_read_b64_tr_b16 v[152:153], v103 offset:0\n\t"
        "ds_read_b64_tr_b16 v[154:155], v103 offset:256\n\t"
        "ds_read_b64_tr_b16 v[156:157], v103 offset:4096\n\t"
        "ds_read_b64_tr_b16 v[158:159], v103 offset:4352\n\t"
        "s_lshl_b32 s34, s43, 21\n\t"
        "s_add_u32 s34, s34, 1572864\n\t"
        "buffer_load_dwordx4 v[224:227], v96, s[16:19], s34 offen nt\n\t"
        "s_add_u32 s34, s34, 0x10000\n\t"
        "buffer_load_dwordx4 v[228:231], v96, s[16:19], s34 offen nt\n\t"
        "s_add_u32 s34, s34, 0x10000\n\t"
        "buffer_load_dwordx4 v[232:235], v96, s[16:19], s34 offen nt\n\t"
        "s_add_u32 s34, s34, 0x10000\n\t"
        "buffer_load_dwordx4 v[236:239], v96, s[16:19], s34 offen nt\n\t"
        "s_add_u32 s34, s34, 0x10000\n\t"
        "buffer_load_dwordx4 v[240:243], v96, s[16:19], s34 offen nt\n\t"
        "s_add_u32 s34, s34, 0x10000\n\t"
        "buffer_load_dwordx4 v[244:247], v96, s[16:19], s34 offen nt\n\t"
        "s_add_u32 s34, s34, 0x10000\n\t"
        "buffer_load_dwordx4 v[248:251], v96, s[16:19], s34 offen nt\n\t"
        "s_add_u32 s34, s34, 0x10000\n\t"
        "buffer_load_dwordx4 v[252:255], v96, s[16:19], s34 offen nt\n\t"
        "s_waitcnt lgkmcnt(0)\n\t"
        "s_waitcnt vmcnt(16)\n\t"
        "ds_write_b128 v98, v[160:163] offset:0\n\t"
        "ds_write_b128 v99, v[164:167] offset:1024\n\t"
        "ds_write_b128 v98, v[168:171] offset:2048\n\t"
        "ds_write_b128 v99, v[172:175] offset:3072\n\t"
        "ds_write_b128 v98, v[176:179] offset:4096\n\t"
        "ds_write_b128 v99, v[180:183] offset:5120\n\t"
        "ds_write_b128 v98, v[184:187] offset:6144\n\t"
        "ds_write_b128 v99, v[188:191] offset:7168\n\t"
        "ds_read_b64_tr_b16 v[160:161], v100 offset:0\n\t"
        "ds_read_b64_tr_b16 v[162:163], v100 offset:256\n\t"
        "ds_read_b64_tr_b16 v[164:165], v100 offset:4096\n\t"
        "ds_read_b64_tr_b16 v[166:167], v100 offset:4352\n\t"
        "s_waitcnt lgkmcnt(8)\n\t"
        "ds_read_b64_tr_b16 v[168:169], v101 offset:0\n\t"
        "ds_read_b64_tr_b16 v[170:171], v101 offset:256\n\t"
        "ds_read_b64_tr_b16 v[172:173], v101 offset:4096\n\t"
        "ds_read_b64_tr_b16 v[174:175], v101 offset:4352\n\t"
        "s_waitcnt lgkmcnt(8)\n\t"
        "ds_read_b64_tr_b16 v[176:177], v102 offset:0\n\t"
        "ds_read_b64_tr_b16 v[178:179], v102 offset:256\n\t"
        "ds_read_b64_tr_b16 v[180:181], v102 offset:4096\n\t"
        "ds_read_b64_tr_b16 v[182:183], v102 offset:4352\n\t"
        "s_waitcnt lgkmcnt(8)\n\t"
        "ds_read_b64_tr_b16 v[184:185], v103 offset:0\n\t"
        "ds_read_b64_tr_b16 v[186:187], v103 offset:256\n\t"
        "ds_read_b64_tr_b16 v[188:189], v103 offset:4096\n\t"
        "ds_read_b64_tr_b16 v[190:191], v103 offset:4352\n\t"
        "s_lshl_b32 s34, s43, 21\n\t"
        "s_add_u32 s34, s34, 0\n\t"
        "buffer_load_dwordx4 a[0:3], v97, s[20:23], s34 offen\n\t"
        "s_add_u32 s34, s34, 0x8000\n\t"
        "buffer_load_dwordx4 a[4:7], v97, s[20:23], s34 offen\n\t"
        "s_add_u32 s34, s34, 0x8000\n\t"
        "buffer_load_dwordx4 a[8:11], v97, s[20:23], s34 offen\n\t"
        "s_add_u32 s34, s34, 0x8000\n\t"
        "buffer_load_dwordx4 a[12:15], v97, s[20:23], s34 offen\n\t"
        "s_add_u32 s34, s34, 0x8000\n\t"
        "buffer_load_dwordx4 a[16:19], v97, s[20:23], s34 offen\n\t"
        "s_add_u32 s34, s34, 0x8000\n\t"
        "buffer_load_dwordx4 a[20:23], v97, s[20:23], s34 offen\n\t"
        "s_add_u32 s34, s34, 0x8000\n\t"
        "buffer_load_dwordx4 a[24:27], v97, s[20:23], s34 offen\n\t"
        "s_add_u32 s34, s34, 0x8000\n\t"
        "buffer_load_dwordx4 a[28:31], v97, s[20:23], s34 offen\n\t"
        "s_waitcnt lgkmcnt(0)\n\t"
        "s_waitcnt vmcnt(16)\n\t"
        "ds_write_b128 v98, v[192:195] offset:0\n\t"
        "ds_write_b128 v99, v[196:199] offset:1024\n\t"
        "ds_write_b128 v98, v[200:203] offset:2048\n\t"
        "ds_write_b128 v99, v[204:207] offset:3072\n\t"
        "ds_write_b128 v98, v[208:211] offset:4096\n\t"
        "ds_write_b128 v99, v[212:215] offset:5120\n\t"
        "ds_write_b128 v98, v[216:219] offset:6144\n\t"
        "ds_write_b128 v99, v[220:223] offset:7168\n\t"
        "ds_read_b64_tr_b16 v[192:193], v100 offset:0\n\t"
        "ds_read_b64_tr_b16 v[194:195], v100 offset:256\n\t"
        "ds_read_b64_tr_b16 v[196:197], v100 offset:4096\n\t"
        "ds_read_b64_tr_b16 v[198:199], v100 offset:4352\n\t"
        "s_waitcnt lgkmcnt(8)\n\t"
        "ds_read_b64_tr_b16 v[200:201], v101 offset:0\n\t"
        "ds_read_b64_tr_b16 v[202:203], v101 offset:256\n\t"
        "ds_read_b64_tr_b16 v[204:205], v101 offset:4096\n\t"
        "ds_read_b64_tr_b16 v[206:207], v101 offset:4352\n\t"
        "s_waitcnt lgkmcnt(8)\n\t"
        "ds_read_b64_tr_b16 v[208:209], v102 offset:0\n\t"
        "ds_read_b64_tr_b16 v[210:211], v102 offset:256\n\t"
        "ds_read_b64_tr_b16 v[212:213], v102 offset:4096\n\t"
        "ds_read_b64_tr_b16 v[214:215], v102 offset:4352\n\t"
        "s_waitcnt lgkmcnt(8)\n\t"
        "ds_read_b64_tr_b16 v[216:217], v103 offset:0\n\t"
        "ds_read_b64_tr_b16 v[218:219], v103 offset:256\n\t"
        "ds_read_b64_tr_b16 v[220:221], v103 offset:4096\n\t"
        "ds_read_b64_tr_b16 v[222:223], v103 offset:4352\n\t"
        "s_lshl_b32 s34, s43, 21\n\t"
        "s_add_u32 s34, s34, 262144\n\t"
        "buffer_load_dwordx4 a[32:35], v97, s[20:23], s34 offen\n\t"
        "s_add_u32 s34, s34, 0x8000\n\t"
        "buffer_load_dwordx4 a[36:39], v97, s[20:23], s34 offen\n\t"
        "s_add_u32 s34, s34, 0x8000\n\t"
        "buffer_load_dwordx4 a[40:43], v97, s[20:23], s34 offen\n\t"
        "s_add_u32 s34, s34, 0x8000\n\t"
        "buffer_load_dwordx4 a[44:47], v97, s[20:23], s34 offen\n\t"
        "s_add_u32 s34, s34, 0x8000\n\t"
        "buffer_load_dwordx4 a[48:51], v97, s[20:23], s34 offen\n\t"
        "s_add_u32 s34, s34, 0x8000\n\t"
        "buffer_load_dwordx4 a[52:55], v97, s[20:23], s34 offen\n\t"
        "s_add_u32 s34, s34, 0x8000\n\t"
        "buffer_load_dwordx4 a[56:59], v97, s[20:23], s34 offen\n\t"
        "s_add_u32 s34, s34, 0x8000\n\t"
        "buffer_load_dwordx4 a[60:63], v97, s[20:23], s34 offen\n\t"
        "s_waitcnt lgkmcnt(0)\n\t"
        "s_waitcnt vmcnt(16)\n\t"
        "ds_write_b128 v98, v[224:227] offset:0\n\t"
        "ds_write_b128 v99, v[228:231] offset:1024\n\t"
        "ds_write_b128 v98, v[232:235] offset:2048\n\t"
        "ds_write_b128 v99, v[236:239] offset:3072\n\t"
        "ds_write_b128 v98, v[240:243] offset:4096\n\t"
        "ds_write_b128 v99, v[244:247] offset:5120\n\t"
        "ds_write_b128 v98, v[248:251] offset:6144\n\t"
        "ds_write_b128 v99, v[252:255] offset:7168\n\t"
        "ds_read_b64_tr_b16 v[224:225], v100 offset:0\n\t"
        "ds_read_b64_tr_b16 v[226:227], v100 offset:256\n\t"
        "ds_read_b64_tr_b16 v[228:229], v100 offset:4096\n\t"
        "ds_read_b64_tr_b16 v[230:231], v100 offset:4352\n\t"
        "s_waitcnt lgkmcnt(8)\n\t"
        "ds_read_b64_tr_b16 v[232:233], v101 offset:0\n\t"
        "ds_read_b64_tr_b16 v[234:235], v101 offset:256\n\t"
        "ds_read_b64_tr_b16 v[236:237], v101 offset:4096\n\t"
        "ds_read_b64_tr_b16 v[238:239], v101 offset:4352\n\t"
        "s_waitcnt lgkmcnt(8)\n\t"
        "ds_read_b64_tr_b16 v[240:241], v102 offset:0\n\t"
        "ds_read_b64_tr_b16 v[242:243], v102 offset:256\n\t"
        "ds_read_b64_tr_b16 v[244:245], v102 offset:4096\n\t"
        "ds_read_b64_tr_b16 v[246:247], v102 offset:4352\n\t"
        "s_waitcnt lgkmcnt(8)\n\t"
        "ds_read_b64_tr_b16 v[248:249], v103 offset:0\n\t"
        "ds_read_b64_tr_b16 v[250:251], v103 offset:256\n\t"
        "ds_read_b64_tr_b16 v[252:253], v103 offset:4096\n\t"
        "ds_read_b64_tr_b16 v[254:255], v103 offset:4352\n\t"
        "s_lshl_b32 s34, s43, 21\n\t"
        "s_add_u32 s34, s34, 524288\n\t"
        "buffer_load_dwordx4 a[64:67], v97, s[20:23], s34 offen\n\t"
        "s_add_u32 s34, s34, 0x8000\n\t"
        "buffer_load_dwordx4 a[68:71], v97, s[20:23], s34 offen\n\t"
        "s_add_u32 s34, s34, 0x8000\n\t"
        "buffer_load_dwordx4 a[72:75], v97, s[20:23], s34 offen\n\t"
        "s_add_u32 s34, s34, 0x8000\n\t"
        "buffer_load_dwordx4 a[76:79], v97, s[20:23], s34 offen\n\t"
        "s_add_u32 s34, s34, 0x8000\n\t"
        "buffer_load_dwordx4 a[80:83], v97, s[20:23], s34 offen\n\t"
        "s_add_u32 s34, s34, 0x8000\n\t"
        "buffer_load_dwordx4 a[84:87], v97, s[20:23], s34 offen\n\t"
        "s_add_u32 s34, s34, 0x8000\n\t"
        "buffer_load_dwordx4 a[88:91], v97, s[20:23], s34 offen\n\t"
        "s_add_u32 s34, s34, 0x8000\n\t"
        "buffer_load_dwordx4 a[92:95], v97, s[20:23], s34 offen\n\t"
        "s_waitcnt lgkmcnt(0)\n\t"
        "s_waitcnt vmcnt(16)\n\t"
        "ds_write_b128 v122, a[0:3] offset:0\n\t"
        "ds_write_b128 v122, a[4:7] offset:1056\n\t"
        "ds_write_b128 v122, a[8:11] offset:2176\n\t"
        "ds_write_b128 v122, a[12:15] offset:3232\n\t"
        "ds_write_b128 v122, a[16:19] offset:4352\n\t"
        "ds_write_b128 v122, a[20:23] offset:5408\n\t"
        "ds_write_b128 v122, a[24:27] offset:6528\n\t"
        "ds_write_b128 v122, a[28:31] offset:7584\n\t"
        "ds_read_b64_tr_b16 a[0:1], v123 offset:0\n\t"
        "ds_read_b64_tr_b16 a[2:3], v123 offset:128\n\t"
        "ds_read_b64_tr_b16 a[4:5], v123 offset:4352\n\t"
        "ds_read_b64_tr_b16 a[6:7], v123 offset:4480\n\t"
        "s_waitcnt lgkmcnt(8)\n\t"
        "ds_read_b64_tr_b16 a[8:9], v123 offset:32\n\t"
        "ds_read_b64_tr_b16 a[10:11], v123 offset:160\n\t"
        "ds_read_b64_tr_b16 a[12:13], v123 offset:4384\n\t"
        "ds_read_b64_tr_b16 a[14:15], v123 offset:4512\n\t"
        "s_waitcnt lgkmcnt(8)\n\t"
        "ds_read_b64_tr_b16 a[16:17], v123 offset:512\n\t"
        "ds_read_b64_tr_b16 a[18:19], v123 offset:640\n\t"
        "ds_read_b64_tr_b16 a[20:21], v123 offset:4864\n\t"
        "ds_read_b64_tr_b16 a[22:23], v123 offset:4992\n\t"
        "s_waitcnt lgkmcnt(8)\n\t"
        "ds_read_b64_tr_b16 a[24:25], v123 offset:544\n\t"
        "ds_read_b64_tr_b16 a[26:27], v123 offset:672\n\t"
        "ds_read_b64_tr_b16 a[28:29], v123 offset:4896\n\t"
        "ds_read_b64_tr_b16 a[30:31], v123 offset:5024\n\t"
        "s_waitcnt lgkmcnt(8)\n\t"
        "s_lshl_b32 s34, s43, 21\n\t"
        "s_add_u32 s34, s34, 786432\n\t"
        "buffer_load_dwordx4 a[96:99], v97, s[20:23], s34 offen\n\t"
        "s_add_u32 s34, s34, 0x8000\n\t"
        "buffer_load_dwordx4 a[100:103], v97, s[20:23], s34 offen\n\t"
        "s_add_u32 s34, s34, 0x8000\n\t"
        "buffer_load_dwordx4 a[104:107], v97, s[20:23], s34 offen\n\t"
        "s_add_u32 s34, s34, 0x8000\n\t"
        "buffer_load_dwordx4 a[108:111], v97, s[20:23], s34 offen\n\t"
        "s_add_u32 s34, s34, 0x8000\n\t"
        "buffer_load_dwordx4 a[112:115], v97, s[20:23], s34 offen\n\t"
        "s_add_u32 s34, s34, 0x8000\n\t"
        "buffer_load_dwordx4 a[116:119], v97, s[20:23], s34 offen\n\t"
        "s_add_u32 s34, s34, 0x8000\n\t"
        "buffer_load_dwordx4 a[120:123], v97, s[20:23], s34 offen\n\t"
        "s_add_u32 s34, s34, 0x8000\n\t"
        "buffer_load_dwordx4 a[124:127], v97, s[20:23], s34 offen\n\t"
        "s_waitcnt lgkmcnt(0)\n\t"
        "s_waitcnt vmcnt(16)\n\t"
        "ds_write_b128 v122, a[32:35] offset:0\n\t"
        "ds_write_b128 v122, a[36:39] offset:1056\n\t"
        "ds_write_b128 v122, a[40:43] offset:2176\n\t"
        "ds_write_b128 v122, a[44:47] offset:3232\n\t"
        "ds_write_b128 v122, a[48:51] offset:4352\n\t"
        "ds_write_b128 v122, a[52:55] offset:5408\n\t"
        "ds_write_b128 v122, a[56:59] offset:6528\n\t"
        "ds_write_b128 v122, a[60:63] offset:7584\n\t"
        "ds_read_b64_tr_b16 a[32:33], v123 offset:0\n\t"
        "ds_read_b64_tr_b16 a[34:35], v123 offset:128\n\t"
        "ds_read_b64_tr_b16 a[36:37], v123 offset:4352\n\t"
        "ds_read_b64_tr_b16 a[38:39], v123 offset:4480\n\t"
        "s_waitcnt lgkmcnt(8)\n\t"
        "ds_read_b64_tr_b16 a[40:41], v123 offset:32\n\t"
        "ds_read_b64_tr_b16 a[42:43], v123 offset:160\n\t"
        "ds_read_b64_tr_b16 a[44:45], v123 offset:4384\n\t"
        "ds_read_b64_tr_b16 a[46:47], v123 offset:4512\n\t"
        "s_waitcnt lgkmcnt(8)\n\t"
        "ds_read_b64_tr_b16 a[48:49], v123 offset:512\n\t"
        "ds_read_b64_tr_b16 a[50:51], v123 offset:640\n\t"
        "ds_read_b64_tr_b16 a[52:53], v123 offset:4864\n\t"
        "ds_read_b64_tr_b16 a[54:55], v123 offset:4992\n\t"
        "s_waitcnt lgkmcnt(8)\n\t"
        "ds_read_b64_tr_b16 a[56:57], v123 offset:544\n\t"
        "ds_read_b64_tr_b16 a[58:59], v123 offset:672\n\t"
        "ds_read_b64_tr_b16 a[60:61], v123 offset:4896\n\t"
        "ds_read_b64_tr_b16 a[62:63], v123 offset:5024\n\t"
        "s_waitcnt lgkmcnt(8)\n\t"
        "s_lshl_b32 s34, s43, 21\n\t"
        "s_add_u32 s34, s34, 1048576\n\t"
        "buffer_load_dwordx4 a[128:131], v97, s[20:23], s34 offen\n\t"
        "s_add_u32 s34, s34, 0x8000\n\t"
        "buffer_load_dwordx4 a[132:135], v97, s[20:23], s34 offen\n\t"
        "s_add_u32 s34, s34, 0x8000\n\t"
        "buffer_load_dwordx4 a[136:139], v97, s[20:23], s34 offen\n\t"
        "s_add_u32 s34, s34, 0x8000\n\t"
        "buffer_load_dwordx4 a[140:143], v97, s[20:23], s34 offen\n\t"
        "s_add_u32 s34, s34, 0x8000\n\t"
        "buffer_load_dwordx4 a[144:147], v97, s[20:23], s34 offen\n\t"
        "s_add_u32 s34, s34, 0x8000\n\t"
        "buffer_load_dwordx4 a[148:151], v97, s[20:23], s34 offen\n\t"
        "s_add_u32 s34, s34, 0x8000\n\t"
        "buffer_load_dwordx4 a[152:155], v97, s[20:23], s34 offen\n\t"
        "s_add_u32 s34, s34, 0x8000\n\t"
        "buffer_load_dwordx4 a[156:159], v97, s[20:23], s34 offen\n\t"
        "s_waitcnt lgkmcnt(0)\n\t"
        "s_waitcnt vmcnt(16)\n\t"
        "ds_write_b128 v122, a[64:67] offset:0\n\t"
        "ds_write_b128 v122, a[68:71] offset:1056\n\t"
        "ds_write_b128 v122, a[72:75] offset:2176\n\t"
        "ds_write_b128 v122, a[76:79] offset:3232\n\t"
        "ds_write_b128 v122, a[80:83] offset:4352\n\t"
        "ds_write_b128 v122, a[84:87] offset:5408\n\t"
        "ds_write_b128 v122, a[88:91] offset:6528\n\t"
        "ds_write_b128 v122, a[92:95] offset:7584\n\t"
        "ds_read_b64_tr_b16 a[64:65], v123 offset:0\n\t"
        "ds_read_b64_tr_b16 a[66:67], v123 offset:128\n\t"
        "ds_read_b64_tr_b16 a[68:69], v123 offset:4352\n\t"
        "ds_read_b64_tr_b16 a[70:71], v123 offset:4480\n\t"
        "s_waitcnt lgkmcnt(8)\n\t"
        "ds_read_b64_tr_b16 a[72:73], v123 offset:32\n\t"
        "ds_read_b64_tr_b16 a[74:75], v123 offset:160\n\t"
        "ds_read_b64_tr_b16 a[76:77], v123 offset:4384\n\t"
        "ds_read_b64_tr_b16 a[78:79], v123 offset:4512\n\t"
        "s_waitcnt lgkmcnt(8)\n\t"
        "ds_read_b64_tr_b16 a[80:81], v123 offset:512\n\t"
        "ds_read_b64_tr_b16 a[82:83], v123 offset:640\n\t"
        "ds_read_b64_tr_b16 a[84:85], v123 offset:4864\n\t"
        "ds_read_b64_tr_b16 a[86:87], v123 offset:4992\n\t"
        "s_waitcnt lgkmcnt(8)\n\t"
        "ds_read_b64_tr_b16 a[88:89], v123 offset:544\n\t"
        "ds_read_b64_tr_b16 a[90:91], v123 offset:672\n\t"
        "ds_read_b64_tr_b16 a[92:93], v123 offset:4896\n\t"
        "ds_read_b64_tr_b16 a[94:95], v123 offset:5024\n\t"
        "s_waitcnt lgkmcnt(8)\n\t"
        "s_lshl_b32 s34, s43, 21\n\t"
        "s_add_u32 s34, s34, 1310720\n\t"
        "buffer_load_dwordx4 a[160:163], v97, s[20:23], s34 offen\n\t"
        "s_add_u32 s34, s34, 0x8000\n\t"
        "buffer_load_dwordx4 a[164:167], v97, s[20:23], s34 offen\n\t"
        "s_add_u32 s34, s34, 0x8000\n\t"
        "buffer_load_dwordx4 a[168:171], v97, s[20:23], s34 offen\n\t"
        "s_add_u32 s34, s34, 0x8000\n\t"
        "buffer_load_dwordx4 a[172:175], v97, s[20:23], s34 offen\n\t"
        "s_add_u32 s34, s34, 0x8000\n\t"
        "buffer_load_dwordx4 a[176:179], v97, s[20:23], s34 offen\n\t"
        "s_add_u32 s34, s34, 0x8000\n\t"
        "buffer_load_dwordx4 a[180:183], v97, s[20:23], s34 offen\n\t"
        "s_add_u32 s34, s34, 0x8000\n\t"
        "buffer_load_dwordx4 a[184:187], v97, s[20:23], s34 offen\n\t"
        "s_add_u32 s34, s34, 0x8000\n\t"
        "buffer_load_dwordx4 a[188:191], v97, s[20:23], s34 offen\n\t"
        "s_waitcnt lgkmcnt(0)\n\t"
        "s_waitcnt vmcnt(16)\n\t"
        "ds_write_b128 v122, a[96:99] offset:0\n\t"
        "ds_write_b128 v122, a[100:103] offset:1056\n\t"
        "ds_write_b128 v122, a[104:107] offset:2176\n\t"
        "ds_write_b128 v122, a[108:111] offset:3232\n\t"
        "ds_write_b128 v122, a[112:115] offset:4352\n\t"
        "ds_write_b128 v122, a[116:119] offset:5408\n\t"
        "ds_write_b128 v122, a[120:123] offset:6528\n\t"
        "ds_write_b128 v122, a[124:127] offset:7584\n\t"
        "ds_read_b64_tr_b16 a[96:97], v123 offset:0\n\t"
        "ds_read_b64_tr_b16 a[98:99], v123 offset:128\n\t"
        "ds_read_b64_tr_b16 a[100:101], v123 offset:4352\n\t"
        "ds_read_b64_tr_b16 a[102:103], v123 offset:4480\n\t"
        "s_waitcnt lgkmcnt(8)\n\t"
        "ds_read_b64_tr_b16 a[104:105], v123 offset:32\n\t"
        "ds_read_b64_tr_b16 a[106:107], v123 offset:160\n\t"
        "ds_read_b64_tr_b16 a[108:109], v123 offset:4384\n\t"
        "ds_read_b64_tr_b16 a[110:111], v123 offset:4512\n\t"
        "s_waitcnt lgkmcnt(8)\n\t"
        "ds_read_b64_tr_b16 a[112:113], v123 offset:512\n\t"
        "ds_read_b64_tr_b16 a[114:115], v123 offset:640\n\t"
        "ds_read_b64_tr_b16 a[116:117], v123 offset:4864\n\t"
        "ds_read_b64_tr_b16 a[118:119], v123 offset:4992\n\t"
        "s_waitcnt lgkmcnt(8)\n\t"
        "ds_read_b64_tr_b16 a[120:121], v123 offset:544\n\t"
        "ds_read_b64_tr_b16 a[122:123], v123 offset:672\n\t"
        "ds_read_b64_tr_b16 a[124:125], v123 offset:4896\n\t"
        "ds_read_b64_tr_b16 a[126:127], v123 offset:5024\n\t"
        "s_waitcnt lgkmcnt(8)\n\t"
        "s_lshl_b32 s34, s43, 21\n\t"
        "s_add_u32 s34, s34, 1572864\n\t"
        "buffer_load_dwordx4 a[192:195], v97, s[20:23], s34 offen\n\t"
        "s_add_u32 s34, s34, 0x8000\n\t"
        "buffer_load_dwordx4 a[196:199], v97, s[20:23], s34 offen\n\t"
        "s_add_u32 s34, s34, 0x8000\n\t"
        "buffer_load_dwordx4 a[200:203], v97, s[20:23], s34 offen\n\t"
        "s_add_u32 s34, s34, 0x8000\n\t"
        "buffer_load_dwordx4 a[204:207], v97, s[20:23], s34 offen\n\t"
        "s_add_u32 s34, s34, 0x8000\n\t"
        "buffer_load_dwordx4 a[208:211], v97, s[20:23], s34 offen\n\t"
        "s_add_u32 s34, s34, 0x8000\n\t"
        "buffer_load_dwordx4 a[212:215], v97, s[20:23], s34 offen\n\t"
        "s_add_u32 s34, s34, 0x8000\n\t"
        "buffer_load_dwordx4 a[216:219], v97, s[20:23], s34 offen\n\t"
        "s_add_u32 s34, s34, 0x8000\n\t"
        "buffer_load_dwordx4 a[220:223], v97, s[20:23], s34 offen\n\t"
        "s_waitcnt lgkmcnt(0)\n\t"
        "s_waitcnt vmcnt(16)\n\t"
        "ds_write_b128 v122, a[128:131] offset:0\n\t"
        "ds_write_b128 v122, a[132:135] offset:1056\n\t"
        "ds_write_b128 v122, a[136:139] offset:2176\n\t"
        "ds_write_b128 v122, a[140:143] offset:3232\n\t"
        "ds_write_b128 v122, a[144:147] offset:4352\n\t"
        "ds_write_b128 v122, a[148:151] offset:5408\n\t"
        "ds_write_b128 v122, a[152:155] offset:6528\n\t"
        "ds_write_b128 v122, a[156:159] offset:7584\n\t"
        "ds_read_b64_tr_b16 a[128:129], v123 offset:0\n\t"
        "ds_read_b64_tr_b16 a[130:131], v123 offset:128\n\t"
        "ds_read_b64_tr_b16 a[132:133], v123 offset:4352\n\t"
        "ds_read_b64_tr_b16 a[134:135], v123 offset:4480\n\t"
        "s_waitcnt lgkmcnt(8)\n\t"
        "ds_read_b64_tr_b16 a[136:137], v123 offset:32\n\t"
        "ds_read_b64_tr_b16 a[138:139], v123 offset:160\n\t"
        "ds_read_b64_tr_b16 a[140:141], v123 offset:4384\n\t"
        "ds_read_b64_tr_b16 a[142:143], v123 offset:4512\n\t"
        "s_waitcnt lgkmcnt(8)\n\t"
        "ds_read_b64_tr_b16 a[144:145], v123 offset:512\n\t"
        "ds_read_b64_tr_b16 a[146:147], v123 offset:640\n\t"
        "ds_read_b64_tr_b16 a[148:149], v123 offset:4864\n\t"
        "ds_read_b64_tr_b16 a[150:151], v123 offset:4992\n\t"
        "s_waitcnt lgkmcnt(8)\n\t"
        "ds_read_b64_tr_b16 a[152:153], v123 offset:544\n\t"
        "ds_read_b64_tr_b16 a[154:155], v123 offset:672\n\t"
        "ds_read_b64_tr_b16 a[156:157], v123 offset:4896\n\t"
        "ds_read_b64_tr_b16 a[158:159], v123 offset:5024\n\t"
        "s_waitcnt lgkmcnt(8)\n\t"
        "s_lshl_b32 s34, s43, 21\n\t"
        "s_add_u32 s34, s34, 1835008\n\t"
        "buffer_load_dwordx4 a[224:227], v97, s[20:23], s34 offen\n\t"
        "s_add_u32 s34, s34, 0x8000\n\t"
        "buffer_load_dwordx4 a[228:231], v97, s[20:23], s34 offen\n\t"
        "s_add_u32 s34, s34, 0x8000\n\t"
        "buffer_load_dwordx4 a[232:235], v97, s[20:23], s34 offen\n\t"
        "s_add_u32 s34, s34, 0x8000\n\t"
        "buffer_load_dwordx4 a[236:239], v97, s[20:23], s34 offen\n\t"
        "s_add_u32 s34, s34, 0x8000\n\t"
        "buffer_load_dwordx4 a[240:243], v97, s[20:23], s34 offen\n\t"
        "s_add_u32 s34, s34, 0x8000\n\t"
        "buffer_load_dwordx4 a[244:247], v97, s[20:23], s34 offen\n\t"
        "s_add_u32 s34, s34, 0x8000\n\t"
        "buffer_load_dwordx4 a[248:251], v97, s[20:23], s34 offen\n\t"
        "s_add_u32 s34, s34, 0x8000\n\t"
        "buffer_load_dwordx4 a[252:255], v97, s[20:23], s34 offen\n\t"
        "s_waitcnt lgkmcnt(0)\n\t"
        "s_waitcnt vmcnt(16)\n\t"
        "ds_write_b128 v122, a[160:163] offset:0\n\t"
        "ds_write_b128 v122, a[164:167] offset:1056\n\t"
        "ds_write_b128 v122, a[168:171] offset:2176\n\t"
        "ds_write_b128 v122, a[172:175] offset:3232\n\t"
        "ds_write_b128 v122, a[176:179] offset:4352\n\t"
        "ds_write_b128 v122, a[180:183] offset:5408\n\t"
        "ds_write_b128 v122, a[184:187] offset:6528\n\t"
        "ds_write_b128 v122, a[188:191] offset:7584\n\t"
        "ds_read_b64_tr_b16 a[160:161], v123 offset:0\n\t"
        "ds_read_b64_tr_b16 a[162:163], v123 offset:128\n\t"
        "ds_read_b64_tr_b16 a[164:165], v123 offset:4352\n\t"
        "ds_read_b64_tr_b16 a[166:167], v123 offset:4480\n\t"
        "s_waitcnt lgkmcnt(8)\n\t"
        "ds_read_b64_tr_b16 a[168:169], v123 offset:32\n\t"
        "ds_read_b64_tr_b16 a[170:171], v123 offset:160\n\t"
        "ds_read_b64_tr_b16 a[172:173], v123 offset:4384\n\t"
        "ds_read_b64_tr_b16 a[174:175], v123 offset:4512\n\t"
        "s_waitcnt lgkmcnt(8)\n\t"
        "ds_read_b64_tr_b16 a[176:177], v123 offset:512\n\t"
        "ds_read_b64_tr_b16 a[178:179], v123 offset:640\n\t"
        "ds_read_b64_tr_b16 a[180:181], v123 offset:4864\n\t"
        "ds_read_b64_tr_b16 a[182:183], v123 offset:4992\n\t"
        "s_waitcnt lgkmcnt(8)\n\t"
        "ds_read_b64_tr_b16 a[184:185], v123 offset:544\n\t"
        "ds_read_b64_tr_b16 a[186:187], v123 offset:672\n\t"
        "ds_read_b64_tr_b16 a[188:189], v123 offset:4896\n\t"
        "ds_read_b64_tr_b16 a[190:191], v123 offset:5024\n\t"
        "s_waitcnt lgkmcnt(8)\n\t"
        "s_waitcnt lgkmcnt(0)\n\t"
        "s_waitcnt vmcnt(8)\n\t"
        "ds_write_b128 v122, a[192:195] offset:0\n\t"
        "ds_write_b128 v122, a[196:199] offset:1056\n\t"
        "ds_write_b128 v122, a[200:203] offset:2176\n\t"
        "ds_write_b128 v122, a[204:207] offset:3232\n\t"
        "ds_write_b128 v122, a[208:211] offset:4352\n\t"
        "ds_write_b128 v122, a[212:215] offset:5408\n\t"
        "ds_write_b128 v122, a[216:219] offset:6528\n\t"
        "ds_write_b128 v122, a[220:223] offset:7584\n\t"
        "ds_read_b64_tr_b16 a[192:193], v123 offset:0\n\t"
        "ds_read_b64_tr_b16 a[194:195], v123 offset:128\n\t"
        "ds_read_b64_tr_b16 a[196:197], v123 offset:4352\n\t"
        "ds_read_b64_tr_b16 a[198:199], v123 offset:4480\n\t"
        "s_waitcnt lgkmcnt(8)\n\t"
        "ds_read_b64_tr_b16 a[200:201], v123 offset:32\n\t"
        "ds_read_b64_tr_b16 a[202:203], v123 offset:160\n\t"
        "ds_read_b64_tr_b16 a[204:205], v123 offset:4384\n\t"
        "ds_read_b64_tr_b16 a[206:207], v123 offset:4512\n\t"
        "s_waitcnt lgkmcnt(8)\n\t"
        "ds_read_b64_tr_b16 a[208:209], v123 offset:512\n\t"
        "ds_read_b64_tr_b16 a[210:211], v123 offset:640\n\t"
        "ds_read_b64_tr_b16 a[212:213], v123 offset:4864\n\t"
        "ds_read_b64_tr_b16 a[214:215], v123 offset:4992\n\t"
        "s_waitcnt lgkmcnt(8)\n\t"
        "ds_read_b64_tr_b16 a[216:217], v123 offset:544\n\t"
        "ds_read_b64_tr_b16 a[218:219], v123 offset:672\n\t"
        "ds_read_b64_tr_b16 a[220:221], v123 offset:4896\n\t"
        "ds_read_b64_tr_b16 a[222:223], v123 offset:5024\n\t"
        "s_waitcnt lgkmcnt(8)\n\t"
        "s_waitcnt lgkmcnt(0)\n\t"
        "s_waitcnt vmcnt(0)\n\t"
        "ds_write_b128 v122, a[224:227] offset:0\n\t"
        "ds_write_b128 v122, a[228:231] offset:1056\n\t"
        "ds_write_b128 v122, a[232:235] offset:2176\n\t"
        "ds_write_b128 v122, a[236:239] offset:3232\n\t"
        "ds_write_b128 v122, a[240:243] offset:4352\n\t"
        "ds_write_b128 v122, a[244:247] offset:5408\n\t"
        "ds_write_b128 v122, a[248:251] offset:6528\n\t"
        "ds_write_b128 v122, a[252:255] offset:7584\n\t"
        "ds_read_b64_tr_b16 a[224:225], v123 offset:0\n\t"
        "ds_read_b64_tr_b16 a[226:227], v123 offset:128\n\t"
        "ds_read_b64_tr_b16 a[228:229], v123 offset:4352\n\t"
        "ds_read_b64_tr_b16 a[230:231], v123 offset:4480\n\t"
        "s_waitcnt lgkmcnt(8)\n\t"
        "ds_read_b64_tr_b16 a[232:233], v123 offset:32\n\t"
        "ds_read_b64_tr_b16 a[234:235], v123 offset:160\n\t"
        "ds_read_b64_tr_b16 a[236:237], v123 offset:4384\n\t"
        "ds_read_b64_tr_b16 a[238:239], v123 offset:4512\n\t"
        "s_waitcnt lgkmcnt(8)\n\t"
        "ds_read_b64_tr_b16 a[240:241], v123 offset:512\n\t"
        "ds_read_b64_tr_b16 a[242:243], v123 offset:640\n\t"
        "ds_read_b64_tr_b16 a[244:245], v123 offset:4864\n\t"
        "ds_read_b64_tr_b16 a[246:247], v123 offset:4992\n\t"
        "s_waitcnt lgkmcnt(8)\n\t"
        "ds_read_b64_tr_b16 a[248:249], v123 offset:544\n\t"
        "ds_read_b64_tr_b16 a[250:251], v123 offset:672\n\t"
        "ds_read_b64_tr_b16 a[252:253], v123 offset:4896\n\t"
        "ds_read_b64_tr_b16 a[254:255], v123 offset:5024\n\t"
        "s_waitcnt lgkmcnt(8)\n\t"
        "s_mov_b32 s35, 33792\n\t"
        "buffer_load_dword v0, off, s[12:15], s35 sc1\n\t"
        "s_waitcnt lgkmcnt(0)\n\t"
        "s_waitcnt vmcnt(0)\n\t"
        "s_mov_b32 s42, 0\n\t"
        "LB_cal%=:\n\t"
        "v_readfirstlane_b32 s34, v0\n\t"
        "s_nop 0\n\t"
        "s_cmp_lg_u32 s34, 0\n\t"
        "s_cbranch_scc1 LB_calok%=\n\t"
        "s_sleep 4\n\t"
        "s_mov_b32 s35, 33792\n\t"
        "buffer_load_dword v0, off, s[12:15], s35 sc1\n\t"
        "s_waitcnt vmcnt(0)\n\t"
        "s_add_u32 s42, s42, 1\n\t"
        "s_cmp_lt_u32 s42, 0x100000\n\t"
        "s_cbranch_scc1 LB_cal%=\n\t"
        "s_movk_i32 s34, 8\n\t"
        "s_branch LB_die%=\n\t"
        "LB_calok%=:\n\t"
        "v_readfirstlane_b32 s34, v0\n\t"
        "s_nop 0\n\t"
        "s_and_b32 s34, s34, 8192\n\t"
        "s_add_u32 s8, s8, s34\n\t"
        "s_addc_u32 s9, s9, 0\n\t"
        "v_min_u32_e32 v109, -2, v109\n\t"
        "buffer_store_dword v109, v116, s[8:11], 0 offen sc1\n\t"
        "s_memrealtime s[32:33]\n\t"
        "s_waitcnt lgkmcnt(0)\n\t"
        "v_mov_b32_e32 v110, s32\n\t"
        "s_lshl_b32 s34, s29, 8\n\t"
        "s_lshl_b32 s35, s30, 20\n\t"
        "s_or_b32 s54, s34, s35\n\t"
        "s_add_u32 s34, s54, 12589056\n\t"
        "v_mov_b32_e32 v111, s34\n\t"
        "s_barrier\n\t"
        "s_cmp_lg_u32 s43, 0\n\t"
        "s_cbranch_scc1 LB_st%=\n\t"
        "v_mov_b32_e32 v2, 1\n\t"
        "s_mov_b32 s35, 34304\n\t"
        "s_mov_b64 exec, 1\n\t"
        "buffer_atomic_add v2, off, s[12:15], s35 sc0\n\t"
        "s_mov_b64 exec, -1\n\t"
        "s_waitcnt vmcnt(0)\n\t"
        "v_readfirstlane_b32 s34, v2\n\t"
        "s_nop 0\n\t"
        "s_cmp_lg_u32 s34, 95\n\t"
        "s_cbranch_scc1 LB_st%=\n\t"
        "s_memrealtime s[32:33]\n\t"
        "v_mbcnt_lo_u32_b32 v3, -1, 0\n\t"
        "v_lshlrev_b32_e32 v3, 7, v3\n\t"
        "s_waitcnt lgkmcnt(0)\n\t"
        "s_add_u32 s34, s32, 50\n\t"
        "s_or_b32 s34, s34, 1\n\t"
        "v_mov_b32_e32 v2, s34\n\t"
        "s_mov_b32 s35, 36864\n\t"
        "s_mov_b64 exec, 0xffff\n\t"
        "buffer_store_dword v2, v3, s[12:15], s35 offen sc1\n\t"
        "s_mov_b64 exec, -1\n\t"
        "LB_st%=:\n\t"
        "s_mov_b32 s24, 0\n\t"
        "s_mov_b32 s26, 0\n\t"
        "s_mov_b32 s27, 0x80000\n\t"
        "s_mov_b32 s28, 0x180000\n\t"
        "s_mov_b32 s31, 0\n\t"
        "s_mov_b32 s47, 0\n\t"
        "s_mov_b32 s38, 0xff00ff00\n\t"
        "s_mov_b32 s39, 0xff00ff00\n\t"
        "s_mov_b32 s40, 0\n\t"
        "s_mov_b32 s41, -1\n\t"
        "v_mov_b64_e32 v[0:1], 0\n\t"
        "v_mov_b64_e32 v[2:3], 0\n\t"
        "v_mov_b64_e32 v[4:5], 0\n\t"
        "v_mov_b64_e32 v[6:7], 0\n\t"
        "v_mov_b64_e32 v[8:9], 0\n\t"
        "v_mov_b64_e32 v[10:11], 0\n\t"
        "v_mov_b64_e32 v[12:13], 0\n\t"
        "v_mov_b64_e32 v[14:15], 0\n\t"
        "s_mov_b32 s42, 0\n\t"
        "LB_gw%=:\n\t"
        "buffer_load_dword v16, off, s[12:15], s45 sc1\n\t"
        "s_waitcnt vmcnt(0)\n\t"
        "v_readfirstlane_b32 s25, v16\n\t"
        "s_nop 0\n\t"
        "s_cmp_lg_u32 s25, 0\n\t"
        "s_cbranch_scc1 LB_loop%=\n\t"
        "s_sleep 2\n\t"
        "s_add_u32 s42, s42, 1\n\t"
        "s_cmp_lt_u32 s42, 0x100000\n\t"
        "s_cbranch_scc1 LB_gw%=\n\t"
        "s_movk_i32 s34, 9\n\t"
        "s_branch LB_die%=\n\t"
        ".p2align 8\n\t"
        "LB_loop%=:\n\t"
        "s_mov_b32 s42, 0\n\t"
        "s_sub_u32 s35, s25, 8\n\t"
        "s_waitcnt lgkmcnt(0)\n\t"
        "s_memrealtime s[2:3]\n\t"
        "s_memrealtime s[4:5]\n\t"
        "s_memrealtime s[6:7]\n\t"
        "LB_wa%=:\n\t"
        "s_memrealtime s[32:33]\n\t"
        "s_waitcnt lgkmcnt(6)\n\t"
        "s_sub_u32 s34, s35, s2\n\t"
        "s_cmpk_lt_u32 s34, 0x2000\n\t"
        "s_cbranch_scc0 LB_wa%=x\n\t"
        "s_memrealtime s[2:3]\n\t"
        "s_waitcnt lgkmcnt(6)\n\t"
        "s_sub_u32 s34, s35, s4\n\t"
        "s_cmpk_lt_u32 s34, 0x2000\n\t"
        "s_cbranch_scc0 LB_wa%=x\n\t"
        "s_memrealtime s[4:5]\n\t"
        "s_waitcnt lgkmcnt(6)\n\t"
        "s_sub_u32 s34, s35, s6\n\t"
        "s_cmpk_lt_u32 s34, 0x2000\n\t"
        "s_cbranch_scc0 LB_wa%=x\n\t"
        "s_memrealtime s[6:7]\n\t"
        "s_waitcnt lgkmcnt(6)\n\t"
        "s_sub_u32 s34, s35, s32\n\t"
        "s_cmpk_lt_u32 s34, 0x2000\n\t"
        "s_cbranch_scc1 LB_wa%=\n\t"
        "s_nop 3\n\t"
        "LB_wa%=x:\n\t"
        "s_add_i32 s34, s34, 4\n\t"
        "s_max_i32 s34, s34, 0\n\t"
        "s_bitcmp1_b32 s34, 1\n\t"
        "s_cbranch_scc0 LB_wa%=y\n\t"
        "s_nop 15\n\t"
        "LB_wa%=y:\n\t"
        "s_bitcmp1_b32 s34, 0\n\t"
        "s_cbranch_scc0 LB_wa%=z\n\t"
        "s_nop 9\n\t"
        "LB_wa%=z:\n\t"
        "LB_rd%=:\n\t"
        "buffer_load_dwordx4 v[16:19], v112, s[8:11], s26 offen offset:0 sc1\n\t"
        "s_nop 9\n\t"
        "buffer_load_dwordx4 v[20:23], v112, s[8:11], s26 offen offset:1024 sc1\n\t"
        "s_nop 9\n\t"
        "buffer_load_dwordx4 v[24:27], v112, s[8:11], s26 offen offset:2048 sc1\n\t"
        "s_nop 9\n\t"
        "buffer_load_dwordx4 v[28:31], v112, s[8:11], s26 offen offset:3072 sc1\n\t"
        "LB_go%=:\n\t"
        "s_waitcnt vmcnt(3)\n\t"
        "v_smfmac_f32_16x16x64_bf16 v[0:3], v[16:19], v[128:135], v106\n\t"
        "v_max3_u32 v108, v16, v17, v18\n\t"
        "v_max_u32_e32 v108, v19, v108\n\t"
        "v_smfmac_f32_16x16x64_bf16 v[4:7], v[16:19], v[136:143], v106\n\t"
        "v_smfmac_f32_16x16x64_bf16 v[8:11], v[16:19], v[144:151], v106\n\t"
        "v_smfmac_f32_16x16x64_bf16 v[12:15], v[16:19], v[152:159], v106\n\t"
        "s_waitcnt vmcnt(2)\n\t"
        "v_smfmac_f32_16x16x64_bf16 v[0:3], v[20:23], v[160:167], v106\n\t"
        "v_max3_u32 v108, v20, v21, v108\n\t"
        "v_max3_u32 v108, v22, v23, v108\n\t"
        "v_smfmac_f32_16x16x64_bf16 v[4:7], v[20:23], v[168:175], v106\n\t"
        "v_smfmac_f32_16x16x64_bf16 v[8:11], v[20:23], v[176:183], v106\n\t"
        "v_smfmac_f32_16x16x64_bf16 v[12:15], v[20:23], v[184:191], v106\n\t"
        "s_waitcnt vmcnt(1)\n\t"
        "v_smfmac_f32_16x16x64_bf16 v[0:3], v[24:27], v[192:199], v106\n\t"
        "v_max3_u32 v108, v24, v25, v108\n\t"
        "v_max3_u32 v108, v26, v27, v108\n\t"
        "v_smfmac_f32_16x16x64_bf16 v[4:7], v[24:27], v[200:207], v106\n\t"
        "v_smfmac_f32_16x16x64_bf16 v[8:11], v[24:27], v[208:215], v106\n\t"
        "v_smfmac_f32_16x16x64_bf16 v[12:15], v[24:27], v[216:223], v106\n\t"
        "s_waitcnt vmcnt(0)\n\t"
        "v_smfmac_f32_16x16x64_bf16 v[0:3], v[28:31], v[224:231], v106\n\t"
        "v_max3_u32 v108, v28, v29, v108\n\t"
        "v_max3_u32 v108, v30, v31, v108\n\t"
        "v_smfmac_f32_16x16x64_bf16 v[4:7], v[28:31], v[232:239], v106\n\t"
        "v_cmp_eq_u32_e32 vcc, -1, v108\n\t"
        "v_smfmac_f32_16x16x64_bf16 v[8:11], v[28:31], v[240:247], v106\n\t"
        "v_smfmac_f32_16x16x64_bf16 v[12:15], v[28:31], v[248:255], v106\n\t"
        "s_cbranch_vccnz LB_rdfail%=\n\t"
        "s_nop 0\n\t"
        "v_add_f32_e32 v0, v0, v1\n\t"
        "v_add_f32_e32 v1, v2, v3\n\t"
        "ds_write_b64 v118, v[0:1] offset:0\n\t"
        "v_add_f32_e32 v4, v4, v5\n\t"
        "v_add_f32_e32 v5, v6, v7\n\t"
        "ds_write_b64 v118, v[4:5] offset:512\n\t"
        "v_add_f32_e32 v8, v8, v9\n\t"
        "v_add_f32_e32 v9, v10, v11\n\t"
        "ds_write_b64 v118, v[8:9] offset:1024\n\t"
        "v_add_f32_e32 v12, v12, v13\n\t"
        "v_add_f32_e32 v13, v14, v15\n\t"
        "ds_write_b64 v118, v[12:13] offset:1536\n\t"
        "buffer_store_dwordx2 v[110:111], v117, s[8:11], s26 offen sc1\n\t"
        "s_waitcnt lgkmcnt(0)\n\t"
        "s_barrier\n\t"
        "ds_read_b128 v[16:19], v119 offset:0\n\t"
        "ds_read_b128 v[20:23], v119 offset:2048\n\t"
        "ds_read_b128 v[24:27], v119 offset:4096\n\t"
        "ds_read_b128 v[28:31], v119 offset:6144\n\t"
        "buffer_load_dwordx4 v[64:67], v124, s[8:11], s28 offen sc1\n\t"
        "s_waitcnt lgkmcnt(2)\n\t"
        "v_pk_add_f32 v[16:17], v[16:17], v[20:21]\n\t"
        "v_pk_add_f32 v[18:19], v[18:19], v[22:23]\n\t"
        "s_waitcnt lgkmcnt(0)\n\t"
        "v_pk_add_f32 v[24:25], v[24:25], v[28:29]\n\t"
        "v_pk_add_f32 v[26:27], v[26:27], v[30:31]\n\t"
        "v_pk_add_f32 v[16:17], v[16:17], v[24:25]\n\t"
        "v_pk_add_f32 v[18:19], v[18:19], v[26:27]\n\t"
        "v_cvt_pk_bf16_f32 v20, v16, v17\n\t"
        "v_cvt_pk_bf16_f32 v21, v18, v19\n\t"
        "buffer_store_dwordx2 v[20:21], v114, s[8:11], s26 offen sc1\n\t"
        "s_cmp_eq_u32 s24, 63\n\t"
        "s_cbranch_scc1 LB_end%=\n\t"
        "s_memrealtime s[52:53]\n\t"
        "v_mov_b64_e32 v[24:25], -1\n\t"
        "v_mov_b64_e32 v[32:33], 0\n\t"
        "v_mov_b64_e32 v[34:35], 0\n\t"
        "v_mov_b64_e32 v[36:37], 0\n\t"
        "v_mov_b64_e32 v[38:39], 0\n\t"
        "v_mov_b64_e32 v[40:41], 0\n\t"
        "v_mov_b64_e32 v[42:43], 0\n\t"
        "v_mov_b64_e32 v[44:45], 0\n\t"
        "v_mov_b64_e32 v[46:47], 0\n\t"
        "v_mov_b64_e32 v[0:1], 0\n\t"
        "v_mov_b64_e32 v[2:3], 0\n\t"
        "v_mov_b64_e32 v[4:5], 0\n\t"
        "v_mov_b64_e32 v[6:7], 0\n\t"
        "v_mov_b64_e32 v[8:9], 0\n\t"
        "v_mov_b64_e32 v[10:11], 0\n\t"
        "v_mov_b64_e32 v[12:13], 0\n\t"
        "v_mov_b64_e32 v[14:15], 0\n\t"
        "s_add_u32 s46, s26, 4096\n\t"
        "s_sleep 1\n\t"
        "buffer_load_dwordx2 v[104:105], v113, s[8:11], s26 offen sc1\n\t"
        "buffer_store_dword v24, v116, s[8:11], s28 offen sc1\n\t"
        "s_xor_b32 s34, s26, 0x100000\n\t"
        "buffer_store_dwordx2 v[24:25], v114, s[8:11], s34 offen sc1\n\t"
        "s_add_u32 s66, s49, s43\n\t"
        "s_cmp_lg_u32 s66, 32\n\t"
        "s_cbranch_scc1 LB_norep%=\n\t"
        "s_waitcnt vmcnt(4)\n\t"
        "s_sub_u32 s66, s24, 1\n\t"
        "v_lshrrev_b32_e32 v68, 16, v64\n\t"
        "v_cmp_eq_u32_e32 vcc, s66, v68\n\t"
        "v_subrev_u32_e32 v64, s59, v64\n\t"
        "v_bfe_i32 v64, v64, 0, 16\n\t"
        "v_cndmask_b32_e32 v64, 0, v64, vcc\n\t"
        "v_lshrrev_b32_e32 v68, 16, v65\n\t"
        "v_cmp_eq_u32_e32 vcc, s66, v68\n\t"
        "v_subrev_u32_e32 v65, s59, v65\n\t"
        "v_bfe_i32 v65, v65, 0, 16\n\t"
        "v_cndmask_b32_e32 v65, 0, v65, vcc\n\t"
        "v_lshrrev_b32_e32 v68, 16, v66\n\t"
        "v_cmp_eq_u32_e32 vcc, s66, v68\n\t"
        "v_subrev_u32_e32 v66, s59, v66\n\t"
        "v_bfe_i32 v66, v66, 0, 16\n\t"
        "v_cndmask_b32_e32 v66, 0, v66, vcc\n\t"
        "v_lshrrev_b32_e32 v68, 16, v67\n\t"
        "v_cmp_eq_u32_e32 vcc, s66, v68\n\t"
        "v_subrev_u32_e32 v67, s59, v67\n\t"
        "v_bfe_i32 v67, v67, 0, 16\n\t"
        "v_cndmask_b32_e32 v67, 0, v67, vcc\n\t"
        "v_max3_i32 v64, v64, v65, v66\n\t"
        "v_max_i32_e32 v64, v64, v67\n\t"
        "s_nop 1\n\t"
        "v_max_i32_dpp v64, v64, v64 quad_perm:[1,0,3,2] row_mask:0xf bank_mask:0xf\n\t"
        "s_nop 1\n\t"
        "v_max_i32_dpp v64, v64, v64 quad_perm:[2,3,0,1] row_mask:0xf bank_mask:0xf\n\t"
        "s_nop 1\n\t"
        "v_max_i32_dpp v64, v64, v64 row_ror:4 row_mask:0xf bank_mask:0xf\n\t"
        "s_nop 1\n\t"
        "v_max_i32_dpp v64, v64, v64 row_ror:8 row_mask:0xf bank_mask:0xf\n\t"
        "v_mov_b32_e32 v65, v64\n\t"
        "s_nop 1\n\t"
        "v_permlane16_swap_b32_e32 v64, v65\n\t"
        "v_max_i32_e32 v64, v64, v65\n\t"
        "v_mov_b32_e32 v65, v64\n\t"
        "s_nop 1\n\t"
        "v_permlane32_swap_b32_e32 v64, v65\n\t"
        "v_max_i32_e32 v64, v64, v65\n\t"
        "s_nop 0\n\t"
        "v_readfirstlane_b32 s64, v64\n\t"
        "LB_norep%=:\n\t"
        "s_waitcnt vmcnt(2) lgkmcnt(0)\n\t"
        "s_mov_b32 s61, s29\n\t"
        "s_mov_b32 s62, s30\n\t"
        "s_mov_b32 s63, s31\n\t"
        "v_and_b32_e32 v30, 0xff, v105\n\t"
        "v_cmp_ne_u32_e32 vcc, s24, v30\n\t"
        "s_nop 0\n\t"
        "s_cbranch_vccnz LB_stre%=\n\t"
        "LB_stok%=:\n\t"
        "v_subrev_u32_e32 v104, s25, v104\n\t"
        "s_nop 1\n\t"
        "v_max_i32_dpp v104, v104, v104 quad_perm:[1,0,3,2] row_mask:0xf bank_mask:0xf\n\t"
        "s_nop 1\n\t"
        "v_max_i32_dpp v104, v104, v104 quad_perm:[2,3,0,1] row_mask:0xf bank_mask:0xf\n\t"
        "s_nop 1\n\t"
        "v_max_i32_dpp v104, v104, v104 row_ror:4 row_mask:0xf bank_mask:0xf\n\t"
        "s_nop 1\n\t"
        "v_max_i32_dpp v104, v104, v104 row_ror:8 row_mask:0xf bank_mask:0xf\n\t"
        "s_nop 1\n\t"
        "v_max_i32_dpp v104, v104, v104 row_bcast:15 row_mask:0xa bank_mask:0xf\n\t"
        "s_nop 1\n\t"
        "v_max_i32_dpp v104, v104, v104 row_bcast:31 row_mask:0xc bank_mask:0xf\n\t"
        "v_readfirstlane_b32 s35, v105\n\t"
        "v_readlane_b32 s36, v104, 63\n\t"
        "s_bfe_u32 s29, s35, 0xc0008\n\t"
        "s_lshr_b32 s30, s35, 20\n\t"
        "s_add_u32 s35, s31, s50\n\t"
        "s_mul_i32 s37, s50, 3\n\t"
        "s_min_u32 s35, s35, s37\n\t"
        "s_add_i32 s34, s36, s51\n\t"
        "s_cmp_gt_i32 s34, 0\n\t"
        "s_cselect_b32 s31, s35, 0\n\t"
        "s_cselect_b32 s34, s34, 0\n\t"
        "s_add_u32 s34, s34, s31\n\t"
        "s_add_u32 s37, s25, s34\n\t"
        "s_add_u32 s45, s37, s29\n\t"
        "s_add_u32 s25, s45, s30\n\t"
        "s_add_u32 s66, s49, s43\n\t"
        "s_cmp_lg_u32 s66, 32\n\t"
        "s_cbranch_scc1 LB_noctl%=\n\t"
        "s_cmp_eq_u32 s24, 0\n\t"
        "s_cbranch_scc1 LB_ctl0%=\n\t"
        "s_cmp_lt_u32 s24, 6\n\t"
        "s_cselect_b32 s68, 32, 8\n\t"
        "s_add_i32 s65, s36, s62\n\t"
        "s_cmp_eq_u32 s24, 1\n\t"
        "s_cselect_b32 s34, 6, 0\n\t"
        "s_cselect_b32 s35, 3, 0\n\t"
        "s_sub_u32 s64, s64, s34\n\t"
        "s_sub_u32 s65, s65, s35\n\t"
        "s_lshl_b32 s66, s65, 1\n\t"
        "s_sub_i32 s66, s66, s56\n\t"
        "s_cmp_gt_i32 s66, 56\n\t"
        "s_cbranch_scc1 LB_c1%=\n\t"
        "s_lshl_b32 s66, s65, 1\n\t"
        "s_add_u32 s34, s56, s68\n\t"
        "s_min_i32 s34, s34, s66\n\t"
        "s_sub_u32 s35, s56, s66\n\t"
        "s_ashr_i32 s35, s35, 1\n\t"
        "s_max_i32 s35, s35, 1\n\t"
        "s_sub_u32 s35, s56, s35\n\t"
        "s_max_i32 s56, s34, s35\n\t"
        "s_branch LB_c2%=\n\t"
        "LB_c1%=:\n\t"
        "s_add_u32 s56, s56, 2\n\t"
        "s_add_i32 s66, s64, s57\n\t"
        "s_cmp_gt_i32 s66, s61\n\t"
        "s_cbranch_scc1 LB_c2%=\n\t"
        "s_add_u32 s57, s57, 4\n\t"
        "s_min_u32 s57, s57, 24\n\t"
        "LB_c2%=:\n\t"
        "s_cmp_lg_u32 s63, 0\n\t"
        "s_cbranch_scc1 LB_c4%=\n\t"
        "s_lshl_b32 s66, s64, 1\n\t"
        "s_sub_i32 s66, s66, s55\n\t"
        "s_cmp_gt_i32 s66, 56\n\t"
        "s_cbranch_scc1 LB_c3%=\n\t"
        "s_lshl_b32 s66, s64, 1\n\t"
        "s_add_u32 s34, s55, s68\n\t"
        "s_min_i32 s34, s34, s66\n\t"
        "s_sub_u32 s35, s55, s66\n\t"
        "s_ashr_i32 s35, s35, 1\n\t"
        "s_max_i32 s35, s35, 1\n\t"
        "s_sub_u32 s35, s55, s35\n\t"
        "s_max_i32 s55, s34, s35\n\t"
        "s_branch LB_c4%=\n\t"
        "LB_c3%=:\n\t"
        "s_add_u32 s55, s55, 2\n\t"
        "s_cmp_lt_i32 s60, s58\n\t"
        "s_cbranch_scc1 LB_c4%=\n\t"
        "s_add_u32 s66, s58, 8\n\t"
        "s_cmp_gt_i32 s60, s66\n\t"
        "s_cbranch_scc1 LB_c4%=\n\t"
        "s_add_u32 s58, s58, 4\n\t"
        "s_min_u32 s58, s58, 22\n\t"
        "LB_c4%=:\n\t"
        "s_and_b32 s66, s24, 3\n\t"
        "s_cmp_eq_u32 s66, 0\n\t"
        "s_cselect_b32 s66, 1, 0\n\t"
        "s_cmp_lt_u32 s24, 30\n\t"
        "s_cselect_b32 s34, 8, 8\n\t"
        "s_cselect_b32 s35, 6, 6\n\t"
        "s_sub_u32 s57, s57, s66\n\t"
        "s_max_i32 s57, s57, s34\n\t"
        "s_sub_u32 s58, s58, s66\n\t"
        "s_max_i32 s58, s58, s35\n\t"
        "s_add_u32 s34, s55, 1\n\t"
        "s_lshr_b32 s34, s34, 1\n\t"
        "s_add_u32 s34, s34, s57\n\t"
        "s_min_u32 s34, s34, 1000\n\t"
        "s_lshl_b32 s34, s34, 8\n\t"
        "s_add_u32 s35, s56, 1\n\t"
        "s_lshr_b32 s35, s35, 1\n\t"
        "s_add_u32 s35, s35, s58\n\t"
        "s_min_u32 s35, s35, 1000\n\t"
        "s_lshl_b32 s35, s35, 20\n\t"
        "s_or_b32 s54, s34, s35\n\t"
        "s_branch LB_ctl1%=\n\t"
        "LB_ctl0%=:\n\t"
        "s_movk_i32 s57, 8\n\t"
        "s_movk_i32 s58, 6\n\t"
        "s_sub_u32 s55, s29, 32\n\t"
        "s_lshl_b32 s55, s55, 1\n\t"
        "s_sub_u32 s56, s30, 18\n\t"
        "s_lshl_b32 s56, s56, 1\n\t"
        "LB_ctl1%=:\n\t"
        "s_mov_b32 s59, s37\n\t"
        "s_sub_u32 s60, 0, s36\n\t"
        "LB_noctl%=:\n\t"
        "s_cmp_eq_u32 s24, 0\n\t"
        "s_cselect_b32 s34, 1537, 1\n\t"
        "s_add_u32 s34, s34, s24\n\t"
        "s_add_u32 s34, s34, s54\n\t"
        "v_mov_b32_e32 v111, s34\n\t"
        "s_mov_b32 s42, 0\n\t"
        "s_sub_u32 s35, s45, 8\n\t"
        "s_waitcnt lgkmcnt(0)\n\t"
        "s_memrealtime s[2:3]\n\t"
        "s_memrealtime s[4:5]\n\t"
        "s_memrealtime s[6:7]\n\t"
        "LB_wo%=:\n\t"
        "s_memrealtime s[32:33]\n\t"
        "s_waitcnt lgkmcnt(6)\n\t"
        "s_sub_u32 s34, s35, s2\n\t"
        "s_cmpk_lt_u32 s34, 0x2000\n\t"
        "s_cbranch_scc0 LB_wo%=x\n\t"
        "s_memrealtime s[2:3]\n\t"
        "s_waitcnt lgkmcnt(6)\n\t"
        "s_sub_u32 s34, s35, s4\n\t"
        "s_cmpk_lt_u32 s34, 0x2000\n\t"
        "s_cbranch_scc0 LB_wo%=x\n\t"
        "s_memrealtime s[4:5]\n\t"
        "s_waitcnt lgkmcnt(6)\n\t"
        "s_sub_u32 s34, s35, s6\n\t"
        "s_cmpk_lt_u32 s34, 0x2000\n\t"
        "s_cbranch_scc0 LB_wo%=x\n\t"
        "s_memrealtime s[6:7]\n\t"
        "s_waitcnt lgkmcnt(6)\n\t"
        "s_sub_u32 s34, s35, s32\n\t"
        "s_cmpk_lt_u32 s34, 0x2000\n\t"
        "s_cbranch_scc1 LB_wo%=\n\t"
        "s_nop 3\n\t"
        "LB_wo%=x:\n\t"
        "s_add_i32 s34, s34, 4\n\t"
        "s_max_i32 s34, s34, 0\n\t"
        "s_bitcmp1_b32 s34, 1\n\t"
        "s_cbranch_scc0 LB_wo%=y\n\t"
        "s_nop 15\n\t"
        "LB_wo%=y:\n\t"
        "s_bitcmp1_b32 s34, 0\n\t"
        "s_cbranch_scc0 LB_wo%=z\n\t"
        "s_nop 9\n\t"
        "LB_wo%=z:\n\t"
        "LB_ro%=:\n\t"
        "buffer_load_dwordx4 v[64:67], v115, s[8:11], s26 offen offset:0 sc1\n\t"
        "s_nop 12\n\t"
        "buffer_load_dwordx4 v[68:71], v115, s[8:11], s26 offen offset:1024 sc1\n\t"
        "s_nop 12\n\t"
        "buffer_load_dwordx4 v[72:75], v115, s[8:11], s26 offen offset:2048 sc1\n\t"
        "s_nop 12\n\t"
        "buffer_load_dwordx4 v[76:79], v115, s[8:11], s26 offen offset:3072 sc1\n\t"
        "s_nop 12\n\t"
        "buffer_load_dwordx4 v[80:83], v115, s[8:11], s46 offen offset:0 sc1\n\t"
        "s_nop 12\n\t"
        "buffer_load_dwordx4 v[84:87], v115, s[8:11], s46 offen offset:1024 sc1\n\t"
        "s_nop 12\n\t"
        "buffer_load_dwordx4 v[88:91], v115, s[8:11], s46 offen offset:2048 sc1\n\t"
        "s_nop 12\n\t"
        "buffer_load_dwordx4 v[92:95], v115, s[8:11], s46 offen offset:3072 sc1\n\t"
        "LB_go2%=:\n\t"
        "s_waitcnt vmcnt(7)\n\t"
        "v_smfmac_f32_16x16x64_bf16 v[32:35], v[64:67], a[0:7], v106\n\t"
        "v_max3_u32 v108, v64, v65, v66\n\t"
        "v_max_u32_e32 v108, v67, v108\n\t"
        "v_smfmac_f32_16x16x64_bf16 v[40:43], v[64:67], a[16:23], v106\n\t"
        "v_smfmac_f32_16x16x64_bf16 v[36:39], v[64:67], a[8:15], v106\n\t"
        "v_smfmac_f32_16x16x64_bf16 v[44:47], v[64:67], a[24:31], v106\n\t"
        "s_waitcnt vmcnt(6)\n\t"
        "v_smfmac_f32_16x16x64_bf16 v[32:35], v[68:71], a[32:39], v106\n\t"
        "v_max3_u32 v108, v68, v69, v108\n\t"
        "v_max3_u32 v108, v70, v71, v108\n\t"
        "v_smfmac_f32_16x16x64_bf16 v[40:43], v[68:71], a[48:55], v106\n\t"
        "v_smfmac_f32_16x16x64_bf16 v[36:39], v[68:71], a[40:47], v106\n\t"
        "v_smfmac_f32_16x16x64_bf16 v[44:47], v[68:71], a[56:63], v106\n\t"
        "s_waitcnt vmcnt(5)\n\t"
        "v_smfmac_f32_16x16x64_bf16 v[32:35], v[72:75], a[64:71], v106\n\t"
        "v_max3_u32 v108, v72, v73, v108\n\t"
        "v_max3_u32 v108, v74, v75, v108\n\t"
        "v_smfmac_f32_16x16x64_bf16 v[40:43], v[72:75], a[80:87], v106\n\t"
        "v_smfmac_f32_16x16x64_bf16 v[36:39], v[72:75], a[72:79], v106\n\t"
        "v_smfmac_f32_16x16x64_bf16 v[44:47], v[72:75], a[88:95], v106\n\t"
        "s_waitcnt vmcnt(4)\n\t"
        "v_smfmac_f32_16x16x64_bf16 v[32:35], v[76:79], a[96:103], v106\n\t"
        "v_max3_u32 v108, v76, v77, v108\n\t"
        "v_max3_u32 v108, v78, v79, v108\n\t"
        "v_smfmac_f32_16x16x64_bf16 v[40:43], v[76:79], a[112:119], v106\n\t"
        "v_smfmac_f32_16x16x64_bf16 v[36:39], v[76:79], a[104:111], v106\n\t"
        "v_smfmac_f32_16x16x64_bf16 v[44:47], v[76:79], a[120:127], v106\n\t"
        "s_waitcnt vmcnt(3)\n\t"
        "v_smfmac_f32_16x16x64_bf16 v[32:35], v[80:83], a[128:135], v106\n\t"
        "v_max3_u32 v108, v80, v81, v108\n\t"
        "v_max3_u32 v108, v82, v83, v108\n\t"
        "v_smfmac_f32_16x16x64_bf16 v[40:43], v[80:83], a[144:151], v106\n\t"
        "v_smfmac_f32_16x16x64_bf16 v[36:39], v[80:83], a[136:143], v106\n\t"
        "v_smfmac_f32_16x16x64_bf16 v[44:47], v[80:83], a[152:159], v106\n\t"
        "s_waitcnt vmcnt(2)\n\t"
        "v_smfmac_f32_16x16x64_bf16 v[32:35], v[84:87], a[160:167], v106\n\t"
        "v_max3_u32 v108, v84, v85, v108\n\t"
        "v_max3_u32 v108, v86, v87, v108\n\t"
        "v_smfmac_f32_16x16x64_bf16 v[40:43], v[84:87], a[176:183], v106\n\t"
        "v_smfmac_f32_16x16x64_bf16 v[36:39], v[84:87], a[168:175], v106\n\t"
        "v_smfmac_f32_16x16x64_bf16 v[44:47], v[84:87], a[184:191], v106\n\t"
        "s_waitcnt vmcnt(1)\n\t"
        "v_smfmac_f32_16x16x64_bf16 v[32:35], v[88:91], a[192:199], v106\n\t"
        "v_max3_u32 v108, v88, v89, v108\n\t"
        "v_max3_u32 v108, v90, v91, v108\n\t"
        "v_smfmac_f32_16x16x64_bf16 v[40:43], v[88:91], a[208:215], v106\n\t"
        "v_smfmac_f32_16x16x64_bf16 v[36:39], v[88:91], a[200:207], v106\n\t"
        "v_smfmac_f32_16x16x64_bf16 v[44:47], v[88:91], a[216:223], v106\n\t"
        "s_waitcnt vmcnt(0)\n\t"
        "v_smfmac_f32_16x16x64_bf16 v[32:35], v[92:95], a[224:231], v106\n\t"
        "v_max3_u32 v108, v92, v93, v108\n\t"
        "v_max3_u32 v108, v94, v95, v108\n\t"
        "v_smfmac_f32_16x16x64_bf16 v[40:43], v[92:95], a[240:247], v106\n\t"
        "v_cmp_eq_u32_e32 vcc, -1, v108\n\t"
        "v_smfmac_f32_16x16x64_bf16 v[36:39], v[92:95], a[232:239], v106\n\t"
        "v_smfmac_f32_16x16x64_bf16 v[44:47], v[92:95], a[248:255], v106\n\t"
        "s_cbranch_vccnz LB_rofail%=\n\t"
        "s_nop 0\n\t"
        "v_add_f32_e32 v32, v32, v33\n\t"
        "s_nop 1\n\t"
        "v_add_f32_e32 v40, v42, v43\n\t"
        "v_add_f32_e32 v32, v32, v40\n\t"
        "v_add_f32_e32 v36, v36, v37\n\t"
        "v_add_f32_e32 v44, v46, v47\n\t"
        "v_add_f32_e32 v36, v36, v44\n\t"
        "ds_write2_b32 v120, v32, v36 offset0:0 offset1:64\n\t"
        "s_waitcnt lgkmcnt(0)\n\t"
        "s_barrier\n\t"
        "ds_read_b64 v[64:65], v121 offset:0\n\t"
        "ds_read_b64 v[66:67], v121 offset:512\n\t"
        "ds_read_b64 v[68:69], v121 offset:1024\n\t"
        "ds_read_b64 v[70:71], v121 offset:1536\n\t"
        "s_waitcnt lgkmcnt(2)\n\t"
        "v_pk_add_f32 v[64:65], v[64:65], v[66:67]\n\t"
        "s_waitcnt lgkmcnt(0)\n\t"
        "v_pk_add_f32 v[68:69], v[68:69], v[70:71]\n\t"
        "v_pk_add_f32 v[64:65], v[64:65], v[68:69]\n\t"
        "v_cvt_pk_bf16_f32 v64, v64, v65\n\t"
        "buffer_store_dword v64, v116, s[8:11], s27 offen sc1\n\t"
        "s_memrealtime s[32:33]\n\t"
        "s_waitcnt lgkmcnt(0)\n\t"
        "v_mov_b32_e32 v110, s32\n\t"
        "s_add_u32 s24, s24, 1\n\t"
        "s_mov_b32 s28, s26\n\t"
        "s_mov_b32 s26, s27\n\t"
        "s_add_u32 s27, s27, 0x80000\n\t"
        "s_and_b32 s27, s27, 0x1fffff\n\t"
        "s_branch LB_loop%=\n\t"
        "LB_end%=:\n\t"
        "s_endpgm\n\t"
        "LB_stre%=:\n\t"
        "s_mov_b32 s42, 0\n\t"
        "LB_str2%=:\n\t"
        "buffer_load_dwordx2 v[104:105], v113, s[8:11], s26 offen sc1\n\t"
        "s_waitcnt vmcnt(0)\n\t"
        "v_and_b32_e32 v30, 0xff, v105\n\t"
        "v_cmp_ne_u32_e32 vcc, s24, v30\n\t"
        "s_nop 0\n\t"
        "s_cbranch_vccz LB_stok%=\n\t"
        "s_add_u32 s42, s42, 1\n\t"
        "s_cmp_lt_u32 s42, 0x100000\n\t"
        "s_cbranch_scc1 LB_str2%=\n\t"
        "s_movk_i32 s34, 5\n\t"
        "s_branch LB_die%=\n\t"
        "LB_rdfail%=:\n\t"
        "s_or_b32 s47, s47, 1\n\t"
        "buffer_load_dwordx4 v[16:19], v112, s[8:11], s26 offen offset:0 sc1\n\t"
        "buffer_load_dwordx4 v[20:23], v112, s[8:11], s26 offen offset:1024 sc1\n\t"
        "buffer_load_dwordx4 v[24:27], v112, s[8:11], s26 offen offset:2048 sc1\n\t"
        "buffer_load_dwordx4 v[28:31], v112, s[8:11], s26 offen offset:3072 sc1\n\t"
        "s_nop 7\n\t"
        "v_mov_b64_e32 v[0:1], 0\n\t"
        "v_mov_b64_e32 v[2:3], 0\n\t"
        "v_mov_b64_e32 v[4:5], 0\n\t"
        "v_mov_b64_e32 v[6:7], 0\n\t"
        "v_mov_b64_e32 v[8:9], 0\n\t"
        "v_mov_b64_e32 v[10:11], 0\n\t"
        "v_mov_b64_e32 v[12:13], 0\n\t"
        "v_mov_b64_e32 v[14:15], 0\n\t"
        "s_branch LB_rdre%=w\n\t"
        "LB_rdre%=:\n\t"
        "buffer_load_dwordx4 v[16:19], v112, s[8:11], s26 offen offset:0 sc1\n\t"
        "buffer_load_dwordx4 v[20:23], v112, s[8:11], s26 offen offset:1024 sc1\n\t"
        "buffer_load_dwordx4 v[24:27], v112, s[8:11], s26 offen offset:2048 sc1\n\t"
        "buffer_load_dwordx4 v[28:31], v112, s[8:11], s26 offen offset:3072 sc1\n\t"
        "LB_rdre%=w:\n\t"
        "s_waitcnt vmcnt(0) lgkmcnt(0)\n\t"
        "v_max3_u32 v108, v16, v17, v18\n\t"
        "v_max3_u32 v108, v108, v19, v20\n\t"
        "v_max3_u32 v108, v108, v21, v22\n\t"
        "v_max3_u32 v108, v108, v23, v24\n\t"
        "v_max3_u32 v108, v108, v25, v26\n\t"
        "v_max3_u32 v108, v108, v27, v28\n\t"
        "v_max3_u32 v108, v108, v29, v30\n\t"
        "v_max_u32_e32 v108, v108, v31\n\t"
        "v_cmp_eq_u32_e32 vcc, -1, v108\n\t"
        "s_nop 0\n\t"
        "s_cbranch_vccz LB_go%=\n\t"
        "s_add_u32 s42, s42, 1\n\t"
        "s_cmp_lt_u32 s42, 0x100000\n\t"
        "s_cbranch_scc1 LB_rdre%=\n\t"
        "s_movk_i32 s34, 3\n\t"
        "s_branch LB_die%=\n\t"
        "LB_rofail%=:\n\t"
        "s_or_b32 s47, s47, 4\n\t"
        "buffer_load_dwordx4 v[64:67], v115, s[8:11], s26 offen offset:0 sc1\n\t"
        "buffer_load_dwordx4 v[68:71], v115, s[8:11], s26 offen offset:1024 sc1\n\t"
        "buffer_load_dwordx4 v[72:75], v115, s[8:11], s26 offen offset:2048 sc1\n\t"
        "buffer_load_dwordx4 v[76:79], v115, s[8:11], s26 offen offset:3072 sc1\n\t"
        "buffer_load_dwordx4 v[80:83], v115, s[8:11], s46 offen offset:0 sc1\n\t"
        "buffer_load_dwordx4 v[84:87], v115, s[8:11], s46 offen offset:1024 sc1\n\t"
        "buffer_load_dwordx4 v[88:91], v115, s[8:11], s46 offen offset:2048 sc1\n\t"
        "buffer_load_dwordx4 v[92:95], v115, s[8:11], s46 offen offset:3072 sc1\n\t"
        "s_nop 7\n\t"
        "v_mov_b64_e32 v[32:33], 0\n\t"
        "v_mov_b64_e32 v[34:35], 0\n\t"
        "v_mov_b64_e32 v[36:37], 0\n\t"
        "v_mov_b64_e32 v[38:39], 0\n\t"
        "v_mov_b64_e32 v[40:41], 0\n\t"
        "v_mov_b64_e32 v[42:43], 0\n\t"
        "v_mov_b64_e32 v[44:45], 0\n\t"
        "v_mov_b64_e32 v[46:47], 0\n\t"
        "s_branch LB_rore%=w\n\t"
        "LB_rore%=:\n\t"
        "buffer_load_dwordx4 v[64:67], v115, s[8:11], s26 offen offset:0 sc1\n\t"
        "buffer_load_dwordx4 v[68:71], v115, s[8:11], s26 offen offset:1024 sc1\n\t"
        "buffer_load_dwordx4 v[72:75], v115, s[8:11], s26 offen offset:2048 sc1\n\t"
        "buffer_load_dwordx4 v[76:79], v115, s[8:11], s26 offen offset:3072 sc1\n\t"
        "buffer_load_dwordx4 v[80:83], v115, s[8:11], s46 offen offset:0 sc1\n\t"
        "buffer_load_dwordx4 v[84:87], v115, s[8:11], s46 offen offset:1024 sc1\n\t"
        "buffer_load_dwordx4 v[88:91], v115, s[8:11], s46 offen offset:2048 sc1\n\t"
        "buffer_load_dwordx4 v[92:95], v115, s[8:11], s46 offen offset:3072 sc1\n\t"
        "LB_rore%=w:\n\t"
        "s_waitcnt vmcnt(0) lgkmcnt(0)\n\t"
        "v_max3_u32 v108, v64, v65, v66\n\t"
        "v_max3_u32 v108, v108, v67, v68\n\t"
        "v_max3_u32 v108, v108, v69, v70\n\t"
        "v_max3_u32 v108, v108, v71, v72\n\t"
        "v_max3_u32 v108, v108, v73, v74\n\t"
        "v_max3_u32 v108, v108, v75, v76\n\t"
        "v_max3_u32 v108, v108, v77, v78\n\t"
        "v_max3_u32 v108, v108, v79, v80\n\t"
        "v_max3_u32 v108, v108, v81, v82\n\t"
        "v_max3_u32 v108, v108, v83, v84\n\t"
        "v_max3_u32 v108, v108, v85, v86\n\t"
        "v_max3_u32 v108, v108, v87, v88\n\t"
        "v_max3_u32 v108, v108, v89, v90\n\t"
        "v_max3_u32 v108, v108, v91, v92\n\t"
        "v_max3_u32 v108, v108, v93, v94\n\t"
        "v_max_u32_e32 v108, v108, v95\n\t"
        "v_cmp_eq_u32_e32 vcc, -1, v108\n\t"
        "s_nop 0\n\t"
        "s_cbranch_vccz LB_go2%=\n\t"
        "s_add_u32 s42, s42, 1\n\t"
        "s_cmp_lt_u32 s42, 0x100000\n\t"
        "s_cbranch_scc1 LB_rore%=\n\t"
        "s_movk_i32 s34, 4\n\t"
        "s_branch LB_die%=\n\t"
        "LB_die%=:\n\t"
        "v_mov_b32_e32 v30, s34\n\t"
        "s_mov_b32 s35, 33024\n\t"
        "s_mov_b64 exec, 1\n\t"
        "buffer_store_dword v30, off, s[12:15], s35 sc1\n\t"
        "s_endpgm\n\t"
        ".Lk_end_B:\n\t"
        :: REC_S_IN, "{v96}"(g_v), "{v97}"(g_o), "{v98}"(wb0), "{v99}"(wb1), "{v100}"(rb0), "{v101}"(rb0 ^ 32u), "{v102}"(rb0 ^ 64u), "{v103}"(rb0 ^ 96u),
           "{v106}"((l & 1) ? 0xEEEEEEEEu : 0x44444444u), "{v[108:109]}"(m0p),
           "{v112}"(a_mem), "{v113}"((24 + cp) * 16384 + l * 32), "{v114}"(a_vmw), "{v115}"(a_or), "{v116}"(a_mw), "{v117}"(a_sw),
           "{v118}"(lb + w * 2048 + (g >> 1) * 256 + n * 16 + (g & 1) * 8), "{v119}"(lb + (w & 1) * 1024 + l * 16), "{v120}"(lb + LB_R + w * 512 + l * 4),
           "{v121}"(lb + LB_R + l * 8), "{v122}"(stg + l * 16), "{v123}"(o_r), "{v124}"(rb + w == 32 ? 28 * 16384 + (l & 31) * 128 : OOB)
        : "memory", "vcc", "scc", "v0", "v255", "a0", "a255", "s101");
    __builtin_unreachable();
}
__device__ __forceinline__ void rec_helper(const Params& p, rsrc_t r, unsigned lb, const int wv, const int hi) {
    const int tid = otid(), l = tid & 63, xq = hi >> 3;
    const rsrc_t rh = __builtin_amdgcn_make_buffer_rsrc(p.ws + ho_base(p.ws), 0, HO_BYTES, 0x00020000);
    const v4u ones = {0xFFFFFFFFu, 0xFFFFFFFFu, 0xFFFFFFFFu, 0xFFFFFFFFu};
    const bool meas = (hi & 7) < 2;
    if (meas) {
        const unsigned ca = l >= 8 ? OOB : (31 + wv * 32) * 16384 + hi * 256 + l * 16, tend = rtnow() + CAL_T;
        unsigned best0 = ~0u, best1 = ~0u;
#pragma unroll 1
        for (int i = 0; i < 64 && (i < 3 || (int)(rtnow() - tend) < 0); ++i) {
            const unsigned t0 = (unsigned)__builtin_amdgcn_s_memtime(); __builtin_amdgcn_s_waitcnt(0);
            st16(rh, ca, ones); DRAIN();
            const unsigned t1 = (unsigned)__builtin_amdgcn_s_memtime(); __builtin_amdgcn_s_waitcnt(0);
            st16(rh, ca + 8192, ones); DRAIN();
            const unsigned t2 = (unsigned)__builtin_amdgcn_s_memtime(); __builtin_amdgcn_s_waitcnt(0);
            best0 = min(best0, t1 - t0); best1 = min(best1, t2 - t1); }
        if (l < 2) __hip_atomic_fetch_max((unsigned*)(p.ws + F_CAL + 128) + l, 65535u - min(l ? best1 : best0, 65535u), __ATOMIC_RELAXED, __HIP_MEMORY_SCOPE_AGENT);
    }
    auto touch = [&](int z0, int z1) {
#pragma unroll 1
        for (int z = z0 + ((hi & 7) - 2) * 4 + wv; z < z1; z += 24) { const bf16_t* q;
            if (z < 48) q = ((z & 1) ? p.Wk : p.Wq) + (size_t)(((((z & 1) * 2 + 1) * 4 + 4 + (z >> 2)) & 15) * 64 + l) * 4096 + (xq * 8 + 1 + (z & 2) * 2) * 128 + 64;
            else if (z < 112) { const int y = z - 48; q = p.Wo + (size_t)(((y & 3) * 8 + (y >> 3)) * 128 + ((y >> 2) & 1) * 64 + l) * 1024 + (xq & 1) * 512 + 192; }
            else { const int s7 = (z - 112) % 7, y = (z - 112) / 7; q = p.Wo + (size_t)(((y & 3) * 8 + 8 - T2U + (y >> 3)) * 128 + ((y >> 2) & 1) * 64 + l) * 1024 + (xq & 1) * 512 + (s7 + (s7 >= 3)) * 64; }
            touch_l2(q, lb + 1024); } };
    if (!meas) touch(0, TPRE);
    st16(r, tid < 64 ? F_KVQ + hi * 1024 + tid * 16 : tid < 72 ? F_PROG + hi * 128 + (tid - 64) * 16 : tid < 80 ? F_CVY + hi * 128 + (tid - 72) * 16 : OOB, (v4u){0u, 0u, 0u, 0u});
#pragma unroll
    for (int i = 0; i < 2; ++i) st16(r, W_OX + 63 * 262144 + hi * 8192 + i * 4096 + tid * 16, ones);
#pragma unroll
    for (int i = 0; i < 16; ++i) st16(rh, hi * 65536 + i * 4096 + tid * 16, ones);
    DRAIN();
    __syncthreads();
    if (tid == 0 && __hip_atomic_fetch_add((unsigned*)(p.ws + F_CAL + 256), 1u, __ATOMIC_RELAXED, __HIP_MEMORY_SCOPE_AGENT) == 31u) {
        const v2u c = ld8(r, F_CAL + 128); st4(r, F_CAL, c.x < c.y ? 8193u : 1u); }
    if (!meas) touch(TPRE, 112 + 56 * T2U);
    DRAIN();
}
constexpr int CV_RP = 80;
constexpr int CV_U = 96 * CV_RP;
constexpr int CV_KP = 4416;
constexpr int CV_K0 = 8 * CV_U;
#define NAP() do { __builtin_amdgcn_sched_barrier(0); __builtin_amdgcn_s_sleep(XSLP); __builtin_amdgcn_sched_barrier(0); } while (0)
__device__ __forceinline__ void gate_block(const Params& p, rsrc_t r, const int hi, const int wv) {
    constexpr int NT = 4;
    const int l = otid() & 63, t0 = (hi * 4 + wv) * NT;
    v4u xg[NT][2], gw[2];
#pragma unroll
    for (int q = 0; q < 2; ++q) gw[q] = *(const v4u*)(p.gate_w + q * 512 + l * 8);
#pragma unroll
    for (int i = 0; i < NT; ++i)
#pragma unroll
        for (int q = 0; q < 2; ++q) { xg[i][q] = *(const v4u*)(p.Aq + (size_t)(t0 + i) * D + q * 512 + l * 8); NAP(); }
    float mine = 0.f;
#pragma unroll
    for (int i = 0; i < NT; ++i) { float acc = 0.f;
#pragma unroll
        for (int q = 0; q < 2; ++q)
#pragma unroll
            for (int e = 0; e < 4; ++e) { const unsigned pr = pack2(blo(xg[i][q][e]) * blo(gw[q][e]), bhi(xg[i][q][e]) * bhi(gw[q][e])); acc += blo(pr); acc += bhi(pr); }
        for (int o = 32; o > 0; o >>= 1) acc += __shfl_xor(acc, o);
        if ((l & 3) == i) mine = acc; }
    if (l < 32) { const float z = rbfh(mine); st4(r, W_GATE + t0 * 32 + l * 4, __float_as_uint(rbfh(1.f / (1.f + expf(-z))))); }
}
__device__ __forceinline__ void conv_prep(const Params& p, rsrc_t r, const int wv, const int hi, const int G) {
    const int tid = otid(), Q = tid >> 2, cg = tid & 3, ch0 = hi * 32 + cg * 8;
    v4u sk[4]; float sv[8][4];
#pragma unroll
    for (int q = 0; q < 4; ++q) sk[q] = *(const v4u*)(p.shift_k + ch0 * 4 + q * 8);
#pragma unroll
    for (int c8 = 0; c8 < 8; ++c8)
#pragma unroll
        for (int tp = 0; tp < 4; ++tp) { const unsigned sw = sk[c8 >> 1][(c8 & 1) * 2 + (tp >> 1)]; sv[c8][tp] = (tp & 1) ? bhi(sw) : blo(sw); }
    const rsrc_t rk = __builtin_amdgcn_make_buffer_rsrc((void*)p.Ak, 0, S * D * 2, 0x00020000), rv = __builtin_amdgcn_make_buffer_rsrc((void*)p.Av, 0, S * D * 2, 0x00020000),
                 rq = __builtin_amdgcn_make_buffer_rsrc((void*)p.Aq, 0, S * D * 2, 0x00020000);
    {
        const unsigned ro = (unsigned)((Q * 32 + G * 8) * D + ch0) * 2u, uo = hi * 131072 + tid * 16;
        v4u kr[11], u[8], qv[8];
#pragma unroll
        for (int i = 0; i < 11; ++i) { kr[i] = __builtin_amdgcn_raw_buffer_load_b128(rk, (i < 3 && Q + G == 0) ? OOB : ro + (i - 3) * 2048, 0, 0); NAP(); }
#pragma unroll
        for (int i = 0; i < 8; ++i) { u[i] = __builtin_amdgcn_raw_buffer_load_b128(rv, ro + i * 2048, 0, 0); NAP(); }
#pragma unroll
        for (int i = 0; i < 8; ++i) { qv[i] = __builtin_amdgcn_raw_buffer_load_b128(rq, ro + i * 2048, 0, 0); NAP(); }
#pragma unroll
        for (int q = 0; q < 4; ++q) {
#pragma unroll
            for (int i = 0; i < 8; ++i) {
                unsigned ks = pack2(blo(kr[i][q]) * sv[2 * q][3], bhi(kr[i][q]) * sv[2 * q + 1][3]), pr;
#pragma unroll
                for (int d = 1; d < 4; ++d) { pr = pack2(blo(kr[i + d][q]) * sv[2 * q][3 - d], bhi(kr[i + d][q]) * sv[2 * q + 1][3 - d]); ks = pack2(blo(ks) + blo(pr), bhi(ks) + bhi(pr)); }
                u[i][q] = pack2(blo(ks) * blo(u[i][q]), bhi(ks) * bhi(u[i][q])); }
            v4u lo, hh;
#pragma unroll
            for (int z = 0; z < 4; ++z) { const unsigned x = u[7 - 2 * z][q], y = u[6 - 2 * z][q]; lo[z] = __builtin_amdgcn_perm(y, x, 0x05040100u); hh[z] = __builtin_amdgcn_perm(y, x, 0x07060302u); }
            st16(r, W_U + uo + q * 32768 + G * 4096, lo); NAP(); st16(r, W_U + uo + q * 32768 + 16384 + G * 4096, hh); NAP();
#pragma unroll
            for (int z = 0; z < 2; ++z) { st16(r, W_Y + uo + q * 32768 + (2 * G + z) * 4096, (v4u){qv[4 * z][q], qv[4 * z + 1][q], qv[4 * z + 2][q], qv[4 * z + 3][q]}); NAP(); }
        }
    }
    DRAIN(); __syncthreads();
    if (tid == 0) __hip_atomic_fetch_add((unsigned*)(p.ws + F_CONV), 1u, __ATOMIC_RELAXED, __HIP_MEMORY_SCOPE_AGENT);
    if (tid == 0) __hip_atomic_fetch_add((unsigned*)(p.ws + F_CVY + hi * 128 + 64), 1u, __ATOMIC_RELAXED, __HIP_MEMORY_SCOPE_AGENT);
}
__device__ __forceinline__ bool conv_core(const Params& p, rsrc_t r, unsigned char* lds, const int wv, const int un) {
    const int tid = otid(), w = wv, l = tid & 63, Q = tid >> 2, cg = tid & 3;
    const rsrc_t rs = __builtin_amdgcn_make_buffer_rsrc((void*)p.ssm_k, 0, S * D * 2, 0x00020000);
    v4u kraw[4]; v2u kext[4];
    auto kload = [&](int n) { const unsigned o = (unsigned)(((un >> 2) * 32 + w * 8 + (un & 3) * 2 + n) * S + l * 8) * 2u;
#pragma unroll
        for (int i = 0; i < 4; ++i) { kraw[i] = __builtin_amdgcn_raw_buffer_load_b128(rs, o + i * 1024, 0, 2);
            kext[i] = __builtin_amdgcn_raw_buffer_load_b64(rs, (i == 3 && l == 63) ? OOB : o + i * 1024 + 16, 0, 0); } };
    kload(0);
    if (!wait_word(r, F_CVY + (un >> 2) * 128 + 64, 4u, 2048u)) return false;
    v4u ur[8], qr[8];
#pragma unroll
    for (int i = 0; i < 8; ++i) ur[i] = ld16(r, W_U + un * 32768 + i * 4096 + tid * 16);
#pragma unroll
    for (int i = 0; i < 8; ++i) qr[i] = ld16(r, W_Y + un * 32768 + i * 4096 + tid * 16);
    const unsigned dv = *(const unsigned*)(p.ssm_D + (un >> 2) * 32 + cg * 8 + (un & 3) * 2);
    for (int i = tid; i < 8 * 160; i += 256) *(v4u*)(lds + (i / 160) * CV_U + (i % 160) * 16) = (v4u){0u, 0u, 0u, 0u};
    unsigned char* kc = lds + CV_K0 + w * 4 * CV_KP;
    if (l < 32) {
#pragma unroll
        for (int q = 0; q < 4; ++q) *(unsigned*)(kc + q * CV_KP + l * 4) = 0u; }
    const int b = l & 31, kg = l >> 5;
    const unsigned kb = (unsigned)(size_t)kc + (b & 3) * CV_KP + ((b & 28) - 8 * kg + 56 - 16) * 2, ub0 = (unsigned)(size_t)lds + (1 + b) * CV_RP + kg * 16;
    unsigned char* const urow = lds + 2 * cg * CV_U + (32 + Q) * CV_RP;
#pragma unroll
    for (int i = 0; i < 8; ++i) *(v4u*)(urow + (i >> 2) * CV_U + (i & 3) * 16) = ur[i];
    __syncthreads();
#pragma unroll 1
        for (int ci = 0; ci < 2; ++ci) {
            const int c8 = 2 * w + ci;
#pragma unroll
            for (int i = 0; i < 4; ++i) { const v4u e = kraw[i]; const v2u f = kext[i]; unsigned char* q = kc + 128 + (i * 64 + l) * 16;
                const unsigned s0 = __builtin_amdgcn_alignbit(e.y, e.x, 16), s1 = __builtin_amdgcn_alignbit(e.z, e.y, 16), s2 = __builtin_amdgcn_alignbit(e.w, e.z, 16),
                               s3 = __builtin_amdgcn_alignbit(f.x, e.w, 16), s4 = __builtin_amdgcn_alignbit(f.y, f.x, 16);
                *(v4u*)q = (v4u){s0, s1, s2, s3}; *(v4u*)(q + CV_KP) = (v4u){e.y, e.z, e.w, f.x}; *(v4u*)(q + 2 * CV_KP) = (v4u){s1, s2, s3, s4}; *(v4u*)(q + 3 * CV_KP) = (v4u){e.z, e.w, f.x, f.y};
                if (i == 0 && l == 0) {
                    *(v2u*)(q - 8) = (v2u){0u, e.x << 16}; *(v2u*)(q + CV_KP - 8) = (v2u){0u, e.x}; *(v2u*)(q + 2 * CV_KP - 8) = (v2u){e.x << 16, s0}; *(v2u*)(q + 3 * CV_KP - 8) = (v2u){e.x, e.y}; } }
            if (ci == 0) kload(1);
            __builtin_amdgcn_wave_barrier();
            f32x16 acc0, acc1;
            asm volatile(
                "ds_read_b64 v[192:193], %[kb] offset:32\n\t"
                "ds_read_b64 v[194:195], %[kb] offset:40\n\t"
                "ds_read_b128 v[196:199], %[ub] offset:5040\n\t"
                "ds_read_b128 v[200:203], %[ub] offset:2480\n\t"
                "ds_read_b64 v[204:205], %[kb] offset:2080\n\t"
                "ds_read_b64 v[206:207], %[kb] offset:2088\n\t"
                "ds_read_b64 v[208:209], %[kb] offset:0\n\t"
                "ds_read_b64 v[210:211], %[kb] offset:8\n\t"
                "ds_read_b128 v[212:215], %[ub] offset:5072\n\t"
                "ds_read_b128 v[216:219], %[ub] offset:2512\n\t"
                "ds_read_b64 v[220:221], %[kb] offset:2048\n\t"
                "ds_read_b64 v[222:223], %[kb] offset:2056\n\t"
                "s_waitcnt lgkmcnt(9)\n\t"
                "v_mfma_f32_32x32x16_bf16 %[a1], v[192:195], v[196:199], 0\n\t"
                "ds_read_b64 v[224:225], %[kb] offset:96\n\t"
                "ds_read_b64 v[226:227], %[kb] offset:104\n\t"
                "ds_read_b128 v[228:231], %[ub] offset:4960\n\t"
                "s_waitcnt lgkmcnt(11)\n\t"
                "v_mfma_f32_32x32x16_bf16 %[a0], v[192:195], v[200:203], 0\n\t"
                "ds_read_b128 v[232:235], %[ub] offset:2400\n\t"
                "s_waitcnt lgkmcnt(10)\n\t"
                "v_mfma_f32_32x32x16_bf16 %[a1], v[204:207], v[200:203], %[a1]\n\t"
                "ds_read_b64 v[236:237], %[kb] offset:2144\n\t"
                "ds_read_b64 v[238:239], %[kb] offset:2152\n\t"
                "s_waitcnt lgkmcnt(9)\n\t"
                "v_mfma_f32_32x32x16_bf16 %[a1], v[208:211], v[212:215], %[a1]\n\t"
                "ds_read_b64 v[240:241], %[kb] offset:64\n\t"
                "ds_read_b64 v[242:243], %[kb] offset:72\n\t"
                "ds_read_b128 v[244:247], %[ub] offset:4992\n\t"
                "s_waitcnt lgkmcnt(11)\n\t"
                "v_mfma_f32_32x32x16_bf16 %[a0], v[208:211], v[216:219], %[a0]\n\t"
                "ds_read_b128 v[248:251], %[ub] offset:2432\n\t"
                "s_waitcnt lgkmcnt(10)\n\t"
                "v_mfma_f32_32x32x16_bf16 %[a1], v[220:223], v[216:219], %[a1]\n\t"
                "ds_read_b64 v[252:253], %[kb] offset:2112\n\t"
                "ds_read_b64 v[254:255], %[kb] offset:2120\n\t"
                "s_waitcnt lgkmcnt(9)\n\t"
                "v_mfma_f32_32x32x16_bf16 %[a1], v[224:227], v[228:231], %[a1]\n\t"
                "ds_read_b64 v[192:193], %[kb] offset:160\n\t"
                "ds_read_b64 v[194:195], %[kb] offset:168\n\t"
                "ds_read_b128 v[196:199], %[ub] offset:4880\n\t"
                "s_waitcnt lgkmcnt(11)\n\t"
                "v_mfma_f32_32x32x16_bf16 %[a0], v[224:227], v[232:235], %[a0]\n\t"
                "ds_read_b128 v[200:203], %[ub] offset:2320\n\t"
                "s_waitcnt lgkmcnt(10)\n\t"
                "v_mfma_f32_32x32x16_bf16 %[a1], v[236:239], v[232:235], %[a1]\n\t"
                "ds_read_b64 v[204:205], %[kb] offset:2208\n\t"
                "ds_read_b64 v[206:207], %[kb] offset:2216\n\t"
                "s_waitcnt lgkmcnt(9)\n\t"
                "v_mfma_f32_32x32x16_bf16 %[a1], v[240:243], v[244:247], %[a1]\n\t"
                "ds_read_b64 v[208:209], %[kb] offset:128\n\t"
                "ds_read_b64 v[210:211], %[kb] offset:136\n\t"
                "ds_read_b128 v[212:215], %[ub] offset:4912\n\t"
                "s_waitcnt lgkmcnt(11)\n\t"
                "v_mfma_f32_32x32x16_bf16 %[a0], v[240:243], v[248:251], %[a0]\n\t"
                "ds_read_b128 v[216:219], %[ub] offset:2352\n\t"
                "s_waitcnt lgkmcnt(10)\n\t"
                "v_mfma_f32_32x32x16_bf16 %[a1], v[252:255], v[248:251], %[a1]\n\t"
                "ds_read_b64 v[220:221], %[kb] offset:2176\n\t"
                "ds_read_b64 v[222:223], %[kb] offset:2184\n\t"
                "s_waitcnt lgkmcnt(9)\n\t"
                "v_mfma_f32_32x32x16_bf16 %[a1], v[192:195], v[196:199], %[a1]\n\t"
                "ds_read_b64 v[224:225], %[kb] offset:224\n\t"
                "ds_read_b64 v[226:227], %[kb] offset:232\n\t"
                "ds_read_b128 v[228:231], %[ub] offset:4800\n\t"
                "s_waitcnt lgkmcnt(11)\n\t"
                "v_mfma_f32_32x32x16_bf16 %[a0], v[192:195], v[200:203], %[a0]\n\t"
                "ds_read_b128 v[232:235], %[ub] offset:2240\n\t"
                "s_waitcnt lgkmcnt(10)\n\t"
                "v_mfma_f32_32x32x16_bf16 %[a1], v[204:207], v[200:203], %[a1]\n\t"
                "ds_read_b64 v[236:237], %[kb] offset:2272\n\t"
                "ds_read_b64 v[238:239], %[kb] offset:2280\n\t"
                "s_waitcnt lgkmcnt(9)\n\t"
                "v_mfma_f32_32x32x16_bf16 %[a1], v[208:211], v[212:215], %[a1]\n\t"
                "ds_read_b64 v[240:241], %[kb] offset:192\n\t"
                "ds_read_b64 v[242:243], %[kb] offset:200\n\t"
                "ds_read_b128 v[244:247], %[ub] offset:4832\n\t"
                "s_waitcnt lgkmcnt(11)\n\t"
                "v_mfma_f32_32x32x16_bf16 %[a0], v[208:211], v[216:219], %[a0]\n\t"
                "ds_read_b128 v[248:251], %[ub] offset:2272\n\t"
                "s_waitcnt lgkmcnt(10)\n\t"
                "v_mfma_f32_32x32x16_bf16 %[a1], v[220:223], v[216:219], %[a1]\n\t"
                "ds_read_b64 v[252:253], %[kb] offset:2240\n\t"
                "ds_read_b64 v[254:255], %[kb] offset:2248\n\t"
                "s_waitcnt lgkmcnt(9)\n\t"
                "v_mfma_f32_32x32x16_bf16 %[a1], v[224:227], v[228:231], %[a1]\n\t"
                "ds_read_b64 v[192:193], %[kb] offset:288\n\t"
                "ds_read_b64 v[194:195], %[kb] offset:296\n\t"
                "ds_read_b128 v[196:199], %[ub] offset:4720\n\t"
                "s_waitcnt lgkmcnt(11)\n\t"
                "v_mfma_f32_32x32x16_bf16 %[a0], v[224:227], v[232:235], %[a0]\n\t"
                "ds_read_b128 v[200:203], %[ub] offset:2160\n\t"
                "s_waitcnt lgkmcnt(10)\n\t"
                "v_mfma_f32_32x32x16_bf16 %[a1], v[236:239], v[232:235], %[a1]\n\t"
                "ds_read_b64 v[204:205], %[kb] offset:2336\n\t"
                "ds_read_b64 v[206:207], %[kb] offset:2344\n\t"
                "s_waitcnt lgkmcnt(9)\n\t"
                "v_mfma_f32_32x32x16_bf16 %[a1], v[240:243], v[244:247], %[a1]\n\t"
                "ds_read_b64 v[208:209], %[kb] offset:256\n\t"
                "ds_read_b64 v[210:211], %[kb] offset:264\n\t"
                "ds_read_b128 v[212:215], %[ub] offset:4752\n\t"
                "s_waitcnt lgkmcnt(11)\n\t"
                "v_mfma_f32_32x32x16_bf16 %[a0], v[240:243], v[248:251], %[a0]\n\t"
                "ds_read_b128 v[216:219], %[ub] offset:2192\n\t"
                "s_waitcnt lgkmcnt(10)\n\t"
                "v_mfma_f32_32x32x16_bf16 %[a1], v[252:255], v[248:251], %[a1]\n\t"
                "ds_read_b64 v[220:221], %[kb] offset:2304\n\t"
                "ds_read_b64 v[222:223], %[kb] offset:2312\n\t"
                "s_waitcnt lgkmcnt(9)\n\t"
                "v_mfma_f32_32x32x16_bf16 %[a1], v[192:195], v[196:199], %[a1]\n\t"
                "ds_read_b64 v[224:225], %[kb] offset:352\n\t"
                "ds_read_b64 v[226:227], %[kb] offset:360\n\t"
                "ds_read_b128 v[228:231], %[ub] offset:4640\n\t"
                "s_waitcnt lgkmcnt(11)\n\t"
                "v_mfma_f32_32x32x16_bf16 %[a0], v[192:195], v[200:203], %[a0]\n\t"
                "ds_read_b128 v[232:235], %[ub] offset:2080\n\t"
                "s_waitcnt lgkmcnt(10)\n\t"
                "v_mfma_f32_32x32x16_bf16 %[a1], v[204:207], v[200:203], %[a1]\n\t"
                "ds_read_b64 v[236:237], %[kb] offset:2400\n\t"
                "ds_read_b64 v[238:239], %[kb] offset:2408\n\t"
                "s_waitcnt lgkmcnt(9)\n\t"
                "v_mfma_f32_32x32x16_bf16 %[a1], v[208:211], v[212:215], %[a1]\n\t"
                "ds_read_b64 v[240:241], %[kb] offset:320\n\t"
                "ds_read_b64 v[242:243], %[kb] offset:328\n\t"
                "ds_read_b128 v[244:247], %[ub] offset:4672\n\t"
                "s_waitcnt lgkmcnt(11)\n\t"
                "v_mfma_f32_32x32x16_bf16 %[a0], v[208:211], v[216:219], %[a0]\n\t"
                "ds_read_b128 v[248:251], %[ub] offset:2112\n\t"
                "s_waitcnt lgkmcnt(10)\n\t"
                "v_mfma_f32_32x32x16_bf16 %[a1], v[220:223], v[216:219], %[a1]\n\t"
                "ds_read_b64 v[252:253], %[kb] offset:2368\n\t"
                "ds_read_b64 v[254:255], %[kb] offset:2376\n\t"
                "s_waitcnt lgkmcnt(9)\n\t"
                "v_mfma_f32_32x32x16_bf16 %[a1], v[224:227], v[228:231], %[a1]\n\t"
                "ds_read_b64 v[192:193], %[kb] offset:416\n\t"
                "ds_read_b64 v[194:195], %[kb] offset:424\n\t"
                "ds_read_b128 v[196:199], %[ub] offset:4560\n\t"
                "s_waitcnt lgkmcnt(11)\n\t"
                "v_mfma_f32_32x32x16_bf16 %[a0], v[224:227], v[232:235], %[a0]\n\t"
                "ds_read_b128 v[200:203], %[ub] offset:2000\n\t"
                "s_waitcnt lgkmcnt(10)\n\t"
                "v_mfma_f32_32x32x16_bf16 %[a1], v[236:239], v[232:235], %[a1]\n\t"
                "ds_read_b64 v[204:205], %[kb] offset:2464\n\t"
                "ds_read_b64 v[206:207], %[kb] offset:2472\n\t"
                "s_waitcnt lgkmcnt(9)\n\t"
                "v_mfma_f32_32x32x16_bf16 %[a1], v[240:243], v[244:247], %[a1]\n\t"
                "ds_read_b64 v[208:209], %[kb] offset:384\n\t"
                "ds_read_b64 v[210:211], %[kb] offset:392\n\t"
                "ds_read_b128 v[212:215], %[ub] offset:4592\n\t"
                "s_waitcnt lgkmcnt(11)\n\t"
                "v_mfma_f32_32x32x16_bf16 %[a0], v[240:243], v[248:251], %[a0]\n\t"
                "ds_read_b128 v[216:219], %[ub] offset:2032\n\t"
                "s_waitcnt lgkmcnt(10)\n\t"
                "v_mfma_f32_32x32x16_bf16 %[a1], v[252:255], v[248:251], %[a1]\n\t"
                "ds_read_b64 v[220:221], %[kb] offset:2432\n\t"
                "ds_read_b64 v[222:223], %[kb] offset:2440\n\t"
                "s_waitcnt lgkmcnt(9)\n\t"
                "v_mfma_f32_32x32x16_bf16 %[a1], v[192:195], v[196:199], %[a1]\n\t"
                "ds_read_b64 v[224:225], %[kb] offset:480\n\t"
                "ds_read_b64 v[226:227], %[kb] offset:488\n\t"
                "ds_read_b128 v[228:231], %[ub] offset:4480\n\t"
                "s_waitcnt lgkmcnt(11)\n\t"
                "v_mfma_f32_32x32x16_bf16 %[a0], v[192:195], v[200:203], %[a0]\n\t"
                "ds_read_b128 v[232:235], %[ub] offset:1920\n\t"
                "s_waitcnt lgkmcnt(10)\n\t"
                "v_mfma_f32_32x32x16_bf16 %[a1], v[204:207], v[200:203], %[a1]\n\t"
                "ds_read_b64 v[236:237], %[kb] offset:2528\n\t"
                "ds_read_b64 v[238:239], %[kb] offset:2536\n\t"
                "s_waitcnt lgkmcnt(9)\n\t"
                "v_mfma_f32_32x32x16_bf16 %[a1], v[208:211], v[212:215], %[a1]\n\t"
                "ds_read_b64 v[240:241], %[kb] offset:448\n\t"
                "ds_read_b64 v[242:243], %[kb] offset:456\n\t"
                "ds_read_b128 v[244:247], %[ub] offset:4512\n\t"
                "s_waitcnt lgkmcnt(11)\n\t"
                "v_mfma_f32_32x32x16_bf16 %[a0], v[208:211], v[216:219], %[a0]\n\t"
                "ds_read_b128 v[248:251], %[ub] offset:1952\n\t"
                "s_waitcnt lgkmcnt(10)\n\t"
                "v_mfma_f32_32x32x16_bf16 %[a1], v[220:223], v[216:219], %[a1]\n\t"
                "ds_read_b64 v[252:253], %[kb] offset:2496\n\t"
                "ds_read_b64 v[254:255], %[kb] offset:2504\n\t"
                "s_waitcnt lgkmcnt(9)\n\t"
                "v_mfma_f32_32x32x16_bf16 %[a1], v[224:227], v[228:231], %[a1]\n\t"
                "ds_read_b64 v[192:193], %[kb] offset:544\n\t"
                "ds_read_b64 v[194:195], %[kb] offset:552\n\t"
                "ds_read_b128 v[196:199], %[ub] offset:4400\n\t"
                "s_waitcnt lgkmcnt(11)\n\t"
                "v_mfma_f32_32x32x16_bf16 %[a0], v[224:227], v[232:235], %[a0]\n\t"
                "ds_read_b128 v[200:203], %[ub] offset:1840\n\t"
                "s_waitcnt lgkmcnt(10)\n\t"
                "v_mfma_f32_32x32x16_bf16 %[a1], v[236:239], v[232:235], %[a1]\n\t"
                "ds_read_b64 v[204:205], %[kb] offset:2592\n\t"
                "ds_read_b64 v[206:207], %[kb] offset:2600\n\t"
                "s_waitcnt lgkmcnt(9)\n\t"
                "v_mfma_f32_32x32x16_bf16 %[a1], v[240:243], v[244:247], %[a1]\n\t"
                "ds_read_b64 v[208:209], %[kb] offset:512\n\t"
                "ds_read_b64 v[210:211], %[kb] offset:520\n\t"
                "ds_read_b128 v[212:215], %[ub] offset:4432\n\t"
                "s_waitcnt lgkmcnt(11)\n\t"
                "v_mfma_f32_32x32x16_bf16 %[a0], v[240:243], v[248:251], %[a0]\n\t"
                "ds_read_b128 v[216:219], %[ub] offset:1872\n\t"
                "s_waitcnt lgkmcnt(10)\n\t"
                "v_mfma_f32_32x32x16_bf16 %[a1], v[252:255], v[248:251], %[a1]\n\t"
                "ds_read_b64 v[220:221], %[kb] offset:2560\n\t"
                "ds_read_b64 v[222:223], %[kb] offset:2568\n\t"
                "s_waitcnt lgkmcnt(9)\n\t"
                "v_mfma_f32_32x32x16_bf16 %[a1], v[192:195], v[196:199], %[a1]\n\t"
                "ds_read_b64 v[224:225], %[kb] offset:608\n\t"
                "ds_read_b64 v[226:227], %[kb] offset:616\n\t"
                "ds_read_b128 v[228:231], %[ub] offset:4320\n\t"
                "s_waitcnt lgkmcnt(11)\n\t"
                "v_mfma_f32_32x32x16_bf16 %[a0], v[192:195], v[200:203], %[a0]\n\t"
                "ds_read_b128 v[232:235], %[ub] offset:1760\n\t"
                "s_waitcnt lgkmcnt(10)\n\t"
                "v_mfma_f32_32x32x16_bf16 %[a1], v[204:207], v[200:203], %[a1]\n\t"
                "ds_read_b64 v[236:237], %[kb] offset:2656\n\t"
                "ds_read_b64 v[238:239], %[kb] offset:2664\n\t"
                "s_waitcnt lgkmcnt(9)\n\t"
                "v_mfma_f32_32x32x16_bf16 %[a1], v[208:211], v[212:215], %[a1]\n\t"
                "ds_read_b64 v[240:241], %[kb] offset:576\n\t"
                "ds_read_b64 v[242:243], %[kb] offset:584\n\t"
                "ds_read_b128 v[244:247], %[ub] offset:4352\n\t"
                "s_waitcnt lgkmcnt(11)\n\t"
                "v_mfma_f32_32x32x16_bf16 %[a0], v[208:211], v[216:219], %[a0]\n\t"
                "ds_read_b128 v[248:251], %[ub] offset:1792\n\t"
                "s_waitcnt lgkmcnt(10)\n\t"
                "v_mfma_f32_32x32x16_bf16 %[a1], v[220:223], v[216:219], %[a1]\n\t"
                "ds_read_b64 v[252:253], %[kb] offset:2624\n\t"
                "ds_read_b64 v[254:255], %[kb] offset:2632\n\t"
                "s_waitcnt lgkmcnt(9)\n\t"
                "v_mfma_f32_32x32x16_bf16 %[a1], v[224:227], v[228:231], %[a1]\n\t"
                "ds_read_b64 v[192:193], %[kb] offset:672\n\t"
                "ds_read_b64 v[194:195], %[kb] offset:680\n\t"
                "ds_read_b128 v[196:199], %[ub] offset:4240\n\t"
                "s_waitcnt lgkmcnt(11)\n\t"
                "v_mfma_f32_32x32x16_bf16 %[a0], v[224:227], v[232:235], %[a0]\n\t"
                "ds_read_b128 v[200:203], %[ub] offset:1680\n\t"
                "s_waitcnt lgkmcnt(10)\n\t"
                "v_mfma_f32_32x32x16_bf16 %[a1], v[236:239], v[232:235], %[a1]\n\t"
                "ds_read_b64 v[204:205], %[kb] offset:2720\n\t"
                "ds_read_b64 v[206:207], %[kb] offset:2728\n\t"
                "s_waitcnt lgkmcnt(9)\n\t"
                "v_mfma_f32_32x32x16_bf16 %[a1], v[240:243], v[244:247], %[a1]\n\t"
                "ds_read_b64 v[208:209], %[kb] offset:640\n\t"
                "ds_read_b64 v[210:211], %[kb] offset:648\n\t"
                "ds_read_b128 v[212:215], %[ub] offset:4272\n\t"
                "s_waitcnt lgkmcnt(11)\n\t"
                "v_mfma_f32_32x32x16_bf16 %[a0], v[240:243], v[248:251], %[a0]\n\t"
                "ds_read_b128 v[216:219], %[ub] offset:1712\n\t"
                "s_waitcnt lgkmcnt(10)\n\t"
                "v_mfma_f32_32x32x16_bf16 %[a1], v[252:255], v[248:251], %[a1]\n\t"
                "ds_read_b64 v[220:221], %[kb] offset:2688\n\t"
                "ds_read_b64 v[222:223], %[kb] offset:2696\n\t"
                "s_waitcnt lgkmcnt(9)\n\t"
                "v_mfma_f32_32x32x16_bf16 %[a1], v[192:195], v[196:199], %[a1]\n\t"
                "ds_read_b64 v[224:225], %[kb] offset:736\n\t"
                "ds_read_b64 v[226:227], %[kb] offset:744\n\t"
                "ds_read_b128 v[228:231], %[ub] offset:4160\n\t"
                "s_waitcnt lgkmcnt(11)\n\t"
                "v_mfma_f32_32x32x16_bf16 %[a0], v[192:195], v[200:203], %[a0]\n\t"
                "ds_read_b128 v[232:235], %[ub] offset:1600\n\t"
                "s_waitcnt lgkmcnt(10)\n\t"
                "v_mfma_f32_32x32x16_bf16 %[a1], v[204:207], v[200:203], %[a1]\n\t"
                "ds_read_b64 v[236:237], %[kb] offset:2784\n\t"
                "ds_read_b64 v[238:239], %[kb] offset:2792\n\t"
                "s_waitcnt lgkmcnt(9)\n\t"
                "v_mfma_f32_32x32x16_bf16 %[a1], v[208:211], v[212:215], %[a1]\n\t"
                "ds_read_b64 v[240:241], %[kb] offset:704\n\t"
                "ds_read_b64 v[242:243], %[kb] offset:712\n\t"
                "ds_read_b128 v[244:247], %[ub] offset:4192\n\t"
                "s_waitcnt lgkmcnt(11)\n\t"
                "v_mfma_f32_32x32x16_bf16 %[a0], v[208:211], v[216:219], %[a0]\n\t"
                "ds_read_b128 v[248:251], %[ub] offset:1632\n\t"
                "s_waitcnt lgkmcnt(10)\n\t"
                "v_mfma_f32_32x32x16_bf16 %[a1], v[220:223], v[216:219], %[a1]\n\t"
                "ds_read_b64 v[252:253], %[kb] offset:2752\n\t"
                "ds_read_b64 v[254:255], %[kb] offset:2760\n\t"
                "s_waitcnt lgkmcnt(9)\n\t"
                "v_mfma_f32_32x32x16_bf16 %[a1], v[224:227], v[228:231], %[a1]\n\t"
                "ds_read_b64 v[192:193], %[kb] offset:800\n\t"
                "ds_read_b64 v[194:195], %[kb] offset:808\n\t"
                "ds_read_b128 v[196:199], %[ub] offset:4080\n\t"
                "s_waitcnt lgkmcnt(11)\n\t"
                "v_mfma_f32_32x32x16_bf16 %[a0], v[224:227], v[232:235], %[a0]\n\t"
                "ds_read_b128 v[200:203], %[ub] offset:1520\n\t"
                "s_waitcnt lgkmcnt(10)\n\t"
                "v_mfma_f32_32x32x16_bf16 %[a1], v[236:239], v[232:235], %[a1]\n\t"
                "ds_read_b64 v[204:205], %[kb] offset:2848\n\t"
                "ds_read_b64 v[206:207], %[kb] offset:2856\n\t"
                "s_waitcnt lgkmcnt(9)\n\t"
                "v_mfma_f32_32x32x16_bf16 %[a1], v[240:243], v[244:247], %[a1]\n\t"
                "ds_read_b64 v[208:209], %[kb] offset:768\n\t"
                "ds_read_b64 v[210:211], %[kb] offset:776\n\t"
                "ds_read_b128 v[212:215], %[ub] offset:4112\n\t"
                "s_waitcnt lgkmcnt(11)\n\t"
                "v_mfma_f32_32x32x16_bf16 %[a0], v[240:243], v[248:251], %[a0]\n\t"
                "ds_read_b128 v[216:219], %[ub] offset:1552\n\t"
                "s_waitcnt lgkmcnt(10)\n\t"
                "v_mfma_f32_32x32x16_bf16 %[a1], v[252:255], v[248:251], %[a1]\n\t"
                "ds_read_b64 v[220:221], %[kb] offset:2816\n\t"
                "ds_read_b64 v[222:223], %[kb] offset:2824\n\t"
                "s_waitcnt lgkmcnt(9)\n\t"
                "v_mfma_f32_32x32x16_bf16 %[a1], v[192:195], v[196:199], %[a1]\n\t"
                "ds_read_b64 v[224:225], %[kb] offset:864\n\t"
                "ds_read_b64 v[226:227], %[kb] offset:872\n\t"
                "ds_read_b128 v[228:231], %[ub] offset:4000\n\t"
                "s_waitcnt lgkmcnt(11)\n\t"
                "v_mfma_f32_32x32x16_bf16 %[a0], v[192:195], v[200:203], %[a0]\n\t"
                "ds_read_b128 v[232:235], %[ub] offset:1440\n\t"
                "s_waitcnt lgkmcnt(10)\n\t"
                "v_mfma_f32_32x32x16_bf16 %[a1], v[204:207], v[200:203], %[a1]\n\t"
                "ds_read_b64 v[236:237], %[kb] offset:2912\n\t"
                "ds_read_b64 v[238:239], %[kb] offset:2920\n\t"
                "s_waitcnt lgkmcnt(9)\n\t"
                "v_mfma_f32_32x32x16_bf16 %[a1], v[208:211], v[212:215], %[a1]\n\t"
                "ds_read_b64 v[240:241], %[kb] offset:832\n\t"
                "ds_read_b64 v[242:243], %[kb] offset:840\n\t"
                "ds_read_b128 v[244:247], %[ub] offset:4032\n\t"
                "s_waitcnt lgkmcnt(11)\n\t"
                "v_mfma_f32_32x32x16_bf16 %[a0], v[208:211], v[216:219], %[a0]\n\t"
                "ds_read_b128 v[248:251], %[ub] offset:1472\n\t"
                "s_waitcnt lgkmcnt(10)\n\t"
                "v_mfma_f32_32x32x16_bf16 %[a1], v[220:223], v[216:219], %[a1]\n\t"
                "ds_read_b64 v[252:253], %[kb] offset:2880\n\t"
                "ds_read_b64 v[254:255], %[kb] offset:2888\n\t"
                "s_waitcnt lgkmcnt(9)\n\t"
                "v_mfma_f32_32x32x16_bf16 %[a1], v[224:227], v[228:231], %[a1]\n\t"
                "ds_read_b64 v[192:193], %[kb] offset:928\n\t"
                "ds_read_b64 v[194:195], %[kb] offset:936\n\t"
                "ds_read_b128 v[196:199], %[ub] offset:3920\n\t"
                "s_waitcnt lgkmcnt(11)\n\t"
                "v_mfma_f32_32x32x16_bf16 %[a0], v[224:227], v[232:235], %[a0]\n\t"
                "ds_read_b128 v[200:203], %[ub] offset:1360\n\t"
                "s_waitcnt lgkmcnt(10)\n\t"
                "v_mfma_f32_32x32x16_bf16 %[a1], v[236:239], v[232:235], %[a1]\n\t"
                "ds_read_b64 v[204:205], %[kb] offset:2976\n\t"
                "ds_read_b64 v[206:207], %[kb] offset:2984\n\t"
                "s_waitcnt lgkmcnt(9)\n\t"
                "v_mfma_f32_32x32x16_bf16 %[a1], v[240:243], v[244:247], %[a1]\n\t"
                "ds_read_b64 v[208:209], %[kb] offset:896\n\t"
                "ds_read_b64 v[210:211], %[kb] offset:904\n\t"
                "ds_read_b128 v[212:215], %[ub] offset:3952\n\t"
                "s_waitcnt lgkmcnt(11)\n\t"
                "v_mfma_f32_32x32x16_bf16 %[a0], v[240:243], v[248:251], %[a0]\n\t"
                "ds_read_b128 v[216:219], %[ub] offset:1392\n\t"
                "s_waitcnt lgkmcnt(10)\n\t"
                "v_mfma_f32_32x32x16_bf16 %[a1], v[252:255], v[248:251], %[a1]\n\t"
                "ds_read_b64 v[220:221], %[kb] offset:2944\n\t"
                "ds_read_b64 v[222:223], %[kb] offset:2952\n\t"
                "s_waitcnt lgkmcnt(9)\n\t"
                "v_mfma_f32_32x32x16_bf16 %[a1], v[192:195], v[196:199], %[a1]\n\t"
                "ds_read_b64 v[224:225], %[kb] offset:992\n\t"
                "ds_read_b64 v[226:227], %[kb] offset:1000\n\t"
                "ds_read_b128 v[228:231], %[ub] offset:3840\n\t"
                "s_waitcnt lgkmcnt(11)\n\t"
                "v_mfma_f32_32x32x16_bf16 %[a0], v[192:195], v[200:203], %[a0]\n\t"
                "ds_read_b128 v[232:235], %[ub] offset:1280\n\t"
                "s_waitcnt lgkmcnt(10)\n\t"
                "v_mfma_f32_32x32x16_bf16 %[a1], v[204:207], v[200:203], %[a1]\n\t"
                "ds_read_b64 v[236:237], %[kb] offset:3040\n\t"
                "ds_read_b64 v[238:239], %[kb] offset:3048\n\t"
                "s_waitcnt lgkmcnt(9)\n\t"
                "v_mfma_f32_32x32x16_bf16 %[a1], v[208:211], v[212:215], %[a1]\n\t"
                "ds_read_b64 v[240:241], %[kb] offset:960\n\t"
                "ds_read_b64 v[242:243], %[kb] offset:968\n\t"
                "ds_read_b128 v[244:247], %[ub] offset:3872\n\t"
                "s_waitcnt lgkmcnt(11)\n\t"
                "v_mfma_f32_32x32x16_bf16 %[a0], v[208:211], v[216:219], %[a0]\n\t"
                "ds_read_b128 v[248:251], %[ub] offset:1312\n\t"
                "s_waitcnt lgkmcnt(10)\n\t"
                "v_mfma_f32_32x32x16_bf16 %[a1], v[220:223], v[216:219], %[a1]\n\t"
                "ds_read_b64 v[252:253], %[kb] offset:3008\n\t"
                "ds_read_b64 v[254:255], %[kb] offset:3016\n\t"
                "s_waitcnt lgkmcnt(9)\n\t"
                "v_mfma_f32_32x32x16_bf16 %[a1], v[224:227], v[228:231], %[a1]\n\t"
                "ds_read_b64 v[192:193], %[kb] offset:1056\n\t"
                "ds_read_b64 v[194:195], %[kb] offset:1064\n\t"
                "ds_read_b128 v[196:199], %[ub] offset:3760\n\t"
                "s_waitcnt lgkmcnt(11)\n\t"
                "v_mfma_f32_32x32x16_bf16 %[a0], v[224:227], v[232:235], %[a0]\n\t"
                "ds_read_b128 v[200:203], %[ub] offset:1200\n\t"
                "s_waitcnt lgkmcnt(10)\n\t"
                "v_mfma_f32_32x32x16_bf16 %[a1], v[236:239], v[232:235], %[a1]\n\t"
                "ds_read_b64 v[204:205], %[kb] offset:3104\n\t"
                "ds_read_b64 v[206:207], %[kb] offset:3112\n\t"
                "s_waitcnt lgkmcnt(9)\n\t"
                "v_mfma_f32_32x32x16_bf16 %[a1], v[240:243], v[244:247], %[a1]\n\t"
                "ds_read_b64 v[208:209], %[kb] offset:1024\n\t"
                "ds_read_b64 v[210:211], %[kb] offset:1032\n\t"
                "ds_read_b128 v[212:215], %[ub] offset:3792\n\t"
                "s_waitcnt lgkmcnt(11)\n\t"
                "v_mfma_f32_32x32x16_bf16 %[a0], v[240:243], v[248:251], %[a0]\n\t"
                "ds_read_b128 v[216:219], %[ub] offset:1232\n\t"
                "s_waitcnt lgkmcnt(10)\n\t"
                "v_mfma_f32_32x32x16_bf16 %[a1], v[252:255], v[248:251], %[a1]\n\t"
                "ds_read_b64 v[220:221], %[kb] offset:3072\n\t"
                "ds_read_b64 v[222:223], %[kb] offset:3080\n\t"
                "s_waitcnt lgkmcnt(9)\n\t"
                "v_mfma_f32_32x32x16_bf16 %[a1], v[192:195], v[196:199], %[a1]\n\t"
                "ds_read_b64 v[224:225], %[kb] offset:1120\n\t"
                "ds_read_b64 v[226:227], %[kb] offset:1128\n\t"
                "ds_read_b128 v[228:231], %[ub] offset:3680\n\t"
                "s_waitcnt lgkmcnt(11)\n\t"
                "v_mfma_f32_32x32x16_bf16 %[a0], v[192:195], v[200:203], %[a0]\n\t"
                "ds_read_b128 v[232:235], %[ub] offset:1120\n\t"
                "s_waitcnt lgkmcnt(10)\n\t"
                "v_mfma_f32_32x32x16_bf16 %[a1], v[204:207], v[200:203], %[a1]\n\t"
                "ds_read_b64 v[236:237], %[kb] offset:3168\n\t"
                "ds_read_b64 v[238:239], %[kb] offset:3176\n\t"
                "s_waitcnt lgkmcnt(9)\n\t"
                "v_mfma_f32_32x32x16_bf16 %[a1], v[208:211], v[212:215], %[a1]\n\t"
                "ds_read_b64 v[240:241], %[kb] offset:1088\n\t"
                "ds_read_b64 v[242:243], %[kb] offset:1096\n\t"
                "ds_read_b128 v[244:247], %[ub] offset:3712\n\t"
                "s_waitcnt lgkmcnt(11)\n\t"
                "v_mfma_f32_32x32x16_bf16 %[a0], v[208:211], v[216:219], %[a0]\n\t"
                "ds_read_b128 v[248:251], %[ub] offset:1152\n\t"
                "s_waitcnt lgkmcnt(10)\n\t"
                "v_mfma_f32_32x32x16_bf16 %[a1], v[220:223], v[216:219], %[a1]\n\t"
                "ds_read_b64 v[252:253], %[kb] offset:3136\n\t"
                "ds_read_b64 v[254:255], %[kb] offset:3144\n\t"
                "s_waitcnt lgkmcnt(9)\n\t"
                "v_mfma_f32_32x32x16_bf16 %[a1], v[224:227], v[228:231], %[a1]\n\t"
                "ds_read_b64 v[192:193], %[kb] offset:1184\n\t"
                "ds_read_b64 v[194:195], %[kb] offset:1192\n\t"
                "ds_read_b128 v[196:199], %[ub] offset:3600\n\t"
                "s_waitcnt lgkmcnt(11)\n\t"
                "v_mfma_f32_32x32x16_bf16 %[a0], v[224:227], v[232:235], %[a0]\n\t"
                "ds_read_b128 v[200:203], %[ub] offset:1040\n\t"
                "s_waitcnt lgkmcnt(10)\n\t"
                "v_mfma_f32_32x32x16_bf16 %[a1], v[236:239], v[232:235], %[a1]\n\t"
                "ds_read_b64 v[204:205], %[kb] offset:3232\n\t"
                "ds_read_b64 v[206:207], %[kb] offset:3240\n\t"
                "s_waitcnt lgkmcnt(9)\n\t"
                "v_mfma_f32_32x32x16_bf16 %[a1], v[240:243], v[244:247], %[a1]\n\t"
                "ds_read_b64 v[208:209], %[kb] offset:1152\n\t"
                "ds_read_b64 v[210:211], %[kb] offset:1160\n\t"
                "ds_read_b128 v[212:215], %[ub] offset:3632\n\t"
                "s_waitcnt lgkmcnt(11)\n\t"
                "v_mfma_f32_32x32x16_bf16 %[a0], v[240:243], v[248:251], %[a0]\n\t"
                "ds_read_b128 v[216:219], %[ub] offset:1072\n\t"
                "s_waitcnt lgkmcnt(10)\n\t"
                "v_mfma_f32_32x32x16_bf16 %[a1], v[252:255], v[248:251], %[a1]\n\t"
                "ds_read_b64 v[220:221], %[kb] offset:3200\n\t"
                "ds_read_b64 v[222:223], %[kb] offset:3208\n\t"
                "s_waitcnt lgkmcnt(9)\n\t"
                "v_mfma_f32_32x32x16_bf16 %[a1], v[192:195], v[196:199], %[a1]\n\t"
                "ds_read_b64 v[224:225], %[kb] offset:1248\n\t"
                "ds_read_b64 v[226:227], %[kb] offset:1256\n\t"
                "ds_read_b128 v[228:231], %[ub] offset:3520\n\t"
                "s_waitcnt lgkmcnt(11)\n\t"
                "v_mfma_f32_32x32x16_bf16 %[a0], v[192:195], v[200:203], %[a0]\n\t"
                "ds_read_b128 v[232:235], %[ub] offset:960\n\t"
                "s_waitcnt lgkmcnt(10)\n\t"
                "v_mfma_f32_32x32x16_bf16 %[a1], v[204:207], v[200:203], %[a1]\n\t"
                "ds_read_b64 v[236:237], %[kb] offset:3296\n\t"
                "ds_read_b64 v[238:239], %[kb] offset:3304\n\t"
                "s_waitcnt lgkmcnt(9)\n\t"
                "v_mfma_f32_32x32x16_bf16 %[a1], v[208:211], v[212:215], %[a1]\n\t"
                "ds_read_b64 v[240:241], %[kb] offset:1216\n\t"
                "ds_read_b64 v[242:243], %[kb] offset:1224\n\t"
                "ds_read_b128 v[244:247], %[ub] offset:3552\n\t"
                "s_waitcnt lgkmcnt(11)\n\t"
                "v_mfma_f32_32x32x16_bf16 %[a0], v[208:211], v[216:219], %[a0]\n\t"
                "ds_read_b128 v[248:251], %[ub] offset:992\n\t"
                "s_waitcnt lgkmcnt(10)\n\t"
                "v_mfma_f32_32x32x16_bf16 %[a1], v[220:223], v[216:219], %[a1]\n\t"
                "ds_read_b64 v[252:253], %[kb] offset:3264\n\t"
                "ds_read_b64 v[254:255], %[kb] offset:3272\n\t"
                "s_waitcnt lgkmcnt(9)\n\t"
                "v_mfma_f32_32x32x16_bf16 %[a1], v[224:227], v[228:231], %[a1]\n\t"
                "ds_read_b64 v[192:193], %[kb] offset:1312\n\t"
                "ds_read_b64 v[194:195], %[kb] offset:1320\n\t"
                "ds_read_b128 v[196:199], %[ub] offset:3440\n\t"
                "s_waitcnt lgkmcnt(11)\n\t"
                "v_mfma_f32_32x32x16_bf16 %[a0], v[224:227], v[232:235], %[a0]\n\t"
                "ds_read_b128 v[200:203], %[ub] offset:880\n\t"
                "s_waitcnt lgkmcnt(10)\n\t"
                "v_mfma_f32_32x32x16_bf16 %[a1], v[236:239], v[232:235], %[a1]\n\t"
                "ds_read_b64 v[204:205], %[kb] offset:3360\n\t"
                "ds_read_b64 v[206:207], %[kb] offset:3368\n\t"
                "s_waitcnt lgkmcnt(9)\n\t"
                "v_mfma_f32_32x32x16_bf16 %[a1], v[240:243], v[244:247], %[a1]\n\t"
                "ds_read_b64 v[208:209], %[kb] offset:1280\n\t"
                "ds_read_b64 v[210:211], %[kb] offset:1288\n\t"
                "ds_read_b128 v[212:215], %[ub] offset:3472\n\t"
                "s_waitcnt lgkmcnt(11)\n\t"
                "v_mfma_f32_32x32x16_bf16 %[a0], v[240:243], v[248:251], %[a0]\n\t"
                "ds_read_b128 v[216:219], %[ub] offset:912\n\t"
                "s_waitcnt lgkmcnt(10)\n\t"
                "v_mfma_f32_32x32x16_bf16 %[a1], v[252:255], v[248:251], %[a1]\n\t"
                "ds_read_b64 v[220:221], %[kb] offset:3328\n\t"
                "ds_read_b64 v[222:223], %[kb] offset:3336\n\t"
                "s_waitcnt lgkmcnt(9)\n\t"
                "v_mfma_f32_32x32x16_bf16 %[a1], v[192:195], v[196:199], %[a1]\n\t"
                "ds_read_b64 v[224:225], %[kb] offset:1376\n\t"
                "ds_read_b64 v[226:227], %[kb] offset:1384\n\t"
                "ds_read_b128 v[228:231], %[ub] offset:3360\n\t"
                "s_waitcnt lgkmcnt(11)\n\t"
                "v_mfma_f32_32x32x16_bf16 %[a0], v[192:195], v[200:203], %[a0]\n\t"
                "ds_read_b128 v[232:235], %[ub] offset:800\n\t"
                "s_waitcnt lgkmcnt(10)\n\t"
                "v_mfma_f32_32x32x16_bf16 %[a1], v[204:207], v[200:203], %[a1]\n\t"
                "ds_read_b64 v[236:237], %[kb] offset:3424\n\t"
                "ds_read_b64 v[238:239], %[kb] offset:3432\n\t"
                "s_waitcnt lgkmcnt(9)\n\t"
                "v_mfma_f32_32x32x16_bf16 %[a1], v[208:211], v[212:215], %[a1]\n\t"
                "ds_read_b64 v[240:241], %[kb] offset:1344\n\t"
                "ds_read_b64 v[242:243], %[kb] offset:1352\n\t"
                "ds_read_b128 v[244:247], %[ub] offset:3392\n\t"
                "s_waitcnt lgkmcnt(11)\n\t"
                "v_mfma_f32_32x32x16_bf16 %[a0], v[208:211], v[216:219], %[a0]\n\t"
                "ds_read_b128 v[248:251], %[ub] offset:832\n\t"
                "s_waitcnt lgkmcnt(10)\n\t"
                "v_mfma_f32_32x32x16_bf16 %[a1], v[220:223], v[216:219], %[a1]\n\t"
                "ds_read_b64 v[252:253], %[kb] offset:3392\n\t"
                "ds_read_b64 v[254:255], %[kb] offset:3400\n\t"
                "s_waitcnt lgkmcnt(9)\n\t"
                "v_mfma_f32_32x32x16_bf16 %[a1], v[224:227], v[228:231], %[a1]\n\t"
                "ds_read_b64 v[192:193], %[kb] offset:1440\n\t"
                "ds_read_b64 v[194:195], %[kb] offset:1448\n\t"
                "ds_read_b128 v[196:199], %[ub] offset:3280\n\t"
                "s_waitcnt lgkmcnt(11)\n\t"
                "v_mfma_f32_32x32x16_bf16 %[a0], v[224:227], v[232:235], %[a0]\n\t"
                "ds_read_b128 v[200:203], %[ub] offset:720\n\t"
                "s_waitcnt lgkmcnt(10)\n\t"
                "v_mfma_f32_32x32x16_bf16 %[a1], v[236:239], v[232:235], %[a1]\n\t"
                "ds_read_b64 v[204:205], %[kb] offset:3488\n\t"
                "ds_read_b64 v[206:207], %[kb] offset:3496\n\t"
                "s_waitcnt lgkmcnt(9)\n\t"
                "v_mfma_f32_32x32x16_bf16 %[a1], v[240:243], v[244:247], %[a1]\n\t"
                "ds_read_b64 v[208:209], %[kb] offset:1408\n\t"
                "ds_read_b64 v[210:211], %[kb] offset:1416\n\t"
                "ds_read_b128 v[212:215], %[ub] offset:3312\n\t"
                "s_waitcnt lgkmcnt(11)\n\t"
                "v_mfma_f32_32x32x16_bf16 %[a0], v[240:243], v[248:251], %[a0]\n\t"
                "ds_read_b128 v[216:219], %[ub] offset:752\n\t"
                "s_waitcnt lgkmcnt(10)\n\t"
                "v_mfma_f32_32x32x16_bf16 %[a1], v[252:255], v[248:251], %[a1]\n\t"
                "ds_read_b64 v[220:221], %[kb] offset:3456\n\t"
                "ds_read_b64 v[222:223], %[kb] offset:3464\n\t"
                "s_waitcnt lgkmcnt(9)\n\t"
                "v_mfma_f32_32x32x16_bf16 %[a1], v[192:195], v[196:199], %[a1]\n\t"
                "ds_read_b64 v[224:225], %[kb] offset:1504\n\t"
                "ds_read_b64 v[226:227], %[kb] offset:1512\n\t"
                "ds_read_b128 v[228:231], %[ub] offset:3200\n\t"
                "s_waitcnt lgkmcnt(11)\n\t"
                "v_mfma_f32_32x32x16_bf16 %[a0], v[192:195], v[200:203], %[a0]\n\t"
                "ds_read_b128 v[232:235], %[ub] offset:640\n\t"
                "s_waitcnt lgkmcnt(10)\n\t"
                "v_mfma_f32_32x32x16_bf16 %[a1], v[204:207], v[200:203], %[a1]\n\t"
                "ds_read_b64 v[236:237], %[kb] offset:3552\n\t"
                "ds_read_b64 v[238:239], %[kb] offset:3560\n\t"
                "s_waitcnt lgkmcnt(9)\n\t"
                "v_mfma_f32_32x32x16_bf16 %[a1], v[208:211], v[212:215], %[a1]\n\t"
                "ds_read_b64 v[240:241], %[kb] offset:1472\n\t"
                "ds_read_b64 v[242:243], %[kb] offset:1480\n\t"
                "ds_read_b128 v[244:247], %[ub] offset:3232\n\t"
                "s_waitcnt lgkmcnt(11)\n\t"
                "v_mfma_f32_32x32x16_bf16 %[a0], v[208:211], v[216:219], %[a0]\n\t"
                "ds_read_b128 v[248:251], %[ub] offset:672\n\t"
                "s_waitcnt lgkmcnt(10)\n\t"
                "v_mfma_f32_32x32x16_bf16 %[a1], v[220:223], v[216:219], %[a1]\n\t"
                "ds_read_b64 v[252:253], %[kb] offset:3520\n\t"
                "ds_read_b64 v[254:255], %[kb] offset:3528\n\t"
                "s_waitcnt lgkmcnt(9)\n\t"
                "v_mfma_f32_32x32x16_bf16 %[a1], v[224:227], v[228:231], %[a1]\n\t"
                "ds_read_b64 v[192:193], %[kb] offset:1568\n\t"
                "ds_read_b64 v[194:195], %[kb] offset:1576\n\t"
                "ds_read_b128 v[196:199], %[ub] offset:3120\n\t"
                "s_waitcnt lgkmcnt(11)\n\t"
                "v_mfma_f32_32x32x16_bf16 %[a0], v[224:227], v[232:235], %[a0]\n\t"
                "ds_read_b128 v[200:203], %[ub] offset:560\n\t"
                "s_waitcnt lgkmcnt(10)\n\t"
                "v_mfma_f32_32x32x16_bf16 %[a1], v[236:239], v[232:235], %[a1]\n\t"
                "ds_read_b64 v[204:205], %[kb] offset:3616\n\t"
                "ds_read_b64 v[206:207], %[kb] offset:3624\n\t"
                "s_waitcnt lgkmcnt(9)\n\t"
                "v_mfma_f32_32x32x16_bf16 %[a1], v[240:243], v[244:247], %[a1]\n\t"
                "ds_read_b64 v[208:209], %[kb] offset:1536\n\t"
                "ds_read_b64 v[210:211], %[kb] offset:1544\n\t"
                "ds_read_b128 v[212:215], %[ub] offset:3152\n\t"
                "s_waitcnt lgkmcnt(11)\n\t"
                "v_mfma_f32_32x32x16_bf16 %[a0], v[240:243], v[248:251], %[a0]\n\t"
                "ds_read_b128 v[216:219], %[ub] offset:592\n\t"
                "s_waitcnt lgkmcnt(10)\n\t"
                "v_mfma_f32_32x32x16_bf16 %[a1], v[252:255], v[248:251], %[a1]\n\t"
                "ds_read_b64 v[220:221], %[kb] offset:3584\n\t"
                "ds_read_b64 v[222:223], %[kb] offset:3592\n\t"
                "s_waitcnt lgkmcnt(9)\n\t"
                "v_mfma_f32_32x32x16_bf16 %[a1], v[192:195], v[196:199], %[a1]\n\t"
                "ds_read_b64 v[224:225], %[kb] offset:1632\n\t"
                "ds_read_b64 v[226:227], %[kb] offset:1640\n\t"
                "ds_read_b128 v[228:231], %[ub] offset:3040\n\t"
                "s_waitcnt lgkmcnt(11)\n\t"
                "v_mfma_f32_32x32x16_bf16 %[a0], v[192:195], v[200:203], %[a0]\n\t"
                "ds_read_b128 v[232:235], %[ub] offset:480\n\t"
                "s_waitcnt lgkmcnt(10)\n\t"
                "v_mfma_f32_32x32x16_bf16 %[a1], v[204:207], v[200:203], %[a1]\n\t"
                "ds_read_b64 v[236:237], %[kb] offset:3680\n\t"
                "ds_read_b64 v[238:239], %[kb] offset:3688\n\t"
                "s_waitcnt lgkmcnt(9)\n\t"
                "v_mfma_f32_32x32x16_bf16 %[a1], v[208:211], v[212:215], %[a1]\n\t"
                "ds_read_b64 v[240:241], %[kb] offset:1600\n\t"
                "ds_read_b64 v[242:243], %[kb] offset:1608\n\t"
                "ds_read_b128 v[244:247], %[ub] offset:3072\n\t"
                "s_waitcnt lgkmcnt(11)\n\t"
                "v_mfma_f32_32x32x16_bf16 %[a0], v[208:211], v[216:219], %[a0]\n\t"
                "ds_read_b128 v[248:251], %[ub] offset:512\n\t"
                "s_waitcnt lgkmcnt(10)\n\t"
                "v_mfma_f32_32x32x16_bf16 %[a1], v[220:223], v[216:219], %[a1]\n\t"
                "ds_read_b64 v[252:253], %[kb] offset:3648\n\t"
                "ds_read_b64 v[254:255], %[kb] offset:3656\n\t"
                "s_waitcnt lgkmcnt(9)\n\t"
                "v_mfma_f32_32x32x16_bf16 %[a1], v[224:227], v[228:231], %[a1]\n\t"
                "ds_read_b64 v[192:193], %[kb] offset:1696\n\t"
                "ds_read_b64 v[194:195], %[kb] offset:1704\n\t"
                "ds_read_b128 v[196:199], %[ub] offset:2960\n\t"
                "s_waitcnt lgkmcnt(11)\n\t"
                "v_mfma_f32_32x32x16_bf16 %[a0], v[224:227], v[232:235], %[a0]\n\t"
                "ds_read_b128 v[200:203], %[ub] offset:400\n\t"
                "s_waitcnt lgkmcnt(10)\n\t"
                "v_mfma_f32_32x32x16_bf16 %[a1], v[236:239], v[232:235], %[a1]\n\t"
                "ds_read_b64 v[204:205], %[kb] offset:3744\n\t"
                "ds_read_b64 v[206:207], %[kb] offset:3752\n\t"
                "s_waitcnt lgkmcnt(9)\n\t"
                "v_mfma_f32_32x32x16_bf16 %[a1], v[240:243], v[244:247], %[a1]\n\t"
                "ds_read_b64 v[208:209], %[kb] offset:1664\n\t"
                "ds_read_b64 v[210:211], %[kb] offset:1672\n\t"
                "ds_read_b128 v[212:215], %[ub] offset:2992\n\t"
                "s_waitcnt lgkmcnt(11)\n\t"
                "v_mfma_f32_32x32x16_bf16 %[a0], v[240:243], v[248:251], %[a0]\n\t"
                "ds_read_b128 v[216:219], %[ub] offset:432\n\t"
                "s_waitcnt lgkmcnt(10)\n\t"
                "v_mfma_f32_32x32x16_bf16 %[a1], v[252:255], v[248:251], %[a1]\n\t"
                "ds_read_b64 v[220:221], %[kb] offset:3712\n\t"
                "ds_read_b64 v[222:223], %[kb] offset:3720\n\t"
                "s_waitcnt lgkmcnt(9)\n\t"
                "v_mfma_f32_32x32x16_bf16 %[a1], v[192:195], v[196:199], %[a1]\n\t"
                "ds_read_b64 v[224:225], %[kb] offset:1760\n\t"
                "ds_read_b64 v[226:227], %[kb] offset:1768\n\t"
                "ds_read_b128 v[228:231], %[ub] offset:2880\n\t"
                "s_waitcnt lgkmcnt(11)\n\t"
                "v_mfma_f32_32x32x16_bf16 %[a0], v[192:195], v[200:203], %[a0]\n\t"
                "ds_read_b128 v[232:235], %[ub] offset:320\n\t"
                "s_waitcnt lgkmcnt(10)\n\t"
                "v_mfma_f32_32x32x16_bf16 %[a1], v[204:207], v[200:203], %[a1]\n\t"
                "ds_read_b64 v[236:237], %[kb] offset:3808\n\t"
                "ds_read_b64 v[238:239], %[kb] offset:3816\n\t"
                "s_waitcnt lgkmcnt(9)\n\t"
                "v_mfma_f32_32x32x16_bf16 %[a1], v[208:211], v[212:215], %[a1]\n\t"
                "ds_read_b64 v[240:241], %[kb] offset:1728\n\t"
                "ds_read_b64 v[242:243], %[kb] offset:1736\n\t"
                "ds_read_b128 v[244:247], %[ub] offset:2912\n\t"
                "s_waitcnt lgkmcnt(11)\n\t"
                "v_mfma_f32_32x32x16_bf16 %[a0], v[208:211], v[216:219], %[a0]\n\t"
                "ds_read_b128 v[248:251], %[ub] offset:352\n\t"
                "s_waitcnt lgkmcnt(10)\n\t"
                "v_mfma_f32_32x32x16_bf16 %[a1], v[220:223], v[216:219], %[a1]\n\t"
                "ds_read_b64 v[252:253], %[kb] offset:3776\n\t"
                "ds_read_b64 v[254:255], %[kb] offset:3784\n\t"
                "s_waitcnt lgkmcnt(9)\n\t"
                "v_mfma_f32_32x32x16_bf16 %[a1], v[224:227], v[228:231], %[a1]\n\t"
                "ds_read_b64 v[192:193], %[kb] offset:1824\n\t"
                "ds_read_b64 v[194:195], %[kb] offset:1832\n\t"
                "ds_read_b128 v[196:199], %[ub] offset:2800\n\t"
                "s_waitcnt lgkmcnt(11)\n\t"
                "v_mfma_f32_32x32x16_bf16 %[a0], v[224:227], v[232:235], %[a0]\n\t"
                "ds_read_b128 v[200:203], %[ub] offset:240\n\t"
                "s_waitcnt lgkmcnt(10)\n\t"
                "v_mfma_f32_32x32x16_bf16 %[a1], v[236:239], v[232:235], %[a1]\n\t"
                "ds_read_b64 v[204:205], %[kb] offset:3872\n\t"
                "ds_read_b64 v[206:207], %[kb] offset:3880\n\t"
                "s_waitcnt lgkmcnt(9)\n\t"
                "v_mfma_f32_32x32x16_bf16 %[a1], v[240:243], v[244:247], %[a1]\n\t"
                "ds_read_b64 v[208:209], %[kb] offset:1792\n\t"
                "ds_read_b64 v[210:211], %[kb] offset:1800\n\t"
                "ds_read_b128 v[212:215], %[ub] offset:2832\n\t"
                "s_waitcnt lgkmcnt(11)\n\t"
                "v_mfma_f32_32x32x16_bf16 %[a0], v[240:243], v[248:251], %[a0]\n\t"
                "ds_read_b128 v[216:219], %[ub] offset:272\n\t"
                "s_waitcnt lgkmcnt(10)\n\t"
                "v_mfma_f32_32x32x16_bf16 %[a1], v[252:255], v[248:251], %[a1]\n\t"
                "ds_read_b64 v[220:221], %[kb] offset:3840\n\t"
                "ds_read_b64 v[222:223], %[kb] offset:3848\n\t"
                "s_waitcnt lgkmcnt(9)\n\t"
                "v_mfma_f32_32x32x16_bf16 %[a1], v[192:195], v[196:199], %[a1]\n\t"
                "ds_read_b64 v[224:225], %[kb] offset:1888\n\t"
                "ds_read_b64 v[226:227], %[kb] offset:1896\n\t"
                "ds_read_b128 v[228:231], %[ub] offset:2720\n\t"
                "s_waitcnt lgkmcnt(11)\n\t"
                "v_mfma_f32_32x32x16_bf16 %[a0], v[192:195], v[200:203], %[a0]\n\t"
                "ds_read_b128 v[232:235], %[ub] offset:160\n\t"
                "s_waitcnt lgkmcnt(10)\n\t"
                "v_mfma_f32_32x32x16_bf16 %[a1], v[204:207], v[200:203], %[a1]\n\t"
                "ds_read_b64 v[236:237], %[kb] offset:3936\n\t"
                "ds_read_b64 v[238:239], %[kb] offset:3944\n\t"
                "s_waitcnt lgkmcnt(9)\n\t"
                "v_mfma_f32_32x32x16_bf16 %[a1], v[208:211], v[212:215], %[a1]\n\t"
                "ds_read_b64 v[240:241], %[kb] offset:1856\n\t"
                "ds_read_b64 v[242:243], %[kb] offset:1864\n\t"
                "ds_read_b128 v[244:247], %[ub] offset:2752\n\t"
                "s_waitcnt lgkmcnt(11)\n\t"
                "v_mfma_f32_32x32x16_bf16 %[a0], v[208:211], v[216:219], %[a0]\n\t"
                "ds_read_b128 v[248:251], %[ub] offset:192\n\t"
                "s_waitcnt lgkmcnt(10)\n\t"
                "v_mfma_f32_32x32x16_bf16 %[a1], v[220:223], v[216:219], %[a1]\n\t"
                "ds_read_b64 v[252:253], %[kb] offset:3904\n\t"
                "ds_read_b64 v[254:255], %[kb] offset:3912\n\t"
                "s_waitcnt lgkmcnt(9)\n\t"
                "v_mfma_f32_32x32x16_bf16 %[a1], v[224:227], v[228:231], %[a1]\n\t"
                "ds_read_b64 v[192:193], %[kb] offset:1952\n\t"
                "ds_read_b64 v[194:195], %[kb] offset:1960\n\t"
                "ds_read_b128 v[196:199], %[ub] offset:2640\n\t"
                "s_waitcnt lgkmcnt(11)\n\t"
                "v_mfma_f32_32x32x16_bf16 %[a0], v[224:227], v[232:235], %[a0]\n\t"
                "ds_read_b128 v[200:203], %[ub] offset:80\n\t"
                "s_waitcnt lgkmcnt(10)\n\t"
                "v_mfma_f32_32x32x16_bf16 %[a1], v[236:239], v[232:235], %[a1]\n\t"
                "ds_read_b64 v[204:205], %[kb] offset:4000\n\t"
                "ds_read_b64 v[206:207], %[kb] offset:4008\n\t"
                "s_waitcnt lgkmcnt(9)\n\t"
                "v_mfma_f32_32x32x16_bf16 %[a1], v[240:243], v[244:247], %[a1]\n\t"
                "ds_read_b64 v[208:209], %[kb] offset:1920\n\t"
                "ds_read_b64 v[210:211], %[kb] offset:1928\n\t"
                "ds_read_b128 v[212:215], %[ub] offset:2672\n\t"
                "s_waitcnt lgkmcnt(11)\n\t"
                "v_mfma_f32_32x32x16_bf16 %[a0], v[240:243], v[248:251], %[a0]\n\t"
                "ds_read_b128 v[216:219], %[ub] offset:112\n\t"
                "s_waitcnt lgkmcnt(10)\n\t"
                "v_mfma_f32_32x32x16_bf16 %[a1], v[252:255], v[248:251], %[a1]\n\t"
                "ds_read_b64 v[220:221], %[kb] offset:3968\n\t"
                "ds_read_b64 v[222:223], %[kb] offset:3976\n\t"
                "s_waitcnt lgkmcnt(9)\n\t"
                "v_mfma_f32_32x32x16_bf16 %[a1], v[192:195], v[196:199], %[a1]\n\t"
                "ds_read_b64 v[224:225], %[kb] offset:2016\n\t"
                "ds_read_b64 v[226:227], %[kb] offset:2024\n\t"
                "ds_read_b128 v[228:231], %[ub] offset:2560\n\t"
                "s_waitcnt lgkmcnt(11)\n\t"
                "v_mfma_f32_32x32x16_bf16 %[a0], v[192:195], v[200:203], %[a0]\n\t"
                "ds_read_b128 v[232:235], %[ub] offset:0\n\t"
                "s_waitcnt lgkmcnt(10)\n\t"
                "v_mfma_f32_32x32x16_bf16 %[a1], v[204:207], v[200:203], %[a1]\n\t"
                "ds_read_b64 v[236:237], %[kb] offset:4064\n\t"
                "ds_read_b64 v[238:239], %[kb] offset:4072\n\t"
                "s_waitcnt lgkmcnt(9)\n\t"
                "v_mfma_f32_32x32x16_bf16 %[a1], v[208:211], v[212:215], %[a1]\n\t"
                "ds_read_b64 v[240:241], %[kb] offset:1984\n\t"
                "ds_read_b64 v[242:243], %[kb] offset:1992\n\t"
                "ds_read_b128 v[244:247], %[ub] offset:2592\n\t"
                "s_waitcnt lgkmcnt(11)\n\t"
                "v_mfma_f32_32x32x16_bf16 %[a0], v[208:211], v[216:219], %[a0]\n\t"
                "ds_read_b128 v[248:251], %[ub] offset:32\n\t"
                "s_waitcnt lgkmcnt(10)\n\t"
                "v_mfma_f32_32x32x16_bf16 %[a1], v[220:223], v[216:219], %[a1]\n\t"
                "ds_read_b64 v[252:253], %[kb] offset:4032\n\t"
                "ds_read_b64 v[254:255], %[kb] offset:4040\n\t"
                "s_waitcnt lgkmcnt(9)\n\t"
                "v_mfma_f32_32x32x16_bf16 %[a1], v[224:227], v[228:231], %[a1]\n\t"
                "s_waitcnt lgkmcnt(8)\n\t"
                "v_mfma_f32_32x32x16_bf16 %[a0], v[224:227], v[232:235], %[a0]\n\t"
                "s_waitcnt lgkmcnt(6)\n\t"
                "v_mfma_f32_32x32x16_bf16 %[a1], v[236:239], v[232:235], %[a1]\n\t"
                "s_waitcnt lgkmcnt(3)\n\t"
                "v_mfma_f32_32x32x16_bf16 %[a1], v[240:243], v[244:247], %[a1]\n\t"
                "s_waitcnt lgkmcnt(2)\n\t"
                "v_mfma_f32_32x32x16_bf16 %[a0], v[240:243], v[248:251], %[a0]\n\t"
                "s_waitcnt lgkmcnt(0)\n\t"
                "v_mfma_f32_32x32x16_bf16 %[a1], v[252:255], v[248:251], %[a1]\n\t"
                "s_nop 7\n\t"
                "s_nop 3\n\t"
                : [a0] "=&v"(acc0), [a1] "=&v"(acc1) : [kb] "v"(kb), [ub] "v"(ub0 + c8 * CV_U) : "memory", "v192", "v193", "v194", "v195", "v196", "v197", "v198", "v199", "v200", "v201", "v202", "v203", "v204", "v205", "v206", "v207", "v208", "v209", "v210", "v211", "v212", "v213", "v214", "v215", "v216", "v217", "v218", "v219", "v220", "v221", "v222", "v223", "v224", "v225", "v226", "v227", "v228", "v229", "v230", "v231", "v232", "v233", "v234", "v235", "v236", "v237", "v238", "v239", "v240", "v241", "v242", "v243", "v244", "v245", "v246", "v247", "v248", "v249", "v250", "v251", "v252", "v253", "v254", "v255");
            unsigned char* uc = lds + c8 * CV_U + (32 + b) * CV_RP + kg * 8;
#pragma unroll
            for (int q = 0; q < 4; ++q) {
                *(v2u*)(uc + q * 16) = (v2u){pack2(acc0[4 * q], acc0[4 * q + 1]), pack2(acc0[4 * q + 2], acc0[4 * q + 3])};
                *(v2u*)(uc + 32 * CV_RP + q * 16) = (v2u){pack2(acc1[4 * q], acc1[4 * q + 1]), pack2(acc1[4 * q + 2], acc1[4 * q + 3])}; }
            __builtin_amdgcn_wave_barrier();
        }
    __syncthreads();
#pragma unroll
    for (int G = 0; G < 4; ++G) { const v4u lo = *(const v4u*)(urow + G * 16), hh = *(const v4u*)(urow + CV_U + G * 16); v4u o[2];
#pragma unroll
        for (int i = 0; i < 8; ++i) { const unsigned u0 = ur[G][(7 - i) >> 1], u1 = ur[4 + G][(7 - i) >> 1], qq = qr[2 * G + (i >> 2)][i & 3];
            const unsigned ud = pack2(((i & 1) ? blo(u0) : bhi(u0)) * blo(dv), ((i & 1) ? blo(u1) : bhi(u1)) * bhi(dv));
            const unsigned y = pack2(((i & 1) ? bhi(lo[i >> 1]) : blo(lo[i >> 1])) + blo(ud), ((i & 1) ? bhi(hh[i >> 1]) : blo(hh[i >> 1])) + bhi(ud));
            o[i >> 2][i & 3] = pack2(blo(qq) * blo(y), bhi(qq) * bhi(y)); }
        st16(r, W_A + un * 32768 + 2 * G * 4096 + tid * 16, o[0]); st16(r, W_A + un * 32768 + (2 * G + 1) * 4096 + tid * 16, o[1]); }
    DRAIN(); __syncthreads();
    if (tid == 0) __hip_atomic_fetch_add((unsigned*)(p.ws + F_CVY + (un >> 3) * 128), 1u, __ATOMIC_RELAXED, __HIP_MEMORY_SCOPE_AGENT);
    return true;
}

__device__ __forceinline__ void proj_block(const Params& p, unsigned char* lds, const int wv, const int h, const int c) {
    const int w = wv, l = otid() & 63, n = l & 15, g = l >> 4, tm = w >> 1, tn = w & 1;
    const unsigned lb = __builtin_amdgcn_readfirstlane((unsigned)(size_t)lds);
    const v4u sx = mk_srd(p.Aq, S * D * 2), sq = mk_srd(c < 2 ? p.Wq : p.Wk, 1024 * 4096 * 2), sv = mk_srd(p.Wv, 1024 * 4096 * 2), sw = mk_srd(p.ws, W_TOTAL);
    const unsigned lx = lb + (32 * tm + n) * 128 + ((g ^ (n & 7)) << 4), lr = lb + 131072, rr = 8 * g + (n >> 2);
    const int cv = ((c & 1) ^ 1) * 2 + (c >> 1);
    const unsigned aq = (c < 2 ? W_QX : W_KX) + (tm * 32 + h) * 8192 + (2 * (c & 1) + tn) * 1024 + l * 16;
    const int hq = h & 7, tk = ((hq - (hq > 1)) * 4 + c) * 4 + w;
    const bf16_t* tp = (hq != 1 && tk < 32) ? p.Aq + (tk * 64 + l) * 64 : p.gate_w;
    unsigned t0, xo, so, cn; unsigned long long tq;
    asm volatile(
        "s_nop 4\n\t"
        "s_mov_b32 %[xo], 0\n\t"
        "s_mov_b32 %[so], 0\n\t"
        "s_mov_b32 %[cn], 1\n\t"
        "global_load_dword v88, v[114:115], off\n\t"
        "s_add_u32 m0, %[ld], 0\n\t"
        "s_add_u32 %[t0], %[xo], 0\n\t"
        "buffer_load_dwordx4 v100, %[sx], %[t0] offen lds\n\t"
        "s_add_u32 m0, %[ld], 1024\n\t"
        "s_add_u32 %[t0], %[xo], 16384\n\t"
        "buffer_load_dwordx4 v100, %[sx], %[t0] offen lds\n\t"
        "s_add_u32 m0, %[ld], 8192\n\t"
        "s_add_u32 %[t0], %[xo], 128\n\t"
        "buffer_load_dwordx4 v100, %[sx], %[t0] offen lds\n\t"
        "s_add_u32 m0, %[ld], 9216\n\t"
        "s_add_u32 %[t0], %[xo], 16512\n\t"
        "buffer_load_dwordx4 v100, %[sx], %[t0] offen lds\n\t"
        "s_add_u32 m0, %[ld], 16384\n\t"
        "s_add_u32 %[t0], %[xo], 256\n\t"
        "buffer_load_dwordx4 v100, %[sx], %[t0] offen lds\n\t"
        "s_add_u32 m0, %[ld], 17408\n\t"
        "s_add_u32 %[t0], %[xo], 16640\n\t"
        "buffer_load_dwordx4 v100, %[sx], %[t0] offen lds\n\t"
        "s_add_u32 m0, %[ld], 24576\n\t"
        "s_add_u32 %[t0], %[xo], 384\n\t"
        "buffer_load_dwordx4 v100, %[sx], %[t0] offen lds\n\t"
        "s_add_u32 m0, %[ld], 25600\n\t"
        "s_add_u32 %[t0], %[xo], 16768\n\t"
        "buffer_load_dwordx4 v100, %[sx], %[t0] offen lds\n\t"
        "s_mov_b32 %[t0], 0\n\t"
        "buffer_load_dwordx4 a[192:195], v101, %[sq], %[t0] offen\n\t"
        "buffer_load_dwordx4 a[200:203], v112, %[sv], %[t0] offen\n\t"
        "s_mov_b32 %[t0], 262144\n\t"
        "buffer_load_dwordx4 a[196:199], v101, %[sq], %[t0] offen\n\t"
        "s_mov_b32 %[t0], 524288\n\t"
        "buffer_load_dwordx4 a[204:207], v101, %[sq], %[t0] offen\n\t"
        "buffer_load_dwordx4 a[212:215], v112, %[sv], %[t0] offen\n\t"
        "s_mov_b32 %[t0], 786432\n\t"
        "buffer_load_dwordx4 a[208:211], v101, %[sq], %[t0] offen\n\t"
        "s_mov_b32 %[t0], 1048576\n\t"
        "buffer_load_dwordx4 a[216:219], v101, %[sq], %[t0] offen\n\t"
        "buffer_load_dwordx4 a[224:227], v112, %[sv], %[t0] offen\n\t"
        "s_mov_b32 %[t0], 1310720\n\t"
        "buffer_load_dwordx4 a[220:223], v101, %[sq], %[t0] offen\n\t"
        "s_mov_b32 %[t0], 1572864\n\t"
        "buffer_load_dwordx4 a[228:231], v101, %[sq], %[t0] offen\n\t"
        "buffer_load_dwordx4 a[236:239], v112, %[sv], %[t0] offen\n\t"
        "s_mov_b32 %[t0], 1835008\n\t"
        "buffer_load_dwordx4 a[232:235], v101, %[sq], %[t0] offen\n\t"
        "s_waitcnt vmcnt(9)\n\t"
        "ds_write_b128 v102, a[192:195] offset:0\n\t"
        "ds_write_b128 v102, a[196:199] offset:4096\n\t"
        "ds_write_b128 v116, a[200:203] offset:8192\n\t"
        "s_waitcnt lgkmcnt(0)\n\t"
        "s_barrier\n\t"
        "ds_read_b64_tr_b16 a[0:1], v103 offset:0\n\t"
        "ds_read_b64_tr_b16 a[2:3], v103 offset:512\n\t"
        "ds_read_b64_tr_b16 a[4:5], v103 offset:8\n\t"
        "ds_read_b64_tr_b16 a[6:7], v103 offset:520\n\t"
        "ds_read_b64_tr_b16 a[8:9], v104 offset:0\n\t"
        "ds_read_b64_tr_b16 a[10:11], v104 offset:256\n\t"
        "s_waitcnt lgkmcnt(5)\n\t"
        "ds_read_b64_tr_b16 a[12:13], v103 offset:4096\n\t"
        "ds_read_b64_tr_b16 a[14:15], v103 offset:4608\n\t"
        "ds_read_b64_tr_b16 a[16:17], v103 offset:4104\n\t"
        "ds_read_b64_tr_b16 a[18:19], v103 offset:4616\n\t"
        "ds_read_b64_tr_b16 a[20:21], v104 offset:2048\n\t"
        "ds_read_b64_tr_b16 a[22:23], v104 offset:2304\n\t"
        "ds_read_b128 v[56:59], v105 offset:0\n\t"
        "ds_read_b128 v[60:63], v105 offset:2048\n\t"
        "ds_read_b128 v[64:67], v106 offset:0\n\t"
        "ds_read_b128 v[68:71], v106 offset:2048\n\t"
        "s_add_u32 m0, %[ld], 32768\n\t"
        "s_add_u32 %[t0], %[xo], 512\n\t"
        "buffer_load_dwordx4 v100, %[sx], %[t0] offen lds\n\t"
        "s_add_u32 m0, %[ld], 33792\n\t"
        "s_add_u32 %[t0], %[xo], 16896\n\t"
        "buffer_load_dwordx4 v100, %[sx], %[t0] offen lds\n\t"
        "s_add_u32 m0, %[ld], 40960\n\t"
        "s_add_u32 %[t0], %[xo], 640\n\t"
        "buffer_load_dwordx4 v100, %[sx], %[t0] offen lds\n\t"
        "s_add_u32 m0, %[ld], 41984\n\t"
        "s_add_u32 %[t0], %[xo], 17024\n\t"
        "buffer_load_dwordx4 v100, %[sx], %[t0] offen lds\n\t"
        "s_add_u32 m0, %[ld], 49152\n\t"
        "s_add_u32 %[t0], %[xo], 768\n\t"
        "buffer_load_dwordx4 v100, %[sx], %[t0] offen lds\n\t"
        "s_add_u32 m0, %[ld], 50176\n\t"
        "s_add_u32 %[t0], %[xo], 17152\n\t"
        "buffer_load_dwordx4 v100, %[sx], %[t0] offen lds\n\t"
        "s_add_u32 m0, %[ld], 57344\n\t"
        "s_add_u32 %[t0], %[xo], 896\n\t"
        "buffer_load_dwordx4 v100, %[sx], %[t0] offen lds\n\t"
        "s_add_u32 m0, %[ld], 58368\n\t"
        "s_add_u32 %[t0], %[xo], 17280\n\t"
        "buffer_load_dwordx4 v100, %[sx], %[t0] offen lds\n\t"
        "s_mov_b32 %[t0], 2097152\n\t"
        "buffer_load_dwordx4 a[240:243], v101, %[sq], %[t0] offen\n\t"
        "buffer_load_dwordx4 a[248:251], v112, %[sv], %[t0] offen\n\t"
        "s_mov_b32 %[t0], 2359296\n\t"
        "buffer_load_dwordx4 a[244:247], v101, %[sq], %[t0] offen\n\t"
        "s_waitcnt vmcnt(17)\n\t"
        "ds_write_b128 v102, a[204:207] offset:16384\n\t"
        "ds_write_b128 v102, a[208:211] offset:20480\n\t"
        "ds_write_b128 v116, a[212:215] offset:24576\n\t"
        "s_waitcnt lgkmcnt(0)\n\t"
        "s_barrier\n\t"
        "v_mfma_f32_16x16x32_bf16 v[8:11], a[0:3], v[56:59], 0\n\t"
        "ds_read_b64_tr_b16 a[24:25], v103 offset:16384\n\t"
        "ds_read_b64_tr_b16 a[26:27], v103 offset:16896\n\t"
        "v_mfma_f32_16x16x32_bf16 v[12:15], a[4:7], v[56:59], 0\n\t"
        "ds_read_b64_tr_b16 a[28:29], v103 offset:16392\n\t"
        "ds_read_b64_tr_b16 a[30:31], v103 offset:16904\n\t"
        "v_mfma_f32_16x16x32_bf16 v[16:19], v[56:59], a[8:11], 0\n\t"
        "ds_read_b64_tr_b16 a[32:33], v104 offset:16384\n\t"
        "ds_read_b64_tr_b16 a[34:35], v104 offset:16640\n\t"
        "v_mfma_f32_16x16x32_bf16 v[20:23], a[0:3], v[60:63], 0\n\t"
        "s_waitcnt lgkmcnt(5)\n\t"
        "ds_read_b64_tr_b16 a[36:37], v103 offset:20480\n\t"
        "v_mfma_f32_16x16x32_bf16 v[24:27], a[4:7], v[60:63], 0\n\t"
        "ds_read_b64_tr_b16 a[38:39], v103 offset:20992\n\t"
        "ds_read_b64_tr_b16 a[40:41], v103 offset:20488\n\t"
        "v_mfma_f32_16x16x32_bf16 v[28:31], v[60:63], a[8:11], 0\n\t"
        "ds_read_b64_tr_b16 a[42:43], v103 offset:21000\n\t"
        "ds_read_b64_tr_b16 a[44:45], v104 offset:18432\n\t"
        "v_mfma_f32_16x16x32_bf16 v[8:11], a[12:15], v[64:67], v[8:11]\n\t"
        "ds_read_b64_tr_b16 a[46:47], v104 offset:18688\n\t"
        "ds_read_b128 v[72:75], v105 offset:8192\n\t"
        "v_mfma_f32_16x16x32_bf16 v[12:15], a[16:19], v[64:67], v[12:15]\n\t"
        "ds_read_b128 v[76:79], v105 offset:10240\n\t"
        "ds_read_b128 v[80:83], v106 offset:8192\n\t"
        "v_mfma_f32_16x16x32_bf16 v[16:19], v[64:67], a[20:23], v[16:19]\n\t"
        "ds_read_b128 v[84:87], v106 offset:10240\n\t"
        "v_mfma_f32_16x16x32_bf16 v[20:23], a[12:15], v[68:71], v[20:23]\n\t"
        "v_mfma_f32_16x16x32_bf16 v[24:27], a[16:19], v[68:71], v[24:27]\n\t"
        "v_mfma_f32_16x16x32_bf16 v[28:31], v[68:71], a[20:23], v[28:31]\n\t"
        "s_mov_b32 %[t0], 2621440\n\t"
        "buffer_load_dwordx4 a[252:255], v101, %[sq], %[t0] offen\n\t"
        "buffer_load_dwordx4 v[132:135], v112, %[sv], %[t0] offen\n\t"
        "s_mov_b32 %[t0], 2883584\n\t"
        "buffer_load_dwordx4 v[128:131], v101, %[sq], %[t0] offen\n\t"
        "s_waitcnt vmcnt(17)\n\t"
        "ds_write_b128 v102, a[216:219] offset:0\n\t"
        "ds_write_b128 v102, a[220:223] offset:4096\n\t"
        "ds_write_b128 v116, a[224:227] offset:8192\n\t"
        "s_waitcnt lgkmcnt(0)\n\t"
        "s_barrier\n\t"
        "v_mfma_f32_16x16x32_bf16 v[8:11], a[24:27], v[72:75], v[8:11]\n\t"
        "ds_read_b64_tr_b16 a[48:49], v103 offset:0\n\t"
        "ds_read_b64_tr_b16 a[50:51], v103 offset:512\n\t"
        "v_mfma_f32_16x16x32_bf16 v[12:15], a[28:31], v[72:75], v[12:15]\n\t"
        "ds_read_b64_tr_b16 a[52:53], v103 offset:8\n\t"
        "ds_read_b64_tr_b16 a[54:55], v103 offset:520\n\t"
        "v_mfma_f32_16x16x32_bf16 v[16:19], v[72:75], a[32:35], v[16:19]\n\t"
        "ds_read_b64_tr_b16 a[56:57], v104 offset:0\n\t"
        "ds_read_b64_tr_b16 a[58:59], v104 offset:256\n\t"
        "v_mfma_f32_16x16x32_bf16 v[20:23], a[24:27], v[76:79], v[20:23]\n\t"
        "s_waitcnt lgkmcnt(5)\n\t"
        "ds_read_b64_tr_b16 a[60:61], v103 offset:4096\n\t"
        "v_mfma_f32_16x16x32_bf16 v[24:27], a[28:31], v[76:79], v[24:27]\n\t"
        "ds_read_b64_tr_b16 a[62:63], v103 offset:4608\n\t"
        "ds_read_b64_tr_b16 a[64:65], v103 offset:4104\n\t"
        "v_mfma_f32_16x16x32_bf16 v[28:31], v[76:79], a[32:35], v[28:31]\n\t"
        "ds_read_b64_tr_b16 a[66:67], v103 offset:4616\n\t"
        "ds_read_b64_tr_b16 a[68:69], v104 offset:2048\n\t"
        "v_mfma_f32_16x16x32_bf16 v[8:11], a[36:39], v[80:83], v[8:11]\n\t"
        "ds_read_b64_tr_b16 a[70:71], v104 offset:2304\n\t"
        "ds_read_b128 v[56:59], v105 offset:16384\n\t"
        "v_mfma_f32_16x16x32_bf16 v[12:15], a[40:43], v[80:83], v[12:15]\n\t"
        "ds_read_b128 v[60:63], v105 offset:18432\n\t"
        "ds_read_b128 v[64:67], v106 offset:16384\n\t"
        "v_mfma_f32_16x16x32_bf16 v[16:19], v[80:83], a[44:47], v[16:19]\n\t"
        "ds_read_b128 v[68:71], v106 offset:18432\n\t"
        "v_mfma_f32_16x16x32_bf16 v[20:23], a[36:39], v[84:87], v[20:23]\n\t"
        "v_mfma_f32_16x16x32_bf16 v[24:27], a[40:43], v[84:87], v[24:27]\n\t"
        "v_mfma_f32_16x16x32_bf16 v[28:31], v[84:87], a[44:47], v[28:31]\n\t"
        "s_mov_b32 %[t0], 3145728\n\t"
        "buffer_load_dwordx4 v[136:139], v101, %[sq], %[t0] offen\n\t"
        "buffer_load_dwordx4 v[144:147], v112, %[sv], %[t0] offen\n\t"
        "s_mov_b32 %[t0], 3407872\n\t"
        "buffer_load_dwordx4 v[140:143], v101, %[sq], %[t0] offen\n\t"
        "s_waitcnt vmcnt(17)\n\t"
        "ds_write_b128 v102, a[228:231] offset:16384\n\t"
        "ds_write_b128 v102, a[232:235] offset:20480\n\t"
        "ds_write_b128 v116, a[236:239] offset:24576\n\t"
        "s_waitcnt lgkmcnt(0)\n\t"
        "s_barrier\n\t"
        "v_mfma_f32_16x16x32_bf16 v[8:11], a[48:51], v[56:59], v[8:11]\n\t"
        "ds_read_b64_tr_b16 a[72:73], v103 offset:16384\n\t"
        "ds_read_b64_tr_b16 a[74:75], v103 offset:16896\n\t"
        "v_mfma_f32_16x16x32_bf16 v[12:15], a[52:55], v[56:59], v[12:15]\n\t"
        "ds_read_b64_tr_b16 a[76:77], v103 offset:16392\n\t"
        "ds_read_b64_tr_b16 a[78:79], v103 offset:16904\n\t"
        "v_mfma_f32_16x16x32_bf16 v[16:19], v[56:59], a[56:59], v[16:19]\n\t"
        "ds_read_b64_tr_b16 a[80:81], v104 offset:16384\n\t"
        "ds_read_b64_tr_b16 a[82:83], v104 offset:16640\n\t"
        "v_mfma_f32_16x16x32_bf16 v[20:23], a[48:51], v[60:63], v[20:23]\n\t"
        "s_waitcnt lgkmcnt(5)\n\t"
        "ds_read_b64_tr_b16 a[84:85], v103 offset:20480\n\t"
        "v_mfma_f32_16x16x32_bf16 v[24:27], a[52:55], v[60:63], v[24:27]\n\t"
        "ds_read_b64_tr_b16 a[86:87], v103 offset:20992\n\t"
        "ds_read_b64_tr_b16 a[88:89], v103 offset:20488\n\t"
        "v_mfma_f32_16x16x32_bf16 v[28:31], v[60:63], a[56:59], v[28:31]\n\t"
        "ds_read_b64_tr_b16 a[90:91], v103 offset:21000\n\t"
        "ds_read_b64_tr_b16 a[92:93], v104 offset:18432\n\t"
        "v_mfma_f32_16x16x32_bf16 v[8:11], a[60:63], v[64:67], v[8:11]\n\t"
        "ds_read_b64_tr_b16 a[94:95], v104 offset:18688\n\t"
        "ds_read_b128 v[72:75], v105 offset:24576\n\t"
        "v_mfma_f32_16x16x32_bf16 v[12:15], a[64:67], v[64:67], v[12:15]\n\t"
        "ds_read_b128 v[76:79], v105 offset:26624\n\t"
        "ds_read_b128 v[80:83], v106 offset:24576\n\t"
        "v_mfma_f32_16x16x32_bf16 v[16:19], v[64:67], a[68:71], v[16:19]\n\t"
        "ds_read_b128 v[84:87], v106 offset:26624\n\t"
        "v_mfma_f32_16x16x32_bf16 v[20:23], a[60:63], v[68:71], v[20:23]\n\t"
        "v_mfma_f32_16x16x32_bf16 v[24:27], a[64:67], v[68:71], v[24:27]\n\t"
        "v_mfma_f32_16x16x32_bf16 v[28:31], v[68:71], a[68:71], v[28:31]\n\t"
        "s_mov_b32 %[t0], 3670016\n\t"
        "buffer_load_dwordx4 v[148:151], v101, %[sq], %[t0] offen\n\t"
        "buffer_load_dwordx4 v[156:159], v112, %[sv], %[t0] offen\n\t"
        "s_mov_b32 %[t0], 3932160\n\t"
        "buffer_load_dwordx4 v[152:155], v101, %[sq], %[t0] offen\n\t"
        "s_waitcnt vmcnt(9)\n\t"
        "ds_write_b128 v102, a[240:243] offset:0\n\t"
        "ds_write_b128 v102, a[244:247] offset:4096\n\t"
        "ds_write_b128 v116, a[248:251] offset:8192\n\t"
        "s_waitcnt lgkmcnt(0)\n\t"
        "s_barrier\n\t"
        "v_mfma_f32_16x16x32_bf16 v[8:11], a[72:75], v[72:75], v[8:11]\n\t"
        "ds_read_b64_tr_b16 a[96:97], v103 offset:0\n\t"
        "ds_read_b64_tr_b16 a[98:99], v103 offset:512\n\t"
        "v_mfma_f32_16x16x32_bf16 v[12:15], a[76:79], v[72:75], v[12:15]\n\t"
        "ds_read_b64_tr_b16 a[100:101], v103 offset:8\n\t"
        "ds_read_b64_tr_b16 a[102:103], v103 offset:520\n\t"
        "v_mfma_f32_16x16x32_bf16 v[16:19], v[72:75], a[80:83], v[16:19]\n\t"
        "ds_read_b64_tr_b16 a[104:105], v104 offset:0\n\t"
        "ds_read_b64_tr_b16 a[106:107], v104 offset:256\n\t"
        "v_mfma_f32_16x16x32_bf16 v[20:23], a[72:75], v[76:79], v[20:23]\n\t"
        "s_waitcnt lgkmcnt(5)\n\t"
        "ds_read_b64_tr_b16 a[108:109], v103 offset:4096\n\t"
        "v_mfma_f32_16x16x32_bf16 v[24:27], a[76:79], v[76:79], v[24:27]\n\t"
        "ds_read_b64_tr_b16 a[110:111], v103 offset:4608\n\t"
        "ds_read_b64_tr_b16 a[112:113], v103 offset:4104\n\t"
        "v_mfma_f32_16x16x32_bf16 v[28:31], v[76:79], a[80:83], v[28:31]\n\t"
        "ds_read_b64_tr_b16 a[114:115], v103 offset:4616\n\t"
        "ds_read_b64_tr_b16 a[116:117], v104 offset:2048\n\t"
        "v_mfma_f32_16x16x32_bf16 v[8:11], a[84:87], v[80:83], v[8:11]\n\t"
        "ds_read_b64_tr_b16 a[118:119], v104 offset:2304\n\t"
        "ds_read_b128 v[56:59], v105 offset:32768\n\t"
        "v_mfma_f32_16x16x32_bf16 v[12:15], a[88:91], v[80:83], v[12:15]\n\t"
        "ds_read_b128 v[60:63], v105 offset:34816\n\t"
        "ds_read_b128 v[64:67], v106 offset:32768\n\t"
        "v_mfma_f32_16x16x32_bf16 v[16:19], v[80:83], a[92:95], v[16:19]\n\t"
        "ds_read_b128 v[68:71], v106 offset:34816\n\t"
        "v_mfma_f32_16x16x32_bf16 v[20:23], a[84:87], v[84:87], v[20:23]\n\t"
        "v_mfma_f32_16x16x32_bf16 v[24:27], a[88:91], v[84:87], v[24:27]\n\t"
        "v_mfma_f32_16x16x32_bf16 v[28:31], v[84:87], a[92:95], v[28:31]\n\t"
        "s_add_u32 m0, %[ld], 65536\n\t"
        "s_add_u32 %[t0], %[xo], 1024\n\t"
        "buffer_load_dwordx4 v100, %[sx], %[t0] offen lds\n\t"
        "s_add_u32 m0, %[ld], 66560\n\t"
        "s_add_u32 %[t0], %[xo], 17408\n\t"
        "buffer_load_dwordx4 v100, %[sx], %[t0] offen lds\n\t"
        "s_add_u32 m0, %[ld], 73728\n\t"
        "s_add_u32 %[t0], %[xo], 1152\n\t"
        "buffer_load_dwordx4 v100, %[sx], %[t0] offen lds\n\t"
        "s_add_u32 m0, %[ld], 74752\n\t"
        "s_add_u32 %[t0], %[xo], 17536\n\t"
        "buffer_load_dwordx4 v100, %[sx], %[t0] offen lds\n\t"
        "s_add_u32 m0, %[ld], 81920\n\t"
        "s_add_u32 %[t0], %[xo], 1280\n\t"
        "buffer_load_dwordx4 v100, %[sx], %[t0] offen lds\n\t"
        "s_add_u32 m0, %[ld], 82944\n\t"
        "s_add_u32 %[t0], %[xo], 17664\n\t"
        "buffer_load_dwordx4 v100, %[sx], %[t0] offen lds\n\t"
        "s_add_u32 m0, %[ld], 90112\n\t"
        "s_add_u32 %[t0], %[xo], 1408\n\t"
        "buffer_load_dwordx4 v100, %[sx], %[t0] offen lds\n\t"
        "s_add_u32 m0, %[ld], 91136\n\t"
        "s_add_u32 %[t0], %[xo], 17792\n\t"
        "buffer_load_dwordx4 v100, %[sx], %[t0] offen lds\n\t"
        "s_mov_b32 %[t0], 4194304\n\t"
        "buffer_load_dwordx4 v[160:163], v101, %[sq], %[t0] offen\n\t"
        "buffer_load_dwordx4 v[168:171], v112, %[sv], %[t0] offen\n\t"
        "s_mov_b32 %[t0], 4456448\n\t"
        "buffer_load_dwordx4 v[164:167], v101, %[sq], %[t0] offen\n\t"
        "s_waitcnt vmcnt(17)\n\t"
        "ds_write_b128 v102, a[252:255] offset:16384\n\t"
        "ds_write_b128 v102, v[128:131] offset:20480\n\t"
        "ds_write_b128 v116, v[132:135] offset:24576\n\t"
        "s_waitcnt lgkmcnt(0)\n\t"
        "s_barrier\n\t"
        "v_mfma_f32_16x16x32_bf16 v[8:11], a[96:99], v[56:59], v[8:11]\n\t"
        "ds_read_b64_tr_b16 a[120:121], v103 offset:16384\n\t"
        "ds_read_b64_tr_b16 a[122:123], v103 offset:16896\n\t"
        "v_mfma_f32_16x16x32_bf16 v[12:15], a[100:103], v[56:59], v[12:15]\n\t"
        "ds_read_b64_tr_b16 a[124:125], v103 offset:16392\n\t"
        "ds_read_b64_tr_b16 a[126:127], v103 offset:16904\n\t"
        "v_mfma_f32_16x16x32_bf16 v[16:19], v[56:59], a[104:107], v[16:19]\n\t"
        "ds_read_b64_tr_b16 a[128:129], v104 offset:16384\n\t"
        "ds_read_b64_tr_b16 a[130:131], v104 offset:16640\n\t"
        "v_mfma_f32_16x16x32_bf16 v[20:23], a[96:99], v[60:63], v[20:23]\n\t"
        "s_waitcnt lgkmcnt(5)\n\t"
        "ds_read_b64_tr_b16 a[132:133], v103 offset:20480\n\t"
        "v_mfma_f32_16x16x32_bf16 v[24:27], a[100:103], v[60:63], v[24:27]\n\t"
        "ds_read_b64_tr_b16 a[134:135], v103 offset:20992\n\t"
        "ds_read_b64_tr_b16 a[136:137], v103 offset:20488\n\t"
        "v_mfma_f32_16x16x32_bf16 v[28:31], v[60:63], a[104:107], v[28:31]\n\t"
        "ds_read_b64_tr_b16 a[138:139], v103 offset:21000\n\t"
        "ds_read_b64_tr_b16 a[140:141], v104 offset:18432\n\t"
        "v_mfma_f32_16x16x32_bf16 v[8:11], a[108:111], v[64:67], v[8:11]\n\t"
        "ds_read_b64_tr_b16 a[142:143], v104 offset:18688\n\t"
        "ds_read_b128 v[72:75], v105 offset:40960\n\t"
        "v_mfma_f32_16x16x32_bf16 v[12:15], a[112:115], v[64:67], v[12:15]\n\t"
        "ds_read_b128 v[76:79], v105 offset:43008\n\t"
        "ds_read_b128 v[80:83], v106 offset:40960\n\t"
        "v_mfma_f32_16x16x32_bf16 v[16:19], v[64:67], a[116:119], v[16:19]\n\t"
        "ds_read_b128 v[84:87], v106 offset:43008\n\t"
        "v_mfma_f32_16x16x32_bf16 v[20:23], a[108:111], v[68:71], v[20:23]\n\t"
        "v_mfma_f32_16x16x32_bf16 v[24:27], a[112:115], v[68:71], v[24:27]\n\t"
        "v_mfma_f32_16x16x32_bf16 v[28:31], v[68:71], a[116:119], v[28:31]\n\t"
        "s_mov_b32 %[t0], 4718592\n\t"
        "buffer_load_dwordx4 v[172:175], v101, %[sq], %[t0] offen\n\t"
        "buffer_load_dwordx4 v[180:183], v112, %[sv], %[t0] offen\n\t"
        "s_mov_b32 %[t0], 4980736\n\t"
        "buffer_load_dwordx4 v[176:179], v101, %[sq], %[t0] offen\n\t"
        "s_waitcnt vmcnt(17)\n\t"
        "ds_write_b128 v102, v[136:139] offset:0\n\t"
        "ds_write_b128 v102, v[140:143] offset:4096\n\t"
        "ds_write_b128 v116, v[144:147] offset:8192\n\t"
        "s_waitcnt lgkmcnt(0)\n\t"
        "s_barrier\n\t"
        "v_mfma_f32_16x16x32_bf16 v[8:11], a[120:123], v[72:75], v[8:11]\n\t"
        "ds_read_b64_tr_b16 a[144:145], v103 offset:0\n\t"
        "ds_read_b64_tr_b16 a[146:147], v103 offset:512\n\t"
        "v_mfma_f32_16x16x32_bf16 v[12:15], a[124:127], v[72:75], v[12:15]\n\t"
        "ds_read_b64_tr_b16 a[148:149], v103 offset:8\n\t"
        "ds_read_b64_tr_b16 a[150:151], v103 offset:520\n\t"
        "v_mfma_f32_16x16x32_bf16 v[16:19], v[72:75], a[128:131], v[16:19]\n\t"
        "ds_read_b64_tr_b16 a[152:153], v104 offset:0\n\t"
        "ds_read_b64_tr_b16 a[154:155], v104 offset:256\n\t"
        "v_mfma_f32_16x16x32_bf16 v[20:23], a[120:123], v[76:79], v[20:23]\n\t"
        "s_waitcnt lgkmcnt(5)\n\t"
        "ds_read_b64_tr_b16 a[156:157], v103 offset:4096\n\t"
        "v_mfma_f32_16x16x32_bf16 v[24:27], a[124:127], v[76:79], v[24:27]\n\t"
        "ds_read_b64_tr_b16 a[158:159], v103 offset:4608\n\t"
        "ds_read_b64_tr_b16 a[160:161], v103 offset:4104\n\t"
        "v_mfma_f32_16x16x32_bf16 v[28:31], v[76:79], a[128:131], v[28:31]\n\t"
        "ds_read_b64_tr_b16 a[162:163], v103 offset:4616\n\t"
        "ds_read_b64_tr_b16 a[164:165], v104 offset:2048\n\t"
        "v_mfma_f32_16x16x32_bf16 v[8:11], a[132:135], v[80:83], v[8:11]\n\t"
        "ds_read_b64_tr_b16 a[166:167], v104 offset:2304\n\t"
        "ds_read_b128 v[56:59], v105 offset:49152\n\t"
        "v_mfma_f32_16x16x32_bf16 v[12:15], a[136:139], v[80:83], v[12:15]\n\t"
        "ds_read_b128 v[60:63], v105 offset:51200\n\t"
        "ds_read_b128 v[64:67], v106 offset:49152\n\t"
        "v_mfma_f32_16x16x32_bf16 v[16:19], v[80:83], a[140:143], v[16:19]\n\t"
        "ds_read_b128 v[68:71], v106 offset:51200\n\t"
        "v_mfma_f32_16x16x32_bf16 v[20:23], a[132:135], v[84:87], v[20:23]\n\t"
        "v_mfma_f32_16x16x32_bf16 v[24:27], a[136:139], v[84:87], v[24:27]\n\t"
        "v_mfma_f32_16x16x32_bf16 v[28:31], v[84:87], a[140:143], v[28:31]\n\t"
        "s_mov_b32 %[t0], 5242880\n\t"
        "buffer_load_dwordx4 v[184:187], v101, %[sq], %[t0] offen\n\t"
        "buffer_load_dwordx4 v[192:195], v112, %[sv], %[t0] offen\n\t"
        "s_mov_b32 %[t0], 5505024\n\t"
        "buffer_load_dwordx4 v[188:191], v101, %[sq], %[t0] offen\n\t"
        "s_waitcnt vmcnt(17)\n\t"
        "ds_write_b128 v102, v[148:151] offset:16384\n\t"
        "ds_write_b128 v102, v[152:155] offset:20480\n\t"
        "ds_write_b128 v116, v[156:159] offset:24576\n\t"
        "s_waitcnt lgkmcnt(0)\n\t"
        "s_barrier\n\t"
        "v_mfma_f32_16x16x32_bf16 v[8:11], a[144:147], v[56:59], v[8:11]\n\t"
        "ds_read_b64_tr_b16 a[168:169], v103 offset:16384\n\t"
        "ds_read_b64_tr_b16 a[170:171], v103 offset:16896\n\t"
        "v_mfma_f32_16x16x32_bf16 v[12:15], a[148:151], v[56:59], v[12:15]\n\t"
        "ds_read_b64_tr_b16 a[172:173], v103 offset:16392\n\t"
        "ds_read_b64_tr_b16 a[174:175], v103 offset:16904\n\t"
        "v_mfma_f32_16x16x32_bf16 v[16:19], v[56:59], a[152:155], v[16:19]\n\t"
        "ds_read_b64_tr_b16 a[176:177], v104 offset:16384\n\t"
        "ds_read_b64_tr_b16 a[178:179], v104 offset:16640\n\t"
        "v_mfma_f32_16x16x32_bf16 v[20:23], a[144:147], v[60:63], v[20:23]\n\t"
        "s_waitcnt lgkmcnt(5)\n\t"
        "ds_read_b64_tr_b16 a[180:181], v103 offset:20480\n\t"
        "v_mfma_f32_16x16x32_bf16 v[24:27], a[148:151], v[60:63], v[24:27]\n\t"
        "ds_read_b64_tr_b16 a[182:183], v103 offset:20992\n\t"
        "ds_read_b64_tr_b16 a[184:185], v103 offset:20488\n\t"
        "v_mfma_f32_16x16x32_bf16 v[28:31], v[60:63], a[152:155], v[28:31]\n\t"
        "ds_read_b64_tr_b16 a[186:187], v103 offset:21000\n\t"
        "ds_read_b64_tr_b16 a[188:189], v104 offset:18432\n\t"
        "v_mfma_f32_16x16x32_bf16 v[8:11], a[156:159], v[64:67], v[8:11]\n\t"
        "ds_read_b64_tr_b16 a[190:191], v104 offset:18688\n\t"
        "ds_read_b128 v[72:75], v105 offset:57344\n\t"
        "v_mfma_f32_16x16x32_bf16 v[12:15], a[160:163], v[64:67], v[12:15]\n\t"
        "ds_read_b128 v[76:79], v105 offset:59392\n\t"
        "ds_read_b128 v[80:83], v106 offset:57344\n\t"
        "v_mfma_f32_16x16x32_bf16 v[16:19], v[64:67], a[164:167], v[16:19]\n\t"
        "ds_read_b128 v[84:87], v106 offset:59392\n\t"
        "v_mfma_f32_16x16x32_bf16 v[20:23], a[156:159], v[68:71], v[20:23]\n\t"
        "v_mfma_f32_16x16x32_bf16 v[24:27], a[160:163], v[68:71], v[24:27]\n\t"
        "v_mfma_f32_16x16x32_bf16 v[28:31], v[68:71], a[164:167], v[28:31]\n\t"
        "s_mov_b32 %[t0], 5767168\n\t"
        "buffer_load_dwordx4 v[196:199], v101, %[sq], %[t0] offen\n\t"
        "buffer_load_dwordx4 v[204:207], v112, %[sv], %[t0] offen\n\t"
        "s_mov_b32 %[t0], 6029312\n\t"
        "buffer_load_dwordx4 v[200:203], v101, %[sq], %[t0] offen\n\t"
        "s_waitcnt vmcnt(9)\n\t"
        "ds_write_b128 v102, v[160:163] offset:0\n\t"
        "ds_write_b128 v102, v[164:167] offset:4096\n\t"
        "ds_write_b128 v116, v[168:171] offset:8192\n\t"
        "s_waitcnt lgkmcnt(0)\n\t"
        "s_barrier\n\t"
        "v_mfma_f32_16x16x32_bf16 v[8:11], a[168:171], v[72:75], v[8:11]\n\t"
        "ds_read_b64_tr_b16 a[192:193], v103 offset:0\n\t"
        "ds_read_b64_tr_b16 a[194:195], v103 offset:512\n\t"
        "v_mfma_f32_16x16x32_bf16 v[12:15], a[172:175], v[72:75], v[12:15]\n\t"
        "ds_read_b64_tr_b16 a[196:197], v103 offset:8\n\t"
        "ds_read_b64_tr_b16 a[198:199], v103 offset:520\n\t"
        "v_mfma_f32_16x16x32_bf16 v[16:19], v[72:75], a[176:179], v[16:19]\n\t"
        "ds_read_b64_tr_b16 a[200:201], v104 offset:0\n\t"
        "ds_read_b64_tr_b16 a[202:203], v104 offset:256\n\t"
        "v_mfma_f32_16x16x32_bf16 v[20:23], a[168:171], v[76:79], v[20:23]\n\t"
        "s_waitcnt lgkmcnt(5)\n\t"
        "ds_read_b64_tr_b16 a[204:205], v103 offset:4096\n\t"
        "v_mfma_f32_16x16x32_bf16 v[24:27], a[172:175], v[76:79], v[24:27]\n\t"
        "ds_read_b64_tr_b16 a[206:207], v103 offset:4608\n\t"
        "ds_read_b64_tr_b16 a[208:209], v103 offset:4104\n\t"
        "v_mfma_f32_16x16x32_bf16 v[28:31], v[76:79], a[176:179], v[28:31]\n\t"
        "ds_read_b64_tr_b16 a[210:211], v103 offset:4616\n\t"
        "ds_read_b64_tr_b16 a[212:213], v104 offset:2048\n\t"
        "v_mfma_f32_16x16x32_bf16 v[8:11], a[180:183], v[80:83], v[8:11]\n\t"
        "ds_read_b64_tr_b16 a[214:215], v104 offset:2304\n\t"
        "ds_read_b128 v[56:59], v107 offset:0\n\t"
        "v_mfma_f32_16x16x32_bf16 v[12:15], a[184:187], v[80:83], v[12:15]\n\t"
        "ds_read_b128 v[60:63], v107 offset:2048\n\t"
        "ds_read_b128 v[64:67], v108 offset:0\n\t"
        "v_mfma_f32_16x16x32_bf16 v[16:19], v[80:83], a[188:191], v[16:19]\n\t"
        "ds_read_b128 v[68:71], v108 offset:2048\n\t"
        "v_mfma_f32_16x16x32_bf16 v[20:23], a[180:183], v[84:87], v[20:23]\n\t"
        "v_mfma_f32_16x16x32_bf16 v[24:27], a[184:187], v[84:87], v[24:27]\n\t"
        "v_mfma_f32_16x16x32_bf16 v[28:31], v[84:87], a[188:191], v[28:31]\n\t"
        "s_add_u32 m0, %[ld], 98304\n\t"
        "s_add_u32 %[t0], %[xo], 1536\n\t"
        "buffer_load_dwordx4 v100, %[sx], %[t0] offen lds\n\t"
        "s_add_u32 m0, %[ld], 99328\n\t"
        "s_add_u32 %[t0], %[xo], 17920\n\t"
        "buffer_load_dwordx4 v100, %[sx], %[t0] offen lds\n\t"
        "s_add_u32 m0, %[ld], 106496\n\t"
        "s_add_u32 %[t0], %[xo], 1664\n\t"
        "buffer_load_dwordx4 v100, %[sx], %[t0] offen lds\n\t"
        "s_add_u32 m0, %[ld], 107520\n\t"
        "s_add_u32 %[t0], %[xo], 18048\n\t"
        "buffer_load_dwordx4 v100, %[sx], %[t0] offen lds\n\t"
        "s_add_u32 m0, %[ld], 114688\n\t"
        "s_add_u32 %[t0], %[xo], 1792\n\t"
        "buffer_load_dwordx4 v100, %[sx], %[t0] offen lds\n\t"
        "s_add_u32 m0, %[ld], 115712\n\t"
        "s_add_u32 %[t0], %[xo], 18176\n\t"
        "buffer_load_dwordx4 v100, %[sx], %[t0] offen lds\n\t"
        "s_add_u32 m0, %[ld], 122880\n\t"
        "s_add_u32 %[t0], %[xo], 1920\n\t"
        "buffer_load_dwordx4 v100, %[sx], %[t0] offen lds\n\t"
        "s_add_u32 m0, %[ld], 123904\n\t"
        "s_add_u32 %[t0], %[xo], 18304\n\t"
        "buffer_load_dwordx4 v100, %[sx], %[t0] offen lds\n\t"
        "s_mov_b32 %[t0], 6291456\n\t"
        "buffer_load_dwordx4 v[208:211], v101, %[sq], %[t0] offen\n\t"
        "buffer_load_dwordx4 v[216:219], v112, %[sv], %[t0] offen\n\t"
        "s_mov_b32 %[t0], 6553600\n\t"
        "buffer_load_dwordx4 v[212:215], v101, %[sq], %[t0] offen\n\t"
        "s_waitcnt vmcnt(17)\n\t"
        "ds_write_b128 v102, v[172:175] offset:16384\n\t"
        "ds_write_b128 v102, v[176:179] offset:20480\n\t"
        "ds_write_b128 v116, v[180:183] offset:24576\n\t"
        "s_waitcnt lgkmcnt(0)\n\t"
        "s_barrier\n\t"
        "v_mfma_f32_16x16x32_bf16 v[8:11], a[192:195], v[56:59], v[8:11]\n\t"
        "ds_read_b64_tr_b16 a[216:217], v103 offset:16384\n\t"
        "ds_read_b64_tr_b16 a[218:219], v103 offset:16896\n\t"
        "v_mfma_f32_16x16x32_bf16 v[12:15], a[196:199], v[56:59], v[12:15]\n\t"
        "ds_read_b64_tr_b16 a[220:221], v103 offset:16392\n\t"
        "ds_read_b64_tr_b16 a[222:223], v103 offset:16904\n\t"
        "v_mfma_f32_16x16x32_bf16 v[16:19], v[56:59], a[200:203], v[16:19]\n\t"
        "ds_read_b64_tr_b16 a[224:225], v104 offset:16384\n\t"
        "ds_read_b64_tr_b16 a[226:227], v104 offset:16640\n\t"
        "v_mfma_f32_16x16x32_bf16 v[20:23], a[192:195], v[60:63], v[20:23]\n\t"
        "s_waitcnt lgkmcnt(5)\n\t"
        "ds_read_b64_tr_b16 a[228:229], v103 offset:20480\n\t"
        "v_mfma_f32_16x16x32_bf16 v[24:27], a[196:199], v[60:63], v[24:27]\n\t"
        "ds_read_b64_tr_b16 a[230:231], v103 offset:20992\n\t"
        "ds_read_b64_tr_b16 a[232:233], v103 offset:20488\n\t"
        "v_mfma_f32_16x16x32_bf16 v[28:31], v[60:63], a[200:203], v[28:31]\n\t"
        "ds_read_b64_tr_b16 a[234:235], v103 offset:21000\n\t"
        "ds_read_b64_tr_b16 a[236:237], v104 offset:18432\n\t"
        "v_mfma_f32_16x16x32_bf16 v[8:11], a[204:207], v[64:67], v[8:11]\n\t"
        "ds_read_b64_tr_b16 a[238:239], v104 offset:18688\n\t"
        "ds_read_b128 v[72:75], v107 offset:8192\n\t"
        "v_mfma_f32_16x16x32_bf16 v[12:15], a[208:211], v[64:67], v[12:15]\n\t"
        "ds_read_b128 v[76:79], v107 offset:10240\n\t"
        "ds_read_b128 v[80:83], v108 offset:8192\n\t"
        "v_mfma_f32_16x16x32_bf16 v[16:19], v[64:67], a[212:215], v[16:19]\n\t"
        "ds_read_b128 v[84:87], v108 offset:10240\n\t"
        "v_mfma_f32_16x16x32_bf16 v[20:23], a[204:207], v[68:71], v[20:23]\n\t"
        "v_mfma_f32_16x16x32_bf16 v[24:27], a[208:211], v[68:71], v[24:27]\n\t"
        "v_mfma_f32_16x16x32_bf16 v[28:31], v[68:71], a[212:215], v[28:31]\n\t"
        "s_mov_b32 %[t0], 6815744\n\t"
        "buffer_load_dwordx4 v[220:223], v101, %[sq], %[t0] offen\n\t"
        "buffer_load_dwordx4 v[228:231], v112, %[sv], %[t0] offen\n\t"
        "s_mov_b32 %[t0], 7077888\n\t"
        "buffer_load_dwordx4 v[224:227], v101, %[sq], %[t0] offen\n\t"
        "s_waitcnt vmcnt(17)\n\t"
        "ds_write_b128 v102, v[184:187] offset:0\n\t"
        "ds_write_b128 v102, v[188:191] offset:4096\n\t"
        "ds_write_b128 v116, v[192:195] offset:8192\n\t"
        "s_waitcnt lgkmcnt(0)\n\t"
        "s_barrier\n\t"
        "v_mfma_f32_16x16x32_bf16 v[8:11], a[216:219], v[72:75], v[8:11]\n\t"
        "ds_read_b64_tr_b16 a[240:241], v103 offset:0\n\t"
        "ds_read_b64_tr_b16 a[242:243], v103 offset:512\n\t"
        "v_mfma_f32_16x16x32_bf16 v[12:15], a[220:223], v[72:75], v[12:15]\n\t"
        "ds_read_b64_tr_b16 a[244:245], v103 offset:8\n\t"
        "ds_read_b64_tr_b16 a[246:247], v103 offset:520\n\t"
        "v_mfma_f32_16x16x32_bf16 v[16:19], v[72:75], a[224:227], v[16:19]\n\t"
        "ds_read_b64_tr_b16 a[248:249], v104 offset:0\n\t"
        "ds_read_b64_tr_b16 a[250:251], v104 offset:256\n\t"
        "v_mfma_f32_16x16x32_bf16 v[20:23], a[216:219], v[76:79], v[20:23]\n\t"
        "s_waitcnt lgkmcnt(5)\n\t"
        "ds_read_b64_tr_b16 a[252:253], v103 offset:4096\n\t"
        "v_mfma_f32_16x16x32_bf16 v[24:27], a[220:223], v[76:79], v[24:27]\n\t"
        "ds_read_b64_tr_b16 a[254:255], v103 offset:4608\n\t"
        "ds_read_b64_tr_b16 v[128:129], v103 offset:4104\n\t"
        "v_mfma_f32_16x16x32_bf16 v[28:31], v[76:79], a[224:227], v[28:31]\n\t"
        "ds_read_b64_tr_b16 v[130:131], v103 offset:4616\n\t"
        "ds_read_b64_tr_b16 v[132:133], v104 offset:2048\n\t"
        "v_mfma_f32_16x16x32_bf16 v[8:11], a[228:231], v[80:83], v[8:11]\n\t"
        "ds_read_b64_tr_b16 v[134:135], v104 offset:2304\n\t"
        "ds_read_b128 v[56:59], v107 offset:16384\n\t"
        "v_mfma_f32_16x16x32_bf16 v[12:15], a[232:235], v[80:83], v[12:15]\n\t"
        "ds_read_b128 v[60:63], v107 offset:18432\n\t"
        "ds_read_b128 v[64:67], v108 offset:16384\n\t"
        "v_mfma_f32_16x16x32_bf16 v[16:19], v[80:83], a[236:239], v[16:19]\n\t"
        "ds_read_b128 v[68:71], v108 offset:18432\n\t"
        "v_mfma_f32_16x16x32_bf16 v[20:23], a[228:231], v[84:87], v[20:23]\n\t"
        "v_mfma_f32_16x16x32_bf16 v[24:27], a[232:235], v[84:87], v[24:27]\n\t"
        "v_mfma_f32_16x16x32_bf16 v[28:31], v[84:87], a[236:239], v[28:31]\n\t"
        "s_mov_b32 %[t0], 7340032\n\t"
        "buffer_load_dwordx4 v[232:235], v101, %[sq], %[t0] offen\n\t"
        "buffer_load_dwordx4 v[240:243], v112, %[sv], %[t0] offen\n\t"
        "s_mov_b32 %[t0], 7602176\n\t"
        "buffer_load_dwordx4 v[236:239], v101, %[sq], %[t0] offen\n\t"
        "s_waitcnt vmcnt(17)\n\t"
        "ds_write_b128 v102, v[196:199] offset:16384\n\t"
        "ds_write_b128 v102, v[200:203] offset:20480\n\t"
        "ds_write_b128 v116, v[204:207] offset:24576\n\t"
        "s_waitcnt lgkmcnt(0)\n\t"
        "s_barrier\n\t"
        "v_mfma_f32_16x16x32_bf16 v[8:11], a[240:243], v[56:59], v[8:11]\n\t"
        "ds_read_b64_tr_b16 v[136:137], v103 offset:16384\n\t"
        "ds_read_b64_tr_b16 v[138:139], v103 offset:16896\n\t"
        "v_mfma_f32_16x16x32_bf16 v[12:15], a[244:247], v[56:59], v[12:15]\n\t"
        "ds_read_b64_tr_b16 v[140:141], v103 offset:16392\n\t"
        "ds_read_b64_tr_b16 v[142:143], v103 offset:16904\n\t"
        "v_mfma_f32_16x16x32_bf16 v[16:19], v[56:59], a[248:251], v[16:19]\n\t"
        "ds_read_b64_tr_b16 v[144:145], v104 offset:16384\n\t"
        "ds_read_b64_tr_b16 v[146:147], v104 offset:16640\n\t"
        "v_mfma_f32_16x16x32_bf16 v[20:23], a[240:243], v[60:63], v[20:23]\n\t"
        "s_waitcnt lgkmcnt(5)\n\t"
        "ds_read_b64_tr_b16 v[148:149], v103 offset:20480\n\t"
        "v_mfma_f32_16x16x32_bf16 v[24:27], a[244:247], v[60:63], v[24:27]\n\t"
        "ds_read_b64_tr_b16 v[150:151], v103 offset:20992\n\t"
        "ds_read_b64_tr_b16 v[152:153], v103 offset:20488\n\t"
        "v_mfma_f32_16x16x32_bf16 v[28:31], v[60:63], a[248:251], v[28:31]\n\t"
        "ds_read_b64_tr_b16 v[154:155], v103 offset:21000\n\t"
        "ds_read_b64_tr_b16 v[156:157], v104 offset:18432\n\t"
        "v_mfma_f32_16x16x32_bf16 v[8:11], a[252:255], v[64:67], v[8:11]\n\t"
        "ds_read_b64_tr_b16 v[158:159], v104 offset:18688\n\t"
        "ds_read_b128 v[72:75], v107 offset:24576\n\t"
        "v_mfma_f32_16x16x32_bf16 v[12:15], v[128:131], v[64:67], v[12:15]\n\t"
        "ds_read_b128 v[76:79], v107 offset:26624\n\t"
        "ds_read_b128 v[80:83], v108 offset:24576\n\t"
        "v_mfma_f32_16x16x32_bf16 v[16:19], v[64:67], v[132:135], v[16:19]\n\t"
        "ds_read_b128 v[84:87], v108 offset:26624\n\t"
        "v_mfma_f32_16x16x32_bf16 v[20:23], a[252:255], v[68:71], v[20:23]\n\t"
        "v_mfma_f32_16x16x32_bf16 v[24:27], v[128:131], v[68:71], v[24:27]\n\t"
        "v_mfma_f32_16x16x32_bf16 v[28:31], v[68:71], v[132:135], v[28:31]\n\t"
        "s_mov_b32 %[t0], 7864320\n\t"
        "buffer_load_dwordx4 v[244:247], v101, %[sq], %[t0] offen\n\t"
        "buffer_load_dwordx4 v[252:255], v112, %[sv], %[t0] offen\n\t"
        "s_mov_b32 %[t0], 8126464\n\t"
        "buffer_load_dwordx4 v[248:251], v101, %[sq], %[t0] offen\n\t"
        "s_waitcnt vmcnt(9)\n\t"
        "ds_write_b128 v102, v[208:211] offset:0\n\t"
        "ds_write_b128 v102, v[212:215] offset:4096\n\t"
        "ds_write_b128 v116, v[216:219] offset:8192\n\t"
        "s_waitcnt lgkmcnt(0)\n\t"
        "s_barrier\n\t"
        "v_mfma_f32_16x16x32_bf16 v[8:11], v[136:139], v[72:75], v[8:11]\n\t"
        "ds_read_b64_tr_b16 v[160:161], v103 offset:0\n\t"
        "ds_read_b64_tr_b16 v[162:163], v103 offset:512\n\t"
        "v_mfma_f32_16x16x32_bf16 v[12:15], v[140:143], v[72:75], v[12:15]\n\t"
        "ds_read_b64_tr_b16 v[164:165], v103 offset:8\n\t"
        "ds_read_b64_tr_b16 v[166:167], v103 offset:520\n\t"
        "v_mfma_f32_16x16x32_bf16 v[16:19], v[72:75], v[144:147], v[16:19]\n\t"
        "ds_read_b64_tr_b16 v[168:169], v104 offset:0\n\t"
        "ds_read_b64_tr_b16 v[170:171], v104 offset:256\n\t"
        "v_mfma_f32_16x16x32_bf16 v[20:23], v[136:139], v[76:79], v[20:23]\n\t"
        "s_waitcnt lgkmcnt(5)\n\t"
        "ds_read_b64_tr_b16 v[172:173], v103 offset:4096\n\t"
        "v_mfma_f32_16x16x32_bf16 v[24:27], v[140:143], v[76:79], v[24:27]\n\t"
        "ds_read_b64_tr_b16 v[174:175], v103 offset:4608\n\t"
        "ds_read_b64_tr_b16 v[176:177], v103 offset:4104\n\t"
        "v_mfma_f32_16x16x32_bf16 v[28:31], v[76:79], v[144:147], v[28:31]\n\t"
        "ds_read_b64_tr_b16 v[178:179], v103 offset:4616\n\t"
        "ds_read_b64_tr_b16 v[180:181], v104 offset:2048\n\t"
        "v_mfma_f32_16x16x32_bf16 v[8:11], v[148:151], v[80:83], v[8:11]\n\t"
        "ds_read_b64_tr_b16 v[182:183], v104 offset:2304\n\t"
        "ds_read_b128 v[56:59], v107 offset:32768\n\t"
        "v_mfma_f32_16x16x32_bf16 v[12:15], v[152:155], v[80:83], v[12:15]\n\t"
        "ds_read_b128 v[60:63], v107 offset:34816\n\t"
        "ds_read_b128 v[64:67], v108 offset:32768\n\t"
        "v_mfma_f32_16x16x32_bf16 v[16:19], v[80:83], v[156:159], v[16:19]\n\t"
        "ds_read_b128 v[68:71], v108 offset:34816\n\t"
        "v_mfma_f32_16x16x32_bf16 v[20:23], v[148:151], v[84:87], v[20:23]\n\t"
        "v_mfma_f32_16x16x32_bf16 v[24:27], v[152:155], v[84:87], v[24:27]\n\t"
        "v_mfma_f32_16x16x32_bf16 v[28:31], v[84:87], v[156:159], v[28:31]\n\t"
        "s_mov_b32 %[xo], 0x20000\n\t"
        "s_add_u32 m0, %[ld], 0\n\t"
        "s_add_u32 %[t0], %[xo], 0\n\t"
        "buffer_load_dwordx4 v100, %[sx], %[t0] offen lds\n\t"
        "s_add_u32 m0, %[ld], 1024\n\t"
        "s_add_u32 %[t0], %[xo], 16384\n\t"
        "buffer_load_dwordx4 v100, %[sx], %[t0] offen lds\n\t"
        "s_add_u32 m0, %[ld], 8192\n\t"
        "s_add_u32 %[t0], %[xo], 128\n\t"
        "buffer_load_dwordx4 v100, %[sx], %[t0] offen lds\n\t"
        "s_add_u32 m0, %[ld], 9216\n\t"
        "s_add_u32 %[t0], %[xo], 16512\n\t"
        "buffer_load_dwordx4 v100, %[sx], %[t0] offen lds\n\t"
        "s_add_u32 m0, %[ld], 16384\n\t"
        "s_add_u32 %[t0], %[xo], 256\n\t"
        "buffer_load_dwordx4 v100, %[sx], %[t0] offen lds\n\t"
        "s_add_u32 m0, %[ld], 17408\n\t"
        "s_add_u32 %[t0], %[xo], 16640\n\t"
        "buffer_load_dwordx4 v100, %[sx], %[t0] offen lds\n\t"
        "s_add_u32 m0, %[ld], 24576\n\t"
        "s_add_u32 %[t0], %[xo], 384\n\t"
        "buffer_load_dwordx4 v100, %[sx], %[t0] offen lds\n\t"
        "s_add_u32 m0, %[ld], 25600\n\t"
        "s_add_u32 %[t0], %[xo], 16768\n\t"
        "buffer_load_dwordx4 v100, %[sx], %[t0] offen lds\n\t"
        "s_mov_b32 %[xo], 0\n\t"
        "s_waitcnt vmcnt(14)\n\t"
        "ds_write_b128 v102, v[220:223] offset:16384\n\t"
        "ds_write_b128 v102, v[224:227] offset:20480\n\t"
        "ds_write_b128 v116, v[228:231] offset:24576\n\t"
        "s_waitcnt lgkmcnt(0)\n\t"
        "s_barrier\n\t"
        "v_mfma_f32_16x16x32_bf16 v[8:11], v[160:163], v[56:59], v[8:11]\n\t"
        "ds_read_b64_tr_b16 v[184:185], v103 offset:16384\n\t"
        "ds_read_b64_tr_b16 v[186:187], v103 offset:16896\n\t"
        "v_mfma_f32_16x16x32_bf16 v[12:15], v[164:167], v[56:59], v[12:15]\n\t"
        "ds_read_b64_tr_b16 v[188:189], v103 offset:16392\n\t"
        "ds_read_b64_tr_b16 v[190:191], v103 offset:16904\n\t"
        "v_mfma_f32_16x16x32_bf16 v[16:19], v[56:59], v[168:171], v[16:19]\n\t"
        "ds_read_b64_tr_b16 v[192:193], v104 offset:16384\n\t"
        "ds_read_b64_tr_b16 v[194:195], v104 offset:16640\n\t"
        "v_mfma_f32_16x16x32_bf16 v[20:23], v[160:163], v[60:63], v[20:23]\n\t"
        "s_waitcnt lgkmcnt(5)\n\t"
        "ds_read_b64_tr_b16 v[196:197], v103 offset:20480\n\t"
        "v_mfma_f32_16x16x32_bf16 v[24:27], v[164:167], v[60:63], v[24:27]\n\t"
        "ds_read_b64_tr_b16 v[198:199], v103 offset:20992\n\t"
        "ds_read_b64_tr_b16 v[200:201], v103 offset:20488\n\t"
        "v_mfma_f32_16x16x32_bf16 v[28:31], v[60:63], v[168:171], v[28:31]\n\t"
        "ds_read_b64_tr_b16 v[202:203], v103 offset:21000\n\t"
        "ds_read_b64_tr_b16 v[204:205], v104 offset:18432\n\t"
        "v_mfma_f32_16x16x32_bf16 v[8:11], v[172:175], v[64:67], v[8:11]\n\t"
        "ds_read_b64_tr_b16 v[206:207], v104 offset:18688\n\t"
        "ds_read_b128 v[72:75], v107 offset:40960\n\t"
        "v_mfma_f32_16x16x32_bf16 v[12:15], v[176:179], v[64:67], v[12:15]\n\t"
        "ds_read_b128 v[76:79], v107 offset:43008\n\t"
        "ds_read_b128 v[80:83], v108 offset:40960\n\t"
        "v_mfma_f32_16x16x32_bf16 v[16:19], v[64:67], v[180:183], v[16:19]\n\t"
        "ds_read_b128 v[84:87], v108 offset:43008\n\t"
        "v_mfma_f32_16x16x32_bf16 v[20:23], v[172:175], v[68:71], v[20:23]\n\t"
        "v_mfma_f32_16x16x32_bf16 v[24:27], v[176:179], v[68:71], v[24:27]\n\t"
        "v_mfma_f32_16x16x32_bf16 v[28:31], v[68:71], v[180:183], v[28:31]\n\t"
        "s_waitcnt vmcnt(11)\n\t"
        "ds_write_b128 v102, v[232:235] offset:0\n\t"
        "ds_write_b128 v102, v[236:239] offset:4096\n\t"
        "ds_write_b128 v116, v[240:243] offset:8192\n\t"
        "s_waitcnt lgkmcnt(0)\n\t"
        "s_barrier\n\t"
        "v_mfma_f32_16x16x32_bf16 v[8:11], v[184:187], v[72:75], v[8:11]\n\t"
        "ds_read_b64_tr_b16 v[208:209], v103 offset:0\n\t"
        "ds_read_b64_tr_b16 v[210:211], v103 offset:512\n\t"
        "v_mfma_f32_16x16x32_bf16 v[12:15], v[188:191], v[72:75], v[12:15]\n\t"
        "ds_read_b64_tr_b16 v[212:213], v103 offset:8\n\t"
        "ds_read_b64_tr_b16 v[214:215], v103 offset:520\n\t"
        "v_mfma_f32_16x16x32_bf16 v[16:19], v[72:75], v[192:195], v[16:19]\n\t"
        "ds_read_b64_tr_b16 v[216:217], v104 offset:0\n\t"
        "ds_read_b64_tr_b16 v[218:219], v104 offset:256\n\t"
        "v_mfma_f32_16x16x32_bf16 v[20:23], v[184:187], v[76:79], v[20:23]\n\t"
        "s_waitcnt lgkmcnt(5)\n\t"
        "ds_read_b64_tr_b16 v[220:221], v103 offset:4096\n\t"
        "v_mfma_f32_16x16x32_bf16 v[24:27], v[188:191], v[76:79], v[24:27]\n\t"
        "ds_read_b64_tr_b16 v[222:223], v103 offset:4608\n\t"
        "ds_read_b64_tr_b16 v[224:225], v103 offset:4104\n\t"
        "v_mfma_f32_16x16x32_bf16 v[28:31], v[76:79], v[192:195], v[28:31]\n\t"
        "ds_read_b64_tr_b16 v[226:227], v103 offset:4616\n\t"
        "ds_read_b64_tr_b16 v[228:229], v104 offset:2048\n\t"
        "v_mfma_f32_16x16x32_bf16 v[8:11], v[196:199], v[80:83], v[8:11]\n\t"
        "ds_read_b64_tr_b16 v[230:231], v104 offset:2304\n\t"
        "ds_read_b128 v[56:59], v107 offset:49152\n\t"
        "v_mfma_f32_16x16x32_bf16 v[12:15], v[200:203], v[80:83], v[12:15]\n\t"
        "ds_read_b128 v[60:63], v107 offset:51200\n\t"
        "ds_read_b128 v[64:67], v108 offset:49152\n\t"
        "v_mfma_f32_16x16x32_bf16 v[16:19], v[80:83], v[204:207], v[16:19]\n\t"
        "ds_read_b128 v[68:71], v108 offset:51200\n\t"
        "v_mfma_f32_16x16x32_bf16 v[20:23], v[196:199], v[84:87], v[20:23]\n\t"
        "v_mfma_f32_16x16x32_bf16 v[24:27], v[200:203], v[84:87], v[24:27]\n\t"
        "v_mfma_f32_16x16x32_bf16 v[28:31], v[84:87], v[204:207], v[28:31]\n\t"
        "s_waitcnt vmcnt(8)\n\t"
        "ds_write_b128 v102, v[244:247] offset:16384\n\t"
        "ds_write_b128 v102, v[248:251] offset:20480\n\t"
        "ds_write_b128 v116, v[252:255] offset:24576\n\t"
        "s_waitcnt lgkmcnt(0)\n\t"
        "s_barrier\n\t"
        "v_mfma_f32_16x16x32_bf16 v[8:11], v[208:211], v[56:59], v[8:11]\n\t"
        "ds_read_b64_tr_b16 v[232:233], v103 offset:16384\n\t"
        "ds_read_b64_tr_b16 v[234:235], v103 offset:16896\n\t"
        "v_mfma_f32_16x16x32_bf16 v[12:15], v[212:215], v[56:59], v[12:15]\n\t"
        "ds_read_b64_tr_b16 v[236:237], v103 offset:16392\n\t"
        "ds_read_b64_tr_b16 v[238:239], v103 offset:16904\n\t"
        "v_mfma_f32_16x16x32_bf16 v[16:19], v[56:59], v[216:219], v[16:19]\n\t"
        "ds_read_b64_tr_b16 v[240:241], v104 offset:16384\n\t"
        "ds_read_b64_tr_b16 v[242:243], v104 offset:16640\n\t"
        "v_mfma_f32_16x16x32_bf16 v[20:23], v[208:211], v[60:63], v[20:23]\n\t"
        "s_waitcnt lgkmcnt(5)\n\t"
        "ds_read_b64_tr_b16 v[244:245], v103 offset:20480\n\t"
        "v_mfma_f32_16x16x32_bf16 v[24:27], v[212:215], v[60:63], v[24:27]\n\t"
        "ds_read_b64_tr_b16 v[246:247], v103 offset:20992\n\t"
        "ds_read_b64_tr_b16 v[248:249], v103 offset:20488\n\t"
        "v_mfma_f32_16x16x32_bf16 v[28:31], v[60:63], v[216:219], v[28:31]\n\t"
        "ds_read_b64_tr_b16 v[250:251], v103 offset:21000\n\t"
        "ds_read_b64_tr_b16 v[252:253], v104 offset:18432\n\t"
        "v_mfma_f32_16x16x32_bf16 v[8:11], v[220:223], v[64:67], v[8:11]\n\t"
        "ds_read_b64_tr_b16 v[254:255], v104 offset:18688\n\t"
        "ds_read_b128 v[72:75], v107 offset:57344\n\t"
        "v_mfma_f32_16x16x32_bf16 v[12:15], v[224:227], v[64:67], v[12:15]\n\t"
        "ds_read_b128 v[76:79], v107 offset:59392\n\t"
        "ds_read_b128 v[80:83], v108 offset:57344\n\t"
        "v_mfma_f32_16x16x32_bf16 v[16:19], v[64:67], v[228:231], v[16:19]\n\t"
        "ds_read_b128 v[84:87], v108 offset:59392\n\t"
        "v_mfma_f32_16x16x32_bf16 v[20:23], v[220:223], v[68:71], v[20:23]\n\t"
        "v_mfma_f32_16x16x32_bf16 v[24:27], v[224:227], v[68:71], v[24:27]\n\t"
        "v_mfma_f32_16x16x32_bf16 v[28:31], v[68:71], v[228:231], v[28:31]\n\t"
        "s_waitcnt vmcnt(0) lgkmcnt(0)\n\t"
        "s_barrier\n\t"
        "v_mfma_f32_16x16x32_bf16 v[8:11], v[232:235], v[72:75], v[8:11]\n\t"
        "v_mfma_f32_16x16x32_bf16 v[12:15], v[236:239], v[72:75], v[12:15]\n\t"
        "v_mfma_f32_16x16x32_bf16 v[16:19], v[72:75], v[240:243], v[16:19]\n\t"
        "v_mfma_f32_16x16x32_bf16 v[20:23], v[232:235], v[76:79], v[20:23]\n\t"
        "v_mfma_f32_16x16x32_bf16 v[24:27], v[236:239], v[76:79], v[24:27]\n\t"
        "v_mfma_f32_16x16x32_bf16 v[28:31], v[76:79], v[240:243], v[28:31]\n\t"
        "v_mfma_f32_16x16x32_bf16 v[8:11], v[244:247], v[80:83], v[8:11]\n\t"
        "v_mfma_f32_16x16x32_bf16 v[12:15], v[248:251], v[80:83], v[12:15]\n\t"
        "v_mfma_f32_16x16x32_bf16 v[16:19], v[80:83], v[252:255], v[16:19]\n\t"
        "v_mfma_f32_16x16x32_bf16 v[20:23], v[244:247], v[84:87], v[20:23]\n\t"
        "v_mfma_f32_16x16x32_bf16 v[24:27], v[248:251], v[84:87], v[24:27]\n\t"
        "v_mfma_f32_16x16x32_bf16 v[28:31], v[84:87], v[252:255], v[28:31]\n\t"
        "s_mov_b32 %[xo], 0x20000\n\t"
        "ds_read_b128 v[56:59], v105 offset:0\n\t"
        "ds_read_b128 v[60:63], v105 offset:2048\n\t"
        "ds_read_b128 v[64:67], v106 offset:0\n\t"
        "ds_read_b128 v[68:71], v106 offset:2048\n\t"
        "ds_read_b128 v[72:75], v105 offset:8192\n\t"
        "ds_read_b128 v[76:79], v105 offset:10240\n\t"
        "ds_read_b128 v[80:83], v106 offset:8192\n\t"
        "v_cvt_pk_bf16_f32 v88, v8, v9\n\t"
        "v_cvt_pk_bf16_f32 v89, v10, v11\n\t"
        "v_cvt_pk_bf16_f32 v90, v12, v13\n\t"
        "v_cvt_pk_bf16_f32 v91, v14, v15\n\t"
        "buffer_store_dwordx4 v[88:91], v109, %[sw], %[so] offen sc1\n\t"
        "v_cvt_pk_bf16_f32 v92, v20, v21\n\t"
        "v_cvt_pk_bf16_f32 v93, v22, v23\n\t"
        "v_cvt_pk_bf16_f32 v94, v24, v25\n\t"
        "v_cvt_pk_bf16_f32 v95, v26, v27\n\t"
        "buffer_store_dwordx4 v[92:95], v110, %[sw], %[so] offen sc1\n\t"
        "v_cvt_pk_bf16_f32 v96, v16, v17\n\t"
        "v_cvt_pk_bf16_f32 v97, v18, v19\n\t"
        "v_cvt_pk_bf16_f32 v98, v28, v29\n\t"
        "v_cvt_pk_bf16_f32 v99, v30, v31\n\t"
        "buffer_store_dwordx4 v[96:99], v111, %[sw], %[so] offen sc1\n\t"
        "s_mov_b32 %[so], 0xf0000000\n\t"
        "s_mov_b32 %[xo], 33792\n\t"
        "s_mov_b32 %[cn], 0\n\t"
        "LG_cal%=:\n\t"
        "buffer_load_dword v118, off, %[sw], %[xo] sc1\n\t"
        "s_waitcnt vmcnt(0)\n\t"
        "v_readfirstlane_b32 %[t0], v118\n\t"
        "s_cmp_lg_u32 %[t0], 0\n\t"
        "s_cbranch_scc1 LG_calok%=\n\t"
        "s_sleep 4\n\t"
        "s_add_u32 %[cn], %[cn], 1\n\t"
        "s_cmp_lt_u32 %[cn], 0x100000\n\t"
        "s_cbranch_scc1 LG_cal%=\n\t"
        "s_endpgm\n\t"
        "LG_calok%=:\n\t"
        "s_mov_b32 %[xo], 0x20000\n\t"
        "s_mov_b32 %[cn], 1\n\t"
        "v_mov_b32_e32 v119, %[cn]\n\t"
        "buffer_store_dword v119, v113, %[sw], 0 offen sc1\n\t"
        "s_add_u32 m0, %[ld], 32768\n\t"
        "s_add_u32 %[t0], %[xo], 512\n\t"
        "buffer_load_dwordx4 v100, %[sx], %[t0] offen lds\n\t"
        "s_add_u32 m0, %[ld], 33792\n\t"
        "s_add_u32 %[t0], %[xo], 16896\n\t"
        "buffer_load_dwordx4 v100, %[sx], %[t0] offen lds\n\t"
        "s_add_u32 m0, %[ld], 40960\n\t"
        "s_add_u32 %[t0], %[xo], 640\n\t"
        "buffer_load_dwordx4 v100, %[sx], %[t0] offen lds\n\t"
        "s_add_u32 m0, %[ld], 41984\n\t"
        "s_add_u32 %[t0], %[xo], 17024\n\t"
        "buffer_load_dwordx4 v100, %[sx], %[t0] offen lds\n\t"
        "s_add_u32 m0, %[ld], 49152\n\t"
        "s_add_u32 %[t0], %[xo], 768\n\t"
        "buffer_load_dwordx4 v100, %[sx], %[t0] offen lds\n\t"
        "s_add_u32 m0, %[ld], 50176\n\t"
        "s_add_u32 %[t0], %[xo], 17152\n\t"
        "buffer_load_dwordx4 v100, %[sx], %[t0] offen lds\n\t"
        "s_add_u32 m0, %[ld], 57344\n\t"
        "s_add_u32 %[t0], %[xo], 896\n\t"
        "buffer_load_dwordx4 v100, %[sx], %[t0] offen lds\n\t"
        "s_add_u32 m0, %[ld], 58368\n\t"
        "s_add_u32 %[t0], %[xo], 17280\n\t"
        "buffer_load_dwordx4 v100, %[sx], %[t0] offen lds\n\t"
        "s_add_u32 m0, %[ld], 65536\n\t"
        "s_add_u32 %[t0], %[xo], 1024\n\t"
        "buffer_load_dwordx4 v100, %[sx], %[t0] offen lds\n\t"
        "s_add_u32 m0, %[ld], 66560\n\t"
        "s_add_u32 %[t0], %[xo], 17408\n\t"
        "buffer_load_dwordx4 v100, %[sx], %[t0] offen lds\n\t"
        "s_add_u32 m0, %[ld], 73728\n\t"
        "s_add_u32 %[t0], %[xo], 1152\n\t"
        "buffer_load_dwordx4 v100, %[sx], %[t0] offen lds\n\t"
        "s_add_u32 m0, %[ld], 74752\n\t"
        "s_add_u32 %[t0], %[xo], 17536\n\t"
        "buffer_load_dwordx4 v100, %[sx], %[t0] offen lds\n\t"
        "s_add_u32 m0, %[ld], 81920\n\t"
        "s_add_u32 %[t0], %[xo], 1280\n\t"
        "buffer_load_dwordx4 v100, %[sx], %[t0] offen lds\n\t"
        "s_add_u32 m0, %[ld], 82944\n\t"
        "s_add_u32 %[t0], %[xo], 17664\n\t"
        "buffer_load_dwordx4 v100, %[sx], %[t0] offen lds\n\t"
        "s_add_u32 m0, %[ld], 90112\n\t"
        "s_add_u32 %[t0], %[xo], 1408\n\t"
        "buffer_load_dwordx4 v100, %[sx], %[t0] offen lds\n\t"
        "s_add_u32 m0, %[ld], 91136\n\t"
        "s_add_u32 %[t0], %[xo], 17792\n\t"
        "buffer_load_dwordx4 v100, %[sx], %[t0] offen lds\n\t"
        "s_add_u32 m0, %[ld], 98304\n\t"
        "s_add_u32 %[t0], %[xo], 1536\n\t"
        "buffer_load_dwordx4 v100, %[sx], %[t0] offen lds\n\t"
        "s_add_u32 m0, %[ld], 99328\n\t"
        "s_add_u32 %[t0], %[xo], 17920\n\t"
        "buffer_load_dwordx4 v100, %[sx], %[t0] offen lds\n\t"
        "s_add_u32 m0, %[ld], 106496\n\t"
        "s_add_u32 %[t0], %[xo], 1664\n\t"
        "buffer_load_dwordx4 v100, %[sx], %[t0] offen lds\n\t"
        "s_branch LG_mid%=\n\t"
        "LG_loop%=:\n\t"
        "s_waitcnt lgkmcnt(6)\n\t"
        "v_mfma_f32_16x16x32_bf16 v[8:11], a[0:3], v[56:59], 0\n\t"
        "v_mfma_f32_16x16x32_bf16 v[12:15], a[4:7], v[56:59], 0\n\t"
        "ds_read_b128 v[84:87], v106 offset:10240\n\t"
        "v_mfma_f32_16x16x32_bf16 v[16:19], v[56:59], a[8:11], 0\n\t"
        "s_add_u32 m0, %[ld], 107520\n\t"
        "s_add_u32 %[t0], %[xo], 18048\n\t"
        "s_waitcnt lgkmcnt(6)\n\t"
        "v_mfma_f32_16x16x32_bf16 v[20:23], a[0:3], v[60:63], 0\n\t"
        "buffer_load_dwordx4 v100, %[sx], %[t0] offen lds\n\t"
        "v_mfma_f32_16x16x32_bf16 v[24:27], a[4:7], v[60:63], 0\n\t"
        "ds_read_b128 v[56:59], v105 offset:16384\n\t"
        "v_mfma_f32_16x16x32_bf16 v[28:31], v[60:63], a[8:11], 0\n\t"
        "s_waitcnt lgkmcnt(6)\n\t"
        "v_mfma_f32_16x16x32_bf16 v[8:11], a[12:15], v[64:67], v[8:11]\n\t"
        "v_cvt_pk_bf16_f32 v88, v32, v33\n\t"
        "v_cvt_pk_bf16_f32 v89, v34, v35\n\t"
        "v_mfma_f32_16x16x32_bf16 v[12:15], a[16:19], v[64:67], v[12:15]\n\t"
        "ds_read_b128 v[60:63], v105 offset:18432\n\t"
        "v_cvt_pk_bf16_f32 v90, v36, v37\n\t"
        "v_mfma_f32_16x16x32_bf16 v[16:19], v[64:67], a[20:23], v[16:19]\n\t"
        "s_add_u32 m0, %[ld], 114688\n\t"
        "s_add_u32 %[t0], %[xo], 1792\n\t"
        "s_waitcnt lgkmcnt(6)\n\t"
        "v_mfma_f32_16x16x32_bf16 v[20:23], a[12:15], v[68:71], v[20:23]\n\t"
        "buffer_load_dwordx4 v100, %[sx], %[t0] offen lds\n\t"
        "v_mfma_f32_16x16x32_bf16 v[24:27], a[16:19], v[68:71], v[24:27]\n\t"
        "ds_read_b128 v[64:67], v106 offset:16384\n\t"
        "v_cvt_pk_bf16_f32 v91, v38, v39\n\t"
        "v_mfma_f32_16x16x32_bf16 v[28:31], v[68:71], a[20:23], v[28:31]\n\t"
        "buffer_store_dwordx4 v[88:91], v109, %[sw], %[so] offen sc1\n\t"
        "s_waitcnt lgkmcnt(6)\n\t"
        "v_mfma_f32_16x16x32_bf16 v[8:11], a[24:27], v[72:75], v[8:11]\n\t"
        "v_cvt_pk_bf16_f32 v92, v44, v45\n\t"
        "v_cvt_pk_bf16_f32 v93, v46, v47\n\t"
        "v_mfma_f32_16x16x32_bf16 v[12:15], a[28:31], v[72:75], v[12:15]\n\t"
        "ds_read_b128 v[68:71], v106 offset:18432\n\t"
        "v_cvt_pk_bf16_f32 v94, v48, v49\n\t"
        "v_mfma_f32_16x16x32_bf16 v[16:19], v[72:75], a[32:35], v[16:19]\n\t"
        "s_add_u32 m0, %[ld], 115712\n\t"
        "s_add_u32 %[t0], %[xo], 18176\n\t"
        "s_waitcnt lgkmcnt(6)\n\t"
        "v_mfma_f32_16x16x32_bf16 v[20:23], a[24:27], v[76:79], v[20:23]\n\t"
        "buffer_load_dwordx4 v100, %[sx], %[t0] offen lds\n\t"
        "v_mfma_f32_16x16x32_bf16 v[24:27], a[28:31], v[76:79], v[24:27]\n\t"
        "ds_read_b128 v[72:75], v105 offset:24576\n\t"
        "v_cvt_pk_bf16_f32 v95, v50, v51\n\t"
        "v_mfma_f32_16x16x32_bf16 v[28:31], v[76:79], a[32:35], v[28:31]\n\t"
        "buffer_store_dwordx4 v[92:95], v110, %[sw], %[so] offen sc1\n\t"
        "s_waitcnt lgkmcnt(6)\n\t"
        "v_mfma_f32_16x16x32_bf16 v[8:11], a[36:39], v[80:83], v[8:11]\n\t"
        "v_cvt_pk_bf16_f32 v96, v40, v41\n\t"
        "v_cvt_pk_bf16_f32 v97, v42, v43\n\t"
        "v_mfma_f32_16x16x32_bf16 v[12:15], a[40:43], v[80:83], v[12:15]\n\t"
        "ds_read_b128 v[76:79], v105 offset:26624\n\t"
        "v_cvt_pk_bf16_f32 v98, v52, v53\n\t"
        "v_mfma_f32_16x16x32_bf16 v[16:19], v[80:83], a[44:47], v[16:19]\n\t"
        "s_add_u32 m0, %[ld], 122880\n\t"
        "s_add_u32 %[t0], %[xo], 1920\n\t"
        "s_waitcnt lgkmcnt(6)\n\t"
        "v_mfma_f32_16x16x32_bf16 v[20:23], a[36:39], v[84:87], v[20:23]\n\t"
        "buffer_load_dwordx4 v100, %[sx], %[t0] offen lds\n\t"
        "v_mfma_f32_16x16x32_bf16 v[24:27], a[40:43], v[84:87], v[24:27]\n\t"
        "ds_read_b128 v[80:83], v106 offset:24576\n\t"
        "v_cvt_pk_bf16_f32 v99, v54, v55\n\t"
        "v_mfma_f32_16x16x32_bf16 v[28:31], v[84:87], a[44:47], v[28:31]\n\t"
        "buffer_store_dwordx4 v[96:99], v111, %[sw], %[so] offen sc1\n\t"
        "s_waitcnt lgkmcnt(6)\n\t"
        "v_mfma_f32_16x16x32_bf16 v[8:11], a[48:51], v[56:59], v[8:11]\n\t"
        "s_lshl_b32 %[so], %[cn], 19\n\t"
        "s_cmp_eq_u32 %[cn], 2\n\t"
        "s_cbranch_scc1 LG_first%=\n\t"
        "LG_back%=:\n\t"
        "v_mfma_f32_16x16x32_bf16 v[12:15], a[52:55], v[56:59], v[12:15]\n\t"
        "ds_read_b128 v[84:87], v106 offset:26624\n\t"
        "v_mfma_f32_16x16x32_bf16 v[16:19], v[56:59], a[56:59], v[16:19]\n\t"
        "s_add_u32 m0, %[ld], 123904\n\t"
        "s_add_u32 %[t0], %[xo], 18304\n\t"
        "s_waitcnt lgkmcnt(6)\n\t"
        "v_mfma_f32_16x16x32_bf16 v[20:23], a[48:51], v[60:63], v[20:23]\n\t"
        "buffer_load_dwordx4 v100, %[sx], %[t0] offen lds\n\t"
        "v_mfma_f32_16x16x32_bf16 v[24:27], a[52:55], v[60:63], v[24:27]\n\t"
        "v_mfma_f32_16x16x32_bf16 v[28:31], v[60:63], a[56:59], v[28:31]\n\t"
        "s_waitcnt lgkmcnt(5)\n\t"
        "v_mfma_f32_16x16x32_bf16 v[8:11], a[60:63], v[64:67], v[8:11]\n\t"
        "s_waitcnt vmcnt(20) lgkmcnt(0)\n\t"
        "s_barrier\n\t"
        "s_add_u32 %[xo], %[xo], 0x20000\n\t"
        "v_mfma_f32_16x16x32_bf16 v[12:15], a[64:67], v[64:67], v[12:15]\n\t"
        "ds_read_b128 v[56:59], v105 offset:32768\n\t"
        "v_mfma_f32_16x16x32_bf16 v[16:19], v[64:67], a[68:71], v[16:19]\n\t"
        "ds_read_b128 v[60:63], v105 offset:34816\n\t"
        "s_add_u32 m0, %[ld], 0\n\t"
        "s_add_u32 %[t0], %[xo], 0\n\t"
        "v_mfma_f32_16x16x32_bf16 v[20:23], a[60:63], v[68:71], v[20:23]\n\t"
        "buffer_load_dwordx4 v100, %[sx], %[t0] offen lds\n\t"
        "v_mfma_f32_16x16x32_bf16 v[24:27], a[64:67], v[68:71], v[24:27]\n\t"
        "ds_read_b128 v[64:67], v106 offset:32768\n\t"
        "v_mfma_f32_16x16x32_bf16 v[28:31], v[68:71], a[68:71], v[28:31]\n\t"
        "v_mfma_f32_16x16x32_bf16 v[8:11], a[72:75], v[72:75], v[8:11]\n\t"
        "v_mfma_f32_16x16x32_bf16 v[12:15], a[76:79], v[72:75], v[12:15]\n\t"
        "ds_read_b128 v[68:71], v106 offset:34816\n\t"
        "v_mfma_f32_16x16x32_bf16 v[16:19], v[72:75], a[80:83], v[16:19]\n\t"
        "s_add_u32 m0, %[ld], 1024\n\t"
        "s_add_u32 %[t0], %[xo], 16384\n\t"
        "v_mfma_f32_16x16x32_bf16 v[20:23], a[72:75], v[76:79], v[20:23]\n\t"
        "buffer_load_dwordx4 v100, %[sx], %[t0] offen lds\n\t"
        "v_mfma_f32_16x16x32_bf16 v[24:27], a[76:79], v[76:79], v[24:27]\n\t"
        "ds_read_b128 v[72:75], v105 offset:40960\n\t"
        "v_mfma_f32_16x16x32_bf16 v[28:31], v[76:79], a[80:83], v[28:31]\n\t"
        "v_mfma_f32_16x16x32_bf16 v[8:11], a[84:87], v[80:83], v[8:11]\n\t"
        "v_mfma_f32_16x16x32_bf16 v[12:15], a[88:91], v[80:83], v[12:15]\n\t"
        "ds_read_b128 v[76:79], v105 offset:43008\n\t"
        "v_mfma_f32_16x16x32_bf16 v[16:19], v[80:83], a[92:95], v[16:19]\n\t"
        "s_add_u32 m0, %[ld], 8192\n\t"
        "s_add_u32 %[t0], %[xo], 128\n\t"
        "v_mfma_f32_16x16x32_bf16 v[20:23], a[84:87], v[84:87], v[20:23]\n\t"
        "buffer_load_dwordx4 v100, %[sx], %[t0] offen lds\n\t"
        "v_mfma_f32_16x16x32_bf16 v[24:27], a[88:91], v[84:87], v[24:27]\n\t"
        "ds_read_b128 v[80:83], v106 offset:40960\n\t"
        "v_mfma_f32_16x16x32_bf16 v[28:31], v[84:87], a[92:95], v[28:31]\n\t"
        "s_waitcnt lgkmcnt(6)\n\t"
        "v_mfma_f32_16x16x32_bf16 v[8:11], a[96:99], v[56:59], v[8:11]\n\t"
        "v_mfma_f32_16x16x32_bf16 v[12:15], a[100:103], v[56:59], v[12:15]\n\t"
        "ds_read_b128 v[84:87], v106 offset:43008\n\t"
        "v_mfma_f32_16x16x32_bf16 v[16:19], v[56:59], a[104:107], v[16:19]\n\t"
        "s_add_u32 m0, %[ld], 9216\n\t"
        "s_add_u32 %[t0], %[xo], 16512\n\t"
        "s_waitcnt lgkmcnt(6)\n\t"
        "v_mfma_f32_16x16x32_bf16 v[20:23], a[96:99], v[60:63], v[20:23]\n\t"
        "buffer_load_dwordx4 v100, %[sx], %[t0] offen lds\n\t"
        "v_mfma_f32_16x16x32_bf16 v[24:27], a[100:103], v[60:63], v[24:27]\n\t"
        "ds_read_b128 v[56:59], v105 offset:49152\n\t"
        "v_mfma_f32_16x16x32_bf16 v[28:31], v[60:63], a[104:107], v[28:31]\n\t"
        "s_waitcnt lgkmcnt(6)\n\t"
        "v_mfma_f32_16x16x32_bf16 v[8:11], a[108:111], v[64:67], v[8:11]\n\t"
        "v_mfma_f32_16x16x32_bf16 v[12:15], a[112:115], v[64:67], v[12:15]\n\t"
        "ds_read_b128 v[60:63], v105 offset:51200\n\t"
        "v_mfma_f32_16x16x32_bf16 v[16:19], v[64:67], a[116:119], v[16:19]\n\t"
        "s_add_u32 m0, %[ld], 16384\n\t"
        "s_add_u32 %[t0], %[xo], 256\n\t"
        "s_waitcnt lgkmcnt(6)\n\t"
        "v_mfma_f32_16x16x32_bf16 v[20:23], a[108:111], v[68:71], v[20:23]\n\t"
        "buffer_load_dwordx4 v100, %[sx], %[t0] offen lds\n\t"
        "v_mfma_f32_16x16x32_bf16 v[24:27], a[112:115], v[68:71], v[24:27]\n\t"
        "ds_read_b128 v[64:67], v106 offset:49152\n\t"
        "v_mfma_f32_16x16x32_bf16 v[28:31], v[68:71], a[116:119], v[28:31]\n\t"
        "s_waitcnt lgkmcnt(6)\n\t"
        "v_mfma_f32_16x16x32_bf16 v[8:11], a[120:123], v[72:75], v[8:11]\n\t"
        "v_mfma_f32_16x16x32_bf16 v[12:15], a[124:127], v[72:75], v[12:15]\n\t"
        "ds_read_b128 v[68:71], v106 offset:51200\n\t"
        "v_mfma_f32_16x16x32_bf16 v[16:19], v[72:75], a[128:131], v[16:19]\n\t"
        "s_add_u32 m0, %[ld], 17408\n\t"
        "s_add_u32 %[t0], %[xo], 16640\n\t"
        "s_waitcnt lgkmcnt(6)\n\t"
        "v_mfma_f32_16x16x32_bf16 v[20:23], a[120:123], v[76:79], v[20:23]\n\t"
        "buffer_load_dwordx4 v100, %[sx], %[t0] offen lds\n\t"
        "v_mfma_f32_16x16x32_bf16 v[24:27], a[124:127], v[76:79], v[24:27]\n\t"
        "ds_read_b128 v[72:75], v105 offset:57344\n\t"
        "v_mfma_f32_16x16x32_bf16 v[28:31], v[76:79], a[128:131], v[28:31]\n\t"
        "s_waitcnt lgkmcnt(6)\n\t"
        "v_mfma_f32_16x16x32_bf16 v[8:11], a[132:135], v[80:83], v[8:11]\n\t"
        "v_mfma_f32_16x16x32_bf16 v[12:15], a[136:139], v[80:83], v[12:15]\n\t"
        "ds_read_b128 v[76:79], v105 offset:59392\n\t"
        "v_mfma_f32_16x16x32_bf16 v[16:19], v[80:83], a[140:143], v[16:19]\n\t"
        "s_add_u32 m0, %[ld], 24576\n\t"
        "s_add_u32 %[t0], %[xo], 384\n\t"
        "s_waitcnt lgkmcnt(6)\n\t"
        "v_mfma_f32_16x16x32_bf16 v[20:23], a[132:135], v[84:87], v[20:23]\n\t"
        "buffer_load_dwordx4 v100, %[sx], %[t0] offen lds\n\t"
        "v_mfma_f32_16x16x32_bf16 v[24:27], a[136:139], v[84:87], v[24:27]\n\t"
        "ds_read_b128 v[80:83], v106 offset:57344\n\t"
        "v_mfma_f32_16x16x32_bf16 v[28:31], v[84:87], a[140:143], v[28:31]\n\t"
        "s_waitcnt lgkmcnt(6)\n\t"
        "v_mfma_f32_16x16x32_bf16 v[8:11], a[144:147], v[56:59], v[8:11]\n\t"
        "v_mfma_f32_16x16x32_bf16 v[12:15], a[148:151], v[56:59], v[12:15]\n\t"
        "ds_read_b128 v[84:87], v106 offset:59392\n\t"
        "v_mfma_f32_16x16x32_bf16 v[16:19], v[56:59], a[152:155], v[16:19]\n\t"
        "s_add_u32 m0, %[ld], 25600\n\t"
        "s_add_u32 %[t0], %[xo], 16768\n\t"
        "s_waitcnt lgkmcnt(6)\n\t"
        "v_mfma_f32_16x16x32_bf16 v[20:23], a[144:147], v[60:63], v[20:23]\n\t"
        "buffer_load_dwordx4 v100, %[sx], %[t0] offen lds\n\t"
        "v_mfma_f32_16x16x32_bf16 v[24:27], a[148:151], v[60:63], v[24:27]\n\t"
        "v_mfma_f32_16x16x32_bf16 v[28:31], v[60:63], a[152:155], v[28:31]\n\t"
        "s_waitcnt lgkmcnt(5)\n\t"
        "v_mfma_f32_16x16x32_bf16 v[8:11], a[156:159], v[64:67], v[8:11]\n\t"
        "s_waitcnt vmcnt(9) lgkmcnt(0)\n\t"
        "s_barrier\n\t"
        "v_mfma_f32_16x16x32_bf16 v[12:15], a[160:163], v[64:67], v[12:15]\n\t"
        "ds_read_b128 v[56:59], v107 offset:0\n\t"
        "v_mfma_f32_16x16x32_bf16 v[16:19], v[64:67], a[164:167], v[16:19]\n\t"
        "ds_read_b128 v[60:63], v107 offset:2048\n\t"
        "s_add_u32 m0, %[ld], 32768\n\t"
        "s_add_u32 %[t0], %[xo], 512\n\t"
        "v_mfma_f32_16x16x32_bf16 v[20:23], a[156:159], v[68:71], v[20:23]\n\t"
        "buffer_load_dwordx4 v100, %[sx], %[t0] offen lds\n\t"
        "v_mfma_f32_16x16x32_bf16 v[24:27], a[160:163], v[68:71], v[24:27]\n\t"
        "ds_read_b128 v[64:67], v108 offset:0\n\t"
        "v_mfma_f32_16x16x32_bf16 v[28:31], v[68:71], a[164:167], v[28:31]\n\t"
        "v_mfma_f32_16x16x32_bf16 v[8:11], a[168:171], v[72:75], v[8:11]\n\t"
        "v_mfma_f32_16x16x32_bf16 v[12:15], a[172:175], v[72:75], v[12:15]\n\t"
        "ds_read_b128 v[68:71], v108 offset:2048\n\t"
        "v_mov_b32_e32 v119, %[cn]\n\t"
        "buffer_store_dword v119, v113, %[sw], 0 offen sc1\n\t"
        "s_add_u32 %[cn], %[cn], 1\n\t"
        "v_mfma_f32_16x16x32_bf16 v[16:19], v[72:75], a[176:179], v[16:19]\n\t"
        "s_add_u32 m0, %[ld], 33792\n\t"
        "s_add_u32 %[t0], %[xo], 16896\n\t"
        "v_mfma_f32_16x16x32_bf16 v[20:23], a[168:171], v[76:79], v[20:23]\n\t"
        "buffer_load_dwordx4 v100, %[sx], %[t0] offen lds\n\t"
        "v_mfma_f32_16x16x32_bf16 v[24:27], a[172:175], v[76:79], v[24:27]\n\t"
        "ds_read_b128 v[72:75], v107 offset:8192\n\t"
        "v_mfma_f32_16x16x32_bf16 v[28:31], v[76:79], a[176:179], v[28:31]\n\t"
        "v_mfma_f32_16x16x32_bf16 v[8:11], a[180:183], v[80:83], v[8:11]\n\t"
        "v_mfma_f32_16x16x32_bf16 v[12:15], a[184:187], v[80:83], v[12:15]\n\t"
        "ds_read_b128 v[76:79], v107 offset:10240\n\t"
        "v_mfma_f32_16x16x32_bf16 v[16:19], v[80:83], a[188:191], v[16:19]\n\t"
        "s_add_u32 m0, %[ld], 40960\n\t"
        "s_add_u32 %[t0], %[xo], 640\n\t"
        "v_mfma_f32_16x16x32_bf16 v[20:23], a[180:183], v[84:87], v[20:23]\n\t"
        "buffer_load_dwordx4 v100, %[sx], %[t0] offen lds\n\t"
        "v_mfma_f32_16x16x32_bf16 v[24:27], a[184:187], v[84:87], v[24:27]\n\t"
        "ds_read_b128 v[80:83], v108 offset:8192\n\t"
        "v_mfma_f32_16x16x32_bf16 v[28:31], v[84:87], a[188:191], v[28:31]\n\t"
        "s_waitcnt lgkmcnt(6)\n\t"
        "v_mfma_f32_16x16x32_bf16 v[8:11], a[192:195], v[56:59], v[8:11]\n\t"
        "v_mfma_f32_16x16x32_bf16 v[12:15], a[196:199], v[56:59], v[12:15]\n\t"
        "ds_read_b128 v[84:87], v108 offset:10240\n\t"
        "v_mfma_f32_16x16x32_bf16 v[16:19], v[56:59], a[200:203], v[16:19]\n\t"
        "s_add_u32 m0, %[ld], 41984\n\t"
        "s_add_u32 %[t0], %[xo], 17024\n\t"
        "s_waitcnt lgkmcnt(6)\n\t"
        "v_mfma_f32_16x16x32_bf16 v[20:23], a[192:195], v[60:63], v[20:23]\n\t"
        "buffer_load_dwordx4 v100, %[sx], %[t0] offen lds\n\t"
        "v_mfma_f32_16x16x32_bf16 v[24:27], a[196:199], v[60:63], v[24:27]\n\t"
        "ds_read_b128 v[56:59], v107 offset:16384\n\t"
        "v_mfma_f32_16x16x32_bf16 v[28:31], v[60:63], a[200:203], v[28:31]\n\t"
        "s_waitcnt lgkmcnt(6)\n\t"
        "v_mfma_f32_16x16x32_bf16 v[8:11], a[204:207], v[64:67], v[8:11]\n\t"
        "v_mfma_f32_16x16x32_bf16 v[12:15], a[208:211], v[64:67], v[12:15]\n\t"
        "ds_read_b128 v[60:63], v107 offset:18432\n\t"
        "v_mfma_f32_16x16x32_bf16 v[16:19], v[64:67], a[212:215], v[16:19]\n\t"
        "s_add_u32 m0, %[ld], 49152\n\t"
        "s_add_u32 %[t0], %[xo], 768\n\t"
        "s_waitcnt lgkmcnt(6)\n\t"
        "v_mfma_f32_16x16x32_bf16 v[20:23], a[204:207], v[68:71], v[20:23]\n\t"
        "buffer_load_dwordx4 v100, %[sx], %[t0] offen lds\n\t"
        "v_mfma_f32_16x16x32_bf16 v[24:27], a[208:211], v[68:71], v[24:27]\n\t"
        "ds_read_b128 v[64:67], v108 offset:16384\n\t"
        "v_mfma_f32_16x16x32_bf16 v[28:31], v[68:71], a[212:215], v[28:31]\n\t"
        "s_waitcnt lgkmcnt(6)\n\t"
        "v_mfma_f32_16x16x32_bf16 v[8:11], a[216:219], v[72:75], v[8:11]\n\t"
        "v_mfma_f32_16x16x32_bf16 v[12:15], a[220:223], v[72:75], v[12:15]\n\t"
        "ds_read_b128 v[68:71], v108 offset:18432\n\t"
        "v_mfma_f32_16x16x32_bf16 v[16:19], v[72:75], a[224:227], v[16:19]\n\t"
        "s_add_u32 m0, %[ld], 50176\n\t"
        "s_add_u32 %[t0], %[xo], 17152\n\t"
        "s_waitcnt lgkmcnt(6)\n\t"
        "v_mfma_f32_16x16x32_bf16 v[20:23], a[216:219], v[76:79], v[20:23]\n\t"
        "buffer_load_dwordx4 v100, %[sx], %[t0] offen lds\n\t"
        "v_mfma_f32_16x16x32_bf16 v[24:27], a[220:223], v[76:79], v[24:27]\n\t"
        "ds_read_b128 v[72:75], v107 offset:24576\n\t"
        "v_mfma_f32_16x16x32_bf16 v[28:31], v[76:79], a[224:227], v[28:31]\n\t"
        "s_waitcnt lgkmcnt(6)\n\t"
        "v_mfma_f32_16x16x32_bf16 v[8:11], a[228:231], v[80:83], v[8:11]\n\t"
        "v_mfma_f32_16x16x32_bf16 v[12:15], a[232:235], v[80:83], v[12:15]\n\t"
        "ds_read_b128 v[76:79], v107 offset:26624\n\t"
        "v_mfma_f32_16x16x32_bf16 v[16:19], v[80:83], a[236:239], v[16:19]\n\t"
        "s_add_u32 m0, %[ld], 57344\n\t"
        "s_add_u32 %[t0], %[xo], 896\n\t"
        "s_waitcnt lgkmcnt(6)\n\t"
        "v_mfma_f32_16x16x32_bf16 v[20:23], a[228:231], v[84:87], v[20:23]\n\t"
        "buffer_load_dwordx4 v100, %[sx], %[t0] offen lds\n\t"
        "v_mfma_f32_16x16x32_bf16 v[24:27], a[232:235], v[84:87], v[24:27]\n\t"
        "ds_read_b128 v[80:83], v108 offset:24576\n\t"
        "v_mfma_f32_16x16x32_bf16 v[28:31], v[84:87], a[236:239], v[28:31]\n\t"
        "s_waitcnt lgkmcnt(6)\n\t"
        "v_mfma_f32_16x16x32_bf16 v[8:11], a[240:243], v[56:59], v[8:11]\n\t"
        "v_mfma_f32_16x16x32_bf16 v[12:15], a[244:247], v[56:59], v[12:15]\n\t"
        "ds_read_b128 v[84:87], v108 offset:26624\n\t"
        "v_mfma_f32_16x16x32_bf16 v[16:19], v[56:59], a[248:251], v[16:19]\n\t"
        "s_add_u32 m0, %[ld], 58368\n\t"
        "s_add_u32 %[t0], %[xo], 17280\n\t"
        "s_waitcnt lgkmcnt(6)\n\t"
        "v_mfma_f32_16x16x32_bf16 v[20:23], a[240:243], v[60:63], v[20:23]\n\t"
        "buffer_load_dwordx4 v100, %[sx], %[t0] offen lds\n\t"
        "v_mfma_f32_16x16x32_bf16 v[24:27], a[244:247], v[60:63], v[24:27]\n\t"
        "v_mfma_f32_16x16x32_bf16 v[28:31], v[60:63], a[248:251], v[28:31]\n\t"
        "s_waitcnt lgkmcnt(5)\n\t"
        "v_mfma_f32_16x16x32_bf16 v[8:11], a[252:255], v[64:67], v[8:11]\n\t"
        "s_waitcnt vmcnt(17) lgkmcnt(0)\n\t"
        "s_barrier\n\t"
        "v_mfma_f32_16x16x32_bf16 v[12:15], v[128:131], v[64:67], v[12:15]\n\t"
        "ds_read_b128 v[56:59], v107 offset:32768\n\t"
        "v_mfma_f32_16x16x32_bf16 v[16:19], v[64:67], v[132:135], v[16:19]\n\t"
        "ds_read_b128 v[60:63], v107 offset:34816\n\t"
        "s_add_u32 m0, %[ld], 65536\n\t"
        "s_add_u32 %[t0], %[xo], 1024\n\t"
        "v_mfma_f32_16x16x32_bf16 v[20:23], a[252:255], v[68:71], v[20:23]\n\t"
        "buffer_load_dwordx4 v100, %[sx], %[t0] offen lds\n\t"
        "v_mfma_f32_16x16x32_bf16 v[24:27], v[128:131], v[68:71], v[24:27]\n\t"
        "ds_read_b128 v[64:67], v108 offset:32768\n\t"
        "v_mfma_f32_16x16x32_bf16 v[28:31], v[68:71], v[132:135], v[28:31]\n\t"
        "v_mfma_f32_16x16x32_bf16 v[8:11], v[136:139], v[72:75], v[8:11]\n\t"
        "v_mfma_f32_16x16x32_bf16 v[12:15], v[140:143], v[72:75], v[12:15]\n\t"
        "ds_read_b128 v[68:71], v108 offset:34816\n\t"
        "v_mfma_f32_16x16x32_bf16 v[16:19], v[72:75], v[144:147], v[16:19]\n\t"
        "s_add_u32 m0, %[ld], 66560\n\t"
        "s_add_u32 %[t0], %[xo], 17408\n\t"
        "v_mfma_f32_16x16x32_bf16 v[20:23], v[136:139], v[76:79], v[20:23]\n\t"
        "buffer_load_dwordx4 v100, %[sx], %[t0] offen lds\n\t"
        "v_mfma_f32_16x16x32_bf16 v[24:27], v[140:143], v[76:79], v[24:27]\n\t"
        "ds_read_b128 v[72:75], v107 offset:40960\n\t"
        "v_mfma_f32_16x16x32_bf16 v[28:31], v[76:79], v[144:147], v[28:31]\n\t"
        "v_mfma_f32_16x16x32_bf16 v[8:11], v[148:151], v[80:83], v[8:11]\n\t"
        "v_mfma_f32_16x16x32_bf16 v[12:15], v[152:155], v[80:83], v[12:15]\n\t"
        "ds_read_b128 v[76:79], v107 offset:43008\n\t"
        "v_mfma_f32_16x16x32_bf16 v[16:19], v[80:83], v[156:159], v[16:19]\n\t"
        "s_add_u32 m0, %[ld], 73728\n\t"
        "s_add_u32 %[t0], %[xo], 1152\n\t"
        "v_mfma_f32_16x16x32_bf16 v[20:23], v[148:151], v[84:87], v[20:23]\n\t"
        "buffer_load_dwordx4 v100, %[sx], %[t0] offen lds\n\t"
        "v_mfma_f32_16x16x32_bf16 v[24:27], v[152:155], v[84:87], v[24:27]\n\t"
        "ds_read_b128 v[80:83], v108 offset:40960\n\t"
        "v_mfma_f32_16x16x32_bf16 v[28:31], v[84:87], v[156:159], v[28:31]\n\t"
        "s_waitcnt lgkmcnt(6)\n\t"
        "v_mfma_f32_16x16x32_bf16 v[8:11], v[160:163], v[56:59], v[8:11]\n\t"
        "v_mfma_f32_16x16x32_bf16 v[12:15], v[164:167], v[56:59], v[12:15]\n\t"
        "ds_read_b128 v[84:87], v108 offset:43008\n\t"
        "v_mfma_f32_16x16x32_bf16 v[16:19], v[56:59], v[168:171], v[16:19]\n\t"
        "s_add_u32 m0, %[ld], 74752\n\t"
        "s_add_u32 %[t0], %[xo], 17536\n\t"
        "s_waitcnt lgkmcnt(6)\n\t"
        "v_mfma_f32_16x16x32_bf16 v[20:23], v[160:163], v[60:63], v[20:23]\n\t"
        "buffer_load_dwordx4 v100, %[sx], %[t0] offen lds\n\t"
        "v_mfma_f32_16x16x32_bf16 v[24:27], v[164:167], v[60:63], v[24:27]\n\t"
        "ds_read_b128 v[56:59], v107 offset:49152\n\t"
        "v_mfma_f32_16x16x32_bf16 v[28:31], v[60:63], v[168:171], v[28:31]\n\t"
        "s_waitcnt lgkmcnt(6)\n\t"
        "v_mfma_f32_16x16x32_bf16 v[8:11], v[172:175], v[64:67], v[8:11]\n\t"
        "v_mfma_f32_16x16x32_bf16 v[12:15], v[176:179], v[64:67], v[12:15]\n\t"
        "ds_read_b128 v[60:63], v107 offset:51200\n\t"
        "v_mfma_f32_16x16x32_bf16 v[16:19], v[64:67], v[180:183], v[16:19]\n\t"
        "s_add_u32 m0, %[ld], 81920\n\t"
        "s_add_u32 %[t0], %[xo], 1280\n\t"
        "s_waitcnt lgkmcnt(6)\n\t"
        "v_mfma_f32_16x16x32_bf16 v[20:23], v[172:175], v[68:71], v[20:23]\n\t"
        "buffer_load_dwordx4 v100, %[sx], %[t0] offen lds\n\t"
        "v_mfma_f32_16x16x32_bf16 v[24:27], v[176:179], v[68:71], v[24:27]\n\t"
        "ds_read_b128 v[64:67], v108 offset:49152\n\t"
        "v_mfma_f32_16x16x32_bf16 v[28:31], v[68:71], v[180:183], v[28:31]\n\t"
        "s_waitcnt lgkmcnt(6)\n\t"
        "v_mfma_f32_16x16x32_bf16 v[8:11], v[184:187], v[72:75], v[8:11]\n\t"
        "v_mfma_f32_16x16x32_bf16 v[12:15], v[188:191], v[72:75], v[12:15]\n\t"
        "ds_read_b128 v[68:71], v108 offset:51200\n\t"
        "v_mfma_f32_16x16x32_bf16 v[16:19], v[72:75], v[192:195], v[16:19]\n\t"
        "s_add_u32 m0, %[ld], 82944\n\t"
        "s_add_u32 %[t0], %[xo], 17664\n\t"
        "s_waitcnt lgkmcnt(6)\n\t"
        "v_mfma_f32_16x16x32_bf16 v[20:23], v[184:187], v[76:79], v[20:23]\n\t"
        "buffer_load_dwordx4 v100, %[sx], %[t0] offen lds\n\t"
        "v_mfma_f32_16x16x32_bf16 v[24:27], v[188:191], v[76:79], v[24:27]\n\t"
        "ds_read_b128 v[72:75], v107 offset:57344\n\t"
        "v_mfma_f32_16x16x32_bf16 v[28:31], v[76:79], v[192:195], v[28:31]\n\t"
        "s_waitcnt lgkmcnt(6)\n\t"
        "v_mfma_f32_16x16x32_bf16 v[8:11], v[196:199], v[80:83], v[8:11]\n\t"
        "v_mfma_f32_16x16x32_bf16 v[12:15], v[200:203], v[80:83], v[12:15]\n\t"
        "ds_read_b128 v[76:79], v107 offset:59392\n\t"
        "v_mfma_f32_16x16x32_bf16 v[16:19], v[80:83], v[204:207], v[16:19]\n\t"
        "s_add_u32 m0, %[ld], 90112\n\t"
        "s_add_u32 %[t0], %[xo], 1408\n\t"
        "s_waitcnt lgkmcnt(6)\n\t"
        "v_mfma_f32_16x16x32_bf16 v[20:23], v[196:199], v[84:87], v[20:23]\n\t"
        "buffer_load_dwordx4 v100, %[sx], %[t0] offen lds\n\t"
        "v_mfma_f32_16x16x32_bf16 v[24:27], v[200:203], v[84:87], v[24:27]\n\t"
        "ds_read_b128 v[80:83], v108 offset:57344\n\t"
        "v_mfma_f32_16x16x32_bf16 v[28:31], v[84:87], v[204:207], v[28:31]\n\t"
        "s_waitcnt lgkmcnt(6)\n\t"
        "v_mfma_f32_16x16x32_bf16 v[8:11], v[208:211], v[56:59], v[8:11]\n\t"
        "v_mfma_f32_16x16x32_bf16 v[12:15], v[212:215], v[56:59], v[12:15]\n\t"
        "ds_read_b128 v[84:87], v108 offset:59392\n\t"
        "v_mfma_f32_16x16x32_bf16 v[16:19], v[56:59], v[216:219], v[16:19]\n\t"
        "s_add_u32 m0, %[ld], 91136\n\t"
        "s_add_u32 %[t0], %[xo], 17792\n\t"
        "s_waitcnt lgkmcnt(6)\n\t"
        "v_mfma_f32_16x16x32_bf16 v[20:23], v[208:211], v[60:63], v[20:23]\n\t"
        "buffer_load_dwordx4 v100, %[sx], %[t0] offen lds\n\t"
        "v_mfma_f32_16x16x32_bf16 v[24:27], v[212:215], v[60:63], v[24:27]\n\t"
        "v_mfma_f32_16x16x32_bf16 v[28:31], v[60:63], v[216:219], v[28:31]\n\t"
        "s_waitcnt lgkmcnt(5)\n\t"
        "v_mfma_f32_16x16x32_bf16 v[8:11], v[220:223], v[64:67], v[8:11]\n\t"
        "s_waitcnt vmcnt(17) lgkmcnt(0)\n\t"
        "s_barrier\n\t"
        "v_mfma_f32_16x16x32_bf16 v[12:15], v[224:227], v[64:67], v[12:15]\n\t"
        "ds_read_b128 v[56:59], v105 offset:0\n\t"
        "v_mfma_f32_16x16x32_bf16 v[16:19], v[64:67], v[228:231], v[16:19]\n\t"
        "ds_read_b128 v[60:63], v105 offset:2048\n\t"
        "s_add_u32 m0, %[ld], 98304\n\t"
        "s_add_u32 %[t0], %[xo], 1536\n\t"
        "v_mfma_f32_16x16x32_bf16 v[20:23], v[220:223], v[68:71], v[20:23]\n\t"
        "buffer_load_dwordx4 v100, %[sx], %[t0] offen lds\n\t"
        "v_mfma_f32_16x16x32_bf16 v[24:27], v[224:227], v[68:71], v[24:27]\n\t"
        "ds_read_b128 v[64:67], v106 offset:0\n\t"
        "v_mfma_f32_16x16x32_bf16 v[28:31], v[68:71], v[228:231], v[28:31]\n\t"
        "v_mfma_f32_16x16x32_bf16 v[8:11], v[232:235], v[72:75], v[8:11]\n\t"
        "v_mfma_f32_16x16x32_bf16 v[12:15], v[236:239], v[72:75], v[12:15]\n\t"
        "ds_read_b128 v[68:71], v106 offset:2048\n\t"
        "v_mfma_f32_16x16x32_bf16 v[16:19], v[72:75], v[240:243], v[16:19]\n\t"
        "s_add_u32 m0, %[ld], 99328\n\t"
        "s_add_u32 %[t0], %[xo], 17920\n\t"
        "v_mfma_f32_16x16x32_bf16 v[20:23], v[232:235], v[76:79], v[20:23]\n\t"
        "buffer_load_dwordx4 v100, %[sx], %[t0] offen lds\n\t"
        "v_mfma_f32_16x16x32_bf16 v[24:27], v[236:239], v[76:79], v[24:27]\n\t"
        "ds_read_b128 v[72:75], v105 offset:8192\n\t"
        "v_mfma_f32_16x16x32_bf16 v[28:31], v[76:79], v[240:243], v[28:31]\n\t"
        "v_mfma_f32_16x16x32_bf16 v[8:11], v[244:247], v[80:83], v[8:11]\n\t"
        "v_mfma_f32_16x16x32_bf16 v[12:15], v[248:251], v[80:83], v[12:15]\n\t"
        "ds_read_b128 v[76:79], v105 offset:10240\n\t"
        "v_mfma_f32_16x16x32_bf16 v[16:19], v[80:83], v[252:255], v[16:19]\n\t"
        "s_add_u32 m0, %[ld], 106496\n\t"
        "s_add_u32 %[t0], %[xo], 1664\n\t"
        "v_mfma_f32_16x16x32_bf16 v[20:23], v[244:247], v[84:87], v[20:23]\n\t"
        "buffer_load_dwordx4 v100, %[sx], %[t0] offen lds\n\t"
        "v_mfma_f32_16x16x32_bf16 v[24:27], v[248:251], v[84:87], v[24:27]\n\t"
        "ds_read_b128 v[80:83], v106 offset:8192\n\t"
        "v_mfma_f32_16x16x32_bf16 v[28:31], v[84:87], v[252:255], v[28:31]\n\t"
        "LG_mid%=:\n\t"
        "s_waitcnt lgkmcnt(6)\n\t"
        "v_mfma_f32_16x16x32_bf16 v[32:35], a[0:3], v[56:59], 0\n\t"
        "v_mfma_f32_16x16x32_bf16 v[36:39], a[4:7], v[56:59], 0\n\t"
        "ds_read_b128 v[84:87], v106 offset:10240\n\t"
        "v_mfma_f32_16x16x32_bf16 v[40:43], v[56:59], a[8:11], 0\n\t"
        "s_add_u32 m0, %[ld], 107520\n\t"
        "s_add_u32 %[t0], %[xo], 18048\n\t"
        "s_waitcnt lgkmcnt(6)\n\t"
        "v_mfma_f32_16x16x32_bf16 v[44:47], a[0:3], v[60:63], 0\n\t"
        "buffer_load_dwordx4 v100, %[sx], %[t0] offen lds\n\t"
        "v_mfma_f32_16x16x32_bf16 v[48:51], a[4:7], v[60:63], 0\n\t"
        "ds_read_b128 v[56:59], v105 offset:16384\n\t"
        "v_mfma_f32_16x16x32_bf16 v[52:55], v[60:63], a[8:11], 0\n\t"
        "s_waitcnt lgkmcnt(6)\n\t"
        "v_mfma_f32_16x16x32_bf16 v[32:35], a[12:15], v[64:67], v[32:35]\n\t"
        "v_cvt_pk_bf16_f32 v88, v8, v9\n\t"
        "v_cvt_pk_bf16_f32 v89, v10, v11\n\t"
        "v_mfma_f32_16x16x32_bf16 v[36:39], a[16:19], v[64:67], v[36:39]\n\t"
        "ds_read_b128 v[60:63], v105 offset:18432\n\t"
        "v_cvt_pk_bf16_f32 v90, v12, v13\n\t"
        "v_mfma_f32_16x16x32_bf16 v[40:43], v[64:67], a[20:23], v[40:43]\n\t"
        "s_add_u32 m0, %[ld], 114688\n\t"
        "s_add_u32 %[t0], %[xo], 1792\n\t"
        "s_waitcnt lgkmcnt(6)\n\t"
        "v_mfma_f32_16x16x32_bf16 v[44:47], a[12:15], v[68:71], v[44:47]\n\t"
        "buffer_load_dwordx4 v100, %[sx], %[t0] offen lds\n\t"
        "v_mfma_f32_16x16x32_bf16 v[48:51], a[16:19], v[68:71], v[48:51]\n\t"
        "ds_read_b128 v[64:67], v106 offset:16384\n\t"
        "v_cvt_pk_bf16_f32 v91, v14, v15\n\t"
        "v_mfma_f32_16x16x32_bf16 v[52:55], v[68:71], a[20:23], v[52:55]\n\t"
        "buffer_store_dwordx4 v[88:91], v109, %[sw], %[so] offen sc1\n\t"
        "s_waitcnt lgkmcnt(6)\n\t"
        "v_mfma_f32_16x16x32_bf16 v[32:35], a[24:27], v[72:75], v[32:35]\n\t"
        "v_cvt_pk_bf16_f32 v92, v20, v21\n\t"
        "v_cvt_pk_bf16_f32 v93, v22, v23\n\t"
        "v_mfma_f32_16x16x32_bf16 v[36:39], a[28:31], v[72:75], v[36:39]\n\t"
        "ds_read_b128 v[68:71], v106 offset:18432\n\t"
        "v_cvt_pk_bf16_f32 v94, v24, v25\n\t"
        "v_mfma_f32_16x16x32_bf16 v[40:43], v[72:75], a[32:35], v[40:43]\n\t"
        "s_add_u32 m0, %[ld], 115712\n\t"
        "s_add_u32 %[t0], %[xo], 18176\n\t"
        "s_waitcnt lgkmcnt(6)\n\t"
        "v_mfma_f32_16x16x32_bf16 v[44:47], a[24:27], v[76:79], v[44:47]\n\t"
        "buffer_load_dwordx4 v100, %[sx], %[t0] offen lds\n\t"
        "v_mfma_f32_16x16x32_bf16 v[48:51], a[28:31], v[76:79], v[48:51]\n\t"
        "ds_read_b128 v[72:75], v105 offset:24576\n\t"
        "v_cvt_pk_bf16_f32 v95, v26, v27\n\t"
        "v_mfma_f32_16x16x32_bf16 v[52:55], v[76:79], a[32:35], v[52:55]\n\t"
        "buffer_store_dwordx4 v[92:95], v110, %[sw], %[so] offen sc1\n\t"
        "s_waitcnt lgkmcnt(6)\n\t"
        "v_mfma_f32_16x16x32_bf16 v[32:35], a[36:39], v[80:83], v[32:35]\n\t"
        "v_cvt_pk_bf16_f32 v96, v16, v17\n\t"
        "v_cvt_pk_bf16_f32 v97, v18, v19\n\t"
        "v_mfma_f32_16x16x32_bf16 v[36:39], a[40:43], v[80:83], v[36:39]\n\t"
        "ds_read_b128 v[76:79], v105 offset:26624\n\t"
        "v_cvt_pk_bf16_f32 v98, v28, v29\n\t"
        "v_mfma_f32_16x16x32_bf16 v[40:43], v[80:83], a[44:47], v[40:43]\n\t"
        "s_add_u32 m0, %[ld], 122880\n\t"
        "s_add_u32 %[t0], %[xo], 1920\n\t"
        "s_waitcnt lgkmcnt(6)\n\t"
        "v_mfma_f32_16x16x32_bf16 v[44:47], a[36:39], v[84:87], v[44:47]\n\t"
        "buffer_load_dwordx4 v100, %[sx], %[t0] offen lds\n\t"
        "v_mfma_f32_16x16x32_bf16 v[48:51], a[40:43], v[84:87], v[48:51]\n\t"
        "ds_read_b128 v[80:83], v106 offset:24576\n\t"
        "v_cvt_pk_bf16_f32 v99, v30, v31\n\t"
        "v_mfma_f32_16x16x32_bf16 v[52:55], v[84:87], a[44:47], v[52:55]\n\t"
        "buffer_store_dwordx4 v[96:99], v111, %[sw], %[so] offen sc1\n\t"
        "s_waitcnt lgkmcnt(6)\n\t"
        "v_mfma_f32_16x16x32_bf16 v[32:35], a[48:51], v[56:59], v[32:35]\n\t"
        "s_lshl_b32 %[so], %[cn], 19\n\t"
        "v_mfma_f32_16x16x32_bf16 v[36:39], a[52:55], v[56:59], v[36:39]\n\t"
        "ds_read_b128 v[84:87], v106 offset:26624\n\t"
        "v_mfma_f32_16x16x32_bf16 v[40:43], v[56:59], a[56:59], v[40:43]\n\t"
        "s_add_u32 m0, %[ld], 123904\n\t"
        "s_add_u32 %[t0], %[xo], 18304\n\t"
        "s_waitcnt lgkmcnt(6)\n\t"
        "v_mfma_f32_16x16x32_bf16 v[44:47], a[48:51], v[60:63], v[44:47]\n\t"
        "buffer_load_dwordx4 v100, %[sx], %[t0] offen lds\n\t"
        "v_mfma_f32_16x16x32_bf16 v[48:51], a[52:55], v[60:63], v[48:51]\n\t"
        "v_mfma_f32_16x16x32_bf16 v[52:55], v[60:63], a[56:59], v[52:55]\n\t"
        "s_waitcnt lgkmcnt(5)\n\t"
        "v_mfma_f32_16x16x32_bf16 v[32:35], a[60:63], v[64:67], v[32:35]\n\t"
        "s_waitcnt vmcnt(19) lgkmcnt(0)\n\t"
        "s_barrier\n\t"
        "s_add_u32 %[xo], %[xo], 0x20000\n\t"
        "v_mfma_f32_16x16x32_bf16 v[36:39], a[64:67], v[64:67], v[36:39]\n\t"
        "ds_read_b128 v[56:59], v105 offset:32768\n\t"
        "v_mfma_f32_16x16x32_bf16 v[40:43], v[64:67], a[68:71], v[40:43]\n\t"
        "ds_read_b128 v[60:63], v105 offset:34816\n\t"
        "s_add_u32 m0, %[ld], 0\n\t"
        "s_add_u32 %[t0], %[xo], 0\n\t"
        "v_mfma_f32_16x16x32_bf16 v[44:47], a[60:63], v[68:71], v[44:47]\n\t"
        "buffer_load_dwordx4 v100, %[sx], %[t0] offen lds\n\t"
        "v_mfma_f32_16x16x32_bf16 v[48:51], a[64:67], v[68:71], v[48:51]\n\t"
        "ds_read_b128 v[64:67], v106 offset:32768\n\t"
        "v_mfma_f32_16x16x32_bf16 v[52:55], v[68:71], a[68:71], v[52:55]\n\t"
        "v_mfma_f32_16x16x32_bf16 v[32:35], a[72:75], v[72:75], v[32:35]\n\t"
        "v_mfma_f32_16x16x32_bf16 v[36:39], a[76:79], v[72:75], v[36:39]\n\t"
        "ds_read_b128 v[68:71], v106 offset:34816\n\t"
        "v_mfma_f32_16x16x32_bf16 v[40:43], v[72:75], a[80:83], v[40:43]\n\t"
        "s_add_u32 m0, %[ld], 1024\n\t"
        "s_add_u32 %[t0], %[xo], 16384\n\t"
        "v_mfma_f32_16x16x32_bf16 v[44:47], a[72:75], v[76:79], v[44:47]\n\t"
        "buffer_load_dwordx4 v100, %[sx], %[t0] offen lds\n\t"
        "v_mfma_f32_16x16x32_bf16 v[48:51], a[76:79], v[76:79], v[48:51]\n\t"
        "ds_read_b128 v[72:75], v105 offset:40960\n\t"
        "v_mfma_f32_16x16x32_bf16 v[52:55], v[76:79], a[80:83], v[52:55]\n\t"
        "v_mfma_f32_16x16x32_bf16 v[32:35], a[84:87], v[80:83], v[32:35]\n\t"
        "v_mfma_f32_16x16x32_bf16 v[36:39], a[88:91], v[80:83], v[36:39]\n\t"
        "ds_read_b128 v[76:79], v105 offset:43008\n\t"
        "v_mfma_f32_16x16x32_bf16 v[40:43], v[80:83], a[92:95], v[40:43]\n\t"
        "s_add_u32 m0, %[ld], 8192\n\t"
        "s_add_u32 %[t0], %[xo], 128\n\t"
        "v_mfma_f32_16x16x32_bf16 v[44:47], a[84:87], v[84:87], v[44:47]\n\t"
        "buffer_load_dwordx4 v100, %[sx], %[t0] offen lds\n\t"
        "v_mfma_f32_16x16x32_bf16 v[48:51], a[88:91], v[84:87], v[48:51]\n\t"
        "ds_read_b128 v[80:83], v106 offset:40960\n\t"
        "v_mfma_f32_16x16x32_bf16 v[52:55], v[84:87], a[92:95], v[52:55]\n\t"
        "s_waitcnt lgkmcnt(6)\n\t"
        "v_mfma_f32_16x16x32_bf16 v[32:35], a[96:99], v[56:59], v[32:35]\n\t"
        "v_mfma_f32_16x16x32_bf16 v[36:39], a[100:103], v[56:59], v[36:39]\n\t"
        "ds_read_b128 v[84:87], v106 offset:43008\n\t"
        "v_mfma_f32_16x16x32_bf16 v[40:43], v[56:59], a[104:107], v[40:43]\n\t"
        "s_add_u32 m0, %[ld], 9216\n\t"
        "s_add_u32 %[t0], %[xo], 16512\n\t"
        "s_waitcnt lgkmcnt(6)\n\t"
        "v_mfma_f32_16x16x32_bf16 v[44:47], a[96:99], v[60:63], v[44:47]\n\t"
        "buffer_load_dwordx4 v100, %[sx], %[t0] offen lds\n\t"
        "v_mfma_f32_16x16x32_bf16 v[48:51], a[100:103], v[60:63], v[48:51]\n\t"
        "ds_read_b128 v[56:59], v105 offset:49152\n\t"
        "v_mfma_f32_16x16x32_bf16 v[52:55], v[60:63], a[104:107], v[52:55]\n\t"
        "s_waitcnt lgkmcnt(6)\n\t"
        "v_mfma_f32_16x16x32_bf16 v[32:35], a[108:111], v[64:67], v[32:35]\n\t"
        "v_mfma_f32_16x16x32_bf16 v[36:39], a[112:115], v[64:67], v[36:39]\n\t"
        "ds_read_b128 v[60:63], v105 offset:51200\n\t"
        "v_mfma_f32_16x16x32_bf16 v[40:43], v[64:67], a[116:119], v[40:43]\n\t"
        "s_add_u32 m0, %[ld], 16384\n\t"
        "s_add_u32 %[t0], %[xo], 256\n\t"
        "s_waitcnt lgkmcnt(6)\n\t"
        "v_mfma_f32_16x16x32_bf16 v[44:47], a[108:111], v[68:71], v[44:47]\n\t"
        "buffer_load_dwordx4 v100, %[sx], %[t0] offen lds\n\t"
        "v_mfma_f32_16x16x32_bf16 v[48:51], a[112:115], v[68:71], v[48:51]\n\t"
        "ds_read_b128 v[64:67], v106 offset:49152\n\t"
        "v_mfma_f32_16x16x32_bf16 v[52:55], v[68:71], a[116:119], v[52:55]\n\t"
        "s_waitcnt lgkmcnt(6)\n\t"
        "v_mfma_f32_16x16x32_bf16 v[32:35], a[120:123], v[72:75], v[32:35]\n\t"
        "v_mfma_f32_16x16x32_bf16 v[36:39], a[124:127], v[72:75], v[36:39]\n\t"
        "ds_read_b128 v[68:71], v106 offset:51200\n\t"
        "v_mfma_f32_16x16x32_bf16 v[40:43], v[72:75], a[128:131], v[40:43]\n\t"
        "s_add_u32 m0, %[ld], 17408\n\t"
        "s_add_u32 %[t0], %[xo], 16640\n\t"
        "s_waitcnt lgkmcnt(6)\n\t"
        "v_mfma_f32_16x16x32_bf16 v[44:47], a[120:123], v[76:79], v[44:47]\n\t"
        "buffer_load_dwordx4 v100, %[sx], %[t0] offen lds\n\t"
        "v_mfma_f32_16x16x32_bf16 v[48:51], a[124:127], v[76:79], v[48:51]\n\t"
        "ds_read_b128 v[72:75], v105 offset:57344\n\t"
        "v_mfma_f32_16x16x32_bf16 v[52:55], v[76:79], a[128:131], v[52:55]\n\t"
        "s_waitcnt lgkmcnt(6)\n\t"
        "v_mfma_f32_16x16x32_bf16 v[32:35], a[132:135], v[80:83], v[32:35]\n\t"
        "v_mfma_f32_16x16x32_bf16 v[36:39], a[136:139], v[80:83], v[36:39]\n\t"
        "ds_read_b128 v[76:79], v105 offset:59392\n\t"
        "v_mfma_f32_16x16x32_bf16 v[40:43], v[80:83], a[140:143], v[40:43]\n\t"
        "s_add_u32 m0, %[ld], 24576\n\t"
        "s_add_u32 %[t0], %[xo], 384\n\t"
        "s_waitcnt lgkmcnt(6)\n\t"
        "v_mfma_f32_16x16x32_bf16 v[44:47], a[132:135], v[84:87], v[44:47]\n\t"
        "buffer_load_dwordx4 v100, %[sx], %[t0] offen lds\n\t"
        "v_mfma_f32_16x16x32_bf16 v[48:51], a[136:139], v[84:87], v[48:51]\n\t"
        "ds_read_b128 v[80:83], v106 offset:57344\n\t"
        "v_mfma_f32_16x16x32_bf16 v[52:55], v[84:87], a[140:143], v[52:55]\n\t"
        "s_waitcnt lgkmcnt(6)\n\t"
        "v_mfma_f32_16x16x32_bf16 v[32:35], a[144:147], v[56:59], v[32:35]\n\t"
        "v_mfma_f32_16x16x32_bf16 v[36:39], a[148:151], v[56:59], v[36:39]\n\t"
        "ds_read_b128 v[84:87], v106 offset:59392\n\t"
        "v_mfma_f32_16x16x32_bf16 v[40:43], v[56:59], a[152:155], v[40:43]\n\t"
        "s_add_u32 m0, %[ld], 25600\n\t"
        "s_add_u32 %[t0], %[xo], 16768\n\t"
        "s_waitcnt lgkmcnt(6)\n\t"
        "v_mfma_f32_16x16x32_bf16 v[44:47], a[144:147], v[60:63], v[44:47]\n\t"
        "buffer_load_dwordx4 v100, %[sx], %[t0] offen lds\n\t"
        "v_mfma_f32_16x16x32_bf16 v[48:51], a[148:151], v[60:63], v[48:51]\n\t"
        "v_mfma_f32_16x16x32_bf16 v[52:55], v[60:63], a[152:155], v[52:55]\n\t"
        "s_waitcnt lgkmcnt(5)\n\t"
        "v_mfma_f32_16x16x32_bf16 v[32:35], a[156:159], v[64:67], v[32:35]\n\t"
        "s_waitcnt vmcnt(9) lgkmcnt(0)\n\t"
        "s_barrier\n\t"
        "v_mfma_f32_16x16x32_bf16 v[36:39], a[160:163], v[64:67], v[36:39]\n\t"
        "ds_read_b128 v[56:59], v107 offset:0\n\t"
        "v_mfma_f32_16x16x32_bf16 v[40:43], v[64:67], a[164:167], v[40:43]\n\t"
        "ds_read_b128 v[60:63], v107 offset:2048\n\t"
        "s_add_u32 m0, %[ld], 32768\n\t"
        "s_add_u32 %[t0], %[xo], 512\n\t"
        "v_mfma_f32_16x16x32_bf16 v[44:47], a[156:159], v[68:71], v[44:47]\n\t"
        "buffer_load_dwordx4 v100, %[sx], %[t0] offen lds\n\t"
        "v_mfma_f32_16x16x32_bf16 v[48:51], a[160:163], v[68:71], v[48:51]\n\t"
        "ds_read_b128 v[64:67], v108 offset:0\n\t"
        "v_mfma_f32_16x16x32_bf16 v[52:55], v[68:71], a[164:167], v[52:55]\n\t"
        "v_mfma_f32_16x16x32_bf16 v[32:35], a[168:171], v[72:75], v[32:35]\n\t"
        "v_mfma_f32_16x16x32_bf16 v[36:39], a[172:175], v[72:75], v[36:39]\n\t"
        "ds_read_b128 v[68:71], v108 offset:2048\n\t"
        "v_mov_b32_e32 v119, %[cn]\n\t"
        "buffer_store_dword v119, v113, %[sw], 0 offen sc1\n\t"
        "s_add_u32 %[cn], %[cn], 1\n\t"
        "v_mfma_f32_16x16x32_bf16 v[40:43], v[72:75], a[176:179], v[40:43]\n\t"
        "s_add_u32 m0, %[ld], 33792\n\t"
        "s_add_u32 %[t0], %[xo], 16896\n\t"
        "v_mfma_f32_16x16x32_bf16 v[44:47], a[168:171], v[76:79], v[44:47]\n\t"
        "buffer_load_dwordx4 v100, %[sx], %[t0] offen lds\n\t"
        "v_mfma_f32_16x16x32_bf16 v[48:51], a[172:175], v[76:79], v[48:51]\n\t"
        "ds_read_b128 v[72:75], v107 offset:8192\n\t"
        "v_mfma_f32_16x16x32_bf16 v[52:55], v[76:79], a[176:179], v[52:55]\n\t"
        "v_mfma_f32_16x16x32_bf16 v[32:35], a[180:183], v[80:83], v[32:35]\n\t"
        "v_mfma_f32_16x16x32_bf16 v[36:39], a[184:187], v[80:83], v[36:39]\n\t"
        "ds_read_b128 v[76:79], v107 offset:10240\n\t"
        "v_mfma_f32_16x16x32_bf16 v[40:43], v[80:83], a[188:191], v[40:43]\n\t"
        "s_add_u32 m0, %[ld], 40960\n\t"
        "s_add_u32 %[t0], %[xo], 640\n\t"
        "v_mfma_f32_16x16x32_bf16 v[44:47], a[180:183], v[84:87], v[44:47]\n\t"
        "buffer_load_dwordx4 v100, %[sx], %[t0] offen lds\n\t"
        "v_mfma_f32_16x16x32_bf16 v[48:51], a[184:187], v[84:87], v[48:51]\n\t"
        "ds_read_b128 v[80:83], v108 offset:8192\n\t"
        "v_mfma_f32_16x16x32_bf16 v[52:55], v[84:87], a[188:191], v[52:55]\n\t"
        "s_waitcnt lgkmcnt(6)\n\t"
        "v_mfma_f32_16x16x32_bf16 v[32:35], a[192:195], v[56:59], v[32:35]\n\t"
        "v_mfma_f32_16x16x32_bf16 v[36:39], a[196:199], v[56:59], v[36:39]\n\t"
        "ds_read_b128 v[84:87], v108 offset:10240\n\t"
        "v_mfma_f32_16x16x32_bf16 v[40:43], v[56:59], a[200:203], v[40:43]\n\t"
        "s_add_u32 m0, %[ld], 41984\n\t"
        "s_add_u32 %[t0], %[xo], 17024\n\t"
        "s_waitcnt lgkmcnt(6)\n\t"
        "v_mfma_f32_16x16x32_bf16 v[44:47], a[192:195], v[60:63], v[44:47]\n\t"
        "buffer_load_dwordx4 v100, %[sx], %[t0] offen lds\n\t"
        "v_mfma_f32_16x16x32_bf16 v[48:51], a[196:199], v[60:63], v[48:51]\n\t"
        "ds_read_b128 v[56:59], v107 offset:16384\n\t"
        "v_mfma_f32_16x16x32_bf16 v[52:55], v[60:63], a[200:203], v[52:55]\n\t"
        "s_waitcnt lgkmcnt(6)\n\t"
        "v_mfma_f32_16x16x32_bf16 v[32:35], a[204:207], v[64:67], v[32:35]\n\t"
        "v_mfma_f32_16x16x32_bf16 v[36:39], a[208:211], v[64:67], v[36:39]\n\t"
        "ds_read_b128 v[60:63], v107 offset:18432\n\t"
        "v_mfma_f32_16x16x32_bf16 v[40:43], v[64:67], a[212:215], v[40:43]\n\t"
        "s_add_u32 m0, %[ld], 49152\n\t"
        "s_add_u32 %[t0], %[xo], 768\n\t"
        "s_waitcnt lgkmcnt(6)\n\t"
        "v_mfma_f32_16x16x32_bf16 v[44:47], a[204:207], v[68:71], v[44:47]\n\t"
        "buffer_load_dwordx4 v100, %[sx], %[t0] offen lds\n\t"
        "v_mfma_f32_16x16x32_bf16 v[48:51], a[208:211], v[68:71], v[48:51]\n\t"
        "ds_read_b128 v[64:67], v108 offset:16384\n\t"
        "v_mfma_f32_16x16x32_bf16 v[52:55], v[68:71], a[212:215], v[52:55]\n\t"
        "s_waitcnt lgkmcnt(6)\n\t"
        "v_mfma_f32_16x16x32_bf16 v[32:35], a[216:219], v[72:75], v[32:35]\n\t"
        "v_mfma_f32_16x16x32_bf16 v[36:39], a[220:223], v[72:75], v[36:39]\n\t"
        "ds_read_b128 v[68:71], v108 offset:18432\n\t"
        "v_mfma_f32_16x16x32_bf16 v[40:43], v[72:75], a[224:227], v[40:43]\n\t"
        "s_add_u32 m0, %[ld], 50176\n\t"
        "s_add_u32 %[t0], %[xo], 17152\n\t"
        "s_waitcnt lgkmcnt(6)\n\t"
        "v_mfma_f32_16x16x32_bf16 v[44:47], a[216:219], v[76:79], v[44:47]\n\t"
        "buffer_load_dwordx4 v100, %[sx], %[t0] offen lds\n\t"
        "v_mfma_f32_16x16x32_bf16 v[48:51], a[220:223], v[76:79], v[48:51]\n\t"
        "ds_read_b128 v[72:75], v107 offset:24576\n\t"
        "v_mfma_f32_16x16x32_bf16 v[52:55], v[76:79], a[224:227], v[52:55]\n\t"
        "s_waitcnt lgkmcnt(6)\n\t"
        "v_mfma_f32_16x16x32_bf16 v[32:35], a[228:231], v[80:83], v[32:35]\n\t"
        "v_mfma_f32_16x16x32_bf16 v[36:39], a[232:235], v[80:83], v[36:39]\n\t"
        "ds_read_b128 v[76:79], v107 offset:26624\n\t"
        "v_mfma_f32_16x16x32_bf16 v[40:43], v[80:83], a[236:239], v[40:43]\n\t"
        "s_add_u32 m0, %[ld], 57344\n\t"
        "s_add_u32 %[t0], %[xo], 896\n\t"
        "s_waitcnt lgkmcnt(6)\n\t"
        "v_mfma_f32_16x16x32_bf16 v[44:47], a[228:231], v[84:87], v[44:47]\n\t"
        "buffer_load_dwordx4 v100, %[sx], %[t0] offen lds\n\t"
        "v_mfma_f32_16x16x32_bf16 v[48:51], a[232:235], v[84:87], v[48:51]\n\t"
        "ds_read_b128 v[80:83], v108 offset:24576\n\t"
        "v_mfma_f32_16x16x32_bf16 v[52:55], v[84:87], a[236:239], v[52:55]\n\t"
        "s_waitcnt lgkmcnt(6)\n\t"
        "v_mfma_f32_16x16x32_bf16 v[32:35], a[240:243], v[56:59], v[32:35]\n\t"
        "v_mfma_f32_16x16x32_bf16 v[36:39], a[244:247], v[56:59], v[36:39]\n\t"
        "ds_read_b128 v[84:87], v108 offset:26624\n\t"
        "s_sub_u32 %[t0], %[cn], 2\n\t"
        "s_cmp_lt_u32 %[t0], 24\n\t"
        "s_cselect_b64 %[tp], %[pv], %[ps]\n\t"
        "s_cmp_lt_u32 %[t0], 16\n\t"
        "s_cselect_b64 %[tp], %[pk], %[tp]\n\t"
        "v_mfma_f32_16x16x32_bf16 v[40:43], v[56:59], a[248:251], v[40:43]\n\t"
        "s_add_u32 m0, %[ld], 58368\n\t"
        "s_add_u32 %[t0], %[xo], 17280\n\t"
        "s_waitcnt lgkmcnt(6)\n\t"
        "v_mfma_f32_16x16x32_bf16 v[44:47], a[240:243], v[60:63], v[44:47]\n\t"
        "buffer_load_dwordx4 v100, %[sx], %[t0] offen lds\n\t"
        "v_mfma_f32_16x16x32_bf16 v[48:51], a[244:247], v[60:63], v[48:51]\n\t"
        "v_mfma_f32_16x16x32_bf16 v[52:55], v[60:63], a[248:251], v[52:55]\n\t"
        "s_waitcnt lgkmcnt(5)\n\t"
        "v_mfma_f32_16x16x32_bf16 v[32:35], a[252:255], v[64:67], v[32:35]\n\t"
        "s_waitcnt vmcnt(17) lgkmcnt(0)\n\t"
        "s_barrier\n\t"
        "v_mfma_f32_16x16x32_bf16 v[36:39], v[128:131], v[64:67], v[36:39]\n\t"
        "ds_read_b128 v[56:59], v107 offset:32768\n\t"
        "s_sub_u32 %[t0], %[cn], 2\n\t"
        "s_cmp_lt_u32 %[t0], 8\n\t"
        "s_cselect_b64 %[tp], %[px], %[tp]\n\t"
        "s_bfe_u32 %[t0], %[t0], 0x20001\n\t"
        "s_cmp_eq_u32 %[t0], %[wv]\n\t"
        "s_cselect_b32 %[t0], -1, 0\n\t"
        "v_and_b32_e32 v118, %[t0], v117\n\t"
        "v_mfma_f32_16x16x32_bf16 v[40:43], v[64:67], v[132:135], v[40:43]\n\t"
        "ds_read_b128 v[60:63], v107 offset:34816\n\t"
        "s_add_u32 m0, %[ld], 65536\n\t"
        "s_add_u32 %[t0], %[xo], 1024\n\t"
        "v_mfma_f32_16x16x32_bf16 v[44:47], a[252:255], v[68:71], v[44:47]\n\t"
        "buffer_load_dwordx4 v100, %[sx], %[t0] offen lds\n\t"
        "v_mfma_f32_16x16x32_bf16 v[48:51], v[128:131], v[68:71], v[48:51]\n\t"
        "ds_read_b128 v[64:67], v108 offset:32768\n\t"
        "v_mfma_f32_16x16x32_bf16 v[52:55], v[68:71], v[132:135], v[52:55]\n\t"
        "v_mfma_f32_16x16x32_bf16 v[32:35], v[136:139], v[72:75], v[32:35]\n\t"
        "v_mfma_f32_16x16x32_bf16 v[36:39], v[140:143], v[72:75], v[36:39]\n\t"
        "ds_read_b128 v[68:71], v108 offset:34816\n\t"
        "global_load_dword v118, v118, %[tp]\n\t"
        "v_mfma_f32_16x16x32_bf16 v[40:43], v[72:75], v[144:147], v[40:43]\n\t"
        "s_add_u32 m0, %[ld], 66560\n\t"
        "s_add_u32 %[t0], %[xo], 17408\n\t"
        "v_mfma_f32_16x16x32_bf16 v[44:47], v[136:139], v[76:79], v[44:47]\n\t"
        "buffer_load_dwordx4 v100, %[sx], %[t0] offen lds\n\t"
        "v_mfma_f32_16x16x32_bf16 v[48:51], v[140:143], v[76:79], v[48:51]\n\t"
        "ds_read_b128 v[72:75], v107 offset:40960\n\t"
        "v_mfma_f32_16x16x32_bf16 v[52:55], v[76:79], v[144:147], v[52:55]\n\t"
        "v_mfma_f32_16x16x32_bf16 v[32:35], v[148:151], v[80:83], v[32:35]\n\t"
        "v_mfma_f32_16x16x32_bf16 v[36:39], v[152:155], v[80:83], v[36:39]\n\t"
        "ds_read_b128 v[76:79], v107 offset:43008\n\t"
        "v_mfma_f32_16x16x32_bf16 v[40:43], v[80:83], v[156:159], v[40:43]\n\t"
        "s_add_u32 m0, %[ld], 73728\n\t"
        "s_add_u32 %[t0], %[xo], 1152\n\t"
        "v_mfma_f32_16x16x32_bf16 v[44:47], v[148:151], v[84:87], v[44:47]\n\t"
        "buffer_load_dwordx4 v100, %[sx], %[t0] offen lds\n\t"
        "v_mfma_f32_16x16x32_bf16 v[48:51], v[152:155], v[84:87], v[48:51]\n\t"
        "ds_read_b128 v[80:83], v108 offset:40960\n\t"
        "v_mfma_f32_16x16x32_bf16 v[52:55], v[84:87], v[156:159], v[52:55]\n\t"
        "s_waitcnt lgkmcnt(6)\n\t"
        "v_mfma_f32_16x16x32_bf16 v[32:35], v[160:163], v[56:59], v[32:35]\n\t"
        "v_mfma_f32_16x16x32_bf16 v[36:39], v[164:167], v[56:59], v[36:39]\n\t"
        "ds_read_b128 v[84:87], v108 offset:43008\n\t"
        "v_mfma_f32_16x16x32_bf16 v[40:43], v[56:59], v[168:171], v[40:43]\n\t"
        "s_add_u32 m0, %[ld], 74752\n\t"
        "s_add_u32 %[t0], %[xo], 17536\n\t"
        "s_waitcnt lgkmcnt(6)\n\t"
        "v_mfma_f32_16x16x32_bf16 v[44:47], v[160:163], v[60:63], v[44:47]\n\t"
        "buffer_load_dwordx4 v100, %[sx], %[t0] offen lds\n\t"
        "v_mfma_f32_16x16x32_bf16 v[48:51], v[164:167], v[60:63], v[48:51]\n\t"
        "ds_read_b128 v[56:59], v107 offset:49152\n\t"
        "v_mfma_f32_16x16x32_bf16 v[52:55], v[60:63], v[168:171], v[52:55]\n\t"
        "s_waitcnt lgkmcnt(6)\n\t"
        "v_mfma_f32_16x16x32_bf16 v[32:35], v[172:175], v[64:67], v[32:35]\n\t"
        "v_mfma_f32_16x16x32_bf16 v[36:39], v[176:179], v[64:67], v[36:39]\n\t"
        "ds_read_b128 v[60:63], v107 offset:51200\n\t"
        "v_mfma_f32_16x16x32_bf16 v[40:43], v[64:67], v[180:183], v[40:43]\n\t"
        "s_add_u32 m0, %[ld], 81920\n\t"
        "s_add_u32 %[t0], %[xo], 1280\n\t"
        "s_waitcnt lgkmcnt(6)\n\t"
        "v_mfma_f32_16x16x32_bf16 v[44:47], v[172:175], v[68:71], v[44:47]\n\t"
        "buffer_load_dwordx4 v100, %[sx], %[t0] offen lds\n\t"
        "v_mfma_f32_16x16x32_bf16 v[48:51], v[176:179], v[68:71], v[48:51]\n\t"
        "ds_read_b128 v[64:67], v108 offset:49152\n\t"
        "v_mfma_f32_16x16x32_bf16 v[52:55], v[68:71], v[180:183], v[52:55]\n\t"
        "s_waitcnt lgkmcnt(6)\n\t"
        "v_mfma_f32_16x16x32_bf16 v[32:35], v[184:187], v[72:75], v[32:35]\n\t"
        "v_mfma_f32_16x16x32_bf16 v[36:39], v[188:191], v[72:75], v[36:39]\n\t"
        "ds_read_b128 v[68:71], v108 offset:51200\n\t"
        "v_mfma_f32_16x16x32_bf16 v[40:43], v[72:75], v[192:195], v[40:43]\n\t"
        "s_add_u32 m0, %[ld], 82944\n\t"
        "s_add_u32 %[t0], %[xo], 17664\n\t"
        "s_waitcnt lgkmcnt(6)\n\t"
        "v_mfma_f32_16x16x32_bf16 v[44:47], v[184:187], v[76:79], v[44:47]\n\t"
        "buffer_load_dwordx4 v100, %[sx], %[t0] offen lds\n\t"
        "v_mfma_f32_16x16x32_bf16 v[48:51], v[188:191], v[76:79], v[48:51]\n\t"
        "ds_read_b128 v[72:75], v107 offset:57344\n\t"
        "v_mfma_f32_16x16x32_bf16 v[52:55], v[76:79], v[192:195], v[52:55]\n\t"
        "s_waitcnt lgkmcnt(6)\n\t"
        "v_mfma_f32_16x16x32_bf16 v[32:35], v[196:199], v[80:83], v[32:35]\n\t"
        "v_mfma_f32_16x16x32_bf16 v[36:39], v[200:203], v[80:83], v[36:39]\n\t"
        "ds_read_b128 v[76:79], v107 offset:59392\n\t"
        "v_mfma_f32_16x16x32_bf16 v[40:43], v[80:83], v[204:207], v[40:43]\n\t"
        "s_add_u32 m0, %[ld], 90112\n\t"
        "s_add_u32 %[t0], %[xo], 1408\n\t"
        "s_waitcnt lgkmcnt(6)\n\t"
        "v_mfma_f32_16x16x32_bf16 v[44:47], v[196:199], v[84:87], v[44:47]\n\t"
        "buffer_load_dwordx4 v100, %[sx], %[t0] offen lds\n\t"
        "v_mfma_f32_16x16x32_bf16 v[48:51], v[200:203], v[84:87], v[48:51]\n\t"
        "ds_read_b128 v[80:83], v108 offset:57344\n\t"
        "v_mfma_f32_16x16x32_bf16 v[52:55], v[84:87], v[204:207], v[52:55]\n\t"
        "s_waitcnt lgkmcnt(6)\n\t"
        "v_mfma_f32_16x16x32_bf16 v[32:35], v[208:211], v[56:59], v[32:35]\n\t"
        "v_mfma_f32_16x16x32_bf16 v[36:39], v[212:215], v[56:59], v[36:39]\n\t"
        "ds_read_b128 v[84:87], v108 offset:59392\n\t"
        "v_mfma_f32_16x16x32_bf16 v[40:43], v[56:59], v[216:219], v[40:43]\n\t"
        "s_add_u32 m0, %[ld], 91136\n\t"
        "s_add_u32 %[t0], %[xo], 17792\n\t"
        "s_waitcnt lgkmcnt(6)\n\t"
        "v_mfma_f32_16x16x32_bf16 v[44:47], v[208:211], v[60:63], v[44:47]\n\t"
        "buffer_load_dwordx4 v100, %[sx], %[t0] offen lds\n\t"
        "v_mfma_f32_16x16x32_bf16 v[48:51], v[212:215], v[60:63], v[48:51]\n\t"
        "v_mfma_f32_16x16x32_bf16 v[52:55], v[60:63], v[216:219], v[52:55]\n\t"
        "s_waitcnt lgkmcnt(5)\n\t"
        "v_mfma_f32_16x16x32_bf16 v[32:35], v[220:223], v[64:67], v[32:35]\n\t"
        "s_waitcnt vmcnt(18) lgkmcnt(0)\n\t"
        "s_barrier\n\t"
        "v_mfma_f32_16x16x32_bf16 v[36:39], v[224:227], v[64:67], v[36:39]\n\t"
        "ds_read_b128 v[56:59], v105 offset:0\n\t"
        "v_mfma_f32_16x16x32_bf16 v[40:43], v[64:67], v[228:231], v[40:43]\n\t"
        "ds_read_b128 v[60:63], v105 offset:2048\n\t"
        "s_add_u32 m0, %[ld], 98304\n\t"
        "s_add_u32 %[t0], %[xo], 1536\n\t"
        "v_mfma_f32_16x16x32_bf16 v[44:47], v[220:223], v[68:71], v[44:47]\n\t"
        "buffer_load_dwordx4 v100, %[sx], %[t0] offen lds\n\t"
        "v_mfma_f32_16x16x32_bf16 v[48:51], v[224:227], v[68:71], v[48:51]\n\t"
        "ds_read_b128 v[64:67], v106 offset:0\n\t"
        "v_mfma_f32_16x16x32_bf16 v[52:55], v[68:71], v[228:231], v[52:55]\n\t"
        "v_mfma_f32_16x16x32_bf16 v[32:35], v[232:235], v[72:75], v[32:35]\n\t"
        "v_mfma_f32_16x16x32_bf16 v[36:39], v[236:239], v[72:75], v[36:39]\n\t"
        "ds_read_b128 v[68:71], v106 offset:2048\n\t"
        "v_mfma_f32_16x16x32_bf16 v[40:43], v[72:75], v[240:243], v[40:43]\n\t"
        "s_add_u32 m0, %[ld], 99328\n\t"
        "s_add_u32 %[t0], %[xo], 17920\n\t"
        "v_mfma_f32_16x16x32_bf16 v[44:47], v[232:235], v[76:79], v[44:47]\n\t"
        "buffer_load_dwordx4 v100, %[sx], %[t0] offen lds\n\t"
        "v_mfma_f32_16x16x32_bf16 v[48:51], v[236:239], v[76:79], v[48:51]\n\t"
        "ds_read_b128 v[72:75], v105 offset:8192\n\t"
        "v_mfma_f32_16x16x32_bf16 v[52:55], v[76:79], v[240:243], v[52:55]\n\t"
        "v_mfma_f32_16x16x32_bf16 v[32:35], v[244:247], v[80:83], v[32:35]\n\t"
        "v_mfma_f32_16x16x32_bf16 v[36:39], v[248:251], v[80:83], v[36:39]\n\t"
        "ds_read_b128 v[76:79], v105 offset:10240\n\t"
        "v_mfma_f32_16x16x32_bf16 v[40:43], v[80:83], v[252:255], v[40:43]\n\t"
        "s_add_u32 m0, %[ld], 106496\n\t"
        "s_add_u32 %[t0], %[xo], 1664\n\t"
        "v_mfma_f32_16x16x32_bf16 v[44:47], v[244:247], v[84:87], v[44:47]\n\t"
        "buffer_load_dwordx4 v100, %[sx], %[t0] offen lds\n\t"
        "v_mfma_f32_16x16x32_bf16 v[48:51], v[248:251], v[84:87], v[48:51]\n\t"
        "ds_read_b128 v[80:83], v106 offset:8192\n\t"
        "v_mfma_f32_16x16x32_bf16 v[52:55], v[84:87], v[252:255], v[52:55]\n\t"
        "s_cmp_lt_u32 %[cn], 32\n\t"
        "s_cbranch_scc1 LG_loop%=\n\t"
        "s_nop 7\n\t"
        "v_cvt_pk_bf16_f32 v88, v32, v33\n\t"
        "v_cvt_pk_bf16_f32 v89, v34, v35\n\t"
        "v_cvt_pk_bf16_f32 v90, v36, v37\n\t"
        "v_cvt_pk_bf16_f32 v91, v38, v39\n\t"
        "buffer_store_dwordx4 v[88:91], v109, %[sw], %[so] offen sc1\n\t"
        "v_cvt_pk_bf16_f32 v92, v44, v45\n\t"
        "v_cvt_pk_bf16_f32 v93, v46, v47\n\t"
        "v_cvt_pk_bf16_f32 v94, v48, v49\n\t"
        "v_cvt_pk_bf16_f32 v95, v50, v51\n\t"
        "buffer_store_dwordx4 v[92:95], v110, %[sw], %[so] offen sc1\n\t"
        "v_cvt_pk_bf16_f32 v96, v40, v41\n\t"
        "v_cvt_pk_bf16_f32 v97, v42, v43\n\t"
        "v_cvt_pk_bf16_f32 v98, v52, v53\n\t"
        "v_cvt_pk_bf16_f32 v99, v54, v55\n\t"
        "buffer_store_dwordx4 v[96:99], v111, %[sw], %[so] offen sc1\n\t"
        "s_waitcnt vmcnt(0) lgkmcnt(0)\n\t"
        "v_mov_b32_e32 v119, %[cn]\n\t"
        "buffer_store_dword v119, v113, %[sw], 0 offen sc1\n\t"
        "s_barrier\n\t"
        "s_branch LG_end%=\n\t"
        "LG_first%=:\n\t"
        "s_waitcnt vmcnt(0)\n\t"
        "v_mov_b32_e32 v119, %[cn]\n\t"
        "buffer_store_dword v119, v113, %[sw], 0 offen sc1\n\t"
        "s_branch LG_back%=\n\t"
        "LG_end%=:\n\t"
        ".Lk_end_G:\n\t"
        : [t0] "=&s"(t0), [xo] "=&s"(xo), [so] "=&s"(so), [cn] "=&s"(cn), [tp] "=&s"(tq)
        : [sx] "s"(sx), [sq] "s"(sq), [sv] "s"(sv), [sw] "s"(sw), [ld] "s"(lb + w * 2048), [wv] "s"(w), [px] "s"(p.Aq), [pk] "s"(p.Ak), [pv] "s"(p.Av), [ps] "s"(p.ssm_k),
          "{v117}"((w * 8192 + (h * 4 + c) * 64 + l) * 128),
          "{v100}"((16 * w + (l >> 3)) * 2048 + (((l & 7) ^ (l >> 3)) << 4)), "{v101}"((8 * w + (l >> 3)) * 8192 + h * 256 + (c & 1) * 128 + (l & 7) * 16),
          "{v112}"((16 * w + (l >> 2)) * 8192 + h * 256 + cv * 64 + (l & 3) * 16), "{v102}"(lb + 131072 + w * 1024 + ((l * 16) ^ ((l & 16) << 2))), "{v116}"(lb + 131072 + w * 1024 + ((l * 16) ^ (l & 32))),
          "{v103}"(lr + rr * 128 + ((tn ^ (n >> 3)) * 64) + (n & 3) * 16), "{v104}"(lr + 8192 + rr * 64 + ((tn ^ (g & 1)) * 32) + (n & 3) * 8), "{v105}"(lx), "{v106}"(lx ^ 64u), "{v107}"(lx + 65536), "{v108}"((lx ^ 64u) + 65536),
          "{v109}"(aq), "{v110}"(aq + 4096), "{v111}"(W_VF + (tm * 32 + h) * 8192 + (2 * cv + tn) * 1024 + l * 16), "{v113}"(l == 0 ? F_KVQ + h * 1024 + (c * 4 + w) * 64 : OOB), "{v[114:115]}"(tp)
        : "memory", "scc", "m0", "v8", "v9", "v10", "v11", "v12", "v13", "v14", "v15", "v16", "v17", "v18", "v19", "v20", "v21", "v22", "v23", "v24", "v25", "v26", "v27", "v28", "v29", "v30", "v31", "v32", "v33", "v34", "v35", "v36", "v37", "v38", "v39", "v40", "v41", "v42", "v43", "v44", "v45", "v46", "v47", "v48", "v49", "v50", "v51", "v52", "v53", "v54", "v55", "v56", "v57", "v58", "v59", "v60", "v61", "v62", "v63", "v64", "v65", "v66", "v67", "v68", "v69", "v70", "v71", "v72", "v73", "v74", "v75", "v76", "v77", "v78", "v79", "v80", "v81", "v82", "v83", "v84", "v85", "v86", "v87", "v88", "v89", "v90", "v91", "v92", "v93", "v94", "v95", "v96", "v97", "v98", "v99", "v118", "v119", "v120", "v121", "v122", "v123", "v124", "v125", "v126", "v127", "v128", "v129", "v130", "v131", "v132", "v133", "v134", "v135", "v136", "v137", "v138", "v139", "v140", "v141", "v142", "v143", "v144", "v145", "v146", "v147", "v148", "v149", "v150", "v151", "v152", "v153", "v154", "v155", "v156", "v157", "v158", "v159", "v160", "v161", "v162", "v163", "v164", "v165", "v166", "v167", "v168", "v169", "v170", "v171", "v172", "v173", "v174", "v175", "v176", "v177", "v178", "v179", "v180", "v181", "v182", "v183", "v184", "v185", "v186", "v187", "v188", "v189", "v190", "v191", "v192", "v193", "v194", "v195", "v196", "v197", "v198", "v199", "v200", "v201", "v202", "v203", "v204", "v205", "v206", "v207", "v208", "v209", "v210", "v211", "v212", "v213", "v214", "v215", "v216", "v217", "v218", "v219", "v220", "v221", "v222", "v223", "v224", "v225", "v226", "v227", "v228", "v229", "v230", "v231", "v232", "v233", "v234", "v235", "v236", "v237", "v238", "v239", "v240", "v241", "v242", "v243", "v244", "v245", "v246", "v247", "v248", "v249", "v250", "v251", "v252", "v253", "v254", "v255", "a0", "a1", "a2", "a3", "a4", "a5", "a6", "a7", "a8", "a9", "a10", "a11", "a12", "a13", "a14", "a15", "a16", "a17", "a18", "a19", "a20", "a21", "a22", "a23", "a24", "a25", "a26", "a27", "a28", "a29", "a30", "a31", "a32", "a33", "a34", "a35", "a36", "a37", "a38", "a39", "a40", "a41", "a42", "a43", "a44", "a45", "a46", "a47", "a48", "a49", "a50", "a51", "a52", "a53", "a54", "a55", "a56", "a57", "a58", "a59", "a60", "a61", "a62", "a63", "a64", "a65", "a66", "a67", "a68", "a69", "a70", "a71", "a72", "a73", "a74", "a75", "a76", "a77", "a78", "a79", "a80", "a81", "a82", "a83", "a84", "a85", "a86", "a87", "a88", "a89", "a90", "a91", "a92", "a93", "a94", "a95", "a96", "a97", "a98", "a99", "a100", "a101", "a102", "a103", "a104", "a105", "a106", "a107", "a108", "a109", "a110", "a111", "a112", "a113", "a114", "a115", "a116", "a117", "a118", "a119", "a120", "a121", "a122", "a123", "a124", "a125", "a126", "a127", "a128", "a129", "a130", "a131", "a132", "a133", "a134", "a135", "a136", "a137", "a138", "a139", "a140", "a141", "a142", "a143", "a144", "a145", "a146", "a147", "a148", "a149", "a150", "a151", "a152", "a153", "a154", "a155", "a156", "a157", "a158", "a159", "a160", "a161", "a162", "a163", "a164", "a165", "a166", "a167", "a168", "a169", "a170", "a171", "a172", "a173", "a174", "a175", "a176", "a177", "a178", "a179", "a180", "a181", "a182", "a183", "a184", "a185", "a186", "a187", "a188", "a189", "a190", "a191", "a192", "a193", "a194", "a195", "a196", "a197", "a198", "a199", "a200", "a201", "a202", "a203", "a204", "a205", "a206", "a207", "a208", "a209", "a210", "a211", "a212", "a213", "a214", "a215", "a216", "a217", "a218", "a219", "a220", "a221", "a222", "a223", "a224", "a225", "a226", "a227", "a228", "a229", "a230", "a231", "a232", "a233", "a234", "a235", "a236", "a237", "a238", "a239", "a240", "a241", "a242", "a243", "a244", "a245", "a246", "a247", "a248", "a249", "a250", "a251", "a252", "a253", "a254", "a255");
}

__device__ __forceinline__ void out_block(const Params& p, unsigned char* lds, const int wv, const int rp, const int nt) {
    const int w = wv, l = otid() & 63, n = l & 15, g = l >> 4;
    const unsigned lb = __builtin_amdgcn_readfirstlane((unsigned)(size_t)lds), stg = lb + 131072 + w * 8192;
    const v4u so = mk_srd(p.Wo, 1024 * 4096 * 2), sw = mk_srd(p.ws, W_TOTAL), su = mk_srd(p.out, S * D * 2);
    const unsigned lr = stg + (4 * g + (n >> 2)) * 128 + ((2 * (g & 1) + (n >> 3)) << 5) + (n & 3) * 8;
    unsigned t0, t1, tl = rp, pg, cu, nx, sp, re;
    asm volatile(
        "s_nop 4\n\t"
        "s_add_u32 %[t1], %[ro], 0\n\t"
        "s_and_b32 %[t1], %[t1], 7\n\t"
        "s_lshl_b32 %[t1], %[t1], 18\n\t"
        "s_add_u32 %[t0], %[t1], 0\n\t"
        "buffer_load_dwordx4 a[0:3], v110, %[so], %[t0] offen\n\t"
        "s_sleep 2\n\t"
        "s_add_u32 %[t0], %[t1], 16384\n\t"
        "buffer_load_dwordx4 a[4:7], v110, %[so], %[t0] offen\n\t"
        "s_sleep 2\n\t"
        "s_add_u32 %[t0], %[t1], 32768\n\t"
        "buffer_load_dwordx4 a[8:11], v110, %[so], %[t0] offen\n\t"
        "s_sleep 2\n\t"
        "s_add_u32 %[t0], %[t1], 49152\n\t"
        "buffer_load_dwordx4 a[12:15], v110, %[so], %[t0] offen\n\t"
        "s_sleep 2\n\t"
        "s_add_u32 %[t0], %[t1], 65536\n\t"
        "buffer_load_dwordx4 a[16:19], v110, %[so], %[t0] offen\n\t"
        "s_sleep 2\n\t"
        "s_add_u32 %[t0], %[t1], 81920\n\t"
        "buffer_load_dwordx4 a[20:23], v110, %[so], %[t0] offen\n\t"
        "s_sleep 2\n\t"
        "s_add_u32 %[t0], %[t1], 98304\n\t"
        "buffer_load_dwordx4 v[24:27], v110, %[so], %[t0] offen\n\t"
        "s_sleep 2\n\t"
        "s_add_u32 %[t0], %[t1], 114688\n\t"
        "buffer_load_dwordx4 v[28:31], v110, %[so], %[t0] offen\n\t"
        "s_sleep 2\n\t"
        "s_add_u32 %[t1], %[ro], 0\n\t"
        "s_and_b32 %[t1], %[t1], 7\n\t"
        "s_lshl_b32 %[t1], %[t1], 18\n\t"
        "s_add_u32 %[t0], %[t1], 131072\n\t"
        "buffer_load_dwordx4 a[24:27], v110, %[so], %[t0] offen\n\t"
        "s_sleep 2\n\t"
        "s_add_u32 %[t0], %[t1], 147456\n\t"
        "buffer_load_dwordx4 a[28:31], v110, %[so], %[t0] offen\n\t"
        "s_sleep 2\n\t"
        "s_add_u32 %[t0], %[t1], 163840\n\t"
        "buffer_load_dwordx4 a[32:35], v110, %[so], %[t0] offen\n\t"
        "s_sleep 2\n\t"
        "s_add_u32 %[t0], %[t1], 180224\n\t"
        "buffer_load_dwordx4 a[36:39], v110, %[so], %[t0] offen\n\t"
        "s_sleep 2\n\t"
        "s_add_u32 %[t0], %[t1], 196608\n\t"
        "buffer_load_dwordx4 a[40:43], v110, %[so], %[t0] offen\n\t"
        "s_sleep 2\n\t"
        "s_add_u32 %[t0], %[t1], 212992\n\t"
        "buffer_load_dwordx4 a[44:47], v110, %[so], %[t0] offen\n\t"
        "s_sleep 2\n\t"
        "s_add_u32 %[t0], %[t1], 229376\n\t"
        "buffer_load_dwordx4 v[32:35], v110, %[so], %[t0] offen\n\t"
        "s_sleep 2\n\t"
        "s_add_u32 %[t0], %[t1], 245760\n\t"
        "buffer_load_dwordx4 v[36:39], v110, %[so], %[t0] offen\n\t"
        "s_sleep 2\n\t"
        "s_waitcnt vmcnt(8)\n\t"
        "ds_write_b128 v111, a[0:3] offset:0\n\t"
        "ds_write_b128 v111, a[4:7] offset:1024\n\t"
        "ds_write_b128 v111, a[8:11] offset:2048\n\t"
        "ds_write_b128 v111, a[12:15] offset:3072\n\t"
        "ds_write_b128 v111, a[16:19] offset:4096\n\t"
        "ds_write_b128 v111, a[20:23] offset:5120\n\t"
        "ds_write_b128 v111, v[24:27] offset:6144\n\t"
        "ds_write_b128 v111, v[28:31] offset:7168\n\t"
        "ds_read_b64_tr_b16 a[0:1], v112 offset:0\n\t"
        "ds_read_b64_tr_b16 a[2:3], v112 offset:2048\n\t"
        "ds_read_b64_tr_b16 a[12:13], v112 offset:4096\n\t"
        "ds_read_b64_tr_b16 a[14:15], v112 offset:6144\n\t"
        "s_waitcnt lgkmcnt(8)\n\t"
        "ds_read_b64_tr_b16 a[4:5], v113 offset:0\n\t"
        "ds_read_b64_tr_b16 a[6:7], v113 offset:2048\n\t"
        "ds_read_b64_tr_b16 a[16:17], v113 offset:4096\n\t"
        "ds_read_b64_tr_b16 a[18:19], v113 offset:6144\n\t"
        "s_waitcnt lgkmcnt(8)\n\t"
        "ds_read_b64_tr_b16 a[8:9], v114 offset:0\n\t"
        "ds_read_b64_tr_b16 a[10:11], v114 offset:2048\n\t"
        "ds_read_b64_tr_b16 a[20:21], v114 offset:4096\n\t"
        "ds_read_b64_tr_b16 a[22:23], v114 offset:6144\n\t"
        "s_waitcnt lgkmcnt(8)\n\t"
        "ds_read_b64_tr_b16 v[56:57], v115 offset:0\n\t"
        "ds_read_b64_tr_b16 v[58:59], v115 offset:2048\n\t"
        "ds_read_b64_tr_b16 v[60:61], v115 offset:4096\n\t"
        "ds_read_b64_tr_b16 v[62:63], v115 offset:6144\n\t"
        "s_add_u32 %[t1], %[ro], 1\n\t"
        "s_and_b32 %[t1], %[t1], 7\n\t"
        "s_lshl_b32 %[t1], %[t1], 18\n\t"
        "s_add_u32 %[t0], %[t1], 0\n\t"
        "buffer_load_dwordx4 a[48:51], v110, %[so], %[t0] offen\n\t"
        "s_sleep 2\n\t"
        "s_add_u32 %[t0], %[t1], 16384\n\t"
        "buffer_load_dwordx4 a[52:55], v110, %[so], %[t0] offen\n\t"
        "s_sleep 2\n\t"
        "s_add_u32 %[t0], %[t1], 32768\n\t"
        "buffer_load_dwordx4 a[56:59], v110, %[so], %[t0] offen\n\t"
        "s_sleep 2\n\t"
        "s_add_u32 %[t0], %[t1], 49152\n\t"
        "buffer_load_dwordx4 a[60:63], v110, %[so], %[t0] offen\n\t"
        "s_sleep 2\n\t"
        "s_add_u32 %[t0], %[t1], 65536\n\t"
        "buffer_load_dwordx4 a[64:67], v110, %[so], %[t0] offen\n\t"
        "s_sleep 2\n\t"
        "s_add_u32 %[t0], %[t1], 81920\n\t"
        "buffer_load_dwordx4 a[68:71], v110, %[so], %[t0] offen\n\t"
        "s_sleep 2\n\t"
        "s_add_u32 %[t0], %[t1], 98304\n\t"
        "buffer_load_dwordx4 v[40:43], v110, %[so], %[t0] offen\n\t"
        "s_sleep 2\n\t"
        "s_add_u32 %[t0], %[t1], 114688\n\t"
        "buffer_load_dwordx4 v[44:47], v110, %[so], %[t0] offen\n\t"
        "s_sleep 2\n\t"
        "s_waitcnt lgkmcnt(0)\n\t"
        "ds_write_b128 v116, v[56:59] offset:0\n\t"
        "ds_write_b128 v116, v[60:63] offset:1024\n\t"
        "s_waitcnt vmcnt(8)\n\t"
        "ds_write_b128 v111, a[24:27] offset:0\n\t"
        "ds_write_b128 v111, a[28:31] offset:1024\n\t"
        "ds_write_b128 v111, a[32:35] offset:2048\n\t"
        "ds_write_b128 v111, a[36:39] offset:3072\n\t"
        "ds_write_b128 v111, a[40:43] offset:4096\n\t"
        "ds_write_b128 v111, a[44:47] offset:5120\n\t"
        "ds_write_b128 v111, v[32:35] offset:6144\n\t"
        "ds_write_b128 v111, v[36:39] offset:7168\n\t"
        "ds_read_b64_tr_b16 a[24:25], v112 offset:0\n\t"
        "ds_read_b64_tr_b16 a[26:27], v112 offset:2048\n\t"
        "ds_read_b64_tr_b16 a[36:37], v112 offset:4096\n\t"
        "ds_read_b64_tr_b16 a[38:39], v112 offset:6144\n\t"
        "s_waitcnt lgkmcnt(8)\n\t"
        "ds_read_b64_tr_b16 a[28:29], v113 offset:0\n\t"
        "ds_read_b64_tr_b16 a[30:31], v113 offset:2048\n\t"
        "ds_read_b64_tr_b16 a[40:41], v113 offset:4096\n\t"
        "ds_read_b64_tr_b16 a[42:43], v113 offset:6144\n\t"
        "s_waitcnt lgkmcnt(8)\n\t"
        "ds_read_b64_tr_b16 a[32:33], v114 offset:0\n\t"
        "ds_read_b64_tr_b16 a[34:35], v114 offset:2048\n\t"
        "ds_read_b64_tr_b16 a[44:45], v114 offset:4096\n\t"
        "ds_read_b64_tr_b16 a[46:47], v114 offset:6144\n\t"
        "s_waitcnt lgkmcnt(8)\n\t"
        "ds_read_b64_tr_b16 v[56:57], v115 offset:0\n\t"
        "ds_read_b64_tr_b16 v[58:59], v115 offset:2048\n\t"
        "ds_read_b64_tr_b16 v[60:61], v115 offset:4096\n\t"
        "ds_read_b64_tr_b16 v[62:63], v115 offset:6144\n\t"
        "s_add_u32 %[t1], %[ro], 1\n\t"
        "s_and_b32 %[t1], %[t1], 7\n\t"
        "s_lshl_b32 %[t1], %[t1], 18\n\t"
        "s_add_u32 %[t0], %[t1], 131072\n\t"
        "buffer_load_dwordx4 a[72:75], v110, %[so], %[t0] offen\n\t"
        "s_sleep 2\n\t"
        "s_add_u32 %[t0], %[t1], 147456\n\t"
        "buffer_load_dwordx4 a[76:79], v110, %[so], %[t0] offen\n\t"
        "s_sleep 2\n\t"
        "s_add_u32 %[t0], %[t1], 163840\n\t"
        "buffer_load_dwordx4 a[80:83], v110, %[so], %[t0] offen\n\t"
        "s_sleep 2\n\t"
        "s_add_u32 %[t0], %[t1], 180224\n\t"
        "buffer_load_dwordx4 a[84:87], v110, %[so], %[t0] offen\n\t"
        "s_sleep 2\n\t"
        "s_add_u32 %[t0], %[t1], 196608\n\t"
        "buffer_load_dwordx4 a[88:91], v110, %[so], %[t0] offen\n\t"
        "s_sleep 2\n\t"
        "s_add_u32 %[t0], %[t1], 212992\n\t"
        "buffer_load_dwordx4 a[92:95], v110, %[so], %[t0] offen\n\t"
        "s_sleep 2\n\t"
        "s_add_u32 %[t0], %[t1], 229376\n\t"
        "buffer_load_dwordx4 v[48:51], v110, %[so], %[t0] offen\n\t"
        "s_sleep 2\n\t"
        "s_add_u32 %[t0], %[t1], 245760\n\t"
        "buffer_load_dwordx4 v[52:55], v110, %[so], %[t0] offen\n\t"
        "s_sleep 2\n\t"
        "s_waitcnt lgkmcnt(0)\n\t"
        "ds_write_b128 v116, v[56:59] offset:2048\n\t"
        "ds_write_b128 v116, v[60:63] offset:3072\n\t"
        "s_waitcnt vmcnt(8)\n\t"
        "ds_write_b128 v111, a[48:51] offset:0\n\t"
        "ds_write_b128 v111, a[52:55] offset:1024\n\t"
        "ds_write_b128 v111, a[56:59] offset:2048\n\t"
        "ds_write_b128 v111, a[60:63] offset:3072\n\t"
        "ds_write_b128 v111, a[64:67] offset:4096\n\t"
        "ds_write_b128 v111, a[68:71] offset:5120\n\t"
        "ds_write_b128 v111, v[40:43] offset:6144\n\t"
        "ds_write_b128 v111, v[44:47] offset:7168\n\t"
        "ds_read_b64_tr_b16 a[48:49], v112 offset:0\n\t"
        "ds_read_b64_tr_b16 a[50:51], v112 offset:2048\n\t"
        "ds_read_b64_tr_b16 a[60:61], v112 offset:4096\n\t"
        "ds_read_b64_tr_b16 a[62:63], v112 offset:6144\n\t"
        "s_waitcnt lgkmcnt(8)\n\t"
        "ds_read_b64_tr_b16 a[52:53], v113 offset:0\n\t"
        "ds_read_b64_tr_b16 a[54:55], v113 offset:2048\n\t"
        "ds_read_b64_tr_b16 a[64:65], v113 offset:4096\n\t"
        "ds_read_b64_tr_b16 a[66:67], v113 offset:6144\n\t"
        "s_waitcnt lgkmcnt(8)\n\t"
        "ds_read_b64_tr_b16 a[56:57], v114 offset:0\n\t"
        "ds_read_b64_tr_b16 a[58:59], v114 offset:2048\n\t"
        "ds_read_b64_tr_b16 a[68:69], v114 offset:4096\n\t"
        "ds_read_b64_tr_b16 a[70:71], v114 offset:6144\n\t"
        "s_waitcnt lgkmcnt(8)\n\t"
        "ds_read_b64_tr_b16 v[56:57], v115 offset:0\n\t"
        "ds_read_b64_tr_b16 v[58:59], v115 offset:2048\n\t"
        "ds_read_b64_tr_b16 v[60:61], v115 offset:4096\n\t"
        "ds_read_b64_tr_b16 v[62:63], v115 offset:6144\n\t"
        "s_add_u32 %[t1], %[ro], 2\n\t"
        "s_and_b32 %[t1], %[t1], 7\n\t"
        "s_lshl_b32 %[t1], %[t1], 18\n\t"
        "s_add_u32 %[t0], %[t1], 0\n\t"
        "buffer_load_dwordx4 a[96:99], v110, %[so], %[t0] offen\n\t"
        "s_sleep 2\n\t"
        "s_add_u32 %[t0], %[t1], 16384\n\t"
        "buffer_load_dwordx4 a[100:103], v110, %[so], %[t0] offen\n\t"
        "s_sleep 2\n\t"
        "s_add_u32 %[t0], %[t1], 32768\n\t"
        "buffer_load_dwordx4 a[104:107], v110, %[so], %[t0] offen\n\t"
        "s_sleep 2\n\t"
        "s_add_u32 %[t0], %[t1], 49152\n\t"
        "buffer_load_dwordx4 a[108:111], v110, %[so], %[t0] offen\n\t"
        "s_sleep 2\n\t"
        "s_add_u32 %[t0], %[t1], 65536\n\t"
        "buffer_load_dwordx4 a[112:115], v110, %[so], %[t0] offen\n\t"
        "s_sleep 2\n\t"
        "s_add_u32 %[t0], %[t1], 81920\n\t"
        "buffer_load_dwordx4 a[116:119], v110, %[so], %[t0] offen\n\t"
        "s_sleep 2\n\t"
        "s_add_u32 %[t0], %[t1], 98304\n\t"
        "buffer_load_dwordx4 v[24:27], v110, %[so], %[t0] offen\n\t"
        "s_sleep 2\n\t"
        "s_add_u32 %[t0], %[t1], 114688\n\t"
        "buffer_load_dwordx4 v[28:31], v110, %[so], %[t0] offen\n\t"
        "s_sleep 2\n\t"
        "s_waitcnt lgkmcnt(0)\n\t"
        "ds_write_b128 v116, v[56:59] offset:4096\n\t"
        "ds_write_b128 v116, v[60:63] offset:5120\n\t"
        "s_waitcnt vmcnt(8)\n\t"
        "ds_write_b128 v111, a[72:75] offset:0\n\t"
        "ds_write_b128 v111, a[76:79] offset:1024\n\t"
        "ds_write_b128 v111, a[80:83] offset:2048\n\t"
        "ds_write_b128 v111, a[84:87] offset:3072\n\t"
        "ds_write_b128 v111, a[88:91] offset:4096\n\t"
        "ds_write_b128 v111, a[92:95] offset:5120\n\t"
        "ds_write_b128 v111, v[48:51] offset:6144\n\t"
        "ds_write_b128 v111, v[52:55] offset:7168\n\t"
        "ds_read_b64_tr_b16 a[72:73], v112 offset:0\n\t"
        "ds_read_b64_tr_b16 a[74:75], v112 offset:2048\n\t"
        "ds_read_b64_tr_b16 a[84:85], v112 offset:4096\n\t"
        "ds_read_b64_tr_b16 a[86:87], v112 offset:6144\n\t"
        "s_waitcnt lgkmcnt(8)\n\t"
        "ds_read_b64_tr_b16 a[76:77], v113 offset:0\n\t"
        "ds_read_b64_tr_b16 a[78:79], v113 offset:2048\n\t"
        "ds_read_b64_tr_b16 a[88:89], v113 offset:4096\n\t"
        "ds_read_b64_tr_b16 a[90:91], v113 offset:6144\n\t"
        "s_waitcnt lgkmcnt(8)\n\t"
        "ds_read_b64_tr_b16 a[80:81], v114 offset:0\n\t"
        "ds_read_b64_tr_b16 a[82:83], v114 offset:2048\n\t"
        "ds_read_b64_tr_b16 a[92:93], v114 offset:4096\n\t"
        "ds_read_b64_tr_b16 a[94:95], v114 offset:6144\n\t"
        "s_waitcnt lgkmcnt(8)\n\t"
        "ds_read_b64_tr_b16 v[56:57], v115 offset:0\n\t"
        "ds_read_b64_tr_b16 v[58:59], v115 offset:2048\n\t"
        "ds_read_b64_tr_b16 v[60:61], v115 offset:4096\n\t"
        "ds_read_b64_tr_b16 v[62:63], v115 offset:6144\n\t"
        "s_add_u32 %[t1], %[ro], 2\n\t"
        "s_and_b32 %[t1], %[t1], 7\n\t"
        "s_lshl_b32 %[t1], %[t1], 18\n\t"
        "s_add_u32 %[t0], %[t1], 131072\n\t"
        "buffer_load_dwordx4 a[120:123], v110, %[so], %[t0] offen\n\t"
        "s_sleep 2\n\t"
        "s_add_u32 %[t0], %[t1], 147456\n\t"
        "buffer_load_dwordx4 a[124:127], v110, %[so], %[t0] offen\n\t"
        "s_sleep 2\n\t"
        "s_add_u32 %[t0], %[t1], 163840\n\t"
        "buffer_load_dwordx4 a[128:131], v110, %[so], %[t0] offen\n\t"
        "s_sleep 2\n\t"
        "s_add_u32 %[t0], %[t1], 180224\n\t"
        "buffer_load_dwordx4 a[132:135], v110, %[so], %[t0] offen\n\t"
        "s_sleep 2\n\t"
        "s_add_u32 %[t0], %[t1], 196608\n\t"
        "buffer_load_dwordx4 a[136:139], v110, %[so], %[t0] offen\n\t"
        "s_sleep 2\n\t"
        "s_add_u32 %[t0], %[t1], 212992\n\t"
        "buffer_load_dwordx4 a[140:143], v110, %[so], %[t0] offen\n\t"
        "s_sleep 2\n\t"
        "s_add_u32 %[t0], %[t1], 229376\n\t"
        "buffer_load_dwordx4 v[32:35], v110, %[so], %[t0] offen\n\t"
        "s_sleep 2\n\t"
        "s_add_u32 %[t0], %[t1], 245760\n\t"
        "buffer_load_dwordx4 v[36:39], v110, %[so], %[t0] offen\n\t"
        "s_sleep 2\n\t"
        "s_waitcnt lgkmcnt(0)\n\t"
        "ds_write_b128 v116, v[56:59] offset:6144\n\t"
        "ds_write_b128 v116, v[60:63] offset:7168\n\t"
        "s_waitcnt vmcnt(8)\n\t"
        "ds_write_b128 v111, a[96:99] offset:0\n\t"
        "ds_write_b128 v111, a[100:103] offset:1024\n\t"
        "ds_write_b128 v111, a[104:107] offset:2048\n\t"
        "ds_write_b128 v111, a[108:111] offset:3072\n\t"
        "ds_write_b128 v111, a[112:115] offset:4096\n\t"
        "ds_write_b128 v111, a[116:119] offset:5120\n\t"
        "ds_write_b128 v111, v[24:27] offset:6144\n\t"
        "ds_write_b128 v111, v[28:31] offset:7168\n\t"
        "ds_read_b64_tr_b16 a[96:97], v112 offset:0\n\t"
        "ds_read_b64_tr_b16 a[98:99], v112 offset:2048\n\t"
        "ds_read_b64_tr_b16 a[108:109], v112 offset:4096\n\t"
        "ds_read_b64_tr_b16 a[110:111], v112 offset:6144\n\t"
        "s_waitcnt lgkmcnt(8)\n\t"
        "ds_read_b64_tr_b16 a[100:101], v113 offset:0\n\t"
        "ds_read_b64_tr_b16 a[102:103], v113 offset:2048\n\t"
        "ds_read_b64_tr_b16 a[112:113], v113 offset:4096\n\t"
        "ds_read_b64_tr_b16 a[114:115], v113 offset:6144\n\t"
        "s_waitcnt lgkmcnt(8)\n\t"
        "ds_read_b64_tr_b16 a[104:105], v114 offset:0\n\t"
        "ds_read_b64_tr_b16 a[106:107], v114 offset:2048\n\t"
        "ds_read_b64_tr_b16 a[116:117], v114 offset:4096\n\t"
        "ds_read_b64_tr_b16 a[118:119], v114 offset:6144\n\t"
        "s_waitcnt lgkmcnt(8)\n\t"
        "ds_read_b64_tr_b16 v[56:57], v115 offset:0\n\t"
        "ds_read_b64_tr_b16 v[58:59], v115 offset:2048\n\t"
        "ds_read_b64_tr_b16 v[60:61], v115 offset:4096\n\t"
        "ds_read_b64_tr_b16 v[62:63], v115 offset:6144\n\t"
        "s_add_u32 %[t1], %[ro], 3\n\t"
        "s_and_b32 %[t1], %[t1], 7\n\t"
        "s_lshl_b32 %[t1], %[t1], 18\n\t"
        "s_add_u32 %[t0], %[t1], 0\n\t"
        "buffer_load_dwordx4 a[144:147], v110, %[so], %[t0] offen\n\t"
        "s_sleep 2\n\t"
        "s_add_u32 %[t0], %[t1], 16384\n\t"
        "buffer_load_dwordx4 a[148:151], v110, %[so], %[t0] offen\n\t"
        "s_sleep 2\n\t"
        "s_add_u32 %[t0], %[t1], 32768\n\t"
        "buffer_load_dwordx4 a[152:155], v110, %[so], %[t0] offen\n\t"
        "s_sleep 2\n\t"
        "s_add_u32 %[t0], %[t1], 49152\n\t"
        "buffer_load_dwordx4 a[156:159], v110, %[so], %[t0] offen\n\t"
        "s_sleep 2\n\t"
        "s_add_u32 %[t0], %[t1], 65536\n\t"
        "buffer_load_dwordx4 a[160:163], v110, %[so], %[t0] offen\n\t"
        "s_sleep 2\n\t"
        "s_add_u32 %[t0], %[t1], 81920\n\t"
        "buffer_load_dwordx4 a[164:167], v110, %[so], %[t0] offen\n\t"
        "s_sleep 2\n\t"
        "s_add_u32 %[t0], %[t1], 98304\n\t"
        "buffer_load_dwordx4 v[40:43], v110, %[so], %[t0] offen\n\t"
        "s_sleep 2\n\t"
        "s_add_u32 %[t0], %[t1], 114688\n\t"
        "buffer_load_dwordx4 v[44:47], v110, %[so], %[t0] offen\n\t"
        "s_sleep 2\n\t"
        "s_waitcnt lgkmcnt(0)\n\t"
        "ds_write_b128 v116, v[56:59] offset:8192\n\t"
        "ds_write_b128 v116, v[60:63] offset:9216\n\t"
        "s_waitcnt vmcnt(8)\n\t"
        "ds_write_b128 v111, a[120:123] offset:0\n\t"
        "ds_write_b128 v111, a[124:127] offset:1024\n\t"
        "ds_write_b128 v111, a[128:131] offset:2048\n\t"
        "ds_write_b128 v111, a[132:135] offset:3072\n\t"
        "ds_write_b128 v111, a[136:139] offset:4096\n\t"
        "ds_write_b128 v111, a[140:143] offset:5120\n\t"
        "ds_write_b128 v111, v[32:35] offset:6144\n\t"
        "ds_write_b128 v111, v[36:39] offset:7168\n\t"
        "ds_read_b64_tr_b16 a[120:121], v112 offset:0\n\t"
        "ds_read_b64_tr_b16 a[122:123], v112 offset:2048\n\t"
        "ds_read_b64_tr_b16 a[132:133], v112 offset:4096\n\t"
        "ds_read_b64_tr_b16 a[134:135], v112 offset:6144\n\t"
        "s_waitcnt lgkmcnt(8)\n\t"
        "ds_read_b64_tr_b16 a[124:125], v113 offset:0\n\t"
        "ds_read_b64_tr_b16 a[126:127], v113 offset:2048\n\t"
        "ds_read_b64_tr_b16 a[136:137], v113 offset:4096\n\t"
        "ds_read_b64_tr_b16 a[138:139], v113 offset:6144\n\t"
        "s_waitcnt lgkmcnt(8)\n\t"
        "ds_read_b64_tr_b16 a[128:129], v114 offset:0\n\t"
        "ds_read_b64_tr_b16 a[130:131], v114 offset:2048\n\t"
        "ds_read_b64_tr_b16 a[140:141], v114 offset:4096\n\t"
        "ds_read_b64_tr_b16 a[142:143], v114 offset:6144\n\t"
        "s_waitcnt lgkmcnt(8)\n\t"
        "ds_read_b64_tr_b16 v[56:57], v115 offset:0\n\t"
        "ds_read_b64_tr_b16 v[58:59], v115 offset:2048\n\t"
        "ds_read_b64_tr_b16 v[60:61], v115 offset:4096\n\t"
        "ds_read_b64_tr_b16 v[62:63], v115 offset:6144\n\t"
        "s_add_u32 %[t1], %[ro], 3\n\t"
        "s_and_b32 %[t1], %[t1], 7\n\t"
        "s_lshl_b32 %[t1], %[t1], 18\n\t"
        "s_add_u32 %[t0], %[t1], 131072\n\t"
        "buffer_load_dwordx4 a[168:171], v110, %[so], %[t0] offen\n\t"
        "s_sleep 2\n\t"
        "s_add_u32 %[t0], %[t1], 147456\n\t"
        "buffer_load_dwordx4 a[172:175], v110, %[so], %[t0] offen\n\t"
        "s_sleep 2\n\t"
        "s_add_u32 %[t0], %[t1], 163840\n\t"
        "buffer_load_dwordx4 a[176:179], v110, %[so], %[t0] offen\n\t"
        "s_sleep 2\n\t"
        "s_add_u32 %[t0], %[t1], 180224\n\t"
        "buffer_load_dwordx4 a[180:183], v110, %[so], %[t0] offen\n\t"
        "s_sleep 2\n\t"
        "s_add_u32 %[t0], %[t1], 196608\n\t"
        "buffer_load_dwordx4 a[184:187], v110, %[so], %[t0] offen\n\t"
        "s_sleep 2\n\t"
        "s_add_u32 %[t0], %[t1], 212992\n\t"
        "buffer_load_dwordx4 a[188:191], v110, %[so], %[t0] offen\n\t"
        "s_sleep 2\n\t"
        "s_add_u32 %[t0], %[t1], 229376\n\t"
        "buffer_load_dwordx4 v[48:51], v110, %[so], %[t0] offen\n\t"
        "s_sleep 2\n\t"
        "s_add_u32 %[t0], %[t1], 245760\n\t"
        "buffer_load_dwordx4 v[52:55], v110, %[so], %[t0] offen\n\t"
        "s_sleep 2\n\t"
        "s_waitcnt lgkmcnt(0)\n\t"
        "ds_write_b128 v116, v[56:59] offset:10240\n\t"
        "ds_write_b128 v116, v[60:63] offset:11264\n\t"
        "s_waitcnt vmcnt(8)\n\t"
        "ds_write_b128 v111, a[144:147] offset:0\n\t"
        "ds_write_b128 v111, a[148:151] offset:1024\n\t"
        "ds_write_b128 v111, a[152:155] offset:2048\n\t"
        "ds_write_b128 v111, a[156:159] offset:3072\n\t"
        "ds_write_b128 v111, a[160:163] offset:4096\n\t"
        "ds_write_b128 v111, a[164:167] offset:5120\n\t"
        "ds_write_b128 v111, v[40:43] offset:6144\n\t"
        "ds_write_b128 v111, v[44:47] offset:7168\n\t"
        "ds_read_b64_tr_b16 a[144:145], v112 offset:0\n\t"
        "ds_read_b64_tr_b16 a[146:147], v112 offset:2048\n\t"
        "ds_read_b64_tr_b16 a[156:157], v112 offset:4096\n\t"
        "ds_read_b64_tr_b16 a[158:159], v112 offset:6144\n\t"
        "s_waitcnt lgkmcnt(8)\n\t"
        "ds_read_b64_tr_b16 a[148:149], v113 offset:0\n\t"
        "ds_read_b64_tr_b16 a[150:151], v113 offset:2048\n\t"
        "ds_read_b64_tr_b16 a[160:161], v113 offset:4096\n\t"
        "ds_read_b64_tr_b16 a[162:163], v113 offset:6144\n\t"
        "s_waitcnt lgkmcnt(8)\n\t"
        "ds_read_b64_tr_b16 a[152:153], v114 offset:0\n\t"
        "ds_read_b64_tr_b16 a[154:155], v114 offset:2048\n\t"
        "ds_read_b64_tr_b16 a[164:165], v114 offset:4096\n\t"
        "ds_read_b64_tr_b16 a[166:167], v114 offset:6144\n\t"
        "s_waitcnt lgkmcnt(8)\n\t"
        "ds_read_b64_tr_b16 v[56:57], v115 offset:0\n\t"
        "ds_read_b64_tr_b16 v[58:59], v115 offset:2048\n\t"
        "ds_read_b64_tr_b16 v[60:61], v115 offset:4096\n\t"
        "ds_read_b64_tr_b16 v[62:63], v115 offset:6144\n\t"
        "s_add_u32 %[t1], %[ro], 4\n\t"
        "s_and_b32 %[t1], %[t1], 7\n\t"
        "s_lshl_b32 %[t1], %[t1], 18\n\t"
        "s_add_u32 %[t0], %[t1], 0\n\t"
        "buffer_load_dwordx4 a[192:195], v110, %[so], %[t0] offen\n\t"
        "s_sleep 2\n\t"
        "s_add_u32 %[t0], %[t1], 16384\n\t"
        "buffer_load_dwordx4 a[196:199], v110, %[so], %[t0] offen\n\t"
        "s_sleep 2\n\t"
        "s_add_u32 %[t0], %[t1], 32768\n\t"
        "buffer_load_dwordx4 a[200:203], v110, %[so], %[t0] offen\n\t"
        "s_sleep 2\n\t"
        "s_add_u32 %[t0], %[t1], 49152\n\t"
        "buffer_load_dwordx4 a[204:207], v110, %[so], %[t0] offen\n\t"
        "s_sleep 2\n\t"
        "s_add_u32 %[t0], %[t1], 65536\n\t"
        "buffer_load_dwordx4 a[208:211], v110, %[so], %[t0] offen\n\t"
        "s_sleep 2\n\t"
        "s_add_u32 %[t0], %[t1], 81920\n\t"
        "buffer_load_dwordx4 a[212:215], v110, %[so], %[t0] offen\n\t"
        "s_sleep 2\n\t"
        "s_add_u32 %[t0], %[t1], 98304\n\t"
        "buffer_load_dwordx4 v[24:27], v110, %[so], %[t0] offen\n\t"
        "s_sleep 2\n\t"
        "s_add_u32 %[t0], %[t1], 114688\n\t"
        "buffer_load_dwordx4 v[28:31], v110, %[so], %[t0] offen\n\t"
        "s_sleep 2\n\t"
        "s_waitcnt lgkmcnt(0)\n\t"
        "ds_write_b128 v116, v[56:59] offset:12288\n\t"
        "ds_write_b128 v116, v[60:63] offset:13312\n\t"
        "s_waitcnt vmcnt(8)\n\t"
        "ds_write_b128 v111, a[168:171] offset:0\n\t"
        "ds_write_b128 v111, a[172:175] offset:1024\n\t"
        "ds_write_b128 v111, a[176:179] offset:2048\n\t"
        "ds_write_b128 v111, a[180:183] offset:3072\n\t"
        "ds_write_b128 v111, a[184:187] offset:4096\n\t"
        "ds_write_b128 v111, a[188:191] offset:5120\n\t"
        "ds_write_b128 v111, v[48:51] offset:6144\n\t"
        "ds_write_b128 v111, v[52:55] offset:7168\n\t"
        "ds_read_b64_tr_b16 a[168:169], v112 offset:0\n\t"
        "ds_read_b64_tr_b16 a[170:171], v112 offset:2048\n\t"
        "ds_read_b64_tr_b16 a[180:181], v112 offset:4096\n\t"
        "ds_read_b64_tr_b16 a[182:183], v112 offset:6144\n\t"
        "s_waitcnt lgkmcnt(8)\n\t"
        "ds_read_b64_tr_b16 a[172:173], v113 offset:0\n\t"
        "ds_read_b64_tr_b16 a[174:175], v113 offset:2048\n\t"
        "ds_read_b64_tr_b16 a[184:185], v113 offset:4096\n\t"
        "ds_read_b64_tr_b16 a[186:187], v113 offset:6144\n\t"
        "s_waitcnt lgkmcnt(8)\n\t"
        "ds_read_b64_tr_b16 a[176:177], v114 offset:0\n\t"
        "ds_read_b64_tr_b16 a[178:179], v114 offset:2048\n\t"
        "ds_read_b64_tr_b16 a[188:189], v114 offset:4096\n\t"
        "ds_read_b64_tr_b16 a[190:191], v114 offset:6144\n\t"
        "s_waitcnt lgkmcnt(8)\n\t"
        "ds_read_b64_tr_b16 v[56:57], v115 offset:0\n\t"
        "ds_read_b64_tr_b16 v[58:59], v115 offset:2048\n\t"
        "ds_read_b64_tr_b16 v[60:61], v115 offset:4096\n\t"
        "ds_read_b64_tr_b16 v[62:63], v115 offset:6144\n\t"
        "s_add_u32 %[t1], %[ro], 4\n\t"
        "s_and_b32 %[t1], %[t1], 7\n\t"
        "s_lshl_b32 %[t1], %[t1], 18\n\t"
        "s_add_u32 %[t0], %[t1], 131072\n\t"
        "buffer_load_dwordx4 a[216:219], v110, %[so], %[t0] offen\n\t"
        "s_sleep 2\n\t"
        "s_add_u32 %[t0], %[t1], 147456\n\t"
        "buffer_load_dwordx4 a[220:223], v110, %[so], %[t0] offen\n\t"
        "s_sleep 2\n\t"
        "s_add_u32 %[t0], %[t1], 163840\n\t"
        "buffer_load_dwordx4 a[224:227], v110, %[so], %[t0] offen\n\t"
        "s_sleep 2\n\t"
        "s_add_u32 %[t0], %[t1], 180224\n\t"
        "buffer_load_dwordx4 a[228:231], v110, %[so], %[t0] offen\n\t"
        "s_sleep 2\n\t"
        "s_add_u32 %[t0], %[t1], 196608\n\t"
        "buffer_load_dwordx4 a[232:235], v110, %[so], %[t0] offen\n\t"
        "s_sleep 2\n\t"
        "s_add_u32 %[t0], %[t1], 212992\n\t"
        "buffer_load_dwordx4 a[236:239], v110, %[so], %[t0] offen\n\t"
        "s_sleep 2\n\t"
        "s_add_u32 %[t0], %[t1], 229376\n\t"
        "buffer_load_dwordx4 v[32:35], v110, %[so], %[t0] offen\n\t"
        "s_sleep 2\n\t"
        "s_add_u32 %[t0], %[t1], 245760\n\t"
        "buffer_load_dwordx4 v[36:39], v110, %[so], %[t0] offen\n\t"
        "s_sleep 2\n\t"
        "s_waitcnt lgkmcnt(0)\n\t"
        "ds_write_b128 v116, v[56:59] offset:14336\n\t"
        "ds_write_b128 v116, v[60:63] offset:15360\n\t"
        "s_waitcnt vmcnt(8)\n\t"
        "ds_write_b128 v111, a[192:195] offset:0\n\t"
        "ds_write_b128 v111, a[196:199] offset:1024\n\t"
        "ds_write_b128 v111, a[200:203] offset:2048\n\t"
        "ds_write_b128 v111, a[204:207] offset:3072\n\t"
        "ds_write_b128 v111, a[208:211] offset:4096\n\t"
        "ds_write_b128 v111, a[212:215] offset:5120\n\t"
        "ds_write_b128 v111, v[24:27] offset:6144\n\t"
        "ds_write_b128 v111, v[28:31] offset:7168\n\t"
        "ds_read_b64_tr_b16 a[192:193], v112 offset:0\n\t"
        "ds_read_b64_tr_b16 a[194:195], v112 offset:2048\n\t"
        "ds_read_b64_tr_b16 a[204:205], v112 offset:4096\n\t"
        "ds_read_b64_tr_b16 a[206:207], v112 offset:6144\n\t"
        "s_waitcnt lgkmcnt(8)\n\t"
        "ds_read_b64_tr_b16 a[196:197], v113 offset:0\n\t"
        "ds_read_b64_tr_b16 a[198:199], v113 offset:2048\n\t"
        "ds_read_b64_tr_b16 a[208:209], v113 offset:4096\n\t"
        "ds_read_b64_tr_b16 a[210:211], v113 offset:6144\n\t"
        "s_waitcnt lgkmcnt(8)\n\t"
        "ds_read_b64_tr_b16 a[200:201], v114 offset:0\n\t"
        "ds_read_b64_tr_b16 a[202:203], v114 offset:2048\n\t"
        "ds_read_b64_tr_b16 a[212:213], v114 offset:4096\n\t"
        "ds_read_b64_tr_b16 a[214:215], v114 offset:6144\n\t"
        "s_waitcnt lgkmcnt(8)\n\t"
        "ds_read_b64_tr_b16 v[56:57], v115 offset:0\n\t"
        "ds_read_b64_tr_b16 v[58:59], v115 offset:2048\n\t"
        "ds_read_b64_tr_b16 v[60:61], v115 offset:4096\n\t"
        "ds_read_b64_tr_b16 v[62:63], v115 offset:6144\n\t"
        "s_add_u32 %[t1], %[ro], 5\n\t"
        "s_and_b32 %[t1], %[t1], 7\n\t"
        "s_lshl_b32 %[t1], %[t1], 18\n\t"
        "s_add_u32 %[t0], %[t1], 0\n\t"
        "buffer_load_dwordx4 a[240:243], v110, %[so], %[t0] offen\n\t"
        "s_sleep 2\n\t"
        "s_add_u32 %[t0], %[t1], 16384\n\t"
        "buffer_load_dwordx4 a[244:247], v110, %[so], %[t0] offen\n\t"
        "s_sleep 2\n\t"
        "s_add_u32 %[t0], %[t1], 32768\n\t"
        "buffer_load_dwordx4 a[248:251], v110, %[so], %[t0] offen\n\t"
        "s_sleep 2\n\t"
        "s_add_u32 %[t0], %[t1], 49152\n\t"
        "buffer_load_dwordx4 a[252:255], v110, %[so], %[t0] offen\n\t"
        "s_sleep 2\n\t"
        "s_add_u32 %[t0], %[t1], 65536\n\t"
        "buffer_load_dwordx4 v[128:131], v110, %[so], %[t0] offen\n\t"
        "s_sleep 2\n\t"
        "s_add_u32 %[t0], %[t1], 81920\n\t"
        "buffer_load_dwordx4 v[132:135], v110, %[so], %[t0] offen\n\t"
        "s_sleep 2\n\t"
        "s_add_u32 %[t0], %[t1], 98304\n\t"
        "buffer_load_dwordx4 v[40:43], v110, %[so], %[t0] offen\n\t"
        "s_sleep 2\n\t"
        "s_add_u32 %[t0], %[t1], 114688\n\t"
        "buffer_load_dwordx4 v[44:47], v110, %[so], %[t0] offen\n\t"
        "s_sleep 2\n\t"
        "s_waitcnt lgkmcnt(0)\n\t"
        "ds_write_b128 v116, v[56:59] offset:16384\n\t"
        "ds_write_b128 v116, v[60:63] offset:17408\n\t"
        "s_waitcnt vmcnt(8)\n\t"
        "ds_write_b128 v111, a[216:219] offset:0\n\t"
        "ds_write_b128 v111, a[220:223] offset:1024\n\t"
        "ds_write_b128 v111, a[224:227] offset:2048\n\t"
        "ds_write_b128 v111, a[228:231] offset:3072\n\t"
        "ds_write_b128 v111, a[232:235] offset:4096\n\t"
        "ds_write_b128 v111, a[236:239] offset:5120\n\t"
        "ds_write_b128 v111, v[32:35] offset:6144\n\t"
        "ds_write_b128 v111, v[36:39] offset:7168\n\t"
        "ds_read_b64_tr_b16 a[216:217], v112 offset:0\n\t"
        "ds_read_b64_tr_b16 a[218:219], v112 offset:2048\n\t"
        "ds_read_b64_tr_b16 a[228:229], v112 offset:4096\n\t"
        "ds_read_b64_tr_b16 a[230:231], v112 offset:6144\n\t"
        "s_waitcnt lgkmcnt(8)\n\t"
        "ds_read_b64_tr_b16 a[220:221], v113 offset:0\n\t"
        "ds_read_b64_tr_b16 a[222:223], v113 offset:2048\n\t"
        "ds_read_b64_tr_b16 a[232:233], v113 offset:4096\n\t"
        "ds_read_b64_tr_b16 a[234:235], v113 offset:6144\n\t"
        "s_waitcnt lgkmcnt(8)\n\t"
        "ds_read_b64_tr_b16 a[224:225], v114 offset:0\n\t"
        "ds_read_b64_tr_b16 a[226:227], v114 offset:2048\n\t"
        "ds_read_b64_tr_b16 a[236:237], v114 offset:4096\n\t"
        "ds_read_b64_tr_b16 a[238:239], v114 offset:6144\n\t"
        "s_waitcnt lgkmcnt(8)\n\t"
        "ds_read_b64_tr_b16 v[56:57], v115 offset:0\n\t"
        "ds_read_b64_tr_b16 v[58:59], v115 offset:2048\n\t"
        "ds_read_b64_tr_b16 v[60:61], v115 offset:4096\n\t"
        "ds_read_b64_tr_b16 v[62:63], v115 offset:6144\n\t"
        "s_add_u32 %[t1], %[ro], 5\n\t"
        "s_and_b32 %[t1], %[t1], 7\n\t"
        "s_lshl_b32 %[t1], %[t1], 18\n\t"
        "s_add_u32 %[t0], %[t1], 131072\n\t"
        "buffer_load_dwordx4 v[136:139], v110, %[so], %[t0] offen\n\t"
        "s_sleep 2\n\t"
        "s_add_u32 %[t0], %[t1], 147456\n\t"
        "buffer_load_dwordx4 v[140:143], v110, %[so], %[t0] offen\n\t"
        "s_sleep 2\n\t"
        "s_add_u32 %[t0], %[t1], 163840\n\t"
        "buffer_load_dwordx4 v[144:147], v110, %[so], %[t0] offen\n\t"
        "s_sleep 2\n\t"
        "s_add_u32 %[t0], %[t1], 180224\n\t"
        "buffer_load_dwordx4 v[148:151], v110, %[so], %[t0] offen\n\t"
        "s_sleep 2\n\t"
        "s_add_u32 %[t0], %[t1], 196608\n\t"
        "buffer_load_dwordx4 v[152:155], v110, %[so], %[t0] offen\n\t"
        "s_sleep 2\n\t"
        "s_add_u32 %[t0], %[t1], 212992\n\t"
        "buffer_load_dwordx4 v[156:159], v110, %[so], %[t0] offen\n\t"
        "s_sleep 2\n\t"
        "s_add_u32 %[t0], %[t1], 229376\n\t"
        "buffer_load_dwordx4 v[48:51], v110, %[so], %[t0] offen\n\t"
        "s_sleep 2\n\t"
        "s_add_u32 %[t0], %[t1], 245760\n\t"
        "buffer_load_dwordx4 v[52:55], v110, %[so], %[t0] offen\n\t"
        "s_sleep 2\n\t"
        "s_waitcnt lgkmcnt(0)\n\t"
        "ds_write_b128 v116, v[56:59] offset:18432\n\t"
        "ds_write_b128 v116, v[60:63] offset:19456\n\t"
        "s_waitcnt vmcnt(8)\n\t"
        "ds_write_b128 v111, a[240:243] offset:0\n\t"
        "ds_write_b128 v111, a[244:247] offset:1024\n\t"
        "ds_write_b128 v111, a[248:251] offset:2048\n\t"
        "ds_write_b128 v111, a[252:255] offset:3072\n\t"
        "ds_write_b128 v111, v[128:131] offset:4096\n\t"
        "ds_write_b128 v111, v[132:135] offset:5120\n\t"
        "ds_write_b128 v111, v[40:43] offset:6144\n\t"
        "ds_write_b128 v111, v[44:47] offset:7168\n\t"
        "ds_read_b64_tr_b16 a[240:241], v112 offset:0\n\t"
        "ds_read_b64_tr_b16 a[242:243], v112 offset:2048\n\t"
        "ds_read_b64_tr_b16 a[252:253], v112 offset:4096\n\t"
        "ds_read_b64_tr_b16 a[254:255], v112 offset:6144\n\t"
        "s_waitcnt lgkmcnt(8)\n\t"
        "ds_read_b64_tr_b16 a[244:245], v113 offset:0\n\t"
        "ds_read_b64_tr_b16 a[246:247], v113 offset:2048\n\t"
        "ds_read_b64_tr_b16 v[128:129], v113 offset:4096\n\t"
        "ds_read_b64_tr_b16 v[130:131], v113 offset:6144\n\t"
        "s_waitcnt lgkmcnt(8)\n\t"
        "ds_read_b64_tr_b16 a[248:249], v114 offset:0\n\t"
        "ds_read_b64_tr_b16 a[250:251], v114 offset:2048\n\t"
        "ds_read_b64_tr_b16 v[132:133], v114 offset:4096\n\t"
        "ds_read_b64_tr_b16 v[134:135], v114 offset:6144\n\t"
        "s_waitcnt lgkmcnt(8)\n\t"
        "ds_read_b64_tr_b16 v[56:57], v115 offset:0\n\t"
        "ds_read_b64_tr_b16 v[58:59], v115 offset:2048\n\t"
        "ds_read_b64_tr_b16 v[60:61], v115 offset:4096\n\t"
        "ds_read_b64_tr_b16 v[62:63], v115 offset:6144\n\t"
        "s_add_u32 %[t1], %[ro], 6\n\t"
        "s_and_b32 %[t1], %[t1], 7\n\t"
        "s_lshl_b32 %[t1], %[t1], 18\n\t"
        "s_add_u32 %[t0], %[t1], 0\n\t"
        "buffer_load_dwordx4 v[160:163], v110, %[so], %[t0] offen\n\t"
        "s_sleep 2\n\t"
        "s_add_u32 %[t0], %[t1], 16384\n\t"
        "buffer_load_dwordx4 v[164:167], v110, %[so], %[t0] offen\n\t"
        "s_sleep 2\n\t"
        "s_add_u32 %[t0], %[t1], 32768\n\t"
        "buffer_load_dwordx4 v[168:171], v110, %[so], %[t0] offen\n\t"
        "s_sleep 2\n\t"
        "s_add_u32 %[t0], %[t1], 49152\n\t"
        "buffer_load_dwordx4 v[172:175], v110, %[so], %[t0] offen\n\t"
        "s_sleep 2\n\t"
        "s_add_u32 %[t0], %[t1], 65536\n\t"
        "buffer_load_dwordx4 v[176:179], v110, %[so], %[t0] offen\n\t"
        "s_sleep 2\n\t"
        "s_add_u32 %[t0], %[t1], 81920\n\t"
        "buffer_load_dwordx4 v[180:183], v110, %[so], %[t0] offen\n\t"
        "s_sleep 2\n\t"
        "s_add_u32 %[t0], %[t1], 98304\n\t"
        "buffer_load_dwordx4 v[24:27], v110, %[so], %[t0] offen\n\t"
        "s_sleep 2\n\t"
        "s_add_u32 %[t0], %[t1], 114688\n\t"
        "buffer_load_dwordx4 v[28:31], v110, %[so], %[t0] offen\n\t"
        "s_sleep 2\n\t"
        "s_waitcnt lgkmcnt(0)\n\t"
        "ds_write_b128 v116, v[56:59] offset:20480\n\t"
        "ds_write_b128 v116, v[60:63] offset:21504\n\t"
        "s_waitcnt vmcnt(8)\n\t"
        "ds_write_b128 v111, v[136:139] offset:0\n\t"
        "ds_write_b128 v111, v[140:143] offset:1024\n\t"
        "ds_write_b128 v111, v[144:147] offset:2048\n\t"
        "ds_write_b128 v111, v[148:151] offset:3072\n\t"
        "ds_write_b128 v111, v[152:155] offset:4096\n\t"
        "ds_write_b128 v111, v[156:159] offset:5120\n\t"
        "ds_write_b128 v111, v[48:51] offset:6144\n\t"
        "ds_write_b128 v111, v[52:55] offset:7168\n\t"
        "ds_read_b64_tr_b16 v[136:137], v112 offset:0\n\t"
        "ds_read_b64_tr_b16 v[138:139], v112 offset:2048\n\t"
        "ds_read_b64_tr_b16 v[148:149], v112 offset:4096\n\t"
        "ds_read_b64_tr_b16 v[150:151], v112 offset:6144\n\t"
        "s_waitcnt lgkmcnt(8)\n\t"
        "ds_read_b64_tr_b16 v[140:141], v113 offset:0\n\t"
        "ds_read_b64_tr_b16 v[142:143], v113 offset:2048\n\t"
        "ds_read_b64_tr_b16 v[152:153], v113 offset:4096\n\t"
        "ds_read_b64_tr_b16 v[154:155], v113 offset:6144\n\t"
        "s_waitcnt lgkmcnt(8)\n\t"
        "ds_read_b64_tr_b16 v[144:145], v114 offset:0\n\t"
        "ds_read_b64_tr_b16 v[146:147], v114 offset:2048\n\t"
        "ds_read_b64_tr_b16 v[156:157], v114 offset:4096\n\t"
        "ds_read_b64_tr_b16 v[158:159], v114 offset:6144\n\t"
        "s_waitcnt lgkmcnt(8)\n\t"
        "ds_read_b64_tr_b16 v[56:57], v115 offset:0\n\t"
        "ds_read_b64_tr_b16 v[58:59], v115 offset:2048\n\t"
        "ds_read_b64_tr_b16 v[60:61], v115 offset:4096\n\t"
        "ds_read_b64_tr_b16 v[62:63], v115 offset:6144\n\t"
        "s_add_u32 %[t1], %[ro], 6\n\t"
        "s_and_b32 %[t1], %[t1], 7\n\t"
        "s_lshl_b32 %[t1], %[t1], 18\n\t"
        "s_add_u32 %[t0], %[t1], 131072\n\t"
        "buffer_load_dwordx4 v[184:187], v110, %[so], %[t0] offen\n\t"
        "s_sleep 2\n\t"
        "s_add_u32 %[t0], %[t1], 147456\n\t"
        "buffer_load_dwordx4 v[188:191], v110, %[so], %[t0] offen\n\t"
        "s_sleep 2\n\t"
        "s_add_u32 %[t0], %[t1], 163840\n\t"
        "buffer_load_dwordx4 v[192:195], v110, %[so], %[t0] offen\n\t"
        "s_sleep 2\n\t"
        "s_add_u32 %[t0], %[t1], 180224\n\t"
        "buffer_load_dwordx4 v[196:199], v110, %[so], %[t0] offen\n\t"
        "s_sleep 2\n\t"
        "s_add_u32 %[t0], %[t1], 196608\n\t"
        "buffer_load_dwordx4 v[200:203], v110, %[so], %[t0] offen\n\t"
        "s_sleep 2\n\t"
        "s_add_u32 %[t0], %[t1], 212992\n\t"
        "buffer_load_dwordx4 v[204:207], v110, %[so], %[t0] offen\n\t"
        "s_sleep 2\n\t"
        "s_add_u32 %[t0], %[t1], 229376\n\t"
        "buffer_load_dwordx4 v[32:35], v110, %[so], %[t0] offen\n\t"
        "s_sleep 2\n\t"
        "s_add_u32 %[t0], %[t1], 245760\n\t"
        "buffer_load_dwordx4 v[36:39], v110, %[so], %[t0] offen\n\t"
        "s_sleep 2\n\t"
        "s_waitcnt lgkmcnt(0)\n\t"
        "ds_write_b128 v116, v[56:59] offset:22528\n\t"
        "ds_write_b128 v116, v[60:63] offset:23552\n\t"
        "s_waitcnt vmcnt(8)\n\t"
        "ds_write_b128 v111, v[160:163] offset:0\n\t"
        "ds_write_b128 v111, v[164:167] offset:1024\n\t"
        "ds_write_b128 v111, v[168:171] offset:2048\n\t"
        "ds_write_b128 v111, v[172:175] offset:3072\n\t"
        "ds_write_b128 v111, v[176:179] offset:4096\n\t"
        "ds_write_b128 v111, v[180:183] offset:5120\n\t"
        "ds_write_b128 v111, v[24:27] offset:6144\n\t"
        "ds_write_b128 v111, v[28:31] offset:7168\n\t"
        "ds_read_b64_tr_b16 v[160:161], v112 offset:0\n\t"
        "ds_read_b64_tr_b16 v[162:163], v112 offset:2048\n\t"
        "ds_read_b64_tr_b16 v[172:173], v112 offset:4096\n\t"
        "ds_read_b64_tr_b16 v[174:175], v112 offset:6144\n\t"
        "s_waitcnt lgkmcnt(8)\n\t"
        "ds_read_b64_tr_b16 v[164:165], v113 offset:0\n\t"
        "ds_read_b64_tr_b16 v[166:167], v113 offset:2048\n\t"
        "ds_read_b64_tr_b16 v[176:177], v113 offset:4096\n\t"
        "ds_read_b64_tr_b16 v[178:179], v113 offset:6144\n\t"
        "s_waitcnt lgkmcnt(8)\n\t"
        "ds_read_b64_tr_b16 v[168:169], v114 offset:0\n\t"
        "ds_read_b64_tr_b16 v[170:171], v114 offset:2048\n\t"
        "ds_read_b64_tr_b16 v[180:181], v114 offset:4096\n\t"
        "ds_read_b64_tr_b16 v[182:183], v114 offset:6144\n\t"
        "s_waitcnt lgkmcnt(8)\n\t"
        "ds_read_b64_tr_b16 v[56:57], v115 offset:0\n\t"
        "ds_read_b64_tr_b16 v[58:59], v115 offset:2048\n\t"
        "ds_read_b64_tr_b16 v[60:61], v115 offset:4096\n\t"
        "ds_read_b64_tr_b16 v[62:63], v115 offset:6144\n\t"
        "s_add_u32 %[t1], %[ro], 7\n\t"
        "s_and_b32 %[t1], %[t1], 7\n\t"
        "s_lshl_b32 %[t1], %[t1], 18\n\t"
        "s_add_u32 %[t0], %[t1], 0\n\t"
        "buffer_load_dwordx4 v[208:211], v110, %[so], %[t0] offen\n\t"
        "s_sleep 2\n\t"
        "s_add_u32 %[t0], %[t1], 16384\n\t"
        "buffer_load_dwordx4 v[212:215], v110, %[so], %[t0] offen\n\t"
        "s_sleep 2\n\t"
        "s_add_u32 %[t0], %[t1], 32768\n\t"
        "buffer_load_dwordx4 v[216:219], v110, %[so], %[t0] offen\n\t"
        "s_sleep 2\n\t"
        "s_add_u32 %[t0], %[t1], 49152\n\t"
        "buffer_load_dwordx4 v[220:223], v110, %[so], %[t0] offen\n\t"
        "s_sleep 2\n\t"
        "s_add_u32 %[t0], %[t1], 65536\n\t"
        "buffer_load_dwordx4 v[224:227], v110, %[so], %[t0] offen\n\t"
        "s_sleep 2\n\t"
        "s_add_u32 %[t0], %[t1], 81920\n\t"
        "buffer_load_dwordx4 v[228:231], v110, %[so], %[t0] offen\n\t"
        "s_sleep 2\n\t"
        "s_add_u32 %[t0], %[t1], 98304\n\t"
        "buffer_load_dwordx4 v[40:43], v110, %[so], %[t0] offen\n\t"
        "s_sleep 2\n\t"
        "s_add_u32 %[t0], %[t1], 114688\n\t"
        "buffer_load_dwordx4 v[44:47], v110, %[so], %[t0] offen\n\t"
        "s_sleep 2\n\t"
        "s_waitcnt lgkmcnt(0)\n\t"
        "ds_write_b128 v116, v[56:59] offset:24576\n\t"
        "ds_write_b128 v116, v[60:63] offset:25600\n\t"
        "s_waitcnt vmcnt(8)\n\t"
        "ds_write_b128 v111, v[184:187] offset:0\n\t"
        "ds_write_b128 v111, v[188:191] offset:1024\n\t"
        "ds_write_b128 v111, v[192:195] offset:2048\n\t"
        "ds_write_b128 v111, v[196:199] offset:3072\n\t"
        "ds_write_b128 v111, v[200:203] offset:4096\n\t"
        "ds_write_b128 v111, v[204:207] offset:5120\n\t"
        "ds_write_b128 v111, v[32:35] offset:6144\n\t"
        "ds_write_b128 v111, v[36:39] offset:7168\n\t"
        "ds_read_b64_tr_b16 v[184:185], v112 offset:0\n\t"
        "ds_read_b64_tr_b16 v[186:187], v112 offset:2048\n\t"
        "ds_read_b64_tr_b16 v[196:197], v112 offset:4096\n\t"
        "ds_read_b64_tr_b16 v[198:199], v112 offset:6144\n\t"
        "s_waitcnt lgkmcnt(8)\n\t"
        "ds_read_b64_tr_b16 v[188:189], v113 offset:0\n\t"
        "ds_read_b64_tr_b16 v[190:191], v113 offset:2048\n\t"
        "ds_read_b64_tr_b16 v[200:201], v113 offset:4096\n\t"
        "ds_read_b64_tr_b16 v[202:203], v113 offset:6144\n\t"
        "s_waitcnt lgkmcnt(8)\n\t"
        "ds_read_b64_tr_b16 v[192:193], v114 offset:0\n\t"
        "ds_read_b64_tr_b16 v[194:195], v114 offset:2048\n\t"
        "ds_read_b64_tr_b16 v[204:205], v114 offset:4096\n\t"
        "ds_read_b64_tr_b16 v[206:207], v114 offset:6144\n\t"
        "s_waitcnt lgkmcnt(8)\n\t"
        "ds_read_b64_tr_b16 v[56:57], v115 offset:0\n\t"
        "ds_read_b64_tr_b16 v[58:59], v115 offset:2048\n\t"
        "ds_read_b64_tr_b16 v[60:61], v115 offset:4096\n\t"
        "ds_read_b64_tr_b16 v[62:63], v115 offset:6144\n\t"
        "s_add_u32 %[t1], %[ro], 7\n\t"
        "s_and_b32 %[t1], %[t1], 7\n\t"
        "s_lshl_b32 %[t1], %[t1], 18\n\t"
        "s_add_u32 %[t0], %[t1], 131072\n\t"
        "buffer_load_dwordx4 v[232:235], v110, %[so], %[t0] offen\n\t"
        "s_sleep 2\n\t"
        "s_add_u32 %[t0], %[t1], 147456\n\t"
        "buffer_load_dwordx4 v[236:239], v110, %[so], %[t0] offen\n\t"
        "s_sleep 2\n\t"
        "s_add_u32 %[t0], %[t1], 163840\n\t"
        "buffer_load_dwordx4 v[240:243], v110, %[so], %[t0] offen\n\t"
        "s_sleep 2\n\t"
        "s_add_u32 %[t0], %[t1], 180224\n\t"
        "buffer_load_dwordx4 v[244:247], v110, %[so], %[t0] offen\n\t"
        "s_sleep 2\n\t"
        "s_add_u32 %[t0], %[t1], 196608\n\t"
        "buffer_load_dwordx4 v[248:251], v110, %[so], %[t0] offen\n\t"
        "s_sleep 2\n\t"
        "s_add_u32 %[t0], %[t1], 212992\n\t"
        "buffer_load_dwordx4 v[252:255], v110, %[so], %[t0] offen\n\t"
        "s_sleep 2\n\t"
        "s_add_u32 %[t0], %[t1], 229376\n\t"
        "buffer_load_dwordx4 v[48:51], v110, %[so], %[t0] offen\n\t"
        "s_sleep 2\n\t"
        "s_add_u32 %[t0], %[t1], 245760\n\t"
        "buffer_load_dwordx4 v[52:55], v110, %[so], %[t0] offen\n\t"
        "s_sleep 2\n\t"
        "s_waitcnt lgkmcnt(0)\n\t"
        "ds_write_b128 v116, v[56:59] offset:26624\n\t"
        "ds_write_b128 v116, v[60:63] offset:27648\n\t"
        "s_waitcnt vmcnt(8)\n\t"
        "ds_write_b128 v111, v[208:211] offset:0\n\t"
        "ds_write_b128 v111, v[212:215] offset:1024\n\t"
        "ds_write_b128 v111, v[216:219] offset:2048\n\t"
        "ds_write_b128 v111, v[220:223] offset:3072\n\t"
        "ds_write_b128 v111, v[224:227] offset:4096\n\t"
        "ds_write_b128 v111, v[228:231] offset:5120\n\t"
        "ds_write_b128 v111, v[40:43] offset:6144\n\t"
        "ds_write_b128 v111, v[44:47] offset:7168\n\t"
        "ds_read_b64_tr_b16 v[208:209], v112 offset:0\n\t"
        "ds_read_b64_tr_b16 v[210:211], v112 offset:2048\n\t"
        "ds_read_b64_tr_b16 v[220:221], v112 offset:4096\n\t"
        "ds_read_b64_tr_b16 v[222:223], v112 offset:6144\n\t"
        "s_waitcnt lgkmcnt(8)\n\t"
        "ds_read_b64_tr_b16 v[212:213], v113 offset:0\n\t"
        "ds_read_b64_tr_b16 v[214:215], v113 offset:2048\n\t"
        "ds_read_b64_tr_b16 v[224:225], v113 offset:4096\n\t"
        "ds_read_b64_tr_b16 v[226:227], v113 offset:6144\n\t"
        "s_waitcnt lgkmcnt(8)\n\t"
        "ds_read_b64_tr_b16 v[216:217], v114 offset:0\n\t"
        "ds_read_b64_tr_b16 v[218:219], v114 offset:2048\n\t"
        "ds_read_b64_tr_b16 v[228:229], v114 offset:4096\n\t"
        "ds_read_b64_tr_b16 v[230:231], v114 offset:6144\n\t"
        "s_waitcnt lgkmcnt(8)\n\t"
        "ds_read_b64_tr_b16 v[56:57], v115 offset:0\n\t"
        "ds_read_b64_tr_b16 v[58:59], v115 offset:2048\n\t"
        "ds_read_b64_tr_b16 v[60:61], v115 offset:4096\n\t"
        "ds_read_b64_tr_b16 v[62:63], v115 offset:6144\n\t"
        "s_waitcnt lgkmcnt(0)\n\t"
        "ds_write_b128 v116, v[56:59] offset:28672\n\t"
        "ds_write_b128 v116, v[60:63] offset:29696\n\t"
        "s_waitcnt vmcnt(0)\n\t"
        "ds_write_b128 v111, v[232:235] offset:0\n\t"
        "ds_write_b128 v111, v[236:239] offset:1024\n\t"
        "ds_write_b128 v111, v[240:243] offset:2048\n\t"
        "ds_write_b128 v111, v[244:247] offset:3072\n\t"
        "ds_write_b128 v111, v[248:251] offset:4096\n\t"
        "ds_write_b128 v111, v[252:255] offset:5120\n\t"
        "ds_write_b128 v111, v[48:51] offset:6144\n\t"
        "ds_write_b128 v111, v[52:55] offset:7168\n\t"
        "ds_read_b64_tr_b16 v[232:233], v112 offset:0\n\t"
        "ds_read_b64_tr_b16 v[234:235], v112 offset:2048\n\t"
        "ds_read_b64_tr_b16 v[244:245], v112 offset:4096\n\t"
        "ds_read_b64_tr_b16 v[246:247], v112 offset:6144\n\t"
        "s_waitcnt lgkmcnt(8)\n\t"
        "ds_read_b64_tr_b16 v[236:237], v113 offset:0\n\t"
        "ds_read_b64_tr_b16 v[238:239], v113 offset:2048\n\t"
        "ds_read_b64_tr_b16 v[248:249], v113 offset:4096\n\t"
        "ds_read_b64_tr_b16 v[250:251], v113 offset:6144\n\t"
        "s_waitcnt lgkmcnt(8)\n\t"
        "ds_read_b64_tr_b16 v[240:241], v114 offset:0\n\t"
        "ds_read_b64_tr_b16 v[242:243], v114 offset:2048\n\t"
        "ds_read_b64_tr_b16 v[252:253], v114 offset:4096\n\t"
        "ds_read_b64_tr_b16 v[254:255], v114 offset:6144\n\t"
        "s_waitcnt lgkmcnt(8)\n\t"
        "ds_read_b64_tr_b16 v[56:57], v115 offset:0\n\t"
        "ds_read_b64_tr_b16 v[58:59], v115 offset:2048\n\t"
        "ds_read_b64_tr_b16 v[60:61], v115 offset:4096\n\t"
        "ds_read_b64_tr_b16 v[62:63], v115 offset:6144\n\t"
        "s_waitcnt lgkmcnt(0)\n\t"
        "ds_write_b128 v116, v[56:59] offset:30720\n\t"
        "ds_write_b128 v116, v[60:63] offset:31744\n\t"
        "ds_read_b128 v[88:91], v116 offset:0\n\t"
        "ds_read_b128 v[92:95], v116 offset:1024\n\t"
        "ds_read_b128 v[96:99], v116 offset:2048\n\t"
        "s_mov_b32 %[sp], 0\n\t"
        "LP_cv%=:\n\t"
        "buffer_load_dword v104, off, %[sw], %[cv] sc1\n\t"
        "s_waitcnt vmcnt(0) lgkmcnt(0)\n\t"
        "v_readfirstlane_b32 %[t0], v104\n\t"
        "s_cmp_ge_u32 %[t0], 8\n\t"
        "s_cbranch_scc1 LP_wait%=\n\t"
        "s_sleep 8\n\t"
        "s_add_u32 %[sp], %[sp], 1\n\t"
        "s_cmp_lt_u32 %[sp], 0x100000\n\t"
        "s_cbranch_scc1 LP_cv%=\n\t"
        "v_mov_b32_e32 v104, 256\n\t"
        "s_mov_b32 %[t0], 33024\n\t"
        "buffer_store_dword v104, off, %[sw], %[t0] sc1\n\t"
        "s_endpgm\n\t"
        "LP_wait%=:\n\t"
        "s_mov_b32 %[re], 0\n\t"
        "LP_again%=:\n\t"
        "s_mov_b32 %[sp], 0\n\t"
        "LP_poll%=:\n\t"
        "buffer_load_dword v103, v118, %[sw], 0 offen sc1\n\t"
        "s_lshr_b32 %[t1], %[tl], 1\n\t"
        "s_waitcnt vmcnt(0)\n\t"
        "v_min_u32_dpp v103, v103, v103 quad_perm:[1,0,3,2] row_mask:0xf bank_mask:0xf\n\t"
        "s_nop 1\n\t"
        "v_min_u32_dpp v103, v103, v103 quad_perm:[2,3,0,1] row_mask:0xf bank_mask:0xf\n\t"
        "s_nop 1\n\t"
        "v_min_u32_dpp v103, v103, v103 row_ror:4 row_mask:0xf bank_mask:0xf\n\t"
        "s_nop 0\n\t"
        "v_readfirstlane_b32 %[pg], v103\n\t"
        "s_cmp_gt_u32 %[pg], %[t1]\n\t"
        "s_cbranch_scc1 LP_have%=\n\t"
        "s_add_u32 %[t0], %[pg], %[re]\n\t"
        "s_cmp_eq_u32 %[t0], 63\n\t"
        "s_cbranch_scc0 LP_no%=\n\t"
        "s_sleep 16\n\t"
        "s_branch LP_have%=\n\t"
        "LP_no%=:\n\t"
        "s_min_u32 %[t1], %[t1], 62\n\t"
        "s_cmp_ge_u32 %[pg], %[t1]\n\t"
        "s_cbranch_scc1 LP_near%=\n\t"
        "s_sleep 24\n\t"
        "LP_near%=:\n\t"
        "s_sleep 2\n\t"
        "s_add_u32 %[sp], %[sp], 1\n\t"
        "s_cmp_lt_u32 %[sp], 0x100000\n\t"
        "s_cbranch_scc1 LP_poll%=\n\t"
        "v_mov_b32_e32 v104, 128\n\t"
        "s_mov_b32 %[t0], 33024\n\t"
        "buffer_store_dword v104, off, %[sw], %[t0] sc1\n\t"
        "s_endpgm\n\t"
        "LP_have%=:\n\t"
        "s_lshl_b32 %[nx], %[tl], 17\n\t"
        "s_add_u32 %[t1], %[t1], 4\n\t"
        "s_cmp_gt_u32 %[pg], %[t1]\n\t"
        "s_cbranch_scc1 LP_run%=\n\t"
        "s_add_u32 %[t0], %[ro], 0\n\t"
        "s_and_b32 %[t0], %[t0], 7\n\t"
        "s_lshl_b32 %[t0], %[t0], 12\n\t"
        "s_add_u32 %[t0], %[t0], %[nx]\n\t"
        "buffer_load_dwordx4 v[24:27], v117, %[sw], %[t0] offen offset:0 sc1\n\t"
        "buffer_load_dwordx4 v[28:31], v117, %[sw], %[t0] offen offset:1024 sc1\n\t"
        "buffer_load_dwordx4 v[32:35], v117, %[sw], %[t0] offen offset:2048 sc1\n\t"
        "buffer_load_dwordx4 v[36:39], v117, %[sw], %[t0] offen offset:3072 sc1\n\t"
        "s_add_u32 %[t0], %[ro], 1\n\t"
        "s_and_b32 %[t0], %[t0], 7\n\t"
        "s_lshl_b32 %[t0], %[t0], 12\n\t"
        "s_add_u32 %[t0], %[t0], %[nx]\n\t"
        "buffer_load_dwordx4 v[40:43], v117, %[sw], %[t0] offen offset:0 sc1\n\t"
        "buffer_load_dwordx4 v[44:47], v117, %[sw], %[t0] offen offset:1024 sc1\n\t"
        "s_waitcnt vmcnt(5) lgkmcnt(2)\n\t"
        "v_mfma_f32_16x16x32_bf16 v[20:23], v[88:91], v[24:27], 0\n\t"
        "ds_read_b128 v[88:91], v116 offset:3072\n\t"
        "v_mfma_f32_16x16x32_bf16 v[8:11], a[0:3], v[24:27], 0\n\t"
        "v_max3_u32 v125, v24, v25, v26\n\t"
        "buffer_load_dwordx4 v[48:51], v117, %[sw], %[t0] offen offset:2048 sc1\n\t"
        "v_mfma_f32_16x16x32_bf16 v[12:15], a[4:7], v[24:27], 0\n\t"
        "v_max_u32_e32 v125, v27, v125\n\t"
        "v_mfma_f32_16x16x32_bf16 v[16:19], a[8:11], v[24:27], 0\n\t"
        "buffer_load_dwordx4 v[52:55], v117, %[sw], %[t0] offen offset:3072 sc1\n\t"
        "s_waitcnt vmcnt(6) lgkmcnt(2)\n\t"
        "v_mfma_f32_16x16x32_bf16 v[20:23], v[92:95], v[28:31], v[20:23]\n\t"
        "ds_read_b128 v[92:95], v116 offset:4096\n\t"
        "v_mfma_f32_16x16x32_bf16 v[8:11], a[12:15], v[28:31], v[8:11]\n\t"
        "v_max3_u32 v125, v28, v29, v125\n\t"
        "s_add_u32 %[t0], %[ro], 2\n\t"
        "s_and_b32 %[t0], %[t0], 7\n\t"
        "s_lshl_b32 %[t0], %[t0], 12\n\t"
        "s_add_u32 %[t0], %[t0], %[nx]\n\t"
        "buffer_load_dwordx4 v[56:59], v117, %[sw], %[t0] offen offset:0 sc1\n\t"
        "v_mfma_f32_16x16x32_bf16 v[12:15], a[16:19], v[28:31], v[12:15]\n\t"
        "v_max3_u32 v125, v30, v31, v125\n\t"
        "v_mfma_f32_16x16x32_bf16 v[16:19], a[20:23], v[28:31], v[16:19]\n\t"
        "buffer_load_dwordx4 v[60:63], v117, %[sw], %[t0] offen offset:1024 sc1\n\t"
        "s_and_b32 %[sp], %[tl], 1\n\t"
        "s_lshl_b32 %[sp], %[sp], 14\n\t"
        "s_lshr_b32 %[t1], %[tl], 1\n\t"
        "s_lshl_b32 %[t1], %[t1], 6\n\t"
        "s_add_u32 %[t1], %[sp], %[t1]\n\t"
        "buffer_load_dword v100, v119, %[sw], %[t1] offen sc1\n\t"
        "s_add_u32 %[t1], %[t1], 32768\n\t"
        "buffer_load_dword v101, v119, %[sw], %[t1] offen sc1\n\t"
        "s_lshl_b32 %[t1], %[tl], 9\n\t"
        "buffer_load_dword v102, v120, %[sw], %[t1] offen sc1\n\t"
        "buffer_load_dword v103, v118, %[sw], 0 offen sc1\n\t"
        "s_waitcnt vmcnt(11) lgkmcnt(2)\n\t"
        "v_mfma_f32_16x16x32_bf16 v[20:23], v[96:99], v[32:35], v[20:23]\n\t"
        "ds_read_b128 v[96:99], v116 offset:5120\n\t"
        "v_mfma_f32_16x16x32_bf16 v[8:11], a[24:27], v[32:35], v[8:11]\n\t"
        "v_max3_u32 v125, v32, v33, v125\n\t"
        "buffer_load_dwordx4 v[64:67], v117, %[sw], %[t0] offen offset:2048 sc1\n\t"
        "v_mfma_f32_16x16x32_bf16 v[12:15], a[28:31], v[32:35], v[12:15]\n\t"
        "v_max3_u32 v125, v34, v35, v125\n\t"
        "v_mfma_f32_16x16x32_bf16 v[16:19], a[32:35], v[32:35], v[16:19]\n\t"
        "buffer_load_dwordx4 v[68:71], v117, %[sw], %[t0] offen offset:3072 sc1\n\t"
        "s_waitcnt vmcnt(12) lgkmcnt(2)\n\t"
        "v_mfma_f32_16x16x32_bf16 v[20:23], v[88:91], v[36:39], v[20:23]\n\t"
        "ds_read_b128 v[88:91], v116 offset:6144\n\t"
        "v_mfma_f32_16x16x32_bf16 v[8:11], a[36:39], v[36:39], v[8:11]\n\t"
        "v_max3_u32 v125, v36, v37, v125\n\t"
        "s_add_u32 %[t0], %[ro], 3\n\t"
        "s_and_b32 %[t0], %[t0], 7\n\t"
        "s_lshl_b32 %[t0], %[t0], 12\n\t"
        "s_add_u32 %[t0], %[t0], %[nx]\n\t"
        "buffer_load_dwordx4 v[72:75], v117, %[sw], %[t0] offen offset:0 sc1\n\t"
        "v_mfma_f32_16x16x32_bf16 v[12:15], a[40:43], v[36:39], v[12:15]\n\t"
        "v_max3_u32 v125, v38, v39, v125\n\t"
        "v_mfma_f32_16x16x32_bf16 v[16:19], a[44:47], v[36:39], v[16:19]\n\t"
        "buffer_load_dwordx4 v[76:79], v117, %[sw], %[t0] offen offset:1024 sc1\n\t"
        "s_waitcnt vmcnt(13) lgkmcnt(2)\n\t"
        "v_mfma_f32_16x16x32_bf16 v[20:23], v[92:95], v[40:43], v[20:23]\n\t"
        "ds_read_b128 v[92:95], v116 offset:7168\n\t"
        "v_mfma_f32_16x16x32_bf16 v[8:11], a[48:51], v[40:43], v[8:11]\n\t"
        "v_max3_u32 v125, v40, v41, v125\n\t"
        "buffer_load_dwordx4 v[80:83], v117, %[sw], %[t0] offen offset:2048 sc1\n\t"
        "v_mfma_f32_16x16x32_bf16 v[12:15], a[52:55], v[40:43], v[12:15]\n\t"
        "v_max3_u32 v125, v42, v43, v125\n\t"
        "v_mfma_f32_16x16x32_bf16 v[16:19], a[56:59], v[40:43], v[16:19]\n\t"
        "buffer_load_dwordx4 v[84:87], v117, %[sw], %[t0] offen offset:3072 sc1\n\t"
        "s_waitcnt vmcnt(14) lgkmcnt(2)\n\t"
        "v_mfma_f32_16x16x32_bf16 v[20:23], v[96:99], v[44:47], v[20:23]\n\t"
        "ds_read_b128 v[96:99], v116 offset:8192\n\t"
        "v_mfma_f32_16x16x32_bf16 v[8:11], a[60:63], v[44:47], v[8:11]\n\t"
        "v_max3_u32 v125, v44, v45, v125\n\t"
        "s_add_u32 %[t0], %[ro], 4\n\t"
        "s_and_b32 %[t0], %[t0], 7\n\t"
        "s_lshl_b32 %[t0], %[t0], 12\n\t"
        "s_add_u32 %[t0], %[t0], %[nx]\n\t"
        "buffer_load_dwordx4 v[24:27], v117, %[sw], %[t0] offen offset:0 sc1\n\t"
        "v_mfma_f32_16x16x32_bf16 v[12:15], a[64:67], v[44:47], v[12:15]\n\t"
        "v_max3_u32 v125, v46, v47, v125\n\t"
        "v_mfma_f32_16x16x32_bf16 v[16:19], a[68:71], v[44:47], v[16:19]\n\t"
        "buffer_load_dwordx4 v[28:31], v117, %[sw], %[t0] offen offset:1024 sc1\n\t"
        "s_waitcnt vmcnt(15) lgkmcnt(2)\n\t"
        "v_mfma_f32_16x16x32_bf16 v[20:23], v[88:91], v[48:51], v[20:23]\n\t"
        "ds_read_b128 v[88:91], v116 offset:9216\n\t"
        "v_mfma_f32_16x16x32_bf16 v[8:11], a[72:75], v[48:51], v[8:11]\n\t"
        "v_max3_u32 v125, v48, v49, v125\n\t"
        "v_mfma_f32_16x16x32_bf16 v[12:15], a[76:79], v[48:51], v[12:15]\n\t"
        "v_max3_u32 v125, v50, v51, v125\n\t"
        "v_mfma_f32_16x16x32_bf16 v[16:19], a[80:83], v[48:51], v[16:19]\n\t"
        "buffer_load_dwordx4 v[32:35], v117, %[sw], %[t0] offen offset:2048 sc1\n\t"
        "s_waitcnt vmcnt(15) lgkmcnt(2)\n\t"
        "v_mfma_f32_16x16x32_bf16 v[20:23], v[92:95], v[52:55], v[20:23]\n\t"
        "ds_read_b128 v[92:95], v116 offset:10240\n\t"
        "v_mfma_f32_16x16x32_bf16 v[8:11], a[84:87], v[52:55], v[8:11]\n\t"
        "v_max3_u32 v125, v52, v53, v125\n\t"
        "v_mfma_f32_16x16x32_bf16 v[12:15], a[88:91], v[52:55], v[12:15]\n\t"
        "v_max3_u32 v125, v54, v55, v125\n\t"
        "v_mfma_f32_16x16x32_bf16 v[16:19], a[92:95], v[52:55], v[16:19]\n\t"
        "buffer_load_dwordx4 v[36:39], v117, %[sw], %[t0] offen offset:3072 sc1\n\t"
        "s_waitcnt vmcnt(15) lgkmcnt(2)\n\t"
        "s_cmp_lg_u32 %[re], 0\n\t"
        "s_cbranch_scc1 LP_nb%=\n\t"
        "s_barrier\n\t"
        "LP_nb%=:\n\t"
        "v_mfma_f32_16x16x32_bf16 v[20:23], v[96:99], v[56:59], v[20:23]\n\t"
        "ds_read_b128 v[96:99], v116 offset:11264\n\t"
        "v_mfma_f32_16x16x32_bf16 v[8:11], a[96:99], v[56:59], v[8:11]\n\t"
        "v_max3_u32 v125, v56, v57, v125\n\t"
        "v_mfma_f32_16x16x32_bf16 v[12:15], a[100:103], v[56:59], v[12:15]\n\t"
        "v_max3_u32 v125, v58, v59, v125\n\t"
        "v_mfma_f32_16x16x32_bf16 v[16:19], a[104:107], v[56:59], v[16:19]\n\t"
        "s_add_u32 %[t0], %[ro], 5\n\t"
        "s_and_b32 %[t0], %[t0], 7\n\t"
        "s_lshl_b32 %[t0], %[t0], 12\n\t"
        "s_add_u32 %[t0], %[t0], %[nx]\n\t"
        "buffer_load_dwordx4 v[40:43], v117, %[sw], %[t0] offen offset:0 sc1\n\t"
        "s_waitcnt vmcnt(15) lgkmcnt(2)\n\t"
        "v_mfma_f32_16x16x32_bf16 v[20:23], v[88:91], v[60:63], v[20:23]\n\t"
        "ds_read_b128 v[88:91], v116 offset:12288\n\t"
        "v_mfma_f32_16x16x32_bf16 v[8:11], a[108:111], v[60:63], v[8:11]\n\t"
        "v_max3_u32 v125, v60, v61, v125\n\t"
        "v_mfma_f32_16x16x32_bf16 v[12:15], a[112:115], v[60:63], v[12:15]\n\t"
        "v_max3_u32 v125, v62, v63, v125\n\t"
        "v_mfma_f32_16x16x32_bf16 v[16:19], a[116:119], v[60:63], v[16:19]\n\t"
        "buffer_load_dwordx4 v[44:47], v117, %[sw], %[t0] offen offset:1024 sc1\n\t"
        "s_waitcnt vmcnt(11) lgkmcnt(2)\n\t"
        "v_mfma_f32_16x16x32_bf16 v[20:23], v[92:95], v[64:67], v[20:23]\n\t"
        "ds_read_b128 v[92:95], v116 offset:13312\n\t"
        "v_mfma_f32_16x16x32_bf16 v[8:11], a[120:123], v[64:67], v[8:11]\n\t"
        "v_max3_u32 v125, v64, v65, v125\n\t"
        "v_mfma_f32_16x16x32_bf16 v[12:15], a[124:127], v[64:67], v[12:15]\n\t"
        "v_max3_u32 v125, v66, v67, v125\n\t"
        "v_mfma_f32_16x16x32_bf16 v[16:19], a[128:131], v[64:67], v[16:19]\n\t"
        "buffer_load_dwordx4 v[48:51], v117, %[sw], %[t0] offen offset:2048 sc1\n\t"
        "s_waitcnt vmcnt(11) lgkmcnt(2)\n\t"
        "v_mfma_f32_16x16x32_bf16 v[20:23], v[96:99], v[68:71], v[20:23]\n\t"
        "ds_read_b128 v[96:99], v116 offset:14336\n\t"
        "v_mfma_f32_16x16x32_bf16 v[8:11], a[132:135], v[68:71], v[8:11]\n\t"
        "v_max3_u32 v125, v68, v69, v125\n\t"
        "v_mfma_f32_16x16x32_bf16 v[12:15], a[136:139], v[68:71], v[12:15]\n\t"
        "v_max3_u32 v125, v70, v71, v125\n\t"
        "v_mfma_f32_16x16x32_bf16 v[16:19], a[140:143], v[68:71], v[16:19]\n\t"
        "buffer_load_dwordx4 v[52:55], v117, %[sw], %[t0] offen offset:3072 sc1\n\t"
        "s_waitcnt vmcnt(11) lgkmcnt(2)\n\t"
        "v_mfma_f32_16x16x32_bf16 v[20:23], v[88:91], v[72:75], v[20:23]\n\t"
        "ds_read_b128 v[88:91], v116 offset:15360\n\t"
        "v_mfma_f32_16x16x32_bf16 v[8:11], a[144:147], v[72:75], v[8:11]\n\t"
        "v_max3_u32 v125, v72, v73, v125\n\t"
        "v_mfma_f32_16x16x32_bf16 v[12:15], a[148:151], v[72:75], v[12:15]\n\t"
        "v_max3_u32 v125, v74, v75, v125\n\t"
        "v_mfma_f32_16x16x32_bf16 v[16:19], a[152:155], v[72:75], v[16:19]\n\t"
        "s_add_u32 %[t0], %[ro], 6\n\t"
        "s_and_b32 %[t0], %[t0], 7\n\t"
        "s_lshl_b32 %[t0], %[t0], 12\n\t"
        "s_add_u32 %[t0], %[t0], %[nx]\n\t"
        "buffer_load_dwordx4 v[56:59], v117, %[sw], %[t0] offen offset:0 sc1\n\t"
        "s_waitcnt vmcnt(11) lgkmcnt(2)\n\t"
        "v_mfma_f32_16x16x32_bf16 v[20:23], v[92:95], v[76:79], v[20:23]\n\t"
        "ds_read_b128 v[92:95], v116 offset:16384\n\t"
        "v_mfma_f32_16x16x32_bf16 v[8:11], a[156:159], v[76:79], v[8:11]\n\t"
        "v_max3_u32 v125, v76, v77, v125\n\t"
        "v_mfma_f32_16x16x32_bf16 v[12:15], a[160:163], v[76:79], v[12:15]\n\t"
        "v_max3_u32 v125, v78, v79, v125\n\t"
        "v_mfma_f32_16x16x32_bf16 v[16:19], a[164:167], v[76:79], v[16:19]\n\t"
        "buffer_load_dwordx4 v[60:63], v117, %[sw], %[t0] offen offset:1024 sc1\n\t"
        "s_waitcnt vmcnt(11) lgkmcnt(2)\n\t"
        "v_mfma_f32_16x16x32_bf16 v[20:23], v[96:99], v[80:83], v[20:23]\n\t"
        "ds_read_b128 v[96:99], v116 offset:17408\n\t"
        "v_mfma_f32_16x16x32_bf16 v[8:11], a[168:171], v[80:83], v[8:11]\n\t"
        "v_max3_u32 v125, v80, v81, v125\n\t"
        "v_mfma_f32_16x16x32_bf16 v[12:15], a[172:175], v[80:83], v[12:15]\n\t"
        "v_max3_u32 v125, v82, v83, v125\n\t"
        "v_mfma_f32_16x16x32_bf16 v[16:19], a[176:179], v[80:83], v[16:19]\n\t"
        "buffer_load_dwordx4 v[64:67], v117, %[sw], %[t0] offen offset:2048 sc1\n\t"
        "s_waitcnt vmcnt(11) lgkmcnt(2)\n\t"
        "v_mfma_f32_16x16x32_bf16 v[20:23], v[88:91], v[84:87], v[20:23]\n\t"
        "ds_read_b128 v[88:91], v116 offset:18432\n\t"
        "v_mfma_f32_16x16x32_bf16 v[8:11], a[180:183], v[84:87], v[8:11]\n\t"
        "v_max3_u32 v125, v84, v85, v125\n\t"
        "v_mfma_f32_16x16x32_bf16 v[12:15], a[184:187], v[84:87], v[12:15]\n\t"
        "v_max3_u32 v125, v86, v87, v125\n\t"
        "v_mfma_f32_16x16x32_bf16 v[16:19], a[188:191], v[84:87], v[16:19]\n\t"
        "buffer_load_dwordx4 v[68:71], v117, %[sw], %[t0] offen offset:3072 sc1\n\t"
        "s_waitcnt vmcnt(11) lgkmcnt(2)\n\t"
        "v_mfma_f32_16x16x32_bf16 v[20:23], v[92:95], v[24:27], v[20:23]\n\t"
        "ds_read_b128 v[92:95], v116 offset:19456\n\t"
        "v_mfma_f32_16x16x32_bf16 v[8:11], a[192:195], v[24:27], v[8:11]\n\t"
        "v_max3_u32 v125, v24, v25, v125\n\t"
        "v_mfma_f32_16x16x32_bf16 v[12:15], a[196:199], v[24:27], v[12:15]\n\t"
        "v_max3_u32 v125, v26, v27, v125\n\t"
        "v_mfma_f32_16x16x32_bf16 v[16:19], a[200:203], v[24:27], v[16:19]\n\t"
        "s_add_u32 %[t0], %[ro], 7\n\t"
        "s_and_b32 %[t0], %[t0], 7\n\t"
        "s_lshl_b32 %[t0], %[t0], 12\n\t"
        "s_add_u32 %[t0], %[t0], %[nx]\n\t"
        "buffer_load_dwordx4 v[72:75], v117, %[sw], %[t0] offen offset:0 sc1\n\t"
        "s_waitcnt vmcnt(11) lgkmcnt(2)\n\t"
        "v_mfma_f32_16x16x32_bf16 v[20:23], v[96:99], v[28:31], v[20:23]\n\t"
        "ds_read_b128 v[96:99], v116 offset:20480\n\t"
        "v_mfma_f32_16x16x32_bf16 v[8:11], a[204:207], v[28:31], v[8:11]\n\t"
        "v_max3_u32 v125, v28, v29, v125\n\t"
        "v_mfma_f32_16x16x32_bf16 v[12:15], a[208:211], v[28:31], v[12:15]\n\t"
        "v_max3_u32 v125, v30, v31, v125\n\t"
        "v_sub_f32_e32 v104, 1.0, v102\n\t"
        "v_mfma_f32_16x16x32_bf16 v[16:19], a[212:215], v[28:31], v[16:19]\n\t"
        "buffer_load_dwordx4 v[76:79], v117, %[sw], %[t0] offen offset:1024 sc1\n\t"
        "s_waitcnt vmcnt(11) lgkmcnt(2)\n\t"
        "v_mfma_f32_16x16x32_bf16 v[20:23], v[88:91], v[32:35], v[20:23]\n\t"
        "ds_read_b128 v[88:91], v116 offset:21504\n\t"
        "v_mfma_f32_16x16x32_bf16 v[8:11], a[216:219], v[32:35], v[8:11]\n\t"
        "v_max3_u32 v125, v32, v33, v125\n\t"
        "v_mfma_f32_16x16x32_bf16 v[12:15], a[220:223], v[32:35], v[12:15]\n\t"
        "v_max3_u32 v125, v34, v35, v125\n\t"
        "v_cvt_pk_bf16_f32 v104, 0, v104\n\t"
        "v_mfma_f32_16x16x32_bf16 v[16:19], a[224:227], v[32:35], v[16:19]\n\t"
        "buffer_load_dwordx4 v[80:83], v117, %[sw], %[t0] offen offset:2048 sc1\n\t"
        "s_waitcnt vmcnt(11) lgkmcnt(2)\n\t"
        "v_mfma_f32_16x16x32_bf16 v[20:23], v[92:95], v[36:39], v[20:23]\n\t"
        "ds_read_b128 v[92:95], v116 offset:22528\n\t"
        "v_mfma_f32_16x16x32_bf16 v[8:11], a[228:231], v[36:39], v[8:11]\n\t"
        "v_max3_u32 v125, v36, v37, v125\n\t"
        "v_mfma_f32_16x16x32_bf16 v[12:15], a[232:235], v[36:39], v[12:15]\n\t"
        "v_max3_u32 v125, v38, v39, v125\n\t"
        "v_lshlrev_b32_e32 v106, 16, v100\n\t"
        "v_mfma_f32_16x16x32_bf16 v[16:19], a[236:239], v[36:39], v[16:19]\n\t"
        "buffer_load_dwordx4 v[84:87], v117, %[sw], %[t0] offen offset:3072 sc1\n\t"
        "s_waitcnt vmcnt(11) lgkmcnt(2)\n\t"
        "v_mfma_f32_16x16x32_bf16 v[20:23], v[96:99], v[40:43], v[20:23]\n\t"
        "ds_read_b128 v[96:99], v116 offset:23552\n\t"
        "v_mfma_f32_16x16x32_bf16 v[8:11], a[240:243], v[40:43], v[8:11]\n\t"
        "v_max3_u32 v125, v40, v41, v125\n\t"
        "v_mfma_f32_16x16x32_bf16 v[12:15], a[244:247], v[40:43], v[12:15]\n\t"
        "v_max3_u32 v125, v42, v43, v125\n\t"
        "v_mul_f32_e32 v106, v102, v106\n\t"
        "v_mfma_f32_16x16x32_bf16 v[16:19], a[248:251], v[40:43], v[16:19]\n\t"
        "s_waitcnt vmcnt(10) lgkmcnt(2)\n\t"
        "v_mfma_f32_16x16x32_bf16 v[20:23], v[88:91], v[44:47], v[20:23]\n\t"
        "ds_read_b128 v[88:91], v116 offset:24576\n\t"
        "v_mfma_f32_16x16x32_bf16 v[8:11], a[252:255], v[44:47], v[8:11]\n\t"
        "v_max3_u32 v125, v44, v45, v125\n\t"
        "v_mfma_f32_16x16x32_bf16 v[12:15], v[128:131], v[44:47], v[12:15]\n\t"
        "v_max3_u32 v125, v46, v47, v125\n\t"
        "v_cvt_pk_bf16_f32 v106, 0, v106\n\t"
        "v_mfma_f32_16x16x32_bf16 v[16:19], v[132:135], v[44:47], v[16:19]\n\t"
        "s_waitcnt vmcnt(9) lgkmcnt(2)\n\t"
        "v_mfma_f32_16x16x32_bf16 v[20:23], v[92:95], v[48:51], v[20:23]\n\t"
        "ds_read_b128 v[92:95], v116 offset:25600\n\t"
        "v_mfma_f32_16x16x32_bf16 v[8:11], v[136:139], v[48:51], v[8:11]\n\t"
        "v_max3_u32 v125, v48, v49, v125\n\t"
        "v_mfma_f32_16x16x32_bf16 v[12:15], v[140:143], v[48:51], v[12:15]\n\t"
        "v_max3_u32 v125, v50, v51, v125\n\t"
        "v_and_b32_e32 v107, 0xffff0000, v100\n\t"
        "v_mfma_f32_16x16x32_bf16 v[16:19], v[144:147], v[48:51], v[16:19]\n\t"
        "s_waitcnt vmcnt(8) lgkmcnt(2)\n\t"
        "v_mfma_f32_16x16x32_bf16 v[20:23], v[96:99], v[52:55], v[20:23]\n\t"
        "ds_read_b128 v[96:99], v116 offset:26624\n\t"
        "v_mfma_f32_16x16x32_bf16 v[8:11], v[148:151], v[52:55], v[8:11]\n\t"
        "v_max3_u32 v125, v52, v53, v125\n\t"
        "v_mfma_f32_16x16x32_bf16 v[12:15], v[152:155], v[52:55], v[12:15]\n\t"
        "v_max3_u32 v125, v54, v55, v125\n\t"
        "v_mul_f32_e32 v107, v102, v107\n\t"
        "v_mfma_f32_16x16x32_bf16 v[16:19], v[156:159], v[52:55], v[16:19]\n\t"
        "s_waitcnt vmcnt(7) lgkmcnt(2)\n\t"
        "v_mfma_f32_16x16x32_bf16 v[20:23], v[88:91], v[56:59], v[20:23]\n\t"
        "ds_read_b128 v[88:91], v116 offset:27648\n\t"
        "v_mfma_f32_16x16x32_bf16 v[8:11], v[160:163], v[56:59], v[8:11]\n\t"
        "v_max3_u32 v125, v56, v57, v125\n\t"
        "v_mfma_f32_16x16x32_bf16 v[12:15], v[164:167], v[56:59], v[12:15]\n\t"
        "v_max3_u32 v125, v58, v59, v125\n\t"
        "v_cvt_pk_bf16_f32 v107, 0, v107\n\t"
        "v_mfma_f32_16x16x32_bf16 v[16:19], v[168:171], v[56:59], v[16:19]\n\t"
        "s_waitcnt vmcnt(6) lgkmcnt(2)\n\t"
        "v_mfma_f32_16x16x32_bf16 v[20:23], v[92:95], v[60:63], v[20:23]\n\t"
        "ds_read_b128 v[92:95], v116 offset:28672\n\t"
        "v_mfma_f32_16x16x32_bf16 v[8:11], v[172:175], v[60:63], v[8:11]\n\t"
        "v_max3_u32 v125, v60, v61, v125\n\t"
        "v_mfma_f32_16x16x32_bf16 v[12:15], v[176:179], v[60:63], v[12:15]\n\t"
        "v_max3_u32 v125, v62, v63, v125\n\t"
        "v_lshlrev_b32_e32 v108, 16, v101\n\t"
        "v_mfma_f32_16x16x32_bf16 v[16:19], v[180:183], v[60:63], v[16:19]\n\t"
        "s_waitcnt vmcnt(5) lgkmcnt(2)\n\t"
        "v_mfma_f32_16x16x32_bf16 v[20:23], v[96:99], v[64:67], v[20:23]\n\t"
        "ds_read_b128 v[96:99], v116 offset:29696\n\t"
        "v_mfma_f32_16x16x32_bf16 v[8:11], v[184:187], v[64:67], v[8:11]\n\t"
        "v_max3_u32 v125, v64, v65, v125\n\t"
        "v_mfma_f32_16x16x32_bf16 v[12:15], v[188:191], v[64:67], v[12:15]\n\t"
        "v_max3_u32 v125, v66, v67, v125\n\t"
        "v_mul_f32_e32 v108, v102, v108\n\t"
        "v_mfma_f32_16x16x32_bf16 v[16:19], v[192:195], v[64:67], v[16:19]\n\t"
        "s_waitcnt vmcnt(4) lgkmcnt(2)\n\t"
        "v_mfma_f32_16x16x32_bf16 v[20:23], v[88:91], v[68:71], v[20:23]\n\t"
        "ds_read_b128 v[88:91], v116 offset:30720\n\t"
        "v_mfma_f32_16x16x32_bf16 v[8:11], v[196:199], v[68:71], v[8:11]\n\t"
        "v_max3_u32 v125, v68, v69, v125\n\t"
        "v_mfma_f32_16x16x32_bf16 v[12:15], v[200:203], v[68:71], v[12:15]\n\t"
        "v_max3_u32 v125, v70, v71, v125\n\t"
        "v_cvt_pk_bf16_f32 v108, 0, v108\n\t"
        "v_mfma_f32_16x16x32_bf16 v[16:19], v[204:207], v[68:71], v[16:19]\n\t"
        "s_waitcnt vmcnt(3) lgkmcnt(2)\n\t"
        "v_mfma_f32_16x16x32_bf16 v[20:23], v[92:95], v[72:75], v[20:23]\n\t"
        "ds_read_b128 v[92:95], v116 offset:31744\n\t"
        "v_mfma_f32_16x16x32_bf16 v[8:11], v[208:211], v[72:75], v[8:11]\n\t"
        "v_max3_u32 v125, v72, v73, v125\n\t"
        "v_mfma_f32_16x16x32_bf16 v[12:15], v[212:215], v[72:75], v[12:15]\n\t"
        "v_max3_u32 v125, v74, v75, v125\n\t"
        "v_and_b32_e32 v109, 0xffff0000, v101\n\t"
        "v_mfma_f32_16x16x32_bf16 v[16:19], v[216:219], v[72:75], v[16:19]\n\t"
        "s_waitcnt vmcnt(2) lgkmcnt(2)\n\t"
        "v_mfma_f32_16x16x32_bf16 v[20:23], v[96:99], v[76:79], v[20:23]\n\t"
        "v_mfma_f32_16x16x32_bf16 v[8:11], v[220:223], v[76:79], v[8:11]\n\t"
        "v_max3_u32 v125, v76, v77, v125\n\t"
        "v_mfma_f32_16x16x32_bf16 v[12:15], v[224:227], v[76:79], v[12:15]\n\t"
        "v_max3_u32 v125, v78, v79, v125\n\t"
        "v_mul_f32_e32 v109, v102, v109\n\t"
        "v_mfma_f32_16x16x32_bf16 v[16:19], v[228:231], v[76:79], v[16:19]\n\t"
        "s_waitcnt vmcnt(1) lgkmcnt(1)\n\t"
        "v_mfma_f32_16x16x32_bf16 v[20:23], v[88:91], v[80:83], v[20:23]\n\t"
        "ds_read_b128 v[88:91], v116 offset:0\n\t"
        "v_mfma_f32_16x16x32_bf16 v[8:11], v[232:235], v[80:83], v[8:11]\n\t"
        "v_max3_u32 v125, v80, v81, v125\n\t"
        "v_mfma_f32_16x16x32_bf16 v[12:15], v[236:239], v[80:83], v[12:15]\n\t"
        "v_max3_u32 v125, v82, v83, v125\n\t"
        "v_cvt_pk_bf16_f32 v109, 0, v109\n\t"
        "v_mfma_f32_16x16x32_bf16 v[16:19], v[240:243], v[80:83], v[16:19]\n\t"
        "s_waitcnt vmcnt(0) lgkmcnt(1)\n\t"
        "v_mfma_f32_16x16x32_bf16 v[20:23], v[92:95], v[84:87], v[20:23]\n\t"
        "ds_read_b128 v[92:95], v116 offset:1024\n\t"
        "v_mfma_f32_16x16x32_bf16 v[8:11], v[244:247], v[84:87], v[8:11]\n\t"
        "ds_read_b128 v[96:99], v116 offset:2048\n\t"
        "v_max3_u32 v125, v84, v85, v125\n\t"
        "v_mfma_f32_16x16x32_bf16 v[12:15], v[248:251], v[84:87], v[12:15]\n\t"
        "v_max3_u32 v125, v86, v87, v125\n\t"
        "v_mfma_f32_16x16x32_bf16 v[16:19], v[252:255], v[84:87], v[16:19]\n\t"
        "v_cmp_eq_u32_e32 vcc, -1, v125\n\t"
        "s_mov_b32 %[nx], 0xf0000000\n\t"
        "s_mov_b32 %[re], 64\n\t"
        "s_cbranch_vccz LP_epi%=\n\t"
        "s_waitcnt lgkmcnt(0)\n\t"
        "s_branch LP_again%=\n\t"
        "LP_run%=:\n\t"
        "s_add_u32 %[t0], %[ro], 0\n\t"
        "s_and_b32 %[t0], %[t0], 7\n\t"
        "s_lshl_b32 %[t0], %[t0], 12\n\t"
        "s_add_u32 %[t0], %[t0], %[nx]\n\t"
        "buffer_load_dwordx4 v[24:27], v117, %[sw], %[t0] offen offset:0 sc1\n\t"
        "buffer_load_dwordx4 v[28:31], v117, %[sw], %[t0] offen offset:1024 sc1\n\t"
        "buffer_load_dwordx4 v[32:35], v117, %[sw], %[t0] offen offset:2048 sc1\n\t"
        "buffer_load_dwordx4 v[36:39], v117, %[sw], %[t0] offen offset:3072 sc1\n\t"
        "s_add_u32 %[t0], %[ro], 1\n\t"
        "s_and_b32 %[t0], %[t0], 7\n\t"
        "s_lshl_b32 %[t0], %[t0], 12\n\t"
        "s_add_u32 %[t0], %[t0], %[nx]\n\t"
        "buffer_load_dwordx4 v[40:43], v117, %[sw], %[t0] offen offset:0 sc1\n\t"
        "buffer_load_dwordx4 v[44:47], v117, %[sw], %[t0] offen offset:1024 sc1\n\t"
        "buffer_load_dwordx4 v[48:51], v117, %[sw], %[t0] offen offset:2048 sc1\n\t"
        "buffer_load_dwordx4 v[52:55], v117, %[sw], %[t0] offen offset:3072 sc1\n\t"
        "s_add_u32 %[t0], %[ro], 2\n\t"
        "s_and_b32 %[t0], %[t0], 7\n\t"
        "s_lshl_b32 %[t0], %[t0], 12\n\t"
        "s_add_u32 %[t0], %[t0], %[nx]\n\t"
        "buffer_load_dwordx4 v[56:59], v117, %[sw], %[t0] offen offset:0 sc1\n\t"
        "buffer_load_dwordx4 v[60:63], v117, %[sw], %[t0] offen offset:1024 sc1\n\t"
        "buffer_load_dwordx4 v[64:67], v117, %[sw], %[t0] offen offset:2048 sc1\n\t"
        "buffer_load_dwordx4 v[68:71], v117, %[sw], %[t0] offen offset:3072 sc1\n\t"
        "s_add_u32 %[t0], %[ro], 3\n\t"
        "s_and_b32 %[t0], %[t0], 7\n\t"
        "s_lshl_b32 %[t0], %[t0], 12\n\t"
        "s_add_u32 %[t0], %[t0], %[nx]\n\t"
        "buffer_load_dwordx4 v[72:75], v117, %[sw], %[t0] offen offset:0 sc1\n\t"
        "buffer_load_dwordx4 v[76:79], v117, %[sw], %[t0] offen offset:1024 sc1\n\t"
        "buffer_load_dwordx4 v[80:83], v117, %[sw], %[t0] offen offset:2048 sc1\n\t"
        "buffer_load_dwordx4 v[84:87], v117, %[sw], %[t0] offen offset:3072 sc1\n\t"
        "buffer_load_dword v103, v118, %[sw], 0 offen sc1\n\t"
        "LP_tile%=:\n\t"
        "s_and_b32 %[sp], %[tl], 1\n\t"
        "s_lshl_b32 %[sp], %[sp], 14\n\t"
        "s_lshr_b32 %[t1], %[tl], 1\n\t"
        "s_lshl_b32 %[t1], %[t1], 6\n\t"
        "s_add_u32 %[t1], %[sp], %[t1]\n\t"
        "buffer_load_dword v100, v119, %[sw], %[t1] offen sc1\n\t"
        "s_add_u32 %[t1], %[t1], 32768\n\t"
        "buffer_load_dword v101, v119, %[sw], %[t1] offen sc1\n\t"
        "s_lshl_b32 %[t1], %[tl], 9\n\t"
        "buffer_load_dword v102, v120, %[sw], %[t1] offen sc1\n\t"
        "buffer_load_dword v103, v118, %[sw], 0 offen sc1\n\t"
        "s_mov_b32 %[cu], %[nx]\n\t"
        "s_waitcnt vmcnt(20) lgkmcnt(2)\n\t"
        "v_mfma_f32_16x16x32_bf16 v[20:23], v[88:91], v[24:27], 0\n\t"
        "ds_read_b128 v[88:91], v116 offset:3072\n\t"
        "v_mfma_f32_16x16x32_bf16 v[8:11], a[0:3], v[24:27], 0\n\t"
        "v_mfma_f32_16x16x32_bf16 v[12:15], a[4:7], v[24:27], 0\n\t"
        "v_mfma_f32_16x16x32_bf16 v[16:19], a[8:11], v[24:27], 0\n\t"
        "s_add_u32 %[t0], %[ro], 4\n\t"
        "s_and_b32 %[t0], %[t0], 7\n\t"
        "s_lshl_b32 %[t0], %[t0], 12\n\t"
        "s_add_u32 %[t0], %[t0], %[cu]\n\t"
        "buffer_load_dwordx4 v[24:27], v117, %[sw], %[t0] offen offset:0 sc1\n\t"
        "s_waitcnt vmcnt(20) lgkmcnt(2)\n\t"
        "v_mfma_f32_16x16x32_bf16 v[20:23], v[92:95], v[28:31], v[20:23]\n\t"
        "ds_read_b128 v[92:95], v116 offset:4096\n\t"
        "v_mfma_f32_16x16x32_bf16 v[8:11], a[12:15], v[28:31], v[8:11]\n\t"
        "v_mfma_f32_16x16x32_bf16 v[12:15], a[16:19], v[28:31], v[12:15]\n\t"
        "v_mfma_f32_16x16x32_bf16 v[16:19], a[20:23], v[28:31], v[16:19]\n\t"
        "buffer_load_dwordx4 v[28:31], v117, %[sw], %[t0] offen offset:1024 sc1\n\t"
        "s_waitcnt vmcnt(20) lgkmcnt(2)\n\t"
        "v_mfma_f32_16x16x32_bf16 v[20:23], v[96:99], v[32:35], v[20:23]\n\t"
        "ds_read_b128 v[96:99], v116 offset:5120\n\t"
        "v_mfma_f32_16x16x32_bf16 v[8:11], a[24:27], v[32:35], v[8:11]\n\t"
        "v_mfma_f32_16x16x32_bf16 v[12:15], a[28:31], v[32:35], v[12:15]\n\t"
        "v_mfma_f32_16x16x32_bf16 v[16:19], a[32:35], v[32:35], v[16:19]\n\t"
        "buffer_load_dwordx4 v[32:35], v117, %[sw], %[t0] offen offset:2048 sc1\n\t"
        "s_waitcnt vmcnt(20) lgkmcnt(2)\n\t"
        "v_mfma_f32_16x16x32_bf16 v[20:23], v[88:91], v[36:39], v[20:23]\n\t"
        "ds_read_b128 v[88:91], v116 offset:6144\n\t"
        "v_mfma_f32_16x16x32_bf16 v[8:11], a[36:39], v[36:39], v[8:11]\n\t"
        "v_mfma_f32_16x16x32_bf16 v[12:15], a[40:43], v[36:39], v[12:15]\n\t"
        "v_mfma_f32_16x16x32_bf16 v[16:19], a[44:47], v[36:39], v[16:19]\n\t"
        "buffer_load_dwordx4 v[36:39], v117, %[sw], %[t0] offen offset:3072 sc1\n\t"
        "s_waitcnt vmcnt(20) lgkmcnt(2)\n\t"
        "v_mfma_f32_16x16x32_bf16 v[20:23], v[92:95], v[40:43], v[20:23]\n\t"
        "ds_read_b128 v[92:95], v116 offset:7168\n\t"
        "v_mfma_f32_16x16x32_bf16 v[8:11], a[48:51], v[40:43], v[8:11]\n\t"
        "v_mfma_f32_16x16x32_bf16 v[12:15], a[52:55], v[40:43], v[12:15]\n\t"
        "v_mfma_f32_16x16x32_bf16 v[16:19], a[56:59], v[40:43], v[16:19]\n\t"
        "s_add_u32 %[t0], %[ro], 5\n\t"
        "s_and_b32 %[t0], %[t0], 7\n\t"
        "s_lshl_b32 %[t0], %[t0], 12\n\t"
        "s_add_u32 %[t0], %[t0], %[cu]\n\t"
        "buffer_load_dwordx4 v[40:43], v117, %[sw], %[t0] offen offset:0 sc1\n\t"
        "s_waitcnt vmcnt(20) lgkmcnt(2)\n\t"
        "v_mfma_f32_16x16x32_bf16 v[20:23], v[96:99], v[44:47], v[20:23]\n\t"
        "ds_read_b128 v[96:99], v116 offset:8192\n\t"
        "v_mfma_f32_16x16x32_bf16 v[8:11], a[60:63], v[44:47], v[8:11]\n\t"
        "v_mfma_f32_16x16x32_bf16 v[12:15], a[64:67], v[44:47], v[12:15]\n\t"
        "v_mfma_f32_16x16x32_bf16 v[16:19], a[68:71], v[44:47], v[16:19]\n\t"
        "buffer_load_dwordx4 v[44:47], v117, %[sw], %[t0] offen offset:1024 sc1\n\t"
        "s_waitcnt vmcnt(20) lgkmcnt(2)\n\t"
        "v_mfma_f32_16x16x32_bf16 v[20:23], v[88:91], v[48:51], v[20:23]\n\t"
        "ds_read_b128 v[88:91], v116 offset:9216\n\t"
        "v_mfma_f32_16x16x32_bf16 v[8:11], a[72:75], v[48:51], v[8:11]\n\t"
        "v_mfma_f32_16x16x32_bf16 v[12:15], a[76:79], v[48:51], v[12:15]\n\t"
        "v_mfma_f32_16x16x32_bf16 v[16:19], a[80:83], v[48:51], v[16:19]\n\t"
        "buffer_load_dwordx4 v[48:51], v117, %[sw], %[t0] offen offset:2048 sc1\n\t"
        "s_waitcnt vmcnt(20) lgkmcnt(2)\n\t"
        "v_mfma_f32_16x16x32_bf16 v[20:23], v[92:95], v[52:55], v[20:23]\n\t"
        "ds_read_b128 v[92:95], v116 offset:10240\n\t"
        "v_mfma_f32_16x16x32_bf16 v[8:11], a[84:87], v[52:55], v[8:11]\n\t"
        "v_mfma_f32_16x16x32_bf16 v[12:15], a[88:91], v[52:55], v[12:15]\n\t"
        "v_mfma_f32_16x16x32_bf16 v[16:19], a[92:95], v[52:55], v[16:19]\n\t"
        "buffer_load_dwordx4 v[52:55], v117, %[sw], %[t0] offen offset:3072 sc1\n\t"
        "s_waitcnt vmcnt(20) lgkmcnt(2)\n\t"
        "s_barrier\n\t"
        "v_mfma_f32_16x16x32_bf16 v[20:23], v[96:99], v[56:59], v[20:23]\n\t"
        "ds_read_b128 v[96:99], v116 offset:11264\n\t"
        "v_mfma_f32_16x16x32_bf16 v[8:11], a[96:99], v[56:59], v[8:11]\n\t"
        "v_mfma_f32_16x16x32_bf16 v[12:15], a[100:103], v[56:59], v[12:15]\n\t"
        "v_mfma_f32_16x16x32_bf16 v[16:19], a[104:107], v[56:59], v[16:19]\n\t"
        "s_add_u32 %[t0], %[ro], 6\n\t"
        "s_and_b32 %[t0], %[t0], 7\n\t"
        "s_lshl_b32 %[t0], %[t0], 12\n\t"
        "s_add_u32 %[t0], %[t0], %[cu]\n\t"
        "buffer_load_dwordx4 v[56:59], v117, %[sw], %[t0] offen offset:0 sc1\n\t"
        "s_waitcnt vmcnt(20) lgkmcnt(2)\n\t"
        "v_mfma_f32_16x16x32_bf16 v[20:23], v[88:91], v[60:63], v[20:23]\n\t"
        "ds_read_b128 v[88:91], v116 offset:12288\n\t"
        "v_mfma_f32_16x16x32_bf16 v[8:11], a[108:111], v[60:63], v[8:11]\n\t"
        "v_mfma_f32_16x16x32_bf16 v[12:15], a[112:115], v[60:63], v[12:15]\n\t"
        "v_mfma_f32_16x16x32_bf16 v[16:19], a[116:119], v[60:63], v[16:19]\n\t"
        "buffer_load_dwordx4 v[60:63], v117, %[sw], %[t0] offen offset:1024 sc1\n\t"
        "s_waitcnt vmcnt(20) lgkmcnt(2)\n\t"
        "v_mfma_f32_16x16x32_bf16 v[20:23], v[92:95], v[64:67], v[20:23]\n\t"
        "ds_read_b128 v[92:95], v116 offset:13312\n\t"
        "v_mfma_f32_16x16x32_bf16 v[8:11], a[120:123], v[64:67], v[8:11]\n\t"
        "v_mfma_f32_16x16x32_bf16 v[12:15], a[124:127], v[64:67], v[12:15]\n\t"
        "v_mfma_f32_16x16x32_bf16 v[16:19], a[128:131], v[64:67], v[16:19]\n\t"
        "buffer_load_dwordx4 v[64:67], v117, %[sw], %[t0] offen offset:2048 sc1\n\t"
        "s_waitcnt vmcnt(20) lgkmcnt(2)\n\t"
        "v_mfma_f32_16x16x32_bf16 v[20:23], v[96:99], v[68:71], v[20:23]\n\t"
        "ds_read_b128 v[96:99], v116 offset:14336\n\t"
        "v_mfma_f32_16x16x32_bf16 v[8:11], a[132:135], v[68:71], v[8:11]\n\t"
        "v_mfma_f32_16x16x32_bf16 v[12:15], a[136:139], v[68:71], v[12:15]\n\t"
        "v_mfma_f32_16x16x32_bf16 v[16:19], a[140:143], v[68:71], v[16:19]\n\t"
        "buffer_load_dwordx4 v[68:71], v117, %[sw], %[t0] offen offset:3072 sc1\n\t"
        "s_waitcnt vmcnt(20) lgkmcnt(2)\n\t"
        "v_mfma_f32_16x16x32_bf16 v[20:23], v[88:91], v[72:75], v[20:23]\n\t"
        "ds_read_b128 v[88:91], v116 offset:15360\n\t"
        "v_mfma_f32_16x16x32_bf16 v[8:11], a[144:147], v[72:75], v[8:11]\n\t"
        "v_mfma_f32_16x16x32_bf16 v[12:15], a[148:151], v[72:75], v[12:15]\n\t"
        "v_mfma_f32_16x16x32_bf16 v[16:19], a[152:155], v[72:75], v[16:19]\n\t"
        "s_add_u32 %[t0], %[ro], 7\n\t"
        "s_and_b32 %[t0], %[t0], 7\n\t"
        "s_lshl_b32 %[t0], %[t0], 12\n\t"
        "s_add_u32 %[t0], %[t0], %[cu]\n\t"
        "buffer_load_dwordx4 v[72:75], v117, %[sw], %[t0] offen offset:0 sc1\n\t"
        "s_waitcnt vmcnt(13) lgkmcnt(2)\n\t"
        "v_mfma_f32_16x16x32_bf16 v[20:23], v[92:95], v[76:79], v[20:23]\n\t"
        "ds_read_b128 v[92:95], v116 offset:16384\n\t"
        "v_min_u32_dpp v103, v103, v103 quad_perm:[1,0,3,2] row_mask:0xf bank_mask:0xf\n\t"
        "s_nop 0\n\t"
        "v_mfma_f32_16x16x32_bf16 v[8:11], a[156:159], v[76:79], v[8:11]\n\t"
        "v_min_u32_dpp v103, v103, v103 quad_perm:[2,3,0,1] row_mask:0xf bank_mask:0xf\n\t"
        "s_nop 0\n\t"
        "v_mfma_f32_16x16x32_bf16 v[12:15], a[160:163], v[76:79], v[12:15]\n\t"
        "v_min_u32_dpp v103, v103, v103 row_ror:4 row_mask:0xf bank_mask:0xf\n\t"
        "s_nop 0\n\t"
        "v_mfma_f32_16x16x32_bf16 v[16:19], a[164:167], v[76:79], v[16:19]\n\t"
        "buffer_load_dwordx4 v[76:79], v117, %[sw], %[t0] offen offset:1024 sc1\n\t"
        "s_waitcnt vmcnt(20) lgkmcnt(2)\n\t"
        "v_mfma_f32_16x16x32_bf16 v[20:23], v[96:99], v[80:83], v[20:23]\n\t"
        "ds_read_b128 v[96:99], v116 offset:17408\n\t"
        "v_readfirstlane_b32 %[pg], v103\n\t"
        "s_add_u32 %[t1], %[tl], 8\n\t"
        "s_lshl_b32 %[nx], %[t1], 17\n\t"
        "s_lshr_b32 %[t1], %[t1], 1\n\t"
        "v_mfma_f32_16x16x32_bf16 v[8:11], a[168:171], v[80:83], v[8:11]\n\t"
        "s_cmp_gt_u32 %[pg], %[t1]\n\t"
        "s_cselect_b32 %[nx], %[nx], 0xf0000000\n\t"
        "v_mfma_f32_16x16x32_bf16 v[12:15], a[172:175], v[80:83], v[12:15]\n\t"
        "v_mfma_f32_16x16x32_bf16 v[16:19], a[176:179], v[80:83], v[16:19]\n\t"
        "buffer_load_dwordx4 v[80:83], v117, %[sw], %[t0] offen offset:2048 sc1\n\t"
        "s_waitcnt vmcnt(20) lgkmcnt(2)\n\t"
        "v_mfma_f32_16x16x32_bf16 v[20:23], v[88:91], v[84:87], v[20:23]\n\t"
        "ds_read_b128 v[88:91], v116 offset:18432\n\t"
        "v_mfma_f32_16x16x32_bf16 v[8:11], a[180:183], v[84:87], v[8:11]\n\t"
        "v_mfma_f32_16x16x32_bf16 v[12:15], a[184:187], v[84:87], v[12:15]\n\t"
        "v_mfma_f32_16x16x32_bf16 v[16:19], a[188:191], v[84:87], v[16:19]\n\t"
        "buffer_load_dwordx4 v[84:87], v117, %[sw], %[t0] offen offset:3072 sc1\n\t"
        "s_waitcnt vmcnt(15) lgkmcnt(2)\n\t"
        "v_mfma_f32_16x16x32_bf16 v[20:23], v[92:95], v[24:27], v[20:23]\n\t"
        "ds_read_b128 v[92:95], v116 offset:19456\n\t"
        "v_mfma_f32_16x16x32_bf16 v[8:11], a[192:195], v[24:27], v[8:11]\n\t"
        "v_mfma_f32_16x16x32_bf16 v[12:15], a[196:199], v[24:27], v[12:15]\n\t"
        "v_mfma_f32_16x16x32_bf16 v[16:19], a[200:203], v[24:27], v[16:19]\n\t"
        "s_add_u32 %[t0], %[ro], 0\n\t"
        "s_and_b32 %[t0], %[t0], 7\n\t"
        "s_lshl_b32 %[t0], %[t0], 12\n\t"
        "s_add_u32 %[t0], %[t0], %[nx]\n\t"
        "buffer_load_dwordx4 v[24:27], v117, %[sw], %[t0] offen offset:0 sc1\n\t"
        "s_waitcnt vmcnt(15) lgkmcnt(2)\n\t"
        "v_mfma_f32_16x16x32_bf16 v[20:23], v[96:99], v[28:31], v[20:23]\n\t"
        "ds_read_b128 v[96:99], v116 offset:20480\n\t"
        "v_mfma_f32_16x16x32_bf16 v[8:11], a[204:207], v[28:31], v[8:11]\n\t"
        "v_mfma_f32_16x16x32_bf16 v[12:15], a[208:211], v[28:31], v[12:15]\n\t"
        "v_sub_f32_e32 v104, 1.0, v102\n\t"
        "v_mfma_f32_16x16x32_bf16 v[16:19], a[212:215], v[28:31], v[16:19]\n\t"
        "buffer_load_dwordx4 v[28:31], v117, %[sw], %[t0] offen offset:1024 sc1\n\t"
        "s_waitcnt vmcnt(15) lgkmcnt(2)\n\t"
        "v_mfma_f32_16x16x32_bf16 v[20:23], v[88:91], v[32:35], v[20:23]\n\t"
        "ds_read_b128 v[88:91], v116 offset:21504\n\t"
        "v_mfma_f32_16x16x32_bf16 v[8:11], a[216:219], v[32:35], v[8:11]\n\t"
        "v_mfma_f32_16x16x32_bf16 v[12:15], a[220:223], v[32:35], v[12:15]\n\t"
        "v_cvt_pk_bf16_f32 v104, 0, v104\n\t"
        "v_mfma_f32_16x16x32_bf16 v[16:19], a[224:227], v[32:35], v[16:19]\n\t"
        "buffer_load_dwordx4 v[32:35], v117, %[sw], %[t0] offen offset:2048 sc1\n\t"
        "s_waitcnt vmcnt(15) lgkmcnt(2)\n\t"
        "v_mfma_f32_16x16x32_bf16 v[20:23], v[92:95], v[36:39], v[20:23]\n\t"
        "ds_read_b128 v[92:95], v116 offset:22528\n\t"
        "v_mfma_f32_16x16x32_bf16 v[8:11], a[228:231], v[36:39], v[8:11]\n\t"
        "v_mfma_f32_16x16x32_bf16 v[12:15], a[232:235], v[36:39], v[12:15]\n\t"
        "v_lshlrev_b32_e32 v106, 16, v100\n\t"
        "v_mfma_f32_16x16x32_bf16 v[16:19], a[236:239], v[36:39], v[16:19]\n\t"
        "buffer_load_dwordx4 v[36:39], v117, %[sw], %[t0] offen offset:3072 sc1\n\t"
        "s_waitcnt vmcnt(15) lgkmcnt(2)\n\t"
        "v_mfma_f32_16x16x32_bf16 v[20:23], v[96:99], v[40:43], v[20:23]\n\t"
        "ds_read_b128 v[96:99], v116 offset:23552\n\t"
        "v_mfma_f32_16x16x32_bf16 v[8:11], a[240:243], v[40:43], v[8:11]\n\t"
        "v_mfma_f32_16x16x32_bf16 v[12:15], a[244:247], v[40:43], v[12:15]\n\t"
        "v_mul_f32_e32 v106, v102, v106\n\t"
        "v_mfma_f32_16x16x32_bf16 v[16:19], a[248:251], v[40:43], v[16:19]\n\t"
        "s_add_u32 %[t0], %[ro], 1\n\t"
        "s_and_b32 %[t0], %[t0], 7\n\t"
        "s_lshl_b32 %[t0], %[t0], 12\n\t"
        "s_add_u32 %[t0], %[t0], %[nx]\n\t"
        "buffer_load_dwordx4 v[40:43], v117, %[sw], %[t0] offen offset:0 sc1\n\t"
        "s_waitcnt vmcnt(15) lgkmcnt(2)\n\t"
        "v_mfma_f32_16x16x32_bf16 v[20:23], v[88:91], v[44:47], v[20:23]\n\t"
        "ds_read_b128 v[88:91], v116 offset:24576\n\t"
        "v_mfma_f32_16x16x32_bf16 v[8:11], a[252:255], v[44:47], v[8:11]\n\t"
        "v_mfma_f32_16x16x32_bf16 v[12:15], v[128:131], v[44:47], v[12:15]\n\t"
        "v_cvt_pk_bf16_f32 v106, 0, v106\n\t"
        "v_mfma_f32_16x16x32_bf16 v[16:19], v[132:135], v[44:47], v[16:19]\n\t"
        "buffer_load_dwordx4 v[44:47], v117, %[sw], %[t0] offen offset:1024 sc1\n\t"
        "s_waitcnt vmcnt(15) lgkmcnt(2)\n\t"
        "v_mfma_f32_16x16x32_bf16 v[20:23], v[92:95], v[48:51], v[20:23]\n\t"
        "ds_read_b128 v[92:95], v116 offset:25600\n\t"
        "v_mfma_f32_16x16x32_bf16 v[8:11], v[136:139], v[48:51], v[8:11]\n\t"
        "v_mfma_f32_16x16x32_bf16 v[12:15], v[140:143], v[48:51], v[12:15]\n\t"
        "v_and_b32_e32 v107, 0xffff0000, v100\n\t"
        "v_mfma_f32_16x16x32_bf16 v[16:19], v[144:147], v[48:51], v[16:19]\n\t"
        "buffer_load_dwordx4 v[48:51], v117, %[sw], %[t0] offen offset:2048 sc1\n\t"
        "s_waitcnt vmcnt(15) lgkmcnt(2)\n\t"
        "v_mfma_f32_16x16x32_bf16 v[20:23], v[96:99], v[52:55], v[20:23]\n\t"
        "ds_read_b128 v[96:99], v116 offset:26624\n\t"
        "v_mfma_f32_16x16x32_bf16 v[8:11], v[148:151], v[52:55], v[8:11]\n\t"
        "v_mfma_f32_16x16x32_bf16 v[12:15], v[152:155], v[52:55], v[12:15]\n\t"
        "v_mul_f32_e32 v107, v102, v107\n\t"
        "v_mfma_f32_16x16x32_bf16 v[16:19], v[156:159], v[52:55], v[16:19]\n\t"
        "buffer_load_dwordx4 v[52:55], v117, %[sw], %[t0] offen offset:3072 sc1\n\t"
        "s_waitcnt vmcnt(15) lgkmcnt(2)\n\t"
        "v_mfma_f32_16x16x32_bf16 v[20:23], v[88:91], v[56:59], v[20:23]\n\t"
        "ds_read_b128 v[88:91], v116 offset:27648\n\t"
        "v_mfma_f32_16x16x32_bf16 v[8:11], v[160:163], v[56:59], v[8:11]\n\t"
        "v_mfma_f32_16x16x32_bf16 v[12:15], v[164:167], v[56:59], v[12:15]\n\t"
        "v_cvt_pk_bf16_f32 v107, 0, v107\n\t"
        "v_mfma_f32_16x16x32_bf16 v[16:19], v[168:171], v[56:59], v[16:19]\n\t"
        "s_add_u32 %[t0], %[ro], 2\n\t"
        "s_and_b32 %[t0], %[t0], 7\n\t"
        "s_lshl_b32 %[t0], %[t0], 12\n\t"
        "s_add_u32 %[t0], %[t0], %[nx]\n\t"
        "buffer_load_dwordx4 v[56:59], v117, %[sw], %[t0] offen offset:0 sc1\n\t"
        "s_waitcnt vmcnt(15) lgkmcnt(2)\n\t"
        "v_mfma_f32_16x16x32_bf16 v[20:23], v[92:95], v[60:63], v[20:23]\n\t"
        "ds_read_b128 v[92:95], v116 offset:28672\n\t"
        "v_mfma_f32_16x16x32_bf16 v[8:11], v[172:175], v[60:63], v[8:11]\n\t"
        "v_mfma_f32_16x16x32_bf16 v[12:15], v[176:179], v[60:63], v[12:15]\n\t"
        "v_lshlrev_b32_e32 v108, 16, v101\n\t"
        "v_mfma_f32_16x16x32_bf16 v[16:19], v[180:183], v[60:63], v[16:19]\n\t"
        "buffer_load_dwordx4 v[60:63], v117, %[sw], %[t0] offen offset:1024 sc1\n\t"
        "s_waitcnt vmcnt(15) lgkmcnt(2)\n\t"
        "v_mfma_f32_16x16x32_bf16 v[20:23], v[96:99], v[64:67], v[20:23]\n\t"
        "ds_read_b128 v[96:99], v116 offset:29696\n\t"
        "v_mfma_f32_16x16x32_bf16 v[8:11], v[184:187], v[64:67], v[8:11]\n\t"
        "v_mfma_f32_16x16x32_bf16 v[12:15], v[188:191], v[64:67], v[12:15]\n\t"
        "v_mul_f32_e32 v108, v102, v108\n\t"
        "v_mfma_f32_16x16x32_bf16 v[16:19], v[192:195], v[64:67], v[16:19]\n\t"
        "buffer_load_dwordx4 v[64:67], v117, %[sw], %[t0] offen offset:2048 sc1\n\t"
        "s_waitcnt vmcnt(15) lgkmcnt(2)\n\t"
        "v_mfma_f32_16x16x32_bf16 v[20:23], v[88:91], v[68:71], v[20:23]\n\t"
        "ds_read_b128 v[88:91], v116 offset:30720\n\t"
        "v_mfma_f32_16x16x32_bf16 v[8:11], v[196:199], v[68:71], v[8:11]\n\t"
        "v_mfma_f32_16x16x32_bf16 v[12:15], v[200:203], v[68:71], v[12:15]\n\t"
        "v_cvt_pk_bf16_f32 v108, 0, v108\n\t"
        "v_mfma_f32_16x16x32_bf16 v[16:19], v[204:207], v[68:71], v[16:19]\n\t"
        "buffer_load_dwordx4 v[68:71], v117, %[sw], %[t0] offen offset:3072 sc1\n\t"
        "s_waitcnt vmcnt(15) lgkmcnt(2)\n\t"
        "v_mfma_f32_16x16x32_bf16 v[20:23], v[92:95], v[72:75], v[20:23]\n\t"
        "ds_read_b128 v[92:95], v116 offset:31744\n\t"
        "v_mfma_f32_16x16x32_bf16 v[8:11], v[208:211], v[72:75], v[8:11]\n\t"
        "v_mfma_f32_16x16x32_bf16 v[12:15], v[212:215], v[72:75], v[12:15]\n\t"
        "v_and_b32_e32 v109, 0xffff0000, v101\n\t"
        "v_mfma_f32_16x16x32_bf16 v[16:19], v[216:219], v[72:75], v[16:19]\n\t"
        "s_add_u32 %[t0], %[ro], 3\n\t"
        "s_and_b32 %[t0], %[t0], 7\n\t"
        "s_lshl_b32 %[t0], %[t0], 12\n\t"
        "s_add_u32 %[t0], %[t0], %[nx]\n\t"
        "buffer_load_dwordx4 v[72:75], v117, %[sw], %[t0] offen offset:0 sc1\n\t"
        "s_waitcnt vmcnt(15) lgkmcnt(2)\n\t"
        "v_mfma_f32_16x16x32_bf16 v[20:23], v[96:99], v[76:79], v[20:23]\n\t"
        "v_mfma_f32_16x16x32_bf16 v[8:11], v[220:223], v[76:79], v[8:11]\n\t"
        "v_mfma_f32_16x16x32_bf16 v[12:15], v[224:227], v[76:79], v[12:15]\n\t"
        "v_mul_f32_e32 v109, v102, v109\n\t"
        "v_mfma_f32_16x16x32_bf16 v[16:19], v[228:231], v[76:79], v[16:19]\n\t"
        "buffer_load_dwordx4 v[76:79], v117, %[sw], %[t0] offen offset:1024 sc1\n\t"
        "s_waitcnt vmcnt(15) lgkmcnt(1)\n\t"
        "v_mfma_f32_16x16x32_bf16 v[20:23], v[88:91], v[80:83], v[20:23]\n\t"
        "ds_read_b128 v[88:91], v116 offset:0\n\t"
        "v_mfma_f32_16x16x32_bf16 v[8:11], v[232:235], v[80:83], v[8:11]\n\t"
        "v_mfma_f32_16x16x32_bf16 v[12:15], v[236:239], v[80:83], v[12:15]\n\t"
        "v_cvt_pk_bf16_f32 v109, 0, v109\n\t"
        "v_mfma_f32_16x16x32_bf16 v[16:19], v[240:243], v[80:83], v[16:19]\n\t"
        "buffer_load_dwordx4 v[80:83], v117, %[sw], %[t0] offen offset:2048 sc1\n\t"
        "s_waitcnt vmcnt(15) lgkmcnt(1)\n\t"
        "v_mfma_f32_16x16x32_bf16 v[20:23], v[92:95], v[84:87], v[20:23]\n\t"
        "ds_read_b128 v[92:95], v116 offset:1024\n\t"
        "v_mfma_f32_16x16x32_bf16 v[8:11], v[244:247], v[84:87], v[8:11]\n\t"
        "ds_read_b128 v[96:99], v116 offset:2048\n\t"
        "v_mfma_f32_16x16x32_bf16 v[12:15], v[248:251], v[84:87], v[12:15]\n\t"
        "v_mfma_f32_16x16x32_bf16 v[16:19], v[252:255], v[84:87], v[16:19]\n\t"
        "buffer_load_dwordx4 v[84:87], v117, %[sw], %[t0] offen offset:3072 sc1\n\t"
        "LP_epi%=:\n\t"
        "s_lshl_b32 %[t0], %[tl], 15\n\t"
        "s_nop 0\n\t"
        "ds_write_b128 v122, v[20:23] offset:192\n\t"
        "s_nop 0\n\t"
        "ds_write_b128 v122, v[8:11] offset:0\n\t"
        "s_nop 0\n\t"
        "ds_write_b128 v122, v[12:15] offset:64\n\t"
        "ds_write_b128 v122, v[16:19] offset:128\n\t"
        "s_waitcnt lgkmcnt(0)\n\t"
        "s_barrier\n\t"
        "ds_read_b128 v[8:11], v123 offset:0\n\t"
        "ds_read_b128 v[12:15], v123 offset:4352\n\t"
        "ds_read_b128 v[16:19], v123 offset:8704\n\t"
        "ds_read_b128 v[20:23], v123 offset:13056\n\t"
        "s_waitcnt lgkmcnt(2)\n\t"
        "v_pk_add_f32 v[8:9], v[8:9], v[12:13]\n\t"
        "v_pk_add_f32 v[10:11], v[10:11], v[14:15]\n\t"
        "s_waitcnt lgkmcnt(0)\n\t"
        "v_pk_add_f32 v[16:17], v[16:17], v[20:21]\n\t"
        "v_pk_add_f32 v[18:19], v[18:19], v[22:23]\n\t"
        "v_pk_add_f32 v[8:9], v[8:9], v[16:17]\n\t"
        "v_pk_add_f32 v[10:11], v[10:11], v[18:19]\n\t"
        "v_cvt_pk_bf16_f32 v8, 0, v8\n\t"
        "v_cvt_pk_bf16_f32 v9, 0, v9\n\t"
        "v_cvt_pk_bf16_f32 v10, 0, v10\n\t"
        "v_cvt_pk_bf16_f32 v11, 0, v11\n\t"
        "v_mul_f32_e32 v8, v104, v8\n\t"
        "v_mul_f32_e32 v9, v104, v9\n\t"
        "v_mul_f32_e32 v10, v104, v10\n\t"
        "v_mul_f32_e32 v11, v104, v11\n\t"
        "v_cvt_pk_bf16_f32 v8, 0, v8\n\t"
        "v_cvt_pk_bf16_f32 v9, 0, v9\n\t"
        "v_cvt_pk_bf16_f32 v10, 0, v10\n\t"
        "v_cvt_pk_bf16_f32 v11, 0, v11\n\t"
        "v_add_f32_e32 v8, v106, v8\n\t"
        "v_add_f32_e32 v9, v107, v9\n\t"
        "v_add_f32_e32 v10, v108, v10\n\t"
        "v_add_f32_e32 v11, v109, v11\n\t"
        "v_cvt_pk_bf16_f32 v104, v8, v9\n\t"
        "v_cvt_pk_bf16_f32 v105, v10, v11\n\t"
        "buffer_store_dwordx2 v[104:105], v121, %[su], %[t0] offen sc1\n\t"
        "s_add_u32 %[tl], %[tl], 8\n\t"
        "s_cmp_ge_u32 %[tl], 128\n\t"
        "s_cbranch_scc1 LP_end%=\n\t"
        "s_cmp_lg_u32 %[nx], 0xf0000000\n\t"
        "s_cbranch_scc1 LP_tile%=\n\t"
        "s_branch LP_wait%=\n\t"
        "LP_end%=:\n\t"
        "s_waitcnt vmcnt(0) lgkmcnt(0)\n\t"
        ".Lk_end_P:\n\t"
        : [t0] "=&s"(t0), [t1] "=&s"(t1), [tl] "+s"(tl), [pg] "=&s"(pg), [cu] "=&s"(cu), [nx] "=&s"(nx), [sp] "=&s"(sp), [re] "=&s"(re)
        : [so] "s"(so), [sw] "s"(sw), [su] "s"(su), [cv] "s"(F_CVY + nt * 128), [ro] "s"((nt + 2 * rp) & 7),
          "{v110}"((w * 1024 + (l >> 3)) * 2048 + nt * 128 + (l & 7) * 16), "{v111}"(stg + ((l * 16) ^ ((l & 48) << 1))), "{v112}"(lr), "{v113}"(lr ^ 32u), "{v114}"(lr ^ 64u), "{v115}"(lr ^ 96u),
          "{v116}"(lb + w * 32768 + l * 16), "{v117}"(W_OX + w * 32768 + l * 16), "{v118}"(F_PROG + (w * 8 + (l & 7)) * 128),
          "{v119}"(W_A + ((nt * 2 + (n >> 3)) * 4 + 2 * (n & 1)) * 32768 + w * 4096 + ((n & 7) >> 1) * 16 + g * 4), "{v120}"(W_GATE + w * 128 + g * 4), "{v121}"((4 * w + g) * 2048 + nt * 128 + n * 8),
          "{v122}"(lb + 131072 + w * 4352 + n * 272 + g * 16), "{v123}"(lb + 131072 + (4 * w + g) * 272 + n * 16)
        : "memory", "scc", "vcc", "v8", "v9", "v10", "v11", "v12", "v13", "v14", "v15", "v16", "v17", "v18", "v19", "v20", "v21", "v22", "v23", "v24", "v25", "v26", "v27", "v28", "v29", "v30", "v31", "v32", "v33", "v34", "v35", "v36", "v37", "v38", "v39", "v40", "v41", "v42", "v43", "v44", "v45", "v46", "v47", "v48", "v49", "v50", "v51", "v52", "v53", "v54", "v55", "v56", "v57", "v58", "v59", "v60", "v61", "v62", "v63", "v64", "v65", "v66", "v67", "v68", "v69", "v70", "v71", "v72", "v73", "v74", "v75", "v76", "v77", "v78", "v79", "v80", "v81", "v82", "v83", "v84", "v85", "v86", "v87", "v88", "v89", "v90", "v91", "v92", "v93", "v94", "v95", "v96", "v97", "v98", "v99", "v100", "v101", "v102", "v103", "v104", "v105", "v106", "v107", "v108", "v109", "v124", "v125", "v126", "v127", "v128", "v129", "v130", "v131", "v132", "v133", "v134", "v135", "v136", "v137", "v138", "v139", "v140", "v141", "v142", "v143", "v144", "v145", "v146", "v147", "v148", "v149", "v150", "v151", "v152", "v153", "v154", "v155", "v156", "v157", "v158", "v159", "v160", "v161", "v162", "v163", "v164", "v165", "v166", "v167", "v168", "v169", "v170", "v171", "v172", "v173", "v174", "v175", "v176", "v177", "v178", "v179", "v180", "v181", "v182", "v183", "v184", "v185", "v186", "v187", "v188", "v189", "v190", "v191", "v192", "v193", "v194", "v195", "v196", "v197", "v198", "v199", "v200", "v201", "v202", "v203", "v204", "v205", "v206", "v207", "v208", "v209", "v210", "v211", "v212", "v213", "v214", "v215", "v216", "v217", "v218", "v219", "v220", "v221", "v222", "v223", "v224", "v225", "v226", "v227", "v228", "v229", "v230", "v231", "v232", "v233", "v234", "v235", "v236", "v237", "v238", "v239", "v240", "v241", "v242", "v243", "v244", "v245", "v246", "v247", "v248", "v249", "v250", "v251", "v252", "v253", "v254", "v255", "a0", "a1", "a2", "a3", "a4", "a5", "a6", "a7", "a8", "a9", "a10", "a11", "a12", "a13", "a14", "a15", "a16", "a17", "a18", "a19", "a20", "a21", "a22", "a23", "a24", "a25", "a26", "a27", "a28", "a29", "a30", "a31", "a32", "a33", "a34", "a35", "a36", "a37", "a38", "a39", "a40", "a41", "a42", "a43", "a44", "a45", "a46", "a47", "a48", "a49", "a50", "a51", "a52", "a53", "a54", "a55", "a56", "a57", "a58", "a59", "a60", "a61", "a62", "a63", "a64", "a65", "a66", "a67", "a68", "a69", "a70", "a71", "a72", "a73", "a74", "a75", "a76", "a77", "a78", "a79", "a80", "a81", "a82", "a83", "a84", "a85", "a86", "a87", "a88", "a89", "a90", "a91", "a92", "a93", "a94", "a95", "a96", "a97", "a98", "a99", "a100", "a101", "a102", "a103", "a104", "a105", "a106", "a107", "a108", "a109", "a110", "a111", "a112", "a113", "a114", "a115", "a116", "a117", "a118", "a119", "a120", "a121", "a122", "a123", "a124", "a125", "a126", "a127", "a128", "a129", "a130", "a131", "a132", "a133", "a134", "a135", "a136", "a137", "a138", "a139", "a140", "a141", "a142", "a143", "a144", "a145", "a146", "a147", "a148", "a149", "a150", "a151", "a152", "a153", "a154", "a155", "a156", "a157", "a158", "a159", "a160", "a161", "a162", "a163", "a164", "a165", "a166", "a167", "a168", "a169", "a170", "a171", "a172", "a173", "a174", "a175", "a176", "a177", "a178", "a179", "a180", "a181", "a182", "a183", "a184", "a185", "a186", "a187", "a188", "a189", "a190", "a191", "a192", "a193", "a194", "a195", "a196", "a197", "a198", "a199", "a200", "a201", "a202", "a203", "a204", "a205", "a206", "a207", "a208", "a209", "a210", "a211", "a212", "a213", "a214", "a215", "a216", "a217", "a218", "a219", "a220", "a221", "a222", "a223", "a224", "a225", "a226", "a227", "a228", "a229", "a230", "a231", "a232", "a233", "a234", "a235", "a236", "a237", "a238", "a239", "a240", "a241", "a242", "a243", "a244", "a245", "a246", "a247", "a248", "a249", "a250", "a251", "a252", "a253", "a254", "a255");
}
__device__ __forceinline__ void bulk_block(const Params& p, rsrc_t r, unsigned char* lds, const int wv, const int j) {
    proj_block(p, lds, wv, (j & 6) * 4 + (j >> 4), ((j >> 2) & 2) | (j & 1));
    const int un = ((j & 7) >> 1) * 32 + (j >> 3) * 2 + (j & 1);
    gate_block(p, r, j, wv);
    conv_prep(p, r, wv, un >> 2, un & 3);
    if (!conv_core(p, r, lds, wv, un)) return;
    if (!wait_word(r, F_CONV, 128u, 4096u)) return;
    out_block(p, lds, wv, ((j & 7) >> 1) + (j & 1) * 4, j >> 3);
}

__device__ __forceinline__ void rec_block(const Params& p, rsrc_t r, unsigned char* lds, const int wv, const int b) {
    const int xq = (b & 7) >> 1, t = (b >> 3) * 2 + (b & 1);
    const unsigned lb = __builtin_amdgcn_readfirstlane((unsigned)(size_t)lds);
    if (t < 8) rec_a(p, lb, wv, xq * 8 + t, xq);
    else if (t < 24) rec_b(p, lb, wv, (xq & 1) * 16 + t - 8, xq >> 1, xq);
    else {
        const int hi = xq * 8 + t - 24;
        rec_helper(p, r, lb, wv, hi);
    }
}

__global__ void __launch_bounds__(256, 1) k_main(Params p) {
    extern __shared__ __attribute__((aligned(16))) unsigned char lds[];
    rsrc_t r = __builtin_amdgcn_make_buffer_rsrc(p.ws, 0, W_TOTAL, 0x00020000);
    const int wv = __builtin_amdgcn_readfirstlane(threadIdx.x >> 6);
    {
        unsigned long long pc; int ea, eb, eg;
        asm volatile("s_getpc_b64 %0\n.Lk_begin:\n\ts_mov_b32 %1, .Lk_end_A-.Lk_begin\n\ts_mov_b32 %2, .Lk_end_B-.Lk_begin\n\ts_mov_b32 %3, .Lk_end_P-.Lk_begin" : "=s"(pc), "=s"(ea), "=s"(eb), "=s"(eg));
        const int o = ((blockIdx.x >> 3) * 64 + (threadIdx.x >> 2)) * 128;
        if ((threadIdx.x & 3) == 0 && o < max(ea, max(eb, eg)) - 4) touch_l2((const unsigned char*)pc + o, __builtin_amdgcn_readfirstlane((unsigned)(size_t)lds) + 1024);
    }
    if (blockIdx.x == 0 && wv == 1) {
        const int l = threadIdx.x & 63; const v4u z = {0u, 0u, 0u, 0u};
#pragma unroll
        for (int i = 0; i < 6; ++i) st16(r, F_CONV + i * 1024 + l * 16, z);
        DRAIN();
        for (unsigned spin = 0; spin < (1u << 20); ++spin) {
            if (l < 8) __hip_atomic_fetch_add((unsigned*)(p.ws + F_BEAT + l * 128), 1u, __ATOMIC_RELAXED, __HIP_MEMORY_SCOPE_AGENT);
            unsigned c = ld4(r, l < 8 ? F_TKT + l * 64 : OOB);
            c += __shfl_xor(c, 1); c += __shfl_xor(c, 2); c += __shfl_xor(c, 4);
            if (__builtin_amdgcn_readfirstlane(c) >= 256u) break; }
    }
    if (threadIdx.x == 0) {
        const unsigned bo = F_BEAT + (blockIdx.x & 7) * 128, b0 = ld4(r, bo);
        for (unsigned spin = 0; spin < (1u << 22); ++spin) { MEMBAR(); if (ld4(r, bo) != b0) break; }
        MEMBAR();
        unsigned xcc; asm volatile("s_getreg_b32 %0, hwreg(HW_REG_XCC_ID)" : "=s"(xcc)); xcc &= 7u;
        unsigned* tk = (unsigned*)(p.ws + F_TKT);
        unsigned t = __hip_atomic_fetch_add(tk + xcc * 16, 1u, __ATOMIC_RELAXED, __HIP_MEMORY_SCOPE_AGENT);
        if (t >= 32u) {
            unsigned o = __hip_atomic_fetch_add(tk + 128, 1u, __ATOMIC_RELAXED, __HIP_MEMORY_SCOPE_AGENT);
            for (unsigned spin = 0; spin < (1u << 22); ++spin) { unsigned tot = 0u;
                for (int x = 0; x < 8; ++x) tot += __hip_atomic_load(tk + x * 16, __ATOMIC_RELAXED, __HIP_MEMORY_SCOPE_AGENT);
                if (tot >= 256u) break; }
            for (int x = 0; x < 8; ++x) { const unsigned cnt = __hip_atomic_load(tk + x * 16, __ATOMIC_RELAXED, __HIP_MEMORY_SCOPE_AGENT), fr = cnt < 32u ? 32u - cnt : 0u;
                if (o < fr) { xcc = (unsigned)x; t = cnt + o; break; }
                o -= fr; }
        }
        *(volatile int*)lds = (int)((xcc >> 2) * 128u + (t >> 1) * 8u + (xcc & 3u) * 2u + (t & 1u));
    }
    DRAIN();
    __syncthreads();
    const int role = __builtin_amdgcn_readfirstlane(*(volatile int*)lds) & 255;
    __syncthreads();
    if (role >= 128) bulk_block(p, r, lds, wv, role - 128); else rec_block(p, r, lds, wv, role);
}

extern "C" void kernel_launch(void* const* d_in, const int* in_sizes, int n_in, void* d_out, int out_size, void* d_ws, size_t ws_size, hipStream_t stream) {
    Params p;
    p.Aq = (const bf16_t*)d_in[0]; p.Ak = (const bf16_t*)d_in[1]; p.Av = (const bf16_t*)d_in[2];
    p.shift_k = (const bf16_t*)d_in[3]; p.ssm_k = (const bf16_t*)d_in[4]; p.ssm_D = (const bf16_t*)d_in[5];
    p.mem0 = (const bf16_t*)d_in[7]; p.Wq = (const bf16_t*)d_in[8]; p.Wk = (const bf16_t*)d_in[9];
    p.Wv = (const bf16_t*)d_in[10]; p.Wo = (const bf16_t*)d_in[11]; p.gate_w = (const bf16_t*)d_in[13];
    p.out = (bf16_t*)d_out; p.ws = (unsigned char*)d_ws;
    (void)hipFuncSetAttribute((const void*)k_main, hipFuncAttributeMaxDynamicSharedMemorySize, L_TOTAL);
    k_main<<<256, 256, L_TOTAL, stream>>>(p);
}
```

```cpp
#include <hip/hip_runtime.h>
#include <stdint.h>
typedef unsigned short bf16_t;
using f32x16 = __attribute__((ext_vector_type(16))) float;
typedef unsigned v4u __attribute__((ext_vector_type(4)));
typedef unsigned v2u __attribute__((ext_vector_type(2)));
typedef __amdgpu_buffer_rsrc_t rsrc_t;
typedef __bf16 hbf2 __attribute__((ext_vector_type(2)));
typedef float f32x2 __attribute__((ext_vector_type(2)));
__device__ __forceinline__ unsigned pack2(float a, float b) { f32x2 v = {a, b}; return __builtin_bit_cast(unsigned, __builtin_convertvector(v, hbf2)); }
__device__ __forceinline__ float rbfh(float f) { return __uint_as_float(pack2(f, 0.f) << 16); }
__device__ __forceinline__ float blo(unsigned x) { return __uint_as_float(x << 16); }
__device__ __forceinline__ float bhi(unsigned x) { return __uint_as_float(x & 0xFFFF0000u); }

constexpr int S = 2048, D = 1024;

constexpr unsigned F_KVQ = 0;
constexpr unsigned F_CONV = 32768;
constexpr unsigned F_ERR = 33024;
constexpr unsigned F_CAL = 33792;
constexpr unsigned F_RDY0 = 34304;
constexpr unsigned F_TKT = 34816;
constexpr unsigned F_GO = 36864;
constexpr unsigned F_BEAT = 40960;
constexpr unsigned F_PROG = 49152;
constexpr unsigned F_CVY = 57344;
constexpr unsigned W_A = 8568832;
constexpr unsigned W_GATE = 65536;
constexpr unsigned W_QX = W_A + S * D * 2 + 8192;
constexpr unsigned W_KX = W_QX + S * 4096 * 2;
constexpr unsigned W_VF = W_KX + S * 4096 * 2;
constexpr unsigned W_OX = W_VF + S * 4096 * 2;
constexpr unsigned W_Y = W_OX + S * 4096 * 2;
constexpr unsigned W_U = 115531776;
constexpr unsigned W_TOTAL = W_U + S * D * 2;
constexpr unsigned OOB = 0xF0000000u;
constexpr int L_TOTAL = 163840;

__device__ __forceinline__ v4u ld16(rsrc_t r, unsigned off) { return __builtin_amdgcn_raw_buffer_load_b128(r, off, 0, 16); }
__device__ __forceinline__ v2u ld8(rsrc_t r, unsigned off) { return __builtin_amdgcn_raw_buffer_load_b64(r, off, 0, 16); }
__device__ __forceinline__ unsigned ld4(rsrc_t r, unsigned off) { return __builtin_amdgcn_raw_buffer_load_b32(r, off, 0, 16); }
__device__ __forceinline__ void st16(rsrc_t r, unsigned off, v4u v) { __builtin_amdgcn_raw_buffer_store_b128(v, r, off, 0, 16); }
__device__ __forceinline__ void st4(rsrc_t r, unsigned off, unsigned v) { __builtin_amdgcn_raw_buffer_store_b32(v, r, off, 0, 16); }
#define D1 128
#define D2 77
#define LT_A 1
#define PEN 48
#define CAL_T 450u
#define XSLP 3
#define T2U 2
#define TPRE 48
#define MEMBAR() asm volatile("" ::: "memory")
#define DRAIN() asm volatile("s_waitcnt vmcnt(0)" ::: "memory")
__device__ __forceinline__ int otid_(int wv) { int t; asm volatile("v_mbcnt_lo_u32_b32 %0, -1, 0\n\tv_mbcnt_hi_u32_b32 %0, -1, %0" : "=v"(t)); return wv * 64 + t; }
#define otid() otid_(wv)
__device__ __forceinline__ bool wait_word(rsrc_t r, unsigned off, unsigned want, unsigned code) {
    for (unsigned spin = 0;; ++spin) { MEMBAR();
        const unsigned v = __builtin_amdgcn_readfirstlane(ld4(r, off));
        if (v >= want) { MEMBAR(); return true; }
        if (spin > (1u << 20)) { st4(r, F_ERR, code); return false; }
        __builtin_amdgcn_s_sleep(8); }
}
__device__ __forceinline__ void touch_l2(const void* q, unsigned ldsdst) { asm volatile("s_mov_b32 m0, %1\n\ts_nop 0\n\tglobal_load_lds_dword %0, off" :: "v"(q), "s"(ldsdst) : "m0", "memory"); }
__device__ __forceinline__ unsigned rtnow() { return (unsigned)__builtin_amdgcn_s_memrealtime(); }

struct Params {
    const bf16_t *Aq, *Ak, *Av, *shift_k, *ssm_k, *ssm_D, *mem0, *Wq, *Wk, *Wv, *Wo, *gate_w;
    bf16_t* out; unsigned char* ws;
};
__device__ __forceinline__ unsigned ho_base(const void* ws) { const size_t a = (size_t)ws; return (unsigned)((((a + W_TOTAL + 0x7FFFFFu) >> 23) << 23) - a); }
__device__ __forceinline__ v4u mk_srd(const void* p, unsigned bytes) { const size_t a = (size_t)p; v4u d;
    d.x = __builtin_amdgcn_readfirstlane((unsigned)a); d.y = __builtin_amdgcn_readfirstlane((unsigned)(a >> 32) & 0xFFFFu); d.z = __builtin_amdgcn_readfirstlane(bytes); d.w = 0x00020000u; return d; }
constexpr unsigned HO_BYTES = 2097152;
constexpr int L_KV = 90112, L_MS = 139264, L_IMG = 155648, IMG_PS = 1056, IMG_MS = 2112, LB_R = 8192, LB_STG = 16384, LB_STGP = 8704;
static_assert(F_KVQ == 0 && F_ERR == 33024 && F_CONV == 32768 && F_CAL == 33792 && F_RDY0 == 34304 && F_GO == 36864 && F_PROG == 49152 && W_QX == 12771328 && W_KX == 29548544 && W_OX == 63102976 && W_VF == 46325760 && L_KV == 90112 && L_MS == 139264 && L_IMG == 155648 && IMG_PS == 1056 && IMG_MS == 2112 && LB_R == 8192 && LB_STG == 16384 && LB_STGP == 8704, "constants of the generated code");
#define REC_S_IN "{s[8:11]}"(sh), "{s[12:15]}"(sw), "{s[16:19]}"(s1), "{s[20:23]}"(s2), "{s29}"(D1), "{s30}"(D2), "{s43}"(w), "{s44}"(id), "{s49}"(rb), "{s50}"(PEN), "{s51}"(LT_A), "{s45}"(F_GO + (cp * 4 + w) * 128)
__device__ __forceinline__ void rec_a(const Params& p, unsigned lb, const int wv, const int id, const int cp) {
    const int w = wv, l = otid() & 63, n = l & 15, g = l >> 4, h = id, rb = id;
    const v4u sh = mk_srd(p.ws + ho_base(p.ws), HO_BYTES - 8192), sw = mk_srd(p.ws, W_TOTAL), s1 = mk_srd(w < 2 ? p.Wq : p.Wk, 1024 * 4096 * 2), s2 = s1;
    const int r8 = l >> 3, pc = l & 7, pp = n >> 3, bb = (n >> 2) & 1, f = (g & 1) | (pp << 1);
    const unsigned stg = lb + 131072 + w * 8192;
    const unsigned g_w = r8 * 8192 + h * 256 + (w & 1) * 128 + pc * 16;
    const unsigned wb0 = stg + r8 * 128 + (((pc >> 1) ^ (r8 >> 2)) << 5) + (pc & 1) * 16, wb1 = wb0 ^ 64u;
    const unsigned rb0 = stg + (16 * (g >> 1) + 8 * pp + 4 * (g & 1) + bb) * 128 + (f << 5) + (n & 3) * 8;
    const unsigned a_mem = cp * 32768 + (w >> 1) * 16384 + (w & 1) * 4096 + l * 16;
    const unsigned l_ms = lb + L_MS + l * 16;
    const unsigned l_iw = lb + L_IMG + (w >> 1) * IMG_MS + (g >> 1) * IMG_PS + (64 * (w & 1) + n) * 8 + (g & 1) * 4;
    const unsigned l_iq = lb + L_IMG + (n & 1) * IMG_PS + (8 * g + (n >> 2)) * 8;
    const unsigned a_vm = (16 + (h >> 2)) * 16384 + (h & 3) * 2048 + w * 512 + (l & 31) * 16;
    const unsigned a_o = n < 8 ? (8 + (n >> 2) * 4 + (h >> 3)) * 16384 + (h & 7) * 1024 + w * 256 + (n & 3) * 64 + g * 16 : OOB;
    const unsigned a_ox = (((w & 1) * 32 + h) * 4 + w) * 1024 + l * 16;
    asm volatile(
        "s_nop 4\n\t"
        "s_lshl_b32 s34, s43, 2\n\t"
        "s_add_u32 s34, s34, 0\n\t"
        "s_and_b32 s34, s34, 15\n\t"
        "s_lshl_b32 s34, s34, 19\n\t"
        "buffer_load_dwordx4 v[224:227], v96, s[16:19], s34 offen nt\n\t"
        "s_add_u32 s34, s34, 0x10000\n\t"
        "buffer_load_dwordx4 v[228:231], v96, s[16:19], s34 offen nt\n\t"
        "s_add_u32 s34, s34, 0x10000\n\t"
        "buffer_load_dwordx4 v[232:235], v96, s[16:19], s34 offen nt\n\t"
        "s_add_u32 s34, s34, 0x10000\n\t"
        "buffer_load_dwordx4 v[236:239], v96, s[16:19], s34 offen nt\n\t"
        "s_add_u32 s34, s34, 0x10000\n\t"
        "buffer_load_dwordx4 v[240:243], v96, s[16:19], s34 offen nt\n\t"
        "s_add_u32 s34, s34, 0x10000\n\t"
        "buffer_load_dwordx4 v[244:247], v96, s[16:19], s34 offen nt\n\t"
        "s_add_u32 s34, s34, 0x10000\n\t"
        "buffer_load_dwordx4 v[248:251], v96, s[16:19], s34 offen nt\n\t"
        "s_add_u32 s34, s34, 0x10000\n\t"
        "buffer_load_dwordx4 v[252:255], v96, s[16:19], s34 offen nt\n\t"
        "s_lshl_b32 s34, s43, 2\n\t"
        "s_add_u32 s34, s34, 1\n\t"
        "s_and_b32 s34, s34, 15\n\t"
        "s_lshl_b32 s34, s34, 19\n\t"
        "buffer_load_dwordx4 v[0:3], v96, s[16:19], s34 offen nt\n\t"
        "s_add_u32 s34, s34, 0x10000\n\t"
        "buffer_load_dwordx4 v[4:7], v96, s[16:19], s34 offen nt\n\t"
        "s_add_u32 s34, s34, 0x10000\n\t"
        "buffer_load_dwordx4 v[8:11], v96, s[16:19], s34 offen nt\n\t"
        "s_add_u32 s34, s34, 0x10000\n\t"
        "buffer_load_dwordx4 v[12:15], v96, s[16:19], s34 offen nt\n\t"
        "s_add_u32 s34, s34, 0x10000\n\t"
        "buffer_load_dwordx4 v[16:19], v96, s[16:19], s34 offen nt\n\t"
        "s_add_u32 s34, s34, 0x10000\n\t"
        "buffer_load_dwordx4 v[20:23], v96, s[16:19], s34 offen nt\n\t"
        "s_add_u32 s34, s34, 0x10000\n\t"
        "buffer_load_dwordx4 v[24:27], v96, s[16:19], s34 offen nt\n\t"
        "s_add_u32 s34, s34, 0x10000\n\t"
        "buffer_load_dwordx4 v[28:31], v96, s[16:19], s34 offen nt\n\t"
        "s_lshl_b32 s34, s43, 2\n\t"
        "s_add_u32 s34, s34, 2\n\t"
        "s_and_b32 s34, s34, 15\n\t"
        "s_lshl_b32 s34, s34, 19\n\t"
        "buffer_load_dwordx4 v[32:35], v96, s[16:19], s34 offen nt\n\t"
        "s_add_u32 s34, s34, 0x10000\n\t"
        "buffer_load_dwordx4 v[36:39], v96, s[16:19], s34 offen nt\n\t"
        "s_add_u32 s34, s34, 0x10000\n\t"
        "buffer_load_dwordx4 v[40:43], v96, s[16:19], s34 offen nt\n\t"
        "s_add_u32 s34, s34, 0x10000\n\t"
        "buffer_load_dwordx4 v[44:47], v96, s[16:19], s34 offen nt\n\t"
        "s_add_u32 s34, s34, 0x10000\n\t"
        "buffer_load_dwordx4 v[48:51], v96, s[16:19], s34 offen nt\n\t"
        "s_add_u32 s34, s34, 0x10000\n\t"
        "buffer_load_dwordx4 v[52:55], v96, s[16:19], s34 offen nt\n\t"
        "s_add_u32 s34, s34, 0x10000\n\t"
        "buffer_load_dwordx4 v[56:59], v96, s[16:19], s34 offen nt\n\t"
        "s_add_u32 s34, s34, 0x10000\n\t"
        "buffer_load_dwordx4 v[60:63], v96, s[16:19], s34 offen nt\n\t"
        "v_cmp_le_i32_e64 s[60:61], 0, v126\n\t"
        "v_cmp_le_i32_e64 s[62:63], 1, v126\n\t"
        "v_cmp_le_i32_e64 s[64:65], 2, v126\n\t"
        "v_cmp_le_i32_e64 s[66:67], 3, v126\n\t"
        "v_cmp_le_i32_e64 s[68:69], 16, v126\n\t"
        "v_cmp_le_i32_e64 s[70:71], 17, v126\n\t"
        "v_cmp_le_i32_e64 s[72:73], 18, v126\n\t"
        "v_cmp_le_i32_e64 s[74:75], 19, v126\n\t"
        "s_mul_i32 s35, s43, 10240\n\t"
        "v_add_u32_e32 v126, s35, v124\n\t"
        "s_waitcnt vmcnt(16)\n\t"
        "ds_write_b128 v97, v[224:227] offset:0\n\t"
        "ds_write_b128 v98, v[228:231] offset:1024\n\t"
        "ds_write_b128 v97, v[232:235] offset:2048\n\t"
        "ds_write_b128 v98, v[236:239] offset:3072\n\t"
        "ds_write_b128 v97, v[240:243] offset:4096\n\t"
        "ds_write_b128 v98, v[244:247] offset:5120\n\t"
        "ds_write_b128 v97, v[248:251] offset:6144\n\t"
        "ds_write_b128 v98, v[252:255] offset:7168\n\t"
        "ds_read_b64_tr_b16 a[0:1], v99 offset:0\n\t"
        "ds_read_b64_tr_b16 a[2:3], v99 offset:256\n\t"
        "ds_read_b64_tr_b16 a[4:5], v99 offset:4096\n\t"
        "ds_read_b64_tr_b16 a[6:7], v99 offset:4352\n\t"
        "s_waitcnt lgkmcnt(8)\n\t"
        "ds_read_b64_tr_b16 a[8:9], v100 offset:0\n\t"
        "ds_read_b64_tr_b16 a[10:11], v100 offset:256\n\t"
        "ds_read_b64_tr_b16 a[12:13], v100 offset:4096\n\t"
        "ds_read_b64_tr_b16 a[14:15], v100 offset:4352\n\t"
        "s_waitcnt lgkmcnt(8)\n\t"
        "ds_read_b64_tr_b16 a[16:17], v101 offset:0\n\t"
        "ds_read_b64_tr_b16 a[18:19], v101 offset:256\n\t"
        "ds_read_b64_tr_b16 a[20:21], v101 offset:4096\n\t"
        "ds_read_b64_tr_b16 a[22:23], v101 offset:4352\n\t"
        "s_waitcnt lgkmcnt(8)\n\t"
        "ds_read_b64_tr_b16 v[104:105], v102 offset:0\n\t"
        "ds_read_b64_tr_b16 v[106:107], v102 offset:256\n\t"
        "ds_read_b64_tr_b16 v[108:109], v102 offset:4096\n\t"
        "ds_read_b64_tr_b16 v[110:111], v102 offset:4352\n\t"
        "s_lshl_b32 s34, s43, 2\n\t"
        "s_add_u32 s34, s34, 3\n\t"
        "s_and_b32 s34, s34, 15\n\t"
        "s_lshl_b32 s34, s34, 19\n\t"
        "buffer_load_dwordx4 v[64:67], v96, s[16:19], s34 offen nt\n\t"
        "s_add_u32 s34, s34, 0x10000\n\t"
        "buffer_load_dwordx4 v[68:71], v96, s[16:19], s34 offen nt\n\t"
        "s_add_u32 s34, s34, 0x10000\n\t"
        "buffer_load_dwordx4 v[72:75], v96, s[16:19], s34 offen nt\n\t"
        "s_add_u32 s34, s34, 0x10000\n\t"
        "buffer_load_dwordx4 v[76:79], v96, s[16:19], s34 offen nt\n\t"
        "s_add_u32 s34, s34, 0x10000\n\t"
        "buffer_load_dwordx4 v[80:83], v96, s[16:19], s34 offen nt\n\t"
        "s_add_u32 s34, s34, 0x10000\n\t"
        "buffer_load_dwordx4 v[84:87], v96, s[16:19], s34 offen nt\n\t"
        "s_add_u32 s34, s34, 0x10000\n\t"
        "buffer_load_dwordx4 v[88:91], v96, s[16:19], s34 offen nt\n\t"
        "s_add_u32 s34, s34, 0x10000\n\t"
        "buffer_load_dwordx4 v[92:95], v96, s[16:19], s34 offen nt\n\t"
        "s_waitcnt lgkmcnt(0)\n\t"
        "ds_write_b128 v117, v[104:107] offset:0\n\t"
        "ds_write_b128 v117, v[108:111] offset:1024\n\t"
        "s_waitcnt vmcnt(16)\n\t"
        "ds_write_b128 v97, v[0:3] offset:0\n\t"
        "ds_write_b128 v98, v[4:7] offset:1024\n\t"
        "ds_write_b128 v97, v[8:11] offset:2048\n\t"
        "ds_write_b128 v98, v[12:15] offset:3072\n\t"
        "ds_write_b128 v97, v[16:19] offset:4096\n\t"
        "ds_write_b128 v98, v[20:23] offset:5120\n\t"
        "ds_write_b128 v97, v[24:27] offset:6144\n\t"
        "ds_write_b128 v98, v[28:31] offset:7168\n\t"
        "ds_read_b64_tr_b16 a[24:25], v99 offset:0\n\t"
        "ds_read_b64_tr_b16 a[26:27], v99 offset:256\n\t"
        "ds_read_b64_tr_b16 a[28:29], v99 offset:4096\n\t"
        "ds_read_b64_tr_b16 a[30:31], v99 offset:4352\n\t"
        "s_waitcnt lgkmcnt(8)\n\t"
        "ds_read_b64_tr_b16 a[32:33], v100 offset:0\n\t"
        "ds_read_b64_tr_b16 a[34:35], v100 offset:256\n\t"
        "ds_read_b64_tr_b16 a[36:37], v100 offset:4096\n\t"
        "ds_read_b64_tr_b16 a[38:39], v100 offset:4352\n\t"
        "s_waitcnt lgkmcnt(8)\n\t"
        "ds_read_b64_tr_b16 a[40:41], v101 offset:0\n\t"
        "ds_read_b64_tr_b16 a[42:43], v101 offset:256\n\t"
        "ds_read_b64_tr_b16 a[44:45], v101 offset:4096\n\t"
        "ds_read_b64_tr_b16 a[46:47], v101 offset:4352\n\t"
        "s_waitcnt lgkmcnt(8)\n\t"
        "ds_read_b64_tr_b16 v[104:105], v102 offset:0\n\t"
        "ds_read_b64_tr_b16 v[106:107], v102 offset:256\n\t"
        "ds_read_b64_tr_b16 v[108:109], v102 offset:4096\n\t"
        "ds_read_b64_tr_b16 v[110:111], v102 offset:4352\n\t"
        "s_lshl_b32 s34, s43, 2\n\t"
        "s_add_u32 s34, s34, 4\n\t"
        "s_and_b32 s34, s34, 15\n\t"
        "s_lshl_b32 s34, s34, 19\n\t"
        "buffer_load_dwordx4 a[96:99], v96, s[16:19], s34 offen nt\n\t"
        "s_add_u32 s34, s34, 0x10000\n\t"
        "buffer_load_dwordx4 a[100:103], v96, s[16:19], s34 offen nt\n\t"
        "s_add_u32 s34, s34, 0x10000\n\t"
        "buffer_load_dwordx4 a[104:107], v96, s[16:19], s34 offen nt\n\t"
        "s_add_u32 s34, s34, 0x10000\n\t"
        "buffer_load_dwordx4 a[108:111], v96, s[16:19], s34 offen nt\n\t"
        "s_add_u32 s34, s34, 0x10000\n\t"
        "buffer_load_dwordx4 a[112:115], v96, s[16:19], s34 offen nt\n\t"
        "s_add_u32 s34, s34, 0x10000\n\t"
        "buffer_load_dwordx4 a[116:119], v96, s[16:19], s34 offen nt\n\t"
        "s_add_u32 s34, s34, 0x10000\n\t"
        "buffer_load_dwordx4 a[120:123], v96, s[16:19], s34 offen nt\n\t"
        "s_add_u32 s34, s34, 0x10000\n\t"
        "buffer_load_dwordx4 a[124:127], v96, s[16:19], s34 offen nt\n\t"
        "s_waitcnt lgkmcnt(0)\n\t"
        "ds_write_b128 v117, v[104:107] offset:2048\n\t"
        "ds_write_b128 v117, v[108:111] offset:3072\n\t"
        "s_waitcnt vmcnt(16)\n\t"
        "ds_write_b128 v97, v[32:35] offset:0\n\t"
        "ds_write_b128 v98, v[36:39] offset:1024\n\t"
        "ds_write_b128 v97, v[40:43] offset:2048\n\t"
        "ds_write_b128 v98, v[44:47] offset:3072\n\t"
        "ds_write_b128 v97, v[48:51] offset:4096\n\t"
        "ds_write_b128 v98, v[52:55] offset:5120\n\t"
        "ds_write_b128 v97, v[56:59] offset:6144\n\t"
        "ds_write_b128 v98, v[60:63] offset:7168\n\t"
        "ds_read_b64_tr_b16 a[48:49], v99 offset:0\n\t"
        "ds_read_b64_tr_b16 a[50:51], v99 offset:256\n\t"
        "ds_read_b64_tr_b16 a[52:53], v99 offset:4096\n\t"
        "ds_read_b64_tr_b16 a[54:55], v99 offset:4352\n\t"
        "s_waitcnt lgkmcnt(8)\n\t"
        "ds_read_b64_tr_b16 a[56:57], v100 offset:0\n\t"
        "ds_read_b64_tr_b16 a[58:59], v100 offset:256\n\t"
        "ds_read_b64_tr_b16 a[60:61], v100 offset:4096\n\t"
        "ds_read_b64_tr_b16 a[62:63], v100 offset:4352\n\t"
        "s_waitcnt lgkmcnt(8)\n\t"
        "ds_read_b64_tr_b16 a[64:65], v101 offset:0\n\t"
        "ds_read_b64_tr_b16 a[66:67], v101 offset:256\n\t"
        "ds_read_b64_tr_b16 a[68:69], v101 offset:4096\n\t"
        "ds_read_b64_tr_b16 a[70:71], v101 offset:4352\n\t"
        "s_waitcnt lgkmcnt(8)\n\t"
        "ds_read_b64_tr_b16 v[104:105], v102 offset:0\n\t"
        "ds_read_b64_tr_b16 v[106:107], v102 offset:256\n\t"
        "ds_read_b64_tr_b16 v[108:109], v102 offset:4096\n\t"
        "ds_read_b64_tr_b16 v[110:111], v102 offset:4352\n\t"
        "s_lshl_b32 s34, s43, 2\n\t"
        "s_add_u32 s34, s34, 5\n\t"
        "s_and_b32 s34, s34, 15\n\t"
        "s_lshl_b32 s34, s34, 19\n\t"
        "buffer_load_dwordx4 a[128:131], v96, s[16:19], s34 offen nt\n\t"
        "s_add_u32 s34, s34, 0x10000\n\t"
        "buffer_load_dwordx4 a[132:135], v96, s[16:19], s34 offen nt\n\t"
        "s_add_u32 s34, s34, 0x10000\n\t"
        "buffer_load_dwordx4 a[136:139], v96, s[16:19], s34 offen nt\n\t"
        "s_add_u32 s34, s34, 0x10000\n\t"
        "buffer_load_dwordx4 a[140:143], v96, s[16:19], s34 offen nt\n\t"
        "s_add_u32 s34, s34, 0x10000\n\t"
        "buffer_load_dwordx4 a[144:147], v96, s[16:19], s34 offen nt\n\t"
        "s_add_u32 s34, s34, 0x10000\n\t"
        "buffer_load_dwordx4 a[148:151], v96, s[16:19], s34 offen nt\n\t"
        "s_add_u32 s34, s34, 0x10000\n\t"
        "buffer_load_dwordx4 a[152:155], v96, s[16:19], s34 offen nt\n\t"
        "s_add_u32 s34, s34, 0x10000\n\t"
        "buffer_load_dwordx4 a[156:159], v96, s[16:19], s34 offen nt\n\t"
        "s_waitcnt lgkmcnt(0)\n\t"
        "ds_write_b128 v117, v[104:107] offset:4096\n\t"
        "ds_write_b128 v117, v[108:111] offset:5120\n\t"
        "s_waitcnt vmcnt(16)\n\t"
        "ds_write_b128 v97, v[64:67] offset:0\n\t"
        "ds_write_b128 v98, v[68:71] offset:1024\n\t"
        "ds_write_b128 v97, v[72:75] offset:2048\n\t"
        "ds_write_b128 v98, v[76:79] offset:3072\n\t"
        "ds_write_b128 v97, v[80:83] offset:4096\n\t"
        "ds_write_b128 v98, v[84:87] offset:5120\n\t"
        "ds_write_b128 v97, v[88:91] offset:6144\n\t"
        "ds_write_b128 v98, v[92:95] offset:7168\n\t"
        "ds_read_b64_tr_b16 a[72:73], v99 offset:0\n\t"
        "ds_read_b64_tr_b16 a[74:75], v99 offset:256\n\t"
        "ds_read_b64_tr_b16 a[76:77], v99 offset:4096\n\t"
        "ds_read_b64_tr_b16 a[78:79], v99 offset:4352\n\t"
        "s_waitcnt lgkmcnt(8)\n\t"
        "ds_read_b64_tr_b16 a[80:81], v100 offset:0\n\t"
        "ds_read_b64_tr_b16 a[82:83], v100 offset:256\n\t"
        "ds_read_b64_tr_b16 a[84:85], v100 offset:4096\n\t"
        "ds_read_b64_tr_b16 a[86:87], v100 offset:4352\n\t"
        "s_waitcnt lgkmcnt(8)\n\t"
        "ds_read_b64_tr_b16 a[88:89], v101 offset:0\n\t"
        "ds_read_b64_tr_b16 a[90:91], v101 offset:256\n\t"
        "ds_read_b64_tr_b16 a[92:93], v101 offset:4096\n\t"
        "ds_read_b64_tr_b16 a[94:95], v101 offset:4352\n\t"
        "s_waitcnt lgkmcnt(8)\n\t"
        "ds_read_b64_tr_b16 v[104:105], v102 offset:0\n\t"
        "ds_read_b64_tr_b16 v[106:107], v102 offset:256\n\t"
        "ds_read_b64_tr_b16 v[108:109], v102 offset:4096\n\t"
        "ds_read_b64_tr_b16 v[110:111], v102 offset:4352\n\t"
        "s_lshl_b32 s34, s43, 2\n\t"
        "s_add_u32 s34, s34, 6\n\t"
        "s_and_b32 s34, s34, 15\n\t"
        "s_lshl_b32 s34, s34, 19\n\t"
        "buffer_load_dwordx4 a[160:163], v96, s[16:19], s34 offen nt\n\t"
        "s_add_u32 s34, s34, 0x10000\n\t"
        "buffer_load_dwordx4 a[164:167], v96, s[16:19], s34 offen nt\n\t"
        "s_add_u32 s34, s34, 0x10000\n\t"
        "buffer_load_dwordx4 a[168:171], v96, s[16:19], s34 offen nt\n\t"
        "s_add_u32 s34, s34, 0x10000\n\t"
        "buffer_load_dwordx4 a[172:175], v96, s[16:19], s34 offen nt\n\t"
        "s_add_u32 s34, s34, 0x10000\n\t"
        "buffer_load_dwordx4 a[176:179], v96, s[16:19], s34 offen nt\n\t"
        "s_add_u32 s34, s34, 0x10000\n\t"
        "buffer_load_dwordx4 a[180:183], v96, s[16:19], s34 offen nt\n\t"
        "s_add_u32 s34, s34, 0x10000\n\t"
        "buffer_load_dwordx4 a[184:187], v96, s[16:19], s34 offen nt\n\t"
        "s_add_u32 s34, s34, 0x10000\n\t"
        "buffer_load_dwordx4 a[188:191], v96, s[16:19], s34 offen nt\n\t"
        "s_waitcnt lgkmcnt(0)\n\t"
        "ds_write_b128 v117, v[104:107] offset:6144\n\t"
        "ds_write_b128 v117, v[108:111] offset:7168\n\t"
        "s_waitcnt vmcnt(16)\n\t"
        "ds_write_b128 v97, a[96:99] offset:0\n\t"
        "ds_write_b128 v98, a[100:103] offset:1024\n\t"
        "ds_write_b128 v97, a[104:107] offset:2048\n\t"
        "ds_write_b128 v98, a[108:111] offset:3072\n\t"
        "ds_write_b128 v97, a[112:115] offset:4096\n\t"
        "ds_write_b128 v98, a[116:119] offset:5120\n\t"
        "ds_write_b128 v97, a[120:123] offset:6144\n\t"
        "ds_write_b128 v98, a[124:127] offset:7168\n\t"
        "ds_read_b64_tr_b16 a[96:97], v99 offset:0\n\t"
        "ds_read_b64_tr_b16 a[98:99], v99 offset:256\n\t"
        "ds_read_b64_tr_b16 a[100:101], v99 offset:4096\n\t"
        "ds_read_b64_tr_b16 a[102:103], v99 offset:4352\n\t"
        "s_waitcnt lgkmcnt(8)\n\t"
        "ds_read_b64_tr_b16 a[104:105], v100 offset:0\n\t"
        "ds_read_b64_tr_b16 a[106:107], v100 offset:256\n\t"
        "ds_read_b64_tr_b16 a[108:109], v100 offset:4096\n\t"
        "ds_read_b64_tr_b16 a[110:111], v100 offset:4352\n\t"
        "s_waitcnt lgkmcnt(8)\n\t"
        "ds_read_b64_tr_b16 a[112:113], v101 offset:0\n\t"
        "ds_read_b64_tr_b16 a[114:115], v101 offset:256\n\t"
        "ds_read_b64_tr_b16 a[116:117], v101 offset:4096\n\t"
        "ds_read_b64_tr_b16 a[118:119], v101 offset:4352\n\t"
        "s_waitcnt lgkmcnt(8)\n\t"
        "ds_read_b64_tr_b16 v[104:105], v102 offset:0\n\t"
        "ds_read_b64_tr_b16 v[106:107], v102 offset:256\n\t"
        "ds_read_b64_tr_b16 v[108:109], v102 offset:4096\n\t"
        "ds_read_b64_tr_b16 v[110:111], v102 offset:4352\n\t"
        "s_lshl_b32 s34, s43, 2\n\t"
        "s_add_u32 s34, s34, 7\n\t"
        "s_and_b32 s34, s34, 15\n\t"
        "s_lshl_b32 s34, s34, 19\n\t"
        "buffer_load_dwordx4 a[192:195], v96, s[16:19], s34 offen nt\n\t"
        "s_add_u32 s34, s34, 0x10000\n\t"
        "buffer_load_dwordx4 a[196:199], v96, s[16:19], s34 offen nt\n\t"
        "s_add_u32 s34, s34, 0x10000\n\t"
        "buffer_load_dwordx4 a[200:203], v96, s[16:19], s34 offen nt\n\t"
        "s_add_u32 s34, s34, 0x10000\n\t"
        "buffer_load_dwordx4 a[204:207], v96, s[16:19], s34 offen nt\n\t"
        "s_add_u32 s34, s34, 0x10000\n\t"
        "buffer_load_dwordx4 a[208:211], v96, s[16:19], s34 offen nt\n\t"
        "s_add_u32 s34, s34, 0x10000\n\t"
        "buffer_load_dwordx4 a[212:215], v96, s[16:19], s34 offen nt\n\t"
        "s_add_u32 s34, s34, 0x10000\n\t"
        "buffer_load_dwordx4 a[216:219], v96, s[16:19], s34 offen nt\n\t"
        "s_add_u32 s34, s34, 0x10000\n\t"
        "buffer_load_dwordx4 a[220:223], v96, s[16:19], s34 offen nt\n\t"
        "s_waitcnt lgkmcnt(0)\n\t"
        "ds_write_b128 v126, v[104:107] offset:0\n\t"
        "ds_write_b128 v126, v[108:111] offset:1024\n\t"
        "s_waitcnt vmcnt(16)\n\t"
        "ds_write_b128 v97, a[128:131] offset:0\n\t"
        "ds_write_b128 v98, a[132:135] offset:1024\n\t"
        "ds_write_b128 v97, a[136:139] offset:2048\n\t"
        "ds_write_b128 v98, a[140:143] offset:3072\n\t"
        "ds_write_b128 v97, a[144:147] offset:4096\n\t"
        "ds_write_b128 v98, a[148:151] offset:5120\n\t"
        "ds_write_b128 v97, a[152:155] offset:6144\n\t"
        "ds_write_b128 v98, a[156:159] offset:7168\n\t"
        "ds_read_b64_tr_b16 a[120:121], v99 offset:0\n\t"
        "ds_read_b64_tr_b16 a[122:123], v99 offset:256\n\t"
        "ds_read_b64_tr_b16 a[124:125], v99 offset:4096\n\t"
        "ds_read_b64_tr_b16 a[126:127], v99 offset:4352\n\t"
        "s_waitcnt lgkmcnt(8)\n\t"
        "ds_read_b64_tr_b16 a[128:129], v100 offset:0\n\t"
        "ds_read_b64_tr_b16 a[130:131], v100 offset:256\n\t"
        "ds_read_b64_tr_b16 a[132:133], v100 offset:4096\n\t"
        "ds_read_b64_tr_b16 a[134:135], v100 offset:4352\n\t"
        "s_waitcnt lgkmcnt(8)\n\t"
        "ds_read_b64_tr_b16 a[136:137], v101 offset:0\n\t"
        "ds_read_b64_tr_b16 a[138:139], v101 offset:256\n\t"
        "ds_read_b64_tr_b16 a[140:141], v101 offset:4096\n\t"
        "ds_read_b64_tr_b16 a[142:143], v101 offset:4352\n\t"
        "s_waitcnt lgkmcnt(8)\n\t"
        "ds_read_b64_tr_b16 v[104:105], v102 offset:0\n\t"
        "ds_read_b64_tr_b16 v[106:107], v102 offset:256\n\t"
        "ds_read_b64_tr_b16 v[108:109], v102 offset:4096\n\t"
        "ds_read_b64_tr_b16 v[110:111], v102 offset:4352\n\t"
        "s_lshl_b32 s34, s43, 2\n\t"
        "s_add_u32 s34, s34, 8\n\t"
        "s_and_b32 s34, s34, 15\n\t"
        "s_lshl_b32 s34, s34, 19\n\t"
        "buffer_load_dwordx4 a[224:227], v96, s[16:19], s34 offen nt\n\t"
        "s_add_u32 s34, s34, 0x10000\n\t"
        "buffer_load_dwordx4 a[228:231], v96, s[16:19], s34 offen nt\n\t"
        "s_add_u32 s34, s34, 0x10000\n\t"
        "buffer_load_dwordx4 a[232:235], v96, s[16:19], s34 offen nt\n\t"
        "s_add_u32 s34, s34, 0x10000\n\t"
        "buffer_load_dwordx4 a[236:239], v96, s[16:19], s34 offen nt\n\t"
        "s_add_u32 s34, s34, 0x10000\n\t"
        "buffer_load_dwordx4 a[240:243], v96, s[16:19], s34 offen nt\n\t"
        "s_add_u32 s34, s34, 0x10000\n\t"
        "buffer_load_dwordx4 a[244:247], v96, s[16:19], s34 offen nt\n\t"
        "s_add_u32 s34, s34, 0x10000\n\t"
        "buffer_load_dwordx4 a[248:251], v96, s[16:19], s34 offen nt\n\t"
        "s_add_u32 s34, s34, 0x10000\n\t"
        "buffer_load_dwordx4 a[252:255], v96, s[16:19], s34 offen nt\n\t"
        "s_waitcnt lgkmcnt(0)\n\t"
        "ds_write_b128 v126, v[104:107] offset:2048\n\t"
        "ds_write_b128 v126, v[108:111] offset:3072\n\t"
        "s_waitcnt vmcnt(16)\n\t"
        "ds_write_b128 v97, a[160:163] offset:0\n\t"
        "ds_write_b128 v98, a[164:167] offset:1024\n\t"
        "ds_write_b128 v97, a[168:171] offset:2048\n\t"
        "ds_write_b128 v98, a[172:175] offset:3072\n\t"
        "ds_write_b128 v97, a[176:179] offset:4096\n\t"
        "ds_write_b128 v98, a[180:183] offset:5120\n\t"
        "ds_write_b128 v97, a[184:187] offset:6144\n\t"
        "ds_write_b128 v98, a[188:191] offset:7168\n\t"
        "ds_read_b64_tr_b16 a[144:145], v99 offset:0\n\t"
        "ds_read_b64_tr_b16 a[146:147], v99 offset:256\n\t"
        "ds_read_b64_tr_b16 a[148:149], v99 offset:4096\n\t"
        "ds_read_b64_tr_b16 a[150:151], v99 offset:4352\n\t"
        "s_waitcnt lgkmcnt(8)\n\t"
        "ds_read_b64_tr_b16 a[152:153], v100 offset:0\n\t"
        "ds_read_b64_tr_b16 a[154:155], v100 offset:256\n\t"
        "ds_read_b64_tr_b16 a[156:157], v100 offset:4096\n\t"
        "ds_read_b64_tr_b16 a[158:159], v100 offset:4352\n\t"
        "s_waitcnt lgkmcnt(8)\n\t"
        "ds_read_b64_tr_b16 a[160:161], v101 offset:0\n\t"
        "ds_read_b64_tr_b16 a[162:163], v101 offset:256\n\t"
        "ds_read_b64_tr_b16 a[164:165], v101 offset:4096\n\t"
        "ds_read_b64_tr_b16 a[166:167], v101 offset:4352\n\t"
        "s_waitcnt lgkmcnt(8)\n\t"
        "ds_read_b64_tr_b16 v[104:105], v102 offset:0\n\t"
        "ds_read_b64_tr_b16 v[106:107], v102 offset:256\n\t"
        "ds_read_b64_tr_b16 v[108:109], v102 offset:4096\n\t"
        "ds_read_b64_tr_b16 v[110:111], v102 offset:4352\n\t"
        "s_lshl_b32 s34, s43, 2\n\t"
        "s_add_u32 s34, s34, 9\n\t"
        "s_and_b32 s34, s34, 15\n\t"
        "s_lshl_b32 s34, s34, 19\n\t"
        "buffer_load_dwordx4 v[128:131], v96, s[16:19], s34 offen nt\n\t"
        "s_add_u32 s34, s34, 0x10000\n\t"
        "buffer_load_dwordx4 v[132:135], v96, s[16:19], s34 offen nt\n\t"
        "s_add_u32 s34, s34, 0x10000\n\t"
        "buffer_load_dwordx4 v[136:139], v96, s[16:19], s34 offen nt\n\t"
        "s_add_u32 s34, s34, 0x10000\n\t"
        "buffer_load_dwordx4 v[140:143], v96, s[16:19], s34 offen nt\n\t"
        "s_add_u32 s34, s34, 0x10000\n\t"
        "buffer_load_dwordx4 v[144:147], v96, s[16:19], s34 offen nt\n\t"
        "s_add_u32 s34, s34, 0x10000\n\t"
        "buffer_load_dwordx4 v[148:151], v96, s[16:19], s34 offen nt\n\t"
        "s_add_u32 s34, s34, 0x10000\n\t"
        "buffer_load_dwordx4 v[152:155], v96, s[16:19], s34 offen nt\n\t"
        "s_add_u32 s34, s34, 0x10000\n\t"
        "buffer_load_dwordx4 v[156:159], v96, s[16:19], s34 offen nt\n\t"
        "s_waitcnt lgkmcnt(0)\n\t"
        "ds_write_b128 v126, v[104:107] offset:4096\n\t"
        "ds_write_b128 v126, v[108:111] offset:5120\n\t"
        "s_waitcnt vmcnt(16)\n\t"
        "ds_write_b128 v97, a[192:195] offset:0\n\t"
        "ds_write_b128 v98, a[196:199] offset:1024\n\t"
        "ds_write_b128 v97, a[200:203] offset:2048\n\t"
        "ds_write_b128 v98, a[204:207] offset:3072\n\t"
        "ds_write_b128 v97, a[208:211] offset:4096\n\t"
        "ds_write_b128 v98, a[212:215] offset:5120\n\t"
        "ds_write_b128 v97, a[216:219] offset:6144\n\t"
        "ds_write_b128 v98, a[220:223] offset:7168\n\t"
        "ds_read_b64_tr_b16 a[168:169], v99 offset:0\n\t"
        "ds_read_b64_tr_b16 a[170:171], v99 offset:256\n\t"
        "ds_read_b64_tr_b16 a[172:173], v99 offset:4096\n\t"
        "ds_read_b64_tr_b16 a[174:175], v99 offset:4352\n\t"
        "s_waitcnt lgkmcnt(8)\n\t"
        "ds_read_b64_tr_b16 a[176:177], v100 offset:0\n\t"
        "ds_read_b64_tr_b16 a[178:179], v100 offset:256\n\t"
        "ds_read_b64_tr_b16 a[180:181], v100 offset:4096\n\t"
        "ds_read_b64_tr_b16 a[182:183], v100 offset:4352\n\t"
        "s_waitcnt lgkmcnt(8)\n\t"
        "ds_read_b64_tr_b16 a[184:185], v101 offset:0\n\t"
        "ds_read_b64_tr_b16 a[186:187], v101 offset:256\n\t"
        "ds_read_b64_tr_b16 a[188:189], v101 offset:4096\n\t"
        "ds_read_b64_tr_b16 a[190:191], v101 offset:4352\n\t"
        "s_waitcnt lgkmcnt(8)\n\t"
        "ds_read_b64_tr_b16 v[104:105], v102 offset:0\n\t"
        "ds_read_b64_tr_b16 v[106:107], v102 offset:256\n\t"
        "ds_read_b64_tr_b16 v[108:109], v102 offset:4096\n\t"
        "ds_read_b64_tr_b16 v[110:111], v102 offset:4352\n\t"
        "s_lshl_b32 s34, s43, 2\n\t"
        "s_add_u32 s34, s34, 10\n\t"
        "s_and_b32 s34, s34, 15\n\t"
        "s_lshl_b32 s34, s34, 19\n\t"
        "buffer_load_dwordx4 v[160:163], v96, s[16:19], s34 offen nt\n\t"
        "s_add_u32 s34, s34, 0x10000\n\t"
        "buffer_load_dwordx4 v[164:167], v96, s[16:19], s34 offen nt\n\t"
        "s_add_u32 s34, s34, 0x10000\n\t"
        "buffer_load_dwordx4 v[168:171], v96, s[16:19], s34 offen nt\n\t"
        "s_add_u32 s34, s34, 0x10000\n\t"
        "buffer_load_dwordx4 v[172:175], v96, s[16:19], s34 offen nt\n\t"
        "s_add_u32 s34, s34, 0x10000\n\t"
        "buffer_load_dwordx4 v[176:179], v96, s[16:19], s34 offen nt\n\t"
        "s_add_u32 s34, s34, 0x10000\n\t"
        "buffer_load_dwordx4 v[180:183], v96, s[16:19], s34 offen nt\n\t"
        "s_add_u32 s34, s34, 0x10000\n\t"
        "buffer_load_dwordx4 v[184:187], v96, s[16:19], s34 offen nt\n\t"
        "s_add_u32 s34, s34, 0x10000\n\t"
        "buffer_load_dwordx4 v[188:191], v96, s[16:19], s34 offen nt\n\t"
        "s_waitcnt lgkmcnt(0)\n\t"
        "ds_write_b128 v126, v[104:107] offset:6144\n\t"
        "ds_write_b128 v126, v[108:111] offset:7168\n\t"
        "s_waitcnt vmcnt(16)\n\t"
        "ds_write_b128 v97, a[224:227] offset:0\n\t"
        "ds_write_b128 v98, a[228:231] offset:1024\n\t"
        "ds_write_b128 v97, a[232:235] offset:2048\n\t"
        "ds_write_b128 v98, a[236:239] offset:3072\n\t"
        "ds_write_b128 v97, a[240:243] offset:4096\n\t"
        "ds_write_b128 v98, a[244:247] offset:5120\n\t"
        "ds_write_b128 v97, a[248:251] offset:6144\n\t"
        "ds_write_b128 v98, a[252:255] offset:7168\n\t"
        "ds_read_b64_tr_b16 a[192:193], v99 offset:0\n\t"
        "ds_read_b64_tr_b16 a[194:195], v99 offset:256\n\t"
        "ds_read_b64_tr_b16 a[196:197], v99 offset:4096\n\t"
        "ds_read_b64_tr_b16 a[198:199], v99 offset:4352\n\t"
        "s_waitcnt lgkmcnt(8)\n\t"
        "ds_read_b64_tr_b16 a[200:201], v100 offset:0\n\t"
        "ds_read_b64_tr_b16 a[202:203], v100 offset:256\n\t"
        "ds_read_b64_tr_b16 a[204:205], v100 offset:4096\n\t"
        "ds_read_b64_tr_b16 a[206:207], v100 offset:4352\n\t"
        "s_waitcnt lgkmcnt(8)\n\t"
        "ds_read_b64_tr_b16 a[208:209], v101 offset:0\n\t"
        "ds_read_b64_tr_b16 a[210:211], v101 offset:256\n\t"
        "ds_read_b64_tr_b16 a[212:213], v101 offset:4096\n\t"
        "ds_read_b64_tr_b16 a[214:215], v101 offset:4352\n\t"
        "s_waitcnt lgkmcnt(8)\n\t"
        "ds_read_b64_tr_b16 v[104:105], v102 offset:0\n\t"
        "ds_read_b64_tr_b16 v[106:107], v102 offset:256\n\t"
        "ds_read_b64_tr_b16 v[108:109], v102 offset:4096\n\t"
        "ds_read_b64_tr_b16 v[110:111], v102 offset:4352\n\t"
        "s_lshl_b32 s34, s43, 2\n\t"
        "s_add_u32 s34, s34, 11\n\t"
        "s_and_b32 s34, s34, 15\n\t"
        "s_lshl_b32 s34, s34, 19\n\t"
        "buffer_load_dwordx4 v[192:195], v96, s[16:19], s34 offen nt\n\t"
        "s_add_u32 s34, s34, 0x10000\n\t"
        "buffer_load_dwordx4 v[196:199], v96, s[16:19], s34 offen nt\n\t"
        "s_add_u32 s34, s34, 0x10000\n\t"
        "buffer_load_dwordx4 v[200:203], v96, s[16:19], s34 offen nt\n\t"
        "s_add_u32 s34, s34, 0x10000\n\t"
        "buffer_load_dwordx4 v[204:207], v96, s[16:19], s34 offen nt\n\t"
        "s_add_u32 s34, s34, 0x10000\n\t"
        "buffer_load_dwordx4 v[208:211], v96, s[16:19], s34 offen nt\n\t"
        "s_add_u32 s34, s34, 0x10000\n\t"
        "buffer_load_dwordx4 v[212:215], v96, s[16:19], s34 offen nt\n\t"
        "s_add_u32 s34, s34, 0x10000\n\t"
        "buffer_load_dwordx4 v[216:219], v96, s[16:19], s34 offen nt\n\t"
        "s_add_u32 s34, s34, 0x10000\n\t"
        "buffer_load_dwordx4 v[220:223], v96, s[16:19], s34 offen nt\n\t"
        "s_waitcnt lgkmcnt(0)\n\t"
        "ds_write_b128 v117, v[104:107] offset:8192\n\t"
        "ds_write_b128 v117, v[108:111] offset:9216\n\t"
        "s_waitcnt vmcnt(16)\n\t"
        "ds_write_b128 v97, v[128:131] offset:0\n\t"
        "ds_write_b128 v98, v[132:135] offset:1024\n\t"
        "ds_write_b128 v97, v[136:139] offset:2048\n\t"
        "ds_write_b128 v98, v[140:143] offset:3072\n\t"
        "ds_write_b128 v97, v[144:147] offset:4096\n\t"
        "ds_write_b128 v98, v[148:151] offset:5120\n\t"
        "ds_write_b128 v97, v[152:155] offset:6144\n\t"
        "ds_write_b128 v98, v[156:159] offset:7168\n\t"
        "ds_read_b64_tr_b16 a[216:217], v99 offset:0\n\t"
        "ds_read_b64_tr_b16 a[218:219], v99 offset:256\n\t"
        "ds_read_b64_tr_b16 a[220:221], v99 offset:4096\n\t"
        "ds_read_b64_tr_b16 a[222:223], v99 offset:4352\n\t"
        "s_waitcnt lgkmcnt(8)\n\t"
        "ds_read_b64_tr_b16 a[224:225], v100 offset:0\n\t"
        "ds_read_b64_tr_b16 a[226:227], v100 offset:256\n\t"
        "ds_read_b64_tr_b16 a[228:229], v100 offset:4096\n\t"
        "ds_read_b64_tr_b16 a[230:231], v100 offset:4352\n\t"
        "s_waitcnt lgkmcnt(8)\n\t"
        "ds_read_b64_tr_b16 a[232:233], v101 offset:0\n\t"
        "ds_read_b64_tr_b16 a[234:235], v101 offset:256\n\t"
        "ds_read_b64_tr_b16 a[236:237], v101 offset:4096\n\t"
        "ds_read_b64_tr_b16 a[238:239], v101 offset:4352\n\t"
        "s_waitcnt lgkmcnt(8)\n\t"
        "ds_read_b64_tr_b16 v[104:105], v102 offset:0\n\t"
        "ds_read_b64_tr_b16 v[106:107], v102 offset:256\n\t"
        "ds_read_b64_tr_b16 v[108:109], v102 offset:4096\n\t"
        "ds_read_b64_tr_b16 v[110:111], v102 offset:4352\n\t"
        "s_lshl_b32 s34, s43, 2\n\t"
        "s_add_u32 s34, s34, 12\n\t"
        "s_and_b32 s34, s34, 15\n\t"
        "s_lshl_b32 s34, s34, 19\n\t"
        "buffer_load_dwordx4 v[224:227], v96, s[16:19], s34 offen nt\n\t"
        "s_add_u32 s34, s34, 0x10000\n\t"
        "buffer_load_dwordx4 v[228:231], v96, s[16:19], s34 offen nt\n\t"
        "s_add_u32 s34, s34, 0x10000\n\t"
        "buffer_load_dwordx4 v[232:235], v96, s[16:19], s34 offen nt\n\t"
        "s_add_u32 s34, s34, 0x10000\n\t"
        "buffer_load_dwordx4 v[236:239], v96, s[16:19], s34 offen nt\n\t"
        "s_add_u32 s34, s34, 0x10000\n\t"
        "buffer_load_dwordx4 v[240:243], v96, s[16:19], s34 offen nt\n\t"
        "s_add_u32 s34, s34, 0x10000\n\t"
        "buffer_load_dwordx4 v[244:247], v96, s[16:19], s34 offen nt\n\t"
        "s_add_u32 s34, s34, 0x10000\n\t"
        "buffer_load_dwordx4 v[248:251], v96, s[16:19], s34 offen nt\n\t"
        "s_add_u32 s34, s34, 0x10000\n\t"
        "buffer_load_dwordx4 v[252:255], v96, s[16:19], s34 offen nt\n\t"
        "s_waitcnt lgkmcnt(0)\n\t"
        "ds_write_b128 v117, v[104:107] offset:10240\n\t"
        "ds_write_b128 v117, v[108:111] offset:11264\n\t"
        "s_waitcnt vmcnt(16)\n\t"
        "ds_write_b128 v97, v[160:163] offset:0\n\t"
        "ds_write_b128 v98, v[164:167] offset:1024\n\t"
        "ds_write_b128 v97, v[168:171] offset:2048\n\t"
        "ds_write_b128 v98, v[172:175] offset:3072\n\t"
        "ds_write_b128 v97, v[176:179] offset:4096\n\t"
        "ds_write_b128 v98, v[180:183] offset:5120\n\t"
        "ds_write_b128 v97, v[184:187] offset:6144\n\t"
        "ds_write_b128 v98, v[188:191] offset:7168\n\t"
        "ds_read_b64_tr_b16 a[240:241], v99 offset:0\n\t"
        "ds_read_b64_tr_b16 a[242:243], v99 offset:256\n\t"
        "ds_read_b64_tr_b16 a[244:245], v99 offset:4096\n\t"
        "ds_read_b64_tr_b16 a[246:247], v99 offset:4352\n\t"
        "s_waitcnt lgkmcnt(8)\n\t"
        "ds_read_b64_tr_b16 a[248:249], v100 offset:0\n\t"
        "ds_read_b64_tr_b16 a[250:251], v100 offset:256\n\t"
        "ds_read_b64_tr_b16 a[252:253], v100 offset:4096\n\t"
        "ds_read_b64_tr_b16 a[254:255], v100 offset:4352\n\t"
        "s_waitcnt lgkmcnt(8)\n\t"
        "ds_read_b64_tr_b16 v[128:129], v101 offset:0\n\t"
        "ds_read_b64_tr_b16 v[130:131], v101 offset:256\n\t"
        "ds_read_b64_tr_b16 v[132:133], v101 offset:4096\n\t"
        "ds_read_b64_tr_b16 v[134:135], v101 offset:4352\n\t"
        "s_waitcnt lgkmcnt(8)\n\t"
        "ds_read_b64_tr_b16 v[104:105], v102 offset:0\n\t"
        "ds_read_b64_tr_b16 v[106:107], v102 offset:256\n\t"
        "ds_read_b64_tr_b16 v[108:109], v102 offset:4096\n\t"
        "ds_read_b64_tr_b16 v[110:111], v102 offset:4352\n\t"
        "s_lshl_b32 s34, s43, 2\n\t"
        "s_add_u32 s34, s34, 13\n\t"
        "s_and_b32 s34, s34, 15\n\t"
        "s_lshl_b32 s34, s34, 19\n\t"
        "buffer_load_dwordx4 v[0:3], v96, s[16:19], s34 offen nt\n\t"
        "s_add_u32 s34, s34, 0x10000\n\t"
        "buffer_load_dwordx4 v[4:7], v96, s[16:19], s34 offen nt\n\t"
        "s_add_u32 s34, s34, 0x10000\n\t"
        "buffer_load_dwordx4 v[8:11], v96, s[16:19], s34 offen nt\n\t"
        "s_add_u32 s34, s34, 0x10000\n\t"
        "buffer_load_dwordx4 v[12:15], v96, s[16:19], s34 offen nt\n\t"
        "s_add_u32 s34, s34, 0x10000\n\t"
        "buffer_load_dwordx4 v[16:19], v96, s[16:19], s34 offen nt\n\t"
        "s_add_u32 s34, s34, 0x10000\n\t"
        "buffer_load_dwordx4 v[20:23], v96, s[16:19], s34 offen nt\n\t"
        "s_add_u32 s34, s34, 0x10000\n\t"
        "buffer_load_dwordx4 v[24:27], v96, s[16:19], s34 offen nt\n\t"
        "s_add_u32 s34, s34, 0x10000\n\t"
        "buffer_load_dwordx4 v[28:31], v96, s[16:19], s34 offen nt\n\t"
        "s_waitcnt lgkmcnt(0)\n\t"
        "ds_write_b128 v117, v[104:107] offset:12288\n\t"
        "ds_write_b128 v117, v[108:111] offset:13312\n\t"
        "s_waitcnt vmcnt(16)\n\t"
        "ds_write_b128 v97, v[192:195] offset:0\n\t"
        "ds_write_b128 v98, v[196:199] offset:1024\n\t"
        "ds_write_b128 v97, v[200:203] offset:2048\n\t"
        "ds_write_b128 v98, v[204:207] offset:3072\n\t"
        "ds_write_b128 v97, v[208:211] offset:4096\n\t"
        "ds_write_b128 v98, v[212:215] offset:5120\n\t"
        "ds_write_b128 v97, v[216:219] offset:6144\n\t"
        "ds_write_b128 v98, v[220:223] offset:7168\n\t"
        "ds_read_b64_tr_b16 v[136:137], v99 offset:0\n\t"
        "ds_read_b64_tr_b16 v[138:139], v99 offset:256\n\t"
        "ds_read_b64_tr_b16 v[140:141], v99 offset:4096\n\t"
        "ds_read_b64_tr_b16 v[142:143], v99 offset:4352\n\t"
        "s_waitcnt lgkmcnt(8)\n\t"
        "ds_read_b64_tr_b16 v[144:145], v100 offset:0\n\t"
        "ds_read_b64_tr_b16 v[146:147], v100 offset:256\n\t"
        "ds_read_b64_tr_b16 v[148:149], v100 offset:4096\n\t"
        "ds_read_b64_tr_b16 v[150:151], v100 offset:4352\n\t"
        "s_waitcnt lgkmcnt(8)\n\t"
        "ds_read_b64_tr_b16 v[152:153], v101 offset:0\n\t"
        "ds_read_b64_tr_b16 v[154:155], v101 offset:256\n\t"
        "ds_read_b64_tr_b16 v[156:157], v101 offset:4096\n\t"
        "ds_read_b64_tr_b16 v[158:159], v101 offset:4352\n\t"
        "s_waitcnt lgkmcnt(8)\n\t"
        "ds_read_b64_tr_b16 v[104:105], v102 offset:0\n\t"
        "ds_read_b64_tr_b16 v[106:107], v102 offset:256\n\t"
        "ds_read_b64_tr_b16 v[108:109], v102 offset:4096\n\t"
        "ds_read_b64_tr_b16 v[110:111], v102 offset:4352\n\t"
        "s_lshl_b32 s34, s43, 2\n\t"
        "s_add_u32 s34, s34, 14\n\t"
        "s_and_b32 s34, s34, 15\n\t"
        "s_lshl_b32 s34, s34, 19\n\t"
        "buffer_load_dwordx4 v[32:35], v96, s[16:19], s34 offen nt\n\t"
        "s_add_u32 s34, s34, 0x10000\n\t"
        "buffer_load_dwordx4 v[36:39], v96, s[16:19], s34 offen nt\n\t"
        "s_add_u32 s34, s34, 0x10000\n\t"
        "buffer_load_dwordx4 v[40:43], v96, s[16:19], s34 offen nt\n\t"
        "s_add_u32 s34, s34, 0x10000\n\t"
        "buffer_load_dwordx4 v[44:47], v96, s[16:19], s34 offen nt\n\t"
        "s_add_u32 s34, s34, 0x10000\n\t"
        "buffer_load_dwordx4 v[48:51], v96, s[16:19], s34 offen nt\n\t"
        "s_add_u32 s34, s34, 0x10000\n\t"
        "buffer_load_dwordx4 v[52:55], v96, s[16:19], s34 offen nt\n\t"
        "s_add_u32 s34, s34, 0x10000\n\t"
        "buffer_load_dwordx4 v[56:59], v96, s[16:19], s34 offen nt\n\t"
        "s_add_u32 s34, s34, 0x10000\n\t"
        "buffer_load_dwordx4 v[60:63], v96, s[16:19], s34 offen nt\n\t"
        "s_waitcnt lgkmcnt(0)\n\t"
        "ds_write_b128 v117, v[104:107] offset:14336\n\t"
        "ds_write_b128 v117, v[108:111] offset:15360\n\t"
        "s_waitcnt vmcnt(16)\n\t"
        "ds_write_b128 v97, v[224:227] offset:0\n\t"
        "ds_write_b128 v98, v[228:231] offset:1024\n\t"
        "ds_write_b128 v97, v[232:235] offset:2048\n\t"
        "ds_write_b128 v98, v[236:239] offset:3072\n\t"
        "ds_write_b128 v97, v[240:243] offset:4096\n\t"
        "ds_write_b128 v98, v[244:247] offset:5120\n\t"
        "ds_write_b128 v97, v[248:251] offset:6144\n\t"
        "ds_write_b128 v98, v[252:255] offset:7168\n\t"
        "ds_read_b64_tr_b16 v[160:161], v99 offset:0\n\t"
        "ds_read_b64_tr_b16 v[162:163], v99 offset:256\n\t"
        "ds_read_b64_tr_b16 v[164:165], v99 offset:4096\n\t"
        "ds_read_b64_tr_b16 v[166:167], v99 offset:4352\n\t"
        "s_waitcnt lgkmcnt(8)\n\t"
        "ds_read_b64_tr_b16 v[168:169], v100 offset:0\n\t"
        "ds_read_b64_tr_b16 v[170:171], v100 offset:256\n\t"
        "ds_read_b64_tr_b16 v[172:173], v100 offset:4096\n\t"
        "ds_read_b64_tr_b16 v[174:175], v100 offset:4352\n\t"
        "s_waitcnt lgkmcnt(8)\n\t"
        "ds_read_b64_tr_b16 v[176:177], v101 offset:0\n\t"
        "ds_read_b64_tr_b16 v[178:179], v101 offset:256\n\t"
        "ds_read_b64_tr_b16 v[180:181], v101 offset:4096\n\t"
        "ds_read_b64_tr_b16 v[182:183], v101 offset:4352\n\t"
        "s_waitcnt lgkmcnt(8)\n\t"
        "ds_read_b64_tr_b16 v[104:105], v102 offset:0\n\t"
        "ds_read_b64_tr_b16 v[106:107], v102 offset:256\n\t"
        "ds_read_b64_tr_b16 v[108:109], v102 offset:4096\n\t"
        "ds_read_b64_tr_b16 v[110:111], v102 offset:4352\n\t"
        "s_lshl_b32 s34, s43, 2\n\t"
        "s_add_u32 s34, s34, 15\n\t"
        "s_and_b32 s34, s34, 15\n\t"
        "s_lshl_b32 s34, s34, 19\n\t"
        "buffer_load_dwordx4 v[64:67], v96, s[16:19], s34 offen nt\n\t"
        "s_add_u32 s34, s34, 0x10000\n\t"
        "buffer_load_dwordx4 v[68:71], v96, s[16:19], s34 offen nt\n\t"
        "s_add_u32 s34, s34, 0x10000\n\t"
        "buffer_load_dwordx4 v[72:75], v96, s[16:19], s34 offen nt\n\t"
        "s_add_u32 s34, s34, 0x10000\n\t"
        "buffer_load_dwordx4 v[76:79], v96, s[16:19], s34 offen nt\n\t"
        "s_add_u32 s34, s34, 0x10000\n\t"
        "buffer_load_dwordx4 v[80:83], v96, s[16:19], s34 offen nt\n\t"
        "s_add_u32 s34, s34, 0x10000\n\t"
        "buffer_load_dwordx4 v[84:87], v96, s[16:19], s34 offen nt\n\t"
        "s_add_u32 s34, s34, 0x10000\n\t"
        "buffer_load_dwordx4 v[88:91], v96, s[16:19], s34 offen nt\n\t"
        "s_add_u32 s34, s34, 0x10000\n\t"
        "buffer_load_dwordx4 v[92:95], v96, s[16:19], s34 offen nt\n\t"
        "s_waitcnt lgkmcnt(0)\n\t"
        "ds_write_b128 v117, v[104:107] offset:16384\n\t"
        "ds_write_b128 v117, v[108:111] offset:17408\n\t"
        "s_waitcnt vmcnt(16)\n\t"
        "ds_write_b128 v97, v[0:3] offset:0\n\t"
        "ds_write_b128 v98, v[4:7] offset:1024\n\t"
        "ds_write_b128 v97, v[8:11] offset:2048\n\t"
        "ds_write_b128 v98, v[12:15] offset:3072\n\t"
        "ds_write_b128 v97, v[16:19] offset:4096\n\t"
        "ds_write_b128 v98, v[20:23] offset:5120\n\t"
        "ds_write_b128 v97, v[24:27] offset:6144\n\t"
        "ds_write_b128 v98, v[28:31] offset:7168\n\t"
        "ds_read_b64_tr_b16 v[184:185], v99 offset:0\n\t"
        "ds_read_b64_tr_b16 v[186:187], v99 offset:256\n\t"
        "ds_read_b64_tr_b16 v[188:189], v99 offset:4096\n\t"
        "ds_read_b64_tr_b16 v[190:191], v99 offset:4352\n\t"
        "s_waitcnt lgkmcnt(8)\n\t"
        "ds_read_b64_tr_b16 v[192:193], v100 offset:0\n\t"
        "ds_read_b64_tr_b16 v[194:195], v100 offset:256\n\t"
        "ds_read_b64_tr_b16 v[196:197], v100 offset:4096\n\t"
        "ds_read_b64_tr_b16 v[198:199], v100 offset:4352\n\t"
        "s_waitcnt lgkmcnt(8)\n\t"
        "ds_read_b64_tr_b16 v[200:201], v101 offset:0\n\t"
        "ds_read_b64_tr_b16 v[202:203], v101 offset:256\n\t"
        "ds_read_b64_tr_b16 v[204:205], v101 offset:4096\n\t"
        "ds_read_b64_tr_b16 v[206:207], v101 offset:4352\n\t"
        "s_waitcnt lgkmcnt(8)\n\t"
        "ds_read_b64_tr_b16 v[104:105], v102 offset:0\n\t"
        "ds_read_b64_tr_b16 v[106:107], v102 offset:256\n\t"
        "ds_read_b64_tr_b16 v[108:109], v102 offset:4096\n\t"
        "ds_read_b64_tr_b16 v[110:111], v102 offset:4352\n\t"
        "s_waitcnt lgkmcnt(0)\n\t"
        "ds_write_b128 v117, v[104:107] offset:18432\n\t"
        "ds_write_b128 v117, v[108:111] offset:19456\n\t"
        "s_waitcnt vmcnt(8)\n\t"
        "ds_write_b128 v97, v[32:35] offset:0\n\t"
        "ds_write_b128 v98, v[36:39] offset:1024\n\t"
        "ds_write_b128 v97, v[40:43] offset:2048\n\t"
        "ds_write_b128 v98, v[44:47] offset:3072\n\t"
        "ds_write_b128 v97, v[48:51] offset:4096\n\t"
        "ds_write_b128 v98, v[52:55] offset:5120\n\t"
        "ds_write_b128 v97, v[56:59] offset:6144\n\t"
        "ds_write_b128 v98, v[60:63] offset:7168\n\t"
        "ds_read_b64_tr_b16 v[208:209], v99 offset:0\n\t"
        "ds_read_b64_tr_b16 v[210:211], v99 offset:256\n\t"
        "ds_read_b64_tr_b16 v[212:213], v99 offset:4096\n\t"
        "ds_read_b64_tr_b16 v[214:215], v99 offset:4352\n\t"
        "s_waitcnt lgkmcnt(8)\n\t"
        "ds_read_b64_tr_b16 v[216:217], v100 offset:0\n\t"
        "ds_read_b64_tr_b16 v[218:219], v100 offset:256\n\t"
        "ds_read_b64_tr_b16 v[220:221], v100 offset:4096\n\t"
        "ds_read_b64_tr_b16 v[222:223], v100 offset:4352\n\t"
        "s_waitcnt lgkmcnt(8)\n\t"
        "ds_read_b64_tr_b16 v[224:225], v101 offset:0\n\t"
        "ds_read_b64_tr_b16 v[226:227], v101 offset:256\n\t"
        "ds_read_b64_tr_b16 v[228:229], v101 offset:4096\n\t"
        "ds_read_b64_tr_b16 v[230:231], v101 offset:4352\n\t"
        "s_waitcnt lgkmcnt(8)\n\t"
        "ds_read_b64_tr_b16 v[104:105], v102 offset:0\n\t"
        "ds_read_b64_tr_b16 v[106:107], v102 offset:256\n\t"
        "ds_read_b64_tr_b16 v[108:109], v102 offset:4096\n\t"
        "ds_read_b64_tr_b16 v[110:111], v102 offset:4352\n\t"
        "s_mov_b32 s35, 33792\n\t"
        "buffer_load_dword v0, off, s[12:15], s35 sc1\n\t"
        "s_waitcnt lgkmcnt(0)\n\t"
        "ds_write_b128 v117, v[104:107] offset:20480\n\t"
        "ds_write_b128 v117, v[108:111] offset:21504\n\t"
        "s_waitcnt vmcnt(0)\n\t"
        "ds_write_b128 v97, v[64:67] offset:0\n\t"
        "ds_write_b128 v98, v[68:71] offset:1024\n\t"
        "ds_write_b128 v97, v[72:75] offset:2048\n\t"
        "ds_write_b128 v98, v[76:79] offset:3072\n\t"
        "ds_write_b128 v97, v[80:83] offset:4096\n\t"
        "ds_write_b128 v98, v[84:87] offset:5120\n\t"
        "ds_write_b128 v97, v[88:91] offset:6144\n\t"
        "ds_write_b128 v98, v[92:95] offset:7168\n\t"
        "ds_read_b64_tr_b16 v[232:233], v99 offset:0\n\t"
        "ds_read_b64_tr_b16 v[234:235], v99 offset:256\n\t"
        "ds_read_b64_tr_b16 v[236:237], v99 offset:4096\n\t"
        "ds_read_b64_tr_b16 v[238:239], v99 offset:4352\n\t"
        "s_waitcnt lgkmcnt(8)\n\t"
        "ds_read_b64_tr_b16 v[240:241], v100 offset:0\n\t"
        "ds_read_b64_tr_b16 v[242:243], v100 offset:256\n\t"
        "ds_read_b64_tr_b16 v[244:245], v100 offset:4096\n\t"
        "ds_read_b64_tr_b16 v[246:247], v100 offset:4352\n\t"
        "s_waitcnt lgkmcnt(8)\n\t"
        "ds_read_b64_tr_b16 v[248:249], v101 offset:0\n\t"
        "ds_read_b64_tr_b16 v[250:251], v101 offset:256\n\t"
        "ds_read_b64_tr_b16 v[252:253], v101 offset:4096\n\t"
        "ds_read_b64_tr_b16 v[254:255], v101 offset:4352\n\t"
        "s_waitcnt lgkmcnt(8)\n\t"
        "ds_read_b64_tr_b16 v[104:105], v102 offset:0\n\t"
        "ds_read_b64_tr_b16 v[106:107], v102 offset:256\n\t"
        "ds_read_b64_tr_b16 v[108:109], v102 offset:4096\n\t"
        "ds_read_b64_tr_b16 v[110:111], v102 offset:4352\n\t"
        "s_mov_b32 s42, 0\n\t"
        "LA_cal%=:\n\t"
        "v_readfirstlane_b32 s34, v0\n\t"
        "s_nop 0\n\t"
        "s_cmp_lg_u32 s34, 0\n\t"
        "s_cbranch_scc1 LA_calok%=\n\t"
        "s_sleep 4\n\t"
        "s_mov_b32 s35, 33792\n\t"
        "buffer_load_dword v0, off, s[12:15], s35 sc1\n\t"
        "s_waitcnt vmcnt(0)\n\t"
        "s_add_u32 s42, s42, 1\n\t"
        "s_cmp_lt_u32 s42, 0x100000\n\t"
        "s_cbranch_scc1 LA_cal%=\n\t"
        "s_movk_i32 s34, 8\n\t"
        "s_branch LA_die%=\n\t"
        "LA_calok%=:\n\t"
        "s_lshl_b32 s35, s44, 10\n\t"
        "v_lshlrev_b32_e32 v1, 2, v122\n\t"
        "v_and_b32_e32 v1, 0x3c0, v1\n\t"
        "buffer_load_dword v1, v1, s[12:15], s35 offen offset:0 sc1\n\t"
        "s_waitcnt lgkmcnt(0)\n\t"
        "ds_write_b128 v126, v[104:107] offset:8192\n\t"
        "ds_write_b128 v126, v[108:111] offset:9216\n\t"
        "s_waitcnt lgkmcnt(0)\n\t"
        "v_mov_b32_e32 v110, v103\n\t"
        "ds_read_b128 v[64:67], v126 offset:0\n\t"
        "ds_read_b128 v[68:71], v126 offset:1024\n\t"
        "ds_read_b128 v[72:75], v126 offset:2048\n\t"
        "ds_read_b128 v[76:79], v126 offset:3072\n\t"
        "ds_read_b128 v[80:83], v126 offset:4096\n\t"
        "ds_read_b128 v[84:87], v126 offset:5120\n\t"
        "ds_read_b128 v[88:91], v126 offset:6144\n\t"
        "ds_read_b128 v[92:95], v126 offset:7168\n\t"
        "ds_read_b128 v[96:99], v126 offset:8192\n\t"
        "ds_read_b128 v[100:103], v126 offset:9216\n\t"
        "s_waitcnt lgkmcnt(0)\n\t"
        "v_mov_b32_e32 v126, 0\n\t"
        "s_barrier\n\t"
        "s_mov_b32 s48, 0\n\t"
        "s_mov_b32 s54, 0\n\t"
        "s_mov_b32 s0, 0x3f803f80\n\t"
        "s_mov_b32 s1, s0\n\t"
        "s_movk_i32 s56, 24576\n\t"
        "s_and_b32 s58, s43, 1\n\t"
        "s_lshl_b32 s58, s58, 12\n\t"
        "s_lshl_b32 s57, s44, 5\n\t"
        "s_add_u32 s57, s57, s43\n\t"
        "s_lshl_b32 s57, s57, 2\n\t"
        "s_add_u32 s57, s57, 458752\n\t"
        "s_waitcnt vmcnt(0)\n\t"
        "v_mov_b32_e32 v111, v1\n\t"
        "v_readfirstlane_b32 s34, v0\n\t"
        "s_nop 0\n\t"
        "s_and_b32 s34, s34, 8192\n\t"
        "s_add_u32 s8, s8, s34\n\t"
        "s_addc_u32 s9, s9, 0\n\t"
        "s_barrier\n\t"
        "s_cmp_lg_u32 s43, 0\n\t"
        "s_cbranch_scc1 LA_st%=\n\t"
        "v_mov_b32_e32 v2, 1\n\t"
        "s_mov_b32 s35, 34304\n\t"
        "s_mov_b64 exec, 1\n\t"
        "buffer_atomic_add v2, off, s[12:15], s35 sc0\n\t"
        "s_mov_b64 exec, -1\n\t"
        "s_waitcnt vmcnt(0)\n\t"
        "v_readfirstlane_b32 s34, v2\n\t"
        "s_nop 0\n\t"
        "s_cmp_lg_u32 s34, 95\n\t"
        "s_cbranch_scc1 LA_st%=\n\t"
        "s_memrealtime s[32:33]\n\t"
        "v_mbcnt_lo_u32_b32 v3, -1, 0\n\t"
        "v_lshlrev_b32_e32 v3, 7, v3\n\t"
        "s_waitcnt lgkmcnt(0)\n\t"
        "s_add_u32 s34, s32, 50\n\t"
        "s_or_b32 s34, s34, 1\n\t"
        "v_mov_b32_e32 v2, s34\n\t"
        "s_mov_b32 s35, 36864\n\t"
        "s_mov_b64 exec, 0xffff\n\t"
        "buffer_store_dword v2, v3, s[12:15], s35 offen sc1\n\t"
        "s_mov_b64 exec, -1\n\t"
        "LA_st%=:\n\t"
        "s_mov_b32 s24, 0\n\t"
        "s_mov_b32 s26, 0\n\t"
        "s_mov_b32 s27, 0x80000\n\t"
        "s_mov_b32 s28, 0x180000\n\t"
        "s_mov_b32 s31, 0\n\t"
        "s_mov_b32 s47, 0\n\t"
        "s_mov_b32 s38, 0xff00ff00\n\t"
        "s_mov_b32 s39, 0xff00ff00\n\t"
        "s_mov_b32 s40, 0\n\t"
        "s_mov_b32 s41, -1\n\t"
        "ds_read_b128 v[32:35], v117 offset:0\n\t"
        "ds_read_b128 v[36:39], v117 offset:1024\n\t"
        "ds_read_b128 v[40:43], v117 offset:2048\n\t"
        "ds_read_b128 v[44:47], v117 offset:3072\n\t"
        "ds_read_b128 v[48:51], v117 offset:4096\n\t"
        "ds_read_b128 v[52:55], v117 offset:5120\n\t"
        "ds_read_b128 v[56:59], v117 offset:6144\n\t"
        "ds_read_b128 v[60:63], v117 offset:7168\n\t"
        "v_mov_b64_e32 v[0:1], 0\n\t"
        "v_mov_b64_e32 v[2:3], 0\n\t"
        "v_mov_b64_e32 v[4:5], 0\n\t"
        "v_mov_b64_e32 v[6:7], 0\n\t"
        "v_mov_b64_e32 v[8:9], 0\n\t"
        "v_mov_b64_e32 v[10:11], 0\n\t"
        "v_mov_b64_e32 v[12:13], 0\n\t"
        "v_mov_b64_e32 v[14:15], 0\n\t"
        "s_lshr_b32 s59, s48, 1\n\t"
        "s_cmp_gt_u32 s54, s59\n\t"
        "s_cbranch_scc1 LA_kv0ok%=\n\t"
        "s_mov_b32 s42, 0\n\t"
        "s_waitcnt vmcnt(0)\n\t"
        "s_branch LA_kv0w%=\n\t"
        "LA_kv0%=:\n\t"
        "s_waitcnt vmcnt(0)\n\t"
        "LA_kv0w%=:\n\t"
        "v_min_u32_dpp v111, v111, v111 quad_perm:[1,0,3,2] row_mask:0xf bank_mask:0xf\n\t"
        "s_nop 1\n\t"
        "v_min_u32_dpp v111, v111, v111 quad_perm:[2,3,0,1] row_mask:0xf bank_mask:0xf\n\t"
        "s_nop 1\n\t"
        "v_min_u32_dpp v111, v111, v111 row_ror:4 row_mask:0xf bank_mask:0xf\n\t"
        "s_nop 1\n\t"
        "v_min_u32_dpp v111, v111, v111 row_ror:8 row_mask:0xf bank_mask:0xf\n\t"
        "s_nop 0\n\t"
        "v_readfirstlane_b32 s54, v111\n\t"
        "s_nop 0\n\t"
        "s_cmp_gt_u32 s54, s59\n\t"
        "s_cbranch_scc1 LA_kv0ok%=\n\t"
        "s_sleep 4\n\t"
        "s_lshl_b32 s35, s44, 10\n\t"
        "v_lshlrev_b32_e32 v111, 2, v122\n\t"
        "v_and_b32_e32 v111, 0x3c0, v111\n\t"
        "buffer_load_dword v111, v111, s[12:15], s35 offen offset:0 sc1\n\t"
        "s_add_u32 s42, s42, 1\n\t"
        "s_cmp_lt_u32 s42, 0x100000\n\t"
        "s_cbranch_scc1 LA_kv0%=\n\t"
        "s_movk_i32 s34, 16\n\t"
        "s_branch LA_die%=\n\t"
        "LA_kv0ok%=:\n\t"
        "s_and_b32 s36, s48, 1\n\t"
        "s_mul_i32 s36, s36, 24576\n\t"
        "s_add_u32 s36, s36, s55\n\t"
        "s_lshl_b32 s37, s48, 18\n\t"
        "s_lshl_b32 s34, s43, 11\n\t"
        "s_add_u32 s37, s37, s34\n\t"
        "s_add_u32 m0, s36, 0\n\t"
        "s_add_u32 s34, s37, 29548544\n\t"
        "buffer_load_dwordx4 v122, s[12:15], s34 offen sc1 lds\n\t"
        "s_add_u32 m0, s36, 1024\n\t"
        "s_add_u32 s34, s37, 29549568\n\t"
        "buffer_load_dwordx4 v122, s[12:15], s34 offen sc1 lds\n\t"
        "s_add_u32 m0, s36, 8192\n\t"
        "s_add_u32 s34, s37, 12771328\n\t"
        "buffer_load_dwordx4 v122, s[12:15], s34 offen sc1 lds\n\t"
        "s_add_u32 m0, s36, 9216\n\t"
        "s_add_u32 s34, s37, 12772352\n\t"
        "buffer_load_dwordx4 v122, s[12:15], s34 offen sc1 lds\n\t"
        "s_add_u32 m0, s36, 16384\n\t"
        "s_add_u32 s34, s37, 46325760\n\t"
        "buffer_load_dwordx4 v122, s[12:15], s34 offen sc1 lds\n\t"
        "s_add_u32 m0, s36, 17408\n\t"
        "s_add_u32 s34, s37, 46326784\n\t"
        "buffer_load_dwordx4 v122, s[12:15], s34 offen sc1 lds\n\t"
        "s_mov_b32 s42, 0\n\t"
        "LA_gw%=:\n\t"
        "buffer_load_dword v16, off, s[12:15], s45 sc1\n\t"
        "s_waitcnt vmcnt(0)\n\t"
        "v_readfirstlane_b32 s25, v16\n\t"
        "s_nop 0\n\t"
        "s_cmp_lg_u32 s25, 0\n\t"
        "s_cbranch_scc1 LA_loop%=\n\t"
        "s_sleep 2\n\t"
        "s_add_u32 s42, s42, 1\n\t"
        "s_cmp_lt_u32 s42, 0x100000\n\t"
        "s_cbranch_scc1 LA_gw%=\n\t"
        "s_movk_i32 s34, 9\n\t"
        "s_branch LA_die%=\n\t"
        ".p2align 8\n\t"
        "LA_loop%=:\n\t"
        "s_mov_b32 s42, 0\n\t"
        "s_sub_u32 s35, s25, 8\n\t"
        "s_waitcnt lgkmcnt(0)\n\t"
        "s_memrealtime s[2:3]\n\t"
        "s_memrealtime s[4:5]\n\t"
        "s_memrealtime s[6:7]\n\t"
        "LA_wa%=:\n\t"
        "s_memrealtime s[32:33]\n\t"
        "s_waitcnt lgkmcnt(6)\n\t"
        "s_sub_u32 s34, s35, s2\n\t"
        "s_cmpk_lt_u32 s34, 0x2000\n\t"
        "s_cbranch_scc0 LA_wa%=x\n\t"
        "s_memrealtime s[2:3]\n\t"
        "s_waitcnt lgkmcnt(6)\n\t"
        "s_sub_u32 s34, s35, s4\n\t"
        "s_cmpk_lt_u32 s34, 0x2000\n\t"
        "s_cbranch_scc0 LA_wa%=x\n\t"
        "s_memrealtime s[4:5]\n\t"
        "s_waitcnt lgkmcnt(6)\n\t"
        "s_sub_u32 s34, s35, s6\n\t"
        "s_cmpk_lt_u32 s34, 0x2000\n\t"
        "s_cbranch_scc0 LA_wa%=x\n\t"
        "s_memrealtime s[6:7]\n\t"
        "s_waitcnt lgkmcnt(6)\n\t"
        "s_sub_u32 s34, s35, s32\n\t"
        "s_cmpk_lt_u32 s34, 0x2000\n\t"
        "s_cbranch_scc1 LA_wa%=\n\t"
        "s_nop 3\n\t"
        "LA_wa%=x:\n\t"
        "s_add_i32 s34, s34, 4\n\t"
        "s_max_i32 s34, s34, 0\n\t"
        "s_bitcmp1_b32 s34, 1\n\t"
        "s_cbranch_scc0 LA_wa%=y\n\t"
        "s_nop 15\n\t"
        "LA_wa%=y:\n\t"
        "s_bitcmp1_b32 s34, 0\n\t"
        "s_cbranch_scc0 LA_wa%=z\n\t"
        "s_nop 9\n\t"
        "LA_wa%=z:\n\t"
        "LA_rd%=:\n\t"
        "buffer_load_dwordx4 v[16:19], v112, s[8:11], s26 offen offset:0 sc1\n\t"
        "s_nop 12\n\t"
        "buffer_load_dwordx4 v[20:23], v112, s[8:11], s26 offen offset:1024 sc1\n\t"
        "s_nop 12\n\t"
        "buffer_load_dwordx4 v[24:27], v112, s[8:11], s26 offen offset:2048 sc1\n\t"
        "s_nop 12\n\t"
        "buffer_load_dwordx4 v[28:31], v112, s[8:11], s26 offen offset:3072 sc1\n\t"
        "LA_go%=:\n\t"
        "s_waitcnt vmcnt(3)\n\t"
        "v_smfmac_f32_16x16x64_bf16 v[0:3], v[16:19], a[0:7], v110\n\t"
        "ds_write_b128 v113, v[16:19] offset:0\n\t"
        "v_smfmac_f32_16x16x64_bf16 v[4:7], v[16:19], a[8:15], v110\n\t"
        "v_max3_u32 v111, v16, v17, v18\n\t"
        "v_max_u32_e32 v111, v19, v111\n\t"
        "v_smfmac_f32_16x16x64_bf16 v[8:11], v[16:19], a[16:23], v110\n\t"
        "v_smfmac_f32_16x16x64_bf16 v[12:15], v[16:19], v[32:39], v110\n\t"
        "s_waitcnt vmcnt(2)\n\t"
        "v_smfmac_f32_16x16x64_bf16 v[0:3], v[20:23], a[24:31], v110\n\t"
        "ds_write_b128 v113, v[20:23] offset:1024\n\t"
        "v_smfmac_f32_16x16x64_bf16 v[4:7], v[20:23], a[32:39], v110\n\t"
        "v_max3_u32 v111, v20, v21, v111\n\t"
        "v_max3_u32 v111, v22, v23, v111\n\t"
        "v_smfmac_f32_16x16x64_bf16 v[8:11], v[20:23], a[40:47], v110\n\t"
        "s_waitcnt vmcnt(1)\n\t"
        "ds_write_b128 v113, v[24:27] offset:2048\n\t"
        "v_smfmac_f32_16x16x64_bf16 v[12:15], v[20:23], v[40:47], v110\n\t"
        "v_max3_u32 v111, v24, v25, v111\n\t"
        "v_max3_u32 v111, v26, v27, v111\n\t"
        "v_smfmac_f32_16x16x64_bf16 v[0:3], v[24:27], a[48:55], v110\n\t"
        "v_smfmac_f32_16x16x64_bf16 v[4:7], v[24:27], a[56:63], v110\n\t"
        "s_waitcnt vmcnt(0)\n\t"
        "ds_write_b128 v113, v[28:31] offset:3072\n\t"
        "v_max3_u32 v111, v28, v29, v111\n\t"
        "v_max3_u32 v111, v30, v31, v111\n\t"
        "v_smfmac_f32_16x16x64_bf16 v[8:11], v[24:27], a[64:71], v110\n\t"
        "v_cmp_eq_u32_e32 vcc, -1, v111\n\t"
        "v_smfmac_f32_16x16x64_bf16 v[12:15], v[24:27], v[48:55], v110\n\t"
        "s_waitcnt lgkmcnt(0)\n\t"
        "s_cbranch_vccnz LA_rdfail%=\n\t"
        "s_barrier\n\t"
        "ds_read_b128 v[16:19], v114 offset:0\n\t"
        "ds_read_b128 v[20:23], v114 offset:1024\n\t"
        "ds_read_b128 v[24:27], v114 offset:2048\n\t"
        "v_smfmac_f32_16x16x64_bf16 v[0:3], v[28:31], a[72:79], v110\n\t"
        "v_smfmac_f32_16x16x64_bf16 v[4:7], v[28:31], a[80:87], v110\n\t"
        "v_smfmac_f32_16x16x64_bf16 v[8:11], v[28:31], a[88:95], v110\n\t"
        "v_smfmac_f32_16x16x64_bf16 v[12:15], v[28:31], v[56:63], v110\n\t"
        "ds_read_b128 v[28:31], v114 offset:3072\n\t"
        "s_waitcnt lgkmcnt(3)\n\t"
        "v_smfmac_f32_16x16x64_bf16 v[12:15], v[16:19], v[64:71], v110\n\t"
        "ds_read_b128 v[32:35], v117 offset:8192\n\t"
        "v_smfmac_f32_16x16x64_bf16 v[0:3], v[16:19], a[96:103], v110\n\t"
        "ds_read_b128 v[36:39], v117 offset:9216\n\t"
        "v_smfmac_f32_16x16x64_bf16 v[4:7], v[16:19], a[104:111], v110\n\t"
        "s_waitcnt lgkmcnt(4)\n\t"
        "v_smfmac_f32_16x16x64_bf16 v[8:11], v[16:19], a[112:119], v110\n\t"
        "ds_read_b128 v[16:19], v115 offset:0\n\t"
        "v_smfmac_f32_16x16x64_bf16 v[12:15], v[20:23], v[72:79], v110\n\t"
        "ds_read_b128 v[40:43], v117 offset:10240\n\t"
        "v_smfmac_f32_16x16x64_bf16 v[0:3], v[20:23], a[120:127], v110\n\t"
        "ds_read_b128 v[44:47], v117 offset:11264\n\t"
        "v_smfmac_f32_16x16x64_bf16 v[4:7], v[20:23], a[128:135], v110\n\t"
        "s_waitcnt lgkmcnt(6)\n\t"
        "v_smfmac_f32_16x16x64_bf16 v[8:11], v[20:23], a[136:143], v110\n\t"
        "ds_read_b128 v[20:23], v115 offset:1024\n\t"
        "v_smfmac_f32_16x16x64_bf16 v[12:15], v[24:27], v[80:87], v110\n\t"
        "ds_read_b128 v[48:51], v117 offset:12288\n\t"
        "v_smfmac_f32_16x16x64_bf16 v[0:3], v[24:27], a[144:151], v110\n\t"
        "ds_read_b128 v[52:55], v117 offset:13312\n\t"
        "v_smfmac_f32_16x16x64_bf16 v[4:7], v[24:27], a[152:159], v110\n\t"
        "s_waitcnt lgkmcnt(8)\n\t"
        "v_smfmac_f32_16x16x64_bf16 v[8:11], v[24:27], a[160:167], v110\n\t"
        "ds_read_b128 v[24:27], v115 offset:2048\n\t"
        "v_smfmac_f32_16x16x64_bf16 v[12:15], v[28:31], v[88:95], v110\n\t"
        "ds_read_b128 v[56:59], v117 offset:14336\n\t"
        "v_smfmac_f32_16x16x64_bf16 v[0:3], v[28:31], a[168:175], v110\n\t"
        "ds_read_b128 v[60:63], v117 offset:15360\n\t"
        "v_smfmac_f32_16x16x64_bf16 v[4:7], v[28:31], a[176:183], v110\n\t"
        "s_waitcnt lgkmcnt(8)\n\t"
        "v_smfmac_f32_16x16x64_bf16 v[8:11], v[28:31], a[184:191], v110\n\t"
        "ds_read_b128 v[28:31], v115 offset:3072\n\t"
        "v_smfmac_f32_16x16x64_bf16 v[12:15], v[16:19], v[32:39], v110\n\t"
        "ds_read_b128 v[32:35], v117 offset:16384\n\t"
        "v_smfmac_f32_16x16x64_bf16 v[0:3], v[16:19], a[192:199], v110\n\t"
        "ds_read_b128 v[36:39], v117 offset:17408\n\t"
        "v_smfmac_f32_16x16x64_bf16 v[4:7], v[16:19], a[200:207], v110\n\t"
        "s_waitcnt lgkmcnt(8)\n\t"
        "v_smfmac_f32_16x16x64_bf16 v[8:11], v[16:19], a[208:215], v110\n\t"
        "ds_read_b128 v[16:19], v116 offset:0\n\t"
        "v_smfmac_f32_16x16x64_bf16 v[12:15], v[20:23], v[40:47], v110\n\t"
        "ds_read_b128 v[40:43], v117 offset:18432\n\t"
        "v_smfmac_f32_16x16x64_bf16 v[0:3], v[20:23], a[216:223], v110\n\t"
        "ds_read_b128 v[44:47], v117 offset:19456\n\t"
        "v_smfmac_f32_16x16x64_bf16 v[4:7], v[20:23], a[224:231], v110\n\t"
        "s_waitcnt lgkmcnt(8)\n\t"
        "v_smfmac_f32_16x16x64_bf16 v[8:11], v[20:23], a[232:239], v110\n\t"
        "ds_read_b128 v[20:23], v116 offset:1024\n\t"
        "v_smfmac_f32_16x16x64_bf16 v[12:15], v[24:27], v[48:55], v110\n\t"
        "ds_read_b128 v[48:51], v117 offset:20480\n\t"
        "v_smfmac_f32_16x16x64_bf16 v[0:3], v[24:27], a[240:247], v110\n\t"
        "ds_read_b128 v[52:55], v117 offset:21504\n\t"
        "v_smfmac_f32_16x16x64_bf16 v[4:7], v[24:27], a[248:255], v110\n\t"
        "s_waitcnt lgkmcnt(8)\n\t"
        "v_smfmac_f32_16x16x64_bf16 v[8:11], v[24:27], v[128:135], v110\n\t"
        "ds_read_b128 v[24:27], v116 offset:2048\n\t"
        "v_smfmac_f32_16x16x64_bf16 v[12:15], v[28:31], v[56:63], v110\n\t"
        "ds_read_b128 v[56:59], v124 offset:3072\n\t"
        "v_smfmac_f32_16x16x64_bf16 v[0:3], v[28:31], v[136:143], v110\n\t"
        "ds_read_b128 v[60:63], v124 offset:7168\n\t"
        "v_smfmac_f32_16x16x64_bf16 v[4:7], v[28:31], v[144:151], v110\n\t"
        "s_waitcnt lgkmcnt(8)\n\t"
        "v_smfmac_f32_16x16x64_bf16 v[8:11], v[28:31], v[152:159], v110\n\t"
        "ds_read_b128 v[28:31], v116 offset:3072\n\t"
        "v_smfmac_f32_16x16x64_bf16 v[12:15], v[16:19], v[32:39], v110\n\t"
        "ds_read_b128 v[32:35], v124 offset:0\n\t"
        "v_smfmac_f32_16x16x64_bf16 v[0:3], v[16:19], v[160:167], v110\n\t"
        "ds_read_b128 v[36:39], v124 offset:4096\n\t"
        "v_smfmac_f32_16x16x64_bf16 v[4:7], v[16:19], v[168:175], v110\n\t"
        "s_waitcnt lgkmcnt(8)\n\t"
        "v_smfmac_f32_16x16x64_bf16 v[8:11], v[16:19], v[176:183], v110\n\t"
        "v_smfmac_f32_16x16x64_bf16 v[12:15], v[20:23], v[40:47], v110\n\t"
        "ds_read_b128 v[40:43], v124 offset:1024\n\t"
        "v_smfmac_f32_16x16x64_bf16 v[8:11], v[20:23], v[200:207], v110\n\t"
        "ds_read_b128 v[44:47], v124 offset:5120\n\t"
        "s_waitcnt lgkmcnt(7)\n\t"
        "v_smfmac_f32_16x16x64_bf16 v[12:15], v[24:27], v[48:55], v110\n\t"
        "ds_read_b128 v[48:51], v124 offset:2048\n\t"
        "v_smfmac_f32_16x16x64_bf16 v[8:11], v[24:27], v[224:231], v110\n\t"
        "ds_read_b128 v[52:55], v124 offset:6144\n\t"
        "s_waitcnt lgkmcnt(6)\n\t"
        "v_smfmac_f32_16x16x64_bf16 v[12:15], v[28:31], v[96:103], v110\n\t"
        "v_smfmac_f32_16x16x64_bf16 v[8:11], v[28:31], v[248:255], v110\n\t"
        "v_smfmac_f32_16x16x64_bf16 v[0:3], v[20:23], v[184:191], v110\n\t"
        "s_lshl_b32 s34, s43, 11\n\t"
        "v_add_u32_e32 v108, s34, v124\n\t"
        "v_smfmac_f32_16x16x64_bf16 v[0:3], v[24:27], v[208:215], v110\n\t"
        "v_add_u32_e32 v109, s58, v124\n\t"
        "v_smfmac_f32_16x16x64_bf16 v[4:7], v[20:23], v[192:199], v110\n\t"
        "v_smfmac_f32_16x16x64_bf16 v[0:3], v[28:31], v[232:239], v110\n\t"
        "v_smfmac_f32_16x16x64_bf16 v[4:7], v[24:27], v[216:223], v110\n\t"
        "buffer_load_dwordx4 v[104:107], v120, s[8:11], s26 offen sc1\n\t"
        "v_smfmac_f32_16x16x64_bf16 v[4:7], v[28:31], v[240:247], v110\n\t"
        "v_add_f32_e32 v12, v12, v13\n\t"
        "v_add_f32_e32 v14, v14, v15\n\t"
        "v_cvt_pk_bf16_f32 v12, v12, v14\n\t"
        "v_add_f32_e32 v8, v8, v9\n\t"
        "v_add_f32_e32 v10, v10, v11\n\t"
        "v_cvt_pk_bf16_f32 v8, v8, v10\n\t"
        "ds_write2_b32 v118, v8, v12 offset0:64 offset1:96\n\t"
        "v_add_f32_e32 v0, v0, v1\n\t"
        "v_add_f32_e32 v2, v2, v3\n\t"
        "v_cvt_pk_bf16_f32 v0, v0, v2\n\t"
        "v_add_f32_e32 v4, v4, v5\n\t"
        "v_add_f32_e32 v6, v6, v7\n\t"
        "v_cvt_pk_bf16_f32 v4, v4, v6\n\t"
        "ds_write2_b32 v118, v0, v4 offset0:0 offset1:32\n\t"
        "s_cmp_eq_u32 s24, 63\n\t"
        "s_waitcnt lgkmcnt(0)\n\t"
        "s_barrier\n\t"
        "s_cbranch_scc1 LA_skip%=\n\t"
        "ds_read_b64_tr_b16 v[16:17], v119 offset:0\n\t"
        "ds_read_b64_tr_b16 v[18:19], v119 offset:32\n\t"
        "ds_read_b64_tr_b16 v[20:21], v119 offset:256\n\t"
        "ds_read_b64_tr_b16 v[22:23], v119 offset:288\n\t"
        "ds_read_b64_tr_b16 v[24:25], v119 offset:512\n\t"
        "ds_read_b64_tr_b16 v[26:27], v119 offset:544\n\t"
        "ds_read_b64_tr_b16 v[28:29], v119 offset:768\n\t"
        "ds_read_b64_tr_b16 v[30:31], v119 offset:800\n\t"
        "buffer_store_dword v126, v127, s[12:15], 0 offen sc1\n\t"
        "s_waitcnt lgkmcnt(6)\n\t"
        "v_mfma_f32_16x16x32_bf16 v[0:3], v[32:35], v[16:19], 0\n\t"
        "ds_read_b64_tr_b16 v[32:33], v119 offset:2112\n\t"
        "ds_read_b64_tr_b16 v[34:35], v119 offset:2144\n\t"
        "v_mfma_f32_16x16x32_bf16 v[4:7], v[36:39], v[16:19], 0\n\t"
        "ds_read_b64_tr_b16 v[36:37], v119 offset:2368\n\t"
        "ds_read_b64_tr_b16 v[38:39], v119 offset:2400\n\t"
        "s_waitcnt lgkmcnt(8)\n\t"
        "v_mfma_f32_16x16x32_bf16 v[0:3], v[40:43], v[20:23], v[0:3]\n\t"
        "ds_read_b64_tr_b16 v[40:41], v119 offset:2624\n\t"
        "ds_read_b64_tr_b16 v[42:43], v119 offset:2656\n\t"
        "v_mfma_f32_16x16x32_bf16 v[4:7], v[44:47], v[20:23], v[4:7]\n\t"
        "ds_read_b64_tr_b16 v[44:45], v119 offset:2880\n\t"
        "ds_read_b64_tr_b16 v[46:47], v119 offset:2912\n\t"
        "s_waitcnt lgkmcnt(10)\n\t"
        "v_mfma_f32_16x16x32_bf16 v[0:3], v[48:51], v[24:27], v[0:3]\n\t"
        "ds_read_b128 v[48:51], v108 offset:16384\n\t"
        "v_mfma_f32_16x16x32_bf16 v[4:7], v[52:55], v[24:27], v[4:7]\n\t"
        "ds_read_b128 v[52:55], v108 offset:17408\n\t"
        "s_waitcnt lgkmcnt(10)\n\t"
        "v_mfma_f32_16x16x32_bf16 v[0:3], v[56:59], v[28:31], v[0:3]\n\t"
        "v_mfma_f32_16x16x32_bf16 v[4:7], v[60:63], v[28:31], v[4:7]\n\t"
        "s_waitcnt lgkmcnt(2)\n\t"
        "v_mfma_f32_16x16x32_bf16 v[8:11], v[32:35], v[16:19], 0\n\t"
        "s_nop 1\n\t"
        "v_mfma_f32_16x16x32_bf16 v[8:11], v[36:39], v[20:23], v[8:11]\n\t"
        "v_mov_b64_e32 v[16:17], s[0:1]\n\t"
        "v_mov_b64_e32 v[18:19], s[0:1]\n\t"
        "v_mfma_f32_16x16x32_bf16 v[8:11], v[40:43], v[24:27], v[8:11]\n\t"
        "v_max3_f32 v59, v0, v1, v2\n\t"
        "v_max3_f32 v59, v59, v3, v4\n\t"
        "v_mfma_f32_16x16x32_bf16 v[8:11], v[44:47], v[28:31], v[8:11]\n\t"
        "v_max3_f32 v59, v59, v5, v6\n\t"
        "v_max_f32_e32 v60, v59, v7\n\t"
        "v_max_f32_e32 v59, v59, v7\n\t"
        "v_cndmask_b32_e64 v0, v0, v4, s[38:39]\n\t"
        "v_cndmask_b32_e64 v1, v1, v5, s[38:39]\n\t"
        "v_permlane32_swap_b32_e32 v59, v60\n\t"
        "v_cndmask_b32_e64 v2, v2, v6, s[38:39]\n\t"
        "v_cndmask_b32_e64 v3, v3, v7, s[38:39]\n\t"
        "v_max3_f32 v59, v59, v60, v8\n\t"
        "v_max3_f32 v59, v59, v9, v10\n\t"
        "v_max_f32_e32 v60, v59, v11\n\t"
        "v_max_f32_e32 v59, v59, v11\n\t"
        "v_cndmask_b32_e64 v8, v8, v9, s[38:39]\n\t"
        "v_cndmask_b32_e64 v10, v10, v11, s[38:39]\n\t"
        "v_permlane16_swap_b32_e32 v59, v60\n\t"
        "v_max_f32_e32 v59, v59, v60\n\t"
        "v_mul_f32_e32 v59, 0xbe02940c, v59\n\t"
        "v_cndmask_b32_e64 v58, v8, v10, s[40:41]\n\t"
        "v_fmamk_f32 v0, v0, 0x3e02940c, v59\n\t"
        "v_exp_f32_e32 v0, v0\n\t"
        "v_fmamk_f32 v1, v1, 0x3e02940c, v59\n\t"
        "v_exp_f32_e32 v1, v1\n\t"
        "v_fmamk_f32 v2, v2, 0x3e02940c, v59\n\t"
        "v_exp_f32_e32 v2, v2\n\t"
        "v_fmamk_f32 v3, v3, 0x3e02940c, v59\n\t"
        "v_exp_f32_e32 v3, v3\n\t"
        "v_fmamk_f32 v58, v58, 0x3e02940c, v59\n\t"
        "v_cvt_pk_bf16_f32 v12, v0, v1\n\t"
        "v_cvt_pk_bf16_f32 v13, v2, v3\n\t"
        "v_exp_f32_e32 v58, v58\n\t"
        "s_waitcnt lgkmcnt(0)\n\t"
        "v_mov_b32_dpp v14, v12 row_ror:8 row_mask:0xf bank_mask:0xf\n\t"
        "v_mov_b32_dpp v15, v13 row_ror:8 row_mask:0xf bank_mask:0xf\n\t"
        "v_mov_b32_dpp v59, v58 row_ror:8 row_mask:0xf bank_mask:0xf\n\t"
        "v_cvt_pk_bf16_f32 v59, v58, v59\n\t"
        "v_mfma_f32_16x16x32_bf16 v[20:23], v[16:19], v[12:15], 0\n\t"
        "v_cndmask_b32_e64 v56, v59, 0, s[40:41]\n\t"
        "v_cndmask_b32_e64 v57, 0, v59, s[40:41]\n\t"
        "v_mfma_f32_16x16x32_bf16 v[4:7], v[48:51], v[12:15], 0\n\t"
        "s_waitcnt vmcnt(1)\n\t"
        "v_max3_u32 v111, v104, v105, v106\n\t"
        "v_mfma_f32_16x16x32_bf16 v[8:11], v[52:55], v[12:15], 0\n\t"
        "v_max_u32_e32 v111, v111, v107\n\t"
        "v_mfma_f32_16x16x16_bf16 v[20:23], v[16:17], v[56:57], v[20:23]\n\t"
        "v_cmp_eq_u32_e32 vcc, -1, v111\n\t"
        "v_mfma_f32_16x16x16_bf16 v[4:7], v[104:105], v[56:57], v[4:7]\n\t"
        "s_nop 1\n\t"
        "v_mfma_f32_16x16x16_bf16 v[8:11], v[106:107], v[56:57], v[8:11]\n\t"
        "s_cbranch_vccnz LA_vmfail%=\n\t"
        "LA_vmok%=:\n\t"
        "s_nop 1\n\t"
        "v_rcp_f32_e32 v59, v20\n\t"
        "s_nop 0\n\t"
        "v_pk_mul_f32 v[4:5], v[4:5], v[58:59] op_sel:[0,1] op_sel_hi:[1,1]\n\t"
        "v_cvt_pk_bf16_f32 v0, v4, v5\n\t"
        "v_pk_mul_f32 v[6:7], v[6:7], v[58:59] op_sel:[0,1] op_sel_hi:[1,1]\n\t"
        "v_cvt_pk_bf16_f32 v1, v6, v7\n\t"
        "v_pk_mul_f32 v[8:9], v[8:9], v[58:59] op_sel:[0,1] op_sel_hi:[1,1]\n\t"
        "v_cvt_pk_bf16_f32 v2, v8, v9\n\t"
        "v_pk_mul_f32 v[10:11], v[10:11], v[58:59] op_sel:[0,1] op_sel_hi:[1,1]\n\t"
        "v_cvt_pk_bf16_f32 v3, v10, v11\n\t"
        "buffer_store_dwordx4 v[0:3], v121, s[8:11], s26 offen sc1\n\t"
        "LA_x%=:\n\t"
        "s_memrealtime s[52:53]\n\t"
        "ds_read_b128 v[16:19], v109 offset:8192\n\t"
        "ds_read_b128 v[20:23], v109 offset:9216\n\t"
        "ds_read_b128 v[24:27], v109 offset:10240\n\t"
        "ds_read_b128 v[28:31], v109 offset:11264\n\t"
        "ds_read_b128 v[56:59], v124 offset:0\n\t"
        "ds_read_b128 v[60:63], v124 offset:4096\n\t"
        "ds_read_b128 v[12:15], v124 offset:1024\n\t"
        "v_xor_b32_e32 v126, 0x800, v108\n\t"
        "s_add_u32 s48, s24, 1\n\t"
        "s_min_u32 s48, s48, 63\n\t"
        "buffer_load_dwordx2 v[108:109], v125, s[8:11], s26 offen sc1\n\t"
        "s_lshl_b32 s35, s44, 10\n\t"
        "s_lshr_b32 s34, s48, 1\n\t"
        "s_add_u32 s34, s34, 1\n\t"
        "s_min_u32 s34, s34, 31\n\t"
        "s_cmp_gt_u32 s54, s34\n\t"
        "s_cselect_b32 s35, 0xf0000000, s35\n\t"
        "v_lshlrev_b32_e32 v111, 2, v122\n\t"
        "v_and_b32_e32 v111, 0x3c0, v111\n\t"
        "buffer_load_dword v111, v111, s[12:15], s35 offen offset:0 sc1\n\t"
        "s_waitcnt lgkmcnt(6)\n\t"
        "v_mfma_f32_16x16x32_bf16 v[8:11], v[32:35], v[16:19], 0\n\t"
        "ds_read_b128 v[32:35], v124 offset:5120\n\t"
        "s_waitcnt lgkmcnt(6)\n\t"
        "v_mfma_f32_16x16x32_bf16 v[8:11], v[36:39], v[20:23], v[8:11]\n\t"
        "ds_read_b128 v[36:39], v124 offset:2048\n\t"
        "s_waitcnt lgkmcnt(6)\n\t"
        "v_mfma_f32_16x16x32_bf16 v[8:11], v[40:43], v[24:27], v[8:11]\n\t"
        "ds_read_b128 v[40:43], v124 offset:6144\n\t"
        "s_waitcnt lgkmcnt(6)\n\t"
        "v_mfma_f32_16x16x32_bf16 v[8:11], v[44:47], v[28:31], v[8:11]\n\t"
        "ds_read_b128 v[44:47], v124 offset:3072\n\t"
        "s_add_u32 s35, s24, 1\n\t"
        "s_waitcnt lgkmcnt(6)\n\t"
        "v_mfma_f32_16x16x32_bf16 v[0:3], v[56:59], v[16:19], 0\n\t"
        "ds_read_b128 v[56:59], v124 offset:7168\n\t"
        "s_waitcnt lgkmcnt(6)\n\t"
        "v_mfma_f32_16x16x32_bf16 v[4:7], v[60:63], v[16:19], 0\n\t"
        "s_and_b32 s36, s35, 1\n\t"
        "s_mul_i32 s36, s36, 24576\n\t"
        "s_waitcnt lgkmcnt(5)\n\t"
        "v_mfma_f32_16x16x32_bf16 v[0:3], v[12:15], v[20:23], v[0:3]\n\t"
        "s_add_u32 s36, s36, s55\n\t"
        "s_lshl_b32 s37, s48, 18\n\t"
        "s_waitcnt lgkmcnt(4)\n\t"
        "v_mfma_f32_16x16x32_bf16 v[4:7], v[32:35], v[20:23], v[4:7]\n\t"
        "v_xor_b32_e32 v32, 0x200, v120\n\t"
        "buffer_load_dwordx4 v[12:15], v32, s[8:11], s26 offen sc1\n\t"
        "s_waitcnt lgkmcnt(3)\n\t"
        "v_mfma_f32_16x16x32_bf16 v[0:3], v[36:39], v[24:27], v[0:3]\n\t"
        "s_lshl_b32 s34, s43, 11\n\t"
        "s_add_u32 s37, s37, s34\n\t"
        "s_waitcnt lgkmcnt(2)\n\t"
        "v_mfma_f32_16x16x32_bf16 v[4:7], v[40:43], v[24:27], v[4:7]\n\t"
        "v_cvt_pk_bf16_f32 v8, 0, v8\n\t"
        "v_cvt_pk_bf16_f32 v9, 0, v9\n\t"
        "v_cvt_pk_bf16_f32 v10, 0, v10\n\t"
        "v_cvt_pk_bf16_f32 v11, 0, v11\n\t"
        "s_waitcnt lgkmcnt(1)\n\t"
        "v_mfma_f32_16x16x32_bf16 v[0:3], v[44:47], v[28:31], v[0:3]\n\t"
        "v_mul_f32_e32 v8, 0x3db504f3, v8\n\t"
        "v_mul_f32_e32 v9, 0x3db504f3, v9\n\t"
        "v_mul_f32_e32 v10, 0x3db504f3, v10\n\t"
        "v_mul_f32_e32 v11, 0x3db504f3, v11\n\t"
        "s_waitcnt lgkmcnt(0)\n\t"
        "v_mfma_f32_16x16x32_bf16 v[4:7], v[56:59], v[28:31], v[4:7]\n\t"
        "v_cvt_pk_bf16_f32 v8, 0, v8\n\t"
        "v_cvt_pk_bf16_f32 v9, 0, v9\n\t"
        "v_cvt_pk_bf16_f32 v10, 0, v10\n\t"
        "v_cvt_pk_bf16_f32 v11, 0, v11\n\t"
        "ds_read_b128 v[56:59], v126 offset:16384\n\t"
        "ds_read_b128 v[60:63], v126 offset:17408\n\t"
        "v_mov_b64_e32 v[36:37], -1\n\t"
        "v_mov_b64_e32 v[38:39], -1\n\t"
        "v_mov_b32_e32 v40, 0xce6e6b28\n\t"
        "v_mov_b32_e32 v46, 0x3db504f3\n\t"
        "v_mov_b32_e32 v47, 0x3fb8aa3b\n\t"
        "v_cvt_pk_bf16_f32 v0, 0, v0\n\t"
        "v_cvt_pk_bf16_f32 v1, 0, v1\n\t"
        "v_cvt_pk_bf16_f32 v2, 0, v2\n\t"
        "v_cvt_pk_bf16_f32 v3, 0, v3\n\t"
        "v_cvt_pk_bf16_f32 v4, 0, v4\n\t"
        "v_cvt_pk_bf16_f32 v5, 0, v5\n\t"
        "v_cvt_pk_bf16_f32 v6, 0, v6\n\t"
        "v_cvt_pk_bf16_f32 v7, 0, v7\n\t"
        "v_pk_mul_f32 v[0:1], v[0:1], v[46:47] op_sel_hi:[1,0]\n\t"
        "v_pk_mul_f32 v[2:3], v[2:3], v[46:47] op_sel_hi:[1,0]\n\t"
        "v_pk_mul_f32 v[4:5], v[4:5], v[46:47] op_sel_hi:[1,0]\n\t"
        "v_pk_mul_f32 v[6:7], v[6:7], v[46:47] op_sel_hi:[1,0]\n\t"
        "s_lshr_b32 s59, s48, 1\n\t"
        "s_cmp_gt_u32 s54, s59\n\t"
        "s_cbranch_scc1 LA_kvok%=\n\t"
        "s_mov_b32 s42, 0\n\t"
        "s_waitcnt vmcnt(1)\n\t"
        "s_branch LA_kvw%=\n\t"
        "LA_kv%=:\n\t"
        "s_waitcnt vmcnt(0)\n\t"
        "LA_kvw%=:\n\t"
        "v_min_u32_dpp v111, v111, v111 quad_perm:[1,0,3,2] row_mask:0xf bank_mask:0xf\n\t"
        "s_nop 1\n\t"
        "v_min_u32_dpp v111, v111, v111 quad_perm:[2,3,0,1] row_mask:0xf bank_mask:0xf\n\t"
        "s_nop 1\n\t"
        "v_min_u32_dpp v111, v111, v111 row_ror:4 row_mask:0xf bank_mask:0xf\n\t"
        "s_nop 1\n\t"
        "v_min_u32_dpp v111, v111, v111 row_ror:8 row_mask:0xf bank_mask:0xf\n\t"
        "s_nop 0\n\t"
        "v_readfirstlane_b32 s54, v111\n\t"
        "s_nop 0\n\t"
        "s_cmp_gt_u32 s54, s59\n\t"
        "s_cbranch_scc1 LA_kvok%=\n\t"
        "s_sleep 4\n\t"
        "s_lshl_b32 s35, s44, 10\n\t"
        "v_lshlrev_b32_e32 v111, 2, v122\n\t"
        "v_and_b32_e32 v111, 0x3c0, v111\n\t"
        "buffer_load_dword v111, v111, s[12:15], s35 offen offset:0 sc1\n\t"
        "s_add_u32 s42, s42, 1\n\t"
        "s_cmp_lt_u32 s42, 0x100000\n\t"
        "s_cbranch_scc1 LA_kv%=\n\t"
        "s_movk_i32 s34, 16\n\t"
        "s_branch LA_die%=\n\t"
        "LA_kvok%=:\n\t"
        "s_add_u32 m0, s36, 0\n\t"
        "s_add_u32 s34, s37, 29548544\n\t"
        "buffer_load_dwordx4 v122, s[12:15], s34 offen sc1 lds\n\t"
        "v_cvt_pk_bf16_f32 v0, 0, v0\n\t"
        "v_cvt_pk_bf16_f32 v1, 0, v1\n\t"
        "v_cvt_pk_bf16_f32 v2, 0, v2\n\t"
        "v_cvt_pk_bf16_f32 v3, 0, v3\n\t"
        "v_cvt_pk_bf16_f32 v4, 0, v4\n\t"
        "v_cvt_pk_bf16_f32 v5, 0, v5\n\t"
        "v_cvt_pk_bf16_f32 v6, 0, v6\n\t"
        "v_cvt_pk_bf16_f32 v7, 0, v7\n\t"
        "s_add_u32 m0, s36, 1024\n\t"
        "s_add_u32 s34, s37, 29549568\n\t"
        "buffer_load_dwordx4 v122, s[12:15], s34 offen sc1 lds\n\t"
        "v_cndmask_b32_e64 v0, v40, v0, s[60:61]\n\t"
        "v_cndmask_b32_e64 v1, v40, v1, s[62:63]\n\t"
        "v_cndmask_b32_e64 v2, v40, v2, s[64:65]\n\t"
        "v_cndmask_b32_e64 v3, v40, v3, s[66:67]\n\t"
        "v_cndmask_b32_e64 v4, v40, v4, s[68:69]\n\t"
        "v_cndmask_b32_e64 v5, v40, v5, s[70:71]\n\t"
        "v_cndmask_b32_e64 v6, v40, v6, s[72:73]\n\t"
        "v_cndmask_b32_e64 v7, v40, v7, s[74:75]\n\t"
        "s_add_u32 m0, s36, 8192\n\t"
        "s_add_u32 s34, s37, 12771328\n\t"
        "buffer_load_dwordx4 v122, s[12:15], s34 offen sc1 lds\n\t"
        "v_max3_f32 v42, v0, v1, v2\n\t"
        "v_max3_f32 v42, v42, v3, v4\n\t"
        "v_max3_f32 v42, v42, v5, v6\n\t"
        "v_max3_f32 v42, v42, v7, v8\n\t"
        "v_max3_f32 v42, v42, v9, v10\n\t"
        "v_mov_b32_e32 v43, v11\n\t"
        "v_max_f32_e32 v44, v42, v43\n\t"
        "v_max_f32_e32 v42, v42, v43\n\t"
        "s_pack_ll_b32_b16 s34, s52, s24\n\t"
        "v_mov_b32_e32 v40, s34\n\t"
        "s_add_u32 s35, s26, s57\n\t"
        "v_permlane16_swap_b32_e32 v42, v44\n\t"
        "v_max_f32_e32 v45, v42, v44\n\t"
        "v_max_f32_e32 v42, v42, v44\n\t"
        "s_mov_b64 exec, 1\n\t"
        "buffer_store_dword v40, off, s[8:11], s35 sc1\n\t"
        "s_mov_b64 exec, -1\n\t"
        "v_permlane32_swap_b32_e32 v42, v45\n\t"
        "v_max_f32_e32 v42, v42, v45\n\t"
        "v_mul_f32_e32 v42, 0xbfb8aa3b, v42\n\t"
        "v_pk_fma_f32 v[0:1], v[0:1], v[46:47], v[42:43] op_sel:[0,1,0] op_sel_hi:[1,1,0]\n\t"
        "s_add_u32 m0, s36, 9216\n\t"
        "s_add_u32 s34, s37, 12772352\n\t"
        "buffer_load_dwordx4 v122, s[12:15], s34 offen sc1 lds\n\t"
        "v_exp_f32_e32 v0, v0\n\t"
        "v_exp_f32_e32 v1, v1\n\t"
        "v_pk_fma_f32 v[2:3], v[2:3], v[46:47], v[42:43] op_sel:[0,1,0] op_sel_hi:[1,1,0]\n\t"
        "v_exp_f32_e32 v2, v2\n\t"
        "v_exp_f32_e32 v3, v3\n\t"
        "v_pk_fma_f32 v[4:5], v[4:5], v[46:47], v[42:43] op_sel:[0,1,0] op_sel_hi:[1,1,0]\n\t"
        "v_exp_f32_e32 v4, v4\n\t"
        "s_add_u32 m0, s36, 16384\n\t"
        "s_add_u32 s34, s37, 46325760\n\t"
        "buffer_load_dwordx4 v122, s[12:15], s34 offen sc1 lds\n\t"
        "v_exp_f32_e32 v5, v5\n\t"
        "v_pk_fma_f32 v[6:7], v[6:7], v[46:47], v[42:43] op_sel:[0,1,0] op_sel_hi:[1,1,0]\n\t"
        "v_exp_f32_e32 v6, v6\n\t"
        "v_exp_f32_e32 v7, v7\n\t"
        "v_pk_fma_f32 v[8:9], v[8:9], v[46:47], v[42:43] op_sel:[0,1,0] op_sel_hi:[1,1,0]\n\t"
        "v_exp_f32_e32 v8, v8\n\t"
        "v_exp_f32_e32 v9, v9\n\t"
        "s_add_u32 m0, s36, 17408\n\t"
        "s_add_u32 s34, s37, 46326784\n\t"
        "buffer_load_dwordx4 v122, s[12:15], s34 offen sc1 lds\n\t"
        "v_pk_fma_f32 v[10:11], v[10:11], v[46:47], v[42:43] op_sel:[0,1,0] op_sel_hi:[1,1,0]\n\t"
        "v_exp_f32_e32 v10, v10\n\t"
        "v_exp_f32_e32 v11, v11\n\t"
        "v_pk_add_f32 v[42:43], v[0:1], v[2:3]\n\t"
        "v_cndmask_b32_e64 v8, v8, 0, s[40:41]\n\t"
        "v_cndmask_b32_e64 v9, v9, 0, s[40:41]\n\t"
        "v_cndmask_b32_e64 v10, v10, 0, s[40:41]\n\t"
        "buffer_store_dwordx4 v[36:39], v121, s[8:11], s28 offen sc1\n\t"
        "v_cndmask_b32_e64 v11, v11, 0, s[40:41]\n\t"
        "v_pk_add_f32 v[42:43], v[42:43], v[4:5]\n\t"
        "v_pk_add_f32 v[42:43], v[42:43], v[6:7]\n\t"
        "v_pk_add_f32 v[42:43], v[42:43], v[8:9]\n\t"
        "v_pk_add_f32 v[42:43], v[42:43], v[10:11]\n\t"
        "v_add_f32_e32 v44, v42, v43\n\t"
        "v_add_f32_e32 v42, v42, v43\n\t"
        "s_lshl_b32 s34, s24, 18\n\t"
        "s_add_u32 s34, s34, 63102976\n\t"
        "v_permlane16_swap_b32_e32 v42, v44\n\t"
        "v_add_f32_e32 v45, v42, v44\n\t"
        "v_add_f32_e32 v42, v42, v44\n\t"
        "s_add_u32 s35, s24, 1\n\t"
        "v_mov_b32_e32 v126, s35\n\t"
        "v_permlane32_swap_b32_e32 v42, v45\n\t"
        "v_add_f32_e32 v42, v42, v45\n\t"
        "v_rcp_f32_e32 v42, v42\n\t"
        "s_waitcnt vmcnt(8) lgkmcnt(0)\n\t"
        "v_max3_u32 v45, v12, v13, v14\n\t"
        "v_max3_u32 v45, v45, v15, v104\n\t"
        "v_max3_u32 v45, v45, v105, v106\n\t"
        "v_max_u32_e32 v45, v45, v107\n\t"
        "v_cmp_eq_u32_e32 vcc, -1, v45\n\t"
        "s_nop 0\n\t"
        "s_cbranch_vccnz LA_v2fail%=\n\t"
        "LA_v2ok%=:\n\t"
        "v_and_b32_e32 v45, 0xff, v109\n\t"
        "v_cmp_ne_u32_e32 vcc, s24, v45\n\t"
        "s_nop 0\n\t"
        "s_cbranch_vccnz LA_stre%=\n\t"
        "LA_stok%=:\n\t"
        "v_pk_mul_f32 v[0:1], v[0:1], v[42:43] op_sel_hi:[1,0]\n\t"
        "v_subrev_u32_e32 v108, s25, v108\n\t"
        "v_min_u32_dpp v111, v111, v111 quad_perm:[1,0,3,2] row_mask:0xf bank_mask:0xf\n\t"
        "v_cvt_pk_bf16_f32 v32, v0, v1\n\t"
        "v_pk_mul_f32 v[2:3], v[2:3], v[42:43] op_sel_hi:[1,0]\n\t"
        "v_cvt_pk_bf16_f32 v33, v2, v3\n\t"
        "v_max_i32_dpp v108, v108, v108 quad_perm:[1,0,3,2] row_mask:0xf bank_mask:0xf\n\t"
        "v_min_u32_dpp v111, v111, v111 quad_perm:[2,3,0,1] row_mask:0xf bank_mask:0xf\n\t"
        "v_pk_mul_f32 v[4:5], v[4:5], v[42:43] op_sel_hi:[1,0]\n\t"
        "v_cvt_pk_bf16_f32 v34, v4, v5\n\t"
        "v_pk_mul_f32 v[6:7], v[6:7], v[42:43] op_sel_hi:[1,0]\n\t"
        "v_max_i32_dpp v108, v108, v108 quad_perm:[2,3,0,1] row_mask:0xf bank_mask:0xf\n\t"
        "v_min_u32_dpp v111, v111, v111 row_ror:4 row_mask:0xf bank_mask:0xf\n\t"
        "v_cvt_pk_bf16_f32 v35, v6, v7\n\t"
        "v_pk_mul_f32 v[8:9], v[8:9], v[42:43] op_sel_hi:[1,0]\n\t"
        "v_cvt_pk_bf16_f32 v36, v8, v9\n\t"
        "v_max_i32_dpp v108, v108, v108 row_ror:4 row_mask:0xf bank_mask:0xf\n\t"
        "v_min_u32_dpp v111, v111, v111 row_ror:8 row_mask:0xf bank_mask:0xf\n\t"
        "v_pk_mul_f32 v[10:11], v[10:11], v[42:43] op_sel_hi:[1,0]\n\t"
        "v_cvt_pk_bf16_f32 v37, v10, v11\n\t"
        "v_mfma_f32_16x16x32_bf16 v[16:19], v[48:51], v[32:35], 0\n\t"
        "v_max_i32_dpp v108, v108, v108 row_ror:8 row_mask:0xf bank_mask:0xf\n\t"
        "v_mfma_f32_16x16x32_bf16 v[20:23], v[52:55], v[32:35], 0\n\t"
        "v_mfma_f32_16x16x32_bf16 v[24:27], v[56:59], v[32:35], 0\n\t"
        "v_mfma_f32_16x16x32_bf16 v[28:31], v[60:63], v[32:35], 0\n\t"
        "v_max_i32_dpp v108, v108, v108 row_bcast:15 row_mask:0xa bank_mask:0xf\n\t"
        "v_xor_b32_e32 v45, 0x400, v123\n\t"
        "v_add_u32_e32 v124, s56, v124\n\t"
        "v_mfma_f32_16x16x16_bf16 v[16:19], v[104:105], v[36:37], v[16:19]\n\t"
        "v_max_i32_dpp v108, v108, v108 row_bcast:31 row_mask:0xc bank_mask:0xf\n\t"
        "v_mfma_f32_16x16x16_bf16 v[20:23], v[106:107], v[36:37], v[20:23]\n\t"
        "v_mfma_f32_16x16x16_bf16 v[24:27], v[12:13], v[36:37], v[24:27]\n\t"
        "v_mfma_f32_16x16x16_bf16 v[28:31], v[14:15], v[36:37], v[28:31]\n\t"
        "ds_read_b128 v[48:51], v117 offset:4096\n\t"
        "ds_read_b128 v[52:55], v117 offset:5120\n\t"
        "ds_read_b128 v[56:59], v117 offset:6144\n\t"
        "ds_read_b128 v[60:63], v117 offset:7168\n\t"
        "s_sub_u32 s56, 0, s56\n\t"
        "v_cvt_pk_bf16_f32 v0, v16, v17\n\t"
        "v_cvt_pk_bf16_f32 v1, v18, v19\n\t"
        "v_cvt_pk_bf16_f32 v2, v20, v21\n\t"
        "v_cvt_pk_bf16_f32 v3, v22, v23\n\t"
        "buffer_store_dwordx4 v[0:3], v123, s[12:15], s34 offen sc1\n\t"
        "v_cvt_pk_bf16_f32 v4, v24, v25\n\t"
        "v_cvt_pk_bf16_f32 v5, v26, v27\n\t"
        "v_cvt_pk_bf16_f32 v6, v28, v29\n\t"
        "v_cvt_pk_bf16_f32 v7, v30, v31\n\t"
        "buffer_store_dwordx4 v[4:7], v45, s[12:15], s34 offen sc1\n\t"
        "s_cmp_eq_u32 s24, 63\n\t"
        "s_cbranch_scc1 LA_last%=\n\t"
        "ds_read_b128 v[32:35], v117 offset:0\n\t"
        "ds_read_b128 v[36:39], v117 offset:1024\n\t"
        "ds_read_b128 v[40:43], v117 offset:2048\n\t"
        "ds_read_b128 v[44:47], v117 offset:3072\n\t"
        "v_mov_b64_e32 v[14:15], 0\n\t"
        "v_mov_b64_e32 v[12:13], 0\n\t"
        "v_mov_b64_e32 v[10:11], 0\n\t"
        "v_mov_b64_e32 v[8:9], 0\n\t"
        "v_mov_b64_e32 v[6:7], 0\n\t"
        "v_mov_b64_e32 v[4:5], 0\n\t"
        "v_mov_b64_e32 v[2:3], 0\n\t"
        "v_mov_b64_e32 v[0:1], 0\n\t"
        "v_readfirstlane_b32 s34, v111\n\t"
        "s_nop 0\n\t"
        "s_max_u32 s54, s54, s34\n\t"
        "v_readfirstlane_b32 s35, v109\n\t"
        "v_readlane_b32 s36, v108, 63\n\t"
        "s_bfe_u32 s29, s35, 0xc0008\n\t"
        "s_lshr_b32 s30, s35, 20\n\t"
        "s_add_u32 s35, s31, s50\n\t"
        "s_mul_i32 s37, s50, 3\n\t"
        "s_min_u32 s35, s35, s37\n\t"
        "s_add_i32 s34, s36, s51\n\t"
        "s_cmp_gt_i32 s34, 0\n\t"
        "s_cselect_b32 s31, s35, 0\n\t"
        "s_cselect_b32 s34, s34, 0\n\t"
        "s_add_u32 s34, s34, s31\n\t"
        "s_add_u32 s37, s25, s34\n\t"
        "s_add_u32 s45, s37, s29\n\t"
        "s_add_u32 s25, s45, s30\n\t"
        "s_add_u32 s24, s24, 1\n\t"
        "s_mov_b32 s28, s26\n\t"
        "s_mov_b32 s26, s27\n\t"
        "s_add_u32 s27, s27, 0x80000\n\t"
        "s_and_b32 s27, s27, 0x1fffff\n\t"
        "s_branch LA_loop%=\n\t"
        "LA_last%=:\n\t"
        "s_waitcnt vmcnt(0)\n\t"
        "s_barrier\n\t"
        "buffer_store_dword v126, v127, s[12:15], 0 offen sc1\n\t"
        "s_endpgm\n\t"
        "LA_skip%=:\n\t"
        "buffer_store_dword v126, v127, s[12:15], 0 offen sc1\n\t"
        "ds_read_b64_tr_b16 v[32:33], v119 offset:2112\n\t"
        "ds_read_b64_tr_b16 v[34:35], v119 offset:2144\n\t"
        "ds_read_b64_tr_b16 v[36:37], v119 offset:2368\n\t"
        "ds_read_b64_tr_b16 v[38:39], v119 offset:2400\n\t"
        "ds_read_b64_tr_b16 v[40:41], v119 offset:2624\n\t"
        "ds_read_b64_tr_b16 v[42:43], v119 offset:2656\n\t"
        "ds_read_b64_tr_b16 v[44:45], v119 offset:2880\n\t"
        "ds_read_b64_tr_b16 v[46:47], v119 offset:2912\n\t"
        "ds_read_b128 v[48:51], v108 offset:16384\n\t"
        "ds_read_b128 v[52:55], v108 offset:17408\n\t"
        "s_waitcnt lgkmcnt(0)\n\t"
        "s_branch LA_x%=\n\t"
        "LA_stre%=:\n\t"
        "s_mov_b32 s42, 0\n\t"
        "LA_str2%=:\n\t"
        "buffer_load_dwordx2 v[108:109], v125, s[8:11], s26 offen sc1\n\t"
        "s_waitcnt vmcnt(0)\n\t"
        "v_and_b32_e32 v45, 0xff, v109\n\t"
        "v_cmp_ne_u32_e32 vcc, s24, v45\n\t"
        "s_nop 0\n\t"
        "s_cbranch_vccz LA_stok%=\n\t"
        "s_add_u32 s42, s42, 1\n\t"
        "s_cmp_lt_u32 s42, 0x100000\n\t"
        "s_cbranch_scc1 LA_str2%=\n\t"
        "s_movk_i32 s34, 5\n\t"
        "s_branch LA_die%=\n\t"
        "LA_v2fail%=:\n\t"
        "s_mov_b32 s42, 0\n\t"
        "LA_v2re%=:\n\t"
        "v_xor_b32_e32 v45, 0x200, v120\n\t"
        "buffer_load_dwordx4 v[12:15], v45, s[8:11], s26 offen sc1\n\t"
        "buffer_load_dwordx4 v[104:107], v120, s[8:11], s26 offen sc1\n\t"
        "s_waitcnt vmcnt(0)\n\t"
        "v_max3_u32 v45, v12, v13, v14\n\t"
        "v_max3_u32 v45, v45, v15, v104\n\t"
        "v_max3_u32 v45, v45, v105, v106\n\t"
        "v_max_u32_e32 v45, v45, v107\n\t"
        "v_cmp_eq_u32_e32 vcc, -1, v45\n\t"
        "s_nop 0\n\t"
        "s_cbranch_vccz LA_v2ok%=\n\t"
        "s_add_u32 s42, s42, 1\n\t"
        "s_cmp_lt_u32 s42, 0x100000\n\t"
        "s_cbranch_scc1 LA_v2re%=\n\t"
        "s_movk_i32 s34, 6\n\t"
        "s_branch LA_die%=\n\t"
        "LA_rdfail%=:\n\t"
        "s_or_b32 s47, s47, 1\n\t"
        "buffer_load_dwordx4 v[16:19], v112, s[8:11], s26 offen offset:0 sc1\n\t"
        "buffer_load_dwordx4 v[20:23], v112, s[8:11], s26 offen offset:1024 sc1\n\t"
        "buffer_load_dwordx4 v[24:27], v112, s[8:11], s26 offen offset:2048 sc1\n\t"
        "buffer_load_dwordx4 v[28:31], v112, s[8:11], s26 offen offset:3072 sc1\n\t"
        "s_nop 7\n\t"
        "v_mov_b64_e32 v[0:1], 0\n\t"
        "v_mov_b64_e32 v[2:3], 0\n\t"
        "v_mov_b64_e32 v[4:5], 0\n\t"
        "v_mov_b64_e32 v[6:7], 0\n\t"
        "v_mov_b64_e32 v[8:9], 0\n\t"
        "v_mov_b64_e32 v[10:11], 0\n\t"
        "v_mov_b64_e32 v[12:13], 0\n\t"
        "v_mov_b64_e32 v[14:15], 0\n\t"
        "s_branch LA_rdre%=w\n\t"
        "LA_rdre%=:\n\t"
        "buffer_load_dwordx4 v[16:19], v112, s[8:11], s26 offen offset:0 sc1\n\t"
        "buffer_load_dwordx4 v[20:23], v112, s[8:11], s26 offen offset:1024 sc1\n\t"
        "buffer_load_dwordx4 v[24:27], v112, s[8:11], s26 offen offset:2048 sc1\n\t"
        "buffer_load_dwordx4 v[28:31], v112, s[8:11], s26 offen offset:3072 sc1\n\t"
        "LA_rdre%=w:\n\t"
        "s_waitcnt vmcnt(0) lgkmcnt(0)\n\t"
        "v_max3_u32 v111, v16, v17, v18\n\t"
        "v_max3_u32 v111, v111, v19, v20\n\t"
        "v_max3_u32 v111, v111, v21, v22\n\t"
        "v_max3_u32 v111, v111, v23, v24\n\t"
        "v_max3_u32 v111, v111, v25, v26\n\t"
        "v_max3_u32 v111, v111, v27, v28\n\t"
        "v_max3_u32 v111, v111, v29, v30\n\t"
        "v_max_u32_e32 v111, v111, v31\n\t"
        "v_cmp_eq_u32_e32 vcc, -1, v111\n\t"
        "s_nop 0\n\t"
        "s_cbranch_vccz LA_go%=\n\t"
        "s_add_u32 s42, s42, 1\n\t"
        "s_cmp_lt_u32 s42, 0x100000\n\t"
        "s_cbranch_scc1 LA_rdre%=\n\t"
        "s_movk_i32 s34, 1\n\t"
        "s_branch LA_die%=\n\t"
        "LA_vmfail%=:\n\t"
        "s_or_b32 s47, s47, 2\n\t"
        "s_mov_b32 s42, 0\n\t"
        "LA_vmre%=:\n\t"
        "buffer_load_dwordx4 v[104:107], v120, s[8:11], s26 offen sc1\n\t"
        "v_mfma_f32_16x16x32_bf16 v[4:7], v[48:51], v[12:15], 0\n\t"
        "v_mfma_f32_16x16x32_bf16 v[8:11], v[52:55], v[12:15], 0\n\t"
        "s_nop 7\n\t"
        "s_waitcnt vmcnt(0)\n\t"
        "v_mfma_f32_16x16x16_bf16 v[4:7], v[104:105], v[56:57], v[4:7]\n\t"
        "v_mfma_f32_16x16x16_bf16 v[8:11], v[106:107], v[56:57], v[8:11]\n\t"
        "v_max3_u32 v111, v104, v105, v106\n\t"
        "v_max_u32_e32 v111, v111, v107\n\t"
        "v_cmp_eq_u32_e32 vcc, -1, v111\n\t"
        "s_nop 7\n\t"
        "s_cbranch_vccz LA_vmok%=\n\t"
        "s_add_u32 s42, s42, 1\n\t"
        "s_cmp_lt_u32 s42, 0x100000\n\t"
        "s_cbranch_scc1 LA_vmre%=\n\t"
        "s_movk_i32 s34, 2\n\t"
        "s_branch LA_die%=\n\t"
        "LA_die%=:\n\t"
        "v_mov_b32_e32 v30, s34\n\t"
        "s_mov_b32 s35, 33024\n\t"
        "s_mov_b64 exec, 1\n\t"
        "buffer_store_dword v30, off, s[12:15], s35 sc1\n\t"
        "s_endpgm\n\t"
        ".Lk_end_A:\n\t"
        :: REC_S_IN, "{v96}"(g_w), "{v97}"(wb0), "{v98}"(wb1), "{v99}"(rb0), "{v100}"(rb0 ^ 32u), "{v101}"(rb0 ^ 64u), "{v102}"(rb0 ^ 96u),
           "{v112}"(a_mem), "{v113}"(l_ms + w * 4096), "{v114}"(l_ms + ((w + 1) & 3) * 4096), "{v115}"(l_ms + ((w + 2) & 3) * 4096), "{v116}"(l_ms + ((w + 3) & 3) * 4096),
           "{v117}"(lb + w * 22528 + l * 16), "{v118}"(l_iw), "{v119}"(l_iq), "{v120}"(a_vm), "{v121}"(a_o), "{v122}"(h * 8192 + l * 16), "{v123}"(a_ox), "{v124}"(lb + L_KV + l * 16), "{s55}"(lb + L_KV + w * 2048),
           "{v125}"((24 + cp) * 16384 + l * 32), "{v126}"(16 * (w & 1) + n - 4 * g), "{v103}"((l & 1) ? 0xEEEEEEEEu : 0x44444444u),
           "{v127}"((l == 0 && w == 0) ? F_PROG + h * 128 : OOB)
        : "memory", "vcc", "scc", "v0", "v255", "a0", "a255", "s101");
    __builtin_unreachable();
}
__device__ __forceinline__ void rec_b(const Params& p, unsigned lb, const int wv, const int id, const int rg, const int cp) {
    const int w = wv, l = otid() & 63, n = l & 15, g = l >> 4, cs = id, rb = 32 + id * 2 + rg;
    const v4u sh = mk_srd(p.ws + ho_base(p.ws), HO_BYTES - 8192), sw = mk_srd(p.ws, W_TOTAL), s1 = mk_srd(p.Wv, 1024 * 4096 * 2), s2 = mk_srd(p.Wo, 1024 * 4096 * 2);
    const int r8 = l >> 3, pc = l & 7, pp = n >> 3, bb = (n >> 2) & 1, f = (g & 1) | (pp << 1);
    const unsigned stg = lb + LB_STG + w * LB_STGP;
    const unsigned g_v = r8 * 8192 + cs * 256 + rg * 128 + pc * 16, g_o = (l >> 2) * 2048 + cs * 64 + (l & 3) * 16;
    const unsigned wb0 = stg + r8 * 128 + (((pc >> 1) ^ (r8 >> 2)) << 5) + (pc & 1) * 16, wb1 = wb0 ^ 64u;
    const unsigned rb0 = stg + (16 * (g >> 1) + 8 * pp + 4 * (g & 1) + bb) * 128 + (f << 5) + (n & 3) * 8;
    const unsigned o_r = stg + (g >> 1) * (2048 + 128) + (g & 1) * (1024 + 32) + (bb + 4 * pp) * 64 + (n & 3) * 8;
    const unsigned a_mem = cp * 32768 + (w >> 1) * 16384 + (w & 1) * 4096 + l * 16;
    const unsigned a_vmw = w < 2 ? (16 + (cs >> 2)) * 16384 + (cs & 3) * 2048 + (2 * rg + w) * 512 + (l & 31) * 16 + (g >> 1) * 8 : OOB;
    const unsigned a_or = (8 + rg * 4 + w) * 16384 + l * 16;
    const unsigned a_mw = w * 32768 + (cs >> 4) * 16384 + ((cs >> 1) & 7) * 1024 + (cs & 1) * 512 + (l >> 5) * 256 + rg * 128 + (l & 31) * 4;
    const unsigned a_sw = (w == 0 && l < 4) ? (24 + l) * 16384 + (id * 2 + rg) * 32 : OOB;
    const bf16_t* m0p = p.mem0 + (4 * rg + ((l >> 3) & 3)) * 1024 + cs * 32 + (l >> 5) * 16 + (l & 7) * 2;
    asm volatile(
        "s_nop 4\n\t"
        "s_memtime s[70:71]\n\t"
        "s_memrealtime s[72:73]\n\t"
        "global_load_dword v109, v[108:109], off\n\t"
        "s_lshl_b32 s34, s43, 21\n\t"
        "s_add_u32 s34, s34, 0\n\t"
        "buffer_load_dwordx4 v[128:131], v96, s[16:19], s34 offen nt\n\t"
        "s_add_u32 s34, s34, 0x10000\n\t"
        "buffer_load_dwordx4 v[132:135], v96, s[16:19], s34 offen nt\n\t"
        "s_add_u32 s34, s34, 0x10000\n\t"
        "buffer_load_dwordx4 v[136:139], v96, s[16:19], s34 offen nt\n\t"
        "s_add_u32 s34, s34, 0x10000\n\t"
        "buffer_load_dwordx4 v[140:143], v96, s[16:19], s34 offen nt\n\t"
        "s_add_u32 s34, s34, 0x10000\n\t"
        "buffer_load_dwordx4 v[144:147], v96, s[16:19], s34 offen nt\n\t"
        "s_add_u32 s34, s34, 0x10000\n\t"
        "buffer_load_dwordx4 v[148:151], v96, s[16:19], s34 offen nt\n\t"
        "s_add_u32 s34, s34, 0x10000\n\t"
        "buffer_load_dwordx4 v[152:155], v96, s[16:19], s34 offen nt\n\t"
        "s_add_u32 s34, s34, 0x10000\n\t"
        "buffer_load_dwordx4 v[156:159], v96, s[16:19], s34 offen nt\n\t"
        "s_lshl_b32 s34, s43, 21\n\t"
        "s_add_u32 s34, s34, 524288\n\t"
        "buffer_load_dwordx4 v[160:163], v96, s[16:19], s34 offen nt\n\t"
        "s_add_u32 s34, s34, 0x10000\n\t"
        "buffer_load_dwordx4 v[164:167], v96, s[16:19], s34 offen nt\n\t"
        "s_add_u32 s34, s34, 0x10000\n\t"
        "buffer_load_dwordx4 v[168:171], v96, s[16:19], s34 offen nt\n\t"
        "s_add_u32 s34, s34, 0x10000\n\t"
        "buffer_load_dwordx4 v[172:175], v96, s[16:19], s34 offen nt\n\t"
        "s_add_u32 s34, s34, 0x10000\n\t"
        "buffer_load_dwordx4 v[176:179], v96, s[16:19], s34 offen nt\n\t"
        "s_add_u32 s34, s34, 0x10000\n\t"
        "buffer_load_dwordx4 v[180:183], v96, s[16:19], s34 offen nt\n\t"
        "s_add_u32 s34, s34, 0x10000\n\t"
        "buffer_load_dwordx4 v[184:187], v96, s[16:19], s34 offen nt\n\t"
        "s_add_u32 s34, s34, 0x10000\n\t"
        "buffer_load_dwordx4 v[188:191], v96, s[16:19], s34 offen nt\n\t"
        "s_lshl_b32 s34, s43, 21\n\t"
        "s_add_u32 s34, s34, 1048576\n\t"
        "buffer_load_dwordx4 v[192:195], v96, s[16:19], s34 offen nt\n\t"
        "s_add_u32 s34, s34, 0x10000\n\t"
        "buffer_load_dwordx4 v[196:199], v96, s[16:19], s34 offen nt\n\t"
        "s_add_u32 s34, s34, 0x10000\n\t"
        "buffer_load_dwordx4 v[200:203], v96, s[16:19], s34 offen nt\n\t"
        "s_add_u32 s34, s34, 0x10000\n\t"
        "buffer_load_dwordx4 v[204:207], v96, s[16:19], s34 offen nt\n\t"
        "s_add_u32 s34, s34, 0x10000\n\t"
        "buffer_load_dwordx4 v[208:211], v96, s[16:19], s34 offen nt\n\t"
        "s_add_u32 s34, s34, 0x10000\n\t"
        "buffer_load_dwordx4 v[212:215], v96, s[16:19], s34 offen nt\n\t"
        "s_add_u32 s34, s34, 0x10000\n\t"
        "buffer_load_dwordx4 v[216:219], v96, s[16:19], s34 offen nt\n\t"
        "s_add_u32 s34, s34, 0x10000\n\t"
        "buffer_load_dwordx4 v[220:223], v96, s[16:19], s34 offen nt\n\t"
        "s_waitcnt vmcnt(16)\n\t"
        "ds_write_b128 v98, v[128:131] offset:0\n\t"
        "ds_write_b128 v99, v[132:135] offset:1024\n\t"
        "ds_write_b128 v98, v[136:139] offset:2048\n\t"
        "ds_write_b128 v99, v[140:143] offset:3072\n\t"
        "ds_write_b128 v98, v[144:147] offset:4096\n\t"
        "ds_write_b128 v99, v[148:151] offset:5120\n\t"
        "ds_write_b128 v98, v[152:155] offset:6144\n\t"
        "ds_write_b128 v99, v[156:159] offset:7168\n\t"
        "ds_read_b64_tr_b16 v[128:129], v100 offset:0\n\t"
        "ds_read_b64_tr_b16 v[130:131], v100 offset:256\n\t"
        "ds_read_b64_tr_b16 v[132:133], v100 offset:4096\n\t"
        "ds_read_b64_tr_b16 v[134:135], v100 offset:4352\n\t"
        "s_waitcnt lgkmcnt(8)\n\t"
        "ds_read_b64_tr_b16 v[136:137], v101 offset:0\n\t"
        "ds_read_b64_tr_b16 v[138:139], v101 offset:256\n\t"
        "ds_read_b64_tr_b16 v[140:141], v101 offset:4096\n\t"
        "ds_read_b64_tr_b16 v[142:143], v101 offset:4352\n\t"
        "s_waitcnt lgkmcnt(8)\n\t"
        "ds_read_b64_tr_b16 v[144:145], v102 offset:0\n\t"
        "ds_read_b64_tr_b16 v[146:147], v102 offset:256\n\t"
        "ds_read_b64_tr_b16 v[148:149], v102 offset:4096\n\t"
        "ds_read_b64_tr_b16 v[150:151], v102 offset:4352\n\t"
        "s_waitcnt lgkmcnt(8)\n\t"
        "ds_read_b64_tr_b16 v[152:153], v103 offset:0\n\t"
        "ds_read_b64_tr_b16 v[154:155], v103 offset:256\n\t"
        "ds_read_b64_tr_b16 v[156:157], v103 offset:4096\n\t"
        "ds_read_b64_tr_b16 v[158:159], v103 offset:4352\n\t"
        "s_lshl_b32 s34, s43, 21\n\t"
        "s_add_u32 s34, s34, 1572864\n\t"
        "buffer_load_dwordx4 v[224:227], v96, s[16:19], s34 offen nt\n\t"
        "s_add_u32 s34, s34, 0x10000\n\t"
        "buffer_load_dwordx4 v[228:231], v96, s[16:19], s34 offen nt\n\t"
        "s_add_u32 s34, s34, 0x10000\n\t"
        "buffer_load_dwordx4 v[232:235], v96, s[16:19], s34 offen nt\n\t"
        "s_add_u32 s34, s34, 0x10000\n\t"
        "buffer_load_dwordx4 v[236:239], v96, s[16:19], s34 offen nt\n\t"
        "s_add_u32 s34, s34, 0x10000\n\t"
        "buffer_load_dwordx4 v[240:243], v96, s[16:19], s34 offen nt\n\t"
        "s_add_u32 s34, s34, 0x10000\n\t"
        "buffer_load_dwordx4 v[244:247], v96, s[16:19], s34 offen nt\n\t"
        "s_add_u32 s34, s34, 0x10000\n\t"
        "buffer_load_dwordx4 v[248:251], v96, s[16:19], s34 offen nt\n\t"
        "s_add_u32 s34, s34, 0x10000\n\t"
        "buffer_load_dwordx4 v[252:255], v96, s[16:19], s34 offen nt\n\t"
        "s_waitcnt lgkmcnt(0)\n\t"
        "s_waitcnt vmcnt(16)\n\t"
        "ds_write_b128 v98, v[160:163] offset:0\n\t"
        "ds_write_b128 v99, v[164:167] offset:1024\n\t"
        "ds_write_b128 v98, v[168:171] offset:2048\n\t"
        "ds_write_b128 v99, v[172:175] offset:3072\n\t"
        "ds_write_b128 v98, v[176:179] offset:4096\n\t"
        "ds_write_b128 v99, v[180:183] offset:5120\n\t"
        "ds_write_b128 v98, v[184:187] offset:6144\n\t"
        "ds_write_b128 v99, v[188:191] offset:7168\n\t"
        "ds_read_b64_tr_b16 v[160:161], v100 offset:0\n\t"
        "ds_read_b64_tr_b16 v[162:163], v100 offset:256\n\t"
        "ds_read_b64_tr_b16 v[164:165], v100 offset:4096\n\t"
        "ds_read_b64_tr_b16 v[166:167], v100 offset:4352\n\t"
        "s_waitcnt lgkmcnt(8)\n\t"
        "ds_read_b64_tr_b16 v[168:169], v101 offset:0\n\t"
        "ds_read_b64_tr_b16 v[170:171], v101 offset:256\n\t"
        "ds_read_b64_tr_b16 v[172:173], v101 offset:4096\n\t"
        "ds_read_b64_tr_b16 v[174:175], v101 offset:4352\n\t"
        "s_waitcnt lgkmcnt(8)\n\t"
        "ds_read_b64_tr_b16 v[176:177], v102 offset:0\n\t"
        "ds_read_b64_tr_b16 v[178:179], v102 offset:256\n\t"
        "ds_read_b64_tr_b16 v[180:181], v102 offset:4096\n\t"
        "ds_read_b64_tr_b16 v[182:183], v102 offset:4352\n\t"
        "s_waitcnt lgkmcnt(8)\n\t"
        "ds_read_b64_tr_b16 v[184:185], v103 offset:0\n\t"
        "ds_read_b64_tr_b16 v[186:187], v103 offset:256\n\t"
        "ds_read_b64_tr_b16 v[188:189], v103 offset:4096\n\t"
        "ds_read_b64_tr_b16 v[190:191], v103 offset:4352\n\t"
        "s_lshl_b32 s34, s43, 21\n\t"
        "s_add_u32 s34, s34, 0\n\t"
        "buffer_load_dwordx4 a[0:3], v97, s[20:23], s34 offen\n\t"
        "s_add_u32 s34, s34, 0x8000\n\t"
        "buffer_load_dwordx4 a[4:7], v97, s[20:23], s34 offen\n\t"
        "s_add_u32 s34, s34, 0x8000\n\t"
        "buffer_load_dwordx4 a[8:11], v97, s[20:23], s34 offen\n\t"
        "s_add_u32 s34, s34, 0x8000\n\t"
        "buffer_load_dwordx4 a[12:15], v97, s[20:23], s34 offen\n\t"
        "s_add_u32 s34, s34, 0x8000\n\t"
        "buffer_load_dwordx4 a[16:19], v97, s[20:23], s34 offen\n\t"
        "s_add_u32 s34, s34, 0x8000\n\t"
        "buffer_load_dwordx4 a[20:23], v97, s[20:23], s34 offen\n\t"
        "s_add_u32 s34, s34, 0x8000\n\t"
        "buffer_load_dwordx4 a[24:27], v97, s[20:23], s34 offen\n\t"
        "s_add_u32 s34, s34, 0x8000\n\t"
        "buffer_load_dwordx4 a[28:31], v97, s[20:23], s34 offen\n\t"
        "s_waitcnt lgkmcnt(0)\n\t"
        "s_waitcnt vmcnt(16)\n\t"
        "ds_write_b128 v98, v[192:195] offset:0\n\t"
        "ds_write_b128 v99, v[196:199] offset:1024\n\t"
        "ds_write_b128 v98, v[200:203] offset:2048\n\t"
        "ds_write_b128 v99, v[204:207] offset:3072\n\t"
        "ds_write_b128 v98, v[208:211] offset:4096\n\t"
        "ds_write_b128 v99, v[212:215] offset:5120\n\t"
        "ds_write_b128 v98, v[216:219] offset:6144\n\t"
        "ds_write_b128 v99, v[220:223] offset:7168\n\t"
        "ds_read_b64_tr_b16 v[192:193], v100 offset:0\n\t"
        "ds_read_b64_tr_b16 v[194:195], v100 offset:256\n\t"
        "ds_read_b64_tr_b16 v[196:197], v100 offset:4096\n\t"
        "ds_read_b64_tr_b16 v[198:199], v100 offset:4352\n\t"
        "s_waitcnt lgkmcnt(8)\n\t"
        "ds_read_b64_tr_b16 v[200:201], v101 offset:0\n\t"
        "ds_read_b64_tr_b16 v[202:203], v101 offset:256\n\t"
        "ds_read_b64_tr_b16 v[204:205], v101 offset:4096\n\t"
        "ds_read_b64_tr_b16 v[206:207], v101 offset:4352\n\t"
        "s_waitcnt lgkmcnt(8)\n\t"
        "ds_read_b64_tr_b16 v[208:209], v102 offset:0\n\t"
        "ds_read_b64_tr_b16 v[210:211], v102 offset:256\n\t"
        "ds_read_b64_tr_b16 v[212:213], v102 offset:4096\n\t"
        "ds_read_b64_tr_b16 v[214:215], v102 offset:4352\n\t"
        "s_waitcnt lgkmcnt(8)\n\t"
        "ds_read_b64_tr_b16 v[216:217], v103 offset:0\n\t"
        "ds_read_b64_tr_b16 v[218:219], v103 offset:256\n\t"
        "ds_read_b64_tr_b16 v[220:221], v103 offset:4096\n\t"
        "ds_read_b64_tr_b16 v[222:223], v103 offset:4352\n\t"
        "s_lshl_b32 s34, s43, 21\n\t"
        "s_add_u32 s34, s34, 262144\n\t"
        "buffer_load_dwordx4 a[32:35], v97, s[20:23], s34 offen\n\t"
        "s_add_u32 s34, s34, 0x8000\n\t"
        "buffer_load_dwordx4 a[36:39], v97, s[20:23], s34 offen\n\t"
        "s_add_u32 s34, s34, 0x8000\n\t"
        "buffer_load_dwordx4 a[40:43], v97, s[20:23], s34 offen\n\t"
        "s_add_u32 s34, s34, 0x8000\n\t"
        "buffer_load_dwordx4 a[44:47], v97, s[20:23], s34 offen\n\t"
        "s_add_u32 s34, s34, 0x8000\n\t"
        "buffer_load_dwordx4 a[48:51], v97, s[20:23], s34 offen\n\t"
        "s_add_u32 s34, s34, 0x8000\n\t"
        "buffer_load_dwordx4 a[52:55], v97, s[20:23], s34 offen\n\t"
        "s_add_u32 s34, s34, 0x8000\n\t"
        "buffer_load_dwordx4 a[56:59], v97, s[20:23], s34 offen\n\t"
        "s_add_u32 s34, s34, 0x8000\n\t"
        "buffer_load_dwordx4 a[60:63], v97, s[20:23], s34 offen\n\t"
        "s_waitcnt lgkmcnt(0)\n\t"
        "s_waitcnt vmcnt(16)\n\t"
        "ds_write_b128 v98, v[224:227] offset:0\n\t"
        "ds_write_b128 v99, v[228:231] offset:1024\n\t"
        "ds_write_b128 v98, v[232:235] offset:2048\n\t"
        "ds_write_b128 v99, v[236:239] offset:3072\n\t"
        "ds_write_b128 v98, v[240:243] offset:4096\n\t"
        "ds_write_b128 v99, v[244:247] offset:5120\n\t"
        "ds_write_b128 v98, v[248:251] offset:6144\n\t"
        "ds_write_b128 v99, v[252:255] offset:7168\n\t"
        "ds_read_b64_tr_b16 v[224:225], v100 offset:0\n\t"
        "ds_read_b64_tr_b16 v[226:227], v100 offset:256\n\t"
        "ds_read_b64_tr_b16 v[228:229], v100 offset:4096\n\t"
        "ds_read_b64_tr_b16 v[230:231], v100 offset:4352\n\t"
        "s_waitcnt lgkmcnt(8)\n\t"
        "ds_read_b64_tr_b16 v[232:233], v101 offset:0\n\t"
        "ds_read_b64_tr_b16 v[234:235], v101 offset:256\n\t"
        "ds_read_b64_tr_b16 v[236:237], v101 offset:4096\n\t"
        "ds_read_b64_tr_b16 v[238:239], v101 offset:4352\n\t"
        "s_waitcnt lgkmcnt(8)\n\t"
        "ds_read_b64_tr_b16 v[240:241], v102 offset:0\n\t"
        "ds_read_b64_tr_b16 v[242:243], v102 offset:256\n\t"
        "ds_read_b64_tr_b16 v[244:245], v102 offset:4096\n\t"
        "ds_read_b64_tr_b16 v[246:247], v102 offset:4352\n\t"
        "s_waitcnt lgkmcnt(8)\n\t"
        "ds_read_b64_tr_b16 v[248:249], v103 offset:0\n\t"
        "ds_read_b64_tr_b16 v[250:251], v103 offset:256\n\t"
        "ds_read_b64_tr_b16 v[252:253], v103 offset:4096\n\t"
        "ds_read_b64_tr_b16 v[254:255], v103 offset:4352\n\t"
        "s_lshl_b32 s34, s43, 21\n\t"
        "s_add_u32 s34, s34, 524288\n\t"
        "buffer_load_dwordx4 a[64:67], v97, s[20:23], s34 offen\n\t"
        "s_add_u32 s34, s34, 0x8000\n\t"
        "buffer_load_dwordx4 a[68:71], v97, s[20:23], s34 offen\n\t"
        "s_add_u32 s34, s34, 0x8000\n\t"
        "buffer_load_dwordx4 a[72:75], v97, s[20:23], s34 offen\n\t"
        "s_add_u32 s34, s34, 0x8000\n\t"
        "buffer_load_dwordx4 a[76:79], v97, s[20:23], s34 offen\n\t"
        "s_add_u32 s34, s34, 0x8000\n\t"
        "buffer_load_dwordx4 a[80:83], v97, s[20:23], s34 offen\n\t"
        "s_add_u32 s34, s34, 0x8000\n\t"
        "buffer_load_dwordx4 a[84:87], v97, s[20:23], s34 offen\n\t"
        "s_add_u32 s34, s34, 0x8000\n\t"
        "buffer_load_dwordx4 a[88:91], v97, s[20:23], s34 offen\n\t"
        "s_add_u32 s34, s34, 0x8000\n\t"
        "buffer_load_dwordx4 a[92:95], v97, s[20:23], s34 offen\n\t"
        "s_waitcnt lgkmcnt(0)\n\t"
        "s_waitcnt vmcnt(16)\n\t"
        "ds_write_b128 v122, a[0:3] offset:0\n\t"
        "ds_write_b128 v122, a[4:7] offset:1056\n\t"
        "ds_write_b128 v122, a[8:11] offset:2176\n\t"
        "ds_write_b128 v122, a[12:15] offset:3232\n\t"
        "ds_write_b128 v122, a[16:19] offset:4352\n\t"
        "ds_write_b128 v122, a[20:23] offset:5408\n\t"
        "ds_write_b128 v122, a[24:27] offset:6528\n\t"
        "ds_write_b128 v122, a[28:31] offset:7584\n\t"
        "ds_read_b64_tr_b16 a[0:1], v123 offset:0\n\t"
        "ds_read_b64_tr_b16 a[2:3], v123 offset:128\n\t"
        "ds_read_b64_tr_b16 a[4:5], v123 offset:4352\n\t"
        "ds_read_b64_tr_b16 a[6:7], v123 offset:4480\n\t"
        "s_waitcnt lgkmcnt(8)\n\t"
        "ds_read_b64_tr_b16 a[8:9], v123 offset:32\n\t"
        "ds_read_b64_tr_b16 a[10:11], v123 offset:160\n\t"
        "ds_read_b64_tr_b16 a[12:13], v123 offset:4384\n\t"
        "ds_read_b64_tr_b16 a[14:15], v123 offset:4512\n\t"
        "s_waitcnt lgkmcnt(8)\n\t"
        "ds_read_b64_tr_b16 a[16:17], v123 offset:512\n\t"
        "ds_read_b64_tr_b16 a[18:19], v123 offset:640\n\t"
        "ds_read_b64_tr_b16 a[20:21], v123 offset:4864\n\t"
        "ds_read_b64_tr_b16 a[22:23], v123 offset:4992\n\t"
        "s_waitcnt lgkmcnt(8)\n\t"
        "ds_read_b64_tr_b16 a[24:25], v123 offset:544\n\t"
        "ds_read_b64_tr_b16 a[26:27], v123 offset:672\n\t"
        "ds_read_b64_tr_b16 a[28:29], v123 offset:4896\n\t"
        "ds_read_b64_tr_b16 a[30:31], v123 offset:5024\n\t"
        "s_waitcnt lgkmcnt(8)\n\t"
        "s_lshl_b32 s34, s43, 21\n\t"
        "s_add_u32 s34, s34, 786432\n\t"
        "buffer_load_dwordx4 a[96:99], v97, s[20:23], s34 offen\n\t"
        "s_add_u32 s34, s34, 0x8000\n\t"
        "buffer_load_dwordx4 a[100:103], v97, s[20:23], s34 offen\n\t"
        "s_add_u32 s34, s34, 0x8000\n\t"
        "buffer_load_dwordx4 a[104:107], v97, s[20:23], s34 offen\n\t"
        "s_add_u32 s34, s34, 0x8000\n\t"
        "buffer_load_dwordx4 a[108:111], v97, s[20:23], s34 offen\n\t"
        "s_add_u32 s34, s34, 0x8000\n\t"
        "buffer_load_dwordx4 a[112:115], v97, s[20:23], s34 offen\n\t"
        "s_add_u32 s34, s34, 0x8000\n\t"
        "buffer_load_dwordx4 a[116:119], v97, s[20:23], s34 offen\n\t"
        "s_add_u32 s34, s34, 0x8000\n\t"
        "buffer_load_dwordx4 a[120:123], v97, s[20:23], s34 offen\n\t"
        "s_add_u32 s34, s34, 0x8000\n\t"
        "buffer_load_dwordx4 a[124:127], v97, s[20:23], s34 offen\n\t"
        "s_waitcnt lgkmcnt(0)\n\t"
        "s_waitcnt vmcnt(16)\n\t"
        "ds_write_b128 v122, a[32:35] offset:0\n\t"
        "ds_write_b128 v122, a[36:39] offset:1056\n\t"
        "ds_write_b128 v122, a[40:43] offset:2176\n\t"
        "ds_write_b128 v122, a[44:47] offset:3232\n\t"
        "ds_write_b128 v122, a[48:51] offset:4352\n\t"
        "ds_write_b128 v122, a[52:55] offset:5408\n\t"
        "ds_write_b128 v122, a[56:59] offset:6528\n\t"
        "ds_write_b128 v122, a[60:63] offset:7584\n\t"
        "ds_read_b64_tr_b16 a[32:33], v123 offset:0\n\t"
        "ds_read_b64_tr_b16 a[34:35], v123 offset:128\n\t"
        "ds_read_b64_tr_b16 a[36:37], v123 offset:4352\n\t"
        "ds_read_b64_tr_b16 a[38:39], v123 offset:4480\n\t"
        "s_waitcnt lgkmcnt(8)\n\t"
        "ds_read_b64_tr_b16 a[40:41], v123 offset:32\n\t"
        "ds_read_b64_tr_b16 a[42:43], v123 offset:160\n\t"
        "ds_read_b64_tr_b16 a[44:45], v123 offset:4384\n\t"
        "ds_read_b64_tr_b16 a[46:47], v123 offset:4512\n\t"
        "s_waitcnt lgkmcnt(8)\n\t"
        "ds_read_b64_tr_b16 a[48:49], v123 offset:512\n\t"
        "ds_read_b64_tr_b16 a[50:51], v123 offset:640\n\t"
        "ds_read_b64_tr_b16 a[52:53], v123 offset:4864\n\t"
        "ds_read_b64_tr_b16 a[54:55], v123 offset:4992\n\t"
        "s_waitcnt lgkmcnt(8)\n\t"
        "ds_read_b64_tr_b16 a[56:57], v123 offset:544\n\t"
        "ds_read_b64_tr_b16 a[58:59], v123 offset:672\n\t"
        "ds_read_b64_tr_b16 a[60:61], v123 offset:4896\n\t"
        "ds_read_b64_tr_b16 a[62:63], v123 offset:5024\n\t"
        "s_waitcnt lgkmcnt(8)\n\t"
        "s_lshl_b32 s34, s43, 21\n\t"
        "s_add_u32 s34, s34, 1048576\n\t"
        "buffer_load_dwordx4 a[128:131], v97, s[20:23], s34 offen\n\t"
        "s_add_u32 s34, s34, 0x8000\n\t"
        "buffer_load_dwordx4 a[132:135], v97, s[20:23], s34 offen\n\t"
        "s_add_u32 s34, s34, 0x8000\n\t"
        "buffer_load_dwordx4 a[136:139], v97, s[20:23], s34 offen\n\t"
        "s_add_u32 s34, s34, 0x8000\n\t"
        "buffer_load_dwordx4 a[140:143], v97, s[20:23], s34 offen\n\t"
        "s_add_u32 s34, s34, 0x8000\n\t"
        "buffer_load_dwordx4 a[144:147], v97, s[20:23], s34 offen\n\t"
        "s_add_u32 s34, s34, 0x8000\n\t"
        "buffer_load_dwordx4 a[148:151], v97, s[20:23], s34 offen\n\t"
        "s_add_u32 s34, s34, 0x8000\n\t"
        "buffer_load_dwordx4 a[152:155], v97, s[20:23], s34 offen\n\t"
        "s_add_u32 s34, s34, 0x8000\n\t"
        "buffer_load_dwordx4 a[156:159], v97, s[20:23], s34 offen\n\t"
        "s_waitcnt lgkmcnt(0)\n\t"
        "s_waitcnt vmcnt(16)\n\t"
        "ds_write_b128 v122, a[64:67] offset:0\n\t"
        "ds_write_b128 v122, a[68:71] offset:1056\n\t"
        "ds_write_b128 v122, a[72:75] offset:2176\n\t"
        "ds_write_b128 v122, a[76:79] offset:3232\n\t"
        "ds_write_b128 v122, a[80:83] offset:4352\n\t"
        "ds_write_b128 v122, a[84:87] offset:5408\n\t"
        "ds_write_b128 v122, a[88:91] offset:6528\n\t"
        "ds_write_b128 v122, a[92:95] offset:7584\n\t"
        "ds_read_b64_tr_b16 a[64:65], v123 offset:0\n\t"
        "ds_read_b64_tr_b16 a[66:67], v123 offset:128\n\t"
        "ds_read_b64_tr_b16 a[68:69], v123 offset:4352\n\t"
        "ds_read_b64_tr_b16 a[70:71], v123 offset:4480\n\t"
        "s_waitcnt lgkmcnt(8)\n\t"
        "ds_read_b64_tr_b16 a[72:73], v123 offset:32\n\t"
        "ds_read_b64_tr_b16 a[74:75], v123 offset:160\n\t"
        "ds_read_b64_tr_b16 a[76:77], v123 offset:4384\n\t"
        "ds_read_b64_tr_b16 a[78:79], v123 offset:4512\n\t"
        "s_waitcnt lgkmcnt(8)\n\t"
        "ds_read_b64_tr_b16 a[80:81], v123 offset:512\n\t"
        "ds_read_b64_tr_b16 a[82:83], v123 offset:640\n\t"
        "ds_read_b64_tr_b16 a[84:85], v123 offset:4864\n\t"
        "ds_read_b64_tr_b16 a[86:87], v123 offset:4992\n\t"
        "s_waitcnt lgkmcnt(8)\n\t"
        "ds_read_b64_tr_b16 a[88:89], v123 offset:544\n\t"
        "ds_read_b64_tr_b16 a[90:91], v123 offset:672\n\t"
        "ds_read_b64_tr_b16 a[92:93], v123 offset:4896\n\t"
        "ds_read_b64_tr_b16 a[94:95], v123 offset:5024\n\t"
        "s_waitcnt lgkmcnt(8)\n\t"
        "s_lshl_b32 s34, s43, 21\n\t"
        "s_add_u32 s34, s34, 1310720\n\t"
        "buffer_load_dwordx4 a[160:163], v97, s[20:23], s34 offen\n\t"
        "s_add_u32 s34, s34, 0x8000\n\t"
        "buffer_load_dwordx4 a[164:167], v97, s[20:23], s34 offen\n\t"
        "s_add_u32 s34, s34, 0x8000\n\t"
        "buffer_load_dwordx4 a[168:171], v97, s[20:23], s34 offen\n\t"
        "s_add_u32 s34, s34, 0x8000\n\t"
        "buffer_load_dwordx4 a[172:175], v97, s[20:23], s34 offen\n\t"
        "s_add_u32 s34, s34, 0x8000\n\t"
        "buffer_load_dwordx4 a[176:179], v97, s[20:23], s34 offen\n\t"
        "s_add_u32 s34, s34, 0x8000\n\t"
        "buffer_load_dwordx4 a[180:183], v97, s[20:23], s34 offen\n\t"
        "s_add_u32 s34, s34, 0x8000\n\t"
        "buffer_load_dwordx4 a[184:187], v97, s[20:23], s34 offen\n\t"
        "s_add_u32 s34, s34, 0x8000\n\t"
        "buffer_load_dwordx4 a[188:191], v97, s[20:23], s34 offen\n\t"
        "s_waitcnt lgkmcnt(0)\n\t"
        "s_waitcnt vmcnt(16)\n\t"
        "ds_write_b128 v122, a[96:99] offset:0\n\t"
        "ds_write_b128 v122, a[100:103] offset:1056\n\t"
        "ds_write_b128 v122, a[104:107] offset:2176\n\t"
        "ds_write_b128 v122, a[108:111] offset:3232\n\t"
        "ds_write_b128 v122, a[112:115] offset:4352\n\t"
        "ds_write_b128 v122, a[116:119] offset:5408\n\t"
        "ds_write_b128 v122, a[120:123] offset:6528\n\t"
        "ds_write_b128 v122, a[124:127] offset:7584\n\t"
        "ds_read_b64_tr_b16 a[96:97], v123 offset:0\n\t"
        "ds_read_b64_tr_b16 a[98:99], v123 offset:128\n\t"
        "ds_read_b64_tr_b16 a[100:101], v123 offset:4352\n\t"
        "ds_read_b64_tr_b16 a[102:103], v123 offset:4480\n\t"
        "s_waitcnt lgkmcnt(8)\n\t"
        "ds_read_b64_tr_b16 a[104:105], v123 offset:32\n\t"
        "ds_read_b64_tr_b16 a[106:107], v123 offset:160\n\t"
        "ds_read_b64_tr_b16 a[108:109], v123 offset:4384\n\t"
        "ds_read_b64_tr_b16 a[110:111], v123 offset:4512\n\t"
        "s_waitcnt lgkmcnt(8)\n\t"
        "ds_read_b64_tr_b16 a[112:113], v123 offset:512\n\t"
        "ds_read_b64_tr_b16 a[114:115], v123 offset:640\n\t"
        "ds_read_b64_tr_b16 a[116:117], v123 offset:4864\n\t"
        "ds_read_b64_tr_b16 a[118:119], v123 offset:4992\n\t"
        "s_waitcnt lgkmcnt(8)\n\t"
        "ds_read_b64_tr_b16 a[120:121], v123 offset:544\n\t"
        "ds_read_b64_tr_b16 a[122:123], v123 offset:672\n\t"
        "ds_read_b64_tr_b16 a[124:125], v123 offset:4896\n\t"
        "ds_read_b64_tr_b16 a[126:127], v123 offset:5024\n\t"
        "s_waitcnt lgkmcnt(8)\n\t"
        "s_lshl_b32 s34, s43, 21\n\t"
        "s_add_u32 s34, s34, 1572864\n\t"
        "buffer_load_dwordx4 a[192:195], v97, s[20:23], s34 offen\n\t"
        "s_add_u32 s34, s34, 0x8000\n\t"
        "buffer_load_dwordx4 a[196:199], v97, s[20:23], s34 offen\n\t"
        "s_add_u32 s34, s34, 0x8000\n\t"
        "buffer_load_dwordx4 a[200:203], v97, s[20:23], s34 offen\n\t"
        "s_add_u32 s34, s34, 0x8000\n\t"
        "buffer_load_dwordx4 a[204:207], v97, s[20:23], s34 offen\n\t"
        "s_add_u32 s34, s34, 0x8000\n\t"
        "buffer_load_dwordx4 a[208:211], v97, s[20:23], s34 offen\n\t"
        "s_add_u32 s34, s34, 0x8000\n\t"
        "buffer_load_dwordx4 a[212:215], v97, s[20:23], s34 offen\n\t"
        "s_add_u32 s34, s34, 0x8000\n\t"
        "buffer_load_dwordx4 a[216:219], v97, s[20:23], s34 offen\n\t"
        "s_add_u32 s34, s34, 0x8000\n\t"
        "buffer_load_dwordx4 a[220:223], v97, s[20:23], s34 offen\n\t"
        "s_waitcnt lgkmcnt(0)\n\t"
        "s_waitcnt vmcnt(16)\n\t"
        "ds_write_b128 v122, a[128:131] offset:0\n\t"
        "ds_write_b128 v122, a[132:135] offset:1056\n\t"
        "ds_write_b128 v122, a[136:139] offset:2176\n\t"
        "ds_write_b128 v122, a[140:143] offset:3232\n\t"
        "ds_write_b128 v122, a[144:147] offset:4352\n\t"
        "ds_write_b128 v122, a[148:151] offset:5408\n\t"
        "ds_write_b128 v122, a[152:155] offset:6528\n\t"
        "ds_write_b128 v122, a[156:159] offset:7584\n\t"
        "ds_read_b64_tr_b16 a[128:129], v123 offset:0\n\t"
        "ds_read_b64_tr_b16 a[130:131], v123 offset:128\n\t"
        "ds_read_b64_tr_b16 a[132:133], v123 offset:4352\n\t"
        "ds_read_b64_tr_b16 a[134:135], v123 offset:4480\n\t"
        "s_waitcnt lgkmcnt(8)\n\t"
        "ds_read_b64_tr_b16 a[136:137], v123 offset:32\n\t"
        "ds_read_b64_tr_b16 a[138:139], v123 offset:160\n\t"
        "ds_read_b64_tr_b16 a[140:141], v123 offset:4384\n\t"
        "ds_read_b64_tr_b16 a[142:143], v123 offset:4512\n\t"
        "s_waitcnt lgkmcnt(8)\n\t"
        "ds_read_b64_tr_b16 a[144:145], v123 offset:512\n\t"
        "ds_read_b64_tr_b16 a[146:147], v123 offset:640\n\t"
        "ds_read_b64_tr_b16 a[148:149], v123 offset:4864\n\t"
        "ds_read_b64_tr_b16 a[150:151], v123 offset:4992\n\t"
        "s_waitcnt lgkmcnt(8)\n\t"
        "ds_read_b64_tr_b16 a[152:153], v123 offset:544\n\t"
        "ds_read_b64_tr_b16 a[154:155], v123 offset:672\n\t"
        "ds_read_b64_tr_b16 a[156:157], v123 offset:4896\n\t"
        "ds_read_b64_tr_b16 a[158:159], v123 offset:5024\n\t"
        "s_waitcnt lgkmcnt(8)\n\t"
        "s_lshl_b32 s34, s43, 21\n\t"
        "s_add_u32 s34, s34, 1835008\n\t"
        "buffer_load_dwordx4 a[224:227], v97, s[20:23], s34 offen\n\t"
        "s_add_u32 s34, s34, 0x8000\n\t"
        "buffer_load_dwordx4 a[228:231], v97, s[20:23], s34 offen\n\t"
        "s_add_u32 s34, s34, 0x8000\n\t"
        "buffer_load_dwordx4 a[232:235], v97, s[20:23], s34 offen\n\t"
        "s_add_u32 s34, s34, 0x8000\n\t"
        "buffer_load_dwordx4 a[236:239], v97, s[20:23], s34 offen\n\t"
        "s_add_u32 s34, s34, 0x8000\n\t"
        "buffer_load_dwordx4 a[240:243], v97, s[20:23], s34 offen\n\t"
        "s_add_u32 s34, s34, 0x8000\n\t"
        "buffer_load_dwordx4 a[244:247], v97, s[20:23], s34 offen\n\t"
        "s_add_u32 s34, s34, 0x8000\n\t"
        "buffer_load_dwordx4 a[248:251], v97, s[20:23], s34 offen\n\t"
        "s_add_u32 s34, s34, 0x8000\n\t"
        "buffer_load_dwordx4 a[252:255], v97, s[20:23], s34 offen\n\t"
        "s_waitcnt lgkmcnt(0)\n\t"
        "s_waitcnt vmcnt(16)\n\t"
        "ds_write_b128 v122, a[160:163] offset:0\n\t"
        "ds_write_b128 v122, a[164:167] offset:1056\n\t"
        "ds_write_b128 v122, a[168:171] offset:2176\n\t"
        "ds_write_b128 v122, a[172:175] offset:3232\n\t"
        "ds_write_b128 v122, a[176:179] offset:4352\n\t"
        "ds_write_b128 v122, a[180:183] offset:5408\n\t"
        "ds_write_b128 v122, a[184:187] offset:6528\n\t"
        "ds_write_b128 v122, a[188:191] offset:7584\n\t"
        "ds_read_b64_tr_b16 a[160:161], v123 offset:0\n\t"
        "ds_read_b64_tr_b16 a[162:163], v123 offset:128\n\t"
        "ds_read_b64_tr_b16 a[164:165], v123 offset:4352\n\t"
        "ds_read_b64_tr_b16 a[166:167], v123 offset:4480\n\t"
        "s_waitcnt lgkmcnt(8)\n\t"
        "ds_read_b64_tr_b16 a[168:169], v123 offset:32\n\t"
        "ds_read_b64_tr_b16 a[170:171], v123 offset:160\n\t"
        "ds_read_b64_tr_b16 a[172:173], v123 offset:4384\n\t"
        "ds_read_b64_tr_b16 a[174:175], v123 offset:4512\n\t"
        "s_waitcnt lgkmcnt(8)\n\t"
        "ds_read_b64_tr_b16 a[176:177], v123 offset:512\n\t"
        "ds_read_b64_tr_b16 a[178:179], v123 offset:640\n\t"
        "ds_read_b64_tr_b16 a[180:181], v123 offset:4864\n\t"
        "ds_read_b64_tr_b16 a[182:183], v123 offset:4992\n\t"
        "s_waitcnt lgkmcnt(8)\n\t"
        "ds_read_b64_tr_b16 a[184:185], v123 offset:544\n\t"
        "ds_read_b64_tr_b16 a[186:187], v123 offset:672\n\t"
        "ds_read_b64_tr_b16 a[188:189], v123 offset:4896\n\t"
        "ds_read_b64_tr_b16 a[190:191], v123 offset:5024\n\t"
        "s_waitcnt lgkmcnt(8)\n\t"
        "s_waitcnt lgkmcnt(0)\n\t"
        "s_waitcnt vmcnt(8)\n\t"
        "ds_write_b128 v122, a[192:195] offset:0\n\t"
        "ds_write_b128 v122, a[196:199] offset:1056\n\t"
        "ds_write_b128 v122, a[200:203] offset:2176\n\t"
        "ds_write_b128 v122, a[204:207] offset:3232\n\t"
        "ds_write_b128 v122, a[208:211] offset:4352\n\t"
        "ds_write_b128 v122, a[212:215] offset:5408\n\t"
        "ds_write_b128 v122, a[216:219] offset:6528\n\t"
        "ds_write_b128 v122, a[220:223] offset:7584\n\t"
        "ds_read_b64_tr_b16 a[192:193], v123 offset:0\n\t"
        "ds_read_b64_tr_b16 a[194:195], v123 offset:128\n\t"
        "ds_read_b64_tr_b16 a[196:197], v123 offset:4352\n\t"
        "ds_read_b64_tr_b16 a[198:199], v123 offset:4480\n\t"
        "s_waitcnt lgkmcnt(8)\n\t"
        "ds_read_b64_tr_b16 a[200:201], v123 offset:32\n\t"
        "ds_read_b64_tr_b16 a[202:203], v123 offset:160\n\t"
        "ds_read_b64_tr_b16 a[204:205], v123 offset:4384\n\t"
        "ds_read_b64_tr_b16 a[206:207], v123 offset:4512\n\t"
        "s_waitcnt lgkmcnt(8)\n\t"
        "ds_read_b64_tr_b16 a[208:209], v123 offset:512\n\t"
        "ds_read_b64_tr_b16 a[210:211], v123 offset:640\n\t"
        "ds_read_b64_tr_b16 a[212:213], v123 offset:4864\n\t"
        "ds_read_b64_tr_b16 a[214:215], v123 offset:4992\n\t"
        "s_waitcnt lgkmcnt(8)\n\t"
        "ds_read_b64_tr_b16 a[216:217], v123 offset:544\n\t"
        "ds_read_b64_tr_b16 a[218:219], v123 offset:672\n\t"
        "ds_read_b64_tr_b16 a[220:221], v123 offset:4896\n\t"
        "ds_read_b64_tr_b16 a[222:223], v123 offset:5024\n\t"
        "s_waitcnt lgkmcnt(8)\n\t"
        "s_waitcnt lgkmcnt(0)\n\t"
        "s_waitcnt vmcnt(0)\n\t"
        "ds_write_b128 v122, a[224:227] offset:0\n\t"
        "ds_write_b128 v122, a[228:231] offset:1056\n\t"
        "ds_write_b128 v122, a[232:235] offset:2176\n\t"
        "ds_write_b128 v122, a[236:239] offset:3232\n\t"
        "ds_write_b128 v122, a[240:243] offset:4352\n\t"
        "ds_write_b128 v122, a[244:247] offset:5408\n\t"
        "ds_write_b128 v122, a[248:251] offset:6528\n\t"
        "ds_write_b128 v122, a[252:255] offset:7584\n\t"
        "ds_read_b64_tr_b16 a[224:225], v123 offset:0\n\t"
        "ds_read_b64_tr_b16 a[226:227], v123 offset:128\n\t"
        "ds_read_b64_tr_b16 a[228:229], v123 offset:4352\n\t"
        "ds_read_b64_tr_b16 a[230:231], v123 offset:4480\n\t"
        "s_waitcnt lgkmcnt(8)\n\t"
        "ds_read_b64_tr_b16 a[232:233], v123 offset:32\n\t"
        "ds_read_b64_tr_b16 a[234:235], v123 offset:160\n\t"
        "ds_read_b64_tr_b16 a[236:237], v123 offset:4384\n\t"
        "ds_read_b64_tr_b16 a[238:239], v123 offset:4512\n\t"
        "s_waitcnt lgkmcnt(8)\n\t"
        "ds_read_b64_tr_b16 a[240:241], v123 offset:512\n\t"
        "ds_read_b64_tr_b16 a[242:243], v123 offset:640\n\t"
        "ds_read_b64_tr_b16 a[244:245], v123 offset:4864\n\t"
        "ds_read_b64_tr_b16 a[246:247], v123 offset:4992\n\t"
        "s_waitcnt lgkmcnt(8)\n\t"
        "ds_read_b64_tr_b16 a[248:249], v123 offset:544\n\t"
        "ds_read_b64_tr_b16 a[250:251], v123 offset:672\n\t"
        "ds_read_b64_tr_b16 a[252:253], v123 offset:4896\n\t"
        "ds_read_b64_tr_b16 a[254:255], v123 offset:5024\n\t"
        "s_waitcnt lgkmcnt(8)\n\t"
        "s_mov_b32 s35, 33792\n\t"
        "buffer_load_dword v0, off, s[12:15], s35 sc1\n\t"
        "s_waitcnt lgkmcnt(0)\n\t"
        "s_waitcnt vmcnt(0)\n\t"
        "s_mov_b32 s42, 0\n\t"
        "LB_cal%=:\n\t"
        "v_readfirstlane_b32 s34, v0\n\t"
        "s_nop 0\n\t"
        "s_cmp_lg_u32 s34, 0\n\t"
        "s_cbranch_scc1 LB_calok%=\n\t"
        "s_sleep 4\n\t"
        "s_mov_b32 s35, 33792\n\t"
        "buffer_load_dword v0, off, s[12:15], s35 sc1\n\t"
        "s_waitcnt vmcnt(0)\n\t"
        "s_add_u32 s42, s42, 1\n\t"
        "s_cmp_lt_u32 s42, 0x100000\n\t"
        "s_cbranch_scc1 LB_cal%=\n\t"
        "s_movk_i32 s34, 8\n\t"
        "s_branch LB_die%=\n\t"
        "LB_calok%=:\n\t"
        "v_readfirstlane_b32 s34, v0\n\t"
        "s_nop 0\n\t"
        "s_and_b32 s34, s34, 8192\n\t"
        "s_add_u32 s8, s8, s34\n\t"
        "s_addc_u32 s9, s9, 0\n\t"
        "v_min_u32_e32 v109, -2, v109\n\t"
        "buffer_store_dword v109, v116, s[8:11], 0 offen sc1\n\t"
        "s_memtime s[74:75]\n\t"
        "s_memrealtime s[32:33]\n\t"
        "s_waitcnt lgkmcnt(0)\n\t"
        "v_mov_b32_e32 v110, s32\n\t"
        "s_sub_u32 s34, s74, s70\n\t"
        "s_sub_u32 s35, s32, s72\n\t"
        "v_cvt_f32_u32_e32 v2, s34\n\t"
        "v_cvt_f32_u32_e32 v3, s35\n\t"
        "v_rcp_f32_e32 v2, v2\n\t"
        "s_nop 0\n\t"
        "v_mul_f32_e32 v2, v2, v3\n\t"
        "v_mul_f32_e32 v3, 0x45354000, v2\n\t"
        "v_cvt_u32_f32_e32 v3, v3\n\t"
        "s_nop 0\n\t"
        "v_readfirstlane_b32 s34, v3\n\t"
        "s_add_u32 s35, s29, 40\n\t"
        "s_min_u32 s34, s34, s35\n\t"
        "s_sub_u32 s35, s29, 10\n\t"
        "s_max_u32 s29, s34, s35\n\t"
        "v_mul_f32_e32 v3, 0x44d70000, v2\n\t"
        "v_cvt_u32_f32_e32 v3, v3\n\t"
        "s_nop 0\n\t"
        "v_readfirstlane_b32 s34, v3\n\t"
        "s_add_u32 s35, s30, 40\n\t"
        "s_min_u32 s34, s34, s35\n\t"
        "s_sub_u32 s35, s30, 10\n\t"
        "s_max_u32 s30, s34, s35\n\t"
        "s_lshl_b32 s34, s29, 8\n\t"
        "s_lshl_b32 s35, s30, 20\n\t"
        "s_or_b32 s54, s34, s35\n\t"
        "s_add_u32 s34, s54, 8392192\n\t"
        "v_mov_b32_e32 v111, s34\n\t"
        "s_barrier\n\t"
        "s_cmp_lg_u32 s43, 0\n\t"
        "s_cbranch_scc1 LB_st%=\n\t"
        "v_mov_b32_e32 v2, 1\n\t"
        "s_mov_b32 s35, 34304\n\t"
        "s_mov_b64 exec, 1\n\t"
        "buffer_atomic_add v2, off, s[12:15], s35 sc0\n\t"
        "s_mov_b64 exec, -1\n\t"
        "s_waitcnt vmcnt(0)\n\t"
        "v_readfirstlane_b32 s34, v2\n\t"
        "s_nop 0\n\t"
        "s_cmp_lg_u32 s34, 95\n\t"
        "s_cbranch_scc1 LB_st%=\n\t"
        "s_memrealtime s[32:33]\n\t"
        "v_mbcnt_lo_u32_b32 v3, -1, 0\n\t"
        "v_lshlrev_b32_e32 v3, 7, v3\n\t"
        "s_waitcnt lgkmcnt(0)\n\t"
        "s_add_u32 s34, s32, 50\n\t"
        "s_or_b32 s34, s34, 1\n\t"
        "v_mov_b32_e32 v2, s34\n\t"
        "s_mov_b32 s35, 36864\n\t"
        "s_mov_b64 exec, 0xffff\n\t"
        "buffer_store_dword v2, v3, s[12:15], s35 offen sc1\n\t"
        "s_mov_b64 exec, -1\n\t"
        "LB_st%=:\n\t"
        "s_mov_b32 s24, 0\n\t"
        "s_mov_b32 s26, 0\n\t"
        "s_mov_b32 s27, 0x80000\n\t"
        "s_mov_b32 s28, 0x180000\n\t"
        "s_mov_b32 s31, 0\n\t"
        "s_mov_b32 s47, 0\n\t"
        "s_mov_b32 s38, 0xff00ff00\n\t"
        "s_mov_b32 s39, 0xff00ff00\n\t"
        "s_mov_b32 s40, 0\n\t"
        "s_mov_b32 s41, -1\n\t"
        "v_mov_b64_e32 v[0:1], 0\n\t"
        "v_mov_b64_e32 v[2:3], 0\n\t"
        "v_mov_b64_e32 v[4:5], 0\n\t"
        "v_mov_b64_e32 v[6:7], 0\n\t"
        "v_mov_b64_e32 v[8:9], 0\n\t"
        "v_mov_b64_e32 v[10:11], 0\n\t"
        "v_mov_b64_e32 v[12:13], 0\n\t"
        "v_mov_b64_e32 v[14:15], 0\n\t"
        "s_mov_b32 s42, 0\n\t"
        "LB_gw%=:\n\t"
        "buffer_load_dword v16, off, s[12:15], s45 sc1\n\t"
        "s_waitcnt vmcnt(0)\n\t"
        "v_readfirstlane_b32 s25, v16\n\t"
        "s_nop 0\n\t"
        "s_cmp_lg_u32 s25, 0\n\t"
        "s_cbranch_scc1 LB_loop%=\n\t"
        "s_sleep 2\n\t"
        "s_add_u32 s42, s42, 1\n\t"
        "s_cmp_lt_u32 s42, 0x100000\n\t"
        "s_cbranch_scc1 LB_gw%=\n\t"
        "s_movk_i32 s34, 9\n\t"
        "s_branch LB_die%=\n\t"
        ".p2align 8\n\t"
        "LB_loop%=:\n\t"
        "s_mov_b32 s42, 0\n\t"
        "s_sub_u32 s35, s25, 8\n\t"
        "s_waitcnt lgkmcnt(0)\n\t"
        "s_memrealtime s[2:3]\n\t"
        "s_memrealtime s[4:5]\n\t"
        "s_memrealtime s[6:7]\n\t"
        "LB_wa%=:\n\t"
        "s_memrealtime s[32:33]\n\t"
        "s_waitcnt lgkmcnt(6)\n\t"
        "s_sub_u32 s34, s35, s2\n\t"
        "s_cmpk_lt_u32 s34, 0x2000\n\t"
        "s_cbranch_scc0 LB_wa%=x\n\t"
        "s_memrealtime s[2:3]\n\t"
        "s_waitcnt lgkmcnt(6)\n\t"
        "s_sub_u32 s34, s35, s4\n\t"
        "s_cmpk_lt_u32 s34, 0x2000\n\t"
        "s_cbranch_scc0 LB_wa%=x\n\t"
        "s_memrealtime s[4:5]\n\t"
        "s_waitcnt lgkmcnt(6)\n\t"
        "s_sub_u32 s34, s35, s6\n\t"
        "s_cmpk_lt_u32 s34, 0x2000\n\t"
        "s_cbranch_scc0 LB_wa%=x\n\t"
        "s_memrealtime s[6:7]\n\t"
        "s_waitcnt lgkmcnt(6)\n\t"
        "s_sub_u32 s34, s35, s32\n\t"
        "s_cmpk_lt_u32 s34, 0x2000\n\t"
        "s_cbranch_scc1 LB_wa%=\n\t"
        "s_nop 3\n\t"
        "LB_wa%=x:\n\t"
        "s_add_i32 s34, s34, 4\n\t"
        "s_max_i32 s34, s34, 0\n\t"
        "s_bitcmp1_b32 s34, 1\n\t"
        "s_cbranch_scc0 LB_wa%=y\n\t"
        "s_nop 15\n\t"
        "LB_wa%=y:\n\t"
        "s_bitcmp1_b32 s34, 0\n\t"
        "s_cbranch_scc0 LB_wa%=z\n\t"
        "s_nop 9\n\t"
        "LB_wa%=z:\n\t"
        "LB_rd%=:\n\t"
        "buffer_load_dwordx4 v[16:19], v112, s[8:11], s26 offen offset:0 sc1\n\t"
        "s_nop 9\n\t"
        "buffer_load_dwordx4 v[20:23], v112, s[8:11], s26 offen offset:1024 sc1\n\t"
        "s_nop 9\n\t"
        "buffer_load_dwordx4 v[24:27], v112, s[8:11], s26 offen offset:2048 sc1\n\t"
        "s_nop 9\n\t"
        "buffer_load_dwordx4 v[28:31], v112, s[8:11], s26 offen offset:3072 sc1\n\t"
        "LB_go%=:\n\t"
        "s_waitcnt vmcnt(3)\n\t"
        "v_smfmac_f32_16x16x64_bf16 v[0:3], v[16:19], v[128:135], v106\n\t"
        "v_max3_u32 v108, v16, v17, v18\n\t"
        "v_max_u32_e32 v108, v19, v108\n\t"
        "v_smfmac_f32_16x16x64_bf16 v[4:7], v[16:19], v[136:143], v106\n\t"
        "v_smfmac_f32_16x16x64_bf16 v[8:11], v[16:19], v[144:151], v106\n\t"
        "v_smfmac_f32_16x16x64_bf16 v[12:15], v[16:19], v[152:159], v106\n\t"
        "s_waitcnt vmcnt(2)\n\t"
        "v_smfmac_f32_16x16x64_bf16 v[0:3], v[20:23], v[160:167], v106\n\t"
        "v_max3_u32 v108, v20, v21, v108\n\t"
        "v_max3_u32 v108, v22, v23, v108\n\t"
        "v_smfmac_f32_16x16x64_bf16 v[4:7], v[20:23], v[168:175], v106\n\t"
        "v_smfmac_f32_16x16x64_bf16 v[8:11], v[20:23], v[176:183], v106\n\t"
        "v_smfmac_f32_16x16x64_bf16 v[12:15], v[20:23], v[184:191], v106\n\t"
        "s_waitcnt vmcnt(1)\n\t"
        "v_smfmac_f32_16x16x64_bf16 v[0:3], v[24:27], v[192:199], v106\n\t"
        "v_max3_u32 v108, v24, v25, v108\n\t"
        "v_max3_u32 v108, v26, v27, v108\n\t"
        "v_smfmac_f32_16x16x64_bf16 v[4:7], v[24:27], v[200:207], v106\n\t"
        "v_smfmac_f32_16x16x64_bf16 v[8:11], v[24:27], v[208:215], v106\n\t"
        "v_smfmac_f32_16x16x64_bf16 v[12:15], v[24:27], v[216:223], v106\n\t"
        "s_waitcnt vmcnt(0)\n\t"
        "v_smfmac_f32_16x16x64_bf16 v[0:3], v[28:31], v[224:231], v106\n\t"
        "v_max3_u32 v108, v28, v29, v108\n\t"
        "v_max3_u32 v108, v30, v31, v108\n\t"
        "v_smfmac_f32_16x16x64_bf16 v[4:7], v[28:31], v[232:239], v106\n\t"
        "v_cmp_eq_u32_e32 vcc, -1, v108\n\t"
        "v_smfmac_f32_16x16x64_bf16 v[8:11], v[28:31], v[240:247], v106\n\t"
        "v_smfmac_f32_16x16x64_bf16 v[12:15], v[28:31], v[248:255], v106\n\t"
        "s_cbranch_vccnz LB_rdfail%=\n\t"
        "s_nop 0\n\t"
        "v_add_f32_e32 v0, v0, v1\n\t"
        "v_add_f32_e32 v1, v2, v3\n\t"
        "ds_write_b64 v118, v[0:1] offset:0\n\t"
        "v_add_f32_e32 v4, v4, v5\n\t"
        "v_add_f32_e32 v5, v6, v7\n\t"
        "ds_write_b64 v118, v[4:5] offset:512\n\t"
        "v_add_f32_e32 v8, v8, v9\n\t"
        "v_add_f32_e32 v9, v10, v11\n\t"
        "ds_write_b64 v118, v[8:9] offset:1024\n\t"
        "v_add_f32_e32 v12, v12, v13\n\t"
        "v_add_f32_e32 v13, v14, v15\n\t"
        "ds_write_b64 v118, v[12:13] offset:1536\n\t"
        "buffer_store_dwordx2 v[110:111], v117, s[8:11], s26 offen sc1\n\t"
        "s_waitcnt lgkmcnt(0)\n\t"
        "s_barrier\n\t"
        "ds_read_b128 v[16:19], v119 offset:0\n\t"
        "ds_read_b128 v[20:23], v119 offset:2048\n\t"
        "ds_read_b128 v[24:27], v119 offset:4096\n\t"
        "ds_read_b128 v[28:31], v119 offset:6144\n\t"
        "buffer_load_dwordx4 v[64:67], v124, s[8:11], s28 offen sc1\n\t"
        "s_waitcnt lgkmcnt(2)\n\t"
        "v_pk_add_f32 v[16:17], v[16:17], v[20:21]\n\t"
        "v_pk_add_f32 v[18:19], v[18:19], v[22:23]\n\t"
        "s_waitcnt lgkmcnt(0)\n\t"
        "v_pk_add_f32 v[24:25], v[24:25], v[28:29]\n\t"
        "v_pk_add_f32 v[26:27], v[26:27], v[30:31]\n\t"
        "v_pk_add_f32 v[16:17], v[16:17], v[24:25]\n\t"
        "v_pk_add_f32 v[18:19], v[18:19], v[26:27]\n\t"
        "v_cvt_pk_bf16_f32 v20, v16, v17\n\t"
        "v_cvt_pk_bf16_f32 v21, v18, v19\n\t"
        "buffer_store_dwordx2 v[20:21], v114, s[8:11], s26 offen sc1\n\t"
        "s_cmp_eq_u32 s24, 63\n\t"
        "s_cbranch_scc1 LB_end%=\n\t"
        "s_memrealtime s[52:53]\n\t"
        "v_mov_b64_e32 v[24:25], -1\n\t"
        "v_mov_b64_e32 v[32:33], 0\n\t"
        "v_mov_b64_e32 v[34:35], 0\n\t"
        "v_mov_b64_e32 v[36:37], 0\n\t"
        "v_mov_b64_e32 v[38:39], 0\n\t"
        "v_mov_b64_e32 v[40:41], 0\n\t"
        "v_mov_b64_e32 v[42:43], 0\n\t"
        "v_mov_b64_e32 v[44:45], 0\n\t"
        "v_mov_b64_e32 v[46:47], 0\n\t"
        "v_mov_b64_e32 v[0:1], 0\n\t"
        "v_mov_b64_e32 v[2:3], 0\n\t"
        "v_mov_b64_e32 v[4:5], 0\n\t"
        "v_mov_b64_e32 v[6:7], 0\n\t"
        "v_mov_b64_e32 v[8:9], 0\n\t"
        "v_mov_b64_e32 v[10:11], 0\n\t"
        "v_mov_b64_e32 v[12:13], 0\n\t"
        "v_mov_b64_e32 v[14:15], 0\n\t"
        "s_add_u32 s46, s26, 4096\n\t"
        "s_sleep 1\n\t"
        "buffer_load_dwordx2 v[104:105], v113, s[8:11], s26 offen sc1\n\t"
        "buffer_store_dword v24, v116, s[8:11], s28 offen sc1\n\t"
        "s_xor_b32 s34, s26, 0x100000\n\t"
        "buffer_store_dwordx2 v[24:25], v114, s[8:11], s34 offen sc1\n\t"
        "s_add_u32 s66, s49, s43\n\t"
        "s_cmp_lg_u32 s66, 32\n\t"
        "s_cbranch_scc1 LB_norep%=\n\t"
        "s_waitcnt vmcnt(4)\n\t"
        "s_sub_u32 s66, s24, 1\n\t"
        "v_lshrrev_b32_e32 v68, 16, v64\n\t"
        "v_cmp_eq_u32_e32 vcc, s66, v68\n\t"
        "v_subrev_u32_e32 v64, s59, v64\n\t"
        "v_bfe_i32 v64, v64, 0, 16\n\t"
        "v_cndmask_b32_e32 v64, 0, v64, vcc\n\t"
        "v_lshrrev_b32_e32 v68, 16, v65\n\t"
        "v_cmp_eq_u32_e32 vcc, s66, v68\n\t"
        "v_subrev_u32_e32 v65, s59, v65\n\t"
        "v_bfe_i32 v65, v65, 0, 16\n\t"
        "v_cndmask_b32_e32 v65, 0, v65, vcc\n\t"
        "v_lshrrev_b32_e32 v68, 16, v66\n\t"
        "v_cmp_eq_u32_e32 vcc, s66, v68\n\t"
        "v_subrev_u32_e32 v66, s59, v66\n\t"
        "v_bfe_i32 v66, v66, 0, 16\n\t"
        "v_cndmask_b32_e32 v66, 0, v66, vcc\n\t"
        "v_lshrrev_b32_e32 v68, 16, v67\n\t"
        "v_cmp_eq_u32_e32 vcc, s66, v68\n\t"
        "v_subrev_u32_e32 v67, s59, v67\n\t"
        "v_bfe_i32 v67, v67, 0, 16\n\t"
        "v_cndmask_b32_e32 v67, 0, v67, vcc\n\t"
        "v_max3_i32 v64, v64, v65, v66\n\t"
        "v_max_i32_e32 v64, v64, v67\n\t"
        "s_nop 1\n\t"
        "v_max_i32_dpp v64, v64, v64 quad_perm:[1,0,3,2] row_mask:0xf bank_mask:0xf\n\t"
        "s_nop 1\n\t"
        "v_max_i32_dpp v64, v64, v64 quad_perm:[2,3,0,1] row_mask:0xf bank_mask:0xf\n\t"
        "s_nop 1\n\t"
        "v_max_i32_dpp v64, v64, v64 row_ror:4 row_mask:0xf bank_mask:0xf\n\t"
        "s_nop 1\n\t"
        "v_max_i32_dpp v64, v64, v64 row_ror:8 row_mask:0xf bank_mask:0xf\n\t"
        "v_mov_b32_e32 v65, v64\n\t"
        "s_nop 1\n\t"
        "v_permlane16_swap_b32_e32 v64, v65\n\t"
        "v_max_i32_e32 v64, v64, v65\n\t"
        "v_mov_b32_e32 v65, v64\n\t"
        "s_nop 1\n\t"
        "v_permlane32_swap_b32_e32 v64, v65\n\t"
        "v_max_i32_e32 v64, v64, v65\n\t"
        "s_nop 0\n\t"
        "v_readfirstlane_b32 s64, v64\n\t"
        "LB_norep%=:\n\t"
        "s_waitcnt vmcnt(2) lgkmcnt(0)\n\t"
        "s_mov_b32 s61, s29\n\t"
        "s_mov_b32 s62, s30\n\t"
        "s_mov_b32 s63, s31\n\t"
        "v_and_b32_e32 v30, 0xff, v105\n\t"
        "v_cmp_ne_u32_e32 vcc, s24, v30\n\t"
        "s_nop 0\n\t"
        "s_cbranch_vccnz LB_stre%=\n\t"
        "LB_stok%=:\n\t"
        "v_subrev_u32_e32 v104, s25, v104\n\t"
        "s_nop 1\n\t"
        "v_max_i32_dpp v104, v104, v104 quad_perm:[1,0,3,2] row_mask:0xf bank_mask:0xf\n\t"
        "s_nop 1\n\t"
        "v_max_i32_dpp v104, v104, v104 quad_perm:[2,3,0,1] row_mask:0xf bank_mask:0xf\n\t"
        "s_nop 1\n\t"
        "v_max_i32_dpp v104, v104, v104 row_ror:4 row_mask:0xf bank_mask:0xf\n\t"
        "s_nop 1\n\t"
        "v_max_i32_dpp v104, v104, v104 row_ror:8 row_mask:0xf bank_mask:0xf\n\t"
        "s_nop 1\n\t"
        "v_max_i32_dpp v104, v104, v104 row_bcast:15 row_mask:0xa bank_mask:0xf\n\t"
        "s_nop 1\n\t"
        "v_max_i32_dpp v104, v104, v104 row_bcast:31 row_mask:0xc bank_mask:0xf\n\t"
        "v_readfirstlane_b32 s35, v105\n\t"
        "v_readlane_b32 s36, v104, 63\n\t"
        "s_bfe_u32 s29, s35, 0xc0008\n\t"
        "s_lshr_b32 s30, s35, 20\n\t"
        "s_add_u32 s35, s31, s50\n\t"
        "s_mul_i32 s37, s50, 3\n\t"
        "s_min_u32 s35, s35, s37\n\t"
        "s_add_i32 s34, s36, s51\n\t"
        "s_cmp_gt_i32 s34, 0\n\t"
        "s_cselect_b32 s31, s35, 0\n\t"
        "s_cselect_b32 s34, s34, 0\n\t"
        "s_add_u32 s34, s34, s31\n\t"
        "s_add_u32 s37, s25, s34\n\t"
        "s_add_u32 s45, s37, s29\n\t"
        "s_add_u32 s25, s45, s30\n\t"
        "s_add_u32 s66, s49, s43\n\t"
        "s_cmp_lg_u32 s66, 32\n\t"
        "s_cbranch_scc1 LB_noctl%=\n\t"
        "s_cmp_eq_u32 s24, 0\n\t"
        "s_cbranch_scc1 LB_ctl0%=\n\t"
        "s_cmp_lt_u32 s24, 6\n\t"
        "s_cselect_b32 s68, 32, 8\n\t"
        "s_add_i32 s65, s36, s62\n\t"
        "s_cmp_eq_u32 s24, 1\n\t"
        "s_cselect_b32 s34, 6, 0\n\t"
        "s_cselect_b32 s35, 3, 0\n\t"
        "s_sub_u32 s64, s64, s34\n\t"
        "s_sub_u32 s65, s65, s35\n\t"
        "s_lshl_b32 s66, s65, 1\n\t"
        "s_sub_i32 s66, s66, s56\n\t"
        "s_cmp_gt_i32 s66, 56\n\t"
        "s_cbranch_scc1 LB_c1%=\n\t"
        "s_lshl_b32 s66, s65, 1\n\t"
        "s_add_u32 s34, s56, s68\n\t"
        "s_min_i32 s34, s34, s66\n\t"
        "s_sub_u32 s35, s56, s66\n\t"
        "s_ashr_i32 s35, s35, 1\n\t"
        "s_max_i32 s35, s35, 1\n\t"
        "s_sub_u32 s35, s56, s35\n\t"
        "s_max_i32 s56, s34, s35\n\t"
        "s_branch LB_c2%=\n\t"
        "LB_c1%=:\n\t"
        "s_add_u32 s56, s56, 2\n\t"
        "s_add_i32 s66, s64, s57\n\t"
        "s_cmp_gt_i32 s66, s61\n\t"
        "s_cbranch_scc1 LB_c2%=\n\t"
        "s_add_u32 s57, s57, 4\n\t"
        "s_min_u32 s57, s57, 24\n\t"
        "LB_c2%=:\n\t"
        "s_cmp_lg_u32 s63, 0\n\t"
        "s_cbranch_scc1 LB_c4%=\n\t"
        "s_lshl_b32 s66, s64, 1\n\t"
        "s_sub_i32 s66, s66, s55\n\t"
        "s_cmp_gt_i32 s66, 56\n\t"
        "s_cbranch_scc1 LB_c3%=\n\t"
        "s_lshl_b32 s66, s64, 1\n\t"
        "s_add_u32 s34, s55, s68\n\t"
        "s_min_i32 s34, s34, s66\n\t"
        "s_sub_u32 s35, s55, s66\n\t"
        "s_ashr_i32 s35, s35, 1\n\t"
        "s_max_i32 s35, s35, 1\n\t"
        "s_sub_u32 s35, s55, s35\n\t"
        "s_max_i32 s55, s34, s35\n\t"
        "s_branch LB_c4%=\n\t"
        "LB_c3%=:\n\t"
        "s_add_u32 s55, s55, 2\n\t"
        "s_cmp_lt_i32 s60, s58\n\t"
        "s_cbranch_scc1 LB_c4%=\n\t"
        "s_add_u32 s66, s58, 8\n\t"
        "s_cmp_gt_i32 s60, s66\n\t"
        "s_cbranch_scc1 LB_c4%=\n\t"
        "s_add_u32 s58, s58, 4\n\t"
        "s_min_u32 s58, s58, 22\n\t"
        "LB_c4%=:\n\t"
        "s_and_b32 s66, s24, 3\n\t"
        "s_cmp_eq_u32 s66, 0\n\t"
        "s_cselect_b32 s66, 1, 0\n\t"
        "s_cmp_lt_u32 s24, 30\n\t"
        "s_cselect_b32 s34, 8, 8\n\t"
        "s_cselect_b32 s35, 6, 6\n\t"
        "s_sub_u32 s57, s57, s66\n\t"
        "s_max_i32 s57, s57, s34\n\t"
        "s_sub_u32 s58, s58, s66\n\t"
        "s_max_i32 s58, s58, s35\n\t"
        "s_add_u32 s34, s55, 1\n\t"
        "s_lshr_b32 s34, s34, 1\n\t"
        "s_add_u32 s34, s34, s57\n\t"
        "s_min_u32 s34, s34, 1000\n\t"
        "s_lshl_b32 s34, s34, 8\n\t"
        "s_add_u32 s35, s56, 1\n\t"
        "s_lshr_b32 s35, s35, 1\n\t"
        "s_add_u32 s35, s35, s58\n\t"
        "s_min_u32 s35, s35, 1000\n\t"
        "s_lshl_b32 s35, s35, 20\n\t"
        "s_or_b32 s54, s34, s35\n\t"
        "s_branch LB_ctl1%=\n\t"
        "LB_ctl0%=:\n\t"
        "s_movk_i32 s57, 8\n\t"
        "s_movk_i32 s58, 6\n\t"
        "s_sub_u32 s55, s29, 22\n\t"
        "s_lshl_b32 s55, s55, 1\n\t"
        "s_sub_u32 s56, s30, 14\n\t"
        "s_lshl_b32 s56, s56, 1\n\t"
        "LB_ctl1%=:\n\t"
        "s_mov_b32 s59, s37\n\t"
        "s_sub_u32 s60, 0, s36\n\t"
        "LB_noctl%=:\n\t"
        "s_cmp_eq_u32 s24, 0\n\t"
        "s_cselect_b32 s34, 1537, 1\n\t"
        "s_add_u32 s34, s34, s24\n\t"
        "s_add_u32 s34, s34, s54\n\t"
        "v_mov_b32_e32 v111, s34\n\t"
        "s_mov_b32 s42, 0\n\t"
        "s_sub_u32 s35, s45, 8\n\t"
        "s_waitcnt lgkmcnt(0)\n\t"
        "s_memrealtime s[2:3]\n\t"
        "s_memrealtime s[4:5]\n\t"
        "s_memrealtime s[6:7]\n\t"
        "LB_wo%=:\n\t"
        "s_memrealtime s[32:33]\n\t"
        "s_waitcnt lgkmcnt(6)\n\t"
        "s_sub_u32 s34, s35, s2\n\t"
        "s_cmpk_lt_u32 s34, 0x2000\n\t"
        "s_cbranch_scc0 LB_wo%=x\n\t"
        "s_memrealtime s[2:3]\n\t"
        "s_waitcnt lgkmcnt(6)\n\t"
        "s_sub_u32 s34, s35, s4\n\t"
        "s_cmpk_lt_u32 s34, 0x2000\n\t"
        "s_cbranch_scc0 LB_wo%=x\n\t"
        "s_memrealtime s[4:5]\n\t"
        "s_waitcnt lgkmcnt(6)\n\t"
        "s_sub_u32 s34, s35, s6\n\t"
        "s_cmpk_lt_u32 s34, 0x2000\n\t"
        "s_cbranch_scc0 LB_wo%=x\n\t"
        "s_memrealtime s[6:7]\n\t"
        "s_waitcnt lgkmcnt(6)\n\t"
        "s_sub_u32 s34, s35, s32\n\t"
        "s_cmpk_lt_u32 s34, 0x2000\n\t"
        "s_cbranch_scc1 LB_wo%=\n\t"
        "s_nop 3\n\t"
        "LB_wo%=x:\n\t"
        "s_add_i32 s34, s34, 4\n\t"
        "s_max_i32 s34, s34, 0\n\t"
        "s_bitcmp1_b32 s34, 1\n\t"
        "s_cbranch_scc0 LB_wo%=y\n\t"
        "s_nop 15\n\t"
        "LB_wo%=y:\n\t"
        "s_bitcmp1_b32 s34, 0\n\t"
        "s_cbranch_scc0 LB_wo%=z\n\t"
        "s_nop 9\n\t"
        "LB_wo%=z:\n\t"
        "LB_ro%=:\n\t"
        "buffer_load_dwordx4 v[64:67], v115, s[8:11], s26 offen offset:0 sc1\n\t"
        "s_nop 12\n\t"
        "buffer_load_dwordx4 v[68:71], v115, s[8:11], s26 offen offset:1024 sc1\n\t"
        "s_nop 12\n\t"
        "buffer_load_dwordx4 v[72:75], v115, s[8:11], s26 offen offset:2048 sc1\n\t"
        "s_nop 12\n\t"
        "buffer_load_dwordx4 v[76:79], v115, s[8:11], s26 offen offset:3072 sc1\n\t"
        "s_nop 12\n\t"
        "buffer_load_dwordx4 v[80:83], v115, s[8:11], s46 offen offset:0 sc1\n\t"
        "s_nop 12\n\t"
        "buffer_load_dwordx4 v[84:87], v115, s[8:11], s46 offen offset:1024 sc1\n\t"
        "s_nop 12\n\t"
        "buffer_load_dwordx4 v[88:91], v115, s[8:11], s46 offen offset:2048 sc1\n\t"
        "s_nop 12\n\t"
        "buffer_load_dwordx4 v[92:95], v115, s[8:11], s46 offen offset:3072 sc1\n\t"
        "LB_go2%=:\n\t"
        "s_waitcnt vmcnt(7)\n\t"
        "v_smfmac_f32_16x16x64_bf16 v[32:35], v[64:67], a[0:7], v106\n\t"
        "v_max3_u32 v108, v64, v65, v66\n\t"
        "v_max_u32_e32 v108, v67, v108\n\t"
        "v_smfmac_f32_16x16x64_bf16 v[40:43], v[64:67], a[16:23], v106\n\t"
        "v_smfmac_f32_16x16x64_bf16 v[36:39], v[64:67], a[8:15], v106\n\t"
        "v_smfmac_f32_16x16x64_bf16 v[44:47], v[64:67], a[24:31], v106\n\t"
        "s_waitcnt vmcnt(6)\n\t"
        "v_smfmac_f32_16x16x64_bf16 v[32:35], v[68:71], a[32:39], v106\n\t"
        "v_max3_u32 v108, v68, v69, v108\n\t"
        "v_max3_u32 v108, v70, v71, v108\n\t"
        "v_smfmac_f32_16x16x64_bf16 v[40:43], v[68:71], a[48:55], v106\n\t"
        "v_smfmac_f32_16x16x64_bf16 v[36:39], v[68:71], a[40:47], v106\n\t"
        "v_smfmac_f32_16x16x64_bf16 v[44:47], v[68:71], a[56:63], v106\n\t"
        "s_waitcnt vmcnt(5)\n\t"
        "v_smfmac_f32_16x16x64_bf16 v[32:35], v[72:75], a[64:71], v106\n\t"
        "v_max3_u32 v108, v72, v73, v108\n\t"
        "v_max3_u32 v108, v74, v75, v108\n\t"
        "v_smfmac_f32_16x16x64_bf16 v[40:43], v[72:75], a[80:87], v106\n\t"
        "v_smfmac_f32_16x16x64_bf16 v[36:39], v[72:75], a[72:79], v106\n\t"
        "v_smfmac_f32_16x16x64_bf16 v[44:47], v[72:75], a[88:95], v106\n\t"
        "s_waitcnt vmcnt(4)\n\t"
        "v_smfmac_f32_16x16x64_bf16 v[32:35], v[76:79], a[96:103], v106\n\t"
        "v_max3_u32 v108, v76, v77, v108\n\t"
        "v_max3_u32 v108, v78, v79, v108\n\t"
        "v_smfmac_f32_16x16x64_bf16 v[40:43], v[76:79], a[112:119], v106\n\t"
        "v_smfmac_f32_16x16x64_bf16 v[36:39], v[76:79], a[104:111], v106\n\t"
        "v_smfmac_f32_16x16x64_bf16 v[44:47], v[76:79], a[120:127], v106\n\t"
        "s_waitcnt vmcnt(3)\n\t"
        "v_smfmac_f32_16x16x64_bf16 v[32:35], v[80:83], a[128:135], v106\n\t"
        "v_max3_u32 v108, v80, v81, v108\n\t"
        "v_max3_u32 v108, v82, v83, v108\n\t"
        "v_smfmac_f32_16x16x64_bf16 v[40:43], v[80:83], a[144:151], v106\n\t"
        "v_smfmac_f32_16x16x64_bf16 v[36:39], v[80:83], a[136:143], v106\n\t"
        "v_smfmac_f32_16x16x64_bf16 v[44:47], v[80:83], a[152:159], v106\n\t"
        "s_waitcnt vmcnt(2)\n\t"
        "v_smfmac_f32_16x16x64_bf16 v[32:35], v[84:87], a[160:167], v106\n\t"
        "v_max3_u32 v108, v84, v85, v108\n\t"
        "v_max3_u32 v108, v86, v87, v108\n\t"
        "v_smfmac_f32_16x16x64_bf16 v[40:43], v[84:87], a[176:183], v106\n\t"
        "v_smfmac_f32_16x16x64_bf16 v[36:39], v[84:87], a[168:175], v106\n\t"
        "v_smfmac_f32_16x16x64_bf16 v[44:47], v[84:87], a[184:191], v106\n\t"
        "s_waitcnt vmcnt(1)\n\t"
        "v_smfmac_f32_16x16x64_bf16 v[32:35], v[88:91], a[192:199], v106\n\t"
        "v_max3_u32 v108, v88, v89, v108\n\t"
        "v_max3_u32 v108, v90, v91, v108\n\t"
        "v_smfmac_f32_16x16x64_bf16 v[40:43], v[88:91], a[208:215], v106\n\t"
        "v_smfmac_f32_16x16x64_bf16 v[36:39], v[88:91], a[200:207], v106\n\t"
        "v_smfmac_f32_16x16x64_bf16 v[44:47], v[88:91], a[216:223], v106\n\t"
        "s_waitcnt vmcnt(0)\n\t"
        "v_smfmac_f32_16x16x64_bf16 v[32:35], v[92:95], a[224:231], v106\n\t"
        "v_max3_u32 v108, v92, v93, v108\n\t"
        "v_max3_u32 v108, v94, v95, v108\n\t"
        "v_smfmac_f32_16x16x64_bf16 v[40:43], v[92:95], a[240:247], v106\n\t"
        "v_cmp_eq_u32_e32 vcc, -1, v108\n\t"
        "v_smfmac_f32_16x16x64_bf16 v[36:39], v[92:95], a[232:239], v106\n\t"
        "v_smfmac_f32_16x16x64_bf16 v[44:47], v[92:95], a[248:255], v106\n\t"
        "s_cbranch_vccnz LB_rofail%=\n\t"
        "s_nop 0\n\t"
        "v_add_f32_e32 v32, v32, v33\n\t"
        "s_nop 1\n\t"
        "v_add_f32_e32 v40, v42, v43\n\t"
        "v_add_f32_e32 v32, v32, v40\n\t"
        "v_add_f32_e32 v36, v36, v37\n\t"
        "v_add_f32_e32 v44, v46, v47\n\t"
        "v_add_f32_e32 v36, v36, v44\n\t"
        "ds_write2_b32 v120, v32, v36 offset0:0 offset1:64\n\t"
        "s_waitcnt lgkmcnt(0)\n\t"
        "s_barrier\n\t"
        "ds_read_b64 v[64:65], v121 offset:0\n\t"
        "ds_read_b64 v[66:67], v121 offset:512\n\t"
        "ds_read_b64 v[68:69], v121 offset:1024\n\t"
        "ds_read_b64 v[70:71], v121 offset:1536\n\t"
        "s_waitcnt lgkmcnt(2)\n\t"
        "v_pk_add_f32 v[64:65], v[64:65], v[66:67]\n\t"
        "s_waitcnt lgkmcnt(0)\n\t"
        "v_pk_add_f32 v[68:69], v[68:69], v[70:71]\n\t"
        "v_pk_add_f32 v[64:65], v[64:65], v[68:69]\n\t"
        "v_cvt_pk_bf16_f32 v64, v64, v65\n\t"
        "buffer_store_dword v64, v116, s[8:11], s27 offen sc1\n\t"
        "s_memrealtime s[32:33]\n\t"
        "s_waitcnt lgkmcnt(0)\n\t"
        "v_mov_b32_e32 v110, s32\n\t"
        "s_add_u32 s24, s24, 1\n\t"
        "s_mov_b32 s28, s26\n\t"
        "s_mov_b32 s26, s27\n\t"
        "s_add_u32 s27, s27, 0x80000\n\t"
        "s_and_b32 s27, s27, 0x1fffff\n\t"
        "s_branch LB_loop%=\n\t"
        "LB_end%=:\n\t"
        "s_endpgm\n\t"
        "LB_stre%=:\n\t"
        "s_mov_b32 s42, 0\n\t"
        "LB_str2%=:\n\t"
        "buffer_load_dwordx2 v[104:105], v113, s[8:11], s26 offen sc1\n\t"
        "s_waitcnt vmcnt(0)\n\t"
        "v_and_b32_e32 v30, 0xff, v105\n\t"
        "v_cmp_ne_u32_e32 vcc, s24, v30\n\t"
        "s_nop 0\n\t"
        "s_cbranch_vccz LB_stok%=\n\t"
        "s_add_u32 s42, s42, 1\n\t"
        "s_cmp_lt_u32 s42, 0x100000\n\t"
        "s_cbranch_scc1 LB_str2%=\n\t"
        "s_movk_i32 s34, 5\n\t"
        "s_branch LB_die%=\n\t"
        "LB_rdfail%=:\n\t"
        "s_or_b32 s47, s47, 1\n\t"
        "buffer_load_dwordx4 v[16:19], v112, s[8:11], s26 offen offset:0 sc1\n\t"
        "buffer_load_dwordx4 v[20:23], v112, s[8:11], s26 offen offset:1024 sc1\n\t"
        "buffer_load_dwordx4 v[24:27], v112, s[8:11], s26 offen offset:2048 sc1\n\t"
        "buffer_load_dwordx4 v[28:31], v112, s[8:11], s26 offen offset:3072 sc1\n\t"
        "s_nop 7\n\t"
        "v_mov_b64_e32 v[0:1], 0\n\t"
        "v_mov_b64_e32 v[2:3], 0\n\t"
        "v_mov_b64_e32 v[4:5], 0\n\t"
        "v_mov_b64_e32 v[6:7], 0\n\t"
        "v_mov_b64_e32 v[8:9], 0\n\t"
        "v_mov_b64_e32 v[10:11], 0\n\t"
        "v_mov_b64_e32 v[12:13], 0\n\t"
        "v_mov_b64_e32 v[14:15], 0\n\t"
        "s_branch LB_rdre%=w\n\t"
        "LB_rdre%=:\n\t"
        "buffer_load_dwordx4 v[16:19], v112, s[8:11], s26 offen offset:0 sc1\n\t"
        "buffer_load_dwordx4 v[20:23], v112, s[8:11], s26 offen offset:1024 sc1\n\t"
        "buffer_load_dwordx4 v[24:27], v112, s[8:11], s26 offen offset:2048 sc1\n\t"
        "buffer_load_dwordx4 v[28:31], v112, s[8:11], s26 offen offset:3072 sc1\n\t"
        "LB_rdre%=w:\n\t"
        "s_waitcnt vmcnt(0) lgkmcnt(0)\n\t"
        "v_max3_u32 v108, v16, v17, v18\n\t"
        "v_max3_u32 v108, v108, v19, v20\n\t"
        "v_max3_u32 v108, v108, v21, v22\n\t"
        "v_max3_u32 v108, v108, v23, v24\n\t"
        "v_max3_u32 v108, v108, v25, v26\n\t"
        "v_max3_u32 v108, v108, v27, v28\n\t"
        "v_max3_u32 v108, v108, v29, v30\n\t"
        "v_max_u32_e32 v108, v108, v31\n\t"
        "v_cmp_eq_u32_e32 vcc, -1, v108\n\t"
        "s_nop 0\n\t"
        "s_cbranch_vccz LB_go%=\n\t"
        "s_add_u32 s42, s42, 1\n\t"
        "s_cmp_lt_u32 s42, 0x100000\n\t"
        "s_cbranch_scc1 LB_rdre%=\n\t"
        "s_movk_i32 s34, 3\n\t"
        "s_branch LB_die%=\n\t"
        "LB_rofail%=:\n\t"
        "s_or_b32 s47, s47, 4\n\t"
        "buffer_load_dwordx4 v[64:67], v115, s[8:11], s26 offen offset:0 sc1\n\t"
        "buffer_load_dwordx4 v[68:71], v115, s[8:11], s26 offen offset:1024 sc1\n\t"
        "buffer_load_dwordx4 v[72:75], v115, s[8:11], s26 offen offset:2048 sc1\n\t"
        "buffer_load_dwordx4 v[76:79], v115, s[8:11], s26 offen offset:3072 sc1\n\t"
        "buffer_load_dwordx4 v[80:83], v115, s[8:11], s46 offen offset:0 sc1\n\t"
        "buffer_load_dwordx4 v[84:87], v115, s[8:11], s46 offen offset:1024 sc1\n\t"
        "buffer_load_dwordx4 v[88:91], v115, s[8:11], s46 offen offset:2048 sc1\n\t"
        "buffer_load_dwordx4 v[92:95], v115, s[8:11], s46 offen offset:3072 sc1\n\t"
        "s_nop 7\n\t"
        "v_mov_b64_e32 v[32:33], 0\n\t"
        "v_mov_b64_e32 v[34:35], 0\n\t"
        "v_mov_b64_e32 v[36:37], 0\n\t"
        "v_mov_b64_e32 v[38:39], 0\n\t"
        "v_mov_b64_e32 v[40:41], 0\n\t"
        "v_mov_b64_e32 v[42:43], 0\n\t"
        "v_mov_b64_e32 v[44:45], 0\n\t"
        "v_mov_b64_e32 v[46:47], 0\n\t"
        "s_branch LB_rore%=w\n\t"
        "LB_rore%=:\n\t"
        "buffer_load_dwordx4 v[64:67], v115, s[8:11], s26 offen offset:0 sc1\n\t"
        "buffer_load_dwordx4 v[68:71], v115, s[8:11], s26 offen offset:1024 sc1\n\t"
        "buffer_load_dwordx4 v[72:75], v115, s[8:11], s26 offen offset:2048 sc1\n\t"
        "buffer_load_dwordx4 v[76:79], v115, s[8:11], s26 offen offset:3072 sc1\n\t"
        "buffer_load_dwordx4 v[80:83], v115, s[8:11], s46 offen offset:0 sc1\n\t"
        "buffer_load_dwordx4 v[84:87], v115, s[8:11], s46 offen offset:1024 sc1\n\t"
        "buffer_load_dwordx4 v[88:91], v115, s[8:11], s46 offen offset:2048 sc1\n\t"
        "buffer_load_dwordx4 v[92:95], v115, s[8:11], s46 offen offset:3072 sc1\n\t"
        "LB_rore%=w:\n\t"
        "s_waitcnt vmcnt(0) lgkmcnt(0)\n\t"
        "v_max3_u32 v108, v64, v65, v66\n\t"
        "v_max3_u32 v108, v108, v67, v68\n\t"
        "v_max3_u32 v108, v108, v69, v70\n\t"
        "v_max3_u32 v108, v108, v71, v72\n\t"
        "v_max3_u32 v108, v108, v73, v74\n\t"
        "v_max3_u32 v108, v108, v75, v76\n\t"
        "v_max3_u32 v108, v108, v77, v78\n\t"
        "v_max3_u32 v108, v108, v79, v80\n\t"
        "v_max3_u32 v108, v108, v81, v82\n\t"
        "v_max3_u32 v108, v108, v83, v84\n\t"
        "v_max3_u32 v108, v108, v85, v86\n\t"
        "v_max3_u32 v108, v108, v87, v88\n\t"
        "v_max3_u32 v108, v108, v89, v90\n\t"
        "v_max3_u32 v108, v108, v91, v92\n\t"
        "v_max3_u32 v108, v108, v93, v94\n\t"
        "v_max_u32_e32 v108, v108, v95\n\t"
        "v_cmp_eq_u32_e32 vcc, -1, v108\n\t"
        "s_nop 0\n\t"
        "s_cbranch_vccz LB_go2%=\n\t"
        "s_add_u32 s42, s42, 1\n\t"
        "s_cmp_lt_u32 s42, 0x100000\n\t"
        "s_cbranch_scc1 LB_rore%=\n\t"
        "s_movk_i32 s34, 4\n\t"
        "s_branch LB_die%=\n\t"
        "LB_die%=:\n\t"
        "v_mov_b32_e32 v30, s34\n\t"
        "s_mov_b32 s35, 33024\n\t"
        "s_mov_b64 exec, 1\n\t"
        "buffer_store_dword v30, off, s[12:15], s35 sc1\n\t"
        "s_endpgm\n\t"
        ".Lk_end_B:\n\t"
        :: REC_S_IN, "{v96}"(g_v), "{v97}"(g_o), "{v98}"(wb0), "{v99}"(wb1), "{v100}"(rb0), "{v101}"(rb0 ^ 32u), "{v102}"(rb0 ^ 64u), "{v103}"(rb0 ^ 96u),
           "{v106}"((l & 1) ? 0xEEEEEEEEu : 0x44444444u), "{v[108:109]}"(m0p),
           "{v112}"(a_mem), "{v113}"((24 + cp) * 16384 + l * 32), "{v114}"(a_vmw), "{v115}"(a_or), "{v116}"(a_mw), "{v117}"(a_sw),
           "{v118}"(lb + w * 2048 + (g >> 1) * 256 + n * 16 + (g & 1) * 8), "{v119}"(lb + (w & 1) * 1024 + l * 16), "{v120}"(lb + LB_R + w * 512 + l * 4),
           "{v121}"(lb + LB_R + l * 8), "{v122}"(stg + l * 16), "{v123}"(o_r), "{v124}"(rb + w == 32 ? 28 * 16384 + (l & 31) * 128 : OOB)
        : "memory", "vcc", "scc", "v0", "v255", "a0", "a255", "s101");
    __builtin_unreachable();
}
__device__ __forceinline__ void rec_helper(const Params& p, rsrc_t r, unsigned lb, const int wv, const int hi) {
    const int tid = otid(), l = tid & 63, xq = hi >> 3;
    const rsrc_t rh = __builtin_amdgcn_make_buffer_rsrc(p.ws + ho_base(p.ws), 0, HO_BYTES, 0x00020000);
    const v4u ones = {0xFFFFFFFFu, 0xFFFFFFFFu, 0xFFFFFFFFu, 0xFFFFFFFFu};
    const bool meas = (hi & 7) < 2;
    if (meas) {
        const unsigned ca = l >= 8 ? OOB : (31 + wv * 32) * 16384 + hi * 256 + l * 16, tend = rtnow() + CAL_T;
        unsigned best0 = ~0u, best1 = ~0u;
#pragma unroll 1
        for (int i = 0; i < 64 && (i < 3 || (int)(rtnow() - tend) < 0); ++i) {
            const unsigned t0 = (unsigned)__builtin_amdgcn_s_memtime(); __builtin_amdgcn_s_waitcnt(0);
            st16(rh, ca, ones); DRAIN();
            const unsigned t1 = (unsigned)__builtin_amdgcn_s_memtime(); __builtin_amdgcn_s_waitcnt(0);
            st16(rh, ca + 8192, ones); DRAIN();
            const unsigned t2 = (unsigned)__builtin_amdgcn_s_memtime(); __builtin_amdgcn_s_waitcnt(0);
            best0 = min(best0, t1 - t0); best1 = min(best1, t2 - t1); }
        if (l < 2) __hip_atomic_fetch_max((unsigned*)(p.ws + F_CAL + 128) + l, 65535u - min(l ? best1 : best0, 65535u), __ATOMIC_RELAXED, __HIP_MEMORY_SCOPE_AGENT);
    }
    auto touch = [&](int z0, int z1) {
#pragma unroll 1
        for (int z = z0 + ((hi & 7) - 2) * 4 + wv; z < z1; z += 24) { const bf16_t* q;
            if (z < 48) q = ((z & 1) ? p.Wk : p.Wq) + (size_t)(((((z & 1) * 2 + 1) * 4 + 4 + (z >> 2)) & 15) * 64 + l) * 4096 + (xq * 8 + 1 + (z & 2) * 2) * 128 + 64;
            else if (z < 112) { const int y = z - 48; q = p.Wo + (size_t)(((y & 3) * 8 + (y >> 3)) * 128 + ((y >> 2) & 1) * 64 + l) * 1024 + (xq & 1) * 512 + 192; }
            else { const int s7 = (z - 112) % 7, y = (z - 112) / 7; q = p.Wo + (size_t)(((y & 3) * 8 + 8 - T2U + (y >> 3)) * 128 + ((y >> 2) & 1) * 64 + l) * 1024 + (xq & 1) * 512 + (s7 + (s7 >= 3)) * 64; }
            touch_l2(q, lb + 1024); } };
    if (!meas) touch(0, TPRE);
    st16(r, tid < 64 ? F_KVQ + hi * 1024 + tid * 16 : tid < 72 ? F_PROG + hi * 128 + (tid - 64) * 16 : tid < 80 ? F_CVY + hi * 128 + (tid - 72) * 16 : OOB, (v4u){0u, 0u, 0u, 0u});
#pragma unroll
    for (int i = 0; i < 2; ++i) st16(r, W_OX + 63 * 262144 + hi * 8192 + i * 4096 + tid * 16, ones);
#pragma unroll
    for (int i = 0; i < 16; ++i) st16(rh, hi * 65536 + i * 4096 + tid * 16, ones);
    DRAIN();
    __syncthreads();
    if (tid == 0 && __hip_atomic_fetch_add((unsigned*)(p.ws + F_CAL + 256), 1u, __ATOMIC_RELAXED, __HIP_MEMORY_SCOPE_AGENT) == 31u) {
        const v2u c = ld8(r, F_CAL + 128); st4(r, F_CAL, c.x < c.y ? 8193u : 1u); }
    if (!meas) touch(TPRE, 112 + 56 * T2U);
    DRAIN();
}
constexpr int CV_RP = 80;
constexpr int CV_U = 96 * CV_RP;
constexpr int CV_KP = 4416;
constexpr int CV_K0 = 8 * CV_U;
#define NAP() do { __builtin_amdgcn_sched_barrier(0); __builtin_amdgcn_s_sleep(XSLP); __builtin_amdgcn_sched_barrier(0); } while (0)
__device__ __forceinline__ void gate_block(const Params& p, rsrc_t r, const int hi, const int wv) {
    constexpr int NT = 4;
    const int l = otid() & 63, t0 = (hi * 4 + wv) * NT;
    v4u xg[NT][2], gw[2];
#pragma unroll
    for (int q = 0; q < 2; ++q) gw[q] = *(const v4u*)(p.gate_w + q * 512 + l * 8);
#pragma unroll
    for (int i = 0; i < NT; ++i)
#pragma unroll
        for (int q = 0; q < 2; ++q) { xg[i][q] = *(const v4u*)(p.Aq + (size_t)(t0 + i) * D + q * 512 + l * 8); NAP(); }
    float mine = 0.f;
#pragma unroll
    for (int i = 0; i < NT; ++i) { float acc = 0.f;
#pragma unroll
        for (int q = 0; q < 2; ++q)
#pragma unroll
            for (int e = 0; e < 4; ++e) { const unsigned pr = pack2(blo(xg[i][q][e]) * blo(gw[q][e]), bhi(xg[i][q][e]) * bhi(gw[q][e])); acc += blo(pr); acc += bhi(pr); }
        for (int o = 32; o > 0; o >>= 1) acc += __shfl_xor(acc, o);
        if ((l & 3) == i) mine = acc; }
    if (l < 32) { const float z = rbfh(mine); st4(r, W_GATE + t0 * 32 + l * 4, __float_as_uint(rbfh(1.f / (1.f + expf(-z))))); }
}
__device__ __forceinline__ void conv_prep(const Params& p, rsrc_t r, const int wv, const int hi, const int G) {
    const int tid = otid(), Q = tid >> 2, cg = tid & 3, ch0 = hi * 32 + cg * 8;
    v4u sk[4]; float sv[8][4];
#pragma unroll
    for (int q = 0; q < 4; ++q) sk[q] = *(const v4u*)(p.shift_k + ch0 * 4 + q * 8);
#pragma unroll
    for (int c8 = 0; c8 < 8; ++c8)
#pragma unroll
        for (int tp = 0; tp < 4; ++tp) { const unsigned sw = sk[c8 >> 1][(c8 & 1) * 2 + (tp >> 1)]; sv[c8][tp] = (tp & 1) ? bhi(sw) : blo(sw); }
    const rsrc_t rk = __builtin_amdgcn_make_buffer_rsrc((void*)p.Ak, 0, S * D * 2, 0x00020000), rv = __builtin_amdgcn_make_buffer_rsrc((void*)p.Av, 0, S * D * 2, 0x00020000),
                 rq = __builtin_amdgcn_make_buffer_rsrc((void*)p.Aq, 0, S * D * 2, 0x00020000);
    {
        const unsigned ro = (unsigned)((Q * 32 + G * 8) * D + ch0) * 2u, uo = hi * 131072 + tid * 16;
        v4u kr[11], u[8], qv[8];
#pragma unroll
        for (int i = 0; i < 11; ++i) { kr[i] = __builtin_amdgcn_raw_buffer_load_b128(rk, (i < 3 && Q + G == 0) ? OOB : ro + (i - 3) * 2048, 0, 0); NAP(); }
#pragma unroll
        for (int i = 0; i < 8; ++i) { u[i] = __builtin_amdgcn_raw_buffer_load_b128(rv, ro + i * 2048, 0, 0); NAP(); }
#pragma unroll
        for (int i = 0; i < 8; ++i) { qv[i] = __builtin_amdgcn_raw_buffer_load_b128(rq, ro + i * 2048, 0, 0); NAP(); }
#pragma unroll
        for (int q = 0; q < 4; ++q) {
#pragma unroll
            for (int i = 0; i < 8; ++i) {
                unsigned ks = pack2(blo(kr[i][q]) * sv[2 * q][3], bhi(kr[i][q]) * sv[2 * q + 1][3]), pr;
#pragma unroll
                for (int d = 1; d < 4; ++d) { pr = pack2(blo(kr[i + d][q]) * sv[2 * q][3 - d], bhi(kr[i + d][q]) * sv[2 * q + 1][3 - d]); ks = pack2(blo(ks) + blo(pr), bhi(ks) + bhi(pr)); }
                u[i][q] = pack2(blo(ks) * blo(u[i][q]), bhi(ks) * bhi(u[i][q])); }
            v4u lo, hh;
#pragma unroll
            for (int z = 0; z < 4; ++z) { const unsigned x = u[7 - 2 * z][q], y = u[6 - 2 * z][q]; lo[z] = __builtin_amdgcn_perm(y, x, 0x05040100u); hh[z] = __builtin_amdgcn_perm(y, x, 0x07060302u); }
            st16(r, W_U + uo + q * 32768 + G * 4096, lo); NAP(); st16(r, W_U + uo + q * 32768 + 16384 + G * 4096, hh); NAP();
#pragma unroll
            for (int z = 0; z < 2; ++z) { st16(r, W_Y + uo + q * 32768 + (2 * G + z) * 4096, (v4u){qv[4 * z][q], qv[4 * z + 1][q], qv[4 * z + 2][q], qv[4 * z + 3][q]}); NAP(); }
        }
    }
    DRAIN(); __syncthreads();
    if (tid == 0) __hip_atomic_fetch_add((unsigned*)(p.ws + F_CONV), 1u, __ATOMIC_RELAXED, __HIP_MEMORY_SCOPE_AGENT);
    if (tid == 0) __hip_atomic_fetch_add((unsigned*)(p.ws + F_CVY + hi * 128 + 64), 1u, __ATOMIC_RELAXED, __HIP_MEMORY_SCOPE_AGENT);
}
__device__ __forceinline__ bool conv_core(const Params& p, rsrc_t r, unsigned char* lds, const int wv, const int un) {
    const int tid = otid(), w = wv, l = tid & 63, Q = tid >> 2, cg = tid & 3;
    const rsrc_t rs = __builtin_amdgcn_make_buffer_rsrc((void*)p.ssm_k, 0, S * D * 2, 0x00020000);
    v4u kraw[4]; v2u kext[4];
    auto kload = [&](int n) { const unsigned o = (unsigned)(((un >> 2) * 32 + w * 8 + (un & 3) * 2 + n) * S + l * 8) * 2u;
#pragma unroll
        for (int i = 0; i < 4; ++i) { kraw[i] = __builtin_amdgcn_raw_buffer_load_b128(rs, o + i * 1024, 0, 2);
            kext[i] = __builtin_amdgcn_raw_buffer_load_b64(rs, (i == 3 && l == 63) ? OOB : o + i * 1024 + 16, 0, 0); } };
    kload(0);
    if (!wait_word(r, F_CVY + (un >> 2) * 128 + 64, 4u, 2048u)) return false;
    v4u ur[8], qr[8];
#pragma unroll
    for (int i = 0; i < 8; ++i) ur[i] = ld16(r, W_U + un * 32768 + i * 4096 + tid * 16);
#pragma unroll
    for (int i = 0; i < 8; ++i) qr[i] = ld16(r, W_Y + un * 32768 + i * 4096 + tid * 16);
    const unsigned dv = *(const unsigned*)(p.ssm_D + (un >> 2) * 32 + cg * 8 + (un & 3) * 2);
    for (int i = tid; i < 8 * 160; i += 256) *(v4u*)(lds + (i / 160) * CV_U + (i % 160) * 16) = (v4u){0u, 0u, 0u, 0u};
    unsigned char* kc = lds + CV_K0 + w * 4 * CV_KP;
    if (l < 32) {
#pragma unroll
        for (int q = 0; q < 4; ++q) *(unsigned*)(kc + q * CV_KP + l * 4) = 0u; }
    const int b = l & 31, kg = l >> 5;
    const unsigned kb = (unsigned)(size_t)kc + (b & 3) * CV_KP + ((b & 28) - 8 * kg + 56 - 16) * 2, ub0 = (unsigned)(size_t)lds + (1 + b) * CV_RP + kg * 16;
    unsigned char* const urow = lds + 2 * cg * CV_U + (32 + Q) * CV_RP;
#pragma unroll
    for (int i = 0; i < 8; ++i) *(v4u*)(urow + (i >> 2) * CV_U + (i & 3) * 16) = ur[i];
    __syncthreads();
#pragma unroll 1
        for (int ci = 0; ci < 2; ++ci) {
            const int c8 = 2 * w + ci;
#pragma unroll
            for (int i = 0; i < 4; ++i) { const v4u e = kraw[i]; const v2u f = kext[i]; unsigned char* q = kc + 128 + (i * 64 + l) * 16;
                const unsigned s0 = __builtin_amdgcn_alignbit(e.y, e.x, 16), s1 = __builtin_amdgcn_alignbit(e.z, e.y, 16), s2 = __builtin_amdgcn_alignbit(e.w, e.z, 16),
                               s3 = __builtin_amdgcn_alignbit(f.x, e.w, 16), s4 = __builtin_amdgcn_alignbit(f.y, f.x, 16);
                *(v4u*)q = (v4u){s0, s1, s2, s3}; *(v4u*)(q + CV_KP) = (v4u){e.y, e.z, e.w, f.x}; *(v4u*)(q + 2 * CV_KP) = (v4u){s1, s2, s3, s4}; *(v4u*)(q + 3 * CV_KP) = (v4u){e.z, e.w, f.x, f.y};
                if (i == 0 && l == 0) {
                    *(v2u*)(q - 8) = (v2u){0u, e.x << 16}; *(v2u*)(q + CV_KP - 8) = (v2u){0u, e.x}; *(v2u*)(q + 2 * CV_KP - 8) = (v2u){e.x << 16, s0}; *(v2u*)(q + 3 * CV_KP - 8) = (v2u){e.x, e.y}; } }
            if (ci == 0) kload(1);
            __builtin_amdgcn_wave_barrier();
            f32x16 acc0, acc1;
            asm volatile(
                "ds_read_b64 v[192:193], %[kb] offset:32\n\t"
                "ds_read_b64 v[194:195], %[kb] offset:40\n\t"
                "ds_read_b128 v[196:199], %[ub] offset:5040\n\t"
                "ds_read_b128 v[200:203], %[ub] offset:2480\n\t"
                "ds_read_b64 v[204:205], %[kb] offset:2080\n\t"
                "ds_read_b64 v[206:207], %[kb] offset:2088\n\t"
                "ds_read_b64 v[208:209], %[kb] offset:0\n\t"
                "ds_read_b64 v[210:211], %[kb] offset:8\n\t"
                "ds_read_b128 v[212:215], %[ub] offset:5072\n\t"
                "ds_read_b128 v[216:219], %[ub] offset:2512\n\t"
                "ds_read_b64 v[220:221], %[kb] offset:2048\n\t"
                "ds_read_b64 v[222:223], %[kb] offset:2056\n\t"
                "s_waitcnt lgkmcnt(9)\n\t"
                "v_mfma_f32_32x32x16_bf16 %[a1], v[192:195], v[196:199], 0\n\t"
                "ds_read_b64 v[224:225], %[kb] offset:96\n\t"
                "ds_read_b64 v[226:227], %[kb] offset:104\n\t"
                "ds_read_b128 v[228:231], %[ub] offset:4960\n\t"
                "s_waitcnt lgkmcnt(11)\n\t"
                "v_mfma_f32_32x32x16_bf16 %[a0], v[192:195], v[200:203], 0\n\t"
                "ds_read_b128 v[232:235], %[ub] offset:2400\n\t"
                "s_waitcnt lgkmcnt(10)\n\t"
                "v_mfma_f32_32x32x16_bf16 %[a1], v[204:207], v[200:203], %[a1]\n\t"
                "ds_read_b64 v[236:237], %[kb] offset:2144\n\t"
                "ds_read_b64 v[238:239], %[kb] offset:2152\n\t"
                "s_waitcnt lgkmcnt(9)\n\t"
                "v_mfma_f32_32x32x16_bf16 %[a1], v[208:211], v[212:215], %[a1]\n\t"
                "ds_read_b64 v[240:241], %[kb] offset:64\n\t"
                "ds_read_b64 v[242:243], %[kb] offset:72\n\t"
                "ds_read_b128 v[244:247], %[ub] offset:4992\n\t"
                "s_waitcnt lgkmcnt(11)\n\t"
                "v_mfma_f32_32x32x16_bf16 %[a0], v[208:211], v[216:219], %[a0]\n\t"
                "ds_read_b128 v[248:251], %[ub] offset:2432\n\t"
                "s_waitcnt lgkmcnt(10)\n\t"
                "v_mfma_f32_32x32x16_bf16 %[a1], v[220:223], v[216:219], %[a1]\n\t"
                "ds_read_b64 v[252:253], %[kb] offset:2112\n\t"
                "ds_read_b64 v[254:255], %[kb] offset:2120\n\t"
                "s_waitcnt lgkmcnt(9)\n\t"
                "v_mfma_f32_32x32x16_bf16 %[a1], v[224:227], v[228:231], %[a1]\n\t"
                "ds_read_b64 v[192:193], %[kb] offset:160\n\t"
                "ds_read_b64 v[194:195], %[kb] offset:168\n\t"
                "ds_read_b128 v[196:199], %[ub] offset:4880\n\t"
                "s_waitcnt lgkmcnt(11)\n\t"
                "v_mfma_f32_32x32x16_bf16 %[a0], v[224:227], v[232:235], %[a0]\n\t"
                "ds_read_b128 v[200:203], %[ub] offset:2320\n\t"
                "s_waitcnt lgkmcnt(10)\n\t"
                "v_mfma_f32_32x32x16_bf16 %[a1], v[236:239], v[232:235], %[a1]\n\t"
                "ds_read_b64 v[204:205], %[kb] offset:2208\n\t"
                "ds_read_b64 v[206:207], %[kb] offset:2216\n\t"
                "s_waitcnt lgkmcnt(9)\n\t"
                "v_mfma_f32_32x32x16_bf16 %[a1], v[240:243], v[244:247], %[a1]\n\t"
                "ds_read_b64 v[208:209], %[kb] offset:128\n\t"
                "ds_read_b64 v[210:211], %[kb] offset:136\n\t"
                "ds_read_b128 v[212:215], %[ub] offset:4912\n\t"
                "s_waitcnt lgkmcnt(11)\n\t"
                "v_mfma_f32_32x32x16_bf16 %[a0], v[240:243], v[248:251], %[a0]\n\t"
                "ds_read_b128 v[216:219], %[ub] offset:2352\n\t"
                "s_waitcnt lgkmcnt(10)\n\t"
                "v_mfma_f32_32x32x16_bf16 %[a1], v[252:255], v[248:251], %[a1]\n\t"
                "ds_read_b64 v[220:221], %[kb] offset:2176\n\t"
                "ds_read_b64 v[222:223], %[kb] offset:2184\n\t"
                "s_waitcnt lgkmcnt(9)\n\t"
                "v_mfma_f32_32x32x16_bf16 %[a1], v[192:195], v[196:199], %[a1]\n\t"
                "ds_read_b64 v[224:225], %[kb] offset:224\n\t"
                "ds_read_b64 v[226:227], %[kb] offset:232\n\t"
                "ds_read_b128 v[228:231], %[ub] offset:4800\n\t"
                "s_waitcnt lgkmcnt(11)\n\t"
                "v_mfma_f32_32x32x16_bf16 %[a0], v[192:195], v[200:203], %[a0]\n\t"
                "ds_read_b128 v[232:235], %[ub] offset:2240\n\t"
                "s_waitcnt lgkmcnt(10)\n\t"
                "v_mfma_f32_32x32x16_bf16 %[a1], v[204:207], v[200:203], %[a1]\n\t"
                "ds_read_b64 v[236:237], %[kb] offset:2272\n\t"
                "ds_read_b64 v[238:239], %[kb] offset:2280\n\t"
                "s_waitcnt lgkmcnt(9)\n\t"
                "v_mfma_f32_32x32x16_bf16 %[a1], v[208:211], v[212:215], %[a1]\n\t"
                "ds_read_b64 v[240:241], %[kb] offset:192\n\t"
                "ds_read_b64 v[242:243], %[kb] offset:200\n\t"
                "ds_read_b128 v[244:247], %[ub] offset:4832\n\t"
                "s_waitcnt lgkmcnt(11)\n\t"
                "v_mfma_f32_32x32x16_bf16 %[a0], v[208:211], v[216:219], %[a0]\n\t"
                "ds_read_b128 v[248:251], %[ub] offset:2272\n\t"
                "s_waitcnt lgkmcnt(10)\n\t"
                "v_mfma_f32_32x32x16_bf16 %[a1], v[220:223], v[216:219], %[a1]\n\t"
                "ds_read_b64 v[252:253], %[kb] offset:2240\n\t"
                "ds_read_b64 v[254:255], %[kb] offset:2248\n\t"
                "s_waitcnt lgkmcnt(9)\n\t"
                "v_mfma_f32_32x32x16_bf16 %[a1], v[224:227], v[228:231], %[a1]\n\t"
                "ds_read_b64 v[192:193], %[kb] offset:288\n\t"
                "ds_read_b64 v[194:195], %[kb] offset:296\n\t"
                "ds_read_b128 v[196:199], %[ub] offset:4720\n\t"
                "s_waitcnt lgkmcnt(11)\n\t"
                "v_mfma_f32_32x32x16_bf16 %[a0], v[224:227], v[232:235], %[a0]\n\t"
                "ds_read_b128 v[200:203], %[ub] offset:2160\n\t"
                "s_waitcnt lgkmcnt(10)\n\t"
                "v_mfma_f32_32x32x16_bf16 %[a1], v[236:239], v[232:235], %[a1]\n\t"
                "ds_read_b64 v[204:205], %[kb] offset:2336\n\t"
                "ds_read_b64 v[206:207], %[kb] offset:2344\n\t"
                "s_waitcnt lgkmcnt(9)\n\t"
                "v_mfma_f32_32x32x16_bf16 %[a1], v[240:243], v[244:247], %[a1]\n\t"
                "ds_read_b64 v[208:209], %[kb] offset:256\n\t"
                "ds_read_b64 v[210:211], %[kb] offset:264\n\t"
                "ds_read_b128 v[212:215], %[ub] offset:4752\n\t"
                "s_waitcnt lgkmcnt(11)\n\t"
                "v_mfma_f32_32x32x16_bf16 %[a0], v[240:243], v[248:251], %[a0]\n\t"
                "ds_read_b128 v[216:219], %[ub] offset:2192\n\t"
                "s_waitcnt lgkmcnt(10)\n\t"
                "v_mfma_f32_32x32x16_bf16 %[a1], v[252:255], v[248:251], %[a1]\n\t"
                "ds_read_b64 v[220:221], %[kb] offset:2304\n\t"
                "ds_read_b64 v[222:223], %[kb] offset:2312\n\t"
                "s_waitcnt lgkmcnt(9)\n\t"
                "v_mfma_f32_32x32x16_bf16 %[a1], v[192:195], v[196:199], %[a1]\n\t"
                "ds_read_b64 v[224:225], %[kb] offset:352\n\t"
                "ds_read_b64 v[226:227], %[kb] offset:360\n\t"
                "ds_read_b128 v[228:231], %[ub] offset:4640\n\t"
                "s_waitcnt lgkmcnt(11)\n\t"
                "v_mfma_f32_32x32x16_bf16 %[a0], v[192:195], v[200:203], %[a0]\n\t"
                "ds_read_b128 v[232:235], %[ub] offset:2080\n\t"
                "s_waitcnt lgkmcnt(10)\n\t"
                "v_mfma_f32_32x32x16_bf16 %[a1], v[204:207], v[200:203], %[a1]\n\t"
                "ds_read_b64 v[236:237], %[kb] offset:2400\n\t"
                "ds_read_b64 v[238:239], %[kb] offset:2408\n\t"
                "s_waitcnt lgkmcnt(9)\n\t"
                "v_mfma_f32_32x32x16_bf16 %[a1], v[208:211], v[212:215], %[a1]\n\t"
                "ds_read_b64 v[240:241], %[kb] offset:320\n\t"
                "ds_read_b64 v[242:243], %[kb] offset:328\n\t"
                "ds_read_b128 v[244:247], %[ub] offset:4672\n\t"
                "s_waitcnt lgkmcnt(11)\n\t"
                "v_mfma_f32_32x32x16_bf16 %[a0], v[208:211], v[216:219], %[a0]\n\t"
                "ds_read_b128 v[248:251], %[ub] offset:2112\n\t"
                "s_waitcnt lgkmcnt(10)\n\t"
                "v_mfma_f32_32x32x16_bf16 %[a1], v[220:223], v[216:219], %[a1]\n\t"
                "ds_read_b64 v[252:253], %[kb] offset:2368\n\t"
                "ds_read_b64 v[254:255], %[kb] offset:2376\n\t"
                "s_waitcnt lgkmcnt(9)\n\t"
                "v_mfma_f32_32x32x16_bf16 %[a1], v[224:227], v[228:231], %[a1]\n\t"
                "ds_read_b64 v[192:193], %[kb] offset:416\n\t"
                "ds_read_b64 v[194:195], %[kb] offset:424\n\t"
                "ds_read_b128 v[196:199], %[ub] offset:4560\n\t"
                "s_waitcnt lgkmcnt(11)\n\t"
                "v_mfma_f32_32x32x16_bf16 %[a0], v[224:227], v[232:235], %[a0]\n\t"
                "ds_read_b128 v[200:203], %[ub] offset:2000\n\t"
                "s_waitcnt lgkmcnt(10)\n\t"
                "v_mfma_f32_32x32x16_bf16 %[a1], v[236:239], v[232:235], %[a1]\n\t"
                "ds_read_b64 v[204:205], %[kb] offset:2464\n\t"
                "ds_read_b64 v[206:207], %[kb] offset:2472\n\t"
                "s_waitcnt lgkmcnt(9)\n\t"
                "v_mfma_f32_32x32x16_bf16 %[a1], v[240:243], v[244:247], %[a1]\n\t"
                "ds_read_b64 v[208:209], %[kb] offset:384\n\t"
                "ds_read_b64 v[210:211], %[kb] offset:392\n\t"
                "ds_read_b128 v[212:215], %[ub] offset:4592\n\t"
                "s_waitcnt lgkmcnt(11)\n\t"
                "v_mfma_f32_32x32x16_bf16 %[a0], v[240:243], v[248:251], %[a0]\n\t"
                "ds_read_b128 v[216:219], %[ub] offset:2032\n\t"
                "s_waitcnt lgkmcnt(10)\n\t"
                "v_mfma_f32_32x32x16_bf16 %[a1], v[252:255], v[248:251], %[a1]\n\t"
                "ds_read_b64 v[220:221], %[kb] offset:2432\n\t"
                "ds_read_b64 v[222:223], %[kb] offset:2440\n\t"
                "s_waitcnt lgkmcnt(9)\n\t"
                "v_mfma_f32_32x32x16_bf16 %[a1], v[192:195], v[196:199], %[a1]\n\t"
                "ds_read_b64 v[224:225], %[kb] offset:480\n\t"
                "ds_read_b64 v[226:227], %[kb] offset:488\n\t"
                "ds_read_b128 v[228:231], %[ub] offset:4480\n\t"
                "s_waitcnt lgkmcnt(11)\n\t"
                "v_mfma_f32_32x32x16_bf16 %[a0], v[192:195], v[200:203], %[a0]\n\t"
                "ds_read_b128 v[232:235], %[ub] offset:1920\n\t"
                "s_waitcnt lgkmcnt(10)\n\t"
                "v_mfma_f32_32x32x16_bf16 %[a1], v[204:207], v[200:203], %[a1]\n\t"
                "ds_read_b64 v[236:237], %[kb] offset:2528\n\t"
                "ds_read_b64 v[238:239], %[kb] offset:2536\n\t"
                "s_waitcnt lgkmcnt(9)\n\t"
                "v_mfma_f32_32x32x16_bf16 %[a1], v[208:211], v[212:215], %[a1]\n\t"
                "ds_read_b64 v[240:241], %[kb] offset:448\n\t"
                "ds_read_b64 v[242:243], %[kb] offset:456\n\t"
                "ds_read_b128 v[244:247], %[ub] offset:4512\n\t"
                "s_waitcnt lgkmcnt(11)\n\t"
                "v_mfma_f32_32x32x16_bf16 %[a0], v[208:211], v[216:219], %[a0]\n\t"
                "ds_read_b128 v[248:251], %[ub] offset:1952\n\t"
                "s_waitcnt lgkmcnt(10)\n\t"
                "v_mfma_f32_32x32x16_bf16 %[a1], v[220:223], v[216:219], %[a1]\n\t"
                "ds_read_b64 v[252:253], %[kb] offset:2496\n\t"
                "ds_read_b64 v[254:255], %[kb] offset:2504\n\t"
                "s_waitcnt lgkmcnt(9)\n\t"
                "v_mfma_f32_32x32x16_bf16 %[a1], v[224:227], v[228:231], %[a1]\n\t"
                "ds_read_b64 v[192:193], %[kb] offset:544\n\t"
                "ds_read_b64 v[194:195], %[kb] offset:552\n\t"
                "ds_read_b128 v[196:199], %[ub] offset:4400\n\t"
                "s_waitcnt lgkmcnt(11)\n\t"
                "v_mfma_f32_32x32x16_bf16 %[a0], v[224:227], v[232:235], %[a0]\n\t"
                "ds_read_b128 v[200:203], %[ub] offset:1840\n\t"
                "s_waitcnt lgkmcnt(10)\n\t"
                "v_mfma_f32_32x32x16_bf16 %[a1], v[236:239], v[232:235], %[a1]\n\t"
                "ds_read_b64 v[204:205], %[kb] offset:2592\n\t"
                "ds_read_b64 v[206:207], %[kb] offset:2600\n\t"
                "s_waitcnt lgkmcnt(9)\n\t"
                "v_mfma_f32_32x32x16_bf16 %[a1], v[240:243], v[244:247], %[a1]\n\t"
                "ds_read_b64 v[208:209], %[kb] offset:512\n\t"
                "ds_read_b64 v[210:211], %[kb] offset:520\n\t"
                "ds_read_b128 v[212:215], %[ub] offset:4432\n\t"
                "s_waitcnt lgkmcnt(11)\n\t"
                "v_mfma_f32_32x32x16_bf16 %[a0], v[240:243], v[248:251], %[a0]\n\t"
                "ds_read_b128 v[216:219], %[ub] offset:1872\n\t"
                "s_waitcnt lgkmcnt(10)\n\t"
                "v_mfma_f32_32x32x16_bf16 %[a1], v[252:255], v[248:251], %[a1]\n\t"
                "ds_read_b64 v[220:221], %[kb] offset:2560\n\t"
                "ds_read_b64 v[222:223], %[kb] offset:2568\n\t"
                "s_waitcnt lgkmcnt(9)\n\t"
                "v_mfma_f32_32x32x16_bf16 %[a1], v[192:195], v[196:199], %[a1]\n\t"
                "ds_read_b64 v[224:225], %[kb] offset:608\n\t"
                "ds_read_b64 v[226:227], %[kb] offset:616\n\t"
                "ds_read_b128 v[228:231], %[ub] offset:4320\n\t"
                "s_waitcnt lgkmcnt(11)\n\t"
                "v_mfma_f32_32x32x16_bf16 %[a0], v[192:195], v[200:203], %[a0]\n\t"
                "ds_read_b128 v[232:235], %[ub] offset:1760\n\t"
                "s_waitcnt lgkmcnt(10)\n\t"
                "v_mfma_f32_32x32x16_bf16 %[a1], v[204:207], v[200:203], %[a1]\n\t"
                "ds_read_b64 v[236:237], %[kb] offset:2656\n\t"
                "ds_read_b64 v[238:239], %[kb] offset:2664\n\t"
                "s_waitcnt lgkmcnt(9)\n\t"
                "v_mfma_f32_32x32x16_bf16 %[a1], v[208:211], v[212:215], %[a1]\n\t"
                "ds_read_b64 v[240:241], %[kb] offset:576\n\t"
                "ds_read_b64 v[242:243], %[kb] offset:584\n\t"
                "ds_read_b128 v[244:247], %[ub] offset:4352\n\t"
                "s_waitcnt lgkmcnt(11)\n\t"
                "v_mfma_f32_32x32x16_bf16 %[a0], v[208:211], v[216:219], %[a0]\n\t"
                "ds_read_b128 v[248:251], %[ub] offset:1792\n\t"
                "s_waitcnt lgkmcnt(10)\n\t"
                "v_mfma_f32_32x32x16_bf16 %[a1], v[220:223], v[216:219], %[a1]\n\t"
                "ds_read_b64 v[252:253], %[kb] offset:2624\n\t"
                "ds_read_b64 v[254:255], %[kb] offset:2632\n\t"
                "s_waitcnt lgkmcnt(9)\n\t"
                "v_mfma_f32_32x32x16_bf16 %[a1], v[224:227], v[228:231], %[a1]\n\t"
                "ds_read_b64 v[192:193], %[kb] offset:672\n\t"
                "ds_read_b64 v[194:195], %[kb] offset:680\n\t"
                "ds_read_b128 v[196:199], %[ub] offset:4240\n\t"
                "s_waitcnt lgkmcnt(11)\n\t"
                "v_mfma_f32_32x32x16_bf16 %[a0], v[224:227], v[232:235], %[a0]\n\t"
                "ds_read_b128 v[200:203], %[ub] offset:1680\n\t"
                "s_waitcnt lgkmcnt(10)\n\t"
                "v_mfma_f32_32x32x16_bf16 %[a1], v[236:239], v[232:235], %[a1]\n\t"
                "ds_read_b64 v[204:205], %[kb] offset:2720\n\t"
                "ds_read_b64 v[206:207], %[kb] offset:2728\n\t"
                "s_waitcnt lgkmcnt(9)\n\t"
                "v_mfma_f32_32x32x16_bf16 %[a1], v[240:243], v[244:247], %[a1]\n\t"
                "ds_read_b64 v[208:209], %[kb] offset:640\n\t"
                "ds_read_b64 v[210:211], %[kb] offset:648\n\t"
                "ds_read_b128 v[212:215], %[ub] offset:4272\n\t"
                "s_waitcnt lgkmcnt(11)\n\t"
                "v_mfma_f32_32x32x16_bf16 %[a0], v[240:243], v[248:251], %[a0]\n\t"
                "ds_read_b128 v[216:219], %[ub] offset:1712\n\t"
                "s_waitcnt lgkmcnt(10)\n\t"
                "v_mfma_f32_32x32x16_bf16 %[a1], v[252:255], v[248:251], %[a1]\n\t"
                "ds_read_b64 v[220:221], %[kb] offset:2688\n\t"
                "ds_read_b64 v[222:223], %[kb] offset:2696\n\t"
                "s_waitcnt lgkmcnt(9)\n\t"
                "v_mfma_f32_32x32x16_bf16 %[a1], v[192:195], v[196:199], %[a1]\n\t"
                "ds_read_b64 v[224:225], %[kb] offset:736\n\t"
                "ds_read_b64 v[226:227], %[kb] offset:744\n\t"
                "ds_read_b128 v[228:231], %[ub] offset:4160\n\t"
                "s_waitcnt lgkmcnt(11)\n\t"
                "v_mfma_f32_32x32x16_bf16 %[a0], v[192:195], v[200:203], %[a0]\n\t"
                "ds_read_b128 v[232:235], %[ub] offset:1600\n\t"
                "s_waitcnt lgkmcnt(10)\n\t"
                "v_mfma_f32_32x32x16_bf16 %[a1], v[204:207], v[200:203], %[a1]\n\t"
                "ds_read_b64 v[236:237], %[kb] offset:2784\n\t"
                "ds_read_b64 v[238:239], %[kb] offset:2792\n\t"
                "s_waitcnt lgkmcnt(9)\n\t"
                "v_mfma_f32_32x32x16_bf16 %[a1], v[208:211], v[212:215], %[a1]\n\t"
                "ds_read_b64 v[240:241], %[kb] offset:704\n\t"
                "ds_read_b64 v[242:243], %[kb] offset:712\n\t"
                "ds_read_b128 v[244:247], %[ub] offset:4192\n\t"
                "s_waitcnt lgkmcnt(11)\n\t"
                "v_mfma_f32_32x32x16_bf16 %[a0], v[208:211], v[216:219], %[a0]\n\t"
                "ds_read_b128 v[248:251], %[ub] offset:1632\n\t"
                "s_waitcnt lgkmcnt(10)\n\t"
                "v_mfma_f32_32x32x16_bf16 %[a1], v[220:223], v[216:219], %[a1]\n\t"
                "ds_read_b64 v[252:253], %[kb] offset:2752\n\t"
                "ds_read_b64 v[254:255], %[kb] offset:2760\n\t"
                "s_waitcnt lgkmcnt(9)\n\t"
                "v_mfma_f32_32x32x16_bf16 %[a1], v[224:227], v[228:231], %[a1]\n\t"
                "ds_read_b64 v[192:193], %[kb] offset:800\n\t"
                "ds_read_b64 v[194:195], %[kb] offset:808\n\t"
                "ds_read_b128 v[196:199], %[ub] offset:4080\n\t"
                "s_waitcnt lgkmcnt(11)\n\t"
                "v_mfma_f32_32x32x16_bf16 %[a0], v[224:227], v[232:235], %[a0]\n\t"
                "ds_read_b128 v[200:203], %[ub] offset:1520\n\t"
                "s_waitcnt lgkmcnt(10)\n\t"
                "v_mfma_f32_32x32x16_bf16 %[a1], v[236:239], v[232:235], %[a1]\n\t"
                "ds_read_b64 v[204:205], %[kb] offset:2848\n\t"
                "ds_read_b64 v[206:207], %[kb] offset:2856\n\t"
                "s_waitcnt lgkmcnt(9)\n\t"
                "v_mfma_f32_32x32x16_bf16 %[a1], v[240:243], v[244:247], %[a1]\n\t"
                "ds_read_b64 v[208:209], %[kb] offset:768\n\t"
                "ds_read_b64 v[210:211], %[kb] offset:776\n\t"
                "ds_read_b128 v[212:215], %[ub] offset:4112\n\t"
                "s_waitcnt lgkmcnt(11)\n\t"
                "v_mfma_f32_32x32x16_bf16 %[a0], v[240:243], v[248:251], %[a0]\n\t"
                "ds_read_b128 v[216:219], %[ub] offset:1552\n\t"
                "s_waitcnt lgkmcnt(10)\n\t"
                "v_mfma_f32_32x32x16_bf16 %[a1], v[252:255], v[248:251], %[a1]\n\t"
                "ds_read_b64 v[220:221], %[kb] offset:2816\n\t"
                "ds_read_b64 v[222:223], %[kb] offset:2824\n\t"
                "s_waitcnt lgkmcnt(9)\n\t"
                "v_mfma_f32_32x32x16_bf16 %[a1], v[192:195], v[196:199], %[a1]\n\t"
                "ds_read_b64 v[224:225], %[kb] offset:864\n\t"
                "ds_read_b64 v[226:227], %[kb] offset:872\n\t"
                "ds_read_b128 v[228:231], %[ub] offset:4000\n\t"
                "s_waitcnt lgkmcnt(11)\n\t"
                "v_mfma_f32_32x32x16_bf16 %[a0], v[192:195], v[200:203], %[a0]\n\t"
                "ds_read_b128 v[232:235], %[ub] offset:1440\n\t"
                "s_waitcnt lgkmcnt(10)\n\t"
                "v_mfma_f32_32x32x16_bf16 %[a1], v[204:207], v[200:203], %[a1]\n\t"
                "ds_read_b64 v[236:237], %[kb] offset:2912\n\t"
                "ds_read_b64 v[238:239], %[kb] offset:2920\n\t"
                "s_waitcnt lgkmcnt(9)\n\t"
                "v_mfma_f32_32x32x16_bf16 %[a1], v[208:211], v[212:215], %[a1]\n\t"
                "ds_read_b64 v[240:241], %[kb] offset:832\n\t"
                "ds_read_b64 v[242:243], %[kb] offset:840\n\t"
                "ds_read_b128 v[244:247], %[ub] offset:4032\n\t"
                "s_waitcnt lgkmcnt(11)\n\t"
                "v_mfma_f32_32x32x16_bf16 %[a0], v[208:211], v[216:219], %[a0]\n\t"
                "ds_read_b128 v[248:251], %[ub] offset:1472\n\t"
                "s_waitcnt lgkmcnt(10)\n\t"
                "v_mfma_f32_32x32x16_bf16 %[a1], v[220:223], v[216:219], %[a1]\n\t"
                "ds_read_b64 v[252:253], %[kb] offset:2880\n\t"
                "ds_read_b64 v[254:255], %[kb] offset:2888\n\t"
                "s_waitcnt lgkmcnt(9)\n\t"
                "v_mfma_f32_32x32x16_bf16 %[a1], v[224:227], v[228:231], %[a1]\n\t"
                "ds_read_b64 v[192:193], %[kb] offset:928\n\t"
                "ds_read_b64 v[194:195], %[kb] offset:936\n\t"
                "ds_read_b128 v[196:199], %[ub] offset:3920\n\t"
                "s_waitcnt lgkmcnt(11)\n\t"
                "v_mfma_f32_32x32x16_bf16 %[a0], v[224:227], v[232:235], %[a0]\n\t"
                "ds_read_b128 v[200:203], %[ub] offset:1360\n\t"
                "s_waitcnt lgkmcnt(10)\n\t"
                "v_mfma_f32_32x32x16_bf16 %[a1], v[236:239], v[232:235], %[a1]\n\t"
                "ds_read_b64 v[204:205], %[kb] offset:2976\n\t"
                "ds_read_b64 v[206:207], %[kb] offset:2984\n\t"
                "s_waitcnt lgkmcnt(9)\n\t"
                "v_mfma_f32_32x32x16_bf16 %[a1], v[240:243], v[244:247], %[a1]\n\t"
                "ds_read_b64 v[208:209], %[kb] offset:896\n\t"
                "ds_read_b64 v[210:211], %[kb] offset:904\n\t"
                "ds_read_b128 v[212:215], %[ub] offset:3952\n\t"
                "s_waitcnt lgkmcnt(11)\n\t"
                "v_mfma_f32_32x32x16_bf16 %[a0], v[240:243], v[248:251], %[a0]\n\t"
                "ds_read_b128 v[216:219], %[ub] offset:1392\n\t"
                "s_waitcnt lgkmcnt(10)\n\t"
                "v_mfma_f32_32x32x16_bf16 %[a1], v[252:255], v[248:251], %[a1]\n\t"
                "ds_read_b64 v[220:221], %[kb] offset:2944\n\t"
                "ds_read_b64 v[222:223], %[kb] offset:2952\n\t"
                "s_waitcnt lgkmcnt(9)\n\t"
                "v_mfma_f32_32x32x16_bf16 %[a1], v[192:195], v[196:199], %[a1]\n\t"
                "ds_read_b64 v[224:225], %[kb] offset:992\n\t"
                "ds_read_b64 v[226:227], %[kb] offset:1000\n\t"
                "ds_read_b128 v[228:231], %[ub] offset:3840\n\t"
                "s_waitcnt lgkmcnt(11)\n\t"
                "v_mfma_f32_32x32x16_bf16 %[a0], v[192:195], v[200:203], %[a0]\n\t"
                "ds_read_b128 v[232:235], %[ub] offset:1280\n\t"
                "s_waitcnt lgkmcnt(10)\n\t"
                "v_mfma_f32_32x32x16_bf16 %[a1], v[204:207], v[200:203], %[a1]\n\t"
                "ds_read_b64 v[236:237], %[kb] offset:3040\n\t"
                "ds_read_b64 v[238:239], %[kb] offset:3048\n\t"
                "s_waitcnt lgkmcnt(9)\n\t"
                "v_mfma_f32_32x32x16_bf16 %[a1], v[208:211], v[212:215], %[a1]\n\t"
                "ds_read_b64 v[240:241], %[kb] offset:960\n\t"
                "ds_read_b64 v[242:243], %[kb] offset:968\n\t"
                "ds_read_b128 v[244:247], %[ub] offset:3872\n\t"
                "s_waitcnt lgkmcnt(11)\n\t"
                "v_mfma_f32_32x32x16_bf16 %[a0], v[208:211], v[216:219], %[a0]\n\t"
                "ds_read_b128 v[248:251], %[ub] offset:1312\n\t"
                "s_waitcnt lgkmcnt(10)\n\t"
                "v_mfma_f32_32x32x16_bf16 %[a1], v[220:223], v[216:219], %[a1]\n\t"
                "ds_read_b64 v[252:253], %[kb] offset:3008\n\t"
                "ds_read_b64 v[254:255], %[kb] offset:3016\n\t"
                "s_waitcnt lgkmcnt(9)\n\t"
                "v_mfma_f32_32x32x16_bf16 %[a1], v[224:227], v[228:231], %[a1]\n\t"
                "ds_read_b64 v[192:193], %[kb] offset:1056\n\t"
                "ds_read_b64 v[194:195], %[kb] offset:1064\n\t"
                "ds_read_b128 v[196:199], %[ub] offset:3760\n\t"
                "s_waitcnt lgkmcnt(11)\n\t"
                "v_mfma_f32_32x32x16_bf16 %[a0], v[224:227], v[232:235], %[a0]\n\t"
                "ds_read_b128 v[200:203], %[ub] offset:1200\n\t"
                "s_waitcnt lgkmcnt(10)\n\t"
                "v_mfma_f32_32x32x16_bf16 %[a1], v[236:239], v[232:235], %[a1]\n\t"
                "ds_read_b64 v[204:205], %[kb] offset:3104\n\t"
                "ds_read_b64 v[206:207], %[kb] offset:3112\n\t"
                "s_waitcnt lgkmcnt(9)\n\t"
                "v_mfma_f32_32x32x16_bf16 %[a1], v[240:243], v[244:247], %[a1]\n\t"
                "ds_read_b64 v[208:209], %[kb] offset:1024\n\t"
                "ds_read_b64 v[210:211], %[kb] offset:1032\n\t"
                "ds_read_b128 v[212:215], %[ub] offset:3792\n\t"
                "s_waitcnt lgkmcnt(11)\n\t"
                "v_mfma_f32_32x32x16_bf16 %[a0], v[240:243], v[248:251], %[a0]\n\t"
                "ds_read_b128 v[216:219], %[ub] offset:1232\n\t"
                "s_waitcnt lgkmcnt(10)\n\t"
                "v_mfma_f32_32x32x16_bf16 %[a1], v[252:255], v[248:251], %[a1]\n\t"
                "ds_read_b64 v[220:221], %[kb] offset:3072\n\t"
                "ds_read_b64 v[222:223], %[kb] offset:3080\n\t"
                "s_waitcnt lgkmcnt(9)\n\t"
                "v_mfma_f32_32x32x16_bf16 %[a1], v[192:195], v[196:199], %[a1]\n\t"
                "ds_read_b64 v[224:225], %[kb] offset:1120\n\t"
                "ds_read_b64 v[226:227], %[kb] offset:1128\n\t"
                "ds_read_b128 v[228:231], %[ub] offset:3680\n\t"
                "s_waitcnt lgkmcnt(11)\n\t"
                "v_mfma_f32_32x32x16_bf16 %[a0], v[192:195], v[200:203], %[a0]\n\t"
                "ds_read_b128 v[232:235], %[ub] offset:1120\n\t"
                "s_waitcnt lgkmcnt(10)\n\t"
                "v_mfma_f32_32x32x16_bf16 %[a1], v[204:207], v[200:203], %[a1]\n\t"
                "ds_read_b64 v[236:237], %[kb] offset:3168\n\t"
                "ds_read_b64 v[238:239], %[kb] offset:3176\n\t"
                "s_waitcnt lgkmcnt(9)\n\t"
                "v_mfma_f32_32x32x16_bf16 %[a1], v[208:211], v[212:215], %[a1]\n\t"
                "ds_read_b64 v[240:241], %[kb] offset:1088\n\t"
                "ds_read_b64 v[242:243], %[kb] offset:1096\n\t"
                "ds_read_b128 v[244:247], %[ub] offset:3712\n\t"
                "s_waitcnt lgkmcnt(11)\n\t"
                "v_mfma_f32_32x32x16_bf16 %[a0], v[208:211], v[216:219], %[a0]\n\t"
                "ds_read_b128 v[248:251], %[ub] offset:1152\n\t"
                "s_waitcnt lgkmcnt(10)\n\t"
                "v_mfma_f32_32x32x16_bf16 %[a1], v[220:223], v[216:219], %[a1]\n\t"
                "ds_read_b64 v[252:253], %[kb] offset:3136\n\t"
                "ds_read_b64 v[254:255], %[kb] offset:3144\n\t"
                "s_waitcnt lgkmcnt(9)\n\t"
                "v_mfma_f32_32x32x16_bf16 %[a1], v[224:227], v[228:231], %[a1]\n\t"
                "ds_read_b64 v[192:193], %[kb] offset:1184\n\t"
                "ds_read_b64 v[194:195], %[kb] offset:1192\n\t"
                "ds_read_b128 v[196:199], %[ub] offset:3600\n\t"
                "s_waitcnt lgkmcnt(11)\n\t"
                "v_mfma_f32_32x32x16_bf16 %[a0], v[224:227], v[232:235], %[a0]\n\t"
                "ds_read_b128 v[200:203], %[ub] offset:1040\n\t"
                "s_waitcnt lgkmcnt(10)\n\t"
                "v_mfma_f32_32x32x16_bf16 %[a1], v[236:239], v[232:235], %[a1]\n\t"
                "ds_read_b64 v[204:205], %[kb] offset:3232\n\t"
                "ds_read_b64 v[206:207], %[kb] offset:3240\n\t"
                "s_waitcnt lgkmcnt(9)\n\t"
                "v_mfma_f32_32x32x16_bf16 %[a1], v[240:243], v[244:247], %[a1]\n\t"
                "ds_read_b64 v[208:209], %[kb] offset:1152\n\t"
                "ds_read_b64 v[210:211], %[kb] offset:1160\n\t"
                "ds_read_b128 v[212:215], %[ub] offset:3632\n\t"
                "s_waitcnt lgkmcnt(11)\n\t"
                "v_mfma_f32_32x32x16_bf16 %[a0], v[240:243], v[248:251], %[a0]\n\t"
                "ds_read_b128 v[216:219], %[ub] offset:1072\n\t"
                "s_waitcnt lgkmcnt(10)\n\t"
                "v_mfma_f32_32x32x16_bf16 %[a1], v[252:255], v[248:251], %[a1]\n\t"
                "ds_read_b64 v[220:221], %[kb] offset:3200\n\t"
                "ds_read_b64 v[222:223], %[kb] offset:3208\n\t"
                "s_waitcnt lgkmcnt(9)\n\t"
                "v_mfma_f32_32x32x16_bf16 %[a1], v[192:195], v[196:199], %[a1]\n\t"
                "ds_read_b64 v[224:225], %[kb] offset:1248\n\t"
                "ds_read_b64 v[226:227], %[kb] offset:1256\n\t"
                "ds_read_b128 v[228:231], %[ub] offset:3520\n\t"
                "s_waitcnt lgkmcnt(11)\n\t"
                "v_mfma_f32_32x32x16_bf16 %[a0], v[192:195], v[200:203], %[a0]\n\t"
                "ds_read_b128 v[232:235], %[ub] offset:960\n\t"
                "s_waitcnt lgkmcnt(10)\n\t"
                "v_mfma_f32_32x32x16_bf16 %[a1], v[204:207], v[200:203], %[a1]\n\t"
                "ds_read_b64 v[236:237], %[kb] offset:3296\n\t"
                "ds_read_b64 v[238:239], %[kb] offset:3304\n\t"
                "s_waitcnt lgkmcnt(9)\n\t"
                "v_mfma_f32_32x32x16_bf16 %[a1], v[208:211], v[212:215], %[a1]\n\t"
                "ds_read_b64 v[240:241], %[kb] offset:1216\n\t"
                "ds_read_b64 v[242:243], %[kb] offset:1224\n\t"
                "ds_read_b128 v[244:247], %[ub] offset:3552\n\t"
                "s_waitcnt lgkmcnt(11)\n\t"
                "v_mfma_f32_32x32x16_bf16 %[a0], v[208:211], v[216:219], %[a0]\n\t"
                "ds_read_b128 v[248:251], %[ub] offset:992\n\t"
                "s_waitcnt lgkmcnt(10)\n\t"
                "v_mfma_f32_32x32x16_bf16 %[a1], v[220:223], v[216:219], %[a1]\n\t"
                "ds_read_b64 v[252:253], %[kb] offset:3264\n\t"
                "ds_read_b64 v[254:255], %[kb] offset:3272\n\t"
                "s_waitcnt lgkmcnt(9)\n\t"
                "v_mfma_f32_32x32x16_bf16 %[a1], v[224:227], v[228:231], %[a1]\n\t"
                "ds_read_b64 v[192:193], %[kb] offset:1312\n\t"
                "ds_read_b64 v[194:195], %[kb] offset:1320\n\t"
                "ds_read_b128 v[196:199], %[ub] offset:3440\n\t"
                "s_waitcnt lgkmcnt(11)\n\t"
                "v_mfma_f32_32x32x16_bf16 %[a0], v[224:227], v[232:235], %[a0]\n\t"
                "ds_read_b128 v[200:203], %[ub] offset:880\n\t"
                "s_waitcnt lgkmcnt(10)\n\t"
                "v_mfma_f32_32x32x16_bf16 %[a1], v[236:239], v[232:235], %[a1]\n\t"
                "ds_read_b64 v[204:205], %[kb] offset:3360\n\t"
                "ds_read_b64 v[206:207], %[kb] offset:3368\n\t"
                "s_waitcnt lgkmcnt(9)\n\t"
                "v_mfma_f32_32x32x16_bf16 %[a1], v[240:243], v[244:247], %[a1]\n\t"
                "ds_read_b64 v[208:209], %[kb] offset:1280\n\t"
                "ds_read_b64 v[210:211], %[kb] offset:1288\n\t"
                "ds_read_b128 v[212:215], %[ub] offset:3472\n\t"
                "s_waitcnt lgkmcnt(11)\n\t"
                "v_mfma_f32_32x32x16_bf16 %[a0], v[240:243], v[248:251], %[a0]\n\t"
                "ds_read_b128 v[216:219], %[ub] offset:912\n\t"
                "s_waitcnt lgkmcnt(10)\n\t"
                "v_mfma_f32_32x32x16_bf16 %[a1], v[252:255], v[248:251], %[a1]\n\t"
                "ds_read_b64 v[220:221], %[kb] offset:3328\n\t"
                "ds_read_b64 v[222:223], %[kb] offset:3336\n\t"
                "s_waitcnt lgkmcnt(9)\n\t"
                "v_mfma_f32_32x32x16_bf16 %[a1], v[192:195], v[196:199], %[a1]\n\t"
                "ds_read_b64 v[224:225], %[kb] offset:1376\n\t"
                "ds_read_b64 v[226:227], %[kb] offset:1384\n\t"
                "ds_read_b128 v[228:231], %[ub] offset:3360\n\t"
                "s_waitcnt lgkmcnt(11)\n\t"
                "v_mfma_f32_32x32x16_bf16 %[a0], v[192:195], v[200:203], %[a0]\n\t"
                "ds_read_b128 v[232:235], %[ub] offset:800\n\t"
                "s_waitcnt lgkmcnt(10)\n\t"
                "v_mfma_f32_32x32x16_bf16 %[a1], v[204:207], v[200:203], %[a1]\n\t"
                "ds_read_b64 v[236:237], %[kb] offset:3424\n\t"
                "ds_read_b64 v[238:239], %[kb] offset:3432\n\t"
                "s_waitcnt lgkmcnt(9)\n\t"
                "v_mfma_f32_32x32x16_bf16 %[a1], v[208:211], v[212:215], %[a1]\n\t"
                "ds_read_b64 v[240:241], %[kb] offset:1344\n\t"
                "ds_read_b64 v[242:243], %[kb] offset:1352\n\t"
                "ds_read_b128 v[244:247], %[ub] offset:3392\n\t"
                "s_waitcnt lgkmcnt(11)\n\t"
                "v_mfma_f32_32x32x16_bf16 %[a0], v[208:211], v[216:219], %[a0]\n\t"
                "ds_read_b128 v[248:251], %[ub] offset:832\n\t"
                "s_waitcnt lgkmcnt(10)\n\t"
                "v_mfma_f32_32x32x16_bf16 %[a1], v[220:223], v[216:219], %[a1]\n\t"
                "ds_read_b64 v[252:253], %[kb] offset:3392\n\t"
                "ds_read_b64 v[254:255], %[kb] offset:3400\n\t"
                "s_waitcnt lgkmcnt(9)\n\t"
                "v_mfma_f32_32x32x16_bf16 %[a1], v[224:227], v[228:231], %[a1]\n\t"
                "ds_read_b64 v[192:193], %[kb] offset:1440\n\t"
                "ds_read_b64 v[194:195], %[kb] offset:1448\n\t"
                "ds_read_b128 v[196:199], %[ub] offset:3280\n\t"
                "s_waitcnt lgkmcnt(11)\n\t"
                "v_mfma_f32_32x32x16_bf16 %[a0], v[224:227], v[232:235], %[a0]\n\t"
                "ds_read_b128 v[200:203], %[ub] offset:720\n\t"
                "s_waitcnt lgkmcnt(10)\n\t"
                "v_mfma_f32_32x32x16_bf16 %[a1], v[236:239], v[232:235], %[a1]\n\t"
                "ds_read_b64 v[204:205], %[kb] offset:3488\n\t"
                "ds_read_b64 v[206:207], %[kb] offset:3496\n\t"
                "s_waitcnt lgkmcnt(9)\n\t"
                "v_mfma_f32_32x32x16_bf16 %[a1], v[240:243], v[244:247], %[a1]\n\t"
                "ds_read_b64 v[208:209], %[kb] offset:1408\n\t"
                "ds_read_b64 v[210:211], %[kb] offset:1416\n\t"
                "ds_read_b128 v[212:215], %[ub] offset:3312\n\t"
                "s_waitcnt lgkmcnt(11)\n\t"
                "v_mfma_f32_32x32x16_bf16 %[a0], v[240:243], v[248:251], %[a0]\n\t"
                "ds_read_b128 v[216:219], %[ub] offset:752\n\t"
                "s_waitcnt lgkmcnt(10)\n\t"
                "v_mfma_f32_32x32x16_bf16 %[a1], v[252:255], v[248:251], %[a1]\n\t"
                "ds_read_b64 v[220:221], %[kb] offset:3456\n\t"
                "ds_read_b64 v[222:223], %[kb] offset:3464\n\t"
                "s_waitcnt lgkmcnt(9)\n\t"
                "v_mfma_f32_32x32x16_bf16 %[a1], v[192:195], v[196:199], %[a1]\n\t"
                "ds_read_b64 v[224:225], %[kb] offset:1504\n\t"
                "ds_read_b64 v[226:227], %[kb] offset:1512\n\t"
                "ds_read_b128 v[228:231], %[ub] offset:3200\n\t"
                "s_waitcnt lgkmcnt(11)\n\t"
                "v_mfma_f32_32x32x16_bf16 %[a0], v[192:195], v[200:203], %[a0]\n\t"
                "ds_read_b128 v[232:235], %[ub] offset:640\n\t"
                "s_waitcnt lgkmcnt(10)\n\t"
                "v_mfma_f32_32x32x16_bf16 %[a1], v[204:207], v[200:203], %[a1]\n\t"
                "ds_read_b64 v[236:237], %[kb] offset:3552\n\t"
                "ds_read_b64 v[238:239], %[kb] offset:3560\n\t"
                "s_waitcnt lgkmcnt(9)\n\t"
                "v_mfma_f32_32x32x16_bf16 %[a1], v[208:211], v[212:215], %[a1]\n\t"
                "ds_read_b64 v[240:241], %[kb] offset:1472\n\t"
                "ds_read_b64 v[242:243], %[kb] offset:1480\n\t"
                "ds_read_b128 v[244:247], %[ub] offset:3232\n\t"
                "s_waitcnt lgkmcnt(11)\n\t"
                "v_mfma_f32_32x32x16_bf16 %[a0], v[208:211], v[216:219], %[a0]\n\t"
                "ds_read_b128 v[248:251], %[ub] offset:672\n\t"
                "s_waitcnt lgkmcnt(10)\n\t"
                "v_mfma_f32_32x32x16_bf16 %[a1], v[220:223], v[216:219], %[a1]\n\t"
                "ds_read_b64 v[252:253], %[kb] offset:3520\n\t"
                "ds_read_b64 v[254:255], %[kb] offset:3528\n\t"
                "s_waitcnt lgkmcnt(9)\n\t"
                "v_mfma_f32_32x32x16_bf16 %[a1], v[224:227], v[228:231], %[a1]\n\t"
                "ds_read_b64 v[192:193], %[kb] offset:1568\n\t"
                "ds_read_b64 v[194:195], %[kb] offset:1576\n\t"
                "ds_read_b128 v[196:199], %[ub] offset:3120\n\t"
                "s_waitcnt lgkmcnt(11)\n\t"
                "v_mfma_f32_32x32x16_bf16 %[a0], v[224:227], v[232:235], %[a0]\n\t"
                "ds_read_b128 v[200:203], %[ub] offset:560\n\t"
                "s_waitcnt lgkmcnt(10)\n\t"
                "v_mfma_f32_32x32x16_bf16 %[a1], v[236:239], v[232:235], %[a1]\n\t"
                "ds_read_b64 v[204:205], %[kb] offset:3616\n\t"
                "ds_read_b64 v[206:207], %[kb] offset:3624\n\t"
                "s_waitcnt lgkmcnt(9)\n\t"
                "v_mfma_f32_32x32x16_bf16 %[a1], v[240:243], v[244:247], %[a1]\n\t"
                "ds_read_b64 v[208:209], %[kb] offset:1536\n\t"
                "ds_read_b64 v[210:211], %[kb] offset:1544\n\t"
                "ds_read_b128 v[212:215], %[ub] offset:3152\n\t"
                "s_waitcnt lgkmcnt(11)\n\t"
                "v_mfma_f32_32x32x16_bf16 %[a0], v[240:243], v[248:251], %[a0]\n\t"
                "ds_read_b128 v[216:219], %[ub] offset:592\n\t"
                "s_waitcnt lgkmcnt(10)\n\t"
                "v_mfma_f32_32x32x16_bf16 %[a1], v[252:255], v[248:251], %[a1]\n\t"
                "ds_read_b64 v[220:221], %[kb] offset:3584\n\t"
                "ds_read_b64 v[222:223], %[kb] offset:3592\n\t"
                "s_waitcnt lgkmcnt(9)\n\t"
                "v_mfma_f32_32x32x16_bf16 %[a1], v[192:195], v[196:199], %[a1]\n\t"
                "ds_read_b64 v[224:225], %[kb] offset:1632\n\t"
                "ds_read_b64 v[226:227], %[kb] offset:1640\n\t"
                "ds_read_b128 v[228:231], %[ub] offset:3040\n\t"
                "s_waitcnt lgkmcnt(11)\n\t"
                "v_mfma_f32_32x32x16_bf16 %[a0], v[192:195], v[200:203], %[a0]\n\t"
                "ds_read_b128 v[232:235], %[ub] offset:480\n\t"
                "s_waitcnt lgkmcnt(10)\n\t"
                "v_mfma_f32_32x32x16_bf16 %[a1], v[204:207], v[200:203], %[a1]\n\t"
                "ds_read_b64 v[236:237], %[kb] offset:3680\n\t"
                "ds_read_b64 v[238:239], %[kb] offset:3688\n\t"
                "s_waitcnt lgkmcnt(9)\n\t"
                "v_mfma_f32_32x32x16_bf16 %[a1], v[208:211], v[212:215], %[a1]\n\t"
                "ds_read_b64 v[240:241], %[kb] offset:1600\n\t"
                "ds_read_b64 v[242:243], %[kb] offset:1608\n\t"
                "ds_read_b128 v[244:247], %[ub] offset:3072\n\t"
                "s_waitcnt lgkmcnt(11)\n\t"
                "v_mfma_f32_32x32x16_bf16 %[a0], v[208:211], v[216:219], %[a0]\n\t"
                "ds_read_b128 v[248:251], %[ub] offset:512\n\t"
                "s_waitcnt lgkmcnt(10)\n\t"
                "v_mfma_f32_32x32x16_bf16 %[a1], v[220:223], v[216:219], %[a1]\n\t"
                "ds_read_b64 v[252:253], %[kb] offset:3648\n\t"
                "ds_read_b64 v[254:255], %[kb] offset:3656\n\t"
                "s_waitcnt lgkmcnt(9)\n\t"
                "v_mfma_f32_32x32x16_bf16 %[a1], v[224:227], v[228:231], %[a1]\n\t"
                "ds_read_b64 v[192:193], %[kb] offset:1696\n\t"
                "ds_read_b64 v[194:195], %[kb] offset:1704\n\t"
                "ds_read_b128 v[196:199], %[ub] offset:2960\n\t"
                "s_waitcnt lgkmcnt(11)\n\t"
                "v_mfma_f32_32x32x16_bf16 %[a0], v[224:227], v[232:235], %[a0]\n\t"
                "ds_read_b128 v[200:203], %[ub] offset:400\n\t"
                "s_waitcnt lgkmcnt(10)\n\t"
                "v_mfma_f32_32x32x16_bf16 %[a1], v[236:239], v[232:235], %[a1]\n\t"
                "ds_read_b64 v[204:205], %[kb] offset:3744\n\t"
                "ds_read_b64 v[206:207], %[kb] offset:3752\n\t"
                "s_waitcnt lgkmcnt(9)\n\t"
                "v_mfma_f32_32x32x16_bf16 %[a1], v[240:243], v[244:247], %[a1]\n\t"
                "ds_read_b64 v[208:209], %[kb] offset:1664\n\t"
                "ds_read_b64 v[210:211], %[kb] offset:1672\n\t"
                "ds_read_b128 v[212:215], %[ub] offset:2992\n\t"
                "s_waitcnt lgkmcnt(11)\n\t"
                "v_mfma_f32_32x32x16_bf16 %[a0], v[240:243], v[248:251], %[a0]\n\t"
                "ds_read_b128 v[216:219], %[ub] offset:432\n\t"
                "s_waitcnt lgkmcnt(10)\n\t"
                "v_mfma_f32_32x32x16_bf16 %[a1], v[252:255], v[248:251], %[a1]\n\t"
                "ds_read_b64 v[220:221], %[kb] offset:3712\n\t"
                "ds_read_b64 v[222:223], %[kb] offset:3720\n\t"
                "s_waitcnt lgkmcnt(9)\n\t"
                "v_mfma_f32_32x32x16_bf16 %[a1], v[192:195], v[196:199], %[a1]\n\t"
                "ds_read_b64 v[224:225], %[kb] offset:1760\n\t"
                "ds_read_b64 v[226:227], %[kb] offset:1768\n\t"
                "ds_read_b128 v[228:231], %[ub] offset:2880\n\t"
                "s_waitcnt lgkmcnt(11)\n\t"
                "v_mfma_f32_32x32x16_bf16 %[a0], v[192:195], v[200:203], %[a0]\n\t"
                "ds_read_b128 v[232:235], %[ub] offset:320\n\t"
                "s_waitcnt lgkmcnt(10)\n\t"
                "v_mfma_f32_32x32x16_bf16 %[a1], v[204:207], v[200:203], %[a1]\n\t"
                "ds_read_b64 v[236:237], %[kb] offset:3808\n\t"
                "ds_read_b64 v[238:239], %[kb] offset:3816\n\t"
                "s_waitcnt lgkmcnt(9)\n\t"
                "v_mfma_f32_32x32x16_bf16 %[a1], v[208:211], v[212:215], %[a1]\n\t"
                "ds_read_b64 v[240:241], %[kb] offset:1728\n\t"
                "ds_read_b64 v[242:243], %[kb] offset:1736\n\t"
                "ds_read_b128 v[244:247], %[ub] offset:2912\n\t"
                "s_waitcnt lgkmcnt(11)\n\t"
                "v_mfma_f32_32x32x16_bf16 %[a0], v[208:211], v[216:219], %[a0]\n\t"
                "ds_read_b128 v[248:251], %[ub] offset:352\n\t"
                "s_waitcnt lgkmcnt(10)\n\t"
                "v_mfma_f32_32x32x16_bf16 %[a1], v[220:223], v[216:219], %[a1]\n\t"
                "ds_read_b64 v[252:253], %[kb] offset:3776\n\t"
                "ds_read_b64 v[254:255], %[kb] offset:3784\n\t"
                "s_waitcnt lgkmcnt(9)\n\t"
                "v_mfma_f32_32x32x16_bf16 %[a1], v[224:227], v[228:231], %[a1]\n\t"
                "ds_read_b64 v[192:193], %[kb] offset:1824\n\t"
                "ds_read_b64 v[194:195], %[kb] offset:1832\n\t"
                "ds_read_b128 v[196:199], %[ub] offset:2800\n\t"
                "s_waitcnt lgkmcnt(11)\n\t"
                "v_mfma_f32_32x32x16_bf16 %[a0], v[224:227], v[232:235], %[a0]\n\t"
                "ds_read_b128 v[200:203], %[ub] offset:240\n\t"
                "s_waitcnt lgkmcnt(10)\n\t"
                "v_mfma_f32_32x32x16_bf16 %[a1], v[236:239], v[232:235], %[a1]\n\t"
                "ds_read_b64 v[204:205], %[kb] offset:3872\n\t"
                "ds_read_b64 v[206:207], %[kb] offset:3880\n\t"
                "s_waitcnt lgkmcnt(9)\n\t"
                "v_mfma_f32_32x32x16_bf16 %[a1], v[240:243], v[244:247], %[a1]\n\t"
                "ds_read_b64 v[208:209], %[kb] offset:1792\n\t"
                "ds_read_b64 v[210:211], %[kb] offset:1800\n\t"
                "ds_read_b128 v[212:215], %[ub] offset:2832\n\t"
                "s_waitcnt lgkmcnt(11)\n\t"
                "v_mfma_f32_32x32x16_bf16 %[a0], v[240:243], v[248:251], %[a0]\n\t"
                "ds_read_b128 v[216:219], %[ub] offset:272\n\t"
                "s_waitcnt lgkmcnt(10)\n\t"
                "v_mfma_f32_32x32x16_bf16 %[a1], v[252:255], v[248:251], %[a1]\n\t"
                "ds_read_b64 v[220:221], %[kb] offset:3840\n\t"
                "ds_read_b64 v[222:223], %[kb] offset:3848\n\t"
                "s_waitcnt lgkmcnt(9)\n\t"
                "v_mfma_f32_32x32x16_bf16 %[a1], v[192:195], v[196:199], %[a1]\n\t"
                "ds_read_b64 v[224:225], %[kb] offset:1888\n\t"
                "ds_read_b64 v[226:227], %[kb] offset:1896\n\t"
                "ds_read_b128 v[228:231], %[ub] offset:2720\n\t"
                "s_waitcnt lgkmcnt(11)\n\t"
                "v_mfma_f32_32x32x16_bf16 %[a0], v[192:195], v[200:203], %[a0]\n\t"
                "ds_read_b128 v[232:235], %[ub] offset:160\n\t"
                "s_waitcnt lgkmcnt(10)\n\t"
                "v_mfma_f32_32x32x16_bf16 %[a1], v[204:207], v[200:203], %[a1]\n\t"
                "ds_read_b64 v[236:237], %[kb] offset:3936\n\t"
                "ds_read_b64 v[238:239], %[kb] offset:3944\n\t"
                "s_waitcnt lgkmcnt(9)\n\t"
                "v_mfma_f32_32x32x16_bf16 %[a1], v[208:211], v[212:215], %[a1]\n\t"
                "ds_read_b64 v[240:241], %[kb] offset:1856\n\t"
                "ds_read_b64 v[242:243], %[kb] offset:1864\n\t"
                "ds_read_b128 v[244:247], %[ub] offset:2752\n\t"
                "s_waitcnt lgkmcnt(11)\n\t"
                "v_mfma_f32_32x32x16_bf16 %[a0], v[208:211], v[216:219], %[a0]\n\t"
                "ds_read_b128 v[248:251], %[ub] offset:192\n\t"
                "s_waitcnt lgkmcnt(10)\n\t"
                "v_mfma_f32_32x32x16_bf16 %[a1], v[220:223], v[216:219], %[a1]\n\t"
                "ds_read_b64 v[252:253], %[kb] offset:3904\n\t"
                "ds_read_b64 v[254:255], %[kb] offset:3912\n\t"
                "s_waitcnt lgkmcnt(9)\n\t"
                "v_mfma_f32_32x32x16_bf16 %[a1], v[224:227], v[228:231], %[a1]\n\t"
                "ds_read_b64 v[192:193], %[kb] offset:1952\n\t"
                "ds_read_b64 v[194:195], %[kb] offset:1960\n\t"
                "ds_read_b128 v[196:199], %[ub] offset:2640\n\t"
                "s_waitcnt lgkmcnt(11)\n\t"
                "v_mfma_f32_32x32x16_bf16 %[a0], v[224:227], v[232:235], %[a0]\n\t"
                "ds_read_b128 v[200:203], %[ub] offset:80\n\t"
                "s_waitcnt lgkmcnt(10)\n\t"
                "v_mfma_f32_32x32x16_bf16 %[a1], v[236:239], v[232:235], %[a1]\n\t"
                "ds_read_b64 v[204:205], %[kb] offset:4000\n\t"
                "ds_read_b64 v[206:207], %[kb] offset:4008\n\t"
                "s_waitcnt lgkmcnt(9)\n\t"
                "v_mfma_f32_32x32x16_bf16 %[a1], v[240:243], v[244:247], %[a1]\n\t"
                "ds_read_b64 v[208:209], %[kb] offset:1920\n\t"
                "ds_read_b64 v[210:211], %[kb] offset:1928\n\t"
                "ds_read_b128 v[212:215], %[ub] offset:2672\n\t"
                "s_waitcnt lgkmcnt(11)\n\t"
                "v_mfma_f32_32x32x16_bf16 %[a0], v[240:243], v[248:251], %[a0]\n\t"
                "ds_read_b128 v[216:219], %[ub] offset:112\n\t"
                "s_waitcnt lgkmcnt(10)\n\t"
                "v_mfma_f32_32x32x16_bf16 %[a1], v[252:255], v[248:251], %[a1]\n\t"
                "ds_read_b64 v[220:221], %[kb] offset:3968\n\t"
                "ds_read_b64 v[222:223], %[kb] offset:3976\n\t"
                "s_waitcnt lgkmcnt(9)\n\t"
                "v_mfma_f32_32x32x16_bf16 %[a1], v[192:195], v[196:199], %[a1]\n\t"
                "ds_read_b64 v[224:225], %[kb] offset:2016\n\t"
                "ds_read_b64 v[226:227], %[kb] offset:2024\n\t"
                "ds_read_b128 v[228:231], %[ub] offset:2560\n\t"
                "s_waitcnt lgkmcnt(11)\n\t"
                "v_mfma_f32_32x32x16_bf16 %[a0], v[192:195], v[200:203], %[a0]\n\t"
                "ds_read_b128 v[232:235], %[ub] offset:0\n\t"
                "s_waitcnt lgkmcnt(10)\n\t"
                "v_mfma_f32_32x32x16_bf16 %[a1], v[204:207], v[200:203], %[a1]\n\t"
                "ds_read_b64 v[236:237], %[kb] offset:4064\n\t"
                "ds_read_b64 v[238:239], %[kb] offset:4072\n\t"
                "s_waitcnt lgkmcnt(9)\n\t"
                "v_mfma_f32_32x32x16_bf16 %[a1], v[208:211], v[212:215], %[a1]\n\t"
                "ds_read_b64 v[240:241], %[kb] offset:1984\n\t"
                "ds_read_b64 v[242:243], %[kb] offset:1992\n\t"
                "ds_read_b128 v[244:247], %[ub] offset:2592\n\t"
                "s_waitcnt lgkmcnt(11)\n\t"
                "v_mfma_f32_32x32x16_bf16 %[a0], v[208:211], v[216:219], %[a0]\n\t"
                "ds_read_b128 v[248:251], %[ub] offset:32\n\t"
                "s_waitcnt lgkmcnt(10)\n\t"
                "v_mfma_f32_32x32x16_bf16 %[a1], v[220:223], v[216:219], %[a1]\n\t"
                "ds_read_b64 v[252:253], %[kb] offset:4032\n\t"
                "ds_read_b64 v[254:255], %[kb] offset:4040\n\t"
                "s_waitcnt lgkmcnt(9)\n\t"
                "v_mfma_f32_32x32x16_bf16 %[a1], v[224:227], v[228:231], %[a1]\n\t"
                "s_waitcnt lgkmcnt(8)\n\t"
                "v_mfma_f32_32x32x16_bf16 %[a0], v[224:227], v[232:235], %[a0]\n\t"
                "s_waitcnt lgkmcnt(6)\n\t"
                "v_mfma_f32_32x32x16_bf16 %[a1], v[236:239], v[232:235], %[a1]\n\t"
                "s_waitcnt lgkmcnt(3)\n\t"
                "v_mfma_f32_32x32x16_bf16 %[a1], v[240:243], v[244:247], %[a1]\n\t"
                "s_waitcnt lgkmcnt(2)\n\t"
                "v_mfma_f32_32x32x16_bf16 %[a0], v[240:243], v[248:251], %[a0]\n\t"
                "s_waitcnt lgkmcnt(0)\n\t"
                "v_mfma_f32_32x32x16_bf16 %[a1], v[252:255], v[248:251], %[a1]\n\t"
                "s_nop 7\n\t"
                "s_nop 3\n\t"
                : [a0] "=&v"(acc0), [a1] "=&v"(acc1) : [kb] "v"(kb), [ub] "v"(ub0 + c8 * CV_U) : "memory", "v192", "v193", "v194", "v195", "v196", "v197", "v198", "v199", "v200", "v201", "v202", "v203", "v204", "v205", "v206", "v207", "v208", "v209", "v210", "v211", "v212", "v213", "v214", "v215", "v216", "v217", "v218", "v219", "v220", "v221", "v222", "v223", "v224", "v225", "v226", "v227", "v228", "v229", "v230", "v231", "v232", "v233", "v234", "v235", "v236", "v237", "v238", "v239", "v240", "v241", "v242", "v243", "v244", "v245", "v246", "v247", "v248", "v249", "v250", "v251", "v252", "v253", "v254", "v255");
            unsigned char* uc = lds + c8 * CV_U + (32 + b) * CV_RP + kg * 8;
#pragma unroll
            for (int q = 0; q < 4; ++q) {
                *(v2u*)(uc + q * 16) = (v2u){pack2(acc0[4 * q], acc0[4 * q + 1]), pack2(acc0[4 * q + 2], acc0[4 * q + 3])};
                *(v2u*)(uc + 32 * CV_RP + q * 16) = (v2u){pack2(acc1[4 * q], acc1[4 * q + 1]), pack2(acc1[4 * q + 2], acc1[4 * q + 3])}; }
            __builtin_amdgcn_wave_barrier();
        }
    __syncthreads();
#pragma unroll
    for (int G = 0; G < 4; ++G) { const v4u lo = *(const v4u*)(urow + G * 16), hh = *(const v4u*)(urow + CV_U + G * 16); v4u o[2];
#pragma unroll
        for (int i = 0; i < 8; ++i) { const unsigned u0 = ur[G][(7 - i) >> 1], u1 = ur[4 + G][(7 - i) >> 1], qq = qr[2 * G + (i >> 2)][i & 3];
            const unsigned ud = pack2(((i & 1) ? blo(u0) : bhi(u0)) * blo(dv), ((i & 1) ? blo(u1) : bhi(u1)) * bhi(dv));
            const unsigned y = pack2(((i & 1) ? bhi(lo[i >> 1]) : blo(lo[i >> 1])) + blo(ud), ((i & 1) ? bhi(hh[i >> 1]) : blo(hh[i >> 1])) + bhi(ud));
            o[i >> 2][i & 3] = pack2(blo(qq) * blo(y), bhi(qq) * bhi(y)); }
        st16(r, W_A + un * 32768 + 2 * G * 4096 + tid * 16, o[0]); st16(r, W_A + un * 32768 + (2 * G + 1) * 4096 + tid * 16, o[1]); }
    DRAIN(); __syncthreads();
    if (tid == 0) __hip_atomic_fetch_add((unsigned*)(p.ws + F_CVY + (un >> 3) * 128), 1u, __ATOMIC_RELAXED, __HIP_MEMORY_SCOPE_AGENT);
    return true;
}

__device__ __forceinline__ void proj_block(const Params& p, unsigned char* lds, const int wv, const int h, const int c) {
    const int w = wv, l = otid() & 63, n = l & 15, g = l >> 4, tm = w >> 1, tn = w & 1;
    const unsigned lb = __builtin_amdgcn_readfirstlane((unsigned)(size_t)lds);
    const v4u sx = mk_srd(p.Aq, S * D * 2), sq = mk_srd(c < 2 ? p.Wq : p.Wk, 1024 * 4096 * 2), sv = mk_srd(p.Wv, 1024 * 4096 * 2), sw = mk_srd(p.ws, W_TOTAL);
    const unsigned lx = lb + (32 * tm + n) * 128 + ((g ^ (n & 7)) << 4), lr = lb + 131072, rr = 8 * g + (n >> 2);
    const int cv = ((c & 1) ^ 1) * 2 + (c >> 1);
    const unsigned aq = (c < 2 ? W_QX : W_KX) + (tm * 32 + h) * 8192 + (2 * (c & 1) + tn) * 1024 + l * 16;
    unsigned t0, xo, so, cn; unsigned long long tq;
    asm volatile(
        "s_nop 4\n\t"
        "s_mov_b32 %[xo], 0\n\t"
        "s_mov_b32 %[so], 0\n\t"
        "s_mov_b32 %[cn], 1\n\t"
        "s_add_u32 m0, %[ld], 0\n\t"
        "s_add_u32 %[t0], %[xo], 0\n\t"
        "buffer_load_dwordx4 v100, %[sx], %[t0] offen lds\n\t"
        "s_add_u32 m0, %[ld], 1024\n\t"
        "s_add_u32 %[t0], %[xo], 16384\n\t"
        "buffer_load_dwordx4 v100, %[sx], %[t0] offen lds\n\t"
        "s_add_u32 m0, %[ld], 8192\n\t"
        "s_add_u32 %[t0], %[xo], 128\n\t"
        "buffer_load_dwordx4 v100, %[sx], %[t0] offen lds\n\t"
        "s_add_u32 m0, %[ld], 9216\n\t"
        "s_add_u32 %[t0], %[xo], 16512\n\t"
        "buffer_load_dwordx4 v100, %[sx], %[t0] offen lds\n\t"
        "s_add_u32 m0, %[ld], 16384\n\t"
        "s_add_u32 %[t0], %[xo], 256\n\t"
        "buffer_load_dwordx4 v100, %[sx], %[t0] offen lds\n\t"
        "s_add_u32 m0, %[ld], 17408\n\t"
        "s_add_u32 %[t0], %[xo], 16640\n\t"
        "buffer_load_dwordx4 v100, %[sx], %[t0] offen lds\n\t"
        "s_add_u32 m0, %[ld], 24576\n\t"
        "s_add_u32 %[t0], %[xo], 384\n\t"
        "buffer_load_dwordx4 v100, %[sx], %[t0] offen lds\n\t"
        "s_add_u32 m0, %[ld], 25600\n\t"
        "s_add_u32 %[t0], %[xo], 16768\n\t"
        "buffer_load_dwordx4 v100, %[sx], %[t0] offen lds\n\t"
        "s_mov_b32 %[t0], 0\n\t"
        "buffer_load_dwordx4 a[192:195], v101, %[sq], %[t0] offen\n\t"
        "buffer_load_dwordx4 a[200:203], v112, %[sv], %[t0] offen\n\t"
        "s_mov_b32 %[t0], 262144\n\t"
        "buffer_load_dwordx4 a[196:199], v101, %[sq], %[t0] offen\n\t"
        "s_mov_b32 %[t0], 524288\n\t"
        "buffer_load_dwordx4 a[204:207], v101, %[sq], %[t0] offen\n\t"
        "buffer_load_dwordx4 a[212:215], v112, %[sv], %[t0] offen\n\t"
        "s_mov_b32 %[t0], 786432\n\t"
        "buffer_load_dwordx4 a[208:211], v101, %[sq], %[t0] offen\n\t"
        "s_mov_b32 %[t0], 1048576\n\t"
        "buffer_load_dwordx4 a[216:219], v101, %[sq], %[t0] offen\n\t"
        "buffer_load_dwordx4 a[224:227], v112, %[sv], %[t0] offen\n\t"
        "s_mov_b32 %[t0], 1310720\n\t"
        "buffer_load_dwordx4 a[220:223], v101, %[sq], %[t0] offen\n\t"
        "s_mov_b32 %[t0], 1572864\n\t"
        "buffer_load_dwordx4 a[228:231], v101, %[sq], %[t0] offen\n\t"
        "buffer_load_dwordx4 a[236:239], v112, %[sv], %[t0] offen\n\t"
        "s_mov_b32 %[t0], 1835008\n\t"
        "buffer_load_dwordx4 a[232:235], v101, %[sq], %[t0] offen\n\t"
        "s_waitcnt vmcnt(9)\n\t"
        "ds_write_b128 v102, a[192:195] offset:0\n\t"
        "ds_write_b128 v102, a[196:199] offset:4096\n\t"
        "ds_write_b128 v116, a[200:203] offset:8192\n\t"
        "s_waitcnt lgkmcnt(0)\n\t"
        "s_barrier\n\t"
        "ds_read_b64_tr_b16 a[0:1], v103 offset:0\n\t"
        "ds_read_b64_tr_b16 a[2:3], v103 offset:512\n\t"
        "ds_read_b64_tr_b16 a[4:5], v103 offset:8\n\t"
        "ds_read_b64_tr_b16 a[6:7], v103 offset:520\n\t"
        "ds_read_b64_tr_b16 a[8:9], v104 offset:0\n\t"
        "ds_read_b64_tr_b16 a[10:11], v104 offset:256\n\t"
        "s_waitcnt lgkmcnt(5)\n\t"
        "ds_read_b64_tr_b16 a[12:13], v103 offset:4096\n\t"
        "ds_read_b64_tr_b16 a[14:15], v103 offset:4608\n\t"
        "ds_read_b64_tr_b16 a[16:17], v103 offset:4104\n\t"
        "ds_read_b64_tr_b16 a[18:19], v103 offset:4616\n\t"
        "ds_read_b64_tr_b16 a[20:21], v104 offset:2048\n\t"
        "ds_read_b64_tr_b16 a[22:23], v104 offset:2304\n\t"
        "ds_read_b128 v[56:59], v105 offset:0\n\t"
        "ds_read_b128 v[60:63], v105 offset:2048\n\t"
        "ds_read_b128 v[64:67], v106 offset:0\n\t"
        "ds_read_b128 v[68:71], v106 offset:2048\n\t"
        "s_add_u32 m0, %[ld], 32768\n\t"
        "s_add_u32 %[t0], %[xo], 512\n\t"
        "buffer_load_dwordx4 v100, %[sx], %[t0] offen lds\n\t"
        "s_add_u32 m0, %[ld], 33792\n\t"
        "s_add_u32 %[t0], %[xo], 16896\n\t"
        "buffer_load_dwordx4 v100, %[sx], %[t0] offen lds\n\t"
        "s_add_u32 m0, %[ld], 40960\n\t"
        "s_add_u32 %[t0], %[xo], 640\n\t"
        "buffer_load_dwordx4 v100, %[sx], %[t0] offen lds\n\t"
        "s_add_u32 m0, %[ld], 41984\n\t"
        "s_add_u32 %[t0], %[xo], 17024\n\t"
        "buffer_load_dwordx4 v100, %[sx], %[t0] offen lds\n\t"
        "s_add_u32 m0, %[ld], 49152\n\t"
        "s_add_u32 %[t0], %[xo], 768\n\t"
        "buffer_load_dwordx4 v100, %[sx], %[t0] offen lds\n\t"
        "s_add_u32 m0, %[ld], 50176\n\t"
        "s_add_u32 %[t0], %[xo], 17152\n\t"
        "buffer_load_dwordx4 v100, %[sx], %[t0] offen lds\n\t"
        "s_add_u32 m0, %[ld], 57344\n\t"
        "s_add_u32 %[t0], %[xo], 896\n\t"
        "buffer_load_dwordx4 v100, %[sx], %[t0] offen lds\n\t"
        "s_add_u32 m0, %[ld], 58368\n\t"
        "s_add_u32 %[t0], %[xo], 17280\n\t"
        "buffer_load_dwordx4 v100, %[sx], %[t0] offen lds\n\t"
        "s_mov_b32 %[t0], 2097152\n\t"
        "buffer_load_dwordx4 a[240:243], v101, %[sq], %[t0] offen\n\t"
        "buffer_load_dwordx4 a[248:251], v112, %[sv], %[t0] offen\n\t"
        "s_mov_b32 %[t0], 2359296\n\t"
        "buffer_load_dwordx4 a[244:247], v101, %[sq], %[t0] offen\n\t"
        "s_waitcnt vmcnt(17)\n\t"
        "ds_write_b128 v102, a[204:207] offset:16384\n\t"
        "ds_write_b128 v102, a[208:211] offset:20480\n\t"
        "ds_write_b128 v116, a[212:215] offset:24576\n\t"
        "s_waitcnt lgkmcnt(0)\n\t"
        "s_barrier\n\t"
        "v_mfma_f32_16x16x32_bf16 v[8:11], a[0:3], v[56:59], 0\n\t"
        "ds_read_b64_tr_b16 a[24:25], v103 offset:16384\n\t"
        "ds_read_b64_tr_b16 a[26:27], v103 offset:16896\n\t"
        "v_mfma_f32_16x16x32_bf16 v[12:15], a[4:7], v[56:59], 0\n\t"
        "ds_read_b64_tr_b16 a[28:29], v103 offset:16392\n\t"
        "ds_read_b64_tr_b16 a[30:31], v103 offset:16904\n\t"
        "v_mfma_f32_16x16x32_bf16 v[16:19], v[56:59], a[8:11], 0\n\t"
        "ds_read_b64_tr_b16 a[32:33], v104 offset:16384\n\t"
        "ds_read_b64_tr_b16 a[34:35], v104 offset:16640\n\t"
        "v_mfma_f32_16x16x32_bf16 v[20:23], a[0:3], v[60:63], 0\n\t"
        "s_waitcnt lgkmcnt(5)\n\t"
        "ds_read_b64_tr_b16 a[36:37], v103 offset:20480\n\t"
        "v_mfma_f32_16x16x32_bf16 v[24:27], a[4:7], v[60:63], 0\n\t"
        "ds_read_b64_tr_b16 a[38:39], v103 offset:20992\n\t"
        "ds_read_b64_tr_b16 a[40:41], v103 offset:20488\n\t"
        "v_mfma_f32_16x16x32_bf16 v[28:31], v[60:63], a[8:11], 0\n\t"
        "ds_read_b64_tr_b16 a[42:43], v103 offset:21000\n\t"
        "ds_read_b64_tr_b16 a[44:45], v104 offset:18432\n\t"
        "v_mfma_f32_16x16x32_bf16 v[8:11], a[12:15], v[64:67], v[8:11]\n\t"
        "ds_read_b64_tr_b16 a[46:47], v104 offset:18688\n\t"
        "ds_read_b128 v[72:75], v105 offset:8192\n\t"
        "v_mfma_f32_16x16x32_bf16 v[12:15], a[16:19], v[64:67], v[12:15]\n\t"
        "ds_read_b128 v[76:79], v105 offset:10240\n\t"
        "ds_read_b128 v[80:83], v106 offset:8192\n\t"
        "v_mfma_f32_16x16x32_bf16 v[16:19], v[64:67], a[20:23], v[16:19]\n\t"
        "ds_read_b128 v[84:87], v106 offset:10240\n\t"
        "v_mfma_f32_16x16x32_bf16 v[20:23], a[12:15], v[68:71], v[20:23]\n\t"
        "v_mfma_f32_16x16x32_bf16 v[24:27], a[16:19], v[68:71], v[24:27]\n\t"
        "v_mfma_f32_16x16x32_bf16 v[28:31], v[68:71], a[20:23], v[28:31]\n\t"
        "s_mov_b32 %[t0], 2621440\n\t"
        "buffer_load_dwordx4 a[252:255], v101, %[sq], %[t0] offen\n\t"
        "buffer_load_dwordx4 v[132:135], v112, %[sv], %[t0] offen\n\t"
        "s_mov_b32 %[t0], 2883584\n\t"
        "buffer_load_dwordx4 v[128:131], v101, %[sq], %[t0] offen\n\t"
        "s_waitcnt vmcnt(17)\n\t"
        "ds_write_b128 v102, a[216:219] offset:0\n\t"
        "ds_write_b128 v102, a[220:223] offset:4096\n\t"
        "ds_write_b128 v116, a[224:227] offset:8192\n\t"
        "s_waitcnt lgkmcnt(0)\n\t"
        "s_barrier\n\t"
        "v_mfma_f32_16x16x32_bf16 v[8:11], a[24:27], v[72:75], v[8:11]\n\t"
        "ds_read_b64_tr_b16 a[48:49], v103 offset:0\n\t"
        "ds_read_b64_tr_b16 a[50:51], v103 offset:512\n\t"
        "v_mfma_f32_16x16x32_bf16 v[12:15], a[28:31], v[72:75], v[12:15]\n\t"
        "ds_read_b64_tr_b16 a[52:53], v103 offset:8\n\t"
        "ds_read_b64_tr_b16 a[54:55], v103 offset:520\n\t"
        "v_mfma_f32_16x16x32_bf16 v[16:19], v[72:75], a[32:35], v[16:19]\n\t"
        "ds_read_b64_tr_b16 a[56:57], v104 offset:0\n\t"
        "ds_read_b64_tr_b16 a[58:59], v104 offset:256\n\t"
        "v_mfma_f32_16x16x32_bf16 v[20:23], a[24:27], v[76:79], v[20:23]\n\t"
        "s_waitcnt lgkmcnt(5)\n\t"
        "ds_read_b64_tr_b16 a[60:61], v103 offset:4096\n\t"
        "v_mfma_f32_16x16x32_bf16 v[24:27], a[28:31], v[76:79], v[24:27]\n\t"
        "ds_read_b64_tr_b16 a[62:63], v103 offset:4608\n\t"
        "ds_read_b64_tr_b16 a[64:65], v103 offset:4104\n\t"
        "v_mfma_f32_16x16x32_bf16 v[28:31], v[76:79], a[32:35], v[28:31]\n\t"
        "ds_read_b64_tr_b16 a[66:67], v103 offset:4616\n\t"
        "ds_read_b64_tr_b16 a[68:69], v104 offset:2048\n\t"
        "v_mfma_f32_16x16x32_bf16 v[8:11], a[36:39], v[80:83], v[8:11]\n\t"
        "ds_read_b64_tr_b16 a[70:71], v104 offset:2304\n\t"
        "ds_read_b128 v[56:59], v105 offset:16384\n\t"
        "v_mfma_f32_16x16x32_bf16 v[12:15], a[40:43], v[80:83], v[12:15]\n\t"
        "ds_read_b128 v[60:63], v105 offset:18432\n\t"
        "ds_read_b128 v[64:67], v106 offset:16384\n\t"
        "v_mfma_f32_16x16x32_bf16 v[16:19], v[80:83], a[44:47], v[16:19]\n\t"
        "ds_read_b128 v[68:71], v106 offset:18432\n\t"
        "v_mfma_f32_16x16x32_bf16 v[20:23], a[36:39], v[84:87], v[20:23]\n\t"
        "v_mfma_f32_16x16x32_bf16 v[24:27], a[40:43], v[84:87], v[24:27]\n\t"
        "v_mfma_f32_16x16x32_bf16 v[28:31], v[84:87], a[44:47], v[28:31]\n\t"
        "s_mov_b32 %[t0], 3145728\n\t"
        "buffer_load_dwordx4 v[136:139], v101, %[sq], %[t0] offen\n\t"
        "buffer_load_dwordx4 v[144:147], v112, %[sv], %[t0] offen\n\t"
        "s_mov_b32 %[t0], 3407872\n\t"
        "buffer_load_dwordx4 v[140:143], v101, %[sq], %[t0] offen\n\t"
        "s_waitcnt vmcnt(17)\n\t"
        "ds_write_b128 v102, a[228:231] offset:16384\n\t"
        "ds_write_b128 v102, a[232:235] offset:20480\n\t"
        "ds_write_b128 v116, a[236:239] offset:24576\n\t"
        "s_waitcnt lgkmcnt(0)\n\t"
        "s_barrier\n\t"
        "v_mfma_f32_16x16x32_bf16 v[8:11], a[48:51], v[56:59], v[8:11]\n\t"
        "ds_read_b64_tr_b16 a[72:73], v103 offset:16384\n\t"
        "ds_read_b64_tr_b16 a[74:75], v103 offset:16896\n\t"
        "v_mfma_f32_16x16x32_bf16 v[12:15], a[52:55], v[56:59], v[12:15]\n\t"
        "ds_read_b64_tr_b16 a[76:77], v103 offset:16392\n\t"
        "ds_read_b64_tr_b16 a[78:79], v103 offset:16904\n\t"
        "v_mfma_f32_16x16x32_bf16 v[16:19], v[56:59], a[56:59], v[16:19]\n\t"
        "ds_read_b64_tr_b16 a[80:81], v104 offset:16384\n\t"
        "ds_read_b64_tr_b16 a[82:83], v104 offset:16640\n\t"
        "v_mfma_f32_16x16x32_bf16 v[20:23], a[48:51], v[60:63], v[20:23]\n\t"
        "s_waitcnt lgkmcnt(5)\n\t"
        "ds_read_b64_tr_b16 a[84:85], v103 offset:20480\n\t"
        "v_mfma_f32_16x16x32_bf16 v[24:27], a[52:55], v[60:63], v[24:27]\n\t"
        "ds_read_b64_tr_b16 a[86:87], v103 offset:20992\n\t"
        "ds_read_b64_tr_b16 a[88:89], v103 offset:20488\n\t"
        "v_mfma_f32_16x16x32_bf16 v[28:31], v[60:63], a[56:59], v[28:31]\n\t"
        "ds_read_b64_tr_b16 a[90:91], v103 offset:21000\n\t"
        "ds_read_b64_tr_b16 a[92:93], v104 offset:18432\n\t"
        "v_mfma_f32_16x16x32_bf16 v[8:11], a[60:63], v[64:67], v[8:11]\n\t"
        "ds_read_b64_tr_b16 a[94:95], v104 offset:18688\n\t"
        "ds_read_b128 v[72:75], v105 offset:24576\n\t"
        "v_mfma_f32_16x16x32_bf16 v[12:15], a[64:67], v[64:67], v[12:15]\n\t"
        "ds_read_b128 v[76:79], v105 offset:26624\n\t"
        "ds_read_b128 v[80:83], v106 offset:24576\n\t"
        "v_mfma_f32_16x16x32_bf16 v[16:19], v[64:67], a[68:71], v[16:19]\n\t"
        "ds_read_b128 v[84:87], v106 offset:26624\n\t"
        "v_mfma_f32_16x16x32_bf16 v[20:23], a[60:63], v[68:71], v[20:23]\n\t"
        "v_mfma_f32_16x16x32_bf16 v[24:27], a[64:67], v[68:71], v[24:27]\n\t"
        "v_mfma_f32_16x16x32_bf16 v[28:31], v[68:71], a[68:71], v[28:31]\n\t"
        "s_mov_b32 %[t0], 3670016\n\t"
        "buffer_load_dwordx4 v[148:151], v101, %[sq], %[t0] offen\n\t"
        "buffer_load_dwordx4 v[156:159], v112, %[sv], %[t0] offen\n\t"
        "s_mov_b32 %[t0], 3932160\n\t"
        "buffer_load_dwordx4 v[152:155], v101, %[sq], %[t0] offen\n\t"
        "s_waitcnt vmcnt(9)\n\t"
        "ds_write_b128 v102, a[240:243] offset:0\n\t"
        "ds_write_b128 v102, a[244:247] offset:4096\n\t"
        "ds_write_b128 v116, a[248:251] offset:8192\n\t"
        "s_waitcnt lgkmcnt(0)\n\t"
        "s_barrier\n\t"
        "v_mfma_f32_16x16x32_bf16 v[8:11], a[72:75], v[72:75], v[8:11]\n\t"
        "ds_read_b64_tr_b16 a[96:97], v103 offset:0\n\t"
        "ds_read_b64_tr_b16 a[98:99], v103 offset:512\n\t"
        "v_mfma_f32_16x16x32_bf16 v[12:15], a[76:79], v[72:75], v[12:15]\n\t"
        "ds_read_b64_tr_b16 a[100:101], v103 offset:8\n\t"
        "ds_read_b64_tr_b16 a[102:103], v103 offset:520\n\t"
        "v_mfma_f32_16x16x32_bf16 v[16:19], v[72:75], a[80:83], v[16:19]\n\t"
        "ds_read_b64_tr_b16 a[104:105], v104 offset:0\n\t"
        "ds_read_b64_tr_b16 a[106:107], v104 offset:256\n\t"
        "v_mfma_f32_16x16x32_bf16 v[20:23], a[72:75], v[76:79], v[20:23]\n\t"
        "s_waitcnt lgkmcnt(5)\n\t"
        "ds_read_b64_tr_b16 a[108:109], v103 offset:4096\n\t"
        "v_mfma_f32_16x16x32_bf16 v[24:27], a[76:79], v[76:79], v[24:27]\n\t"
        "ds_read_b64_tr_b16 a[110:111], v103 offset:4608\n\t"
        "ds_read_b64_tr_b16 a[112:113], v103 offset:4104\n\t"
        "v_mfma_f32_16x16x32_bf16 v[28:31], v[76:79], a[80:83], v[28:31]\n\t"
        "ds_read_b64_tr_b16 a[114:115], v103 offset:4616\n\t"
        "ds_read_b64_tr_b16 a[116:117], v104 offset:2048\n\t"
        "v_mfma_f32_16x16x32_bf16 v[8:11], a[84:87], v[80:83], v[8:11]\n\t"
        "ds_read_b64_tr_b16 a[118:119], v104 offset:2304\n\t"
        "ds_read_b128 v[56:59], v105 offset:32768\n\t"
        "v_mfma_f32_16x16x32_bf16 v[12:15], a[88:91], v[80:83], v[12:15]\n\t"
        "ds_read_b128 v[60:63], v105 offset:34816\n\t"
        "ds_read_b128 v[64:67], v106 offset:32768\n\t"
        "v_mfma_f32_16x16x32_bf16 v[16:19], v[80:83], a[92:95], v[16:19]\n\t"
        "ds_read_b128 v[68:71], v106 offset:34816\n\t"
        "v_mfma_f32_16x16x32_bf16 v[20:23], a[84:87], v[84:87], v[20:23]\n\t"
        "v_mfma_f32_16x16x32_bf16 v[24:27], a[88:91], v[84:87], v[24:27]\n\t"
        "v_mfma_f32_16x16x32_bf16 v[28:31], v[84:87], a[92:95], v[28:31]\n\t"
        "s_add_u32 m0, %[ld], 65536\n\t"
        "s_add_u32 %[t0], %[xo], 1024\n\t"
        "buffer_load_dwordx4 v100, %[sx], %[t0] offen lds\n\t"
        "s_add_u32 m0, %[ld], 66560\n\t"
        "s_add_u32 %[t0], %[xo], 17408\n\t"
        "buffer_load_dwordx4 v100, %[sx], %[t0] offen lds\n\t"
        "s_add_u32 m0, %[ld], 73728\n\t"
        "s_add_u32 %[t0], %[xo], 1152\n\t"
        "buffer_load_dwordx4 v100, %[sx], %[t0] offen lds\n\t"
        "s_add_u32 m0, %[ld], 74752\n\t"
        "s_add_u32 %[t0], %[xo], 17536\n\t"
        "buffer_load_dwordx4 v100, %[sx], %[t0] offen lds\n\t"
        "s_add_u32 m0, %[ld], 81920\n\t"
        "s_add_u32 %[t0], %[xo], 1280\n\t"
        "buffer_load_dwordx4 v100, %[sx], %[t0] offen lds\n\t"
        "s_add_u32 m0, %[ld], 82944\n\t"
        "s_add_u32 %[t0], %[xo], 17664\n\t"
        "buffer_load_dwordx4 v100, %[sx], %[t0] offen lds\n\t"
        "s_add_u32 m0, %[ld], 90112\n\t"
        "s_add_u32 %[t0], %[xo], 1408\n\t"
        "buffer_load_dwordx4 v100, %[sx], %[t0] offen lds\n\t"
        "s_add_u32 m0, %[ld], 91136\n\t"
        "s_add_u32 %[t0], %[xo], 17792\n\t"
        "buffer_load_dwordx4 v100, %[sx], %[t0] offen lds\n\t"
        "s_mov_b32 %[t0], 4194304\n\t"
        "buffer_load_dwordx4 v[160:163], v101, %[sq], %[t0] offen\n\t"
        "buffer_load_dwordx4 v[168:171], v112, %[sv], %[t0] offen\n\t"
        "s_mov_b32 %[t0], 4456448\n\t"
        "buffer_load_dwordx4 v[164:167], v101, %[sq], %[t0] offen\n\t"
        "s_waitcnt vmcnt(17)\n\t"
        "ds_write_b128 v102, a[252:255] offset:16384\n\t"
        "ds_write_b128 v102, v[128:131] offset:20480\n\t"
        "ds_write_b128 v116, v[132:135] offset:24576\n\t"
        "s_waitcnt lgkmcnt(0)\n\t"
        "s_barrier\n\t"
        "v_mfma_f32_16x16x32_bf16 v[8:11], a[96:99], v[56:59], v[8:11]\n\t"
        "ds_read_b64_tr_b16 a[120:121], v103 offset:16384\n\t"
        "ds_read_b64_tr_b16 a[122:123], v103 offset:16896\n\t"
        "v_mfma_f32_16x16x32_bf16 v[12:15], a[100:103], v[56:59], v[12:15]\n\t"
        "ds_read_b64_tr_b16 a[124:125], v103 offset:16392\n\t"
        "ds_read_b64_tr_b16 a[126:127], v103 offset:16904\n\t"
        "v_mfma_f32_16x16x32_bf16 v[16:19], v[56:59], a[104:107], v[16:19]\n\t"
        "ds_read_b64_tr_b16 a[128:129], v104 offset:16384\n\t"
        "ds_read_b64_tr_b16 a[130:131], v104 offset:16640\n\t"
        "v_mfma_f32_16x16x32_bf16 v[20:23], a[96:99], v[60:63], v[20:23]\n\t"
        "s_waitcnt lgkmcnt(5)\n\t"
        "ds_read_b64_tr_b16 a[132:133], v103 offset:20480\n\t"
        "v_mfma_f32_16x16x32_bf16 v[24:27], a[100:103], v[60:63], v[24:27]\n\t"
        "ds_read_b64_tr_b16 a[134:135], v103 offset:20992\n\t"
        "ds_read_b64_tr_b16 a[136:137], v103 offset:20488\n\t"
        "v_mfma_f32_16x16x32_bf16 v[28:31], v[60:63], a[104:107], v[28:31]\n\t"
        "ds_read_b64_tr_b16 a[138:139], v103 offset:21000\n\t"
        "ds_read_b64_tr_b16 a[140:141], v104 offset:18432\n\t"
        "v_mfma_f32_16x16x32_bf16 v[8:11], a[108:111], v[64:67], v[8:11]\n\t"
        "ds_read_b64_tr_b16 a[142:143], v104 offset:18688\n\t"
        "ds_read_b128 v[72:75], v105 offset:40960\n\t"
        "v_mfma_f32_16x16x32_bf16 v[12:15], a[112:115], v[64:67], v[12:15]\n\t"
        "ds_read_b128 v[76:79], v105 offset:43008\n\t"
        "ds_read_b128 v[80:83], v106 offset:40960\n\t"
        "v_mfma_f32_16x16x32_bf16 v[16:19], v[64:67], a[116:119], v[16:19]\n\t"
        "ds_read_b128 v[84:87], v106 offset:43008\n\t"
        "v_mfma_f32_16x16x32_bf16 v[20:23], a[108:111], v[68:71], v[20:23]\n\t"
        "v_mfma_f32_16x16x32_bf16 v[24:27], a[112:115], v[68:71], v[24:27]\n\t"
        "v_mfma_f32_16x16x32_bf16 v[28:31], v[68:71], a[116:119], v[28:31]\n\t"
        "s_mov_b32 %[t0], 4718592\n\t"
        "buffer_load_dwordx4 v[172:175], v101, %[sq], %[t0] offen\n\t"
        "buffer_load_dwordx4 v[180:183], v112, %[sv], %[t0] offen\n\t"
        "s_mov_b32 %[t0], 4980736\n\t"
        "buffer_load_dwordx4 v[176:179], v101, %[sq], %[t0] offen\n\t"
        "s_waitcnt vmcnt(17)\n\t"
        "ds_write_b128 v102, v[136:139] offset:0\n\t"
        "ds_write_b128 v102, v[140:143] offset:4096\n\t"
        "ds_write_b128 v116, v[144:147] offset:8192\n\t"
        "s_waitcnt lgkmcnt(0)\n\t"
        "s_barrier\n\t"
        "v_mfma_f32_16x16x32_bf16 v[8:11], a[120:123], v[72:75], v[8:11]\n\t"
        "ds_read_b64_tr_b16 a[144:145], v103 offset:0\n\t"
        "ds_read_b64_tr_b16 a[146:147], v103 offset:512\n\t"
        "v_mfma_f32_16x16x32_bf16 v[12:15], a[124:127], v[72:75], v[12:15]\n\t"
        "ds_read_b64_tr_b16 a[148:149], v103 offset:8\n\t"
        "ds_read_b64_tr_b16 a[150:151], v103 offset:520\n\t"
        "v_mfma_f32_16x16x32_bf16 v[16:19], v[72:75], a[128:131], v[16:19]\n\t"
        "ds_read_b64_tr_b16 a[152:153], v104 offset:0\n\t"
        "ds_read_b64_tr_b16 a[154:155], v104 offset:256\n\t"
        "v_mfma_f32_16x16x32_bf16 v[20:23], a[120:123], v[76:79], v[20:23]\n\t"
        "s_waitcnt lgkmcnt(5)\n\t"
        "ds_read_b64_tr_b16 a[156:157], v103 offset:4096\n\t"
        "v_mfma_f32_16x16x32_bf16 v[24:27], a[124:127], v[76:79], v[24:27]\n\t"
        "ds_read_b64_tr_b16 a[158:159], v103 offset:4608\n\t"
        "ds_read_b64_tr_b16 a[160:161], v103 offset:4104\n\t"
        "v_mfma_f32_16x16x32_bf16 v[28:31], v[76:79], a[128:131], v[28:31]\n\t"
        "ds_read_b64_tr_b16 a[162:163], v103 offset:4616\n\t"
        "ds_read_b64_tr_b16 a[164:165], v104 offset:2048\n\t"
        "v_mfma_f32_16x16x32_bf16 v[8:11], a[132:135], v[80:83], v[8:11]\n\t"
        "ds_read_b64_tr_b16 a[166:167], v104 offset:2304\n\t"
        "ds_read_b128 v[56:59], v105 offset:49152\n\t"
        "v_mfma_f32_16x16x32_bf16 v[12:15], a[136:139], v[80:83], v[12:15]\n\t"
        "ds_read_b128 v[60:63], v105 offset:51200\n\t"
        "ds_read_b128 v[64:67], v106 offset:49152\n\t"
        "v_mfma_f32_16x16x32_bf16 v[16:19], v[80:83], a[140:143], v[16:19]\n\t"
        "ds_read_b128 v[68:71], v106 offset:51200\n\t"
        "v_mfma_f32_16x16x32_bf16 v[20:23], a[132:135], v[84:87], v[20:23]\n\t"
        "v_mfma_f32_16x16x32_bf16 v[24:27], a[136:139], v[84:87], v[24:27]\n\t"
        "v_mfma_f32_16x16x32_bf16 v[28:31], v[84:87], a[140:143], v[28:31]\n\t"
        "s_mov_b32 %[t0], 5242880\n\t"
        "buffer_load_dwordx4 v[184:187], v101, %[sq], %[t0] offen\n\t"
        "buffer_load_dwordx4 v[192:195], v112, %[sv], %[t0] offen\n\t"
        "s_mov_b32 %[t0], 5505024\n\t"
        "buffer_load_dwordx4 v[188:191], v101, %[sq], %[t0] offen\n\t"
        "s_waitcnt vmcnt(17)\n\t"
        "ds_write_b128 v102, v[148:151] offset:16384\n\t"
        "ds_write_b128 v102, v[152:155] offset:20480\n\t"
        "ds_write_b128 v116, v[156:159] offset:24576\n\t"
        "s_waitcnt lgkmcnt(0)\n\t"
        "s_barrier\n\t"
        "v_mfma_f32_16x16x32_bf16 v[8:11], a[144:147], v[56:59], v[8:11]\n\t"
        "ds_read_b64_tr_b16 a[168:169], v103 offset:16384\n\t"
        "ds_read_b64_tr_b16 a[170:171], v103 offset:16896\n\t"
        "v_mfma_f32_16x16x32_bf16 v[12:15], a[148:151], v[56:59], v[12:15]\n\t"
        "ds_read_b64_tr_b16 a[172:173], v103 offset:16392\n\t"
        "ds_read_b64_tr_b16 a[174:175], v103 offset:16904\n\t"
        "v_mfma_f32_16x16x32_bf16 v[16:19], v[56:59], a[152:155], v[16:19]\n\t"
        "ds_read_b64_tr_b16 a[176:177], v104 offset:16384\n\t"
        "ds_read_b64_tr_b16 a[178:179], v104 offset:16640\n\t"
        "v_mfma_f32_16x16x32_bf16 v[20:23], a[144:147], v[60:63], v[20:23]\n\t"
        "s_waitcnt lgkmcnt(5)\n\t"
        "ds_read_b64_tr_b16 a[180:181], v103 offset:20480\n\t"
        "v_mfma_f32_16x16x32_bf16 v[24:27], a[148:151], v[60:63], v[24:27]\n\t"
        "ds_read_b64_tr_b16 a[182:183], v103 offset:20992\n\t"
        "ds_read_b64_tr_b16 a[184:185], v103 offset:20488\n\t"
        "v_mfma_f32_16x16x32_bf16 v[28:31], v[60:63], a[152:155], v[28:31]\n\t"
        "ds_read_b64_tr_b16 a[186:187], v103 offset:21000\n\t"
        "ds_read_b64_tr_b16 a[188:189], v104 offset:18432\n\t"
        "v_mfma_f32_16x16x32_bf16 v[8:11], a[156:159], v[64:67], v[8:11]\n\t"
        "ds_read_b64_tr_b16 a[190:191], v104 offset:18688\n\t"
        "ds_read_b128 v[72:75], v105 offset:57344\n\t"
        "v_mfma_f32_16x16x32_bf16 v[12:15], a[160:163], v[64:67], v[12:15]\n\t"
        "ds_read_b128 v[76:79], v105 offset:59392\n\t"
        "ds_read_b128 v[80:83], v106 offset:57344\n\t"
        "v_mfma_f32_16x16x32_bf16 v[16:19], v[64:67], a[164:167], v[16:19]\n\t"
        "ds_read_b128 v[84:87], v106 offset:59392\n\t"
        "v_mfma_f32_16x16x32_bf16 v[20:23], a[156:159], v[68:71], v[20:23]\n\t"
        "v_mfma_f32_16x16x32_bf16 v[24:27], a[160:163], v[68:71], v[24:27]\n\t"
        "v_mfma_f32_16x16x32_bf16 v[28:31], v[68:71], a[164:167], v[28:31]\n\t"
        "s_mov_b32 %[t0], 5767168\n\t"
        "buffer_load_dwordx4 v[196:199], v101, %[sq], %[t0] offen\n\t"
        "buffer_load_dwordx4 v[204:207], v112, %[sv], %[t0] offen\n\t"
        "s_mov_b32 %[t0], 6029312\n\t"
        "buffer_load_dwordx4 v[200:203], v101, %[sq], %[t0] offen\n\t"
        "s_waitcnt vmcnt(9)\n\t"
        "ds_write_b128 v102, v[160:163] offset:0\n\t"
        "ds_write_b128 v102, v[164:167] offset:4096\n\t"
        "ds_write_b128 v116, v[168:171] offset:8192\n\t"
        "s_waitcnt lgkmcnt(0)\n\t"
        "s_barrier\n\t"
        "v_mfma_f32_16x16x32_bf16 v[8:11], a[168:171], v[72:75], v[8:11]\n\t"
        "ds_read_b64_tr_b16 a[192:193], v103 offset:0\n\t"
        "ds_read_b64_tr_b16 a[194:195], v103 offset:512\n\t"
        "v_mfma_f32_16x16x32_bf16 v[12:15], a[172:175], v[72:75], v[12:15]\n\t"
        "ds_read_b64_tr_b16 a[196:197], v103 offset:8\n\t"
        "ds_read_b64_tr_b16 a[198:199], v103 offset:520\n\t"
        "v_mfma_f32_16x16x32_bf16 v[16:19], v[72:75], a[176:179], v[16:19]\n\t"
        "ds_read_b64_tr_b16 a[200:201], v104 offset:0\n\t"
        "ds_read_b64_tr_b16 a[202:203], v104 offset:256\n\t"
        "v_mfma_f32_16x16x32_bf16 v[20:23], a[168:171], v[76:79], v[20:23]\n\t"
        "s_waitcnt lgkmcnt(5)\n\t"
        "ds_read_b64_tr_b16 a[204:205], v103 offset:4096\n\t"
        "v_mfma_f32_16x16x32_bf16 v[24:27], a[172:175], v[76:79], v[24:27]\n\t"
        "ds_read_b64_tr_b16 a[206:207], v103 offset:4608\n\t"
        "ds_read_b64_tr_b16 a[208:209], v103 offset:4104\n\t"
        "v_mfma_f32_16x16x32_bf16 v[28:31], v[76:79], a[176:179], v[28:31]\n\t"
        "ds_read_b64_tr_b16 a[210:211], v103 offset:4616\n\t"
        "ds_read_b64_tr_b16 a[212:213], v104 offset:2048\n\t"
        "v_mfma_f32_16x16x32_bf16 v[8:11], a[180:183], v[80:83], v[8:11]\n\t"
        "ds_read_b64_tr_b16 a[214:215], v104 offset:2304\n\t"
        "ds_read_b128 v[56:59], v107 offset:0\n\t"
        "v_mfma_f32_16x16x32_bf16 v[12:15], a[184:187], v[80:83], v[12:15]\n\t"
        "ds_read_b128 v[60:63], v107 offset:2048\n\t"
        "ds_read_b128 v[64:67], v108 offset:0\n\t"
        "v_mfma_f32_16x16x32_bf16 v[16:19], v[80:83], a[188:191], v[16:19]\n\t"
        "ds_read_b128 v[68:71], v108 offset:2048\n\t"
        "v_mfma_f32_16x16x32_bf16 v[20:23], a[180:183], v[84:87], v[20:23]\n\t"
        "v_mfma_f32_16x16x32_bf16 v[24:27], a[184:187], v[84:87], v[24:27]\n\t"
        "v_mfma_f32_16x16x32_bf16 v[28:31], v[84:87], a[188:191], v[28:31]\n\t"
        "s_add_u32 m0, %[ld], 98304\n\t"
        "s_add_u32 %[t0], %[xo], 1536\n\t"
        "buffer_load_dwordx4 v100, %[sx], %[t0] offen lds\n\t"
        "s_add_u32 m0, %[ld], 99328\n\t"
        "s_add_u32 %[t0], %[xo], 17920\n\t"
        "buffer_load_dwordx4 v100, %[sx], %[t0] offen lds\n\t"
        "s_add_u32 m0, %[ld], 106496\n\t"
        "s_add_u32 %[t0], %[xo], 1664\n\t"
        "buffer_load_dwordx4 v100, %[sx], %[t0] offen lds\n\t"
        "s_add_u32 m0, %[ld], 107520\n\t"
        "s_add_u32 %[t0], %[xo], 18048\n\t"
        "buffer_load_dwordx4 v100, %[sx], %[t0] offen lds\n\t"
        "s_add_u32 m0, %[ld], 114688\n\t"
        "s_add_u32 %[t0], %[xo], 1792\n\t"
        "buffer_load_dwordx4 v100, %[sx], %[t0] offen lds\n\t"
        "s_add_u32 m0, %[ld], 115712\n\t"
        "s_add_u32 %[t0], %[xo], 18176\n\t"
        "buffer_load_dwordx4 v100, %[sx], %[t0] offen lds\n\t"
        "s_add_u32 m0, %[ld], 122880\n\t"
        "s_add_u32 %[t0], %[xo], 1920\n\t"
        "buffer_load_dwordx4 v100, %[sx], %[t0] offen lds\n\t"
        "s_add_u32 m0, %[ld], 123904\n\t"
        "s_add_u32 %[t0], %[xo], 18304\n\t"
        "buffer_load_dwordx4 v100, %[sx], %[t0] offen lds\n\t"
        "s_mov_b32 %[t0], 6291456\n\t"
        "buffer_load_dwordx4 v[208:211], v101, %[sq], %[t0] offen\n\t"
        "buffer_load_dwordx4 v[216:219], v112, %[sv], %[t0] offen\n\t"
        "s_mov_b32 %[t0], 6553600\n\t"
        "buffer_load_dwordx4 v[212:215], v101, %[sq], %[t0] offen\n\t"
        "s_waitcnt vmcnt(17)\n\t"
        "ds_write_b128 v102, v[172:175] offset:16384\n\t"
        "ds_write_b128 v102, v[176:179] offset:20480\n\t"
        "ds_write_b128 v116, v[180:183] offset:24576\n\t"
        "s_waitcnt lgkmcnt(0)\n\t"
        "s_barrier\n\t"
        "v_mfma_f32_16x16x32_bf16 v[8:11], a[192:195], v[56:59], v[8:11]\n\t"
        "ds_read_b64_tr_b16 a[216:217], v103 offset:16384\n\t"
        "ds_read_b64_tr_b16 a[218:219], v103 offset:16896\n\t"
        "v_mfma_f32_16x16x32_bf16 v[12:15], a[196:199], v[56:59], v[12:15]\n\t"
        "ds_read_b64_tr_b16 a[220:221], v103 offset:16392\n\t"
        "ds_read_b64_tr_b16 a[222:223], v103 offset:16904\n\t"
        "v_mfma_f32_16x16x32_bf16 v[16:19], v[56:59], a[200:203], v[16:19]\n\t"
        "ds_read_b64_tr_b16 a[224:225], v104 offset:16384\n\t"
        "ds_read_b64_tr_b16 a[226:227], v104 offset:16640\n\t"
        "v_mfma_f32_16x16x32_bf16 v[20:23], a[192:195], v[60:63], v[20:23]\n\t"
        "s_waitcnt lgkmcnt(5)\n\t"
        "ds_read_b64_tr_b16 a[228:229], v103 offset:20480\n\t"
        "v_mfma_f32_16x16x32_bf16 v[24:27], a[196:199], v[60:63], v[24:27]\n\t"
        "ds_read_b64_tr_b16 a[230:231], v103 offset:20992\n\t"
        "ds_read_b64_tr_b16 a[232:233], v103 offset:20488\n\t"
        "v_mfma_f32_16x16x32_bf16 v[28:31], v[60:63], a[200:203], v[28:31]\n\t"
        "ds_read_b64_tr_b16 a[234:235], v103 offset:21000\n\t"
        "ds_read_b64_tr_b16 a[236:237], v104 offset:18432\n\t"
        "v_mfma_f32_16x16x32_bf16 v[8:11], a[204:207], v[64:67], v[8:11]\n\t"
        "ds_read_b64_tr_b16 a[238:239], v104 offset:18688\n\t"
        "ds_read_b128 v[72:75], v107 offset:8192\n\t"
        "v_mfma_f32_16x16x32_bf16 v[12:15], a[208:211], v[64:67], v[12:15]\n\t"
        "ds_read_b128 v[76:79], v107 offset:10240\n\t"
        "ds_read_b128 v[80:83], v108 offset:8192\n\t"
        "v_mfma_f32_16x16x32_bf16 v[16:19], v[64:67], a[212:215], v[16:19]\n\t"
        "ds_read_b128 v[84:87], v108 offset:10240\n\t"
        "v_mfma_f32_16x16x32_bf16 v[20:23], a[204:207], v[68:71], v[20:23]\n\t"
        "v_mfma_f32_16x16x32_bf16 v[24:27], a[208:211], v[68:71], v[24:27]\n\t"
        "v_mfma_f32_16x16x32_bf16 v[28:31], v[68:71], a[212:215], v[28:31]\n\t"
        "s_mov_b32 %[t0], 6815744\n\t"
        "buffer_load_dwordx4 v[220:223], v101, %[sq], %[t0] offen\n\t"
        "buffer_load_dwordx4 v[228:231], v112, %[sv], %[t0] offen\n\t"
        "s_mov_b32 %[t0], 7077888\n\t"
        "buffer_load_dwordx4 v[224:227], v101, %[sq], %[t0] offen\n\t"
        "s_waitcnt vmcnt(17)\n\t"
        "ds_write_b128 v102, v[184:187] offset:0\n\t"
        "ds_write_b128 v102, v[188:191] offset:4096\n\t"
        "ds_write_b128 v116, v[192:195] offset:8192\n\t"
        "s_waitcnt lgkmcnt(0)\n\t"
        "s_barrier\n\t"
        "v_mfma_f32_16x16x32_bf16 v[8:11], a[216:219], v[72:75], v[8:11]\n\t"
        "ds_read_b64_tr_b16 a[240:241], v103 offset:0\n\t"
        "ds_read_b64_tr_b16 a[242:243], v103 offset:512\n\t"
        "v_mfma_f32_16x16x32_bf16 v[12:15], a[220:223], v[72:75], v[12:15]\n\t"
        "ds_read_b64_tr_b16 a[244:245], v103 offset:8\n\t"
        "ds_read_b64_tr_b16 a[246:247], v103 offset:520\n\t"
        "v_mfma_f32_16x16x32_bf16 v[16:19], v[72:75], a[224:227], v[16:19]\n\t"
        "ds_read_b64_tr_b16 a[248:249], v104 offset:0\n\t"
        "ds_read_b64_tr_b16 a[250:251], v104 offset:256\n\t"
        "v_mfma_f32_16x16x32_bf16 v[20:23], a[216:219], v[76:79], v[20:23]\n\t"
        "s_waitcnt lgkmcnt(5)\n\t"
        "ds_read_b64_tr_b16 a[252:253], v103 offset:4096\n\t"
        "v_mfma_f32_16x16x32_bf16 v[24:27], a[220:223], v[76:79], v[24:27]\n\t"
        "ds_read_b64_tr_b16 a[254:255], v103 offset:4608\n\t"
        "ds_read_b64_tr_b16 v[128:129], v103 offset:4104\n\t"
        "v_mfma_f32_16x16x32_bf16 v[28:31], v[76:79], a[224:227], v[28:31]\n\t"
        "ds_read_b64_tr_b16 v[130:131], v103 offset:4616\n\t"
        "ds_read_b64_tr_b16 v[132:133], v104 offset:2048\n\t"
        "v_mfma_f32_16x16x32_bf16 v[8:11], a[228:231], v[80:83], v[8:11]\n\t"
        "ds_read_b64_tr_b16 v[134:135], v104 offset:2304\n\t"
        "ds_read_b128 v[56:59], v107 offset:16384\n\t"
        "v_mfma_f32_16x16x32_bf16 v[12:15], a[232:235], v[80:83], v[12:15]\n\t"
        "ds_read_b128 v[60:63], v107 offset:18432\n\t"
        "ds_read_b128 v[64:67], v108 offset:16384\n\t"
        "v_mfma_f32_16x16x32_bf16 v[16:19], v[80:83], a[236:239], v[16:19]\n\t"
        "ds_read_b128 v[68:71], v108 offset:18432\n\t"
        "v_mfma_f32_16x16x32_bf16 v[20:23], a[228:231], v[84:87], v[20:23]\n\t"
        "v_mfma_f32_16x16x32_bf16 v[24:27], a[232:235], v[84:87], v[24:27]\n\t"
        "v_mfma_f32_16x16x32_bf16 v[28:31], v[84:87], a[236:239], v[28:31]\n\t"
        "s_mov_b32 %[t0], 7340032\n\t"
        "buffer_load_dwordx4 v[232:235], v101, %[sq], %[t0] offen\n\t"
        "buffer_load_dwordx4 v[240:243], v112, %[sv], %[t0] offen\n\t"
        "s_mov_b32 %[t0], 7602176\n\t"
        "buffer_load_dwordx4 v[236:239], v101, %[sq], %[t0] offen\n\t"
        "s_waitcnt vmcnt(17)\n\t"
        "ds_write_b128 v102, v[196:199] offset:16384\n\t"
        "ds_write_b128 v102, v[200:203] offset:20480\n\t"
        "ds_write_b128 v116, v[204:207] offset:24576\n\t"
        "s_waitcnt lgkmcnt(0)\n\t"
        "s_barrier\n\t"
        "v_mfma_f32_16x16x32_bf16 v[8:11], a[240:243], v[56:59], v[8:11]\n\t"
        "ds_read_b64_tr_b16 v[136:137], v103 offset:16384\n\t"
        "ds_read_b64_tr_b16 v[138:139], v103 offset:16896\n\t"
        "v_mfma_f32_16x16x32_bf16 v[12:15], a[244:247], v[56:59], v[12:15]\n\t"
        "ds_read_b64_tr_b16 v[140:141], v103 offset:16392\n\t"
        "ds_read_b64_tr_b16 v[142:143], v103 offset:16904\n\t"
        "v_mfma_f32_16x16x32_bf16 v[16:19], v[56:59], a[248:251], v[16:19]\n\t"
        "ds_read_b64_tr_b16 v[144:145], v104 offset:16384\n\t"
        "ds_read_b64_tr_b16 v[146:147], v104 offset:16640\n\t"
        "v_mfma_f32_16x16x32_bf16 v[20:23], a[240:243], v[60:63], v[20:23]\n\t"
        "s_waitcnt lgkmcnt(5)\n\t"
        "ds_read_b64_tr_b16 v[148:149], v103 offset:20480\n\t"
        "v_mfma_f32_16x16x32_bf16 v[24:27], a[244:247], v[60:63], v[24:27]\n\t"
        "ds_read_b64_tr_b16 v[150:151], v103 offset:20992\n\t"
        "ds_read_b64_tr_b16 v[152:153], v103 offset:20488\n\t"
        "v_mfma_f32_16x16x32_bf16 v[28:31], v[60:63], a[248:251], v[28:31]\n\t"
        "ds_read_b64_tr_b16 v[154:155], v103 offset:21000\n\t"
        "ds_read_b64_tr_b16 v[156:157], v104 offset:18432\n\t"
        "v_mfma_f32_16x16x32_bf16 v[8:11], a[252:255], v[64:67], v[8:11]\n\t"
        "ds_read_b64_tr_b16 v[158:159], v104 offset:18688\n\t"
        "ds_read_b128 v[72:75], v107 offset:24576\n\t"
        "v_mfma_f32_16x16x32_bf16 v[12:15], v[128:131], v[64:67], v[12:15]\n\t"
        "ds_read_b128 v[76:79], v107 offset:26624\n\t"
        "ds_read_b128 v[80:83], v108 offset:24576\n\t"
        "v_mfma_f32_16x16x32_bf16 v[16:19], v[64:67], v[132:135], v[16:19]\n\t"
        "ds_read_b128 v[84:87], v108 offset:26624\n\t"
        "v_mfma_f32_16x16x32_bf16 v[20:23], a[252:255], v[68:71], v[20:23]\n\t"
        "v_mfma_f32_16x16x32_bf16 v[24:27], v[128:131], v[68:71], v[24:27]\n\t"
        "v_mfma_f32_16x16x32_bf16 v[28:31], v[68:71], v[132:135], v[28:31]\n\t"
        "s_mov_b32 %[t0], 7864320\n\t"
        "buffer_load_dwordx4 v[244:247], v101, %[sq], %[t0] offen\n\t"
        "buffer_load_dwordx4 v[252:255], v112, %[sv], %[t0] offen\n\t"
        "s_mov_b32 %[t0], 8126464\n\t"
        "buffer_load_dwordx4 v[248:251], v101, %[sq], %[t0] offen\n\t"
        "s_waitcnt vmcnt(9)\n\t"
        "ds_write_b128 v102, v[208:211] offset:0\n\t"
        "ds_write_b128 v102, v[212:215] offset:4096\n\t"
        "ds_write_b128 v116, v[216:219] offset:8192\n\t"
        "s_waitcnt lgkmcnt(0)\n\t"
        "s_barrier\n\t"
        "v_mfma_f32_16x16x32_bf16 v[8:11], v[136:139], v[72:75], v[8:11]\n\t"
        "ds_read_b64_tr_b16 v[160:161], v103 offset:0\n\t"
        "ds_read_b64_tr_b16 v[162:163], v103 offset:512\n\t"
        "v_mfma_f32_16x16x32_bf16 v[12:15], v[140:143], v[72:75], v[12:15]\n\t"
        "ds_read_b64_tr_b16 v[164:165], v103 offset:8\n\t"
        "ds_read_b64_tr_b16 v[166:167], v103 offset:520\n\t"
        "v_mfma_f32_16x16x32_bf16 v[16:19], v[72:75], v[144:147], v[16:19]\n\t"
        "ds_read_b64_tr_b16 v[168:169], v104 offset:0\n\t"
        "ds_read_b64_tr_b16 v[170:171], v104 offset:256\n\t"
        "v_mfma_f32_16x16x32_bf16 v[20:23], v[136:139], v[76:79], v[20:23]\n\t"
        "s_waitcnt lgkmcnt(5)\n\t"
        "ds_read_b64_tr_b16 v[172:173], v103 offset:4096\n\t"
        "v_mfma_f32_16x16x32_bf16 v[24:27], v[140:143], v[76:79], v[24:27]\n\t"
        "ds_read_b64_tr_b16 v[174:175], v103 offset:4608\n\t"
        "ds_read_b64_tr_b16 v[176:177], v103 offset:4104\n\t"
        "v_mfma_f32_16x16x32_bf16 v[28:31], v[76:79], v[144:147], v[28:31]\n\t"
        "ds_read_b64_tr_b16 v[178:179], v103 offset:4616\n\t"
        "ds_read_b64_tr_b16 v[180:181], v104 offset:2048\n\t"
        "v_mfma_f32_16x16x32_bf16 v[8:11], v[148:151], v[80:83], v[8:11]\n\t"
        "ds_read_b64_tr_b16 v[182:183], v104 offset:2304\n\t"
        "ds_read_b128 v[56:59], v107 offset:32768\n\t"
        "v_mfma_f32_16x16x32_bf16 v[12:15], v[152:155], v[80:83], v[12:15]\n\t"
        "ds_read_b128 v[60:63], v107 offset:34816\n\t"
        "ds_read_b128 v[64:67], v108 offset:32768\n\t"
        "v_mfma_f32_16x16x32_bf16 v[16:19], v[80:83], v[156:159], v[16:19]\n\t"
        "ds_read_b128 v[68:71], v108 offset:34816\n\t"
        "v_mfma_f32_16x16x32_bf16 v[20:23], v[148:151], v[84:87], v[20:23]\n\t"
        "v_mfma_f32_16x16x32_bf16 v[24:27], v[152:155], v[84:87], v[24:27]\n\t"
        "v_mfma_f32_16x16x32_bf16 v[28:31], v[84:87], v[156:159], v[28:31]\n\t"
        "s_mov_b32 %[xo], 0x20000\n\t"
        "s_add_u32 m0, %[ld], 0\n\t"
        "s_add_u32 %[t0], %[xo], 0\n\t"
        "buffer_load_dwordx4 v100, %[sx], %[t0] offen lds\n\t"
        "s_add_u32 m0, %[ld], 1024\n\t"
        "s_add_u32 %[t0], %[xo], 16384\n\t"
        "buffer_load_dwordx4 v100, %[sx], %[t0] offen lds\n\t"
        "s_add_u32 m0, %[ld], 8192\n\t"
        "s_add_u32 %[t0], %[xo], 128\n\t"
        "buffer_load_dwordx4 v100, %[sx], %[t0] offen lds\n\t"
        "s_add_u32 m0, %[ld], 9216\n\t"
        "s_add_u32 %[t0], %[xo], 16512\n\t"
        "buffer_load_dwordx4 v100, %[sx], %[t0] offen lds\n\t"
        "s_add_u32 m0, %[ld], 16384\n\t"
        "s_add_u32 %[t0], %[xo], 256\n\t"
        "buffer_load_dwordx4 v100, %[sx], %[t0] offen lds\n\t"
        "s_add_u32 m0, %[ld], 17408\n\t"
        "s_add_u32 %[t0], %[xo], 16640\n\t"
        "buffer_load_dwordx4 v100, %[sx], %[t0] offen lds\n\t"
        "s_add_u32 m0, %[ld], 24576\n\t"
        "s_add_u32 %[t0], %[xo], 384\n\t"
        "buffer_load_dwordx4 v100, %[sx], %[t0] offen lds\n\t"
        "s_add_u32 m0, %[ld], 25600\n\t"
        "s_add_u32 %[t0], %[xo], 16768\n\t"
        "buffer_load_dwordx4 v100, %[sx], %[t0] offen lds\n\t"
        "s_mov_b32 %[xo], 0\n\t"
        "s_waitcnt vmcnt(14)\n\t"
        "ds_write_b128 v102, v[220:223] offset:16384\n\t"
        "ds_write_b128 v102, v[224:227] offset:20480\n\t"
        "ds_write_b128 v116, v[228:231] offset:24576\n\t"
        "s_waitcnt lgkmcnt(0)\n\t"
        "s_barrier\n\t"
        "v_mfma_f32_16x16x32_bf16 v[8:11], v[160:163], v[56:59], v[8:11]\n\t"
        "ds_read_b64_tr_b16 v[184:185], v103 offset:16384\n\t"
        "ds_read_b64_tr_b16 v[186:187], v103 offset:16896\n\t"
        "v_mfma_f32_16x16x32_bf16 v[12:15], v[164:167], v[56:59], v[12:15]\n\t"
        "ds_read_b64_tr_b16 v[188:189], v103 offset:16392\n\t"
        "ds_read_b64_tr_b16 v[190:191], v103 offset:16904\n\t"
        "v_mfma_f32_16x16x32_bf16 v[16:19], v[56:59], v[168:171], v[16:19]\n\t"
        "ds_read_b64_tr_b16 v[192:193], v104 offset:16384\n\t"
        "ds_read_b64_tr_b16 v[194:195], v104 offset:16640\n\t"
        "v_mfma_f32_16x16x32_bf16 v[20:23], v[160:163], v[60:63], v[20:23]\n\t"
        "s_waitcnt lgkmcnt(5)\n\t"
        "ds_read_b64_tr_b16 v[196:197], v103 offset:20480\n\t"
        "v_mfma_f32_16x16x32_bf16 v[24:27], v[164:167], v[60:63], v[24:27]\n\t"
        "ds_read_b64_tr_b16 v[198:199], v103 offset:20992\n\t"
        "ds_read_b64_tr_b16 v[200:201], v103 offset:20488\n\t"
        "v_mfma_f32_16x16x32_bf16 v[28:31], v[60:63], v[168:171], v[28:31]\n\t"
        "ds_read_b64_tr_b16 v[202:203], v103 offset:21000\n\t"
        "ds_read_b64_tr_b16 v[204:205], v104 offset:18432\n\t"
        "v_mfma_f32_16x16x32_bf16 v[8:11], v[172:175], v[64:67], v[8:11]\n\t"
        "ds_read_b64_tr_b16 v[206:207], v104 offset:18688\n\t"
        "ds_read_b128 v[72:75], v107 offset:40960\n\t"
        "v_mfma_f32_16x16x32_bf16 v[12:15], v[176:179], v[64:67], v[12:15]\n\t"
        "ds_read_b128 v[76:79], v107 offset:43008\n\t"
        "ds_read_b128 v[80:83], v108 offset:40960\n\t"
        "v_mfma_f32_16x16x32_bf16 v[16:19], v[64:67], v[180:183], v[16:19]\n\t"
        "ds_read_b128 v[84:87], v108 offset:43008\n\t"
        "v_mfma_f32_16x16x32_bf16 v[20:23], v[172:175], v[68:71], v[20:23]\n\t"
        "v_mfma_f32_16x16x32_bf16 v[24:27], v[176:179], v[68:71], v[24:27]\n\t"
        "v_mfma_f32_16x16x32_bf16 v[28:31], v[68:71], v[180:183], v[28:31]\n\t"
        "s_waitcnt vmcnt(11)\n\t"
        "ds_write_b128 v102, v[232:235] offset:0\n\t"
        "ds_write_b128 v102, v[236:239] offset:4096\n\t"
        "ds_write_b128 v116, v[240:243] offset:8192\n\t"
        "s_waitcnt lgkmcnt(0)\n\t"
        "s_barrier\n\t"
        "v_mfma_f32_16x16x32_bf16 v[8:11], v[184:187], v[72:75], v[8:11]\n\t"
        "ds_read_b64_tr_b16 v[208:209], v103 offset:0\n\t"
        "ds_read_b64_tr_b16 v[210:211], v103 offset:512\n\t"
        "v_mfma_f32_16x16x32_bf16 v[12:15], v[188:191], v[72:75], v[12:15]\n\t"
        "ds_read_b64_tr_b16 v[212:213], v103 offset:8\n\t"
        "ds_read_b64_tr_b16 v[214:215], v103 offset:520\n\t"
        "v_mfma_f32_16x16x32_bf16 v[16:19], v[72:75], v[192:195], v[16:19]\n\t"
        "ds_read_b64_tr_b16 v[216:217], v104 offset:0\n\t"
        "ds_read_b64_tr_b16 v[218:219], v104 offset:256\n\t"
        "v_mfma_f32_16x16x32_bf16 v[20:23], v[184:187], v[76:79], v[20:23]\n\t"
        "s_waitcnt lgkmcnt(5)\n\t"
        "ds_read_b64_tr_b16 v[220:221], v103 offset:4096\n\t"
        "v_mfma_f32_16x16x32_bf16 v[24:27], v[188:191], v[76:79], v[24:27]\n\t"
        "ds_read_b64_tr_b16 v[222:223], v103 offset:4608\n\t"
        "ds_read_b64_tr_b16 v[224:225], v103 offset:4104\n\t"
        "v_mfma_f32_16x16x32_bf16 v[28:31], v[76:79], v[192:195], v[28:31]\n\t"
        "ds_read_b64_tr_b16 v[226:227], v103 offset:4616\n\t"
        "ds_read_b64_tr_b16 v[228:229], v104 offset:2048\n\t"
        "v_mfma_f32_16x16x32_bf16 v[8:11], v[196:199], v[80:83], v[8:11]\n\t"
        "ds_read_b64_tr_b16 v[230:231], v104 offset:2304\n\t"
        "ds_read_b128 v[56:59], v107 offset:49152\n\t"
        "v_mfma_f32_16x16x32_bf16 v[12:15], v[200:203], v[80:83], v[12:15]\n\t"
        "ds_read_b128 v[60:63], v107 offset:51200\n\t"
        "ds_read_b128 v[64:67], v108 offset:49152\n\t"
        "v_mfma_f32_16x16x32_bf16 v[16:19], v[80:83], v[204:207], v[16:19]\n\t"
        "ds_read_b128 v[68:71], v108 offset:51200\n\t"
        "v_mfma_f32_16x16x32_bf16 v[20:23], v[196:199], v[84:87], v[20:23]\n\t"
        "v_mfma_f32_16x16x32_bf16 v[24:27], v[200:203], v[84:87], v[24:27]\n\t"
        "v_mfma_f32_16x16x32_bf16 v[28:31], v[84:87], v[204:207], v[28:31]\n\t"
        "s_waitcnt vmcnt(8)\n\t"
        "ds_write_b128 v102, v[244:247] offset:16384\n\t"
        "ds_write_b128 v102, v[248:251] offset:20480\n\t"
        "ds_write_b128 v116, v[252:255] offset:24576\n\t"
        "s_waitcnt lgkmcnt(0)\n\t"
        "s_barrier\n\t"
        "v_mfma_f32_16x16x32_bf16 v[8:11], v[208:211], v[56:59], v[8:11]\n\t"
        "ds_read_b64_tr_b16 v[232:233], v103 offset:16384\n\t"
        "ds_read_b64_tr_b16 v[234:235], v103 offset:16896\n\t"
        "v_mfma_f32_16x16x32_bf16 v[12:15], v[212:215], v[56:59], v[12:15]\n\t"
        "ds_read_b64_tr_b16 v[236:237], v103 offset:16392\n\t"
        "ds_read_b64_tr_b16 v[238:239], v103 offset:16904\n\t"
        "v_mfma_f32_16x16x32_bf16 v[16:19], v[56:59], v[216:219], v[16:19]\n\t"
        "ds_read_b64_tr_b16 v[240:241], v104 offset:16384\n\t"
        "ds_read_b64_tr_b16 v[242:243], v104 offset:16640\n\t"
        "v_mfma_f32_16x16x32_bf16 v[20:23], v[208:211], v[60:63], v[20:23]\n\t"
        "s_waitcnt lgkmcnt(5)\n\t"
        "ds_read_b64_tr_b16 v[244:245], v103 offset:20480\n\t"
        "v_mfma_f32_16x16x32_bf16 v[24:27], v[212:215], v[60:63], v[24:27]\n\t"
        "ds_read_b64_tr_b16 v[246:247], v103 offset:20992\n\t"
        "ds_read_b64_tr_b16 v[248:249], v103 offset:20488\n\t"
        "v_mfma_f32_16x16x32_bf16 v[28:31], v[60:63], v[216:219], v[28:31]\n\t"
        "ds_read_b64_tr_b16 v[250:251], v103 offset:21000\n\t"
        "ds_read_b64_tr_b16 v[252:253], v104 offset:18432\n\t"
        "v_mfma_f32_16x16x32_bf16 v[8:11], v[220:223], v[64:67], v[8:11]\n\t"
        "ds_read_b64_tr_b16 v[254:255], v104 offset:18688\n\t"
        "ds_read_b128 v[72:75], v107 offset:57344\n\t"
        "v_mfma_f32_16x16x32_bf16 v[12:15], v[224:227], v[64:67], v[12:15]\n\t"
        "ds_read_b128 v[76:79], v107 offset:59392\n\t"
        "ds_read_b128 v[80:83], v108 offset:57344\n\t"
        "v_mfma_f32_16x16x32_bf16 v[16:19], v[64:67], v[228:231], v[16:19]\n\t"
        "ds_read_b128 v[84:87], v108 offset:59392\n\t"
        "v_mfma_f32_16x16x32_bf16 v[20:23], v[220:223], v[68:71], v[20:23]\n\t"
        "v_mfma_f32_16x16x32_bf16 v[24:27], v[224:227], v[68:71], v[24:27]\n\t"
        "v_mfma_f32_16x16x32_bf16 v[28:31], v[68:71], v[228:231], v[28:31]\n\t"
        "s_waitcnt vmcnt(0) lgkmcnt(0)\n\t"
        "s_barrier\n\t"
        "v_mfma_f32_16x16x32_bf16 v[8:11], v[232:235], v[72:75], v[8:11]\n\t"
        "v_mfma_f32_16x16x32_bf16 v[12:15], v[236:239], v[72:75], v[12:15]\n\t"
        "v_mfma_f32_16x16x32_bf16 v[16:19], v[72:75], v[240:243], v[16:19]\n\t"
        "v_mfma_f32_16x16x32_bf16 v[20:23], v[232:235], v[76:79], v[20:23]\n\t"
        "v_mfma_f32_16x16x32_bf16 v[24:27], v[236:239], v[76:79], v[24:27]\n\t"
        "v_mfma_f32_16x16x32_bf16 v[28:31], v[76:79], v[240:243], v[28:31]\n\t"
        "v_mfma_f32_16x16x32_bf16 v[8:11], v[244:247], v[80:83], v[8:11]\n\t"
        "v_mfma_f32_16x16x32_bf16 v[12:15], v[248:251], v[80:83], v[12:15]\n\t"
        "v_mfma_f32_16x16x32_bf16 v[16:19], v[80:83], v[252:255], v[16:19]\n\t"
        "v_mfma_f32_16x16x32_bf16 v[20:23], v[244:247], v[84:87], v[20:23]\n\t"
        "v_mfma_f32_16x16x32_bf16 v[24:27], v[248:251], v[84:87], v[24:27]\n\t"
        "v_mfma_f32_16x16x32_bf16 v[28:31], v[84:87], v[252:255], v[28:31]\n\t"
        "s_mov_b32 %[xo], 0x20000\n\t"
        "ds_read_b128 v[56:59], v105 offset:0\n\t"
        "ds_read_b128 v[60:63], v105 offset:2048\n\t"
        "ds_read_b128 v[64:67], v106 offset:0\n\t"
        "ds_read_b128 v[68:71], v106 offset:2048\n\t"
        "ds_read_b128 v[72:75], v105 offset:8192\n\t"
        "ds_read_b128 v[76:79], v105 offset:10240\n\t"
        "ds_read_b128 v[80:83], v106 offset:8192\n\t"
        "v_cvt_pk_bf16_f32 v88, v8, v9\n\t"
        "v_cvt_pk_bf16_f32 v89, v10, v11\n\t"
        "v_cvt_pk_bf16_f32 v90, v12, v13\n\t"
        "v_cvt_pk_bf16_f32 v91, v14, v15\n\t"
        "buffer_store_dwordx4 v[88:91], v109, %[sw], %[so] offen sc1\n\t"
        "v_cvt_pk_bf16_f32 v92, v20, v21\n\t"
        "v_cvt_pk_bf16_f32 v93, v22, v23\n\t"
        "v_cvt_pk_bf16_f32 v94, v24, v25\n\t"
        "v_cvt_pk_bf16_f32 v95, v26, v27\n\t"
        "buffer_store_dwordx4 v[92:95], v110, %[sw], %[so] offen sc1\n\t"
        "v_cvt_pk_bf16_f32 v96, v16, v17\n\t"
        "v_cvt_pk_bf16_f32 v97, v18, v19\n\t"
        "v_cvt_pk_bf16_f32 v98, v28, v29\n\t"
        "v_cvt_pk_bf16_f32 v99, v30, v31\n\t"
        "buffer_store_dwordx4 v[96:99], v111, %[sw], %[so] offen sc1\n\t"
        "s_mov_b32 %[so], 0xf0000000\n\t"
        "s_mov_b32 %[xo], 33792\n\t"
        "s_mov_b32 %[cn], 0\n\t"
        "LG_cal%=:\n\t"
        "buffer_load_dword v118, off, %[sw], %[xo] sc1\n\t"
        "s_waitcnt vmcnt(0)\n\t"
        "v_readfirstlane_b32 %[t0], v118\n\t"
        "s_cmp_lg_u32 %[t0], 0\n\t"
        "s_cbranch_scc1 LG_calok%=\n\t"
        "s_sleep 4\n\t"
        "s_add_u32 %[cn], %[cn], 1\n\t"
        "s_cmp_lt_u32 %[cn], 0x100000\n\t"
        "s_cbranch_scc1 LG_cal%=\n\t"
        "s_endpgm\n\t"
        "LG_calok%=:\n\t"
        "s_mov_b32 %[xo], 0x20000\n\t"
        "s_mov_b32 %[cn], 1\n\t"
        "v_mov_b32_e32 v119, %[cn]\n\t"
        "buffer_store_dword v119, v113, %[sw], 0 offen sc1\n\t"
        "s_add_u32 m0, %[ld], 32768\n\t"
        "s_add_u32 %[t0], %[xo], 512\n\t"
        "buffer_load_dwordx4 v100, %[sx], %[t0] offen lds\n\t"
        "s_add_u32 m0, %[ld], 33792\n\t"
        "s_add_u32 %[t0], %[xo], 16896\n\t"
        "buffer_load_dwordx4 v100, %[sx], %[t0] offen lds\n\t"
        "s_add_u32 m0, %[ld], 40960\n\t"
        "s_add_u32 %[t0], %[xo], 640\n\t"
        "buffer_load_dwordx4 v100, %[sx], %[t0] offen lds\n\t"
        "s_add_u32 m0, %[ld], 41984\n\t"
        "s_add_u32 %[t0], %[xo], 17024\n\t"
        "buffer_load_dwordx4 v100, %[sx], %[t0] offen lds\n\t"
        "s_add_u32 m0, %[ld], 49152\n\t"
        "s_add_u32 %[t0], %[xo], 768\n\t"
        "buffer_load_dwordx4 v100, %[sx], %[t0] offen lds\n\t"
        "s_add_u32 m0, %[ld], 50176\n\t"
        "s_add_u32 %[t0], %[xo], 17152\n\t"
        "buffer_load_dwordx4 v100, %[sx], %[t0] offen lds\n\t"
        "s_add_u32 m0, %[ld], 57344\n\t"
        "s_add_u32 %[t0], %[xo], 896\n\t"
        "buffer_load_dwordx4 v100, %[sx], %[t0] offen lds\n\t"
        "s_add_u32 m0, %[ld], 58368\n\t"
        "s_add_u32 %[t0], %[xo], 17280\n\t"
        "buffer_load_dwordx4 v100, %[sx], %[t0] offen lds\n\t"
        "s_add_u32 m0, %[ld], 65536\n\t"
        "s_add_u32 %[t0], %[xo], 1024\n\t"
        "buffer_load_dwordx4 v100, %[sx], %[t0] offen lds\n\t"
        "s_add_u32 m0, %[ld], 66560\n\t"
        "s_add_u32 %[t0], %[xo], 17408\n\t"
        "buffer_load_dwordx4 v100, %[sx], %[t0] offen lds\n\t"
        "s_add_u32 m0, %[ld], 73728\n\t"
        "s_add_u32 %[t0], %[xo], 1152\n\t"
        "buffer_load_dwordx4 v100, %[sx], %[t0] offen lds\n\t"
        "s_add_u32 m0, %[ld], 74752\n\t"
        "s_add_u32 %[t0], %[xo], 17536\n\t"
        "buffer_load_dwordx4 v100, %[sx], %[t0] offen lds\n\t"
        "s_add_u32 m0, %[ld], 81920\n\t"
        "s_add_u32 %[t0], %[xo], 1280\n\t"
        "buffer_load_dwordx4 v100, %[sx], %[t0] offen lds\n\t"
        "s_add_u32 m0, %[ld], 82944\n\t"
        "s_add_u32 %[t0], %[xo], 17664\n\t"
        "buffer_load_dwordx4 v100, %[sx], %[t0] offen lds\n\t"
        "s_add_u32 m0, %[ld], 90112\n\t"
        "s_add_u32 %[t0], %[xo], 1408\n\t"
        "buffer_load_dwordx4 v100, %[sx], %[t0] offen lds\n\t"
        "s_add_u32 m0, %[ld], 91136\n\t"
        "s_add_u32 %[t0], %[xo], 17792\n\t"
        "buffer_load_dwordx4 v100, %[sx], %[t0] offen lds\n\t"
        "s_add_u32 m0, %[ld], 98304\n\t"
        "s_add_u32 %[t0], %[xo], 1536\n\t"
        "buffer_load_dwordx4 v100, %[sx], %[t0] offen lds\n\t"
        "s_add_u32 m0, %[ld], 99328\n\t"
        "s_add_u32 %[t0], %[xo], 17920\n\t"
        "buffer_load_dwordx4 v100, %[sx], %[t0] offen lds\n\t"
        "s_add_u32 m0, %[ld], 106496\n\t"
        "s_add_u32 %[t0], %[xo], 1664\n\t"
        "buffer_load_dwordx4 v100, %[sx], %[t0] offen lds\n\t"
        "s_branch LG_mid%=\n\t"
        "LG_loop%=:\n\t"
        "s_waitcnt lgkmcnt(6)\n\t"
        "v_mfma_f32_16x16x32_bf16 v[8:11], a[0:3], v[56:59], 0\n\t"
        "v_mfma_f32_16x16x32_bf16 v[12:15], a[4:7], v[56:59], 0\n\t"
        "ds_read_b128 v[84:87], v106 offset:10240\n\t"
        "v_mfma_f32_16x16x32_bf16 v[16:19], v[56:59], a[8:11], 0\n\t"
        "s_add_u32 m0, %[ld], 107520\n\t"
        "s_add_u32 %[t0], %[xo], 18048\n\t"
        "s_waitcnt lgkmcnt(6)\n\t"
        "v_mfma_f32_16x16x32_bf16 v[20:23], a[0:3], v[60:63], 0\n\t"
        "buffer_load_dwordx4 v100, %[sx], %[t0] offen lds\n\t"
        "v_mfma_f32_16x16x32_bf16 v[24:27], a[4:7], v[60:63], 0\n\t"
        "ds_read_b128 v[56:59], v105 offset:16384\n\t"
        "v_mfma_f32_16x16x32_bf16 v[28:31], v[60:63], a[8:11], 0\n\t"
        "s_waitcnt lgkmcnt(6)\n\t"
        "v_mfma_f32_16x16x32_bf16 v[8:11], a[12:15], v[64:67], v[8:11]\n\t"
        "v_cvt_pk_bf16_f32 v88, v32, v33\n\t"
        "v_cvt_pk_bf16_f32 v89, v34, v35\n\t"
        "v_mfma_f32_16x16x32_bf16 v[12:15], a[16:19], v[64:67], v[12:15]\n\t"
        "ds_read_b128 v[60:63], v105 offset:18432\n\t"
        "v_cvt_pk_bf16_f32 v90, v36, v37\n\t"
        "v_mfma_f32_16x16x32_bf16 v[16:19], v[64:67], a[20:23], v[16:19]\n\t"
        "s_add_u32 m0, %[ld], 114688\n\t"
        "s_add_u32 %[t0], %[xo], 1792\n\t"
        "s_waitcnt lgkmcnt(6)\n\t"
        "v_mfma_f32_16x16x32_bf16 v[20:23], a[12:15], v[68:71], v[20:23]\n\t"
        "buffer_load_dwordx4 v100, %[sx], %[t0] offen lds\n\t"
        "v_mfma_f32_16x16x32_bf16 v[24:27], a[16:19], v[68:71], v[24:27]\n\t"
        "ds_read_b128 v[64:67], v106 offset:16384\n\t"
        "v_cvt_pk_bf16_f32 v91, v38, v39\n\t"
        "v_mfma_f32_16x16x32_bf16 v[28:31], v[68:71], a[20:23], v[28:31]\n\t"
        "buffer_store_dwordx4 v[88:91], v109, %[sw], %[so] offen sc1\n\t"
        "s_waitcnt lgkmcnt(6)\n\t"
        "v_mfma_f32_16x16x32_bf16 v[8:11], a[24:27], v[72:75], v[8:11]\n\t"
        "v_cvt_pk_bf16_f32 v92, v44, v45\n\t"
        "v_cvt_pk_bf16_f32 v93, v46, v47\n\t"
        "v_mfma_f32_16x16x32_bf16 v[12:15], a[28:31], v[72:75], v[12:15]\n\t"
        "ds_read_b128 v[68:71], v106 offset:18432\n\t"
        "v_cvt_pk_bf16_f32 v94, v48, v49\n\t"
        "v_mfma_f32_16x16x32_bf16 v[16:19], v[72:75], a[32:35], v[16:19]\n\t"
        "s_add_u32 m0, %[ld], 115712\n\t"
        "s_add_u32 %[t0], %[xo], 18176\n\t"
        "s_waitcnt lgkmcnt(6)\n\t"
        "v_mfma_f32_16x16x32_bf16 v[20:23], a[24:27], v[76:79], v[20:23]\n\t"
        "buffer_load_dwordx4 v100, %[sx], %[t0] offen lds\n\t"
        "v_mfma_f32_16x16x32_bf16 v[24:27], a[28:31], v[76:79], v[24:27]\n\t"
        "ds_read_b128 v[72:75], v105 offset:24576\n\t"
        "v_cvt_pk_bf16_f32 v95, v50, v51\n\t"
        "v_mfma_f32_16x16x32_bf16 v[28:31], v[76:79], a[32:35], v[28:31]\n\t"
        "buffer_store_dwordx4 v[92:95], v110, %[sw], %[so] offen sc1\n\t"
        "s_waitcnt lgkmcnt(6)\n\t"
        "v_mfma_f32_16x16x32_bf16 v[8:11], a[36:39], v[80:83], v[8:11]\n\t"
        "v_cvt_pk_bf16_f32 v96, v40, v41\n\t"
        "v_cvt_pk_bf16_f32 v97, v42, v43\n\t"
        "v_mfma_f32_16x16x32_bf16 v[12:15], a[40:43], v[80:83], v[12:15]\n\t"
        "ds_read_b128 v[76:79], v105 offset:26624\n\t"
        "v_cvt_pk_bf16_f32 v98, v52, v53\n\t"
        "v_mfma_f32_16x16x32_bf16 v[16:19], v[80:83], a[44:47], v[16:19]\n\t"
        "s_add_u32 m0, %[ld], 122880\n\t"
        "s_add_u32 %[t0], %[xo], 1920\n\t"
        "s_waitcnt lgkmcnt(6)\n\t"
        "v_mfma_f32_16x16x32_bf16 v[20:23], a[36:39], v[84:87], v[20:23]\n\t"
        "buffer_load_dwordx4 v100, %[sx], %[t0] offen lds\n\t"
        "v_mfma_f32_16x16x32_bf16 v[24:27], a[40:43], v[84:87], v[24:27]\n\t"
        "ds_read_b128 v[80:83], v106 offset:24576\n\t"
        "v_cvt_pk_bf16_f32 v99, v54, v55\n\t"
        "v_mfma_f32_16x16x32_bf16 v[28:31], v[84:87], a[44:47], v[28:31]\n\t"
        "buffer_store_dwordx4 v[96:99], v111, %[sw], %[so] offen sc1\n\t"
        "s_waitcnt lgkmcnt(6)\n\t"
        "v_mfma_f32_16x16x32_bf16 v[8:11], a[48:51], v[56:59], v[8:11]\n\t"
        "s_lshl_b32 %[so], %[cn], 19\n\t"
        "s_cmp_eq_u32 %[cn], 2\n\t"
        "s_cbranch_scc1 LG_first%=\n\t"
        "LG_back%=:\n\t"
        "v_mfma_f32_16x16x32_bf16 v[12:15], a[52:55], v[56:59], v[12:15]\n\t"
        "ds_read_b128 v[84:87], v106 offset:26624\n\t"
        "v_mfma_f32_16x16x32_bf16 v[16:19], v[56:59], a[56:59], v[16:19]\n\t"
        "s_add_u32 m0, %[ld], 123904\n\t"
        "s_add_u32 %[t0], %[xo], 18304\n\t"
        "s_waitcnt lgkmcnt(6)\n\t"
        "v_mfma_f32_16x16x32_bf16 v[20:23], a[48:51], v[60:63], v[20:23]\n\t"
        "buffer_load_dwordx4 v100, %[sx], %[t0] offen lds\n\t"
        "v_mfma_f32_16x16x32_bf16 v[24:27], a[52:55], v[60:63], v[24:27]\n\t"
        "v_mfma_f32_16x16x32_bf16 v[28:31], v[60:63], a[56:59], v[28:31]\n\t"
        "s_waitcnt lgkmcnt(5)\n\t"
        "v_mfma_f32_16x16x32_bf16 v[8:11], a[60:63], v[64:67], v[8:11]\n\t"
        "s_waitcnt vmcnt(20) lgkmcnt(0)\n\t"
        "s_barrier\n\t"
        "s_add_u32 %[xo], %[xo], 0x20000\n\t"
        "v_mfma_f32_16x16x32_bf16 v[12:15], a[64:67], v[64:67], v[12:15]\n\t"
        "ds_read_b128 v[56:59], v105 offset:32768\n\t"
        "v_mfma_f32_16x16x32_bf16 v[16:19], v[64:67], a[68:71], v[16:19]\n\t"
        "ds_read_b128 v[60:63], v105 offset:34816\n\t"
        "s_add_u32 m0, %[ld], 0\n\t"
        "s_add_u32 %[t0], %[xo], 0\n\t"
        "v_mfma_f32_16x16x32_bf16 v[20:23], a[60:63], v[68:71], v[20:23]\n\t"
        "buffer_load_dwordx4 v100, %[sx], %[t0] offen lds\n\t"
        "v_mfma_f32_16x16x32_bf16 v[24:27], a[64:67], v[68:71], v[24:27]\n\t"
        "ds_read_b128 v[64:67], v106 offset:32768\n\t"
        "v_mfma_f32_16x16x32_bf16 v[28:31], v[68:71], a[68:71], v[28:31]\n\t"
        "v_mfma_f32_16x16x32_bf16 v[8:11], a[72:75], v[72:75], v[8:11]\n\t"
        "v_mfma_f32_16x16x32_bf16 v[12:15], a[76:79], v[72:75], v[12:15]\n\t"
        "ds_read_b128 v[68:71], v106 offset:34816\n\t"
        "v_mfma_f32_16x16x32_bf16 v[16:19], v[72:75], a[80:83], v[16:19]\n\t"
        "s_add_u32 m0, %[ld], 1024\n\t"
        "s_add_u32 %[t0], %[xo], 16384\n\t"
        "v_mfma_f32_16x16x32_bf16 v[20:23], a[72:75], v[76:79], v[20:23]\n\t"
        "buffer_load_dwordx4 v100, %[sx], %[t0] offen lds\n\t"
        "v_mfma_f32_16x16x32_bf16 v[24:27], a[76:79], v[76:79], v[24:27]\n\t"
        "ds_read_b128 v[72:75], v105 offset:40960\n\t"
        "v_mfma_f32_16x16x32_bf16 v[28:31], v[76:79], a[80:83], v[28:31]\n\t"
        "v_mfma_f32_16x16x32_bf16 v[8:11], a[84:87], v[80:83], v[8:11]\n\t"
        "v_mfma_f32_16x16x32_bf16 v[12:15], a[88:91], v[80:83], v[12:15]\n\t"
        "ds_read_b128 v[76:79], v105 offset:43008\n\t"
        "v_mfma_f32_16x16x32_bf16 v[16:19], v[80:83], a[92:95], v[16:19]\n\t"
        "s_add_u32 m0, %[ld], 8192\n\t"
        "s_add_u32 %[t0], %[xo], 128\n\t"
        "v_mfma_f32_16x16x32_bf16 v[20:23], a[84:87], v[84:87], v[20:23]\n\t"
        "buffer_load_dwordx4 v100, %[sx], %[t0] offen lds\n\t"
        "v_mfma_f32_16x16x32_bf16 v[24:27], a[88:91], v[84:87], v[24:27]\n\t"
        "ds_read_b128 v[80:83], v106 offset:40960\n\t"
        "v_mfma_f32_16x16x32_bf16 v[28:31], v[84:87], a[92:95], v[28:31]\n\t"
        "s_waitcnt lgkmcnt(6)\n\t"
        "v_mfma_f32_16x16x32_bf16 v[8:11], a[96:99], v[56:59], v[8:11]\n\t"
        "v_mfma_f32_16x16x32_bf16 v[12:15], a[100:103], v[56:59], v[12:15]\n\t"
        "ds_read_b128 v[84:87], v106 offset:43008\n\t"
        "v_mfma_f32_16x16x32_bf16 v[16:19], v[56:59], a[104:107], v[16:19]\n\t"
        "s_add_u32 m0, %[ld], 9216\n\t"
        "s_add_u32 %[t0], %[xo], 16512\n\t"
        "s_waitcnt lgkmcnt(6)\n\t"
        "v_mfma_f32_16x16x32_bf16 v[20:23], a[96:99], v[60:63], v[20:23]\n\t"
        "buffer_load_dwordx4 v100, %[sx], %[t0] offen lds\n\t"
        "v_mfma_f32_16x16x32_bf16 v[24:27], a[100:103], v[60:63], v[24:27]\n\t"
        "ds_read_b128 v[56:59], v105 offset:49152\n\t"
        "v_mfma_f32_16x16x32_bf16 v[28:31], v[60:63], a[104:107], v[28:31]\n\t"
        "s_waitcnt lgkmcnt(6)\n\t"
        "v_mfma_f32_16x16x32_bf16 v[8:11], a[108:111], v[64:67], v[8:11]\n\t"
        "v_mfma_f32_16x16x32_bf16 v[12:15], a[112:115], v[64:67], v[12:15]\n\t"
        "ds_read_b128 v[60:63], v105 offset:51200\n\t"
        "v_mfma_f32_16x16x32_bf16 v[16:19], v[64:67], a[116:119], v[16:19]\n\t"
        "s_add_u32 m0, %[ld], 16384\n\t"
        "s_add_u32 %[t0], %[xo], 256\n\t"
        "s_waitcnt lgkmcnt(6)\n\t"
        "v_mfma_f32_16x16x32_bf16 v[20:23], a[108:111], v[68:71], v[20:23]\n\t"
        "buffer_load_dwordx4 v100, %[sx], %[t0] offen lds\n\t"
        "v_mfma_f32_16x16x32_bf16 v[24:27], a[112:115], v[68:71], v[24:27]\n\t"
        "ds_read_b128 v[64:67], v106 offset:49152\n\t"
        "v_mfma_f32_16x16x32_bf16 v[28:31], v[68:71], a[116:119], v[28:31]\n\t"
        "s_waitcnt lgkmcnt(6)\n\t"
        "v_mfma_f32_16x16x32_bf16 v[8:11], a[120:123], v[72:75], v[8:11]\n\t"
        "v_mfma_f32_16x16x32_bf16 v[12:15], a[124:127], v[72:75], v[12:15]\n\t"
        "ds_read_b128 v[68:71], v106 offset:51200\n\t"
        "v_mfma_f32_16x16x32_bf16 v[16:19], v[72:75], a[128:131], v[16:19]\n\t"
        "s_add_u32 m0, %[ld], 17408\n\t"
        "s_add_u32 %[t0], %[xo], 16640\n\t"
        "s_waitcnt lgkmcnt(6)\n\t"
        "v_mfma_f32_16x16x32_bf16 v[20:23], a[120:123], v[76:79], v[20:23]\n\t"
        "buffer_load_dwordx4 v100, %[sx], %[t0] offen lds\n\t"
        "v_mfma_f32_16x16x32_bf16 v[24:27], a[124:127], v[76:79], v[24:27]\n\t"
        "ds_read_b128 v[72:75], v105 offset:57344\n\t"
        "v_mfma_f32_16x16x32_bf16 v[28:31], v[76:79], a[128:131], v[28:31]\n\t"
        "s_waitcnt lgkmcnt(6)\n\t"
        "v_mfma_f32_16x16x32_bf16 v[8:11], a[132:135], v[80:83], v[8:11]\n\t"
        "v_mfma_f32_16x16x32_bf16 v[12:15], a[136:139], v[80:83], v[12:15]\n\t"
        "ds_read_b128 v[76:79], v105 offset:59392\n\t"
        "v_mfma_f32_16x16x32_bf16 v[16:19], v[80:83], a[140:143], v[16:19]\n\t"
        "s_add_u32 m0, %[ld], 24576\n\t"
        "s_add_u32 %[t0], %[xo], 384\n\t"
        "s_waitcnt lgkmcnt(6)\n\t"
        "v_mfma_f32_16x16x32_bf16 v[20:23], a[132:135], v[84:87], v[20:23]\n\t"
        "buffer_load_dwordx4 v100, %[sx], %[t0] offen lds\n\t"
        "v_mfma_f32_16x16x32_bf16 v[24:27], a[136:139], v[84:87], v[24:27]\n\t"
        "ds_read_b128 v[80:83], v106 offset:57344\n\t"
        "v_mfma_f32_16x16x32_bf16 v[28:31], v[84:87], a[140:143], v[28:31]\n\t"
        "s_waitcnt lgkmcnt(6)\n\t"
        "v_mfma_f32_16x16x32_bf16 v[8:11], a[144:147], v[56:59], v[8:11]\n\t"
        "v_mfma_f32_16x16x32_bf16 v[12:15], a[148:151], v[56:59], v[12:15]\n\t"
        "ds_read_b128 v[84:87], v106 offset:59392\n\t"
        "v_mfma_f32_16x16x32_bf16 v[16:19], v[56:59], a[152:155], v[16:19]\n\t"
        "s_add_u32 m0, %[ld], 25600\n\t"
        "s_add_u32 %[t0], %[xo], 16768\n\t"
        "s_waitcnt lgkmcnt(6)\n\t"
        "v_mfma_f32_16x16x32_bf16 v[20:23], a[144:147], v[60:63], v[20:23]\n\t"
        "buffer_load_dwordx4 v100, %[sx], %[t0] offen lds\n\t"
        "v_mfma_f32_16x16x32_bf16 v[24:27], a[148:151], v[60:63], v[24:27]\n\t"
        "v_mfma_f32_16x16x32_bf16 v[28:31], v[60:63], a[152:155], v[28:31]\n\t"
        "s_waitcnt lgkmcnt(5)\n\t"
        "v_mfma_f32_16x16x32_bf16 v[8:11], a[156:159], v[64:67], v[8:11]\n\t"
        "s_waitcnt vmcnt(9) lgkmcnt(0)\n\t"
        "s_barrier\n\t"
        "v_mfma_f32_16x16x32_bf16 v[12:15], a[160:163], v[64:67], v[12:15]\n\t"
        "ds_read_b128 v[56:59], v107 offset:0\n\t"
        "v_mfma_f32_16x16x32_bf16 v[16:19], v[64:67], a[164:167], v[16:19]\n\t"
        "ds_read_b128 v[60:63], v107 offset:2048\n\t"
        "s_add_u32 m0, %[ld], 32768\n\t"
        "s_add_u32 %[t0], %[xo], 512\n\t"
        "v_mfma_f32_16x16x32_bf16 v[20:23], a[156:159], v[68:71], v[20:23]\n\t"
        "buffer_load_dwordx4 v100, %[sx], %[t0] offen lds\n\t"
        "v_mfma_f32_16x16x32_bf16 v[24:27], a[160:163], v[68:71], v[24:27]\n\t"
        "ds_read_b128 v[64:67], v108 offset:0\n\t"
        "v_mfma_f32_16x16x32_bf16 v[28:31], v[68:71], a[164:167], v[28:31]\n\t"
        "v_mfma_f32_16x16x32_bf16 v[8:11], a[168:171], v[72:75], v[8:11]\n\t"
        "v_mfma_f32_16x16x32_bf16 v[12:15], a[172:175], v[72:75], v[12:15]\n\t"
        "ds_read_b128 v[68:71], v108 offset:2048\n\t"
        "v_mov_b32_e32 v119, %[cn]\n\t"
        "buffer_store_dword v119, v113, %[sw], 0 offen sc1\n\t"
        "s_add_u32 %[cn], %[cn], 1\n\t"
        "v_mfma_f32_16x16x32_bf16 v[16:19], v[72:75], a[176:179], v[16:19]\n\t"
        "s_add_u32 m0, %[ld], 33792\n\t"
        "s_add_u32 %[t0], %[xo], 16896\n\t"
        "v_mfma_f32_16x16x32_bf16 v[20:23], a[168:171], v[76:79], v[20:23]\n\t"
        "buffer_load_dwordx4 v100, %[sx], %[t0] offen lds\n\t"
        "v_mfma_f32_16x16x32_bf16 v[24:27], a[172:175], v[76:79], v[24:27]\n\t"
        "ds_read_b128 v[72:75], v107 offset:8192\n\t"
        "v_mfma_f32_16x16x32_bf16 v[28:31], v[76:79], a[176:179], v[28:31]\n\t"
        "v_mfma_f32_16x16x32_bf16 v[8:11], a[180:183], v[80:83], v[8:11]\n\t"
        "v_mfma_f32_16x16x32_bf16 v[12:15], a[184:187], v[80:83], v[12:15]\n\t"
        "ds_read_b128 v[76:79], v107 offset:10240\n\t"
        "v_mfma_f32_16x16x32_bf16 v[16:19], v[80:83], a[188:191], v[16:19]\n\t"
        "s_add_u32 m0, %[ld], 40960\n\t"
        "s_add_u32 %[t0], %[xo], 640\n\t"
        "v_mfma_f32_16x16x32_bf16 v[20:23], a[180:183], v[84:87], v[20:23]\n\t"
        "buffer_load_dwordx4 v100, %[sx], %[t0] offen lds\n\t"
        "v_mfma_f32_16x16x32_bf16 v[24:27], a[184:187], v[84:87], v[24:27]\n\t"
        "ds_read_b128 v[80:83], v108 offset:8192\n\t"
        "v_mfma_f32_16x16x32_bf16 v[28:31], v[84:87], a[188:191], v[28:31]\n\t"
        "s_waitcnt lgkmcnt(6)\n\t"
        "v_mfma_f32_16x16x32_bf16 v[8:11], a[192:195], v[56:59], v[8:11]\n\t"
        "v_mfma_f32_16x16x32_bf16 v[12:15], a[196:199], v[56:59], v[12:15]\n\t"
        "ds_read_b128 v[84:87], v108 offset:10240\n\t"
        "v_mfma_f32_16x16x32_bf16 v[16:19], v[56:59], a[200:203], v[16:19]\n\t"
        "s_add_u32 m0, %[ld], 41984\n\t"
        "s_add_u32 %[t0], %[xo], 17024\n\t"
        "s_waitcnt lgkmcnt(6)\n\t"
        "v_mfma_f32_16x16x32_bf16 v[20:23], a[192:195], v[60:63], v[20:23]\n\t"
        "buffer_load_dwordx4 v100, %[sx], %[t0] offen lds\n\t"
        "v_mfma_f32_16x16x32_bf16 v[24:27], a[196:199], v[60:63], v[24:27]\n\t"
        "ds_read_b128 v[56:59], v107 offset:16384\n\t"
        "v_mfma_f32_16x16x32_bf16 v[28:31], v[60:63], a[200:203], v[28:31]\n\t"
        "s_waitcnt lgkmcnt(6)\n\t"
        "v_mfma_f32_16x16x32_bf16 v[8:11], a[204:207], v[64:67], v[8:11]\n\t"
        "v_mfma_f32_16x16x32_bf16 v[12:15], a[208:211], v[64:67], v[12:15]\n\t"
        "ds_read_b128 v[60:63], v107 offset:18432\n\t"
        "v_mfma_f32_16x16x32_bf16 v[16:19], v[64:67], a[212:215], v[16:19]\n\t"
        "s_add_u32 m0, %[ld], 49152\n\t"
        "s_add_u32 %[t0], %[xo], 768\n\t"
        "s_waitcnt lgkmcnt(6)\n\t"
        "v_mfma_f32_16x16x32_bf16 v[20:23], a[204:207], v[68:71], v[20:23]\n\t"
        "buffer_load_dwordx4 v100, %[sx], %[t0] offen lds\n\t"
        "v_mfma_f32_16x16x32_bf16 v[24:27], a[208:211], v[68:71], v[24:27]\n\t"
        "ds_read_b128 v[64:67], v108 offset:16384\n\t"
        "v_mfma_f32_16x16x32_bf16 v[28:31], v[68:71], a[212:215], v[28:31]\n\t"
        "s_waitcnt lgkmcnt(6)\n\t"
        "v_mfma_f32_16x16x32_bf16 v[8:11], a[216:219], v[72:75], v[8:11]\n\t"
        "v_mfma_f32_16x16x32_bf16 v[12:15], a[220:223], v[72:75], v[12:15]\n\t"
        "ds_read_b128 v[68:71], v108 offset:18432\n\t"
        "v_mfma_f32_16x16x32_bf16 v[16:19], v[72:75], a[224:227], v[16:19]\n\t"
        "s_add_u32 m0, %[ld], 50176\n\t"
        "s_add_u32 %[t0], %[xo], 17152\n\t"
        "s_waitcnt lgkmcnt(6)\n\t"
        "v_mfma_f32_16x16x32_bf16 v[20:23], a[216:219], v[76:79], v[20:23]\n\t"
        "buffer_load_dwordx4 v100, %[sx], %[t0] offen lds\n\t"
        "v_mfma_f32_16x16x32_bf16 v[24:27], a[220:223], v[76:79], v[24:27]\n\t"
        "ds_read_b128 v[72:75], v107 offset:24576\n\t"
        "v_mfma_f32_16x16x32_bf16 v[28:31], v[76:79], a[224:227], v[28:31]\n\t"
        "s_waitcnt lgkmcnt(6)\n\t"
        "v_mfma_f32_16x16x32_bf16 v[8:11], a[228:231], v[80:83], v[8:11]\n\t"
        "v_mfma_f32_16x16x32_bf16 v[12:15], a[232:235], v[80:83], v[12:15]\n\t"
        "ds_read_b128 v[76:79], v107 offset:26624\n\t"
        "v_mfma_f32_16x16x32_bf16 v[16:19], v[80:83], a[236:239], v[16:19]\n\t"
        "s_add_u32 m0, %[ld], 57344\n\t"
        "s_add_u32 %[t0], %[xo], 896\n\t"
        "s_waitcnt lgkmcnt(6)\n\t"
        "v_mfma_f32_16x16x32_bf16 v[20:23], a[228:231], v[84:87], v[20:23]\n\t"
        "buffer_load_dwordx4 v100, %[sx], %[t0] offen lds\n\t"
        "v_mfma_f32_16x16x32_bf16 v[24:27], a[232:235], v[84:87], v[24:27]\n\t"
        "ds_read_b128 v[80:83], v108 offset:24576\n\t"
        "v_mfma_f32_16x16x32_bf16 v[28:31], v[84:87], a[236:239], v[28:31]\n\t"
        "s_waitcnt lgkmcnt(6)\n\t"
        "v_mfma_f32_16x16x32_bf16 v[8:11], a[240:243], v[56:59], v[8:11]\n\t"
        "v_mfma_f32_16x16x32_bf16 v[12:15], a[244:247], v[56:59], v[12:15]\n\t"
        "ds_read_b128 v[84:87], v108 offset:26624\n\t"
        "v_mfma_f32_16x16x32_bf16 v[16:19], v[56:59], a[248:251], v[16:19]\n\t"
        "s_add_u32 m0, %[ld], 58368\n\t"
        "s_add_u32 %[t0], %[xo], 17280\n\t"
        "s_waitcnt lgkmcnt(6)\n\t"
        "v_mfma_f32_16x16x32_bf16 v[20:23], a[240:243], v[60:63], v[20:23]\n\t"
        "buffer_load_dwordx4 v100, %[sx], %[t0] offen lds\n\t"
        "v_mfma_f32_16x16x32_bf16 v[24:27], a[244:247], v[60:63], v[24:27]\n\t"
        "v_mfma_f32_16x16x32_bf16 v[28:31], v[60:63], a[248:251], v[28:31]\n\t"
        "s_waitcnt lgkmcnt(5)\n\t"
        "v_mfma_f32_16x16x32_bf16 v[8:11], a[252:255], v[64:67], v[8:11]\n\t"
        "s_waitcnt vmcnt(17) lgkmcnt(0)\n\t"
        "s_barrier\n\t"
        "v_mfma_f32_16x16x32_bf16 v[12:15], v[128:131], v[64:67], v[12:15]\n\t"
        "ds_read_b128 v[56:59], v107 offset:32768\n\t"
        "v_mfma_f32_16x16x32_bf16 v[16:19], v[64:67], v[132:135], v[16:19]\n\t"
        "ds_read_b128 v[60:63], v107 offset:34816\n\t"
        "s_add_u32 m0, %[ld], 65536\n\t"
        "s_add_u32 %[t0], %[xo], 1024\n\t"
        "v_mfma_f32_16x16x32_bf16 v[20:23], a[252:255], v[68:71], v[20:23]\n\t"
        "buffer_load_dwordx4 v100, %[sx], %[t0] offen lds\n\t"
        "v_mfma_f32_16x16x32_bf16 v[24:27], v[128:131], v[68:71], v[24:27]\n\t"
        "ds_read_b128 v[64:67], v108 offset:32768\n\t"
        "v_mfma_f32_16x16x32_bf16 v[28:31], v[68:71], v[132:135], v[28:31]\n\t"
        "v_mfma_f32_16x16x32_bf16 v[8:11], v[136:139], v[72:75], v[8:11]\n\t"
        "v_mfma_f32_16x16x32_bf16 v[12:15], v[140:143], v[72:75], v[12:15]\n\t"
        "ds_read_b128 v[68:71], v108 offset:34816\n\t"
        "v_mfma_f32_16x16x32_bf16 v[16:19], v[72:75], v[144:147], v[16:19]\n\t"
        "s_add_u32 m0, %[ld], 66560\n\t"
        "s_add_u32 %[t0], %[xo], 17408\n\t"
        "v_mfma_f32_16x16x32_bf16 v[20:23], v[136:139], v[76:79], v[20:23]\n\t"
        "buffer_load_dwordx4 v100, %[sx], %[t0] offen lds\n\t"
        "v_mfma_f32_16x16x32_bf16 v[24:27], v[140:143], v[76:79], v[24:27]\n\t"
        "ds_read_b128 v[72:75], v107 offset:40960\n\t"
        "v_mfma_f32_16x16x32_bf16 v[28:31], v[76:79], v[144:147], v[28:31]\n\t"
        "v_mfma_f32_16x16x32_bf16 v[8:11], v[148:151], v[80:83], v[8:11]\n\t"
        "v_mfma_f32_16x16x32_bf16 v[12:15], v[152:155], v[80:83], v[12:15]\n\t"
        "ds_read_b128 v[76:79], v107 offset:43008\n\t"
        "v_mfma_f32_16x16x32_bf16 v[16:19], v[80:83], v[156:159], v[16:19]\n\t"
        "s_add_u32 m0, %[ld], 73728\n\t"
        "s_add_u32 %[t0], %[xo], 1152\n\t"
        "v_mfma_f32_16x16x32_bf16 v[20:23], v[148:151], v[84:87], v[20:23]\n\t"
        "buffer_load_dwordx4 v100, %[sx], %[t0] offen lds\n\t"
        "v_mfma_f32_16x16x32_bf16 v[24:27], v[152:155], v[84:87], v[24:27]\n\t"
        "ds_read_b128 v[80:83], v108 offset:40960\n\t"
        "v_mfma_f32_16x16x32_bf16 v[28:31], v[84:87], v[156:159], v[28:31]\n\t"
        "s_waitcnt lgkmcnt(6)\n\t"
        "v_mfma_f32_16x16x32_bf16 v[8:11], v[160:163], v[56:59], v[8:11]\n\t"
        "v_mfma_f32_16x16x32_bf16 v[12:15], v[164:167], v[56:59], v[12:15]\n\t"
        "ds_read_b128 v[84:87], v108 offset:43008\n\t"
        "v_mfma_f32_16x16x32_bf16 v[16:19], v[56:59], v[168:171], v[16:19]\n\t"
        "s_add_u32 m0, %[ld], 74752\n\t"
        "s_add_u32 %[t0], %[xo], 17536\n\t"
        "s_waitcnt lgkmcnt(6)\n\t"
        "v_mfma_f32_16x16x32_bf16 v[20:23], v[160:163], v[60:63], v[20:23]\n\t"
        "buffer_load_dwordx4 v100, %[sx], %[t0] offen lds\n\t"
        "v_mfma_f32_16x16x32_bf16 v[24:27], v[164:167], v[60:63], v[24:27]\n\t"
        "ds_read_b128 v[56:59], v107 offset:49152\n\t"
        "v_mfma_f32_16x16x32_bf16 v[28:31], v[60:63], v[168:171], v[28:31]\n\t"
        "s_waitcnt lgkmcnt(6)\n\t"
        "v_mfma_f32_16x16x32_bf16 v[8:11], v[172:175], v[64:67], v[8:11]\n\t"
        "v_mfma_f32_16x16x32_bf16 v[12:15], v[176:179], v[64:67], v[12:15]\n\t"
        "ds_read_b128 v[60:63], v107 offset:51200\n\t"
        "v_mfma_f32_16x16x32_bf16 v[16:19], v[64:67], v[180:183], v[16:19]\n\t"
        "s_add_u32 m0, %[ld], 81920\n\t"
        "s_add_u32 %[t0], %[xo], 1280\n\t"
        "s_waitcnt lgkmcnt(6)\n\t"
        "v_mfma_f32_16x16x32_bf16 v[20:23], v[172:175], v[68:71], v[20:23]\n\t"
        "buffer_load_dwordx4 v100, %[sx], %[t0] offen lds\n\t"
        "v_mfma_f32_16x16x32_bf16 v[24:27], v[176:179], v[68:71], v[24:27]\n\t"
        "ds_read_b128 v[64:67], v108 offset:49152\n\t"
        "v_mfma_f32_16x16x32_bf16 v[28:31], v[68:71], v[180:183], v[28:31]\n\t"
        "s_waitcnt lgkmcnt(6)\n\t"
        "v_mfma_f32_16x16x32_bf16 v[8:11], v[184:187], v[72:75], v[8:11]\n\t"
        "v_mfma_f32_16x16x32_bf16 v[12:15], v[188:191], v[72:75], v[12:15]\n\t"
        "ds_read_b128 v[68:71], v108 offset:51200\n\t"
        "v_mfma_f32_16x16x32_bf16 v[16:19], v[72:75], v[192:195], v[16:19]\n\t"
        "s_add_u32 m0, %[ld], 82944\n\t"
        "s_add_u32 %[t0], %[xo], 17664\n\t"
        "s_waitcnt lgkmcnt(6)\n\t"
        "v_mfma_f32_16x16x32_bf16 v[20:23], v[184:187], v[76:79], v[20:23]\n\t"
        "buffer_load_dwordx4 v100, %[sx], %[t0] offen lds\n\t"
        "v_mfma_f32_16x16x32_bf16 v[24:27], v[188:191], v[76:79], v[24:27]\n\t"
        "ds_read_b128 v[72:75], v107 offset:57344\n\t"
        "v_mfma_f32_16x16x32_bf16 v[28:31], v[76:79], v[192:195], v[28:31]\n\t"
        "s_waitcnt lgkmcnt(6)\n\t"
        "v_mfma_f32_16x16x32_bf16 v[8:11], v[196:199], v[80:83], v[8:11]\n\t"
        "v_mfma_f32_16x16x32_bf16 v[12:15], v[200:203], v[80:83], v[12:15]\n\t"
        "ds_read_b128 v[76:79], v107 offset:59392\n\t"
        "v_mfma_f32_16x16x32_bf16 v[16:19], v[80:83], v[204:207], v[16:19]\n\t"
        "s_add_u32 m0, %[ld], 90112\n\t"
        "s_add_u32 %[t0], %[xo], 1408\n\t"
        "s_waitcnt lgkmcnt(6)\n\t"
        "v_mfma_f32_16x16x32_bf16 v[20:23], v[196:199], v[84:87], v[20:23]\n\t"
        "buffer_load_dwordx4 v100, %[sx], %[t0] offen lds\n\t"
        "v_mfma_f32_16x16x32_bf16 v[24:27], v[200:203], v[84:87], v[24:27]\n\t"
        "ds_read_b128 v[80:83], v108 offset:57344\n\t"
        "v_mfma_f32_16x16x32_bf16 v[28:31], v[84:87], v[204:207], v[28:31]\n\t"
        "s_waitcnt lgkmcnt(6)\n\t"
        "v_mfma_f32_16x16x32_bf16 v[8:11], v[208:211], v[56:59], v[8:11]\n\t"
        "v_mfma_f32_16x16x32_bf16 v[12:15], v[212:215], v[56:59], v[12:15]\n\t"
        "ds_read_b128 v[84:87], v108 offset:59392\n\t"
        "v_mfma_f32_16x16x32_bf16 v[16:19], v[56:59], v[216:219], v[16:19]\n\t"
        "s_add_u32 m0, %[ld], 91136\n\t"
        "s_add_u32 %[t0], %[xo], 17792\n\t"
        "s_waitcnt lgkmcnt(6)\n\t"
        "v_mfma_f32_16x16x32_bf16 v[20:23], v[208:211], v[60:63], v[20:23]\n\t"
        "buffer_load_dwordx4 v100, %[sx], %[t0] offen lds\n\t"
        "v_mfma_f32_16x16x32_bf16 v[24:27], v[212:215], v[60:63], v[24:27]\n\t"
        "v_mfma_f32_16x16x32_bf16 v[28:31], v[60:63], v[216:219], v[28:31]\n\t"
        "s_waitcnt lgkmcnt(5)\n\t"
        "v_mfma_f32_16x16x32_bf16 v[8:11], v[220:223], v[64:67], v[8:11]\n\t"
        "s_waitcnt vmcnt(17) lgkmcnt(0)\n\t"
        "s_barrier\n\t"
        "v_mfma_f32_16x16x32_bf16 v[12:15], v[224:227], v[64:67], v[12:15]\n\t"
        "ds_read_b128 v[56:59], v105 offset:0\n\t"
        "v_mfma_f32_16x16x32_bf16 v[16:19], v[64:67], v[228:231], v[16:19]\n\t"
        "ds_read_b128 v[60:63], v105 offset:2048\n\t"
        "s_add_u32 m0, %[ld], 98304\n\t"
        "s_add_u32 %[t0], %[xo], 1536\n\t"
        "v_mfma_f32_16x16x32_bf16 v[20:23], v[220:223], v[68:71], v[20:23]\n\t"
        "buffer_load_dwordx4 v100, %[sx], %[t0] offen lds\n\t"
        "v_mfma_f32_16x16x32_bf16 v[24:27], v[224:227], v[68:71], v[24:27]\n\t"
        "ds_read_b128 v[64:67], v106 offset:0\n\t"
        "v_mfma_f32_16x16x32_bf16 v[28:31], v[68:71], v[228:231], v[28:31]\n\t"
        "v_mfma_f32_16x16x32_bf16 v[8:11], v[232:235], v[72:75], v[8:11]\n\t"
        "v_mfma_f32_16x16x32_bf16 v[12:15], v[236:239], v[72:75], v[12:15]\n\t"
        "ds_read_b128 v[68:71], v106 offset:2048\n\t"
        "v_mfma_f32_16x16x32_bf16 v[16:19], v[72:75], v[240:243], v[16:19]\n\t"
        "s_add_u32 m0, %[ld], 99328\n\t"
        "s_add_u32 %[t0], %[xo], 17920\n\t"
        "v_mfma_f32_16x16x32_bf16 v[20:23], v[232:235], v[76:79], v[20:23]\n\t"
        "buffer_load_dwordx4 v100, %[sx], %[t0] offen lds\n\t"
        "v_mfma_f32_16x16x32_bf16 v[24:27], v[236:239], v[76:79], v[24:27]\n\t"
        "ds_read_b128 v[72:75], v105 offset:8192\n\t"
        "v_mfma_f32_16x16x32_bf16 v[28:31], v[76:79], v[240:243], v[28:31]\n\t"
        "v_mfma_f32_16x16x32_bf16 v[8:11], v[244:247], v[80:83], v[8:11]\n\t"
        "v_mfma_f32_16x16x32_bf16 v[12:15], v[248:251], v[80:83], v[12:15]\n\t"
        "ds_read_b128 v[76:79], v105 offset:10240\n\t"
        "v_mfma_f32_16x16x32_bf16 v[16:19], v[80:83], v[252:255], v[16:19]\n\t"
        "s_add_u32 m0, %[ld], 106496\n\t"
        "s_add_u32 %[t0], %[xo], 1664\n\t"
        "v_mfma_f32_16x16x32_bf16 v[20:23], v[244:247], v[84:87], v[20:23]\n\t"
        "buffer_load_dwordx4 v100, %[sx], %[t0] offen lds\n\t"
        "v_mfma_f32_16x16x32_bf16 v[24:27], v[248:251], v[84:87], v[24:27]\n\t"
        "ds_read_b128 v[80:83], v106 offset:8192\n\t"
        "v_mfma_f32_16x16x32_bf16 v[28:31], v[84:87], v[252:255], v[28:31]\n\t"
        "LG_mid%=:\n\t"
        "s_waitcnt lgkmcnt(6)\n\t"
        "v_mfma_f32_16x16x32_bf16 v[32:35], a[0:3], v[56:59], 0\n\t"
        "v_mfma_f32_16x16x32_bf16 v[36:39], a[4:7], v[56:59], 0\n\t"
        "ds_read_b128 v[84:87], v106 offset:10240\n\t"
        "v_mfma_f32_16x16x32_bf16 v[40:43], v[56:59], a[8:11], 0\n\t"
        "s_add_u32 m0, %[ld], 107520\n\t"
        "s_add_u32 %[t0], %[xo], 18048\n\t"
        "s_waitcnt lgkmcnt(6)\n\t"
        "v_mfma_f32_16x16x32_bf16 v[44:47], a[0:3], v[60:63], 0\n\t"
        "buffer_load_dwordx4 v100, %[sx], %[t0] offen lds\n\t"
        "v_mfma_f32_16x16x32_bf16 v[48:51], a[4:7], v[60:63], 0\n\t"
        "ds_read_b128 v[56:59], v105 offset:16384\n\t"
        "v_mfma_f32_16x16x32_bf16 v[52:55], v[60:63], a[8:11], 0\n\t"
        "s_waitcnt lgkmcnt(6)\n\t"
        "v_mfma_f32_16x16x32_bf16 v[32:35], a[12:15], v[64:67], v[32:35]\n\t"
        "v_cvt_pk_bf16_f32 v88, v8, v9\n\t"
        "v_cvt_pk_bf16_f32 v89, v10, v11\n\t"
        "v_mfma_f32_16x16x32_bf16 v[36:39], a[16:19], v[64:67], v[36:39]\n\t"
        "ds_read_b128 v[60:63], v105 offset:18432\n\t"
        "v_cvt_pk_bf16_f32 v90, v12, v13\n\t"
        "v_mfma_f32_16x16x32_bf16 v[40:43], v[64:67], a[20:23], v[40:43]\n\t"
        "s_add_u32 m0, %[ld], 114688\n\t"
        "s_add_u32 %[t0], %[xo], 1792\n\t"
        "s_waitcnt lgkmcnt(6)\n\t"
        "v_mfma_f32_16x16x32_bf16 v[44:47], a[12:15], v[68:71], v[44:47]\n\t"
        "buffer_load_dwordx4 v100, %[sx], %[t0] offen lds\n\t"
        "v_mfma_f32_16x16x32_bf16 v[48:51], a[16:19], v[68:71], v[48:51]\n\t"
        "ds_read_b128 v[64:67], v106 offset:16384\n\t"
        "v_cvt_pk_bf16_f32 v91, v14, v15\n\t"
        "v_mfma_f32_16x16x32_bf16 v[52:55], v[68:71], a[20:23], v[52:55]\n\t"
        "buffer_store_dwordx4 v[88:91], v109, %[sw], %[so] offen sc1\n\t"
        "s_waitcnt lgkmcnt(6)\n\t"
        "v_mfma_f32_16x16x32_bf16 v[32:35], a[24:27], v[72:75], v[32:35]\n\t"
        "v_cvt_pk_bf16_f32 v92, v20, v21\n\t"
        "v_cvt_pk_bf16_f32 v93, v22, v23\n\t"
        "v_mfma_f32_16x16x32_bf16 v[36:39], a[28:31], v[72:75], v[36:39]\n\t"
        "ds_read_b128 v[68:71], v106 offset:18432\n\t"
        "v_cvt_pk_bf16_f32 v94, v24, v25\n\t"
        "v_mfma_f32_16x16x32_bf16 v[40:43], v[72:75], a[32:35], v[40:43]\n\t"
        "s_add_u32 m0, %[ld], 115712\n\t"
        "s_add_u32 %[t0], %[xo], 18176\n\t"
        "s_waitcnt lgkmcnt(6)\n\t"
        "v_mfma_f32_16x16x32_bf16 v[44:47], a[24:27], v[76:79], v[44:47]\n\t"
        "buffer_load_dwordx4 v100, %[sx], %[t0] offen lds\n\t"
        "v_mfma_f32_16x16x32_bf16 v[48:51], a[28:31], v[76:79], v[48:51]\n\t"
        "ds_read_b128 v[72:75], v105 offset:24576\n\t"
        "v_cvt_pk_bf16_f32 v95, v26, v27\n\t"
        "v_mfma_f32_16x16x32_bf16 v[52:55], v[76:79], a[32:35], v[52:55]\n\t"
        "buffer_store_dwordx4 v[92:95], v110, %[sw], %[so] offen sc1\n\t"
        "s_waitcnt lgkmcnt(6)\n\t"
        "v_mfma_f32_16x16x32_bf16 v[32:35], a[36:39], v[80:83], v[32:35]\n\t"
        "v_cvt_pk_bf16_f32 v96, v16, v17\n\t"
        "v_cvt_pk_bf16_f32 v97, v18, v19\n\t"
        "v_mfma_f32_16x16x32_bf16 v[36:39], a[40:43], v[80:83], v[36:39]\n\t"
        "ds_read_b128 v[76:79], v105 offset:26624\n\t"
        "v_cvt_pk_bf16_f32 v98, v28, v29\n\t"
        "v_mfma_f32_16x16x32_bf16 v[40:43], v[80:83], a[44:47], v[40:43]\n\t"
        "s_add_u32 m0, %[ld], 122880\n\t"
        "s_add_u32 %[t0], %[xo], 1920\n\t"
        "s_waitcnt lgkmcnt(6)\n\t"
        "v_mfma_f32_16x16x32_bf16 v[44:47], a[36:39], v[84:87], v[44:47]\n\t"
        "buffer_load_dwordx4 v100, %[sx], %[t0] offen lds\n\t"
        "v_mfma_f32_16x16x32_bf16 v[48:51], a[40:43], v[84:87], v[48:51]\n\t"
        "ds_read_b128 v[80:83], v106 offset:24576\n\t"
        "v_cvt_pk_bf16_f32 v99, v30, v31\n\t"
        "v_mfma_f32_16x16x32_bf16 v[52:55], v[84:87], a[44:47], v[52:55]\n\t"
        "buffer_store_dwordx4 v[96:99], v111, %[sw], %[so] offen sc1\n\t"
        "s_waitcnt lgkmcnt(6)\n\t"
        "v_mfma_f32_16x16x32_bf16 v[32:35], a[48:51], v[56:59], v[32:35]\n\t"
        "s_lshl_b32 %[so], %[cn], 19\n\t"
        "v_mfma_f32_16x16x32_bf16 v[36:39], a[52:55], v[56:59], v[36:39]\n\t"
        "ds_read_b128 v[84:87], v106 offset:26624\n\t"
        "v_mfma_f32_16x16x32_bf16 v[40:43], v[56:59], a[56:59], v[40:43]\n\t"
        "s_add_u32 m0, %[ld], 123904\n\t"
        "s_add_u32 %[t0], %[xo], 18304\n\t"
        "s_waitcnt lgkmcnt(6)\n\t"
        "v_mfma_f32_16x16x32_bf16 v[44:47], a[48:51], v[60:63], v[44:47]\n\t"
        "buffer_load_dwordx4 v100, %[sx], %[t0] offen lds\n\t"
        "v_mfma_f32_16x16x32_bf16 v[48:51], a[52:55], v[60:63], v[48:51]\n\t"
        "v_mfma_f32_16x16x32_bf16 v[52:55], v[60:63], a[56:59], v[52:55]\n\t"
        "s_waitcnt lgkmcnt(5)\n\t"
        "v_mfma_f32_16x16x32_bf16 v[32:35], a[60:63], v[64:67], v[32:35]\n\t"
        "s_waitcnt vmcnt(19) lgkmcnt(0)\n\t"
        "s_barrier\n\t"
        "s_add_u32 %[xo], %[xo], 0x20000\n\t"
        "v_mfma_f32_16x16x32_bf16 v[36:39], a[64:67], v[64:67], v[36:39]\n\t"
        "ds_read_b128 v[56:59], v105 offset:32768\n\t"
        "v_mfma_f32_16x16x32_bf16 v[40:43], v[64:67], a[68:71], v[40:43]\n\t"
        "ds_read_b128 v[60:63], v105 offset:34816\n\t"
        "s_add_u32 m0, %[ld], 0\n\t"
        "s_add_u32 %[t0], %[xo], 0\n\t"
        "v_mfma_f32_16x16x32_bf16 v[44:47], a[60:63], v[68:71], v[44:47]\n\t"
        "buffer_load_dwordx4 v100, %[sx], %[t0] offen lds\n\t"
        "v_mfma_f32_16x16x32_bf16 v[48:51], a[64:67], v[68:71], v[48:51]\n\t"
        "ds_read_b128 v[64:67], v106 offset:32768\n\t"
        "v_mfma_f32_16x16x32_bf16 v[52:55], v[68:71], a[68:71], v[52:55]\n\t"
        "v_mfma_f32_16x16x32_bf16 v[32:35], a[72:75], v[72:75], v[32:35]\n\t"
        "v_mfma_f32_16x16x32_bf16 v[36:39], a[76:79], v[72:75], v[36:39]\n\t"
        "ds_read_b128 v[68:71], v106 offset:34816\n\t"
        "v_mfma_f32_16x16x32_bf16 v[40:43], v[72:75], a[80:83], v[40:43]\n\t"
        "s_add_u32 m0, %[ld], 1024\n\t"
        "s_add_u32 %[t0], %[xo], 16384\n\t"
        "v_mfma_f32_16x16x32_bf16 v[44:47], a[72:75], v[76:79], v[44:47]\n\t"
        "buffer_load_dwordx4 v100, %[sx], %[t0] offen lds\n\t"
        "v_mfma_f32_16x16x32_bf16 v[48:51], a[76:79], v[76:79], v[48:51]\n\t"
        "ds_read_b128 v[72:75], v105 offset:40960\n\t"
        "v_mfma_f32_16x16x32_bf16 v[52:55], v[76:79], a[80:83], v[52:55]\n\t"
        "v_mfma_f32_16x16x32_bf16 v[32:35], a[84:87], v[80:83], v[32:35]\n\t"
        "v_mfma_f32_16x16x32_bf16 v[36:39], a[88:91], v[80:83], v[36:39]\n\t"
        "ds_read_b128 v[76:79], v105 offset:43008\n\t"
        "v_mfma_f32_16x16x32_bf16 v[40:43], v[80:83], a[92:95], v[40:43]\n\t"
        "s_add_u32 m0, %[ld], 8192\n\t"
        "s_add_u32 %[t0], %[xo], 128\n\t"
        "v_mfma_f32_16x16x32_bf16 v[44:47], a[84:87], v[84:87], v[44:47]\n\t"
        "buffer_load_dwordx4 v100, %[sx], %[t0] offen lds\n\t"
        "v_mfma_f32_16x16x32_bf16 v[48:51], a[88:91], v[84:87], v[48:51]\n\t"
        "ds_read_b128 v[80:83], v106 offset:40960\n\t"
        "v_mfma_f32_16x16x32_bf16 v[52:55], v[84:87], a[92:95], v[52:55]\n\t"
        "s_waitcnt lgkmcnt(6)\n\t"
        "v_mfma_f32_16x16x32_bf16 v[32:35], a[96:99], v[56:59], v[32:35]\n\t"
        "v_mfma_f32_16x16x32_bf16 v[36:39], a[100:103], v[56:59], v[36:39]\n\t"
        "ds_read_b128 v[84:87], v106 offset:43008\n\t"
        "v_mfma_f32_16x16x32_bf16 v[40:43], v[56:59], a[104:107], v[40:43]\n\t"
        "s_add_u32 m0, %[ld], 9216\n\t"
        "s_add_u32 %[t0], %[xo], 16512\n\t"
        "s_waitcnt lgkmcnt(6)\n\t"
        "v_mfma_f32_16x16x32_bf16 v[44:47], a[96:99], v[60:63], v[44:47]\n\t"
        "buffer_load_dwordx4 v100, %[sx], %[t0] offen lds\n\t"
        "v_mfma_f32_16x16x32_bf16 v[48:51], a[100:103], v[60:63], v[48:51]\n\t"
        "ds_read_b128 v[56:59], v105 offset:49152\n\t"
        "v_mfma_f32_16x16x32_bf16 v[52:55], v[60:63], a[104:107], v[52:55]\n\t"
        "s_waitcnt lgkmcnt(6)\n\t"
        "v_mfma_f32_16x16x32_bf16 v[32:35], a[108:111], v[64:67], v[32:35]\n\t"
        "v_mfma_f32_16x16x32_bf16 v[36:39], a[112:115], v[64:67], v[36:39]\n\t"
        "ds_read_b128 v[60:63], v105 offset:51200\n\t"
        "v_mfma_f32_16x16x32_bf16 v[40:43], v[64:67], a[116:119], v[40:43]\n\t"
        "s_add_u32 m0, %[ld], 16384\n\t"
        "s_add_u32 %[t0], %[xo], 256\n\t"
        "s_waitcnt lgkmcnt(6)\n\t"
        "v_mfma_f32_16x16x32_bf16 v[44:47], a[108:111], v[68:71], v[44:47]\n\t"
        "buffer_load_dwordx4 v100, %[sx], %[t0] offen lds\n\t"
        "v_mfma_f32_16x16x32_bf16 v[48:51], a[112:115], v[68:71], v[48:51]\n\t"
        "ds_read_b128 v[64:67], v106 offset:49152\n\t"
        "v_mfma_f32_16x16x32_bf16 v[52:55], v[68:71], a[116:119], v[52:55]\n\t"
        "s_waitcnt lgkmcnt(6)\n\t"
        "v_mfma_f32_16x16x32_bf16 v[32:35], a[120:123], v[72:75], v[32:35]\n\t"
        "v_mfma_f32_16x16x32_bf16 v[36:39], a[124:127], v[72:75], v[36:39]\n\t"
        "ds_read_b128 v[68:71], v106 offset:51200\n\t"
        "v_mfma_f32_16x16x32_bf16 v[40:43], v[72:75], a[128:131], v[40:43]\n\t"
        "s_add_u32 m0, %[ld], 17408\n\t"
        "s_add_u32 %[t0], %[xo], 16640\n\t"
        "s_waitcnt lgkmcnt(6)\n\t"
        "v_mfma_f32_16x16x32_bf16 v[44:47], a[120:123], v[76:79], v[44:47]\n\t"
        "buffer_load_dwordx4 v100, %[sx], %[t0] offen lds\n\t"
        "v_mfma_f32_16x16x32_bf16 v[48:51], a[124:127], v[76:79], v[48:51]\n\t"
        "ds_read_b128 v[72:75], v105 offset:57344\n\t"
        "v_mfma_f32_16x16x32_bf16 v[52:55], v[76:79], a[128:131], v[52:55]\n\t"
        "s_waitcnt lgkmcnt(6)\n\t"
        "v_mfma_f32_16x16x32_bf16 v[32:35], a[132:135], v[80:83], v[32:35]\n\t"
        "v_mfma_f32_16x16x32_bf16 v[36:39], a[136:139], v[80:83], v[36:39]\n\t"
        "ds_read_b128 v[76:79], v105 offset:59392\n\t"
        "v_mfma_f32_16x16x32_bf16 v[40:43], v[80:83], a[140:143], v[40:43]\n\t"
        "s_add_u32 m0, %[ld], 24576\n\t"
        "s_add_u32 %[t0], %[xo], 384\n\t"
        "s_waitcnt lgkmcnt(6)\n\t"
        "v_mfma_f32_16x16x32_bf16 v[44:47], a[132:135], v[84:87], v[44:47]\n\t"
        "buffer_load_dwordx4 v100, %[sx], %[t0] offen lds\n\t"
        "v_mfma_f32_16x16x32_bf16 v[48:51], a[136:139], v[84:87], v[48:51]\n\t"
        "ds_read_b128 v[80:83], v106 offset:57344\n\t"
        "v_mfma_f32_16x16x32_bf16 v[52:55], v[84:87], a[140:143], v[52:55]\n\t"
        "s_waitcnt lgkmcnt(6)\n\t"
        "v_mfma_f32_16x16x32_bf16 v[32:35], a[144:147], v[56:59], v[32:35]\n\t"
        "v_mfma_f32_16x16x32_bf16 v[36:39], a[148:151], v[56:59], v[36:39]\n\t"
        "ds_read_b128 v[84:87], v106 offset:59392\n\t"
        "v_mfma_f32_16x16x32_bf16 v[40:43], v[56:59], a[152:155], v[40:43]\n\t"
        "s_add_u32 m0, %[ld], 25600\n\t"
        "s_add_u32 %[t0], %[xo], 16768\n\t"
        "s_waitcnt lgkmcnt(6)\n\t"
        "v_mfma_f32_16x16x32_bf16 v[44:47], a[144:147], v[60:63], v[44:47]\n\t"
        "buffer_load_dwordx4 v100, %[sx], %[t0] offen lds\n\t"
        "v_mfma_f32_16x16x32_bf16 v[48:51], a[148:151], v[60:63], v[48:51]\n\t"
        "v_mfma_f32_16x16x32_bf16 v[52:55], v[60:63], a[152:155], v[52:55]\n\t"
        "s_waitcnt lgkmcnt(5)\n\t"
        "v_mfma_f32_16x16x32_bf16 v[32:35], a[156:159], v[64:67], v[32:35]\n\t"
        "s_waitcnt vmcnt(9) lgkmcnt(0)\n\t"
        "s_barrier\n\t"
        "v_mfma_f32_16x16x32_bf16 v[36:39], a[160:163], v[64:67], v[36:39]\n\t"
        "ds_read_b128 v[56:59], v107 offset:0\n\t"
        "v_mfma_f32_16x16x32_bf16 v[40:43], v[64:67], a[164:167], v[40:43]\n\t"
        "ds_read_b128 v[60:63], v107 offset:2048\n\t"
        "s_add_u32 m0, %[ld], 32768\n\t"
        "s_add_u32 %[t0], %[xo], 512\n\t"
        "v_mfma_f32_16x16x32_bf16 v[44:47], a[156:159], v[68:71], v[44:47]\n\t"
        "buffer_load_dwordx4 v100, %[sx], %[t0] offen lds\n\t"
        "v_mfma_f32_16x16x32_bf16 v[48:51], a[160:163], v[68:71], v[48:51]\n\t"
        "ds_read_b128 v[64:67], v108 offset:0\n\t"
        "v_mfma_f32_16x16x32_bf16 v[52:55], v[68:71], a[164:167], v[52:55]\n\t"
        "v_mfma_f32_16x16x32_bf16 v[32:35], a[168:171], v[72:75], v[32:35]\n\t"
        "v_mfma_f32_16x16x32_bf16 v[36:39], a[172:175], v[72:75], v[36:39]\n\t"
        "ds_read_b128 v[68:71], v108 offset:2048\n\t"
        "v_mov_b32_e32 v119, %[cn]\n\t"
        "buffer_store_dword v119, v113, %[sw], 0 offen sc1\n\t"
        "s_add_u32 %[cn], %[cn], 1\n\t"
        "v_mfma_f32_16x16x32_bf16 v[40:43], v[72:75], a[176:179], v[40:43]\n\t"
        "s_add_u32 m0, %[ld], 33792\n\t"
        "s_add_u32 %[t0], %[xo], 16896\n\t"
        "v_mfma_f32_16x16x32_bf16 v[44:47], a[168:171], v[76:79], v[44:47]\n\t"
        "buffer_load_dwordx4 v100, %[sx], %[t0] offen lds\n\t"
        "v_mfma_f32_16x16x32_bf16 v[48:51], a[172:175], v[76:79], v[48:51]\n\t"
        "ds_read_b128 v[72:75], v107 offset:8192\n\t"
        "v_mfma_f32_16x16x32_bf16 v[52:55], v[76:79], a[176:179], v[52:55]\n\t"
        "v_mfma_f32_16x16x32_bf16 v[32:35], a[180:183], v[80:83], v[32:35]\n\t"
        "v_mfma_f32_16x16x32_bf16 v[36:39], a[184:187], v[80:83], v[36:39]\n\t"
        "ds_read_b128 v[76:79], v107 offset:10240\n\t"
        "v_mfma_f32_16x16x32_bf16 v[40:43], v[80:83], a[188:191], v[40:43]\n\t"
        "s_add_u32 m0, %[ld], 40960\n\t"
        "s_add_u32 %[t0], %[xo], 640\n\t"
        "v_mfma_f32_16x16x32_bf16 v[44:47], a[180:183], v[84:87], v[44:47]\n\t"
        "buffer_load_dwordx4 v100, %[sx], %[t0] offen lds\n\t"
        "v_mfma_f32_16x16x32_bf16 v[48:51], a[184:187], v[84:87], v[48:51]\n\t"
        "ds_read_b128 v[80:83], v108 offset:8192\n\t"
        "v_mfma_f32_16x16x32_bf16 v[52:55], v[84:87], a[188:191], v[52:55]\n\t"
        "s_waitcnt lgkmcnt(6)\n\t"
        "v_mfma_f32_16x16x32_bf16 v[32:35], a[192:195], v[56:59], v[32:35]\n\t"
        "v_mfma_f32_16x16x32_bf16 v[36:39], a[196:199], v[56:59], v[36:39]\n\t"
        "ds_read_b128 v[84:87], v108 offset:10240\n\t"
        "v_mfma_f32_16x16x32_bf16 v[40:43], v[56:59], a[200:203], v[40:43]\n\t"
        "s_add_u32 m0, %[ld], 41984\n\t"
        "s_add_u32 %[t0], %[xo], 17024\n\t"
        "s_waitcnt lgkmcnt(6)\n\t"
        "v_mfma_f32_16x16x32_bf16 v[44:47], a[192:195], v[60:63], v[44:47]\n\t"
        "buffer_load_dwordx4 v100, %[sx], %[t0] offen lds\n\t"
        "v_mfma_f32_16x16x32_bf16 v[48:51], a[196:199], v[60:63], v[48:51]\n\t"
        "ds_read_b128 v[56:59], v107 offset:16384\n\t"
        "v_mfma_f32_16x16x32_bf16 v[52:55], v[60:63], a[200:203], v[52:55]\n\t"
        "s_waitcnt lgkmcnt(6)\n\t"
        "v_mfma_f32_16x16x32_bf16 v[32:35], a[204:207], v[64:67], v[32:35]\n\t"
        "v_mfma_f32_16x16x32_bf16 v[36:39], a[208:211], v[64:67], v[36:39]\n\t"
        "ds_read_b128 v[60:63], v107 offset:18432\n\t"
        "v_mfma_f32_16x16x32_bf16 v[40:43], v[64:67], a[212:215], v[40:43]\n\t"
        "s_add_u32 m0, %[ld], 49152\n\t"
        "s_add_u32 %[t0], %[xo], 768\n\t"
        "s_waitcnt lgkmcnt(6)\n\t"
        "v_mfma_f32_16x16x32_bf16 v[44:47], a[204:207], v[68:71], v[44:47]\n\t"
        "buffer_load_dwordx4 v100, %[sx], %[t0] offen lds\n\t"
        "v_mfma_f32_16x16x32_bf16 v[48:51], a[208:211], v[68:71], v[48:51]\n\t"
        "ds_read_b128 v[64:67], v108 offset:16384\n\t"
        "v_mfma_f32_16x16x32_bf16 v[52:55], v[68:71], a[212:215], v[52:55]\n\t"
        "s_waitcnt lgkmcnt(6)\n\t"
        "v_mfma_f32_16x16x32_bf16 v[32:35], a[216:219], v[72:75], v[32:35]\n\t"
        "v_mfma_f32_16x16x32_bf16 v[36:39], a[220:223], v[72:75], v[36:39]\n\t"
        "ds_read_b128 v[68:71], v108 offset:18432\n\t"
        "v_mfma_f32_16x16x32_bf16 v[40:43], v[72:75], a[224:227], v[40:43]\n\t"
        "s_add_u32 m0, %[ld], 50176\n\t"
        "s_add_u32 %[t0], %[xo], 17152\n\t"
        "s_waitcnt lgkmcnt(6)\n\t"
        "v_mfma_f32_16x16x32_bf16 v[44:47], a[216:219], v[76:79], v[44:47]\n\t"
        "buffer_load_dwordx4 v100, %[sx], %[t0] offen lds\n\t"
        "v_mfma_f32_16x16x32_bf16 v[48:51], a[220:223], v[76:79], v[48:51]\n\t"
        "ds_read_b128 v[72:75], v107 offset:24576\n\t"
        "v_mfma_f32_16x16x32_bf16 v[52:55], v[76:79], a[224:227], v[52:55]\n\t"
        "s_waitcnt lgkmcnt(6)\n\t"
        "v_mfma_f32_16x16x32_bf16 v[32:35], a[228:231], v[80:83], v[32:35]\n\t"
        "v_mfma_f32_16x16x32_bf16 v[36:39], a[232:235], v[80:83], v[36:39]\n\t"
        "ds_read_b128 v[76:79], v107 offset:26624\n\t"
        "v_mfma_f32_16x16x32_bf16 v[40:43], v[80:83], a[236:239], v[40:43]\n\t"
        "s_add_u32 m0, %[ld], 57344\n\t"
        "s_add_u32 %[t0], %[xo], 896\n\t"
        "s_waitcnt lgkmcnt(6)\n\t"
        "v_mfma_f32_16x16x32_bf16 v[44:47], a[228:231], v[84:87], v[44:47]\n\t"
        "buffer_load_dwordx4 v100, %[sx], %[t0] offen lds\n\t"
        "v_mfma_f32_16x16x32_bf16 v[48:51], a[232:235], v[84:87], v[48:51]\n\t"
        "ds_read_b128 v[80:83], v108 offset:24576\n\t"
        "v_mfma_f32_16x16x32_bf16 v[52:55], v[84:87], a[236:239], v[52:55]\n\t"
        "s_waitcnt lgkmcnt(6)\n\t"
        "v_mfma_f32_16x16x32_bf16 v[32:35], a[240:243], v[56:59], v[32:35]\n\t"
        "v_mfma_f32_16x16x32_bf16 v[36:39], a[244:247], v[56:59], v[36:39]\n\t"
        "ds_read_b128 v[84:87], v108 offset:26624\n\t"
        "s_sub_u32 %[t0], %[cn], 2\n\t"
        "s_cmp_lt_u32 %[t0], 24\n\t"
        "s_cselect_b64 %[tp], %[pv], %[ps]\n\t"
        "s_cmp_lt_u32 %[t0], 16\n\t"
        "s_cselect_b64 %[tp], %[pk], %[tp]\n\t"
        "v_mfma_f32_16x16x32_bf16 v[40:43], v[56:59], a[248:251], v[40:43]\n\t"
        "s_add_u32 m0, %[ld], 58368\n\t"
        "s_add_u32 %[t0], %[xo], 17280\n\t"
        "s_waitcnt lgkmcnt(6)\n\t"
        "v_mfma_f32_16x16x32_bf16 v[44:47], a[240:243], v[60:63], v[44:47]\n\t"
        "buffer_load_dwordx4 v100, %[sx], %[t0] offen lds\n\t"
        "v_mfma_f32_16x16x32_bf16 v[48:51], a[244:247], v[60:63], v[48:51]\n\t"
        "v_mfma_f32_16x16x32_bf16 v[52:55], v[60:63], a[248:251], v[52:55]\n\t"
        "s_waitcnt lgkmcnt(5)\n\t"
        "v_mfma_f32_16x16x32_bf16 v[32:35], a[252:255], v[64:67], v[32:35]\n\t"
        "s_waitcnt vmcnt(17) lgkmcnt(0)\n\t"
        "s_barrier\n\t"
        "v_mfma_f32_16x16x32_bf16 v[36:39], v[128:131], v[64:67], v[36:39]\n\t"
        "ds_read_b128 v[56:59], v107 offset:32768\n\t"
        "s_sub_u32 %[t0], %[cn], 2\n\t"
        "s_cmp_lt_u32 %[t0], 8\n\t"
        "s_cselect_b64 %[tp], %[px], %[tp]\n\t"
        "s_bfe_u32 %[t0], %[t0], 0x20001\n\t"
        "s_cmp_eq_u32 %[t0], %[wv]\n\t"
        "s_cselect_b32 %[t0], -1, 0\n\t"
        "v_and_b32_e32 v118, %[t0], v117\n\t"
        "v_mfma_f32_16x16x32_bf16 v[40:43], v[64:67], v[132:135], v[40:43]\n\t"
        "ds_read_b128 v[60:63], v107 offset:34816\n\t"
        "s_add_u32 m0, %[ld], 65536\n\t"
        "s_add_u32 %[t0], %[xo], 1024\n\t"
        "v_mfma_f32_16x16x32_bf16 v[44:47], a[252:255], v[68:71], v[44:47]\n\t"
        "buffer_load_dwordx4 v100, %[sx], %[t0] offen lds\n\t"
        "v_mfma_f32_16x16x32_bf16 v[48:51], v[128:131], v[68:71], v[48:51]\n\t"
        "ds_read_b128 v[64:67], v108 offset:32768\n\t"
        "v_mfma_f32_16x16x32_bf16 v[52:55], v[68:71], v[132:135], v[52:55]\n\t"
        "v_mfma_f32_16x16x32_bf16 v[32:35], v[136:139], v[72:75], v[32:35]\n\t"
        "v_mfma_f32_16x16x32_bf16 v[36:39], v[140:143], v[72:75], v[36:39]\n\t"
        "ds_read_b128 v[68:71], v108 offset:34816\n\t"
        "global_load_dword v118, v118, %[tp]\n\t"
        "v_mfma_f32_16x16x32_bf16 v[40:43], v[72:75], v[144:147], v[40:43]\n\t"
        "s_add_u32 m0, %[ld], 66560\n\t"
        "s_add_u32 %[t0], %[xo], 17408\n\t"
        "v_mfma_f32_16x16x32_bf16 v[44:47], v[136:139], v[76:79], v[44:47]\n\t"
        "buffer_load_dwordx4 v100, %[sx], %[t0] offen lds\n\t"
        "v_mfma_f32_16x16x32_bf16 v[48:51], v[140:143], v[76:79], v[48:51]\n\t"
        "ds_read_b128 v[72:75], v107 offset:40960\n\t"
        "v_mfma_f32_16x16x32_bf16 v[52:55], v[76:79], v[144:147], v[52:55]\n\t"
        "v_mfma_f32_16x16x32_bf16 v[32:35], v[148:151], v[80:83], v[32:35]\n\t"
        "v_mfma_f32_16x16x32_bf16 v[36:39], v[152:155], v[80:83], v[36:39]\n\t"
        "ds_read_b128 v[76:79], v107 offset:43008\n\t"
        "v_mfma_f32_16x16x32_bf16 v[40:43], v[80:83], v[156:159], v[40:43]\n\t"
        "s_add_u32 m0, %[ld], 73728\n\t"
        "s_add_u32 %[t0], %[xo], 1152\n\t"
        "v_mfma_f32_16x16x32_bf16 v[44:47], v[148:151], v[84:87], v[44:47]\n\t"
        "buffer_load_dwordx4 v100, %[sx], %[t0] offen lds\n\t"
        "v_mfma_f32_16x16x32_bf16 v[48:51], v[152:155], v[84:87], v[48:51]\n\t"
        "ds_read_b128 v[80:83], v108 offset:40960\n\t"
        "v_mfma_f32_16x16x32_bf16 v[52:55], v[84:87], v[156:159], v[52:55]\n\t"
        "s_waitcnt lgkmcnt(6)\n\t"
        "v_mfma_f32_16x16x32_bf16 v[32:35], v[160:163], v[56:59], v[32:35]\n\t"
        "v_mfma_f32_16x16x32_bf16 v[36:39], v[164:167], v[56:59], v[36:39]\n\t"
        "ds_read_b128 v[84:87], v108 offset:43008\n\t"
        "v_mfma_f32_16x16x32_bf16 v[40:43], v[56:59], v[168:171], v[40:43]\n\t"
        "s_add_u32 m0, %[ld], 74752\n\t"
        "s_add_u32 %[t0], %[xo], 17536\n\t"
        "s_waitcnt lgkmcnt(6)\n\t"
        "v_mfma_f32_16x16x32_bf16 v[44:47], v[160:163], v[60:63], v[44:47]\n\t"
        "buffer_load_dwordx4 v100, %[sx], %[t0] offen lds\n\t"
        "v_mfma_f32_16x16x32_bf16 v[48:51], v[164:167], v[60:63], v[48:51]\n\t"
        "ds_read_b128 v[56:59], v107 offset:49152\n\t"
        "v_mfma_f32_16x16x32_bf16 v[52:55], v[60:63], v[168:171], v[52:55]\n\t"
        "s_waitcnt lgkmcnt(6)\n\t"
        "v_mfma_f32_16x16x32_bf16 v[32:35], v[172:175], v[64:67], v[32:35]\n\t"
        "v_mfma_f32_16x16x32_bf16 v[36:39], v[176:179], v[64:67], v[36:39]\n\t"
        "ds_read_b128 v[60:63], v107 offset:51200\n\t"
        "v_mfma_f32_16x16x32_bf16 v[40:43], v[64:67], v[180:183], v[40:43]\n\t"
        "s_add_u32 m0, %[ld], 81920\n\t"
        "s_add_u32 %[t0], %[xo], 1280\n\t"
        "s_waitcnt lgkmcnt(6)\n\t"
        "v_mfma_f32_16x16x32_bf16 v[44:47], v[172:175], v[68:71], v[44:47]\n\t"
        "buffer_load_dwordx4 v100, %[sx], %[t0] offen lds\n\t"
        "v_mfma_f32_16x16x32_bf16 v[48:51], v[176:179], v[68:71], v[48:51]\n\t"
        "ds_read_b128 v[64:67], v108 offset:49152\n\t"
        "v_mfma_f32_16x16x32_bf16 v[52:55], v[68:71], v[180:183], v[52:55]\n\t"
        "s_waitcnt lgkmcnt(6)\n\t"
        "v_mfma_f32_16x16x32_bf16 v[32:35], v[184:187], v[72:75], v[32:35]\n\t"
        "v_mfma_f32_16x16x32_bf16 v[36:39], v[188:191], v[72:75], v[36:39]\n\t"
        "ds_read_b128 v[68:71], v108 offset:51200\n\t"
        "v_mfma_f32_16x16x32_bf16 v[40:43], v[72:75], v[192:195], v[40:43]\n\t"
        "s_add_u32 m0, %[ld], 82944\n\t"
        "s_add_u32 %[t0], %[xo], 17664\n\t"
        "s_waitcnt lgkmcnt(6)\n\t"
        "v_mfma_f32_16x16x32_bf16 v[44:47], v[184:187], v[76:79], v[44:47]\n\t"
        "buffer_load_dwordx4 v100, %[sx], %[t0] offen lds\n\t"
        "v_mfma_f32_16x16x32_bf16 v[48:51], v[188:191], v[76:79], v[48:51]\n\t"
        "ds_read_b128 v[72:75], v107 offset:57344\n\t"
        "v_mfma_f32_16x16x32_bf16 v[52:55], v[76:79], v[192:195], v[52:55]\n\t"
        "s_waitcnt lgkmcnt(6)\n\t"
        "v_mfma_f32_16x16x32_bf16 v[32:35], v[196:199], v[80:83], v[32:35]\n\t"
        "v_mfma_f32_16x16x32_bf16 v[36:39], v[200:203], v[80:83], v[36:39]\n\t"
        "ds_read_b128 v[76:79], v107 offset:59392\n\t"
        "v_mfma_f32_16x16x32_bf16 v[40:43], v[80:83], v[204:207], v[40:43]\n\t"
        "s_add_u32 m0, %[ld], 90112\n\t"
        "s_add_u32 %[t0], %[xo], 1408\n\t"
        "s_waitcnt lgkmcnt(6)\n\t"
        "v_mfma_f32_16x16x32_bf16 v[44:47], v[196:199], v[84:87], v[44:47]\n\t"
        "buffer_load_dwordx4 v100, %[sx], %[t0] offen lds\n\t"
        "v_mfma_f32_16x16x32_bf16 v[48:51], v[200:203], v[84:87], v[48:51]\n\t"
        "ds_read_b128 v[80:83], v108 offset:57344\n\t"
        "v_mfma_f32_16x16x32_bf16 v[52:55], v[84:87], v[204:207], v[52:55]\n\t"
        "s_waitcnt lgkmcnt(6)\n\t"
        "v_mfma_f32_16x16x32_bf16 v[32:35], v[208:211], v[56:59], v[32:35]\n\t"
        "v_mfma_f32_16x16x32_bf16 v[36:39], v[212:215], v[56:59], v[36:39]\n\t"
        "ds_read_b128 v[84:87], v108 offset:59392\n\t"
        "v_mfma_f32_16x16x32_bf16 v[40:43], v[56:59], v[216:219], v[40:43]\n\t"
        "s_add_u32 m0, %[ld], 91136\n\t"
        "s_add_u32 %[t0], %[xo], 17792\n\t"
        "s_waitcnt lgkmcnt(6)\n\t"
        "v_mfma_f32_16x16x32_bf16 v[44:47], v[208:211], v[60:63], v[44:47]\n\t"
        "buffer_load_dwordx4 v100, %[sx], %[t0] offen lds\n\t"
        "v_mfma_f32_16x16x32_bf16 v[48:51], v[212:215], v[60:63], v[48:51]\n\t"
        "v_mfma_f32_16x16x32_bf16 v[52:55], v[60:63], v[216:219], v[52:55]\n\t"
        "s_waitcnt lgkmcnt(5)\n\t"
        "v_mfma_f32_16x16x32_bf16 v[32:35], v[220:223], v[64:67], v[32:35]\n\t"
        "s_waitcnt vmcnt(18) lgkmcnt(0)\n\t"
        "s_barrier\n\t"
        "v_mfma_f32_16x16x32_bf16 v[36:39], v[224:227], v[64:67], v[36:39]\n\t"
        "ds_read_b128 v[56:59], v105 offset:0\n\t"
        "v_mfma_f32_16x16x32_bf16 v[40:43], v[64:67], v[228:231], v[40:43]\n\t"
        "ds_read_b128 v[60:63], v105 offset:2048\n\t"
        "s_add_u32 m0, %[ld], 98304\n\t"
        "s_add_u32 %[t0], %[xo], 1536\n\t"
        "v_mfma_f32_16x16x32_bf16 v[44:47], v[220:223], v[68:71], v[44:47]\n\t"
        "buffer_load_dwordx4 v100, %[sx], %[t0] offen lds\n\t"
        "v_mfma_f32_16x16x32_bf16 v[48:51], v[224:227], v[68:71], v[48:51]\n\t"
        "ds_read_b128 v[64:67], v106 offset:0\n\t"
        "v_mfma_f32_16x16x32_bf16 v[52:55], v[68:71], v[228:231], v[52:55]\n\t"
        "v_mfma_f32_16x16x32_bf16 v[32:35], v[232:235], v[72:75], v[32:35]\n\t"
        "v_mfma_f32_16x16x32_bf16 v[36:39], v[236:239], v[72:75], v[36:39]\n\t"
        "ds_read_b128 v[68:71], v106 offset:2048\n\t"
        "v_mfma_f32_16x16x32_bf16 v[40:43], v[72:75], v[240:243], v[40:43]\n\t"
        "s_add_u32 m0, %[ld], 99328\n\t"
        "s_add_u32 %[t0], %[xo], 17920\n\t"
        "v_mfma_f32_16x16x32_bf16 v[44:47], v[232:235], v[76:79], v[44:47]\n\t"
        "buffer_load_dwordx4 v100, %[sx], %[t0] offen lds\n\t"
        "v_mfma_f32_16x16x32_bf16 v[48:51], v[236:239], v[76:79], v[48:51]\n\t"
        "ds_read_b128 v[72:75], v105 offset:8192\n\t"
        "v_mfma_f32_16x16x32_bf16 v[52:55], v[76:79], v[240:243], v[52:55]\n\t"
        "v_mfma_f32_16x16x32_bf16 v[32:35], v[244:247], v[80:83], v[32:35]\n\t"
        "v_mfma_f32_16x16x32_bf16 v[36:39], v[248:251], v[80:83], v[36:39]\n\t"
        "ds_read_b128 v[76:79], v105 offset:10240\n\t"
        "v_mfma_f32_16x16x32_bf16 v[40:43], v[80:83], v[252:255], v[40:43]\n\t"
        "s_add_u32 m0, %[ld], 106496\n\t"
        "s_add_u32 %[t0], %[xo], 1664\n\t"
        "v_mfma_f32_16x16x32_bf16 v[44:47], v[244:247], v[84:87], v[44:47]\n\t"
        "buffer_load_dwordx4 v100, %[sx], %[t0] offen lds\n\t"
        "v_mfma_f32_16x16x32_bf16 v[48:51], v[248:251], v[84:87], v[48:51]\n\t"
        "ds_read_b128 v[80:83], v106 offset:8192\n\t"
        "v_mfma_f32_16x16x32_bf16 v[52:55], v[84:87], v[252:255], v[52:55]\n\t"
        "s_cmp_lt_u32 %[cn], 32\n\t"
        "s_cbranch_scc1 LG_loop%=\n\t"
        "s_nop 7\n\t"
        "v_cvt_pk_bf16_f32 v88, v32, v33\n\t"
        "v_cvt_pk_bf16_f32 v89, v34, v35\n\t"
        "v_cvt_pk_bf16_f32 v90, v36, v37\n\t"
        "v_cvt_pk_bf16_f32 v91, v38, v39\n\t"
        "buffer_store_dwordx4 v[88:91], v109, %[sw], %[so] offen sc1\n\t"
        "v_cvt_pk_bf16_f32 v92, v44, v45\n\t"
        "v_cvt_pk_bf16_f32 v93, v46, v47\n\t"
        "v_cvt_pk_bf16_f32 v94, v48, v49\n\t"
        "v_cvt_pk_bf16_f32 v95, v50, v51\n\t"
        "buffer_store_dwordx4 v[92:95], v110, %[sw], %[so] offen sc1\n\t"
        "v_cvt_pk_bf16_f32 v96, v40, v41\n\t"
        "v_cvt_pk_bf16_f32 v97, v42, v43\n\t"
        "v_cvt_pk_bf16_f32 v98, v52, v53\n\t"
        "v_cvt_pk_bf16_f32 v99, v54, v55\n\t"
        "buffer_store_dwordx4 v[96:99], v111, %[sw], %[so] offen sc1\n\t"
        "s_waitcnt vmcnt(0) lgkmcnt(0)\n\t"
        "v_mov_b32_e32 v119, %[cn]\n\t"
        "buffer_store_dword v119, v113, %[sw], 0 offen sc1\n\t"
        "s_barrier\n\t"
        "s_branch LG_end%=\n\t"
        "LG_first%=:\n\t"
        "s_waitcnt vmcnt(0)\n\t"
        "v_mov_b32_e32 v119, %[cn]\n\t"
        "buffer_store_dword v119, v113, %[sw], 0 offen sc1\n\t"
        "s_branch LG_back%=\n\t"
        "LG_end%=:\n\t"
        ".Lk_end_G:\n\t"
        : [t0] "=&s"(t0), [xo] "=&s"(xo), [so] "=&s"(so), [cn] "=&s"(cn), [tp] "=&s"(tq)
        : [sx] "s"(sx), [sq] "s"(sq), [sv] "s"(sv), [sw] "s"(sw), [ld] "s"(lb + w * 2048), [wv] "s"(w), [px] "s"(p.Aq), [pk] "s"(p.Ak), [pv] "s"(p.Av), [ps] "s"(p.ssm_k),
          "{v117}"((w * 8192 + (h * 4 + c) * 64 + l) * 128),
          "{v100}"((16 * w + (l >> 3)) * 2048 + (((l & 7) ^ (l >> 3)) << 4)), "{v101}"((8 * w + (l >> 3)) * 8192 + h * 256 + (c & 1) * 128 + (l & 7) * 16),
          "{v112}"((16 * w + (l >> 2)) * 8192 + h * 256 + cv * 64 + (l & 3) * 16), "{v102}"(lb + 131072 + w * 1024 + ((l * 16) ^ ((l & 16) << 2))), "{v116}"(lb + 131072 + w * 1024 + ((l * 16) ^ (l & 32))),
          "{v103}"(lr + rr * 128 + ((tn ^ (n >> 3)) * 64) + (n & 3) * 16), "{v104}"(lr + 8192 + rr * 64 + ((tn ^ (g & 1)) * 32) + (n & 3) * 8), "{v105}"(lx), "{v106}"(lx ^ 64u), "{v107}"(lx + 65536), "{v108}"((lx ^ 64u) + 65536),
          "{v109}"(aq), "{v110}"(aq + 4096), "{v111}"(W_VF + (tm * 32 + h) * 8192 + (2 * cv + tn) * 1024 + l * 16), "{v113}"(l == 0 ? F_KVQ + h * 1024 + (c * 4 + w) * 64 : OOB)
        : "memory", "scc", "m0", "v8", "v9", "v10", "v11", "v12", "v13", "v14", "v15", "v16", "v17", "v18", "v19", "v20", "v21", "v22", "v23", "v24", "v25", "v26", "v27", "v28", "v29", "v30", "v31", "v32", "v33", "v34", "v35", "v36", "v37", "v38", "v39", "v40", "v41", "v42", "v43", "v44", "v45", "v46", "v47", "v48", "v49", "v50", "v51", "v52", "v53", "v54", "v55", "v56", "v57", "v58", "v59", "v60", "v61", "v62", "v63", "v64", "v65", "v66", "v67", "v68", "v69", "v70", "v71", "v72", "v73", "v74", "v75", "v76", "v77", "v78", "v79", "v80", "v81", "v82", "v83", "v84", "v85", "v86", "v87", "v88", "v89", "v90", "v91", "v92", "v93", "v94", "v95", "v96", "v97", "v98", "v99", "v118", "v119", "v120", "v121", "v122", "v123", "v124", "v125", "v126", "v127", "v128", "v129", "v130", "v131", "v132", "v133", "v134", "v135", "v136", "v137", "v138", "v139", "v140", "v141", "v142", "v143", "v144", "v145", "v146", "v147", "v148", "v149", "v150", "v151", "v152", "v153", "v154", "v155", "v156", "v157", "v158", "v159", "v160", "v161", "v162", "v163", "v164", "v165", "v166", "v167", "v168", "v169", "v170", "v171", "v172", "v173", "v174", "v175", "v176", "v177", "v178", "v179", "v180", "v181", "v182", "v183", "v184", "v185", "v186", "v187", "v188", "v189", "v190", "v191", "v192", "v193", "v194", "v195", "v196", "v197", "v198", "v199", "v200", "v201", "v202", "v203", "v204", "v205", "v206", "v207", "v208", "v209", "v210", "v211", "v212", "v213", "v214", "v215", "v216", "v217", "v218", "v219", "v220", "v221", "v222", "v223", "v224", "v225", "v226", "v227", "v228", "v229", "v230", "v231", "v232", "v233", "v234", "v235", "v236", "v237", "v238", "v239", "v240", "v241", "v242", "v243", "v244", "v245", "v246", "v247", "v248", "v249", "v250", "v251", "v252", "v253", "v254", "v255", "a0", "a1", "a2", "a3", "a4", "a5", "a6", "a7", "a8", "a9", "a10", "a11", "a12", "a13", "a14", "a15", "a16", "a17", "a18", "a19", "a20", "a21", "a22", "a23", "a24", "a25", "a26", "a27", "a28", "a29", "a30", "a31", "a32", "a33", "a34", "a35", "a36", "a37", "a38", "a39", "a40", "a41", "a42", "a43", "a44", "a45", "a46", "a47", "a48", "a49", "a50", "a51", "a52", "a53", "a54", "a55", "a56", "a57", "a58", "a59", "a60", "a61", "a62", "a63", "a64", "a65", "a66", "a67", "a68", "a69", "a70", "a71", "a72", "a73", "a74", "a75", "a76", "a77", "a78", "a79", "a80", "a81", "a82", "a83", "a84", "a85", "a86", "a87", "a88", "a89", "a90", "a91", "a92", "a93", "a94", "a95", "a96", "a97", "a98", "a99", "a100", "a101", "a102", "a103", "a104", "a105", "a106", "a107", "a108", "a109", "a110", "a111", "a112", "a113", "a114", "a115", "a116", "a117", "a118", "a119", "a120", "a121", "a122", "a123", "a124", "a125", "a126", "a127", "a128", "a129", "a130", "a131", "a132", "a133", "a134", "a135", "a136", "a137", "a138", "a139", "a140", "a141", "a142", "a143", "a144", "a145", "a146", "a147", "a148", "a149", "a150", "a151", "a152", "a153", "a154", "a155", "a156", "a157", "a158", "a159", "a160", "a161", "a162", "a163", "a164", "a165", "a166", "a167", "a168", "a169", "a170", "a171", "a172", "a173", "a174", "a175", "a176", "a177", "a178", "a179", "a180", "a181", "a182", "a183", "a184", "a185", "a186", "a187", "a188", "a189", "a190", "a191", "a192", "a193", "a194", "a195", "a196", "a197", "a198", "a199", "a200", "a201", "a202", "a203", "a204", "a205", "a206", "a207", "a208", "a209", "a210", "a211", "a212", "a213", "a214", "a215", "a216", "a217", "a218", "a219", "a220", "a221", "a222", "a223", "a224", "a225", "a226", "a227", "a228", "a229", "a230", "a231", "a232", "a233", "a234", "a235", "a236", "a237", "a238", "a239", "a240", "a241", "a242", "a243", "a244", "a245", "a246", "a247", "a248", "a249", "a250", "a251", "a252", "a253", "a254", "a255");
}

__device__ __forceinline__ void out_block(const Params& p, unsigned char* lds, const int wv, const int rp, const int nt) {
    const int w = wv, l = otid() & 63, n = l & 15, g = l >> 4;
    const unsigned lb = __builtin_amdgcn_readfirstlane((unsigned)(size_t)lds), stg = lb + 131072 + w * 8192;
    const v4u so = mk_srd(p.Wo, 1024 * 4096 * 2), sw = mk_srd(p.ws, W_TOTAL), su = mk_srd(p.out, S * D * 2);
    const unsigned lr = stg + (4 * g + (n >> 2)) * 128 + ((2 * (g & 1) + (n >> 3)) << 5) + (n & 3) * 8;
    unsigned t0, t1, tl = rp, pg, cu, nx, sp, re;
    asm volatile(
        "s_nop 4\n\t"
        "s_add_u32 %[t1], %[ro], 0\n\t"
        "s_and_b32 %[t1], %[t1], 7\n\t"
        "s_lshl_b32 %[t1], %[t1], 18\n\t"
        "s_add_u32 %[t0], %[t1], 0\n\t"
        "buffer_load_dwordx4 a[0:3], v110, %[so], %[t0] offen\n\t"
        "s_sleep 2\n\t"
        "s_add_u32 %[t0], %[t1], 16384\n\t"
        "buffer_load_dwordx4 a[4:7], v110, %[so], %[t0] offen\n\t"
        "s_sleep 2\n\t"
        "s_add_u32 %[t0], %[t1], 32768\n\t"
        "buffer_load_dwordx4 a[8:11], v110, %[so], %[t0] offen\n\t"
        "s_sleep 2\n\t"
        "s_add_u32 %[t0], %[t1], 49152\n\t"
        "buffer_load_dwordx4 a[12:15], v110, %[so], %[t0] offen\n\t"
        "s_sleep 2\n\t"
        "s_add_u32 %[t0], %[t1], 65536\n\t"
        "buffer_load_dwordx4 a[16:19], v110, %[so], %[t0] offen\n\t"
        "s_sleep 2\n\t"
        "s_add_u32 %[t0], %[t1], 81920\n\t"
        "buffer_load_dwordx4 a[20:23], v110, %[so], %[t0] offen\n\t"
        "s_sleep 2\n\t"
        "s_add_u32 %[t0], %[t1], 98304\n\t"
        "buffer_load_dwordx4 v[24:27], v110, %[so], %[t0] offen\n\t"
        "s_sleep 2\n\t"
        "s_add_u32 %[t0], %[t1], 114688\n\t"
        "buffer_load_dwordx4 v[28:31], v110, %[so], %[t0] offen\n\t"
        "s_sleep 2\n\t"
        "s_add_u32 %[t1], %[ro], 0\n\t"
        "s_and_b32 %[t1], %[t1], 7\n\t"
        "s_lshl_b32 %[t1], %[t1], 18\n\t"
        "s_add_u32 %[t0], %[t1], 131072\n\t"
        "buffer_load_dwordx4 a[24:27], v110, %[so], %[t0] offen\n\t"
        "s_sleep 2\n\t"
        "s_add_u32 %[t0], %[t1], 147456\n\t"
        "buffer_load_dwordx4 a[28:31], v110, %[so], %[t0] offen\n\t"
        "s_sleep 2\n\t"
        "s_add_u32 %[t0], %[t1], 163840\n\t"
        "buffer_load_dwordx4 a[32:35], v110, %[so], %[t0] offen\n\t"
        "s_sleep 2\n\t"
        "s_add_u32 %[t0], %[t1], 180224\n\t"
        "buffer_load_dwordx4 a[36:39], v110, %[so], %[t0] offen\n\t"
        "s_sleep 2\n\t"
        "s_add_u32 %[t0], %[t1], 196608\n\t"
        "buffer_load_dwordx4 a[40:43], v110, %[so], %[t0] offen\n\t"
        "s_sleep 2\n\t"
        "s_add_u32 %[t0], %[t1], 212992\n\t"
        "buffer_load_dwordx4 a[44:47], v110, %[so], %[t0] offen\n\t"
        "s_sleep 2\n\t"
        "s_add_u32 %[t0], %[t1], 229376\n\t"
        "buffer_load_dwordx4 v[32:35], v110, %[so], %[t0] offen\n\t"
        "s_sleep 2\n\t"
        "s_add_u32 %[t0], %[t1], 245760\n\t"
        "buffer_load_dwordx4 v[36:39], v110, %[so], %[t0] offen\n\t"
        "s_sleep 2\n\t"
        "s_waitcnt vmcnt(8)\n\t"
        "ds_write_b128 v111, a[0:3] offset:0\n\t"
        "ds_write_b128 v111, a[4:7] offset:1024\n\t"
        "ds_write_b128 v111, a[8:11] offset:2048\n\t"
        "ds_write_b128 v111, a[12:15] offset:3072\n\t"
        "ds_write_b128 v111, a[16:19] offset:4096\n\t"
        "ds_write_b128 v111, a[20:23] offset:5120\n\t"
        "ds_write_b128 v111, v[24:27] offset:6144\n\t"
        "ds_write_b128 v111, v[28:31] offset:7168\n\t"
        "ds_read_b64_tr_b16 a[0:1], v112 offset:0\n\t"
        "ds_read_b64_tr_b16 a[2:3], v112 offset:2048\n\t"
        "ds_read_b64_tr_b16 a[12:13], v112 offset:4096\n\t"
        "ds_read_b64_tr_b16 a[14:15], v112 offset:6144\n\t"
        "s_waitcnt lgkmcnt(8)\n\t"
        "ds_read_b64_tr_b16 a[4:5], v113 offset:0\n\t"
        "ds_read_b64_tr_b16 a[6:7], v113 offset:2048\n\t"
        "ds_read_b64_tr_b16 a[16:17], v113 offset:4096\n\t"
        "ds_read_b64_tr_b16 a[18:19], v113 offset:6144\n\t"
        "s_waitcnt lgkmcnt(8)\n\t"
        "ds_read_b64_tr_b16 a[8:9], v114 offset:0\n\t"
        "ds_read_b64_tr_b16 a[10:11], v114 offset:2048\n\t"
        "ds_read_b64_tr_b16 a[20:21], v114 offset:4096\n\t"
        "ds_read_b64_tr_b16 a[22:23], v114 offset:6144\n\t"
        "s_waitcnt lgkmcnt(8)\n\t"
        "ds_read_b64_tr_b16 v[56:57], v115 offset:0\n\t"
        "ds_read_b64_tr_b16 v[58:59], v115 offset:2048\n\t"
        "ds_read_b64_tr_b16 v[60:61], v115 offset:4096\n\t"
        "ds_read_b64_tr_b16 v[62:63], v115 offset:6144\n\t"
        "s_add_u32 %[t1], %[ro], 1\n\t"
        "s_and_b32 %[t1], %[t1], 7\n\t"
        "s_lshl_b32 %[t1], %[t1], 18\n\t"
        "s_add_u32 %[t0], %[t1], 0\n\t"
        "buffer_load_dwordx4 a[48:51], v110, %[so], %[t0] offen\n\t"
        "s_sleep 2\n\t"
        "s_add_u32 %[t0], %[t1], 16384\n\t"
        "buffer_load_dwordx4 a[52:55], v110, %[so], %[t0] offen\n\t"
        "s_sleep 2\n\t"
        "s_add_u32 %[t0], %[t1], 32768\n\t"
        "buffer_load_dwordx4 a[56:59], v110, %[so], %[t0] offen\n\t"
        "s_sleep 2\n\t"
        "s_add_u32 %[t0], %[t1], 49152\n\t"
        "buffer_load_dwordx4 a[60:63], v110, %[so], %[t0] offen\n\t"
        "s_sleep 2\n\t"
        "s_add_u32 %[t0], %[t1], 65536\n\t"
        "buffer_load_dwordx4 a[64:67], v110, %[so], %[t0] offen\n\t"
        "s_sleep 2\n\t"
        "s_add_u32 %[t0], %[t1], 81920\n\t"
        "buffer_load_dwordx4 a[68:71], v110, %[so], %[t0] offen\n\t"
        "s_sleep 2\n\t"
        "s_add_u32 %[t0], %[t1], 98304\n\t"
        "buffer_load_dwordx4 v[40:43], v110, %[so], %[t0] offen\n\t"
        "s_sleep 2\n\t"
        "s_add_u32 %[t0], %[t1], 114688\n\t"
        "buffer_load_dwordx4 v[44:47], v110, %[so], %[t0] offen\n\t"
        "s_sleep 2\n\t"
        "s_waitcnt lgkmcnt(0)\n\t"
        "ds_write_b128 v116, v[56:59] offset:0\n\t"
        "ds_write_b128 v116, v[60:63] offset:1024\n\t"
        "s_waitcnt vmcnt(8)\n\t"
        "ds_write_b128 v111, a[24:27] offset:0\n\t"
        "ds_write_b128 v111, a[28:31] offset:1024\n\t"
        "ds_write_b128 v111, a[32:35] offset:2048\n\t"
        "ds_write_b128 v111, a[36:39] offset:3072\n\t"
        "ds_write_b128 v111, a[40:43] offset:4096\n\t"
        "ds_write_b128 v111, a[44:47] offset:5120\n\t"
        "ds_write_b128 v111, v[32:35] offset:6144\n\t"
        "ds_write_b128 v111, v[36:39] offset:7168\n\t"
        "ds_read_b64_tr_b16 a[24:25], v112 offset:0\n\t"
        "ds_read_b64_tr_b16 a[26:27], v112 offset:2048\n\t"
        "ds_read_b64_tr_b16 a[36:37], v112 offset:4096\n\t"
        "ds_read_b64_tr_b16 a[38:39], v112 offset:6144\n\t"
        "s_waitcnt lgkmcnt(8)\n\t"
        "ds_read_b64_tr_b16 a[28:29], v113 offset:0\n\t"
        "ds_read_b64_tr_b16 a[30:31], v113 offset:2048\n\t"
        "ds_read_b64_tr_b16 a[40:41], v113 offset:4096\n\t"
        "ds_read_b64_tr_b16 a[42:43], v113 offset:6144\n\t"
        "s_waitcnt lgkmcnt(8)\n\t"
        "ds_read_b64_tr_b16 a[32:33], v114 offset:0\n\t"
        "ds_read_b64_tr_b16 a[34:35], v114 offset:2048\n\t"
        "ds_read_b64_tr_b16 a[44:45], v114 offset:4096\n\t"
        "ds_read_b64_tr_b16 a[46:47], v114 offset:6144\n\t"
        "s_waitcnt lgkmcnt(8)\n\t"
        "ds_read_b64_tr_b16 v[56:57], v115 offset:0\n\t"
        "ds_read_b64_tr_b16 v[58:59], v115 offset:2048\n\t"
        "ds_read_b64_tr_b16 v[60:61], v115 offset:4096\n\t"
        "ds_read_b64_tr_b16 v[62:63], v115 offset:6144\n\t"
        "s_add_u32 %[t1], %[ro], 1\n\t"
        "s_and_b32 %[t1], %[t1], 7\n\t"
        "s_lshl_b32 %[t1], %[t1], 18\n\t"
        "s_add_u32 %[t0], %[t1], 131072\n\t"
        "buffer_load_dwordx4 a[72:75], v110, %[so], %[t0] offen\n\t"
        "s_sleep 2\n\t"
        "s_add_u32 %[t0], %[t1], 147456\n\t"
        "buffer_load_dwordx4 a[76:79], v110, %[so], %[t0] offen\n\t"
        "s_sleep 2\n\t"
        "s_add_u32 %[t0], %[t1], 163840\n\t"
        "buffer_load_dwordx4 a[80:83], v110, %[so], %[t0] offen\n\t"
        "s_sleep 2\n\t"
        "s_add_u32 %[t0], %[t1], 180224\n\t"
        "buffer_load_dwordx4 a[84:87], v110, %[so], %[t0] offen\n\t"
        "s_sleep 2\n\t"
        "s_add_u32 %[t0], %[t1], 196608\n\t"
        "buffer_load_dwordx4 a[88:91], v110, %[so], %[t0] offen\n\t"
        "s_sleep 2\n\t"
        "s_add_u32 %[t0], %[t1], 212992\n\t"
        "buffer_load_dwordx4 a[92:95], v110, %[so], %[t0] offen\n\t"
        "s_sleep 2\n\t"
        "s_add_u32 %[t0], %[t1], 229376\n\t"
        "buffer_load_dwordx4 v[48:51], v110, %[so], %[t0] offen\n\t"
        "s_sleep 2\n\t"
        "s_add_u32 %[t0], %[t1], 245760\n\t"
        "buffer_load_dwordx4 v[52:55], v110, %[so], %[t0] offen\n\t"
        "s_sleep 2\n\t"
        "s_waitcnt lgkmcnt(0)\n\t"
        "ds_write_b128 v116, v[56:59] offset:2048\n\t"
        "ds_write_b128 v116, v[60:63] offset:3072\n\t"
        "s_waitcnt vmcnt(8)\n\t"
        "ds_write_b128 v111, a[48:51] offset:0\n\t"
        "ds_write_b128 v111, a[52:55] offset:1024\n\t"
        "ds_write_b128 v111, a[56:59] offset:2048\n\t"
        "ds_write_b128 v111, a[60:63] offset:3072\n\t"
        "ds_write_b128 v111, a[64:67] offset:4096\n\t"
        "ds_write_b128 v111, a[68:71] offset:5120\n\t"
        "ds_write_b128 v111, v[40:43] offset:6144\n\t"
        "ds_write_b128 v111, v[44:47] offset:7168\n\t"
        "ds_read_b64_tr_b16 a[48:49], v112 offset:0\n\t"
        "ds_read_b64_tr_b16 a[50:51], v112 offset:2048\n\t"
        "ds_read_b64_tr_b16 a[60:61], v112 offset:4096\n\t"
        "ds_read_b64_tr_b16 a[62:63], v112 offset:6144\n\t"
        "s_waitcnt lgkmcnt(8)\n\t"
        "ds_read_b64_tr_b16 a[52:53], v113 offset:0\n\t"
        "ds_read_b64_tr_b16 a[54:55], v113 offset:2048\n\t"
        "ds_read_b64_tr_b16 a[64:65], v113 offset:4096\n\t"
        "ds_read_b64_tr_b16 a[66:67], v113 offset:6144\n\t"
        "s_waitcnt lgkmcnt(8)\n\t"
        "ds_read_b64_tr_b16 a[56:57], v114 offset:0\n\t"
        "ds_read_b64_tr_b16 a[58:59], v114 offset:2048\n\t"
        "ds_read_b64_tr_b16 a[68:69], v114 offset:4096\n\t"
        "ds_read_b64_tr_b16 a[70:71], v114 offset:6144\n\t"
        "s_waitcnt lgkmcnt(8)\n\t"
        "ds_read_b64_tr_b16 v[56:57], v115 offset:0\n\t"
        "ds_read_b64_tr_b16 v[58:59], v115 offset:2048\n\t"
        "ds_read_b64_tr_b16 v[60:61], v115 offset:4096\n\t"
        "ds_read_b64_tr_b16 v[62:63], v115 offset:6144\n\t"
        "s_add_u32 %[t1], %[ro], 2\n\t"
        "s_and_b32 %[t1], %[t1], 7\n\t"
        "s_lshl_b32 %[t1], %[t1], 18\n\t"
        "s_add_u32 %[t0], %[t1], 0\n\t"
        "buffer_load_dwordx4 a[96:99], v110, %[so], %[t0] offen\n\t"
        "s_sleep 2\n\t"
        "s_add_u32 %[t0], %[t1], 16384\n\t"
        "buffer_load_dwordx4 a[100:103], v110, %[so], %[t0] offen\n\t"
        "s_sleep 2\n\t"
        "s_add_u32 %[t0], %[t1], 32768\n\t"
        "buffer_load_dwordx4 a[104:107], v110, %[so], %[t0] offen\n\t"
        "s_sleep 2\n\t"
        "s_add_u32 %[t0], %[t1], 49152\n\t"
        "buffer_load_dwordx4 a[108:111], v110, %[so], %[t0] offen\n\t"
        "s_sleep 2\n\t"
        "s_add_u32 %[t0], %[t1], 65536\n\t"
        "buffer_load_dwordx4 a[112:115], v110, %[so], %[t0] offen\n\t"
        "s_sleep 2\n\t"
        "s_add_u32 %[t0], %[t1], 81920\n\t"
        "buffer_load_dwordx4 a[116:119], v110, %[so], %[t0] offen\n\t"
        "s_sleep 2\n\t"
        "s_add_u32 %[t0], %[t1], 98304\n\t"
        "buffer_load_dwordx4 v[24:27], v110, %[so], %[t0] offen\n\t"
        "s_sleep 2\n\t"
        "s_add_u32 %[t0], %[t1], 114688\n\t"
        "buffer_load_dwordx4 v[28:31], v110, %[so], %[t0] offen\n\t"
        "s_sleep 2\n\t"
        "s_waitcnt lgkmcnt(0)\n\t"
        "ds_write_b128 v116, v[56:59] offset:4096\n\t"
        "ds_write_b128 v116, v[60:63] offset:5120\n\t"
        "s_waitcnt vmcnt(8)\n\t"
        "ds_write_b128 v111, a[72:75] offset:0\n\t"
        "ds_write_b128 v111, a[76:79] offset:1024\n\t"
        "ds_write_b128 v111, a[80:83] offset:2048\n\t"
        "ds_write_b128 v111, a[84:87] offset:3072\n\t"
        "ds_write_b128 v111, a[88:91] offset:4096\n\t"
        "ds_write_b128 v111, a[92:95] offset:5120\n\t"
        "ds_write_b128 v111, v[48:51] offset:6144\n\t"
        "ds_write_b128 v111, v[52:55] offset:7168\n\t"
        "ds_read_b64_tr_b16 a[72:73], v112 offset:0\n\t"
        "ds_read_b64_tr_b16 a[74:75], v112 offset:2048\n\t"
        "ds_read_b64_tr_b16 a[84:85], v112 offset:4096\n\t"
        "ds_read_b64_tr_b16 a[86:87], v112 offset:6144\n\t"
        "s_waitcnt lgkmcnt(8)\n\t"
        "ds_read_b64_tr_b16 a[76:77], v113 offset:0\n\t"
        "ds_read_b64_tr_b16 a[78:79], v113 offset:2048\n\t"
        "ds_read_b64_tr_b16 a[88:89], v113 offset:4096\n\t"
        "ds_read_b64_tr_b16 a[90:91], v113 offset:6144\n\t"
        "s_waitcnt lgkmcnt(8)\n\t"
        "ds_read_b64_tr_b16 a[80:81], v114 offset:0\n\t"
        "ds_read_b64_tr_b16 a[82:83], v114 offset:2048\n\t"
        "ds_read_b64_tr_b16 a[92:93], v114 offset:4096\n\t"
        "ds_read_b64_tr_b16 a[94:95], v114 offset:6144\n\t"
        "s_waitcnt lgkmcnt(8)\n\t"
        "ds_read_b64_tr_b16 v[56:57], v115 offset:0\n\t"
        "ds_read_b64_tr_b16 v[58:59], v115 offset:2048\n\t"
        "ds_read_b64_tr_b16 v[60:61], v115 offset:4096\n\t"
        "ds_read_b64_tr_b16 v[62:63], v115 offset:6144\n\t"
        "s_add_u32 %[t1], %[ro], 2\n\t"
        "s_and_b32 %[t1], %[t1], 7\n\t"
        "s_lshl_b32 %[t1], %[t1], 18\n\t"
        "s_add_u32 %[t0], %[t1], 131072\n\t"
        "buffer_load_dwordx4 a[120:123], v110, %[so], %[t0] offen\n\t"
        "s_sleep 2\n\t"
        "s_add_u32 %[t0], %[t1], 147456\n\t"
        "buffer_load_dwordx4 a[124:127], v110, %[so], %[t0] offen\n\t"
        "s_sleep 2\n\t"
        "s_add_u32 %[t0], %[t1], 163840\n\t"
        "buffer_load_dwordx4 a[128:131], v110, %[so], %[t0] offen\n\t"
        "s_sleep 2\n\t"
        "s_add_u32 %[t0], %[t1], 180224\n\t"
        "buffer_load_dwordx4 a[132:135], v110, %[so], %[t0] offen\n\t"
        "s_sleep 2\n\t"
        "s_add_u32 %[t0], %[t1], 196608\n\t"
        "buffer_load_dwordx4 a[136:139], v110, %[so], %[t0] offen\n\t"
        "s_sleep 2\n\t"
        "s_add_u32 %[t0], %[t1], 212992\n\t"
        "buffer_load_dwordx4 a[140:143], v110, %[so], %[t0] offen\n\t"
        "s_sleep 2\n\t"
        "s_add_u32 %[t0], %[t1], 229376\n\t"
        "buffer_load_dwordx4 v[32:35], v110, %[so], %[t0] offen\n\t"
        "s_sleep 2\n\t"
        "s_add_u32 %[t0], %[t1], 245760\n\t"
        "buffer_load_dwordx4 v[36:39], v110, %[so], %[t0] offen\n\t"
        "s_sleep 2\n\t"
        "s_waitcnt lgkmcnt(0)\n\t"
        "ds_write_b128 v116, v[56:59] offset:6144\n\t"
        "ds_write_b128 v116, v[60:63] offset:7168\n\t"
        "s_waitcnt vmcnt(8)\n\t"
        "ds_write_b128 v111, a[96:99] offset:0\n\t"
        "ds_write_b128 v111, a[100:103] offset:1024\n\t"
        "ds_write_b128 v111, a[104:107] offset:2048\n\t"
        "ds_write_b128 v111, a[108:111] offset:3072\n\t"
        "ds_write_b128 v111, a[112:115] offset:4096\n\t"
        "ds_write_b128 v111, a[116:119] offset:5120\n\t"
        "ds_write_b128 v111, v[24:27] offset:6144\n\t"
        "ds_write_b128 v111, v[28:31] offset:7168\n\t"
        "ds_read_b64_tr_b16 a[96:97], v112 offset:0\n\t"
        "ds_read_b64_tr_b16 a[98:99], v112 offset:2048\n\t"
        "ds_read_b64_tr_b16 a[108:109], v112 offset:4096\n\t"
        "ds_read_b64_tr_b16 a[110:111], v112 offset:6144\n\t"
        "s_waitcnt lgkmcnt(8)\n\t"
        "ds_read_b64_tr_b16 a[100:101], v113 offset:0\n\t"
        "ds_read_b64_tr_b16 a[102:103], v113 offset:2048\n\t"
        "ds_read_b64_tr_b16 a[112:113], v113 offset:4096\n\t"
        "ds_read_b64_tr_b16 a[114:115], v113 offset:6144\n\t"
        "s_waitcnt lgkmcnt(8)\n\t"
        "ds_read_b64_tr_b16 a[104:105], v114 offset:0\n\t"
        "ds_read_b64_tr_b16 a[106:107], v114 offset:2048\n\t"
        "ds_read_b64_tr_b16 a[116:117], v114 offset:4096\n\t"
        "ds_read_b64_tr_b16 a[118:119], v114 offset:6144\n\t"
        "s_waitcnt lgkmcnt(8)\n\t"
        "ds_read_b64_tr_b16 v[56:57], v115 offset:0\n\t"
        "ds_read_b64_tr_b16 v[58:59], v115 offset:2048\n\t"
        "ds_read_b64_tr_b16 v[60:61], v115 offset:4096\n\t"
        "ds_read_b64_tr_b16 v[62:63], v115 offset:6144\n\t"
        "s_add_u32 %[t1], %[ro], 3\n\t"
        "s_and_b32 %[t1], %[t1], 7\n\t"
        "s_lshl_b32 %[t1], %[t1], 18\n\t"
        "s_add_u32 %[t0], %[t1], 0\n\t"
        "buffer_load_dwordx4 a[144:147], v110, %[so], %[t0] offen\n\t"
        "s_sleep 2\n\t"
        "s_add_u32 %[t0], %[t1], 16384\n\t"
        "buffer_load_dwordx4 a[148:151], v110, %[so], %[t0] offen\n\t"
        "s_sleep 2\n\t"
        "s_add_u32 %[t0], %[t1], 32768\n\t"
        "buffer_load_dwordx4 a[152:155], v110, %[so], %[t0] offen\n\t"
        "s_sleep 2\n\t"
        "s_add_u32 %[t0], %[t1], 49152\n\t"
        "buffer_load_dwordx4 a[156:159], v110, %[so], %[t0] offen\n\t"
        "s_sleep 2\n\t"
        "s_add_u32 %[t0], %[t1], 65536\n\t"
        "buffer_load_dwordx4 a[160:163], v110, %[so], %[t0] offen\n\t"
        "s_sleep 2\n\t"
        "s_add_u32 %[t0], %[t1], 81920\n\t"
        "buffer_load_dwordx4 a[164:167], v110, %[so], %[t0] offen\n\t"
        "s_sleep 2\n\t"
        "s_add_u32 %[t0], %[t1], 98304\n\t"
        "buffer_load_dwordx4 v[40:43], v110, %[so], %[t0] offen\n\t"
        "s_sleep 2\n\t"
        "s_add_u32 %[t0], %[t1], 114688\n\t"
        "buffer_load_dwordx4 v[44:47], v110, %[so], %[t0] offen\n\t"
        "s_sleep 2\n\t"
        "s_waitcnt lgkmcnt(0)\n\t"
        "ds_write_b128 v116, v[56:59] offset:8192\n\t"
        "ds_write_b128 v116, v[60:63] offset:9216\n\t"
        "s_waitcnt vmcnt(8)\n\t"
        "ds_write_b128 v111, a[120:123] offset:0\n\t"
        "ds_write_b128 v111, a[124:127] offset:1024\n\t"
        "ds_write_b128 v111, a[128:131] offset:2048\n\t"
        "ds_write_b128 v111, a[132:135] offset:3072\n\t"
        "ds_write_b128 v111, a[136:139] offset:4096\n\t"
        "ds_write_b128 v111, a[140:143] offset:5120\n\t"
        "ds_write_b128 v111, v[32:35] offset:6144\n\t"
        "ds_write_b128 v111, v[36:39] offset:7168\n\t"
        "ds_read_b64_tr_b16 a[120:121], v112 offset:0\n\t"
        "ds_read_b64_tr_b16 a[122:123], v112 offset:2048\n\t"
        "ds_read_b64_tr_b16 a[132:133], v112 offset:4096\n\t"
        "ds_read_b64_tr_b16 a[134:135], v112 offset:6144\n\t"
        "s_waitcnt lgkmcnt(8)\n\t"
        "ds_read_b64_tr_b16 a[124:125], v113 offset:0\n\t"
        "ds_read_b64_tr_b16 a[126:127], v113 offset:2048\n\t"
        "ds_read_b64_tr_b16 a[136:137], v113 offset:4096\n\t"
        "ds_read_b64_tr_b16 a[138:139], v113 offset:6144\n\t"
        "s_waitcnt lgkmcnt(8)\n\t"
        "ds_read_b64_tr_b16 a[128:129], v114 offset:0\n\t"
        "ds_read_b64_tr_b16 a[130:131], v114 offset:2048\n\t"
        "ds_read_b64_tr_b16 a[140:141], v114 offset:4096\n\t"
        "ds_read_b64_tr_b16 a[142:143], v114 offset:6144\n\t"
        "s_waitcnt lgkmcnt(8)\n\t"
        "ds_read_b64_tr_b16 v[56:57], v115 offset:0\n\t"
        "ds_read_b64_tr_b16 v[58:59], v115 offset:2048\n\t"
        "ds_read_b64_tr_b16 v[60:61], v115 offset:4096\n\t"
        "ds_read_b64_tr_b16 v[62:63], v115 offset:6144\n\t"
        "s_add_u32 %[t1], %[ro], 3\n\t"
        "s_and_b32 %[t1], %[t1], 7\n\t"
        "s_lshl_b32 %[t1], %[t1], 18\n\t"
        "s_add_u32 %[t0], %[t1], 131072\n\t"
        "buffer_load_dwordx4 a[168:171], v110, %[so], %[t0] offen\n\t"
        "s_sleep 2\n\t"
        "s_add_u32 %[t0], %[t1], 147456\n\t"
        "buffer_load_dwordx4 a[172:175], v110, %[so], %[t0] offen\n\t"
        "s_sleep 2\n\t"
        "s_add_u32 %[t0], %[t1], 163840\n\t"
        "buffer_load_dwordx4 a[176:179], v110, %[so], %[t0] offen\n\t"
        "s_sleep 2\n\t"
        "s_add_u32 %[t0], %[t1], 180224\n\t"
        "buffer_load_dwordx4 a[180:183], v110, %[so], %[t0] offen\n\t"
        "s_sleep 2\n\t"
        "s_add_u32 %[t0], %[t1], 196608\n\t"
        "buffer_load_dwordx4 a[184:187], v110, %[so], %[t0] offen\n\t"
        "s_sleep 2\n\t"
        "s_add_u32 %[t0], %[t1], 212992\n\t"
        "buffer_load_dwordx4 a[188:191], v110, %[so], %[t0] offen\n\t"
        "s_sleep 2\n\t"
        "s_add_u32 %[t0], %[t1], 229376\n\t"
        "buffer_load_dwordx4 v[48:51], v110, %[so], %[t0] offen\n\t"
        "s_sleep 2\n\t"
        "s_add_u32 %[t0], %[t1], 245760\n\t"
        "buffer_load_dwordx4 v[52:55], v110, %[so], %[t0] offen\n\t"
        "s_sleep 2\n\t"
        "s_waitcnt lgkmcnt(0)\n\t"
        "ds_write_b128 v116, v[56:59] offset:10240\n\t"
        "ds_write_b128 v116, v[60:63] offset:11264\n\t"
        "s_waitcnt vmcnt(8)\n\t"
        "ds_write_b128 v111, a[144:147] offset:0\n\t"
        "ds_write_b128 v111, a[148:151] offset:1024\n\t"
        "ds_write_b128 v111, a[152:155] offset:2048\n\t"
        "ds_write_b128 v111, a[156:159] offset:3072\n\t"
        "ds_write_b128 v111, a[160:163] offset:4096\n\t"
        "ds_write_b128 v111, a[164:167] offset:5120\n\t"
        "ds_write_b128 v111, v[40:43] offset:6144\n\t"
        "ds_write_b128 v111, v[44:47] offset:7168\n\t"
        "ds_read_b64_tr_b16 a[144:145], v112 offset:0\n\t"
        "ds_read_b64_tr_b16 a[146:147], v112 offset:2048\n\t"
        "ds_read_b64_tr_b16 a[156:157], v112 offset:4096\n\t"
        "ds_read_b64_tr_b16 a[158:159], v112 offset:6144\n\t"
        "s_waitcnt lgkmcnt(8)\n\t"
        "ds_read_b64_tr_b16 a[148:149], v113 offset:0\n\t"
        "ds_read_b64_tr_b16 a[150:151], v113 offset:2048\n\t"
        "ds_read_b64_tr_b16 a[160:161], v113 offset:4096\n\t"
        "ds_read_b64_tr_b16 a[162:163], v113 offset:6144\n\t"
        "s_waitcnt lgkmcnt(8)\n\t"
        "ds_read_b64_tr_b16 a[152:153], v114 offset:0\n\t"
        "ds_read_b64_tr_b16 a[154:155], v114 offset:2048\n\t"
        "ds_read_b64_tr_b16 a[164:165], v114 offset:4096\n\t"
        "ds_read_b64_tr_b16 a[166:167], v114 offset:6144\n\t"
        "s_waitcnt lgkmcnt(8)\n\t"
        "ds_read_b64_tr_b16 v[56:57], v115 offset:0\n\t"
        "ds_read_b64_tr_b16 v[58:59], v115 offset:2048\n\t"
        "ds_read_b64_tr_b16 v[60:61], v115 offset:4096\n\t"
        "ds_read_b64_tr_b16 v[62:63], v115 offset:6144\n\t"
        "s_add_u32 %[t1], %[ro], 4\n\t"
        "s_and_b32 %[t1], %[t1], 7\n\t"
        "s_lshl_b32 %[t1], %[t1], 18\n\t"
        "s_add_u32 %[t0], %[t1], 0\n\t"
        "buffer_load_dwordx4 a[192:195], v110, %[so], %[t0] offen\n\t"
        "s_sleep 2\n\t"
        "s_add_u32 %[t0], %[t1], 16384\n\t"
        "buffer_load_dwordx4 a[196:199], v110, %[so], %[t0] offen\n\t"
        "s_sleep 2\n\t"
        "s_add_u32 %[t0], %[t1], 32768\n\t"
        "buffer_load_dwordx4 a[200:203], v110, %[so], %[t0] offen\n\t"
        "s_sleep 2\n\t"
        "s_add_u32 %[t0], %[t1], 49152\n\t"
        "buffer_load_dwordx4 a[204:207], v110, %[so], %[t0] offen\n\t"
        "s_sleep 2\n\t"
        "s_add_u32 %[t0], %[t1], 65536\n\t"
        "buffer_load_dwordx4 a[208:211], v110, %[so], %[t0] offen\n\t"
        "s_sleep 2\n\t"
        "s_add_u32 %[t0], %[t1], 81920\n\t"
        "buffer_load_dwordx4 a[212:215], v110, %[so], %[t0] offen\n\t"
        "s_sleep 2\n\t"
        "s_add_u32 %[t0], %[t1], 98304\n\t"
        "buffer_load_dwordx4 v[24:27], v110, %[so], %[t0] offen\n\t"
        "s_sleep 2\n\t"
        "s_add_u32 %[t0], %[t1], 114688\n\t"
        "buffer_load_dwordx4 v[28:31], v110, %[so], %[t0] offen\n\t"
        "s_sleep 2\n\t"
        "s_waitcnt lgkmcnt(0)\n\t"
        "ds_write_b128 v116, v[56:59] offset:12288\n\t"
        "ds_write_b128 v116, v[60:63] offset:13312\n\t"
        "s_waitcnt vmcnt(8)\n\t"
        "ds_write_b128 v111, a[168:171] offset:0\n\t"
        "ds_write_b128 v111, a[172:175] offset:1024\n\t"
        "ds_write_b128 v111, a[176:179] offset:2048\n\t"
        "ds_write_b128 v111, a[180:183] offset:3072\n\t"
        "ds_write_b128 v111, a[184:187] offset:4096\n\t"
        "ds_write_b128 v111, a[188:191] offset:5120\n\t"
        "ds_write_b128 v111, v[48:51] offset:6144\n\t"
        "ds_write_b128 v111, v[52:55] offset:7168\n\t"
        "ds_read_b64_tr_b16 a[168:169], v112 offset:0\n\t"
        "ds_read_b64_tr_b16 a[170:171], v112 offset:2048\n\t"
        "ds_read_b64_tr_b16 a[180:181], v112 offset:4096\n\t"
        "ds_read_b64_tr_b16 a[182:183], v112 offset:6144\n\t"
        "s_waitcnt lgkmcnt(8)\n\t"
        "ds_read_b64_tr_b16 a[172:173], v113 offset:0\n\t"
        "ds_read_b64_tr_b16 a[174:175], v113 offset:2048\n\t"
        "ds_read_b64_tr_b16 a[184:185], v113 offset:4096\n\t"
        "ds_read_b64_tr_b16 a[186:187], v113 offset:6144\n\t"
        "s_waitcnt lgkmcnt(8)\n\t"
        "ds_read_b64_tr_b16 a[176:177], v114 offset:0\n\t"
        "ds_read_b64_tr_b16 a[178:179], v114 offset:2048\n\t"
        "ds_read_b64_tr_b16 a[188:189], v114 offset:4096\n\t"
        "ds_read_b64_tr_b16 a[190:191], v114 offset:6144\n\t"
        "s_waitcnt lgkmcnt(8)\n\t"
        "ds_read_b64_tr_b16 v[56:57], v115 offset:0\n\t"
        "ds_read_b64_tr_b16 v[58:59], v115 offset:2048\n\t"
        "ds_read_b64_tr_b16 v[60:61], v115 offset:4096\n\t"
        "ds_read_b64_tr_b16 v[62:63], v115 offset:6144\n\t"
        "s_add_u32 %[t1], %[ro], 4\n\t"
        "s_and_b32 %[t1], %[t1], 7\n\t"
        "s_lshl_b32 %[t1], %[t1], 18\n\t"
        "s_add_u32 %[t0], %[t1], 131072\n\t"
        "buffer_load_dwordx4 a[216:219], v110, %[so], %[t0] offen\n\t"
        "s_sleep 2\n\t"
        "s_add_u32 %[t0], %[t1], 147456\n\t"
        "buffer_load_dwordx4 a[220:223], v110, %[so], %[t0] offen\n\t"
        "s_sleep 2\n\t"
        "s_add_u32 %[t0], %[t1], 163840\n\t"
        "buffer_load_dwordx4 a[224:227], v110, %[so], %[t0] offen\n\t"
        "s_sleep 2\n\t"
        "s_add_u32 %[t0], %[t1], 180224\n\t"
        "buffer_load_dwordx4 a[228:231], v110, %[so], %[t0] offen\n\t"
        "s_sleep 2\n\t"
        "s_add_u32 %[t0], %[t1], 196608\n\t"
        "buffer_load_dwordx4 a[232:235], v110, %[so], %[t0] offen\n\t"
        "s_sleep 2\n\t"
        "s_add_u32 %[t0], %[t1], 212992\n\t"
        "buffer_load_dwordx4 a[236:239], v110, %[so], %[t0] offen\n\t"
        "s_sleep 2\n\t"
        "s_add_u32 %[t0], %[t1], 229376\n\t"
        "buffer_load_dwordx4 v[32:35], v110, %[so], %[t0] offen\n\t"
        "s_sleep 2\n\t"
        "s_add_u32 %[t0], %[t1], 245760\n\t"
        "buffer_load_dwordx4 v[36:39], v110, %[so], %[t0] offen\n\t"
        "s_sleep 2\n\t"
        "s_waitcnt lgkmcnt(0)\n\t"
        "ds_write_b128 v116, v[56:59] offset:14336\n\t"
        "ds_write_b128 v116, v[60:63] offset:15360\n\t"
        "s_waitcnt vmcnt(8)\n\t"
        "ds_write_b128 v111, a[192:195] offset:0\n\t"
        "ds_write_b128 v111, a[196:199] offset:1024\n\t"
        "ds_write_b128 v111, a[200:203] offset:2048\n\t"
        "ds_write_b128 v111, a[204:207] offset:3072\n\t"
        "ds_write_b128 v111, a[208:211] offset:4096\n\t"
        "ds_write_b128 v111, a[212:215] offset:5120\n\t"
        "ds_write_b128 v111, v[24:27] offset:6144\n\t"
        "ds_write_b128 v111, v[28:31] offset:7168\n\t"
        "ds_read_b64_tr_b16 a[192:193], v112 offset:0\n\t"
        "ds_read_b64_tr_b16 a[194:195], v112 offset:2048\n\t"
        "ds_read_b64_tr_b16 a[204:205], v112 offset:4096\n\t"
        "ds_read_b64_tr_b16 a[206:207], v112 offset:6144\n\t"
        "s_waitcnt lgkmcnt(8)\n\t"
        "ds_read_b64_tr_b16 a[196:197], v113 offset:0\n\t"
        "ds_read_b64_tr_b16 a[198:199], v113 offset:2048\n\t"
        "ds_read_b64_tr_b16 a[208:209], v113 offset:4096\n\t"
        "ds_read_b64_tr_b16 a[210:211], v113 offset:6144\n\t"
        "s_waitcnt lgkmcnt(8)\n\t"
        "ds_read_b64_tr_b16 a[200:201], v114 offset:0\n\t"
        "ds_read_b64_tr_b16 a[202:203], v114 offset:2048\n\t"
        "ds_read_b64_tr_b16 a[212:213], v114 offset:4096\n\t"
        "ds_read_b64_tr_b16 a[214:215], v114 offset:6144\n\t"
        "s_waitcnt lgkmcnt(8)\n\t"
        "ds_read_b64_tr_b16 v[56:57], v115 offset:0\n\t"
        "ds_read_b64_tr_b16 v[58:59], v115 offset:2048\n\t"
        "ds_read_b64_tr_b16 v[60:61], v115 offset:4096\n\t"
        "ds_read_b64_tr_b16 v[62:63], v115 offset:6144\n\t"
        "s_add_u32 %[t1], %[ro], 5\n\t"
        "s_and_b32 %[t1], %[t1], 7\n\t"
        "s_lshl_b32 %[t1], %[t1], 18\n\t"
        "s_add_u32 %[t0], %[t1], 0\n\t"
        "buffer_load_dwordx4 a[240:243], v110, %[so], %[t0] offen\n\t"
        "s_sleep 2\n\t"
        "s_add_u32 %[t0], %[t1], 16384\n\t"
        "buffer_load_dwordx4 a[244:247], v110, %[so], %[t0] offen\n\t"
        "s_sleep 2\n\t"
        "s_add_u32 %[t0], %[t1], 32768\n\t"
        "buffer_load_dwordx4 a[248:251], v110, %[so], %[t0] offen\n\t"
        "s_sleep 2\n\t"
        "s_add_u32 %[t0], %[t1], 49152\n\t"
        "buffer_load_dwordx4 a[252:255], v110, %[so], %[t0] offen\n\t"
        "s_sleep 2\n\t"
        "s_add_u32 %[t0], %[t1], 65536\n\t"
        "buffer_load_dwordx4 v[128:131], v110, %[so], %[t0] offen\n\t"
        "s_sleep 2\n\t"
        "s_add_u32 %[t0], %[t1], 81920\n\t"
        "buffer_load_dwordx4 v[132:135], v110, %[so], %[t0] offen\n\t"
        "s_sleep 2\n\t"
        "s_add_u32 %[t0], %[t1], 98304\n\t"
        "buffer_load_dwordx4 v[40:43], v110, %[so], %[t0] offen\n\t"
        "s_sleep 2\n\t"
        "s_add_u32 %[t0], %[t1], 114688\n\t"
        "buffer_load_dwordx4 v[44:47], v110, %[so], %[t0] offen\n\t"
        "s_sleep 2\n\t"
        "s_waitcnt lgkmcnt(0)\n\t"
        "ds_write_b128 v116, v[56:59] offset:16384\n\t"
        "ds_write_b128 v116, v[60:63] offset:17408\n\t"
        "s_waitcnt vmcnt(8)\n\t"
        "ds_write_b128 v111, a[216:219] offset:0\n\t"
        "ds_write_b128 v111, a[220:223] offset:1024\n\t"
        "ds_write_b128 v111, a[224:227] offset:2048\n\t"
        "ds_write_b128 v111, a[228:231] offset:3072\n\t"
        "ds_write_b128 v111, a[232:235] offset:4096\n\t"
        "ds_write_b128 v111, a[236:239] offset:5120\n\t"
        "ds_write_b128 v111, v[32:35] offset:6144\n\t"
        "ds_write_b128 v111, v[36:39] offset:7168\n\t"
        "ds_read_b64_tr_b16 a[216:217], v112 offset:0\n\t"
        "ds_read_b64_tr_b16 a[218:219], v112 offset:2048\n\t"
        "ds_read_b64_tr_b16 a[228:229], v112 offset:4096\n\t"
        "ds_read_b64_tr_b16 a[230:231], v112 offset:6144\n\t"
        "s_waitcnt lgkmcnt(8)\n\t"
        "ds_read_b64_tr_b16 a[220:221], v113 offset:0\n\t"
        "ds_read_b64_tr_b16 a[222:223], v113 offset:2048\n\t"
        "ds_read_b64_tr_b16 a[232:233], v113 offset:4096\n\t"
        "ds_read_b64_tr_b16 a[234:235], v113 offset:6144\n\t"
        "s_waitcnt lgkmcnt(8)\n\t"
        "ds_read_b64_tr_b16 a[224:225], v114 offset:0\n\t"
        "ds_read_b64_tr_b16 a[226:227], v114 offset:2048\n\t"
        "ds_read_b64_tr_b16 a[236:237], v114 offset:4096\n\t"
        "ds_read_b64_tr_b16 a[238:239], v114 offset:6144\n\t"
        "s_waitcnt lgkmcnt(8)\n\t"
        "ds_read_b64_tr_b16 v[56:57], v115 offset:0\n\t"
        "ds_read_b64_tr_b16 v[58:59], v115 offset:2048\n\t"
        "ds_read_b64_tr_b16 v[60:61], v115 offset:4096\n\t"
        "ds_read_b64_tr_b16 v[62:63], v115 offset:6144\n\t"
        "s_add_u32 %[t1], %[ro], 5\n\t"
        "s_and_b32 %[t1], %[t1], 7\n\t"
        "s_lshl_b32 %[t1], %[t1], 18\n\t"
        "s_add_u32 %[t0], %[t1], 131072\n\t"
        "buffer_load_dwordx4 v[136:139], v110, %[so], %[t0] offen\n\t"
        "s_sleep 2\n\t"
        "s_add_u32 %[t0], %[t1], 147456\n\t"
        "buffer_load_dwordx4 v[140:143], v110, %[so], %[t0] offen\n\t"
        "s_sleep 2\n\t"
        "s_add_u32 %[t0], %[t1], 163840\n\t"
        "buffer_load_dwordx4 v[144:147], v110, %[so], %[t0] offen\n\t"
        "s_sleep 2\n\t"
        "s_add_u32 %[t0], %[t1], 180224\n\t"
        "buffer_load_dwordx4 v[148:151], v110, %[so], %[t0] offen\n\t"
        "s_sleep 2\n\t"
        "s_add_u32 %[t0], %[t1], 196608\n\t"
        "buffer_load_dwordx4 v[152:155], v110, %[so], %[t0] offen\n\t"
        "s_sleep 2\n\t"
        "s_add_u32 %[t0], %[t1], 212992\n\t"
        "buffer_load_dwordx4 v[156:159], v110, %[so], %[t0] offen\n\t"
        "s_sleep 2\n\t"
        "s_add_u32 %[t0], %[t1], 229376\n\t"
        "buffer_load_dwordx4 v[48:51], v110, %[so], %[t0] offen\n\t"
        "s_sleep 2\n\t"
        "s_add_u32 %[t0], %[t1], 245760\n\t"
        "buffer_load_dwordx4 v[52:55], v110, %[so], %[t0] offen\n\t"
        "s_sleep 2\n\t"
        "s_waitcnt lgkmcnt(0)\n\t"
        "ds_write_b128 v116, v[56:59] offset:18432\n\t"
        "ds_write_b128 v116, v[60:63] offset:19456\n\t"
        "s_waitcnt vmcnt(8)\n\t"
        "ds_write_b128 v111, a[240:243] offset:0\n\t"
        "ds_write_b128 v111, a[244:247] offset:1024\n\t"
        "ds_write_b128 v111, a[248:251] offset:2048\n\t"
        "ds_write_b128 v111, a[252:255] offset:3072\n\t"
        "ds_write_b128 v111, v[128:131] offset:4096\n\t"
        "ds_write_b128 v111, v[132:135] offset:5120\n\t"
        "ds_write_b128 v111, v[40:43] offset:6144\n\t"
        "ds_write_b128 v111, v[44:47] offset:7168\n\t"
        "ds_read_b64_tr_b16 a[240:241], v112 offset:0\n\t"
        "ds_read_b64_tr_b16 a[242:243], v112 offset:2048\n\t"
        "ds_read_b64_tr_b16 a[252:253], v112 offset:4096\n\t"
        "ds_read_b64_tr_b16 a[254:255], v112 offset:6144\n\t"
        "s_waitcnt lgkmcnt(8)\n\t"
        "ds_read_b64_tr_b16 a[244:245], v113 offset:0\n\t"
        "ds_read_b64_tr_b16 a[246:247], v113 offset:2048\n\t"
        "ds_read_b64_tr_b16 v[128:129], v113 offset:4096\n\t"
        "ds_read_b64_tr_b16 v[130:131], v113 offset:6144\n\t"
        "s_waitcnt lgkmcnt(8)\n\t"
        "ds_read_b64_tr_b16 a[248:249], v114 offset:0\n\t"
        "ds_read_b64_tr_b16 a[250:251], v114 offset:2048\n\t"
        "ds_read_b64_tr_b16 v[132:133], v114 offset:4096\n\t"
        "ds_read_b64_tr_b16 v[134:135], v114 offset:6144\n\t"
        "s_waitcnt lgkmcnt(8)\n\t"
        "ds_read_b64_tr_b16 v[56:57], v115 offset:0\n\t"
        "ds_read_b64_tr_b16 v[58:59], v115 offset:2048\n\t"
        "ds_read_b64_tr_b16 v[60:61], v115 offset:4096\n\t"
        "ds_read_b64_tr_b16 v[62:63], v115 offset:6144\n\t"
        "s_add_u32 %[t1], %[ro], 6\n\t"
        "s_and_b32 %[t1], %[t1], 7\n\t"
        "s_lshl_b32 %[t1], %[t1], 18\n\t"
        "s_add_u32 %[t0], %[t1], 0\n\t"
        "buffer_load_dwordx4 v[160:163], v110, %[so], %[t0] offen\n\t"
        "s_sleep 2\n\t"
        "s_add_u32 %[t0], %[t1], 16384\n\t"
        "buffer_load_dwordx4 v[164:167], v110, %[so], %[t0] offen\n\t"
        "s_sleep 2\n\t"
        "s_add_u32 %[t0], %[t1], 32768\n\t"
        "buffer_load_dwordx4 v[168:171], v110, %[so], %[t0] offen\n\t"
        "s_sleep 2\n\t"
        "s_add_u32 %[t0], %[t1], 49152\n\t"
        "buffer_load_dwordx4 v[172:175], v110, %[so], %[t0] offen\n\t"
        "s_sleep 2\n\t"
        "s_add_u32 %[t0], %[t1], 65536\n\t"
        "buffer_load_dwordx4 v[176:179], v110, %[so], %[t0] offen\n\t"
        "s_sleep 2\n\t"
        "s_add_u32 %[t0], %[t1], 81920\n\t"
        "buffer_load_dwordx4 v[180:183], v110, %[so], %[t0] offen\n\t"
        "s_sleep 2\n\t"
        "s_add_u32 %[t0], %[t1], 98304\n\t"
        "buffer_load_dwordx4 v[24:27], v110, %[so], %[t0] offen\n\t"
        "s_sleep 2\n\t"
        "s_add_u32 %[t0], %[t1], 114688\n\t"
        "buffer_load_dwordx4 v[28:31], v110, %[so], %[t0] offen\n\t"
        "s_sleep 2\n\t"
        "s_waitcnt lgkmcnt(0)\n\t"
        "ds_write_b128 v116, v[56:59] offset:20480\n\t"
        "ds_write_b128 v116, v[60:63] offset:21504\n\t"
        "s_waitcnt vmcnt(8)\n\t"
        "ds_write_b128 v111, v[136:139] offset:0\n\t"
        "ds_write_b128 v111, v[140:143] offset:1024\n\t"
        "ds_write_b128 v111, v[144:147] offset:2048\n\t"
        "ds_write_b128 v111, v[148:151] offset:3072\n\t"
        "ds_write_b128 v111, v[152:155] offset:4096\n\t"
        "ds_write_b128 v111, v[156:159] offset:5120\n\t"
        "ds_write_b128 v111, v[48:51] offset:6144\n\t"
        "ds_write_b128 v111, v[52:55] offset:7168\n\t"
        "ds_read_b64_tr_b16 v[136:137], v112 offset:0\n\t"
        "ds_read_b64_tr_b16 v[138:139], v112 offset:2048\n\t"
        "ds_read_b64_tr_b16 v[148:149], v112 offset:4096\n\t"
        "ds_read_b64_tr_b16 v[150:151], v112 offset:6144\n\t"
        "s_waitcnt lgkmcnt(8)\n\t"
        "ds_read_b64_tr_b16 v[140:141], v113 offset:0\n\t"
        "ds_read_b64_tr_b16 v[142:143], v113 offset:2048\n\t"
        "ds_read_b64_tr_b16 v[152:153], v113 offset:4096\n\t"
        "ds_read_b64_tr_b16 v[154:155], v113 offset:6144\n\t"
        "s_waitcnt lgkmcnt(8)\n\t"
        "ds_read_b64_tr_b16 v[144:145], v114 offset:0\n\t"
        "ds_read_b64_tr_b16 v[146:147], v114 offset:2048\n\t"
        "ds_read_b64_tr_b16 v[156:157], v114 offset:4096\n\t"
        "ds_read_b64_tr_b16 v[158:159], v114 offset:6144\n\t"
        "s_waitcnt lgkmcnt(8)\n\t"
        "ds_read_b64_tr_b16 v[56:57], v115 offset:0\n\t"
        "ds_read_b64_tr_b16 v[58:59], v115 offset:2048\n\t"
        "ds_read_b64_tr_b16 v[60:61], v115 offset:4096\n\t"
        "ds_read_b64_tr_b16 v[62:63], v115 offset:6144\n\t"
        "s_add_u32 %[t1], %[ro], 6\n\t"
        "s_and_b32 %[t1], %[t1], 7\n\t"
        "s_lshl_b32 %[t1], %[t1], 18\n\t"
        "s_add_u32 %[t0], %[t1], 131072\n\t"
        "buffer_load_dwordx4 v[184:187], v110, %[so], %[t0] offen\n\t"
        "s_sleep 2\n\t"
        "s_add_u32 %[t0], %[t1], 147456\n\t"
        "buffer_load_dwordx4 v[188:191], v110, %[so], %[t0] offen\n\t"
        "s_sleep 2\n\t"
        "s_add_u32 %[t0], %[t1], 163840\n\t"
        "buffer_load_dwordx4 v[192:195], v110, %[so], %[t0] offen\n\t"
        "s_sleep 2\n\t"
        "s_add_u32 %[t0], %[t1], 180224\n\t"
        "buffer_load_dwordx4 v[196:199], v110, %[so], %[t0] offen\n\t"
        "s_sleep 2\n\t"
        "s_add_u32 %[t0], %[t1], 196608\n\t"
        "buffer_load_dwordx4 v[200:203], v110, %[so], %[t0] offen\n\t"
        "s_sleep 2\n\t"
        "s_add_u32 %[t0], %[t1], 212992\n\t"
        "buffer_load_dwordx4 v[204:207], v110, %[so], %[t0] offen\n\t"
        "s_sleep 2\n\t"
        "s_add_u32 %[t0], %[t1], 229376\n\t"
        "buffer_load_dwordx4 v[32:35], v110, %[so], %[t0] offen\n\t"
        "s_sleep 2\n\t"
        "s_add_u32 %[t0], %[t1], 245760\n\t"
        "buffer_load_dwordx4 v[36:39], v110, %[so], %[t0] offen\n\t"
        "s_sleep 2\n\t"
        "s_waitcnt lgkmcnt(0)\n\t"
        "ds_write_b128 v116, v[56:59] offset:22528\n\t"
        "ds_write_b128 v116, v[60:63] offset:23552\n\t"
        "s_waitcnt vmcnt(8)\n\t"
        "ds_write_b128 v111, v[160:163] offset:0\n\t"
        "ds_write_b128 v111, v[164:167] offset:1024\n\t"
        "ds_write_b128 v111, v[168:171] offset:2048\n\t"
        "ds_write_b128 v111, v[172:175] offset:3072\n\t"
        "ds_write_b128 v111, v[176:179] offset:4096\n\t"
        "ds_write_b128 v111, v[180:183] offset:5120\n\t"
        "ds_write_b128 v111, v[24:27] offset:6144\n\t"
        "ds_write_b128 v111, v[28:31] offset:7168\n\t"
        "ds_read_b64_tr_b16 v[160:161], v112 offset:0\n\t"
        "ds_read_b64_tr_b16 v[162:163], v112 offset:2048\n\t"
        "ds_read_b64_tr_b16 v[172:173], v112 offset:4096\n\t"
        "ds_read_b64_tr_b16 v[174:175], v112 offset:6144\n\t"
        "s_waitcnt lgkmcnt(8)\n\t"
        "ds_read_b64_tr_b16 v[164:165], v113 offset:0\n\t"
        "ds_read_b64_tr_b16 v[166:167], v113 offset:2048\n\t"
        "ds_read_b64_tr_b16 v[176:177], v113 offset:4096\n\t"
        "ds_read_b64_tr_b16 v[178:179], v113 offset:6144\n\t"
        "s_waitcnt lgkmcnt(8)\n\t"
        "ds_read_b64_tr_b16 v[168:169], v114 offset:0\n\t"
        "ds_read_b64_tr_b16 v[170:171], v114 offset:2048\n\t"
        "ds_read_b64_tr_b16 v[180:181], v114 offset:4096\n\t"
        "ds_read_b64_tr_b16 v[182:183], v114 offset:6144\n\t"
        "s_waitcnt lgkmcnt(8)\n\t"
        "ds_read_b64_tr_b16 v[56:57], v115 offset:0\n\t"
        "ds_read_b64_tr_b16 v[58:59], v115 offset:2048\n\t"
        "ds_read_b64_tr_b16 v[60:61], v115 offset:4096\n\t"
        "ds_read_b64_tr_b16 v[62:63], v115 offset:6144\n\t"
        "s_add_u32 %[t1], %[ro], 7\n\t"
        "s_and_b32 %[t1], %[t1], 7\n\t"
        "s_lshl_b32 %[t1], %[t1], 18\n\t"
        "s_add_u32 %[t0], %[t1], 0\n\t"
        "buffer_load_dwordx4 v[208:211], v110, %[so], %[t0] offen\n\t"
        "s_sleep 2\n\t"
        "s_add_u32 %[t0], %[t1], 16384\n\t"
        "buffer_load_dwordx4 v[212:215], v110, %[so], %[t0] offen\n\t"
        "s_sleep 2\n\t"
        "s_add_u32 %[t0], %[t1], 32768\n\t"
        "buffer_load_dwordx4 v[216:219], v110, %[so], %[t0] offen\n\t"
        "s_sleep 2\n\t"
        "s_add_u32 %[t0], %[t1], 49152\n\t"
        "buffer_load_dwordx4 v[220:223], v110, %[so], %[t0] offen\n\t"
        "s_sleep 2\n\t"
        "s_add_u32 %[t0], %[t1], 65536\n\t"
        "buffer_load_dwordx4 v[224:227], v110, %[so], %[t0] offen\n\t"
        "s_sleep 2\n\t"
        "s_add_u32 %[t0], %[t1], 81920\n\t"
        "buffer_load_dwordx4 v[228:231], v110, %[so], %[t0] offen\n\t"
        "s_sleep 2\n\t"
        "s_add_u32 %[t0], %[t1], 98304\n\t"
        "buffer_load_dwordx4 v[40:43], v110, %[so], %[t0] offen\n\t"
        "s_sleep 2\n\t"
        "s_add_u32 %[t0], %[t1], 114688\n\t"
        "buffer_load_dwordx4 v[44:47], v110, %[so], %[t0] offen\n\t"
        "s_sleep 2\n\t"
        "s_waitcnt lgkmcnt(0)\n\t"
        "ds_write_b128 v116, v[56:59] offset:24576\n\t"
        "ds_write_b128 v116, v[60:63] offset:25600\n\t"
        "s_waitcnt vmcnt(8)\n\t"
        "ds_write_b128 v111, v[184:187] offset:0\n\t"
        "ds_write_b128 v111, v[188:191] offset:1024\n\t"
        "ds_write_b128 v111, v[192:195] offset:2048\n\t"
        "ds_write_b128 v111, v[196:199] offset:3072\n\t"
        "ds_write_b128 v111, v[200:203] offset:4096\n\t"
        "ds_write_b128 v111, v[204:207] offset:5120\n\t"
        "ds_write_b128 v111, v[32:35] offset:6144\n\t"
        "ds_write_b128 v111, v[36:39] offset:7168\n\t"
        "ds_read_b64_tr_b16 v[184:185], v112 offset:0\n\t"
        "ds_read_b64_tr_b16 v[186:187], v112 offset:2048\n\t"
        "ds_read_b64_tr_b16 v[196:197], v112 offset:4096\n\t"
        "ds_read_b64_tr_b16 v[198:199], v112 offset:6144\n\t"
        "s_waitcnt lgkmcnt(8)\n\t"
        "ds_read_b64_tr_b16 v[188:189], v113 offset:0\n\t"
        "ds_read_b64_tr_b16 v[190:191], v113 offset:2048\n\t"
        "ds_read_b64_tr_b16 v[200:201], v113 offset:4096\n\t"
        "ds_read_b64_tr_b16 v[202:203], v113 offset:6144\n\t"
        "s_waitcnt lgkmcnt(8)\n\t"
        "ds_read_b64_tr_b16 v[192:193], v114 offset:0\n\t"
        "ds_read_b64_tr_b16 v[194:195], v114 offset:2048\n\t"
        "ds_read_b64_tr_b16 v[204:205], v114 offset:4096\n\t"
        "ds_read_b64_tr_b16 v[206:207], v114 offset:6144\n\t"
        "s_waitcnt lgkmcnt(8)\n\t"
        "ds_read_b64_tr_b16 v[56:57], v115 offset:0\n\t"
        "ds_read_b64_tr_b16 v[58:59], v115 offset:2048\n\t"
        "ds_read_b64_tr_b16 v[60:61], v115 offset:4096\n\t"
        "ds_read_b64_tr_b16 v[62:63], v115 offset:6144\n\t"
        "s_add_u32 %[t1], %[ro], 7\n\t"
        "s_and_b32 %[t1], %[t1], 7\n\t"
        "s_lshl_b32 %[t1], %[t1], 18\n\t"
        "s_add_u32 %[t0], %[t1], 131072\n\t"
        "buffer_load_dwordx4 v[232:235], v110, %[so], %[t0] offen\n\t"
        "s_sleep 2\n\t"
        "s_add_u32 %[t0], %[t1], 147456\n\t"
        "buffer_load_dwordx4 v[236:239], v110, %[so], %[t0] offen\n\t"
        "s_sleep 2\n\t"
        "s_add_u32 %[t0], %[t1], 163840\n\t"
        "buffer_load_dwordx4 v[240:243], v110, %[so], %[t0] offen\n\t"
        "s_sleep 2\n\t"
        "s_add_u32 %[t0], %[t1], 180224\n\t"
        "buffer_load_dwordx4 v[244:247], v110, %[so], %[t0] offen\n\t"
        "s_sleep 2\n\t"
        "s_add_u32 %[t0], %[t1], 196608\n\t"
        "buffer_load_dwordx4 v[248:251], v110, %[so], %[t0] offen\n\t"
        "s_sleep 2\n\t"
        "s_add_u32 %[t0], %[t1], 212992\n\t"
        "buffer_load_dwordx4 v[252:255], v110, %[so], %[t0] offen\n\t"
        "s_sleep 2\n\t"
        "s_add_u32 %[t0], %[t1], 229376\n\t"
        "buffer_load_dwordx4 v[48:51], v110, %[so], %[t0] offen\n\t"
        "s_sleep 2\n\t"
        "s_add_u32 %[t0], %[t1], 245760\n\t"
        "buffer_load_dwordx4 v[52:55], v110, %[so], %[t0] offen\n\t"
        "s_sleep 2\n\t"
        "s_waitcnt lgkmcnt(0)\n\t"
        "ds_write_b128 v116, v[56:59] offset:26624\n\t"
        "ds_write_b128 v116, v[60:63] offset:27648\n\t"
        "s_waitcnt vmcnt(8)\n\t"
        "ds_write_b128 v111, v[208:211] offset:0\n\t"
        "ds_write_b128 v111, v[212:215] offset:1024\n\t"
        "ds_write_b128 v111, v[216:219] offset:2048\n\t"
        "ds_write_b128 v111, v[220:223] offset:3072\n\t"
        "ds_write_b128 v111, v[224:227] offset:4096\n\t"
        "ds_write_b128 v111, v[228:231] offset:5120\n\t"
        "ds_write_b128 v111, v[40:43] offset:6144\n\t"
        "ds_write_b128 v111, v[44:47] offset:7168\n\t"
        "ds_read_b64_tr_b16 v[208:209], v112 offset:0\n\t"
        "ds_read_b64_tr_b16 v[210:211], v112 offset:2048\n\t"
        "ds_read_b64_tr_b16 v[220:221], v112 offset:4096\n\t"
        "ds_read_b64_tr_b16 v[222:223], v112 offset:6144\n\t"
        "s_waitcnt lgkmcnt(8)\n\t"
        "ds_read_b64_tr_b16 v[212:213], v113 offset:0\n\t"
        "ds_read_b64_tr_b16 v[214:215], v113 offset:2048\n\t"
        "ds_read_b64_tr_b16 v[224:225], v113 offset:4096\n\t"
        "ds_read_b64_tr_b16 v[226:227], v113 offset:6144\n\t"
        "s_waitcnt lgkmcnt(8)\n\t"
        "ds_read_b64_tr_b16 v[216:217], v114 offset:0\n\t"
        "ds_read_b64_tr_b16 v[218:219], v114 offset:2048\n\t"
        "ds_read_b64_tr_b16 v[228:229], v114 offset:4096\n\t"
        "ds_read_b64_tr_b16 v[230:231], v114 offset:6144\n\t"
        "s_waitcnt lgkmcnt(8)\n\t"
        "ds_read_b64_tr_b16 v[56:57], v115 offset:0\n\t"
        "ds_read_b64_tr_b16 v[58:59], v115 offset:2048\n\t"
        "ds_read_b64_tr_b16 v[60:61], v115 offset:4096\n\t"
        "ds_read_b64_tr_b16 v[62:63], v115 offset:6144\n\t"
        "s_waitcnt lgkmcnt(0)\n\t"
        "ds_write_b128 v116, v[56:59] offset:28672\n\t"
        "ds_write_b128 v116, v[60:63] offset:29696\n\t"
        "s_waitcnt vmcnt(0)\n\t"
        "ds_write_b128 v111, v[232:235] offset:0\n\t"
        "ds_write_b128 v111, v[236:239] offset:1024\n\t"
        "ds_write_b128 v111, v[240:243] offset:2048\n\t"
        "ds_write_b128 v111, v[244:247] offset:3072\n\t"
        "ds_write_b128 v111, v[248:251] offset:4096\n\t"
        "ds_write_b128 v111, v[252:255] offset:5120\n\t"
        "ds_write_b128 v111, v[48:51] offset:6144\n\t"
        "ds_write_b128 v111, v[52:55] offset:7168\n\t"
        "ds_read_b64_tr_b16 v[232:233], v112 offset:0\n\t"
        "ds_read_b64_tr_b16 v[234:235], v112 offset:2048\n\t"
        "ds_read_b64_tr_b16 v[244:245], v112 offset:4096\n\t"
        "ds_read_b64_tr_b16 v[246:247], v112 offset:6144\n\t"
        "s_waitcnt lgkmcnt(8)\n\t"
        "ds_read_b64_tr_b16 v[236:237], v113 offset:0\n\t"
        "ds_read_b64_tr_b16 v[238:239], v113 offset:2048\n\t"
        "ds_read_b64_tr_b16 v[248:249], v113 offset:4096\n\t"
        "ds_read_b64_tr_b16 v[250:251], v113 offset:6144\n\t"
        "s_waitcnt lgkmcnt(8)\n\t"
        "ds_read_b64_tr_b16 v[240:241], v114 offset:0\n\t"
        "ds_read_b64_tr_b16 v[242:243], v114 offset:2048\n\t"
        "ds_read_b64_tr_b16 v[252:253], v114 offset:4096\n\t"
        "ds_read_b64_tr_b16 v[254:255], v114 offset:6144\n\t"
        "s_waitcnt lgkmcnt(8)\n\t"
        "ds_read_b64_tr_b16 v[56:57], v115 offset:0\n\t"
        "ds_read_b64_tr_b16 v[58:59], v115 offset:2048\n\t"
        "ds_read_b64_tr_b16 v[60:61], v115 offset:4096\n\t"
        "ds_read_b64_tr_b16 v[62:63], v115 offset:6144\n\t"
        "s_waitcnt lgkmcnt(0)\n\t"
        "ds_write_b128 v116, v[56:59] offset:30720\n\t"
        "ds_write_b128 v116, v[60:63] offset:31744\n\t"
        "ds_read_b128 v[88:91], v116 offset:0\n\t"
        "ds_read_b128 v[92:95], v116 offset:1024\n\t"
        "ds_read_b128 v[96:99], v116 offset:2048\n\t"
        "s_mov_b32 %[sp], 0\n\t"
        "LP_cv%=:\n\t"
        "buffer_load_dword v104, off, %[sw], %[cv] sc1\n\t"
        "s_waitcnt vmcnt(0) lgkmcnt(0)\n\t"
        "v_readfirstlane_b32 %[t0], v104\n\t"
        "s_cmp_ge_u32 %[t0], 8\n\t"
        "s_cbranch_scc1 LP_wait%=\n\t"
        "s_sleep 8\n\t"
        "s_add_u32 %[sp], %[sp], 1\n\t"
        "s_cmp_lt_u32 %[sp], 0x100000\n\t"
        "s_cbranch_scc1 LP_cv%=\n\t"
        "v_mov_b32_e32 v104, 256\n\t"
        "s_mov_b32 %[t0], 33024\n\t"
        "buffer_store_dword v104, off, %[sw], %[t0] sc1\n\t"
        "s_endpgm\n\t"
        "LP_wait%=:\n\t"
        "s_mov_b32 %[re], 0\n\t"
        "LP_again%=:\n\t"
        "s_mov_b32 %[sp], 0\n\t"
        "LP_poll%=:\n\t"
        "buffer_load_dword v103, v118, %[sw], 0 offen sc1\n\t"
        "s_lshr_b32 %[t1], %[tl], 1\n\t"
        "s_waitcnt vmcnt(0)\n\t"
        "v_min_u32_dpp v103, v103, v103 quad_perm:[1,0,3,2] row_mask:0xf bank_mask:0xf\n\t"
        "s_nop 1\n\t"
        "v_min_u32_dpp v103, v103, v103 quad_perm:[2,3,0,1] row_mask:0xf bank_mask:0xf\n\t"
        "s_nop 1\n\t"
        "v_min_u32_dpp v103, v103, v103 row_ror:4 row_mask:0xf bank_mask:0xf\n\t"
        "s_nop 0\n\t"
        "v_readfirstlane_b32 %[pg], v103\n\t"
        "s_cmp_gt_u32 %[pg], %[t1]\n\t"
        "s_cbranch_scc1 LP_have%=\n\t"
        "s_add_u32 %[t0], %[pg], %[re]\n\t"
        "s_cmp_eq_u32 %[t0], 63\n\t"
        "s_cbranch_scc0 LP_no%=\n\t"
        "s_sleep 16\n\t"
        "s_branch LP_have%=\n\t"
        "LP_no%=:\n\t"
        "s_min_u32 %[t1], %[t1], 62\n\t"
        "s_cmp_ge_u32 %[pg], %[t1]\n\t"
        "s_cbranch_scc1 LP_near%=\n\t"
        "s_sleep 24\n\t"
        "LP_near%=:\n\t"
        "s_sleep 2\n\t"
        "s_add_u32 %[sp], %[sp], 1\n\t"
        "s_cmp_lt_u32 %[sp], 0x100000\n\t"
        "s_cbranch_scc1 LP_poll%=\n\t"
        "v_mov_b32_e32 v104, 128\n\t"
        "s_mov_b32 %[t0], 33024\n\t"
        "buffer_store_dword v104, off, %[sw], %[t0] sc1\n\t"
        "s_endpgm\n\t"
        "LP_have%=:\n\t"
        "s_lshl_b32 %[nx], %[tl], 17\n\t"
        "s_add_u32 %[t1], %[t1], 4\n\t"
        "s_cmp_gt_u32 %[pg], %[t1]\n\t"
        "s_cbranch_scc1 LP_run%=\n\t"
        "s_add_u32 %[t0], %[ro], 0\n\t"
        "s_and_b32 %[t0], %[t0], 7\n\t"
        "s_lshl_b32 %[t0], %[t0], 12\n\t"
        "s_add_u32 %[t0], %[t0], %[nx]\n\t"
        "buffer_load_dwordx4 v[24:27], v117, %[sw], %[t0] offen offset:0 sc1\n\t"
        "buffer_load_dwordx4 v[28:31], v117, %[sw], %[t0] offen offset:1024 sc1\n\t"
        "buffer_load_dwordx4 v[32:35], v117, %[sw], %[t0] offen offset:2048 sc1\n\t"
        "buffer_load_dwordx4 v[36:39], v117, %[sw], %[t0] offen offset:3072 sc1\n\t"
        "s_add_u32 %[t0], %[ro], 1\n\t"
        "s_and_b32 %[t0], %[t0], 7\n\t"
        "s_lshl_b32 %[t0], %[t0], 12\n\t"
        "s_add_u32 %[t0], %[t0], %[nx]\n\t"
        "buffer_load_dwordx4 v[40:43], v117, %[sw], %[t0] offen offset:0 sc1\n\t"
        "buffer_load_dwordx4 v[44:47], v117, %[sw], %[t0] offen offset:1024 sc1\n\t"
        "s_waitcnt vmcnt(5) lgkmcnt(2)\n\t"
        "v_mfma_f32_16x16x32_bf16 v[20:23], v[88:91], v[24:27], 0\n\t"
        "ds_read_b128 v[88:91], v116 offset:3072\n\t"
        "v_mfma_f32_16x16x32_bf16 v[8:11], a[0:3], v[24:27], 0\n\t"
        "v_max3_u32 v125, v24, v25, v26\n\t"
        "buffer_load_dwordx4 v[48:51], v117, %[sw], %[t0] offen offset:2048 sc1\n\t"
        "v_mfma_f32_16x16x32_bf16 v[12:15], a[4:7], v[24:27], 0\n\t"
        "v_max_u32_e32 v125, v27, v125\n\t"
        "v_mfma_f32_16x16x32_bf16 v[16:19], a[8:11], v[24:27], 0\n\t"
        "buffer_load_dwordx4 v[52:55], v117, %[sw], %[t0] offen offset:3072 sc1\n\t"
        "s_waitcnt vmcnt(6) lgkmcnt(2)\n\t"
        "v_mfma_f32_16x16x32_bf16 v[20:23], v[92:95], v[28:31], v[20:23]\n\t"
        "ds_read_b128 v[92:95], v116 offset:4096\n\t"
        "v_mfma_f32_16x16x32_bf16 v[8:11], a[12:15], v[28:31], v[8:11]\n\t"
        "v_max3_u32 v125, v28, v29, v125\n\t"
        "s_add_u32 %[t0], %[ro], 2\n\t"
        "s_and_b32 %[t0], %[t0], 7\n\t"
        "s_lshl_b32 %[t0], %[t0], 12\n\t"
        "s_add_u32 %[t0], %[t0], %[nx]\n\t"
        "buffer_load_dwordx4 v[56:59], v117, %[sw], %[t0] offen offset:0 sc1\n\t"
        "v_mfma_f32_16x16x32_bf16 v[12:15], a[16:19], v[28:31], v[12:15]\n\t"
        "v_max3_u32 v125, v30, v31, v125\n\t"
        "v_mfma_f32_16x16x32_bf16 v[16:19], a[20:23], v[28:31], v[16:19]\n\t"
        "buffer_load_dwordx4 v[60:63], v117, %[sw], %[t0] offen offset:1024 sc1\n\t"
        "s_and_b32 %[sp], %[tl], 1\n\t"
        "s_lshl_b32 %[sp], %[sp], 14\n\t"
        "s_lshr_b32 %[t1], %[tl], 1\n\t"
        "s_lshl_b32 %[t1], %[t1], 6\n\t"
        "s_add_u32 %[t1], %[sp], %[t1]\n\t"
        "buffer_load_dword v100, v119, %[sw], %[t1] offen sc1\n\t"
        "s_add_u32 %[t1], %[t1], 32768\n\t"
        "buffer_load_dword v101, v119, %[sw], %[t1] offen sc1\n\t"
        "s_lshl_b32 %[t1], %[tl], 9\n\t"
        "buffer_load_dword v102, v120, %[sw], %[t1] offen sc1\n\t"
        "buffer_load_dword v103, v118, %[sw], 0 offen sc1\n\t"
        "s_waitcnt vmcnt(11) lgkmcnt(2)\n\t"
        "v_mfma_f32_16x16x32_bf16 v[20:23], v[96:99], v[32:35], v[20:23]\n\t"
        "ds_read_b128 v[96:99], v116 offset:5120\n\t"
        "v_mfma_f32_16x16x32_bf16 v[8:11], a[24:27], v[32:35], v[8:11]\n\t"
        "v_max3_u32 v125, v32, v33, v125\n\t"
        "buffer_load_dwordx4 v[64:67], v117, %[sw], %[t0] offen offset:2048 sc1\n\t"
        "v_mfma_f32_16x16x32_bf16 v[12:15], a[28:31], v[32:35], v[12:15]\n\t"
        "v_max3_u32 v125, v34, v35, v125\n\t"
        "v_mfma_f32_16x16x32_bf16 v[16:19], a[32:35], v[32:35], v[16:19]\n\t"
        "buffer_load_dwordx4 v[68:71], v117, %[sw], %[t0] offen offset:3072 sc1\n\t"
        "s_waitcnt vmcnt(12) lgkmcnt(2)\n\t"
        "v_mfma_f32_16x16x32_bf16 v[20:23], v[88:91], v[36:39], v[20:23]\n\t"
        "ds_read_b128 v[88:91], v116 offset:6144\n\t"
        "v_mfma_f32_16x16x32_bf16 v[8:11], a[36:39], v[36:39], v[8:11]\n\t"
        "v_max3_u32 v125, v36, v37, v125\n\t"
        "s_add_u32 %[t0], %[ro], 3\n\t"
        "s_and_b32 %[t0], %[t0], 7\n\t"
        "s_lshl_b32 %[t0], %[t0], 12\n\t"
        "s_add_u32 %[t0], %[t0], %[nx]\n\t"
        "buffer_load_dwordx4 v[72:75], v117, %[sw], %[t0] offen offset:0 sc1\n\t"
        "v_mfma_f32_16x16x32_bf16 v[12:15], a[40:43], v[36:39], v[12:15]\n\t"
        "v_max3_u32 v125, v38, v39, v125\n\t"
        "v_mfma_f32_16x16x32_bf16 v[16:19], a[44:47], v[36:39], v[16:19]\n\t"
        "buffer_load_dwordx4 v[76:79], v117, %[sw], %[t0] offen offset:1024 sc1\n\t"
        "s_waitcnt vmcnt(13) lgkmcnt(2)\n\t"
        "v_mfma_f32_16x16x32_bf16 v[20:23], v[92:95], v[40:43], v[20:23]\n\t"
        "ds_read_b128 v[92:95], v116 offset:7168\n\t"
        "v_mfma_f32_16x16x32_bf16 v[8:11], a[48:51], v[40:43], v[8:11]\n\t"
        "v_max3_u32 v125, v40, v41, v125\n\t"
        "buffer_load_dwordx4 v[80:83], v117, %[sw], %[t0] offen offset:2048 sc1\n\t"
        "v_mfma_f32_16x16x32_bf16 v[12:15], a[52:55], v[40:43], v[12:15]\n\t"
        "v_max3_u32 v125, v42, v43, v125\n\t"
        "v_mfma_f32_16x16x32_bf16 v[16:19], a[56:59], v[40:43], v[16:19]\n\t"
        "buffer_load_dwordx4 v[84:87], v117, %[sw], %[t0] offen offset:3072 sc1\n\t"
        "s_waitcnt vmcnt(14) lgkmcnt(2)\n\t"
        "v_mfma_f32_16x16x32_bf16 v[20:23], v[96:99], v[44:47], v[20:23]\n\t"
        "ds_read_b128 v[96:99], v116 offset:8192\n\t"
        "v_mfma_f32_16x16x32_bf16 v[8:11], a[60:63], v[44:47], v[8:11]\n\t"
        "v_max3_u32 v125, v44, v45, v125\n\t"
        "s_add_u32 %[t0], %[ro], 4\n\t"
        "s_and_b32 %[t0], %[t0], 7\n\t"
        "s_lshl_b32 %[t0], %[t0], 12\n\t"
        "s_add_u32 %[t0], %[t0], %[nx]\n\t"
        "buffer_load_dwordx4 v[24:27], v117, %[sw], %[t0] offen offset:0 sc1\n\t"
        "v_mfma_f32_16x16x32_bf16 v[12:15], a[64:67], v[44:47], v[12:15]\n\t"
        "v_max3_u32 v125, v46, v47, v125\n\t"
        "v_mfma_f32_16x16x32_bf16 v[16:19], a[68:71], v[44:47], v[16:19]\n\t"
        "buffer_load_dwordx4 v[28:31], v117, %[sw], %[t0] offen offset:1024 sc1\n\t"
        "s_waitcnt vmcnt(15) lgkmcnt(2)\n\t"
        "v_mfma_f32_16x16x32_bf16 v[20:23], v[88:91], v[48:51], v[20:23]\n\t"
        "ds_read_b128 v[88:91], v116 offset:9216\n\t"
        "v_mfma_f32_16x16x32_bf16 v[8:11], a[72:75], v[48:51], v[8:11]\n\t"
        "v_max3_u32 v125, v48, v49, v125\n\t"
        "v_mfma_f32_16x16x32_bf16 v[12:15], a[76:79], v[48:51], v[12:15]\n\t"
        "v_max3_u32 v125, v50, v51, v125\n\t"
        "v_mfma_f32_16x16x32_bf16 v[16:19], a[80:83], v[48:51], v[16:19]\n\t"
        "buffer_load_dwordx4 v[32:35], v117, %[sw], %[t0] offen offset:2048 sc1\n\t"
        "s_waitcnt vmcnt(15) lgkmcnt(2)\n\t"
        "v_mfma_f32_16x16x32_bf16 v[20:23], v[92:95], v[52:55], v[20:23]\n\t"
        "ds_read_b128 v[92:95], v116 offset:10240\n\t"
        "v_mfma_f32_16x16x32_bf16 v[8:11], a[84:87], v[52:55], v[8:11]\n\t"
        "v_max3_u32 v125, v52, v53, v125\n\t"
        "v_mfma_f32_16x16x32_bf16 v[12:15], a[88:91], v[52:55], v[12:15]\n\t"
        "v_max3_u32 v125, v54, v55, v125\n\t"
        "v_mfma_f32_16x16x32_bf16 v[16:19], a[92:95], v[52:55], v[16:19]\n\t"
        "buffer_load_dwordx4 v[36:39], v117, %[sw], %[t0] offen offset:3072 sc1\n\t"
        "s_waitcnt vmcnt(15) lgkmcnt(2)\n\t"
        "s_cmp_lg_u32 %[re], 0\n\t"
        "s_cbranch_scc1 LP_nb%=\n\t"
        "s_barrier\n\t"
        "LP_nb%=:\n\t"
        "v_mfma_f32_16x16x32_bf16 v[20:23], v[96:99], v[56:59], v[20:23]\n\t"
        "ds_read_b128 v[96:99], v116 offset:11264\n\t"
        "v_mfma_f32_16x16x32_bf16 v[8:11], a[96:99], v[56:59], v[8:11]\n\t"
        "v_max3_u32 v125, v56, v57, v125\n\t"
        "v_mfma_f32_16x16x32_bf16 v[12:15], a[100:103], v[56:59], v[12:15]\n\t"
        "v_max3_u32 v125, v58, v59, v125\n\t"
        "v_mfma_f32_16x16x32_bf16 v[16:19], a[104:107], v[56:59], v[16:19]\n\t"
        "s_add_u32 %[t0], %[ro], 5\n\t"
        "s_and_b32 %[t0], %[t0], 7\n\t"
        "s_lshl_b32 %[t0], %[t0], 12\n\t"
        "s_add_u32 %[t0], %[t0], %[nx]\n\t"
        "buffer_load_dwordx4 v[40:43], v117, %[sw], %[t0] offen offset:0 sc1\n\t"
        "s_waitcnt vmcnt(15) lgkmcnt(2)\n\t"
        "v_mfma_f32_16x16x32_bf16 v[20:23], v[88:91], v[60:63], v[20:23]\n\t"
        "ds_read_b128 v[88:91], v116 offset:12288\n\t"
        "v_mfma_f32_16x16x32_bf16 v[8:11], a[108:111], v[60:63], v[8:11]\n\t"
        "v_max3_u32 v125, v60, v61, v125\n\t"
        "v_mfma_f32_16x16x32_bf16 v[12:15], a[112:115], v[60:63], v[12:15]\n\t"
        "v_max3_u32 v125, v62, v63, v125\n\t"
        "v_mfma_f32_16x16x32_bf16 v[16:19], a[116:119], v[60:63], v[16:19]\n\t"
        "buffer_load_dwordx4 v[44:47], v117, %[sw], %[t0] offen offset:1024 sc1\n\t"
        "s_waitcnt vmcnt(11) lgkmcnt(2)\n\t"
        "v_mfma_f32_16x16x32_bf16 v[20:23], v[92:95], v[64:67], v[20:23]\n\t"
        "ds_read_b128 v[92:95], v116 offset:13312\n\t"
        "v_mfma_f32_16x16x32_bf16 v[8:11], a[120:123], v[64:67], v[8:11]\n\t"
        "v_max3_u32 v125, v64, v65, v125\n\t"
        "v_mfma_f32_16x16x32_bf16 v[12:15], a[124:127], v[64:67], v[12:15]\n\t"
        "v_max3_u32 v125, v66, v67, v125\n\t"
        "v_mfma_f32_16x16x32_bf16 v[16:19], a[128:131], v[64:67], v[16:19]\n\t"
        "buffer_load_dwordx4 v[48:51], v117, %[sw], %[t0] offen offset:2048 sc1\n\t"
        "s_waitcnt vmcnt(11) lgkmcnt(2)\n\t"
        "v_mfma_f32_16x16x32_bf16 v[20:23], v[96:99], v[68:71], v[20:23]\n\t"
        "ds_read_b128 v[96:99], v116 offset:14336\n\t"
        "v_mfma_f32_16x16x32_bf16 v[8:11], a[132:135], v[68:71], v[8:11]\n\t"
        "v_max3_u32 v125, v68, v69, v125\n\t"
        "v_mfma_f32_16x16x32_bf16 v[12:15], a[136:139], v[68:71], v[12:15]\n\t"
        "v_max3_u32 v125, v70, v71, v125\n\t"
        "v_mfma_f32_16x16x32_bf16 v[16:19], a[140:143], v[68:71], v[16:19]\n\t"
        "buffer_load_dwordx4 v[52:55], v117, %[sw], %[t0] offen offset:3072 sc1\n\t"
        "s_waitcnt vmcnt(11) lgkmcnt(2)\n\t"
        "v_mfma_f32_16x16x32_bf16 v[20:23], v[88:91], v[72:75], v[20:23]\n\t"
        "ds_read_b128 v[88:91], v116 offset:15360\n\t"
        "v_mfma_f32_16x16x32_bf16 v[8:11], a[144:147], v[72:75], v[8:11]\n\t"
        "v_max3_u32 v125, v72, v73, v125\n\t"
        "v_mfma_f32_16x16x32_bf16 v[12:15], a[148:151], v[72:75], v[12:15]\n\t"
        "v_max3_u32 v125, v74, v75, v125\n\t"
        "v_mfma_f32_16x16x32_bf16 v[16:19], a[152:155], v[72:75], v[16:19]\n\t"
        "s_add_u32 %[t0], %[ro], 6\n\t"
        "s_and_b32 %[t0], %[t0], 7\n\t"
        "s_lshl_b32 %[t0], %[t0], 12\n\t"
        "s_add_u32 %[t0], %[t0], %[nx]\n\t"
        "buffer_load_dwordx4 v[56:59], v117, %[sw], %[t0] offen offset:0 sc1\n\t"
        "s_waitcnt vmcnt(11) lgkmcnt(2)\n\t"
        "v_mfma_f32_16x16x32_bf16 v[20:23], v[92:95], v[76:79], v[20:23]\n\t"
        "ds_read_b128 v[92:95], v116 offset:16384\n\t"
        "v_mfma_f32_16x16x32_bf16 v[8:11], a[156:159], v[76:79], v[8:11]\n\t"
        "v_max3_u32 v125, v76, v77, v125\n\t"
        "v_mfma_f32_16x16x32_bf16 v[12:15], a[160:163], v[76:79], v[12:15]\n\t"
        "v_max3_u32 v125, v78, v79, v125\n\t"
        "v_mfma_f32_16x16x32_bf16 v[16:19], a[164:167], v[76:79], v[16:19]\n\t"
        "buffer_load_dwordx4 v[60:63], v117, %[sw], %[t0] offen offset:1024 sc1\n\t"
        "s_waitcnt vmcnt(11) lgkmcnt(2)\n\t"
        "v_mfma_f32_16x16x32_bf16 v[20:23], v[96:99], v[80:83], v[20:23]\n\t"
        "ds_read_b128 v[96:99], v116 offset:17408\n\t"
        "v_mfma_f32_16x16x32_bf16 v[8:11], a[168:171], v[80:83], v[8:11]\n\t"
        "v_max3_u32 v125, v80, v81, v125\n\t"
        "v_mfma_f32_16x16x32_bf16 v[12:15], a[172:175], v[80:83], v[12:15]\n\t"
        "v_max3_u32 v125, v82, v83, v125\n\t"
        "v_mfma_f32_16x16x32_bf16 v[16:19], a[176:179], v[80:83], v[16:19]\n\t"
        "buffer_load_dwordx4 v[64:67], v117, %[sw], %[t0] offen offset:2048 sc1\n\t"
        "s_waitcnt vmcnt(11) lgkmcnt(2)\n\t"
        "v_mfma_f32_16x16x32_bf16 v[20:23], v[88:91], v[84:87], v[20:23]\n\t"
        "ds_read_b128 v[88:91], v116 offset:18432\n\t"
        "v_mfma_f32_16x16x32_bf16 v[8:11], a[180:183], v[84:87], v[8:11]\n\t"
        "v_max3_u32 v125, v84, v85, v125\n\t"
        "v_mfma_f32_16x16x32_bf16 v[12:15], a[184:187], v[84:87], v[12:15]\n\t"
        "v_max3_u32 v125, v86, v87, v125\n\t"
        "v_mfma_f32_16x16x32_bf16 v[16:19], a[188:191], v[84:87], v[16:19]\n\t"
        "buffer_load_dwordx4 v[68:71], v117, %[sw], %[t0] offen offset:3072 sc1\n\t"
        "s_waitcnt vmcnt(11) lgkmcnt(2)\n\t"
        "v_mfma_f32_16x16x32_bf16 v[20:23], v[92:95], v[24:27], v[20:23]\n\t"
        "ds_read_b128 v[92:95], v116 offset:19456\n\t"
        "v_mfma_f32_16x16x32_bf16 v[8:11], a[192:195], v[24:27], v[8:11]\n\t"
        "v_max3_u32 v125, v24, v25, v125\n\t"
        "v_mfma_f32_16x16x32_bf16 v[12:15], a[196:199], v[24:27], v[12:15]\n\t"
        "v_max3_u32 v125, v26, v27, v125\n\t"
        "v_mfma_f32_16x16x32_bf16 v[16:19], a[200:203], v[24:27], v[16:19]\n\t"
        "s_add_u32 %[t0], %[ro], 7\n\t"
        "s_and_b32 %[t0], %[t0], 7\n\t"
        "s_lshl_b32 %[t0], %[t0], 12\n\t"
        "s_add_u32 %[t0], %[t0], %[nx]\n\t"
        "buffer_load_dwordx4 v[72:75], v117, %[sw], %[t0] offen offset:0 sc1\n\t"
        "s_waitcnt vmcnt(11) lgkmcnt(2)\n\t"
        "v_mfma_f32_16x16x32_bf16 v[20:23], v[96:99], v[28:31], v[20:23]\n\t"
        "ds_read_b128 v[96:99], v116 offset:20480\n\t"
        "v_mfma_f32_16x16x32_bf16 v[8:11], a[204:207], v[28:31], v[8:11]\n\t"
        "v_max3_u32 v125, v28, v29, v125\n\t"
        "v_mfma_f32_16x16x32_bf16 v[12:15], a[208:211], v[28:31], v[12:15]\n\t"
        "v_max3_u32 v125, v30, v31, v125\n\t"
        "v_sub_f32_e32 v104, 1.0, v102\n\t"
        "v_mfma_f32_16x16x32_bf16 v[16:19], a[212:215], v[28:31], v[16:19]\n\t"
        "buffer_load_dwordx4 v[76:79], v117, %[sw], %[t0] offen offset:1024 sc1\n\t"
        "s_waitcnt vmcnt(11) lgkmcnt(2)\n\t"
        "v_mfma_f32_16x16x32_bf16 v[20:23], v[88:91], v[32:35], v[20:23]\n\t"
        "ds_read_b128 v[88:91], v116 offset:21504\n\t"
        "v_mfma_f32_16x16x32_bf16 v[8:11], a[216:219], v[32:35], v[8:11]\n\t"
        "v_max3_u32 v125, v32, v33, v125\n\t"
        "v_mfma_f32_16x16x32_bf16 v[12:15], a[220:223], v[32:35], v[12:15]\n\t"
        "v_max3_u32 v125, v34, v35, v125\n\t"
        "v_cvt_pk_bf16_f32 v104, 0, v104\n\t"
        "v_mfma_f32_16x16x32_bf16 v[16:19], a[224:227], v[32:35], v[16:19]\n\t"
        "buffer_load_dwordx4 v[80:83], v117, %[sw], %[t0] offen offset:2048 sc1\n\t"
        "s_waitcnt vmcnt(11) lgkmcnt(2)\n\t"
        "v_mfma_f32_16x16x32_bf16 v[20:23], v[92:95], v[36:39], v[20:23]\n\t"
        "ds_read_b128 v[92:95], v116 offset:22528\n\t"
        "v_mfma_f32_16x16x32_bf16 v[8:11], a[228:231], v[36:39], v[8:11]\n\t"
        "v_max3_u32 v125, v36, v37, v125\n\t"
        "v_mfma_f32_16x16x32_bf16 v[12:15], a[232:235], v[36:39], v[12:15]\n\t"
        "v_max3_u32 v125, v38, v39, v125\n\t"
        "v_lshlrev_b32_e32 v106, 16, v100\n\t"
        "v_mfma_f32_16x16x32_bf16 v[16:19], a[236:239], v[36:39], v[16:19]\n\t"
        "buffer_load_dwordx4 v[84:87], v117, %[sw], %[t0] offen offset:3072 sc1\n\t"
        "s_waitcnt vmcnt(11) lgkmcnt(2)\n\t"
        "v_mfma_f32_16x16x32_bf16 v[20:23], v[96:99], v[40:43], v[20:23]\n\t"
        "ds_read_b128 v[96:99], v116 offset:23552\n\t"
        "v_mfma_f32_16x16x32_bf16 v[8:11], a[240:243], v[40:43], v[8:11]\n\t"
        "v_max3_u32 v125, v40, v41, v125\n\t"
        "v_mfma_f32_16x16x32_bf16 v[12:15], a[244:247], v[40:43], v[12:15]\n\t"
        "v_max3_u32 v125, v42, v43, v125\n\t"
        "v_mul_f32_e32 v106, v102, v106\n\t"
        "v_mfma_f32_16x16x32_bf16 v[16:19], a[248:251], v[40:43], v[16:19]\n\t"
        "s_waitcnt vmcnt(10) lgkmcnt(2)\n\t"
        "v_mfma_f32_16x16x32_bf16 v[20:23], v[88:91], v[44:47], v[20:23]\n\t"
        "ds_read_b128 v[88:91], v116 offset:24576\n\t"
        "v_mfma_f32_16x16x32_bf16 v[8:11], a[252:255], v[44:47], v[8:11]\n\t"
        "v_max3_u32 v125, v44, v45, v125\n\t"
        "v_mfma_f32_16x16x32_bf16 v[12:15], v[128:131], v[44:47], v[12:15]\n\t"
        "v_max3_u32 v125, v46, v47, v125\n\t"
        "v_cvt_pk_bf16_f32 v106, 0, v106\n\t"
        "v_mfma_f32_16x16x32_bf16 v[16:19], v[132:135], v[44:47], v[16:19]\n\t"
        "s_waitcnt vmcnt(9) lgkmcnt(2)\n\t"
        "v_mfma_f32_16x16x32_bf16 v[20:23], v[92:95], v[48:51], v[20:23]\n\t"
        "ds_read_b128 v[92:95], v116 offset:25600\n\t"
        "v_mfma_f32_16x16x32_bf16 v[8:11], v[136:139], v[48:51], v[8:11]\n\t"
        "v_max3_u32 v125, v48, v49, v125\n\t"
        "v_mfma_f32_16x16x32_bf16 v[12:15], v[140:143], v[48:51], v[12:15]\n\t"
        "v_max3_u32 v125, v50, v51, v125\n\t"
        "v_and_b32_e32 v107, 0xffff0000, v100\n\t"
        "v_mfma_f32_16x16x32_bf16 v[16:19], v[144:147], v[48:51], v[16:19]\n\t"
        "s_waitcnt vmcnt(8) lgkmcnt(2)\n\t"
        "v_mfma_f32_16x16x32_bf16 v[20:23], v[96:99], v[52:55], v[20:23]\n\t"
        "ds_read_b128 v[96:99], v116 offset:26624\n\t"
        "v_mfma_f32_16x16x32_bf16 v[8:11], v[148:151], v[52:55], v[8:11]\n\t"
        "v_max3_u32 v125, v52, v53, v125\n\t"
        "v_mfma_f32_16x16x32_bf16 v[12:15], v[152:155], v[52:55], v[12:15]\n\t"
        "v_max3_u32 v125, v54, v55, v125\n\t"
        "v_mul_f32_e32 v107, v102, v107\n\t"
        "v_mfma_f32_16x16x32_bf16 v[16:19], v[156:159], v[52:55], v[16:19]\n\t"
        "s_waitcnt vmcnt(7) lgkmcnt(2)\n\t"
        "v_mfma_f32_16x16x32_bf16 v[20:23], v[88:91], v[56:59], v[20:23]\n\t"
        "ds_read_b128 v[88:91], v116 offset:27648\n\t"
        "v_mfma_f32_16x16x32_bf16 v[8:11], v[160:163], v[56:59], v[8:11]\n\t"
        "v_max3_u32 v125, v56, v57, v125\n\t"
        "v_mfma_f32_16x16x32_bf16 v[12:15], v[164:167], v[56:59], v[12:15]\n\t"
        "v_max3_u32 v125, v58, v59, v125\n\t"
        "v_cvt_pk_bf16_f32 v107, 0, v107\n\t"
        "v_mfma_f32_16x16x32_bf16 v[16:19], v[168:171], v[56:59], v[16:19]\n\t"
        "s_waitcnt vmcnt(6) lgkmcnt(2)\n\t"
        "v_mfma_f32_16x16x32_bf16 v[20:23], v[92:95], v[60:63], v[20:23]\n\t"
        "ds_read_b128 v[92:95], v116 offset:28672\n\t"
        "v_mfma_f32_16x16x32_bf16 v[8:11], v[172:175], v[60:63], v[8:11]\n\t"
        "v_max3_u32 v125, v60, v61, v125\n\t"
        "v_mfma_f32_16x16x32_bf16 v[12:15], v[176:179], v[60:63], v[12:15]\n\t"
        "v_max3_u32 v125, v62, v63, v125\n\t"
        "v_lshlrev_b32_e32 v108, 16, v101\n\t"
        "v_mfma_f32_16x16x32_bf16 v[16:19], v[180:183], v[60:63], v[16:19]\n\t"
        "s_waitcnt vmcnt(5) lgkmcnt(2)\n\t"
        "v_mfma_f32_16x16x32_bf16 v[20:23], v[96:99], v[64:67], v[20:23]\n\t"
        "ds_read_b128 v[96:99], v116 offset:29696\n\t"
        "v_mfma_f32_16x16x32_bf16 v[8:11], v[184:187], v[64:67], v[8:11]\n\t"
        "v_max3_u32 v125, v64, v65, v125\n\t"
        "v_mfma_f32_16x16x32_bf16 v[12:15], v[188:191], v[64:67], v[12:15]\n\t"
        "v_max3_u32 v125, v66, v67, v125\n\t"
        "v_mul_f32_e32 v108, v102, v108\n\t"
        "v_mfma_f32_16x16x32_bf16 v[16:19], v[192:195], v[64:67], v[16:19]\n\t"
        "s_waitcnt vmcnt(4) lgkmcnt(2)\n\t"
        "v_mfma_f32_16x16x32_bf16 v[20:23], v[88:91], v[68:71], v[20:23]\n\t"
        "ds_read_b128 v[88:91], v116 offset:30720\n\t"
        "v_mfma_f32_16x16x32_bf16 v[8:11], v[196:199], v[68:71], v[8:11]\n\t"
        "v_max3_u32 v125, v68, v69, v125\n\t"
        "v_mfma_f32_16x16x32_bf16 v[12:15], v[200:203], v[68:71], v[12:15]\n\t"
        "v_max3_u32 v125, v70, v71, v125\n\t"
        "v_cvt_pk_bf16_f32 v108, 0, v108\n\t"
        "v_mfma_f32_16x16x32_bf16 v[16:19], v[204:207], v[68:71], v[16:19]\n\t"
        "s_waitcnt vmcnt(3) lgkmcnt(2)\n\t"
        "v_mfma_f32_16x16x32_bf16 v[20:23], v[92:95], v[72:75], v[20:23]\n\t"
        "ds_read_b128 v[92:95], v116 offset:31744\n\t"
        "v_mfma_f32_16x16x32_bf16 v[8:11], v[208:211], v[72:75], v[8:11]\n\t"
        "v_max3_u32 v125, v72, v73, v125\n\t"
        "v_mfma_f32_16x16x32_bf16 v[12:15], v[212:215], v[72:75], v[12:15]\n\t"
        "v_max3_u32 v125, v74, v75, v125\n\t"
        "v_and_b32_e32 v109, 0xffff0000, v101\n\t"
        "v_mfma_f32_16x16x32_bf16 v[16:19], v[216:219], v[72:75], v[16:19]\n\t"
        "s_waitcnt vmcnt(2) lgkmcnt(2)\n\t"
        "v_mfma_f32_16x16x32_bf16 v[20:23], v[96:99], v[76:79], v[20:23]\n\t"
        "v_mfma_f32_16x16x32_bf16 v[8:11], v[220:223], v[76:79], v[8:11]\n\t"
        "v_max3_u32 v125, v76, v77, v125\n\t"
        "v_mfma_f32_16x16x32_bf16 v[12:15], v[224:227], v[76:79], v[12:15]\n\t"
        "v_max3_u32 v125, v78, v79, v125\n\t"
        "v_mul_f32_e32 v109, v102, v109\n\t"
        "v_mfma_f32_16x16x32_bf16 v[16:19], v[228:231], v[76:79], v[16:19]\n\t"
        "s_waitcnt vmcnt(1) lgkmcnt(1)\n\t"
        "v_mfma_f32_16x16x32_bf16 v[20:23], v[88:91], v[80:83], v[20:23]\n\t"
        "ds_read_b128 v[88:91], v116 offset:0\n\t"
        "v_mfma_f32_16x16x32_bf16 v[8:11], v[232:235], v[80:83], v[8:11]\n\t"
        "v_max3_u32 v125, v80, v81, v125\n\t"
        "v_mfma_f32_16x16x32_bf16 v[12:15], v[236:239], v[80:83], v[12:15]\n\t"
        "v_max3_u32 v125, v82, v83, v125\n\t"
        "v_cvt_pk_bf16_f32 v109, 0, v109\n\t"
        "v_mfma_f32_16x16x32_bf16 v[16:19], v[240:243], v[80:83], v[16:19]\n\t"
        "s_waitcnt vmcnt(0) lgkmcnt(1)\n\t"
        "v_mfma_f32_16x16x32_bf16 v[20:23], v[92:95], v[84:87], v[20:23]\n\t"
        "ds_read_b128 v[92:95], v116 offset:1024\n\t"
        "v_mfma_f32_16x16x32_bf16 v[8:11], v[244:247], v[84:87], v[8:11]\n\t"
        "ds_read_b128 v[96:99], v116 offset:2048\n\t"
        "v_max3_u32 v125, v84, v85, v125\n\t"
        "v_mfma_f32_16x16x32_bf16 v[12:15], v[248:251], v[84:87], v[12:15]\n\t"
        "v_max3_u32 v125, v86, v87, v125\n\t"
        "v_mfma_f32_16x16x32_bf16 v[16:19], v[252:255], v[84:87], v[16:19]\n\t"
        "v_cmp_eq_u32_e32 vcc, -1, v125\n\t"
        "s_mov_b32 %[nx], 0xf0000000\n\t"
        "s_mov_b32 %[re], 64\n\t"
        "s_cbranch_vccz LP_epi%=\n\t"
        "s_waitcnt lgkmcnt(0)\n\t"
        "s_branch LP_again%=\n\t"
        "LP_run%=:\n\t"
        "s_add_u32 %[t0], %[ro], 0\n\t"
        "s_and_b32 %[t0], %[t0], 7\n\t"
        "s_lshl_b32 %[t0], %[t0], 12\n\t"
        "s_add_u32 %[t0], %[t0], %[nx]\n\t"
        "buffer_load_dwordx4 v[24:27], v117, %[sw], %[t0] offen offset:0 sc1\n\t"
        "buffer_load_dwordx4 v[28:31], v117, %[sw], %[t0] offen offset:1024 sc1\n\t"
        "buffer_load_dwordx4 v[32:35], v117, %[sw], %[t0] offen offset:2048 sc1\n\t"
        "buffer_load_dwordx4 v[36:39], v117, %[sw], %[t0] offen offset:3072 sc1\n\t"
        "s_add_u32 %[t0], %[ro], 1\n\t"
        "s_and_b32 %[t0], %[t0], 7\n\t"
        "s_lshl_b32 %[t0], %[t0], 12\n\t"
        "s_add_u32 %[t0], %[t0], %[nx]\n\t"
        "buffer_load_dwordx4 v[40:43], v117, %[sw], %[t0] offen offset:0 sc1\n\t"
        "buffer_load_dwordx4 v[44:47], v117, %[sw], %[t0] offen offset:1024 sc1\n\t"
        "buffer_load_dwordx4 v[48:51], v117, %[sw], %[t0] offen offset:2048 sc1\n\t"
        "buffer_load_dwordx4 v[52:55], v117, %[sw], %[t0] offen offset:3072 sc1\n\t"
        "s_add_u32 %[t0], %[ro], 2\n\t"
        "s_and_b32 %[t0], %[t0], 7\n\t"
        "s_lshl_b32 %[t0], %[t0], 12\n\t"
        "s_add_u32 %[t0], %[t0], %[nx]\n\t"
        "buffer_load_dwordx4 v[56:59], v117, %[sw], %[t0] offen offset:0 sc1\n\t"
        "buffer_load_dwordx4 v[60:63], v117, %[sw], %[t0] offen offset:1024 sc1\n\t"
        "buffer_load_dwordx4 v[64:67], v117, %[sw], %[t0] offen offset:2048 sc1\n\t"
        "buffer_load_dwordx4 v[68:71], v117, %[sw], %[t0] offen offset:3072 sc1\n\t"
        "s_add_u32 %[t0], %[ro], 3\n\t"
        "s_and_b32 %[t0], %[t0], 7\n\t"
        "s_lshl_b32 %[t0], %[t0], 12\n\t"
        "s_add_u32 %[t0], %[t0], %[nx]\n\t"
        "buffer_load_dwordx4 v[72:75], v117, %[sw], %[t0] offen offset:0 sc1\n\t"
        "buffer_load_dwordx4 v[76:79], v117, %[sw], %[t0] offen offset:1024 sc1\n\t"
        "buffer_load_dwordx4 v[80:83], v117, %[sw], %[t0] offen offset:2048 sc1\n\t"
        "buffer_load_dwordx4 v[84:87], v117, %[sw], %[t0] offen offset:3072 sc1\n\t"
        "buffer_load_dword v103, v118, %[sw], 0 offen sc1\n\t"
        "LP_tile%=:\n\t"
        "s_and_b32 %[sp], %[tl], 1\n\t"
        "s_lshl_b32 %[sp], %[sp], 14\n\t"
        "s_lshr_b32 %[t1], %[tl], 1\n\t"
        "s_lshl_b32 %[t1], %[t1], 6\n\t"
        "s_add_u32 %[t1], %[sp], %[t1]\n\t"
        "buffer_load_dword v100, v119, %[sw], %[t1] offen sc1\n\t"
        "s_add_u32 %[t1], %[t1], 32768\n\t"
        "buffer_load_dword v101, v119, %[sw], %[t1] offen sc1\n\t"
        "s_lshl_b32 %[t1], %[tl], 9\n\t"
        "buffer_load_dword v102, v120, %[sw], %[t1] offen sc1\n\t"
        "buffer_load_dword v103, v118, %[sw], 0 offen sc1\n\t"
        "s_mov_b32 %[cu], %[nx]\n\t"
        "s_waitcnt vmcnt(20) lgkmcnt(2)\n\t"
        "v_mfma_f32_16x16x32_bf16 v[20:23], v[88:91], v[24:27], 0\n\t"
        "ds_read_b128 v[88:91], v116 offset:3072\n\t"
        "v_mfma_f32_16x16x32_bf16 v[8:11], a[0:3], v[24:27], 0\n\t"
        "v_mfma_f32_16x16x32_bf16 v[12:15], a[4:7], v[24:27], 0\n\t"
        "v_mfma_f32_16x16x32_bf16 v[16:19], a[8:11], v[24:27], 0\n\t"
        "s_add_u32 %[t0], %[ro], 4\n\t"
        "s_and_b32 %[t0], %[t0], 7\n\t"
        "s_lshl_b32 %[t0], %[t0], 12\n\t"
        "s_add_u32 %[t0], %[t0], %[cu]\n\t"
        "buffer_load_dwordx4 v[24:27], v117, %[sw], %[t0] offen offset:0 sc1\n\t"
        "s_waitcnt vmcnt(20) lgkmcnt(2)\n\t"
        "v_mfma_f32_16x16x32_bf16 v[20:23], v[92:95], v[28:31], v[20:23]\n\t"
        "ds_read_b128 v[92:95], v116 offset:4096\n\t"
        "v_mfma_f32_16x16x32_bf16 v[8:11], a[12:15], v[28:31], v[8:11]\n\t"
        "v_mfma_f32_16x16x32_bf16 v[12:15], a[16:19], v[28:31], v[12:15]\n\t"
        "v_mfma_f32_16x16x32_bf16 v[16:19], a[20:23], v[28:31], v[16:19]\n\t"
        "buffer_load_dwordx4 v[28:31], v117, %[sw], %[t0] offen offset:1024 sc1\n\t"
        "s_waitcnt vmcnt(20) lgkmcnt(2)\n\t"
        "v_mfma_f32_16x16x32_bf16 v[20:23], v[96:99], v[32:35], v[20:23]\n\t"
        "ds_read_b128 v[96:99], v116 offset:5120\n\t"
        "v_mfma_f32_16x16x32_bf16 v[8:11], a[24:27], v[32:35], v[8:11]\n\t"
        "v_mfma_f32_16x16x32_bf16 v[12:15], a[28:31], v[32:35], v[12:15]\n\t"
        "v_mfma_f32_16x16x32_bf16 v[16:19], a[32:35], v[32:35], v[16:19]\n\t"
        "buffer_load_dwordx4 v[32:35], v117, %[sw], %[t0] offen offset:2048 sc1\n\t"
        "s_waitcnt vmcnt(20) lgkmcnt(2)\n\t"
        "v_mfma_f32_16x16x32_bf16 v[20:23], v[88:91], v[36:39], v[20:23]\n\t"
        "ds_read_b128 v[88:91], v116 offset:6144\n\t"
        "v_mfma_f32_16x16x32_bf16 v[8:11], a[36:39], v[36:39], v[8:11]\n\t"
        "v_mfma_f32_16x16x32_bf16 v[12:15], a[40:43], v[36:39], v[12:15]\n\t"
        "v_mfma_f32_16x16x32_bf16 v[16:19], a[44:47], v[36:39], v[16:19]\n\t"
        "buffer_load_dwordx4 v[36:39], v117, %[sw], %[t0] offen offset:3072 sc1\n\t"
        "s_waitcnt vmcnt(20) lgkmcnt(2)\n\t"
        "v_mfma_f32_16x16x32_bf16 v[20:23], v[92:95], v[40:43], v[20:23]\n\t"
        "ds_read_b128 v[92:95], v116 offset:7168\n\t"
        "v_mfma_f32_16x16x32_bf16 v[8:11], a[48:51], v[40:43], v[8:11]\n\t"
        "v_mfma_f32_16x16x32_bf16 v[12:15], a[52:55], v[40:43], v[12:15]\n\t"
        "v_mfma_f32_16x16x32_bf16 v[16:19], a[56:59], v[40:43], v[16:19]\n\t"
        "s_add_u32 %[t0], %[ro], 5\n\t"
        "s_and_b32 %[t0], %[t0], 7\n\t"
        "s_lshl_b32 %[t0], %[t0], 12\n\t"
        "s_add_u32 %[t0], %[t0], %[cu]\n\t"
        "buffer_load_dwordx4 v[40:43], v117, %[sw], %[t0] offen offset:0 sc1\n\t"
        "s_waitcnt vmcnt(20) lgkmcnt(2)\n\t"
        "v_mfma_f32_16x16x32_bf16 v[20:23], v[96:99], v[44:47], v[20:23]\n\t"
        "ds_read_b128 v[96:99], v116 offset:8192\n\t"
        "v_mfma_f32_16x16x32_bf16 v[8:11], a[60:63], v[44:47], v[8:11]\n\t"
        "v_mfma_f32_16x16x32_bf16 v[12:15], a[64:67], v[44:47], v[12:15]\n\t"
        "v_mfma_f32_16x16x32_bf16 v[16:19], a[68:71], v[44:47], v[16:19]\n\t"
        "buffer_load_dwordx4 v[44:47], v117, %[sw], %[t0] offen offset:1024 sc1\n\t"
        "s_waitcnt vmcnt(20) lgkmcnt(2)\n\t"
        "v_mfma_f32_16x16x32_bf16 v[20:23], v[88:91], v[48:51], v[20:23]\n\t"
        "ds_read_b128 v[88:91], v116 offset:9216\n\t"
        "v_mfma_f32_16x16x32_bf16 v[8:11], a[72:75], v[48:51], v[8:11]\n\t"
        "v_mfma_f32_16x16x32_bf16 v[12:15], a[76:79], v[48:51], v[12:15]\n\t"
        "v_mfma_f32_16x16x32_bf16 v[16:19], a[80:83], v[48:51], v[16:19]\n\t"
        "buffer_load_dwordx4 v[48:51], v117, %[sw], %[t0] offen offset:2048 sc1\n\t"
        "s_waitcnt vmcnt(20) lgkmcnt(2)\n\t"
        "v_mfma_f32_16x16x32_bf16 v[20:23], v[92:95], v[52:55], v[20:23]\n\t"
        "ds_read_b128 v[92:95], v116 offset:10240\n\t"
        "v_mfma_f32_16x16x32_bf16 v[8:11], a[84:87], v[52:55], v[8:11]\n\t"
        "v_mfma_f32_16x16x32_bf16 v[12:15], a[88:91], v[52:55], v[12:15]\n\t"
        "v_mfma_f32_16x16x32_bf16 v[16:19], a[92:95], v[52:55], v[16:19]\n\t"
        "buffer_load_dwordx4 v[52:55], v117, %[sw], %[t0] offen offset:3072 sc1\n\t"
        "s_waitcnt vmcnt(20) lgkmcnt(2)\n\t"
        "s_barrier\n\t"
        "v_mfma_f32_16x16x32_bf16 v[20:23], v[96:99], v[56:59], v[20:23]\n\t"
        "ds_read_b128 v[96:99], v116 offset:11264\n\t"
        "v_mfma_f32_16x16x32_bf16 v[8:11], a[96:99], v[56:59], v[8:11]\n\t"
        "v_mfma_f32_16x16x32_bf16 v[12:15], a[100:103], v[56:59], v[12:15]\n\t"
        "v_mfma_f32_16x16x32_bf16 v[16:19], a[104:107], v[56:59], v[16:19]\n\t"
        "s_add_u32 %[t0], %[ro], 6\n\t"
        "s_and_b32 %[t0], %[t0], 7\n\t"
        "s_lshl_b32 %[t0], %[t0], 12\n\t"
        "s_add_u32 %[t0], %[t0], %[cu]\n\t"
        "buffer_load_dwordx4 v[56:59], v117, %[sw], %[t0] offen offset:0 sc1\n\t"
        "s_waitcnt vmcnt(20) lgkmcnt(2)\n\t"
        "v_mfma_f32_16x16x32_bf16 v[20:23], v[88:91], v[60:63], v[20:23]\n\t"
        "ds_read_b128 v[88:91], v116 offset:12288\n\t"
        "v_mfma_f32_16x16x32_bf16 v[8:11], a[108:111], v[60:63], v[8:11]\n\t"
        "v_mfma_f32_16x16x32_bf16 v[12:15], a[112:115], v[60:63], v[12:15]\n\t"
        "v_mfma_f32_16x16x32_bf16 v[16:19], a[116:119], v[60:63], v[16:19]\n\t"
        "buffer_load_dwordx4 v[60:63], v117, %[sw], %[t0] offen offset:1024 sc1\n\t"
        "s_waitcnt vmcnt(20) lgkmcnt(2)\n\t"
        "v_mfma_f32_16x16x32_bf16 v[20:23], v[92:95], v[64:67], v[20:23]\n\t"
        "ds_read_b128 v[92:95], v116 offset:13312\n\t"
        "v_mfma_f32_16x16x32_bf16 v[8:11], a[120:123], v[64:67], v[8:11]\n\t"
        "v_mfma_f32_16x16x32_bf16 v[12:15], a[124:127], v[64:67], v[12:15]\n\t"
        "v_mfma_f32_16x16x32_bf16 v[16:19], a[128:131], v[64:67], v[16:19]\n\t"
        "buffer_load_dwordx4 v[64:67], v117, %[sw], %[t0] offen offset:2048 sc1\n\t"
        "s_waitcnt vmcnt(20) lgkmcnt(2)\n\t"
        "v_mfma_f32_16x16x32_bf16 v[20:23], v[96:99], v[68:71], v[20:23]\n\t"
        "ds_read_b128 v[96:99], v116 offset:14336\n\t"
        "v_mfma_f32_16x16x32_bf16 v[8:11], a[132:135], v[68:71], v[8:11]\n\t"
        "v_mfma_f32_16x16x32_bf16 v[12:15], a[136:139], v[68:71], v[12:15]\n\t"
        "v_mfma_f32_16x16x32_bf16 v[16:19], a[140:143], v[68:71], v[16:19]\n\t"
        "buffer_load_dwordx4 v[68:71], v117, %[sw], %[t0] offen offset:3072 sc1\n\t"
        "s_waitcnt vmcnt(20) lgkmcnt(2)\n\t"
        "v_mfma_f32_16x16x32_bf16 v[20:23], v[88:91], v[72:75], v[20:23]\n\t"
        "ds_read_b128 v[88:91], v116 offset:15360\n\t"
        "v_mfma_f32_16x16x32_bf16 v[8:11], a[144:147], v[72:75], v[8:11]\n\t"
        "v_mfma_f32_16x16x32_bf16 v[12:15], a[148:151], v[72:75], v[12:15]\n\t"
        "v_mfma_f32_16x16x32_bf16 v[16:19], a[152:155], v[72:75], v[16:19]\n\t"
        "s_add_u32 %[t0], %[ro], 7\n\t"
        "s_and_b32 %[t0], %[t0], 7\n\t"
        "s_lshl_b32 %[t0], %[t0], 12\n\t"
        "s_add_u32 %[t0], %[t0], %[cu]\n\t"
        "buffer_load_dwordx4 v[72:75], v117, %[sw], %[t0] offen offset:0 sc1\n\t"
        "s_waitcnt vmcnt(13) lgkmcnt(2)\n\t"
        "v_mfma_f32_16x16x32_bf16 v[20:23], v[92:95], v[76:79], v[20:23]\n\t"
        "ds_read_b128 v[92:95], v116 offset:16384\n\t"
        "v_min_u32_dpp v103, v103, v103 quad_perm:[1,0,3,2] row_mask:0xf bank_mask:0xf\n\t"
        "s_nop 0\n\t"
        "v_mfma_f32_16x16x32_bf16 v[8:11], a[156:159], v[76:79], v[8:11]\n\t"
        "v_min_u32_dpp v103, v103, v103 quad_perm:[2,3,0,1] row_mask:0xf bank_mask:0xf\n\t"
        "s_nop 0\n\t"
        "v_mfma_f32_16x16x32_bf16 v[12:15], a[160:163], v[76:79], v[12:15]\n\t"
        "v_min_u32_dpp v103, v103, v103 row_ror:4 row_mask:0xf bank_mask:0xf\n\t"
        "s_nop 0\n\t"
        "v_mfma_f32_16x16x32_bf16 v[16:19], a[164:167], v[76:79], v[16:19]\n\t"
        "buffer_load_dwordx4 v[76:79], v117, %[sw], %[t0] offen offset:1024 sc1\n\t"
        "s_waitcnt vmcnt(20) lgkmcnt(2)\n\t"
        "v_mfma_f32_16x16x32_bf16 v[20:23], v[96:99], v[80:83], v[20:23]\n\t"
        "ds_read_b128 v[96:99], v116 offset:17408\n\t"
        "v_readfirstlane_b32 %[pg], v103\n\t"
        "s_add_u32 %[t1], %[tl], 8\n\t"
        "s_lshl_b32 %[nx], %[t1], 17\n\t"
        "s_lshr_b32 %[t1], %[t1], 1\n\t"
        "v_mfma_f32_16x16x32_bf16 v[8:11], a[168:171], v[80:83], v[8:11]\n\t"
        "s_cmp_gt_u32 %[pg], %[t1]\n\t"
        "s_cselect_b32 %[nx], %[nx], 0xf0000000\n\t"
        "v_mfma_f32_16x16x32_bf16 v[12:15], a[172:175], v[80:83], v[12:15]\n\t"
        "v_mfma_f32_16x16x32_bf16 v[16:19], a[176:179], v[80:83], v[16:19]\n\t"
        "buffer_load_dwordx4 v[80:83], v117, %[sw], %[t0] offen offset:2048 sc1\n\t"
        "s_waitcnt vmcnt(20) lgkmcnt(2)\n\t"
        "v_mfma_f32_16x16x32_bf16 v[20:23], v[88:91], v[84:87], v[20:23]\n\t"
        "ds_read_b128 v[88:91], v116 offset:18432\n\t"
        "v_mfma_f32_16x16x32_bf16 v[8:11], a[180:183], v[84:87], v[8:11]\n\t"
        "v_mfma_f32_16x16x32_bf16 v[12:15], a[184:187], v[84:87], v[12:15]\n\t"
        "v_mfma_f32_16x16x32_bf16 v[16:19], a[188:191], v[84:87], v[16:19]\n\t"
        "buffer_load_dwordx4 v[84:87], v117, %[sw], %[t0] offen offset:3072 sc1\n\t"
        "s_waitcnt vmcnt(15) lgkmcnt(2)\n\t"
        "v_mfma_f32_16x16x32_bf16 v[20:23], v[92:95], v[24:27], v[20:23]\n\t"
        "ds_read_b128 v[92:95], v116 offset:19456\n\t"
        "v_mfma_f32_16x16x32_bf16 v[8:11], a[192:195], v[24:27], v[8:11]\n\t"
        "v_mfma_f32_16x16x32_bf16 v[12:15], a[196:199], v[24:27], v[12:15]\n\t"
        "v_mfma_f32_16x16x32_bf16 v[16:19], a[200:203], v[24:27], v[16:19]\n\t"
        "s_add_u32 %[t0], %[ro], 0\n\t"
        "s_and_b32 %[t0], %[t0], 7\n\t"
        "s_lshl_b32 %[t0], %[t0], 12\n\t"
        "s_add_u32 %[t0], %[t0], %[nx]\n\t"
        "buffer_load_dwordx4 v[24:27], v117, %[sw], %[t0] offen offset:0 sc1\n\t"
        "s_waitcnt vmcnt(15) lgkmcnt(2)\n\t"
        "v_mfma_f32_16x16x32_bf16 v[20:23], v[96:99], v[28:31], v[20:23]\n\t"
        "ds_read_b128 v[96:99], v116 offset:20480\n\t"
        "v_mfma_f32_16x16x32_bf16 v[8:11], a[204:207], v[28:31], v[8:11]\n\t"
        "v_mfma_f32_16x16x32_bf16 v[12:15], a[208:211], v[28:31], v[12:15]\n\t"
        "v_sub_f32_e32 v104, 1.0, v102\n\t"
        "v_mfma_f32_16x16x32_bf16 v[16:19], a[212:215], v[28:31], v[16:19]\n\t"
        "buffer_load_dwordx4 v[28:31], v117, %[sw], %[t0] offen offset:1024 sc1\n\t"
        "s_waitcnt vmcnt(15) lgkmcnt(2)\n\t"
        "v_mfma_f32_16x16x32_bf16 v[20:23], v[88:91], v[32:35], v[20:23]\n\t"
        "ds_read_b128 v[88:91], v116 offset:21504\n\t"
        "v_mfma_f32_16x16x32_bf16 v[8:11], a[216:219], v[32:35], v[8:11]\n\t"
        "v_mfma_f32_16x16x32_bf16 v[12:15], a[220:223], v[32:35], v[12:15]\n\t"
        "v_cvt_pk_bf16_f32 v104, 0, v104\n\t"
        "v_mfma_f32_16x16x32_bf16 v[16:19], a[224:227], v[32:35], v[16:19]\n\t"
        "buffer_load_dwordx4 v[32:35], v117, %[sw], %[t0] offen offset:2048 sc1\n\t"
        "s_waitcnt vmcnt(15) lgkmcnt(2)\n\t"
        "v_mfma_f32_16x16x32_bf16 v[20:23], v[92:95], v[36:39], v[20:23]\n\t"
        "ds_read_b128 v[92:95], v116 offset:22528\n\t"
        "v_mfma_f32_16x16x32_bf16 v[8:11], a[228:231], v[36:39], v[8:11]\n\t"
        "v_mfma_f32_16x16x32_bf16 v[12:15], a[232:235], v[36:39], v[12:15]\n\t"
        "v_lshlrev_b32_e32 v106, 16, v100\n\t"
        "v_mfma_f32_16x16x32_bf16 v[16:19], a[236:239], v[36:39], v[16:19]\n\t"
        "buffer_load_dwordx4 v[36:39], v117, %[sw], %[t0] offen offset:3072 sc1\n\t"
        "s_waitcnt vmcnt(15) lgkmcnt(2)\n\t"
        "v_mfma_f32_16x16x32_bf16 v[20:23], v[96:99], v[40:43], v[20:23]\n\t"
        "ds_read_b128 v[96:99], v116 offset:23552\n\t"
        "v_mfma_f32_16x16x32_bf16 v[8:11], a[240:243], v[40:43], v[8:11]\n\t"
        "v_mfma_f32_16x16x32_bf16 v[12:15], a[244:247], v[40:43], v[12:15]\n\t"
        "v_mul_f32_e32 v106, v102, v106\n\t"
        "v_mfma_f32_16x16x32_bf16 v[16:19], a[248:251], v[40:43], v[16:19]\n\t"
        "s_add_u32 %[t0], %[ro], 1\n\t"
        "s_and_b32 %[t0], %[t0], 7\n\t"
        "s_lshl_b32 %[t0], %[t0], 12\n\t"
        "s_add_u32 %[t0], %[t0], %[nx]\n\t"
        "buffer_load_dwordx4 v[40:43], v117, %[sw], %[t0] offen offset:0 sc1\n\t"
        "s_waitcnt vmcnt(15) lgkmcnt(2)\n\t"
        "v_mfma_f32_16x16x32_bf16 v[20:23], v[88:91], v[44:47], v[20:23]\n\t"
        "ds_read_b128 v[88:91], v116 offset:24576\n\t"
        "v_mfma_f32_16x16x32_bf16 v[8:11], a[252:255], v[44:47], v[8:11]\n\t"
        "v_mfma_f32_16x16x32_bf16 v[12:15], v[128:131], v[44:47], v[12:15]\n\t"
        "v_cvt_pk_bf16_f32 v106, 0, v106\n\t"
        "v_mfma_f32_16x16x32_bf16 v[16:19], v[132:135], v[44:47], v[16:19]\n\t"
        "buffer_load_dwordx4 v[44:47], v117, %[sw], %[t0] offen offset:1024 sc1\n\t"
        "s_waitcnt vmcnt(15) lgkmcnt(2)\n\t"
        "v_mfma_f32_16x16x32_bf16 v[20:23], v[92:95], v[48:51], v[20:23]\n\t"
        "ds_read_b128 v[92:95], v116 offset:25600\n\t"
        "v_mfma_f32_16x16x32_bf16 v[8:11], v[136:139], v[48:51], v[8:11]\n\t"
        "v_mfma_f32_16x16x32_bf16 v[12:15], v[140:143], v[48:51], v[12:15]\n\t"
        "v_and_b32_e32 v107, 0xffff0000, v100\n\t"
        "v_mfma_f32_16x16x32_bf16 v[16:19], v[144:147], v[48:51], v[16:19]\n\t"
        "buffer_load_dwordx4 v[48:51], v117, %[sw], %[t0] offen offset:2048 sc1\n\t"
        "s_waitcnt vmcnt(15) lgkmcnt(2)\n\t"
        "v_mfma_f32_16x16x32_bf16 v[20:23], v[96:99], v[52:55], v[20:23]\n\t"
        "ds_read_b128 v[96:99], v116 offset:26624\n\t"
        "v_mfma_f32_16x16x32_bf16 v[8:11], v[148:151], v[52:55], v[8:11]\n\t"
        "v_mfma_f32_16x16x32_bf16 v[12:15], v[152:155], v[52:55], v[12:15]\n\t"
        "v_mul_f32_e32 v107, v102, v107\n\t"
        "v_mfma_f32_16x16x32_bf16 v[16:19], v[156:159], v[52:55], v[16:19]\n\t"
        "buffer_load_dwordx4 v[52:55], v117, %[sw], %[t0] offen offset:3072 sc1\n\t"
        "s_waitcnt vmcnt(15) lgkmcnt(2)\n\t"
        "v_mfma_f32_16x16x32_bf16 v[20:23], v[88:91], v[56:59], v[20:23]\n\t"
        "ds_read_b128 v[88:91], v116 offset:27648\n\t"
        "v_mfma_f32_16x16x32_bf16 v[8:11], v[160:163], v[56:59], v[8:11]\n\t"
        "v_mfma_f32_16x16x32_bf16 v[12:15], v[164:167], v[56:59], v[12:15]\n\t"
        "v_cvt_pk_bf16_f32 v107, 0, v107\n\t"
        "v_mfma_f32_16x16x32_bf16 v[16:19], v[168:171], v[56:59], v[16:19]\n\t"
        "s_add_u32 %[t0], %[ro], 2\n\t"
        "s_and_b32 %[t0], %[t0], 7\n\t"
        "s_lshl_b32 %[t0], %[t0], 12\n\t"
        "s_add_u32 %[t0], %[t0], %[nx]\n\t"
        "buffer_load_dwordx4 v[56:59], v117, %[sw], %[t0] offen offset:0 sc1\n\t"
        "s_waitcnt vmcnt(15) lgkmcnt(2)\n\t"
        "v_mfma_f32_16x16x32_bf16 v[20:23], v[92:95], v[60:63], v[20:23]\n\t"
        "ds_read_b128 v[92:95], v116 offset:28672\n\t"
        "v_mfma_f32_16x16x32_bf16 v[8:11], v[172:175], v[60:63], v[8:11]\n\t"
        "v_mfma_f32_16x16x32_bf16 v[12:15], v[176:179], v[60:63], v[12:15]\n\t"
        "v_lshlrev_b32_e32 v108, 16, v101\n\t"
        "v_mfma_f32_16x16x32_bf16 v[16:19], v[180:183], v[60:63], v[16:19]\n\t"
        "buffer_load_dwordx4 v[60:63], v117, %[sw], %[t0] offen offset:1024 sc1\n\t"
        "s_waitcnt vmcnt(15) lgkmcnt(2)\n\t"
        "v_mfma_f32_16x16x32_bf16 v[20:23], v[96:99], v[64:67], v[20:23]\n\t"
        "ds_read_b128 v[96:99], v116 offset:29696\n\t"
        "v_mfma_f32_16x16x32_bf16 v[8:11], v[184:187], v[64:67], v[8:11]\n\t"
        "v_mfma_f32_16x16x32_bf16 v[12:15], v[188:191], v[64:67], v[12:15]\n\t"
        "v_mul_f32_e32 v108, v102, v108\n\t"
        "v_mfma_f32_16x16x32_bf16 v[16:19], v[192:195], v[64:67], v[16:19]\n\t"
        "buffer_load_dwordx4 v[64:67], v117, %[sw], %[t0] offen offset:2048 sc1\n\t"
        "s_waitcnt vmcnt(15) lgkmcnt(2)\n\t"
        "v_mfma_f32_16x16x32_bf16 v[20:23], v[88:91], v[68:71], v[20:23]\n\t"
        "ds_read_b128 v[88:91], v116 offset:30720\n\t"
        "v_mfma_f32_16x16x32_bf16 v[8:11], v[196:199], v[68:71], v[8:11]\n\t"
        "v_mfma_f32_16x16x32_bf16 v[12:15], v[200:203], v[68:71], v[12:15]\n\t"
        "v_cvt_pk_bf16_f32 v108, 0, v108\n\t"
        "v_mfma_f32_16x16x32_bf16 v[16:19], v[204:207], v[68:71], v[16:19]\n\t"
        "buffer_load_dwordx4 v[68:71], v117, %[sw], %[t0] offen offset:3072 sc1\n\t"
        "s_waitcnt vmcnt(15) lgkmcnt(2)\n\t"
        "v_mfma_f32_16x16x32_bf16 v[20:23], v[92:95], v[72:75], v[20:23]\n\t"
        "ds_read_b128 v[92:95], v116 offset:31744\n\t"
        "v_mfma_f32_16x16x32_bf16 v[8:11], v[208:211], v[72:75], v[8:11]\n\t"
        "v_mfma_f32_16x16x32_bf16 v[12:15], v[212:215], v[72:75], v[12:15]\n\t"
        "v_and_b32_e32 v109, 0xffff0000, v101\n\t"
        "v_mfma_f32_16x16x32_bf16 v[16:19], v[216:219], v[72:75], v[16:19]\n\t"
        "s_add_u32 %[t0], %[ro], 3\n\t"
        "s_and_b32 %[t0], %[t0], 7\n\t"
        "s_lshl_b32 %[t0], %[t0], 12\n\t"
        "s_add_u32 %[t0], %[t0], %[nx]\n\t"
        "buffer_load_dwordx4 v[72:75], v117, %[sw], %[t0] offen offset:0 sc1\n\t"
        "s_waitcnt vmcnt(15) lgkmcnt(2)\n\t"
        "v_mfma_f32_16x16x32_bf16 v[20:23], v[96:99], v[76:79], v[20:23]\n\t"
        "v_mfma_f32_16x16x32_bf16 v[8:11], v[220:223], v[76:79], v[8:11]\n\t"
        "v_mfma_f32_16x16x32_bf16 v[12:15], v[224:227], v[76:79], v[12:15]\n\t"
        "v_mul_f32_e32 v109, v102, v109\n\t"
        "v_mfma_f32_16x16x32_bf16 v[16:19], v[228:231], v[76:79], v[16:19]\n\t"
        "buffer_load_dwordx4 v[76:79], v117, %[sw], %[t0] offen offset:1024 sc1\n\t"
        "s_waitcnt vmcnt(15) lgkmcnt(1)\n\t"
        "v_mfma_f32_16x16x32_bf16 v[20:23], v[88:91], v[80:83], v[20:23]\n\t"
        "ds_read_b128 v[88:91], v116 offset:0\n\t"
        "v_mfma_f32_16x16x32_bf16 v[8:11], v[232:235], v[80:83], v[8:11]\n\t"
        "v_mfma_f32_16x16x32_bf16 v[12:15], v[236:239], v[80:83], v[12:15]\n\t"
        "v_cvt_pk_bf16_f32 v109, 0, v109\n\t"
        "v_mfma_f32_16x16x32_bf16 v[16:19], v[240:243], v[80:83], v[16:19]\n\t"
        "buffer_load_dwordx4 v[80:83], v117, %[sw], %[t0] offen offset:2048 sc1\n\t"
        "s_waitcnt vmcnt(15) lgkmcnt(1)\n\t"
        "v_mfma_f32_16x16x32_bf16 v[20:23], v[92:95], v[84:87], v[20:23]\n\t"
        "ds_read_b128 v[92:95], v116 offset:1024\n\t"
        "v_mfma_f32_16x16x32_bf16 v[8:11], v[244:247], v[84:87], v[8:11]\n\t"
        "ds_read_b128 v[96:99], v116 offset:2048\n\t"
        "v_mfma_f32_16x16x32_bf16 v[12:15], v[248:251], v[84:87], v[12:15]\n\t"
        "v_mfma_f32_16x16x32_bf16 v[16:19], v[252:255], v[84:87], v[16:19]\n\t"
        "buffer_load_dwordx4 v[84:87], v117, %[sw], %[t0] offen offset:3072 sc1\n\t"
        "LP_epi%=:\n\t"
        "s_lshl_b32 %[t0], %[tl], 15\n\t"
        "s_nop 0\n\t"
        "ds_write_b128 v122, v[20:23] offset:192\n\t"
        "s_nop 0\n\t"
        "ds_write_b128 v122, v[8:11] offset:0\n\t"
        "s_nop 0\n\t"
        "ds_write_b128 v122, v[12:15] offset:64\n\t"
        "ds_write_b128 v122, v[16:19] offset:128\n\t"
        "s_waitcnt lgkmcnt(0)\n\t"
        "s_barrier\n\t"
        "ds_read_b128 v[8:11], v123 offset:0\n\t"
        "ds_read_b128 v[12:15], v123 offset:4352\n\t"
        "ds_read_b128 v[16:19], v123 offset:8704\n\t"
        "ds_read_b128 v[20:23], v123 offset:13056\n\t"
        "s_waitcnt lgkmcnt(2)\n\t"
        "v_pk_add_f32 v[8:9], v[8:9], v[12:13]\n\t"
        "v_pk_add_f32 v[10:11], v[10:11], v[14:15]\n\t"
        "s_waitcnt lgkmcnt(0)\n\t"
        "v_pk_add_f32 v[16:17], v[16:17], v[20:21]\n\t"
        "v_pk_add_f32 v[18:19], v[18:19], v[22:23]\n\t"
        "v_pk_add_f32 v[8:9], v[8:9], v[16:17]\n\t"
        "v_pk_add_f32 v[10:11], v[10:11], v[18:19]\n\t"
        "v_cvt_pk_bf16_f32 v8, 0, v8\n\t"
        "v_cvt_pk_bf16_f32 v9, 0, v9\n\t"
        "v_cvt_pk_bf16_f32 v10, 0, v10\n\t"
        "v_cvt_pk_bf16_f32 v11, 0, v11\n\t"
        "v_mul_f32_e32 v8, v104, v8\n\t"
        "v_mul_f32_e32 v9, v104, v9\n\t"
        "v_mul_f32_e32 v10, v104, v10\n\t"
        "v_mul_f32_e32 v11, v104, v11\n\t"
        "v_cvt_pk_bf16_f32 v8, 0, v8\n\t"
        "v_cvt_pk_bf16_f32 v9, 0, v9\n\t"
        "v_cvt_pk_bf16_f32 v10, 0, v10\n\t"
        "v_cvt_pk_bf16_f32 v11, 0, v11\n\t"
        "v_add_f32_e32 v8, v106, v8\n\t"
        "v_add_f32_e32 v9, v107, v9\n\t"
        "v_add_f32_e32 v10, v108, v10\n\t"
        "v_add_f32_e32 v11, v109, v11\n\t"
        "v_cvt_pk_bf16_f32 v104, v8, v9\n\t"
        "v_cvt_pk_bf16_f32 v105, v10, v11\n\t"
        "buffer_store_dwordx2 v[104:105], v121, %[su], %[t0] offen sc1\n\t"
        "s_add_u32 %[tl], %[tl], 8\n\t"
        "s_cmp_ge_u32 %[tl], 128\n\t"
        "s_cbranch_scc1 LP_end%=\n\t"
        "s_cmp_lg_u32 %[nx], 0xf0000000\n\t"
        "s_cbranch_scc1 LP_tile%=\n\t"
        "s_branch LP_wait%=\n\t"
        "LP_end%=:\n\t"
        "s_waitcnt vmcnt(0) lgkmcnt(0)\n\t"
        ".Lk_end_P:\n\t"
        : [t0] "=&s"(t0), [t1] "=&s"(t1), [tl] "+s"(tl), [pg] "=&s"(pg), [cu] "=&s"(cu), [nx] "=&s"(nx), [sp] "=&s"(sp), [re] "=&s"(re)
        : [so] "s"(so), [sw] "s"(sw), [su] "s"(su), [cv] "s"(F_CVY + nt * 128), [ro] "s"((nt + 2 * rp) & 7),
          "{v110}"((w * 1024 + (l >> 3)) * 2048 + nt * 128 + (l & 7) * 16), "{v111}"(stg + ((l * 16) ^ ((l & 48) << 1))), "{v112}"(lr), "{v113}"(lr ^ 32u), "{v114}"(lr ^ 64u), "{v115}"(lr ^ 96u),
          "{v116}"(lb + w * 32768 + l * 16), "{v117}"(W_OX + w * 32768 + l * 16), "{v118}"(F_PROG + (w * 8 + (l & 7)) * 128),
          "{v119}"(W_A + ((nt * 2 + (n >> 3)) * 4 + 2 * (n & 1)) * 32768 + w * 4096 + ((n & 7) >> 1) * 16 + g * 4), "{v120}"(W_GATE + w * 128 + g * 4), "{v121}"((4 * w + g) * 2048 + nt * 128 + n * 8),
          "{v122}"(lb + 131072 + w * 4352 + n * 272 + g * 16), "{v123}"(lb + 131072 + (4 * w + g) * 272 + n * 16)
        : "memory", "scc", "vcc", "v8", "v9", "v10", "v11", "v12", "v13", "v14", "v15", "v16", "v17", "v18", "v19", "v20", "v21", "v22", "v23", "v24", "v25", "v26", "v27", "v28", "v29", "v30", "v31", "v32", "v33", "v34", "v35", "v36", "v37", "v38", "v39", "v40", "v41", "v42", "v43", "v44", "v45", "v46", "v47", "v48", "v49", "v50", "v51", "v52", "v53", "v54", "v55", "v56", "v57", "v58", "v59", "v60", "v61", "v62", "v63", "v64", "v65", "v66", "v67", "v68", "v69", "v70", "v71", "v72", "v73", "v74", "v75", "v76", "v77", "v78", "v79", "v80", "v81", "v82", "v83", "v84", "v85", "v86", "v87", "v88", "v89", "v90", "v91", "v92", "v93", "v94", "v95", "v96", "v97", "v98", "v99", "v100", "v101", "v102", "v103", "v104", "v105", "v106", "v107", "v108", "v109", "v124", "v125", "v126", "v127", "v128", "v129", "v130", "v131", "v132", "v133", "v134", "v135", "v136", "v137", "v138", "v139", "v140", "v141", "v142", "v143", "v144", "v145", "v146", "v147", "v148", "v149", "v150", "v151", "v152", "v153", "v154", "v155", "v156", "v157", "v158", "v159", "v160", "v161", "v162", "v163", "v164", "v165", "v166", "v167", "v168", "v169", "v170", "v171", "v172", "v173", "v174", "v175", "v176", "v177", "v178", "v179", "v180", "v181", "v182", "v183", "v184", "v185", "v186", "v187", "v188", "v189", "v190", "v191", "v192", "v193", "v194", "v195", "v196", "v197", "v198", "v199", "v200", "v201", "v202", "v203", "v204", "v205", "v206", "v207", "v208", "v209", "v210", "v211", "v212", "v213", "v214", "v215", "v216", "v217", "v218", "v219", "v220", "v221", "v222", "v223", "v224", "v225", "v226", "v227", "v228", "v229", "v230", "v231", "v232", "v233", "v234", "v235", "v236", "v237", "v238", "v239", "v240", "v241", "v242", "v243", "v244", "v245", "v246", "v247", "v248", "v249", "v250", "v251", "v252", "v253", "v254", "v255", "a0", "a1", "a2", "a3", "a4", "a5", "a6", "a7", "a8", "a9", "a10", "a11", "a12", "a13", "a14", "a15", "a16", "a17", "a18", "a19", "a20", "a21", "a22", "a23", "a24", "a25", "a26", "a27", "a28", "a29", "a30", "a31", "a32", "a33", "a34", "a35", "a36", "a37", "a38", "a39", "a40", "a41", "a42", "a43", "a44", "a45", "a46", "a47", "a48", "a49", "a50", "a51", "a52", "a53", "a54", "a55", "a56", "a57", "a58", "a59", "a60", "a61", "a62", "a63", "a64", "a65", "a66", "a67", "a68", "a69", "a70", "a71", "a72", "a73", "a74", "a75", "a76", "a77", "a78", "a79", "a80", "a81", "a82", "a83", "a84", "a85", "a86", "a87", "a88", "a89", "a90", "a91", "a92", "a93", "a94", "a95", "a96", "a97", "a98", "a99", "a100", "a101", "a102", "a103", "a104", "a105", "a106", "a107", "a108", "a109", "a110", "a111", "a112", "a113", "a114", "a115", "a116", "a117", "a118", "a119", "a120", "a121", "a122", "a123", "a124", "a125", "a126", "a127", "a128", "a129", "a130", "a131", "a132", "a133", "a134", "a135", "a136", "a137", "a138", "a139", "a140", "a141", "a142", "a143", "a144", "a145", "a146", "a147", "a148", "a149", "a150", "a151", "a152", "a153", "a154", "a155", "a156", "a157", "a158", "a159", "a160", "a161", "a162", "a163", "a164", "a165", "a166", "a167", "a168", "a169", "a170", "a171", "a172", "a173", "a174", "a175", "a176", "a177", "a178", "a179", "a180", "a181", "a182", "a183", "a184", "a185", "a186", "a187", "a188", "a189", "a190", "a191", "a192", "a193", "a194", "a195", "a196", "a197", "a198", "a199", "a200", "a201", "a202", "a203", "a204", "a205", "a206", "a207", "a208", "a209", "a210", "a211", "a212", "a213", "a214", "a215", "a216", "a217", "a218", "a219", "a220", "a221", "a222", "a223", "a224", "a225", "a226", "a227", "a228", "a229", "a230", "a231", "a232", "a233", "a234", "a235", "a236", "a237", "a238", "a239", "a240", "a241", "a242", "a243", "a244", "a245", "a246", "a247", "a248", "a249", "a250", "a251", "a252", "a253", "a254", "a255");
}
__device__ __forceinline__ void bulk_block(const Params& p, rsrc_t r, unsigned char* lds, const int wv, const int j) {
    proj_block(p, lds, wv, (j & 6) * 4 + (j >> 4), ((j >> 2) & 2) | (j & 1));
    const int un = ((j & 7) >> 1) * 32 + (j >> 3) * 2 + (j & 1);
    gate_block(p, r, j, wv);
    conv_prep(p, r, wv, un >> 2, un & 3);
    if (!conv_core(p, r, lds, wv, un)) return;
    if (!wait_word(r, F_CONV, 128u, 4096u)) return;
    out_block(p, lds, wv, ((j & 7) >> 1) + (j & 1) * 4, j >> 3);
}

__device__ __forceinline__ void rec_block(const Params& p, rsrc_t r, unsigned char* lds, const int wv, const int b) {
    const int xq = (b & 7) >> 1, t = (b >> 3) * 2 + (b & 1);
    const unsigned lb = __builtin_amdgcn_readfirstlane((unsigned)(size_t)lds);
    if (t < 8) rec_a(p, lb, wv, xq * 8 + t, xq);
    else if (t < 24) rec_b(p, lb, wv, (xq & 1) * 16 + t - 8, xq >> 1, xq);
    else {
        const int hi = xq * 8 + t - 24;
        rec_helper(p, r, lb, wv, hi);
    }
}

__global__ void __launch_bounds__(256, 1) k_main(Params p) {
    extern __shared__ __attribute__((aligned(16))) unsigned char lds[];
    rsrc_t r = __builtin_amdgcn_make_buffer_rsrc(p.ws, 0, W_TOTAL, 0x00020000);
    const int wv = __builtin_amdgcn_readfirstlane(threadIdx.x >> 6);
    {
        unsigned long long pc; int ea, eb, eg;
        asm volatile("s_getpc_b64 %0\n.Lk_begin:\n\ts_mov_b32 %1, .Lk_end_A-.Lk_begin\n\ts_mov_b32 %2, .Lk_end_B-.Lk_begin\n\ts_mov_b32 %3, .Lk_end_P-.Lk_begin" : "=s"(pc), "=s"(ea), "=s"(eb), "=s"(eg));
        const int o = ((blockIdx.x >> 3) * 64 + (threadIdx.x >> 2)) * 128;
        if ((threadIdx.x & 3) == 0 && o < max(ea, max(eb, eg)) - 4) touch_l2((const unsigned char*)pc + o, __builtin_amdgcn_readfirstlane((unsigned)(size_t)lds) + 1024);
    }
    if (wv == 3) {
        unsigned xc; asm volatile("s_getreg_b32 %0, hwreg(HW_REG_XCC_ID)" : "=s"(xc));
        if (xc & 4u) touch_l2(p.Aq + ((blockIdx.x >> 3) * 64 + (threadIdx.x & 63)) * 64, __builtin_amdgcn_readfirstlane((unsigned)(size_t)lds) + 2048);
    }
    if (blockIdx.x == 0 && wv == 1) {
        const int l = threadIdx.x & 63; const v4u z = {0u, 0u, 0u, 0u};
#pragma unroll
        for (int i = 0; i < 6; ++i) st16(r, F_CONV + i * 1024 + l * 16, z);
        DRAIN();
        for (unsigned spin = 0; spin < (1u << 20); ++spin) {
            if (l < 8) __hip_atomic_fetch_add((unsigned*)(p.ws + F_BEAT + l * 128), 1u, __ATOMIC_RELAXED, __HIP_MEMORY_SCOPE_AGENT);
            unsigned c = ld4(r, l < 8 ? F_TKT + l * 64 : OOB);
            c += __shfl_xor(c, 1); c += __shfl_xor(c, 2); c += __shfl_xor(c, 4);
            if (__builtin_amdgcn_readfirstlane(c) >= 256u) break; }
    }
    if (threadIdx.x == 0) {
        const unsigned bo = F_BEAT + (blockIdx.x & 7) * 128, b0 = ld4(r, bo);
        for (unsigned spin = 0; spin < (1u << 22); ++spin) { MEMBAR(); if (ld4(r, bo) != b0) break; }
        MEMBAR();
        unsigned xcc; asm volatile("s_getreg_b32 %0, hwreg(HW_REG_XCC_ID)" : "=s"(xcc)); xcc &= 7u;
        unsigned* tk = (unsigned*)(p.ws + F_TKT);
        unsigned t = __hip_atomic_fetch_add(tk + xcc * 16, 1u, __ATOMIC_RELAXED, __HIP_MEMORY_SCOPE_AGENT);
        if (t >= 32u) {
            unsigned o = __hip_atomic_fetch_add(tk + 128, 1u, __ATOMIC_RELAXED, __HIP_MEMORY_SCOPE_AGENT);
            for (unsigned spin = 0; spin < (1u << 22); ++spin) { unsigned tot = 0u;
                for (int x = 0; x < 8; ++x) tot += __hip_atomic_load(tk + x * 16, __ATOMIC_RELAXED, __HIP_MEMORY_SCOPE_AGENT);
                if (tot >= 256u) break; }
            for (int x = 0; x < 8; ++x) { const unsigned cnt = __hip_atomic_load(tk + x * 16, __ATOMIC_RELAXED, __HIP_MEMORY_SCOPE_AGENT), fr = cnt < 32u ? 32u - cnt : 0u;
                if (o < fr) { xcc = (unsigned)x; t = cnt + o; break; }
                o -= fr; }
        }
        *(volatile int*)lds = (int)((xcc >> 2) * 128u + (t >> 1) * 8u + (xcc & 3u) * 2u + (t & 1u));
    }
    DRAIN();
    __syncthreads();
    const int role = __builtin_amdgcn_readfirstlane(*(volatile int*)lds) & 255;
    __syncthreads();
    if (role >= 128) bulk_block(p, r, lds, wv, role - 128); else rec_block(p, r, lds, wv, role);
}

extern "C" void kernel_launch(void* const* d_in, const int* in_sizes, int n_in, void* d_out, int out_size, void* d_ws, size_t ws_size, hipStream_t stream) {
    Params p;
    p.Aq = (const bf16_t*)d_in[0]; p.Ak = (const bf16_t*)d_in[1]; p.Av = (const bf16_t*)d_in[2];
    p.shift_k = (const bf16_t*)d_in[3]; p.ssm_k = (const bf16_t*)d_in[4]; p.ssm_D = (const bf16_t*)d_in[5];
    p.mem0 = (const bf16_t*)d_in[7]; p.Wq = (const bf16_t*)d_in[8]; p.Wk = (const bf16_t*)d_in[9];
    p.Wv = (const bf16_t*)d_in[10]; p.Wo = (const bf16_t*)d_in[11]; p.gate_w = (const bf16_t*)d_in[13];
    p.out = (bf16_t*)d_out; p.ws = (unsigned char*)d_ws;
    (void)hipFuncSetAttribute((const void*)k_main, hipFuncAttributeMaxDynamicSharedMemorySize, L_TOTAL);
    k_main<<<256, 256, L_TOTAL, stream>>>(p);
}
```

```cpp
#include <hip/hip_runtime.h>
#include <stdint.h>
typedef unsigned short bf16_t;
using f32x16 = __attribute__((ext_vector_type(16))) float;
typedef unsigned v4u __attribute__((ext_vector_type(4)));
typedef unsigned v2u __attribute__((ext_vector_type(2)));
typedef __amdgpu_buffer_rsrc_t rsrc_t;
typedef __bf16 hbf2 __attribute__((ext_vector_type(2)));
typedef float f32x2 __attribute__((ext_vector_type(2)));
__device__ __forceinline__ unsigned pack2(float a, float b) { f32x2 v = {a, b}; return __builtin_bit_cast(unsigned, __builtin_convertvector(v, hbf2)); }
__device__ __forceinline__ float rbfh(float f) { return __uint_as_float(pack2(f, 0.f) << 16); }
__device__ __forceinline__ float blo(unsigned x) { return __uint_as_float(x << 16); }
__device__ __forceinline__ float bhi(unsigned x) { return __uint_as_float(x & 0xFFFF0000u); }

constexpr int S = 2048, D = 1024;

constexpr unsigned F_KVQ = 0;
constexpr unsigned F_CONV = 32768;
constexpr unsigned F_ERR = 33024;
constexpr unsigned F_CAL = 33792;
constexpr unsigned F_RDY0 = 34304;
constexpr unsigned F_TKT = 34816;
constexpr unsigned F_GO = 36864;
constexpr unsigned F_BEAT = 40960;
constexpr unsigned F_PROG = 49152;
constexpr unsigned F_CVY = 57344;
constexpr unsigned W_A = 8568832;
constexpr unsigned W_GATE = 65536;
constexpr unsigned W_QX = W_A + S * D * 2 + 8192;
constexpr unsigned W_KX = W_QX + S * 4096 * 2;
constexpr unsigned W_VF = W_KX + S * 4096 * 2;
constexpr unsigned W_OX = W_VF + S * 4096 * 2;
constexpr unsigned W_Y = W_OX + S * 4096 * 2;
constexpr unsigned W_U = 115531776;
constexpr unsigned W_TOTAL = W_U + S * D * 2;
constexpr unsigned OOB = 0xF0000000u;
constexpr int L_TOTAL = 163840;

__device__ __forceinline__ v4u ld16(rsrc_t r, unsigned off) { return __builtin_amdgcn_raw_buffer_load_b128(r, off, 0, 16); }
__device__ __forceinline__ v2u ld8(rsrc_t r, unsigned off) { return __builtin_amdgcn_raw_buffer_load_b64(r, off, 0, 16); }
__device__ __forceinline__ unsigned ld4(rsrc_t r, unsigned off) { return __builtin_amdgcn_raw_buffer_load_b32(r, off, 0, 16); }
__device__ __forceinline__ void st16(rsrc_t r, unsigned off, v4u v) { __builtin_amdgcn_raw_buffer_store_b128(v, r, off, 0, 16); }
__device__ __forceinline__ void st4(rsrc_t r, unsigned off, unsigned v) { __builtin_amdgcn_raw_buffer_store_b32(v, r, off, 0, 16); }
#define D1 128
#define D2 77
#define LT_A 1
#define PEN 48
#define CAL_T 450u
#define XSLP 1
#define T2U 2
#define TPRE 48
#define MEMBAR() asm volatile("" ::: "memory")
#define DRAIN() asm volatile("s_waitcnt vmcnt(0)" ::: "memory")
__device__ __forceinline__ int otid_(int wv) { int t; asm volatile("v_mbcnt_lo_u32_b32 %0, -1, 0\n\tv_mbcnt_hi_u32_b32 %0, -1, %0" : "=v"(t)); return wv * 64 + t; }
#define otid() otid_(wv)
__device__ __forceinline__ bool wait_word(rsrc_t r, unsigned off, unsigned want, unsigned code) {
    for (unsigned spin = 0;; ++spin) { MEMBAR();
        const unsigned v = __builtin_amdgcn_readfirstlane(ld4(r, off));
        if (v >= want) { MEMBAR(); return true; }
        if (spin > (1u << 20)) { st4(r, F_ERR, code); return false; }
        __builtin_amdgcn_s_sleep(8); }
}
__device__ __forceinline__ void touch_l2(const void* q, unsigned ldsdst) { asm volatile("s_mov_b32 m0, %1\n\ts_nop 0\n\tglobal_load_lds_dword %0, off" :: "v"(q), "s"(ldsdst) : "m0", "memory"); }
__device__ __forceinline__ unsigned rtnow() { return (unsigned)__builtin_amdgcn_s_memrealtime(); }

struct Params {
    const bf16_t *Aq, *Ak, *Av, *shift_k, *ssm_k, *ssm_D, *mem0, *Wq, *Wk, *Wv, *Wo, *gate_w;
    bf16_t* out; unsigned char* ws;
};
__device__ __forceinline__ unsigned ho_base(const void* ws) { const size_t a = (size_t)ws; return (unsigned)((((a + W_TOTAL + 0x7FFFFFu) >> 23) << 23) - a); }
__device__ __forceinline__ v4u mk_srd(const void* p, unsigned bytes) { const size_t a = (size_t)p; v4u d;
    d.x = __builtin_amdgcn_readfirstlane((unsigned)a); d.y = __builtin_amdgcn_readfirstlane((unsigned)(a >> 32) & 0xFFFFu); d.z = __builtin_amdgcn_readfirstlane(bytes); d.w = 0x00020000u; return d; }
constexpr unsigned HO_BYTES = 2097152;
constexpr int L_KV = 90112, L_MS = 139264, L_IMG = 155648, IMG_PS = 1056, IMG_MS = 2112, LB_R = 8192, LB_STG = 16384, LB_STGP = 8704;
static_assert(F_KVQ == 0 && F_ERR == 33024 && F_CONV == 32768 && F_CAL == 33792 && F_RDY0 == 34304 && F_GO == 36864 && F_PROG == 49152 && W_QX == 12771328 && W_KX == 29548544 && W_OX == 63102976 && W_VF == 46325760 && L_KV == 90112 && L_MS == 139264 && L_IMG == 155648 && IMG_PS == 1056 && IMG_MS == 2112 && LB_R == 8192 && LB_STG == 16384 && LB_STGP == 8704, "constants of the generated code");
#define REC_S_IN "{s[8:11]}"(sh), "{s[12:15]}"(sw), "{s[16:19]}"(s1), "{s[20:23]}"(s2), "{s29}"(D1), "{s30}"(D2), "{s43}"(w), "{s44}"(id), "{s49}"(rb), "{s50}"(PEN), "{s51}"(LT_A), "{s45}"(F_GO + (cp * 4 + w) * 128)
__device__ __forceinline__ void rec_a(const Params& p, unsigned lb, const int wv, const int id, const int cp) {
    const int w = wv, l = otid() & 63, n = l & 15, g = l >> 4, h = id, rb = id;
    const v4u sh = mk_srd(p.ws + ho_base(p.ws), HO_BYTES - 8192), sw = mk_srd(p.ws, W_TOTAL), s1 = mk_srd(w < 2 ? p.Wq : p.Wk, 1024 * 4096 * 2), s2 = s1;
    const int r8 = l >> 3, pc = l & 7, pp = n >> 3, bb = (n >> 2) & 1, f = (g & 1) | (pp << 1);
    const unsigned stg = lb + 131072 + w * 8192;
    const unsigned g_w = r8 * 8192 + h * 256 + (w & 1) * 128 + pc * 16;
    const unsigned wb0 = stg + r8 * 128 + (((pc >> 1) ^ (r8 >> 2)) << 5) + (pc & 1) * 16, wb1 = wb0 ^ 64u;
    const unsigned rb0 = stg + (16 * (g >> 1) + 8 * pp + 4 * (g & 1) + bb) * 128 + (f << 5) + (n & 3) * 8;
    const unsigned a_mem = cp * 32768 + (w >> 1) * 16384 + (w & 1) * 4096 + l * 16;
    const unsigned l_ms = lb + L_MS + l * 16;
    const unsigned l_iw = lb + L_IMG + (w >> 1) * IMG_MS + (g >> 1) * IMG_PS + (64 * (w & 1) + n) * 8 + (g & 1) * 4;
    const unsigned l_iq = lb + L_IMG + (n & 1) * IMG_PS + (8 * g + (n >> 2)) * 8;
    const unsigned a_vm = (16 + (h >> 2)) * 16384 + (h & 3) * 2048 + w * 512 + (l & 31) * 16;
    const unsigned a_o = n < 8 ? (8 + (n >> 2) * 4 + (h >> 3)) * 16384 + (h & 7) * 1024 + w * 256 + (n & 3) * 64 + g * 16 : OOB;
    const unsigned a_ox = (((w & 1) * 32 + h) * 4 + w) * 1024 + l * 16;
    asm volatile(
        "s_nop 4\n\t"
        "s_lshl_b32 s34, s43, 2\n\t"
        "s_add_u32 s34, s34, 0\n\t"
        "s_and_b32 s34, s34, 15\n\t"
        "s_lshl_b32 s34, s34, 19\n\t"
        "buffer_load_dwordx4 v[224:227], v96, s[16:19], s34 offen nt\n\t"
        "s_add_u32 s34, s34, 0x10000\n\t"
        "buffer_load_dwordx4 v[228:231], v96, s[16:19], s34 offen nt\n\t"
        "s_add_u32 s34, s34, 0x10000\n\t"
        "buffer_load_dwordx4 v[232:235], v96, s[16:19], s34 offen nt\n\t"
        "s_add_u32 s34, s34, 0x10000\n\t"
        "buffer_load_dwordx4 v[236:239], v96, s[16:19], s34 offen nt\n\t"
        "s_add_u32 s34, s34, 0x10000\n\t"
        "buffer_load_dwordx4 v[240:243], v96, s[16:19], s34 offen nt\n\t"
        "s_add_u32 s34, s34, 0x10000\n\t"
        "buffer_load_dwordx4 v[244:247], v96, s[16:19], s34 offen nt\n\t"
        "s_add_u32 s34, s34, 0x10000\n\t"
        "buffer_load_dwordx4 v[248:251], v96, s[16:19], s34 offen nt\n\t"
        "s_add_u32 s34, s34, 0x10000\n\t"
        "buffer_load_dwordx4 v[252:255], v96, s[16:19], s34 offen nt\n\t"
        "s_lshl_b32 s34, s43, 2\n\t"
        "s_add_u32 s34, s34, 1\n\t"
        "s_and_b32 s34, s34, 15\n\t"
        "s_lshl_b32 s34, s34, 19\n\t"
        "buffer_load_dwordx4 v[0:3], v96, s[16:19], s34 offen nt\n\t"
        "s_add_u32 s34, s34, 0x10000\n\t"
        "buffer_load_dwordx4 v[4:7], v96, s[16:19], s34 offen nt\n\t"
        "s_add_u32 s34, s34, 0x10000\n\t"
        "buffer_load_dwordx4 v[8:11], v96, s[16:19], s34 offen nt\n\t"
        "s_add_u32 s34, s34, 0x10000\n\t"
        "buffer_load_dwordx4 v[12:15], v96, s[16:19], s34 offen nt\n\t"
        "s_add_u32 s34, s34, 0x10000\n\t"
        "buffer_load_dwordx4 v[16:19], v96, s[16:19], s34 offen nt\n\t"
        "s_add_u32 s34, s34, 0x10000\n\t"
        "buffer_load_dwordx4 v[20:23], v96, s[16:19], s34 offen nt\n\t"
        "s_add_u32 s34, s34, 0x10000\n\t"
        "buffer_load_dwordx4 v[24:27], v96, s[16:19], s34 offen nt\n\t"
        "s_add_u32 s34, s34, 0x10000\n\t"
        "buffer_load_dwordx4 v[28:31], v96, s[16:19], s34 offen nt\n\t"
        "s_lshl_b32 s34, s43, 2\n\t"
        "s_add_u32 s34, s34, 2\n\t"
        "s_and_b32 s34, s34, 15\n\t"
        "s_lshl_b32 s34, s34, 19\n\t"
        "buffer_load_dwordx4 v[32:35], v96, s[16:19], s34 offen nt\n\t"
        "s_add_u32 s34, s34, 0x10000\n\t"
        "buffer_load_dwordx4 v[36:39], v96, s[16:19], s34 offen nt\n\t"
        "s_add_u32 s34, s34, 0x10000\n\t"
        "buffer_load_dwordx4 v[40:43], v96, s[16:19], s34 offen nt\n\t"
        "s_add_u32 s34, s34, 0x10000\n\t"
        "buffer_load_dwordx4 v[44:47], v96, s[16:19], s34 offen nt\n\t"
        "s_add_u32 s34, s34, 0x10000\n\t"
        "buffer_load_dwordx4 v[48:51], v96, s[16:19], s34 offen nt\n\t"
        "s_add_u32 s34, s34, 0x10000\n\t"
        "buffer_load_dwordx4 v[52:55], v96, s[16:19], s34 offen nt\n\t"
        "s_add_u32 s34, s34, 0x10000\n\t"
        "buffer_load_dwordx4 v[56:59], v96, s[16:19], s34 offen nt\n\t"
        "s_add_u32 s34, s34, 0x10000\n\t"
        "buffer_load_dwordx4 v[60:63], v96, s[16:19], s34 offen nt\n\t"
        "v_cmp_le_i32_e64 s[60:61], 0, v126\n\t"
        "v_cmp_le_i32_e64 s[62:63], 1, v126\n\t"
        "v_cmp_le_i32_e64 s[64:65], 2, v126\n\t"
        "v_cmp_le_i32_e64 s[66:67], 3, v126\n\t"
        "v_cmp_le_i32_e64 s[68:69], 16, v126\n\t"
        "v_cmp_le_i32_e64 s[70:71], 17, v126\n\t"
        "v_cmp_le_i32_e64 s[72:73], 18, v126\n\t"
        "v_cmp_le_i32_e64 s[74:75], 19, v126\n\t"
        "s_mul_i32 s35, s43, 10240\n\t"
        "v_add_u32_e32 v126, s35, v124\n\t"
        "s_waitcnt vmcnt(16)\n\t"
        "ds_write_b128 v97, v[224:227] offset:0\n\t"
        "ds_write_b128 v98, v[228:231] offset:1024\n\t"
        "ds_write_b128 v97, v[232:235] offset:2048\n\t"
        "ds_write_b128 v98, v[236:239] offset:3072\n\t"
        "ds_write_b128 v97, v[240:243] offset:4096\n\t"
        "ds_write_b128 v98, v[244:247] offset:5120\n\t"
        "ds_write_b128 v97, v[248:251] offset:6144\n\t"
        "ds_write_b128 v98, v[252:255] offset:7168\n\t"
        "ds_read_b64_tr_b16 a[0:1], v99 offset:0\n\t"
        "ds_read_b64_tr_b16 a[2:3], v99 offset:256\n\t"
        "ds_read_b64_tr_b16 a[4:5], v99 offset:4096\n\t"
        "ds_read_b64_tr_b16 a[6:7], v99 offset:4352\n\t"
        "s_waitcnt lgkmcnt(8)\n\t"
        "ds_read_b64_tr_b16 a[8:9], v100 offset:0\n\t"
        "ds_read_b64_tr_b16 a[10:11], v100 offset:256\n\t"
        "ds_read_b64_tr_b16 a[12:13], v100 offset:4096\n\t"
        "ds_read_b64_tr_b16 a[14:15], v100 offset:4352\n\t"
        "s_waitcnt lgkmcnt(8)\n\t"
        "ds_read_b64_tr_b16 a[16:17], v101 offset:0\n\t"
        "ds_read_b64_tr_b16 a[18:19], v101 offset:256\n\t"
        "ds_read_b64_tr_b16 a[20:21], v101 offset:4096\n\t"
        "ds_read_b64_tr_b16 a[22:23], v101 offset:4352\n\t"
        "s_waitcnt lgkmcnt(8)\n\t"
        "ds_read_b64_tr_b16 v[104:105], v102 offset:0\n\t"
        "ds_read_b64_tr_b16 v[106:107], v102 offset:256\n\t"
        "ds_read_b64_tr_b16 v[108:109], v102 offset:4096\n\t"
        "ds_read_b64_tr_b16 v[110:111], v102 offset:4352\n\t"
        "s_lshl_b32 s34, s43, 2\n\t"
        "s_add_u32 s34, s34, 3\n\t"
        "s_and_b32 s34, s34, 15\n\t"
        "s_lshl_b32 s34, s34, 19\n\t"
        "buffer_load_dwordx4 v[64:67], v96, s[16:19], s34 offen nt\n\t"
        "s_add_u32 s34, s34, 0x10000\n\t"
        "buffer_load_dwordx4 v[68:71], v96, s[16:19], s34 offen nt\n\t"
        "s_add_u32 s34, s34, 0x10000\n\t"
        "buffer_load_dwordx4 v[72:75], v96, s[16:19], s34 offen nt\n\t"
        "s_add_u32 s34, s34, 0x10000\n\t"
        "buffer_load_dwordx4 v[76:79], v96, s[16:19], s34 offen nt\n\t"
        "s_add_u32 s34, s34, 0x10000\n\t"
        "buffer_load_dwordx4 v[80:83], v96, s[16:19], s34 offen nt\n\t"
        "s_add_u32 s34, s34, 0x10000\n\t"
        "buffer_load_dwordx4 v[84:87], v96, s[16:19], s34 offen nt\n\t"
        "s_add_u32 s34, s34, 0x10000\n\t"
        "buffer_load_dwordx4 v[88:91], v96, s[16:19], s34 offen nt\n\t"
        "s_add_u32 s34, s34, 0x10000\n\t"
        "buffer_load_dwordx4 v[92:95], v96, s[16:19], s34 offen nt\n\t"
        "s_waitcnt lgkmcnt(0)\n\t"
        "ds_write_b128 v117, v[104:107] offset:0\n\t"
        "ds_write_b128 v117, v[108:111] offset:1024\n\t"
        "s_waitcnt vmcnt(16)\n\t"
        "ds_write_b128 v97, v[0:3] offset:0\n\t"
        "ds_write_b128 v98, v[4:7] offset:1024\n\t"
        "ds_write_b128 v97, v[8:11] offset:2048\n\t"
        "ds_write_b128 v98, v[12:15] offset:3072\n\t"
        "ds_write_b128 v97, v[16:19] offset:4096\n\t"
        "ds_write_b128 v98, v[20:23] offset:5120\n\t"
        "ds_write_b128 v97, v[24:27] offset:6144\n\t"
        "ds_write_b128 v98, v[28:31] offset:7168\n\t"
        "ds_read_b64_tr_b16 a[24:25], v99 offset:0\n\t"
        "ds_read_b64_tr_b16 a[26:27], v99 offset:256\n\t"
        "ds_read_b64_tr_b16 a[28:29], v99 offset:4096\n\t"
        "ds_read_b64_tr_b16 a[30:31], v99 offset:4352\n\t"
        "s_waitcnt lgkmcnt(8)\n\t"
        "ds_read_b64_tr_b16 a[32:33], v100 offset:0\n\t"
        "ds_read_b64_tr_b16 a[34:35], v100 offset:256\n\t"
        "ds_read_b64_tr_b16 a[36:37], v100 offset:4096\n\t"
        "ds_read_b64_tr_b16 a[38:39], v100 offset:4352\n\t"
        "s_waitcnt lgkmcnt(8)\n\t"
        "ds_read_b64_tr_b16 a[40:41], v101 offset:0\n\t"
        "ds_read_b64_tr_b16 a[42:43], v101 offset:256\n\t"
        "ds_read_b64_tr_b16 a[44:45], v101 offset:4096\n\t"
        "ds_read_b64_tr_b16 a[46:47], v101 offset:4352\n\t"
        "s_waitcnt lgkmcnt(8)\n\t"
        "ds_read_b64_tr_b16 v[104:105], v102 offset:0\n\t"
        "ds_read_b64_tr_b16 v[106:107], v102 offset:256\n\t"
        "ds_read_b64_tr_b16 v[108:109], v102 offset:4096\n\t"
        "ds_read_b64_tr_b16 v[110:111], v102 offset:4352\n\t"
        "s_lshl_b32 s34, s43, 2\n\t"
        "s_add_u32 s34, s34, 4\n\t"
        "s_and_b32 s34, s34, 15\n\t"
        "s_lshl_b32 s34, s34, 19\n\t"
        "buffer_load_dwordx4 a[96:99], v96, s[16:19], s34 offen nt\n\t"
        "s_add_u32 s34, s34, 0x10000\n\t"
        "buffer_load_dwordx4 a[100:103], v96, s[16:19], s34 offen nt\n\t"
        "s_add_u32 s34, s34, 0x10000\n\t"
        "buffer_load_dwordx4 a[104:107], v96, s[16:19], s34 offen nt\n\t"
        "s_add_u32 s34, s34, 0x10000\n\t"
        "buffer_load_dwordx4 a[108:111], v96, s[16:19], s34 offen nt\n\t"
        "s_add_u32 s34, s34, 0x10000\n\t"
        "buffer_load_dwordx4 a[112:115], v96, s[16:19], s34 offen nt\n\t"
        "s_add_u32 s34, s34, 0x10000\n\t"
        "buffer_load_dwordx4 a[116:119], v96, s[16:19], s34 offen nt\n\t"
        "s_add_u32 s34, s34, 0x10000\n\t"
        "buffer_load_dwordx4 a[120:123], v96, s[16:19], s34 offen nt\n\t"
        "s_add_u32 s34, s34, 0x10000\n\t"
        "buffer_load_dwordx4 a[124:127], v96, s[16:19], s34 offen nt\n\t"
        "s_waitcnt lgkmcnt(0)\n\t"
        "ds_write_b128 v117, v[104:107] offset:2048\n\t"
        "ds_write_b128 v117, v[108:111] offset:3072\n\t"
        "s_waitcnt vmcnt(16)\n\t"
        "ds_write_b128 v97, v[32:35] offset:0\n\t"
        "ds_write_b128 v98, v[36:39] offset:1024\n\t"
        "ds_write_b128 v97, v[40:43] offset:2048\n\t"
        "ds_write_b128 v98, v[44:47] offset:3072\n\t"
        "ds_write_b128 v97, v[48:51] offset:4096\n\t"
        "ds_write_b128 v98, v[52:55] offset:5120\n\t"
        "ds_write_b128 v97, v[56:59] offset:6144\n\t"
        "ds_write_b128 v98, v[60:63] offset:7168\n\t"
        "ds_read_b64_tr_b16 a[48:49], v99 offset:0\n\t"
        "ds_read_b64_tr_b16 a[50:51], v99 offset:256\n\t"
        "ds_read_b64_tr_b16 a[52:53], v99 offset:4096\n\t"
        "ds_read_b64_tr_b16 a[54:55], v99 offset:4352\n\t"
        "s_waitcnt lgkmcnt(8)\n\t"
        "ds_read_b64_tr_b16 a[56:57], v100 offset:0\n\t"
        "ds_read_b64_tr_b16 a[58:59], v100 offset:256\n\t"
        "ds_read_b64_tr_b16 a[60:61], v100 offset:4096\n\t"
        "ds_read_b64_tr_b16 a[62:63], v100 offset:4352\n\t"
        "s_waitcnt lgkmcnt(8)\n\t"
        "ds_read_b64_tr_b16 a[64:65], v101 offset:0\n\t"
        "ds_read_b64_tr_b16 a[66:67], v101 offset:256\n\t"
        "ds_read_b64_tr_b16 a[68:69], v101 offset:4096\n\t"
        "ds_read_b64_tr_b16 a[70:71], v101 offset:4352\n\t"
        "s_waitcnt lgkmcnt(8)\n\t"
        "ds_read_b64_tr_b16 v[104:105], v102 offset:0\n\t"
        "ds_read_b64_tr_b16 v[106:107], v102 offset:256\n\t"
        "ds_read_b64_tr_b16 v[108:109], v102 offset:4096\n\t"
        "ds_read_b64_tr_b16 v[110:111], v102 offset:4352\n\t"
        "s_lshl_b32 s34, s43, 2\n\t"
        "s_add_u32 s34, s34, 5\n\t"
        "s_and_b32 s34, s34, 15\n\t"
        "s_lshl_b32 s34, s34, 19\n\t"
        "buffer_load_dwordx4 a[128:131], v96, s[16:19], s34 offen nt\n\t"
        "s_add_u32 s34, s34, 0x10000\n\t"
        "buffer_load_dwordx4 a[132:135], v96, s[16:19], s34 offen nt\n\t"
        "s_add_u32 s34, s34, 0x10000\n\t"
        "buffer_load_dwordx4 a[136:139], v96, s[16:19], s34 offen nt\n\t"
        "s_add_u32 s34, s34, 0x10000\n\t"
        "buffer_load_dwordx4 a[140:143], v96, s[16:19], s34 offen nt\n\t"
        "s_add_u32 s34, s34, 0x10000\n\t"
        "buffer_load_dwordx4 a[144:147], v96, s[16:19], s34 offen nt\n\t"
        "s_add_u32 s34, s34, 0x10000\n\t"
        "buffer_load_dwordx4 a[148:151], v96, s[16:19], s34 offen nt\n\t"
        "s_add_u32 s34, s34, 0x10000\n\t"
        "buffer_load_dwordx4 a[152:155], v96, s[16:19], s34 offen nt\n\t"
        "s_add_u32 s34, s34, 0x10000\n\t"
        "buffer_load_dwordx4 a[156:159], v96, s[16:19], s34 offen nt\n\t"
        "s_waitcnt lgkmcnt(0)\n\t"
        "ds_write_b128 v117, v[104:107] offset:4096\n\t"
        "ds_write_b128 v117, v[108:111] offset:5120\n\t"
        "s_waitcnt vmcnt(16)\n\t"
        "ds_write_b128 v97, v[64:67] offset:0\n\t"
        "ds_write_b128 v98, v[68:71] offset:1024\n\t"
        "ds_write_b128 v97, v[72:75] offset:2048\n\t"
        "ds_write_b128 v98, v[76:79] offset:3072\n\t"
        "ds_write_b128 v97, v[80:83] offset:4096\n\t"
        "ds_write_b128 v98, v[84:87] offset:5120\n\t"
        "ds_write_b128 v97, v[88:91] offset:6144\n\t"
        "ds_write_b128 v98, v[92:95] offset:7168\n\t"
        "ds_read_b64_tr_b16 a[72:73], v99 offset:0\n\t"
        "ds_read_b64_tr_b16 a[74:75], v99 offset:256\n\t"
        "ds_read_b64_tr_b16 a[76:77], v99 offset:4096\n\t"
        "ds_read_b64_tr_b16 a[78:79], v99 offset:4352\n\t"
        "s_waitcnt lgkmcnt(8)\n\t"
        "ds_read_b64_tr_b16 a[80:81], v100 offset:0\n\t"
        "ds_read_b64_tr_b16 a[82:83], v100 offset:256\n\t"
        "ds_read_b64_tr_b16 a[84:85], v100 offset:4096\n\t"
        "ds_read_b64_tr_b16 a[86:87], v100 offset:4352\n\t"
        "s_waitcnt lgkmcnt(8)\n\t"
        "ds_read_b64_tr_b16 a[88:89], v101 offset:0\n\t"
        "ds_read_b64_tr_b16 a[90:91], v101 offset:256\n\t"
        "ds_read_b64_tr_b16 a[92:93], v101 offset:4096\n\t"
        "ds_read_b64_tr_b16 a[94:95], v101 offset:4352\n\t"
        "s_waitcnt lgkmcnt(8)\n\t"
        "ds_read_b64_tr_b16 v[104:105], v102 offset:0\n\t"
        "ds_read_b64_tr_b16 v[106:107], v102 offset:256\n\t"
        "ds_read_b64_tr_b16 v[108:109], v102 offset:4096\n\t"
        "ds_read_b64_tr_b16 v[110:111], v102 offset:4352\n\t"
        "s_lshl_b32 s34, s43, 2\n\t"
        "s_add_u32 s34, s34, 6\n\t"
        "s_and_b32 s34, s34, 15\n\t"
        "s_lshl_b32 s34, s34, 19\n\t"
        "buffer_load_dwordx4 a[160:163], v96, s[16:19], s34 offen nt\n\t"
        "s_add_u32 s34, s34, 0x10000\n\t"
        "buffer_load_dwordx4 a[164:167], v96, s[16:19], s34 offen nt\n\t"
        "s_add_u32 s34, s34, 0x10000\n\t"
        "buffer_load_dwordx4 a[168:171], v96, s[16:19], s34 offen nt\n\t"
        "s_add_u32 s34, s34, 0x10000\n\t"
        "buffer_load_dwordx4 a[172:175], v96, s[16:19], s34 offen nt\n\t"
        "s_add_u32 s34, s34, 0x10000\n\t"
        "buffer_load_dwordx4 a[176:179], v96, s[16:19], s34 offen nt\n\t"
        "s_add_u32 s34, s34, 0x10000\n\t"
        "buffer_load_dwordx4 a[180:183], v96, s[16:19], s34 offen nt\n\t"
        "s_add_u32 s34, s34, 0x10000\n\t"
        "buffer_load_dwordx4 a[184:187], v96, s[16:19], s34 offen nt\n\t"
        "s_add_u32 s34, s34, 0x10000\n\t"
        "buffer_load_dwordx4 a[188:191], v96, s[16:19], s34 offen nt\n\t"
        "s_waitcnt lgkmcnt(0)\n\t"
        "ds_write_b128 v117, v[104:107] offset:6144\n\t"
        "ds_write_b128 v117, v[108:111] offset:7168\n\t"
        "s_waitcnt vmcnt(16)\n\t"
        "ds_write_b128 v97, a[96:99] offset:0\n\t"
        "ds_write_b128 v98, a[100:103] offset:1024\n\t"
        "ds_write_b128 v97, a[104:107] offset:2048\n\t"
        "ds_write_b128 v98, a[108:111] offset:3072\n\t"
        "ds_write_b128 v97, a[112:115] offset:4096\n\t"
        "ds_write_b128 v98, a[116:119] offset:5120\n\t"
        "ds_write_b128 v97, a[120:123] offset:6144\n\t"
        "ds_write_b128 v98, a[124:127] offset:7168\n\t"
        "ds_read_b64_tr_b16 a[96:97], v99 offset:0\n\t"
        "ds_read_b64_tr_b16 a[98:99], v99 offset:256\n\t"
        "ds_read_b64_tr_b16 a[100:101], v99 offset:4096\n\t"
        "ds_read_b64_tr_b16 a[102:103], v99 offset:4352\n\t"
        "s_waitcnt lgkmcnt(8)\n\t"
        "ds_read_b64_tr_b16 a[104:105], v100 offset:0\n\t"
        "ds_read_b64_tr_b16 a[106:107], v100 offset:256\n\t"
        "ds_read_b64_tr_b16 a[108:109], v100 offset:4096\n\t"
        "ds_read_b64_tr_b16 a[110:111], v100 offset:4352\n\t"
        "s_waitcnt lgkmcnt(8)\n\t"
        "ds_read_b64_tr_b16 a[112:113], v101 offset:0\n\t"
        "ds_read_b64_tr_b16 a[114:115], v101 offset:256\n\t"
        "ds_read_b64_tr_b16 a[116:117], v101 offset:4096\n\t"
        "ds_read_b64_tr_b16 a[118:119], v101 offset:4352\n\t"
        "s_waitcnt lgkmcnt(8)\n\t"
        "ds_read_b64_tr_b16 v[104:105], v102 offset:0\n\t"
        "ds_read_b64_tr_b16 v[106:107], v102 offset:256\n\t"
        "ds_read_b64_tr_b16 v[108:109], v102 offset:4096\n\t"
        "ds_read_b64_tr_b16 v[110:111], v102 offset:4352\n\t"
        "s_lshl_b32 s34, s43, 2\n\t"
        "s_add_u32 s34, s34, 7\n\t"
        "s_and_b32 s34, s34, 15\n\t"
        "s_lshl_b32 s34, s34, 19\n\t"
        "buffer_load_dwordx4 a[192:195], v96, s[16:19], s34 offen nt\n\t"
        "s_add_u32 s34, s34, 0x10000\n\t"
        "buffer_load_dwordx4 a[196:199], v96, s[16:19], s34 offen nt\n\t"
        "s_add_u32 s34, s34, 0x10000\n\t"
        "buffer_load_dwordx4 a[200:203], v96, s[16:19], s34 offen nt\n\t"
        "s_add_u32 s34, s34, 0x10000\n\t"
        "buffer_load_dwordx4 a[204:207], v96, s[16:19], s34 offen nt\n\t"
        "s_add_u32 s34, s34, 0x10000\n\t"
        "buffer_load_dwordx4 a[208:211], v96, s[16:19], s34 offen nt\n\t"
        "s_add_u32 s34, s34, 0x10000\n\t"
        "buffer_load_dwordx4 a[212:215], v96, s[16:19], s34 offen nt\n\t"
        "s_add_u32 s34, s34, 0x10000\n\t"
        "buffer_load_dwordx4 a[216:219], v96, s[16:19], s34 offen nt\n\t"
        "s_add_u32 s34, s34, 0x10000\n\t"
        "buffer_load_dwordx4 a[220:223], v96, s[16:19], s34 offen nt\n\t"
        "s_waitcnt lgkmcnt(0)\n\t"
        "ds_write_b128 v126, v[104:107] offset:0\n\t"
        "ds_write_b128 v126, v[108:111] offset:1024\n\t"
        "s_waitcnt vmcnt(16)\n\t"
        "ds_write_b128 v97, a[128:131] offset:0\n\t"
        "ds_write_b128 v98, a[132:135] offset:1024\n\t"
        "ds_write_b128 v97, a[136:139] offset:2048\n\t"
        "ds_write_b128 v98, a[140:143] offset:3072\n\t"
        "ds_write_b128 v97, a[144:147] offset:4096\n\t"
        "ds_write_b128 v98, a[148:151] offset:5120\n\t"
        "ds_write_b128 v97, a[152:155] offset:6144\n\t"
        "ds_write_b128 v98, a[156:159] offset:7168\n\t"
        "ds_read_b64_tr_b16 a[120:121], v99 offset:0\n\t"
        "ds_read_b64_tr_b16 a[122:123], v99 offset:256\n\t"
        "ds_read_b64_tr_b16 a[124:125], v99 offset:4096\n\t"
        "ds_read_b64_tr_b16 a[126:127], v99 offset:4352\n\t"
        "s_waitcnt lgkmcnt(8)\n\t"
        "ds_read_b64_tr_b16 a[128:129], v100 offset:0\n\t"
        "ds_read_b64_tr_b16 a[130:131], v100 offset:256\n\t"
        "ds_read_b64_tr_b16 a[132:133], v100 offset:4096\n\t"
        "ds_read_b64_tr_b16 a[134:135], v100 offset:4352\n\t"
        "s_waitcnt lgkmcnt(8)\n\t"
        "ds_read_b64_tr_b16 a[136:137], v101 offset:0\n\t"
        "ds_read_b64_tr_b16 a[138:139], v101 offset:256\n\t"
        "ds_read_b64_tr_b16 a[140:141], v101 offset:4096\n\t"
        "ds_read_b64_tr_b16 a[142:143], v101 offset:4352\n\t"
        "s_waitcnt lgkmcnt(8)\n\t"
        "ds_read_b64_tr_b16 v[104:105], v102 offset:0\n\t"
        "ds_read_b64_tr_b16 v[106:107], v102 offset:256\n\t"
        "ds_read_b64_tr_b16 v[108:109], v102 offset:4096\n\t"
        "ds_read_b64_tr_b16 v[110:111], v102 offset:4352\n\t"
        "s_lshl_b32 s34, s43, 2\n\t"
        "s_add_u32 s34, s34, 8\n\t"
        "s_and_b32 s34, s34, 15\n\t"
        "s_lshl_b32 s34, s34, 19\n\t"
        "buffer_load_dwordx4 a[224:227], v96, s[16:19], s34 offen nt\n\t"
        "s_add_u32 s34, s34, 0x10000\n\t"
        "buffer_load_dwordx4 a[228:231], v96, s[16:19], s34 offen nt\n\t"
        "s_add_u32 s34, s34, 0x10000\n\t"
        "buffer_load_dwordx4 a[232:235], v96, s[16:19], s34 offen nt\n\t"
        "s_add_u32 s34, s34, 0x10000\n\t"
        "buffer_load_dwordx4 a[236:239], v96, s[16:19], s34 offen nt\n\t"
        "s_add_u32 s34, s34, 0x10000\n\t"
        "buffer_load_dwordx4 a[240:243], v96, s[16:19], s34 offen nt\n\t"
        "s_add_u32 s34, s34, 0x10000\n\t"
        "buffer_load_dwordx4 a[244:247], v96, s[16:19], s34 offen nt\n\t"
        "s_add_u32 s34, s34, 0x10000\n\t"
        "buffer_load_dwordx4 a[248:251], v96, s[16:19], s34 offen nt\n\t"
        "s_add_u32 s34, s34, 0x10000\n\t"
        "buffer_load_dwordx4 a[252:255], v96, s[16:19], s34 offen nt\n\t"
        "s_waitcnt lgkmcnt(0)\n\t"
        "ds_write_b128 v126, v[104:107] offset:2048\n\t"
        "ds_write_b128 v126, v[108:111] offset:3072\n\t"
        "s_waitcnt vmcnt(16)\n\t"
        "ds_write_b128 v97, a[160:163] offset:0\n\t"
        "ds_write_b128 v98, a[164:167] offset:1024\n\t"
        "ds_write_b128 v97, a[168:171] offset:2048\n\t"
        "ds_write_b128 v98, a[172:175] offset:3072\n\t"
        "ds_write_b128 v97, a[176:179] offset:4096\n\t"
        "ds_write_b128 v98, a[180:183] offset:5120\n\t"
        "ds_write_b128 v97, a[184:187] offset:6144\n\t"
        "ds_write_b128 v98, a[188:191] offset:7168\n\t"
        "ds_read_b64_tr_b16 a[144:145], v99 offset:0\n\t"
        "ds_read_b64_tr_b16 a[146:147], v99 offset:256\n\t"
        "ds_read_b64_tr_b16 a[148:149], v99 offset:4096\n\t"
        "ds_read_b64_tr_b16 a[150:151], v99 offset:4352\n\t"
        "s_waitcnt lgkmcnt(8)\n\t"
        "ds_read_b64_tr_b16 a[152:153], v100 offset:0\n\t"
        "ds_read_b64_tr_b16 a[154:155], v100 offset:256\n\t"
        "ds_read_b64_tr_b16 a[156:157], v100 offset:4096\n\t"
        "ds_read_b64_tr_b16 a[158:159], v100 offset:4352\n\t"
        "s_waitcnt lgkmcnt(8)\n\t"
        "ds_read_b64_tr_b16 a[160:161], v101 offset:0\n\t"
        "ds_read_b64_tr_b16 a[162:163], v101 offset:256\n\t"
        "ds_read_b64_tr_b16 a[164:165], v101 offset:4096\n\t"
        "ds_read_b64_tr_b16 a[166:167], v101 offset:4352\n\t"
        "s_waitcnt lgkmcnt(8)\n\t"
        "ds_read_b64_tr_b16 v[104:105], v102 offset:0\n\t"
        "ds_read_b64_tr_b16 v[106:107], v102 offset:256\n\t"
        "ds_read_b64_tr_b16 v[108:109], v102 offset:4096\n\t"
        "ds_read_b64_tr_b16 v[110:111], v102 offset:4352\n\t"
        "s_lshl_b32 s34, s43, 2\n\t"
        "s_add_u32 s34, s34, 9\n\t"
        "s_and_b32 s34, s34, 15\n\t"
        "s_lshl_b32 s34, s34, 19\n\t"
        "buffer_load_dwordx4 v[128:131], v96, s[16:19], s34 offen nt\n\t"
        "s_add_u32 s34, s34, 0x10000\n\t"
        "buffer_load_dwordx4 v[132:135], v96, s[16:19], s34 offen nt\n\t"
        "s_add_u32 s34, s34, 0x10000\n\t"
        "buffer_load_dwordx4 v[136:139], v96, s[16:19], s34 offen nt\n\t"
        "s_add_u32 s34, s34, 0x10000\n\t"
        "buffer_load_dwordx4 v[140:143], v96, s[16:19], s34 offen nt\n\t"
        "s_add_u32 s34, s34, 0x10000\n\t"
        "buffer_load_dwordx4 v[144:147], v96, s[16:19], s34 offen nt\n\t"
        "s_add_u32 s34, s34, 0x10000\n\t"
        "buffer_load_dwordx4 v[148:151], v96, s[16:19], s34 offen nt\n\t"
        "s_add_u32 s34, s34, 0x10000\n\t"
        "buffer_load_dwordx4 v[152:155], v96, s[16:19], s34 offen nt\n\t"
        "s_add_u32 s34, s34, 0x10000\n\t"
        "buffer_load_dwordx4 v[156:159], v96, s[16:19], s34 offen nt\n\t"
        "s_waitcnt lgkmcnt(0)\n\t"
        "ds_write_b128 v126, v[104:107] offset:4096\n\t"
        "ds_write_b128 v126, v[108:111] offset:5120\n\t"
        "s_waitcnt vmcnt(16)\n\t"
        "ds_write_b128 v97, a[192:195] offset:0\n\t"
        "ds_write_b128 v98, a[196:199] offset:1024\n\t"
        "ds_write_b128 v97, a[200:203] offset:2048\n\t"
        "ds_write_b128 v98, a[204:207] offset:3072\n\t"
        "ds_write_b128 v97, a[208:211] offset:4096\n\t"
        "ds_write_b128 v98, a[212:215] offset:5120\n\t"
        "ds_write_b128 v97, a[216:219] offset:6144\n\t"
        "ds_write_b128 v98, a[220:223] offset:7168\n\t"
        "ds_read_b64_tr_b16 a[168:169], v99 offset:0\n\t"
        "ds_read_b64_tr_b16 a[170:171], v99 offset:256\n\t"
        "ds_read_b64_tr_b16 a[172:173], v99 offset:4096\n\t"
        "ds_read_b64_tr_b16 a[174:175], v99 offset:4352\n\t"
        "s_waitcnt lgkmcnt(8)\n\t"
        "ds_read_b64_tr_b16 a[176:177], v100 offset:0\n\t"
        "ds_read_b64_tr_b16 a[178:179], v100 offset:256\n\t"
        "ds_read_b64_tr_b16 a[180:181], v100 offset:4096\n\t"
        "ds_read_b64_tr_b16 a[182:183], v100 offset:4352\n\t"
        "s_waitcnt lgkmcnt(8)\n\t"
        "ds_read_b64_tr_b16 a[184:185], v101 offset:0\n\t"
        "ds_read_b64_tr_b16 a[186:187], v101 offset:256\n\t"
        "ds_read_b64_tr_b16 a[188:189], v101 offset:4096\n\t"
        "ds_read_b64_tr_b16 a[190:191], v101 offset:4352\n\t"
        "s_waitcnt lgkmcnt(8)\n\t"
        "ds_read_b64_tr_b16 v[104:105], v102 offset:0\n\t"
        "ds_read_b64_tr_b16 v[106:107], v102 offset:256\n\t"
        "ds_read_b64_tr_b16 v[108:109], v102 offset:4096\n\t"
        "ds_read_b64_tr_b16 v[110:111], v102 offset:4352\n\t"
        "s_lshl_b32 s34, s43, 2\n\t"
        "s_add_u32 s34, s34, 10\n\t"
        "s_and_b32 s34, s34, 15\n\t"
        "s_lshl_b32 s34, s34, 19\n\t"
        "buffer_load_dwordx4 v[160:163], v96, s[16:19], s34 offen nt\n\t"
        "s_add_u32 s34, s34, 0x10000\n\t"
        "buffer_load_dwordx4 v[164:167], v96, s[16:19], s34 offen nt\n\t"
        "s_add_u32 s34, s34, 0x10000\n\t"
        "buffer_load_dwordx4 v[168:171], v96, s[16:19], s34 offen nt\n\t"
        "s_add_u32 s34, s34, 0x10000\n\t"
        "buffer_load_dwordx4 v[172:175], v96, s[16:19], s34 offen nt\n\t"
        "s_add_u32 s34, s34, 0x10000\n\t"
        "buffer_load_dwordx4 v[176:179], v96, s[16:19], s34 offen nt\n\t"
        "s_add_u32 s34, s34, 0x10000\n\t"
        "buffer_load_dwordx4 v[180:183], v96, s[16:19], s34 offen nt\n\t"
        "s_add_u32 s34, s34, 0x10000\n\t"
        "buffer_load_dwordx4 v[184:187], v96, s[16:19], s34 offen nt\n\t"
        "s_add_u32 s34, s34, 0x10000\n\t"
        "buffer_load_dwordx4 v[188:191], v96, s[16:19], s34 offen nt\n\t"
        "s_waitcnt lgkmcnt(0)\n\t"
        "ds_write_b128 v126, v[104:107] offset:6144\n\t"
        "ds_write_b128 v126, v[108:111] offset:7168\n\t"
        "s_waitcnt vmcnt(16)\n\t"
        "ds_write_b128 v97, a[224:227] offset:0\n\t"
        "ds_write_b128 v98, a[228:231] offset:1024\n\t"
        "ds_write_b128 v97, a[232:235] offset:2048\n\t"
        "ds_write_b128 v98, a[236:239] offset:3072\n\t"
        "ds_write_b128 v97, a[240:243] offset:4096\n\t"
        "ds_write_b128 v98, a[244:247] offset:5120\n\t"
        "ds_write_b128 v97, a[248:251] offset:6144\n\t"
        "ds_write_b128 v98, a[252:255] offset:7168\n\t"
        "ds_read_b64_tr_b16 a[192:193], v99 offset:0\n\t"
        "ds_read_b64_tr_b16 a[194:195], v99 offset:256\n\t"
        "ds_read_b64_tr_b16 a[196:197], v99 offset:4096\n\t"
        "ds_read_b64_tr_b16 a[198:199], v99 offset:4352\n\t"
        "s_waitcnt lgkmcnt(8)\n\t"
        "ds_read_b64_tr_b16 a[200:201], v100 offset:0\n\t"
        "ds_read_b64_tr_b16 a[202:203], v100 offset:256\n\t"
        "ds_read_b64_tr_b16 a[204:205], v100 offset:4096\n\t"
        "ds_read_b64_tr_b16 a[206:207], v100 offset:4352\n\t"
        "s_waitcnt lgkmcnt(8)\n\t"
        "ds_read_b64_tr_b16 a[208:209], v101 offset:0\n\t"
        "ds_read_b64_tr_b16 a[210:211], v101 offset:256\n\t"
        "ds_read_b64_tr_b16 a[212:213], v101 offset:4096\n\t"
        "ds_read_b64_tr_b16 a[214:215], v101 offset:4352\n\t"
        "s_waitcnt lgkmcnt(8)\n\t"
        "ds_read_b64_tr_b16 v[104:105], v102 offset:0\n\t"
        "ds_read_b64_tr_b16 v[106:107], v102 offset:256\n\t"
        "ds_read_b64_tr_b16 v[108:109], v102 offset:4096\n\t"
        "ds_read_b64_tr_b16 v[110:111], v102 offset:4352\n\t"
        "s_lshl_b32 s34, s43, 2\n\t"
        "s_add_u32 s34, s34, 11\n\t"
        "s_and_b32 s34, s34, 15\n\t"
        "s_lshl_b32 s34, s34, 19\n\t"
        "buffer_load_dwordx4 v[192:195], v96, s[16:19], s34 offen nt\n\t"
        "s_add_u32 s34, s34, 0x10000\n\t"
        "buffer_load_dwordx4 v[196:199], v96, s[16:19], s34 offen nt\n\t"
        "s_add_u32 s34, s34, 0x10000\n\t"
        "buffer_load_dwordx4 v[200:203], v96, s[16:19], s34 offen nt\n\t"
        "s_add_u32 s34, s34, 0x10000\n\t"
        "buffer_load_dwordx4 v[204:207], v96, s[16:19], s34 offen nt\n\t"
        "s_add_u32 s34, s34, 0x10000\n\t"
        "buffer_load_dwordx4 v[208:211], v96, s[16:19], s34 offen nt\n\t"
        "s_add_u32 s34, s34, 0x10000\n\t"
        "buffer_load_dwordx4 v[212:215], v96, s[16:19], s34 offen nt\n\t"
        "s_add_u32 s34, s34, 0x10000\n\t"
        "buffer_load_dwordx4 v[216:219], v96, s[16:19], s34 offen nt\n\t"
        "s_add_u32 s34, s34, 0x10000\n\t"
        "buffer_load_dwordx4 v[220:223], v96, s[16:19], s34 offen nt\n\t"
        "s_waitcnt lgkmcnt(0)\n\t"
        "ds_write_b128 v117, v[104:107] offset:8192\n\t"
        "ds_write_b128 v117, v[108:111] offset:9216\n\t"
        "s_waitcnt vmcnt(16)\n\t"
        "ds_write_b128 v97, v[128:131] offset:0\n\t"
        "ds_write_b128 v98, v[132:135] offset:1024\n\t"
        "ds_write_b128 v97, v[136:139] offset:2048\n\t"
        "ds_write_b128 v98, v[140:143] offset:3072\n\t"
        "ds_write_b128 v97, v[144:147] offset:4096\n\t"
        "ds_write_b128 v98, v[148:151] offset:5120\n\t"
        "ds_write_b128 v97, v[152:155] offset:6144\n\t"
        "ds_write_b128 v98, v[156:159] offset:7168\n\t"
        "ds_read_b64_tr_b16 a[216:217], v99 offset:0\n\t"
        "ds_read_b64_tr_b16 a[218:219], v99 offset:256\n\t"
        "ds_read_b64_tr_b16 a[220:221], v99 offset:4096\n\t"
        "ds_read_b64_tr_b16 a[222:223], v99 offset:4352\n\t"
        "s_waitcnt lgkmcnt(8)\n\t"
        "ds_read_b64_tr_b16 a[224:225], v100 offset:0\n\t"
        "ds_read_b64_tr_b16 a[226:227], v100 offset:256\n\t"
        "ds_read_b64_tr_b16 a[228:229], v100 offset:4096\n\t"
        "ds_read_b64_tr_b16 a[230:231], v100 offset:4352\n\t"
        "s_waitcnt lgkmcnt(8)\n\t"
        "ds_read_b64_tr_b16 a[232:233], v101 offset:0\n\t"
        "ds_read_b64_tr_b16 a[234:235], v101 offset:256\n\t"
        "ds_read_b64_tr_b16 a[236:237], v101 offset:4096\n\t"
        "ds_read_b64_tr_b16 a[238:239], v101 offset:4352\n\t"
        "s_waitcnt lgkmcnt(8)\n\t"
        "ds_read_b64_tr_b16 v[104:105], v102 offset:0\n\t"
        "ds_read_b64_tr_b16 v[106:107], v102 offset:256\n\t"
        "ds_read_b64_tr_b16 v[108:109], v102 offset:4096\n\t"
        "ds_read_b64_tr_b16 v[110:111], v102 offset:4352\n\t"
        "s_lshl_b32 s34, s43, 2\n\t"
        "s_add_u32 s34, s34, 12\n\t"
        "s_and_b32 s34, s34, 15\n\t"
        "s_lshl_b32 s34, s34, 19\n\t"
        "buffer_load_dwordx4 v[224:227], v96, s[16:19], s34 offen nt\n\t"
        "s_add_u32 s34, s34, 0x10000\n\t"
        "buffer_load_dwordx4 v[228:231], v96, s[16:19], s34 offen nt\n\t"
        "s_add_u32 s34, s34, 0x10000\n\t"
        "buffer_load_dwordx4 v[232:235], v96, s[16:19], s34 offen nt\n\t"
        "s_add_u32 s34, s34, 0x10000\n\t"
        "buffer_load_dwordx4 v[236:239], v96, s[16:19], s34 offen nt\n\t"
        "s_add_u32 s34, s34, 0x10000\n\t"
        "buffer_load_dwordx4 v[240:243], v96, s[16:19], s34 offen nt\n\t"
        "s_add_u32 s34, s34, 0x10000\n\t"
        "buffer_load_dwordx4 v[244:247], v96, s[16:19], s34 offen nt\n\t"
        "s_add_u32 s34, s34, 0x10000\n\t"
        "buffer_load_dwordx4 v[248:251], v96, s[16:19], s34 offen nt\n\t"
        "s_add_u32 s34, s34, 0x10000\n\t"
        "buffer_load_dwordx4 v[252:255], v96, s[16:19], s34 offen nt\n\t"
        "s_waitcnt lgkmcnt(0)\n\t"
        "ds_write_b128 v117, v[104:107] offset:10240\n\t"
        "ds_write_b128 v117, v[108:111] offset:11264\n\t"
        "s_waitcnt vmcnt(16)\n\t"
        "ds_write_b128 v97, v[160:163] offset:0\n\t"
        "ds_write_b128 v98, v[164:167] offset:1024\n\t"
        "ds_write_b128 v97, v[168:171] offset:2048\n\t"
        "ds_write_b128 v98, v[172:175] offset:3072\n\t"
        "ds_write_b128 v97, v[176:179] offset:4096\n\t"
        "ds_write_b128 v98, v[180:183] offset:5120\n\t"
        "ds_write_b128 v97, v[184:187] offset:6144\n\t"
        "ds_write_b128 v98, v[188:191] offset:7168\n\t"
        "ds_read_b64_tr_b16 a[240:241], v99 offset:0\n\t"
        "ds_read_b64_tr_b16 a[242:243], v99 offset:256\n\t"
        "ds_read_b64_tr_b16 a[244:245], v99 offset:4096\n\t"
        "ds_read_b64_tr_b16 a[246:247], v99 offset:4352\n\t"
        "s_waitcnt lgkmcnt(8)\n\t"
        "ds_read_b64_tr_b16 a[248:249], v100 offset:0\n\t"
        "ds_read_b64_tr_b16 a[250:251], v100 offset:256\n\t"
        "ds_read_b64_tr_b16 a[252:253], v100 offset:4096\n\t"
        "ds_read_b64_tr_b16 a[254:255], v100 offset:4352\n\t"
        "s_waitcnt lgkmcnt(8)\n\t"
        "ds_read_b64_tr_b16 v[128:129], v101 offset:0\n\t"
        "ds_read_b64_tr_b16 v[130:131], v101 offset:256\n\t"
        "ds_read_b64_tr_b16 v[132:133], v101 offset:4096\n\t"
        "ds_read_b64_tr_b16 v[134:135], v101 offset:4352\n\t"
        "s_waitcnt lgkmcnt(8)\n\t"
        "ds_read_b64_tr_b16 v[104:105], v102 offset:0\n\t"
        "ds_read_b64_tr_b16 v[106:107], v102 offset:256\n\t"
        "ds_read_b64_tr_b16 v[108:109], v102 offset:4096\n\t"
        "ds_read_b64_tr_b16 v[110:111], v102 offset:4352\n\t"
        "s_lshl_b32 s34, s43, 2\n\t"
        "s_add_u32 s34, s34, 13\n\t"
        "s_and_b32 s34, s34, 15\n\t"
        "s_lshl_b32 s34, s34, 19\n\t"
        "buffer_load_dwordx4 v[0:3], v96, s[16:19], s34 offen nt\n\t"
        "s_add_u32 s34, s34, 0x10000\n\t"
        "buffer_load_dwordx4 v[4:7], v96, s[16:19], s34 offen nt\n\t"
        "s_add_u32 s34, s34, 0x10000\n\t"
        "buffer_load_dwordx4 v[8:11], v96, s[16:19], s34 offen nt\n\t"
        "s_add_u32 s34, s34, 0x10000\n\t"
        "buffer_load_dwordx4 v[12:15], v96, s[16:19], s34 offen nt\n\t"
        "s_add_u32 s34, s34, 0x10000\n\t"
        "buffer_load_dwordx4 v[16:19], v96, s[16:19], s34 offen nt\n\t"
        "s_add_u32 s34, s34, 0x10000\n\t"
        "buffer_load_dwordx4 v[20:23], v96, s[16:19], s34 offen nt\n\t"
        "s_add_u32 s34, s34, 0x10000\n\t"
        "buffer_load_dwordx4 v[24:27], v96, s[16:19], s34 offen nt\n\t"
        "s_add_u32 s34, s34, 0x10000\n\t"
        "buffer_load_dwordx4 v[28:31], v96, s[16:19], s34 offen nt\n\t"
        "s_waitcnt lgkmcnt(0)\n\t"
        "ds_write_b128 v117, v[104:107] offset:12288\n\t"
        "ds_write_b128 v117, v[108:111] offset:13312\n\t"
        "s_waitcnt vmcnt(16)\n\t"
        "ds_write_b128 v97, v[192:195] offset:0\n\t"
        "ds_write_b128 v98, v[196:199] offset:1024\n\t"
        "ds_write_b128 v97, v[200:203] offset:2048\n\t"
        "ds_write_b128 v98, v[204:207] offset:3072\n\t"
        "ds_write_b128 v97, v[208:211] offset:4096\n\t"
        "ds_write_b128 v98, v[212:215] offset:5120\n\t"
        "ds_write_b128 v97, v[216:219] offset:6144\n\t"
        "ds_write_b128 v98, v[220:223] offset:7168\n\t"
        "ds_read_b64_tr_b16 v[136:137], v99 offset:0\n\t"
        "ds_read_b64_tr_b16 v[138:139], v99 offset:256\n\t"
        "ds_read_b64_tr_b16 v[140:141], v99 offset:4096\n\t"
        "ds_read_b64_tr_b16 v[142:143], v99 offset:4352\n\t"
        "s_waitcnt lgkmcnt(8)\n\t"
        "ds_read_b64_tr_b16 v[144:145], v100 offset:0\n\t"
        "ds_read_b64_tr_b16 v[146:147], v100 offset:256\n\t"
        "ds_read_b64_tr_b16 v[148:149], v100 offset:4096\n\t"
        "ds_read_b64_tr_b16 v[150:151], v100 offset:4352\n\t"
        "s_waitcnt lgkmcnt(8)\n\t"
        "ds_read_b64_tr_b16 v[152:153], v101 offset:0\n\t"
        "ds_read_b64_tr_b16 v[154:155], v101 offset:256\n\t"
        "ds_read_b64_tr_b16 v[156:157], v101 offset:4096\n\t"
        "ds_read_b64_tr_b16 v[158:159], v101 offset:4352\n\t"
        "s_waitcnt lgkmcnt(8)\n\t"
        "ds_read_b64_tr_b16 v[104:105], v102 offset:0\n\t"
        "ds_read_b64_tr_b16 v[106:107], v102 offset:256\n\t"
        "ds_read_b64_tr_b16 v[108:109], v102 offset:4096\n\t"
        "ds_read_b64_tr_b16 v[110:111], v102 offset:4352\n\t"
        "s_lshl_b32 s34, s43, 2\n\t"
        "s_add_u32 s34, s34, 14\n\t"
        "s_and_b32 s34, s34, 15\n\t"
        "s_lshl_b32 s34, s34, 19\n\t"
        "buffer_load_dwordx4 v[32:35], v96, s[16:19], s34 offen nt\n\t"
        "s_add_u32 s34, s34, 0x10000\n\t"
        "buffer_load_dwordx4 v[36:39], v96, s[16:19], s34 offen nt\n\t"
        "s_add_u32 s34, s34, 0x10000\n\t"
        "buffer_load_dwordx4 v[40:43], v96, s[16:19], s34 offen nt\n\t"
        "s_add_u32 s34, s34, 0x10000\n\t"
        "buffer_load_dwordx4 v[44:47], v96, s[16:19], s34 offen nt\n\t"
        "s_add_u32 s34, s34, 0x10000\n\t"
        "buffer_load_dwordx4 v[48:51], v96, s[16:19], s34 offen nt\n\t"
        "s_add_u32 s34, s34, 0x10000\n\t"
        "buffer_load_dwordx4 v[52:55], v96, s[16:19], s34 offen nt\n\t"
        "s_add_u32 s34, s34, 0x10000\n\t"
        "buffer_load_dwordx4 v[56:59], v96, s[16:19], s34 offen nt\n\t"
        "s_add_u32 s34, s34, 0x10000\n\t"
        "buffer_load_dwordx4 v[60:63], v96, s[16:19], s34 offen nt\n\t"
        "s_waitcnt lgkmcnt(0)\n\t"
        "ds_write_b128 v117, v[104:107] offset:14336\n\t"
        "ds_write_b128 v117, v[108:111] offset:15360\n\t"
        "s_waitcnt vmcnt(16)\n\t"
        "ds_write_b128 v97, v[224:227] offset:0\n\t"
        "ds_write_b128 v98, v[228:231] offset:1024\n\t"
        "ds_write_b128 v97, v[232:235] offset:2048\n\t"
        "ds_write_b128 v98, v[236:239] offset:3072\n\t"
        "ds_write_b128 v97, v[240:243] offset:4096\n\t"
        "ds_write_b128 v98, v[244:247] offset:5120\n\t"
        "ds_write_b128 v97, v[248:251] offset:6144\n\t"
        "ds_write_b128 v98, v[252:255] offset:7168\n\t"
        "ds_read_b64_tr_b16 v[160:161], v99 offset:0\n\t"
        "ds_read_b64_tr_b16 v[162:163], v99 offset:256\n\t"
        "ds_read_b64_tr_b16 v[164:165], v99 offset:4096\n\t"
        "ds_read_b64_tr_b16 v[166:167], v99 offset:4352\n\t"
        "s_waitcnt lgkmcnt(8)\n\t"
        "ds_read_b64_tr_b16 v[168:169], v100 offset:0\n\t"
        "ds_read_b64_tr_b16 v[170:171], v100 offset:256\n\t"
        "ds_read_b64_tr_b16 v[172:173], v100 offset:4096\n\t"
        "ds_read_b64_tr_b16 v[174:175], v100 offset:4352\n\t"
        "s_waitcnt lgkmcnt(8)\n\t"
        "ds_read_b64_tr_b16 v[176:177], v101 offset:0\n\t"
        "ds_read_b64_tr_b16 v[178:179], v101 offset:256\n\t"
        "ds_read_b64_tr_b16 v[180:181], v101 offset:4096\n\t"
        "ds_read_b64_tr_b16 v[182:183], v101 offset:4352\n\t"
        "s_waitcnt lgkmcnt(8)\n\t"
        "ds_read_b64_tr_b16 v[104:105], v102 offset:0\n\t"
        "ds_read_b64_tr_b16 v[106:107], v102 offset:256\n\t"
        "ds_read_b64_tr_b16 v[108:109], v102 offset:4096\n\t"
        "ds_read_b64_tr_b16 v[110:111], v102 offset:4352\n\t"
        "s_lshl_b32 s34, s43, 2\n\t"
        "s_add_u32 s34, s34, 15\n\t"
        "s_and_b32 s34, s34, 15\n\t"
        "s_lshl_b32 s34, s34, 19\n\t"
        "buffer_load_dwordx4 v[64:67], v96, s[16:19], s34 offen nt\n\t"
        "s_add_u32 s34, s34, 0x10000\n\t"
        "buffer_load_dwordx4 v[68:71], v96, s[16:19], s34 offen nt\n\t"
        "s_add_u32 s34, s34, 0x10000\n\t"
        "buffer_load_dwordx4 v[72:75], v96, s[16:19], s34 offen nt\n\t"
        "s_add_u32 s34, s34, 0x10000\n\t"
        "buffer_load_dwordx4 v[76:79], v96, s[16:19], s34 offen nt\n\t"
        "s_add_u32 s34, s34, 0x10000\n\t"
        "buffer_load_dwordx4 v[80:83], v96, s[16:19], s34 offen nt\n\t"
        "s_add_u32 s34, s34, 0x10000\n\t"
        "buffer_load_dwordx4 v[84:87], v96, s[16:19], s34 offen nt\n\t"
        "s_add_u32 s34, s34, 0x10000\n\t"
        "buffer_load_dwordx4 v[88:91], v96, s[16:19], s34 offen nt\n\t"
        "s_add_u32 s34, s34, 0x10000\n\t"
        "buffer_load_dwordx4 v[92:95], v96, s[16:19], s34 offen nt\n\t"
        "s_waitcnt lgkmcnt(0)\n\t"
        "ds_write_b128 v117, v[104:107] offset:16384\n\t"
        "ds_write_b128 v117, v[108:111] offset:17408\n\t"
        "s_waitcnt vmcnt(16)\n\t"
        "ds_write_b128 v97, v[0:3] offset:0\n\t"
        "ds_write_b128 v98, v[4:7] offset:1024\n\t"
        "ds_write_b128 v97, v[8:11] offset:2048\n\t"
        "ds_write_b128 v98, v[12:15] offset:3072\n\t"
        "ds_write_b128 v97, v[16:19] offset:4096\n\t"
        "ds_write_b128 v98, v[20:23] offset:5120\n\t"
        "ds_write_b128 v97, v[24:27] offset:6144\n\t"
        "ds_write_b128 v98, v[28:31] offset:7168\n\t"
        "ds_read_b64_tr_b16 v[184:185], v99 offset:0\n\t"
        "ds_read_b64_tr_b16 v[186:187], v99 offset:256\n\t"
        "ds_read_b64_tr_b16 v[188:189], v99 offset:4096\n\t"
        "ds_read_b64_tr_b16 v[190:191], v99 offset:4352\n\t"
        "s_waitcnt lgkmcnt(8)\n\t"
        "ds_read_b64_tr_b16 v[192:193], v100 offset:0\n\t"
        "ds_read_b64_tr_b16 v[194:195], v100 offset:256\n\t"
        "ds_read_b64_tr_b16 v[196:197], v100 offset:4096\n\t"
        "ds_read_b64_tr_b16 v[198:199], v100 offset:4352\n\t"
        "s_waitcnt lgkmcnt(8)\n\t"
        "ds_read_b64_tr_b16 v[200:201], v101 offset:0\n\t"
        "ds_read_b64_tr_b16 v[202:203], v101 offset:256\n\t"
        "ds_read_b64_tr_b16 v[204:205], v101 offset:4096\n\t"
        "ds_read_b64_tr_b16 v[206:207], v101 offset:4352\n\t"
        "s_waitcnt lgkmcnt(8)\n\t"
        "ds_read_b64_tr_b16 v[104:105], v102 offset:0\n\t"
        "ds_read_b64_tr_b16 v[106:107], v102 offset:256\n\t"
        "ds_read_b64_tr_b16 v[108:109], v102 offset:4096\n\t"
        "ds_read_b64_tr_b16 v[110:111], v102 offset:4352\n\t"
        "s_waitcnt lgkmcnt(0)\n\t"
        "ds_write_b128 v117, v[104:107] offset:18432\n\t"
        "ds_write_b128 v117, v[108:111] offset:19456\n\t"
        "s_waitcnt vmcnt(8)\n\t"
        "ds_write_b128 v97, v[32:35] offset:0\n\t"
        "ds_write_b128 v98, v[36:39] offset:1024\n\t"
        "ds_write_b128 v97, v[40:43] offset:2048\n\t"
        "ds_write_b128 v98, v[44:47] offset:3072\n\t"
        "ds_write_b128 v97, v[48:51] offset:4096\n\t"
        "ds_write_b128 v98, v[52:55] offset:5120\n\t"
        "ds_write_b128 v97, v[56:59] offset:6144\n\t"
        "ds_write_b128 v98, v[60:63] offset:7168\n\t"
        "ds_read_b64_tr_b16 v[208:209], v99 offset:0\n\t"
        "ds_read_b64_tr_b16 v[210:211], v99 offset:256\n\t"
        "ds_read_b64_tr_b16 v[212:213], v99 offset:4096\n\t"
        "ds_read_b64_tr_b16 v[214:215], v99 offset:4352\n\t"
        "s_waitcnt lgkmcnt(8)\n\t"
        "ds_read_b64_tr_b16 v[216:217], v100 offset:0\n\t"
        "ds_read_b64_tr_b16 v[218:219], v100 offset:256\n\t"
        "ds_read_b64_tr_b16 v[220:221], v100 offset:4096\n\t"
        "ds_read_b64_tr_b16 v[222:223], v100 offset:4352\n\t"
        "s_waitcnt lgkmcnt(8)\n\t"
        "ds_read_b64_tr_b16 v[224:225], v101 offset:0\n\t"
        "ds_read_b64_tr_b16 v[226:227], v101 offset:256\n\t"
        "ds_read_b64_tr_b16 v[228:229], v101 offset:4096\n\t"
        "ds_read_b64_tr_b16 v[230:231], v101 offset:4352\n\t"
        "s_waitcnt lgkmcnt(8)\n\t"
        "ds_read_b64_tr_b16 v[104:105], v102 offset:0\n\t"
        "ds_read_b64_tr_b16 v[106:107], v102 offset:256\n\t"
        "ds_read_b64_tr_b16 v[108:109], v102 offset:4096\n\t"
        "ds_read_b64_tr_b16 v[110:111], v102 offset:4352\n\t"
        "s_mov_b32 s35, 33792\n\t"
        "buffer_load_dword v0, off, s[12:15], s35 sc1\n\t"
        "s_waitcnt lgkmcnt(0)\n\t"
        "ds_write_b128 v117, v[104:107] offset:20480\n\t"
        "ds_write_b128 v117, v[108:111] offset:21504\n\t"
        "s_waitcnt vmcnt(0)\n\t"
        "ds_write_b128 v97, v[64:67] offset:0\n\t"
        "ds_write_b128 v98, v[68:71] offset:1024\n\t"
        "ds_write_b128 v97, v[72:75] offset:2048\n\t"
        "ds_write_b128 v98, v[76:79] offset:3072\n\t"
        "ds_write_b128 v97, v[80:83] offset:4096\n\t"
        "ds_write_b128 v98, v[84:87] offset:5120\n\t"
        "ds_write_b128 v97, v[88:91] offset:6144\n\t"
        "ds_write_b128 v98, v[92:95] offset:7168\n\t"
        "ds_read_b64_tr_b16 v[232:233], v99 offset:0\n\t"
        "ds_read_b64_tr_b16 v[234:235], v99 offset:256\n\t"
        "ds_read_b64_tr_b16 v[236:237], v99 offset:4096\n\t"
        "ds_read_b64_tr_b16 v[238:239], v99 offset:4352\n\t"
        "s_waitcnt lgkmcnt(8)\n\t"
        "ds_read_b64_tr_b16 v[240:241], v100 offset:0\n\t"
        "ds_read_b64_tr_b16 v[242:243], v100 offset:256\n\t"
        "ds_read_b64_tr_b16 v[244:245], v100 offset:4096\n\t"
        "ds_read_b64_tr_b16 v[246:247], v100 offset:4352\n\t"
        "s_waitcnt lgkmcnt(8)\n\t"
        "ds_read_b64_tr_b16 v[248:249], v101 offset:0\n\t"
        "ds_read_b64_tr_b16 v[250:251], v101 offset:256\n\t"
        "ds_read_b64_tr_b16 v[252:253], v101 offset:4096\n\t"
        "ds_read_b64_tr_b16 v[254:255], v101 offset:4352\n\t"
        "s_waitcnt lgkmcnt(8)\n\t"
        "ds_read_b64_tr_b16 v[104:105], v102 offset:0\n\t"
        "ds_read_b64_tr_b16 v[106:107], v102 offset:256\n\t"
        "ds_read_b64_tr_b16 v[108:109], v102 offset:4096\n\t"
        "ds_read_b64_tr_b16 v[110:111], v102 offset:4352\n\t"
        "s_mov_b32 s42, 0\n\t"
        "LA_cal%=:\n\t"
        "v_readfirstlane_b32 s34, v0\n\t"
        "s_nop 0\n\t"
        "s_cmp_lg_u32 s34, 0\n\t"
        "s_cbranch_scc1 LA_calok%=\n\t"
        "s_sleep 4\n\t"
        "s_mov_b32 s35, 33792\n\t"
        "buffer_load_dword v0, off, s[12:15], s35 sc1\n\t"
        "s_waitcnt vmcnt(0)\n\t"
        "s_add_u32 s42, s42, 1\n\t"
        "s_cmp_lt_u32 s42, 0x100000\n\t"
        "s_cbranch_scc1 LA_cal%=\n\t"
        "s_movk_i32 s34, 8\n\t"
        "s_branch LA_die%=\n\t"
        "LA_calok%=:\n\t"
        "s_lshl_b32 s35, s44, 10\n\t"
        "v_lshlrev_b32_e32 v1, 2, v122\n\t"
        "v_and_b32_e32 v1, 0x3c0, v1\n\t"
        "buffer_load_dword v1, v1, s[12:15], s35 offen offset:0 sc1\n\t"
        "s_waitcnt lgkmcnt(0)\n\t"
        "ds_write_b128 v126, v[104:107] offset:8192\n\t"
        "ds_write_b128 v126, v[108:111] offset:9216\n\t"
        "s_waitcnt lgkmcnt(0)\n\t"
        "v_mov_b32_e32 v110, v103\n\t"
        "ds_read_b128 v[64:67], v126 offset:0\n\t"
        "ds_read_b128 v[68:71], v126 offset:1024\n\t"
        "ds_read_b128 v[72:75], v126 offset:2048\n\t"
        "ds_read_b128 v[76:79], v126 offset:3072\n\t"
        "ds_read_b128 v[80:83], v126 offset:4096\n\t"
        "ds_read_b128 v[84:87], v126 offset:5120\n\t"
        "ds_read_b128 v[88:91], v126 offset:6144\n\t"
        "ds_read_b128 v[92:95], v126 offset:7168\n\t"
        "ds_read_b128 v[96:99], v126 offset:8192\n\t"
        "ds_read_b128 v[100:103], v126 offset:9216\n\t"
        "s_waitcnt lgkmcnt(0)\n\t"
        "v_mov_b32_e32 v126, 0\n\t"
        "s_barrier\n\t"
        "s_mov_b32 s48, 0\n\t"
        "s_mov_b32 s54, 0\n\t"
        "s_mov_b32 s0, 0x3f803f80\n\t"
        "s_mov_b32 s1, s0\n\t"
        "s_movk_i32 s56, 24576\n\t"
        "s_and_b32 s58, s43, 1\n\t"
        "s_lshl_b32 s58, s58, 12\n\t"
        "s_lshl_b32 s57, s44, 5\n\t"
        "s_add_u32 s57, s57, s43\n\t"
        "s_lshl_b32 s57, s57, 2\n\t"
        "s_add_u32 s57, s57, 458752\n\t"
        "s_waitcnt vmcnt(0)\n\t"
        "v_mov_b32_e32 v111, v1\n\t"
        "v_readfirstlane_b32 s34, v0\n\t"
        "s_nop 0\n\t"
        "s_and_b32 s34, s34, 8192\n\t"
        "s_add_u32 s8, s8, s34\n\t"
        "s_addc_u32 s9, s9, 0\n\t"
        "s_barrier\n\t"
        "s_cmp_lg_u32 s43, 0\n\t"
        "s_cbranch_scc1 LA_st%=\n\t"
        "v_mov_b32_e32 v2, 1\n\t"
        "s_mov_b32 s35, 34304\n\t"
        "s_mov_b64 exec, 1\n\t"
        "buffer_atomic_add v2, off, s[12:15], s35 sc0\n\t"
        "s_mov_b64 exec, -1\n\t"
        "s_waitcnt vmcnt(0)\n\t"
        "v_readfirstlane_b32 s34, v2\n\t"
        "s_nop 0\n\t"
        "s_cmp_lg_u32 s34, 95\n\t"
        "s_cbranch_scc1 LA_st%=\n\t"
        "s_memrealtime s[32:33]\n\t"
        "v_mbcnt_lo_u32_b32 v3, -1, 0\n\t"
        "v_lshlrev_b32_e32 v3, 7, v3\n\t"
        "s_waitcnt lgkmcnt(0)\n\t"
        "s_add_u32 s34, s32, 50\n\t"
        "s_or_b32 s34, s34, 1\n\t"
        "v_mov_b32_e32 v2, s34\n\t"
        "s_mov_b32 s35, 36864\n\t"
        "s_mov_b64 exec, 0xffff\n\t"
        "buffer_store_dword v2, v3, s[12:15], s35 offen sc1\n\t"
        "s_mov_b64 exec, -1\n\t"
        "LA_st%=:\n\t"
        "s_mov_b32 s24, 0\n\t"
        "s_mov_b32 s26, 0\n\t"
        "s_mov_b32 s27, 0x80000\n\t"
        "s_mov_b32 s28, 0x180000\n\t"
        "s_mov_b32 s31, 0\n\t"
        "s_mov_b32 s47, 0\n\t"
        "s_mov_b32 s38, 0xff00ff00\n\t"
        "s_mov_b32 s39, 0xff00ff00\n\t"
        "s_mov_b32 s40, 0\n\t"
        "s_mov_b32 s41, -1\n\t"
        "ds_read_b128 v[32:35], v117 offset:0\n\t"
        "ds_read_b128 v[36:39], v117 offset:1024\n\t"
        "ds_read_b128 v[40:43], v117 offset:2048\n\t"
        "ds_read_b128 v[44:47], v117 offset:3072\n\t"
        "ds_read_b128 v[48:51], v117 offset:4096\n\t"
        "ds_read_b128 v[52:55], v117 offset:5120\n\t"
        "ds_read_b128 v[56:59], v117 offset:6144\n\t"
        "ds_read_b128 v[60:63], v117 offset:7168\n\t"
        "v_mov_b64_e32 v[0:1], 0\n\t"
        "v_mov_b64_e32 v[2:3], 0\n\t"
        "v_mov_b64_e32 v[4:5], 0\n\t"
        "v_mov_b64_e32 v[6:7], 0\n\t"
        "v_mov_b64_e32 v[8:9], 0\n\t"
        "v_mov_b64_e32 v[10:11], 0\n\t"
        "v_mov_b64_e32 v[12:13], 0\n\t"
        "v_mov_b64_e32 v[14:15], 0\n\t"
        "s_lshr_b32 s59, s48, 1\n\t"
        "s_cmp_gt_u32 s54, s59\n\t"
        "s_cbranch_scc1 LA_kv0ok%=\n\t"
        "s_mov_b32 s42, 0\n\t"
        "s_waitcnt vmcnt(0)\n\t"
        "s_branch LA_kv0w%=\n\t"
        "LA_kv0%=:\n\t"
        "s_waitcnt vmcnt(0)\n\t"
        "LA_kv0w%=:\n\t"
        "v_min_u32_dpp v111, v111, v111 quad_perm:[1,0,3,2] row_mask:0xf bank_mask:0xf\n\t"
        "s_nop 1\n\t"
        "v_min_u32_dpp v111, v111, v111 quad_perm:[2,3,0,1] row_mask:0xf bank_mask:0xf\n\t"
        "s_nop 1\n\t"
        "v_min_u32_dpp v111, v111, v111 row_ror:4 row_mask:0xf bank_mask:0xf\n\t"
        "s_nop 1\n\t"
        "v_min_u32_dpp v111, v111, v111 row_ror:8 row_mask:0xf bank_mask:0xf\n\t"
        "s_nop 0\n\t"
        "v_readfirstlane_b32 s54, v111\n\t"
        "s_nop 0\n\t"
        "s_cmp_gt_u32 s54, s59\n\t"
        "s_cbranch_scc1 LA_kv0ok%=\n\t"
        "s_sleep 4\n\t"
        "s_lshl_b32 s35, s44, 10\n\t"
        "v_lshlrev_b32_e32 v111, 2, v122\n\t"
        "v_and_b32_e32 v111, 0x3c0, v111\n\t"
        "buffer_load_dword v111, v111, s[12:15], s35 offen offset:0 sc1\n\t"
        "s_add_u32 s42, s42, 1\n\t"
        "s_cmp_lt_u32 s42, 0x100000\n\t"
        "s_cbranch_scc1 LA_kv0%=\n\t"
        "s_movk_i32 s34, 16\n\t"
        "s_branch LA_die%=\n\t"
        "LA_kv0ok%=:\n\t"
        "s_and_b32 s36, s48, 1\n\t"
        "s_mul_i32 s36, s36, 24576\n\t"
        "s_add_u32 s36, s36, s55\n\t"
        "s_lshl_b32 s37, s48, 18\n\t"
        "s_lshl_b32 s34, s43, 11\n\t"
        "s_add_u32 s37, s37, s34\n\t"
        "s_add_u32 m0, s36, 0\n\t"
        "s_add_u32 s34, s37, 29548544\n\t"
        "buffer_load_dwordx4 v122, s[12:15], s34 offen sc1 lds\n\t"
        "s_add_u32 m0, s36, 1024\n\t"
        "s_add_u32 s34, s37, 29549568\n\t"
        "buffer_load_dwordx4 v122, s[12:15], s34 offen sc1 lds\n\t"
        "s_add_u32 m0, s36, 8192\n\t"
        "s_add_u32 s34, s37, 12771328\n\t"
        "buffer_load_dwordx4 v122, s[12:15], s34 offen sc1 lds\n\t"
        "s_add_u32 m0, s36, 9216\n\t"
        "s_add_u32 s34, s37, 12772352\n\t"
        "buffer_load_dwordx4 v122, s[12:15], s34 offen sc1 lds\n\t"
        "s_add_u32 m0, s36, 16384\n\t"
        "s_add_u32 s34, s37, 46325760\n\t"
        "buffer_load_dwordx4 v122, s[12:15], s34 offen sc1 lds\n\t"
        "s_add_u32 m0, s36, 17408\n\t"
        "s_add_u32 s34, s37, 46326784\n\t"
        "buffer_load_dwordx4 v122, s[12:15], s34 offen sc1 lds\n\t"
        "s_mov_b32 s42, 0\n\t"
        "LA_gw%=:\n\t"
        "buffer_load_dword v16, off, s[12:15], s45 sc1\n\t"
        "s_waitcnt vmcnt(0)\n\t"
        "v_readfirstlane_b32 s25, v16\n\t"
        "s_nop 0\n\t"
        "s_cmp_lg_u32 s25, 0\n\t"
        "s_cbranch_scc1 LA_loop%=\n\t"
        "s_sleep 2\n\t"
        "s_add_u32 s42, s42, 1\n\t"
        "s_cmp_lt_u32 s42, 0x100000\n\t"
        "s_cbranch_scc1 LA_gw%=\n\t"
        "s_movk_i32 s34, 9\n\t"
        "s_branch LA_die%=\n\t"
        ".p2align 8\n\t"
        "LA_loop%=:\n\t"
        "s_mov_b32 s42, 0\n\t"
        "s_sub_u32 s35, s25, 8\n\t"
        "s_waitcnt lgkmcnt(0)\n\t"
        "s_memrealtime s[2:3]\n\t"
        "s_memrealtime s[4:5]\n\t"
        "s_memrealtime s[6:7]\n\t"
        "LA_wa%=:\n\t"
        "s_memrealtime s[32:33]\n\t"
        "s_waitcnt lgkmcnt(6)\n\t"
        "s_sub_u32 s34, s35, s2\n\t"
        "s_cmpk_lt_u32 s34, 0x2000\n\t"
        "s_cbranch_scc0 LA_wa%=x\n\t"
        "s_memrealtime s[2:3]\n\t"
        "s_waitcnt lgkmcnt(6)\n\t"
        "s_sub_u32 s34, s35, s4\n\t"
        "s_cmpk_lt_u32 s34, 0x2000\n\t"
        "s_cbranch_scc0 LA_wa%=x\n\t"
        "s_memrealtime s[4:5]\n\t"
        "s_waitcnt lgkmcnt(6)\n\t"
        "s_sub_u32 s34, s35, s6\n\t"
        "s_cmpk_lt_u32 s34, 0x2000\n\t"
        "s_cbranch_scc0 LA_wa%=x\n\t"
        "s_memrealtime s[6:7]\n\t"
        "s_waitcnt lgkmcnt(6)\n\t"
        "s_sub_u32 s34, s35, s32\n\t"
        "s_cmpk_lt_u32 s34, 0x2000\n\t"
        "s_cbranch_scc1 LA_wa%=\n\t"
        "s_nop 3\n\t"
        "LA_wa%=x:\n\t"
        "s_add_i32 s34, s34, 4\n\t"
        "s_max_i32 s34, s34, 0\n\t"
        "s_bitcmp1_b32 s34, 1\n\t"
        "s_cbranch_scc0 LA_wa%=y\n\t"
        "s_nop 15\n\t"
        "LA_wa%=y:\n\t"
        "s_bitcmp1_b32 s34, 0\n\t"
        "s_cbranch_scc0 LA_wa%=z\n\t"
        "s_nop 9\n\t"
        "LA_wa%=z:\n\t"
        "LA_rd%=:\n\t"
        "buffer_load_dwordx4 v[16:19], v112, s[8:11], s26 offen offset:0 sc1\n\t"
        "s_nop 12\n\t"
        "buffer_load_dwordx4 v[20:23], v112, s[8:11], s26 offen offset:1024 sc1\n\t"
        "s_nop 12\n\t"
        "buffer_load_dwordx4 v[24:27], v112, s[8:11], s26 offen offset:2048 sc1\n\t"
        "s_nop 12\n\t"
        "buffer_load_dwordx4 v[28:31], v112, s[8:11], s26 offen offset:3072 sc1\n\t"
        "LA_go%=:\n\t"
        "s_waitcnt vmcnt(3)\n\t"
        "v_smfmac_f32_16x16x64_bf16 v[0:3], v[16:19], a[0:7], v110\n\t"
        "ds_write_b128 v113, v[16:19] offset:0\n\t"
        "v_smfmac_f32_16x16x64_bf16 v[4:7], v[16:19], a[8:15], v110\n\t"
        "v_max3_u32 v111, v16, v17, v18\n\t"
        "v_max_u32_e32 v111, v19, v111\n\t"
        "v_smfmac_f32_16x16x64_bf16 v[8:11], v[16:19], a[16:23], v110\n\t"
        "v_smfmac_f32_16x16x64_bf16 v[12:15], v[16:19], v[32:39], v110\n\t"
        "s_waitcnt vmcnt(2)\n\t"
        "v_smfmac_f32_16x16x64_bf16 v[0:3], v[20:23], a[24:31], v110\n\t"
        "ds_write_b128 v113, v[20:23] offset:1024\n\t"
        "v_smfmac_f32_16x16x64_bf16 v[4:7], v[20:23], a[32:39], v110\n\t"
        "v_max3_u32 v111, v20, v21, v111\n\t"
        "v_max3_u32 v111, v22, v23, v111\n\t"
        "v_smfmac_f32_16x16x64_bf16 v[8:11], v[20:23], a[40:47], v110\n\t"
        "s_waitcnt vmcnt(1)\n\t"
        "ds_write_b128 v113, v[24:27] offset:2048\n\t"
        "v_smfmac_f32_16x16x64_bf16 v[12:15], v[20:23], v[40:47], v110\n\t"
        "v_max3_u32 v111, v24, v25, v111\n\t"
        "v_max3_u32 v111, v26, v27, v111\n\t"
        "v_smfmac_f32_16x16x64_bf16 v[0:3], v[24:27], a[48:55], v110\n\t"
        "v_smfmac_f32_16x16x64_bf16 v[4:7], v[24:27], a[56:63], v110\n\t"
        "s_waitcnt vmcnt(0)\n\t"
        "ds_write_b128 v113, v[28:31] offset:3072\n\t"
        "v_max3_u32 v111, v28, v29, v111\n\t"
        "v_max3_u32 v111, v30, v31, v111\n\t"
        "v_smfmac_f32_16x16x64_bf16 v[8:11], v[24:27], a[64:71], v110\n\t"
        "v_cmp_eq_u32_e32 vcc, -1, v111\n\t"
        "v_smfmac_f32_16x16x64_bf16 v[12:15], v[24:27], v[48:55], v110\n\t"
        "s_waitcnt lgkmcnt(0)\n\t"
        "s_cbranch_vccnz LA_rdfail%=\n\t"
        "s_barrier\n\t"
        "ds_read_b128 v[16:19], v114 offset:0\n\t"
        "ds_read_b128 v[20:23], v114 offset:1024\n\t"
        "ds_read_b128 v[24:27], v114 offset:2048\n\t"
        "v_smfmac_f32_16x16x64_bf16 v[0:3], v[28:31], a[72:79], v110\n\t"
        "v_smfmac_f32_16x16x64_bf16 v[4:7], v[28:31], a[80:87], v110\n\t"
        "v_smfmac_f32_16x16x64_bf16 v[8:11], v[28:31], a[88:95], v110\n\t"
        "v_smfmac_f32_16x16x64_bf16 v[12:15], v[28:31], v[56:63], v110\n\t"
        "ds_read_b128 v[28:31], v114 offset:3072\n\t"
        "s_waitcnt lgkmcnt(3)\n\t"
        "v_smfmac_f32_16x16x64_bf16 v[12:15], v[16:19], v[64:71], v110\n\t"
        "ds_read_b128 v[32:35], v117 offset:8192\n\t"
        "v_smfmac_f32_16x16x64_bf16 v[0:3], v[16:19], a[96:103], v110\n\t"
        "ds_read_b128 v[36:39], v117 offset:9216\n\t"
        "v_smfmac_f32_16x16x64_bf16 v[4:7], v[16:19], a[104:111], v110\n\t"
        "s_waitcnt lgkmcnt(4)\n\t"
        "v_smfmac_f32_16x16x64_bf16 v[8:11], v[16:19], a[112:119], v110\n\t"
        "ds_read_b128 v[16:19], v115 offset:0\n\t"
        "v_smfmac_f32_16x16x64_bf16 v[12:15], v[20:23], v[72:79], v110\n\t"
        "ds_read_b128 v[40:43], v117 offset:10240\n\t"
        "v_smfmac_f32_16x16x64_bf16 v[0:3], v[20:23], a[120:127], v110\n\t"
        "ds_read_b128 v[44:47], v117 offset:11264\n\t"
        "v_smfmac_f32_16x16x64_bf16 v[4:7], v[20:23], a[128:135], v110\n\t"
        "s_waitcnt lgkmcnt(6)\n\t"
        "v_smfmac_f32_16x16x64_bf16 v[8:11], v[20:23], a[136:143], v110\n\t"
        "ds_read_b128 v[20:23], v115 offset:1024\n\t"
        "v_smfmac_f32_16x16x64_bf16 v[12:15], v[24:27], v[80:87], v110\n\t"
        "ds_read_b128 v[48:51], v117 offset:12288\n\t"
        "v_smfmac_f32_16x16x64_bf16 v[0:3], v[24:27], a[144:151], v110\n\t"
        "ds_read_b128 v[52:55], v117 offset:13312\n\t"
        "v_smfmac_f32_16x16x64_bf16 v[4:7], v[24:27], a[152:159], v110\n\t"
        "s_waitcnt lgkmcnt(8)\n\t"
        "v_smfmac_f32_16x16x64_bf16 v[8:11], v[24:27], a[160:167], v110\n\t"
        "ds_read_b128 v[24:27], v115 offset:2048\n\t"
        "v_smfmac_f32_16x16x64_bf16 v[12:15], v[28:31], v[88:95], v110\n\t"
        "ds_read_b128 v[56:59], v117 offset:14336\n\t"
        "v_smfmac_f32_16x16x64_bf16 v[0:3], v[28:31], a[168:175], v110\n\t"
        "ds_read_b128 v[60:63], v117 offset:15360\n\t"
        "v_smfmac_f32_16x16x64_bf16 v[4:7], v[28:31], a[176:183], v110\n\t"
        "s_waitcnt lgkmcnt(8)\n\t"
        "v_smfmac_f32_16x16x64_bf16 v[8:11], v[28:31], a[184:191], v110\n\t"
        "ds_read_b128 v[28:31], v115 offset:3072\n\t"
        "v_smfmac_f32_16x16x64_bf16 v[12:15], v[16:19], v[32:39], v110\n\t"
        "ds_read_b128 v[32:35], v117 offset:16384\n\t"
        "v_smfmac_f32_16x16x64_bf16 v[0:3], v[16:19], a[192:199], v110\n\t"
        "ds_read_b128 v[36:39], v117 offset:17408\n\t"
        "v_smfmac_f32_16x16x64_bf16 v[4:7], v[16:19], a[200:207], v110\n\t"
        "s_waitcnt lgkmcnt(8)\n\t"
        "v_smfmac_f32_16x16x64_bf16 v[8:11], v[16:19], a[208:215], v110\n\t"
        "ds_read_b128 v[16:19], v116 offset:0\n\t"
        "v_smfmac_f32_16x16x64_bf16 v[12:15], v[20:23], v[40:47], v110\n\t"
        "ds_read_b128 v[40:43], v117 offset:18432\n\t"
        "v_smfmac_f32_16x16x64_bf16 v[0:3], v[20:23], a[216:223], v110\n\t"
        "ds_read_b128 v[44:47], v117 offset:19456\n\t"
        "v_smfmac_f32_16x16x64_bf16 v[4:7], v[20:23], a[224:231], v110\n\t"
        "s_waitcnt lgkmcnt(8)\n\t"
        "v_smfmac_f32_16x16x64_bf16 v[8:11], v[20:23], a[232:239], v110\n\t"
        "ds_read_b128 v[20:23], v116 offset:1024\n\t"
        "v_smfmac_f32_16x16x64_bf16 v[12:15], v[24:27], v[48:55], v110\n\t"
        "ds_read_b128 v[48:51], v117 offset:20480\n\t"
        "v_smfmac_f32_16x16x64_bf16 v[0:3], v[24:27], a[240:247], v110\n\t"
        "ds_read_b128 v[52:55], v117 offset:21504\n\t"
        "v_smfmac_f32_16x16x64_bf16 v[4:7], v[24:27], a[248:255], v110\n\t"
        "s_waitcnt lgkmcnt(8)\n\t"
        "v_smfmac_f32_16x16x64_bf16 v[8:11], v[24:27], v[128:135], v110\n\t"
        "ds_read_b128 v[24:27], v116 offset:2048\n\t"
        "v_smfmac_f32_16x16x64_bf16 v[12:15], v[28:31], v[56:63], v110\n\t"
        "ds_read_b128 v[56:59], v124 offset:3072\n\t"
        "v_smfmac_f32_16x16x64_bf16 v[0:3], v[28:31], v[136:143], v110\n\t"
        "ds_read_b128 v[60:63], v124 offset:7168\n\t"
        "v_smfmac_f32_16x16x64_bf16 v[4:7], v[28:31], v[144:151], v110\n\t"
        "s_waitcnt lgkmcnt(8)\n\t"
        "v_smfmac_f32_16x16x64_bf16 v[8:11], v[28:31], v[152:159], v110\n\t"
        "ds_read_b128 v[28:31], v116 offset:3072\n\t"
        "v_smfmac_f32_16x16x64_bf16 v[12:15], v[16:19], v[32:39], v110\n\t"
        "ds_read_b128 v[32:35], v124 offset:0\n\t"
        "v_smfmac_f32_16x16x64_bf16 v[0:3], v[16:19], v[160:167], v110\n\t"
        "ds_read_b128 v[36:39], v124 offset:4096\n\t"
        "v_smfmac_f32_16x16x64_bf16 v[4:7], v[16:19], v[168:175], v110\n\t"
        "s_waitcnt lgkmcnt(8)\n\t"
        "v_smfmac_f32_16x16x64_bf16 v[8:11], v[16:19], v[176:183], v110\n\t"
        "v_smfmac_f32_16x16x64_bf16 v[12:15], v[20:23], v[40:47], v110\n\t"
        "ds_read_b128 v[40:43], v124 offset:1024\n\t"
        "v_smfmac_f32_16x16x64_bf16 v[8:11], v[20:23], v[200:207], v110\n\t"
        "ds_read_b128 v[44:47], v124 offset:5120\n\t"
        "s_waitcnt lgkmcnt(7)\n\t"
        "v_smfmac_f32_16x16x64_bf16 v[12:15], v[24:27], v[48:55], v110\n\t"
        "ds_read_b128 v[48:51], v124 offset:2048\n\t"
        "v_smfmac_f32_16x16x64_bf16 v[8:11], v[24:27], v[224:231], v110\n\t"
        "ds_read_b128 v[52:55], v124 offset:6144\n\t"
        "s_waitcnt lgkmcnt(6)\n\t"
        "v_smfmac_f32_16x16x64_bf16 v[12:15], v[28:31], v[96:103], v110\n\t"
        "v_smfmac_f32_16x16x64_bf16 v[8:11], v[28:31], v[248:255], v110\n\t"
        "v_smfmac_f32_16x16x64_bf16 v[0:3], v[20:23], v[184:191], v110\n\t"
        "s_lshl_b32 s34, s43, 11\n\t"
        "v_add_u32_e32 v108, s34, v124\n\t"
        "v_smfmac_f32_16x16x64_bf16 v[0:3], v[24:27], v[208:215], v110\n\t"
        "v_add_u32_e32 v109, s58, v124\n\t"
        "v_smfmac_f32_16x16x64_bf16 v[4:7], v[20:23], v[192:199], v110\n\t"
        "v_smfmac_f32_16x16x64_bf16 v[0:3], v[28:31], v[232:239], v110\n\t"
        "v_smfmac_f32_16x16x64_bf16 v[4:7], v[24:27], v[216:223], v110\n\t"
        "buffer_load_dwordx4 v[104:107], v120, s[8:11], s26 offen sc1\n\t"
        "v_smfmac_f32_16x16x64_bf16 v[4:7], v[28:31], v[240:247], v110\n\t"
        "v_add_f32_e32 v12, v12, v13\n\t"
        "v_add_f32_e32 v14, v14, v15\n\t"
        "v_cvt_pk_bf16_f32 v12, v12, v14\n\t"
        "v_add_f32_e32 v8, v8, v9\n\t"
        "v_add_f32_e32 v10, v10, v11\n\t"
        "v_cvt_pk_bf16_f32 v8, v8, v10\n\t"
        "ds_write2_b32 v118, v8, v12 offset0:64 offset1:96\n\t"
        "v_add_f32_e32 v0, v0, v1\n\t"
        "v_add_f32_e32 v2, v2, v3\n\t"
        "v_cvt_pk_bf16_f32 v0, v0, v2\n\t"
        "v_add_f32_e32 v4, v4, v5\n\t"
        "v_add_f32_e32 v6, v6, v7\n\t"
        "v_cvt_pk_bf16_f32 v4, v4, v6\n\t"
        "ds_write2_b32 v118, v0, v4 offset0:0 offset1:32\n\t"
        "s_cmp_eq_u32 s24, 63\n\t"
        "s_waitcnt lgkmcnt(0)\n\t"
        "s_barrier\n\t"
        "s_cbranch_scc1 LA_skip%=\n\t"
        "ds_read_b64_tr_b16 v[16:17], v119 offset:0\n\t"
        "ds_read_b64_tr_b16 v[18:19], v119 offset:32\n\t"
        "ds_read_b64_tr_b16 v[20:21], v119 offset:256\n\t"
        "ds_read_b64_tr_b16 v[22:23], v119 offset:288\n\t"
        "ds_read_b64_tr_b16 v[24:25], v119 offset:512\n\t"
        "ds_read_b64_tr_b16 v[26:27], v119 offset:544\n\t"
        "ds_read_b64_tr_b16 v[28:29], v119 offset:768\n\t"
        "ds_read_b64_tr_b16 v[30:31], v119 offset:800\n\t"
        "buffer_store_dword v126, v127, s[12:15], 0 offen sc1\n\t"
        "s_waitcnt lgkmcnt(6)\n\t"
        "v_mfma_f32_16x16x32_bf16 v[0:3], v[32:35], v[16:19], 0\n\t"
        "ds_read_b64_tr_b16 v[32:33], v119 offset:2112\n\t"
        "ds_read_b64_tr_b16 v[34:35], v119 offset:2144\n\t"
        "v_mfma_f32_16x16x32_bf16 v[4:7], v[36:39], v[16:19], 0\n\t"
        "ds_read_b64_tr_b16 v[36:37], v119 offset:2368\n\t"
        "ds_read_b64_tr_b16 v[38:39], v119 offset:2400\n\t"
        "s_waitcnt lgkmcnt(8)\n\t"
        "v_mfma_f32_16x16x32_bf16 v[0:3], v[40:43], v[20:23], v[0:3]\n\t"
        "ds_read_b64_tr_b16 v[40:41], v119 offset:2624\n\t"
        "ds_read_b64_tr_b16 v[42:43], v119 offset:2656\n\t"
        "v_mfma_f32_16x16x32_bf16 v[4:7], v[44:47], v[20:23], v[4:7]\n\t"
        "ds_read_b64_tr_b16 v[44:45], v119 offset:2880\n\t"
        "ds_read_b64_tr_b16 v[46:47], v119 offset:2912\n\t"
        "s_waitcnt lgkmcnt(10)\n\t"
        "v_mfma_f32_16x16x32_bf16 v[0:3], v[48:51], v[24:27], v[0:3]\n\t"
        "ds_read_b128 v[48:51], v108 offset:16384\n\t"
        "v_mfma_f32_16x16x32_bf16 v[4:7], v[52:55], v[24:27], v[4:7]\n\t"
        "ds_read_b128 v[52:55], v108 offset:17408\n\t"
        "s_waitcnt lgkmcnt(10)\n\t"
        "v_mfma_f32_16x16x32_bf16 v[0:3], v[56:59], v[28:31], v[0:3]\n\t"
        "v_mfma_f32_16x16x32_bf16 v[4:7], v[60:63], v[28:31], v[4:7]\n\t"
        "s_waitcnt lgkmcnt(2)\n\t"
        "v_mfma_f32_16x16x32_bf16 v[8:11], v[32:35], v[16:19], 0\n\t"
        "s_nop 1\n\t"
        "v_mfma_f32_16x16x32_bf16 v[8:11], v[36:39], v[20:23], v[8:11]\n\t"
        "v_mov_b64_e32 v[16:17], s[0:1]\n\t"
        "v_mov_b64_e32 v[18:19], s[0:1]\n\t"
        "v_mfma_f32_16x16x32_bf16 v[8:11], v[40:43], v[24:27], v[8:11]\n\t"
        "v_max3_f32 v59, v0, v1, v2\n\t"
        "v_max3_f32 v59, v59, v3, v4\n\t"
        "v_mfma_f32_16x16x32_bf16 v[8:11], v[44:47], v[28:31], v[8:11]\n\t"
        "v_max3_f32 v59, v59, v5, v6\n\t"
        "v_max_f32_e32 v60, v59, v7\n\t"
        "v_max_f32_e32 v59, v59, v7\n\t"
        "v_cndmask_b32_e64 v0, v0, v4, s[38:39]\n\t"
        "v_cndmask_b32_e64 v1, v1, v5, s[38:39]\n\t"
        "v_permlane32_swap_b32_e32 v59, v60\n\t"
        "v_cndmask_b32_e64 v2, v2, v6, s[38:39]\n\t"
        "v_cndmask_b32_e64 v3, v3, v7, s[38:39]\n\t"
        "v_max3_f32 v59, v59, v60, v8\n\t"
        "v_max3_f32 v59, v59, v9, v10\n\t"
        "v_max_f32_e32 v60, v59, v11\n\t"
        "v_max_f32_e32 v59, v59, v11\n\t"
        "v_cndmask_b32_e64 v8, v8, v9, s[38:39]\n\t"
        "v_cndmask_b32_e64 v10, v10, v11, s[38:39]\n\t"
        "v_permlane16_swap_b32_e32 v59, v60\n\t"
        "v_max_f32_e32 v59, v59, v60\n\t"
        "v_mul_f32_e32 v59, 0xbe02940c, v59\n\t"
        "v_cndmask_b32_e64 v58, v8, v10, s[40:41]\n\t"
        "v_fmamk_f32 v0, v0, 0x3e02940c, v59\n\t"
        "v_exp_f32_e32 v0, v0\n\t"
        "v_fmamk_f32 v1, v1, 0x3e02940c, v59\n\t"
        "v_exp_f32_e32 v1, v1\n\t"
        "v_fmamk_f32 v2, v2, 0x3e02940c, v59\n\t"
        "v_exp_f32_e32 v2, v2\n\t"
        "v_fmamk_f32 v3, v3, 0x3e02940c, v59\n\t"
        "v_exp_f32_e32 v3, v3\n\t"
        "v_fmamk_f32 v58, v58, 0x3e02940c, v59\n\t"
        "v_cvt_pk_bf16_f32 v12, v0, v1\n\t"
        "v_cvt_pk_bf16_f32 v13, v2, v3\n\t"
        "v_exp_f32_e32 v58, v58\n\t"
        "s_waitcnt lgkmcnt(0)\n\t"
        "v_mov_b32_dpp v14, v12 row_ror:8 row_mask:0xf bank_mask:0xf\n\t"
        "v_mov_b32_dpp v15, v13 row_ror:8 row_mask:0xf bank_mask:0xf\n\t"
        "v_mov_b32_dpp v59, v58 row_ror:8 row_mask:0xf bank_mask:0xf\n\t"
        "v_cvt_pk_bf16_f32 v59, v58, v59\n\t"
        "v_mfma_f32_16x16x32_bf16 v[20:23], v[16:19], v[12:15], 0\n\t"
        "v_cndmask_b32_e64 v56, v59, 0, s[40:41]\n\t"
        "v_cndmask_b32_e64 v57, 0, v59, s[40:41]\n\t"
        "v_mfma_f32_16x16x32_bf16 v[4:7], v[48:51], v[12:15], 0\n\t"
        "s_waitcnt vmcnt(1)\n\t"
        "v_max3_u32 v111, v104, v105, v106\n\t"
        "v_mfma_f32_16x16x32_bf16 v[8:11], v[52:55], v[12:15], 0\n\t"
        "v_max_u32_e32 v111, v111, v107\n\t"
        "v_mfma_f32_16x16x16_bf16 v[20:23], v[16:17], v[56:57], v[20:23]\n\t"
        "v_cmp_eq_u32_e32 vcc, -1, v111\n\t"
        "v_mfma_f32_16x16x16_bf16 v[4:7], v[104:105], v[56:57], v[4:7]\n\t"
        "s_nop 1\n\t"
        "v_mfma_f32_16x16x16_bf16 v[8:11], v[106:107], v[56:57], v[8:11]\n\t"
        "s_cbranch_vccnz LA_vmfail%=\n\t"
        "LA_vmok%=:\n\t"
        "s_nop 1\n\t"
        "v_rcp_f32_e32 v59, v20\n\t"
        "s_nop 0\n\t"
        "v_pk_mul_f32 v[4:5], v[4:5], v[58:59] op_sel:[0,1] op_sel_hi:[1,1]\n\t"
        "v_cvt_pk_bf16_f32 v0, v4, v5\n\t"
        "v_pk_mul_f32 v[6:7], v[6:7], v[58:59] op_sel:[0,1] op_sel_hi:[1,1]\n\t"
        "v_cvt_pk_bf16_f32 v1, v6, v7\n\t"
        "v_pk_mul_f32 v[8:9], v[8:9], v[58:59] op_sel:[0,1] op_sel_hi:[1,1]\n\t"
        "v_cvt_pk_bf16_f32 v2, v8, v9\n\t"
        "v_pk_mul_f32 v[10:11], v[10:11], v[58:59] op_sel:[0,1] op_sel_hi:[1,1]\n\t"
        "v_cvt_pk_bf16_f32 v3, v10, v11\n\t"
        "buffer_store_dwordx4 v[0:3], v121, s[8:11], s26 offen sc1\n\t"
        "LA_x%=:\n\t"
        "s_memrealtime s[52:53]\n\t"
        "ds_read_b128 v[16:19], v109 offset:8192\n\t"
        "ds_read_b128 v[20:23], v109 offset:9216\n\t"
        "ds_read_b128 v[24:27], v109 offset:10240\n\t"
        "ds_read_b128 v[28:31], v109 offset:11264\n\t"
        "ds_read_b128 v[56:59], v124 offset:0\n\t"
        "ds_read_b128 v[60:63], v124 offset:4096\n\t"
        "ds_read_b128 v[12:15], v124 offset:1024\n\t"
        "v_xor_b32_e32 v126, 0x800, v108\n\t"
        "s_add_u32 s48, s24, 1\n\t"
        "s_min_u32 s48, s48, 63\n\t"
        "buffer_load_dwordx2 v[108:109], v125, s[8:11], s26 offen sc1\n\t"
        "s_lshl_b32 s35, s44, 10\n\t"
        "s_lshr_b32 s34, s48, 1\n\t"
        "s_add_u32 s34, s34, 1\n\t"
        "s_min_u32 s34, s34, 31\n\t"
        "s_cmp_gt_u32 s54, s34\n\t"
        "s_cselect_b32 s35, 0xf0000000, s35\n\t"
        "v_lshlrev_b32_e32 v111, 2, v122\n\t"
        "v_and_b32_e32 v111, 0x3c0, v111\n\t"
        "buffer_load_dword v111, v111, s[12:15], s35 offen offset:0 sc1\n\t"
        "s_waitcnt lgkmcnt(6)\n\t"
        "v_mfma_f32_16x16x32_bf16 v[8:11], v[32:35], v[16:19], 0\n\t"
        "ds_read_b128 v[32:35], v124 offset:5120\n\t"
        "s_waitcnt lgkmcnt(6)\n\t"
        "v_mfma_f32_16x16x32_bf16 v[8:11], v[36:39], v[20:23], v[8:11]\n\t"
        "ds_read_b128 v[36:39], v124 offset:2048\n\t"
        "s_waitcnt lgkmcnt(6)\n\t"
        "v_mfma_f32_16x16x32_bf16 v[8:11], v[40:43], v[24:27], v[8:11]\n\t"
        "ds_read_b128 v[40:43], v124 offset:6144\n\t"
        "s_waitcnt lgkmcnt(6)\n\t"
        "v_mfma_f32_16x16x32_bf16 v[8:11], v[44:47], v[28:31], v[8:11]\n\t"
        "ds_read_b128 v[44:47], v124 offset:3072\n\t"
        "s_add_u32 s35, s24, 1\n\t"
        "s_waitcnt lgkmcnt(6)\n\t"
        "v_mfma_f32_16x16x32_bf16 v[0:3], v[56:59], v[16:19], 0\n\t"
        "ds_read_b128 v[56:59], v124 offset:7168\n\t"
        "s_waitcnt lgkmcnt(6)\n\t"
        "v_mfma_f32_16x16x32_bf16 v[4:7], v[60:63], v[16:19], 0\n\t"
        "s_and_b32 s36, s35, 1\n\t"
        "s_mul_i32 s36, s36, 24576\n\t"
        "s_waitcnt lgkmcnt(5)\n\t"
        "v_mfma_f32_16x16x32_bf16 v[0:3], v[12:15], v[20:23], v[0:3]\n\t"
        "s_add_u32 s36, s36, s55\n\t"
        "s_lshl_b32 s37, s48, 18\n\t"
        "s_waitcnt lgkmcnt(4)\n\t"
        "v_mfma_f32_16x16x32_bf16 v[4:7], v[32:35], v[20:23], v[4:7]\n\t"
        "v_xor_b32_e32 v32, 0x200, v120\n\t"
        "buffer_load_dwordx4 v[12:15], v32, s[8:11], s26 offen sc1\n\t"
        "s_waitcnt lgkmcnt(3)\n\t"
        "v_mfma_f32_16x16x32_bf16 v[0:3], v[36:39], v[24:27], v[0:3]\n\t"
        "s_lshl_b32 s34, s43, 11\n\t"
        "s_add_u32 s37, s37, s34\n\t"
        "s_waitcnt lgkmcnt(2)\n\t"
        "v_mfma_f32_16x16x32_bf16 v[4:7], v[40:43], v[24:27], v[4:7]\n\t"
        "v_cvt_pk_bf16_f32 v8, 0, v8\n\t"
        "v_cvt_pk_bf16_f32 v9, 0, v9\n\t"
        "v_cvt_pk_bf16_f32 v10, 0, v10\n\t"
        "v_cvt_pk_bf16_f32 v11, 0, v11\n\t"
        "s_waitcnt lgkmcnt(1)\n\t"
        "v_mfma_f32_16x16x32_bf16 v[0:3], v[44:47], v[28:31], v[0:3]\n\t"
        "v_mul_f32_e32 v8, 0x3db504f3, v8\n\t"
        "v_mul_f32_e32 v9, 0x3db504f3, v9\n\t"
        "v_mul_f32_e32 v10, 0x3db504f3, v10\n\t"
        "v_mul_f32_e32 v11, 0x3db504f3, v11\n\t"
        "s_waitcnt lgkmcnt(0)\n\t"
        "v_mfma_f32_16x16x32_bf16 v[4:7], v[56:59], v[28:31], v[4:7]\n\t"
        "v_cvt_pk_bf16_f32 v8, 0, v8\n\t"
        "v_cvt_pk_bf16_f32 v9, 0, v9\n\t"
        "v_cvt_pk_bf16_f32 v10, 0, v10\n\t"
        "v_cvt_pk_bf16_f32 v11, 0, v11\n\t"
        "ds_read_b128 v[56:59], v126 offset:16384\n\t"
        "ds_read_b128 v[60:63], v126 offset:17408\n\t"
        "v_mov_b64_e32 v[36:37], -1\n\t"
        "v_mov_b64_e32 v[38:39], -1\n\t"
        "v_mov_b32_e32 v40, 0xce6e6b28\n\t"
        "v_mov_b32_e32 v46, 0x3db504f3\n\t"
        "v_mov_b32_e32 v47, 0x3fb8aa3b\n\t"
        "v_cvt_pk_bf16_f32 v0, 0, v0\n\t"
        "v_cvt_pk_bf16_f32 v1, 0, v1\n\t"
        "v_cvt_pk_bf16_f32 v2, 0, v2\n\t"
        "v_cvt_pk_bf16_f32 v3, 0, v3\n\t"
        "v_cvt_pk_bf16_f32 v4, 0, v4\n\t"
        "v_cvt_pk_bf16_f32 v5, 0, v5\n\t"
        "v_cvt_pk_bf16_f32 v6, 0, v6\n\t"
        "v_cvt_pk_bf16_f32 v7, 0, v7\n\t"
        "v_pk_mul_f32 v[0:1], v[0:1], v[46:47] op_sel_hi:[1,0]\n\t"
        "v_pk_mul_f32 v[2:3], v[2:3], v[46:47] op_sel_hi:[1,0]\n\t"
        "v_pk_mul_f32 v[4:5], v[4:5], v[46:47] op_sel_hi:[1,0]\n\t"
        "v_pk_mul_f32 v[6:7], v[6:7], v[46:47] op_sel_hi:[1,0]\n\t"
        "s_lshr_b32 s59, s48, 1\n\t"
        "s_cmp_gt_u32 s54, s59\n\t"
        "s_cbranch_scc1 LA_kvok%=\n\t"
        "s_mov_b32 s42, 0\n\t"
        "s_waitcnt vmcnt(1)\n\t"
        "s_branch LA_kvw%=\n\t"
        "LA_kv%=:\n\t"
        "s_waitcnt vmcnt(0)\n\t"
        "LA_kvw%=:\n\t"
        "v_min_u32_dpp v111, v111, v111 quad_perm:[1,0,3,2] row_mask:0xf bank_mask:0xf\n\t"
        "s_nop 1\n\t"
        "v_min_u32_dpp v111, v111, v111 quad_perm:[2,3,0,1] row_mask:0xf bank_mask:0xf\n\t"
        "s_nop 1\n\t"
        "v_min_u32_dpp v111, v111, v111 row_ror:4 row_mask:0xf bank_mask:0xf\n\t"
        "s_nop 1\n\t"
        "v_min_u32_dpp v111, v111, v111 row_ror:8 row_mask:0xf bank_mask:0xf\n\t"
        "s_nop 0\n\t"
        "v_readfirstlane_b32 s54, v111\n\t"
        "s_nop 0\n\t"
        "s_cmp_gt_u32 s54, s59\n\t"
        "s_cbranch_scc1 LA_kvok%=\n\t"
        "s_sleep 4\n\t"
        "s_lshl_b32 s35, s44, 10\n\t"
        "v_lshlrev_b32_e32 v111, 2, v122\n\t"
        "v_and_b32_e32 v111, 0x3c0, v111\n\t"
        "buffer_load_dword v111, v111, s[12:15], s35 offen offset:0 sc1\n\t"
        "s_add_u32 s42, s42, 1\n\t"
        "s_cmp_lt_u32 s42, 0x100000\n\t"
        "s_cbranch_scc1 LA_kv%=\n\t"
        "s_movk_i32 s34, 16\n\t"
        "s_branch LA_die%=\n\t"
        "LA_kvok%=:\n\t"
        "s_add_u32 m0, s36, 0\n\t"
        "s_add_u32 s34, s37, 29548544\n\t"
        "buffer_load_dwordx4 v122, s[12:15], s34 offen sc1 lds\n\t"
        "v_cvt_pk_bf16_f32 v0, 0, v0\n\t"
        "v_cvt_pk_bf16_f32 v1, 0, v1\n\t"
        "v_cvt_pk_bf16_f32 v2, 0, v2\n\t"
        "v_cvt_pk_bf16_f32 v3, 0, v3\n\t"
        "v_cvt_pk_bf16_f32 v4, 0, v4\n\t"
        "v_cvt_pk_bf16_f32 v5, 0, v5\n\t"
        "v_cvt_pk_bf16_f32 v6, 0, v6\n\t"
        "v_cvt_pk_bf16_f32 v7, 0, v7\n\t"
        "s_add_u32 m0, s36, 1024\n\t"
        "s_add_u32 s34, s37, 29549568\n\t"
        "buffer_load_dwordx4 v122, s[12:15], s34 offen sc1 lds\n\t"
        "v_cndmask_b32_e64 v0, v40, v0, s[60:61]\n\t"
        "v_cndmask_b32_e64 v1, v40, v1, s[62:63]\n\t"
        "v_cndmask_b32_e64 v2, v40, v2, s[64:65]\n\t"
        "v_cndmask_b32_e64 v3, v40, v3, s[66:67]\n\t"
        "v_cndmask_b32_e64 v4, v40, v4, s[68:69]\n\t"
        "v_cndmask_b32_e64 v5, v40, v5, s[70:71]\n\t"
        "v_cndmask_b32_e64 v6, v40, v6, s[72:73]\n\t"
        "v_cndmask_b32_e64 v7, v40, v7, s[74:75]\n\t"
        "s_add_u32 m0, s36, 8192\n\t"
        "s_add_u32 s34, s37, 12771328\n\t"
        "buffer_load_dwordx4 v122, s[12:15], s34 offen sc1 lds\n\t"
        "v_max3_f32 v42, v0, v1, v2\n\t"
        "v_max3_f32 v42, v42, v3, v4\n\t"
        "v_max3_f32 v42, v42, v5, v6\n\t"
        "v_max3_f32 v42, v42, v7, v8\n\t"
        "v_max3_f32 v42, v42, v9, v10\n\t"
        "v_mov_b32_e32 v43, v11\n\t"
        "v_max_f32_e32 v44, v42, v43\n\t"
        "v_max_f32_e32 v42, v42, v43\n\t"
        "s_pack_ll_b32_b16 s34, s52, s24\n\t"
        "v_mov_b32_e32 v40, s34\n\t"
        "s_add_u32 s35, s26, s57\n\t"
        "v_permlane16_swap_b32_e32 v42, v44\n\t"
        "v_max_f32_e32 v45, v42, v44\n\t"
        "v_max_f32_e32 v42, v42, v44\n\t"
        "s_mov_b64 exec, 1\n\t"
        "buffer_store_dword v40, off, s[8:11], s35 sc1\n\t"
        "s_mov_b64 exec, -1\n\t"
        "v_permlane32_swap_b32_e32 v42, v45\n\t"
        "v_max_f32_e32 v42, v42, v45\n\t"
        "v_mul_f32_e32 v42, 0xbfb8aa3b, v42\n\t"
        "v_pk_fma_f32 v[0:1], v[0:1], v[46:47], v[42:43] op_sel:[0,1,0] op_sel_hi:[1,1,0]\n\t"
        "s_add_u32 m0, s36, 9216\n\t"
        "s_add_u32 s34, s37, 12772352\n\t"
        "buffer_load_dwordx4 v122, s[12:15], s34 offen sc1 lds\n\t"
        "v_exp_f32_e32 v0, v0\n\t"
        "v_exp_f32_e32 v1, v1\n\t"
        "v_pk_fma_f32 v[2:3], v[2:3], v[46:47], v[42:43] op_sel:[0,1,0] op_sel_hi:[1,1,0]\n\t"
        "v_exp_f32_e32 v2, v2\n\t"
        "v_exp_f32_e32 v3, v3\n\t"
        "v_pk_fma_f32 v[4:5], v[4:5], v[46:47], v[42:43] op_sel:[0,1,0] op_sel_hi:[1,1,0]\n\t"
        "v_exp_f32_e32 v4, v4\n\t"
        "s_add_u32 m0, s36, 16384\n\t"
        "s_add_u32 s34, s37, 46325760\n\t"
        "buffer_load_dwordx4 v122, s[12:15], s34 offen sc1 lds\n\t"
        "v_exp_f32_e32 v5, v5\n\t"
        "v_pk_fma_f32 v[6:7], v[6:7], v[46:47], v[42:43] op_sel:[0,1,0] op_sel_hi:[1,1,0]\n\t"
        "v_exp_f32_e32 v6, v6\n\t"
        "v_exp_f32_e32 v7, v7\n\t"
        "v_pk_fma_f32 v[8:9], v[8:9], v[46:47], v[42:43] op_sel:[0,1,0] op_sel_hi:[1,1,0]\n\t"
        "v_exp_f32_e32 v8, v8\n\t"
        "v_exp_f32_e32 v9, v9\n\t"
        "s_add_u32 m0, s36, 17408\n\t"
        "s_add_u32 s34, s37, 46326784\n\t"
        "buffer_load_dwordx4 v122, s[12:15], s34 offen sc1 lds\n\t"
        "v_pk_fma_f32 v[10:11], v[10:11], v[46:47], v[42:43] op_sel:[0,1,0] op_sel_hi:[1,1,0]\n\t"
        "v_exp_f32_e32 v10, v10\n\t"
        "v_exp_f32_e32 v11, v11\n\t"
        "v_pk_add_f32 v[42:43], v[0:1], v[2:3]\n\t"
        "v_cndmask_b32_e64 v8, v8, 0, s[40:41]\n\t"
        "v_cndmask_b32_e64 v9, v9, 0, s[40:41]\n\t"
        "v_cndmask_b32_e64 v10, v10, 0, s[40:41]\n\t"
        "buffer_store_dwordx4 v[36:39], v121, s[8:11], s28 offen sc1\n\t"
        "v_cndmask_b32_e64 v11, v11, 0, s[40:41]\n\t"
        "v_pk_add_f32 v[42:43], v[42:43], v[4:5]\n\t"
        "v_pk_add_f32 v[42:43], v[42:43], v[6:7]\n\t"
        "v_pk_add_f32 v[42:43], v[42:43], v[8:9]\n\t"
        "v_pk_add_f32 v[42:43], v[42:43], v[10:11]\n\t"
        "v_add_f32_e32 v44, v42, v43\n\t"
        "v_add_f32_e32 v42, v42, v43\n\t"
        "s_lshl_b32 s34, s24, 18\n\t"
        "s_add_u32 s34, s34, 63102976\n\t"
        "v_permlane16_swap_b32_e32 v42, v44\n\t"
        "v_add_f32_e32 v45, v42, v44\n\t"
        "v_add_f32_e32 v42, v42, v44\n\t"
        "s_add_u32 s35, s24, 1\n\t"
        "v_mov_b32_e32 v126, s35\n\t"
        "v_permlane32_swap_b32_e32 v42, v45\n\t"
        "v_add_f32_e32 v42, v42, v45\n\t"
        "v_rcp_f32_e32 v42, v42\n\t"
        "s_waitcnt vmcnt(8) lgkmcnt(0)\n\t"
        "v_max3_u32 v45, v12, v13, v14\n\t"
        "v_max3_u32 v45, v45, v15, v104\n\t"
        "v_max3_u32 v45, v45, v105, v106\n\t"
        "v_max_u32_e32 v45, v45, v107\n\t"
        "v_cmp_eq_u32_e32 vcc, -1, v45\n\t"
        "s_nop 0\n\t"
        "s_cbranch_vccnz LA_v2fail%=\n\t"
        "LA_v2ok%=:\n\t"
        "v_and_b32_e32 v45, 0xff, v109\n\t"
        "v_cmp_ne_u32_e32 vcc, s24, v45\n\t"
        "s_nop 0\n\t"
        "s_cbranch_vccnz LA_stre%=\n\t"
        "LA_stok%=:\n\t"
        "v_pk_mul_f32 v[0:1], v[0:1], v[42:43] op_sel_hi:[1,0]\n\t"
        "v_subrev_u32_e32 v108, s25, v108\n\t"
        "v_min_u32_dpp v111, v111, v111 quad_perm:[1,0,3,2] row_mask:0xf bank_mask:0xf\n\t"
        "v_cvt_pk_bf16_f32 v32, v0, v1\n\t"
        "v_pk_mul_f32 v[2:3], v[2:3], v[42:43] op_sel_hi:[1,0]\n\t"
        "v_cvt_pk_bf16_f32 v33, v2, v3\n\t"
        "v_max_i32_dpp v108, v108, v108 quad_perm:[1,0,3,2] row_mask:0xf bank_mask:0xf\n\t"
        "v_min_u32_dpp v111, v111, v111 quad_perm:[2,3,0,1] row_mask:0xf bank_mask:0xf\n\t"
        "v_pk_mul_f32 v[4:5], v[4:5], v[42:43] op_sel_hi:[1,0]\n\t"
        "v_cvt_pk_bf16_f32 v34, v4, v5\n\t"
        "v_pk_mul_f32 v[6:7], v[6:7], v[42:43] op_sel_hi:[1,0]\n\t"
        "v_max_i32_dpp v108, v108, v108 quad_perm:[2,3,0,1] row_mask:0xf bank_mask:0xf\n\t"
        "v_min_u32_dpp v111, v111, v111 row_ror:4 row_mask:0xf bank_mask:0xf\n\t"
        "v_cvt_pk_bf16_f32 v35, v6, v7\n\t"
        "v_pk_mul_f32 v[8:9], v[8:9], v[42:43] op_sel_hi:[1,0]\n\t"
        "v_cvt_pk_bf16_f32 v36, v8, v9\n\t"
        "v_max_i32_dpp v108, v108, v108 row_ror:4 row_mask:0xf bank_mask:0xf\n\t"
        "v_min_u32_dpp v111, v111, v111 row_ror:8 row_mask:0xf bank_mask:0xf\n\t"
        "v_pk_mul_f32 v[10:11], v[10:11], v[42:43] op_sel_hi:[1,0]\n\t"
        "v_cvt_pk_bf16_f32 v37, v10, v11\n\t"
        "v_mfma_f32_16x16x32_bf16 v[16:19], v[48:51], v[32:35], 0\n\t"
        "v_max_i32_dpp v108, v108, v108 row_ror:8 row_mask:0xf bank_mask:0xf\n\t"
        "v_mfma_f32_16x16x32_bf16 v[20:23], v[52:55], v[32:35], 0\n\t"
        "v_mfma_f32_16x16x32_bf16 v[24:27], v[56:59], v[32:35], 0\n\t"
        "v_mfma_f32_16x16x32_bf16 v[28:31], v[60:63], v[32:35], 0\n\t"
        "v_max_i32_dpp v108, v108, v108 row_bcast:15 row_mask:0xa bank_mask:0xf\n\t"
        "v_xor_b32_e32 v45, 0x400, v123\n\t"
        "v_add_u32_e32 v124, s56, v124\n\t"
        "v_mfma_f32_16x16x16_bf16 v[16:19], v[104:105], v[36:37], v[16:19]\n\t"
        "v_max_i32_dpp v108, v108, v108 row_bcast:31 row_mask:0xc bank_mask:0xf\n\t"
        "v_mfma_f32_16x16x16_bf16 v[20:23], v[106:107], v[36:37], v[20:23]\n\t"
        "v_mfma_f32_16x16x16_bf16 v[24:27], v[12:13], v[36:37], v[24:27]\n\t"
        "v_mfma_f32_16x16x16_bf16 v[28:31], v[14:15], v[36:37], v[28:31]\n\t"
        "ds_read_b128 v[48:51], v117 offset:4096\n\t"
        "ds_read_b128 v[52:55], v117 offset:5120\n\t"
        "ds_read_b128 v[56:59], v117 offset:6144\n\t"
        "ds_read_b128 v[60:63], v117 offset:7168\n\t"
        "s_sub_u32 s56, 0, s56\n\t"
        "v_cvt_pk_bf16_f32 v0, v16, v17\n\t"
        "v_cvt_pk_bf16_f32 v1, v18, v19\n\t"
        "v_cvt_pk_bf16_f32 v2, v20, v21\n\t"
        "v_cvt_pk_bf16_f32 v3, v22, v23\n\t"
        "buffer_store_dwordx4 v[0:3], v123, s[12:15], s34 offen sc1\n\t"
        "v_cvt_pk_bf16_f32 v4, v24, v25\n\t"
        "v_cvt_pk_bf16_f32 v5, v26, v27\n\t"
        "v_cvt_pk_bf16_f32 v6, v28, v29\n\t"
        "v_cvt_pk_bf16_f32 v7, v30, v31\n\t"
        "buffer_store_dwordx4 v[4:7], v45, s[12:15], s34 offen sc1\n\t"
        "s_cmp_eq_u32 s24, 63\n\t"
        "s_cbranch_scc1 LA_last%=\n\t"
        "ds_read_b128 v[32:35], v117 offset:0\n\t"
        "ds_read_b128 v[36:39], v117 offset:1024\n\t"
        "ds_read_b128 v[40:43], v117 offset:2048\n\t"
        "ds_read_b128 v[44:47], v117 offset:3072\n\t"
        "v_mov_b64_e32 v[14:15], 0\n\t"
        "v_mov_b64_e32 v[12:13], 0\n\t"
        "v_mov_b64_e32 v[10:11], 0\n\t"
        "v_mov_b64_e32 v[8:9], 0\n\t"
        "v_mov_b64_e32 v[6:7], 0\n\t"
        "v_mov_b64_e32 v[4:5], 0\n\t"
        "v_mov_b64_e32 v[2:3], 0\n\t"
        "v_mov_b64_e32 v[0:1], 0\n\t"
        "v_readfirstlane_b32 s34, v111\n\t"
        "s_nop 0\n\t"
        "s_max_u32 s54, s54, s34\n\t"
        "v_readfirstlane_b32 s35, v109\n\t"
        "v_readlane_b32 s36, v108, 63\n\t"
        "s_bfe_u32 s29, s35, 0xc0008\n\t"
        "s_lshr_b32 s30, s35, 20\n\t"
        "s_add_u32 s35, s31, s50\n\t"
        "s_mul_i32 s37, s50, 3\n\t"
        "s_min_u32 s35, s35, s37\n\t"
        "s_add_i32 s34, s36, s51\n\t"
        "s_cmp_gt_i32 s34, 0\n\t"
        "s_cselect_b32 s31, s35, 0\n\t"
        "s_cselect_b32 s34, s34, 0\n\t"
        "s_add_u32 s34, s34, s31\n\t"
        "s_add_u32 s37, s25, s34\n\t"
        "s_add_u32 s45, s37, s29\n\t"
        "s_add_u32 s25, s45, s30\n\t"
        "s_add_u32 s24, s24, 1\n\t"
        "s_mov_b32 s28, s26\n\t"
        "s_mov_b32 s26, s27\n\t"
        "s_add_u32 s27, s27, 0x80000\n\t"
        "s_and_b32 s27, s27, 0x1fffff\n\t"
        "s_branch LA_loop%=\n\t"
        "LA_last%=:\n\t"
        "s_waitcnt vmcnt(0)\n\t"
        "s_barrier\n\t"
        "buffer_store_dword v126, v127, s[12:15], 0 offen sc1\n\t"
        "s_endpgm\n\t"
        "LA_skip%=:\n\t"
        "buffer_store_dword v126, v127, s[12:15], 0 offen sc1\n\t"
        "ds_read_b64_tr_b16 v[32:33], v119 offset:2112\n\t"
        "ds_read_b64_tr_b16 v[34:35], v119 offset:2144\n\t"
        "ds_read_b64_tr_b16 v[36:37], v119 offset:2368\n\t"
        "ds_read_b64_tr_b16 v[38:39], v119 offset:2400\n\t"
        "ds_read_b64_tr_b16 v[40:41], v119 offset:2624\n\t"
        "ds_read_b64_tr_b16 v[42:43], v119 offset:2656\n\t"
        "ds_read_b64_tr_b16 v[44:45], v119 offset:2880\n\t"
        "ds_read_b64_tr_b16 v[46:47], v119 offset:2912\n\t"
        "ds_read_b128 v[48:51], v108 offset:16384\n\t"
        "ds_read_b128 v[52:55], v108 offset:17408\n\t"
        "s_waitcnt lgkmcnt(0)\n\t"
        "s_branch LA_x%=\n\t"
        "LA_stre%=:\n\t"
        "s_mov_b32 s42, 0\n\t"
        "LA_str2%=:\n\t"
        "buffer_load_dwordx2 v[108:109], v125, s[8:11], s26 offen sc1\n\t"
        "s_waitcnt vmcnt(0)\n\t"
        "v_and_b32_e32 v45, 0xff, v109\n\t"
        "v_cmp_ne_u32_e32 vcc, s24, v45\n\t"
        "s_nop 0\n\t"
        "s_cbranch_vccz LA_stok%=\n\t"
        "s_add_u32 s42, s42, 1\n\t"
        "s_cmp_lt_u32 s42, 0x100000\n\t"
        "s_cbranch_scc1 LA_str2%=\n\t"
        "s_movk_i32 s34, 5\n\t"
        "s_branch LA_die%=\n\t"
        "LA_v2fail%=:\n\t"
        "s_mov_b32 s42, 0\n\t"
        "LA_v2re%=:\n\t"
        "v_xor_b32_e32 v45, 0x200, v120\n\t"
        "buffer_load_dwordx4 v[12:15], v45, s[8:11], s26 offen sc1\n\t"
        "buffer_load_dwordx4 v[104:107], v120, s[8:11], s26 offen sc1\n\t"
        "s_waitcnt vmcnt(0)\n\t"
        "v_max3_u32 v45, v12, v13, v14\n\t"
        "v_max3_u32 v45, v45, v15, v104\n\t"
        "v_max3_u32 v45, v45, v105, v106\n\t"
        "v_max_u32_e32 v45, v45, v107\n\t"
        "v_cmp_eq_u32_e32 vcc, -1, v45\n\t"
        "s_nop 0\n\t"
        "s_cbranch_vccz LA_v2ok%=\n\t"
        "s_add_u32 s42, s42, 1\n\t"
        "s_cmp_lt_u32 s42, 0x100000\n\t"
        "s_cbranch_scc1 LA_v2re%=\n\t"
        "s_movk_i32 s34, 6\n\t"
        "s_branch LA_die%=\n\t"
        "LA_rdfail%=:\n\t"
        "s_or_b32 s47, s47, 1\n\t"
        "buffer_load_dwordx4 v[16:19], v112, s[8:11], s26 offen offset:0 sc1\n\t"
        "buffer_load_dwordx4 v[20:23], v112, s[8:11], s26 offen offset:1024 sc1\n\t"
        "buffer_load_dwordx4 v[24:27], v112, s[8:11], s26 offen offset:2048 sc1\n\t"
        "buffer_load_dwordx4 v[28:31], v112, s[8:11], s26 offen offset:3072 sc1\n\t"
        "s_nop 7\n\t"
        "v_mov_b64_e32 v[0:1], 0\n\t"
        "v_mov_b64_e32 v[2:3], 0\n\t"
        "v_mov_b64_e32 v[4:5], 0\n\t"
        "v_mov_b64_e32 v[6:7], 0\n\t"
        "v_mov_b64_e32 v[8:9], 0\n\t"
        "v_mov_b64_e32 v[10:11], 0\n\t"
        "v_mov_b64_e32 v[12:13], 0\n\t"
        "v_mov_b64_e32 v[14:15], 0\n\t"
        "s_branch LA_rdre%=w\n\t"
        "LA_rdre%=:\n\t"
        "buffer_load_dwordx4 v[16:19], v112, s[8:11], s26 offen offset:0 sc1\n\t"
        "buffer_load_dwordx4 v[20:23], v112, s[8:11], s26 offen offset:1024 sc1\n\t"
        "buffer_load_dwordx4 v[24:27], v112, s[8:11], s26 offen offset:2048 sc1\n\t"
        "buffer_load_dwordx4 v[28:31], v112, s[8:11], s26 offen offset:3072 sc1\n\t"
        "LA_rdre%=w:\n\t"
        "s_waitcnt vmcnt(0) lgkmcnt(0)\n\t"
        "v_max3_u32 v111, v16, v17, v18\n\t"
        "v_max3_u32 v111, v111, v19, v20\n\t"
        "v_max3_u32 v111, v111, v21, v22\n\t"
        "v_max3_u32 v111, v111, v23, v24\n\t"
        "v_max3_u32 v111, v111, v25, v26\n\t"
        "v_max3_u32 v111, v111, v27, v28\n\t"
        "v_max3_u32 v111, v111, v29, v30\n\t"
        "v_max_u32_e32 v111, v111, v31\n\t"
        "v_cmp_eq_u32_e32 vcc, -1, v111\n\t"
        "s_nop 0\n\t"
        "s_cbranch_vccz LA_go%=\n\t"
        "s_add_u32 s42, s42, 1\n\t"
        "s_cmp_lt_u32 s42, 0x100000\n\t"
        "s_cbranch_scc1 LA_rdre%=\n\t"
        "s_movk_i32 s34, 1\n\t"
        "s_branch LA_die%=\n\t"
        "LA_vmfail%=:\n\t"
        "s_or_b32 s47, s47, 2\n\t"
        "s_mov_b32 s42, 0\n\t"
        "LA_vmre%=:\n\t"
        "buffer_load_dwordx4 v[104:107], v120, s[8:11], s26 offen sc1\n\t"
        "v_mfma_f32_16x16x32_bf16 v[4:7], v[48:51], v[12:15], 0\n\t"
        "v_mfma_f32_16x16x32_bf16 v[8:11], v[52:55], v[12:15], 0\n\t"
        "s_nop 7\n\t"
        "s_waitcnt vmcnt(0)\n\t"
        "v_mfma_f32_16x16x16_bf16 v[4:7], v[104:105], v[56:57], v[4:7]\n\t"
        "v_mfma_f32_16x16x16_bf16 v[8:11], v[106:107], v[56:57], v[8:11]\n\t"
        "v_max3_u32 v111, v104, v105, v106\n\t"
        "v_max_u32_e32 v111, v111, v107\n\t"
        "v_cmp_eq_u32_e32 vcc, -1, v111\n\t"
        "s_nop 7\n\t"
        "s_cbranch_vccz LA_vmok%=\n\t"
        "s_add_u32 s42, s42, 1\n\t"
        "s_cmp_lt_u32 s42, 0x100000\n\t"
        "s_cbranch_scc1 LA_vmre%=\n\t"
        "s_movk_i32 s34, 2\n\t"
        "s_branch LA_die%=\n\t"
        "LA_die%=:\n\t"
        "v_mov_b32_e32 v30, s34\n\t"
        "s_mov_b32 s35, 33024\n\t"
        "s_mov_b64 exec, 1\n\t"
        "buffer_store_dword v30, off, s[12:15], s35 sc1\n\t"
        "s_endpgm\n\t"
        ".Lk_end_A:\n\t"
        :: REC_S_IN, "{v96}"(g_w), "{v97}"(wb0), "{v98}"(wb1), "{v99}"(rb0), "{v100}"(rb0 ^ 32u), "{v101}"(rb0 ^ 64u), "{v102}"(rb0 ^ 96u),
           "{v112}"(a_mem), "{v113}"(l_ms + w * 4096), "{v114}"(l_ms + ((w + 1) & 3) * 4096), "{v115}"(l_ms + ((w + 2) & 3) * 4096), "{v116}"(l_ms + ((w + 3) & 3) * 4096),
           "{v117}"(lb + w * 22528 + l * 16), "{v118}"(l_iw), "{v119}"(l_iq), "{v120}"(a_vm), "{v121}"(a_o), "{v122}"(h * 8192 + l * 16), "{v123}"(a_ox), "{v124}"(lb + L_KV + l * 16), "{s55}"(lb + L_KV + w * 2048),
           "{v125}"((24 + cp) * 16384 + l * 32), "{v126}"(16 * (w & 1) + n - 4 * g), "{v103}"((l & 1) ? 0xEEEEEEEEu : 0x44444444u),
           "{v127}"((l == 0 && w == 0) ? F_PROG + h * 128 : OOB)
        : "memory", "vcc", "scc", "v0", "v255", "a0", "a255", "s101");
    __builtin_unreachable();
}
__device__ __forceinline__ void rec_b(const Params& p, unsigned lb, const int wv, const int id, const int rg, const int cp) {
    const int w = wv, l = otid() & 63, n = l & 15, g = l >> 4, cs = id, rb = 32 + id * 2 + rg;
    const v4u sh = mk_srd(p.ws + ho_base(p.ws), HO_BYTES - 8192), sw = mk_srd(p.ws, W_TOTAL), s1 = mk_srd(p.Wv, 1024 * 4096 * 2), s2 = mk_srd(p.Wo, 1024 * 4096 * 2);
    const int r8 = l >> 3, pc = l & 7, pp = n >> 3, bb = (n >> 2) & 1, f = (g & 1) | (pp << 1);
    const unsigned stg = lb + LB_STG + w * LB_STGP;
    const unsigned g_v = r8 * 8192 + cs * 256 + rg * 128 + pc * 16, g_o = (l >> 2) * 2048 + cs * 64 + (l & 3) * 16;
    const unsigned wb0 = stg + r8 * 128 + (((pc >> 1) ^ (r8 >> 2)) << 5) + (pc & 1) * 16, wb1 = wb0 ^ 64u;
    const unsigned rb0 = stg + (16 * (g >> 1) + 8 * pp + 4 * (g & 1) + bb) * 128 + (f << 5) + (n & 3) * 8;
    const unsigned o_r = stg + (g >> 1) * (2048 + 128) + (g & 1) * (1024 + 32) + (bb + 4 * pp) * 64 + (n & 3) * 8;
    const unsigned a_mem = cp * 32768 + (w >> 1) * 16384 + (w & 1) * 4096 + l * 16;
    const unsigned a_vmw = w < 2 ? (16 + (cs >> 2)) * 16384 + (cs & 3) * 2048 + (2 * rg + w) * 512 + (l & 31) * 16 + (g >> 1) * 8 : OOB;
    const unsigned a_or = (8 + rg * 4 + w) * 16384 + l * 16;
    const unsigned a_mw = w * 32768 + (cs >> 4) * 16384 + ((cs >> 1) & 7) * 1024 + (cs & 1) * 512 + (l >> 5) * 256 + rg * 128 + (l & 31) * 4;
    const unsigned a_sw = (w == 0 && l < 4) ? (24 + l) * 16384 + (id * 2 + rg) * 32 : OOB;
    const bf16_t* m0p = p.mem0 + (4 * rg + ((l >> 3) & 3)) * 1024 + cs * 32 + (l >> 5) * 16 + (l & 7) * 2;
    asm volatile(
        "s_nop 4\n\t"
        "s_memtime s[70:71]\n\t"
        "s_memrealtime s[72:73]\n\t"
        "global_load_dword v109, v[108:109], off\n\t"
        "s_lshl_b32 s34, s43, 21\n\t"
        "s_add_u32 s34, s34, 0\n\t"
        "buffer_load_dwordx4 v[128:131], v96, s[16:19], s34 offen nt\n\t"
        "s_add_u32 s34, s34, 0x10000\n\t"
        "buffer_load_dwordx4 v[132:135], v96, s[16:19], s34 offen nt\n\t"
        "s_add_u32 s34, s34, 0x10000\n\t"
        "buffer_load_dwordx4 v[136:139], v96, s[16:19], s34 offen nt\n\t"
        "s_add_u32 s34, s34, 0x10000\n\t"
        "buffer_load_dwordx4 v[140:143], v96, s[16:19], s34 offen nt\n\t"
        "s_add_u32 s34, s34, 0x10000\n\t"
        "buffer_load_dwordx4 v[144:147], v96, s[16:19], s34 offen nt\n\t"
        "s_add_u32 s34, s34, 0x10000\n\t"
        "buffer_load_dwordx4 v[148:151], v96, s[16:19], s34 offen nt\n\t"
        "s_add_u32 s34, s34, 0x10000\n\t"
        "buffer_load_dwordx4 v[152:155], v96, s[16:19], s34 offen nt\n\t"
        "s_add_u32 s34, s34, 0x10000\n\t"
        "buffer_load_dwordx4 v[156:159], v96, s[16:19], s34 offen nt\n\t"
        "s_lshl_b32 s34, s43, 21\n\t"
        "s_add_u32 s34, s34, 524288\n\t"
        "buffer_load_dwordx4 v[160:163], v96, s[16:19], s34 offen nt\n\t"
        "s_add_u32 s34, s34, 0x10000\n\t"
        "buffer_load_dwordx4 v[164:167], v96, s[16:19], s34 offen nt\n\t"
        "s_add_u32 s34, s34, 0x10000\n\t"
        "buffer_load_dwordx4 v[168:171], v96, s[16:19], s34 offen nt\n\t"
        "s_add_u32 s34, s34, 0x10000\n\t"
        "buffer_load_dwordx4 v[172:175], v96, s[16:19], s34 offen nt\n\t"
        "s_add_u32 s34, s34, 0x10000\n\t"
        "buffer_load_dwordx4 v[176:179], v96, s[16:19], s34 offen nt\n\t"
        "s_add_u32 s34, s34, 0x10000\n\t"
        "buffer_load_dwordx4 v[180:183], v96, s[16:19], s34 offen nt\n\t"
        "s_add_u32 s34, s34, 0x10000\n\t"
        "buffer_load_dwordx4 v[184:187], v96, s[16:19], s34 offen nt\n\t"
        "s_add_u32 s34, s34, 0x10000\n\t"
        "buffer_load_dwordx4 v[188:191], v96, s[16:19], s34 offen nt\n\t"
        "s_lshl_b32 s34, s43, 21\n\t"
        "s_add_u32 s34, s34, 1048576\n\t"
        "buffer_load_dwordx4 v[192:195], v96, s[16:19], s34 offen nt\n\t"
        "s_add_u32 s34, s34, 0x10000\n\t"
        "buffer_load_dwordx4 v[196:199], v96, s[16:19], s34 offen nt\n\t"
        "s_add_u32 s34, s34, 0x10000\n\t"
        "buffer_load_dwordx4 v[200:203], v96, s[16:19], s34 offen nt\n\t"
        "s_add_u32 s34, s34, 0x10000\n\t"
        "buffer_load_dwordx4 v[204:207], v96, s[16:19], s34 offen nt\n\t"
        "s_add_u32 s34, s34, 0x10000\n\t"
        "buffer_load_dwordx4 v[208:211], v96, s[16:19], s34 offen nt\n\t"
        "s_add_u32 s34, s34, 0x10000\n\t"
        "buffer_load_dwordx4 v[212:215], v96, s[16:19], s34 offen nt\n\t"
        "s_add_u32 s34, s34, 0x10000\n\t"
        "buffer_load_dwordx4 v[216:219], v96, s[16:19], s34 offen nt\n\t"
        "s_add_u32 s34, s34, 0x10000\n\t"
        "buffer_load_dwordx4 v[220:223], v96, s[16:19], s34 offen nt\n\t"
        "s_waitcnt vmcnt(16)\n\t"
        "ds_write_b128 v98, v[128:131] offset:0\n\t"
        "ds_write_b128 v99, v[132:135] offset:1024\n\t"
        "ds_write_b128 v98, v[136:139] offset:2048\n\t"
        "ds_write_b128 v99, v[140:143] offset:3072\n\t"
        "ds_write_b128 v98, v[144:147] offset:4096\n\t"
        "ds_write_b128 v99, v[148:151] offset:5120\n\t"
        "ds_write_b128 v98, v[152:155] offset:6144\n\t"
        "ds_write_b128 v99, v[156:159] offset:7168\n\t"
        "ds_read_b64_tr_b16 v[128:129], v100 offset:0\n\t"
        "ds_read_b64_tr_b16 v[130:131], v100 offset:256\n\t"
        "ds_read_b64_tr_b16 v[132:133], v100 offset:4096\n\t"
        "ds_read_b64_tr_b16 v[134:135], v100 offset:4352\n\t"
        "s_waitcnt lgkmcnt(8)\n\t"
        "ds_read_b64_tr_b16 v[136:137], v101 offset:0\n\t"
        "ds_read_b64_tr_b16 v[138:139], v101 offset:256\n\t"
        "ds_read_b64_tr_b16 v[140:141], v101 offset:4096\n\t"
        "ds_read_b64_tr_b16 v[142:143], v101 offset:4352\n\t"
        "s_waitcnt lgkmcnt(8)\n\t"
        "ds_read_b64_tr_b16 v[144:145], v102 offset:0\n\t"
        "ds_read_b64_tr_b16 v[146:147], v102 offset:256\n\t"
        "ds_read_b64_tr_b16 v[148:149], v102 offset:4096\n\t"
        "ds_read_b64_tr_b16 v[150:151], v102 offset:4352\n\t"
        "s_waitcnt lgkmcnt(8)\n\t"
        "ds_read_b64_tr_b16 v[152:153], v103 offset:0\n\t"
        "ds_read_b64_tr_b16 v[154:155], v103 offset:256\n\t"
        "ds_read_b64_tr_b16 v[156:157], v103 offset:4096\n\t"
        "ds_read_b64_tr_b16 v[158:159], v103 offset:4352\n\t"
        "s_lshl_b32 s34, s43, 21\n\t"
        "s_add_u32 s34, s34, 1572864\n\t"
        "buffer_load_dwordx4 v[224:227], v96, s[16:19], s34 offen nt\n\t"
        "s_add_u32 s34, s34, 0x10000\n\t"
        "buffer_load_dwordx4 v[228:231], v96, s[16:19], s34 offen nt\n\t"
        "s_add_u32 s34, s34, 0x10000\n\t"
        "buffer_load_dwordx4 v[232:235], v96, s[16:19], s34 offen nt\n\t"
        "s_add_u32 s34, s34, 0x10000\n\t"
        "buffer_load_dwordx4 v[236:239], v96, s[16:19], s34 offen nt\n\t"
        "s_add_u32 s34, s34, 0x10000\n\t"
        "buffer_load_dwordx4 v[240:243], v96, s[16:19], s34 offen nt\n\t"
        "s_add_u32 s34, s34, 0x10000\n\t"
        "buffer_load_dwordx4 v[244:247], v96, s[16:19], s34 offen nt\n\t"
        "s_add_u32 s34, s34, 0x10000\n\t"
        "buffer_load_dwordx4 v[248:251], v96, s[16:19], s34 offen nt\n\t"
        "s_add_u32 s34, s34, 0x10000\n\t"
        "buffer_load_dwordx4 v[252:255], v96, s[16:19], s34 offen nt\n\t"
        "s_waitcnt lgkmcnt(0)\n\t"
        "s_waitcnt vmcnt(16)\n\t"
        "ds_write_b128 v98, v[160:163] offset:0\n\t"
        "ds_write_b128 v99, v[164:167] offset:1024\n\t"
        "ds_write_b128 v98, v[168:171] offset:2048\n\t"
        "ds_write_b128 v99, v[172:175] offset:3072\n\t"
        "ds_write_b128 v98, v[176:179] offset:4096\n\t"
        "ds_write_b128 v99, v[180:183] offset:5120\n\t"
        "ds_write_b128 v98, v[184:187] offset:6144\n\t"
        "ds_write_b128 v99, v[188:191] offset:7168\n\t"
        "ds_read_b64_tr_b16 v[160:161], v100 offset:0\n\t"
        "ds_read_b64_tr_b16 v[162:163], v100 offset:256\n\t"
        "ds_read_b64_tr_b16 v[164:165], v100 offset:4096\n\t"
        "ds_read_b64_tr_b16 v[166:167], v100 offset:4352\n\t"
        "s_waitcnt lgkmcnt(8)\n\t"
        "ds_read_b64_tr_b16 v[168:169], v101 offset:0\n\t"
        "ds_read_b64_tr_b16 v[170:171], v101 offset:256\n\t"
        "ds_read_b64_tr_b16 v[172:173], v101 offset:4096\n\t"
        "ds_read_b64_tr_b16 v[174:175], v101 offset:4352\n\t"
        "s_waitcnt lgkmcnt(8)\n\t"
        "ds_read_b64_tr_b16 v[176:177], v102 offset:0\n\t"
        "ds_read_b64_tr_b16 v[178:179], v102 offset:256\n\t"
        "ds_read_b64_tr_b16 v[180:181], v102 offset:4096\n\t"
        "ds_read_b64_tr_b16 v[182:183], v102 offset:4352\n\t"
        "s_waitcnt lgkmcnt(8)\n\t"
        "ds_read_b64_tr_b16 v[184:185], v103 offset:0\n\t"
        "ds_read_b64_tr_b16 v[186:187], v103 offset:256\n\t"
        "ds_read_b64_tr_b16 v[188:189], v103 offset:4096\n\t"
        "ds_read_b64_tr_b16 v[190:191], v103 offset:4352\n\t"
        "s_lshl_b32 s34, s43, 21\n\t"
        "s_add_u32 s34, s34, 0\n\t"
        "buffer_load_dwordx4 a[0:3], v97, s[20:23], s34 offen\n\t"
        "s_add_u32 s34, s34, 0x8000\n\t"
        "buffer_load_dwordx4 a[4:7], v97, s[20:23], s34 offen\n\t"
        "s_add_u32 s34, s34, 0x8000\n\t"
        "buffer_load_dwordx4 a[8:11], v97, s[20:23], s34 offen\n\t"
        "s_add_u32 s34, s34, 0x8000\n\t"
        "buffer_load_dwordx4 a[12:15], v97, s[20:23], s34 offen\n\t"
        "s_add_u32 s34, s34, 0x8000\n\t"
        "buffer_load_dwordx4 a[16:19], v97, s[20:23], s34 offen\n\t"
        "s_add_u32 s34, s34, 0x8000\n\t"
        "buffer_load_dwordx4 a[20:23], v97, s[20:23], s34 offen\n\t"
        "s_add_u32 s34, s34, 0x8000\n\t"
        "buffer_load_dwordx4 a[24:27], v97, s[20:23], s34 offen\n\t"
        "s_add_u32 s34, s34, 0x8000\n\t"
        "buffer_load_dwordx4 a[28:31], v97, s[20:23], s34 offen\n\t"
        "s_waitcnt lgkmcnt(0)\n\t"
        "s_waitcnt vmcnt(16)\n\t"
        "ds_write_b128 v98, v[192:195] offset:0\n\t"
        "ds_write_b128 v99, v[196:199] offset:1024\n\t"
        "ds_write_b128 v98, v[200:203] offset:2048\n\t"
        "ds_write_b128 v99, v[204:207] offset:3072\n\t"
        "ds_write_b128 v98, v[208:211] offset:4096\n\t"
        "ds_write_b128 v99, v[212:215] offset:5120\n\t"
        "ds_write_b128 v98, v[216:219] offset:6144\n\t"
        "ds_write_b128 v99, v[220:223] offset:7168\n\t"
        "ds_read_b64_tr_b16 v[192:193], v100 offset:0\n\t"
        "ds_read_b64_tr_b16 v[194:195], v100 offset:256\n\t"
        "ds_read_b64_tr_b16 v[196:197], v100 offset:4096\n\t"
        "ds_read_b64_tr_b16 v[198:199], v100 offset:4352\n\t"
        "s_waitcnt lgkmcnt(8)\n\t"
        "ds_read_b64_tr_b16 v[200:201], v101 offset:0\n\t"
        "ds_read_b64_tr_b16 v[202:203], v101 offset:256\n\t"
        "ds_read_b64_tr_b16 v[204:205], v101 offset:4096\n\t"
        "ds_read_b64_tr_b16 v[206:207], v101 offset:4352\n\t"
        "s_waitcnt lgkmcnt(8)\n\t"
        "ds_read_b64_tr_b16 v[208:209], v102 offset:0\n\t"
        "ds_read_b64_tr_b16 v[210:211], v102 offset:256\n\t"
        "ds_read_b64_tr_b16 v[212:213], v102 offset:4096\n\t"
        "ds_read_b64_tr_b16 v[214:215], v102 offset:4352\n\t"
        "s_waitcnt lgkmcnt(8)\n\t"
        "ds_read_b64_tr_b16 v[216:217], v103 offset:0\n\t"
        "ds_read_b64_tr_b16 v[218:219], v103 offset:256\n\t"
        "ds_read_b64_tr_b16 v[220:221], v103 offset:4096\n\t"
        "ds_read_b64_tr_b16 v[222:223], v103 offset:4352\n\t"
        "s_lshl_b32 s34, s43, 21\n\t"
        "s_add_u32 s34, s34, 262144\n\t"
        "buffer_load_dwordx4 a[32:35], v97, s[20:23], s34 offen\n\t"
        "s_add_u32 s34, s34, 0x8000\n\t"
        "buffer_load_dwordx4 a[36:39], v97, s[20:23], s34 offen\n\t"
        "s_add_u32 s34, s34, 0x8000\n\t"
        "buffer_load_dwordx4 a[40:43], v97, s[20:23], s34 offen\n\t"
        "s_add_u32 s34, s34, 0x8000\n\t"
        "buffer_load_dwordx4 a[44:47], v97, s[20:23], s34 offen\n\t"
        "s_add_u32 s34, s34, 0x8000\n\t"
        "buffer_load_dwordx4 a[48:51], v97, s[20:23], s34 offen\n\t"
        "s_add_u32 s34, s34, 0x8000\n\t"
        "buffer_load_dwordx4 a[52:55], v97, s[20:23], s34 offen\n\t"
        "s_add_u32 s34, s34, 0x8000\n\t"
        "buffer_load_dwordx4 a[56:59], v97, s[20:23], s34 offen\n\t"
        "s_add_u32 s34, s34, 0x8000\n\t"
        "buffer_load_dwordx4 a[60:63], v97, s[20:23], s34 offen\n\t"
        "s_waitcnt lgkmcnt(0)\n\t"
        "s_waitcnt vmcnt(16)\n\t"
        "ds_write_b128 v98, v[224:227] offset:0\n\t"
        "ds_write_b128 v99, v[228:231] offset:1024\n\t"
        "ds_write_b128 v98, v[232:235] offset:2048\n\t"
        "ds_write_b128 v99, v[236:239] offset:3072\n\t"
        "ds_write_b128 v98, v[240:243] offset:4096\n\t"
        "ds_write_b128 v99, v[244:247] offset:5120\n\t"
        "ds_write_b128 v98, v[248:251] offset:6144\n\t"
        "ds_write_b128 v99, v[252:255] offset:7168\n\t"
        "ds_read_b64_tr_b16 v[224:225], v100 offset:0\n\t"
        "ds_read_b64_tr_b16 v[226:227], v100 offset:256\n\t"
        "ds_read_b64_tr_b16 v[228:229], v100 offset:4096\n\t"
        "ds_read_b64_tr_b16 v[230:231], v100 offset:4352\n\t"
        "s_waitcnt lgkmcnt(8)\n\t"
        "ds_read_b64_tr_b16 v[232:233], v101 offset:0\n\t"
        "ds_read_b64_tr_b16 v[234:235], v101 offset:256\n\t"
        "ds_read_b64_tr_b16 v[236:237], v101 offset:4096\n\t"
        "ds_read_b64_tr_b16 v[238:239], v101 offset:4352\n\t"
        "s_waitcnt lgkmcnt(8)\n\t"
        "ds_read_b64_tr_b16 v[240:241], v102 offset:0\n\t"
        "ds_read_b64_tr_b16 v[242:243], v102 offset:256\n\t"
        "ds_read_b64_tr_b16 v[244:245], v102 offset:4096\n\t"
        "ds_read_b64_tr_b16 v[246:247], v102 offset:4352\n\t"
        "s_waitcnt lgkmcnt(8)\n\t"
        "ds_read_b64_tr_b16 v[248:249], v103 offset:0\n\t"
        "ds_read_b64_tr_b16 v[250:251], v103 offset:256\n\t"
        "ds_read_b64_tr_b16 v[252:253], v103 offset:4096\n\t"
        "ds_read_b64_tr_b16 v[254:255], v103 offset:4352\n\t"
        "s_lshl_b32 s34, s43, 21\n\t"
        "s_add_u32 s34, s34, 524288\n\t"
        "buffer_load_dwordx4 a[64:67], v97, s[20:23], s34 offen\n\t"
        "s_add_u32 s34, s34, 0x8000\n\t"
        "buffer_load_dwordx4 a[68:71], v97, s[20:23], s34 offen\n\t"
        "s_add_u32 s34, s34, 0x8000\n\t"
        "buffer_load_dwordx4 a[72:75], v97, s[20:23], s34 offen\n\t"
        "s_add_u32 s34, s34, 0x8000\n\t"
        "buffer_load_dwordx4 a[76:79], v97, s[20:23], s34 offen\n\t"
        "s_add_u32 s34, s34, 0x8000\n\t"
        "buffer_load_dwordx4 a[80:83], v97, s[20:23], s34 offen\n\t"
        "s_add_u32 s34, s34, 0x8000\n\t"
        "buffer_load_dwordx4 a[84:87], v97, s[20:23], s34 offen\n\t"
        "s_add_u32 s34, s34, 0x8000\n\t"
        "buffer_load_dwordx4 a[88:91], v97, s[20:23], s34 offen\n\t"
        "s_add_u32 s34, s34, 0x8000\n\t"
        "buffer_load_dwordx4 a[92:95], v97, s[20:23], s34 offen\n\t"
        "s_waitcnt lgkmcnt(0)\n\t"
        "s_waitcnt vmcnt(16)\n\t"
        "ds_write_b128 v122, a[0:3] offset:0\n\t"
        "ds_write_b128 v122, a[4:7] offset:1056\n\t"
        "ds_write_b128 v122, a[8:11] offset:2176\n\t"
        "ds_write_b128 v122, a[12:15] offset:3232\n\t"
        "ds_write_b128 v122, a[16:19] offset:4352\n\t"
        "ds_write_b128 v122, a[20:23] offset:5408\n\t"
        "ds_write_b128 v122, a[24:27] offset:6528\n\t"
        "ds_write_b128 v122, a[28:31] offset:7584\n\t"
        "ds_read_b64_tr_b16 a[0:1], v123 offset:0\n\t"
        "ds_read_b64_tr_b16 a[2:3], v123 offset:128\n\t"
        "ds_read_b64_tr_b16 a[4:5], v123 offset:4352\n\t"
        "ds_read_b64_tr_b16 a[6:7], v123 offset:4480\n\t"
        "s_waitcnt lgkmcnt(8)\n\t"
        "ds_read_b64_tr_b16 a[8:9], v123 offset:32\n\t"
        "ds_read_b64_tr_b16 a[10:11], v123 offset:160\n\t"
        "ds_read_b64_tr_b16 a[12:13], v123 offset:4384\n\t"
        "ds_read_b64_tr_b16 a[14:15], v123 offset:4512\n\t"
        "s_waitcnt lgkmcnt(8)\n\t"
        "ds_read_b64_tr_b16 a[16:17], v123 offset:512\n\t"
        "ds_read_b64_tr_b16 a[18:19], v123 offset:640\n\t"
        "ds_read_b64_tr_b16 a[20:21], v123 offset:4864\n\t"
        "ds_read_b64_tr_b16 a[22:23], v123 offset:4992\n\t"
        "s_waitcnt lgkmcnt(8)\n\t"
        "ds_read_b64_tr_b16 a[24:25], v123 offset:544\n\t"
        "ds_read_b64_tr_b16 a[26:27], v123 offset:672\n\t"
        "ds_read_b64_tr_b16 a[28:29], v123 offset:4896\n\t"
        "ds_read_b64_tr_b16 a[30:31], v123 offset:5024\n\t"
        "s_waitcnt lgkmcnt(8)\n\t"
        "s_lshl_b32 s34, s43, 21\n\t"
        "s_add_u32 s34, s34, 786432\n\t"
        "buffer_load_dwordx4 a[96:99], v97, s[20:23], s34 offen\n\t"
        "s_add_u32 s34, s34, 0x8000\n\t"
        "buffer_load_dwordx4 a[100:103], v97, s[20:23], s34 offen\n\t"
        "s_add_u32 s34, s34, 0x8000\n\t"
        "buffer_load_dwordx4 a[104:107], v97, s[20:23], s34 offen\n\t"
        "s_add_u32 s34, s34, 0x8000\n\t"
        "buffer_load_dwordx4 a[108:111], v97, s[20:23], s34 offen\n\t"
        "s_add_u32 s34, s34, 0x8000\n\t"
        "buffer_load_dwordx4 a[112:115], v97, s[20:23], s34 offen\n\t"
        "s_add_u32 s34, s34, 0x8000\n\t"
        "buffer_load_dwordx4 a[116:119], v97, s[20:23], s34 offen\n\t"
        "s_add_u32 s34, s34, 0x8000\n\t"
        "buffer_load_dwordx4 a[120:123], v97, s[20:23], s34 offen\n\t"
        "s_add_u32 s34, s34, 0x8000\n\t"
        "buffer_load_dwordx4 a[124:127], v97, s[20:23], s34 offen\n\t"
        "s_waitcnt lgkmcnt(0)\n\t"
        "s_waitcnt vmcnt(16)\n\t"
        "ds_write_b128 v122, a[32:35] offset:0\n\t"
        "ds_write_b128 v122, a[36:39] offset:1056\n\t"
        "ds_write_b128 v122, a[40:43] offset:2176\n\t"
        "ds_write_b128 v122, a[44:47] offset:3232\n\t"
        "ds_write_b128 v122, a[48:51] offset:4352\n\t"
        "ds_write_b128 v122, a[52:55] offset:5408\n\t"
        "ds_write_b128 v122, a[56:59] offset:6528\n\t"
        "ds_write_b128 v122, a[60:63] offset:7584\n\t"
        "ds_read_b64_tr_b16 a[32:33], v123 offset:0\n\t"
        "ds_read_b64_tr_b16 a[34:35], v123 offset:128\n\t"
        "ds_read_b64_tr_b16 a[36:37], v123 offset:4352\n\t"
        "ds_read_b64_tr_b16 a[38:39], v123 offset:4480\n\t"
        "s_waitcnt lgkmcnt(8)\n\t"
        "ds_read_b64_tr_b16 a[40:41], v123 offset:32\n\t"
        "ds_read_b64_tr_b16 a[42:43], v123 offset:160\n\t"
        "ds_read_b64_tr_b16 a[44:45], v123 offset:4384\n\t"
        "ds_read_b64_tr_b16 a[46:47], v123 offset:4512\n\t"
        "s_waitcnt lgkmcnt(8)\n\t"
        "ds_read_b64_tr_b16 a[48:49], v123 offset:512\n\t"
        "ds_read_b64_tr_b16 a[50:51], v123 offset:640\n\t"
        "ds_read_b64_tr_b16 a[52:53], v123 offset:4864\n\t"
        "ds_read_b64_tr_b16 a[54:55], v123 offset:4992\n\t"
        "s_waitcnt lgkmcnt(8)\n\t"
        "ds_read_b64_tr_b16 a[56:57], v123 offset:544\n\t"
        "ds_read_b64_tr_b16 a[58:59], v123 offset:672\n\t"
        "ds_read_b64_tr_b16 a[60:61], v123 offset:4896\n\t"
        "ds_read_b64_tr_b16 a[62:63], v123 offset:5024\n\t"
        "s_waitcnt lgkmcnt(8)\n\t"
        "s_lshl_b32 s34, s43, 21\n\t"
        "s_add_u32 s34, s34, 1048576\n\t"
        "buffer_load_dwordx4 a[128:131], v97, s[20:23], s34 offen\n\t"
        "s_add_u32 s34, s34, 0x8000\n\t"
        "buffer_load_dwordx4 a[132:135], v97, s[20:23], s34 offen\n\t"
        "s_add_u32 s34, s34, 0x8000\n\t"
        "buffer_load_dwordx4 a[136:139], v97, s[20:23], s34 offen\n\t"
        "s_add_u32 s34, s34, 0x8000\n\t"
        "buffer_load_dwordx4 a[140:143], v97, s[20:23], s34 offen\n\t"
        "s_add_u32 s34, s34, 0x8000\n\t"
        "buffer_load_dwordx4 a[144:147], v97, s[20:23], s34 offen\n\t"
        "s_add_u32 s34, s34, 0x8000\n\t"
        "buffer_load_dwordx4 a[148:151], v97, s[20:23], s34 offen\n\t"
        "s_add_u32 s34, s34, 0x8000\n\t"
        "buffer_load_dwordx4 a[152:155], v97, s[20:23], s34 offen\n\t"
        "s_add_u32 s34, s34, 0x8000\n\t"
        "buffer_load_dwordx4 a[156:159], v97, s[20:23], s34 offen\n\t"
        "s_waitcnt lgkmcnt(0)\n\t"
        "s_waitcnt vmcnt(16)\n\t"
        "ds_write_b128 v122, a[64:67] offset:0\n\t"
        "ds_write_b128 v122, a[68:71] offset:1056\n\t"
        "ds_write_b128 v122, a[72:75] offset:2176\n\t"
        "ds_write_b128 v122, a[76:79] offset:3232\n\t"
        "ds_write_b128 v122, a[80:83] offset:4352\n\t"
        "ds_write_b128 v122, a[84:87] offset:5408\n\t"
        "ds_write_b128 v122, a[88:91] offset:6528\n\t"
        "ds_write_b128 v122, a[92:95] offset:7584\n\t"
        "ds_read_b64_tr_b16 a[64:65], v123 offset:0\n\t"
        "ds_read_b64_tr_b16 a[66:67], v123 offset:128\n\t"
        "ds_read_b64_tr_b16 a[68:69], v123 offset:4352\n\t"
        "ds_read_b64_tr_b16 a[70:71], v123 offset:4480\n\t"
        "s_waitcnt lgkmcnt(8)\n\t"
        "ds_read_b64_tr_b16 a[72:73], v123 offset:32\n\t"
        "ds_read_b64_tr_b16 a[74:75], v123 offset:160\n\t"
        "ds_read_b64_tr_b16 a[76:77], v123 offset:4384\n\t"
        "ds_read_b64_tr_b16 a[78:79], v123 offset:4512\n\t"
        "s_waitcnt lgkmcnt(8)\n\t"
        "ds_read_b64_tr_b16 a[80:81], v123 offset:512\n\t"
        "ds_read_b64_tr_b16 a[82:83], v123 offset:640\n\t"
        "ds_read_b64_tr_b16 a[84:85], v123 offset:4864\n\t"
        "ds_read_b64_tr_b16 a[86:87], v123 offset:4992\n\t"
        "s_waitcnt lgkmcnt(8)\n\t"
        "ds_read_b64_tr_b16 a[88:89], v123 offset:544\n\t"
        "ds_read_b64_tr_b16 a[90:91], v123 offset:672\n\t"
        "ds_read_b64_tr_b16 a[92:93], v123 offset:4896\n\t"
        "ds_read_b64_tr_b16 a[94:95], v123 offset:5024\n\t"
        "s_waitcnt lgkmcnt(8)\n\t"
        "s_lshl_b32 s34, s43, 21\n\t"
        "s_add_u32 s34, s34, 1310720\n\t"
        "buffer_load_dwordx4 a[160:163], v97, s[20:23], s34 offen\n\t"
        "s_add_u32 s34, s34, 0x8000\n\t"
        "buffer_load_dwordx4 a[164:167], v97, s[20:23], s34 offen\n\t"
        "s_add_u32 s34, s34, 0x8000\n\t"
        "buffer_load_dwordx4 a[168:171], v97, s[20:23], s34 offen\n\t"
        "s_add_u32 s34, s34, 0x8000\n\t"
        "buffer_load_dwordx4 a[172:175], v97, s[20:23], s34 offen\n\t"
        "s_add_u32 s34, s34, 0x8000\n\t"
        "buffer_load_dwordx4 a[176:179], v97, s[20:23], s34 offen\n\t"
        "s_add_u32 s34, s34, 0x8000\n\t"
        "buffer_load_dwordx4 a[180:183], v97, s[20:23], s34 offen\n\t"
        "s_add_u32 s34, s34, 0x8000\n\t"
        "buffer_load_dwordx4 a[184:187], v97, s[20:23], s34 offen\n\t"
        "s_add_u32 s34, s34, 0x8000\n\t"
        "buffer_load_dwordx4 a[188:191], v97, s[20:23], s34 offen\n\t"
        "s_waitcnt lgkmcnt(0)\n\t"
        "s_waitcnt vmcnt(16)\n\t"
        "ds_write_b128 v122, a[96:99] offset:0\n\t"
        "ds_write_b128 v122, a[100:103] offset:1056\n\t"
        "ds_write_b128 v122, a[104:107] offset:2176\n\t"
        "ds_write_b128 v122, a[108:111] offset:3232\n\t"
        "ds_write_b128 v122, a[112:115] offset:4352\n\t"
        "ds_write_b128 v122, a[116:119] offset:5408\n\t"
        "ds_write_b128 v122, a[120:123] offset:6528\n\t"
        "ds_write_b128 v122, a[124:127] offset:7584\n\t"
        "ds_read_b64_tr_b16 a[96:97], v123 offset:0\n\t"
        "ds_read_b64_tr_b16 a[98:99], v123 offset:128\n\t"
        "ds_read_b64_tr_b16 a[100:101], v123 offset:4352\n\t"
        "ds_read_b64_tr_b16 a[102:103], v123 offset:4480\n\t"
        "s_waitcnt lgkmcnt(8)\n\t"
        "ds_read_b64_tr_b16 a[104:105], v123 offset:32\n\t"
        "ds_read_b64_tr_b16 a[106:107], v123 offset:160\n\t"
        "ds_read_b64_tr_b16 a[108:109], v123 offset:4384\n\t"
        "ds_read_b64_tr_b16 a[110:111], v123 offset:4512\n\t"
        "s_waitcnt lgkmcnt(8)\n\t"
        "ds_read_b64_tr_b16 a[112:113], v123 offset:512\n\t"
        "ds_read_b64_tr_b16 a[114:115], v123 offset:640\n\t"
        "ds_read_b64_tr_b16 a[116:117], v123 offset:4864\n\t"
        "ds_read_b64_tr_b16 a[118:119], v123 offset:4992\n\t"
        "s_waitcnt lgkmcnt(8)\n\t"
        "ds_read_b64_tr_b16 a[120:121], v123 offset:544\n\t"
        "ds_read_b64_tr_b16 a[122:123], v123 offset:672\n\t"
        "ds_read_b64_tr_b16 a[124:125], v123 offset:4896\n\t"
        "ds_read_b64_tr_b16 a[126:127], v123 offset:5024\n\t"
        "s_waitcnt lgkmcnt(8)\n\t"
        "s_lshl_b32 s34, s43, 21\n\t"
        "s_add_u32 s34, s34, 1572864\n\t"
        "buffer_load_dwordx4 a[192:195], v97, s[20:23], s34 offen\n\t"
        "s_add_u32 s34, s34, 0x8000\n\t"
        "buffer_load_dwordx4 a[196:199], v97, s[20:23], s34 offen\n\t"
        "s_add_u32 s34, s34, 0x8000\n\t"
        "buffer_load_dwordx4 a[200:203], v97, s[20:23], s34 offen\n\t"
        "s_add_u32 s34, s34, 0x8000\n\t"
        "buffer_load_dwordx4 a[204:207], v97, s[20:23], s34 offen\n\t"
        "s_add_u32 s34, s34, 0x8000\n\t"
        "buffer_load_dwordx4 a[208:211], v97, s[20:23], s34 offen\n\t"
        "s_add_u32 s34, s34, 0x8000\n\t"
        "buffer_load_dwordx4 a[212:215], v97, s[20:23], s34 offen\n\t"
        "s_add_u32 s34, s34, 0x8000\n\t"
        "buffer_load_dwordx4 a[216:219], v97, s[20:23], s34 offen\n\t"
        "s_add_u32 s34, s34, 0x8000\n\t"
        "buffer_load_dwordx4 a[220:223], v97, s[20:23], s34 offen\n\t"
        "s_waitcnt lgkmcnt(0)\n\t"
        "s_waitcnt vmcnt(16)\n\t"
        "ds_write_b128 v122, a[128:131] offset:0\n\t"
        "ds_write_b128 v122, a[132:135] offset:1056\n\t"
        "ds_write_b128 v122, a[136:139] offset:2176\n\t"
        "ds_write_b128 v122, a[140:143] offset:3232\n\t"
        "ds_write_b128 v122, a[144:147] offset:4352\n\t"
        "ds_write_b128 v122, a[148:151] offset:5408\n\t"
        "ds_write_b128 v122, a[152:155] offset:6528\n\t"
        "ds_write_b128 v122, a[156:159] offset:7584\n\t"
        "ds_read_b64_tr_b16 a[128:129], v123 offset:0\n\t"
        "ds_read_b64_tr_b16 a[130:131], v123 offset:128\n\t"
        "ds_read_b64_tr_b16 a[132:133], v123 offset:4352\n\t"
        "ds_read_b64_tr_b16 a[134:135], v123 offset:4480\n\t"
        "s_waitcnt lgkmcnt(8)\n\t"
        "ds_read_b64_tr_b16 a[136:137], v123 offset:32\n\t"
        "ds_read_b64_tr_b16 a[138:139], v123 offset:160\n\t"
        "ds_read_b64_tr_b16 a[140:141], v123 offset:4384\n\t"
        "ds_read_b64_tr_b16 a[142:143], v123 offset:4512\n\t"
        "s_waitcnt lgkmcnt(8)\n\t"
        "ds_read_b64_tr_b16 a[144:145], v123 offset:512\n\t"
        "ds_read_b64_tr_b16 a[146:147], v123 offset:640\n\t"
        "ds_read_b64_tr_b16 a[148:149], v123 offset:4864\n\t"
        "ds_read_b64_tr_b16 a[150:151], v123 offset:4992\n\t"
        "s_waitcnt lgkmcnt(8)\n\t"
        "ds_read_b64_tr_b16 a[152:153], v123 offset:544\n\t"
        "ds_read_b64_tr_b16 a[154:155], v123 offset:672\n\t"
        "ds_read_b64_tr_b16 a[156:157], v123 offset:4896\n\t"
        "ds_read_b64_tr_b16 a[158:159], v123 offset:5024\n\t"
        "s_waitcnt lgkmcnt(8)\n\t"
        "s_lshl_b32 s34, s43, 21\n\t"
        "s_add_u32 s34, s34, 1835008\n\t"
        "buffer_load_dwordx4 a[224:227], v97, s[20:23], s34 offen\n\t"
        "s_add_u32 s34, s34, 0x8000\n\t"
        "buffer_load_dwordx4 a[228:231], v97, s[20:23], s34 offen\n\t"
        "s_add_u32 s34, s34, 0x8000\n\t"
        "buffer_load_dwordx4 a[232:235], v97, s[20:23], s34 offen\n\t"
        "s_add_u32 s34, s34, 0x8000\n\t"
        "buffer_load_dwordx4 a[236:239], v97, s[20:23], s34 offen\n\t"
        "s_add_u32 s34, s34, 0x8000\n\t"
        "buffer_load_dwordx4 a[240:243], v97, s[20:23], s34 offen\n\t"
        "s_add_u32 s34, s34, 0x8000\n\t"
        "buffer_load_dwordx4 a[244:247], v97, s[20:23], s34 offen\n\t"
        "s_add_u32 s34, s34, 0x8000\n\t"
        "buffer_load_dwordx4 a[248:251], v97, s[20:23], s34 offen\n\t"
        "s_add_u32 s34, s34, 0x8000\n\t"
        "buffer_load_dwordx4 a[252:255], v97, s[20:23], s34 offen\n\t"
        "s_waitcnt lgkmcnt(0)\n\t"
        "s_waitcnt vmcnt(16)\n\t"
        "ds_write_b128 v122, a[160:163] offset:0\n\t"
        "ds_write_b128 v122, a[164:167] offset:1056\n\t"
        "ds_write_b128 v122, a[168:171] offset:2176\n\t"
        "ds_write_b128 v122, a[172:175] offset:3232\n\t"
        "ds_write_b128 v122, a[176:179] offset:4352\n\t"
        "ds_write_b128 v122, a[180:183] offset:5408\n\t"
        "ds_write_b128 v122, a[184:187] offset:6528\n\t"
        "ds_write_b128 v122, a[188:191] offset:7584\n\t"
        "ds_read_b64_tr_b16 a[160:161], v123 offset:0\n\t"
        "ds_read_b64_tr_b16 a[162:163], v123 offset:128\n\t"
        "ds_read_b64_tr_b16 a[164:165], v123 offset:4352\n\t"
        "ds_read_b64_tr_b16 a[166:167], v123 offset:4480\n\t"
        "s_waitcnt lgkmcnt(8)\n\t"
        "ds_read_b64_tr_b16 a[168:169], v123 offset:32\n\t"
        "ds_read_b64_tr_b16 a[170:171], v123 offset:160\n\t"
        "ds_read_b64_tr_b16 a[172:173], v123 offset:4384\n\t"
        "ds_read_b64_tr_b16 a[174:175], v123 offset:4512\n\t"
        "s_waitcnt lgkmcnt(8)\n\t"
        "ds_read_b64_tr_b16 a[176:177], v123 offset:512\n\t"
        "ds_read_b64_tr_b16 a[178:179], v123 offset:640\n\t"
        "ds_read_b64_tr_b16 a[180:181], v123 offset:4864\n\t"
        "ds_read_b64_tr_b16 a[182:183], v123 offset:4992\n\t"
        "s_waitcnt lgkmcnt(8)\n\t"
        "ds_read_b64_tr_b16 a[184:185], v123 offset:544\n\t"
        "ds_read_b64_tr_b16 a[186:187], v123 offset:672\n\t"
        "ds_read_b64_tr_b16 a[188:189], v123 offset:4896\n\t"
        "ds_read_b64_tr_b16 a[190:191], v123 offset:5024\n\t"
        "s_waitcnt lgkmcnt(8)\n\t"
        "s_waitcnt lgkmcnt(0)\n\t"
        "s_waitcnt vmcnt(8)\n\t"
        "ds_write_b128 v122, a[192:195] offset:0\n\t"
        "ds_write_b128 v122, a[196:199] offset:1056\n\t"
        "ds_write_b128 v122, a[200:203] offset:2176\n\t"
        "ds_write_b128 v122, a[204:207] offset:3232\n\t"
        "ds_write_b128 v122, a[208:211] offset:4352\n\t"
        "ds_write_b128 v122, a[212:215] offset:5408\n\t"
        "ds_write_b128 v122, a[216:219] offset:6528\n\t"
        "ds_write_b128 v122, a[220:223] offset:7584\n\t"
        "ds_read_b64_tr_b16 a[192:193], v123 offset:0\n\t"
        "ds_read_b64_tr_b16 a[194:195], v123 offset:128\n\t"
        "ds_read_b64_tr_b16 a[196:197], v123 offset:4352\n\t"
        "ds_read_b64_tr_b16 a[198:199], v123 offset:4480\n\t"
        "s_waitcnt lgkmcnt(8)\n\t"
        "ds_read_b64_tr_b16 a[200:201], v123 offset:32\n\t"
        "ds_read_b64_tr_b16 a[202:203], v123 offset:160\n\t"
        "ds_read_b64_tr_b16 a[204:205], v123 offset:4384\n\t"
        "ds_read_b64_tr_b16 a[206:207], v123 offset:4512\n\t"
        "s_waitcnt lgkmcnt(8)\n\t"
        "ds_read_b64_tr_b16 a[208:209], v123 offset:512\n\t"
        "ds_read_b64_tr_b16 a[210:211], v123 offset:640\n\t"
        "ds_read_b64_tr_b16 a[212:213], v123 offset:4864\n\t"
        "ds_read_b64_tr_b16 a[214:215], v123 offset:4992\n\t"
        "s_waitcnt lgkmcnt(8)\n\t"
        "ds_read_b64_tr_b16 a[216:217], v123 offset:544\n\t"
        "ds_read_b64_tr_b16 a[218:219], v123 offset:672\n\t"
        "ds_read_b64_tr_b16 a[220:221], v123 offset:4896\n\t"
        "ds_read_b64_tr_b16 a[222:223], v123 offset:5024\n\t"
        "s_waitcnt lgkmcnt(8)\n\t"
        "s_waitcnt lgkmcnt(0)\n\t"
        "s_waitcnt vmcnt(0)\n\t"
        "ds_write_b128 v122, a[224:227] offset:0\n\t"
        "ds_write_b128 v122, a[228:231] offset:1056\n\t"
        "ds_write_b128 v122, a[232:235] offset:2176\n\t"
        "ds_write_b128 v122, a[236:239] offset:3232\n\t"
        "ds_write_b128 v122, a[240:243] offset:4352\n\t"
        "ds_write_b128 v122, a[244:247] offset:5408\n\t"
        "ds_write_b128 v122, a[248:251] offset:6528\n\t"
        "ds_write_b128 v122, a[252:255] offset:7584\n\t"
        "ds_read_b64_tr_b16 a[224:225], v123 offset:0\n\t"
        "ds_read_b64_tr_b16 a[226:227], v123 offset:128\n\t"
        "ds_read_b64_tr_b16 a[228:229], v123 offset:4352\n\t"
        "ds_read_b64_tr_b16 a[230:231], v123 offset:4480\n\t"
        "s_waitcnt lgkmcnt(8)\n\t"
        "ds_read_b64_tr_b16 a[232:233], v123 offset:32\n\t"
        "ds_read_b64_tr_b16 a[234:235], v123 offset:160\n\t"
        "ds_read_b64_tr_b16 a[236:237], v123 offset:4384\n\t"
        "ds_read_b64_tr_b16 a[238:239], v123 offset:4512\n\t"
        "s_waitcnt lgkmcnt(8)\n\t"
        "ds_read_b64_tr_b16 a[240:241], v123 offset:512\n\t"
        "ds_read_b64_tr_b16 a[242:243], v123 offset:640\n\t"
        "ds_read_b64_tr_b16 a[244:245], v123 offset:4864\n\t"
        "ds_read_b64_tr_b16 a[246:247], v123 offset:4992\n\t"
        "s_waitcnt lgkmcnt(8)\n\t"
        "ds_read_b64_tr_b16 a[248:249], v123 offset:544\n\t"
        "ds_read_b64_tr_b16 a[250:251], v123 offset:672\n\t"
        "ds_read_b64_tr_b16 a[252:253], v123 offset:4896\n\t"
        "ds_read_b64_tr_b16 a[254:255], v123 offset:5024\n\t"
        "s_waitcnt lgkmcnt(8)\n\t"
        "s_mov_b32 s35, 33792\n\t"
        "buffer_load_dword v0, off, s[12:15], s35 sc1\n\t"
        "s_waitcnt lgkmcnt(0)\n\t"
        "s_waitcnt vmcnt(0)\n\t"
        "s_mov_b32 s42, 0\n\t"
        "LB_cal%=:\n\t"
        "v_readfirstlane_b32 s34, v0\n\t"
        "s_nop 0\n\t"
        "s_cmp_lg_u32 s34, 0\n\t"
        "s_cbranch_scc1 LB_calok%=\n\t"
        "s_sleep 4\n\t"
        "s_mov_b32 s35, 33792\n\t"
        "buffer_load_dword v0, off, s[12:15], s35 sc1\n\t"
        "s_waitcnt vmcnt(0)\n\t"
        "s_add_u32 s42, s42, 1\n\t"
        "s_cmp_lt_u32 s42, 0x100000\n\t"
        "s_cbranch_scc1 LB_cal%=\n\t"
        "s_movk_i32 s34, 8\n\t"
        "s_branch LB_die%=\n\t"
        "LB_calok%=:\n\t"
        "v_readfirstlane_b32 s34, v0\n\t"
        "s_nop 0\n\t"
        "s_and_b32 s34, s34, 8192\n\t"
        "s_add_u32 s8, s8, s34\n\t"
        "s_addc_u32 s9, s9, 0\n\t"
        "v_min_u32_e32 v109, -2, v109\n\t"
        "buffer_store_dword v109, v116, s[8:11], 0 offen sc1\n\t"
        "s_memtime s[74:75]\n\t"
        "s_memrealtime s[32:33]\n\t"
        "s_waitcnt lgkmcnt(0)\n\t"
        "v_mov_b32_e32 v110, s32\n\t"
        "s_sub_u32 s34, s74, s70\n\t"
        "s_sub_u32 s35, s32, s72\n\t"
        "v_cvt_f32_u32_e32 v2, s34\n\t"
        "v_cvt_f32_u32_e32 v3, s35\n\t"
        "v_rcp_f32_e32 v2, v2\n\t"
        "s_nop 0\n\t"
        "v_mul_f32_e32 v2, v2, v3\n\t"
        "v_mul_f32_e32 v3, 0x45354000, v2\n\t"
        "v_cvt_u32_f32_e32 v3, v3\n\t"
        "s_nop 0\n\t"
        "v_readfirstlane_b32 s34, v3\n\t"
        "s_add_u32 s35, s29, 40\n\t"
        "s_min_u32 s34, s34, s35\n\t"
        "s_sub_u32 s35, s29, 10\n\t"
        "s_max_u32 s29, s34, s35\n\t"
        "v_mul_f32_e32 v3, 0x44d70000, v2\n\t"
        "v_cvt_u32_f32_e32 v3, v3\n\t"
        "s_nop 0\n\t"
        "v_readfirstlane_b32 s34, v3\n\t"
        "s_add_u32 s35, s30, 40\n\t"
        "s_min_u32 s34, s34, s35\n\t"
        "s_sub_u32 s35, s30, 10\n\t"
        "s_max_u32 s30, s34, s35\n\t"
        "s_lshl_b32 s34, s29, 8\n\t"
        "s_lshl_b32 s35, s30, 20\n\t"
        "s_or_b32 s54, s34, s35\n\t"
        "s_add_u32 s34, s54, 8392192\n\t"
        "v_mov_b32_e32 v111, s34\n\t"
        "s_barrier\n\t"
        "s_cmp_lg_u32 s43, 0\n\t"
        "s_cbranch_scc1 LB_st%=\n\t"
        "v_mov_b32_e32 v2, 1\n\t"
        "s_mov_b32 s35, 34304\n\t"
        "s_mov_b64 exec, 1\n\t"
        "buffer_atomic_add v2, off, s[12:15], s35 sc0\n\t"
        "s_mov_b64 exec, -1\n\t"
        "s_waitcnt vmcnt(0)\n\t"
        "v_readfirstlane_b32 s34, v2\n\t"
        "s_nop 0\n\t"
        "s_cmp_lg_u32 s34, 95\n\t"
        "s_cbranch_scc1 LB_st%=\n\t"
        "s_memrealtime s[32:33]\n\t"
        "v_mbcnt_lo_u32_b32 v3, -1, 0\n\t"
        "v_lshlrev_b32_e32 v3, 7, v3\n\t"
        "s_waitcnt lgkmcnt(0)\n\t"
        "s_add_u32 s34, s32, 50\n\t"
        "s_or_b32 s34, s34, 1\n\t"
        "v_mov_b32_e32 v2, s34\n\t"
        "s_mov_b32 s35, 36864\n\t"
        "s_mov_b64 exec, 0xffff\n\t"
        "buffer_store_dword v2, v3, s[12:15], s35 offen sc1\n\t"
        "s_mov_b64 exec, -1\n\t"
        "LB_st%=:\n\t"
        "s_mov_b32 s24, 0\n\t"
        "s_mov_b32 s26, 0\n\t"
        "s_mov_b32 s27, 0x80000\n\t"
        "s_mov_b32 s28, 0x180000\n\t"
        "s_mov_b32 s31, 0\n\t"
        "s_mov_b32 s47, 0\n\t"
        "s_mov_b32 s38, 0xff00ff00\n\t"
        "s_mov_b32 s39, 0xff00ff00\n\t"
        "s_mov_b32 s40, 0\n\t"
        "s_mov_b32 s41, -1\n\t"
        "v_mov_b64_e32 v[0:1], 0\n\t"
        "v_mov_b64_e32 v[2:3], 0\n\t"
        "v_mov_b64_e32 v[4:5], 0\n\t"
        "v_mov_b64_e32 v[6:7], 0\n\t"
        "v_mov_b64_e32 v[8:9], 0\n\t"
        "v_mov_b64_e32 v[10:11], 0\n\t"
        "v_mov_b64_e32 v[12:13], 0\n\t"
        "v_mov_b64_e32 v[14:15], 0\n\t"
        "s_mov_b32 s42, 0\n\t"
        "LB_gw%=:\n\t"
        "buffer_load_dword v16, off, s[12:15], s45 sc1\n\t"
        "s_waitcnt vmcnt(0)\n\t"
        "v_readfirstlane_b32 s25, v16\n\t"
        "s_nop 0\n\t"
        "s_cmp_lg_u32 s25, 0\n\t"
        "s_cbranch_scc1 LB_loop%=\n\t"
        "s_sleep 2\n\t"
        "s_add_u32 s42, s42, 1\n\t"
        "s_cmp_lt_u32 s42, 0x100000\n\t"
        "s_cbranch_scc1 LB_gw%=\n\t"
        "s_movk_i32 s34, 9\n\t"
        "s_branch LB_die%=\n\t"
        ".p2align 8\n\t"
        "LB_loop%=:\n\t"
        "s_mov_b32 s42, 0\n\t"
        "s_sub_u32 s35, s25, 8\n\t"
        "s_waitcnt lgkmcnt(0)\n\t"
        "s_memrealtime s[2:3]\n\t"
        "s_memrealtime s[4:5]\n\t"
        "s_memrealtime s[6:7]\n\t"
        "LB_wa%=:\n\t"
        "s_memrealtime s[32:33]\n\t"
        "s_waitcnt lgkmcnt(6)\n\t"
        "s_sub_u32 s34, s35, s2\n\t"
        "s_cmpk_lt_u32 s34, 0x2000\n\t"
        "s_cbranch_scc0 LB_wa%=x\n\t"
        "s_memrealtime s[2:3]\n\t"
        "s_waitcnt lgkmcnt(6)\n\t"
        "s_sub_u32 s34, s35, s4\n\t"
        "s_cmpk_lt_u32 s34, 0x2000\n\t"
        "s_cbranch_scc0 LB_wa%=x\n\t"
        "s_memrealtime s[4:5]\n\t"
        "s_waitcnt lgkmcnt(6)\n\t"
        "s_sub_u32 s34, s35, s6\n\t"
        "s_cmpk_lt_u32 s34, 0x2000\n\t"
        "s_cbranch_scc0 LB_wa%=x\n\t"
        "s_memrealtime s[6:7]\n\t"
        "s_waitcnt lgkmcnt(6)\n\t"
        "s_sub_u32 s34, s35, s32\n\t"
        "s_cmpk_lt_u32 s34, 0x2000\n\t"
        "s_cbranch_scc1 LB_wa%=\n\t"
        "s_nop 3\n\t"
        "LB_wa%=x:\n\t"
        "s_add_i32 s34, s34, 4\n\t"
        "s_max_i32 s34, s34, 0\n\t"
        "s_bitcmp1_b32 s34, 1\n\t"
        "s_cbranch_scc0 LB_wa%=y\n\t"
        "s_nop 15\n\t"
        "LB_wa%=y:\n\t"
        "s_bitcmp1_b32 s34, 0\n\t"
        "s_cbranch_scc0 LB_wa%=z\n\t"
        "s_nop 9\n\t"
        "LB_wa%=z:\n\t"
        "LB_rd%=:\n\t"
        "buffer_load_dwordx4 v[16:19], v112, s[8:11], s26 offen offset:0 sc1\n\t"
        "s_nop 9\n\t"
        "buffer_load_dwordx4 v[20:23], v112, s[8:11], s26 offen offset:1024 sc1\n\t"
        "s_nop 9\n\t"
        "buffer_load_dwordx4 v[24:27], v112, s[8:11], s26 offen offset:2048 sc1\n\t"
        "s_nop 9\n\t"
        "buffer_load_dwordx4 v[28:31], v112, s[8:11], s26 offen offset:3072 sc1\n\t"
        "LB_go%=:\n\t"
        "s_waitcnt vmcnt(3)\n\t"
        "v_smfmac_f32_16x16x64_bf16 v[0:3], v[16:19], v[128:135], v106\n\t"
        "v_max3_u32 v108, v16, v17, v18\n\t"
        "v_max_u32_e32 v108, v19, v108\n\t"
        "v_smfmac_f32_16x16x64_bf16 v[4:7], v[16:19], v[136:143], v106\n\t"
        "v_smfmac_f32_16x16x64_bf16 v[8:11], v[16:19], v[144:151], v106\n\t"
        "v_smfmac_f32_16x16x64_bf16 v[12:15], v[16:19], v[152:159], v106\n\t"
        "s_waitcnt vmcnt(2)\n\t"
        "v_smfmac_f32_16x16x64_bf16 v[0:3], v[20:23], v[160:167], v106\n\t"
        "v_max3_u32 v108, v20, v21, v108\n\t"
        "v_max3_u32 v108, v22, v23, v108\n\t"
        "v_smfmac_f32_16x16x64_bf16 v[4:7], v[20:23], v[168:175], v106\n\t"
        "v_smfmac_f32_16x16x64_bf16 v[8:11], v[20:23], v[176:183], v106\n\t"
        "v_smfmac_f32_16x16x64_bf16 v[12:15], v[20:23], v[184:191], v106\n\t"
        "s_waitcnt vmcnt(1)\n\t"
        "v_smfmac_f32_16x16x64_bf16 v[0:3], v[24:27], v[192:199], v106\n\t"
        "v_max3_u32 v108, v24, v25, v108\n\t"
        "v_max3_u32 v108, v26, v27, v108\n\t"
        "v_smfmac_f32_16x16x64_bf16 v[4:7], v[24:27], v[200:207], v106\n\t"
        "v_smfmac_f32_16x16x64_bf16 v[8:11], v[24:27], v[208:215], v106\n\t"
        "v_smfmac_f32_16x16x64_bf16 v[12:15], v[24:27], v[216:223], v106\n\t"
        "s_waitcnt vmcnt(0)\n\t"
        "v_smfmac_f32_16x16x64_bf16 v[0:3], v[28:31], v[224:231], v106\n\t"
        "v_max3_u32 v108, v28, v29, v108\n\t"
        "v_max3_u32 v108, v30, v31, v108\n\t"
        "v_smfmac_f32_16x16x64_bf16 v[4:7], v[28:31], v[232:239], v106\n\t"
        "v_cmp_eq_u32_e32 vcc, -1, v108\n\t"
        "v_smfmac_f32_16x16x64_bf16 v[8:11], v[28:31], v[240:247], v106\n\t"
        "v_smfmac_f32_16x16x64_bf16 v[12:15], v[28:31], v[248:255], v106\n\t"
        "s_cbranch_vccnz LB_rdfail%=\n\t"
        "s_nop 0\n\t"
        "v_add_f32_e32 v0, v0, v1\n\t"
        "v_add_f32_e32 v1, v2, v3\n\t"
        "ds_write_b64 v118, v[0:1] offset:0\n\t"
        "v_add_f32_e32 v4, v4, v5\n\t"
        "v_add_f32_e32 v5, v6, v7\n\t"
        "ds_write_b64 v118, v[4:5] offset:512\n\t"
        "v_add_f32_e32 v8, v8, v9\n\t"
        "v_add_f32_e32 v9, v10, v11\n\t"
        "ds_write_b64 v118, v[8:9] offset:1024\n\t"
        "v_add_f32_e32 v12, v12, v13\n\t"
        "v_add_f32_e32 v13, v14, v15\n\t"
        "ds_write_b64 v118, v[12:13] offset:1536\n\t"
        "buffer_store_dwordx2 v[110:111], v117, s[8:11], s26 offen sc1\n\t"
        "s_waitcnt lgkmcnt(0)\n\t"
        "s_barrier\n\t"
        "ds_read_b128 v[16:19], v119 offset:0\n\t"
        "ds_read_b128 v[20:23], v119 offset:2048\n\t"
        "ds_read_b128 v[24:27], v119 offset:4096\n\t"
        "ds_read_b128 v[28:31], v119 offset:6144\n\t"
        "buffer_load_dwordx4 v[64:67], v124, s[8:11], s28 offen sc1\n\t"
        "s_waitcnt lgkmcnt(2)\n\t"
        "v_pk_add_f32 v[16:17], v[16:17], v[20:21]\n\t"
        "v_pk_add_f32 v[18:19], v[18:19], v[22:23]\n\t"
        "s_waitcnt lgkmcnt(0)\n\t"
        "v_pk_add_f32 v[24:25], v[24:25], v[28:29]\n\t"
        "v_pk_add_f32 v[26:27], v[26:27], v[30:31]\n\t"
        "v_pk_add_f32 v[16:17], v[16:17], v[24:25]\n\t"
        "v_pk_add_f32 v[18:19], v[18:19], v[26:27]\n\t"
        "v_cvt_pk_bf16_f32 v20, v16, v17\n\t"
        "v_cvt_pk_bf16_f32 v21, v18, v19\n\t"
        "buffer_store_dwordx2 v[20:21], v114, s[8:11], s26 offen sc1\n\t"
        "s_cmp_eq_u32 s24, 63\n\t"
        "s_cbranch_scc1 LB_end%=\n\t"
        "s_memrealtime s[52:53]\n\t"
        "v_mov_b64_e32 v[24:25], -1\n\t"
        "v_mov_b64_e32 v[32:33], 0\n\t"
        "v_mov_b64_e32 v[34:35], 0\n\t"
        "v_mov_b64_e32 v[36:37], 0\n\t"
        "v_mov_b64_e32 v[38:39], 0\n\t"
        "v_mov_b64_e32 v[40:41], 0\n\t"
        "v_mov_b64_e32 v[42:43], 0\n\t"
        "v_mov_b64_e32 v[44:45], 0\n\t"
        "v_mov_b64_e32 v[46:47], 0\n\t"
        "v_mov_b64_e32 v[0:1], 0\n\t"
        "v_mov_b64_e32 v[2:3], 0\n\t"
        "v_mov_b64_e32 v[4:5], 0\n\t"
        "v_mov_b64_e32 v[6:7], 0\n\t"
        "v_mov_b64_e32 v[8:9], 0\n\t"
        "v_mov_b64_e32 v[10:11], 0\n\t"
        "v_mov_b64_e32 v[12:13], 0\n\t"
        "v_mov_b64_e32 v[14:15], 0\n\t"
        "s_add_u32 s46, s26, 4096\n\t"
        "s_sleep 1\n\t"
        "buffer_load_dwordx2 v[104:105], v113, s[8:11], s26 offen sc1\n\t"
        "buffer_store_dword v24, v116, s[8:11], s28 offen sc1\n\t"
        "s_xor_b32 s34, s26, 0x100000\n\t"
        "buffer_store_dwordx2 v[24:25], v114, s[8:11], s34 offen sc1\n\t"
        "s_add_u32 s66, s49, s43\n\t"
        "s_cmp_lg_u32 s66, 32\n\t"
        "s_cbranch_scc1 LB_norep%=\n\t"
        "s_waitcnt vmcnt(4)\n\t"
        "s_sub_u32 s66, s24, 1\n\t"
        "v_lshrrev_b32_e32 v68, 16, v64\n\t"
        "v_cmp_eq_u32_e32 vcc, s66, v68\n\t"
        "v_subrev_u32_e32 v64, s59, v64\n\t"
        "v_bfe_i32 v64, v64, 0, 16\n\t"
        "v_cndmask_b32_e32 v64, 0, v64, vcc\n\t"
        "v_lshrrev_b32_e32 v68, 16, v65\n\t"
        "v_cmp_eq_u32_e32 vcc, s66, v68\n\t"
        "v_subrev_u32_e32 v65, s59, v65\n\t"
        "v_bfe_i32 v65, v65, 0, 16\n\t"
        "v_cndmask_b32_e32 v65, 0, v65, vcc\n\t"
        "v_lshrrev_b32_e32 v68, 16, v66\n\t"
        "v_cmp_eq_u32_e32 vcc, s66, v68\n\t"
        "v_subrev_u32_e32 v66, s59, v66\n\t"
        "v_bfe_i32 v66, v66, 0, 16\n\t"
        "v_cndmask_b32_e32 v66, 0, v66, vcc\n\t"
        "v_lshrrev_b32_e32 v68, 16, v67\n\t"
        "v_cmp_eq_u32_e32 vcc, s66, v68\n\t"
        "v_subrev_u32_e32 v67, s59, v67\n\t"
        "v_bfe_i32 v67, v67, 0, 16\n\t"
        "v_cndmask_b32_e32 v67, 0, v67, vcc\n\t"
        "v_max3_i32 v64, v64, v65, v66\n\t"
        "v_max_i32_e32 v64, v64, v67\n\t"
        "s_nop 1\n\t"
        "v_max_i32_dpp v64, v64, v64 quad_perm:[1,0,3,2] row_mask:0xf bank_mask:0xf\n\t"
        "s_nop 1\n\t"
        "v_max_i32_dpp v64, v64, v64 quad_perm:[2,3,0,1] row_mask:0xf bank_mask:0xf\n\t"
        "s_nop 1\n\t"
        "v_max_i32_dpp v64, v64, v64 row_ror:4 row_mask:0xf bank_mask:0xf\n\t"
        "s_nop 1\n\t"
        "v_max_i32_dpp v64, v64, v64 row_ror:8 row_mask:0xf bank_mask:0xf\n\t"
        "v_mov_b32_e32 v65, v64\n\t"
        "s_nop 1\n\t"
        "v_permlane16_swap_b32_e32 v64, v65\n\t"
        "v_max_i32_e32 v64, v64, v65\n\t"
        "v_mov_b32_e32 v65, v64\n\t"
        "s_nop 1\n\t"
        "v_permlane32_swap_b32_e32 v64, v65\n\t"
        "v_max_i32_e32 v64, v64, v65\n\t"
        "s_nop 0\n\t"
        "v_readfirstlane_b32 s64, v64\n\t"
        "LB_norep%=:\n\t"
        "s_waitcnt vmcnt(2) lgkmcnt(0)\n\t"
        "s_mov_b32 s61, s29\n\t"
        "s_mov_b32 s62, s30\n\t"
        "s_mov_b32 s63, s31\n\t"
        "v_and_b32_e32 v30, 0xff, v105\n\t"
        "v_cmp_ne_u32_e32 vcc, s24, v30\n\t"
        "s_nop 0\n\t"
        "s_cbranch_vccnz LB_stre%=\n\t"
        "LB_stok%=:\n\t"
        "v_subrev_u32_e32 v104, s25, v104\n\t"
        "s_nop 1\n\t"
        "v_max_i32_dpp v104, v104, v104 quad_perm:[1,0,3,2] row_mask:0xf bank_mask:0xf\n\t"
        "s_nop 1\n\t"
        "v_max_i32_dpp v104, v104, v104 quad_perm:[2,3,0,1] row_mask:0xf bank_mask:0xf\n\t"
        "s_nop 1\n\t"
        "v_max_i32_dpp v104, v104, v104 row_ror:4 row_mask:0xf bank_mask:0xf\n\t"
        "s_nop 1\n\t"
        "v_max_i32_dpp v104, v104, v104 row_ror:8 row_mask:0xf bank_mask:0xf\n\t"
        "s_nop 1\n\t"
        "v_max_i32_dpp v104, v104, v104 row_bcast:15 row_mask:0xa bank_mask:0xf\n\t"
        "s_nop 1\n\t"
        "v_max_i32_dpp v104, v104, v104 row_bcast:31 row_mask:0xc bank_mask:0xf\n\t"
        "v_readfirstlane_b32 s35, v105\n\t"
        "v_readlane_b32 s36, v104, 63\n\t"
        "s_bfe_u32 s29, s35, 0xc0008\n\t"
        "s_lshr_b32 s30, s35, 20\n\t"
        "s_add_u32 s35, s31, s50\n\t"
        "s_mul_i32 s37, s50, 3\n\t"
        "s_min_u32 s35, s35, s37\n\t"
        "s_add_i32 s34, s36, s51\n\t"
        "s_cmp_gt_i32 s34, 0\n\t"
        "s_cselect_b32 s31, s35, 0\n\t"
        "s_cselect_b32 s34, s34, 0\n\t"
        "s_add_u32 s34, s34, s31\n\t"
        "s_add_u32 s37, s25, s34\n\t"
        "s_add_u32 s45, s37, s29\n\t"
        "s_add_u32 s25, s45, s30\n\t"
        "s_add_u32 s66, s49, s43\n\t"
        "s_cmp_lg_u32 s66, 32\n\t"
        "s_cbranch_scc1 LB_noctl%=\n\t"
        "s_cmp_eq_u32 s24, 0\n\t"
        "s_cbranch_scc1 LB_ctl0%=\n\t"
        "s_cmp_lt_u32 s24, 6\n\t"
        "s_cselect_b32 s68, 32, 8\n\t"
        "s_add_i32 s65, s36, s62\n\t"
        "s_cmp_eq_u32 s24, 1\n\t"
        "s_cselect_b32 s34, 6, 0\n\t"
        "s_cselect_b32 s35, 3, 0\n\t"
        "s_sub_u32 s64, s64, s34\n\t"
        "s_sub_u32 s65, s65, s35\n\t"
        "s_lshl_b32 s66, s65, 1\n\t"
        "s_sub_i32 s66, s66, s56\n\t"
        "s_cmp_gt_i32 s66, 56\n\t"
        "s_cbranch_scc1 LB_c1%=\n\t"
        "s_lshl_b32 s66, s65, 1\n\t"
        "s_add_u32 s34, s56, s68\n\t"
        "s_min_i32 s34, s34, s66\n\t"
        "s_sub_u32 s35, s56, s66\n\t"
        "s_ashr_i32 s35, s35, 1\n\t"
        "s_max_i32 s35, s35, 1\n\t"
        "s_sub_u32 s35, s56, s35\n\t"
        "s_max_i32 s56, s34, s35\n\t"
        "s_branch LB_c2%=\n\t"
        "LB_c1%=:\n\t"
        "s_add_u32 s56, s56, 2\n\t"
        "s_add_i32 s66, s64, s57\n\t"
        "s_cmp_gt_i32 s66, s61\n\t"
        "s_cbranch_scc1 LB_c2%=\n\t"
        "s_add_u32 s57, s57, 4\n\t"
        "s_min_u32 s57, s57, 24\n\t"
        "LB_c2%=:\n\t"
        "s_cmp_lg_u32 s63, 0\n\t"
        "s_cbranch_scc1 LB_c4%=\n\t"
        "s_lshl_b32 s66, s64, 1\n\t"
        "s_sub_i32 s66, s66, s55\n\t"
        "s_cmp_gt_i32 s66, 56\n\t"
        "s_cbranch_scc1 LB_c3%=\n\t"
        "s_lshl_b32 s66, s64, 1\n\t"
        "s_add_u32 s34, s55, s68\n\t"
        "s_min_i32 s34, s34, s66\n\t"
        "s_sub_u32 s35, s55, s66\n\t"
        "s_ashr_i32 s35, s35, 1\n\t"
        "s_max_i32 s35, s35, 1\n\t"
        "s_sub_u32 s35, s55, s35\n\t"
        "s_max_i32 s55, s34, s35\n\t"
        "s_branch LB_c4%=\n\t"
        "LB_c3%=:\n\t"
        "s_add_u32 s55, s55, 2\n\t"
        "s_cmp_lt_i32 s60, s58\n\t"
        "s_cbranch_scc1 LB_c4%=\n\t"
        "s_add_u32 s66, s58, 8\n\t"
        "s_cmp_gt_i32 s60, s66\n\t"
        "s_cbranch_scc1 LB_c4%=\n\t"
        "s_add_u32 s58, s58, 4\n\t"
        "s_min_u32 s58, s58, 22\n\t"
        "LB_c4%=:\n\t"
        "s_and_b32 s66, s24, 3\n\t"
        "s_cmp_eq_u32 s66, 0\n\t"
        "s_cselect_b32 s66, 1, 0\n\t"
        "s_cmp_lt_u32 s24, 30\n\t"
        "s_cselect_b32 s34, 8, 8\n\t"
        "s_cselect_b32 s35, 6, 6\n\t"
        "s_sub_u32 s57, s57, s66\n\t"
        "s_max_i32 s57, s57, s34\n\t"
        "s_sub_u32 s58, s58, s66\n\t"
        "s_max_i32 s58, s58, s35\n\t"
        "s_add_u32 s34, s55, 1\n\t"
        "s_lshr_b32 s34, s34, 1\n\t"
        "s_add_u32 s34, s34, s57\n\t"
        "s_min_u32 s34, s34, 1000\n\t"
        "s_lshl_b32 s34, s34, 8\n\t"
        "s_add_u32 s35, s56, 1\n\t"
        "s_lshr_b32 s35, s35, 1\n\t"
        "s_add_u32 s35, s35, s58\n\t"
        "s_min_u32 s35, s35, 1000\n\t"
        "s_lshl_b32 s35, s35, 20\n\t"
        "s_or_b32 s54, s34, s35\n\t"
        "s_branch LB_ctl1%=\n\t"
        "LB_ctl0%=:\n\t"
        "s_movk_i32 s57, 8\n\t"
        "s_movk_i32 s58, 6\n\t"
        "s_sub_u32 s55, s29, 22\n\t"
        "s_lshl_b32 s55, s55, 1\n\t"
        "s_sub_u32 s56, s30, 14\n\t"
        "s_lshl_b32 s56, s56, 1\n\t"
        "LB_ctl1%=:\n\t"
        "s_mov_b32 s59, s37\n\t"
        "s_sub_u32 s60, 0, s36\n\t"
        "LB_noctl%=:\n\t"
        "s_cmp_eq_u32 s24, 0\n\t"
        "s_cselect_b32 s34, 1537, 1\n\t"
        "s_add_u32 s34, s34, s24\n\t"
        "s_add_u32 s34, s34, s54\n\t"
        "v_mov_b32_e32 v111, s34\n\t"
        "s_mov_b32 s42, 0\n\t"
        "s_sub_u32 s35, s45, 8\n\t"
        "s_waitcnt lgkmcnt(0)\n\t"
        "s_memrealtime s[2:3]\n\t"
        "s_memrealtime s[4:5]\n\t"
        "s_memrealtime s[6:7]\n\t"
        "LB_wo%=:\n\t"
        "s_memrealtime s[32:33]\n\t"
        "s_waitcnt lgkmcnt(6)\n\t"
        "s_sub_u32 s34, s35, s2\n\t"
        "s_cmpk_lt_u32 s34, 0x2000\n\t"
        "s_cbranch_scc0 LB_wo%=x\n\t"
        "s_memrealtime s[2:3]\n\t"
        "s_waitcnt lgkmcnt(6)\n\t"
        "s_sub_u32 s34, s35, s4\n\t"
        "s_cmpk_lt_u32 s34, 0x2000\n\t"
        "s_cbranch_scc0 LB_wo%=x\n\t"
        "s_memrealtime s[4:5]\n\t"
        "s_waitcnt lgkmcnt(6)\n\t"
        "s_sub_u32 s34, s35, s6\n\t"
        "s_cmpk_lt_u32 s34, 0x2000\n\t"
        "s_cbranch_scc0 LB_wo%=x\n\t"
        "s_memrealtime s[6:7]\n\t"
        "s_waitcnt lgkmcnt(6)\n\t"
        "s_sub_u32 s34, s35, s32\n\t"
        "s_cmpk_lt_u32 s34, 0x2000\n\t"
        "s_cbranch_scc1 LB_wo%=\n\t"
        "s_nop 3\n\t"
        "LB_wo%=x:\n\t"
        "s_add_i32 s34, s34, 4\n\t"
        "s_max_i32 s34, s34, 0\n\t"
        "s_bitcmp1_b32 s34, 1\n\t"
        "s_cbranch_scc0 LB_wo%=y\n\t"
        "s_nop 15\n\t"
        "LB_wo%=y:\n\t"
        "s_bitcmp1_b32 s34, 0\n\t"
        "s_cbranch_scc0 LB_wo%=z\n\t"
        "s_nop 9\n\t"
        "LB_wo%=z:\n\t"
        "LB_ro%=:\n\t"
        "buffer_load_dwordx4 v[64:67], v115, s[8:11], s26 offen offset:0 sc1\n\t"
        "s_nop 12\n\t"
        "buffer_load_dwordx4 v[68:71], v115, s[8:11], s26 offen offset:1024 sc1\n\t"
        "s_nop 12\n\t"
        "buffer_load_dwordx4 v[72:75], v115, s[8:11], s26 offen offset:2048 sc1\n\t"
        "s_nop 12\n\t"
        "buffer_load_dwordx4 v[76:79], v115, s[8:11], s26 offen offset:3072 sc1\n\t"
        "s_nop 12\n\t"
        "buffer_load_dwordx4 v[80:83], v115, s[8:11], s46 offen offset:0 sc1\n\t"
        "s_nop 12\n\t"
        "buffer_load_dwordx4 v[84:87], v115, s[8:11], s46 offen offset:1024 sc1\n\t"
        "s_nop 12\n\t"
        "buffer_load_dwordx4 v[88:91], v115, s[8:11], s46 offen offset:2048 sc1\n\t"
        "s_nop 12\n\t"
        "buffer_load_dwordx4 v[92:95], v115, s[8:11], s46 offen offset:3072 sc1\n\t"
        "LB_go2%=:\n\t"
        "s_waitcnt vmcnt(7)\n\t"
        "v_smfmac_f32_16x16x64_bf16 v[32:35], v[64:67], a[0:7], v106\n\t"
        "v_max3_u32 v108, v64, v65, v66\n\t"
        "v_max_u32_e32 v108, v67, v108\n\t"
        "v_smfmac_f32_16x16x64_bf16 v[40:43], v[64:67], a[16:23], v106\n\t"
        "v_smfmac_f32_16x16x64_bf16 v[36:39], v[64:67], a[8:15], v106\n\t"
        "v_smfmac_f32_16x16x64_bf16 v[44:47], v[64:67], a[24:31], v106\n\t"
        "s_waitcnt vmcnt(6)\n\t"
        "v_smfmac_f32_16x16x64_bf16 v[32:35], v[68:71], a[32:39], v106\n\t"
        "v_max3_u32 v108, v68, v69, v108\n\t"
        "v_max3_u32 v108, v70, v71, v108\n\t"
        "v_smfmac_f32_16x16x64_bf16 v[40:43], v[68:71], a[48:55], v106\n\t"
        "v_smfmac_f32_16x16x64_bf16 v[36:39], v[68:71], a[40:47], v106\n\t"
        "v_smfmac_f32_16x16x64_bf16 v[44:47], v[68:71], a[56:63], v106\n\t"
        "s_waitcnt vmcnt(5)\n\t"
        "v_smfmac_f32_16x16x64_bf16 v[32:35], v[72:75], a[64:71], v106\n\t"
        "v_max3_u32 v108, v72, v73, v108\n\t"
        "v_max3_u32 v108, v74, v75, v108\n\t"
        "v_smfmac_f32_16x16x64_bf16 v[40:43], v[72:75], a[80:87], v106\n\t"
        "v_smfmac_f32_16x16x64_bf16 v[36:39], v[72:75], a[72:79], v106\n\t"
        "v_smfmac_f32_16x16x64_bf16 v[44:47], v[72:75], a[88:95], v106\n\t"
        "s_waitcnt vmcnt(4)\n\t"
        "v_smfmac_f32_16x16x64_bf16 v[32:35], v[76:79], a[96:103], v106\n\t"
        "v_max3_u32 v108, v76, v77, v108\n\t"
        "v_max3_u32 v108, v78, v79, v108\n\t"
        "v_smfmac_f32_16x16x64_bf16 v[40:43], v[76:79], a[112:119], v106\n\t"
        "v_smfmac_f32_16x16x64_bf16 v[36:39], v[76:79], a[104:111], v106\n\t"
        "v_smfmac_f32_16x16x64_bf16 v[44:47], v[76:79], a[120:127], v106\n\t"
        "s_waitcnt vmcnt(3)\n\t"
        "v_smfmac_f32_16x16x64_bf16 v[32:35], v[80:83], a[128:135], v106\n\t"
        "v_max3_u32 v108, v80, v81, v108\n\t"
        "v_max3_u32 v108, v82, v83, v108\n\t"
        "v_smfmac_f32_16x16x64_bf16 v[40:43], v[80:83], a[144:151], v106\n\t"
        "v_smfmac_f32_16x16x64_bf16 v[36:39], v[80:83], a[136:143], v106\n\t"
        "v_smfmac_f32_16x16x64_bf16 v[44:47], v[80:83], a[152:159], v106\n\t"
        "s_waitcnt vmcnt(2)\n\t"
        "v_smfmac_f32_16x16x64_bf16 v[32:35], v[84:87], a[160:167], v106\n\t"
        "v_max3_u32 v108, v84, v85, v108\n\t"
        "v_max3_u32 v108, v86, v87, v108\n\t"
        "v_smfmac_f32_16x16x64_bf16 v[40:43], v[84:87], a[176:183], v106\n\t"
        "v_smfmac_f32_16x16x64_bf16 v[36:39], v[84:87], a[168:175], v106\n\t"
        "v_smfmac_f32_16x16x64_bf16 v[44:47], v[84:87], a[184:191], v106\n\t"
        "s_waitcnt vmcnt(1)\n\t"
        "v_smfmac_f32_16x16x64_bf16 v[32:35], v[88:91], a[192:199], v106\n\t"
        "v_max3_u32 v108, v88, v89, v108\n\t"
        "v_max3_u32 v108, v90, v91, v108\n\t"
        "v_smfmac_f32_16x16x64_bf16 v[40:43], v[88:91], a[208:215], v106\n\t"
        "v_smfmac_f32_16x16x64_bf16 v[36:39], v[88:91], a[200:207], v106\n\t"
        "v_smfmac_f32_16x16x64_bf16 v[44:47], v[88:91], a[216:223], v106\n\t"
        "s_waitcnt vmcnt(0)\n\t"
        "v_smfmac_f32_16x16x64_bf16 v[32:35], v[92:95], a[224:231], v106\n\t"
        "v_max3_u32 v108, v92, v93, v108\n\t"
        "v_max3_u32 v108, v94, v95, v108\n\t"
        "v_smfmac_f32_16x16x64_bf16 v[40:43], v[92:95], a[240:247], v106\n\t"
        "v_cmp_eq_u32_e32 vcc, -1, v108\n\t"
        "v_smfmac_f32_16x16x64_bf16 v[36:39], v[92:95], a[232:239], v106\n\t"
        "v_smfmac_f32_16x16x64_bf16 v[44:47], v[92:95], a[248:255], v106\n\t"
        "s_cbranch_vccnz LB_rofail%=\n\t"
        "s_nop 0\n\t"
        "v_add_f32_e32 v32, v32, v33\n\t"
        "s_nop 1\n\t"
        "v_add_f32_e32 v40, v42, v43\n\t"
        "v_add_f32_e32 v32, v32, v40\n\t"
        "v_add_f32_e32 v36, v36, v37\n\t"
        "v_add_f32_e32 v44, v46, v47\n\t"
        "v_add_f32_e32 v36, v36, v44\n\t"
        "ds_write2_b32 v120, v32, v36 offset0:0 offset1:64\n\t"
        "s_waitcnt lgkmcnt(0)\n\t"
        "s_barrier\n\t"
        "ds_read_b64 v[64:65], v121 offset:0\n\t"
        "ds_read_b64 v[66:67], v121 offset:512\n\t"
        "ds_read_b64 v[68:69], v121 offset:1024\n\t"
        "ds_read_b64 v[70:71], v121 offset:1536\n\t"
        "s_waitcnt lgkmcnt(2)\n\t"
        "v_pk_add_f32 v[64:65], v[64:65], v[66:67]\n\t"
        "s_waitcnt lgkmcnt(0)\n\t"
        "v_pk_add_f32 v[68:69], v[68:69], v[70:71]\n\t"
        "v_pk_add_f32 v[64:65], v[64:65], v[68:69]\n\t"
        "v_cvt_pk_bf16_f32 v64, v64, v65\n\t"
        "buffer_store_dword v64, v116, s[8:11], s27 offen sc1\n\t"
        "s_memrealtime s[32:33]\n\t"
        "s_waitcnt lgkmcnt(0)\n\t"
        "v_mov_b32_e32 v110, s32\n\t"
        "s_add_u32 s24, s24, 1\n\t"
        "s_mov_b32 s28, s26\n\t"
        "s_mov_b32 s26, s27\n\t"
        "s_add_u32 s27, s27, 0x80000\n\t"
        "s_and_b32 s27, s27, 0x1fffff\n\t"
        "s_branch LB_loop%=\n\t"
        "LB_end%=:\n\t"
        "s_endpgm\n\t"
        "LB_stre%=:\n\t"
        "s_mov_b32 s42, 0\n\t"
        "LB_str2%=:\n\t"
        "buffer_load_dwordx2 v[104:105], v113, s[8:11], s26 offen sc1\n\t"
        "s_waitcnt vmcnt(0)\n\t"
        "v_and_b32_e32 v30, 0xff, v105\n\t"
        "v_cmp_ne_u32_e32 vcc, s24, v30\n\t"
        "s_nop 0\n\t"
        "s_cbranch_vccz LB_stok%=\n\t"
        "s_add_u32 s42, s42, 1\n\t"
        "s_cmp_lt_u32 s42, 0x100000\n\t"
        "s_cbranch_scc1 LB_str2%=\n\t"
        "s_movk_i32 s34, 5\n\t"
        "s_branch LB_die%=\n\t"
        "LB_rdfail%=:\n\t"
        "s_or_b32 s47, s47, 1\n\t"
        "buffer_load_dwordx4 v[16:19], v112, s[8:11], s26 offen offset:0 sc1\n\t"
        "buffer_load_dwordx4 v[20:23], v112, s[8:11], s26 offen offset:1024 sc1\n\t"
        "buffer_load_dwordx4 v[24:27], v112, s[8:11], s26 offen offset:2048 sc1\n\t"
        "buffer_load_dwordx4 v[28:31], v112, s[8:11], s26 offen offset:3072 sc1\n\t"
        "s_nop 7\n\t"
        "v_mov_b64_e32 v[0:1], 0\n\t"
        "v_mov_b64_e32 v[2:3], 0\n\t"
        "v_mov_b64_e32 v[4:5], 0\n\t"
        "v_mov_b64_e32 v[6:7], 0\n\t"
        "v_mov_b64_e32 v[8:9], 0\n\t"
        "v_mov_b64_e32 v[10:11], 0\n\t"
        "v_mov_b64_e32 v[12:13], 0\n\t"
        "v_mov_b64_e32 v[14:15], 0\n\t"
        "s_branch LB_rdre%=w\n\t"
        "LB_rdre%=:\n\t"
        "buffer_load_dwordx4 v[16:19], v112, s[8:11], s26 offen offset:0 sc1\n\t"
        "buffer_load_dwordx4 v[20:23], v112, s[8:11], s26 offen offset:1024 sc1\n\t"
        "buffer_load_dwordx4 v[24:27], v112, s[8:11], s26 offen offset:2048 sc1\n\t"
        "buffer_load_dwordx4 v[28:31], v112, s[8:11], s26 offen offset:3072 sc1\n\t"
        "LB_rdre%=w:\n\t"
        "s_waitcnt vmcnt(0) lgkmcnt(0)\n\t"
        "v_max3_u32 v108, v16, v17, v18\n\t"
        "v_max3_u32 v108, v108, v19, v20\n\t"
        "v_max3_u32 v108, v108, v21, v22\n\t"
        "v_max3_u32 v108, v108, v23, v24\n\t"
        "v_max3_u32 v108, v108, v25, v26\n\t"
        "v_max3_u32 v108, v108, v27, v28\n\t"
        "v_max3_u32 v108, v108, v29, v30\n\t"
        "v_max_u32_e32 v108, v108, v31\n\t"
        "v_cmp_eq_u32_e32 vcc, -1, v108\n\t"
        "s_nop 0\n\t"
        "s_cbranch_vccz LB_go%=\n\t"
        "s_add_u32 s42, s42, 1\n\t"
        "s_cmp_lt_u32 s42, 0x100000\n\t"
        "s_cbranch_scc1 LB_rdre%=\n\t"
        "s_movk_i32 s34, 3\n\t"
        "s_branch LB_die%=\n\t"
        "LB_rofail%=:\n\t"
        "s_or_b32 s47, s47, 4\n\t"
        "buffer_load_dwordx4 v[64:67], v115, s[8:11], s26 offen offset:0 sc1\n\t"
        "buffer_load_dwordx4 v[68:71], v115, s[8:11], s26 offen offset:1024 sc1\n\t"
        "buffer_load_dwordx4 v[72:75], v115, s[8:11], s26 offen offset:2048 sc1\n\t"
        "buffer_load_dwordx4 v[76:79], v115, s[8:11], s26 offen offset:3072 sc1\n\t"
        "buffer_load_dwordx4 v[80:83], v115, s[8:11], s46 offen offset:0 sc1\n\t"
        "buffer_load_dwordx4 v[84:87], v115, s[8:11], s46 offen offset:1024 sc1\n\t"
        "buffer_load_dwordx4 v[88:91], v115, s[8:11], s46 offen offset:2048 sc1\n\t"
        "buffer_load_dwordx4 v[92:95], v115, s[8:11], s46 offen offset:3072 sc1\n\t"
        "s_nop 7\n\t"
        "v_mov_b64_e32 v[32:33], 0\n\t"
        "v_mov_b64_e32 v[34:35], 0\n\t"
        "v_mov_b64_e32 v[36:37], 0\n\t"
        "v_mov_b64_e32 v[38:39], 0\n\t"
        "v_mov_b64_e32 v[40:41], 0\n\t"
        "v_mov_b64_e32 v[42:43], 0\n\t"
        "v_mov_b64_e32 v[44:45], 0\n\t"
        "v_mov_b64_e32 v[46:47], 0\n\t"
        "s_branch LB_rore%=w\n\t"
        "LB_rore%=:\n\t"
        "buffer_load_dwordx4 v[64:67], v115, s[8:11], s26 offen offset:0 sc1\n\t"
        "buffer_load_dwordx4 v[68:71], v115, s[8:11], s26 offen offset:1024 sc1\n\t"
        "buffer_load_dwordx4 v[72:75], v115, s[8:11], s26 offen offset:2048 sc1\n\t"
        "buffer_load_dwordx4 v[76:79], v115, s[8:11], s26 offen offset:3072 sc1\n\t"
        "buffer_load_dwordx4 v[80:83], v115, s[8:11], s46 offen offset:0 sc1\n\t"
        "buffer_load_dwordx4 v[84:87], v115, s[8:11], s46 offen offset:1024 sc1\n\t"
        "buffer_load_dwordx4 v[88:91], v115, s[8:11], s46 offen offset:2048 sc1\n\t"
        "buffer_load_dwordx4 v[92:95], v115, s[8:11], s46 offen offset:3072 sc1\n\t"
        "LB_rore%=w:\n\t"
        "s_waitcnt vmcnt(0) lgkmcnt(0)\n\t"
        "v_max3_u32 v108, v64, v65, v66\n\t"
        "v_max3_u32 v108, v108, v67, v68\n\t"
        "v_max3_u32 v108, v108, v69, v70\n\t"
        "v_max3_u32 v108, v108, v71, v72\n\t"
        "v_max3_u32 v108, v108, v73, v74\n\t"
        "v_max3_u32 v108, v108, v75, v76\n\t"
        "v_max3_u32 v108, v108, v77, v78\n\t"
        "v_max3_u32 v108, v108, v79, v80\n\t"
        "v_max3_u32 v108, v108, v81, v82\n\t"
        "v_max3_u32 v108, v108, v83, v84\n\t"
        "v_max3_u32 v108, v108, v85, v86\n\t"
        "v_max3_u32 v108, v108, v87, v88\n\t"
        "v_max3_u32 v108, v108, v89, v90\n\t"
        "v_max3_u32 v108, v108, v91, v92\n\t"
        "v_max3_u32 v108, v108, v93, v94\n\t"
        "v_max_u32_e32 v108, v108, v95\n\t"
        "v_cmp_eq_u32_e32 vcc, -1, v108\n\t"
        "s_nop 0\n\t"
        "s_cbranch_vccz LB_go2%=\n\t"
        "s_add_u32 s42, s42, 1\n\t"
        "s_cmp_lt_u32 s42, 0x100000\n\t"
        "s_cbranch_scc1 LB_rore%=\n\t"
        "s_movk_i32 s34, 4\n\t"
        "s_branch LB_die%=\n\t"
        "LB_die%=:\n\t"
        "v_mov_b32_e32 v30, s34\n\t"
        "s_mov_b32 s35, 33024\n\t"
        "s_mov_b64 exec, 1\n\t"
        "buffer_store_dword v30, off, s[12:15], s35 sc1\n\t"
        "s_endpgm\n\t"
        ".Lk_end_B:\n\t"
        :: REC_S_IN, "{v96}"(g_v), "{v97}"(g_o), "{v98}"(wb0), "{v99}"(wb1), "{v100}"(rb0), "{v101}"(rb0 ^ 32u), "{v102}"(rb0 ^ 64u), "{v103}"(rb0 ^ 96u),
           "{v106}"((l & 1) ? 0xEEEEEEEEu : 0x44444444u), "{v[108:109]}"(m0p),
           "{v112}"(a_mem), "{v113}"((24 + cp) * 16384 + l * 32), "{v114}"(a_vmw), "{v115}"(a_or), "{v116}"(a_mw), "{v117}"(a_sw),
           "{v118}"(lb + w * 2048 + (g >> 1) * 256 + n * 16 + (g & 1) * 8), "{v119}"(lb + (w & 1) * 1024 + l * 16), "{v120}"(lb + LB_R + w * 512 + l * 4),
           "{v121}"(lb + LB_R + l * 8), "{v122}"(stg + l * 16), "{v123}"(o_r), "{v124}"(rb + w == 32 ? 28 * 16384 + (l & 31) * 128 : OOB)
        : "memory", "vcc", "scc", "v0", "v255", "a0", "a255", "s101");
    __builtin_unreachable();
}
__device__ __forceinline__ void rec_helper(const Params& p, rsrc_t r, unsigned lb, const int wv, const int hi) {
    const int tid = otid(), l = tid & 63, xq = hi >> 3;
    const rsrc_t rh = __builtin_amdgcn_make_buffer_rsrc(p.ws + ho_base(p.ws), 0, HO_BYTES, 0x00020000);
    const v4u ones = {0xFFFFFFFFu, 0xFFFFFFFFu, 0xFFFFFFFFu, 0xFFFFFFFFu};
    const bool meas = (hi & 7) < 2;
    if (meas) {
        const unsigned ca = l >= 8 ? OOB : (31 + wv * 32) * 16384 + hi * 256 + l * 16, tend = rtnow() + CAL_T;
        unsigned best0 = ~0u, best1 = ~0u;
#pragma unroll 1
        for (int i = 0; i < 64 && (i < 3 || (int)(rtnow() - tend) < 0); ++i) {
            const unsigned t0 = (unsigned)__builtin_amdgcn_s_memtime(); __builtin_amdgcn_s_waitcnt(0);
            st16(rh, ca, ones); DRAIN();
            const unsigned t1 = (unsigned)__builtin_amdgcn_s_memtime(); __builtin_amdgcn_s_waitcnt(0);
            st16(rh, ca + 8192, ones); DRAIN();
            const unsigned t2 = (unsigned)__builtin_amdgcn_s_memtime(); __builtin_amdgcn_s_waitcnt(0);
            best0 = min(best0, t1 - t0); best1 = min(best1, t2 - t1); }
        if (l < 2) __hip_atomic_fetch_max((unsigned*)(p.ws + F_CAL + 128) + l, 65535u - min(l ? best1 : best0, 65535u), __ATOMIC_RELAXED, __HIP_MEMORY_SCOPE_AGENT);
    }
    auto touch = [&](int z0, int z1) {
#pragma unroll 1
        for (int z = z0 + ((hi & 7) - 2) * 4 + wv; z < z1; z += 24) { const bf16_t* q;
            if (z < 48) q = ((z & 1) ? p.Wk : p.Wq) + (size_t)(((((z & 1) * 2 + 1) * 4 + 4 + (z >> 2)) & 15) * 64 + l) * 4096 + (xq * 8 + 1 + (z & 2) * 2) * 128 + 64;
            else if (z < 112) { const int y = z - 48; q = p.Wo + (size_t)(((y & 3) * 8 + (y >> 3)) * 128 + ((y >> 2) & 1) * 64 + l) * 1024 + (xq & 1) * 512 + 192; }
            else { const int s7 = (z - 112) % 7, y = (z - 112) / 7; q = p.Wo + (size_t)(((y & 3) * 8 + 8 - T2U + (y >> 3)) * 128 + ((y >> 2) & 1) * 64 + l) * 1024 + (xq & 1) * 512 + (s7 + (s7 >= 3)) * 64; }
            touch_l2(q, lb + 1024); } };
    if (!meas) touch(0, TPRE);
    st16(r, tid < 64 ? F_KVQ + hi * 1024 + tid * 16 : tid < 72 ? F_PROG + hi * 128 + (tid - 64) * 16 : tid < 80 ? F_CVY + hi * 128 + (tid - 72) * 16 : OOB, (v4u){0u, 0u, 0u, 0u});
#pragma unroll
    for (int i = 0; i < 2; ++i) st16(r, W_OX + 63 * 262144 + hi * 8192 + i * 4096 + tid * 16, ones);
#pragma unroll
    for (int i = 0; i < 16; ++i) st16(rh, hi * 65536 + i * 4096 + tid * 16, ones);
    DRAIN();
    __syncthreads();
    if (tid == 0 && __hip_atomic_fetch_add((unsigned*)(p.ws + F_CAL + 256), 1u, __ATOMIC_RELAXED, __HIP_MEMORY_SCOPE_AGENT) == 31u) {
        const v2u c = ld8(r, F_CAL + 128); st4(r, F_CAL, c.x < c.y ? 8193u : 1u); }
    if (!meas) touch(TPRE, 112 + 56 * T2U);
    DRAIN();
}
constexpr int CV_RP = 80;
constexpr int CV_U = 96 * CV_RP;
constexpr int CV_KP = 4416;
constexpr int CV_K0 = 8 * CV_U;
#define NAP() do { __builtin_amdgcn_sched_barrier(0); __builtin_amdgcn_s_sleep(XSLP); __builtin_amdgcn_sched_barrier(0); } while (0)
__device__ __forceinline__ void gate_block(const Params& p, rsrc_t r, const int hi, const int wv) {
    constexpr int NT = 4;
    const int l = otid() & 63, t0 = (hi * 4 + wv) * NT;
    v4u xg[NT][2], gw[2];
#pragma unroll
    for (int q = 0; q < 2; ++q) gw[q] = *(const v4u*)(p.gate_w + q * 512 + l * 8);
#pragma unroll
    for (int i = 0; i < NT; ++i)
#pragma unroll
        for (int q = 0; q < 2; ++q) { xg[i][q] = *(const v4u*)(p.Aq + (size_t)(t0 + i) * D + q * 512 + l * 8); NAP(); }
    float mine = 0.f;
#pragma unroll
    for (int i = 0; i < NT; ++i) { float acc = 0.f;
#pragma unroll
        for (int q = 0; q < 2; ++q)
#pragma unroll
            for (int e = 0; e < 4; ++e) { const unsigned pr = pack2(blo(xg[i][q][e]) * blo(gw[q][e]), bhi(xg[i][q][e]) * bhi(gw[q][e])); acc += blo(pr); acc += bhi(pr); }
        for (int o = 32; o > 0; o >>= 1) acc += __shfl_xor(acc, o);
        if ((l & 3) == i) mine = acc; }
    if (l < 32) { const float z = rbfh(mine); st4(r, W_GATE + t0 * 32 + l * 4, __float_as_uint(rbfh(1.f / (1.f + expf(-z))))); }
}
__device__ __forceinline__ void conv_prep(const Params& p, rsrc_t r, const int wv, const int hi, const int G) {
    const int tid = otid(), Q = tid >> 2, cg = tid & 3, ch0 = hi * 32 + cg * 8;
    v4u sk[4]; float sv[8][4];
#pragma unroll
    for (int q = 0; q < 4; ++q) sk[q] = *(const v4u*)(p.shift_k + ch0 * 4 + q * 8);
#pragma unroll
    for (int c8 = 0; c8 < 8; ++c8)
#pragma unroll
        for (int tp = 0; tp < 4; ++tp) { const unsigned sw = sk[c8 >> 1][(c8 & 1) * 2 + (tp >> 1)]; sv[c8][tp] = (tp & 1) ? bhi(sw) : blo(sw); }
    const rsrc_t rk = __builtin_amdgcn_make_buffer_rsrc((void*)p.Ak, 0, S * D * 2, 0x00020000), rv = __builtin_amdgcn_make_buffer_rsrc((void*)p.Av, 0, S * D * 2, 0x00020000),
                 rq = __builtin_amdgcn_make_buffer_rsrc((void*)p.Aq, 0, S * D * 2, 0x00020000);
    {
        const unsigned ro = (unsigned)((Q * 32 + G * 8) * D + ch0) * 2u, uo = hi * 131072 + tid * 16;
        v4u kr[11], u[8], qv[8];
#pragma unroll
        for (int i = 0; i < 11; ++i) { kr[i] = __builtin_amdgcn_raw_buffer_load_b128(rk, (i < 3 && Q + G == 0) ? OOB : ro + (i - 3) * 2048, 0, 2); NAP(); }
#pragma unroll
        for (int i = 0; i < 8; ++i) { u[i] = __builtin_amdgcn_raw_buffer_load_b128(rv, ro + i * 2048, 0, 2); NAP(); }
#pragma unroll
        for (int i = 0; i < 8; ++i) { qv[i] = __builtin_amdgcn_raw_buffer_load_b128(rq, ro + i * 2048, 0, 0); NAP(); }
#pragma unroll
        for (int q = 0; q < 4; ++q) {
#pragma unroll
            for (int i = 0; i < 8; ++i) {
                unsigned ks = pack2(blo(kr[i][q]) * sv[2 * q][3], bhi(kr[i][q]) * sv[2 * q + 1][3]), pr;
#pragma unroll
                for (int d = 1; d < 4; ++d) { pr = pack2(blo(kr[i + d][q]) * sv[2 * q][3 - d], bhi(kr[i + d][q]) * sv[2 * q + 1][3 - d]); ks = pack2(blo(ks) + blo(pr), bhi(ks) + bhi(pr)); }
                u[i][q] = pack2(blo(ks) * blo(u[i][q]), bhi(ks) * bhi(u[i][q])); }
            v4u lo, hh;
#pragma unroll
            for (int z = 0; z < 4; ++z) { const unsigned x = u[7 - 2 * z][q], y = u[6 - 2 * z][q]; lo[z] = __builtin_amdgcn_perm(y, x, 0x05040100u); hh[z] = __builtin_amdgcn_perm(y, x, 0x07060302u); }
            st16(r, W_U + uo + q * 32768 + G * 4096, lo); NAP(); st16(r, W_U + uo + q * 32768 + 16384 + G * 4096, hh); NAP();
#pragma unroll
            for (int z = 0; z < 2; ++z) { st16(r, W_Y + uo + q * 32768 + (2 * G + z) * 4096, (v4u){qv[4 * z][q], qv[4 * z + 1][q], qv[4 * z + 2][q], qv[4 * z + 3][q]}); NAP(); }
        }
    }
    DRAIN(); __syncthreads();
    if (tid == 0) __hip_atomic_fetch_add((unsigned*)(p.ws + F_CONV), 1u, __ATOMIC_RELAXED, __HIP_MEMORY_SCOPE_AGENT);
    if (tid == 0) __hip_atomic_fetch_add((unsigned*)(p.ws + F_CVY + hi * 128 + 64), 1u, __ATOMIC_RELAXED, __HIP_MEMORY_SCOPE_AGENT);
}
__device__ __forceinline__ bool conv_core(const Params& p, rsrc_t r, unsigned char* lds, const int wv, const int un, const int nt, const int ro) {
    const int tid = otid(), w = wv, l = tid & 63, Q = tid >> 2, cg = tid & 3;
    const rsrc_t rs = __builtin_amdgcn_make_buffer_rsrc((void*)p.ssm_k, 0, S * D * 2, 0x00020000);
    v4u kraw[4]; v2u kext[4];
    auto kload = [&](int n) { const unsigned o = (unsigned)(((un >> 2) * 32 + w * 8 + (un & 3) * 2 + n) * S + l * 8) * 2u;
#pragma unroll
        for (int i = 0; i < 4; ++i) { kraw[i] = __builtin_amdgcn_raw_buffer_load_b128(rs, o + i * 1024, 0, 2);
            kext[i] = __builtin_amdgcn_raw_buffer_load_b64(rs, (i == 3 && l == 63) ? OOB : o + i * 1024 + 16, 0, 2); } };
    kload(0);
    for (int i = tid; i < 8 * 160; i += 256) *(v4u*)(lds + (i / 160) * CV_U + (i % 160) * 16) = (v4u){0u, 0u, 0u, 0u};
    unsigned char* kc = lds + CV_K0 + w * 4 * CV_KP;
    if (l < 32) {
#pragma unroll
        for (int q = 0; q < 4; ++q) *(unsigned*)(kc + q * CV_KP + l * 4) = 0u; }
    if (!wait_word(r, F_CVY + (un >> 2) * 128 + 64, 4u, 2048u)) return false;
    v4u ur[8], qr[8];
#pragma unroll
    for (int i = 0; i < 8; ++i) ur[i] = ld16(r, W_U + un * 32768 + i * 4096 + tid * 16);
#pragma unroll
    for (int i = 0; i < 8; ++i) qr[i] = ld16(r, W_Y + un * 32768 + i * 4096 + tid * 16);
    const unsigned dv = *(const unsigned*)(p.ssm_D + (un >> 2) * 32 + cg * 8 + (un & 3) * 2);
    const int b = l & 31, kg = l >> 5;
    const unsigned kb = (unsigned)(size_t)kc + (b & 3) * CV_KP + ((b & 28) - 8 * kg + 56 - 16) * 2, ub0 = (unsigned)(size_t)lds + (1 + b) * CV_RP + kg * 16;
    unsigned char* const urow = lds + 2 * cg * CV_U + (32 + Q) * CV_RP;
#pragma unroll
    for (int i = 0; i < 8; ++i) *(v4u*)(urow + (i >> 2) * CV_U + (i & 3) * 16) = ur[i];
    __syncthreads();
    const v4u so = mk_srd(p.Wo, 1024 * 4096 * 2); const unsigned go = (w * 1024 + (l >> 3)) * 2048 + nt * 128 + (l & 7) * 16; unsigned t0, t1;
#pragma unroll
        for (int ci = 0; ci < 2; ++ci) {
            const int c8 = 2 * w + ci;
#pragma unroll
            for (int i = 0; i < 4; ++i) { const v4u e = kraw[i]; const v2u f = kext[i]; unsigned char* q = kc + 128 + (i * 64 + l) * 16;
                const unsigned s0 = __builtin_amdgcn_alignbit(e.y, e.x, 16), s1 = __builtin_amdgcn_alignbit(e.z, e.y, 16), s2 = __builtin_amdgcn_alignbit(e.w, e.z, 16),
                               s3 = __builtin_amdgcn_alignbit(f.x, e.w, 16), s4 = __builtin_amdgcn_alignbit(f.y, f.x, 16);
                *(v4u*)q = (v4u){s0, s1, s2, s3}; *(v4u*)(q + CV_KP) = (v4u){e.y, e.z, e.w, f.x}; *(v4u*)(q + 2 * CV_KP) = (v4u){s1, s2, s3, s4}; *(v4u*)(q + 3 * CV_KP) = (v4u){e.z, e.w, f.x, f.y};
                if (i == 0 && l == 0) {
                    *(v2u*)(q - 8) = (v2u){0u, e.x << 16}; *(v2u*)(q + CV_KP - 8) = (v2u){0u, e.x}; *(v2u*)(q + 2 * CV_KP - 8) = (v2u){e.x << 16, s0}; *(v2u*)(q + 3 * CV_KP - 8) = (v2u){e.x, e.y}; } }
            if (ci == 0) kload(1);
            __builtin_amdgcn_wave_barrier();
            f32x16 acc0, acc1;
            if (ci == 0) asm volatile(
                "ds_read_b64 v[192:193], %[kb] offset:32\n\t"
                "ds_read_b64 v[194:195], %[kb] offset:40\n\t"
                "ds_read_b128 v[196:199], %[ub] offset:5040\n\t"
                "ds_read_b128 v[200:203], %[ub] offset:2480\n\t"
                "ds_read_b64 v[204:205], %[kb] offset:2080\n\t"
                "ds_read_b64 v[206:207], %[kb] offset:2088\n\t"
                "ds_read_b64 v[208:209], %[kb] offset:0\n\t"
                "ds_read_b64 v[210:211], %[kb] offset:8\n\t"
                "ds_read_b128 v[212:215], %[ub] offset:5072\n\t"
                "ds_read_b128 v[216:219], %[ub] offset:2512\n\t"
                "ds_read_b64 v[220:221], %[kb] offset:2048\n\t"
                "ds_read_b64 v[222:223], %[kb] offset:2056\n\t"
                "s_waitcnt lgkmcnt(9)\n\t"
                "v_mfma_f32_32x32x16_bf16 %[a1], v[192:195], v[196:199], 0\n\t"
                "ds_read_b64 v[224:225], %[kb] offset:96\n\t"
                "ds_read_b64 v[226:227], %[kb] offset:104\n\t"
                "ds_read_b128 v[228:231], %[ub] offset:4960\n\t"
                "s_waitcnt lgkmcnt(11)\n\t"
                "v_mfma_f32_32x32x16_bf16 %[a0], v[192:195], v[200:203], 0\n\t"
                "ds_read_b128 v[232:235], %[ub] offset:2400\n\t"
                "s_add_u32 %[t1], %[ro], 0\n\t"
                "s_and_b32 %[t1], %[t1], 7\n\t"
                "s_lshl_b32 %[t1], %[t1], 18\n\t"
                "s_add_u32 %[t0], %[t1], 0\n\t"
                "buffer_load_dwordx4 a[0:3], %[go], %[so], %[t0] offen\n\t"
                "s_waitcnt lgkmcnt(10)\n\t"
                "v_mfma_f32_32x32x16_bf16 %[a1], v[204:207], v[200:203], %[a1]\n\t"
                "ds_read_b64 v[236:237], %[kb] offset:2144\n\t"
                "ds_read_b64 v[238:239], %[kb] offset:2152\n\t"
                "s_waitcnt lgkmcnt(9)\n\t"
                "v_mfma_f32_32x32x16_bf16 %[a1], v[208:211], v[212:215], %[a1]\n\t"
                "ds_read_b64 v[240:241], %[kb] offset:64\n\t"
                "ds_read_b64 v[242:243], %[kb] offset:72\n\t"
                "ds_read_b128 v[244:247], %[ub] offset:4992\n\t"
                "s_waitcnt lgkmcnt(11)\n\t"
                "v_mfma_f32_32x32x16_bf16 %[a0], v[208:211], v[216:219], %[a0]\n\t"
                "ds_read_b128 v[248:251], %[ub] offset:2432\n\t"
                "s_waitcnt lgkmcnt(10)\n\t"
                "v_mfma_f32_32x32x16_bf16 %[a1], v[220:223], v[216:219], %[a1]\n\t"
                "ds_read_b64 v[252:253], %[kb] offset:2112\n\t"
                "ds_read_b64 v[254:255], %[kb] offset:2120\n\t"
                "s_waitcnt lgkmcnt(9)\n\t"
                "v_mfma_f32_32x32x16_bf16 %[a1], v[224:227], v[228:231], %[a1]\n\t"
                "ds_read_b64 v[192:193], %[kb] offset:160\n\t"
                "ds_read_b64 v[194:195], %[kb] offset:168\n\t"
                "ds_read_b128 v[196:199], %[ub] offset:4880\n\t"
                "s_waitcnt lgkmcnt(11)\n\t"
                "v_mfma_f32_32x32x16_bf16 %[a0], v[224:227], v[232:235], %[a0]\n\t"
                "ds_read_b128 v[200:203], %[ub] offset:2320\n\t"
                "s_add_u32 %[t0], %[t1], 16384\n\t"
                "buffer_load_dwordx4 a[4:7], %[go], %[so], %[t0] offen\n\t"
                "s_waitcnt lgkmcnt(10)\n\t"
                "v_mfma_f32_32x32x16_bf16 %[a1], v[236:239], v[232:235], %[a1]\n\t"
                "ds_read_b64 v[204:205], %[kb] offset:2208\n\t"
                "ds_read_b64 v[206:207], %[kb] offset:2216\n\t"
                "s_waitcnt lgkmcnt(9)\n\t"
                "v_mfma_f32_32x32x16_bf16 %[a1], v[240:243], v[244:247], %[a1]\n\t"
                "ds_read_b64 v[208:209], %[kb] offset:128\n\t"
                "ds_read_b64 v[210:211], %[kb] offset:136\n\t"
                "ds_read_b128 v[212:215], %[ub] offset:4912\n\t"
                "s_waitcnt lgkmcnt(11)\n\t"
                "v_mfma_f32_32x32x16_bf16 %[a0], v[240:243], v[248:251], %[a0]\n\t"
                "ds_read_b128 v[216:219], %[ub] offset:2352\n\t"
                "s_waitcnt lgkmcnt(10)\n\t"
                "v_mfma_f32_32x32x16_bf16 %[a1], v[252:255], v[248:251], %[a1]\n\t"
                "ds_read_b64 v[220:221], %[kb] offset:2176\n\t"
                "ds_read_b64 v[222:223], %[kb] offset:2184\n\t"
                "s_waitcnt lgkmcnt(9)\n\t"
                "v_mfma_f32_32x32x16_bf16 %[a1], v[192:195], v[196:199], %[a1]\n\t"
                "ds_read_b64 v[224:225], %[kb] offset:224\n\t"
                "ds_read_b64 v[226:227], %[kb] offset:232\n\t"
                "ds_read_b128 v[228:231], %[ub] offset:4800\n\t"
                "s_waitcnt lgkmcnt(11)\n\t"
                "v_mfma_f32_32x32x16_bf16 %[a0], v[192:195], v[200:203], %[a0]\n\t"
                "ds_read_b128 v[232:235], %[ub] offset:2240\n\t"
                "s_add_u32 %[t0], %[t1], 32768\n\t"
                "buffer_load_dwordx4 a[8:11], %[go], %[so], %[t0] offen\n\t"
                "s_waitcnt lgkmcnt(10)\n\t"
                "v_mfma_f32_32x32x16_bf16 %[a1], v[204:207], v[200:203], %[a1]\n\t"
                "ds_read_b64 v[236:237], %[kb] offset:2272\n\t"
                "ds_read_b64 v[238:239], %[kb] offset:2280\n\t"
                "s_waitcnt lgkmcnt(9)\n\t"
                "v_mfma_f32_32x32x16_bf16 %[a1], v[208:211], v[212:215], %[a1]\n\t"
                "ds_read_b64 v[240:241], %[kb] offset:192\n\t"
                "ds_read_b64 v[242:243], %[kb] offset:200\n\t"
                "ds_read_b128 v[244:247], %[ub] offset:4832\n\t"
                "s_waitcnt lgkmcnt(11)\n\t"
                "v_mfma_f32_32x32x16_bf16 %[a0], v[208:211], v[216:219], %[a0]\n\t"
                "ds_read_b128 v[248:251], %[ub] offset:2272\n\t"
                "s_waitcnt lgkmcnt(10)\n\t"
                "v_mfma_f32_32x32x16_bf16 %[a1], v[220:223], v[216:219], %[a1]\n\t"
                "ds_read_b64 v[252:253], %[kb] offset:2240\n\t"
                "ds_read_b64 v[254:255], %[kb] offset:2248\n\t"
                "s_waitcnt lgkmcnt(9)\n\t"
                "v_mfma_f32_32x32x16_bf16 %[a1], v[224:227], v[228:231], %[a1]\n\t"
                "ds_read_b64 v[192:193], %[kb] offset:288\n\t"
                "ds_read_b64 v[194:195], %[kb] offset:296\n\t"
                "ds_read_b128 v[196:199], %[ub] offset:4720\n\t"
                "s_waitcnt lgkmcnt(11)\n\t"
                "v_mfma_f32_32x32x16_bf16 %[a0], v[224:227], v[232:235], %[a0]\n\t"
                "ds_read_b128 v[200:203], %[ub] offset:2160\n\t"
                "s_add_u32 %[t0], %[t1], 49152\n\t"
                "buffer_load_dwordx4 a[12:15], %[go], %[so], %[t0] offen\n\t"
                "s_waitcnt lgkmcnt(10)\n\t"
                "v_mfma_f32_32x32x16_bf16 %[a1], v[236:239], v[232:235], %[a1]\n\t"
                "ds_read_b64 v[204:205], %[kb] offset:2336\n\t"
                "ds_read_b64 v[206:207], %[kb] offset:2344\n\t"
                "s_waitcnt lgkmcnt(9)\n\t"
                "v_mfma_f32_32x32x16_bf16 %[a1], v[240:243], v[244:247], %[a1]\n\t"
                "ds_read_b64 v[208:209], %[kb] offset:256\n\t"
                "ds_read_b64 v[210:211], %[kb] offset:264\n\t"
                "ds_read_b128 v[212:215], %[ub] offset:4752\n\t"
                "s_waitcnt lgkmcnt(11)\n\t"
                "v_mfma_f32_32x32x16_bf16 %[a0], v[240:243], v[248:251], %[a0]\n\t"
                "ds_read_b128 v[216:219], %[ub] offset:2192\n\t"
                "s_add_u32 %[t0], %[t1], 65536\n\t"
                "buffer_load_dwordx4 a[16:19], %[go], %[so], %[t0] offen\n\t"
                "s_waitcnt lgkmcnt(10)\n\t"
                "v_mfma_f32_32x32x16_bf16 %[a1], v[252:255], v[248:251], %[a1]\n\t"
                "ds_read_b64 v[220:221], %[kb] offset:2304\n\t"
                "ds_read_b64 v[222:223], %[kb] offset:2312\n\t"
                "s_waitcnt lgkmcnt(9)\n\t"
                "v_mfma_f32_32x32x16_bf16 %[a1], v[192:195], v[196:199], %[a1]\n\t"
                "ds_read_b64 v[224:225], %[kb] offset:352\n\t"
                "ds_read_b64 v[226:227], %[kb] offset:360\n\t"
                "ds_read_b128 v[228:231], %[ub] offset:4640\n\t"
                "s_waitcnt lgkmcnt(11)\n\t"
                "v_mfma_f32_32x32x16_bf16 %[a0], v[192:195], v[200:203], %[a0]\n\t"
                "ds_read_b128 v[232:235], %[ub] offset:2080\n\t"
                "s_waitcnt lgkmcnt(10)\n\t"
                "v_mfma_f32_32x32x16_bf16 %[a1], v[204:207], v[200:203], %[a1]\n\t"
                "ds_read_b64 v[236:237], %[kb] offset:2400\n\t"
                "ds_read_b64 v[238:239], %[kb] offset:2408\n\t"
                "s_waitcnt lgkmcnt(9)\n\t"
                "v_mfma_f32_32x32x16_bf16 %[a1], v[208:211], v[212:215], %[a1]\n\t"
                "ds_read_b64 v[240:241], %[kb] offset:320\n\t"
                "ds_read_b64 v[242:243], %[kb] offset:328\n\t"
                "ds_read_b128 v[244:247], %[ub] offset:4672\n\t"
                "s_waitcnt lgkmcnt(11)\n\t"
                "v_mfma_f32_32x32x16_bf16 %[a0], v[208:211], v[216:219], %[a0]\n\t"
                "ds_read_b128 v[248:251], %[ub] offset:2112\n\t"
                "s_add_u32 %[t0], %[t1], 81920\n\t"
                "buffer_load_dwordx4 a[20:23], %[go], %[so], %[t0] offen\n\t"
                "s_waitcnt lgkmcnt(10)\n\t"
                "v_mfma_f32_32x32x16_bf16 %[a1], v[220:223], v[216:219], %[a1]\n\t"
                "ds_read_b64 v[252:253], %[kb] offset:2368\n\t"
                "ds_read_b64 v[254:255], %[kb] offset:2376\n\t"
                "s_waitcnt lgkmcnt(9)\n\t"
                "v_mfma_f32_32x32x16_bf16 %[a1], v[224:227], v[228:231], %[a1]\n\t"
                "ds_read_b64 v[192:193], %[kb] offset:416\n\t"
                "ds_read_b64 v[194:195], %[kb] offset:424\n\t"
                "ds_read_b128 v[196:199], %[ub] offset:4560\n\t"
                "s_waitcnt lgkmcnt(11)\n\t"
                "v_mfma_f32_32x32x16_bf16 %[a0], v[224:227], v[232:235], %[a0]\n\t"
                "ds_read_b128 v[200:203], %[ub] offset:2000\n\t"
                "s_waitcnt lgkmcnt(10)\n\t"
                "v_mfma_f32_32x32x16_bf16 %[a1], v[236:239], v[232:235], %[a1]\n\t"
                "ds_read_b64 v[204:205], %[kb] offset:2464\n\t"
                "ds_read_b64 v[206:207], %[kb] offset:2472\n\t"
                "s_waitcnt lgkmcnt(9)\n\t"
                "v_mfma_f32_32x32x16_bf16 %[a1], v[240:243], v[244:247], %[a1]\n\t"
                "ds_read_b64 v[208:209], %[kb] offset:384\n\t"
                "ds_read_b64 v[210:211], %[kb] offset:392\n\t"
                "ds_read_b128 v[212:215], %[ub] offset:4592\n\t"
                "s_waitcnt lgkmcnt(11)\n\t"
                "v_mfma_f32_32x32x16_bf16 %[a0], v[240:243], v[248:251], %[a0]\n\t"
                "ds_read_b128 v[216:219], %[ub] offset:2032\n\t"
                "s_add_u32 %[t0], %[t1], 98304\n\t"
                "buffer_load_dwordx4 a[24:27], %[go], %[so], %[t0] offen\n\t"
                "s_waitcnt lgkmcnt(10)\n\t"
                "v_mfma_f32_32x32x16_bf16 %[a1], v[252:255], v[248:251], %[a1]\n\t"
                "ds_read_b64 v[220:221], %[kb] offset:2432\n\t"
                "ds_read_b64 v[222:223], %[kb] offset:2440\n\t"
                "s_waitcnt lgkmcnt(9)\n\t"
                "v_mfma_f32_32x32x16_bf16 %[a1], v[192:195], v[196:199], %[a1]\n\t"
                "ds_read_b64 v[224:225], %[kb] offset:480\n\t"
                "ds_read_b64 v[226:227], %[kb] offset:488\n\t"
                "ds_read_b128 v[228:231], %[ub] offset:4480\n\t"
                "s_waitcnt lgkmcnt(11)\n\t"
                "v_mfma_f32_32x32x16_bf16 %[a0], v[192:195], v[200:203], %[a0]\n\t"
                "ds_read_b128 v[232:235], %[ub] offset:1920\n\t"
                "s_waitcnt lgkmcnt(10)\n\t"
                "v_mfma_f32_32x32x16_bf16 %[a1], v[204:207], v[200:203], %[a1]\n\t"
                "ds_read_b64 v[236:237], %[kb] offset:2528\n\t"
                "ds_read_b64 v[238:239], %[kb] offset:2536\n\t"
                "s_waitcnt lgkmcnt(9)\n\t"
                "v_mfma_f32_32x32x16_bf16 %[a1], v[208:211], v[212:215], %[a1]\n\t"
                "ds_read_b64 v[240:241], %[kb] offset:448\n\t"
                "ds_read_b64 v[242:243], %[kb] offset:456\n\t"
                "ds_read_b128 v[244:247], %[ub] offset:4512\n\t"
                "s_waitcnt lgkmcnt(11)\n\t"
                "v_mfma_f32_32x32x16_bf16 %[a0], v[208:211], v[216:219], %[a0]\n\t"
                "ds_read_b128 v[248:251], %[ub] offset:1952\n\t"
                "s_add_u32 %[t0], %[t1], 114688\n\t"
                "buffer_load_dwordx4 a[28:31], %[go], %[so], %[t0] offen\n\t"
                "s_waitcnt lgkmcnt(10)\n\t"
                "v_mfma_f32_32x32x16_bf16 %[a1], v[220:223], v[216:219], %[a1]\n\t"
                "ds_read_b64 v[252:253], %[kb] offset:2496\n\t"
                "ds_read_b64 v[254:255], %[kb] offset:2504\n\t"
                "s_waitcnt lgkmcnt(9)\n\t"
                "v_mfma_f32_32x32x16_bf16 %[a1], v[224:227], v[228:231], %[a1]\n\t"
                "ds_read_b64 v[192:193], %[kb] offset:544\n\t"
                "ds_read_b64 v[194:195], %[kb] offset:552\n\t"
                "ds_read_b128 v[196:199], %[ub] offset:4400\n\t"
                "s_waitcnt lgkmcnt(11)\n\t"
                "v_mfma_f32_32x32x16_bf16 %[a0], v[224:227], v[232:235], %[a0]\n\t"
                "ds_read_b128 v[200:203], %[ub] offset:1840\n\t"
                "s_add_u32 %[t1], %[ro], 0\n\t"
                "s_and_b32 %[t1], %[t1], 7\n\t"
                "s_lshl_b32 %[t1], %[t1], 18\n\t"
                "s_add_u32 %[t0], %[t1], 131072\n\t"
                "buffer_load_dwordx4 a[32:35], %[go], %[so], %[t0] offen\n\t"
                "s_waitcnt lgkmcnt(10)\n\t"
                "v_mfma_f32_32x32x16_bf16 %[a1], v[236:239], v[232:235], %[a1]\n\t"
                "ds_read_b64 v[204:205], %[kb] offset:2592\n\t"
                "ds_read_b64 v[206:207], %[kb] offset:2600\n\t"
                "s_waitcnt lgkmcnt(9)\n\t"
                "v_mfma_f32_32x32x16_bf16 %[a1], v[240:243], v[244:247], %[a1]\n\t"
                "ds_read_b64 v[208:209], %[kb] offset:512\n\t"
                "ds_read_b64 v[210:211], %[kb] offset:520\n\t"
                "ds_read_b128 v[212:215], %[ub] offset:4432\n\t"
                "s_waitcnt lgkmcnt(11)\n\t"
                "v_mfma_f32_32x32x16_bf16 %[a0], v[240:243], v[248:251], %[a0]\n\t"
                "ds_read_b128 v[216:219], %[ub] offset:1872\n\t"
                "s_waitcnt lgkmcnt(10)\n\t"
                "v_mfma_f32_32x32x16_bf16 %[a1], v[252:255], v[248:251], %[a1]\n\t"
                "ds_read_b64 v[220:221], %[kb] offset:2560\n\t"
                "ds_read_b64 v[222:223], %[kb] offset:2568\n\t"
                "s_waitcnt lgkmcnt(9)\n\t"
                "v_mfma_f32_32x32x16_bf16 %[a1], v[192:195], v[196:199], %[a1]\n\t"
                "ds_read_b64 v[224:225], %[kb] offset:608\n\t"
                "ds_read_b64 v[226:227], %[kb] offset:616\n\t"
                "ds_read_b128 v[228:231], %[ub] offset:4320\n\t"
                "s_waitcnt lgkmcnt(11)\n\t"
                "v_mfma_f32_32x32x16_bf16 %[a0], v[192:195], v[200:203], %[a0]\n\t"
                "ds_read_b128 v[232:235], %[ub] offset:1760\n\t"
                "s_add_u32 %[t0], %[t1], 147456\n\t"
                "buffer_load_dwordx4 a[36:39], %[go], %[so], %[t0] offen\n\t"
                "s_waitcnt lgkmcnt(10)\n\t"
                "v_mfma_f32_32x32x16_bf16 %[a1], v[204:207], v[200:203], %[a1]\n\t"
                "ds_read_b64 v[236:237], %[kb] offset:2656\n\t"
                "ds_read_b64 v[238:239], %[kb] offset:2664\n\t"
                "s_waitcnt lgkmcnt(9)\n\t"
                "v_mfma_f32_32x32x16_bf16 %[a1], v[208:211], v[212:215], %[a1]\n\t"
                "ds_read_b64 v[240:241], %[kb] offset:576\n\t"
                "ds_read_b64 v[242:243], %[kb] offset:584\n\t"
                "ds_read_b128 v[244:247], %[ub] offset:4352\n\t"
                "s_waitcnt lgkmcnt(11)\n\t"
                "v_mfma_f32_32x32x16_bf16 %[a0], v[208:211], v[216:219], %[a0]\n\t"
                "ds_read_b128 v[248:251], %[ub] offset:1792\n\t"
                "s_waitcnt lgkmcnt(10)\n\t"
                "v_mfma_f32_32x32x16_bf16 %[a1], v[220:223], v[216:219], %[a1]\n\t"
                "ds_read_b64 v[252:253], %[kb] offset:2624\n\t"
                "ds_read_b64 v[254:255], %[kb] offset:2632\n\t"
                "s_waitcnt lgkmcnt(9)\n\t"
                "v_mfma_f32_32x32x16_bf16 %[a1], v[224:227], v[228:231], %[a1]\n\t"
                "ds_read_b64 v[192:193], %[kb] offset:672\n\t"
                "ds_read_b64 v[194:195], %[kb] offset:680\n\t"
                "ds_read_b128 v[196:199], %[ub] offset:4240\n\t"
                "s_waitcnt lgkmcnt(11)\n\t"
                "v_mfma_f32_32x32x16_bf16 %[a0], v[224:227], v[232:235], %[a0]\n\t"
                "ds_read_b128 v[200:203], %[ub] offset:1680\n\t"
                "s_add_u32 %[t0], %[t1], 163840\n\t"
                "buffer_load_dwordx4 a[40:43], %[go], %[so], %[t0] offen\n\t"
                "s_waitcnt lgkmcnt(10)\n\t"
                "v_mfma_f32_32x32x16_bf16 %[a1], v[236:239], v[232:235], %[a1]\n\t"
                "ds_read_b64 v[204:205], %[kb] offset:2720\n\t"
                "ds_read_b64 v[206:207], %[kb] offset:2728\n\t"
                "s_waitcnt lgkmcnt(9)\n\t"
                "v_mfma_f32_32x32x16_bf16 %[a1], v[240:243], v[244:247], %[a1]\n\t"
                "ds_read_b64 v[208:209], %[kb] offset:640\n\t"
                "ds_read_b64 v[210:211], %[kb] offset:648\n\t"
                "ds_read_b128 v[212:215], %[ub] offset:4272\n\t"
                "s_waitcnt lgkmcnt(11)\n\t"
                "v_mfma_f32_32x32x16_bf16 %[a0], v[240:243], v[248:251], %[a0]\n\t"
                "ds_read_b128 v[216:219], %[ub] offset:1712\n\t"
                "s_waitcnt lgkmcnt(10)\n\t"
                "v_mfma_f32_32x32x16_bf16 %[a1], v[252:255], v[248:251], %[a1]\n\t"
                "ds_read_b64 v[220:221], %[kb] offset:2688\n\t"
                "ds_read_b64 v[222:223], %[kb] offset:2696\n\t"
                "s_waitcnt lgkmcnt(9)\n\t"
                "v_mfma_f32_32x32x16_bf16 %[a1], v[192:195], v[196:199], %[a1]\n\t"
                "ds_read_b64 v[224:225], %[kb] offset:736\n\t"
                "ds_read_b64 v[226:227], %[kb] offset:744\n\t"
                "ds_read_b128 v[228:231], %[ub] offset:4160\n\t"
                "s_waitcnt lgkmcnt(11)\n\t"
                "v_mfma_f32_32x32x16_bf16 %[a0], v[192:195], v[200:203], %[a0]\n\t"
                "ds_read_b128 v[232:235], %[ub] offset:1600\n\t"
                "s_add_u32 %[t0], %[t1], 180224\n\t"
                "buffer_load_dwordx4 a[44:47], %[go], %[so], %[t0] offen\n\t"
                "s_waitcnt lgkmcnt(10)\n\t"
                "v_mfma_f32_32x32x16_bf16 %[a1], v[204:207], v[200:203], %[a1]\n\t"
                "ds_read_b64 v[236:237], %[kb] offset:2784\n\t"
                "ds_read_b64 v[238:239], %[kb] offset:2792\n\t"
                "s_waitcnt lgkmcnt(9)\n\t"
                "v_mfma_f32_32x32x16_bf16 %[a1], v[208:211], v[212:215], %[a1]\n\t"
                "ds_read_b64 v[240:241], %[kb] offset:704\n\t"
                "ds_read_b64 v[242:243], %[kb] offset:712\n\t"
                "ds_read_b128 v[244:247], %[ub] offset:4192\n\t"
                "s_waitcnt lgkmcnt(11)\n\t"
                "v_mfma_f32_32x32x16_bf16 %[a0], v[208:211], v[216:219], %[a0]\n\t"
                "ds_read_b128 v[248:251], %[ub] offset:1632\n\t"
                "s_add_u32 %[t0], %[t1], 196608\n\t"
                "buffer_load_dwordx4 a[48:51], %[go], %[so], %[t0] offen\n\t"
                "s_waitcnt lgkmcnt(10)\n\t"
                "v_mfma_f32_32x32x16_bf16 %[a1], v[220:223], v[216:219], %[a1]\n\t"
                "ds_read_b64 v[252:253], %[kb] offset:2752\n\t"
                "ds_read_b64 v[254:255], %[kb] offset:2760\n\t"
                "s_waitcnt lgkmcnt(9)\n\t"
                "v_mfma_f32_32x32x16_bf16 %[a1], v[224:227], v[228:231], %[a1]\n\t"
                "ds_read_b64 v[192:193], %[kb] offset:800\n\t"
                "ds_read_b64 v[194:195], %[kb] offset:808\n\t"
                "ds_read_b128 v[196:199], %[ub] offset:4080\n\t"
                "s_waitcnt lgkmcnt(11)\n\t"
                "v_mfma_f32_32x32x16_bf16 %[a0], v[224:227], v[232:235], %[a0]\n\t"
                "ds_read_b128 v[200:203], %[ub] offset:1520\n\t"
                "s_waitcnt lgkmcnt(10)\n\t"
                "v_mfma_f32_32x32x16_bf16 %[a1], v[236:239], v[232:235], %[a1]\n\t"
                "ds_read_b64 v[204:205], %[kb] offset:2848\n\t"
                "ds_read_b64 v[206:207], %[kb] offset:2856\n\t"
                "s_waitcnt lgkmcnt(9)\n\t"
                "v_mfma_f32_32x32x16_bf16 %[a1], v[240:243], v[244:247], %[a1]\n\t"
                "ds_read_b64 v[208:209], %[kb] offset:768\n\t"
                "ds_read_b64 v[210:211], %[kb] offset:776\n\t"
                "ds_read_b128 v[212:215], %[ub] offset:4112\n\t"
                "s_waitcnt lgkmcnt(11)\n\t"
                "v_mfma_f32_32x32x16_bf16 %[a0], v[240:243], v[248:251], %[a0]\n\t"
                "ds_read_b128 v[216:219], %[ub] offset:1552\n\t"
                "s_add_u32 %[t0], %[t1], 212992\n\t"
                "buffer_load_dwordx4 a[52:55], %[go], %[so], %[t0] offen\n\t"
                "s_waitcnt lgkmcnt(10)\n\t"
                "v_mfma_f32_32x32x16_bf16 %[a1], v[252:255], v[248:251], %[a1]\n\t"
                "ds_read_b64 v[220:221], %[kb] offset:2816\n\t"
                "ds_read_b64 v[222:223], %[kb] offset:2824\n\t"
                "s_waitcnt lgkmcnt(9)\n\t"
                "v_mfma_f32_32x32x16_bf16 %[a1], v[192:195], v[196:199], %[a1]\n\t"
                "ds_read_b64 v[224:225], %[kb] offset:864\n\t"
                "ds_read_b64 v[226:227], %[kb] offset:872\n\t"
                "ds_read_b128 v[228:231], %[ub] offset:4000\n\t"
                "s_waitcnt lgkmcnt(11)\n\t"
                "v_mfma_f32_32x32x16_bf16 %[a0], v[192:195], v[200:203], %[a0]\n\t"
                "ds_read_b128 v[232:235], %[ub] offset:1440\n\t"
                "s_waitcnt lgkmcnt(10)\n\t"
                "v_mfma_f32_32x32x16_bf16 %[a1], v[204:207], v[200:203], %[a1]\n\t"
                "ds_read_b64 v[236:237], %[kb] offset:2912\n\t"
                "ds_read_b64 v[238:239], %[kb] offset:2920\n\t"
                "s_waitcnt lgkmcnt(9)\n\t"
                "v_mfma_f32_32x32x16_bf16 %[a1], v[208:211], v[212:215], %[a1]\n\t"
                "ds_read_b64 v[240:241], %[kb] offset:832\n\t"
                "ds_read_b64 v[242:243], %[kb] offset:840\n\t"
                "ds_read_b128 v[244:247], %[ub] offset:4032\n\t"
                "s_waitcnt lgkmcnt(11)\n\t"
                "v_mfma_f32_32x32x16_bf16 %[a0], v[208:211], v[216:219], %[a0]\n\t"
                "ds_read_b128 v[248:251], %[ub] offset:1472\n\t"
                "s_add_u32 %[t0], %[t1], 229376\n\t"
                "buffer_load_dwordx4 a[56:59], %[go], %[so], %[t0] offen\n\t"
                "s_waitcnt lgkmcnt(10)\n\t"
                "v_mfma_f32_32x32x16_bf16 %[a1], v[220:223], v[216:219], %[a1]\n\t"
                "ds_read_b64 v[252:253], %[kb] offset:2880\n\t"
                "ds_read_b64 v[254:255], %[kb] offset:2888\n\t"
                "s_waitcnt lgkmcnt(9)\n\t"
                "v_mfma_f32_32x32x16_bf16 %[a1], v[224:227], v[228:231], %[a1]\n\t"
                "ds_read_b64 v[192:193], %[kb] offset:928\n\t"
                "ds_read_b64 v[194:195], %[kb] offset:936\n\t"
                "ds_read_b128 v[196:199], %[ub] offset:3920\n\t"
                "s_waitcnt lgkmcnt(11)\n\t"
                "v_mfma_f32_32x32x16_bf16 %[a0], v[224:227], v[232:235], %[a0]\n\t"
                "ds_read_b128 v[200:203], %[ub] offset:1360\n\t"
                "s_waitcnt lgkmcnt(10)\n\t"
                "v_mfma_f32_32x32x16_bf16 %[a1], v[236:239], v[232:235], %[a1]\n\t"
                "ds_read_b64 v[204:205], %[kb] offset:2976\n\t"
                "ds_read_b64 v[206:207], %[kb] offset:2984\n\t"
                "s_waitcnt lgkmcnt(9)\n\t"
                "v_mfma_f32_32x32x16_bf16 %[a1], v[240:243], v[244:247], %[a1]\n\t"
                "ds_read_b64 v[208:209], %[kb] offset:896\n\t"
                "ds_read_b64 v[210:211], %[kb] offset:904\n\t"
                "ds_read_b128 v[212:215], %[ub] offset:3952\n\t"
                "s_waitcnt lgkmcnt(11)\n\t"
                "v_mfma_f32_32x32x16_bf16 %[a0], v[240:243], v[248:251], %[a0]\n\t"
                "ds_read_b128 v[216:219], %[ub] offset:1392\n\t"
                "s_add_u32 %[t0], %[t1], 245760\n\t"
                "buffer_load_dwordx4 a[60:63], %[go], %[so], %[t0] offen\n\t"
                "s_waitcnt lgkmcnt(10)\n\t"
                "v_mfma_f32_32x32x16_bf16 %[a1], v[252:255], v[248:251], %[a1]\n\t"
                "ds_read_b64 v[220:221], %[kb] offset:2944\n\t"
                "ds_read_b64 v[222:223], %[kb] offset:2952\n\t"
                "s_waitcnt lgkmcnt(9)\n\t"
                "v_mfma_f32_32x32x16_bf16 %[a1], v[192:195], v[196:199], %[a1]\n\t"
                "ds_read_b64 v[224:225], %[kb] offset:992\n\t"
                "ds_read_b64 v[226:227], %[kb] offset:1000\n\t"
                "ds_read_b128 v[228:231], %[ub] offset:3840\n\t"
                "s_waitcnt lgkmcnt(11)\n\t"
                "v_mfma_f32_32x32x16_bf16 %[a0], v[192:195], v[200:203], %[a0]\n\t"
                "ds_read_b128 v[232:235], %[ub] offset:1280\n\t"
                "s_add_u32 %[t1], %[ro], 1\n\t"
                "s_and_b32 %[t1], %[t1], 7\n\t"
                "s_lshl_b32 %[t1], %[t1], 18\n\t"
                "s_add_u32 %[t0], %[t1], 0\n\t"
                "buffer_load_dwordx4 a[64:67], %[go], %[so], %[t0] offen\n\t"
                "s_waitcnt lgkmcnt(10)\n\t"
                "v_mfma_f32_32x32x16_bf16 %[a1], v[204:207], v[200:203], %[a1]\n\t"
                "ds_read_b64 v[236:237], %[kb] offset:3040\n\t"
                "ds_read_b64 v[238:239], %[kb] offset:3048\n\t"
                "s_waitcnt lgkmcnt(9)\n\t"
                "v_mfma_f32_32x32x16_bf16 %[a1], v[208:211], v[212:215], %[a1]\n\t"
                "ds_read_b64 v[240:241], %[kb] offset:960\n\t"
                "ds_read_b64 v[242:243], %[kb] offset:968\n\t"
                "ds_read_b128 v[244:247], %[ub] offset:3872\n\t"
                "s_waitcnt lgkmcnt(11)\n\t"
                "v_mfma_f32_32x32x16_bf16 %[a0], v[208:211], v[216:219], %[a0]\n\t"
                "ds_read_b128 v[248:251], %[ub] offset:1312\n\t"
                "s_waitcnt lgkmcnt(10)\n\t"
                "v_mfma_f32_32x32x16_bf16 %[a1], v[220:223], v[216:219], %[a1]\n\t"
                "ds_read_b64 v[252:253], %[kb] offset:3008\n\t"
                "ds_read_b64 v[254:255], %[kb] offset:3016\n\t"
                "s_waitcnt lgkmcnt(9)\n\t"
                "v_mfma_f32_32x32x16_bf16 %[a1], v[224:227], v[228:231], %[a1]\n\t"
                "ds_read_b64 v[192:193], %[kb] offset:1056\n\t"
                "ds_read_b64 v[194:195], %[kb] offset:1064\n\t"
                "ds_read_b128 v[196:199], %[ub] offset:3760\n\t"
                "s_waitcnt lgkmcnt(11)\n\t"
                "v_mfma_f32_32x32x16_bf16 %[a0], v[224:227], v[232:235], %[a0]\n\t"
                "ds_read_b128 v[200:203], %[ub] offset:1200\n\t"
                "s_add_u32 %[t0], %[t1], 16384\n\t"
                "buffer_load_dwordx4 a[68:71], %[go], %[so], %[t0] offen\n\t"
                "s_waitcnt lgkmcnt(10)\n\t"
                "v_mfma_f32_32x32x16_bf16 %[a1], v[236:239], v[232:235], %[a1]\n\t"
                "ds_read_b64 v[204:205], %[kb] offset:3104\n\t"
                "ds_read_b64 v[206:207], %[kb] offset:3112\n\t"
                "s_waitcnt lgkmcnt(9)\n\t"
                "v_mfma_f32_32x32x16_bf16 %[a1], v[240:243], v[244:247], %[a1]\n\t"
                "ds_read_b64 v[208:209], %[kb] offset:1024\n\t"
                "ds_read_b64 v[210:211], %[kb] offset:1032\n\t"
                "ds_read_b128 v[212:215], %[ub] offset:3792\n\t"
                "s_waitcnt lgkmcnt(11)\n\t"
                "v_mfma_f32_32x32x16_bf16 %[a0], v[240:243], v[248:251], %[a0]\n\t"
                "ds_read_b128 v[216:219], %[ub] offset:1232\n\t"
                "s_waitcnt lgkmcnt(10)\n\t"
                "v_mfma_f32_32x32x16_bf16 %[a1], v[252:255], v[248:251], %[a1]\n\t"
                "ds_read_b64 v[220:221], %[kb] offset:3072\n\t"
                "ds_read_b64 v[222:223], %[kb] offset:3080\n\t"
                "s_waitcnt lgkmcnt(9)\n\t"
                "v_mfma_f32_32x32x16_bf16 %[a1], v[192:195], v[196:199], %[a1]\n\t"
                "ds_read_b64 v[224:225], %[kb] offset:1120\n\t"
                "ds_read_b64 v[226:227], %[kb] offset:1128\n\t"
                "ds_read_b128 v[228:231], %[ub] offset:3680\n\t"
                "s_waitcnt lgkmcnt(11)\n\t"
                "v_mfma_f32_32x32x16_bf16 %[a0], v[192:195], v[200:203], %[a0]\n\t"
                "ds_read_b128 v[232:235], %[ub] offset:1120\n\t"
                "s_add_u32 %[t0], %[t1], 32768\n\t"
                "buffer_load_dwordx4 a[72:75], %[go], %[so], %[t0] offen\n\t"
                "s_waitcnt lgkmcnt(10)\n\t"
                "v_mfma_f32_32x32x16_bf16 %[a1], v[204:207], v[200:203], %[a1]\n\t"
                "ds_read_b64 v[236:237], %[kb] offset:3168\n\t"
                "ds_read_b64 v[238:239], %[kb] offset:3176\n\t"
                "s_waitcnt lgkmcnt(9)\n\t"
                "v_mfma_f32_32x32x16_bf16 %[a1], v[208:211], v[212:215], %[a1]\n\t"
                "ds_read_b64 v[240:241], %[kb] offset:1088\n\t"
                "ds_read_b64 v[242:243], %[kb] offset:1096\n\t"
                "ds_read_b128 v[244:247], %[ub] offset:3712\n\t"
                "s_waitcnt lgkmcnt(11)\n\t"
                "v_mfma_f32_32x32x16_bf16 %[a0], v[208:211], v[216:219], %[a0]\n\t"
                "ds_read_b128 v[248:251], %[ub] offset:1152\n\t"
                "s_waitcnt lgkmcnt(10)\n\t"
                "v_mfma_f32_32x32x16_bf16 %[a1], v[220:223], v[216:219], %[a1]\n\t"
                "ds_read_b64 v[252:253], %[kb] offset:3136\n\t"
                "ds_read_b64 v[254:255], %[kb] offset:3144\n\t"
                "s_waitcnt lgkmcnt(9)\n\t"
                "v_mfma_f32_32x32x16_bf16 %[a1], v[224:227], v[228:231], %[a1]\n\t"
                "ds_read_b64 v[192:193], %[kb] offset:1184\n\t"
                "ds_read_b64 v[194:195], %[kb] offset:1192\n\t"
                "ds_read_b128 v[196:199], %[ub] offset:3600\n\t"
                "s_waitcnt lgkmcnt(11)\n\t"
                "v_mfma_f32_32x32x16_bf16 %[a0], v[224:227], v[232:235], %[a0]\n\t"
                "ds_read_b128 v[200:203], %[ub] offset:1040\n\t"
                "s_add_u32 %[t0], %[t1], 49152\n\t"
                "buffer_load_dwordx4 a[76:79], %[go], %[so], %[t0] offen\n\t"
                "s_waitcnt lgkmcnt(10)\n\t"
                "v_mfma_f32_32x32x16_bf16 %[a1], v[236:239], v[232:235], %[a1]\n\t"
                "ds_read_b64 v[204:205], %[kb] offset:3232\n\t"
                "ds_read_b64 v[206:207], %[kb] offset:3240\n\t"
                "s_waitcnt lgkmcnt(9)\n\t"
                "v_mfma_f32_32x32x16_bf16 %[a1], v[240:243], v[244:247], %[a1]\n\t"
                "ds_read_b64 v[208:209], %[kb] offset:1152\n\t"
                "ds_read_b64 v[210:211], %[kb] offset:1160\n\t"
                "ds_read_b128 v[212:215], %[ub] offset:3632\n\t"
                "s_waitcnt lgkmcnt(11)\n\t"
                "v_mfma_f32_32x32x16_bf16 %[a0], v[240:243], v[248:251], %[a0]\n\t"
                "ds_read_b128 v[216:219], %[ub] offset:1072\n\t"
                "s_add_u32 %[t0], %[t1], 65536\n\t"
                "buffer_load_dwordx4 a[80:83], %[go], %[so], %[t0] offen\n\t"
                "s_waitcnt lgkmcnt(10)\n\t"
                "v_mfma_f32_32x32x16_bf16 %[a1], v[252:255], v[248:251], %[a1]\n\t"
                "ds_read_b64 v[220:221], %[kb] offset:3200\n\t"
                "ds_read_b64 v[222:223], %[kb] offset:3208\n\t"
                "s_waitcnt lgkmcnt(9)\n\t"
                "v_mfma_f32_32x32x16_bf16 %[a1], v[192:195], v[196:199], %[a1]\n\t"
                "ds_read_b64 v[224:225], %[kb] offset:1248\n\t"
                "ds_read_b64 v[226:227], %[kb] offset:1256\n\t"
                "ds_read_b128 v[228:231], %[ub] offset:3520\n\t"
                "s_waitcnt lgkmcnt(11)\n\t"
                "v_mfma_f32_32x32x16_bf16 %[a0], v[192:195], v[200:203], %[a0]\n\t"
                "ds_read_b128 v[232:235], %[ub] offset:960\n\t"
                "s_waitcnt lgkmcnt(10)\n\t"
                "v_mfma_f32_32x32x16_bf16 %[a1], v[204:207], v[200:203], %[a1]\n\t"
                "ds_read_b64 v[236:237], %[kb] offset:3296\n\t"
                "ds_read_b64 v[238:239], %[kb] offset:3304\n\t"
                "s_waitcnt lgkmcnt(9)\n\t"
                "v_mfma_f32_32x32x16_bf16 %[a1], v[208:211], v[212:215], %[a1]\n\t"
                "ds_read_b64 v[240:241], %[kb] offset:1216\n\t"
                "ds_read_b64 v[242:243], %[kb] offset:1224\n\t"
                "ds_read_b128 v[244:247], %[ub] offset:3552\n\t"
                "s_waitcnt lgkmcnt(11)\n\t"
                "v_mfma_f32_32x32x16_bf16 %[a0], v[208:211], v[216:219], %[a0]\n\t"
                "ds_read_b128 v[248:251], %[ub] offset:992\n\t"
                "s_add_u32 %[t0], %[t1], 81920\n\t"
                "buffer_load_dwordx4 a[84:87], %[go], %[so], %[t0] offen\n\t"
                "s_waitcnt lgkmcnt(10)\n\t"
                "v_mfma_f32_32x32x16_bf16 %[a1], v[220:223], v[216:219], %[a1]\n\t"
                "ds_read_b64 v[252:253], %[kb] offset:3264\n\t"
                "ds_read_b64 v[254:255], %[kb] offset:3272\n\t"
                "s_waitcnt lgkmcnt(9)\n\t"
                "v_mfma_f32_32x32x16_bf16 %[a1], v[224:227], v[228:231], %[a1]\n\t"
                "ds_read_b64 v[192:193], %[kb] offset:1312\n\t"
                "ds_read_b64 v[194:195], %[kb] offset:1320\n\t"
                "ds_read_b128 v[196:199], %[ub] offset:3440\n\t"
                "s_waitcnt lgkmcnt(11)\n\t"
                "v_mfma_f32_32x32x16_bf16 %[a0], v[224:227], v[232:235], %[a0]\n\t"
                "ds_read_b128 v[200:203], %[ub] offset:880\n\t"
                "s_waitcnt lgkmcnt(10)\n\t"
                "v_mfma_f32_32x32x16_bf16 %[a1], v[236:239], v[232:235], %[a1]\n\t"
                "ds_read_b64 v[204:205], %[kb] offset:3360\n\t"
                "ds_read_b64 v[206:207], %[kb] offset:3368\n\t"
                "s_waitcnt lgkmcnt(9)\n\t"
                "v_mfma_f32_32x32x16_bf16 %[a1], v[240:243], v[244:247], %[a1]\n\t"
                "ds_read_b64 v[208:209], %[kb] offset:1280\n\t"
                "ds_read_b64 v[210:211], %[kb] offset:1288\n\t"
                "ds_read_b128 v[212:215], %[ub] offset:3472\n\t"
                "s_waitcnt lgkmcnt(11)\n\t"
                "v_mfma_f32_32x32x16_bf16 %[a0], v[240:243], v[248:251], %[a0]\n\t"
                "ds_read_b128 v[216:219], %[ub] offset:912\n\t"
                "s_add_u32 %[t0], %[t1], 98304\n\t"
                "buffer_load_dwordx4 a[88:91], %[go], %[so], %[t0] offen\n\t"
                "s_waitcnt lgkmcnt(10)\n\t"
                "v_mfma_f32_32x32x16_bf16 %[a1], v[252:255], v[248:251], %[a1]\n\t"
                "ds_read_b64 v[220:221], %[kb] offset:3328\n\t"
                "ds_read_b64 v[222:223], %[kb] offset:3336\n\t"
                "s_waitcnt lgkmcnt(9)\n\t"
                "v_mfma_f32_32x32x16_bf16 %[a1], v[192:195], v[196:199], %[a1]\n\t"
                "ds_read_b64 v[224:225], %[kb] offset:1376\n\t"
                "ds_read_b64 v[226:227], %[kb] offset:1384\n\t"
                "ds_read_b128 v[228:231], %[ub] offset:3360\n\t"
                "s_waitcnt lgkmcnt(11)\n\t"
                "v_mfma_f32_32x32x16_bf16 %[a0], v[192:195], v[200:203], %[a0]\n\t"
                "ds_read_b128 v[232:235], %[ub] offset:800\n\t"
                "s_waitcnt lgkmcnt(10)\n\t"
                "v_mfma_f32_32x32x16_bf16 %[a1], v[204:207], v[200:203], %[a1]\n\t"
                "ds_read_b64 v[236:237], %[kb] offset:3424\n\t"
                "ds_read_b64 v[238:239], %[kb] offset:3432\n\t"
                "s_waitcnt lgkmcnt(9)\n\t"
                "v_mfma_f32_32x32x16_bf16 %[a1], v[208:211], v[212:215], %[a1]\n\t"
                "ds_read_b64 v[240:241], %[kb] offset:1344\n\t"
                "ds_read_b64 v[242:243], %[kb] offset:1352\n\t"
                "ds_read_b128 v[244:247], %[ub] offset:3392\n\t"
                "s_waitcnt lgkmcnt(11)\n\t"
                "v_mfma_f32_32x32x16_bf16 %[a0], v[208:211], v[216:219], %[a0]\n\t"
                "ds_read_b128 v[248:251], %[ub] offset:832\n\t"
                "s_add_u32 %[t0], %[t1], 114688\n\t"
                "buffer_load_dwordx4 a[92:95], %[go], %[so], %[t0] offen\n\t"
                "s_waitcnt lgkmcnt(10)\n\t"
                "v_mfma_f32_32x32x16_bf16 %[a1], v[220:223], v[216:219], %[a1]\n\t"
                "ds_read_b64 v[252:253], %[kb] offset:3392\n\t"
                "ds_read_b64 v[254:255], %[kb] offset:3400\n\t"
                "s_waitcnt lgkmcnt(9)\n\t"
                "v_mfma_f32_32x32x16_bf16 %[a1], v[224:227], v[228:231], %[a1]\n\t"
                "ds_read_b64 v[192:193], %[kb] offset:1440\n\t"
                "ds_read_b64 v[194:195], %[kb] offset:1448\n\t"
                "ds_read_b128 v[196:199], %[ub] offset:3280\n\t"
                "s_waitcnt lgkmcnt(11)\n\t"
                "v_mfma_f32_32x32x16_bf16 %[a0], v[224:227], v[232:235], %[a0]\n\t"
                "ds_read_b128 v[200:203], %[ub] offset:720\n\t"
                "s_add_u32 %[t1], %[ro], 1\n\t"
                "s_and_b32 %[t1], %[t1], 7\n\t"
                "s_lshl_b32 %[t1], %[t1], 18\n\t"
                "s_add_u32 %[t0], %[t1], 131072\n\t"
                "buffer_load_dwordx4 a[96:99], %[go], %[so], %[t0] offen\n\t"
                "s_waitcnt lgkmcnt(10)\n\t"
                "v_mfma_f32_32x32x16_bf16 %[a1], v[236:239], v[232:235], %[a1]\n\t"
                "ds_read_b64 v[204:205], %[kb] offset:3488\n\t"
                "ds_read_b64 v[206:207], %[kb] offset:3496\n\t"
                "s_waitcnt lgkmcnt(9)\n\t"
                "v_mfma_f32_32x32x16_bf16 %[a1], v[240:243], v[244:247], %[a1]\n\t"
                "ds_read_b64 v[208:209], %[kb] offset:1408\n\t"
                "ds_read_b64 v[210:211], %[kb] offset:1416\n\t"
                "ds_read_b128 v[212:215], %[ub] offset:3312\n\t"
                "s_waitcnt lgkmcnt(11)\n\t"
                "v_mfma_f32_32x32x16_bf16 %[a0], v[240:243], v[248:251], %[a0]\n\t"
                "ds_read_b128 v[216:219], %[ub] offset:752\n\t"
                "s_waitcnt lgkmcnt(10)\n\t"
                "v_mfma_f32_32x32x16_bf16 %[a1], v[252:255], v[248:251], %[a1]\n\t"
                "ds_read_b64 v[220:221], %[kb] offset:3456\n\t"
                "ds_read_b64 v[222:223], %[kb] offset:3464\n\t"
                "s_waitcnt lgkmcnt(9)\n\t"
                "v_mfma_f32_32x32x16_bf16 %[a1], v[192:195], v[196:199], %[a1]\n\t"
                "ds_read_b64 v[224:225], %[kb] offset:1504\n\t"
                "ds_read_b64 v[226:227], %[kb] offset:1512\n\t"
                "ds_read_b128 v[228:231], %[ub] offset:3200\n\t"
                "s_waitcnt lgkmcnt(11)\n\t"
                "v_mfma_f32_32x32x16_bf16 %[a0], v[192:195], v[200:203], %[a0]\n\t"
                "ds_read_b128 v[232:235], %[ub] offset:640\n\t"
                "s_add_u32 %[t0], %[t1], 147456\n\t"
                "buffer_load_dwordx4 a[100:103], %[go], %[so], %[t0] offen\n\t"
                "s_waitcnt lgkmcnt(10)\n\t"
                "v_mfma_f32_32x32x16_bf16 %[a1], v[204:207], v[200:203], %[a1]\n\t"
                "ds_read_b64 v[236:237], %[kb] offset:3552\n\t"
                "ds_read_b64 v[238:239], %[kb] offset:3560\n\t"
                "s_waitcnt lgkmcnt(9)\n\t"
                "v_mfma_f32_32x32x16_bf16 %[a1], v[208:211], v[212:215], %[a1]\n\t"
                "ds_read_b64 v[240:241], %[kb] offset:1472\n\t"
                "ds_read_b64 v[242:243], %[kb] offset:1480\n\t"
                "ds_read_b128 v[244:247], %[ub] offset:3232\n\t"
                "s_waitcnt lgkmcnt(11)\n\t"
                "v_mfma_f32_32x32x16_bf16 %[a0], v[208:211], v[216:219], %[a0]\n\t"
                "ds_read_b128 v[248:251], %[ub] offset:672\n\t"
                "s_waitcnt lgkmcnt(10)\n\t"
                "v_mfma_f32_32x32x16_bf16 %[a1], v[220:223], v[216:219], %[a1]\n\t"
                "ds_read_b64 v[252:253], %[kb] offset:3520\n\t"
                "ds_read_b64 v[254:255], %[kb] offset:3528\n\t"
                "s_waitcnt lgkmcnt(9)\n\t"
                "v_mfma_f32_32x32x16_bf16 %[a1], v[224:227], v[228:231], %[a1]\n\t"
                "ds_read_b64 v[192:193], %[kb] offset:1568\n\t"
                "ds_read_b64 v[194:195], %[kb] offset:1576\n\t"
                "ds_read_b128 v[196:199], %[ub] offset:3120\n\t"
                "s_waitcnt lgkmcnt(11)\n\t"
                "v_mfma_f32_32x32x16_bf16 %[a0], v[224:227], v[232:235], %[a0]\n\t"
                "ds_read_b128 v[200:203], %[ub] offset:560\n\t"
                "s_add_u32 %[t0], %[t1], 163840\n\t"
                "buffer_load_dwordx4 a[104:107], %[go], %[so], %[t0] offen\n\t"
                "s_waitcnt lgkmcnt(10)\n\t"
                "v_mfma_f32_32x32x16_bf16 %[a1], v[236:239], v[232:235], %[a1]\n\t"
                "ds_read_b64 v[204:205], %[kb] offset:3616\n\t"
                "ds_read_b64 v[206:207], %[kb] offset:3624\n\t"
                "s_waitcnt lgkmcnt(9)\n\t"
                "v_mfma_f32_32x32x16_bf16 %[a1], v[240:243], v[244:247], %[a1]\n\t"
                "ds_read_b64 v[208:209], %[kb] offset:1536\n\t"
                "ds_read_b64 v[210:211], %[kb] offset:1544\n\t"
                "ds_read_b128 v[212:215], %[ub] offset:3152\n\t"
                "s_waitcnt lgkmcnt(11)\n\t"
                "v_mfma_f32_32x32x16_bf16 %[a0], v[240:243], v[248:251], %[a0]\n\t"
                "ds_read_b128 v[216:219], %[ub] offset:592\n\t"
                "s_waitcnt lgkmcnt(10)\n\t"
                "v_mfma_f32_32x32x16_bf16 %[a1], v[252:255], v[248:251], %[a1]\n\t"
                "ds_read_b64 v[220:221], %[kb] offset:3584\n\t"
                "ds_read_b64 v[222:223], %[kb] offset:3592\n\t"
                "s_waitcnt lgkmcnt(9)\n\t"
                "v_mfma_f32_32x32x16_bf16 %[a1], v[192:195], v[196:199], %[a1]\n\t"
                "ds_read_b64 v[224:225], %[kb] offset:1632\n\t"
                "ds_read_b64 v[226:227], %[kb] offset:1640\n\t"
                "ds_read_b128 v[228:231], %[ub] offset:3040\n\t"
                "s_waitcnt lgkmcnt(11)\n\t"
                "v_mfma_f32_32x32x16_bf16 %[a0], v[192:195], v[200:203], %[a0]\n\t"
                "ds_read_b128 v[232:235], %[ub] offset:480\n\t"
                "s_add_u32 %[t0], %[t1], 180224\n\t"
                "buffer_load_dwordx4 a[108:111], %[go], %[so], %[t0] offen\n\t"
                "s_waitcnt lgkmcnt(10)\n\t"
                "v_mfma_f32_32x32x16_bf16 %[a1], v[204:207], v[200:203], %[a1]\n\t"
                "ds_read_b64 v[236:237], %[kb] offset:3680\n\t"
                "ds_read_b64 v[238:239], %[kb] offset:3688\n\t"
                "s_waitcnt lgkmcnt(9)\n\t"
                "v_mfma_f32_32x32x16_bf16 %[a1], v[208:211], v[212:215], %[a1]\n\t"
                "ds_read_b64 v[240:241], %[kb] offset:1600\n\t"
                "ds_read_b64 v[242:243], %[kb] offset:1608\n\t"
                "ds_read_b128 v[244:247], %[ub] offset:3072\n\t"
                "s_waitcnt lgkmcnt(11)\n\t"
                "v_mfma_f32_32x32x16_bf16 %[a0], v[208:211], v[216:219], %[a0]\n\t"
                "ds_read_b128 v[248:251], %[ub] offset:512\n\t"
                "s_add_u32 %[t0], %[t1], 196608\n\t"
                "buffer_load_dwordx4 a[112:115], %[go], %[so], %[t0] offen\n\t"
                "s_waitcnt lgkmcnt(10)\n\t"
                "v_mfma_f32_32x32x16_bf16 %[a1], v[220:223], v[216:219], %[a1]\n\t"
                "ds_read_b64 v[252:253], %[kb] offset:3648\n\t"
                "ds_read_b64 v[254:255], %[kb] offset:3656\n\t"
                "s_waitcnt lgkmcnt(9)\n\t"
                "v_mfma_f32_32x32x16_bf16 %[a1], v[224:227], v[228:231], %[a1]\n\t"
                "ds_read_b64 v[192:193], %[kb] offset:1696\n\t"
                "ds_read_b64 v[194:195], %[kb] offset:1704\n\t"
                "ds_read_b128 v[196:199], %[ub] offset:2960\n\t"
                "s_waitcnt lgkmcnt(11)\n\t"
                "v_mfma_f32_32x32x16_bf16 %[a0], v[224:227], v[232:235], %[a0]\n\t"
                "ds_read_b128 v[200:203], %[ub] offset:400\n\t"
                "s_waitcnt lgkmcnt(10)\n\t"
                "v_mfma_f32_32x32x16_bf16 %[a1], v[236:239], v[232:235], %[a1]\n\t"
                "ds_read_b64 v[204:205], %[kb] offset:3744\n\t"
                "ds_read_b64 v[206:207], %[kb] offset:3752\n\t"
                "s_waitcnt lgkmcnt(9)\n\t"
                "v_mfma_f32_32x32x16_bf16 %[a1], v[240:243], v[244:247], %[a1]\n\t"
                "ds_read_b64 v[208:209], %[kb] offset:1664\n\t"
                "ds_read_b64 v[210:211], %[kb] offset:1672\n\t"
                "ds_read_b128 v[212:215], %[ub] offset:2992\n\t"
                "s_waitcnt lgkmcnt(11)\n\t"
                "v_mfma_f32_32x32x16_bf16 %[a0], v[240:243], v[248:251], %[a0]\n\t"
                "ds_read_b128 v[216:219], %[ub] offset:432\n\t"
                "s_add_u32 %[t0], %[t1], 212992\n\t"
                "buffer_load_dwordx4 a[116:119], %[go], %[so], %[t0] offen\n\t"
                "s_waitcnt lgkmcnt(10)\n\t"
                "v_mfma_f32_32x32x16_bf16 %[a1], v[252:255], v[248:251], %[a1]\n\t"
                "ds_read_b64 v[220:221], %[kb] offset:3712\n\t"
                "ds_read_b64 v[222:223], %[kb] offset:3720\n\t"
                "s_waitcnt lgkmcnt(9)\n\t"
                "v_mfma_f32_32x32x16_bf16 %[a1], v[192:195], v[196:199], %[a1]\n\t"
                "ds_read_b64 v[224:225], %[kb] offset:1760\n\t"
                "ds_read_b64 v[226:227], %[kb] offset:1768\n\t"
                "ds_read_b128 v[228:231], %[ub] offset:2880\n\t"
                "s_waitcnt lgkmcnt(11)\n\t"
                "v_mfma_f32_32x32x16_bf16 %[a0], v[192:195], v[200:203], %[a0]\n\t"
                "ds_read_b128 v[232:235], %[ub] offset:320\n\t"
                "s_waitcnt lgkmcnt(10)\n\t"
                "v_mfma_f32_32x32x16_bf16 %[a1], v[204:207], v[200:203], %[a1]\n\t"
                "ds_read_b64 v[236:237], %[kb] offset:3808\n\t"
                "ds_read_b64 v[238:239], %[kb] offset:3816\n\t"
                "s_waitcnt lgkmcnt(9)\n\t"
                "v_mfma_f32_32x32x16_bf16 %[a1], v[208:211], v[212:215], %[a1]\n\t"
                "ds_read_b64 v[240:241], %[kb] offset:1728\n\t"
                "ds_read_b64 v[242:243], %[kb] offset:1736\n\t"
                "ds_read_b128 v[244:247], %[ub] offset:2912\n\t"
                "s_waitcnt lgkmcnt(11)\n\t"
                "v_mfma_f32_32x32x16_bf16 %[a0], v[208:211], v[216:219], %[a0]\n\t"
                "ds_read_b128 v[248:251], %[ub] offset:352\n\t"
                "s_add_u32 %[t0], %[t1], 229376\n\t"
                "buffer_load_dwordx4 a[120:123], %[go], %[so], %[t0] offen\n\t"
                "s_waitcnt lgkmcnt(10)\n\t"
                "v_mfma_f32_32x32x16_bf16 %[a1], v[220:223], v[216:219], %[a1]\n\t"
                "ds_read_b64 v[252:253], %[kb] offset:3776\n\t"
                "ds_read_b64 v[254:255], %[kb] offset:3784\n\t"
                "s_waitcnt lgkmcnt(9)\n\t"
                "v_mfma_f32_32x32x16_bf16 %[a1], v[224:227], v[228:231], %[a1]\n\t"
                "ds_read_b64 v[192:193], %[kb] offset:1824\n\t"
                "ds_read_b64 v[194:195], %[kb] offset:1832\n\t"
                "ds_read_b128 v[196:199], %[ub] offset:2800\n\t"
                "s_waitcnt lgkmcnt(11)\n\t"
                "v_mfma_f32_32x32x16_bf16 %[a0], v[224:227], v[232:235], %[a0]\n\t"
                "ds_read_b128 v[200:203], %[ub] offset:240\n\t"
                "s_waitcnt lgkmcnt(10)\n\t"
                "v_mfma_f32_32x32x16_bf16 %[a1], v[236:239], v[232:235], %[a1]\n\t"
                "ds_read_b64 v[204:205], %[kb] offset:3872\n\t"
                "ds_read_b64 v[206:207], %[kb] offset:3880\n\t"
                "s_waitcnt lgkmcnt(9)\n\t"
                "v_mfma_f32_32x32x16_bf16 %[a1], v[240:243], v[244:247], %[a1]\n\t"
                "ds_read_b64 v[208:209], %[kb] offset:1792\n\t"
                "ds_read_b64 v[210:211], %[kb] offset:1800\n\t"
                "ds_read_b128 v[212:215], %[ub] offset:2832\n\t"
                "s_waitcnt lgkmcnt(11)\n\t"
                "v_mfma_f32_32x32x16_bf16 %[a0], v[240:243], v[248:251], %[a0]\n\t"
                "ds_read_b128 v[216:219], %[ub] offset:272\n\t"
                "s_add_u32 %[t0], %[t1], 245760\n\t"
                "buffer_load_dwordx4 a[124:127], %[go], %[so], %[t0] offen\n\t"
                "s_waitcnt lgkmcnt(10)\n\t"
                "v_mfma_f32_32x32x16_bf16 %[a1], v[252:255], v[248:251], %[a1]\n\t"
                "ds_read_b64 v[220:221], %[kb] offset:3840\n\t"
                "ds_read_b64 v[222:223], %[kb] offset:3848\n\t"
                "s_waitcnt lgkmcnt(9)\n\t"
                "v_mfma_f32_32x32x16_bf16 %[a1], v[192:195], v[196:199], %[a1]\n\t"
                "ds_read_b64 v[224:225], %[kb] offset:1888\n\t"
                "ds_read_b64 v[226:227], %[kb] offset:1896\n\t"
                "ds_read_b128 v[228:231], %[ub] offset:2720\n\t"
                "s_waitcnt lgkmcnt(11)\n\t"
                "v_mfma_f32_32x32x16_bf16 %[a0], v[192:195], v[200:203], %[a0]\n\t"
                "ds_read_b128 v[232:235], %[ub] offset:160\n\t"
                "s_waitcnt lgkmcnt(10)\n\t"
                "v_mfma_f32_32x32x16_bf16 %[a1], v[204:207], v[200:203], %[a1]\n\t"
                "ds_read_b64 v[236:237], %[kb] offset:3936\n\t"
                "ds_read_b64 v[238:239], %[kb] offset:3944\n\t"
                "s_waitcnt lgkmcnt(9)\n\t"
                "v_mfma_f32_32x32x16_bf16 %[a1], v[208:211], v[212:215], %[a1]\n\t"
                "ds_read_b64 v[240:241], %[kb] offset:1856\n\t"
                "ds_read_b64 v[242:243], %[kb] offset:1864\n\t"
                "ds_read_b128 v[244:247], %[ub] offset:2752\n\t"
                "s_waitcnt lgkmcnt(11)\n\t"
                "v_mfma_f32_32x32x16_bf16 %[a0], v[208:211], v[216:219], %[a0]\n\t"
                "ds_read_b128 v[248:251], %[ub] offset:192\n\t"
                "s_waitcnt lgkmcnt(10)\n\t"
                "v_mfma_f32_32x32x16_bf16 %[a1], v[220:223], v[216:219], %[a1]\n\t"
                "ds_read_b64 v[252:253], %[kb] offset:3904\n\t"
                "ds_read_b64 v[254:255], %[kb] offset:3912\n\t"
                "s_waitcnt lgkmcnt(9)\n\t"
                "v_mfma_f32_32x32x16_bf16 %[a1], v[224:227], v[228:231], %[a1]\n\t"
                "ds_read_b64 v[192:193], %[kb] offset:1952\n\t"
                "ds_read_b64 v[194:195], %[kb] offset:1960\n\t"
                "ds_read_b128 v[196:199], %[ub] offset:2640\n\t"
                "s_waitcnt lgkmcnt(11)\n\t"
                "v_mfma_f32_32x32x16_bf16 %[a0], v[224:227], v[232:235], %[a0]\n\t"
                "ds_read_b128 v[200:203], %[ub] offset:80\n\t"
                "s_waitcnt lgkmcnt(10)\n\t"
                "v_mfma_f32_32x32x16_bf16 %[a1], v[236:239], v[232:235], %[a1]\n\t"
                "ds_read_b64 v[204:205], %[kb] offset:4000\n\t"
                "ds_read_b64 v[206:207], %[kb] offset:4008\n\t"
                "s_waitcnt lgkmcnt(9)\n\t"
                "v_mfma_f32_32x32x16_bf16 %[a1], v[240:243], v[244:247], %[a1]\n\t"
                "ds_read_b64 v[208:209], %[kb] offset:1920\n\t"
                "ds_read_b64 v[210:211], %[kb] offset:1928\n\t"
                "ds_read_b128 v[212:215], %[ub] offset:2672\n\t"
                "s_waitcnt lgkmcnt(11)\n\t"
                "v_mfma_f32_32x32x16_bf16 %[a0], v[240:243], v[248:251], %[a0]\n\t"
                "ds_read_b128 v[216:219], %[ub] offset:112\n\t"
                "s_waitcnt lgkmcnt(10)\n\t"
                "v_mfma_f32_32x32x16_bf16 %[a1], v[252:255], v[248:251], %[a1]\n\t"
                "ds_read_b64 v[220:221], %[kb] offset:3968\n\t"
                "ds_read_b64 v[222:223], %[kb] offset:3976\n\t"
                "s_waitcnt lgkmcnt(9)\n\t"
                "v_mfma_f32_32x32x16_bf16 %[a1], v[192:195], v[196:199], %[a1]\n\t"
                "ds_read_b64 v[224:225], %[kb] offset:2016\n\t"
                "ds_read_b64 v[226:227], %[kb] offset:2024\n\t"
                "ds_read_b128 v[228:231], %[ub] offset:2560\n\t"
                "s_waitcnt lgkmcnt(11)\n\t"
                "v_mfma_f32_32x32x16_bf16 %[a0], v[192:195], v[200:203], %[a0]\n\t"
                "ds_read_b128 v[232:235], %[ub] offset:0\n\t"
                "s_waitcnt lgkmcnt(10)\n\t"
                "v_mfma_f32_32x32x16_bf16 %[a1], v[204:207], v[200:203], %[a1]\n\t"
                "ds_read_b64 v[236:237], %[kb] offset:4064\n\t"
                "ds_read_b64 v[238:239], %[kb] offset:4072\n\t"
                "s_waitcnt lgkmcnt(9)\n\t"
                "v_mfma_f32_32x32x16_bf16 %[a1], v[208:211], v[212:215], %[a1]\n\t"
                "ds_read_b64 v[240:241], %[kb] offset:1984\n\t"
                "ds_read_b64 v[242:243], %[kb] offset:1992\n\t"
                "ds_read_b128 v[244:247], %[ub] offset:2592\n\t"
                "s_waitcnt lgkmcnt(11)\n\t"
                "v_mfma_f32_32x32x16_bf16 %[a0], v[208:211], v[216:219], %[a0]\n\t"
                "ds_read_b128 v[248:251], %[ub] offset:32\n\t"
                "s_waitcnt lgkmcnt(10)\n\t"
                "v_mfma_f32_32x32x16_bf16 %[a1], v[220:223], v[216:219], %[a1]\n\t"
                "ds_read_b64 v[252:253], %[kb] offset:4032\n\t"
                "ds_read_b64 v[254:255], %[kb] offset:4040\n\t"
                "s_waitcnt lgkmcnt(9)\n\t"
                "v_mfma_f32_32x32x16_bf16 %[a1], v[224:227], v[228:231], %[a1]\n\t"
                "s_waitcnt lgkmcnt(8)\n\t"
                "v_mfma_f32_32x32x16_bf16 %[a0], v[224:227], v[232:235], %[a0]\n\t"
                "s_waitcnt lgkmcnt(6)\n\t"
                "v_mfma_f32_32x32x16_bf16 %[a1], v[236:239], v[232:235], %[a1]\n\t"
                "s_waitcnt lgkmcnt(3)\n\t"
                "v_mfma_f32_32x32x16_bf16 %[a1], v[240:243], v[244:247], %[a1]\n\t"
                "s_waitcnt lgkmcnt(2)\n\t"
                "v_mfma_f32_32x32x16_bf16 %[a0], v[240:243], v[248:251], %[a0]\n\t"
                "s_waitcnt lgkmcnt(0)\n\t"
                "v_mfma_f32_32x32x16_bf16 %[a1], v[252:255], v[248:251], %[a1]\n\t"
                "s_waitcnt vmcnt(0)\n\t"
                "s_nop 7\n\t"
                "s_nop 3\n\t"
                : [a0] "=&v"(acc0), [a1] "=&v"(acc1), [t0] "=&s"(t0), [t1] "=&s"(t1)
                : [kb] "v"(kb), [ub] "v"(ub0 + c8 * CV_U), [go] "v"(go), [so] "s"(so), [ro] "s"(ro) : "memory", "scc", "v192", "v193", "v194", "v195", "v196", "v197", "v198", "v199", "v200", "v201", "v202", "v203", "v204", "v205", "v206", "v207", "v208", "v209", "v210", "v211", "v212", "v213", "v214", "v215", "v216", "v217", "v218", "v219", "v220", "v221", "v222", "v223", "v224", "v225", "v226", "v227", "v228", "v229", "v230", "v231", "v232", "v233", "v234", "v235", "v236", "v237", "v238", "v239", "v240", "v241", "v242", "v243", "v244", "v245", "v246", "v247", "v248", "v249", "v250", "v251", "v252", "v253", "v254", "v255", "a0", "a1", "a2", "a3", "a4", "a5", "a6", "a7", "a8", "a9", "a10", "a11", "a12", "a13", "a14", "a15", "a16", "a17", "a18", "a19", "a20", "a21", "a22", "a23", "a24", "a25", "a26", "a27", "a28", "a29", "a30", "a31", "a32", "a33", "a34", "a35", "a36", "a37", "a38", "a39", "a40", "a41", "a42", "a43", "a44", "a45", "a46", "a47", "a48", "a49", "a50", "a51", "a52", "a53", "a54", "a55", "a56", "a57", "a58", "a59", "a60", "a61", "a62", "a63", "a64", "a65", "a66", "a67", "a68", "a69", "a70", "a71", "a72", "a73", "a74", "a75", "a76", "a77", "a78", "a79", "a80", "a81", "a82", "a83", "a84", "a85", "a86", "a87", "a88", "a89", "a90", "a91", "a92", "a93", "a94", "a95", "a96", "a97", "a98", "a99", "a100", "a101", "a102", "a103", "a104", "a105", "a106", "a107", "a108", "a109", "a110", "a111", "a112", "a113", "a114", "a115", "a116", "a117", "a118", "a119", "a120", "a121", "a122", "a123", "a124", "a125", "a126", "a127", "a128", "a129", "a130", "a131", "a132", "a133", "a134", "a135", "a136", "a137", "a138", "a139", "a140", "a141", "a142", "a143", "a144", "a145", "a146", "a147", "a148", "a149", "a150", "a151", "a152", "a153", "a154", "a155", "a156", "a157", "a158", "a159", "a160", "a161", "a162", "a163", "a164", "a165", "a166", "a167", "a168", "a169", "a170", "a171", "a172", "a173", "a174", "a175", "a176", "a177", "a178", "a179", "a180", "a181", "a182", "a183", "a184", "a185", "a186", "a187", "a188", "a189", "a190", "a191", "a192", "a193", "a194", "a195", "a196", "a197", "a198", "a199", "a200", "a201", "a202", "a203", "a204", "a205", "a206", "a207", "a208", "a209", "a210", "a211", "a212", "a213", "a214", "a215", "a216", "a217", "a218", "a219", "a220", "a221", "a222", "a223", "a224", "a225", "a226", "a227", "a228", "a229", "a230", "a231", "a232", "a233", "a234", "a235", "a236", "a237", "a238", "a239", "a240", "a241", "a242", "a243", "a244", "a245", "a246", "a247", "a248", "a249", "a250", "a251", "a252", "a253", "a254", "a255");
            else asm volatile(
                "ds_read_b64 v[192:193], %[kb] offset:32\n\t"
                "ds_read_b64 v[194:195], %[kb] offset:40\n\t"
                "ds_read_b128 v[196:199], %[ub] offset:5040\n\t"
                "ds_read_b128 v[200:203], %[ub] offset:2480\n\t"
                "ds_read_b64 v[204:205], %[kb] offset:2080\n\t"
                "ds_read_b64 v[206:207], %[kb] offset:2088\n\t"
                "ds_read_b64 v[208:209], %[kb] offset:0\n\t"
                "ds_read_b64 v[210:211], %[kb] offset:8\n\t"
                "ds_read_b128 v[212:215], %[ub] offset:5072\n\t"
                "ds_read_b128 v[216:219], %[ub] offset:2512\n\t"
                "ds_read_b64 v[220:221], %[kb] offset:2048\n\t"
                "ds_read_b64 v[222:223], %[kb] offset:2056\n\t"
                "s_waitcnt lgkmcnt(9)\n\t"
                "v_mfma_f32_32x32x16_bf16 %[a1], v[192:195], v[196:199], 0\n\t"
                "ds_read_b64 v[224:225], %[kb] offset:96\n\t"
                "ds_read_b64 v[226:227], %[kb] offset:104\n\t"
                "ds_read_b128 v[228:231], %[ub] offset:4960\n\t"
                "s_waitcnt lgkmcnt(11)\n\t"
                "v_mfma_f32_32x32x16_bf16 %[a0], v[192:195], v[200:203], 0\n\t"
                "ds_read_b128 v[232:235], %[ub] offset:2400\n\t"
                "s_add_u32 %[t1], %[ro], 2\n\t"
                "s_and_b32 %[t1], %[t1], 7\n\t"
                "s_lshl_b32 %[t1], %[t1], 18\n\t"
                "s_add_u32 %[t0], %[t1], 0\n\t"
                "buffer_load_dwordx4 a[128:131], %[go], %[so], %[t0] offen\n\t"
                "s_waitcnt lgkmcnt(10)\n\t"
                "v_mfma_f32_32x32x16_bf16 %[a1], v[204:207], v[200:203], %[a1]\n\t"
                "ds_read_b64 v[236:237], %[kb] offset:2144\n\t"
                "ds_read_b64 v[238:239], %[kb] offset:2152\n\t"
                "s_waitcnt lgkmcnt(9)\n\t"
                "v_mfma_f32_32x32x16_bf16 %[a1], v[208:211], v[212:215], %[a1]\n\t"
                "ds_read_b64 v[240:241], %[kb] offset:64\n\t"
                "ds_read_b64 v[242:243], %[kb] offset:72\n\t"
                "ds_read_b128 v[244:247], %[ub] offset:4992\n\t"
                "s_waitcnt lgkmcnt(11)\n\t"
                "v_mfma_f32_32x32x16_bf16 %[a0], v[208:211], v[216:219], %[a0]\n\t"
                "ds_read_b128 v[248:251], %[ub] offset:2432\n\t"
                "s_waitcnt lgkmcnt(10)\n\t"
                "v_mfma_f32_32x32x16_bf16 %[a1], v[220:223], v[216:219], %[a1]\n\t"
                "ds_read_b64 v[252:253], %[kb] offset:2112\n\t"
                "ds_read_b64 v[254:255], %[kb] offset:2120\n\t"
                "s_waitcnt lgkmcnt(9)\n\t"
                "v_mfma_f32_32x32x16_bf16 %[a1], v[224:227], v[228:231], %[a1]\n\t"
                "ds_read_b64 v[192:193], %[kb] offset:160\n\t"
                "ds_read_b64 v[194:195], %[kb] offset:168\n\t"
                "ds_read_b128 v[196:199], %[ub] offset:4880\n\t"
                "s_waitcnt lgkmcnt(11)\n\t"
                "v_mfma_f32_32x32x16_bf16 %[a0], v[224:227], v[232:235], %[a0]\n\t"
                "ds_read_b128 v[200:203], %[ub] offset:2320\n\t"
                "s_add_u32 %[t0], %[t1], 16384\n\t"
                "buffer_load_dwordx4 a[132:135], %[go], %[so], %[t0] offen\n\t"
                "s_waitcnt lgkmcnt(10)\n\t"
                "v_mfma_f32_32x32x16_bf16 %[a1], v[236:239], v[232:235], %[a1]\n\t"
                "ds_read_b64 v[204:205], %[kb] offset:2208\n\t"
                "ds_read_b64 v[206:207], %[kb] offset:2216\n\t"
                "s_waitcnt lgkmcnt(9)\n\t"
                "v_mfma_f32_32x32x16_bf16 %[a1], v[240:243], v[244:247], %[a1]\n\t"
                "ds_read_b64 v[208:209], %[kb] offset:128\n\t"
                "ds_read_b64 v[210:211], %[kb] offset:136\n\t"
                "ds_read_b128 v[212:215], %[ub] offset:4912\n\t"
                "s_waitcnt lgkmcnt(11)\n\t"
                "v_mfma_f32_32x32x16_bf16 %[a0], v[240:243], v[248:251], %[a0]\n\t"
                "ds_read_b128 v[216:219], %[ub] offset:2352\n\t"
                "s_waitcnt lgkmcnt(10)\n\t"
                "v_mfma_f32_32x32x16_bf16 %[a1], v[252:255], v[248:251], %[a1]\n\t"
                "ds_read_b64 v[220:221], %[kb] offset:2176\n\t"
                "ds_read_b64 v[222:223], %[kb] offset:2184\n\t"
                "s_waitcnt lgkmcnt(9)\n\t"
                "v_mfma_f32_32x32x16_bf16 %[a1], v[192:195], v[196:199], %[a1]\n\t"
                "ds_read_b64 v[224:225], %[kb] offset:224\n\t"
                "ds_read_b64 v[226:227], %[kb] offset:232\n\t"
                "ds_read_b128 v[228:231], %[ub] offset:4800\n\t"
                "s_waitcnt lgkmcnt(11)\n\t"
                "v_mfma_f32_32x32x16_bf16 %[a0], v[192:195], v[200:203], %[a0]\n\t"
                "ds_read_b128 v[232:235], %[ub] offset:2240\n\t"
                "s_add_u32 %[t0], %[t1], 32768\n\t"
                "buffer_load_dwordx4 a[136:139], %[go], %[so], %[t0] offen\n\t"
                "s_waitcnt lgkmcnt(10)\n\t"
                "v_mfma_f32_32x32x16_bf16 %[a1], v[204:207], v[200:203], %[a1]\n\t"
                "ds_read_b64 v[236:237], %[kb] offset:2272\n\t"
                "ds_read_b64 v[238:239], %[kb] offset:2280\n\t"
                "s_waitcnt lgkmcnt(9)\n\t"
                "v_mfma_f32_32x32x16_bf16 %[a1], v[208:211], v[212:215], %[a1]\n\t"
                "ds_read_b64 v[240:241], %[kb] offset:192\n\t"
                "ds_read_b64 v[242:243], %[kb] offset:200\n\t"
                "ds_read_b128 v[244:247], %[ub] offset:4832\n\t"
                "s_waitcnt lgkmcnt(11)\n\t"
                "v_mfma_f32_32x32x16_bf16 %[a0], v[208:211], v[216:219], %[a0]\n\t"
                "ds_read_b128 v[248:251], %[ub] offset:2272\n\t"
                "s_waitcnt lgkmcnt(10)\n\t"
                "v_mfma_f32_32x32x16_bf16 %[a1], v[220:223], v[216:219], %[a1]\n\t"
                "ds_read_b64 v[252:253], %[kb] offset:2240\n\t"
                "ds_read_b64 v[254:255], %[kb] offset:2248\n\t"
                "s_waitcnt lgkmcnt(9)\n\t"
                "v_mfma_f32_32x32x16_bf16 %[a1], v[224:227], v[228:231], %[a1]\n\t"
                "ds_read_b64 v[192:193], %[kb] offset:288\n\t"
                "ds_read_b64 v[194:195], %[kb] offset:296\n\t"
                "ds_read_b128 v[196:199], %[ub] offset:4720\n\t"
                "s_waitcnt lgkmcnt(11)\n\t"
                "v_mfma_f32_32x32x16_bf16 %[a0], v[224:227], v[232:235], %[a0]\n\t"
                "ds_read_b128 v[200:203], %[ub] offset:2160\n\t"
                "s_add_u32 %[t0], %[t1], 49152\n\t"
                "buffer_load_dwordx4 a[140:143], %[go], %[so], %[t0] offen\n\t"
                "s_waitcnt lgkmcnt(10)\n\t"
                "v_mfma_f32_32x32x16_bf16 %[a1], v[236:239], v[232:235], %[a1]\n\t"
                "ds_read_b64 v[204:205], %[kb] offset:2336\n\t"
                "ds_read_b64 v[206:207], %[kb] offset:2344\n\t"
                "s_waitcnt lgkmcnt(9)\n\t"
                "v_mfma_f32_32x32x16_bf16 %[a1], v[240:243], v[244:247], %[a1]\n\t"
                "ds_read_b64 v[208:209], %[kb] offset:256\n\t"
                "ds_read_b64 v[210:211], %[kb] offset:264\n\t"
                "ds_read_b128 v[212:215], %[ub] offset:4752\n\t"
                "s_waitcnt lgkmcnt(11)\n\t"
                "v_mfma_f32_32x32x16_bf16 %[a0], v[240:243], v[248:251], %[a0]\n\t"
                "ds_read_b128 v[216:219], %[ub] offset:2192\n\t"
                "s_add_u32 %[t0], %[t1], 65536\n\t"
                "buffer_load_dwordx4 a[144:147], %[go], %[so], %[t0] offen\n\t"
                "s_waitcnt lgkmcnt(10)\n\t"
                "v_mfma_f32_32x32x16_bf16 %[a1], v[252:255], v[248:251], %[a1]\n\t"
                "ds_read_b64 v[220:221], %[kb] offset:2304\n\t"
                "ds_read_b64 v[222:223], %[kb] offset:2312\n\t"
                "s_waitcnt lgkmcnt(9)\n\t"
                "v_mfma_f32_32x32x16_bf16 %[a1], v[192:195], v[196:199], %[a1]\n\t"
                "ds_read_b64 v[224:225], %[kb] offset:352\n\t"
                "ds_read_b64 v[226:227], %[kb] offset:360\n\t"
                "ds_read_b128 v[228:231], %[ub] offset:4640\n\t"
                "s_waitcnt lgkmcnt(11)\n\t"
                "v_mfma_f32_32x32x16_bf16 %[a0], v[192:195], v[200:203], %[a0]\n\t"
                "ds_read_b128 v[232:235], %[ub] offset:2080\n\t"
                "s_waitcnt lgkmcnt(10)\n\t"
                "v_mfma_f32_32x32x16_bf16 %[a1], v[204:207], v[200:203], %[a1]\n\t"
                "ds_read_b64 v[236:237], %[kb] offset:2400\n\t"
                "ds_read_b64 v[238:239], %[kb] offset:2408\n\t"
                "s_waitcnt lgkmcnt(9)\n\t"
                "v_mfma_f32_32x32x16_bf16 %[a1], v[208:211], v[212:215], %[a1]\n\t"
                "ds_read_b64 v[240:241], %[kb] offset:320\n\t"
                "ds_read_b64 v[242:243], %[kb] offset:328\n\t"
                "ds_read_b128 v[244:247], %[ub] offset:4672\n\t"
                "s_waitcnt lgkmcnt(11)\n\t"
                "v_mfma_f32_32x32x16_bf16 %[a0], v[208:211], v[216:219], %[a0]\n\t"
                "ds_read_b128 v[248:251], %[ub] offset:2112\n\t"
                "s_add_u32 %[t0], %[t1], 81920\n\t"
                "buffer_load_dwordx4 a[148:151], %[go], %[so], %[t0] offen\n\t"
                "s_waitcnt lgkmcnt(10)\n\t"
                "v_mfma_f32_32x32x16_bf16 %[a1], v[220:223], v[216:219], %[a1]\n\t"
                "ds_read_b64 v[252:253], %[kb] offset:2368\n\t"
                "ds_read_b64 v[254:255], %[kb] offset:2376\n\t"
                "s_waitcnt lgkmcnt(9)\n\t"
                "v_mfma_f32_32x32x16_bf16 %[a1], v[224:227], v[228:231], %[a1]\n\t"
                "ds_read_b64 v[192:193], %[kb] offset:416\n\t"
                "ds_read_b64 v[194:195], %[kb] offset:424\n\t"
                "ds_read_b128 v[196:199], %[ub] offset:4560\n\t"
                "s_waitcnt lgkmcnt(11)\n\t"
                "v_mfma_f32_32x32x16_bf16 %[a0], v[224:227], v[232:235], %[a0]\n\t"
                "ds_read_b128 v[200:203], %[ub] offset:2000\n\t"
                "s_waitcnt lgkmcnt(10)\n\t"
                "v_mfma_f32_32x32x16_bf16 %[a1], v[236:239], v[232:235], %[a1]\n\t"
                "ds_read_b64 v[204:205], %[kb] offset:2464\n\t"
                "ds_read_b64 v[206:207], %[kb] offset:2472\n\t"
                "s_waitcnt lgkmcnt(9)\n\t"
                "v_mfma_f32_32x32x16_bf16 %[a1], v[240:243], v[244:247], %[a1]\n\t"
                "ds_read_b64 v[208:209], %[kb] offset:384\n\t"
                "ds_read_b64 v[210:211], %[kb] offset:392\n\t"
                "ds_read_b128 v[212:215], %[ub] offset:4592\n\t"
                "s_waitcnt lgkmcnt(11)\n\t"
                "v_mfma_f32_32x32x16_bf16 %[a0], v[240:243], v[248:251], %[a0]\n\t"
                "ds_read_b128 v[216:219], %[ub] offset:2032\n\t"
                "s_add_u32 %[t0], %[t1], 98304\n\t"
                "buffer_load_dwordx4 a[152:155], %[go], %[so], %[t0] offen\n\t"
                "s_waitcnt lgkmcnt(10)\n\t"
                "v_mfma_f32_32x32x16_bf16 %[a1], v[252:255], v[248:251], %[a1]\n\t"
                "ds_read_b64 v[220:221], %[kb] offset:2432\n\t"
                "ds_read_b64 v[222:223], %[kb] offset:2440\n\t"
                "s_waitcnt lgkmcnt(9)\n\t"
                "v_mfma_f32_32x32x16_bf16 %[a1], v[192:195], v[196:199], %[a1]\n\t"
                "ds_read_b64 v[224:225], %[kb] offset:480\n\t"
                "ds_read_b64 v[226:227], %[kb] offset:488\n\t"
                "ds_read_b128 v[228:231], %[ub] offset:4480\n\t"
                "s_waitcnt lgkmcnt(11)\n\t"
                "v_mfma_f32_32x32x16_bf16 %[a0], v[192:195], v[200:203], %[a0]\n\t"
                "ds_read_b128 v[232:235], %[ub] offset:1920\n\t"
                "s_waitcnt lgkmcnt(10)\n\t"
                "v_mfma_f32_32x32x16_bf16 %[a1], v[204:207], v[200:203], %[a1]\n\t"
                "ds_read_b64 v[236:237], %[kb] offset:2528\n\t"
                "ds_read_b64 v[238:239], %[kb] offset:2536\n\t"
                "s_waitcnt lgkmcnt(9)\n\t"
                "v_mfma_f32_32x32x16_bf16 %[a1], v[208:211], v[212:215], %[a1]\n\t"
                "ds_read_b64 v[240:241], %[kb] offset:448\n\t"
                "ds_read_b64 v[242:243], %[kb] offset:456\n\t"
                "ds_read_b128 v[244:247], %[ub] offset:4512\n\t"
                "s_waitcnt lgkmcnt(11)\n\t"
                "v_mfma_f32_32x32x16_bf16 %[a0], v[208:211], v[216:219], %[a0]\n\t"
                "ds_read_b128 v[248:251], %[ub] offset:1952\n\t"
                "s_add_u32 %[t0], %[t1], 114688\n\t"
                "buffer_load_dwordx4 a[156:159], %[go], %[so], %[t0] offen\n\t"
                "s_waitcnt lgkmcnt(10)\n\t"
                "v_mfma_f32_32x32x16_bf16 %[a1], v[220:223], v[216:219], %[a1]\n\t"
                "ds_read_b64 v[252:253], %[kb] offset:2496\n\t"
                "ds_read_b64 v[254:255], %[kb] offset:2504\n\t"
                "s_waitcnt lgkmcnt(9)\n\t"
                "v_mfma_f32_32x32x16_bf16 %[a1], v[224:227], v[228:231], %[a1]\n\t"
                "ds_read_b64 v[192:193], %[kb] offset:544\n\t"
                "ds_read_b64 v[194:195], %[kb] offset:552\n\t"
                "ds_read_b128 v[196:199], %[ub] offset:4400\n\t"
                "s_waitcnt lgkmcnt(11)\n\t"
                "v_mfma_f32_32x32x16_bf16 %[a0], v[224:227], v[232:235], %[a0]\n\t"
                "ds_read_b128 v[200:203], %[ub] offset:1840\n\t"
                "s_add_u32 %[t1], %[ro], 2\n\t"
                "s_and_b32 %[t1], %[t1], 7\n\t"
                "s_lshl_b32 %[t1], %[t1], 18\n\t"
                "s_add_u32 %[t0], %[t1], 131072\n\t"
                "buffer_load_dwordx4 a[160:163], %[go], %[so], %[t0] offen\n\t"
                "s_waitcnt lgkmcnt(10)\n\t"
                "v_mfma_f32_32x32x16_bf16 %[a1], v[236:239], v[232:235], %[a1]\n\t"
                "ds_read_b64 v[204:205], %[kb] offset:2592\n\t"
                "ds_read_b64 v[206:207], %[kb] offset:2600\n\t"
                "s_waitcnt lgkmcnt(9)\n\t"
                "v_mfma_f32_32x32x16_bf16 %[a1], v[240:243], v[244:247], %[a1]\n\t"
                "ds_read_b64 v[208:209], %[kb] offset:512\n\t"
                "ds_read_b64 v[210:211], %[kb] offset:520\n\t"
                "ds_read_b128 v[212:215], %[ub] offset:4432\n\t"
                "s_waitcnt lgkmcnt(11)\n\t"
                "v_mfma_f32_32x32x16_bf16 %[a0], v[240:243], v[248:251], %[a0]\n\t"
                "ds_read_b128 v[216:219], %[ub] offset:1872\n\t"
                "s_waitcnt lgkmcnt(10)\n\t"
                "v_mfma_f32_32x32x16_bf16 %[a1], v[252:255], v[248:251], %[a1]\n\t"
                "ds_read_b64 v[220:221], %[kb] offset:2560\n\t"
                "ds_read_b64 v[222:223], %[kb] offset:2568\n\t"
                "s_waitcnt lgkmcnt(9)\n\t"
                "v_mfma_f32_32x32x16_bf16 %[a1], v[192:195], v[196:199], %[a1]\n\t"
                "ds_read_b64 v[224:225], %[kb] offset:608\n\t"
                "ds_read_b64 v[226:227], %[kb] offset:616\n\t"
                "ds_read_b128 v[228:231], %[ub] offset:4320\n\t"
                "s_waitcnt lgkmcnt(11)\n\t"
                "v_mfma_f32_32x32x16_bf16 %[a0], v[192:195], v[200:203], %[a0]\n\t"
                "ds_read_b128 v[232:235], %[ub] offset:1760\n\t"
                "s_add_u32 %[t0], %[t1], 147456\n\t"
                "buffer_load_dwordx4 a[164:167], %[go], %[so], %[t0] offen\n\t"
                "s_waitcnt lgkmcnt(10)\n\t"
                "v_mfma_f32_32x32x16_bf16 %[a1], v[204:207], v[200:203], %[a1]\n\t"
                "ds_read_b64 v[236:237], %[kb] offset:2656\n\t"
                "ds_read_b64 v[238:239], %[kb] offset:2664\n\t"
                "s_waitcnt lgkmcnt(9)\n\t"
                "v_mfma_f32_32x32x16_bf16 %[a1], v[208:211], v[212:215], %[a1]\n\t"
                "ds_read_b64 v[240:241], %[kb] offset:576\n\t"
                "ds_read_b64 v[242:243], %[kb] offset:584\n\t"
                "ds_read_b128 v[244:247], %[ub] offset:4352\n\t"
                "s_waitcnt lgkmcnt(11)\n\t"
                "v_mfma_f32_32x32x16_bf16 %[a0], v[208:211], v[216:219], %[a0]\n\t"
                "ds_read_b128 v[248:251], %[ub] offset:1792\n\t"
                "s_waitcnt lgkmcnt(10)\n\t"
                "v_mfma_f32_32x32x16_bf16 %[a1], v[220:223], v[216:219], %[a1]\n\t"
                "ds_read_b64 v[252:253], %[kb] offset:2624\n\t"
                "ds_read_b64 v[254:255], %[kb] offset:2632\n\t"
                "s_waitcnt lgkmcnt(9)\n\t"
                "v_mfma_f32_32x32x16_bf16 %[a1], v[224:227], v[228:231], %[a1]\n\t"
                "ds_read_b64 v[192:193], %[kb] offset:672\n\t"
                "ds_read_b64 v[194:195], %[kb] offset:680\n\t"
                "ds_read_b128 v[196:199], %[ub] offset:4240\n\t"
                "s_waitcnt lgkmcnt(11)\n\t"
                "v_mfma_f32_32x32x16_bf16 %[a0], v[224:227], v[232:235], %[a0]\n\t"
                "ds_read_b128 v[200:203], %[ub] offset:1680\n\t"
                "s_add_u32 %[t0], %[t1], 163840\n\t"
                "buffer_load_dwordx4 a[168:171], %[go], %[so], %[t0] offen\n\t"
                "s_waitcnt lgkmcnt(10)\n\t"
                "v_mfma_f32_32x32x16_bf16 %[a1], v[236:239], v[232:235], %[a1]\n\t"
                "ds_read_b64 v[204:205], %[kb] offset:2720\n\t"
                "ds_read_b64 v[206:207], %[kb] offset:2728\n\t"
                "s_waitcnt lgkmcnt(9)\n\t"
                "v_mfma_f32_32x32x16_bf16 %[a1], v[240:243], v[244:247], %[a1]\n\t"
                "ds_read_b64 v[208:209], %[kb] offset:640\n\t"
                "ds_read_b64 v[210:211], %[kb] offset:648\n\t"
                "ds_read_b128 v[212:215], %[ub] offset:4272\n\t"
                "s_waitcnt lgkmcnt(11)\n\t"
                "v_mfma_f32_32x32x16_bf16 %[a0], v[240:243], v[248:251], %[a0]\n\t"
                "ds_read_b128 v[216:219], %[ub] offset:1712\n\t"
                "s_waitcnt lgkmcnt(10)\n\t"
                "v_mfma_f32_32x32x16_bf16 %[a1], v[252:255], v[248:251], %[a1]\n\t"
                "ds_read_b64 v[220:221], %[kb] offset:2688\n\t"
                "ds_read_b64 v[222:223], %[kb] offset:2696\n\t"
                "s_waitcnt lgkmcnt(9)\n\t"
                "v_mfma_f32_32x32x16_bf16 %[a1], v[192:195], v[196:199], %[a1]\n\t"
                "ds_read_b64 v[224:225], %[kb] offset:736\n\t"
                "ds_read_b64 v[226:227], %[kb] offset:744\n\t"
                "ds_read_b128 v[228:231], %[ub] offset:4160\n\t"
                "s_waitcnt lgkmcnt(11)\n\t"
                "v_mfma_f32_32x32x16_bf16 %[a0], v[192:195], v[200:203], %[a0]\n\t"
                "ds_read_b128 v[232:235], %[ub] offset:1600\n\t"
                "s_add_u32 %[t0], %[t1], 180224\n\t"
                "buffer_load_dwordx4 a[172:175], %[go], %[so], %[t0] offen\n\t"
                "s_waitcnt lgkmcnt(10)\n\t"
                "v_mfma_f32_32x32x16_bf16 %[a1], v[204:207], v[200:203], %[a1]\n\t"
                "ds_read_b64 v[236:237], %[kb] offset:2784\n\t"
                "ds_read_b64 v[238:239], %[kb] offset:2792\n\t"
                "s_waitcnt lgkmcnt(9)\n\t"
                "v_mfma_f32_32x32x16_bf16 %[a1], v[208:211], v[212:215], %[a1]\n\t"
                "ds_read_b64 v[240:241], %[kb] offset:704\n\t"
                "ds_read_b64 v[242:243], %[kb] offset:712\n\t"
                "ds_read_b128 v[244:247], %[ub] offset:4192\n\t"
                "s_waitcnt lgkmcnt(11)\n\t"
                "v_mfma_f32_32x32x16_bf16 %[a0], v[208:211], v[216:219], %[a0]\n\t"
                "ds_read_b128 v[248:251], %[ub] offset:1632\n\t"
                "s_add_u32 %[t0], %[t1], 196608\n\t"
                "buffer_load_dwordx4 a[176:179], %[go], %[so], %[t0] offen\n\t"
                "s_waitcnt lgkmcnt(10)\n\t"
                "v_mfma_f32_32x32x16_bf16 %[a1], v[220:223], v[216:219], %[a1]\n\t"
                "ds_read_b64 v[252:253], %[kb] offset:2752\n\t"
                "ds_read_b64 v[254:255], %[kb] offset:2760\n\t"
                "s_waitcnt lgkmcnt(9)\n\t"
                "v_mfma_f32_32x32x16_bf16 %[a1], v[224:227], v[228:231], %[a1]\n\t"
                "ds_read_b64 v[192:193], %[kb] offset:800\n\t"
                "ds_read_b64 v[194:195], %[kb] offset:808\n\t"
                "ds_read_b128 v[196:199], %[ub] offset:4080\n\t"
                "s_waitcnt lgkmcnt(11)\n\t"
                "v_mfma_f32_32x32x16_bf16 %[a0], v[224:227], v[232:235], %[a0]\n\t"
                "ds_read_b128 v[200:203], %[ub] offset:1520\n\t"
                "s_waitcnt lgkmcnt(10)\n\t"
                "v_mfma_f32_32x32x16_bf16 %[a1], v[236:239], v[232:235], %[a1]\n\t"
                "ds_read_b64 v[204:205], %[kb] offset:2848\n\t"
                "ds_read_b64 v[206:207], %[kb] offset:2856\n\t"
                "s_waitcnt lgkmcnt(9)\n\t"
                "v_mfma_f32_32x32x16_bf16 %[a1], v[240:243], v[244:247], %[a1]\n\t"
                "ds_read_b64 v[208:209], %[kb] offset:768\n\t"
                "ds_read_b64 v[210:211], %[kb] offset:776\n\t"
                "ds_read_b128 v[212:215], %[ub] offset:4112\n\t"
                "s_waitcnt lgkmcnt(11)\n\t"
                "v_mfma_f32_32x32x16_bf16 %[a0], v[240:243], v[248:251], %[a0]\n\t"
                "ds_read_b128 v[216:219], %[ub] offset:1552\n\t"
                "s_add_u32 %[t0], %[t1], 212992\n\t"
                "buffer_load_dwordx4 a[180:183], %[go], %[so], %[t0] offen\n\t"
                "s_waitcnt lgkmcnt(10)\n\t"
                "v_mfma_f32_32x32x16_bf16 %[a1], v[252:255], v[248:251], %[a1]\n\t"
                "ds_read_b64 v[220:221], %[kb] offset:2816\n\t"
                "ds_read_b64 v[222:223], %[kb] offset:2824\n\t"
                "s_waitcnt lgkmcnt(9)\n\t"
                "v_mfma_f32_32x32x16_bf16 %[a1], v[192:195], v[196:199], %[a1]\n\t"
                "ds_read_b64 v[224:225], %[kb] offset:864\n\t"
                "ds_read_b64 v[226:227], %[kb] offset:872\n\t"
                "ds_read_b128 v[228:231], %[ub] offset:4000\n\t"
                "s_waitcnt lgkmcnt(11)\n\t"
                "v_mfma_f32_32x32x16_bf16 %[a0], v[192:195], v[200:203], %[a0]\n\t"
                "ds_read_b128 v[232:235], %[ub] offset:1440\n\t"
                "s_waitcnt lgkmcnt(10)\n\t"
                "v_mfma_f32_32x32x16_bf16 %[a1], v[204:207], v[200:203], %[a1]\n\t"
                "ds_read_b64 v[236:237], %[kb] offset:2912\n\t"
                "ds_read_b64 v[238:239], %[kb] offset:2920\n\t"
                "s_waitcnt lgkmcnt(9)\n\t"
                "v_mfma_f32_32x32x16_bf16 %[a1], v[208:211], v[212:215], %[a1]\n\t"
                "ds_read_b64 v[240:241], %[kb] offset:832\n\t"
                "ds_read_b64 v[242:243], %[kb] offset:840\n\t"
                "ds_read_b128 v[244:247], %[ub] offset:4032\n\t"
                "s_waitcnt lgkmcnt(11)\n\t"
                "v_mfma_f32_32x32x16_bf16 %[a0], v[208:211], v[216:219], %[a0]\n\t"
                "ds_read_b128 v[248:251], %[ub] offset:1472\n\t"
                "s_add_u32 %[t0], %[t1], 229376\n\t"
                "buffer_load_dwordx4 a[184:187], %[go], %[so], %[t0] offen\n\t"
                "s_waitcnt lgkmcnt(10)\n\t"
                "v_mfma_f32_32x32x16_bf16 %[a1], v[220:223], v[216:219], %[a1]\n\t"
                "ds_read_b64 v[252:253], %[kb] offset:2880\n\t"
                "ds_read_b64 v[254:255], %[kb] offset:2888\n\t"
                "s_waitcnt lgkmcnt(9)\n\t"
                "v_mfma_f32_32x32x16_bf16 %[a1], v[224:227], v[228:231], %[a1]\n\t"
                "ds_read_b64 v[192:193], %[kb] offset:928\n\t"
                "ds_read_b64 v[194:195], %[kb] offset:936\n\t"
                "ds_read_b128 v[196:199], %[ub] offset:3920\n\t"
                "s_waitcnt lgkmcnt(11)\n\t"
                "v_mfma_f32_32x32x16_bf16 %[a0], v[224:227], v[232:235], %[a0]\n\t"
                "ds_read_b128 v[200:203], %[ub] offset:1360\n\t"
                "s_waitcnt lgkmcnt(10)\n\t"
                "v_mfma_f32_32x32x16_bf16 %[a1], v[236:239], v[232:235], %[a1]\n\t"
                "ds_read_b64 v[204:205], %[kb] offset:2976\n\t"
                "ds_read_b64 v[206:207], %[kb] offset:2984\n\t"
                "s_waitcnt lgkmcnt(9)\n\t"
                "v_mfma_f32_32x32x16_bf16 %[a1], v[240:243], v[244:247], %[a1]\n\t"
                "ds_read_b64 v[208:209], %[kb] offset:896\n\t"
                "ds_read_b64 v[210:211], %[kb] offset:904\n\t"
                "ds_read_b128 v[212:215], %[ub] offset:3952\n\t"
                "s_waitcnt lgkmcnt(11)\n\t"
                "v_mfma_f32_32x32x16_bf16 %[a0], v[240:243], v[248:251], %[a0]\n\t"
                "ds_read_b128 v[216:219], %[ub] offset:1392\n\t"
                "s_add_u32 %[t0], %[t1], 245760\n\t"
                "buffer_load_dwordx4 a[188:191], %[go], %[so], %[t0] offen\n\t"
                "s_waitcnt lgkmcnt(10)\n\t"
                "v_mfma_f32_32x32x16_bf16 %[a1], v[252:255], v[248:251], %[a1]\n\t"
                "ds_read_b64 v[220:221], %[kb] offset:2944\n\t"
                "ds_read_b64 v[222:223], %[kb] offset:2952\n\t"
                "s_waitcnt lgkmcnt(9)\n\t"
                "v_mfma_f32_32x32x16_bf16 %[a1], v[192:195], v[196:199], %[a1]\n\t"
                "ds_read_b64 v[224:225], %[kb] offset:992\n\t"
                "ds_read_b64 v[226:227], %[kb] offset:1000\n\t"
                "ds_read_b128 v[228:231], %[ub] offset:3840\n\t"
                "s_waitcnt lgkmcnt(11)\n\t"
                "v_mfma_f32_32x32x16_bf16 %[a0], v[192:195], v[200:203], %[a0]\n\t"
                "ds_read_b128 v[232:235], %[ub] offset:1280\n\t"
                "s_add_u32 %[t1], %[ro], 3\n\t"
                "s_and_b32 %[t1], %[t1], 7\n\t"
                "s_lshl_b32 %[t1], %[t1], 18\n\t"
                "s_add_u32 %[t0], %[t1], 0\n\t"
                "buffer_load_dwordx4 a[192:195], %[go], %[so], %[t0] offen\n\t"
                "s_waitcnt lgkmcnt(10)\n\t"
                "v_mfma_f32_32x32x16_bf16 %[a1], v[204:207], v[200:203], %[a1]\n\t"
                "ds_read_b64 v[236:237], %[kb] offset:3040\n\t"
                "ds_read_b64 v[238:239], %[kb] offset:3048\n\t"
                "s_waitcnt lgkmcnt(9)\n\t"
                "v_mfma_f32_32x32x16_bf16 %[a1], v[208:211], v[212:215], %[a1]\n\t"
                "ds_read_b64 v[240:241], %[kb] offset:960\n\t"
                "ds_read_b64 v[242:243], %[kb] offset:968\n\t"
                "ds_read_b128 v[244:247], %[ub] offset:3872\n\t"
                "s_waitcnt lgkmcnt(11)\n\t"
                "v_mfma_f32_32x32x16_bf16 %[a0], v[208:211], v[216:219], %[a0]\n\t"
                "ds_read_b128 v[248:251], %[ub] offset:1312\n\t"
                "s_waitcnt lgkmcnt(10)\n\t"
                "v_mfma_f32_32x32x16_bf16 %[a1], v[220:223], v[216:219], %[a1]\n\t"
                "ds_read_b64 v[252:253], %[kb] offset:3008\n\t"
                "ds_read_b64 v[254:255], %[kb] offset:3016\n\t"
                "s_waitcnt lgkmcnt(9)\n\t"
                "v_mfma_f32_32x32x16_bf16 %[a1], v[224:227], v[228:231], %[a1]\n\t"
                "ds_read_b64 v[192:193], %[kb] offset:1056\n\t"
                "ds_read_b64 v[194:195], %[kb] offset:1064\n\t"
                "ds_read_b128 v[196:199], %[ub] offset:3760\n\t"
                "s_waitcnt lgkmcnt(11)\n\t"
                "v_mfma_f32_32x32x16_bf16 %[a0], v[224:227], v[232:235], %[a0]\n\t"
                "ds_read_b128 v[200:203], %[ub] offset:1200\n\t"
                "s_add_u32 %[t0], %[t1], 16384\n\t"
                "buffer_load_dwordx4 a[196:199], %[go], %[so], %[t0] offen\n\t"
                "s_waitcnt lgkmcnt(10)\n\t"
                "v_mfma_f32_32x32x16_bf16 %[a1], v[236:239], v[232:235], %[a1]\n\t"
                "ds_read_b64 v[204:205], %[kb] offset:3104\n\t"
                "ds_read_b64 v[206:207], %[kb] offset:3112\n\t"
                "s_waitcnt lgkmcnt(9)\n\t"
                "v_mfma_f32_32x32x16_bf16 %[a1], v[240:243], v[244:247], %[a1]\n\t"
                "ds_read_b64 v[208:209], %[kb] offset:1024\n\t"
                "ds_read_b64 v[210:211], %[kb] offset:1032\n\t"
                "ds_read_b128 v[212:215], %[ub] offset:3792\n\t"
                "s_waitcnt lgkmcnt(11)\n\t"
                "v_mfma_f32_32x32x16_bf16 %[a0], v[240:243], v[248:251], %[a0]\n\t"
                "ds_read_b128 v[216:219], %[ub] offset:1232\n\t"
                "s_waitcnt lgkmcnt(10)\n\t"
                "v_mfma_f32_32x32x16_bf16 %[a1], v[252:255], v[248:251], %[a1]\n\t"
                "ds_read_b64 v[220:221], %[kb] offset:3072\n\t"
                "ds_read_b64 v[222:223], %[kb] offset:3080\n\t"
                "s_waitcnt lgkmcnt(9)\n\t"
                "v_mfma_f32_32x32x16_bf16 %[a1], v[192:195], v[196:199], %[a1]\n\t"
                "ds_read_b64 v[224:225], %[kb] offset:1120\n\t"
                "ds_read_b64 v[226:227], %[kb] offset:1128\n\t"
                "ds_read_b128 v[228:231], %[ub] offset:3680\n\t"
                "s_waitcnt lgkmcnt(11)\n\t"
                "v_mfma_f32_32x32x16_bf16 %[a0], v[192:195], v[200:203], %[a0]\n\t"
                "ds_read_b128 v[232:235], %[ub] offset:1120\n\t"
                "s_add_u32 %[t0], %[t1], 32768\n\t"
                "buffer_load_dwordx4 a[200:203], %[go], %[so], %[t0] offen\n\t"
                "s_waitcnt lgkmcnt(10)\n\t"
                "v_mfma_f32_32x32x16_bf16 %[a1], v[204:207], v[200:203], %[a1]\n\t"
                "ds_read_b64 v[236:237], %[kb] offset:3168\n\t"
                "ds_read_b64 v[238:239], %[kb] offset:3176\n\t"
                "s_waitcnt lgkmcnt(9)\n\t"
                "v_mfma_f32_32x32x16_bf16 %[a1], v[208:211], v[212:215], %[a1]\n\t"
                "ds_read_b64 v[240:241], %[kb] offset:1088\n\t"
                "ds_read_b64 v[242:243], %[kb] offset:1096\n\t"
                "ds_read_b128 v[244:247], %[ub] offset:3712\n\t"
                "s_waitcnt lgkmcnt(11)\n\t"
                "v_mfma_f32_32x32x16_bf16 %[a0], v[208:211], v[216:219], %[a0]\n\t"
                "ds_read_b128 v[248:251], %[ub] offset:1152\n\t"
                "s_waitcnt lgkmcnt(10)\n\t"
                "v_mfma_f32_32x32x16_bf16 %[a1], v[220:223], v[216:219], %[a1]\n\t"
                "ds_read_b64 v[252:253], %[kb] offset:3136\n\t"
                "ds_read_b64 v[254:255], %[kb] offset:3144\n\t"
                "s_waitcnt lgkmcnt(9)\n\t"
                "v_mfma_f32_32x32x16_bf16 %[a1], v[224:227], v[228:231], %[a1]\n\t"
                "ds_read_b64 v[192:193], %[kb] offset:1184\n\t"
                "ds_read_b64 v[194:195], %[kb] offset:1192\n\t"
                "ds_read_b128 v[196:199], %[ub] offset:3600\n\t"
                "s_waitcnt lgkmcnt(11)\n\t"
                "v_mfma_f32_32x32x16_bf16 %[a0], v[224:227], v[232:235], %[a0]\n\t"
                "ds_read_b128 v[200:203], %[ub] offset:1040\n\t"
                "s_add_u32 %[t0], %[t1], 49152\n\t"
                "buffer_load_dwordx4 a[204:207], %[go], %[so], %[t0] offen\n\t"
                "s_waitcnt lgkmcnt(10)\n\t"
                "v_mfma_f32_32x32x16_bf16 %[a1], v[236:239], v[232:235], %[a1]\n\t"
                "ds_read_b64 v[204:205], %[kb] offset:3232\n\t"
                "ds_read_b64 v[206:207], %[kb] offset:3240\n\t"
                "s_waitcnt lgkmcnt(9)\n\t"
                "v_mfma_f32_32x32x16_bf16 %[a1], v[240:243], v[244:247], %[a1]\n\t"
                "ds_read_b64 v[208:209], %[kb] offset:1152\n\t"
                "ds_read_b64 v[210:211], %[kb] offset:1160\n\t"
                "ds_read_b128 v[212:215], %[ub] offset:3632\n\t"
                "s_waitcnt lgkmcnt(11)\n\t"
                "v_mfma_f32_32x32x16_bf16 %[a0], v[240:243], v[248:251], %[a0]\n\t"
                "ds_read_b128 v[216:219], %[ub] offset:1072\n\t"
                "s_add_u32 %[t0], %[t1], 65536\n\t"
                "buffer_load_dwordx4 a[208:211], %[go], %[so], %[t0] offen\n\t"
                "s_waitcnt lgkmcnt(10)\n\t"
                "v_mfma_f32_32x32x16_bf16 %[a1], v[252:255], v[248:251], %[a1]\n\t"
                "ds_read_b64 v[220:221], %[kb] offset:3200\n\t"
                "ds_read_b64 v[222:223], %[kb] offset:3208\n\t"
                "s_waitcnt lgkmcnt(9)\n\t"
                "v_mfma_f32_32x32x16_bf16 %[a1], v[192:195], v[196:199], %[a1]\n\t"
                "ds_read_b64 v[224:225], %[kb] offset:1248\n\t"
                "ds_read_b64 v[226:227], %[kb] offset:1256\n\t"
                "ds_read_b128 v[228:231], %[ub] offset:3520\n\t"
                "s_waitcnt lgkmcnt(11)\n\t"
                "v_mfma_f32_32x32x16_bf16 %[a0], v[192:195], v[200:203], %[a0]\n\t"
                "ds_read_b128 v[232:235], %[ub] offset:960\n\t"
                "s_waitcnt lgkmcnt(10)\n\t"
                "v_mfma_f32_32x32x16_bf16 %[a1], v[204:207], v[200:203], %[a1]\n\t"
                "ds_read_b64 v[236:237], %[kb] offset:3296\n\t"
                "ds_read_b64 v[238:239], %[kb] offset:3304\n\t"
                "s_waitcnt lgkmcnt(9)\n\t"
                "v_mfma_f32_32x32x16_bf16 %[a1], v[208:211], v[212:215], %[a1]\n\t"
                "ds_read_b64 v[240:241], %[kb] offset:1216\n\t"
                "ds_read_b64 v[242:243], %[kb] offset:1224\n\t"
                "ds_read_b128 v[244:247], %[ub] offset:3552\n\t"
                "s_waitcnt lgkmcnt(11)\n\t"
                "v_mfma_f32_32x32x16_bf16 %[a0], v[208:211], v[216:219], %[a0]\n\t"
                "ds_read_b128 v[248:251], %[ub] offset:992\n\t"
                "s_add_u32 %[t0], %[t1], 81920\n\t"
                "buffer_load_dwordx4 a[212:215], %[go], %[so], %[t0] offen\n\t"
                "s_waitcnt lgkmcnt(10)\n\t"
                "v_mfma_f32_32x32x16_bf16 %[a1], v[220:223], v[216:219], %[a1]\n\t"
                "ds_read_b64 v[252:253], %[kb] offset:3264\n\t"
                "ds_read_b64 v[254:255], %[kb] offset:3272\n\t"
                "s_waitcnt lgkmcnt(9)\n\t"
                "v_mfma_f32_32x32x16_bf16 %[a1], v[224:227], v[228:231], %[a1]\n\t"
                "ds_read_b64 v[192:193], %[kb] offset:1312\n\t"
                "ds_read_b64 v[194:195], %[kb] offset:1320\n\t"
                "ds_read_b128 v[196:199], %[ub] offset:3440\n\t"
                "s_waitcnt lgkmcnt(11)\n\t"
                "v_mfma_f32_32x32x16_bf16 %[a0], v[224:227], v[232:235], %[a0]\n\t"
                "ds_read_b128 v[200:203], %[ub] offset:880\n\t"
                "s_waitcnt lgkmcnt(10)\n\t"
                "v_mfma_f32_32x32x16_bf16 %[a1], v[236:239], v[232:235], %[a1]\n\t"
                "ds_read_b64 v[204:205], %[kb] offset:3360\n\t"
                "ds_read_b64 v[206:207], %[kb] offset:3368\n\t"
                "s_waitcnt lgkmcnt(9)\n\t"
                "v_mfma_f32_32x32x16_bf16 %[a1], v[240:243], v[244:247], %[a1]\n\t"
                "ds_read_b64 v[208:209], %[kb] offset:1280\n\t"
                "ds_read_b64 v[210:211], %[kb] offset:1288\n\t"
                "ds_read_b128 v[212:215], %[ub] offset:3472\n\t"
                "s_waitcnt lgkmcnt(11)\n\t"
                "v_mfma_f32_32x32x16_bf16 %[a0], v[240:243], v[248:251], %[a0]\n\t"
                "ds_read_b128 v[216:219], %[ub] offset:912\n\t"
                "s_add_u32 %[t0], %[t1], 98304\n\t"
                "buffer_load_dwordx4 a[216:219], %[go], %[so], %[t0] offen\n\t"
                "s_waitcnt lgkmcnt(10)\n\t"
                "v_mfma_f32_32x32x16_bf16 %[a1], v[252:255], v[248:251], %[a1]\n\t"
                "ds_read_b64 v[220:221], %[kb] offset:3328\n\t"
                "ds_read_b64 v[222:223], %[kb] offset:3336\n\t"
                "s_waitcnt lgkmcnt(9)\n\t"
                "v_mfma_f32_32x32x16_bf16 %[a1], v[192:195], v[196:199], %[a1]\n\t"
                "ds_read_b64 v[224:225], %[kb] offset:1376\n\t"
                "ds_read_b64 v[226:227], %[kb] offset:1384\n\t"
                "ds_read_b128 v[228:231], %[ub] offset:3360\n\t"
                "s_waitcnt lgkmcnt(11)\n\t"
                "v_mfma_f32_32x32x16_bf16 %[a0], v[192:195], v[200:203], %[a0]\n\t"
                "ds_read_b128 v[232:235], %[ub] offset:800\n\t"
                "s_waitcnt lgkmcnt(10)\n\t"
                "v_mfma_f32_32x32x16_bf16 %[a1], v[204:207], v[200:203], %[a1]\n\t"
                "ds_read_b64 v[236:237], %[kb] offset:3424\n\t"
                "ds_read_b64 v[238:239], %[kb] offset:3432\n\t"
                "s_waitcnt lgkmcnt(9)\n\t"
                "v_mfma_f32_32x32x16_bf16 %[a1], v[208:211], v[212:215], %[a1]\n\t"
                "ds_read_b64 v[240:241], %[kb] offset:1344\n\t"
                "ds_read_b64 v[242:243], %[kb] offset:1352\n\t"
                "ds_read_b128 v[244:247], %[ub] offset:3392\n\t"
                "s_waitcnt lgkmcnt(11)\n\t"
                "v_mfma_f32_32x32x16_bf16 %[a0], v[208:211], v[216:219], %[a0]\n\t"
                "ds_read_b128 v[248:251], %[ub] offset:832\n\t"
                "s_add_u32 %[t0], %[t1], 114688\n\t"
                "buffer_load_dwordx4 a[220:223], %[go], %[so], %[t0] offen\n\t"
                "s_waitcnt lgkmcnt(10)\n\t"
                "v_mfma_f32_32x32x16_bf16 %[a1], v[220:223], v[216:219], %[a1]\n\t"
                "ds_read_b64 v[252:253], %[kb] offset:3392\n\t"
                "ds_read_b64 v[254:255], %[kb] offset:3400\n\t"
                "s_waitcnt lgkmcnt(9)\n\t"
                "v_mfma_f32_32x32x16_bf16 %[a1], v[224:227], v[228:231], %[a1]\n\t"
                "ds_read_b64 v[192:193], %[kb] offset:1440\n\t"
                "ds_read_b64 v[194:195], %[kb] offset:1448\n\t"
                "ds_read_b128 v[196:199], %[ub] offset:3280\n\t"
                "s_waitcnt lgkmcnt(11)\n\t"
                "v_mfma_f32_32x32x16_bf16 %[a0], v[224:227], v[232:235], %[a0]\n\t"
                "ds_read_b128 v[200:203], %[ub] offset:720\n\t"
                "s_add_u32 %[t1], %[ro], 3\n\t"
                "s_and_b32 %[t1], %[t1], 7\n\t"
                "s_lshl_b32 %[t1], %[t1], 18\n\t"
                "s_add_u32 %[t0], %[t1], 131072\n\t"
                "buffer_load_dwordx4 a[224:227], %[go], %[so], %[t0] offen\n\t"
                "s_waitcnt lgkmcnt(10)\n\t"
                "v_mfma_f32_32x32x16_bf16 %[a1], v[236:239], v[232:235], %[a1]\n\t"
                "ds_read_b64 v[204:205], %[kb] offset:3488\n\t"
                "ds_read_b64 v[206:207], %[kb] offset:3496\n\t"
                "s_waitcnt lgkmcnt(9)\n\t"
                "v_mfma_f32_32x32x16_bf16 %[a1], v[240:243], v[244:247], %[a1]\n\t"
                "ds_read_b64 v[208:209], %[kb] offset:1408\n\t"
                "ds_read_b64 v[210:211], %[kb] offset:1416\n\t"
                "ds_read_b128 v[212:215], %[ub] offset:3312\n\t"
                "s_waitcnt lgkmcnt(11)\n\t"
                "v_mfma_f32_32x32x16_bf16 %[a0], v[240:243], v[248:251], %[a0]\n\t"
                "ds_read_b128 v[216:219], %[ub] offset:752\n\t"
                "s_waitcnt lgkmcnt(10)\n\t"
                "v_mfma_f32_32x32x16_bf16 %[a1], v[252:255], v[248:251], %[a1]\n\t"
                "ds_read_b64 v[220:221], %[kb] offset:3456\n\t"
                "ds_read_b64 v[222:223], %[kb] offset:3464\n\t"
                "s_waitcnt lgkmcnt(9)\n\t"
                "v_mfma_f32_32x32x16_bf16 %[a1], v[192:195], v[196:199], %[a1]\n\t"
                "ds_read_b64 v[224:225], %[kb] offset:1504\n\t"
                "ds_read_b64 v[226:227], %[kb] offset:1512\n\t"
                "ds_read_b128 v[228:231], %[ub] offset:3200\n\t"
                "s_waitcnt lgkmcnt(11)\n\t"
                "v_mfma_f32_32x32x16_bf16 %[a0], v[192:195], v[200:203], %[a0]\n\t"
                "ds_read_b128 v[232:235], %[ub] offset:640\n\t"
                "s_add_u32 %[t0], %[t1], 147456\n\t"
                "buffer_load_dwordx4 a[228:231], %[go], %[so], %[t0] offen\n\t"
                "s_waitcnt lgkmcnt(10)\n\t"
                "v_mfma_f32_32x32x16_bf16 %[a1], v[204:207], v[200:203], %[a1]\n\t"
                "ds_read_b64 v[236:237], %[kb] offset:3552\n\t"
                "ds_read_b64 v[238:239], %[kb] offset:3560\n\t"
                "s_waitcnt lgkmcnt(9)\n\t"
                "v_mfma_f32_32x32x16_bf16 %[a1], v[208:211], v[212:215], %[a1]\n\t"
                "ds_read_b64 v[240:241], %[kb] offset:1472\n\t"
                "ds_read_b64 v[242:243], %[kb] offset:1480\n\t"
                "ds_read_b128 v[244:247], %[ub] offset:3232\n\t"
                "s_waitcnt lgkmcnt(11)\n\t"
                "v_mfma_f32_32x32x16_bf16 %[a0], v[208:211], v[216:219], %[a0]\n\t"
                "ds_read_b128 v[248:251], %[ub] offset:672\n\t"
                "s_waitcnt lgkmcnt(10)\n\t"
                "v_mfma_f32_32x32x16_bf16 %[a1], v[220:223], v[216:219], %[a1]\n\t"
                "ds_read_b64 v[252:253], %[kb] offset:3520\n\t"
                "ds_read_b64 v[254:255], %[kb] offset:3528\n\t"
                "s_waitcnt lgkmcnt(9)\n\t"
                "v_mfma_f32_32x32x16_bf16 %[a1], v[224:227], v[228:231], %[a1]\n\t"
                "ds_read_b64 v[192:193], %[kb] offset:1568\n\t"
                "ds_read_b64 v[194:195], %[kb] offset:1576\n\t"
                "ds_read_b128 v[196:199], %[ub] offset:3120\n\t"
                "s_waitcnt lgkmcnt(11)\n\t"
                "v_mfma_f32_32x32x16_bf16 %[a0], v[224:227], v[232:235], %[a0]\n\t"
                "ds_read_b128 v[200:203], %[ub] offset:560\n\t"
                "s_add_u32 %[t0], %[t1], 163840\n\t"
                "buffer_load_dwordx4 a[232:235], %[go], %[so], %[t0] offen\n\t"
                "s_waitcnt lgkmcnt(10)\n\t"
                "v_mfma_f32_32x32x16_bf16 %[a1], v[236:239], v[232:235], %[a1]\n\t"
                "ds_read_b64 v[204:205], %[kb] offset:3616\n\t"
                "ds_read_b64 v[206:207], %[kb] offset:3624\n\t"
                "s_waitcnt lgkmcnt(9)\n\t"
                "v_mfma_f32_32x32x16_bf16 %[a1], v[240:243], v[244:247], %[a1]\n\t"
                "ds_read_b64 v[208:209], %[kb] offset:1536\n\t"
                "ds_read_b64 v[210:211], %[kb] offset:1544\n\t"
                "ds_read_b128 v[212:215], %[ub] offset:3152\n\t"
                "s_waitcnt lgkmcnt(11)\n\t"
                "v_mfma_f32_32x32x16_bf16 %[a0], v[240:243], v[248:251], %[a0]\n\t"
                "ds_read_b128 v[216:219], %[ub] offset:592\n\t"
                "s_waitcnt lgkmcnt(10)\n\t"
                "v_mfma_f32_32x32x16_bf16 %[a1], v[252:255], v[248:251], %[a1]\n\t"
                "ds_read_b64 v[220:221], %[kb] offset:3584\n\t"
                "ds_read_b64 v[222:223], %[kb] offset:3592\n\t"
                "s_waitcnt lgkmcnt(9)\n\t"
                "v_mfma_f32_32x32x16_bf16 %[a1], v[192:195], v[196:199], %[a1]\n\t"
                "ds_read_b64 v[224:225], %[kb] offset:1632\n\t"
                "ds_read_b64 v[226:227], %[kb] offset:1640\n\t"
                "ds_read_b128 v[228:231], %[ub] offset:3040\n\t"
                "s_waitcnt lgkmcnt(11)\n\t"
                "v_mfma_f32_32x32x16_bf16 %[a0], v[192:195], v[200:203], %[a0]\n\t"
                "ds_read_b128 v[232:235], %[ub] offset:480\n\t"
                "s_add_u32 %[t0], %[t1], 180224\n\t"
                "buffer_load_dwordx4 a[236:239], %[go], %[so], %[t0] offen\n\t"
                "s_waitcnt lgkmcnt(10)\n\t"
                "v_mfma_f32_32x32x16_bf16 %[a1], v[204:207], v[200:203], %[a1]\n\t"
                "ds_read_b64 v[236:237], %[kb] offset:3680\n\t"
                "ds_read_b64 v[238:239], %[kb] offset:3688\n\t"
                "s_waitcnt lgkmcnt(9)\n\t"
                "v_mfma_f32_32x32x16_bf16 %[a1], v[208:211], v[212:215], %[a1]\n\t"
                "ds_read_b64 v[240:241], %[kb] offset:1600\n\t"
                "ds_read_b64 v[242:243], %[kb] offset:1608\n\t"
                "ds_read_b128 v[244:247], %[ub] offset:3072\n\t"
                "s_waitcnt lgkmcnt(11)\n\t"
                "v_mfma_f32_32x32x16_bf16 %[a0], v[208:211], v[216:219], %[a0]\n\t"
                "ds_read_b128 v[248:251], %[ub] offset:512\n\t"
                "s_add_u32 %[t0], %[t1], 196608\n\t"
                "buffer_load_dwordx4 a[240:243], %[go], %[so], %[t0] offen\n\t"
                "s_waitcnt lgkmcnt(10)\n\t"
                "v_mfma_f32_32x32x16_bf16 %[a1], v[220:223], v[216:219], %[a1]\n\t"
                "ds_read_b64 v[252:253], %[kb] offset:3648\n\t"
                "ds_read_b64 v[254:255], %[kb] offset:3656\n\t"
                "s_waitcnt lgkmcnt(9)\n\t"
                "v_mfma_f32_32x32x16_bf16 %[a1], v[224:227], v[228:231], %[a1]\n\t"
                "ds_read_b64 v[192:193], %[kb] offset:1696\n\t"
                "ds_read_b64 v[194:195], %[kb] offset:1704\n\t"
                "ds_read_b128 v[196:199], %[ub] offset:2960\n\t"
                "s_waitcnt lgkmcnt(11)\n\t"
                "v_mfma_f32_32x32x16_bf16 %[a0], v[224:227], v[232:235], %[a0]\n\t"
                "ds_read_b128 v[200:203], %[ub] offset:400\n\t"
                "s_waitcnt lgkmcnt(10)\n\t"
                "v_mfma_f32_32x32x16_bf16 %[a1], v[236:239], v[232:235], %[a1]\n\t"
                "ds_read_b64 v[204:205], %[kb] offset:3744\n\t"
                "ds_read_b64 v[206:207], %[kb] offset:3752\n\t"
                "s_waitcnt lgkmcnt(9)\n\t"
                "v_mfma_f32_32x32x16_bf16 %[a1], v[240:243], v[244:247], %[a1]\n\t"
                "ds_read_b64 v[208:209], %[kb] offset:1664\n\t"
                "ds_read_b64 v[210:211], %[kb] offset:1672\n\t"
                "ds_read_b128 v[212:215], %[ub] offset:2992\n\t"
                "s_waitcnt lgkmcnt(11)\n\t"
                "v_mfma_f32_32x32x16_bf16 %[a0], v[240:243], v[248:251], %[a0]\n\t"
                "ds_read_b128 v[216:219], %[ub] offset:432\n\t"
                "s_add_u32 %[t0], %[t1], 212992\n\t"
                "buffer_load_dwordx4 a[244:247], %[go], %[so], %[t0] offen\n\t"
                "s_waitcnt lgkmcnt(10)\n\t"
                "v_mfma_f32_32x32x16_bf16 %[a1], v[252:255], v[248:251], %[a1]\n\t"
                "ds_read_b64 v[220:221], %[kb] offset:3712\n\t"
                "ds_read_b64 v[222:223], %[kb] offset:3720\n\t"
                "s_waitcnt lgkmcnt(9)\n\t"
                "v_mfma_f32_32x32x16_bf16 %[a1], v[192:195], v[196:199], %[a1]\n\t"
                "ds_read_b64 v[224:225], %[kb] offset:1760\n\t"
                "ds_read_b64 v[226:227], %[kb] offset:1768\n\t"
                "ds_read_b128 v[228:231], %[ub] offset:2880\n\t"
                "s_waitcnt lgkmcnt(11)\n\t"
                "v_mfma_f32_32x32x16_bf16 %[a0], v[192:195], v[200:203], %[a0]\n\t"
                "ds_read_b128 v[232:235], %[ub] offset:320\n\t"
                "s_waitcnt lgkmcnt(10)\n\t"
                "v_mfma_f32_32x32x16_bf16 %[a1], v[204:207], v[200:203], %[a1]\n\t"
                "ds_read_b64 v[236:237], %[kb] offset:3808\n\t"
                "ds_read_b64 v[238:239], %[kb] offset:3816\n\t"
                "s_waitcnt lgkmcnt(9)\n\t"
                "v_mfma_f32_32x32x16_bf16 %[a1], v[208:211], v[212:215], %[a1]\n\t"
                "ds_read_b64 v[240:241], %[kb] offset:1728\n\t"
                "ds_read_b64 v[242:243], %[kb] offset:1736\n\t"
                "ds_read_b128 v[244:247], %[ub] offset:2912\n\t"
                "s_waitcnt lgkmcnt(11)\n\t"
                "v_mfma_f32_32x32x16_bf16 %[a0], v[208:211], v[216:219], %[a0]\n\t"
                "ds_read_b128 v[248:251], %[ub] offset:352\n\t"
                "s_add_u32 %[t0], %[t1], 229376\n\t"
                "buffer_load_dwordx4 a[248:251], %[go], %[so], %[t0] offen\n\t"
                "s_waitcnt lgkmcnt(10)\n\t"
                "v_mfma_f32_32x32x16_bf16 %[a1], v[220:223], v[216:219], %[a1]\n\t"
                "ds_read_b64 v[252:253], %[kb] offset:3776\n\t"
                "ds_read_b64 v[254:255], %[kb] offset:3784\n\t"
                "s_waitcnt lgkmcnt(9)\n\t"
                "v_mfma_f32_32x32x16_bf16 %[a1], v[224:227], v[228:231], %[a1]\n\t"
                "ds_read_b64 v[192:193], %[kb] offset:1824\n\t"
                "ds_read_b64 v[194:195], %[kb] offset:1832\n\t"
                "ds_read_b128 v[196:199], %[ub] offset:2800\n\t"
                "s_waitcnt lgkmcnt(11)\n\t"
                "v_mfma_f32_32x32x16_bf16 %[a0], v[224:227], v[232:235], %[a0]\n\t"
                "ds_read_b128 v[200:203], %[ub] offset:240\n\t"
                "s_waitcnt lgkmcnt(10)\n\t"
                "v_mfma_f32_32x32x16_bf16 %[a1], v[236:239], v[232:235], %[a1]\n\t"
                "ds_read_b64 v[204:205], %[kb] offset:3872\n\t"
                "ds_read_b64 v[206:207], %[kb] offset:3880\n\t"
                "s_waitcnt lgkmcnt(9)\n\t"
                "v_mfma_f32_32x32x16_bf16 %[a1], v[240:243], v[244:247], %[a1]\n\t"
                "ds_read_b64 v[208:209], %[kb] offset:1792\n\t"
                "ds_read_b64 v[210:211], %[kb] offset:1800\n\t"
                "ds_read_b128 v[212:215], %[ub] offset:2832\n\t"
                "s_waitcnt lgkmcnt(11)\n\t"
                "v_mfma_f32_32x32x16_bf16 %[a0], v[240:243], v[248:251], %[a0]\n\t"
                "ds_read_b128 v[216:219], %[ub] offset:272\n\t"
                "s_add_u32 %[t0], %[t1], 245760\n\t"
                "buffer_load_dwordx4 a[252:255], %[go], %[so], %[t0] offen\n\t"
                "s_waitcnt lgkmcnt(10)\n\t"
                "v_mfma_f32_32x32x16_bf16 %[a1], v[252:255], v[248:251], %[a1]\n\t"
                "ds_read_b64 v[220:221], %[kb] offset:3840\n\t"
                "ds_read_b64 v[222:223], %[kb] offset:3848\n\t"
                "s_waitcnt lgkmcnt(9)\n\t"
                "v_mfma_f32_32x32x16_bf16 %[a1], v[192:195], v[196:199], %[a1]\n\t"
                "ds_read_b64 v[224:225], %[kb] offset:1888\n\t"
                "ds_read_b64 v[226:227], %[kb] offset:1896\n\t"
                "ds_read_b128 v[228:231], %[ub] offset:2720\n\t"
                "s_waitcnt lgkmcnt(11)\n\t"
                "v_mfma_f32_32x32x16_bf16 %[a0], v[192:195], v[200:203], %[a0]\n\t"
                "ds_read_b128 v[232:235], %[ub] offset:160\n\t"
                "s_waitcnt lgkmcnt(10)\n\t"
                "v_mfma_f32_32x32x16_bf16 %[a1], v[204:207], v[200:203], %[a1]\n\t"
                "ds_read_b64 v[236:237], %[kb] offset:3936\n\t"
                "ds_read_b64 v[238:239], %[kb] offset:3944\n\t"
                "s_waitcnt lgkmcnt(9)\n\t"
                "v_mfma_f32_32x32x16_bf16 %[a1], v[208:211], v[212:215], %[a1]\n\t"
                "ds_read_b64 v[240:241], %[kb] offset:1856\n\t"
                "ds_read_b64 v[242:243], %[kb] offset:1864\n\t"
                "ds_read_b128 v[244:247], %[ub] offset:2752\n\t"
                "s_waitcnt lgkmcnt(11)\n\t"
                "v_mfma_f32_32x32x16_bf16 %[a0], v[208:211], v[216:219], %[a0]\n\t"
                "ds_read_b128 v[248:251], %[ub] offset:192\n\t"
                "s_waitcnt lgkmcnt(10)\n\t"
                "v_mfma_f32_32x32x16_bf16 %[a1], v[220:223], v[216:219], %[a1]\n\t"
                "ds_read_b64 v[252:253], %[kb] offset:3904\n\t"
                "ds_read_b64 v[254:255], %[kb] offset:3912\n\t"
                "s_waitcnt lgkmcnt(9)\n\t"
                "v_mfma_f32_32x32x16_bf16 %[a1], v[224:227], v[228:231], %[a1]\n\t"
                "ds_read_b64 v[192:193], %[kb] offset:1952\n\t"
                "ds_read_b64 v[194:195], %[kb] offset:1960\n\t"
                "ds_read_b128 v[196:199], %[ub] offset:2640\n\t"
                "s_waitcnt lgkmcnt(11)\n\t"
                "v_mfma_f32_32x32x16_bf16 %[a0], v[224:227], v[232:235], %[a0]\n\t"
                "ds_read_b128 v[200:203], %[ub] offset:80\n\t"
                "s_waitcnt lgkmcnt(10)\n\t"
                "v_mfma_f32_32x32x16_bf16 %[a1], v[236:239], v[232:235], %[a1]\n\t"
                "ds_read_b64 v[204:205], %[kb] offset:4000\n\t"
                "ds_read_b64 v[206:207], %[kb] offset:4008\n\t"
                "s_waitcnt lgkmcnt(9)\n\t"
                "v_mfma_f32_32x32x16_bf16 %[a1], v[240:243], v[244:247], %[a1]\n\t"
                "ds_read_b64 v[208:209], %[kb] offset:1920\n\t"
                "ds_read_b64 v[210:211], %[kb] offset:1928\n\t"
                "ds_read_b128 v[212:215], %[ub] offset:2672\n\t"
                "s_waitcnt lgkmcnt(11)\n\t"
                "v_mfma_f32_32x32x16_bf16 %[a0], v[240:243], v[248:251], %[a0]\n\t"
                "ds_read_b128 v[216:219], %[ub] offset:112\n\t"
                "s_waitcnt lgkmcnt(10)\n\t"
                "v_mfma_f32_32x32x16_bf16 %[a1], v[252:255], v[248:251], %[a1]\n\t"
                "ds_read_b64 v[220:221], %[kb] offset:3968\n\t"
                "ds_read_b64 v[222:223], %[kb] offset:3976\n\t"
                "s_waitcnt lgkmcnt(9)\n\t"
                "v_mfma_f32_32x32x16_bf16 %[a1], v[192:195], v[196:199], %[a1]\n\t"
                "ds_read_b64 v[224:225], %[kb] offset:2016\n\t"
                "ds_read_b64 v[226:227], %[kb] offset:2024\n\t"
                "ds_read_b128 v[228:231], %[ub] offset:2560\n\t"
                "s_waitcnt lgkmcnt(11)\n\t"
                "v_mfma_f32_32x32x16_bf16 %[a0], v[192:195], v[200:203], %[a0]\n\t"
                "ds_read_b128 v[232:235], %[ub] offset:0\n\t"
                "s_waitcnt lgkmcnt(10)\n\t"
                "v_mfma_f32_32x32x16_bf16 %[a1], v[204:207], v[200:203], %[a1]\n\t"
                "ds_read_b64 v[236:237], %[kb] offset:4064\n\t"
                "ds_read_b64 v[238:239], %[kb] offset:4072\n\t"
                "s_waitcnt lgkmcnt(9)\n\t"
                "v_mfma_f32_32x32x16_bf16 %[a1], v[208:211], v[212:215], %[a1]\n\t"
                "ds_read_b64 v[240:241], %[kb] offset:1984\n\t"
                "ds_read_b64 v[242:243], %[kb] offset:1992\n\t"
                "ds_read_b128 v[244:247], %[ub] offset:2592\n\t"
                "s_waitcnt lgkmcnt(11)\n\t"
                "v_mfma_f32_32x32x16_bf16 %[a0], v[208:211], v[216:219], %[a0]\n\t"
                "ds_read_b128 v[248:251], %[ub] offset:32\n\t"
                "s_waitcnt lgkmcnt(10)\n\t"
                "v_mfma_f32_32x32x16_bf16 %[a1], v[220:223], v[216:219], %[a1]\n\t"
                "ds_read_b64 v[252:253], %[kb] offset:4032\n\t"
                "ds_read_b64 v[254:255], %[kb] offset:4040\n\t"
                "s_waitcnt lgkmcnt(9)\n\t"
                "v_mfma_f32_32x32x16_bf16 %[a1], v[224:227], v[228:231], %[a1]\n\t"
                "s_waitcnt lgkmcnt(8)\n\t"
                "v_mfma_f32_32x32x16_bf16 %[a0], v[224:227], v[232:235], %[a0]\n\t"
                "s_waitcnt lgkmcnt(6)\n\t"
                "v_mfma_f32_32x32x16_bf16 %[a1], v[236:239], v[232:235], %[a1]\n\t"
                "s_waitcnt lgkmcnt(3)\n\t"
                "v_mfma_f32_32x32x16_bf16 %[a1], v[240:243], v[244:247], %[a1]\n\t"
                "s_waitcnt lgkmcnt(2)\n\t"
                "v_mfma_f32_32x32x16_bf16 %[a0], v[240:243], v[248:251], %[a0]\n\t"
                "s_waitcnt lgkmcnt(0)\n\t"
                "v_mfma_f32_32x32x16_bf16 %[a1], v[252:255], v[248:251], %[a1]\n\t"
                "s_waitcnt vmcnt(0)\n\t"
                "s_nop 7\n\t"
                "s_nop 3\n\t"
                : [a0] "=&v"(acc0), [a1] "=&v"(acc1), [t0] "=&s"(t0), [t1] "=&s"(t1)
                : [kb] "v"(kb), [ub] "v"(ub0 + c8 * CV_U), [go] "v"(go), [so] "s"(so), [ro] "s"(ro) : "memory", "scc", "v192", "v193", "v194", "v195", "v196", "v197", "v198", "v199", "v200", "v201", "v202", "v203", "v204", "v205", "v206", "v207", "v208", "v209", "v210", "v211", "v212", "v213", "v214", "v215", "v216", "v217", "v218", "v219", "v220", "v221", "v222", "v223", "v224", "v225", "v226", "v227", "v228", "v229", "v230", "v231", "v232", "v233", "v234", "v235", "v236", "v237", "v238", "v239", "v240", "v241", "v242", "v243", "v244", "v245", "v246", "v247", "v248", "v249", "v250", "v251", "v252", "v253", "v254", "v255", "a0", "a1", "a2", "a3", "a4", "a5", "a6", "a7", "a8", "a9", "a10", "a11", "a12", "a13", "a14", "a15", "a16", "a17", "a18", "a19", "a20", "a21", "a22", "a23", "a24", "a25", "a26", "a27", "a28", "a29", "a30", "a31", "a32", "a33", "a34", "a35", "a36", "a37", "a38", "a39", "a40", "a41", "a42", "a43", "a44", "a45", "a46", "a47", "a48", "a49", "a50", "a51", "a52", "a53", "a54", "a55", "a56", "a57", "a58", "a59", "a60", "a61", "a62", "a63", "a64", "a65", "a66", "a67", "a68", "a69", "a70", "a71", "a72", "a73", "a74", "a75", "a76", "a77", "a78", "a79", "a80", "a81", "a82", "a83", "a84", "a85", "a86", "a87", "a88", "a89", "a90", "a91", "a92", "a93", "a94", "a95", "a96", "a97", "a98", "a99", "a100", "a101", "a102", "a103", "a104", "a105", "a106", "a107", "a108", "a109", "a110", "a111", "a112", "a113", "a114", "a115", "a116", "a117", "a118", "a119", "a120", "a121", "a122", "a123", "a124", "a125", "a126", "a127", "a128", "a129", "a130", "a131", "a132", "a133", "a134", "a135", "a136", "a137", "a138", "a139", "a140", "a141", "a142", "a143", "a144", "a145", "a146", "a147", "a148", "a149", "a150", "a151", "a152", "a153", "a154", "a155", "a156", "a157", "a158", "a159", "a160", "a161", "a162", "a163", "a164", "a165", "a166", "a167", "a168", "a169", "a170", "a171", "a172", "a173", "a174", "a175", "a176", "a177", "a178", "a179", "a180", "a181", "a182", "a183", "a184", "a185", "a186", "a187", "a188", "a189", "a190", "a191", "a192", "a193", "a194", "a195", "a196", "a197", "a198", "a199", "a200", "a201", "a202", "a203", "a204", "a205", "a206", "a207", "a208", "a209", "a210", "a211", "a212", "a213", "a214", "a215", "a216", "a217", "a218", "a219", "a220", "a221", "a222", "a223", "a224", "a225", "a226", "a227", "a228", "a229", "a230", "a231", "a232", "a233", "a234", "a235", "a236", "a237", "a238", "a239", "a240", "a241", "a242", "a243", "a244", "a245", "a246", "a247", "a248", "a249", "a250", "a251", "a252", "a253", "a254", "a255");
            unsigned char* uc = lds + c8 * CV_U + (32 + b) * CV_RP + kg * 8;
#pragma unroll
            for (int q = 0; q < 4; ++q) {
                *(v2u*)(uc + q * 16) = (v2u){pack2(acc0[4 * q], acc0[4 * q + 1]), pack2(acc0[4 * q + 2], acc0[4 * q + 3])};
                *(v2u*)(uc + 32 * CV_RP + q * 16) = (v2u){pack2(acc1[4 * q], acc1[4 * q + 1]), pack2(acc1[4 * q + 2], acc1[4 * q + 3])}; }
            __builtin_amdgcn_wave_barrier();
        }
    __syncthreads();
#pragma unroll
    for (int G = 0; G < 4; ++G) { const v4u lo = *(const v4u*)(urow + G * 16), hh = *(const v4u*)(urow + CV_U + G * 16); v4u o[2];
#pragma unroll
        for (int i = 0; i < 8; ++i) { const unsigned u0 = ur[G][(7 - i) >> 1], u1 = ur[4 + G][(7 - i) >> 1], qq = qr[2 * G + (i >> 2)][i & 3];
            const unsigned ud = pack2(((i & 1) ? blo(u0) : bhi(u0)) * blo(dv), ((i & 1) ? blo(u1) : bhi(u1)) * bhi(dv));
            const unsigned y = pack2(((i & 1) ? bhi(lo[i >> 1]) : blo(lo[i >> 1])) + blo(ud), ((i & 1) ? bhi(hh[i >> 1]) : blo(hh[i >> 1])) + bhi(ud));
            o[i >> 2][i & 3] = pack2(blo(qq) * blo(y), bhi(qq) * bhi(y)); }
        st16(r, W_A + un * 32768 + 2 * G * 4096 + tid * 16, o[0]); st16(r, W_A + un * 32768 + (2 * G + 1) * 4096 + tid * 16, o[1]); }
    DRAIN(); __syncthreads();
    if (tid == 0) __hip_atomic_fetch_add((unsigned*)(p.ws + F_CVY + (un >> 3) * 128), 1u, __ATOMIC_RELAXED, __HIP_MEMORY_SCOPE_AGENT);
    return true;
}

__device__ __forceinline__ void proj_block(const Params& p, unsigned char* lds, const int wv, const int h, const int c) {
    const int w = wv, l = otid() & 63, n = l & 15, g = l >> 4, tm = w >> 1, tn = w & 1;
    const unsigned lb = __builtin_amdgcn_readfirstlane((unsigned)(size_t)lds);
    const v4u sx = mk_srd(p.Aq, S * D * 2), sq = mk_srd(c < 2 ? p.Wq : p.Wk, 1024 * 4096 * 2), sv = mk_srd(p.Wv, 1024 * 4096 * 2), sw = mk_srd(p.ws, W_TOTAL);
    const unsigned lx = lb + (32 * tm + n) * 128 + ((g ^ (n & 7)) << 4), lr = lb + 131072, rr = 8 * g + (n >> 2);
    const int cv = ((c & 1) ^ 1) * 2 + (c >> 1);
    const unsigned aq = (c < 2 ? W_QX : W_KX) + (tm * 32 + h) * 8192 + (2 * (c & 1) + tn) * 1024 + l * 16;
    unsigned t0, xo, so, cn;
    asm volatile(
        "s_nop 4\n\t"
        "s_mov_b32 %[xo], 0\n\t"
        "s_mov_b32 %[so], 0\n\t"
        "s_mov_b32 %[cn], 1\n\t"
        "s_add_u32 m0, %[ld], 0\n\t"
        "s_add_u32 %[t0], %[xo], 0\n\t"
        "buffer_load_dwordx4 v100, %[sx], %[t0] offen lds\n\t"
        "s_add_u32 m0, %[ld], 1024\n\t"
        "s_add_u32 %[t0], %[xo], 16384\n\t"
        "buffer_load_dwordx4 v100, %[sx], %[t0] offen lds\n\t"
        "s_add_u32 m0, %[ld], 8192\n\t"
        "s_add_u32 %[t0], %[xo], 128\n\t"
        "buffer_load_dwordx4 v100, %[sx], %[t0] offen lds\n\t"
        "s_add_u32 m0, %[ld], 9216\n\t"
        "s_add_u32 %[t0], %[xo], 16512\n\t"
        "buffer_load_dwordx4 v100, %[sx], %[t0] offen lds\n\t"
        "s_add_u32 m0, %[ld], 16384\n\t"
        "s_add_u32 %[t0], %[xo], 256\n\t"
        "buffer_load_dwordx4 v100, %[sx], %[t0] offen lds\n\t"
        "s_add_u32 m0, %[ld], 17408\n\t"
        "s_add_u32 %[t0], %[xo], 16640\n\t"
        "buffer_load_dwordx4 v100, %[sx], %[t0] offen lds\n\t"
        "s_add_u32 m0, %[ld], 24576\n\t"
        "s_add_u32 %[t0], %[xo], 384\n\t"
        "buffer_load_dwordx4 v100, %[sx], %[t0] offen lds\n\t"
        "s_add_u32 m0, %[ld], 25600\n\t"
        "s_add_u32 %[t0], %[xo], 16768\n\t"
        "buffer_load_dwordx4 v100, %[sx], %[t0] offen lds\n\t"
        "s_mov_b32 %[t0], 0\n\t"
        "buffer_load_dwordx4 a[192:195], v101, %[sq], %[t0] offen\n\t"
        "buffer_load_dwordx4 a[200:203], v112, %[sv], %[t0] offen\n\t"
        "s_mov_b32 %[t0], 262144\n\t"
        "buffer_load_dwordx4 a[196:199], v101, %[sq], %[t0] offen\n\t"
        "s_mov_b32 %[t0], 524288\n\t"
        "buffer_load_dwordx4 a[204:207], v101, %[sq], %[t0] offen\n\t"
        "buffer_load_dwordx4 a[212:215], v112, %[sv], %[t0] offen\n\t"
        "s_mov_b32 %[t0], 786432\n\t"
        "buffer_load_dwordx4 a[208:211], v101, %[sq], %[t0] offen\n\t"
        "s_mov_b32 %[t0], 1048576\n\t"
        "buffer_load_dwordx4 a[216:219], v101, %[sq], %[t0] offen\n\t"
        "buffer_load_dwordx4 a[224:227], v112, %[sv], %[t0] offen\n\t"
        "s_mov_b32 %[t0], 1310720\n\t"
        "buffer_load_dwordx4 a[220:223], v101, %[sq], %[t0] offen\n\t"
        "s_mov_b32 %[t0], 1572864\n\t"
        "buffer_load_dwordx4 a[228:231], v101, %[sq], %[t0] offen\n\t"
        "buffer_load_dwordx4 a[236:239], v112, %[sv], %[t0] offen\n\t"
        "s_mov_b32 %[t0], 1835008\n\t"
        "buffer_load_dwordx4 a[232:235], v101, %[sq], %[t0] offen\n\t"
        "s_waitcnt vmcnt(9)\n\t"
        "ds_write_b128 v102, a[192:195] offset:0\n\t"
        "ds_write_b128 v102, a[196:199] offset:4096\n\t"
        "ds_write_b128 v116, a[200:203] offset:8192\n\t"
        "s_waitcnt lgkmcnt(0)\n\t"
        "s_barrier\n\t"
        "ds_read_b64_tr_b16 a[0:1], v103 offset:0\n\t"
        "ds_read_b64_tr_b16 a[2:3], v103 offset:512\n\t"
        "ds_read_b64_tr_b16 a[4:5], v103 offset:8\n\t"
        "ds_read_b64_tr_b16 a[6:7], v103 offset:520\n\t"
        "ds_read_b64_tr_b16 a[8:9], v104 offset:0\n\t"
        "ds_read_b64_tr_b16 a[10:11], v104 offset:256\n\t"
        "s_waitcnt lgkmcnt(5)\n\t"
        "ds_read_b64_tr_b16 a[12:13], v103 offset:4096\n\t"
        "ds_read_b64_tr_b16 a[14:15], v103 offset:4608\n\t"
        "ds_read_b64_tr_b16 a[16:17], v103 offset:4104\n\t"
        "ds_read_b64_tr_b16 a[18:19], v103 offset:4616\n\t"
        "ds_read_b64_tr_b16 a[20:21], v104 offset:2048\n\t"
        "ds_read_b64_tr_b16 a[22:23], v104 offset:2304\n\t"
        "ds_read_b128 v[56:59], v105 offset:0\n\t"
        "ds_read_b128 v[60:63], v105 offset:2048\n\t"
        "ds_read_b128 v[64:67], v106 offset:0\n\t"
        "ds_read_b128 v[68:71], v106 offset:2048\n\t"
        "s_add_u32 m0, %[ld], 32768\n\t"
        "s_add_u32 %[t0], %[xo], 512\n\t"
        "buffer_load_dwordx4 v100, %[sx], %[t0] offen lds\n\t"
        "s_add_u32 m0, %[ld], 33792\n\t"
        "s_add_u32 %[t0], %[xo], 16896\n\t"
        "buffer_load_dwordx4 v100, %[sx], %[t0] offen lds\n\t"
        "s_add_u32 m0, %[ld], 40960\n\t"
        "s_add_u32 %[t0], %[xo], 640\n\t"
        "buffer_load_dwordx4 v100, %[sx], %[t0] offen lds\n\t"
        "s_add_u32 m0, %[ld], 41984\n\t"
        "s_add_u32 %[t0], %[xo], 17024\n\t"
        "buffer_load_dwordx4 v100, %[sx], %[t0] offen lds\n\t"
        "s_add_u32 m0, %[ld], 49152\n\t"
        "s_add_u32 %[t0], %[xo], 768\n\t"
        "buffer_load_dwordx4 v100, %[sx], %[t0] offen lds\n\t"
        "s_add_u32 m0, %[ld], 50176\n\t"
        "s_add_u32 %[t0], %[xo], 17152\n\t"
        "buffer_load_dwordx4 v100, %[sx], %[t0] offen lds\n\t"
        "s_add_u32 m0, %[ld], 57344\n\t"
        "s_add_u32 %[t0], %[xo], 896\n\t"
        "buffer_load_dwordx4 v100, %[sx], %[t0] offen lds\n\t"
        "s_add_u32 m0, %[ld], 58368\n\t"
        "s_add_u32 %[t0], %[xo], 17280\n\t"
        "buffer_load_dwordx4 v100, %[sx], %[t0] offen lds\n\t"
        "s_mov_b32 %[t0], 2097152\n\t"
        "buffer_load_dwordx4 a[240:243], v101, %[sq], %[t0] offen\n\t"
        "buffer_load_dwordx4 a[248:251], v112, %[sv], %[t0] offen\n\t"
        "s_mov_b32 %[t0], 2359296\n\t"
        "buffer_load_dwordx4 a[244:247], v101, %[sq], %[t0] offen\n\t"
        "s_waitcnt vmcnt(17)\n\t"
        "ds_write_b128 v102, a[204:207] offset:16384\n\t"
        "ds_write_b128 v102, a[208:211] offset:20480\n\t"
        "ds_write_b128 v116, a[212:215] offset:24576\n\t"
        "s_waitcnt lgkmcnt(0)\n\t"
        "s_barrier\n\t"
        "v_mfma_f32_16x16x32_bf16 v[8:11], a[0:3], v[56:59], 0\n\t"
        "ds_read_b64_tr_b16 a[24:25], v103 offset:16384\n\t"
        "ds_read_b64_tr_b16 a[26:27], v103 offset:16896\n\t"
        "v_mfma_f32_16x16x32_bf16 v[12:15], a[4:7], v[56:59], 0\n\t"
        "ds_read_b64_tr_b16 a[28:29], v103 offset:16392\n\t"
        "ds_read_b64_tr_b16 a[30:31], v103 offset:16904\n\t"
        "v_mfma_f32_16x16x32_bf16 v[16:19], v[56:59], a[8:11], 0\n\t"
        "ds_read_b64_tr_b16 a[32:33], v104 offset:16384\n\t"
        "ds_read_b64_tr_b16 a[34:35], v104 offset:16640\n\t"
        "v_mfma_f32_16x16x32_bf16 v[20:23], a[0:3], v[60:63], 0\n\t"
        "s_waitcnt lgkmcnt(5)\n\t"
        "ds_read_b64_tr_b16 a[36:37], v103 offset:20480\n\t"
        "v_mfma_f32_16x16x32_bf16 v[24:27], a[4:7], v[60:63], 0\n\t"
        "ds_read_b64_tr_b16 a[38:39], v103 offset:20992\n\t"
        "ds_read_b64_tr_b16 a[40:41], v103 offset:20488\n\t"
        "v_mfma_f32_16x16x32_bf16 v[28:31], v[60:63], a[8:11], 0\n\t"
        "ds_read_b64_tr_b16 a[42:43], v103 offset:21000\n\t"
        "ds_read_b64_tr_b16 a[44:45], v104 offset:18432\n\t"
        "v_mfma_f32_16x16x32_bf16 v[8:11], a[12:15], v[64:67], v[8:11]\n\t"
        "ds_read_b64_tr_b16 a[46:47], v104 offset:18688\n\t"
        "ds_read_b128 v[72:75], v105 offset:8192\n\t"
        "v_mfma_f32_16x16x32_bf16 v[12:15], a[16:19], v[64:67], v[12:15]\n\t"
        "ds_read_b128 v[76:79], v105 offset:10240\n\t"
        "ds_read_b128 v[80:83], v106 offset:8192\n\t"
        "v_mfma_f32_16x16x32_bf16 v[16:19], v[64:67], a[20:23], v[16:19]\n\t"
        "ds_read_b128 v[84:87], v106 offset:10240\n\t"
        "v_mfma_f32_16x16x32_bf16 v[20:23], a[12:15], v[68:71], v[20:23]\n\t"
        "v_mfma_f32_16x16x32_bf16 v[24:27], a[16:19], v[68:71], v[24:27]\n\t"
        "v_mfma_f32_16x16x32_bf16 v[28:31], v[68:71], a[20:23], v[28:31]\n\t"
        "s_mov_b32 %[t0], 2621440\n\t"
        "buffer_load_dwordx4 a[252:255], v101, %[sq], %[t0] offen\n\t"
        "buffer_load_dwordx4 v[132:135], v112, %[sv], %[t0] offen\n\t"
        "s_mov_b32 %[t0], 2883584\n\t"
        "buffer_load_dwordx4 v[128:131], v101, %[sq], %[t0] offen\n\t"
        "s_waitcnt vmcnt(17)\n\t"
        "ds_write_b128 v102, a[216:219] offset:0\n\t"
        "ds_write_b128 v102, a[220:223] offset:4096\n\t"
        "ds_write_b128 v116, a[224:227] offset:8192\n\t"
        "s_waitcnt lgkmcnt(0)\n\t"
        "s_barrier\n\t"
        "v_mfma_f32_16x16x32_bf16 v[8:11], a[24:27], v[72:75], v[8:11]\n\t"
        "ds_read_b64_tr_b16 a[48:49], v103 offset:0\n\t"
        "ds_read_b64_tr_b16 a[50:51], v103 offset:512\n\t"
        "v_mfma_f32_16x16x32_bf16 v[12:15], a[28:31], v[72:75], v[12:15]\n\t"
        "ds_read_b64_tr_b16 a[52:53], v103 offset:8\n\t"
        "ds_read_b64_tr_b16 a[54:55], v103 offset:520\n\t"
        "v_mfma_f32_16x16x32_bf16 v[16:19], v[72:75], a[32:35], v[16:19]\n\t"
        "ds_read_b64_tr_b16 a[56:57], v104 offset:0\n\t"
        "ds_read_b64_tr_b16 a[58:59], v104 offset:256\n\t"
        "v_mfma_f32_16x16x32_bf16 v[20:23], a[24:27], v[76:79], v[20:23]\n\t"
        "s_waitcnt lgkmcnt(5)\n\t"
        "ds_read_b64_tr_b16 a[60:61], v103 offset:4096\n\t"
        "v_mfma_f32_16x16x32_bf16 v[24:27], a[28:31], v[76:79], v[24:27]\n\t"
        "ds_read_b64_tr_b16 a[62:63], v103 offset:4608\n\t"
        "ds_read_b64_tr_b16 a[64:65], v103 offset:4104\n\t"
        "v_mfma_f32_16x16x32_bf16 v[28:31], v[76:79], a[32:35], v[28:31]\n\t"
        "ds_read_b64_tr_b16 a[66:67], v103 offset:4616\n\t"
        "ds_read_b64_tr_b16 a[68:69], v104 offset:2048\n\t"
        "v_mfma_f32_16x16x32_bf16 v[8:11], a[36:39], v[80:83], v[8:11]\n\t"
        "ds_read_b64_tr_b16 a[70:71], v104 offset:2304\n\t"
        "ds_read_b128 v[56:59], v105 offset:16384\n\t"
        "v_mfma_f32_16x16x32_bf16 v[12:15], a[40:43], v[80:83], v[12:15]\n\t"
        "ds_read_b128 v[60:63], v105 offset:18432\n\t"
        "ds_read_b128 v[64:67], v106 offset:16384\n\t"
        "v_mfma_f32_16x16x32_bf16 v[16:19], v[80:83], a[44:47], v[16:19]\n\t"
        "ds_read_b128 v[68:71], v106 offset:18432\n\t"
        "v_mfma_f32_16x16x32_bf16 v[20:23], a[36:39], v[84:87], v[20:23]\n\t"
        "v_mfma_f32_16x16x32_bf16 v[24:27], a[40:43], v[84:87], v[24:27]\n\t"
        "v_mfma_f32_16x16x32_bf16 v[28:31], v[84:87], a[44:47], v[28:31]\n\t"
        "s_mov_b32 %[t0], 3145728\n\t"
        "buffer_load_dwordx4 v[136:139], v101, %[sq], %[t0] offen\n\t"
        "buffer_load_dwordx4 v[144:147], v112, %[sv], %[t0] offen\n\t"
        "s_mov_b32 %[t0], 3407872\n\t"
        "buffer_load_dwordx4 v[140:143], v101, %[sq], %[t0] offen\n\t"
        "s_waitcnt vmcnt(17)\n\t"
        "ds_write_b128 v102, a[228:231] offset:16384\n\t"
        "ds_write_b128 v102, a[232:235] offset:20480\n\t"
        "ds_write_b128 v116, a[236:239] offset:24576\n\t"
        "s_waitcnt lgkmcnt(0)\n\t"
        "s_barrier\n\t"
        "v_mfma_f32_16x16x32_bf16 v[8:11], a[48:51], v[56:59], v[8:11]\n\t"
        "ds_read_b64_tr_b16 a[72:73], v103 offset:16384\n\t"
        "ds_read_b64_tr_b16 a[74:75], v103 offset:16896\n\t"
        "v_mfma_f32_16x16x32_bf16 v[12:15], a[52:55], v[56:59], v[12:15]\n\t"
        "ds_read_b64_tr_b16 a[76:77], v103 offset:16392\n\t"
        "ds_read_b64_tr_b16 a[78:79], v103 offset:16904\n\t"
        "v_mfma_f32_16x16x32_bf16 v[16:19], v[56:59], a[56:59], v[16:19]\n\t"
        "ds_read_b64_tr_b16 a[80:81], v104 offset:16384\n\t"
        "ds_read_b64_tr_b16 a[82:83], v104 offset:16640\n\t"
        "v_mfma_f32_16x16x32_bf16 v[20:23], a[48:51], v[60:63], v[20:23]\n\t"
        "s_waitcnt lgkmcnt(5)\n\t"
        "ds_read_b64_tr_b16 a[84:85], v103 offset:20480\n\t"
        "v_mfma_f32_16x16x32_bf16 v[24:27], a[52:55], v[60:63], v[24:27]\n\t"
        "ds_read_b64_tr_b16 a[86:87], v103 offset:20992\n\t"
        "ds_read_b64_tr_b16 a[88:89], v103 offset:20488\n\t"
        "v_mfma_f32_16x16x32_bf16 v[28:31], v[60:63], a[56:59], v[28:31]\n\t"
        "ds_read_b64_tr_b16 a[90:91], v103 offset:21000\n\t"
        "ds_read_b64_tr_b16 a[92:93], v104 offset:18432\n\t"
        "v_mfma_f32_16x16x32_bf16 v[8:11], a[60:63], v[64:67], v[8:11]\n\t"
        "ds_read_b64_tr_b16 a[94:95], v104 offset:18688\n\t"
        "ds_read_b128 v[72:75], v105 offset:24576\n\t"
        "v_mfma_f32_16x16x32_bf16 v[12:15], a[64:67], v[64:67], v[12:15]\n\t"
        "ds_read_b128 v[76:79], v105 offset:26624\n\t"
        "ds_read_b128 v[80:83], v106 offset:24576\n\t"
        "v_mfma_f32_16x16x32_bf16 v[16:19], v[64:67], a[68:71], v[16:19]\n\t"
        "ds_read_b128 v[84:87], v106 offset:26624\n\t"
        "v_mfma_f32_16x16x32_bf16 v[20:23], a[60:63], v[68:71], v[20:23]\n\t"
        "v_mfma_f32_16x16x32_bf16 v[24:27], a[64:67], v[68:71], v[24:27]\n\t"
        "v_mfma_f32_16x16x32_bf16 v[28:31], v[68:71], a[68:71], v[28:31]\n\t"
        "s_mov_b32 %[t0], 3670016\n\t"
        "buffer_load_dwordx4 v[148:151], v101, %[sq], %[t0] offen\n\t"
        "buffer_load_dwordx4 v[156:159], v112, %[sv], %[t0] offen\n\t"
        "s_mov_b32 %[t0], 3932160\n\t"
        "buffer_load_dwordx4 v[152:155], v101, %[sq], %[t0] offen\n\t"
        "s_waitcnt vmcnt(9)\n\t"
        "ds_write_b128 v102, a[240:243] offset:0\n\t"
        "ds_write_b128 v102, a[244:247] offset:4096\n\t"
        "ds_write_b128 v116, a[248:251] offset:8192\n\t"
        "s_waitcnt lgkmcnt(0)\n\t"
        "s_barrier\n\t"
        "v_mfma_f32_16x16x32_bf16 v[8:11], a[72:75], v[72:75], v[8:11]\n\t"
        "ds_read_b64_tr_b16 a[96:97], v103 offset:0\n\t"
        "ds_read_b64_tr_b16 a[98:99], v103 offset:512\n\t"
        "v_mfma_f32_16x16x32_bf16 v[12:15], a[76:79], v[72:75], v[12:15]\n\t"
        "ds_read_b64_tr_b16 a[100:101], v103 offset:8\n\t"
        "ds_read_b64_tr_b16 a[102:103], v103 offset:520\n\t"
        "v_mfma_f32_16x16x32_bf16 v[16:19], v[72:75], a[80:83], v[16:19]\n\t"
        "ds_read_b64_tr_b16 a[104:105], v104 offset:0\n\t"
        "ds_read_b64_tr_b16 a[106:107], v104 offset:256\n\t"
        "v_mfma_f32_16x16x32_bf16 v[20:23], a[72:75], v[76:79], v[20:23]\n\t"
        "s_waitcnt lgkmcnt(5)\n\t"
        "ds_read_b64_tr_b16 a[108:109], v103 offset:4096\n\t"
        "v_mfma_f32_16x16x32_bf16 v[24:27], a[76:79], v[76:79], v[24:27]\n\t"
        "ds_read_b64_tr_b16 a[110:111], v103 offset:4608\n\t"
        "ds_read_b64_tr_b16 a[112:113], v103 offset:4104\n\t"
        "v_mfma_f32_16x16x32_bf16 v[28:31], v[76:79], a[80:83], v[28:31]\n\t"
        "ds_read_b64_tr_b16 a[114:115], v103 offset:4616\n\t"
        "ds_read_b64_tr_b16 a[116:117], v104 offset:2048\n\t"
        "v_mfma_f32_16x16x32_bf16 v[8:11], a[84:87], v[80:83], v[8:11]\n\t"
        "ds_read_b64_tr_b16 a[118:119], v104 offset:2304\n\t"
        "ds_read_b128 v[56:59], v105 offset:32768\n\t"
        "v_mfma_f32_16x16x32_bf16 v[12:15], a[88:91], v[80:83], v[12:15]\n\t"
        "ds_read_b128 v[60:63], v105 offset:34816\n\t"
        "ds_read_b128 v[64:67], v106 offset:32768\n\t"
        "v_mfma_f32_16x16x32_bf16 v[16:19], v[80:83], a[92:95], v[16:19]\n\t"
        "ds_read_b128 v[68:71], v106 offset:34816\n\t"
        "v_mfma_f32_16x16x32_bf16 v[20:23], a[84:87], v[84:87], v[20:23]\n\t"
        "v_mfma_f32_16x16x32_bf16 v[24:27], a[88:91], v[84:87], v[24:27]\n\t"
        "v_mfma_f32_16x16x32_bf16 v[28:31], v[84:87], a[92:95], v[28:31]\n\t"
        "s_add_u32 m0, %[ld], 65536\n\t"
        "s_add_u32 %[t0], %[xo], 1024\n\t"
        "buffer_load_dwordx4 v100, %[sx], %[t0] offen lds\n\t"
        "s_add_u32 m0, %[ld], 66560\n\t"
        "s_add_u32 %[t0], %[xo], 17408\n\t"
        "buffer_load_dwordx4 v100, %[sx], %[t0] offen lds\n\t"
        "s_add_u32 m0, %[ld], 73728\n\t"
        "s_add_u32 %[t0], %[xo], 1152\n\t"
        "buffer_load_dwordx4 v100, %[sx], %[t0] offen lds\n\t"
        "s_add_u32 m0, %[ld], 74752\n\t"
        "s_add_u32 %[t0], %[xo], 17536\n\t"
        "buffer_load_dwordx4 v100, %[sx], %[t0] offen lds\n\t"
        "s_add_u32 m0, %[ld], 81920\n\t"
        "s_add_u32 %[t0], %[xo], 1280\n\t"
        "buffer_load_dwordx4 v100, %[sx], %[t0] offen lds\n\t"
        "s_add_u32 m0, %[ld], 82944\n\t"
        "s_add_u32 %[t0], %[xo], 17664\n\t"
        "buffer_load_dwordx4 v100, %[sx], %[t0] offen lds\n\t"
        "s_add_u32 m0, %[ld], 90112\n\t"
        "s_add_u32 %[t0], %[xo], 1408\n\t"
        "buffer_load_dwordx4 v100, %[sx], %[t0] offen lds\n\t"
        "s_add_u32 m0, %[ld], 91136\n\t"
        "s_add_u32 %[t0], %[xo], 17792\n\t"
        "buffer_load_dwordx4 v100, %[sx], %[t0] offen lds\n\t"
        "s_mov_b32 %[t0], 4194304\n\t"
        "buffer_load_dwordx4 v[160:163], v101, %[sq], %[t0] offen\n\t"
        "buffer_load_dwordx4 v[168:171], v112, %[sv], %[t0] offen\n\t"
        "s_mov_b32 %[t0], 4456448\n\t"
        "buffer_load_dwordx4 v[164:167], v101, %[sq], %[t0] offen\n\t"
        "s_waitcnt vmcnt(17)\n\t"
        "ds_write_b128 v102, a[252:255] offset:16384\n\t"
        "ds_write_b128 v102, v[128:131] offset:20480\n\t"
        "ds_write_b128 v116, v[132:135] offset:24576\n\t"
        "s_waitcnt lgkmcnt(0)\n\t"
        "s_barrier\n\t"
        "v_mfma_f32_16x16x32_bf16 v[8:11], a[96:99], v[56:59], v[8:11]\n\t"
        "ds_read_b64_tr_b16 a[120:121], v103 offset:16384\n\t"
        "ds_read_b64_tr_b16 a[122:123], v103 offset:16896\n\t"
        "v_mfma_f32_16x16x32_bf16 v[12:15], a[100:103], v[56:59], v[12:15]\n\t"
        "ds_read_b64_tr_b16 a[124:125], v103 offset:16392\n\t"
        "ds_read_b64_tr_b16 a[126:127], v103 offset:16904\n\t"
        "v_mfma_f32_16x16x32_bf16 v[16:19], v[56:59], a[104:107], v[16:19]\n\t"
        "ds_read_b64_tr_b16 a[128:129], v104 offset:16384\n\t"
        "ds_read_b64_tr_b16 a[130:131], v104 offset:16640\n\t"
        "v_mfma_f32_16x16x32_bf16 v[20:23], a[96:99], v[60:63], v[20:23]\n\t"
        "s_waitcnt lgkmcnt(5)\n\t"
        "ds_read_b64_tr_b16 a[132:133], v103 offset:20480\n\t"
        "v_mfma_f32_16x16x32_bf16 v[24:27], a[100:103], v[60:63], v[24:27]\n\t"
        "ds_read_b64_tr_b16 a[134:135], v103 offset:20992\n\t"
        "ds_read_b64_tr_b16 a[136:137], v103 offset:20488\n\t"
        "v_mfma_f32_16x16x32_bf16 v[28:31], v[60:63], a[104:107], v[28:31]\n\t"
        "ds_read_b64_tr_b16 a[138:139], v103 offset:21000\n\t"
        "ds_read_b64_tr_b16 a[140:141], v104 offset:18432\n\t"
        "v_mfma_f32_16x16x32_bf16 v[8:11], a[108:111], v[64:67], v[8:11]\n\t"
        "ds_read_b64_tr_b16 a[142:143], v104 offset:18688\n\t"
        "ds_read_b128 v[72:75], v105 offset:40960\n\t"
        "v_mfma_f32_16x16x32_bf16 v[12:15], a[112:115], v[64:67], v[12:15]\n\t"
        "ds_read_b128 v[76:79], v105 offset:43008\n\t"
        "ds_read_b128 v[80:83], v106 offset:40960\n\t"
        "v_mfma_f32_16x16x32_bf16 v[16:19], v[64:67], a[116:119], v[16:19]\n\t"
        "ds_read_b128 v[84:87], v106 offset:43008\n\t"
        "v_mfma_f32_16x16x32_bf16 v[20:23], a[108:111], v[68:71], v[20:23]\n\t"
        "v_mfma_f32_16x16x32_bf16 v[24:27], a[112:115], v[68:71], v[24:27]\n\t"
        "v_mfma_f32_16x16x32_bf16 v[28:31], v[68:71], a[116:119], v[28:31]\n\t"
        "s_mov_b32 %[t0], 4718592\n\t"
        "buffer_load_dwordx4 v[172:175], v101, %[sq], %[t0] offen\n\t"
        "buffer_load_dwordx4 v[180:183], v112, %[sv], %[t0] offen\n\t"
        "s_mov_b32 %[t0], 4980736\n\t"
        "buffer_load_dwordx4 v[176:179], v101, %[sq], %[t0] offen\n\t"
        "s_waitcnt vmcnt(17)\n\t"
        "ds_write_b128 v102, v[136:139] offset:0\n\t"
        "ds_write_b128 v102, v[140:143] offset:4096\n\t"
        "ds_write_b128 v116, v[144:147] offset:8192\n\t"
        "s_waitcnt lgkmcnt(0)\n\t"
        "s_barrier\n\t"
        "v_mfma_f32_16x16x32_bf16 v[8:11], a[120:123], v[72:75], v[8:11]\n\t"
        "ds_read_b64_tr_b16 a[144:145], v103 offset:0\n\t"
        "ds_read_b64_tr_b16 a[146:147], v103 offset:512\n\t"
        "v_mfma_f32_16x16x32_bf16 v[12:15], a[124:127], v[72:75], v[12:15]\n\t"
        "ds_read_b64_tr_b16 a[148:149], v103 offset:8\n\t"
        "ds_read_b64_tr_b16 a[150:151], v103 offset:520\n\t"
        "v_mfma_f32_16x16x32_bf16 v[16:19], v[72:75], a[128:131], v[16:19]\n\t"
        "ds_read_b64_tr_b16 a[152:153], v104 offset:0\n\t"
        "ds_read_b64_tr_b16 a[154:155], v104 offset:256\n\t"
        "v_mfma_f32_16x16x32_bf16 v[20:23], a[120:123], v[76:79], v[20:23]\n\t"
        "s_waitcnt lgkmcnt(5)\n\t"
        "ds_read_b64_tr_b16 a[156:157], v103 offset:4096\n\t"
        "v_mfma_f32_16x16x32_bf16 v[24:27], a[124:127], v[76:79], v[24:27]\n\t"
        "ds_read_b64_tr_b16 a[158:159], v103 offset:4608\n\t"
        "ds_read_b64_tr_b16 a[160:161], v103 offset:4104\n\t"
        "v_mfma_f32_16x16x32_bf16 v[28:31], v[76:79], a[128:131], v[28:31]\n\t"
        "ds_read_b64_tr_b16 a[162:163], v103 offset:4616\n\t"
        "ds_read_b64_tr_b16 a[164:165], v104 offset:2048\n\t"
        "v_mfma_f32_16x16x32_bf16 v[8:11], a[132:135], v[80:83], v[8:11]\n\t"
        "ds_read_b64_tr_b16 a[166:167], v104 offset:2304\n\t"
        "ds_read_b128 v[56:59], v105 offset:49152\n\t"
        "v_mfma_f32_16x16x32_bf16 v[12:15], a[136:139], v[80:83], v[12:15]\n\t"
        "ds_read_b128 v[60:63], v105 offset:51200\n\t"
        "ds_read_b128 v[64:67], v106 offset:49152\n\t"
        "v_mfma_f32_16x16x32_bf16 v[16:19], v[80:83], a[140:143], v[16:19]\n\t"
        "ds_read_b128 v[68:71], v106 offset:51200\n\t"
        "v_mfma_f32_16x16x32_bf16 v[20:23], a[132:135], v[84:87], v[20:23]\n\t"
        "v_mfma_f32_16x16x32_bf16 v[24:27], a[136:139], v[84:87], v[24:27]\n\t"
        "v_mfma_f32_16x16x32_bf16 v[28:31], v[84:87], a[140:143], v[28:31]\n\t"
        "s_mov_b32 %[t0], 5242880\n\t"
        "buffer_load_dwordx4 v[184:187], v101, %[sq], %[t0] offen\n\t"
        "buffer_load_dwordx4 v[192:195], v112, %[sv], %[t0] offen\n\t"
        "s_mov_b32 %[t0], 5505024\n\t"
        "buffer_load_dwordx4 v[188:191], v101, %[sq], %[t0] offen\n\t"
        "s_waitcnt vmcnt(17)\n\t"
        "ds_write_b128 v102, v[148:151] offset:16384\n\t"
        "ds_write_b128 v102, v[152:155] offset:20480\n\t"
        "ds_write_b128 v116, v[156:159] offset:24576\n\t"
        "s_waitcnt lgkmcnt(0)\n\t"
        "s_barrier\n\t"
        "v_mfma_f32_16x16x32_bf16 v[8:11], a[144:147], v[56:59], v[8:11]\n\t"
        "ds_read_b64_tr_b16 a[168:169], v103 offset:16384\n\t"
        "ds_read_b64_tr_b16 a[170:171], v103 offset:16896\n\t"
        "v_mfma_f32_16x16x32_bf16 v[12:15], a[148:151], v[56:59], v[12:15]\n\t"
        "ds_read_b64_tr_b16 a[172:173], v103 offset:16392\n\t"
        "ds_read_b64_tr_b16 a[174:175], v103 offset:16904\n\t"
        "v_mfma_f32_16x16x32_bf16 v[16:19], v[56:59], a[152:155], v[16:19]\n\t"
        "ds_read_b64_tr_b16 a[176:177], v104 offset:16384\n\t"
        "ds_read_b64_tr_b16 a[178:179], v104 offset:16640\n\t"
        "v_mfma_f32_16x16x32_bf16 v[20:23], a[144:147], v[60:63], v[20:23]\n\t"
        "s_waitcnt lgkmcnt(5)\n\t"
        "ds_read_b64_tr_b16 a[180:181], v103 offset:20480\n\t"
        "v_mfma_f32_16x16x32_bf16 v[24:27], a[148:151], v[60:63], v[24:27]\n\t"
        "ds_read_b64_tr_b16 a[182:183], v103 offset:20992\n\t"
        "ds_read_b64_tr_b16 a[184:185], v103 offset:20488\n\t"
        "v_mfma_f32_16x16x32_bf16 v[28:31], v[60:63], a[152:155], v[28:31]\n\t"
        "ds_read_b64_tr_b16 a[186:187], v103 offset:21000\n\t"
        "ds_read_b64_tr_b16 a[188:189], v104 offset:18432\n\t"
        "v_mfma_f32_16x16x32_bf16 v[8:11], a[156:159], v[64:67], v[8:11]\n\t"
        "ds_read_b64_tr_b16 a[190:191], v104 offset:18688\n\t"
        "ds_read_b128 v[72:75], v105 offset:57344\n\t"
        "v_mfma_f32_16x16x32_bf16 v[12:15], a[160:163], v[64:67], v[12:15]\n\t"
        "ds_read_b128 v[76:79], v105 offset:59392\n\t"
        "ds_read_b128 v[80:83], v106 offset:57344\n\t"
        "v_mfma_f32_16x16x32_bf16 v[16:19], v[64:67], a[164:167], v[16:19]\n\t"
        "ds_read_b128 v[84:87], v106 offset:59392\n\t"
        "v_mfma_f32_16x16x32_bf16 v[20:23], a[156:159], v[68:71], v[20:23]\n\t"
        "v_mfma_f32_16x16x32_bf16 v[24:27], a[160:163], v[68:71], v[24:27]\n\t"
        "v_mfma_f32_16x16x32_bf16 v[28:31], v[68:71], a[164:167], v[28:31]\n\t"
        "s_mov_b32 %[t0], 5767168\n\t"
        "buffer_load_dwordx4 v[196:199], v101, %[sq], %[t0] offen\n\t"
        "buffer_load_dwordx4 v[204:207], v112, %[sv], %[t0] offen\n\t"
        "s_mov_b32 %[t0], 6029312\n\t"
        "buffer_load_dwordx4 v[200:203], v101, %[sq], %[t0] offen\n\t"
        "s_waitcnt vmcnt(9)\n\t"
        "ds_write_b128 v102, v[160:163] offset:0\n\t"
        "ds_write_b128 v102, v[164:167] offset:4096\n\t"
        "ds_write_b128 v116, v[168:171] offset:8192\n\t"
        "s_waitcnt lgkmcnt(0)\n\t"
        "s_barrier\n\t"
        "v_mfma_f32_16x16x32_bf16 v[8:11], a[168:171], v[72:75], v[8:11]\n\t"
        "ds_read_b64_tr_b16 a[192:193], v103 offset:0\n\t"
        "ds_read_b64_tr_b16 a[194:195], v103 offset:512\n\t"
        "v_mfma_f32_16x16x32_bf16 v[12:15], a[172:175], v[72:75], v[12:15]\n\t"
        "ds_read_b64_tr_b16 a[196:197], v103 offset:8\n\t"
        "ds_read_b64_tr_b16 a[198:199], v103 offset:520\n\t"
        "v_mfma_f32_16x16x32_bf16 v[16:19], v[72:75], a[176:179], v[16:19]\n\t"
        "ds_read_b64_tr_b16 a[200:201], v104 offset:0\n\t"
        "ds_read_b64_tr_b16 a[202:203], v104 offset:256\n\t"
        "v_mfma_f32_16x16x32_bf16 v[20:23], a[168:171], v[76:79], v[20:23]\n\t"
        "s_waitcnt lgkmcnt(5)\n\t"
        "ds_read_b64_tr_b16 a[204:205], v103 offset:4096\n\t"
        "v_mfma_f32_16x16x32_bf16 v[24:27], a[172:175], v[76:79], v[24:27]\n\t"
        "ds_read_b64_tr_b16 a[206:207], v103 offset:4608\n\t"
        "ds_read_b64_tr_b16 a[208:209], v103 offset:4104\n\t"
        "v_mfma_f32_16x16x32_bf16 v[28:31], v[76:79], a[176:179], v[28:31]\n\t"
        "ds_read_b64_tr_b16 a[210:211], v103 offset:4616\n\t"
        "ds_read_b64_tr_b16 a[212:213], v104 offset:2048\n\t"
        "v_mfma_f32_16x16x32_bf16 v[8:11], a[180:183], v[80:83], v[8:11]\n\t"
        "ds_read_b64_tr_b16 a[214:215], v104 offset:2304\n\t"
        "ds_read_b128 v[56:59], v107 offset:0\n\t"
        "v_mfma_f32_16x16x32_bf16 v[12:15], a[184:187], v[80:83], v[12:15]\n\t"
        "ds_read_b128 v[60:63], v107 offset:2048\n\t"
        "ds_read_b128 v[64:67], v108 offset:0\n\t"
        "v_mfma_f32_16x16x32_bf16 v[16:19], v[80:83], a[188:191], v[16:19]\n\t"
        "ds_read_b128 v[68:71], v108 offset:2048\n\t"
        "v_mfma_f32_16x16x32_bf16 v[20:23], a[180:183], v[84:87], v[20:23]\n\t"
        "v_mfma_f32_16x16x32_bf16 v[24:27], a[184:187], v[84:87], v[24:27]\n\t"
        "v_mfma_f32_16x16x32_bf16 v[28:31], v[84:87], a[188:191], v[28:31]\n\t"
        "s_add_u32 m0, %[ld], 98304\n\t"
        "s_add_u32 %[t0], %[xo], 1536\n\t"
        "buffer_load_dwordx4 v100, %[sx], %[t0] offen lds\n\t"
        "s_add_u32 m0, %[ld], 99328\n\t"
        "s_add_u32 %[t0], %[xo], 17920\n\t"
        "buffer_load_dwordx4 v100, %[sx], %[t0] offen lds\n\t"
        "s_add_u32 m0, %[ld], 106496\n\t"
        "s_add_u32 %[t0], %[xo], 1664\n\t"
        "buffer_load_dwordx4 v100, %[sx], %[t0] offen lds\n\t"
        "s_add_u32 m0, %[ld], 107520\n\t"
        "s_add_u32 %[t0], %[xo], 18048\n\t"
        "buffer_load_dwordx4 v100, %[sx], %[t0] offen lds\n\t"
        "s_add_u32 m0, %[ld], 114688\n\t"
        "s_add_u32 %[t0], %[xo], 1792\n\t"
        "buffer_load_dwordx4 v100, %[sx], %[t0] offen lds\n\t"
        "s_add_u32 m0, %[ld], 115712\n\t"
        "s_add_u32 %[t0], %[xo], 18176\n\t"
        "buffer_load_dwordx4 v100, %[sx], %[t0] offen lds\n\t"
        "s_add_u32 m0, %[ld], 122880\n\t"
        "s_add_u32 %[t0], %[xo], 1920\n\t"
        "buffer_load_dwordx4 v100, %[sx], %[t0] offen lds\n\t"
        "s_add_u32 m0, %[ld], 123904\n\t"
        "s_add_u32 %[t0], %[xo], 18304\n\t"
        "buffer_load_dwordx4 v100, %[sx], %[t0] offen lds\n\t"
        "s_mov_b32 %[t0], 6291456\n\t"
        "buffer_load_dwordx4 v[208:211], v101, %[sq], %[t0] offen\n\t"
        "buffer_load_dwordx4 v[216:219], v112, %[sv], %[t0] offen\n\t"
        "s_mov_b32 %[t0], 6553600\n\t"
        "buffer_load_dwordx4 v[212:215], v101, %[sq], %[t0] offen\n\t"
        "s_waitcnt vmcnt(17)\n\t"
        "ds_write_b128 v102, v[172:175] offset:16384\n\t"
        "ds_write_b128 v102, v[176:179] offset:20480\n\t"
        "ds_write_b128 v116, v[180:183] offset:24576\n\t"
        "s_waitcnt lgkmcnt(0)\n\t"
        "s_barrier\n\t"
        "v_mfma_f32_16x16x32_bf16 v[8:11], a[192:195], v[56:59], v[8:11]\n\t"
        "ds_read_b64_tr_b16 a[216:217], v103 offset:16384\n\t"
        "ds_read_b64_tr_b16 a[218:219], v103 offset:16896\n\t"
        "v_mfma_f32_16x16x32_bf16 v[12:15], a[196:199], v[56:59], v[12:15]\n\t"
        "ds_read_b64_tr_b16 a[220:221], v103 offset:16392\n\t"
        "ds_read_b64_tr_b16 a[222:223], v103 offset:16904\n\t"
        "v_mfma_f32_16x16x32_bf16 v[16:19], v[56:59], a[200:203], v[16:19]\n\t"
        "ds_read_b64_tr_b16 a[224:225], v104 offset:16384\n\t"
        "ds_read_b64_tr_b16 a[226:227], v104 offset:16640\n\t"
        "v_mfma_f32_16x16x32_bf16 v[20:23], a[192:195], v[60:63], v[20:23]\n\t"
        "s_waitcnt lgkmcnt(5)\n\t"
        "ds_read_b64_tr_b16 a[228:229], v103 offset:20480\n\t"
        "v_mfma_f32_16x16x32_bf16 v[24:27], a[196:199], v[60:63], v[24:27]\n\t"
        "ds_read_b64_tr_b16 a[230:231], v103 offset:20992\n\t"
        "ds_read_b64_tr_b16 a[232:233], v103 offset:20488\n\t"
        "v_mfma_f32_16x16x32_bf16 v[28:31], v[60:63], a[200:203], v[28:31]\n\t"
        "ds_read_b64_tr_b16 a[234:235], v103 offset:21000\n\t"
        "ds_read_b64_tr_b16 a[236:237], v104 offset:18432\n\t"
        "v_mfma_f32_16x16x32_bf16 v[8:11], a[204:207], v[64:67], v[8:11]\n\t"
        "ds_read_b64_tr_b16 a[238:239], v104 offset:18688\n\t"
        "ds_read_b128 v[72:75], v107 offset:8192\n\t"
        "v_mfma_f32_16x16x32_bf16 v[12:15], a[208:211], v[64:67], v[12:15]\n\t"
        "ds_read_b128 v[76:79], v107 offset:10240\n\t"
        "ds_read_b128 v[80:83], v108 offset:8192\n\t"
        "v_mfma_f32_16x16x32_bf16 v[16:19], v[64:67], a[212:215], v[16:19]\n\t"
        "ds_read_b128 v[84:87], v108 offset:10240\n\t"
        "v_mfma_f32_16x16x32_bf16 v[20:23], a[204:207], v[68:71], v[20:23]\n\t"
        "v_mfma_f32_16x16x32_bf16 v[24:27], a[208:211], v[68:71], v[24:27]\n\t"
        "v_mfma_f32_16x16x32_bf16 v[28:31], v[68:71], a[212:215], v[28:31]\n\t"
        "s_mov_b32 %[t0], 6815744\n\t"
        "buffer_load_dwordx4 v[220:223], v101, %[sq], %[t0] offen\n\t"
        "buffer_load_dwordx4 v[228:231], v112, %[sv], %[t0] offen\n\t"
        "s_mov_b32 %[t0], 7077888\n\t"
        "buffer_load_dwordx4 v[224:227], v101, %[sq], %[t0] offen\n\t"
        "s_waitcnt vmcnt(17)\n\t"
        "ds_write_b128 v102, v[184:187] offset:0\n\t"
        "ds_write_b128 v102, v[188:191] offset:4096\n\t"
        "ds_write_b128 v116, v[192:195] offset:8192\n\t"
        "s_waitcnt lgkmcnt(0)\n\t"
        "s_barrier\n\t"
        "v_mfma_f32_16x16x32_bf16 v[8:11], a[216:219], v[72:75], v[8:11]\n\t"
        "ds_read_b64_tr_b16 a[240:241], v103 offset:0\n\t"
        "ds_read_b64_tr_b16 a[242:243], v103 offset:512\n\t"
        "v_mfma_f32_16x16x32_bf16 v[12:15], a[220:223], v[72:75], v[12:15]\n\t"
        "ds_read_b64_tr_b16 a[244:245], v103 offset:8\n\t"
        "ds_read_b64_tr_b16 a[246:247], v103 offset:520\n\t"
        "v_mfma_f32_16x16x32_bf16 v[16:19], v[72:75], a[224:227], v[16:19]\n\t"
        "ds_read_b64_tr_b16 a[248:249], v104 offset:0\n\t"
        "ds_read_b64_tr_b16 a[250:251], v104 offset:256\n\t"
        "v_mfma_f32_16x16x32_bf16 v[20:23], a[216:219], v[76:79], v[20:23]\n\t"
        "s_waitcnt lgkmcnt(5)\n\t"
        "ds_read_b64_tr_b16 a[252:253], v103 offset:4096\n\t"
        "v_mfma_f32_16x16x32_bf16 v[24:27], a[220:223], v[76:79], v[24:27]\n\t"
        "ds_read_b64_tr_b16 a[254:255], v103 offset:4608\n\t"
        "ds_read_b64_tr_b16 v[128:129], v103 offset:4104\n\t"
        "v_mfma_f32_16x16x32_bf16 v[28:31], v[76:79], a[224:227], v[28:31]\n\t"
        "ds_read_b64_tr_b16 v[130:131], v103 offset:4616\n\t"
        "ds_read_b64_tr_b16 v[132:133], v104 offset:2048\n\t"
        "v_mfma_f32_16x16x32_bf16 v[8:11], a[228:231], v[80:83], v[8:11]\n\t"
        "ds_read_b64_tr_b16 v[134:135], v104 offset:2304\n\t"
        "ds_read_b128 v[56:59], v107 offset:16384\n\t"
        "v_mfma_f32_16x16x32_bf16 v[12:15], a[232:235], v[80:83], v[12:15]\n\t"
        "ds_read_b128 v[60:63], v107 offset:18432\n\t"
        "ds_read_b128 v[64:67], v108 offset:16384\n\t"
        "v_mfma_f32_16x16x32_bf16 v[16:19], v[80:83], a[236:239], v[16:19]\n\t"
        "ds_read_b128 v[68:71], v108 offset:18432\n\t"
        "v_mfma_f32_16x16x32_bf16 v[20:23], a[228:231], v[84:87], v[20:23]\n\t"
        "v_mfma_f32_16x16x32_bf16 v[24:27], a[232:235], v[84:87], v[24:27]\n\t"
        "v_mfma_f32_16x16x32_bf16 v[28:31], v[84:87], a[236:239], v[28:31]\n\t"
        "s_mov_b32 %[t0], 7340032\n\t"
        "buffer_load_dwordx4 v[232:235], v101, %[sq], %[t0] offen\n\t"
        "buffer_load_dwordx4 v[240:243], v112, %[sv], %[t0] offen\n\t"
        "s_mov_b32 %[t0], 7602176\n\t"
        "buffer_load_dwordx4 v[236:239], v101, %[sq], %[t0] offen\n\t"
        "s_waitcnt vmcnt(17)\n\t"
        "ds_write_b128 v102, v[196:199] offset:16384\n\t"
        "ds_write_b128 v102, v[200:203] offset:20480\n\t"
        "ds_write_b128 v116, v[204:207] offset:24576\n\t"
        "s_waitcnt lgkmcnt(0)\n\t"
        "s_barrier\n\t"
        "v_mfma_f32_16x16x32_bf16 v[8:11], a[240:243], v[56:59], v[8:11]\n\t"
        "ds_read_b64_tr_b16 v[136:137], v103 offset:16384\n\t"
        "ds_read_b64_tr_b16 v[138:139], v103 offset:16896\n\t"
        "v_mfma_f32_16x16x32_bf16 v[12:15], a[244:247], v[56:59], v[12:15]\n\t"
        "ds_read_b64_tr_b16 v[140:141], v103 offset:16392\n\t"
        "ds_read_b64_tr_b16 v[142:143], v103 offset:16904\n\t"
        "v_mfma_f32_16x16x32_bf16 v[16:19], v[56:59], a[248:251], v[16:19]\n\t"
        "ds_read_b64_tr_b16 v[144:145], v104 offset:16384\n\t"
        "ds_read_b64_tr_b16 v[146:147], v104 offset:16640\n\t"
        "v_mfma_f32_16x16x32_bf16 v[20:23], a[240:243], v[60:63], v[20:23]\n\t"
        "s_waitcnt lgkmcnt(5)\n\t"
        "ds_read_b64_tr_b16 v[148:149], v103 offset:20480\n\t"
        "v_mfma_f32_16x16x32_bf16 v[24:27], a[244:247], v[60:63], v[24:27]\n\t"
        "ds_read_b64_tr_b16 v[150:151], v103 offset:20992\n\t"
        "ds_read_b64_tr_b16 v[152:153], v103 offset:20488\n\t"
        "v_mfma_f32_16x16x32_bf16 v[28:31], v[60:63], a[248:251], v[28:31]\n\t"
        "ds_read_b64_tr_b16 v[154:155], v103 offset:21000\n\t"
        "ds_read_b64_tr_b16 v[156:157], v104 offset:18432\n\t"
        "v_mfma_f32_16x16x32_bf16 v[8:11], a[252:255], v[64:67], v[8:11]\n\t"
        "ds_read_b64_tr_b16 v[158:159], v104 offset:18688\n\t"
        "ds_read_b128 v[72:75], v107 offset:24576\n\t"
        "v_mfma_f32_16x16x32_bf16 v[12:15], v[128:131], v[64:67], v[12:15]\n\t"
        "ds_read_b128 v[76:79], v107 offset:26624\n\t"
        "ds_read_b128 v[80:83], v108 offset:24576\n\t"
        "v_mfma_f32_16x16x32_bf16 v[16:19], v[64:67], v[132:135], v[16:19]\n\t"
        "ds_read_b128 v[84:87], v108 offset:26624\n\t"
        "v_mfma_f32_16x16x32_bf16 v[20:23], a[252:255], v[68:71], v[20:23]\n\t"
        "v_mfma_f32_16x16x32_bf16 v[24:27], v[128:131], v[68:71], v[24:27]\n\t"
        "v_mfma_f32_16x16x32_bf16 v[28:31], v[68:71], v[132:135], v[28:31]\n\t"
        "s_mov_b32 %[t0], 7864320\n\t"
        "buffer_load_dwordx4 v[244:247], v101, %[sq], %[t0] offen\n\t"
        "buffer_load_dwordx4 v[252:255], v112, %[sv], %[t0] offen\n\t"
        "s_mov_b32 %[t0], 8126464\n\t"
        "buffer_load_dwordx4 v[248:251], v101, %[sq], %[t0] offen\n\t"
        "s_waitcnt vmcnt(9)\n\t"
        "ds_write_b128 v102, v[208:211] offset:0\n\t"
        "ds_write_b128 v102, v[212:215] offset:4096\n\t"
        "ds_write_b128 v116, v[216:219] offset:8192\n\t"
        "s_waitcnt lgkmcnt(0)\n\t"
        "s_barrier\n\t"
        "v_mfma_f32_16x16x32_bf16 v[8:11], v[136:139], v[72:75], v[8:11]\n\t"
        "ds_read_b64_tr_b16 v[160:161], v103 offset:0\n\t"
        "ds_read_b64_tr_b16 v[162:163], v103 offset:512\n\t"
        "v_mfma_f32_16x16x32_bf16 v[12:15], v[140:143], v[72:75], v[12:15]\n\t"
        "ds_read_b64_tr_b16 v[164:165], v103 offset:8\n\t"
        "ds_read_b64_tr_b16 v[166:167], v103 offset:520\n\t"
        "v_mfma_f32_16x16x32_bf16 v[16:19], v[72:75], v[144:147], v[16:19]\n\t"
        "ds_read_b64_tr_b16 v[168:169], v104 offset:0\n\t"
        "ds_read_b64_tr_b16 v[170:171], v104 offset:256\n\t"
        "v_mfma_f32_16x16x32_bf16 v[20:23], v[136:139], v[76:79], v[20:23]\n\t"
        "s_waitcnt lgkmcnt(5)\n\t"
        "ds_read_b64_tr_b16 v[172:173], v103 offset:4096\n\t"
        "v_mfma_f32_16x16x32_bf16 v[24:27], v[140:143], v[76:79], v[24:27]\n\t"
        "ds_read_b64_tr_b16 v[174:175], v103 offset:4608\n\t"
        "ds_read_b64_tr_b16 v[176:177], v103 offset:4104\n\t"
        "v_mfma_f32_16x16x32_bf16 v[28:31], v[76:79], v[144:147], v[28:31]\n\t"
        "ds_read_b64_tr_b16 v[178:179], v103 offset:4616\n\t"
        "ds_read_b64_tr_b16 v[180:181], v104 offset:2048\n\t"
        "v_mfma_f32_16x16x32_bf16 v[8:11], v[148:151], v[80:83], v[8:11]\n\t"
        "ds_read_b64_tr_b16 v[182:183], v104 offset:2304\n\t"
        "ds_read_b128 v[56:59], v107 offset:32768\n\t"
        "v_mfma_f32_16x16x32_bf16 v[12:15], v[152:155], v[80:83], v[12:15]\n\t"
        "ds_read_b128 v[60:63], v107 offset:34816\n\t"
        "ds_read_b128 v[64:67], v108 offset:32768\n\t"
        "v_mfma_f32_16x16x32_bf16 v[16:19], v[80:83], v[156:159], v[16:19]\n\t"
        "ds_read_b128 v[68:71], v108 offset:34816\n\t"
        "v_mfma_f32_16x16x32_bf16 v[20:23], v[148:151], v[84:87], v[20:23]\n\t"
        "v_mfma_f32_16x16x32_bf16 v[24:27], v[152:155], v[84:87], v[24:27]\n\t"
        "v_mfma_f32_16x16x32_bf16 v[28:31], v[84:87], v[156:159], v[28:31]\n\t"
        "s_mov_b32 %[xo], 0x20000\n\t"
        "s_add_u32 m0, %[ld], 0\n\t"
        "s_add_u32 %[t0], %[xo], 0\n\t"
        "buffer_load_dwordx4 v100, %[sx], %[t0] offen lds\n\t"
        "s_add_u32 m0, %[ld], 1024\n\t"
        "s_add_u32 %[t0], %[xo], 16384\n\t"
        "buffer_load_dwordx4 v100, %[sx], %[t0] offen lds\n\t"
        "s_add_u32 m0, %[ld], 8192\n\t"
        "s_add_u32 %[t0], %[xo], 128\n\t"
        "buffer_load_dwordx4 v100, %[sx], %[t0] offen lds\n\t"
        "s_add_u32 m0, %[ld], 9216\n\t"
        "s_add_u32 %[t0], %[xo], 16512\n\t"
        "buffer_load_dwordx4 v100, %[sx], %[t0] offen lds\n\t"
        "s_add_u32 m0, %[ld], 16384\n\t"
        "s_add_u32 %[t0], %[xo], 256\n\t"
        "buffer_load_dwordx4 v100, %[sx], %[t0] offen lds\n\t"
        "s_add_u32 m0, %[ld], 17408\n\t"
        "s_add_u32 %[t0], %[xo], 16640\n\t"
        "buffer_load_dwordx4 v100, %[sx], %[t0] offen lds\n\t"
        "s_add_u32 m0, %[ld], 24576\n\t"
        "s_add_u32 %[t0], %[xo], 384\n\t"
        "buffer_load_dwordx4 v100, %[sx], %[t0] offen lds\n\t"
        "s_add_u32 m0, %[ld], 25600\n\t"
        "s_add_u32 %[t0], %[xo], 16768\n\t"
        "buffer_load_dwordx4 v100, %[sx], %[t0] offen lds\n\t"
        "s_mov_b32 %[xo], 0\n\t"
        "s_waitcnt vmcnt(14)\n\t"
        "ds_write_b128 v102, v[220:223] offset:16384\n\t"
        "ds_write_b128 v102, v[224:227] offset:20480\n\t"
        "ds_write_b128 v116, v[228:231] offset:24576\n\t"
        "s_waitcnt lgkmcnt(0)\n\t"
        "s_barrier\n\t"
        "v_mfma_f32_16x16x32_bf16 v[8:11], v[160:163], v[56:59], v[8:11]\n\t"
        "ds_read_b64_tr_b16 v[184:185], v103 offset:16384\n\t"
        "ds_read_b64_tr_b16 v[186:187], v103 offset:16896\n\t"
        "v_mfma_f32_16x16x32_bf16 v[12:15], v[164:167], v[56:59], v[12:15]\n\t"
        "ds_read_b64_tr_b16 v[188:189], v103 offset:16392\n\t"
        "ds_read_b64_tr_b16 v[190:191], v103 offset:16904\n\t"
        "v_mfma_f32_16x16x32_bf16 v[16:19], v[56:59], v[168:171], v[16:19]\n\t"
        "ds_read_b64_tr_b16 v[192:193], v104 offset:16384\n\t"
        "ds_read_b64_tr_b16 v[194:195], v104 offset:16640\n\t"
        "v_mfma_f32_16x16x32_bf16 v[20:23], v[160:163], v[60:63], v[20:23]\n\t"
        "s_waitcnt lgkmcnt(5)\n\t"
        "ds_read_b64_tr_b16 v[196:197], v103 offset:20480\n\t"
        "v_mfma_f32_16x16x32_bf16 v[24:27], v[164:167], v[60:63], v[24:27]\n\t"
        "ds_read_b64_tr_b16 v[198:199], v103 offset:20992\n\t"
        "ds_read_b64_tr_b16 v[200:201], v103 offset:20488\n\t"
        "v_mfma_f32_16x16x32_bf16 v[28:31], v[60:63], v[168:171], v[28:31]\n\t"
        "ds_read_b64_tr_b16 v[202:203], v103 offset:21000\n\t"
        "ds_read_b64_tr_b16 v[204:205], v104 offset:18432\n\t"
        "v_mfma_f32_16x16x32_bf16 v[8:11], v[172:175], v[64:67], v[8:11]\n\t"
        "ds_read_b64_tr_b16 v[206:207], v104 offset:18688\n\t"
        "ds_read_b128 v[72:75], v107 offset:40960\n\t"
        "v_mfma_f32_16x16x32_bf16 v[12:15], v[176:179], v[64:67], v[12:15]\n\t"
        "ds_read_b128 v[76:79], v107 offset:43008\n\t"
        "ds_read_b128 v[80:83], v108 offset:40960\n\t"
        "v_mfma_f32_16x16x32_bf16 v[16:19], v[64:67], v[180:183], v[16:19]\n\t"
        "ds_read_b128 v[84:87], v108 offset:43008\n\t"
        "v_mfma_f32_16x16x32_bf16 v[20:23], v[172:175], v[68:71], v[20:23]\n\t"
        "v_mfma_f32_16x16x32_bf16 v[24:27], v[176:179], v[68:71], v[24:27]\n\t"
        "v_mfma_f32_16x16x32_bf16 v[28:31], v[68:71], v[180:183], v[28:31]\n\t"
        "s_waitcnt vmcnt(11)\n\t"
        "ds_write_b128 v102, v[232:235] offset:0\n\t"
        "ds_write_b128 v102, v[236:239] offset:4096\n\t"
        "ds_write_b128 v116, v[240:243] offset:8192\n\t"
        "s_waitcnt lgkmcnt(0)\n\t"
        "s_barrier\n\t"
        "v_mfma_f32_16x16x32_bf16 v[8:11], v[184:187], v[72:75], v[8:11]\n\t"
        "ds_read_b64_tr_b16 v[208:209], v103 offset:0\n\t"
        "ds_read_b64_tr_b16 v[210:211], v103 offset:512\n\t"
        "v_mfma_f32_16x16x32_bf16 v[12:15], v[188:191], v[72:75], v[12:15]\n\t"
        "ds_read_b64_tr_b16 v[212:213], v103 offset:8\n\t"
        "ds_read_b64_tr_b16 v[214:215], v103 offset:520\n\t"
        "v_mfma_f32_16x16x32_bf16 v[16:19], v[72:75], v[192:195], v[16:19]\n\t"
        "ds_read_b64_tr_b16 v[216:217], v104 offset:0\n\t"
        "ds_read_b64_tr_b16 v[218:219], v104 offset:256\n\t"
        "v_mfma_f32_16x16x32_bf16 v[20:23], v[184:187], v[76:79], v[20:23]\n\t"
        "s_waitcnt lgkmcnt(5)\n\t"
        "ds_read_b64_tr_b16 v[220:221], v103 offset:4096\n\t"
        "v_mfma_f32_16x16x32_bf16 v[24:27], v[188:191], v[76:79], v[24:27]\n\t"
        "ds_read_b64_tr_b16 v[222:223], v103 offset:4608\n\t"
        "ds_read_b64_tr_b16 v[224:225], v103 offset:4104\n\t"
        "v_mfma_f32_16x16x32_bf16 v[28:31], v[76:79], v[192:195], v[28:31]\n\t"
        "ds_read_b64_tr_b16 v[226:227], v103 offset:4616\n\t"
        "ds_read_b64_tr_b16 v[228:229], v104 offset:2048\n\t"
        "v_mfma_f32_16x16x32_bf16 v[8:11], v[196:199], v[80:83], v[8:11]\n\t"
        "ds_read_b64_tr_b16 v[230:231], v104 offset:2304\n\t"
        "ds_read_b128 v[56:59], v107 offset:49152\n\t"
        "v_mfma_f32_16x16x32_bf16 v[12:15], v[200:203], v[80:83], v[12:15]\n\t"
        "ds_read_b128 v[60:63], v107 offset:51200\n\t"
        "ds_read_b128 v[64:67], v108 offset:49152\n\t"
        "v_mfma_f32_16x16x32_bf16 v[16:19], v[80:83], v[204:207], v[16:19]\n\t"
        "ds_read_b128 v[68:71], v108 offset:51200\n\t"
        "v_mfma_f32_16x16x32_bf16 v[20:23], v[196:199], v[84:87], v[20:23]\n\t"
        "v_mfma_f32_16x16x32_bf16 v[24:27], v[200:203], v[84:87], v[24:27]\n\t"
        "v_mfma_f32_16x16x32_bf16 v[28:31], v[84:87], v[204:207], v[28:31]\n\t"
        "s_waitcnt vmcnt(8)\n\t"
        "ds_write_b128 v102, v[244:247] offset:16384\n\t"
        "ds_write_b128 v102, v[248:251] offset:20480\n\t"
        "ds_write_b128 v116, v[252:255] offset:24576\n\t"
        "s_waitcnt lgkmcnt(0)\n\t"
        "s_barrier\n\t"
        "v_mfma_f32_16x16x32_bf16 v[8:11], v[208:211], v[56:59], v[8:11]\n\t"
        "ds_read_b64_tr_b16 v[232:233], v103 offset:16384\n\t"
        "ds_read_b64_tr_b16 v[234:235], v103 offset:16896\n\t"
        "v_mfma_f32_16x16x32_bf16 v[12:15], v[212:215], v[56:59], v[12:15]\n\t"
        "ds_read_b64_tr_b16 v[236:237], v103 offset:16392\n\t"
        "ds_read_b64_tr_b16 v[238:239], v103 offset:16904\n\t"
        "v_mfma_f32_16x16x32_bf16 v[16:19], v[56:59], v[216:219], v[16:19]\n\t"
        "ds_read_b64_tr_b16 v[240:241], v104 offset:16384\n\t"
        "ds_read_b64_tr_b16 v[242:243], v104 offset:16640\n\t"
        "v_mfma_f32_16x16x32_bf16 v[20:23], v[208:211], v[60:63], v[20:23]\n\t"
        "s_waitcnt lgkmcnt(5)\n\t"
        "ds_read_b64_tr_b16 v[244:245], v103 offset:20480\n\t"
        "v_mfma_f32_16x16x32_bf16 v[24:27], v[212:215], v[60:63], v[24:27]\n\t"
        "ds_read_b64_tr_b16 v[246:247], v103 offset:20992\n\t"
        "ds_read_b64_tr_b16 v[248:249], v103 offset:20488\n\t"
        "v_mfma_f32_16x16x32_bf16 v[28:31], v[60:63], v[216:219], v[28:31]\n\t"
        "ds_read_b64_tr_b16 v[250:251], v103 offset:21000\n\t"
        "ds_read_b64_tr_b16 v[252:253], v104 offset:18432\n\t"
        "v_mfma_f32_16x16x32_bf16 v[8:11], v[220:223], v[64:67], v[8:11]\n\t"
        "ds_read_b64_tr_b16 v[254:255], v104 offset:18688\n\t"
        "ds_read_b128 v[72:75], v107 offset:57344\n\t"
        "v_mfma_f32_16x16x32_bf16 v[12:15], v[224:227], v[64:67], v[12:15]\n\t"
        "ds_read_b128 v[76:79], v107 offset:59392\n\t"
        "ds_read_b128 v[80:83], v108 offset:57344\n\t"
        "v_mfma_f32_16x16x32_bf16 v[16:19], v[64:67], v[228:231], v[16:19]\n\t"
        "ds_read_b128 v[84:87], v108 offset:59392\n\t"
        "v_mfma_f32_16x16x32_bf16 v[20:23], v[220:223], v[68:71], v[20:23]\n\t"
        "v_mfma_f32_16x16x32_bf16 v[24:27], v[224:227], v[68:71], v[24:27]\n\t"
        "v_mfma_f32_16x16x32_bf16 v[28:31], v[68:71], v[228:231], v[28:31]\n\t"
        "s_waitcnt vmcnt(0) lgkmcnt(0)\n\t"
        "s_barrier\n\t"
        "v_mfma_f32_16x16x32_bf16 v[8:11], v[232:235], v[72:75], v[8:11]\n\t"
        "v_mfma_f32_16x16x32_bf16 v[12:15], v[236:239], v[72:75], v[12:15]\n\t"
        "v_mfma_f32_16x16x32_bf16 v[16:19], v[72:75], v[240:243], v[16:19]\n\t"
        "v_mfma_f32_16x16x32_bf16 v[20:23], v[232:235], v[76:79], v[20:23]\n\t"
        "v_mfma_f32_16x16x32_bf16 v[24:27], v[236:239], v[76:79], v[24:27]\n\t"
        "v_mfma_f32_16x16x32_bf16 v[28:31], v[76:79], v[240:243], v[28:31]\n\t"
        "v_mfma_f32_16x16x32_bf16 v[8:11], v[244:247], v[80:83], v[8:11]\n\t"
        "v_mfma_f32_16x16x32_bf16 v[12:15], v[248:251], v[80:83], v[12:15]\n\t"
        "v_mfma_f32_16x16x32_bf16 v[16:19], v[80:83], v[252:255], v[16:19]\n\t"
        "v_mfma_f32_16x16x32_bf16 v[20:23], v[244:247], v[84:87], v[20:23]\n\t"
        "v_mfma_f32_16x16x32_bf16 v[24:27], v[248:251], v[84:87], v[24:27]\n\t"
        "v_mfma_f32_16x16x32_bf16 v[28:31], v[84:87], v[252:255], v[28:31]\n\t"
        "s_mov_b32 %[xo], 0x20000\n\t"
        "ds_read_b128 v[56:59], v105 offset:0\n\t"
        "ds_read_b128 v[60:63], v105 offset:2048\n\t"
        "ds_read_b128 v[64:67], v106 offset:0\n\t"
        "ds_read_b128 v[68:71], v106 offset:2048\n\t"
        "ds_read_b128 v[72:75], v105 offset:8192\n\t"
        "ds_read_b128 v[76:79], v105 offset:10240\n\t"
        "ds_read_b128 v[80:83], v106 offset:8192\n\t"
        "v_cvt_pk_bf16_f32 v88, v8, v9\n\t"
        "v_cvt_pk_bf16_f32 v89, v10, v11\n\t"
        "v_cvt_pk_bf16_f32 v90, v12, v13\n\t"
        "v_cvt_pk_bf16_f32 v91, v14, v15\n\t"
        "buffer_store_dwordx4 v[88:91], v109, %[sw], %[so] offen sc1\n\t"
        "v_cvt_pk_bf16_f32 v92, v20, v21\n\t"
        "v_cvt_pk_bf16_f32 v93, v22, v23\n\t"
        "v_cvt_pk_bf16_f32 v94, v24, v25\n\t"
        "v_cvt_pk_bf16_f32 v95, v26, v27\n\t"
        "buffer_store_dwordx4 v[92:95], v110, %[sw], %[so] offen sc1\n\t"
        "v_cvt_pk_bf16_f32 v96, v16, v17\n\t"
        "v_cvt_pk_bf16_f32 v97, v18, v19\n\t"
        "v_cvt_pk_bf16_f32 v98, v28, v29\n\t"
        "v_cvt_pk_bf16_f32 v99, v30, v31\n\t"
        "buffer_store_dwordx4 v[96:99], v111, %[sw], %[so] offen sc1\n\t"
        "s_mov_b32 %[so], 0xf0000000\n\t"
        "s_mov_b32 %[xo], 33792\n\t"
        "s_mov_b32 %[cn], 0\n\t"
        "LG_cal%=:\n\t"
        "buffer_load_dword v118, off, %[sw], %[xo] sc1\n\t"
        "s_waitcnt vmcnt(0)\n\t"
        "v_readfirstlane_b32 %[t0], v118\n\t"
        "s_cmp_lg_u32 %[t0], 0\n\t"
        "s_cbranch_scc1 LG_calok%=\n\t"
        "s_sleep 4\n\t"
        "s_add_u32 %[cn], %[cn], 1\n\t"
        "s_cmp_lt_u32 %[cn], 0x100000\n\t"
        "s_cbranch_scc1 LG_cal%=\n\t"
        "s_endpgm\n\t"
        "LG_calok%=:\n\t"
        "s_mov_b32 %[xo], 0x20000\n\t"
        "s_mov_b32 %[cn], 1\n\t"
        "v_mov_b32_e32 v119, %[cn]\n\t"
        "buffer_store_dword v119, v113, %[sw], 0 offen sc1\n\t"
        "s_add_u32 m0, %[ld], 32768\n\t"
        "s_add_u32 %[t0], %[xo], 512\n\t"
        "buffer_load_dwordx4 v100, %[sx], %[t0] offen lds\n\t"
        "s_add_u32 m0, %[ld], 33792\n\t"
        "s_add_u32 %[t0], %[xo], 16896\n\t"
        "buffer_load_dwordx4 v100, %[sx], %[t0] offen lds\n\t"
        "s_add_u32 m0, %[ld], 40960\n\t"
        "s_add_u32 %[t0], %[xo], 640\n\t"
        "buffer_load_dwordx4 v100, %[sx], %[t0] offen lds\n\t"
        "s_add_u32 m0, %[ld], 41984\n\t"
        "s_add_u32 %[t0], %[xo], 17024\n\t"
        "buffer_load_dwordx4 v100, %[sx], %[t0] offen lds\n\t"
        "s_add_u32 m0, %[ld], 49152\n\t"
        "s_add_u32 %[t0], %[xo], 768\n\t"
        "buffer_load_dwordx4 v100, %[sx], %[t0] offen lds\n\t"
        "s_add_u32 m0, %[ld], 50176\n\t"
        "s_add_u32 %[t0], %[xo], 17152\n\t"
        "buffer_load_dwordx4 v100, %[sx], %[t0] offen lds\n\t"
        "s_add_u32 m0, %[ld], 57344\n\t"
        "s_add_u32 %[t0], %[xo], 896\n\t"
        "buffer_load_dwordx4 v100, %[sx], %[t0] offen lds\n\t"
        "s_add_u32 m0, %[ld], 58368\n\t"
        "s_add_u32 %[t0], %[xo], 17280\n\t"
        "buffer_load_dwordx4 v100, %[sx], %[t0] offen lds\n\t"
        "s_add_u32 m0, %[ld], 65536\n\t"
        "s_add_u32 %[t0], %[xo], 1024\n\t"
        "buffer_load_dwordx4 v100, %[sx], %[t0] offen lds\n\t"
        "s_add_u32 m0, %[ld], 66560\n\t"
        "s_add_u32 %[t0], %[xo], 17408\n\t"
        "buffer_load_dwordx4 v100, %[sx], %[t0] offen lds\n\t"
        "s_add_u32 m0, %[ld], 73728\n\t"
        "s_add_u32 %[t0], %[xo], 1152\n\t"
        "buffer_load_dwordx4 v100, %[sx], %[t0] offen lds\n\t"
        "s_add_u32 m0, %[ld], 74752\n\t"
        "s_add_u32 %[t0], %[xo], 17536\n\t"
        "buffer_load_dwordx4 v100, %[sx], %[t0] offen lds\n\t"
        "s_add_u32 m0, %[ld], 81920\n\t"
        "s_add_u32 %[t0], %[xo], 1280\n\t"
        "buffer_load_dwordx4 v100, %[sx], %[t0] offen lds\n\t"
        "s_add_u32 m0, %[ld], 82944\n\t"
        "s_add_u32 %[t0], %[xo], 17664\n\t"
        "buffer_load_dwordx4 v100, %[sx], %[t0] offen lds\n\t"
        "s_add_u32 m0, %[ld], 90112\n\t"
        "s_add_u32 %[t0], %[xo], 1408\n\t"
        "buffer_load_dwordx4 v100, %[sx], %[t0] offen lds\n\t"
        "s_add_u32 m0, %[ld], 91136\n\t"
        "s_add_u32 %[t0], %[xo], 17792\n\t"
        "buffer_load_dwordx4 v100, %[sx], %[t0] offen lds\n\t"
        "s_add_u32 m0, %[ld], 98304\n\t"
        "s_add_u32 %[t0], %[xo], 1536\n\t"
        "buffer_load_dwordx4 v100, %[sx], %[t0] offen lds\n\t"
        "s_add_u32 m0, %[ld], 99328\n\t"
        "s_add_u32 %[t0], %[xo], 17920\n\t"
        "buffer_load_dwordx4 v100, %[sx], %[t0] offen lds\n\t"
        "s_add_u32 m0, %[ld], 106496\n\t"
        "s_add_u32 %[t0], %[xo], 1664\n\t"
        "buffer_load_dwordx4 v100, %[sx], %[t0] offen lds\n\t"
        "s_branch LG_mid%=\n\t"
        "LG_loop%=:\n\t"
        "s_waitcnt lgkmcnt(6)\n\t"
        "v_mfma_f32_16x16x32_bf16 v[8:11], a[0:3], v[56:59], 0\n\t"
        "v_mfma_f32_16x16x32_bf16 v[12:15], a[4:7], v[56:59], 0\n\t"
        "ds_read_b128 v[84:87], v106 offset:10240\n\t"
        "v_mfma_f32_16x16x32_bf16 v[16:19], v[56:59], a[8:11], 0\n\t"
        "s_add_u32 m0, %[ld], 107520\n\t"
        "s_add_u32 %[t0], %[xo], 18048\n\t"
        "s_waitcnt lgkmcnt(6)\n\t"
        "v_mfma_f32_16x16x32_bf16 v[20:23], a[0:3], v[60:63], 0\n\t"
        "buffer_load_dwordx4 v100, %[sx], %[t0] offen lds\n\t"
        "v_mfma_f32_16x16x32_bf16 v[24:27], a[4:7], v[60:63], 0\n\t"
        "ds_read_b128 v[56:59], v105 offset:16384\n\t"
        "v_mfma_f32_16x16x32_bf16 v[28:31], v[60:63], a[8:11], 0\n\t"
        "s_waitcnt lgkmcnt(6)\n\t"
        "v_mfma_f32_16x16x32_bf16 v[8:11], a[12:15], v[64:67], v[8:11]\n\t"
        "v_cvt_pk_bf16_f32 v88, v32, v33\n\t"
        "v_cvt_pk_bf16_f32 v89, v34, v35\n\t"
        "v_mfma_f32_16x16x32_bf16 v[12:15], a[16:19], v[64:67], v[12:15]\n\t"
        "ds_read_b128 v[60:63], v105 offset:18432\n\t"
        "v_cvt_pk_bf16_f32 v90, v36, v37\n\t"
        "v_mfma_f32_16x16x32_bf16 v[16:19], v[64:67], a[20:23], v[16:19]\n\t"
        "s_add_u32 m0, %[ld], 114688\n\t"
        "s_add_u32 %[t0], %[xo], 1792\n\t"
        "s_waitcnt lgkmcnt(6)\n\t"
        "v_mfma_f32_16x16x32_bf16 v[20:23], a[12:15], v[68:71], v[20:23]\n\t"
        "buffer_load_dwordx4 v100, %[sx], %[t0] offen lds\n\t"
        "v_mfma_f32_16x16x32_bf16 v[24:27], a[16:19], v[68:71], v[24:27]\n\t"
        "ds_read_b128 v[64:67], v106 offset:16384\n\t"
        "v_cvt_pk_bf16_f32 v91, v38, v39\n\t"
        "v_mfma_f32_16x16x32_bf16 v[28:31], v[68:71], a[20:23], v[28:31]\n\t"
        "buffer_store_dwordx4 v[88:91], v109, %[sw], %[so] offen sc1\n\t"
        "s_waitcnt lgkmcnt(6)\n\t"
        "v_mfma_f32_16x16x32_bf16 v[8:11], a[24:27], v[72:75], v[8:11]\n\t"
        "v_cvt_pk_bf16_f32 v92, v44, v45\n\t"
        "v_cvt_pk_bf16_f32 v93, v46, v47\n\t"
        "v_mfma_f32_16x16x32_bf16 v[12:15], a[28:31], v[72:75], v[12:15]\n\t"
        "ds_read_b128 v[68:71], v106 offset:18432\n\t"
        "v_cvt_pk_bf16_f32 v94, v48, v49\n\t"
        "v_mfma_f32_16x16x32_bf16 v[16:19], v[72:75], a[32:35], v[16:19]\n\t"
        "s_add_u32 m0, %[ld], 115712\n\t"
        "s_add_u32 %[t0], %[xo], 18176\n\t"
        "s_waitcnt lgkmcnt(6)\n\t"
        "v_mfma_f32_16x16x32_bf16 v[20:23], a[24:27], v[76:79], v[20:23]\n\t"
        "buffer_load_dwordx4 v100, %[sx], %[t0] offen lds\n\t"
        "v_mfma_f32_16x16x32_bf16 v[24:27], a[28:31], v[76:79], v[24:27]\n\t"
        "ds_read_b128 v[72:75], v105 offset:24576\n\t"
        "v_cvt_pk_bf16_f32 v95, v50, v51\n\t"
        "v_mfma_f32_16x16x32_bf16 v[28:31], v[76:79], a[32:35], v[28:31]\n\t"
        "buffer_store_dwordx4 v[92:95], v110, %[sw], %[so] offen sc1\n\t"
        "s_waitcnt lgkmcnt(6)\n\t"
        "v_mfma_f32_16x16x32_bf16 v[8:11], a[36:39], v[80:83], v[8:11]\n\t"
        "v_cvt_pk_bf16_f32 v96, v40, v41\n\t"
        "v_cvt_pk_bf16_f32 v97, v42, v43\n\t"
        "v_mfma_f32_16x16x32_bf16 v[12:15], a[40:43], v[80:83], v[12:15]\n\t"
        "ds_read_b128 v[76:79], v105 offset:26624\n\t"
        "v_cvt_pk_bf16_f32 v98, v52, v53\n\t"
        "v_mfma_f32_16x16x32_bf16 v[16:19], v[80:83], a[44:47], v[16:19]\n\t"
        "s_add_u32 m0, %[ld], 122880\n\t"
        "s_add_u32 %[t0], %[xo], 1920\n\t"
        "s_waitcnt lgkmcnt(6)\n\t"
        "v_mfma_f32_16x16x32_bf16 v[20:23], a[36:39], v[84:87], v[20:23]\n\t"
        "buffer_load_dwordx4 v100, %[sx], %[t0] offen lds\n\t"
        "v_mfma_f32_16x16x32_bf16 v[24:27], a[40:43], v[84:87], v[24:27]\n\t"
        "ds_read_b128 v[80:83], v106 offset:24576\n\t"
        "v_cvt_pk_bf16_f32 v99, v54, v55\n\t"
        "v_mfma_f32_16x16x32_bf16 v[28:31], v[84:87], a[44:47], v[28:31]\n\t"
        "buffer_store_dwordx4 v[96:99], v111, %[sw], %[so] offen sc1\n\t"
        "s_waitcnt lgkmcnt(6)\n\t"
        "v_mfma_f32_16x16x32_bf16 v[8:11], a[48:51], v[56:59], v[8:11]\n\t"
        "s_lshl_b32 %[so], %[cn], 19\n\t"
        "s_cmp_eq_u32 %[cn], 2\n\t"
        "s_cbranch_scc1 LG_first%=\n\t"
        "LG_back%=:\n\t"
        "v_mfma_f32_16x16x32_bf16 v[12:15], a[52:55], v[56:59], v[12:15]\n\t"
        "ds_read_b128 v[84:87], v106 offset:26624\n\t"
        "v_mfma_f32_16x16x32_bf16 v[16:19], v[56:59], a[56:59], v[16:19]\n\t"
        "s_add_u32 m0, %[ld], 123904\n\t"
        "s_add_u32 %[t0], %[xo], 18304\n\t"
        "s_waitcnt lgkmcnt(6)\n\t"
        "v_mfma_f32_16x16x32_bf16 v[20:23], a[48:51], v[60:63], v[20:23]\n\t"
        "buffer_load_dwordx4 v100, %[sx], %[t0] offen lds\n\t"
        "v_mfma_f32_16x16x32_bf16 v[24:27], a[52:55], v[60:63], v[24:27]\n\t"
        "v_mfma_f32_16x16x32_bf16 v[28:31], v[60:63], a[56:59], v[28:31]\n\t"
        "s_waitcnt lgkmcnt(5)\n\t"
        "v_mfma_f32_16x16x32_bf16 v[8:11], a[60:63], v[64:67], v[8:11]\n\t"
        "s_waitcnt vmcnt(20) lgkmcnt(0)\n\t"
        "s_barrier\n\t"
        "s_add_u32 %[xo], %[xo], 0x20000\n\t"
        "v_mfma_f32_16x16x32_bf16 v[12:15], a[64:67], v[64:67], v[12:15]\n\t"
        "ds_read_b128 v[56:59], v105 offset:32768\n\t"
        "v_mfma_f32_16x16x32_bf16 v[16:19], v[64:67], a[68:71], v[16:19]\n\t"
        "ds_read_b128 v[60:63], v105 offset:34816\n\t"
        "s_add_u32 m0, %[ld], 0\n\t"
        "s_add_u32 %[t0], %[xo], 0\n\t"
        "v_mfma_f32_16x16x32_bf16 v[20:23], a[60:63], v[68:71], v[20:23]\n\t"
        "buffer_load_dwordx4 v100, %[sx], %[t0] offen lds\n\t"
        "v_mfma_f32_16x16x32_bf16 v[24:27], a[64:67], v[68:71], v[24:27]\n\t"
        "ds_read_b128 v[64:67], v106 offset:32768\n\t"
        "v_mfma_f32_16x16x32_bf16 v[28:31], v[68:71], a[68:71], v[28:31]\n\t"
        "v_mfma_f32_16x16x32_bf16 v[8:11], a[72:75], v[72:75], v[8:11]\n\t"
        "v_mfma_f32_16x16x32_bf16 v[12:15], a[76:79], v[72:75], v[12:15]\n\t"
        "ds_read_b128 v[68:71], v106 offset:34816\n\t"
        "v_mfma_f32_16x16x32_bf16 v[16:19], v[72:75], a[80:83], v[16:19]\n\t"
        "s_add_u32 m0, %[ld], 1024\n\t"
        "s_add_u32 %[t0], %[xo], 16384\n\t"
        "v_mfma_f32_16x16x32_bf16 v[20:23], a[72:75], v[76:79], v[20:23]\n\t"
        "buffer_load_dwordx4 v100, %[sx], %[t0] offen lds\n\t"
        "v_mfma_f32_16x16x32_bf16 v[24:27], a[76:79], v[76:79], v[24:27]\n\t"
        "ds_read_b128 v[72:75], v105 offset:40960\n\t"
        "v_mfma_f32_16x16x32_bf16 v[28:31], v[76:79], a[80:83], v[28:31]\n\t"
        "v_mfma_f32_16x16x32_bf16 v[8:11], a[84:87], v[80:83], v[8:11]\n\t"
        "v_mfma_f32_16x16x32_bf16 v[12:15], a[88:91], v[80:83], v[12:15]\n\t"
        "ds_read_b128 v[76:79], v105 offset:43008\n\t"
        "v_mfma_f32_16x16x32_bf16 v[16:19], v[80:83], a[92:95], v[16:19]\n\t"
        "s_add_u32 m0, %[ld], 8192\n\t"
        "s_add_u32 %[t0], %[xo], 128\n\t"
        "v_mfma_f32_16x16x32_bf16 v[20:23], a[84:87], v[84:87], v[20:23]\n\t"
        "buffer_load_dwordx4 v100, %[sx], %[t0] offen lds\n\t"
        "v_mfma_f32_16x16x32_bf16 v[24:27], a[88:91], v[84:87], v[24:27]\n\t"
        "ds_read_b128 v[80:83], v106 offset:40960\n\t"
        "v_mfma_f32_16x16x32_bf16 v[28:31], v[84:87], a[92:95], v[28:31]\n\t"
        "s_waitcnt lgkmcnt(6)\n\t"
        "v_mfma_f32_16x16x32_bf16 v[8:11], a[96:99], v[56:59], v[8:11]\n\t"
        "v_mfma_f32_16x16x32_bf16 v[12:15], a[100:103], v[56:59], v[12:15]\n\t"
        "ds_read_b128 v[84:87], v106 offset:43008\n\t"
        "v_mfma_f32_16x16x32_bf16 v[16:19], v[56:59], a[104:107], v[16:19]\n\t"
        "s_add_u32 m0, %[ld], 9216\n\t"
        "s_add_u32 %[t0], %[xo], 16512\n\t"
        "s_waitcnt lgkmcnt(6)\n\t"
        "v_mfma_f32_16x16x32_bf16 v[20:23], a[96:99], v[60:63], v[20:23]\n\t"
        "buffer_load_dwordx4 v100, %[sx], %[t0] offen lds\n\t"
        "v_mfma_f32_16x16x32_bf16 v[24:27], a[100:103], v[60:63], v[24:27]\n\t"
        "ds_read_b128 v[56:59], v105 offset:49152\n\t"
        "v_mfma_f32_16x16x32_bf16 v[28:31], v[60:63], a[104:107], v[28:31]\n\t"
        "s_waitcnt lgkmcnt(6)\n\t"
        "v_mfma_f32_16x16x32_bf16 v[8:11], a[108:111], v[64:67], v[8:11]\n\t"
        "v_mfma_f32_16x16x32_bf16 v[12:15], a[112:115], v[64:67], v[12:15]\n\t"
        "ds_read_b128 v[60:63], v105 offset:51200\n\t"
        "v_mfma_f32_16x16x32_bf16 v[16:19], v[64:67], a[116:119], v[16:19]\n\t"
        "s_add_u32 m0, %[ld], 16384\n\t"
        "s_add_u32 %[t0], %[xo], 256\n\t"
        "s_waitcnt lgkmcnt(6)\n\t"
        "v_mfma_f32_16x16x32_bf16 v[20:23], a[108:111], v[68:71], v[20:23]\n\t"
        "buffer_load_dwordx4 v100, %[sx], %[t0] offen lds\n\t"
        "v_mfma_f32_16x16x32_bf16 v[24:27], a[112:115], v[68:71], v[24:27]\n\t"
        "ds_read_b128 v[64:67], v106 offset:49152\n\t"
        "v_mfma_f32_16x16x32_bf16 v[28:31], v[68:71], a[116:119], v[28:31]\n\t"
        "s_waitcnt lgkmcnt(6)\n\t"
        "v_mfma_f32_16x16x32_bf16 v[8:11], a[120:123], v[72:75], v[8:11]\n\t"
        "v_mfma_f32_16x16x32_bf16 v[12:15], a[124:127], v[72:75], v[12:15]\n\t"
        "ds_read_b128 v[68:71], v106 offset:51200\n\t"
        "v_mfma_f32_16x16x32_bf16 v[16:19], v[72:75], a[128:131], v[16:19]\n\t"
        "s_add_u32 m0, %[ld], 17408\n\t"
        "s_add_u32 %[t0], %[xo], 16640\n\t"
        "s_waitcnt lgkmcnt(6)\n\t"
        "v_mfma_f32_16x16x32_bf16 v[20:23], a[120:123], v[76:79], v[20:23]\n\t"
        "buffer_load_dwordx4 v100, %[sx], %[t0] offen lds\n\t"
        "v_mfma_f32_16x16x32_bf16 v[24:27], a[124:127], v[76:79], v[24:27]\n\t"
        "ds_read_b128 v[72:75], v105 offset:57344\n\t"
        "v_mfma_f32_16x16x32_bf16 v[28:31], v[76:79], a[128:131], v[28:31]\n\t"
        "s_waitcnt lgkmcnt(6)\n\t"
        "v_mfma_f32_16x16x32_bf16 v[8:11], a[132:135], v[80:83], v[8:11]\n\t"
        "v_mfma_f32_16x16x32_bf16 v[12:15], a[136:139], v[80:83], v[12:15]\n\t"
        "ds_read_b128 v[76:79], v105 offset:59392\n\t"
        "v_mfma_f32_16x16x32_bf16 v[16:19], v[80:83], a[140:143], v[16:19]\n\t"
        "s_add_u32 m0, %[ld], 24576\n\t"
        "s_add_u32 %[t0], %[xo], 384\n\t"
        "s_waitcnt lgkmcnt(6)\n\t"
        "v_mfma_f32_16x16x32_bf16 v[20:23], a[132:135], v[84:87], v[20:23]\n\t"
        "buffer_load_dwordx4 v100, %[sx], %[t0] offen lds\n\t"
        "v_mfma_f32_16x16x32_bf16 v[24:27], a[136:139], v[84:87], v[24:27]\n\t"
        "ds_read_b128 v[80:83], v106 offset:57344\n\t"
        "v_mfma_f32_16x16x32_bf16 v[28:31], v[84:87], a[140:143], v[28:31]\n\t"
        "s_waitcnt lgkmcnt(6)\n\t"
        "v_mfma_f32_16x16x32_bf16 v[8:11], a[144:147], v[56:59], v[8:11]\n\t"
        "v_mfma_f32_16x16x32_bf16 v[12:15], a[148:151], v[56:59], v[12:15]\n\t"
        "ds_read_b128 v[84:87], v106 offset:59392\n\t"
        "v_mfma_f32_16x16x32_bf16 v[16:19], v[56:59], a[152:155], v[16:19]\n\t"
        "s_add_u32 m0, %[ld], 25600\n\t"
        "s_add_u32 %[t0], %[xo], 16768\n\t"
        "s_waitcnt lgkmcnt(6)\n\t"
        "v_mfma_f32_16x16x32_bf16 v[20:23], a[144:147], v[60:63], v[20:23]\n\t"
        "buffer_load_dwordx4 v100, %[sx], %[t0] offen lds\n\t"
        "v_mfma_f32_16x16x32_bf16 v[24:27], a[148:151], v[60:63], v[24:27]\n\t"
        "v_mfma_f32_16x16x32_bf16 v[28:31], v[60:63], a[152:155], v[28:31]\n\t"
        "s_waitcnt lgkmcnt(5)\n\t"
        "v_mfma_f32_16x16x32_bf16 v[8:11], a[156:159], v[64:67], v[8:11]\n\t"
        "s_waitcnt vmcnt(9) lgkmcnt(0)\n\t"
        "s_barrier\n\t"
        "v_mfma_f32_16x16x32_bf16 v[12:15], a[160:163], v[64:67], v[12:15]\n\t"
        "ds_read_b128 v[56:59], v107 offset:0\n\t"
        "v_mfma_f32_16x16x32_bf16 v[16:19], v[64:67], a[164:167], v[16:19]\n\t"
        "ds_read_b128 v[60:63], v107 offset:2048\n\t"
        "s_add_u32 m0, %[ld], 32768\n\t"
        "s_add_u32 %[t0], %[xo], 512\n\t"
        "v_mfma_f32_16x16x32_bf16 v[20:23], a[156:159], v[68:71], v[20:23]\n\t"
        "buffer_load_dwordx4 v100, %[sx], %[t0] offen lds\n\t"
        "v_mfma_f32_16x16x32_bf16 v[24:27], a[160:163], v[68:71], v[24:27]\n\t"
        "ds_read_b128 v[64:67], v108 offset:0\n\t"
        "v_mfma_f32_16x16x32_bf16 v[28:31], v[68:71], a[164:167], v[28:31]\n\t"
        "v_mfma_f32_16x16x32_bf16 v[8:11], a[168:171], v[72:75], v[8:11]\n\t"
        "v_mfma_f32_16x16x32_bf16 v[12:15], a[172:175], v[72:75], v[12:15]\n\t"
        "ds_read_b128 v[68:71], v108 offset:2048\n\t"
        "v_mov_b32_e32 v119, %[cn]\n\t"
        "buffer_store_dword v119, v113, %[sw], 0 offen sc1\n\t"
        "s_add_u32 %[cn], %[cn], 1\n\t"
        "v_mfma_f32_16x16x32_bf16 v[16:19], v[72:75], a[176:179], v[16:19]\n\t"
        "s_add_u32 m0, %[ld], 33792\n\t"
        "s_add_u32 %[t0], %[xo], 16896\n\t"
        "v_mfma_f32_16x16x32_bf16 v[20:23], a[168:171], v[76:79], v[20:23]\n\t"
        "buffer_load_dwordx4 v100, %[sx], %[t0] offen lds\n\t"
        "v_mfma_f32_16x16x32_bf16 v[24:27], a[172:175], v[76:79], v[24:27]\n\t"
        "ds_read_b128 v[72:75], v107 offset:8192\n\t"
        "v_mfma_f32_16x16x32_bf16 v[28:31], v[76:79], a[176:179], v[28:31]\n\t"
        "v_mfma_f32_16x16x32_bf16 v[8:11], a[180:183], v[80:83], v[8:11]\n\t"
        "v_mfma_f32_16x16x32_bf16 v[12:15], a[184:187], v[80:83], v[12:15]\n\t"
        "ds_read_b128 v[76:79], v107 offset:10240\n\t"
        "v_mfma_f32_16x16x32_bf16 v[16:19], v[80:83], a[188:191], v[16:19]\n\t"
        "s_add_u32 m0, %[ld], 40960\n\t"
        "s_add_u32 %[t0], %[xo], 640\n\t"
        "v_mfma_f32_16x16x32_bf16 v[20:23], a[180:183], v[84:87], v[20:23]\n\t"
        "buffer_load_dwordx4 v100, %[sx], %[t0] offen lds\n\t"
        "v_mfma_f32_16x16x32_bf16 v[24:27], a[184:187], v[84:87], v[24:27]\n\t"
        "ds_read_b128 v[80:83], v108 offset:8192\n\t"
        "v_mfma_f32_16x16x32_bf16 v[28:31], v[84:87], a[188:191], v[28:31]\n\t"
        "s_waitcnt lgkmcnt(6)\n\t"
        "v_mfma_f32_16x16x32_bf16 v[8:11], a[192:195], v[56:59], v[8:11]\n\t"
        "v_mfma_f32_16x16x32_bf16 v[12:15], a[196:199], v[56:59], v[12:15]\n\t"
        "ds_read_b128 v[84:87], v108 offset:10240\n\t"
        "v_mfma_f32_16x16x32_bf16 v[16:19], v[56:59], a[200:203], v[16:19]\n\t"
        "s_add_u32 m0, %[ld], 41984\n\t"
        "s_add_u32 %[t0], %[xo], 17024\n\t"
        "s_waitcnt lgkmcnt(6)\n\t"
        "v_mfma_f32_16x16x32_bf16 v[20:23], a[192:195], v[60:63], v[20:23]\n\t"
        "buffer_load_dwordx4 v100, %[sx], %[t0] offen lds\n\t"
        "v_mfma_f32_16x16x32_bf16 v[24:27], a[196:199], v[60:63], v[24:27]\n\t"
        "ds_read_b128 v[56:59], v107 offset:16384\n\t"
        "v_mfma_f32_16x16x32_bf16 v[28:31], v[60:63], a[200:203], v[28:31]\n\t"
        "s_waitcnt lgkmcnt(6)\n\t"
        "v_mfma_f32_16x16x32_bf16 v[8:11], a[204:207], v[64:67], v[8:11]\n\t"
        "v_mfma_f32_16x16x32_bf16 v[12:15], a[208:211], v[64:67], v[12:15]\n\t"
        "ds_read_b128 v[60:63], v107 offset:18432\n\t"
        "v_mfma_f32_16x16x32_bf16 v[16:19], v[64:67], a[212:215], v[16:19]\n\t"
        "s_add_u32 m0, %[ld], 49152\n\t"
        "s_add_u32 %[t0], %[xo], 768\n\t"
        "s_waitcnt lgkmcnt(6)\n\t"
        "v_mfma_f32_16x16x32_bf16 v[20:23], a[204:207], v[68:71], v[20:23]\n\t"
        "buffer_load_dwordx4 v100, %[sx], %[t0] offen lds\n\t"
        "v_mfma_f32_16x16x32_bf16 v[24:27], a[208:211], v[68:71], v[24:27]\n\t"
        "ds_read_b128 v[64:67], v108 offset:16384\n\t"
        "v_mfma_f32_16x16x32_bf16 v[28:31], v[68:71], a[212:215], v[28:31]\n\t"
        "s_waitcnt lgkmcnt(6)\n\t"
        "v_mfma_f32_16x16x32_bf16 v[8:11], a[216:219], v[72:75], v[8:11]\n\t"
        "v_mfma_f32_16x16x32_bf16 v[12:15], a[220:223], v[72:75], v[12:15]\n\t"
        "ds_read_b128 v[68:71], v108 offset:18432\n\t"
        "v_mfma_f32_16x16x32_bf16 v[16:19], v[72:75], a[224:227], v[16:19]\n\t"
        "s_add_u32 m0, %[ld], 50176\n\t"
        "s_add_u32 %[t0], %[xo], 17152\n\t"
        "s_waitcnt lgkmcnt(6)\n\t"
        "v_mfma_f32_16x16x32_bf16 v[20:23], a[216:219], v[76:79], v[20:23]\n\t"
        "buffer_load_dwordx4 v100, %[sx], %[t0] offen lds\n\t"
        "v_mfma_f32_16x16x32_bf16 v[24:27], a[220:223], v[76:79], v[24:27]\n\t"
        "ds_read_b128 v[72:75], v107 offset:24576\n\t"
        "v_mfma_f32_16x16x32_bf16 v[28:31], v[76:79], a[224:227], v[28:31]\n\t"
        "s_waitcnt lgkmcnt(6)\n\t"
        "v_mfma_f32_16x16x32_bf16 v[8:11], a[228:231], v[80:83], v[8:11]\n\t"
        "v_mfma_f32_16x16x32_bf16 v[12:15], a[232:235], v[80:83], v[12:15]\n\t"
        "ds_read_b128 v[76:79], v107 offset:26624\n\t"
        "v_mfma_f32_16x16x32_bf16 v[16:19], v[80:83], a[236:239], v[16:19]\n\t"
        "s_add_u32 m0, %[ld], 57344\n\t"
        "s_add_u32 %[t0], %[xo], 896\n\t"
        "s_waitcnt lgkmcnt(6)\n\t"
        "v_mfma_f32_16x16x32_bf16 v[20:23], a[228:231], v[84:87], v[20:23]\n\t"
        "buffer_load_dwordx4 v100, %[sx], %[t0] offen lds\n\t"
        "v_mfma_f32_16x16x32_bf16 v[24:27], a[232:235], v[84:87], v[24:27]\n\t"
        "ds_read_b128 v[80:83], v108 offset:24576\n\t"
        "v_mfma_f32_16x16x32_bf16 v[28:31], v[84:87], a[236:239], v[28:31]\n\t"
        "s_waitcnt lgkmcnt(6)\n\t"
        "v_mfma_f32_16x16x32_bf16 v[8:11], a[240:243], v[56:59], v[8:11]\n\t"
        "v_mfma_f32_16x16x32_bf16 v[12:15], a[244:247], v[56:59], v[12:15]\n\t"
        "ds_read_b128 v[84:87], v108 offset:26624\n\t"
        "v_mfma_f32_16x16x32_bf16 v[16:19], v[56:59], a[248:251], v[16:19]\n\t"
        "s_add_u32 m0, %[ld], 58368\n\t"
        "s_add_u32 %[t0], %[xo], 17280\n\t"
        "s_waitcnt lgkmcnt(6)\n\t"
        "v_mfma_f32_16x16x32_bf16 v[20:23], a[240:243], v[60:63], v[20:23]\n\t"
        "buffer_load_dwordx4 v100, %[sx], %[t0] offen lds\n\t"
        "v_mfma_f32_16x16x32_bf16 v[24:27], a[244:247], v[60:63], v[24:27]\n\t"
        "v_mfma_f32_16x16x32_bf16 v[28:31], v[60:63], a[248:251], v[28:31]\n\t"
        "s_waitcnt lgkmcnt(5)\n\t"
        "v_mfma_f32_16x16x32_bf16 v[8:11], a[252:255], v[64:67], v[8:11]\n\t"
        "s_waitcnt vmcnt(17) lgkmcnt(0)\n\t"
        "s_barrier\n\t"
        "v_mfma_f32_16x16x32_bf16 v[12:15], v[128:131], v[64:67], v[12:15]\n\t"
        "ds_read_b128 v[56:59], v107 offset:32768\n\t"
        "v_mfma_f32_16x16x32_bf16 v[16:19], v[64:67], v[132:135], v[16:19]\n\t"
        "ds_read_b128 v[60:63], v107 offset:34816\n\t"
        "s_add_u32 m0, %[ld], 65536\n\t"
        "s_add_u32 %[t0], %[xo], 1024\n\t"
        "v_mfma_f32_16x16x32_bf16 v[20:23], a[252:255], v[68:71], v[20:23]\n\t"
        "buffer_load_dwordx4 v100, %[sx], %[t0] offen lds\n\t"
        "v_mfma_f32_16x16x32_bf16 v[24:27], v[128:131], v[68:71], v[24:27]\n\t"
        "ds_read_b128 v[64:67], v108 offset:32768\n\t"
        "v_mfma_f32_16x16x32_bf16 v[28:31], v[68:71], v[132:135], v[28:31]\n\t"
        "v_mfma_f32_16x16x32_bf16 v[8:11], v[136:139], v[72:75], v[8:11]\n\t"
        "v_mfma_f32_16x16x32_bf16 v[12:15], v[140:143], v[72:75], v[12:15]\n\t"
        "ds_read_b128 v[68:71], v108 offset:34816\n\t"
        "v_mfma_f32_16x16x32_bf16 v[16:19], v[72:75], v[144:147], v[16:19]\n\t"
        "s_add_u32 m0, %[ld], 66560\n\t"
        "s_add_u32 %[t0], %[xo], 17408\n\t"
        "v_mfma_f32_16x16x32_bf16 v[20:23], v[136:139], v[76:79], v[20:23]\n\t"
        "buffer_load_dwordx4 v100, %[sx], %[t0] offen lds\n\t"
        "v_mfma_f32_16x16x32_bf16 v[24:27], v[140:143], v[76:79], v[24:27]\n\t"
        "ds_read_b128 v[72:75], v107 offset:40960\n\t"
        "v_mfma_f32_16x16x32_bf16 v[28:31], v[76:79], v[144:147], v[28:31]\n\t"
        "v_mfma_f32_16x16x32_bf16 v[8:11], v[148:151], v[80:83], v[8:11]\n\t"
        "v_mfma_f32_16x16x32_bf16 v[12:15], v[152:155], v[80:83], v[12:15]\n\t"
        "ds_read_b128 v[76:79], v107 offset:43008\n\t"
        "v_mfma_f32_16x16x32_bf16 v[16:19], v[80:83], v[156:159], v[16:19]\n\t"
        "s_add_u32 m0, %[ld], 73728\n\t"
        "s_add_u32 %[t0], %[xo], 1152\n\t"
        "v_mfma_f32_16x16x32_bf16 v[20:23], v[148:151], v[84:87], v[20:23]\n\t"
        "buffer_load_dwordx4 v100, %[sx], %[t0] offen lds\n\t"
        "v_mfma_f32_16x16x32_bf16 v[24:27], v[152:155], v[84:87], v[24:27]\n\t"
        "ds_read_b128 v[80:83], v108 offset:40960\n\t"
        "v_mfma_f32_16x16x32_bf16 v[28:31], v[84:87], v[156:159], v[28:31]\n\t"
        "s_waitcnt lgkmcnt(6)\n\t"
        "v_mfma_f32_16x16x32_bf16 v[8:11], v[160:163], v[56:59], v[8:11]\n\t"
        "v_mfma_f32_16x16x32_bf16 v[12:15], v[164:167], v[56:59], v[12:15]\n\t"
        "ds_read_b128 v[84:87], v108 offset:43008\n\t"
        "v_mfma_f32_16x16x32_bf16 v[16:19], v[56:59], v[168:171], v[16:19]\n\t"
        "s_add_u32 m0, %[ld], 74752\n\t"
        "s_add_u32 %[t0], %[xo], 17536\n\t"
        "s_waitcnt lgkmcnt(6)\n\t"
        "v_mfma_f32_16x16x32_bf16 v[20:23], v[160:163], v[60:63], v[20:23]\n\t"
        "buffer_load_dwordx4 v100, %[sx], %[t0] offen lds\n\t"
        "v_mfma_f32_16x16x32_bf16 v[24:27], v[164:167], v[60:63], v[24:27]\n\t"
        "ds_read_b128 v[56:59], v107 offset:49152\n\t"
        "v_mfma_f32_16x16x32_bf16 v[28:31], v[60:63], v[168:171], v[28:31]\n\t"
        "s_waitcnt lgkmcnt(6)\n\t"
        "v_mfma_f32_16x16x32_bf16 v[8:11], v[172:175], v[64:67], v[8:11]\n\t"
        "v_mfma_f32_16x16x32_bf16 v[12:15], v[176:179], v[64:67], v[12:15]\n\t"
        "ds_read_b128 v[60:63], v107 offset:51200\n\t"
        "v_mfma_f32_16x16x32_bf16 v[16:19], v[64:67], v[180:183], v[16:19]\n\t"
        "s_add_u32 m0, %[ld], 81920\n\t"
        "s_add_u32 %[t0], %[xo], 1280\n\t"
        "s_waitcnt lgkmcnt(6)\n\t"
        "v_mfma_f32_16x16x32_bf16 v[20:23], v[172:175], v[68:71], v[20:23]\n\t"
        "buffer_load_dwordx4 v100, %[sx], %[t0] offen lds\n\t"
        "v_mfma_f32_16x16x32_bf16 v[24:27], v[176:179], v[68:71], v[24:27]\n\t"
        "ds_read_b128 v[64:67], v108 offset:49152\n\t"
        "v_mfma_f32_16x16x32_bf16 v[28:31], v[68:71], v[180:183], v[28:31]\n\t"
        "s_waitcnt lgkmcnt(6)\n\t"
        "v_mfma_f32_16x16x32_bf16 v[8:11], v[184:187], v[72:75], v[8:11]\n\t"
        "v_mfma_f32_16x16x32_bf16 v[12:15], v[188:191], v[72:75], v[12:15]\n\t"
        "ds_read_b128 v[68:71], v108 offset:51200\n\t"
        "v_mfma_f32_16x16x32_bf16 v[16:19], v[72:75], v[192:195], v[16:19]\n\t"
        "s_add_u32 m0, %[ld], 82944\n\t"
        "s_add_u32 %[t0], %[xo], 17664\n\t"
        "s_waitcnt lgkmcnt(6)\n\t"
        "v_mfma_f32_16x16x32_bf16 v[20:23], v[184:187], v[76:79], v[20:23]\n\t"
        "buffer_load_dwordx4 v100, %[sx], %[t0] offen lds\n\t"
        "v_mfma_f32_16x16x32_bf16 v[24:27], v[188:191], v[76:79], v[24:27]\n\t"
        "ds_read_b128 v[72:75], v107 offset:57344\n\t"
        "v_mfma_f32_16x16x32_bf16 v[28:31], v[76:79], v[192:195], v[28:31]\n\t"
        "s_waitcnt lgkmcnt(6)\n\t"
        "v_mfma_f32_16x16x32_bf16 v[8:11], v[196:199], v[80:83], v[8:11]\n\t"
        "v_mfma_f32_16x16x32_bf16 v[12:15], v[200:203], v[80:83], v[12:15]\n\t"
        "ds_read_b128 v[76:79], v107 offset:59392\n\t"
        "v_mfma_f32_16x16x32_bf16 v[16:19], v[80:83], v[204:207], v[16:19]\n\t"
        "s_add_u32 m0, %[ld], 90112\n\t"
        "s_add_u32 %[t0], %[xo], 1408\n\t"
        "s_waitcnt lgkmcnt(6)\n\t"
        "v_mfma_f32_16x16x32_bf16 v[20:23], v[196:199], v[84:87], v[20:23]\n\t"
        "buffer_load_dwordx4 v100, %[sx], %[t0] offen lds\n\t"
        "v_mfma_f32_16x16x32_bf16 v[24:27], v[200:203], v[84:87], v[24:27]\n\t"
        "ds_read_b128 v[80:83], v108 offset:57344\n\t"
        "v_mfma_f32_16x16x32_bf16 v[28:31], v[84:87], v[204:207], v[28:31]\n\t"
        "s_waitcnt lgkmcnt(6)\n\t"
        "v_mfma_f32_16x16x32_bf16 v[8:11], v[208:211], v[56:59], v[8:11]\n\t"
        "v_mfma_f32_16x16x32_bf16 v[12:15], v[212:215], v[56:59], v[12:15]\n\t"
        "ds_read_b128 v[84:87], v108 offset:59392\n\t"
        "v_mfma_f32_16x16x32_bf16 v[16:19], v[56:59], v[216:219], v[16:19]\n\t"
        "s_add_u32 m0, %[ld], 91136\n\t"
        "s_add_u32 %[t0], %[xo], 17792\n\t"
        "s_waitcnt lgkmcnt(6)\n\t"
        "v_mfma_f32_16x16x32_bf16 v[20:23], v[208:211], v[60:63], v[20:23]\n\t"
        "buffer_load_dwordx4 v100, %[sx], %[t0] offen lds\n\t"
        "v_mfma_f32_16x16x32_bf16 v[24:27], v[212:215], v[60:63], v[24:27]\n\t"
        "v_mfma_f32_16x16x32_bf16 v[28:31], v[60:63], v[216:219], v[28:31]\n\t"
        "s_waitcnt lgkmcnt(5)\n\t"
        "v_mfma_f32_16x16x32_bf16 v[8:11], v[220:223], v[64:67], v[8:11]\n\t"
        "s_waitcnt vmcnt(17) lgkmcnt(0)\n\t"
        "s_barrier\n\t"
        "v_mfma_f32_16x16x32_bf16 v[12:15], v[224:227], v[64:67], v[12:15]\n\t"
        "ds_read_b128 v[56:59], v105 offset:0\n\t"
        "v_mfma_f32_16x16x32_bf16 v[16:19], v[64:67], v[228:231], v[16:19]\n\t"
        "ds_read_b128 v[60:63], v105 offset:2048\n\t"
        "s_add_u32 m0, %[ld], 98304\n\t"
        "s_add_u32 %[t0], %[xo], 1536\n\t"
        "v_mfma_f32_16x16x32_bf16 v[20:23], v[220:223], v[68:71], v[20:23]\n\t"
        "buffer_load_dwordx4 v100, %[sx], %[t0] offen lds\n\t"
        "v_mfma_f32_16x16x32_bf16 v[24:27], v[224:227], v[68:71], v[24:27]\n\t"
        "ds_read_b128 v[64:67], v106 offset:0\n\t"
        "v_mfma_f32_16x16x32_bf16 v[28:31], v[68:71], v[228:231], v[28:31]\n\t"
        "v_mfma_f32_16x16x32_bf16 v[8:11], v[232:235], v[72:75], v[8:11]\n\t"
        "v_mfma_f32_16x16x32_bf16 v[12:15], v[236:239], v[72:75], v[12:15]\n\t"
        "ds_read_b128 v[68:71], v106 offset:2048\n\t"
        "v_mfma_f32_16x16x32_bf16 v[16:19], v[72:75], v[240:243], v[16:19]\n\t"
        "s_add_u32 m0, %[ld], 99328\n\t"
        "s_add_u32 %[t0], %[xo], 17920\n\t"
        "v_mfma_f32_16x16x32_bf16 v[20:23], v[232:235], v[76:79], v[20:23]\n\t"
        "buffer_load_dwordx4 v100, %[sx], %[t0] offen lds\n\t"
        "v_mfma_f32_16x16x32_bf16 v[24:27], v[236:239], v[76:79], v[24:27]\n\t"
        "ds_read_b128 v[72:75], v105 offset:8192\n\t"
        "v_mfma_f32_16x16x32_bf16 v[28:31], v[76:79], v[240:243], v[28:31]\n\t"
        "v_mfma_f32_16x16x32_bf16 v[8:11], v[244:247], v[80:83], v[8:11]\n\t"
        "v_mfma_f32_16x16x32_bf16 v[12:15], v[248:251], v[80:83], v[12:15]\n\t"
        "ds_read_b128 v[76:79], v105 offset:10240\n\t"
        "v_mfma_f32_16x16x32_bf16 v[16:19], v[80:83], v[252:255], v[16:19]\n\t"
        "s_add_u32 m0, %[ld], 106496\n\t"
        "s_add_u32 %[t0], %[xo], 1664\n\t"
        "v_mfma_f32_16x16x32_bf16 v[20:23], v[244:247], v[84:87], v[20:23]\n\t"
        "buffer_load_dwordx4 v100, %[sx], %[t0] offen lds\n\t"
        "v_mfma_f32_16x16x32_bf16 v[24:27], v[248:251], v[84:87], v[24:27]\n\t"
        "ds_read_b128 v[80:83], v106 offset:8192\n\t"
        "v_mfma_f32_16x16x32_bf16 v[28:31], v[84:87], v[252:255], v[28:31]\n\t"
        "LG_mid%=:\n\t"
        "s_waitcnt lgkmcnt(6)\n\t"
        "v_mfma_f32_16x16x32_bf16 v[32:35], a[0:3], v[56:59], 0\n\t"
        "v_mfma_f32_16x16x32_bf16 v[36:39], a[4:7], v[56:59], 0\n\t"
        "ds_read_b128 v[84:87], v106 offset:10240\n\t"
        "v_mfma_f32_16x16x32_bf16 v[40:43], v[56:59], a[8:11], 0\n\t"
        "s_add_u32 m0, %[ld], 107520\n\t"
        "s_add_u32 %[t0], %[xo], 18048\n\t"
        "s_waitcnt lgkmcnt(6)\n\t"
        "v_mfma_f32_16x16x32_bf16 v[44:47], a[0:3], v[60:63], 0\n\t"
        "buffer_load_dwordx4 v100, %[sx], %[t0] offen lds\n\t"
        "v_mfma_f32_16x16x32_bf16 v[48:51], a[4:7], v[60:63], 0\n\t"
        "ds_read_b128 v[56:59], v105 offset:16384\n\t"
        "v_mfma_f32_16x16x32_bf16 v[52:55], v[60:63], a[8:11], 0\n\t"
        "s_waitcnt lgkmcnt(6)\n\t"
        "v_mfma_f32_16x16x32_bf16 v[32:35], a[12:15], v[64:67], v[32:35]\n\t"
        "v_cvt_pk_bf16_f32 v88, v8, v9\n\t"
        "v_cvt_pk_bf16_f32 v89, v10, v11\n\t"
        "v_mfma_f32_16x16x32_bf16 v[36:39], a[16:19], v[64:67], v[36:39]\n\t"
        "ds_read_b128 v[60:63], v105 offset:18432\n\t"
        "v_cvt_pk_bf16_f32 v90, v12, v13\n\t"
        "v_mfma_f32_16x16x32_bf16 v[40:43], v[64:67], a[20:23], v[40:43]\n\t"
        "s_add_u32 m0, %[ld], 114688\n\t"
        "s_add_u32 %[t0], %[xo], 1792\n\t"
        "s_waitcnt lgkmcnt(6)\n\t"
        "v_mfma_f32_16x16x32_bf16 v[44:47], a[12:15], v[68:71], v[44:47]\n\t"
        "buffer_load_dwordx4 v100, %[sx], %[t0] offen lds\n\t"
        "v_mfma_f32_16x16x32_bf16 v[48:51], a[16:19], v[68:71], v[48:51]\n\t"
        "ds_read_b128 v[64:67], v106 offset:16384\n\t"
        "v_cvt_pk_bf16_f32 v91, v14, v15\n\t"
        "v_mfma_f32_16x16x32_bf16 v[52:55], v[68:71], a[20:23], v[52:55]\n\t"
        "buffer_store_dwordx4 v[88:91], v109, %[sw], %[so] offen sc1\n\t"
        "s_waitcnt lgkmcnt(6)\n\t"
        "v_mfma_f32_16x16x32_bf16 v[32:35], a[24:27], v[72:75], v[32:35]\n\t"
        "v_cvt_pk_bf16_f32 v92, v20, v21\n\t"
        "v_cvt_pk_bf16_f32 v93, v22, v23\n\t"
        "v_mfma_f32_16x16x32_bf16 v[36:39], a[28:31], v[72:75], v[36:39]\n\t"
        "ds_read_b128 v[68:71], v106 offset:18432\n\t"
        "v_cvt_pk_bf16_f32 v94, v24, v25\n\t"
        "v_mfma_f32_16x16x32_bf16 v[40:43], v[72:75], a[32:35], v[40:43]\n\t"
        "s_add_u32 m0, %[ld], 115712\n\t"
        "s_add_u32 %[t0], %[xo], 18176\n\t"
        "s_waitcnt lgkmcnt(6)\n\t"
        "v_mfma_f32_16x16x32_bf16 v[44:47], a[24:27], v[76:79], v[44:47]\n\t"
        "buffer_load_dwordx4 v100, %[sx], %[t0] offen lds\n\t"
        "v_mfma_f32_16x16x32_bf16 v[48:51], a[28:31], v[76:79], v[48:51]\n\t"
        "ds_read_b128 v[72:75], v105 offset:24576\n\t"
        "v_cvt_pk_bf16_f32 v95, v26, v27\n\t"
        "v_mfma_f32_16x16x32_bf16 v[52:55], v[76:79], a[32:35], v[52:55]\n\t"
        "buffer_store_dwordx4 v[92:95], v110, %[sw], %[so] offen sc1\n\t"
        "s_waitcnt lgkmcnt(6)\n\t"
        "v_mfma_f32_16x16x32_bf16 v[32:35], a[36:39], v[80:83], v[32:35]\n\t"
        "v_cvt_pk_bf16_f32 v96, v16, v17\n\t"
        "v_cvt_pk_bf16_f32 v97, v18, v19\n\t"
        "v_mfma_f32_16x16x32_bf16 v[36:39], a[40:43], v[80:83], v[36:39]\n\t"
        "ds_read_b128 v[76:79], v105 offset:26624\n\t"
        "v_cvt_pk_bf16_f32 v98, v28, v29\n\t"
        "v_mfma_f32_16x16x32_bf16 v[40:43], v[80:83], a[44:47], v[40:43]\n\t"
        "s_add_u32 m0, %[ld], 122880\n\t"
        "s_add_u32 %[t0], %[xo], 1920\n\t"
        "s_waitcnt lgkmcnt(6)\n\t"
        "v_mfma_f32_16x16x32_bf16 v[44:47], a[36:39], v[84:87], v[44:47]\n\t"
        "buffer_load_dwordx4 v100, %[sx], %[t0] offen lds\n\t"
        "v_mfma_f32_16x16x32_bf16 v[48:51], a[40:43], v[84:87], v[48:51]\n\t"
        "ds_read_b128 v[80:83], v106 offset:24576\n\t"
        "v_cvt_pk_bf16_f32 v99, v30, v31\n\t"
        "v_mfma_f32_16x16x32_bf16 v[52:55], v[84:87], a[44:47], v[52:55]\n\t"
        "buffer_store_dwordx4 v[96:99], v111, %[sw], %[so] offen sc1\n\t"
        "s_waitcnt lgkmcnt(6)\n\t"
        "v_mfma_f32_16x16x32_bf16 v[32:35], a[48:51], v[56:59], v[32:35]\n\t"
        "s_lshl_b32 %[so], %[cn], 19\n\t"
        "v_mfma_f32_16x16x32_bf16 v[36:39], a[52:55], v[56:59], v[36:39]\n\t"
        "ds_read_b128 v[84:87], v106 offset:26624\n\t"
        "v_mfma_f32_16x16x32_bf16 v[40:43], v[56:59], a[56:59], v[40:43]\n\t"
        "s_add_u32 m0, %[ld], 123904\n\t"
        "s_add_u32 %[t0], %[xo], 18304\n\t"
        "s_waitcnt lgkmcnt(6)\n\t"
        "v_mfma_f32_16x16x32_bf16 v[44:47], a[48:51], v[60:63], v[44:47]\n\t"
        "buffer_load_dwordx4 v100, %[sx], %[t0] offen lds\n\t"
        "v_mfma_f32_16x16x32_bf16 v[48:51], a[52:55], v[60:63], v[48:51]\n\t"
        "v_mfma_f32_16x16x32_bf16 v[52:55], v[60:63], a[56:59], v[52:55]\n\t"
        "s_waitcnt lgkmcnt(5)\n\t"
        "v_mfma_f32_16x16x32_bf16 v[32:35], a[60:63], v[64:67], v[32:35]\n\t"
        "s_waitcnt vmcnt(19) lgkmcnt(0)\n\t"
        "s_barrier\n\t"
        "s_add_u32 %[xo], %[xo], 0x20000\n\t"
        "v_mfma_f32_16x16x32_bf16 v[36:39], a[64:67], v[64:67], v[36:39]\n\t"
        "ds_read_b128 v[56:59], v105 offset:32768\n\t"
        "v_mfma_f32_16x16x32_bf16 v[40:43], v[64:67], a[68:71], v[40:43]\n\t"
        "ds_read_b128 v[60:63], v105 offset:34816\n\t"
        "s_add_u32 m0, %[ld], 0\n\t"
        "s_add_u32 %[t0], %[xo], 0\n\t"
        "v_mfma_f32_16x16x32_bf16 v[44:47], a[60:63], v[68:71], v[44:47]\n\t"
        "buffer_load_dwordx4 v100, %[sx], %[t0] offen lds\n\t"
        "v_mfma_f32_16x16x32_bf16 v[48:51], a[64:67], v[68:71], v[48:51]\n\t"
        "ds_read_b128 v[64:67], v106 offset:32768\n\t"
        "v_mfma_f32_16x16x32_bf16 v[52:55], v[68:71], a[68:71], v[52:55]\n\t"
        "v_mfma_f32_16x16x32_bf16 v[32:35], a[72:75], v[72:75], v[32:35]\n\t"
        "v_mfma_f32_16x16x32_bf16 v[36:39], a[76:79], v[72:75], v[36:39]\n\t"
        "ds_read_b128 v[68:71], v106 offset:34816\n\t"
        "v_mfma_f32_16x16x32_bf16 v[40:43], v[72:75], a[80:83], v[40:43]\n\t"
        "s_add_u32 m0, %[ld], 1024\n\t"
        "s_add_u32 %[t0], %[xo], 16384\n\t"
        "v_mfma_f32_16x16x32_bf16 v[44:47], a[72:75], v[76:79], v[44:47]\n\t"
        "buffer_load_dwordx4 v100, %[sx], %[t0] offen lds\n\t"
        "v_mfma_f32_16x16x32_bf16 v[48:51], a[76:79], v[76:79], v[48:51]\n\t"
        "ds_read_b128 v[72:75], v105 offset:40960\n\t"
        "v_mfma_f32_16x16x32_bf16 v[52:55], v[76:79], a[80:83], v[52:55]\n\t"
        "v_mfma_f32_16x16x32_bf16 v[32:35], a[84:87], v[80:83], v[32:35]\n\t"
        "v_mfma_f32_16x16x32_bf16 v[36:39], a[88:91], v[80:83], v[36:39]\n\t"
        "ds_read_b128 v[76:79], v105 offset:43008\n\t"
        "v_mfma_f32_16x16x32_bf16 v[40:43], v[80:83], a[92:95], v[40:43]\n\t"
        "s_add_u32 m0, %[ld], 8192\n\t"
        "s_add_u32 %[t0], %[xo], 128\n\t"
        "v_mfma_f32_16x16x32_bf16 v[44:47], a[84:87], v[84:87], v[44:47]\n\t"
        "buffer_load_dwordx4 v100, %[sx], %[t0] offen lds\n\t"
        "v_mfma_f32_16x16x32_bf16 v[48:51], a[88:91], v[84:87], v[48:51]\n\t"
        "ds_read_b128 v[80:83], v106 offset:40960\n\t"
        "v_mfma_f32_16x16x32_bf16 v[52:55], v[84:87], a[92:95], v[52:55]\n\t"
        "s_waitcnt lgkmcnt(6)\n\t"
        "v_mfma_f32_16x16x32_bf16 v[32:35], a[96:99], v[56:59], v[32:35]\n\t"
        "v_mfma_f32_16x16x32_bf16 v[36:39], a[100:103], v[56:59], v[36:39]\n\t"
        "ds_read_b128 v[84:87], v106 offset:43008\n\t"
        "v_mfma_f32_16x16x32_bf16 v[40:43], v[56:59], a[104:107], v[40:43]\n\t"
        "s_add_u32 m0, %[ld], 9216\n\t"
        "s_add_u32 %[t0], %[xo], 16512\n\t"
        "s_waitcnt lgkmcnt(6)\n\t"
        "v_mfma_f32_16x16x32_bf16 v[44:47], a[96:99], v[60:63], v[44:47]\n\t"
        "buffer_load_dwordx4 v100, %[sx], %[t0] offen lds\n\t"
        "v_mfma_f32_16x16x32_bf16 v[48:51], a[100:103], v[60:63], v[48:51]\n\t"
        "ds_read_b128 v[56:59], v105 offset:49152\n\t"
        "v_mfma_f32_16x16x32_bf16 v[52:55], v[60:63], a[104:107], v[52:55]\n\t"
        "s_waitcnt lgkmcnt(6)\n\t"
        "v_mfma_f32_16x16x32_bf16 v[32:35], a[108:111], v[64:67], v[32:35]\n\t"
        "v_mfma_f32_16x16x32_bf16 v[36:39], a[112:115], v[64:67], v[36:39]\n\t"
        "ds_read_b128 v[60:63], v105 offset:51200\n\t"
        "v_mfma_f32_16x16x32_bf16 v[40:43], v[64:67], a[116:119], v[40:43]\n\t"
        "s_add_u32 m0, %[ld], 16384\n\t"
        "s_add_u32 %[t0], %[xo], 256\n\t"
        "s_waitcnt lgkmcnt(6)\n\t"
        "v_mfma_f32_16x16x32_bf16 v[44:47], a[108:111], v[68:71], v[44:47]\n\t"
        "buffer_load_dwordx4 v100, %[sx], %[t0] offen lds\n\t"
        "v_mfma_f32_16x16x32_bf16 v[48:51], a[112:115], v[68:71], v[48:51]\n\t"
        "ds_read_b128 v[64:67], v106 offset:49152\n\t"
        "v_mfma_f32_16x16x32_bf16 v[52:55], v[68:71], a[116:119], v[52:55]\n\t"
        "s_waitcnt lgkmcnt(6)\n\t"
        "v_mfma_f32_16x16x32_bf16 v[32:35], a[120:123], v[72:75], v[32:35]\n\t"
        "v_mfma_f32_16x16x32_bf16 v[36:39], a[124:127], v[72:75], v[36:39]\n\t"
        "ds_read_b128 v[68:71], v106 offset:51200\n\t"
        "v_mfma_f32_16x16x32_bf16 v[40:43], v[72:75], a[128:131], v[40:43]\n\t"
        "s_add_u32 m0, %[ld], 17408\n\t"
        "s_add_u32 %[t0], %[xo], 16640\n\t"
        "s_waitcnt lgkmcnt(6)\n\t"
        "v_mfma_f32_16x16x32_bf16 v[44:47], a[120:123], v[76:79], v[44:47]\n\t"
        "buffer_load_dwordx4 v100, %[sx], %[t0] offen lds\n\t"
        "v_mfma_f32_16x16x32_bf16 v[48:51], a[124:127], v[76:79], v[48:51]\n\t"
        "ds_read_b128 v[72:75], v105 offset:57344\n\t"
        "v_mfma_f32_16x16x32_bf16 v[52:55], v[76:79], a[128:131], v[52:55]\n\t"
        "s_waitcnt lgkmcnt(6)\n\t"
        "v_mfma_f32_16x16x32_bf16 v[32:35], a[132:135], v[80:83], v[32:35]\n\t"
        "v_mfma_f32_16x16x32_bf16 v[36:39], a[136:139], v[80:83], v[36:39]\n\t"
        "ds_read_b128 v[76:79], v105 offset:59392\n\t"
        "v_mfma_f32_16x16x32_bf16 v[40:43], v[80:83], a[140:143], v[40:43]\n\t"
        "s_add_u32 m0, %[ld], 24576\n\t"
        "s_add_u32 %[t0], %[xo], 384\n\t"
        "s_waitcnt lgkmcnt(6)\n\t"
        "v_mfma_f32_16x16x32_bf16 v[44:47], a[132:135], v[84:87], v[44:47]\n\t"
        "buffer_load_dwordx4 v100, %[sx], %[t0] offen lds\n\t"
        "v_mfma_f32_16x16x32_bf16 v[48:51], a[136:139], v[84:87], v[48:51]\n\t"
        "ds_read_b128 v[80:83], v106 offset:57344\n\t"
        "v_mfma_f32_16x16x32_bf16 v[52:55], v[84:87], a[140:143], v[52:55]\n\t"
        "s_waitcnt lgkmcnt(6)\n\t"
        "v_mfma_f32_16x16x32_bf16 v[32:35], a[144:147], v[56:59], v[32:35]\n\t"
        "v_mfma_f32_16x16x32_bf16 v[36:39], a[148:151], v[56:59], v[36:39]\n\t"
        "ds_read_b128 v[84:87], v106 offset:59392\n\t"
        "v_mfma_f32_16x16x32_bf16 v[40:43], v[56:59], a[152:155], v[40:43]\n\t"
        "s_add_u32 m0, %[ld], 25600\n\t"
        "s_add_u32 %[t0], %[xo], 16768\n\t"
        "s_waitcnt lgkmcnt(6)\n\t"
        "v_mfma_f32_16x16x32_bf16 v[44:47], a[144:147], v[60:63], v[44:47]\n\t"
        "buffer_load_dwordx4 v100, %[sx], %[t0] offen lds\n\t"
        "v_mfma_f32_16x16x32_bf16 v[48:51], a[148:151], v[60:63], v[48:51]\n\t"
        "v_mfma_f32_16x16x32_bf16 v[52:55], v[60:63], a[152:155], v[52:55]\n\t"
        "s_waitcnt lgkmcnt(5)\n\t"
        "v_mfma_f32_16x16x32_bf16 v[32:35], a[156:159], v[64:67], v[32:35]\n\t"
        "s_waitcnt vmcnt(9) lgkmcnt(0)\n\t"
        "s_barrier\n\t"
        "v_mfma_f32_16x16x32_bf16 v[36:39], a[160:163], v[64:67], v[36:39]\n\t"
        "ds_read_b128 v[56:59], v107 offset:0\n\t"
        "v_mfma_f32_16x16x32_bf16 v[40:43], v[64:67], a[164:167], v[40:43]\n\t"
        "ds_read_b128 v[60:63], v107 offset:2048\n\t"
        "s_add_u32 m0, %[ld], 32768\n\t"
        "s_add_u32 %[t0], %[xo], 512\n\t"
        "v_mfma_f32_16x16x32_bf16 v[44:47], a[156:159], v[68:71], v[44:47]\n\t"
        "buffer_load_dwordx4 v100, %[sx], %[t0] offen lds\n\t"
        "v_mfma_f32_16x16x32_bf16 v[48:51], a[160:163], v[68:71], v[48:51]\n\t"
        "ds_read_b128 v[64:67], v108 offset:0\n\t"
        "v_mfma_f32_16x16x32_bf16 v[52:55], v[68:71], a[164:167], v[52:55]\n\t"
        "v_mfma_f32_16x16x32_bf16 v[32:35], a[168:171], v[72:75], v[32:35]\n\t"
        "v_mfma_f32_16x16x32_bf16 v[36:39], a[172:175], v[72:75], v[36:39]\n\t"
        "ds_read_b128 v[68:71], v108 offset:2048\n\t"
        "v_mov_b32_e32 v119, %[cn]\n\t"
        "buffer_store_dword v119, v113, %[sw], 0 offen sc1\n\t"
        "s_add_u32 %[cn], %[cn], 1\n\t"
        "v_mfma_f32_16x16x32_bf16 v[40:43], v[72:75], a[176:179], v[40:43]\n\t"
        "s_add_u32 m0, %[ld], 33792\n\t"
        "s_add_u32 %[t0], %[xo], 16896\n\t"
        "v_mfma_f32_16x16x32_bf16 v[44:47], a[168:171], v[76:79], v[44:47]\n\t"
        "buffer_load_dwordx4 v100, %[sx], %[t0] offen lds\n\t"
        "v_mfma_f32_16x16x32_bf16 v[48:51], a[172:175], v[76:79], v[48:51]\n\t"
        "ds_read_b128 v[72:75], v107 offset:8192\n\t"
        "v_mfma_f32_16x16x32_bf16 v[52:55], v[76:79], a[176:179], v[52:55]\n\t"
        "v_mfma_f32_16x16x32_bf16 v[32:35], a[180:183], v[80:83], v[32:35]\n\t"
        "v_mfma_f32_16x16x32_bf16 v[36:39], a[184:187], v[80:83], v[36:39]\n\t"
        "ds_read_b128 v[76:79], v107 offset:10240\n\t"
        "v_mfma_f32_16x16x32_bf16 v[40:43], v[80:83], a[188:191], v[40:43]\n\t"
        "s_add_u32 m0, %[ld], 40960\n\t"
        "s_add_u32 %[t0], %[xo], 640\n\t"
        "v_mfma_f32_16x16x32_bf16 v[44:47], a[180:183], v[84:87], v[44:47]\n\t"
        "buffer_load_dwordx4 v100, %[sx], %[t0] offen lds\n\t"
        "v_mfma_f32_16x16x32_bf16 v[48:51], a[184:187], v[84:87], v[48:51]\n\t"
        "ds_read_b128 v[80:83], v108 offset:8192\n\t"
        "v_mfma_f32_16x16x32_bf16 v[52:55], v[84:87], a[188:191], v[52:55]\n\t"
        "s_waitcnt lgkmcnt(6)\n\t"
        "v_mfma_f32_16x16x32_bf16 v[32:35], a[192:195], v[56:59], v[32:35]\n\t"
        "v_mfma_f32_16x16x32_bf16 v[36:39], a[196:199], v[56:59], v[36:39]\n\t"
        "ds_read_b128 v[84:87], v108 offset:10240\n\t"
        "v_mfma_f32_16x16x32_bf16 v[40:43], v[56:59], a[200:203], v[40:43]\n\t"
        "s_add_u32 m0, %[ld], 41984\n\t"
        "s_add_u32 %[t0], %[xo], 17024\n\t"
        "s_waitcnt lgkmcnt(6)\n\t"
        "v_mfma_f32_16x16x32_bf16 v[44:47], a[192:195], v[60:63], v[44:47]\n\t"
        "buffer_load_dwordx4 v100, %[sx], %[t0] offen lds\n\t"
        "v_mfma_f32_16x16x32_bf16 v[48:51], a[196:199], v[60:63], v[48:51]\n\t"
        "ds_read_b128 v[56:59], v107 offset:16384\n\t"
        "v_mfma_f32_16x16x32_bf16 v[52:55], v[60:63], a[200:203], v[52:55]\n\t"
        "s_waitcnt lgkmcnt(6)\n\t"
        "v_mfma_f32_16x16x32_bf16 v[32:35], a[204:207], v[64:67], v[32:35]\n\t"
        "v_mfma_f32_16x16x32_bf16 v[36:39], a[208:211], v[64:67], v[36:39]\n\t"
        "ds_read_b128 v[60:63], v107 offset:18432\n\t"
        "v_mfma_f32_16x16x32_bf16 v[40:43], v[64:67], a[212:215], v[40:43]\n\t"
        "s_add_u32 m0, %[ld], 49152\n\t"
        "s_add_u32 %[t0], %[xo], 768\n\t"
        "s_waitcnt lgkmcnt(6)\n\t"
        "v_mfma_f32_16x16x32_bf16 v[44:47], a[204:207], v[68:71], v[44:47]\n\t"
        "buffer_load_dwordx4 v100, %[sx], %[t0] offen lds\n\t"
        "v_mfma_f32_16x16x32_bf16 v[48:51], a[208:211], v[68:71], v[48:51]\n\t"
        "ds_read_b128 v[64:67], v108 offset:16384\n\t"
        "v_mfma_f32_16x16x32_bf16 v[52:55], v[68:71], a[212:215], v[52:55]\n\t"
        "s_waitcnt lgkmcnt(6)\n\t"
        "v_mfma_f32_16x16x32_bf16 v[32:35], a[216:219], v[72:75], v[32:35]\n\t"
        "v_mfma_f32_16x16x32_bf16 v[36:39], a[220:223], v[72:75], v[36:39]\n\t"
        "ds_read_b128 v[68:71], v108 offset:18432\n\t"
        "v_mfma_f32_16x16x32_bf16 v[40:43], v[72:75], a[224:227], v[40:43]\n\t"
        "s_add_u32 m0, %[ld], 50176\n\t"
        "s_add_u32 %[t0], %[xo], 17152\n\t"
        "s_waitcnt lgkmcnt(6)\n\t"
        "v_mfma_f32_16x16x32_bf16 v[44:47], a[216:219], v[76:79], v[44:47]\n\t"
        "buffer_load_dwordx4 v100, %[sx], %[t0] offen lds\n\t"
        "v_mfma_f32_16x16x32_bf16 v[48:51], a[220:223], v[76:79], v[48:51]\n\t"
        "ds_read_b128 v[72:75], v107 offset:24576\n\t"
        "v_mfma_f32_16x16x32_bf16 v[52:55], v[76:79], a[224:227], v[52:55]\n\t"
        "s_waitcnt lgkmcnt(6)\n\t"
        "v_mfma_f32_16x16x32_bf16 v[32:35], a[228:231], v[80:83], v[32:35]\n\t"
        "v_mfma_f32_16x16x32_bf16 v[36:39], a[232:235], v[80:83], v[36:39]\n\t"
        "ds_read_b128 v[76:79], v107 offset:26624\n\t"
        "v_mfma_f32_16x16x32_bf16 v[40:43], v[80:83], a[236:239], v[40:43]\n\t"
        "s_add_u32 m0, %[ld], 57344\n\t"
        "s_add_u32 %[t0], %[xo], 896\n\t"
        "s_waitcnt lgkmcnt(6)\n\t"
        "v_mfma_f32_16x16x32_bf16 v[44:47], a[228:231], v[84:87], v[44:47]\n\t"
        "buffer_load_dwordx4 v100, %[sx], %[t0] offen lds\n\t"
        "v_mfma_f32_16x16x32_bf16 v[48:51], a[232:235], v[84:87], v[48:51]\n\t"
        "ds_read_b128 v[80:83], v108 offset:24576\n\t"
        "v_mfma_f32_16x16x32_bf16 v[52:55], v[84:87], a[236:239], v[52:55]\n\t"
        "s_waitcnt lgkmcnt(6)\n\t"
        "v_mfma_f32_16x16x32_bf16 v[32:35], a[240:243], v[56:59], v[32:35]\n\t"
        "v_mfma_f32_16x16x32_bf16 v[36:39], a[244:247], v[56:59], v[36:39]\n\t"
        "ds_read_b128 v[84:87], v108 offset:26624\n\t"
        "v_mfma_f32_16x16x32_bf16 v[40:43], v[56:59], a[248:251], v[40:43]\n\t"
        "s_add_u32 m0, %[ld], 58368\n\t"
        "s_add_u32 %[t0], %[xo], 17280\n\t"
        "s_waitcnt lgkmcnt(6)\n\t"
        "v_mfma_f32_16x16x32_bf16 v[44:47], a[240:243], v[60:63], v[44:47]\n\t"
        "buffer_load_dwordx4 v100, %[sx], %[t0] offen lds\n\t"
        "v_mfma_f32_16x16x32_bf16 v[48:51], a[244:247], v[60:63], v[48:51]\n\t"
        "v_mfma_f32_16x16x32_bf16 v[52:55], v[60:63], a[248:251], v[52:55]\n\t"
        "s_waitcnt lgkmcnt(5)\n\t"
        "v_mfma_f32_16x16x32_bf16 v[32:35], a[252:255], v[64:67], v[32:35]\n\t"
        "s_waitcnt vmcnt(17) lgkmcnt(0)\n\t"
        "s_barrier\n\t"
        "v_mfma_f32_16x16x32_bf16 v[36:39], v[128:131], v[64:67], v[36:39]\n\t"
        "ds_read_b128 v[56:59], v107 offset:32768\n\t"
        "s_cmp_eq_u32 %[cn], %[wv]\n\t"
        "s_cselect_b32 %[t0], 0, 0xf0000000\n\t"
        "buffer_load_dword v118, v117, %[sx], %[t0] offen\n\t"
        "v_mfma_f32_16x16x32_bf16 v[40:43], v[64:67], v[132:135], v[40:43]\n\t"
        "ds_read_b128 v[60:63], v107 offset:34816\n\t"
        "s_add_u32 m0, %[ld], 65536\n\t"
        "s_add_u32 %[t0], %[xo], 1024\n\t"
        "v_mfma_f32_16x16x32_bf16 v[44:47], a[252:255], v[68:71], v[44:47]\n\t"
        "buffer_load_dwordx4 v100, %[sx], %[t0] offen lds\n\t"
        "v_mfma_f32_16x16x32_bf16 v[48:51], v[128:131], v[68:71], v[48:51]\n\t"
        "ds_read_b128 v[64:67], v108 offset:32768\n\t"
        "v_mfma_f32_16x16x32_bf16 v[52:55], v[68:71], v[132:135], v[52:55]\n\t"
        "v_mfma_f32_16x16x32_bf16 v[32:35], v[136:139], v[72:75], v[32:35]\n\t"
        "v_mfma_f32_16x16x32_bf16 v[36:39], v[140:143], v[72:75], v[36:39]\n\t"
        "ds_read_b128 v[68:71], v108 offset:34816\n\t"
        "v_mfma_f32_16x16x32_bf16 v[40:43], v[72:75], v[144:147], v[40:43]\n\t"
        "s_add_u32 m0, %[ld], 66560\n\t"
        "s_add_u32 %[t0], %[xo], 17408\n\t"
        "v_mfma_f32_16x16x32_bf16 v[44:47], v[136:139], v[76:79], v[44:47]\n\t"
        "buffer_load_dwordx4 v100, %[sx], %[t0] offen lds\n\t"
        "v_mfma_f32_16x16x32_bf16 v[48:51], v[140:143], v[76:79], v[48:51]\n\t"
        "ds_read_b128 v[72:75], v107 offset:40960\n\t"
        "v_mfma_f32_16x16x32_bf16 v[52:55], v[76:79], v[144:147], v[52:55]\n\t"
        "v_mfma_f32_16x16x32_bf16 v[32:35], v[148:151], v[80:83], v[32:35]\n\t"
        "v_mfma_f32_16x16x32_bf16 v[36:39], v[152:155], v[80:83], v[36:39]\n\t"
        "ds_read_b128 v[76:79], v107 offset:43008\n\t"
        "v_mfma_f32_16x16x32_bf16 v[40:43], v[80:83], v[156:159], v[40:43]\n\t"
        "s_add_u32 m0, %[ld], 73728\n\t"
        "s_add_u32 %[t0], %[xo], 1152\n\t"
        "v_mfma_f32_16x16x32_bf16 v[44:47], v[148:151], v[84:87], v[44:47]\n\t"
        "buffer_load_dwordx4 v100, %[sx], %[t0] offen lds\n\t"
        "v_mfma_f32_16x16x32_bf16 v[48:51], v[152:155], v[84:87], v[48:51]\n\t"
        "ds_read_b128 v[80:83], v108 offset:40960\n\t"
        "v_mfma_f32_16x16x32_bf16 v[52:55], v[84:87], v[156:159], v[52:55]\n\t"
        "s_waitcnt lgkmcnt(6)\n\t"
        "v_mfma_f32_16x16x32_bf16 v[32:35], v[160:163], v[56:59], v[32:35]\n\t"
        "v_mfma_f32_16x16x32_bf16 v[36:39], v[164:167], v[56:59], v[36:39]\n\t"
        "ds_read_b128 v[84:87], v108 offset:43008\n\t"
        "v_mfma_f32_16x16x32_bf16 v[40:43], v[56:59], v[168:171], v[40:43]\n\t"
        "s_add_u32 m0, %[ld], 74752\n\t"
        "s_add_u32 %[t0], %[xo], 17536\n\t"
        "s_waitcnt lgkmcnt(6)\n\t"
        "v_mfma_f32_16x16x32_bf16 v[44:47], v[160:163], v[60:63], v[44:47]\n\t"
        "buffer_load_dwordx4 v100, %[sx], %[t0] offen lds\n\t"
        "v_mfma_f32_16x16x32_bf16 v[48:51], v[164:167], v[60:63], v[48:51]\n\t"
        "ds_read_b128 v[56:59], v107 offset:49152\n\t"
        "v_mfma_f32_16x16x32_bf16 v[52:55], v[60:63], v[168:171], v[52:55]\n\t"
        "s_waitcnt lgkmcnt(6)\n\t"
        "v_mfma_f32_16x16x32_bf16 v[32:35], v[172:175], v[64:67], v[32:35]\n\t"
        "v_mfma_f32_16x16x32_bf16 v[36:39], v[176:179], v[64:67], v[36:39]\n\t"
        "ds_read_b128 v[60:63], v107 offset:51200\n\t"
        "v_mfma_f32_16x16x32_bf16 v[40:43], v[64:67], v[180:183], v[40:43]\n\t"
        "s_add_u32 m0, %[ld], 81920\n\t"
        "s_add_u32 %[t0], %[xo], 1280\n\t"
        "s_waitcnt lgkmcnt(6)\n\t"
        "v_mfma_f32_16x16x32_bf16 v[44:47], v[172:175], v[68:71], v[44:47]\n\t"
        "buffer_load_dwordx4 v100, %[sx], %[t0] offen lds\n\t"
        "v_mfma_f32_16x16x32_bf16 v[48:51], v[176:179], v[68:71], v[48:51]\n\t"
        "ds_read_b128 v[64:67], v108 offset:49152\n\t"
        "v_mfma_f32_16x16x32_bf16 v[52:55], v[68:71], v[180:183], v[52:55]\n\t"
        "s_waitcnt lgkmcnt(6)\n\t"
        "v_mfma_f32_16x16x32_bf16 v[32:35], v[184:187], v[72:75], v[32:35]\n\t"
        "v_mfma_f32_16x16x32_bf16 v[36:39], v[188:191], v[72:75], v[36:39]\n\t"
        "ds_read_b128 v[68:71], v108 offset:51200\n\t"
        "v_mfma_f32_16x16x32_bf16 v[40:43], v[72:75], v[192:195], v[40:43]\n\t"
        "s_add_u32 m0, %[ld], 82944\n\t"
        "s_add_u32 %[t0], %[xo], 17664\n\t"
        "s_waitcnt lgkmcnt(6)\n\t"
        "v_mfma_f32_16x16x32_bf16 v[44:47], v[184:187], v[76:79], v[44:47]\n\t"
        "buffer_load_dwordx4 v100, %[sx], %[t0] offen lds\n\t"
        "v_mfma_f32_16x16x32_bf16 v[48:51], v[188:191], v[76:79], v[48:51]\n\t"
        "ds_read_b128 v[72:75], v107 offset:57344\n\t"
        "v_mfma_f32_16x16x32_bf16 v[52:55], v[76:79], v[192:195], v[52:55]\n\t"
        "s_waitcnt lgkmcnt(6)\n\t"
        "v_mfma_f32_16x16x32_bf16 v[32:35], v[196:199], v[80:83], v[32:35]\n\t"
        "v_mfma_f32_16x16x32_bf16 v[36:39], v[200:203], v[80:83], v[36:39]\n\t"
        "ds_read_b128 v[76:79], v107 offset:59392\n\t"
        "v_mfma_f32_16x16x32_bf16 v[40:43], v[80:83], v[204:207], v[40:43]\n\t"
        "s_add_u32 m0, %[ld], 90112\n\t"
        "s_add_u32 %[t0], %[xo], 1408\n\t"
        "s_waitcnt lgkmcnt(6)\n\t"
        "v_mfma_f32_16x16x32_bf16 v[44:47], v[196:199], v[84:87], v[44:47]\n\t"
        "buffer_load_dwordx4 v100, %[sx], %[t0] offen lds\n\t"
        "v_mfma_f32_16x16x32_bf16 v[48:51], v[200:203], v[84:87], v[48:51]\n\t"
        "ds_read_b128 v[80:83], v108 offset:57344\n\t"
        "v_mfma_f32_16x16x32_bf16 v[52:55], v[84:87], v[204:207], v[52:55]\n\t"
        "s_waitcnt lgkmcnt(6)\n\t"
        "v_mfma_f32_16x16x32_bf16 v[32:35], v[208:211], v[56:59], v[32:35]\n\t"
        "v_mfma_f32_16x16x32_bf16 v[36:39], v[212:215], v[56:59], v[36:39]\n\t"
        "ds_read_b128 v[84:87], v108 offset:59392\n\t"
        "v_mfma_f32_16x16x32_bf16 v[40:43], v[56:59], v[216:219], v[40:43]\n\t"
        "s_add_u32 m0, %[ld], 91136\n\t"
        "s_add_u32 %[t0], %[xo], 17792\n\t"
        "s_waitcnt lgkmcnt(6)\n\t"
        "v_mfma_f32_16x16x32_bf16 v[44:47], v[208:211], v[60:63], v[44:47]\n\t"
        "buffer_load_dwordx4 v100, %[sx], %[t0] offen lds\n\t"
        "v_mfma_f32_16x16x32_bf16 v[48:51], v[212:215], v[60:63], v[48:51]\n\t"
        "v_mfma_f32_16x16x32_bf16 v[52:55], v[60:63], v[216:219], v[52:55]\n\t"
        "s_waitcnt lgkmcnt(5)\n\t"
        "v_mfma_f32_16x16x32_bf16 v[32:35], v[220:223], v[64:67], v[32:35]\n\t"
        "s_waitcnt vmcnt(18) lgkmcnt(0)\n\t"
        "s_barrier\n\t"
        "v_mfma_f32_16x16x32_bf16 v[36:39], v[224:227], v[64:67], v[36:39]\n\t"
        "ds_read_b128 v[56:59], v105 offset:0\n\t"
        "v_mfma_f32_16x16x32_bf16 v[40:43], v[64:67], v[228:231], v[40:43]\n\t"
        "ds_read_b128 v[60:63], v105 offset:2048\n\t"
        "s_add_u32 m0, %[ld], 98304\n\t"
        "s_add_u32 %[t0], %[xo], 1536\n\t"
        "v_mfma_f32_16x16x32_bf16 v[44:47], v[220:223], v[68:71], v[44:47]\n\t"
        "buffer_load_dwordx4 v100, %[sx], %[t0] offen lds\n\t"
        "v_mfma_f32_16x16x32_bf16 v[48:51], v[224:227], v[68:71], v[48:51]\n\t"
        "ds_read_b128 v[64:67], v106 offset:0\n\t"
        "v_mfma_f32_16x16x32_bf16 v[52:55], v[68:71], v[228:231], v[52:55]\n\t"
        "v_mfma_f32_16x16x32_bf16 v[32:35], v[232:235], v[72:75], v[32:35]\n\t"
        "v_mfma_f32_16x16x32_bf16 v[36:39], v[236:239], v[72:75], v[36:39]\n\t"
        "ds_read_b128 v[68:71], v106 offset:2048\n\t"
        "v_mfma_f32_16x16x32_bf16 v[40:43], v[72:75], v[240:243], v[40:43]\n\t"
        "s_add_u32 m0, %[ld], 99328\n\t"
        "s_add_u32 %[t0], %[xo], 17920\n\t"
        "v_mfma_f32_16x16x32_bf16 v[44:47], v[232:235], v[76:79], v[44:47]\n\t"
        "buffer_load_dwordx4 v100, %[sx], %[t0] offen lds\n\t"
        "v_mfma_f32_16x16x32_bf16 v[48:51], v[236:239], v[76:79], v[48:51]\n\t"
        "ds_read_b128 v[72:75], v105 offset:8192\n\t"
        "v_mfma_f32_16x16x32_bf16 v[52:55], v[76:79], v[240:243], v[52:55]\n\t"
        "v_mfma_f32_16x16x32_bf16 v[32:35], v[244:247], v[80:83], v[32:35]\n\t"
        "v_mfma_f32_16x16x32_bf16 v[36:39], v[248:251], v[80:83], v[36:39]\n\t"
        "ds_read_b128 v[76:79], v105 offset:10240\n\t"
        "v_mfma_f32_16x16x32_bf16 v[40:43], v[80:83], v[252:255], v[40:43]\n\t"
        "s_add_u32 m0, %[ld], 106496\n\t"
        "s_add_u32 %[t0], %[xo], 1664\n\t"
        "v_mfma_f32_16x16x32_bf16 v[44:47], v[244:247], v[84:87], v[44:47]\n\t"
        "buffer_load_dwordx4 v100, %[sx], %[t0] offen lds\n\t"
        "v_mfma_f32_16x16x32_bf16 v[48:51], v[248:251], v[84:87], v[48:51]\n\t"
        "ds_read_b128 v[80:83], v106 offset:8192\n\t"
        "v_mfma_f32_16x16x32_bf16 v[52:55], v[84:87], v[252:255], v[52:55]\n\t"
        "s_cmp_lt_u32 %[cn], 32\n\t"
        "s_cbranch_scc1 LG_loop%=\n\t"
        "s_nop 7\n\t"
        "v_cvt_pk_bf16_f32 v88, v32, v33\n\t"
        "v_cvt_pk_bf16_f32 v89, v34, v35\n\t"
        "v_cvt_pk_bf16_f32 v90, v36, v37\n\t"
        "v_cvt_pk_bf16_f32 v91, v38, v39\n\t"
        "buffer_store_dwordx4 v[88:91], v109, %[sw], %[so] offen sc1\n\t"
        "v_cvt_pk_bf16_f32 v92, v44, v45\n\t"
        "v_cvt_pk_bf16_f32 v93, v46, v47\n\t"
        "v_cvt_pk_bf16_f32 v94, v48, v49\n\t"
        "v_cvt_pk_bf16_f32 v95, v50, v51\n\t"
        "buffer_store_dwordx4 v[92:95], v110, %[sw], %[so] offen sc1\n\t"
        "v_cvt_pk_bf16_f32 v96, v40, v41\n\t"
        "v_cvt_pk_bf16_f32 v97, v42, v43\n\t"
        "v_cvt_pk_bf16_f32 v98, v52, v53\n\t"
        "v_cvt_pk_bf16_f32 v99, v54, v55\n\t"
        "buffer_store_dwordx4 v[96:99], v111, %[sw], %[so] offen sc1\n\t"
        "s_waitcnt vmcnt(0) lgkmcnt(0)\n\t"
        "v_mov_b32_e32 v119, %[cn]\n\t"
        "buffer_store_dword v119, v113, %[sw], 0 offen sc1\n\t"
        "s_barrier\n\t"
        "s_branch LG_end%=\n\t"
        "LG_first%=:\n\t"
        "s_waitcnt vmcnt(0)\n\t"
        "v_mov_b32_e32 v119, %[cn]\n\t"
        "buffer_store_dword v119, v113, %[sw], 0 offen sc1\n\t"
        "s_branch LG_back%=\n\t"
        "LG_end%=:\n\t"
        ".Lk_end_G:\n\t"
        : [t0] "=&s"(t0), [xo] "=&s"(xo), [so] "=&s"(so), [cn] "=&s"(cn)
        : [sx] "s"(sx), [sq] "s"(sq), [sv] "s"(sv), [sw] "s"(sw), [ld] "s"(lb + w * 2048), [wv] "s"(2 * w + 2),
          "{v117}"((w * 8192 + (h * 4 + c) * 64 + l) * 128),
          "{v100}"((16 * w + (l >> 3)) * 2048 + (((l & 7) ^ (l >> 3)) << 4)), "{v101}"((8 * w + (l >> 3)) * 8192 + h * 256 + (c & 1) * 128 + (l & 7) * 16),
          "{v112}"((16 * w + (l >> 2)) * 8192 + h * 256 + cv * 64 + (l & 3) * 16), "{v102}"(lb + 131072 + w * 1024 + ((l * 16) ^ ((l & 16) << 2))), "{v116}"(lb + 131072 + w * 1024 + ((l * 16) ^ (l & 32))),
          "{v103}"(lr + rr * 128 + ((tn ^ (n >> 3)) * 64) + (n & 3) * 16), "{v104}"(lr + 8192 + rr * 64 + ((tn ^ (g & 1)) * 32) + (n & 3) * 8), "{v105}"(lx), "{v106}"(lx ^ 64u), "{v107}"(lx + 65536), "{v108}"((lx ^ 64u) + 65536),
          "{v109}"(aq), "{v110}"(aq + 4096), "{v111}"(W_VF + (tm * 32 + h) * 8192 + (2 * cv + tn) * 1024 + l * 16), "{v113}"(l == 0 ? F_KVQ + h * 1024 + (c * 4 + w) * 64 : OOB)
        : "memory", "scc", "m0", "v8", "v9", "v10", "v11", "v12", "v13", "v14", "v15", "v16", "v17", "v18", "v19", "v20", "v21", "v22", "v23", "v24", "v25", "v26", "v27", "v28", "v29", "v30", "v31", "v32", "v33", "v34", "v35", "v36", "v37", "v38", "v39", "v40", "v41", "v42", "v43", "v44", "v45", "v46", "v47", "v48", "v49", "v50", "v51", "v52", "v53", "v54", "v55", "v56", "v57", "v58", "v59", "v60", "v61", "v62", "v63", "v64", "v65", "v66", "v67", "v68", "v69", "v70", "v71", "v72", "v73", "v74", "v75", "v76", "v77", "v78", "v79", "v80", "v81", "v82", "v83", "v84", "v85", "v86", "v87", "v88", "v89", "v90", "v91", "v92", "v93", "v94", "v95", "v96", "v97", "v98", "v99", "v118", "v119", "v120", "v121", "v122", "v123", "v124", "v125", "v126", "v127", "v128", "v129", "v130", "v131", "v132", "v133", "v134", "v135", "v136", "v137", "v138", "v139", "v140", "v141", "v142", "v143", "v144", "v145", "v146", "v147", "v148", "v149", "v150", "v151", "v152", "v153", "v154", "v155", "v156", "v157", "v158", "v159", "v160", "v161", "v162", "v163", "v164", "v165", "v166", "v167", "v168", "v169", "v170", "v171", "v172", "v173", "v174", "v175", "v176", "v177", "v178", "v179", "v180", "v181", "v182", "v183", "v184", "v185", "v186", "v187", "v188", "v189", "v190", "v191", "v192", "v193", "v194", "v195", "v196", "v197", "v198", "v199", "v200", "v201", "v202", "v203", "v204", "v205", "v206", "v207", "v208", "v209", "v210", "v211", "v212", "v213", "v214", "v215", "v216", "v217", "v218", "v219", "v220", "v221", "v222", "v223", "v224", "v225", "v226", "v227", "v228", "v229", "v230", "v231", "v232", "v233", "v234", "v235", "v236", "v237", "v238", "v239", "v240", "v241", "v242", "v243", "v244", "v245", "v246", "v247", "v248", "v249", "v250", "v251", "v252", "v253", "v254", "v255", "a0", "a1", "a2", "a3", "a4", "a5", "a6", "a7", "a8", "a9", "a10", "a11", "a12", "a13", "a14", "a15", "a16", "a17", "a18", "a19", "a20", "a21", "a22", "a23", "a24", "a25", "a26", "a27", "a28", "a29", "a30", "a31", "a32", "a33", "a34", "a35", "a36", "a37", "a38", "a39", "a40", "a41", "a42", "a43", "a44", "a45", "a46", "a47", "a48", "a49", "a50", "a51", "a52", "a53", "a54", "a55", "a56", "a57", "a58", "a59", "a60", "a61", "a62", "a63", "a64", "a65", "a66", "a67", "a68", "a69", "a70", "a71", "a72", "a73", "a74", "a75", "a76", "a77", "a78", "a79", "a80", "a81", "a82", "a83", "a84", "a85", "a86", "a87", "a88", "a89", "a90", "a91", "a92", "a93", "a94", "a95", "a96", "a97", "a98", "a99", "a100", "a101", "a102", "a103", "a104", "a105", "a106", "a107", "a108", "a109", "a110", "a111", "a112", "a113", "a114", "a115", "a116", "a117", "a118", "a119", "a120", "a121", "a122", "a123", "a124", "a125", "a126", "a127", "a128", "a129", "a130", "a131", "a132", "a133", "a134", "a135", "a136", "a137", "a138", "a139", "a140", "a141", "a142", "a143", "a144", "a145", "a146", "a147", "a148", "a149", "a150", "a151", "a152", "a153", "a154", "a155", "a156", "a157", "a158", "a159", "a160", "a161", "a162", "a163", "a164", "a165", "a166", "a167", "a168", "a169", "a170", "a171", "a172", "a173", "a174", "a175", "a176", "a177", "a178", "a179", "a180", "a181", "a182", "a183", "a184", "a185", "a186", "a187", "a188", "a189", "a190", "a191", "a192", "a193", "a194", "a195", "a196", "a197", "a198", "a199", "a200", "a201", "a202", "a203", "a204", "a205", "a206", "a207", "a208", "a209", "a210", "a211", "a212", "a213", "a214", "a215", "a216", "a217", "a218", "a219", "a220", "a221", "a222", "a223", "a224", "a225", "a226", "a227", "a228", "a229", "a230", "a231", "a232", "a233", "a234", "a235", "a236", "a237", "a238", "a239", "a240", "a241", "a242", "a243", "a244", "a245", "a246", "a247", "a248", "a249", "a250", "a251", "a252", "a253", "a254", "a255");
}

__device__ __forceinline__ void out_block(const Params& p, unsigned char* lds, const int wv, const int rp, const int ro, const int nt) {
    const int w = wv, l = otid() & 63, n = l & 15, g = l >> 4;
    const unsigned lb = __builtin_amdgcn_readfirstlane((unsigned)(size_t)lds), stg = lb + 131072 + w * 8192;
    const v4u so = mk_srd(p.Wo, 1024 * 4096 * 2), sw = mk_srd(p.ws, W_TOTAL), su = mk_srd(p.out, S * D * 2);
    const unsigned lr = stg + (4 * g + (n >> 2)) * 128 + ((2 * (g & 1) + (n >> 3)) << 5) + (n & 3) * 8;
    unsigned t0, t1, tl = rp, pg, cu, nx, sp, re;
    asm volatile(
        "s_nop 4\n\t"
        "buffer_load_dword v103, v118, %[sw], 0 offen sc1\n\t"
        "s_waitcnt vmcnt(0)\n\t"
        "v_cmp_le_u32_e32 vcc, 45, v103\n\t"
        "s_add_u32 %[t1], %[ro], 7\n\t"
        "s_and_b32 %[t1], %[t1], 7\n\t"
        "s_lshl_b32 %[t1], %[t1], 18\n\t"
        "s_add_u32 %[t0], %[t1], 131072\n\t"
        "buffer_load_dwordx4 v[232:235], v110, %[so], %[t0] offen\n\t"
        "s_cbranch_vccnz 1\n\t"
        "s_sleep 2\n\t"
        "s_add_u32 %[t0], %[t1], 147456\n\t"
        "buffer_load_dwordx4 v[236:239], v110, %[so], %[t0] offen\n\t"
        "s_cbranch_vccnz 1\n\t"
        "s_sleep 2\n\t"
        "s_add_u32 %[t0], %[t1], 163840\n\t"
        "buffer_load_dwordx4 v[240:243], v110, %[so], %[t0] offen\n\t"
        "s_cbranch_vccnz 1\n\t"
        "s_sleep 2\n\t"
        "s_add_u32 %[t0], %[t1], 180224\n\t"
        "buffer_load_dwordx4 v[244:247], v110, %[so], %[t0] offen\n\t"
        "s_cbranch_vccnz 1\n\t"
        "s_sleep 2\n\t"
        "s_add_u32 %[t0], %[t1], 196608\n\t"
        "buffer_load_dwordx4 v[248:251], v110, %[so], %[t0] offen\n\t"
        "s_cbranch_vccnz 1\n\t"
        "s_sleep 2\n\t"
        "s_add_u32 %[t0], %[t1], 212992\n\t"
        "buffer_load_dwordx4 v[252:255], v110, %[so], %[t0] offen\n\t"
        "s_cbranch_vccnz 1\n\t"
        "s_sleep 2\n\t"
        "s_add_u32 %[t0], %[t1], 229376\n\t"
        "buffer_load_dwordx4 v[48:51], v110, %[so], %[t0] offen\n\t"
        "s_cbranch_vccnz 1\n\t"
        "s_sleep 2\n\t"
        "s_add_u32 %[t0], %[t1], 245760\n\t"
        "buffer_load_dwordx4 v[52:55], v110, %[so], %[t0] offen\n\t"
        "s_cbranch_vccnz 1\n\t"
        "s_sleep 2\n\t"
        "ds_write_b128 v111, a[0:3] offset:0\n\t"
        "ds_write_b128 v111, a[4:7] offset:1024\n\t"
        "ds_write_b128 v111, a[8:11] offset:2048\n\t"
        "ds_write_b128 v111, a[12:15] offset:3072\n\t"
        "ds_write_b128 v111, a[16:19] offset:4096\n\t"
        "ds_write_b128 v111, a[20:23] offset:5120\n\t"
        "ds_write_b128 v111, a[24:27] offset:6144\n\t"
        "ds_write_b128 v111, a[28:31] offset:7168\n\t"
        "ds_read_b64_tr_b16 a[0:1], v112 offset:0\n\t"
        "ds_read_b64_tr_b16 a[2:3], v112 offset:2048\n\t"
        "ds_read_b64_tr_b16 a[12:13], v112 offset:4096\n\t"
        "ds_read_b64_tr_b16 a[14:15], v112 offset:6144\n\t"
        "s_waitcnt lgkmcnt(8)\n\t"
        "ds_read_b64_tr_b16 a[4:5], v113 offset:0\n\t"
        "ds_read_b64_tr_b16 a[6:7], v113 offset:2048\n\t"
        "ds_read_b64_tr_b16 a[16:17], v113 offset:4096\n\t"
        "ds_read_b64_tr_b16 a[18:19], v113 offset:6144\n\t"
        "s_waitcnt lgkmcnt(8)\n\t"
        "ds_read_b64_tr_b16 a[8:9], v114 offset:0\n\t"
        "ds_read_b64_tr_b16 a[10:11], v114 offset:2048\n\t"
        "ds_read_b64_tr_b16 a[20:21], v114 offset:4096\n\t"
        "ds_read_b64_tr_b16 a[22:23], v114 offset:6144\n\t"
        "s_waitcnt lgkmcnt(8)\n\t"
        "ds_read_b64_tr_b16 v[56:57], v115 offset:0\n\t"
        "ds_read_b64_tr_b16 v[58:59], v115 offset:2048\n\t"
        "ds_read_b64_tr_b16 v[60:61], v115 offset:4096\n\t"
        "ds_read_b64_tr_b16 v[62:63], v115 offset:6144\n\t"
        "s_waitcnt lgkmcnt(0)\n\t"
        "ds_write_b128 v116, v[56:59] offset:0\n\t"
        "ds_write_b128 v116, v[60:63] offset:1024\n\t"
        "ds_write_b128 v111, a[32:35] offset:0\n\t"
        "ds_write_b128 v111, a[36:39] offset:1024\n\t"
        "ds_write_b128 v111, a[40:43] offset:2048\n\t"
        "ds_write_b128 v111, a[44:47] offset:3072\n\t"
        "ds_write_b128 v111, a[48:51] offset:4096\n\t"
        "ds_write_b128 v111, a[52:55] offset:5120\n\t"
        "ds_write_b128 v111, a[56:59] offset:6144\n\t"
        "ds_write_b128 v111, a[60:63] offset:7168\n\t"
        "ds_read_b64_tr_b16 a[24:25], v112 offset:0\n\t"
        "ds_read_b64_tr_b16 a[26:27], v112 offset:2048\n\t"
        "ds_read_b64_tr_b16 a[36:37], v112 offset:4096\n\t"
        "ds_read_b64_tr_b16 a[38:39], v112 offset:6144\n\t"
        "s_waitcnt lgkmcnt(8)\n\t"
        "ds_read_b64_tr_b16 a[28:29], v113 offset:0\n\t"
        "ds_read_b64_tr_b16 a[30:31], v113 offset:2048\n\t"
        "ds_read_b64_tr_b16 a[40:41], v113 offset:4096\n\t"
        "ds_read_b64_tr_b16 a[42:43], v113 offset:6144\n\t"
        "s_waitcnt lgkmcnt(8)\n\t"
        "ds_read_b64_tr_b16 a[32:33], v114 offset:0\n\t"
        "ds_read_b64_tr_b16 a[34:35], v114 offset:2048\n\t"
        "ds_read_b64_tr_b16 a[44:45], v114 offset:4096\n\t"
        "ds_read_b64_tr_b16 a[46:47], v114 offset:6144\n\t"
        "s_waitcnt lgkmcnt(8)\n\t"
        "ds_read_b64_tr_b16 v[56:57], v115 offset:0\n\t"
        "ds_read_b64_tr_b16 v[58:59], v115 offset:2048\n\t"
        "ds_read_b64_tr_b16 v[60:61], v115 offset:4096\n\t"
        "ds_read_b64_tr_b16 v[62:63], v115 offset:6144\n\t"
        "s_add_u32 %[t1], %[ro], 7\n\t"
        "s_and_b32 %[t1], %[t1], 7\n\t"
        "s_lshl_b32 %[t1], %[t1], 18\n\t"
        "s_add_u32 %[t0], %[t1], 0\n\t"
        "buffer_load_dwordx4 v[208:211], v110, %[so], %[t0] offen\n\t"
        "s_cbranch_vccnz 1\n\t"
        "s_sleep 2\n\t"
        "s_add_u32 %[t0], %[t1], 16384\n\t"
        "buffer_load_dwordx4 v[212:215], v110, %[so], %[t0] offen\n\t"
        "s_cbranch_vccnz 1\n\t"
        "s_sleep 2\n\t"
        "s_add_u32 %[t0], %[t1], 32768\n\t"
        "buffer_load_dwordx4 v[216:219], v110, %[so], %[t0] offen\n\t"
        "s_cbranch_vccnz 1\n\t"
        "s_sleep 2\n\t"
        "s_add_u32 %[t0], %[t1], 49152\n\t"
        "buffer_load_dwordx4 v[220:223], v110, %[so], %[t0] offen\n\t"
        "s_cbranch_vccnz 1\n\t"
        "s_sleep 2\n\t"
        "s_add_u32 %[t0], %[t1], 65536\n\t"
        "buffer_load_dwordx4 v[224:227], v110, %[so], %[t0] offen\n\t"
        "s_cbranch_vccnz 1\n\t"
        "s_sleep 2\n\t"
        "s_add_u32 %[t0], %[t1], 81920\n\t"
        "buffer_load_dwordx4 v[228:231], v110, %[so], %[t0] offen\n\t"
        "s_cbranch_vccnz 1\n\t"
        "s_sleep 2\n\t"
        "s_add_u32 %[t0], %[t1], 98304\n\t"
        "buffer_load_dwordx4 v[40:43], v110, %[so], %[t0] offen\n\t"
        "s_cbranch_vccnz 1\n\t"
        "s_sleep 2\n\t"
        "s_add_u32 %[t0], %[t1], 114688\n\t"
        "buffer_load_dwordx4 v[44:47], v110, %[so], %[t0] offen\n\t"
        "s_cbranch_vccnz 1\n\t"
        "s_sleep 2\n\t"
        "s_waitcnt lgkmcnt(0)\n\t"
        "ds_write_b128 v116, v[56:59] offset:2048\n\t"
        "ds_write_b128 v116, v[60:63] offset:3072\n\t"
        "ds_write_b128 v111, a[64:67] offset:0\n\t"
        "ds_write_b128 v111, a[68:71] offset:1024\n\t"
        "ds_write_b128 v111, a[72:75] offset:2048\n\t"
        "ds_write_b128 v111, a[76:79] offset:3072\n\t"
        "ds_write_b128 v111, a[80:83] offset:4096\n\t"
        "ds_write_b128 v111, a[84:87] offset:5120\n\t"
        "ds_write_b128 v111, a[88:91] offset:6144\n\t"
        "ds_write_b128 v111, a[92:95] offset:7168\n\t"
        "ds_read_b64_tr_b16 a[48:49], v112 offset:0\n\t"
        "ds_read_b64_tr_b16 a[50:51], v112 offset:2048\n\t"
        "ds_read_b64_tr_b16 a[60:61], v112 offset:4096\n\t"
        "ds_read_b64_tr_b16 a[62:63], v112 offset:6144\n\t"
        "s_waitcnt lgkmcnt(8)\n\t"
        "ds_read_b64_tr_b16 a[52:53], v113 offset:0\n\t"
        "ds_read_b64_tr_b16 a[54:55], v113 offset:2048\n\t"
        "ds_read_b64_tr_b16 a[64:65], v113 offset:4096\n\t"
        "ds_read_b64_tr_b16 a[66:67], v113 offset:6144\n\t"
        "s_waitcnt lgkmcnt(8)\n\t"
        "ds_read_b64_tr_b16 a[56:57], v114 offset:0\n\t"
        "ds_read_b64_tr_b16 a[58:59], v114 offset:2048\n\t"
        "ds_read_b64_tr_b16 a[68:69], v114 offset:4096\n\t"
        "ds_read_b64_tr_b16 a[70:71], v114 offset:6144\n\t"
        "s_waitcnt lgkmcnt(8)\n\t"
        "ds_read_b64_tr_b16 v[56:57], v115 offset:0\n\t"
        "ds_read_b64_tr_b16 v[58:59], v115 offset:2048\n\t"
        "ds_read_b64_tr_b16 v[60:61], v115 offset:4096\n\t"
        "ds_read_b64_tr_b16 v[62:63], v115 offset:6144\n\t"
        "s_waitcnt lgkmcnt(0)\n\t"
        "ds_write_b128 v116, v[56:59] offset:4096\n\t"
        "ds_write_b128 v116, v[60:63] offset:5120\n\t"
        "ds_write_b128 v111, a[96:99] offset:0\n\t"
        "ds_write_b128 v111, a[100:103] offset:1024\n\t"
        "ds_write_b128 v111, a[104:107] offset:2048\n\t"
        "ds_write_b128 v111, a[108:111] offset:3072\n\t"
        "ds_write_b128 v111, a[112:115] offset:4096\n\t"
        "ds_write_b128 v111, a[116:119] offset:5120\n\t"
        "ds_write_b128 v111, a[120:123] offset:6144\n\t"
        "ds_write_b128 v111, a[124:127] offset:7168\n\t"
        "ds_read_b64_tr_b16 a[72:73], v112 offset:0\n\t"
        "ds_read_b64_tr_b16 a[74:75], v112 offset:2048\n\t"
        "ds_read_b64_tr_b16 a[84:85], v112 offset:4096\n\t"
        "ds_read_b64_tr_b16 a[86:87], v112 offset:6144\n\t"
        "s_waitcnt lgkmcnt(8)\n\t"
        "ds_read_b64_tr_b16 a[76:77], v113 offset:0\n\t"
        "ds_read_b64_tr_b16 a[78:79], v113 offset:2048\n\t"
        "ds_read_b64_tr_b16 a[88:89], v113 offset:4096\n\t"
        "ds_read_b64_tr_b16 a[90:91], v113 offset:6144\n\t"
        "s_waitcnt lgkmcnt(8)\n\t"
        "ds_read_b64_tr_b16 a[80:81], v114 offset:0\n\t"
        "ds_read_b64_tr_b16 a[82:83], v114 offset:2048\n\t"
        "ds_read_b64_tr_b16 a[92:93], v114 offset:4096\n\t"
        "ds_read_b64_tr_b16 a[94:95], v114 offset:6144\n\t"
        "s_waitcnt lgkmcnt(8)\n\t"
        "ds_read_b64_tr_b16 v[56:57], v115 offset:0\n\t"
        "ds_read_b64_tr_b16 v[58:59], v115 offset:2048\n\t"
        "ds_read_b64_tr_b16 v[60:61], v115 offset:4096\n\t"
        "ds_read_b64_tr_b16 v[62:63], v115 offset:6144\n\t"
        "s_add_u32 %[t1], %[ro], 6\n\t"
        "s_and_b32 %[t1], %[t1], 7\n\t"
        "s_lshl_b32 %[t1], %[t1], 18\n\t"
        "s_add_u32 %[t0], %[t1], 131072\n\t"
        "buffer_load_dwordx4 v[184:187], v110, %[so], %[t0] offen\n\t"
        "s_cbranch_vccnz 1\n\t"
        "s_sleep 2\n\t"
        "s_add_u32 %[t0], %[t1], 147456\n\t"
        "buffer_load_dwordx4 v[188:191], v110, %[so], %[t0] offen\n\t"
        "s_cbranch_vccnz 1\n\t"
        "s_sleep 2\n\t"
        "s_add_u32 %[t0], %[t1], 163840\n\t"
        "buffer_load_dwordx4 v[192:195], v110, %[so], %[t0] offen\n\t"
        "s_cbranch_vccnz 1\n\t"
        "s_sleep 2\n\t"
        "s_add_u32 %[t0], %[t1], 180224\n\t"
        "buffer_load_dwordx4 v[196:199], v110, %[so], %[t0] offen\n\t"
        "s_cbranch_vccnz 1\n\t"
        "s_sleep 2\n\t"
        "s_add_u32 %[t0], %[t1], 196608\n\t"
        "buffer_load_dwordx4 v[200:203], v110, %[so], %[t0] offen\n\t"
        "s_cbranch_vccnz 1\n\t"
        "s_sleep 2\n\t"
        "s_add_u32 %[t0], %[t1], 212992\n\t"
        "buffer_load_dwordx4 v[204:207], v110, %[so], %[t0] offen\n\t"
        "s_cbranch_vccnz 1\n\t"
        "s_sleep 2\n\t"
        "s_add_u32 %[t0], %[t1], 229376\n\t"
        "buffer_load_dwordx4 v[32:35], v110, %[so], %[t0] offen\n\t"
        "s_cbranch_vccnz 1\n\t"
        "s_sleep 2\n\t"
        "s_add_u32 %[t0], %[t1], 245760\n\t"
        "buffer_load_dwordx4 v[36:39], v110, %[so], %[t0] offen\n\t"
        "s_cbranch_vccnz 1\n\t"
        "s_sleep 2\n\t"
        "s_waitcnt lgkmcnt(0)\n\t"
        "ds_write_b128 v116, v[56:59] offset:6144\n\t"
        "ds_write_b128 v116, v[60:63] offset:7168\n\t"
        "ds_write_b128 v111, a[128:131] offset:0\n\t"
        "ds_write_b128 v111, a[132:135] offset:1024\n\t"
        "ds_write_b128 v111, a[136:139] offset:2048\n\t"
        "ds_write_b128 v111, a[140:143] offset:3072\n\t"
        "ds_write_b128 v111, a[144:147] offset:4096\n\t"
        "ds_write_b128 v111, a[148:151] offset:5120\n\t"
        "ds_write_b128 v111, a[152:155] offset:6144\n\t"
        "ds_write_b128 v111, a[156:159] offset:7168\n\t"
        "ds_read_b64_tr_b16 a[96:97], v112 offset:0\n\t"
        "ds_read_b64_tr_b16 a[98:99], v112 offset:2048\n\t"
        "ds_read_b64_tr_b16 a[108:109], v112 offset:4096\n\t"
        "ds_read_b64_tr_b16 a[110:111], v112 offset:6144\n\t"
        "s_waitcnt lgkmcnt(8)\n\t"
        "ds_read_b64_tr_b16 a[100:101], v113 offset:0\n\t"
        "ds_read_b64_tr_b16 a[102:103], v113 offset:2048\n\t"
        "ds_read_b64_tr_b16 a[112:113], v113 offset:4096\n\t"
        "ds_read_b64_tr_b16 a[114:115], v113 offset:6144\n\t"
        "s_waitcnt lgkmcnt(8)\n\t"
        "ds_read_b64_tr_b16 a[104:105], v114 offset:0\n\t"
        "ds_read_b64_tr_b16 a[106:107], v114 offset:2048\n\t"
        "ds_read_b64_tr_b16 a[116:117], v114 offset:4096\n\t"
        "ds_read_b64_tr_b16 a[118:119], v114 offset:6144\n\t"
        "s_waitcnt lgkmcnt(8)\n\t"
        "ds_read_b64_tr_b16 v[56:57], v115 offset:0\n\t"
        "ds_read_b64_tr_b16 v[58:59], v115 offset:2048\n\t"
        "ds_read_b64_tr_b16 v[60:61], v115 offset:4096\n\t"
        "ds_read_b64_tr_b16 v[62:63], v115 offset:6144\n\t"
        "s_waitcnt lgkmcnt(0)\n\t"
        "ds_write_b128 v116, v[56:59] offset:8192\n\t"
        "ds_write_b128 v116, v[60:63] offset:9216\n\t"
        "ds_write_b128 v111, a[160:163] offset:0\n\t"
        "ds_write_b128 v111, a[164:167] offset:1024\n\t"
        "ds_write_b128 v111, a[168:171] offset:2048\n\t"
        "ds_write_b128 v111, a[172:175] offset:3072\n\t"
        "ds_write_b128 v111, a[176:179] offset:4096\n\t"
        "ds_write_b128 v111, a[180:183] offset:5120\n\t"
        "ds_write_b128 v111, a[184:187] offset:6144\n\t"
        "ds_write_b128 v111, a[188:191] offset:7168\n\t"
        "ds_read_b64_tr_b16 a[120:121], v112 offset:0\n\t"
        "ds_read_b64_tr_b16 a[122:123], v112 offset:2048\n\t"
        "ds_read_b64_tr_b16 a[132:133], v112 offset:4096\n\t"
        "ds_read_b64_tr_b16 a[134:135], v112 offset:6144\n\t"
        "s_waitcnt lgkmcnt(8)\n\t"
        "ds_read_b64_tr_b16 a[124:125], v113 offset:0\n\t"
        "ds_read_b64_tr_b16 a[126:127], v113 offset:2048\n\t"
        "ds_read_b64_tr_b16 a[136:137], v113 offset:4096\n\t"
        "ds_read_b64_tr_b16 a[138:139], v113 offset:6144\n\t"
        "s_waitcnt lgkmcnt(8)\n\t"
        "ds_read_b64_tr_b16 a[128:129], v114 offset:0\n\t"
        "ds_read_b64_tr_b16 a[130:131], v114 offset:2048\n\t"
        "ds_read_b64_tr_b16 a[140:141], v114 offset:4096\n\t"
        "ds_read_b64_tr_b16 a[142:143], v114 offset:6144\n\t"
        "s_waitcnt lgkmcnt(8)\n\t"
        "ds_read_b64_tr_b16 v[56:57], v115 offset:0\n\t"
        "ds_read_b64_tr_b16 v[58:59], v115 offset:2048\n\t"
        "ds_read_b64_tr_b16 v[60:61], v115 offset:4096\n\t"
        "ds_read_b64_tr_b16 v[62:63], v115 offset:6144\n\t"
        "s_add_u32 %[t1], %[ro], 6\n\t"
        "s_and_b32 %[t1], %[t1], 7\n\t"
        "s_lshl_b32 %[t1], %[t1], 18\n\t"
        "s_add_u32 %[t0], %[t1], 0\n\t"
        "buffer_load_dwordx4 v[160:163], v110, %[so], %[t0] offen\n\t"
        "s_cbranch_vccnz 1\n\t"
        "s_sleep 2\n\t"
        "s_add_u32 %[t0], %[t1], 16384\n\t"
        "buffer_load_dwordx4 v[164:167], v110, %[so], %[t0] offen\n\t"
        "s_cbranch_vccnz 1\n\t"
        "s_sleep 2\n\t"
        "s_add_u32 %[t0], %[t1], 32768\n\t"
        "buffer_load_dwordx4 v[168:171], v110, %[so], %[t0] offen\n\t"
        "s_cbranch_vccnz 1\n\t"
        "s_sleep 2\n\t"
        "s_add_u32 %[t0], %[t1], 49152\n\t"
        "buffer_load_dwordx4 v[172:175], v110, %[so], %[t0] offen\n\t"
        "s_cbranch_vccnz 1\n\t"
        "s_sleep 2\n\t"
        "s_add_u32 %[t0], %[t1], 65536\n\t"
        "buffer_load_dwordx4 v[176:179], v110, %[so], %[t0] offen\n\t"
        "s_cbranch_vccnz 1\n\t"
        "s_sleep 2\n\t"
        "s_add_u32 %[t0], %[t1], 81920\n\t"
        "buffer_load_dwordx4 v[180:183], v110, %[so], %[t0] offen\n\t"
        "s_cbranch_vccnz 1\n\t"
        "s_sleep 2\n\t"
        "s_add_u32 %[t0], %[t1], 98304\n\t"
        "buffer_load_dwordx4 v[24:27], v110, %[so], %[t0] offen\n\t"
        "s_cbranch_vccnz 1\n\t"
        "s_sleep 2\n\t"
        "s_add_u32 %[t0], %[t1], 114688\n\t"
        "buffer_load_dwordx4 v[28:31], v110, %[so], %[t0] offen\n\t"
        "s_cbranch_vccnz 1\n\t"
        "s_sleep 2\n\t"
        "s_waitcnt lgkmcnt(0)\n\t"
        "ds_write_b128 v116, v[56:59] offset:10240\n\t"
        "ds_write_b128 v116, v[60:63] offset:11264\n\t"
        "ds_write_b128 v111, a[192:195] offset:0\n\t"
        "ds_write_b128 v111, a[196:199] offset:1024\n\t"
        "ds_write_b128 v111, a[200:203] offset:2048\n\t"
        "ds_write_b128 v111, a[204:207] offset:3072\n\t"
        "ds_write_b128 v111, a[208:211] offset:4096\n\t"
        "ds_write_b128 v111, a[212:215] offset:5120\n\t"
        "ds_write_b128 v111, a[216:219] offset:6144\n\t"
        "ds_write_b128 v111, a[220:223] offset:7168\n\t"
        "ds_read_b64_tr_b16 a[144:145], v112 offset:0\n\t"
        "ds_read_b64_tr_b16 a[146:147], v112 offset:2048\n\t"
        "ds_read_b64_tr_b16 a[156:157], v112 offset:4096\n\t"
        "ds_read_b64_tr_b16 a[158:159], v112 offset:6144\n\t"
        "s_waitcnt lgkmcnt(8)\n\t"
        "ds_read_b64_tr_b16 a[148:149], v113 offset:0\n\t"
        "ds_read_b64_tr_b16 a[150:151], v113 offset:2048\n\t"
        "ds_read_b64_tr_b16 a[160:161], v113 offset:4096\n\t"
        "ds_read_b64_tr_b16 a[162:163], v113 offset:6144\n\t"
        "s_waitcnt lgkmcnt(8)\n\t"
        "ds_read_b64_tr_b16 a[152:153], v114 offset:0\n\t"
        "ds_read_b64_tr_b16 a[154:155], v114 offset:2048\n\t"
        "ds_read_b64_tr_b16 a[164:165], v114 offset:4096\n\t"
        "ds_read_b64_tr_b16 a[166:167], v114 offset:6144\n\t"
        "s_waitcnt lgkmcnt(8)\n\t"
        "ds_read_b64_tr_b16 v[56:57], v115 offset:0\n\t"
        "ds_read_b64_tr_b16 v[58:59], v115 offset:2048\n\t"
        "ds_read_b64_tr_b16 v[60:61], v115 offset:4096\n\t"
        "ds_read_b64_tr_b16 v[62:63], v115 offset:6144\n\t"
        "s_waitcnt lgkmcnt(0)\n\t"
        "ds_write_b128 v116, v[56:59] offset:12288\n\t"
        "ds_write_b128 v116, v[60:63] offset:13312\n\t"
        "ds_write_b128 v111, a[224:227] offset:0\n\t"
        "ds_write_b128 v111, a[228:231] offset:1024\n\t"
        "ds_write_b128 v111, a[232:235] offset:2048\n\t"
        "ds_write_b128 v111, a[236:239] offset:3072\n\t"
        "ds_write_b128 v111, a[240:243] offset:4096\n\t"
        "ds_write_b128 v111, a[244:247] offset:5120\n\t"
        "ds_write_b128 v111, a[248:251] offset:6144\n\t"
        "ds_write_b128 v111, a[252:255] offset:7168\n\t"
        "ds_read_b64_tr_b16 a[168:169], v112 offset:0\n\t"
        "ds_read_b64_tr_b16 a[170:171], v112 offset:2048\n\t"
        "ds_read_b64_tr_b16 a[180:181], v112 offset:4096\n\t"
        "ds_read_b64_tr_b16 a[182:183], v112 offset:6144\n\t"
        "s_waitcnt lgkmcnt(8)\n\t"
        "ds_read_b64_tr_b16 a[172:173], v113 offset:0\n\t"
        "ds_read_b64_tr_b16 a[174:175], v113 offset:2048\n\t"
        "ds_read_b64_tr_b16 a[184:185], v113 offset:4096\n\t"
        "ds_read_b64_tr_b16 a[186:187], v113 offset:6144\n\t"
        "s_waitcnt lgkmcnt(8)\n\t"
        "ds_read_b64_tr_b16 a[176:177], v114 offset:0\n\t"
        "ds_read_b64_tr_b16 a[178:179], v114 offset:2048\n\t"
        "ds_read_b64_tr_b16 a[188:189], v114 offset:4096\n\t"
        "ds_read_b64_tr_b16 a[190:191], v114 offset:6144\n\t"
        "s_waitcnt lgkmcnt(8)\n\t"
        "ds_read_b64_tr_b16 v[56:57], v115 offset:0\n\t"
        "ds_read_b64_tr_b16 v[58:59], v115 offset:2048\n\t"
        "ds_read_b64_tr_b16 v[60:61], v115 offset:4096\n\t"
        "ds_read_b64_tr_b16 v[62:63], v115 offset:6144\n\t"
        "s_waitcnt lgkmcnt(0)\n\t"
        "ds_write_b128 v116, v[56:59] offset:14336\n\t"
        "ds_write_b128 v116, v[60:63] offset:15360\n\t"
        "s_waitcnt vmcnt(24)\n\t"
        "ds_write_b128 v111, v[232:235] offset:0\n\t"
        "ds_write_b128 v111, v[236:239] offset:1024\n\t"
        "ds_write_b128 v111, v[240:243] offset:2048\n\t"
        "ds_write_b128 v111, v[244:247] offset:3072\n\t"
        "ds_write_b128 v111, v[248:251] offset:4096\n\t"
        "ds_write_b128 v111, v[252:255] offset:5120\n\t"
        "ds_write_b128 v111, v[48:51] offset:6144\n\t"
        "ds_write_b128 v111, v[52:55] offset:7168\n\t"
        "ds_read_b64_tr_b16 v[232:233], v112 offset:0\n\t"
        "ds_read_b64_tr_b16 v[234:235], v112 offset:2048\n\t"
        "ds_read_b64_tr_b16 v[244:245], v112 offset:4096\n\t"
        "ds_read_b64_tr_b16 v[246:247], v112 offset:6144\n\t"
        "s_waitcnt lgkmcnt(8)\n\t"
        "ds_read_b64_tr_b16 v[236:237], v113 offset:0\n\t"
        "ds_read_b64_tr_b16 v[238:239], v113 offset:2048\n\t"
        "ds_read_b64_tr_b16 v[248:249], v113 offset:4096\n\t"
        "ds_read_b64_tr_b16 v[250:251], v113 offset:6144\n\t"
        "s_waitcnt lgkmcnt(8)\n\t"
        "ds_read_b64_tr_b16 v[240:241], v114 offset:0\n\t"
        "ds_read_b64_tr_b16 v[242:243], v114 offset:2048\n\t"
        "ds_read_b64_tr_b16 v[252:253], v114 offset:4096\n\t"
        "ds_read_b64_tr_b16 v[254:255], v114 offset:6144\n\t"
        "s_waitcnt lgkmcnt(8)\n\t"
        "ds_read_b64_tr_b16 v[56:57], v115 offset:0\n\t"
        "ds_read_b64_tr_b16 v[58:59], v115 offset:2048\n\t"
        "ds_read_b64_tr_b16 v[60:61], v115 offset:4096\n\t"
        "ds_read_b64_tr_b16 v[62:63], v115 offset:6144\n\t"
        "s_add_u32 %[t1], %[ro], 5\n\t"
        "s_and_b32 %[t1], %[t1], 7\n\t"
        "s_lshl_b32 %[t1], %[t1], 18\n\t"
        "s_add_u32 %[t0], %[t1], 131072\n\t"
        "buffer_load_dwordx4 v[136:139], v110, %[so], %[t0] offen\n\t"
        "s_cbranch_vccnz 1\n\t"
        "s_sleep 2\n\t"
        "s_add_u32 %[t0], %[t1], 147456\n\t"
        "buffer_load_dwordx4 v[140:143], v110, %[so], %[t0] offen\n\t"
        "s_cbranch_vccnz 1\n\t"
        "s_sleep 2\n\t"
        "s_add_u32 %[t0], %[t1], 163840\n\t"
        "buffer_load_dwordx4 v[144:147], v110, %[so], %[t0] offen\n\t"
        "s_cbranch_vccnz 1\n\t"
        "s_sleep 2\n\t"
        "s_add_u32 %[t0], %[t1], 180224\n\t"
        "buffer_load_dwordx4 v[148:151], v110, %[so], %[t0] offen\n\t"
        "s_cbranch_vccnz 1\n\t"
        "s_sleep 2\n\t"
        "s_add_u32 %[t0], %[t1], 196608\n\t"
        "buffer_load_dwordx4 v[152:155], v110, %[so], %[t0] offen\n\t"
        "s_cbranch_vccnz 1\n\t"
        "s_sleep 2\n\t"
        "s_add_u32 %[t0], %[t1], 212992\n\t"
        "buffer_load_dwordx4 v[156:159], v110, %[so], %[t0] offen\n\t"
        "s_cbranch_vccnz 1\n\t"
        "s_sleep 2\n\t"
        "s_add_u32 %[t0], %[t1], 229376\n\t"
        "buffer_load_dwordx4 v[48:51], v110, %[so], %[t0] offen\n\t"
        "s_cbranch_vccnz 1\n\t"
        "s_sleep 2\n\t"
        "s_add_u32 %[t0], %[t1], 245760\n\t"
        "buffer_load_dwordx4 v[52:55], v110, %[so], %[t0] offen\n\t"
        "s_cbranch_vccnz 1\n\t"
        "s_sleep 2\n\t"
        "s_waitcnt lgkmcnt(0)\n\t"
        "ds_write_b128 v116, v[56:59] offset:30720\n\t"
        "ds_write_b128 v116, v[60:63] offset:31744\n\t"
        "s_waitcnt vmcnt(24)\n\t"
        "ds_write_b128 v111, v[208:211] offset:0\n\t"
        "ds_write_b128 v111, v[212:215] offset:1024\n\t"
        "ds_write_b128 v111, v[216:219] offset:2048\n\t"
        "ds_write_b128 v111, v[220:223] offset:3072\n\t"
        "ds_write_b128 v111, v[224:227] offset:4096\n\t"
        "ds_write_b128 v111, v[228:231] offset:5120\n\t"
        "ds_write_b128 v111, v[40:43] offset:6144\n\t"
        "ds_write_b128 v111, v[44:47] offset:7168\n\t"
        "ds_read_b64_tr_b16 v[208:209], v112 offset:0\n\t"
        "ds_read_b64_tr_b16 v[210:211], v112 offset:2048\n\t"
        "ds_read_b64_tr_b16 v[220:221], v112 offset:4096\n\t"
        "ds_read_b64_tr_b16 v[222:223], v112 offset:6144\n\t"
        "s_waitcnt lgkmcnt(8)\n\t"
        "ds_read_b64_tr_b16 v[212:213], v113 offset:0\n\t"
        "ds_read_b64_tr_b16 v[214:215], v113 offset:2048\n\t"
        "ds_read_b64_tr_b16 v[224:225], v113 offset:4096\n\t"
        "ds_read_b64_tr_b16 v[226:227], v113 offset:6144\n\t"
        "s_waitcnt lgkmcnt(8)\n\t"
        "ds_read_b64_tr_b16 v[216:217], v114 offset:0\n\t"
        "ds_read_b64_tr_b16 v[218:219], v114 offset:2048\n\t"
        "ds_read_b64_tr_b16 v[228:229], v114 offset:4096\n\t"
        "ds_read_b64_tr_b16 v[230:231], v114 offset:6144\n\t"
        "s_waitcnt lgkmcnt(8)\n\t"
        "ds_read_b64_tr_b16 v[56:57], v115 offset:0\n\t"
        "ds_read_b64_tr_b16 v[58:59], v115 offset:2048\n\t"
        "ds_read_b64_tr_b16 v[60:61], v115 offset:4096\n\t"
        "ds_read_b64_tr_b16 v[62:63], v115 offset:6144\n\t"
        "s_add_u32 %[t1], %[ro], 5\n\t"
        "s_and_b32 %[t1], %[t1], 7\n\t"
        "s_lshl_b32 %[t1], %[t1], 18\n\t"
        "s_add_u32 %[t0], %[t1], 0\n\t"
        "buffer_load_dwordx4 a[240:243], v110, %[so], %[t0] offen\n\t"
        "s_cbranch_vccnz 1\n\t"
        "s_sleep 2\n\t"
        "s_add_u32 %[t0], %[t1], 16384\n\t"
        "buffer_load_dwordx4 a[244:247], v110, %[so], %[t0] offen\n\t"
        "s_cbranch_vccnz 1\n\t"
        "s_sleep 2\n\t"
        "s_add_u32 %[t0], %[t1], 32768\n\t"
        "buffer_load_dwordx4 a[248:251], v110, %[so], %[t0] offen\n\t"
        "s_cbranch_vccnz 1\n\t"
        "s_sleep 2\n\t"
        "s_add_u32 %[t0], %[t1], 49152\n\t"
        "buffer_load_dwordx4 a[252:255], v110, %[so], %[t0] offen\n\t"
        "s_cbranch_vccnz 1\n\t"
        "s_sleep 2\n\t"
        "s_add_u32 %[t0], %[t1], 65536\n\t"
        "buffer_load_dwordx4 v[128:131], v110, %[so], %[t0] offen\n\t"
        "s_cbranch_vccnz 1\n\t"
        "s_sleep 2\n\t"
        "s_add_u32 %[t0], %[t1], 81920\n\t"
        "buffer_load_dwordx4 v[132:135], v110, %[so], %[t0] offen\n\t"
        "s_cbranch_vccnz 1\n\t"
        "s_sleep 2\n\t"
        "s_add_u32 %[t0], %[t1], 98304\n\t"
        "buffer_load_dwordx4 v[40:43], v110, %[so], %[t0] offen\n\t"
        "s_cbranch_vccnz 1\n\t"
        "s_sleep 2\n\t"
        "s_add_u32 %[t0], %[t1], 114688\n\t"
        "buffer_load_dwordx4 v[44:47], v110, %[so], %[t0] offen\n\t"
        "s_cbranch_vccnz 1\n\t"
        "s_sleep 2\n\t"
        "s_waitcnt lgkmcnt(0)\n\t"
        "ds_write_b128 v116, v[56:59] offset:28672\n\t"
        "ds_write_b128 v116, v[60:63] offset:29696\n\t"
        "s_waitcnt vmcnt(24)\n\t"
        "ds_write_b128 v111, v[184:187] offset:0\n\t"
        "ds_write_b128 v111, v[188:191] offset:1024\n\t"
        "ds_write_b128 v111, v[192:195] offset:2048\n\t"
        "ds_write_b128 v111, v[196:199] offset:3072\n\t"
        "ds_write_b128 v111, v[200:203] offset:4096\n\t"
        "ds_write_b128 v111, v[204:207] offset:5120\n\t"
        "ds_write_b128 v111, v[32:35] offset:6144\n\t"
        "ds_write_b128 v111, v[36:39] offset:7168\n\t"
        "ds_read_b64_tr_b16 v[184:185], v112 offset:0\n\t"
        "ds_read_b64_tr_b16 v[186:187], v112 offset:2048\n\t"
        "ds_read_b64_tr_b16 v[196:197], v112 offset:4096\n\t"
        "ds_read_b64_tr_b16 v[198:199], v112 offset:6144\n\t"
        "s_waitcnt lgkmcnt(8)\n\t"
        "ds_read_b64_tr_b16 v[188:189], v113 offset:0\n\t"
        "ds_read_b64_tr_b16 v[190:191], v113 offset:2048\n\t"
        "ds_read_b64_tr_b16 v[200:201], v113 offset:4096\n\t"
        "ds_read_b64_tr_b16 v[202:203], v113 offset:6144\n\t"
        "s_waitcnt lgkmcnt(8)\n\t"
        "ds_read_b64_tr_b16 v[192:193], v114 offset:0\n\t"
        "ds_read_b64_tr_b16 v[194:195], v114 offset:2048\n\t"
        "ds_read_b64_tr_b16 v[204:205], v114 offset:4096\n\t"
        "ds_read_b64_tr_b16 v[206:207], v114 offset:6144\n\t"
        "s_waitcnt lgkmcnt(8)\n\t"
        "ds_read_b64_tr_b16 v[56:57], v115 offset:0\n\t"
        "ds_read_b64_tr_b16 v[58:59], v115 offset:2048\n\t"
        "ds_read_b64_tr_b16 v[60:61], v115 offset:4096\n\t"
        "ds_read_b64_tr_b16 v[62:63], v115 offset:6144\n\t"
        "s_add_u32 %[t1], %[ro], 4\n\t"
        "s_and_b32 %[t1], %[t1], 7\n\t"
        "s_lshl_b32 %[t1], %[t1], 18\n\t"
        "s_add_u32 %[t0], %[t1], 131072\n\t"
        "buffer_load_dwordx4 a[216:219], v110, %[so], %[t0] offen\n\t"
        "s_cbranch_vccnz 1\n\t"
        "s_sleep 2\n\t"
        "s_add_u32 %[t0], %[t1], 147456\n\t"
        "buffer_load_dwordx4 a[220:223], v110, %[so], %[t0] offen\n\t"
        "s_cbranch_vccnz 1\n\t"
        "s_sleep 2\n\t"
        "s_add_u32 %[t0], %[t1], 163840\n\t"
        "buffer_load_dwordx4 a[224:227], v110, %[so], %[t0] offen\n\t"
        "s_cbranch_vccnz 1\n\t"
        "s_sleep 2\n\t"
        "s_add_u32 %[t0], %[t1], 180224\n\t"
        "buffer_load_dwordx4 a[228:231], v110, %[so], %[t0] offen\n\t"
        "s_cbranch_vccnz 1\n\t"
        "s_sleep 2\n\t"
        "s_add_u32 %[t0], %[t1], 196608\n\t"
        "buffer_load_dwordx4 a[232:235], v110, %[so], %[t0] offen\n\t"
        "s_cbranch_vccnz 1\n\t"
        "s_sleep 2\n\t"
        "s_add_u32 %[t0], %[t1], 212992\n\t"
        "buffer_load_dwordx4 a[236:239], v110, %[so], %[t0] offen\n\t"
        "s_cbranch_vccnz 1\n\t"
        "s_sleep 2\n\t"
        "s_add_u32 %[t0], %[t1], 229376\n\t"
        "buffer_load_dwordx4 v[32:35], v110, %[so], %[t0] offen\n\t"
        "s_cbranch_vccnz 1\n\t"
        "s_sleep 2\n\t"
        "s_add_u32 %[t0], %[t1], 245760\n\t"
        "buffer_load_dwordx4 v[36:39], v110, %[so], %[t0] offen\n\t"
        "s_cbranch_vccnz 1\n\t"
        "s_sleep 2\n\t"
        "s_waitcnt lgkmcnt(0)\n\t"
        "ds_write_b128 v116, v[56:59] offset:26624\n\t"
        "ds_write_b128 v116, v[60:63] offset:27648\n\t"
        "s_waitcnt vmcnt(24)\n\t"
        "ds_write_b128 v111, v[160:163] offset:0\n\t"
        "ds_write_b128 v111, v[164:167] offset:1024\n\t"
        "ds_write_b128 v111, v[168:171] offset:2048\n\t"
        "ds_write_b128 v111, v[172:175] offset:3072\n\t"
        "ds_write_b128 v111, v[176:179] offset:4096\n\t"
        "ds_write_b128 v111, v[180:183] offset:5120\n\t"
        "ds_write_b128 v111, v[24:27] offset:6144\n\t"
        "ds_write_b128 v111, v[28:31] offset:7168\n\t"
        "ds_read_b64_tr_b16 v[160:161], v112 offset:0\n\t"
        "ds_read_b64_tr_b16 v[162:163], v112 offset:2048\n\t"
        "ds_read_b64_tr_b16 v[172:173], v112 offset:4096\n\t"
        "ds_read_b64_tr_b16 v[174:175], v112 offset:6144\n\t"
        "s_waitcnt lgkmcnt(8)\n\t"
        "ds_read_b64_tr_b16 v[164:165], v113 offset:0\n\t"
        "ds_read_b64_tr_b16 v[166:167], v113 offset:2048\n\t"
        "ds_read_b64_tr_b16 v[176:177], v113 offset:4096\n\t"
        "ds_read_b64_tr_b16 v[178:179], v113 offset:6144\n\t"
        "s_waitcnt lgkmcnt(8)\n\t"
        "ds_read_b64_tr_b16 v[168:169], v114 offset:0\n\t"
        "ds_read_b64_tr_b16 v[170:171], v114 offset:2048\n\t"
        "ds_read_b64_tr_b16 v[180:181], v114 offset:4096\n\t"
        "ds_read_b64_tr_b16 v[182:183], v114 offset:6144\n\t"
        "s_waitcnt lgkmcnt(8)\n\t"
        "ds_read_b64_tr_b16 v[56:57], v115 offset:0\n\t"
        "ds_read_b64_tr_b16 v[58:59], v115 offset:2048\n\t"
        "ds_read_b64_tr_b16 v[60:61], v115 offset:4096\n\t"
        "ds_read_b64_tr_b16 v[62:63], v115 offset:6144\n\t"
        "s_add_u32 %[t1], %[ro], 4\n\t"
        "s_and_b32 %[t1], %[t1], 7\n\t"
        "s_lshl_b32 %[t1], %[t1], 18\n\t"
        "s_add_u32 %[t0], %[t1], 0\n\t"
        "buffer_load_dwordx4 a[192:195], v110, %[so], %[t0] offen\n\t"
        "s_cbranch_vccnz 1\n\t"
        "s_sleep 2\n\t"
        "s_add_u32 %[t0], %[t1], 16384\n\t"
        "buffer_load_dwordx4 a[196:199], v110, %[so], %[t0] offen\n\t"
        "s_cbranch_vccnz 1\n\t"
        "s_sleep 2\n\t"
        "s_add_u32 %[t0], %[t1], 32768\n\t"
        "buffer_load_dwordx4 a[200:203], v110, %[so], %[t0] offen\n\t"
        "s_cbranch_vccnz 1\n\t"
        "s_sleep 2\n\t"
        "s_add_u32 %[t0], %[t1], 49152\n\t"
        "buffer_load_dwordx4 a[204:207], v110, %[so], %[t0] offen\n\t"
        "s_cbranch_vccnz 1\n\t"
        "s_sleep 2\n\t"
        "s_add_u32 %[t0], %[t1], 65536\n\t"
        "buffer_load_dwordx4 a[208:211], v110, %[so], %[t0] offen\n\t"
        "s_cbranch_vccnz 1\n\t"
        "s_sleep 2\n\t"
        "s_add_u32 %[t0], %[t1], 81920\n\t"
        "buffer_load_dwordx4 a[212:215], v110, %[so], %[t0] offen\n\t"
        "s_cbranch_vccnz 1\n\t"
        "s_sleep 2\n\t"
        "s_add_u32 %[t0], %[t1], 98304\n\t"
        "buffer_load_dwordx4 v[24:27], v110, %[so], %[t0] offen\n\t"
        "s_cbranch_vccnz 1\n\t"
        "s_sleep 2\n\t"
        "s_add_u32 %[t0], %[t1], 114688\n\t"
        "buffer_load_dwordx4 v[28:31], v110, %[so], %[t0] offen\n\t"
        "s_cbranch_vccnz 1\n\t"
        "s_sleep 2\n\t"
        "s_waitcnt lgkmcnt(0)\n\t"
        "ds_write_b128 v116, v[56:59] offset:24576\n\t"
        "ds_write_b128 v116, v[60:63] offset:25600\n\t"
        "s_waitcnt vmcnt(24)\n\t"
        "ds_write_b128 v111, v[136:139] offset:0\n\t"
        "ds_write_b128 v111, v[140:143] offset:1024\n\t"
        "ds_write_b128 v111, v[144:147] offset:2048\n\t"
        "ds_write_b128 v111, v[148:151] offset:3072\n\t"
        "ds_write_b128 v111, v[152:155] offset:4096\n\t"
        "ds_write_b128 v111, v[156:159] offset:5120\n\t"
        "ds_write_b128 v111, v[48:51] offset:6144\n\t"
        "ds_write_b128 v111, v[52:55] offset:7168\n\t"
        "ds_read_b64_tr_b16 v[136:137], v112 offset:0\n\t"
        "ds_read_b64_tr_b16 v[138:139], v112 offset:2048\n\t"
        "ds_read_b64_tr_b16 v[148:149], v112 offset:4096\n\t"
        "ds_read_b64_tr_b16 v[150:151], v112 offset:6144\n\t"
        "s_waitcnt lgkmcnt(8)\n\t"
        "ds_read_b64_tr_b16 v[140:141], v113 offset:0\n\t"
        "ds_read_b64_tr_b16 v[142:143], v113 offset:2048\n\t"
        "ds_read_b64_tr_b16 v[152:153], v113 offset:4096\n\t"
        "ds_read_b64_tr_b16 v[154:155], v113 offset:6144\n\t"
        "s_waitcnt lgkmcnt(8)\n\t"
        "ds_read_b64_tr_b16 v[144:145], v114 offset:0\n\t"
        "ds_read_b64_tr_b16 v[146:147], v114 offset:2048\n\t"
        "ds_read_b64_tr_b16 v[156:157], v114 offset:4096\n\t"
        "ds_read_b64_tr_b16 v[158:159], v114 offset:6144\n\t"
        "s_waitcnt lgkmcnt(8)\n\t"
        "ds_read_b64_tr_b16 v[56:57], v115 offset:0\n\t"
        "ds_read_b64_tr_b16 v[58:59], v115 offset:2048\n\t"
        "ds_read_b64_tr_b16 v[60:61], v115 offset:4096\n\t"
        "ds_read_b64_tr_b16 v[62:63], v115 offset:6144\n\t"
        "s_waitcnt lgkmcnt(0)\n\t"
        "ds_write_b128 v116, v[56:59] offset:22528\n\t"
        "ds_write_b128 v116, v[60:63] offset:23552\n\t"
        "s_waitcnt vmcnt(16)\n\t"
        "ds_write_b128 v111, a[240:243] offset:0\n\t"
        "ds_write_b128 v111, a[244:247] offset:1024\n\t"
        "ds_write_b128 v111, a[248:251] offset:2048\n\t"
        "ds_write_b128 v111, a[252:255] offset:3072\n\t"
        "ds_write_b128 v111, v[128:131] offset:4096\n\t"
        "ds_write_b128 v111, v[132:135] offset:5120\n\t"
        "ds_write_b128 v111, v[40:43] offset:6144\n\t"
        "ds_write_b128 v111, v[44:47] offset:7168\n\t"
        "ds_read_b64_tr_b16 a[240:241], v112 offset:0\n\t"
        "ds_read_b64_tr_b16 a[242:243], v112 offset:2048\n\t"
        "ds_read_b64_tr_b16 a[252:253], v112 offset:4096\n\t"
        "ds_read_b64_tr_b16 a[254:255], v112 offset:6144\n\t"
        "s_waitcnt lgkmcnt(8)\n\t"
        "ds_read_b64_tr_b16 a[244:245], v113 offset:0\n\t"
        "ds_read_b64_tr_b16 a[246:247], v113 offset:2048\n\t"
        "ds_read_b64_tr_b16 v[128:129], v113 offset:4096\n\t"
        "ds_read_b64_tr_b16 v[130:131], v113 offset:6144\n\t"
        "s_waitcnt lgkmcnt(8)\n\t"
        "ds_read_b64_tr_b16 a[248:249], v114 offset:0\n\t"
        "ds_read_b64_tr_b16 a[250:251], v114 offset:2048\n\t"
        "ds_read_b64_tr_b16 v[132:133], v114 offset:4096\n\t"
        "ds_read_b64_tr_b16 v[134:135], v114 offset:6144\n\t"
        "s_waitcnt lgkmcnt(8)\n\t"
        "ds_read_b64_tr_b16 v[56:57], v115 offset:0\n\t"
        "ds_read_b64_tr_b16 v[58:59], v115 offset:2048\n\t"
        "ds_read_b64_tr_b16 v[60:61], v115 offset:4096\n\t"
        "ds_read_b64_tr_b16 v[62:63], v115 offset:6144\n\t"
        "s_waitcnt lgkmcnt(0)\n\t"
        "ds_write_b128 v116, v[56:59] offset:20480\n\t"
        "ds_write_b128 v116, v[60:63] offset:21504\n\t"
        "s_waitcnt vmcnt(8)\n\t"
        "ds_write_b128 v111, a[216:219] offset:0\n\t"
        "ds_write_b128 v111, a[220:223] offset:1024\n\t"
        "ds_write_b128 v111, a[224:227] offset:2048\n\t"
        "ds_write_b128 v111, a[228:231] offset:3072\n\t"
        "ds_write_b128 v111, a[232:235] offset:4096\n\t"
        "ds_write_b128 v111, a[236:239] offset:5120\n\t"
        "ds_write_b128 v111, v[32:35] offset:6144\n\t"
        "ds_write_b128 v111, v[36:39] offset:7168\n\t"
        "ds_read_b64_tr_b16 a[216:217], v112 offset:0\n\t"
        "ds_read_b64_tr_b16 a[218:219], v112 offset:2048\n\t"
        "ds_read_b64_tr_b16 a[228:229], v112 offset:4096\n\t"
        "ds_read_b64_tr_b16 a[230:231], v112 offset:6144\n\t"
        "s_waitcnt lgkmcnt(8)\n\t"
        "ds_read_b64_tr_b16 a[220:221], v113 offset:0\n\t"
        "ds_read_b64_tr_b16 a[222:223], v113 offset:2048\n\t"
        "ds_read_b64_tr_b16 a[232:233], v113 offset:4096\n\t"
        "ds_read_b64_tr_b16 a[234:235], v113 offset:6144\n\t"
        "s_waitcnt lgkmcnt(8)\n\t"
        "ds_read_b64_tr_b16 a[224:225], v114 offset:0\n\t"
        "ds_read_b64_tr_b16 a[226:227], v114 offset:2048\n\t"
        "ds_read_b64_tr_b16 a[236:237], v114 offset:4096\n\t"
        "ds_read_b64_tr_b16 a[238:239], v114 offset:6144\n\t"
        "s_waitcnt lgkmcnt(8)\n\t"
        "ds_read_b64_tr_b16 v[56:57], v115 offset:0\n\t"
        "ds_read_b64_tr_b16 v[58:59], v115 offset:2048\n\t"
        "ds_read_b64_tr_b16 v[60:61], v115 offset:4096\n\t"
        "ds_read_b64_tr_b16 v[62:63], v115 offset:6144\n\t"
        "s_waitcnt lgkmcnt(0)\n\t"
        "ds_write_b128 v116, v[56:59] offset:18432\n\t"
        "ds_write_b128 v116, v[60:63] offset:19456\n\t"
        "s_waitcnt vmcnt(0)\n\t"
        "ds_write_b128 v111, a[192:195] offset:0\n\t"
        "ds_write_b128 v111, a[196:199] offset:1024\n\t"
        "ds_write_b128 v111, a[200:203] offset:2048\n\t"
        "ds_write_b128 v111, a[204:207] offset:3072\n\t"
        "ds_write_b128 v111, a[208:211] offset:4096\n\t"
        "ds_write_b128 v111, a[212:215] offset:5120\n\t"
        "ds_write_b128 v111, v[24:27] offset:6144\n\t"
        "ds_write_b128 v111, v[28:31] offset:7168\n\t"
        "ds_read_b64_tr_b16 a[192:193], v112 offset:0\n\t"
        "ds_read_b64_tr_b16 a[194:195], v112 offset:2048\n\t"
        "ds_read_b64_tr_b16 a[204:205], v112 offset:4096\n\t"
        "ds_read_b64_tr_b16 a[206:207], v112 offset:6144\n\t"
        "s_waitcnt lgkmcnt(8)\n\t"
        "ds_read_b64_tr_b16 a[196:197], v113 offset:0\n\t"
        "ds_read_b64_tr_b16 a[198:199], v113 offset:2048\n\t"
        "ds_read_b64_tr_b16 a[208:209], v113 offset:4096\n\t"
        "ds_read_b64_tr_b16 a[210:211], v113 offset:6144\n\t"
        "s_waitcnt lgkmcnt(8)\n\t"
        "ds_read_b64_tr_b16 a[200:201], v114 offset:0\n\t"
        "ds_read_b64_tr_b16 a[202:203], v114 offset:2048\n\t"
        "ds_read_b64_tr_b16 a[212:213], v114 offset:4096\n\t"
        "ds_read_b64_tr_b16 a[214:215], v114 offset:6144\n\t"
        "s_waitcnt lgkmcnt(8)\n\t"
        "ds_read_b64_tr_b16 v[56:57], v115 offset:0\n\t"
        "ds_read_b64_tr_b16 v[58:59], v115 offset:2048\n\t"
        "ds_read_b64_tr_b16 v[60:61], v115 offset:4096\n\t"
        "ds_read_b64_tr_b16 v[62:63], v115 offset:6144\n\t"
        "s_waitcnt lgkmcnt(0)\n\t"
        "ds_write_b128 v116, v[56:59] offset:16384\n\t"
        "ds_write_b128 v116, v[60:63] offset:17408\n\t"
        "ds_read_b128 v[88:91], v116 offset:0\n\t"
        "ds_read_b128 v[92:95], v116 offset:1024\n\t"
        "ds_read_b128 v[96:99], v116 offset:2048\n\t"
        "s_mov_b32 %[sp], 0\n\t"
        "LP_cv%=:\n\t"
        "buffer_load_dword v104, off, %[sw], %[cv] sc1\n\t"
        "s_waitcnt vmcnt(0) lgkmcnt(0)\n\t"
        "v_readfirstlane_b32 %[t0], v104\n\t"
        "s_cmp_ge_u32 %[t0], 8\n\t"
        "s_cbranch_scc1 LP_wait%=\n\t"
        "s_sleep 8\n\t"
        "s_add_u32 %[sp], %[sp], 1\n\t"
        "s_cmp_lt_u32 %[sp], 0x100000\n\t"
        "s_cbranch_scc1 LP_cv%=\n\t"
        "v_mov_b32_e32 v104, 256\n\t"
        "s_mov_b32 %[t0], 33024\n\t"
        "buffer_store_dword v104, off, %[sw], %[t0] sc1\n\t"
        "s_endpgm\n\t"
        "LP_wait%=:\n\t"
        "s_mov_b32 %[re], 0\n\t"
        "LP_again%=:\n\t"
        "s_mov_b32 %[sp], 0\n\t"
        "LP_poll%=:\n\t"
        "buffer_load_dword v103, v118, %[sw], 0 offen sc1\n\t"
        "s_lshr_b32 %[t1], %[tl], 1\n\t"
        "s_waitcnt vmcnt(0)\n\t"
        "v_min_u32_dpp v103, v103, v103 quad_perm:[1,0,3,2] row_mask:0xf bank_mask:0xf\n\t"
        "s_nop 1\n\t"
        "v_min_u32_dpp v103, v103, v103 quad_perm:[2,3,0,1] row_mask:0xf bank_mask:0xf\n\t"
        "s_nop 1\n\t"
        "v_min_u32_dpp v103, v103, v103 row_ror:4 row_mask:0xf bank_mask:0xf\n\t"
        "s_nop 0\n\t"
        "v_readfirstlane_b32 %[pg], v103\n\t"
        "s_cmp_gt_u32 %[pg], %[t1]\n\t"
        "s_cbranch_scc1 LP_have%=\n\t"
        "s_add_u32 %[t0], %[pg], %[re]\n\t"
        "s_cmp_eq_u32 %[t0], 63\n\t"
        "s_cbranch_scc0 LP_no%=\n\t"
        "s_sleep 16\n\t"
        "s_branch LP_have%=\n\t"
        "LP_no%=:\n\t"
        "s_min_u32 %[t1], %[t1], 62\n\t"
        "s_cmp_ge_u32 %[pg], %[t1]\n\t"
        "s_cbranch_scc1 LP_near%=\n\t"
        "s_sleep 24\n\t"
        "LP_near%=:\n\t"
        "s_sleep 2\n\t"
        "s_add_u32 %[sp], %[sp], 1\n\t"
        "s_cmp_lt_u32 %[sp], 0x100000\n\t"
        "s_cbranch_scc1 LP_poll%=\n\t"
        "v_mov_b32_e32 v104, 128\n\t"
        "s_mov_b32 %[t0], 33024\n\t"
        "buffer_store_dword v104, off, %[sw], %[t0] sc1\n\t"
        "s_endpgm\n\t"
        "LP_have%=:\n\t"
        "s_lshl_b32 %[nx], %[tl], 17\n\t"
        "s_add_u32 %[t1], %[t1], 4\n\t"
        "s_cmp_gt_u32 %[pg], %[t1]\n\t"
        "s_cbranch_scc1 LP_run%=\n\t"
        "s_add_u32 %[t0], %[ro], 0\n\t"
        "s_and_b32 %[t0], %[t0], 7\n\t"
        "s_lshl_b32 %[t0], %[t0], 12\n\t"
        "s_add_u32 %[t0], %[t0], %[nx]\n\t"
        "buffer_load_dwordx4 v[24:27], v117, %[sw], %[t0] offen offset:0 sc1\n\t"
        "buffer_load_dwordx4 v[28:31], v117, %[sw], %[t0] offen offset:1024 sc1\n\t"
        "buffer_load_dwordx4 v[32:35], v117, %[sw], %[t0] offen offset:2048 sc1\n\t"
        "buffer_load_dwordx4 v[36:39], v117, %[sw], %[t0] offen offset:3072 sc1\n\t"
        "s_add_u32 %[t0], %[ro], 1\n\t"
        "s_and_b32 %[t0], %[t0], 7\n\t"
        "s_lshl_b32 %[t0], %[t0], 12\n\t"
        "s_add_u32 %[t0], %[t0], %[nx]\n\t"
        "buffer_load_dwordx4 v[40:43], v117, %[sw], %[t0] offen offset:0 sc1\n\t"
        "buffer_load_dwordx4 v[44:47], v117, %[sw], %[t0] offen offset:1024 sc1\n\t"
        "s_waitcnt vmcnt(5) lgkmcnt(2)\n\t"
        "v_mfma_f32_16x16x32_bf16 v[20:23], v[88:91], v[24:27], 0\n\t"
        "ds_read_b128 v[88:91], v116 offset:3072\n\t"
        "v_mfma_f32_16x16x32_bf16 v[8:11], a[0:3], v[24:27], 0\n\t"
        "v_max3_u32 v125, v24, v25, v26\n\t"
        "buffer_load_dwordx4 v[48:51], v117, %[sw], %[t0] offen offset:2048 sc1\n\t"
        "v_mfma_f32_16x16x32_bf16 v[12:15], a[4:7], v[24:27], 0\n\t"
        "v_max_u32_e32 v125, v27, v125\n\t"
        "v_mfma_f32_16x16x32_bf16 v[16:19], a[8:11], v[24:27], 0\n\t"
        "buffer_load_dwordx4 v[52:55], v117, %[sw], %[t0] offen offset:3072 sc1\n\t"
        "s_waitcnt vmcnt(6) lgkmcnt(2)\n\t"
        "v_mfma_f32_16x16x32_bf16 v[20:23], v[92:95], v[28:31], v[20:23]\n\t"
        "ds_read_b128 v[92:95], v116 offset:4096\n\t"
        "v_mfma_f32_16x16x32_bf16 v[8:11], a[12:15], v[28:31], v[8:11]\n\t"
        "v_max3_u32 v125, v28, v29, v125\n\t"
        "s_add_u32 %[t0], %[ro], 2\n\t"
        "s_and_b32 %[t0], %[t0], 7\n\t"
        "s_lshl_b32 %[t0], %[t0], 12\n\t"
        "s_add_u32 %[t0], %[t0], %[nx]\n\t"
        "buffer_load_dwordx4 v[56:59], v117, %[sw], %[t0] offen offset:0 sc1\n\t"
        "v_mfma_f32_16x16x32_bf16 v[12:15], a[16:19], v[28:31], v[12:15]\n\t"
        "v_max3_u32 v125, v30, v31, v125\n\t"
        "v_mfma_f32_16x16x32_bf16 v[16:19], a[20:23], v[28:31], v[16:19]\n\t"
        "buffer_load_dwordx4 v[60:63], v117, %[sw], %[t0] offen offset:1024 sc1\n\t"
        "s_and_b32 %[sp], %[tl], 1\n\t"
        "s_lshl_b32 %[sp], %[sp], 14\n\t"
        "s_lshr_b32 %[t1], %[tl], 1\n\t"
        "s_lshl_b32 %[t1], %[t1], 6\n\t"
        "s_add_u32 %[t1], %[sp], %[t1]\n\t"
        "buffer_load_dword v100, v119, %[sw], %[t1] offen sc1\n\t"
        "s_add_u32 %[t1], %[t1], 32768\n\t"
        "buffer_load_dword v101, v119, %[sw], %[t1] offen sc1\n\t"
        "s_lshl_b32 %[t1], %[tl], 9\n\t"
        "buffer_load_dword v102, v120, %[sw], %[t1] offen sc1\n\t"
        "buffer_load_dword v103, v118, %[sw], 0 offen sc1\n\t"
        "s_waitcnt vmcnt(11) lgkmcnt(2)\n\t"
        "v_mfma_f32_16x16x32_bf16 v[20:23], v[96:99], v[32:35], v[20:23]\n\t"
        "ds_read_b128 v[96:99], v116 offset:5120\n\t"
        "v_mfma_f32_16x16x32_bf16 v[8:11], a[24:27], v[32:35], v[8:11]\n\t"
        "v_max3_u32 v125, v32, v33, v125\n\t"
        "buffer_load_dwordx4 v[64:67], v117, %[sw], %[t0] offen offset:2048 sc1\n\t"
        "v_mfma_f32_16x16x32_bf16 v[12:15], a[28:31], v[32:35], v[12:15]\n\t"
        "v_max3_u32 v125, v34, v35, v125\n\t"
        "v_mfma_f32_16x16x32_bf16 v[16:19], a[32:35], v[32:35], v[16:19]\n\t"
        "buffer_load_dwordx4 v[68:71], v117, %[sw], %[t0] offen offset:3072 sc1\n\t"
        "s_waitcnt vmcnt(12) lgkmcnt(2)\n\t"
        "v_mfma_f32_16x16x32_bf16 v[20:23], v[88:91], v[36:39], v[20:23]\n\t"
        "ds_read_b128 v[88:91], v116 offset:6144\n\t"
        "v_mfma_f32_16x16x32_bf16 v[8:11], a[36:39], v[36:39], v[8:11]\n\t"
        "v_max3_u32 v125, v36, v37, v125\n\t"
        "s_add_u32 %[t0], %[ro], 3\n\t"
        "s_and_b32 %[t0], %[t0], 7\n\t"
        "s_lshl_b32 %[t0], %[t0], 12\n\t"
        "s_add_u32 %[t0], %[t0], %[nx]\n\t"
        "buffer_load_dwordx4 v[72:75], v117, %[sw], %[t0] offen offset:0 sc1\n\t"
        "v_mfma_f32_16x16x32_bf16 v[12:15], a[40:43], v[36:39], v[12:15]\n\t"
        "v_max3_u32 v125, v38, v39, v125\n\t"
        "v_mfma_f32_16x16x32_bf16 v[16:19], a[44:47], v[36:39], v[16:19]\n\t"
        "buffer_load_dwordx4 v[76:79], v117, %[sw], %[t0] offen offset:1024 sc1\n\t"
        "s_waitcnt vmcnt(13) lgkmcnt(2)\n\t"
        "v_mfma_f32_16x16x32_bf16 v[20:23], v[92:95], v[40:43], v[20:23]\n\t"
        "ds_read_b128 v[92:95], v116 offset:7168\n\t"
        "v_mfma_f32_16x16x32_bf16 v[8:11], a[48:51], v[40:43], v[8:11]\n\t"
        "v_max3_u32 v125, v40, v41, v125\n\t"
        "buffer_load_dwordx4 v[80:83], v117, %[sw], %[t0] offen offset:2048 sc1\n\t"
        "v_mfma_f32_16x16x32_bf16 v[12:15], a[52:55], v[40:43], v[12:15]\n\t"
        "v_max3_u32 v125, v42, v43, v125\n\t"
        "v_mfma_f32_16x16x32_bf16 v[16:19], a[56:59], v[40:43], v[16:19]\n\t"
        "buffer_load_dwordx4 v[84:87], v117, %[sw], %[t0] offen offset:3072 sc1\n\t"
        "s_waitcnt vmcnt(14) lgkmcnt(2)\n\t"
        "v_mfma_f32_16x16x32_bf16 v[20:23], v[96:99], v[44:47], v[20:23]\n\t"
        "ds_read_b128 v[96:99], v116 offset:8192\n\t"
        "v_mfma_f32_16x16x32_bf16 v[8:11], a[60:63], v[44:47], v[8:11]\n\t"
        "v_max3_u32 v125, v44, v45, v125\n\t"
        "s_add_u32 %[t0], %[ro], 4\n\t"
        "s_and_b32 %[t0], %[t0], 7\n\t"
        "s_lshl_b32 %[t0], %[t0], 12\n\t"
        "s_add_u32 %[t0], %[t0], %[nx]\n\t"
        "buffer_load_dwordx4 v[24:27], v117, %[sw], %[t0] offen offset:0 sc1\n\t"
        "v_mfma_f32_16x16x32_bf16 v[12:15], a[64:67], v[44:47], v[12:15]\n\t"
        "v_max3_u32 v125, v46, v47, v125\n\t"
        "v_mfma_f32_16x16x32_bf16 v[16:19], a[68:71], v[44:47], v[16:19]\n\t"
        "buffer_load_dwordx4 v[28:31], v117, %[sw], %[t0] offen offset:1024 sc1\n\t"
        "s_waitcnt vmcnt(15) lgkmcnt(2)\n\t"
        "v_mfma_f32_16x16x32_bf16 v[20:23], v[88:91], v[48:51], v[20:23]\n\t"
        "ds_read_b128 v[88:91], v116 offset:9216\n\t"
        "v_mfma_f32_16x16x32_bf16 v[8:11], a[72:75], v[48:51], v[8:11]\n\t"
        "v_max3_u32 v125, v48, v49, v125\n\t"
        "v_mfma_f32_16x16x32_bf16 v[12:15], a[76:79], v[48:51], v[12:15]\n\t"
        "v_max3_u32 v125, v50, v51, v125\n\t"
        "v_mfma_f32_16x16x32_bf16 v[16:19], a[80:83], v[48:51], v[16:19]\n\t"
        "buffer_load_dwordx4 v[32:35], v117, %[sw], %[t0] offen offset:2048 sc1\n\t"
        "s_waitcnt vmcnt(15) lgkmcnt(2)\n\t"
        "v_mfma_f32_16x16x32_bf16 v[20:23], v[92:95], v[52:55], v[20:23]\n\t"
        "ds_read_b128 v[92:95], v116 offset:10240\n\t"
        "v_mfma_f32_16x16x32_bf16 v[8:11], a[84:87], v[52:55], v[8:11]\n\t"
        "v_max3_u32 v125, v52, v53, v125\n\t"
        "v_mfma_f32_16x16x32_bf16 v[12:15], a[88:91], v[52:55], v[12:15]\n\t"
        "v_max3_u32 v125, v54, v55, v125\n\t"
        "v_mfma_f32_16x16x32_bf16 v[16:19], a[92:95], v[52:55], v[16:19]\n\t"
        "buffer_load_dwordx4 v[36:39], v117, %[sw], %[t0] offen offset:3072 sc1\n\t"
        "s_waitcnt vmcnt(15) lgkmcnt(2)\n\t"
        "s_cmp_lg_u32 %[re], 0\n\t"
        "s_cbranch_scc1 LP_nb%=\n\t"
        "s_barrier\n\t"
        "LP_nb%=:\n\t"
        "v_mfma_f32_16x16x32_bf16 v[20:23], v[96:99], v[56:59], v[20:23]\n\t"
        "ds_read_b128 v[96:99], v116 offset:11264\n\t"
        "v_mfma_f32_16x16x32_bf16 v[8:11], a[96:99], v[56:59], v[8:11]\n\t"
        "v_max3_u32 v125, v56, v57, v125\n\t"
        "v_mfma_f32_16x16x32_bf16 v[12:15], a[100:103], v[56:59], v[12:15]\n\t"
        "v_max3_u32 v125, v58, v59, v125\n\t"
        "v_mfma_f32_16x16x32_bf16 v[16:19], a[104:107], v[56:59], v[16:19]\n\t"
        "s_add_u32 %[t0], %[ro], 5\n\t"
        "s_and_b32 %[t0], %[t0], 7\n\t"
        "s_lshl_b32 %[t0], %[t0], 12\n\t"
        "s_add_u32 %[t0], %[t0], %[nx]\n\t"
        "buffer_load_dwordx4 v[40:43], v117, %[sw], %[t0] offen offset:0 sc1\n\t"
        "s_waitcnt vmcnt(15) lgkmcnt(2)\n\t"
        "v_mfma_f32_16x16x32_bf16 v[20:23], v[88:91], v[60:63], v[20:23]\n\t"
        "ds_read_b128 v[88:91], v116 offset:12288\n\t"
        "v_mfma_f32_16x16x32_bf16 v[8:11], a[108:111], v[60:63], v[8:11]\n\t"
        "v_max3_u32 v125, v60, v61, v125\n\t"
        "v_mfma_f32_16x16x32_bf16 v[12:15], a[112:115], v[60:63], v[12:15]\n\t"
        "v_max3_u32 v125, v62, v63, v125\n\t"
        "v_mfma_f32_16x16x32_bf16 v[16:19], a[116:119], v[60:63], v[16:19]\n\t"
        "buffer_load_dwordx4 v[44:47], v117, %[sw], %[t0] offen offset:1024 sc1\n\t"
        "s_waitcnt vmcnt(11) lgkmcnt(2)\n\t"
        "v_mfma_f32_16x16x32_bf16 v[20:23], v[92:95], v[64:67], v[20:23]\n\t"
        "ds_read_b128 v[92:95], v116 offset:13312\n\t"
        "v_mfma_f32_16x16x32_bf16 v[8:11], a[120:123], v[64:67], v[8:11]\n\t"
        "v_max3_u32 v125, v64, v65, v125\n\t"
        "v_mfma_f32_16x16x32_bf16 v[12:15], a[124:127], v[64:67], v[12:15]\n\t"
        "v_max3_u32 v125, v66, v67, v125\n\t"
        "v_mfma_f32_16x16x32_bf16 v[16:19], a[128:131], v[64:67], v[16:19]\n\t"
        "buffer_load_dwordx4 v[48:51], v117, %[sw], %[t0] offen offset:2048 sc1\n\t"
        "s_waitcnt vmcnt(11) lgkmcnt(2)\n\t"
        "v_mfma_f32_16x16x32_bf16 v[20:23], v[96:99], v[68:71], v[20:23]\n\t"
        "ds_read_b128 v[96:99], v116 offset:14336\n\t"
        "v_mfma_f32_16x16x32_bf16 v[8:11], a[132:135], v[68:71], v[8:11]\n\t"
        "v_max3_u32 v125, v68, v69, v125\n\t"
        "v_mfma_f32_16x16x32_bf16 v[12:15], a[136:139], v[68:71], v[12:15]\n\t"
        "v_max3_u32 v125, v70, v71, v125\n\t"
        "v_mfma_f32_16x16x32_bf16 v[16:19], a[140:143], v[68:71], v[16:19]\n\t"
        "buffer_load_dwordx4 v[52:55], v117, %[sw], %[t0] offen offset:3072 sc1\n\t"
        "s_waitcnt vmcnt(11) lgkmcnt(2)\n\t"
        "v_mfma_f32_16x16x32_bf16 v[20:23], v[88:91], v[72:75], v[20:23]\n\t"
        "ds_read_b128 v[88:91], v116 offset:15360\n\t"
        "v_mfma_f32_16x16x32_bf16 v[8:11], a[144:147], v[72:75], v[8:11]\n\t"
        "v_max3_u32 v125, v72, v73, v125\n\t"
        "v_mfma_f32_16x16x32_bf16 v[12:15], a[148:151], v[72:75], v[12:15]\n\t"
        "v_max3_u32 v125, v74, v75, v125\n\t"
        "v_mfma_f32_16x16x32_bf16 v[16:19], a[152:155], v[72:75], v[16:19]\n\t"
        "s_add_u32 %[t0], %[ro], 6\n\t"
        "s_and_b32 %[t0], %[t0], 7\n\t"
        "s_lshl_b32 %[t0], %[t0], 12\n\t"
        "s_add_u32 %[t0], %[t0], %[nx]\n\t"
        "buffer_load_dwordx4 v[56:59], v117, %[sw], %[t0] offen offset:0 sc1\n\t"
        "s_waitcnt vmcnt(11) lgkmcnt(2)\n\t"
        "v_mfma_f32_16x16x32_bf16 v[20:23], v[92:95], v[76:79], v[20:23]\n\t"
        "ds_read_b128 v[92:95], v116 offset:16384\n\t"
        "v_mfma_f32_16x16x32_bf16 v[8:11], a[156:159], v[76:79], v[8:11]\n\t"
        "v_max3_u32 v125, v76, v77, v125\n\t"
        "v_mfma_f32_16x16x32_bf16 v[12:15], a[160:163], v[76:79], v[12:15]\n\t"
        "v_max3_u32 v125, v78, v79, v125\n\t"
        "v_mfma_f32_16x16x32_bf16 v[16:19], a[164:167], v[76:79], v[16:19]\n\t"
        "buffer_load_dwordx4 v[60:63], v117, %[sw], %[t0] offen offset:1024 sc1\n\t"
        "s_waitcnt vmcnt(11) lgkmcnt(2)\n\t"
        "v_mfma_f32_16x16x32_bf16 v[20:23], v[96:99], v[80:83], v[20:23]\n\t"
        "ds_read_b128 v[96:99], v116 offset:17408\n\t"
        "v_mfma_f32_16x16x32_bf16 v[8:11], a[168:171], v[80:83], v[8:11]\n\t"
        "v_max3_u32 v125, v80, v81, v125\n\t"
        "v_mfma_f32_16x16x32_bf16 v[12:15], a[172:175], v[80:83], v[12:15]\n\t"
        "v_max3_u32 v125, v82, v83, v125\n\t"
        "v_mfma_f32_16x16x32_bf16 v[16:19], a[176:179], v[80:83], v[16:19]\n\t"
        "buffer_load_dwordx4 v[64:67], v117, %[sw], %[t0] offen offset:2048 sc1\n\t"
        "s_waitcnt vmcnt(11) lgkmcnt(2)\n\t"
        "v_mfma_f32_16x16x32_bf16 v[20:23], v[88:91], v[84:87], v[20:23]\n\t"
        "ds_read_b128 v[88:91], v116 offset:18432\n\t"
        "v_mfma_f32_16x16x32_bf16 v[8:11], a[180:183], v[84:87], v[8:11]\n\t"
        "v_max3_u32 v125, v84, v85, v125\n\t"
        "v_mfma_f32_16x16x32_bf16 v[12:15], a[184:187], v[84:87], v[12:15]\n\t"
        "v_max3_u32 v125, v86, v87, v125\n\t"
        "v_mfma_f32_16x16x32_bf16 v[16:19], a[188:191], v[84:87], v[16:19]\n\t"
        "buffer_load_dwordx4 v[68:71], v117, %[sw], %[t0] offen offset:3072 sc1\n\t"
        "s_waitcnt vmcnt(11) lgkmcnt(2)\n\t"
        "v_mfma_f32_16x16x32_bf16 v[20:23], v[92:95], v[24:27], v[20:23]\n\t"
        "ds_read_b128 v[92:95], v116 offset:19456\n\t"
        "v_mfma_f32_16x16x32_bf16 v[8:11], a[192:195], v[24:27], v[8:11]\n\t"
        "v_max3_u32 v125, v24, v25, v125\n\t"
        "v_mfma_f32_16x16x32_bf16 v[12:15], a[196:199], v[24:27], v[12:15]\n\t"
        "v_max3_u32 v125, v26, v27, v125\n\t"
        "v_mfma_f32_16x16x32_bf16 v[16:19], a[200:203], v[24:27], v[16:19]\n\t"
        "s_add_u32 %[t0], %[ro], 7\n\t"
        "s_and_b32 %[t0], %[t0], 7\n\t"
        "s_lshl_b32 %[t0], %[t0], 12\n\t"
        "s_add_u32 %[t0], %[t0], %[nx]\n\t"
        "buffer_load_dwordx4 v[72:75], v117, %[sw], %[t0] offen offset:0 sc1\n\t"
        "s_waitcnt vmcnt(11) lgkmcnt(2)\n\t"
        "v_mfma_f32_16x16x32_bf16 v[20:23], v[96:99], v[28:31], v[20:23]\n\t"
        "ds_read_b128 v[96:99], v116 offset:20480\n\t"
        "v_mfma_f32_16x16x32_bf16 v[8:11], a[204:207], v[28:31], v[8:11]\n\t"
        "v_max3_u32 v125, v28, v29, v125\n\t"
        "v_mfma_f32_16x16x32_bf16 v[12:15], a[208:211], v[28:31], v[12:15]\n\t"
        "v_max3_u32 v125, v30, v31, v125\n\t"
        "v_sub_f32_e32 v104, 1.0, v102\n\t"
        "v_mfma_f32_16x16x32_bf16 v[16:19], a[212:215], v[28:31], v[16:19]\n\t"
        "buffer_load_dwordx4 v[76:79], v117, %[sw], %[t0] offen offset:1024 sc1\n\t"
        "s_waitcnt vmcnt(11) lgkmcnt(2)\n\t"
        "v_mfma_f32_16x16x32_bf16 v[20:23], v[88:91], v[32:35], v[20:23]\n\t"
        "ds_read_b128 v[88:91], v116 offset:21504\n\t"
        "v_mfma_f32_16x16x32_bf16 v[8:11], a[216:219], v[32:35], v[8:11]\n\t"
        "v_max3_u32 v125, v32, v33, v125\n\t"
        "v_mfma_f32_16x16x32_bf16 v[12:15], a[220:223], v[32:35], v[12:15]\n\t"
        "v_max3_u32 v125, v34, v35, v125\n\t"
        "v_cvt_pk_bf16_f32 v104, 0, v104\n\t"
        "v_mfma_f32_16x16x32_bf16 v[16:19], a[224:227], v[32:35], v[16:19]\n\t"
        "buffer_load_dwordx4 v[80:83], v117, %[sw], %[t0] offen offset:2048 sc1\n\t"
        "s_waitcnt vmcnt(11) lgkmcnt(2)\n\t"
        "v_mfma_f32_16x16x32_bf16 v[20:23], v[92:95], v[36:39], v[20:23]\n\t"
        "ds_read_b128 v[92:95], v116 offset:22528\n\t"
        "v_mfma_f32_16x16x32_bf16 v[8:11], a[228:231], v[36:39], v[8:11]\n\t"
        "v_max3_u32 v125, v36, v37, v125\n\t"
        "v_mfma_f32_16x16x32_bf16 v[12:15], a[232:235], v[36:39], v[12:15]\n\t"
        "v_max3_u32 v125, v38, v39, v125\n\t"
        "v_lshlrev_b32_e32 v106, 16, v100\n\t"
        "v_mfma_f32_16x16x32_bf16 v[16:19], a[236:239], v[36:39], v[16:19]\n\t"
        "buffer_load_dwordx4 v[84:87], v117, %[sw], %[t0] offen offset:3072 sc1\n\t"
        "s_waitcnt vmcnt(11) lgkmcnt(2)\n\t"
        "v_mfma_f32_16x16x32_bf16 v[20:23], v[96:99], v[40:43], v[20:23]\n\t"
        "ds_read_b128 v[96:99], v116 offset:23552\n\t"
        "v_mfma_f32_16x16x32_bf16 v[8:11], a[240:243], v[40:43], v[8:11]\n\t"
        "v_max3_u32 v125, v40, v41, v125\n\t"
        "v_mfma_f32_16x16x32_bf16 v[12:15], a[244:247], v[40:43], v[12:15]\n\t"
        "v_max3_u32 v125, v42, v43, v125\n\t"
        "v_mul_f32_e32 v106, v102, v106\n\t"
        "v_mfma_f32_16x16x32_bf16 v[16:19], a[248:251], v[40:43], v[16:19]\n\t"
        "s_waitcnt vmcnt(10) lgkmcnt(2)\n\t"
        "v_mfma_f32_16x16x32_bf16 v[20:23], v[88:91], v[44:47], v[20:23]\n\t"
        "ds_read_b128 v[88:91], v116 offset:24576\n\t"
        "v_mfma_f32_16x16x32_bf16 v[8:11], a[252:255], v[44:47], v[8:11]\n\t"
        "v_max3_u32 v125, v44, v45, v125\n\t"
        "v_mfma_f32_16x16x32_bf16 v[12:15], v[128:131], v[44:47], v[12:15]\n\t"
        "v_max3_u32 v125, v46, v47, v125\n\t"
        "v_cvt_pk_bf16_f32 v106, 0, v106\n\t"
        "v_mfma_f32_16x16x32_bf16 v[16:19], v[132:135], v[44:47], v[16:19]\n\t"
        "s_waitcnt vmcnt(9) lgkmcnt(2)\n\t"
        "v_mfma_f32_16x16x32_bf16 v[20:23], v[92:95], v[48:51], v[20:23]\n\t"
        "ds_read_b128 v[92:95], v116 offset:25600\n\t"
        "v_mfma_f32_16x16x32_bf16 v[8:11], v[136:139], v[48:51], v[8:11]\n\t"
        "v_max3_u32 v125, v48, v49, v125\n\t"
        "v_mfma_f32_16x16x32_bf16 v[12:15], v[140:143], v[48:51], v[12:15]\n\t"
        "v_max3_u32 v125, v50, v51, v125\n\t"
        "v_and_b32_e32 v107, 0xffff0000, v100\n\t"
        "v_mfma_f32_16x16x32_bf16 v[16:19], v[144:147], v[48:51], v[16:19]\n\t"
        "s_waitcnt vmcnt(8) lgkmcnt(2)\n\t"
        "v_mfma_f32_16x16x32_bf16 v[20:23], v[96:99], v[52:55], v[20:23]\n\t"
        "ds_read_b128 v[96:99], v116 offset:26624\n\t"
        "v_mfma_f32_16x16x32_bf16 v[8:11], v[148:151], v[52:55], v[8:11]\n\t"
        "v_max3_u32 v125, v52, v53, v125\n\t"
        "v_mfma_f32_16x16x32_bf16 v[12:15], v[152:155], v[52:55], v[12:15]\n\t"
        "v_max3_u32 v125, v54, v55, v125\n\t"
        "v_mul_f32_e32 v107, v102, v107\n\t"
        "v_mfma_f32_16x16x32_bf16 v[16:19], v[156:159], v[52:55], v[16:19]\n\t"
        "s_waitcnt vmcnt(7) lgkmcnt(2)\n\t"
        "v_mfma_f32_16x16x32_bf16 v[20:23], v[88:91], v[56:59], v[20:23]\n\t"
        "ds_read_b128 v[88:91], v116 offset:27648\n\t"
        "v_mfma_f32_16x16x32_bf16 v[8:11], v[160:163], v[56:59], v[8:11]\n\t"
        "v_max3_u32 v125, v56, v57, v125\n\t"
        "v_mfma_f32_16x16x32_bf16 v[12:15], v[164:167], v[56:59], v[12:15]\n\t"
        "v_max3_u32 v125, v58, v59, v125\n\t"
        "v_cvt_pk_bf16_f32 v107, 0, v107\n\t"
        "v_mfma_f32_16x16x32_bf16 v[16:19], v[168:171], v[56:59], v[16:19]\n\t"
        "s_waitcnt vmcnt(6) lgkmcnt(2)\n\t"
        "v_mfma_f32_16x16x32_bf16 v[20:23], v[92:95], v[60:63], v[20:23]\n\t"
        "ds_read_b128 v[92:95], v116 offset:28672\n\t"
        "v_mfma_f32_16x16x32_bf16 v[8:11], v[172:175], v[60:63], v[8:11]\n\t"
        "v_max3_u32 v125, v60, v61, v125\n\t"
        "v_mfma_f32_16x16x32_bf16 v[12:15], v[176:179], v[60:63], v[12:15]\n\t"
        "v_max3_u32 v125, v62, v63, v125\n\t"
        "v_lshlrev_b32_e32 v108, 16, v101\n\t"
        "v_mfma_f32_16x16x32_bf16 v[16:19], v[180:183], v[60:63], v[16:19]\n\t"
        "s_waitcnt vmcnt(5) lgkmcnt(2)\n\t"
        "v_mfma_f32_16x16x32_bf16 v[20:23], v[96:99], v[64:67], v[20:23]\n\t"
        "ds_read_b128 v[96:99], v116 offset:29696\n\t"
        "v_mfma_f32_16x16x32_bf16 v[8:11], v[184:187], v[64:67], v[8:11]\n\t"
        "v_max3_u32 v125, v64, v65, v125\n\t"
        "v_mfma_f32_16x16x32_bf16 v[12:15], v[188:191], v[64:67], v[12:15]\n\t"
        "v_max3_u32 v125, v66, v67, v125\n\t"
        "v_mul_f32_e32 v108, v102, v108\n\t"
        "v_mfma_f32_16x16x32_bf16 v[16:19], v[192:195], v[64:67], v[16:19]\n\t"
        "s_waitcnt vmcnt(4) lgkmcnt(2)\n\t"
        "v_mfma_f32_16x16x32_bf16 v[20:23], v[88:91], v[68:71], v[20:23]\n\t"
        "ds_read_b128 v[88:91], v116 offset:30720\n\t"
        "v_mfma_f32_16x16x32_bf16 v[8:11], v[196:199], v[68:71], v[8:11]\n\t"
        "v_max3_u32 v125, v68, v69, v125\n\t"
        "v_mfma_f32_16x16x32_bf16 v[12:15], v[200:203], v[68:71], v[12:15]\n\t"
        "v_max3_u32 v125, v70, v71, v125\n\t"
        "v_cvt_pk_bf16_f32 v108, 0, v108\n\t"
        "v_mfma_f32_16x16x32_bf16 v[16:19], v[204:207], v[68:71], v[16:19]\n\t"
        "s_waitcnt vmcnt(3) lgkmcnt(2)\n\t"
        "v_mfma_f32_16x16x32_bf16 v[20:23], v[92:95], v[72:75], v[20:23]\n\t"
        "ds_read_b128 v[92:95], v116 offset:31744\n\t"
        "v_mfma_f32_16x16x32_bf16 v[8:11], v[208:211], v[72:75], v[8:11]\n\t"
        "v_max3_u32 v125, v72, v73, v125\n\t"
        "v_mfma_f32_16x16x32_bf16 v[12:15], v[212:215], v[72:75], v[12:15]\n\t"
        "v_max3_u32 v125, v74, v75, v125\n\t"
        "v_and_b32_e32 v109, 0xffff0000, v101\n\t"
        "v_mfma_f32_16x16x32_bf16 v[16:19], v[216:219], v[72:75], v[16:19]\n\t"
        "s_waitcnt vmcnt(2) lgkmcnt(2)\n\t"
        "v_mfma_f32_16x16x32_bf16 v[20:23], v[96:99], v[76:79], v[20:23]\n\t"
        "v_mfma_f32_16x16x32_bf16 v[8:11], v[220:223], v[76:79], v[8:11]\n\t"
        "v_max3_u32 v125, v76, v77, v125\n\t"
        "v_mfma_f32_16x16x32_bf16 v[12:15], v[224:227], v[76:79], v[12:15]\n\t"
        "v_max3_u32 v125, v78, v79, v125\n\t"
        "v_mul_f32_e32 v109, v102, v109\n\t"
        "v_mfma_f32_16x16x32_bf16 v[16:19], v[228:231], v[76:79], v[16:19]\n\t"
        "s_waitcnt vmcnt(1) lgkmcnt(1)\n\t"
        "v_mfma_f32_16x16x32_bf16 v[20:23], v[88:91], v[80:83], v[20:23]\n\t"
        "ds_read_b128 v[88:91], v116 offset:0\n\t"
        "v_mfma_f32_16x16x32_bf16 v[8:11], v[232:235], v[80:83], v[8:11]\n\t"
        "v_max3_u32 v125, v80, v81, v125\n\t"
        "v_mfma_f32_16x16x32_bf16 v[12:15], v[236:239], v[80:83], v[12:15]\n\t"
        "v_max3_u32 v125, v82, v83, v125\n\t"
        "v_cvt_pk_bf16_f32 v109, 0, v109\n\t"
        "v_mfma_f32_16x16x32_bf16 v[16:19], v[240:243], v[80:83], v[16:19]\n\t"
        "s_waitcnt vmcnt(0) lgkmcnt(1)\n\t"
        "v_mfma_f32_16x16x32_bf16 v[20:23], v[92:95], v[84:87], v[20:23]\n\t"
        "ds_read_b128 v[92:95], v116 offset:1024\n\t"
        "v_mfma_f32_16x16x32_bf16 v[8:11], v[244:247], v[84:87], v[8:11]\n\t"
        "ds_read_b128 v[96:99], v116 offset:2048\n\t"
        "v_max3_u32 v125, v84, v85, v125\n\t"
        "v_mfma_f32_16x16x32_bf16 v[12:15], v[248:251], v[84:87], v[12:15]\n\t"
        "v_max3_u32 v125, v86, v87, v125\n\t"
        "v_mfma_f32_16x16x32_bf16 v[16:19], v[252:255], v[84:87], v[16:19]\n\t"
        "v_cmp_eq_u32_e32 vcc, -1, v125\n\t"
        "s_mov_b32 %[nx], 0xf0000000\n\t"
        "s_mov_b32 %[re], 64\n\t"
        "s_cbranch_vccz LP_epi%=\n\t"
        "s_waitcnt lgkmcnt(0)\n\t"
        "s_branch LP_again%=\n\t"
        "LP_run%=:\n\t"
        "s_add_u32 %[t0], %[ro], 0\n\t"
        "s_and_b32 %[t0], %[t0], 7\n\t"
        "s_lshl_b32 %[t0], %[t0], 12\n\t"
        "s_add_u32 %[t0], %[t0], %[nx]\n\t"
        "buffer_load_dwordx4 v[24:27], v117, %[sw], %[t0] offen offset:0 sc1\n\t"
        "buffer_load_dwordx4 v[28:31], v117, %[sw], %[t0] offen offset:1024 sc1\n\t"
        "buffer_load_dwordx4 v[32:35], v117, %[sw], %[t0] offen offset:2048 sc1\n\t"
        "buffer_load_dwordx4 v[36:39], v117, %[sw], %[t0] offen offset:3072 sc1\n\t"
        "s_add_u32 %[t0], %[ro], 1\n\t"
        "s_and_b32 %[t0], %[t0], 7\n\t"
        "s_lshl_b32 %[t0], %[t0], 12\n\t"
        "s_add_u32 %[t0], %[t0], %[nx]\n\t"
        "buffer_load_dwordx4 v[40:43], v117, %[sw], %[t0] offen offset:0 sc1\n\t"
        "buffer_load_dwordx4 v[44:47], v117, %[sw], %[t0] offen offset:1024 sc1\n\t"
        "buffer_load_dwordx4 v[48:51], v117, %[sw], %[t0] offen offset:2048 sc1\n\t"
        "buffer_load_dwordx4 v[52:55], v117, %[sw], %[t0] offen offset:3072 sc1\n\t"
        "s_add_u32 %[t0], %[ro], 2\n\t"
        "s_and_b32 %[t0], %[t0], 7\n\t"
        "s_lshl_b32 %[t0], %[t0], 12\n\t"
        "s_add_u32 %[t0], %[t0], %[nx]\n\t"
        "buffer_load_dwordx4 v[56:59], v117, %[sw], %[t0] offen offset:0 sc1\n\t"
        "buffer_load_dwordx4 v[60:63], v117, %[sw], %[t0] offen offset:1024 sc1\n\t"
        "buffer_load_dwordx4 v[64:67], v117, %[sw], %[t0] offen offset:2048 sc1\n\t"
        "buffer_load_dwordx4 v[68:71], v117, %[sw], %[t0] offen offset:3072 sc1\n\t"
        "s_add_u32 %[t0], %[ro], 3\n\t"
        "s_and_b32 %[t0], %[t0], 7\n\t"
        "s_lshl_b32 %[t0], %[t0], 12\n\t"
        "s_add_u32 %[t0], %[t0], %[nx]\n\t"
        "buffer_load_dwordx4 v[72:75], v117, %[sw], %[t0] offen offset:0 sc1\n\t"
        "buffer_load_dwordx4 v[76:79], v117, %[sw], %[t0] offen offset:1024 sc1\n\t"
        "buffer_load_dwordx4 v[80:83], v117, %[sw], %[t0] offen offset:2048 sc1\n\t"
        "buffer_load_dwordx4 v[84:87], v117, %[sw], %[t0] offen offset:3072 sc1\n\t"
        "buffer_load_dword v103, v118, %[sw], 0 offen sc1\n\t"
        "LP_tile%=:\n\t"
        "s_and_b32 %[sp], %[tl], 1\n\t"
        "s_lshl_b32 %[sp], %[sp], 14\n\t"
        "s_lshr_b32 %[t1], %[tl], 1\n\t"
        "s_lshl_b32 %[t1], %[t1], 6\n\t"
        "s_add_u32 %[t1], %[sp], %[t1]\n\t"
        "buffer_load_dword v100, v119, %[sw], %[t1] offen sc1\n\t"
        "s_add_u32 %[t1], %[t1], 32768\n\t"
        "buffer_load_dword v101, v119, %[sw], %[t1] offen sc1\n\t"
        "s_lshl_b32 %[t1], %[tl], 9\n\t"
        "buffer_load_dword v102, v120, %[sw], %[t1] offen sc1\n\t"
        "buffer_load_dword v103, v118, %[sw], 0 offen sc1\n\t"
        "s_mov_b32 %[cu], %[nx]\n\t"
        "s_waitcnt vmcnt(20) lgkmcnt(2)\n\t"
        "v_mfma_f32_16x16x32_bf16 v[20:23], v[88:91], v[24:27], 0\n\t"
        "ds_read_b128 v[88:91], v116 offset:3072\n\t"
        "v_mfma_f32_16x16x32_bf16 v[8:11], a[0:3], v[24:27], 0\n\t"
        "v_mfma_f32_16x16x32_bf16 v[12:15], a[4:7], v[24:27], 0\n\t"
        "v_mfma_f32_16x16x32_bf16 v[16:19], a[8:11], v[24:27], 0\n\t"
        "s_add_u32 %[t0], %[ro], 4\n\t"
        "s_and_b32 %[t0], %[t0], 7\n\t"
        "s_lshl_b32 %[t0], %[t0], 12\n\t"
        "s_add_u32 %[t0], %[t0], %[cu]\n\t"
        "buffer_load_dwordx4 v[24:27], v117, %[sw], %[t0] offen offset:0 sc1\n\t"
        "s_waitcnt vmcnt(20) lgkmcnt(2)\n\t"
        "v_mfma_f32_16x16x32_bf16 v[20:23], v[92:95], v[28:31], v[20:23]\n\t"
        "ds_read_b128 v[92:95], v116 offset:4096\n\t"
        "v_mfma_f32_16x16x32_bf16 v[8:11], a[12:15], v[28:31], v[8:11]\n\t"
        "v_mfma_f32_16x16x32_bf16 v[12:15], a[16:19], v[28:31], v[12:15]\n\t"
        "v_mfma_f32_16x16x32_bf16 v[16:19], a[20:23], v[28:31], v[16:19]\n\t"
        "buffer_load_dwordx4 v[28:31], v117, %[sw], %[t0] offen offset:1024 sc1\n\t"
        "s_waitcnt vmcnt(20) lgkmcnt(2)\n\t"
        "v_mfma_f32_16x16x32_bf16 v[20:23], v[96:99], v[32:35], v[20:23]\n\t"
        "ds_read_b128 v[96:99], v116 offset:5120\n\t"
        "v_mfma_f32_16x16x32_bf16 v[8:11], a[24:27], v[32:35], v[8:11]\n\t"
        "v_mfma_f32_16x16x32_bf16 v[12:15], a[28:31], v[32:35], v[12:15]\n\t"
        "v_mfma_f32_16x16x32_bf16 v[16:19], a[32:35], v[32:35], v[16:19]\n\t"
        "buffer_load_dwordx4 v[32:35], v117, %[sw], %[t0] offen offset:2048 sc1\n\t"
        "s_waitcnt vmcnt(20) lgkmcnt(2)\n\t"
        "v_mfma_f32_16x16x32_bf16 v[20:23], v[88:91], v[36:39], v[20:23]\n\t"
        "ds_read_b128 v[88:91], v116 offset:6144\n\t"
        "v_mfma_f32_16x16x32_bf16 v[8:11], a[36:39], v[36:39], v[8:11]\n\t"
        "v_mfma_f32_16x16x32_bf16 v[12:15], a[40:43], v[36:39], v[12:15]\n\t"
        "v_mfma_f32_16x16x32_bf16 v[16:19], a[44:47], v[36:39], v[16:19]\n\t"
        "buffer_load_dwordx4 v[36:39], v117, %[sw], %[t0] offen offset:3072 sc1\n\t"
        "s_waitcnt vmcnt(20) lgkmcnt(2)\n\t"
        "v_mfma_f32_16x16x32_bf16 v[20:23], v[92:95], v[40:43], v[20:23]\n\t"
        "ds_read_b128 v[92:95], v116 offset:7168\n\t"
        "v_mfma_f32_16x16x32_bf16 v[8:11], a[48:51], v[40:43], v[8:11]\n\t"
        "v_mfma_f32_16x16x32_bf16 v[12:15], a[52:55], v[40:43], v[12:15]\n\t"
        "v_mfma_f32_16x16x32_bf16 v[16:19], a[56:59], v[40:43], v[16:19]\n\t"
        "s_add_u32 %[t0], %[ro], 5\n\t"
        "s_and_b32 %[t0], %[t0], 7\n\t"
        "s_lshl_b32 %[t0], %[t0], 12\n\t"
        "s_add_u32 %[t0], %[t0], %[cu]\n\t"
        "buffer_load_dwordx4 v[40:43], v117, %[sw], %[t0] offen offset:0 sc1\n\t"
        "s_waitcnt vmcnt(20) lgkmcnt(2)\n\t"
        "v_mfma_f32_16x16x32_bf16 v[20:23], v[96:99], v[44:47], v[20:23]\n\t"
        "ds_read_b128 v[96:99], v116 offset:8192\n\t"
        "v_mfma_f32_16x16x32_bf16 v[8:11], a[60:63], v[44:47], v[8:11]\n\t"
        "v_mfma_f32_16x16x32_bf16 v[12:15], a[64:67], v[44:47], v[12:15]\n\t"
        "v_mfma_f32_16x16x32_bf16 v[16:19], a[68:71], v[44:47], v[16:19]\n\t"
        "buffer_load_dwordx4 v[44:47], v117, %[sw], %[t0] offen offset:1024 sc1\n\t"
        "s_waitcnt vmcnt(20) lgkmcnt(2)\n\t"
        "v_mfma_f32_16x16x32_bf16 v[20:23], v[88:91], v[48:51], v[20:23]\n\t"
        "ds_read_b128 v[88:91], v116 offset:9216\n\t"
        "v_mfma_f32_16x16x32_bf16 v[8:11], a[72:75], v[48:51], v[8:11]\n\t"
        "v_mfma_f32_16x16x32_bf16 v[12:15], a[76:79], v[48:51], v[12:15]\n\t"
        "v_mfma_f32_16x16x32_bf16 v[16:19], a[80:83], v[48:51], v[16:19]\n\t"
        "buffer_load_dwordx4 v[48:51], v117, %[sw], %[t0] offen offset:2048 sc1\n\t"
        "s_waitcnt vmcnt(20) lgkmcnt(2)\n\t"
        "v_mfma_f32_16x16x32_bf16 v[20:23], v[92:95], v[52:55], v[20:23]\n\t"
        "ds_read_b128 v[92:95], v116 offset:10240\n\t"
        "v_mfma_f32_16x16x32_bf16 v[8:11], a[84:87], v[52:55], v[8:11]\n\t"
        "v_mfma_f32_16x16x32_bf16 v[12:15], a[88:91], v[52:55], v[12:15]\n\t"
        "v_mfma_f32_16x16x32_bf16 v[16:19], a[92:95], v[52:55], v[16:19]\n\t"
        "buffer_load_dwordx4 v[52:55], v117, %[sw], %[t0] offen offset:3072 sc1\n\t"
        "s_waitcnt vmcnt(20) lgkmcnt(2)\n\t"
        "s_barrier\n\t"
        "v_mfma_f32_16x16x32_bf16 v[20:23], v[96:99], v[56:59], v[20:23]\n\t"
        "ds_read_b128 v[96:99], v116 offset:11264\n\t"
        "v_mfma_f32_16x16x32_bf16 v[8:11], a[96:99], v[56:59], v[8:11]\n\t"
        "v_mfma_f32_16x16x32_bf16 v[12:15], a[100:103], v[56:59], v[12:15]\n\t"
        "v_mfma_f32_16x16x32_bf16 v[16:19], a[104:107], v[56:59], v[16:19]\n\t"
        "s_add_u32 %[t0], %[ro], 6\n\t"
        "s_and_b32 %[t0], %[t0], 7\n\t"
        "s_lshl_b32 %[t0], %[t0], 12\n\t"
        "s_add_u32 %[t0], %[t0], %[cu]\n\t"
        "buffer_load_dwordx4 v[56:59], v117, %[sw], %[t0] offen offset:0 sc1\n\t"
        "s_waitcnt vmcnt(20) lgkmcnt(2)\n\t"
        "v_mfma_f32_16x16x32_bf16 v[20:23], v[88:91], v[60:63], v[20:23]\n\t"
        "ds_read_b128 v[88:91], v116 offset:12288\n\t"
        "v_mfma_f32_16x16x32_bf16 v[8:11], a[108:111], v[60:63], v[8:11]\n\t"
        "v_mfma_f32_16x16x32_bf16 v[12:15], a[112:115], v[60:63], v[12:15]\n\t"
        "v_mfma_f32_16x16x32_bf16 v[16:19], a[116:119], v[60:63], v[16:19]\n\t"
        "buffer_load_dwordx4 v[60:63], v117, %[sw], %[t0] offen offset:1024 sc1\n\t"
        "s_waitcnt vmcnt(20) lgkmcnt(2)\n\t"
        "v_mfma_f32_16x16x32_bf16 v[20:23], v[92:95], v[64:67], v[20:23]\n\t"
        "ds_read_b128 v[92:95], v116 offset:13312\n\t"
        "v_mfma_f32_16x16x32_bf16 v[8:11], a[120:123], v[64:67], v[8:11]\n\t"
        "v_mfma_f32_16x16x32_bf16 v[12:15], a[124:127], v[64:67], v[12:15]\n\t"
        "v_mfma_f32_16x16x32_bf16 v[16:19], a[128:131], v[64:67], v[16:19]\n\t"
        "buffer_load_dwordx4 v[64:67], v117, %[sw], %[t0] offen offset:2048 sc1\n\t"
        "s_waitcnt vmcnt(20) lgkmcnt(2)\n\t"
        "v_mfma_f32_16x16x32_bf16 v[20:23], v[96:99], v[68:71], v[20:23]\n\t"
        "ds_read_b128 v[96:99], v116 offset:14336\n\t"
        "v_mfma_f32_16x16x32_bf16 v[8:11], a[132:135], v[68:71], v[8:11]\n\t"
        "v_mfma_f32_16x16x32_bf16 v[12:15], a[136:139], v[68:71], v[12:15]\n\t"
        "v_mfma_f32_16x16x32_bf16 v[16:19], a[140:143], v[68:71], v[16:19]\n\t"
        "buffer_load_dwordx4 v[68:71], v117, %[sw], %[t0] offen offset:3072 sc1\n\t"
        "s_waitcnt vmcnt(20) lgkmcnt(2)\n\t"
        "v_mfma_f32_16x16x32_bf16 v[20:23], v[88:91], v[72:75], v[20:23]\n\t"
        "ds_read_b128 v[88:91], v116 offset:15360\n\t"
        "v_mfma_f32_16x16x32_bf16 v[8:11], a[144:147], v[72:75], v[8:11]\n\t"
        "v_mfma_f32_16x16x32_bf16 v[12:15], a[148:151], v[72:75], v[12:15]\n\t"
        "v_mfma_f32_16x16x32_bf16 v[16:19], a[152:155], v[72:75], v[16:19]\n\t"
        "s_add_u32 %[t0], %[ro], 7\n\t"
        "s_and_b32 %[t0], %[t0], 7\n\t"
        "s_lshl_b32 %[t0], %[t0], 12\n\t"
        "s_add_u32 %[t0], %[t0], %[cu]\n\t"
        "buffer_load_dwordx4 v[72:75], v117, %[sw], %[t0] offen offset:0 sc1\n\t"
        "s_waitcnt vmcnt(13) lgkmcnt(2)\n\t"
        "v_mfma_f32_16x16x32_bf16 v[20:23], v[92:95], v[76:79], v[20:23]\n\t"
        "ds_read_b128 v[92:95], v116 offset:16384\n\t"
        "v_min_u32_dpp v103, v103, v103 quad_perm:[1,0,3,2] row_mask:0xf bank_mask:0xf\n\t"
        "s_nop 0\n\t"
        "v_mfma_f32_16x16x32_bf16 v[8:11], a[156:159], v[76:79], v[8:11]\n\t"
        "v_min_u32_dpp v103, v103, v103 quad_perm:[2,3,0,1] row_mask:0xf bank_mask:0xf\n\t"
        "s_nop 0\n\t"
        "v_mfma_f32_16x16x32_bf16 v[12:15], a[160:163], v[76:79], v[12:15]\n\t"
        "v_min_u32_dpp v103, v103, v103 row_ror:4 row_mask:0xf bank_mask:0xf\n\t"
        "s_nop 0\n\t"
        "v_mfma_f32_16x16x32_bf16 v[16:19], a[164:167], v[76:79], v[16:19]\n\t"
        "buffer_load_dwordx4 v[76:79], v117, %[sw], %[t0] offen offset:1024 sc1\n\t"
        "s_waitcnt vmcnt(20) lgkmcnt(2)\n\t"
        "v_mfma_f32_16x16x32_bf16 v[20:23], v[96:99], v[80:83], v[20:23]\n\t"
        "ds_read_b128 v[96:99], v116 offset:17408\n\t"
        "v_readfirstlane_b32 %[pg], v103\n\t"
        "s_add_u32 %[t1], %[tl], 8\n\t"
        "s_lshl_b32 %[nx], %[t1], 17\n\t"
        "s_lshr_b32 %[t1], %[t1], 1\n\t"
        "v_mfma_f32_16x16x32_bf16 v[8:11], a[168:171], v[80:83], v[8:11]\n\t"
        "s_cmp_gt_u32 %[pg], %[t1]\n\t"
        "s_cselect_b32 %[nx], %[nx], 0xf0000000\n\t"
        "v_mfma_f32_16x16x32_bf16 v[12:15], a[172:175], v[80:83], v[12:15]\n\t"
        "v_mfma_f32_16x16x32_bf16 v[16:19], a[176:179], v[80:83], v[16:19]\n\t"
        "buffer_load_dwordx4 v[80:83], v117, %[sw], %[t0] offen offset:2048 sc1\n\t"
        "s_waitcnt vmcnt(20) lgkmcnt(2)\n\t"
        "v_mfma_f32_16x16x32_bf16 v[20:23], v[88:91], v[84:87], v[20:23]\n\t"
        "ds_read_b128 v[88:91], v116 offset:18432\n\t"
        "v_mfma_f32_16x16x32_bf16 v[8:11], a[180:183], v[84:87], v[8:11]\n\t"
        "v_mfma_f32_16x16x32_bf16 v[12:15], a[184:187], v[84:87], v[12:15]\n\t"
        "v_mfma_f32_16x16x32_bf16 v[16:19], a[188:191], v[84:87], v[16:19]\n\t"
        "buffer_load_dwordx4 v[84:87], v117, %[sw], %[t0] offen offset:3072 sc1\n\t"
        "s_waitcnt vmcnt(15) lgkmcnt(2)\n\t"
        "v_mfma_f32_16x16x32_bf16 v[20:23], v[92:95], v[24:27], v[20:23]\n\t"
        "ds_read_b128 v[92:95], v116 offset:19456\n\t"
        "v_mfma_f32_16x16x32_bf16 v[8:11], a[192:195], v[24:27], v[8:11]\n\t"
        "v_mfma_f32_16x16x32_bf16 v[12:15], a[196:199], v[24:27], v[12:15]\n\t"
        "v_mfma_f32_16x16x32_bf16 v[16:19], a[200:203], v[24:27], v[16:19]\n\t"
        "s_add_u32 %[t0], %[ro], 0\n\t"
        "s_and_b32 %[t0], %[t0], 7\n\t"
        "s_lshl_b32 %[t0], %[t0], 12\n\t"
        "s_add_u32 %[t0], %[t0], %[nx]\n\t"
        "buffer_load_dwordx4 v[24:27], v117, %[sw], %[t0] offen offset:0 sc1\n\t"
        "s_waitcnt vmcnt(15) lgkmcnt(2)\n\t"
        "v_mfma_f32_16x16x32_bf16 v[20:23], v[96:99], v[28:31], v[20:23]\n\t"
        "ds_read_b128 v[96:99], v116 offset:20480\n\t"
        "v_mfma_f32_16x16x32_bf16 v[8:11], a[204:207], v[28:31], v[8:11]\n\t"
        "v_mfma_f32_16x16x32_bf16 v[12:15], a[208:211], v[28:31], v[12:15]\n\t"
        "v_sub_f32_e32 v104, 1.0, v102\n\t"
        "v_mfma_f32_16x16x32_bf16 v[16:19], a[212:215], v[28:31], v[16:19]\n\t"
        "buffer_load_dwordx4 v[28:31], v117, %[sw], %[t0] offen offset:1024 sc1\n\t"
        "s_waitcnt vmcnt(15) lgkmcnt(2)\n\t"
        "v_mfma_f32_16x16x32_bf16 v[20:23], v[88:91], v[32:35], v[20:23]\n\t"
        "ds_read_b128 v[88:91], v116 offset:21504\n\t"
        "v_mfma_f32_16x16x32_bf16 v[8:11], a[216:219], v[32:35], v[8:11]\n\t"
        "v_mfma_f32_16x16x32_bf16 v[12:15], a[220:223], v[32:35], v[12:15]\n\t"
        "v_cvt_pk_bf16_f32 v104, 0, v104\n\t"
        "v_mfma_f32_16x16x32_bf16 v[16:19], a[224:227], v[32:35], v[16:19]\n\t"
        "buffer_load_dwordx4 v[32:35], v117, %[sw], %[t0] offen offset:2048 sc1\n\t"
        "s_waitcnt vmcnt(15) lgkmcnt(2)\n\t"
        "v_mfma_f32_16x16x32_bf16 v[20:23], v[92:95], v[36:39], v[20:23]\n\t"
        "ds_read_b128 v[92:95], v116 offset:22528\n\t"
        "v_mfma_f32_16x16x32_bf16 v[8:11], a[228:231], v[36:39], v[8:11]\n\t"
        "v_mfma_f32_16x16x32_bf16 v[12:15], a[232:235], v[36:39], v[12:15]\n\t"
        "v_lshlrev_b32_e32 v106, 16, v100\n\t"
        "v_mfma_f32_16x16x32_bf16 v[16:19], a[236:239], v[36:39], v[16:19]\n\t"
        "buffer_load_dwordx4 v[36:39], v117, %[sw], %[t0] offen offset:3072 sc1\n\t"
        "s_waitcnt vmcnt(15) lgkmcnt(2)\n\t"
        "v_mfma_f32_16x16x32_bf16 v[20:23], v[96:99], v[40:43], v[20:23]\n\t"
        "ds_read_b128 v[96:99], v116 offset:23552\n\t"
        "v_mfma_f32_16x16x32_bf16 v[8:11], a[240:243], v[40:43], v[8:11]\n\t"
        "v_mfma_f32_16x16x32_bf16 v[12:15], a[244:247], v[40:43], v[12:15]\n\t"
        "v_mul_f32_e32 v106, v102, v106\n\t"
        "v_mfma_f32_16x16x32_bf16 v[16:19], a[248:251], v[40:43], v[16:19]\n\t"
        "s_add_u32 %[t0], %[ro], 1\n\t"
        "s_and_b32 %[t0], %[t0], 7\n\t"
        "s_lshl_b32 %[t0], %[t0], 12\n\t"
        "s_add_u32 %[t0], %[t0], %[nx]\n\t"
        "buffer_load_dwordx4 v[40:43], v117, %[sw], %[t0] offen offset:0 sc1\n\t"
        "s_waitcnt vmcnt(15) lgkmcnt(2)\n\t"
        "v_mfma_f32_16x16x32_bf16 v[20:23], v[88:91], v[44:47], v[20:23]\n\t"
        "ds_read_b128 v[88:91], v116 offset:24576\n\t"
        "v_mfma_f32_16x16x32_bf16 v[8:11], a[252:255], v[44:47], v[8:11]\n\t"
        "v_mfma_f32_16x16x32_bf16 v[12:15], v[128:131], v[44:47], v[12:15]\n\t"
        "v_cvt_pk_bf16_f32 v106, 0, v106\n\t"
        "v_mfma_f32_16x16x32_bf16 v[16:19], v[132:135], v[44:47], v[16:19]\n\t"
        "buffer_load_dwordx4 v[44:47], v117, %[sw], %[t0] offen offset:1024 sc1\n\t"
        "s_waitcnt vmcnt(15) lgkmcnt(2)\n\t"
        "v_mfma_f32_16x16x32_bf16 v[20:23], v[92:95], v[48:51], v[20:23]\n\t"
        "ds_read_b128 v[92:95], v116 offset:25600\n\t"
        "v_mfma_f32_16x16x32_bf16 v[8:11], v[136:139], v[48:51], v[8:11]\n\t"
        "v_mfma_f32_16x16x32_bf16 v[12:15], v[140:143], v[48:51], v[12:15]\n\t"
        "v_and_b32_e32 v107, 0xffff0000, v100\n\t"
        "v_mfma_f32_16x16x32_bf16 v[16:19], v[144:147], v[48:51], v[16:19]\n\t"
        "buffer_load_dwordx4 v[48:51], v117, %[sw], %[t0] offen offset:2048 sc1\n\t"
        "s_waitcnt vmcnt(15) lgkmcnt(2)\n\t"
        "v_mfma_f32_16x16x32_bf16 v[20:23], v[96:99], v[52:55], v[20:23]\n\t"
        "ds_read_b128 v[96:99], v116 offset:26624\n\t"
        "v_mfma_f32_16x16x32_bf16 v[8:11], v[148:151], v[52:55], v[8:11]\n\t"
        "v_mfma_f32_16x16x32_bf16 v[12:15], v[152:155], v[52:55], v[12:15]\n\t"
        "v_mul_f32_e32 v107, v102, v107\n\t"
        "v_mfma_f32_16x16x32_bf16 v[16:19], v[156:159], v[52:55], v[16:19]\n\t"
        "buffer_load_dwordx4 v[52:55], v117, %[sw], %[t0] offen offset:3072 sc1\n\t"
        "s_waitcnt vmcnt(15) lgkmcnt(2)\n\t"
        "v_mfma_f32_16x16x32_bf16 v[20:23], v[88:91], v[56:59], v[20:23]\n\t"
        "ds_read_b128 v[88:91], v116 offset:27648\n\t"
        "v_mfma_f32_16x16x32_bf16 v[8:11], v[160:163], v[56:59], v[8:11]\n\t"
        "v_mfma_f32_16x16x32_bf16 v[12:15], v[164:167], v[56:59], v[12:15]\n\t"
        "v_cvt_pk_bf16_f32 v107, 0, v107\n\t"
        "v_mfma_f32_16x16x32_bf16 v[16:19], v[168:171], v[56:59], v[16:19]\n\t"
        "s_add_u32 %[t0], %[ro], 2\n\t"
        "s_and_b32 %[t0], %[t0], 7\n\t"
        "s_lshl_b32 %[t0], %[t0], 12\n\t"
        "s_add_u32 %[t0], %[t0], %[nx]\n\t"
        "buffer_load_dwordx4 v[56:59], v117, %[sw], %[t0] offen offset:0 sc1\n\t"
        "s_waitcnt vmcnt(15) lgkmcnt(2)\n\t"
        "v_mfma_f32_16x16x32_bf16 v[20:23], v[92:95], v[60:63], v[20:23]\n\t"
        "ds_read_b128 v[92:95], v116 offset:28672\n\t"
        "v_mfma_f32_16x16x32_bf16 v[8:11], v[172:175], v[60:63], v[8:11]\n\t"
        "v_mfma_f32_16x16x32_bf16 v[12:15], v[176:179], v[60:63], v[12:15]\n\t"
        "v_lshlrev_b32_e32 v108, 16, v101\n\t"
        "v_mfma_f32_16x16x32_bf16 v[16:19], v[180:183], v[60:63], v[16:19]\n\t"
        "buffer_load_dwordx4 v[60:63], v117, %[sw], %[t0] offen offset:1024 sc1\n\t"
        "s_waitcnt vmcnt(15) lgkmcnt(2)\n\t"
        "v_mfma_f32_16x16x32_bf16 v[20:23], v[96:99], v[64:67], v[20:23]\n\t"
        "ds_read_b128 v[96:99], v116 offset:29696\n\t"
        "v_mfma_f32_16x16x32_bf16 v[8:11], v[184:187], v[64:67], v[8:11]\n\t"
        "v_mfma_f32_16x16x32_bf16 v[12:15], v[188:191], v[64:67], v[12:15]\n\t"
        "v_mul_f32_e32 v108, v102, v108\n\t"
        "v_mfma_f32_16x16x32_bf16 v[16:19], v[192:195], v[64:67], v[16:19]\n\t"
        "buffer_load_dwordx4 v[64:67], v117, %[sw], %[t0] offen offset:2048 sc1\n\t"
        "s_waitcnt vmcnt(15) lgkmcnt(2)\n\t"
        "v_mfma_f32_16x16x32_bf16 v[20:23], v[88:91], v[68:71], v[20:23]\n\t"
        "ds_read_b128 v[88:91], v116 offset:30720\n\t"
        "v_mfma_f32_16x16x32_bf16 v[8:11], v[196:199], v[68:71], v[8:11]\n\t"
        "v_mfma_f32_16x16x32_bf16 v[12:15], v[200:203], v[68:71], v[12:15]\n\t"
        "v_cvt_pk_bf16_f32 v108, 0, v108\n\t"
        "v_mfma_f32_16x16x32_bf16 v[16:19], v[204:207], v[68:71], v[16:19]\n\t"
        "buffer_load_dwordx4 v[68:71], v117, %[sw], %[t0] offen offset:3072 sc1\n\t"
        "s_waitcnt vmcnt(15) lgkmcnt(2)\n\t"
        "v_mfma_f32_16x16x32_bf16 v[20:23], v[92:95], v[72:75], v[20:23]\n\t"
        "ds_read_b128 v[92:95], v116 offset:31744\n\t"
        "v_mfma_f32_16x16x32_bf16 v[8:11], v[208:211], v[72:75], v[8:11]\n\t"
        "v_mfma_f32_16x16x32_bf16 v[12:15], v[212:215], v[72:75], v[12:15]\n\t"
        "v_and_b32_e32 v109, 0xffff0000, v101\n\t"
        "v_mfma_f32_16x16x32_bf16 v[16:19], v[216:219], v[72:75], v[16:19]\n\t"
        "s_add_u32 %[t0], %[ro], 3\n\t"
        "s_and_b32 %[t0], %[t0], 7\n\t"
        "s_lshl_b32 %[t0], %[t0], 12\n\t"
        "s_add_u32 %[t0], %[t0], %[nx]\n\t"
        "buffer_load_dwordx4 v[72:75], v117, %[sw], %[t0] offen offset:0 sc1\n\t"
        "s_waitcnt vmcnt(15) lgkmcnt(2)\n\t"
        "v_mfma_f32_16x16x32_bf16 v[20:23], v[96:99], v[76:79], v[20:23]\n\t"
        "v_mfma_f32_16x16x32_bf16 v[8:11], v[220:223], v[76:79], v[8:11]\n\t"
        "v_mfma_f32_16x16x32_bf16 v[12:15], v[224:227], v[76:79], v[12:15]\n\t"
        "v_mul_f32_e32 v109, v102, v109\n\t"
        "v_mfma_f32_16x16x32_bf16 v[16:19], v[228:231], v[76:79], v[16:19]\n\t"
        "buffer_load_dwordx4 v[76:79], v117, %[sw], %[t0] offen offset:1024 sc1\n\t"
        "s_waitcnt vmcnt(15) lgkmcnt(1)\n\t"
        "v_mfma_f32_16x16x32_bf16 v[20:23], v[88:91], v[80:83], v[20:23]\n\t"
        "ds_read_b128 v[88:91], v116 offset:0\n\t"
        "v_mfma_f32_16x16x32_bf16 v[8:11], v[232:235], v[80:83], v[8:11]\n\t"
        "v_mfma_f32_16x16x32_bf16 v[12:15], v[236:239], v[80:83], v[12:15]\n\t"
        "v_cvt_pk_bf16_f32 v109, 0, v109\n\t"
        "v_mfma_f32_16x16x32_bf16 v[16:19], v[240:243], v[80:83], v[16:19]\n\t"
        "buffer_load_dwordx4 v[80:83], v117, %[sw], %[t0] offen offset:2048 sc1\n\t"
        "s_waitcnt vmcnt(15) lgkmcnt(1)\n\t"
        "v_mfma_f32_16x16x32_bf16 v[20:23], v[92:95], v[84:87], v[20:23]\n\t"
        "ds_read_b128 v[92:95], v116 offset:1024\n\t"
        "v_mfma_f32_16x16x32_bf16 v[8:11], v[244:247], v[84:87], v[8:11]\n\t"
        "ds_read_b128 v[96:99], v116 offset:2048\n\t"
        "v_mfma_f32_16x16x32_bf16 v[12:15], v[248:251], v[84:87], v[12:15]\n\t"
        "v_mfma_f32_16x16x32_bf16 v[16:19], v[252:255], v[84:87], v[16:19]\n\t"
        "buffer_load_dwordx4 v[84:87], v117, %[sw], %[t0] offen offset:3072 sc1\n\t"
        "LP_epi%=:\n\t"
        "s_lshl_b32 %[t0], %[tl], 15\n\t"
        "s_nop 0\n\t"
        "ds_write_b128 v122, v[20:23] offset:192\n\t"
        "s_nop 0\n\t"
        "ds_write_b128 v122, v[8:11] offset:0\n\t"
        "s_nop 0\n\t"
        "ds_write_b128 v122, v[12:15] offset:64\n\t"
        "ds_write_b128 v122, v[16:19] offset:128\n\t"
        "s_waitcnt lgkmcnt(0)\n\t"
        "s_barrier\n\t"
        "ds_read_b128 v[8:11], v123 offset:0\n\t"
        "ds_read_b128 v[12:15], v123 offset:4352\n\t"
        "ds_read_b128 v[16:19], v123 offset:8704\n\t"
        "ds_read_b128 v[20:23], v123 offset:13056\n\t"
        "s_waitcnt lgkmcnt(2)\n\t"
        "v_pk_add_f32 v[8:9], v[8:9], v[12:13]\n\t"
        "v_pk_add_f32 v[10:11], v[10:11], v[14:15]\n\t"
        "s_waitcnt lgkmcnt(0)\n\t"
        "v_pk_add_f32 v[16:17], v[16:17], v[20:21]\n\t"
        "v_pk_add_f32 v[18:19], v[18:19], v[22:23]\n\t"
        "v_pk_add_f32 v[8:9], v[8:9], v[16:17]\n\t"
        "v_pk_add_f32 v[10:11], v[10:11], v[18:19]\n\t"
        "v_cvt_pk_bf16_f32 v8, 0, v8\n\t"
        "v_cvt_pk_bf16_f32 v9, 0, v9\n\t"
        "v_cvt_pk_bf16_f32 v10, 0, v10\n\t"
        "v_cvt_pk_bf16_f32 v11, 0, v11\n\t"
        "v_mul_f32_e32 v8, v104, v8\n\t"
        "v_mul_f32_e32 v9, v104, v9\n\t"
        "v_mul_f32_e32 v10, v104, v10\n\t"
        "v_mul_f32_e32 v11, v104, v11\n\t"
        "v_cvt_pk_bf16_f32 v8, 0, v8\n\t"
        "v_cvt_pk_bf16_f32 v9, 0, v9\n\t"
        "v_cvt_pk_bf16_f32 v10, 0, v10\n\t"
        "v_cvt_pk_bf16_f32 v11, 0, v11\n\t"
        "v_add_f32_e32 v8, v106, v8\n\t"
        "v_add_f32_e32 v9, v107, v9\n\t"
        "v_add_f32_e32 v10, v108, v10\n\t"
        "v_add_f32_e32 v11, v109, v11\n\t"
        "v_cvt_pk_bf16_f32 v104, v8, v9\n\t"
        "v_cvt_pk_bf16_f32 v105, v10, v11\n\t"
        "buffer_store_dwordx2 v[104:105], v121, %[su], %[t0] offen sc1\n\t"
        "s_add_u32 %[tl], %[tl], 8\n\t"
        "s_cmp_ge_u32 %[tl], 128\n\t"
        "s_cbranch_scc1 LP_end%=\n\t"
        "s_cmp_lg_u32 %[nx], 0xf0000000\n\t"
        "s_cbranch_scc1 LP_tile%=\n\t"
        "s_branch LP_wait%=\n\t"
        "LP_end%=:\n\t"
        "s_waitcnt vmcnt(0) lgkmcnt(0)\n\t"
        ".Lk_end_P:\n\t"
        : [t0] "=&s"(t0), [t1] "=&s"(t1), [tl] "+s"(tl), [pg] "=&s"(pg), [cu] "=&s"(cu), [nx] "=&s"(nx), [sp] "=&s"(sp), [re] "=&s"(re)
        : [so] "s"(so), [sw] "s"(sw), [su] "s"(su), [cv] "s"(F_CVY + nt * 128), [ro] "s"(ro),
          "{v110}"((w * 1024 + (l >> 3)) * 2048 + nt * 128 + (l & 7) * 16), "{v111}"(stg + ((l * 16) ^ ((l & 48) << 1))), "{v112}"(lr), "{v113}"(lr ^ 32u), "{v114}"(lr ^ 64u), "{v115}"(lr ^ 96u),
          "{v116}"(lb + w * 32768 + l * 16), "{v117}"(W_OX + w * 32768 + l * 16), "{v118}"(F_PROG + (w * 8 + (l & 7)) * 128),
          "{v119}"(W_A + ((nt * 2 + (n >> 3)) * 4 + 2 * (n & 1)) * 32768 + w * 4096 + ((n & 7) >> 1) * 16 + g * 4), "{v120}"(W_GATE + w * 128 + g * 4), "{v121}"((4 * w + g) * 2048 + nt * 128 + n * 8),
          "{v122}"(lb + 131072 + w * 4352 + n * 272 + g * 16), "{v123}"(lb + 131072 + (4 * w + g) * 272 + n * 16)
        : "memory", "scc", "vcc", "v8", "v9", "v10", "v11", "v12", "v13", "v14", "v15", "v16", "v17", "v18", "v19", "v20", "v21", "v22", "v23", "v24", "v25", "v26", "v27", "v28", "v29", "v30", "v31", "v32", "v33", "v34", "v35", "v36", "v37", "v38", "v39", "v40", "v41", "v42", "v43", "v44", "v45", "v46", "v47", "v48", "v49", "v50", "v51", "v52", "v53", "v54", "v55", "v56", "v57", "v58", "v59", "v60", "v61", "v62", "v63", "v64", "v65", "v66", "v67", "v68", "v69", "v70", "v71", "v72", "v73", "v74", "v75", "v76", "v77", "v78", "v79", "v80", "v81", "v82", "v83", "v84", "v85", "v86", "v87", "v88", "v89", "v90", "v91", "v92", "v93", "v94", "v95", "v96", "v97", "v98", "v99", "v100", "v101", "v102", "v103", "v104", "v105", "v106", "v107", "v108", "v109", "v124", "v125", "v126", "v127", "v128", "v129", "v130", "v131", "v132", "v133", "v134", "v135", "v136", "v137", "v138", "v139", "v140", "v141", "v142", "v143", "v144", "v145", "v146", "v147", "v148", "v149", "v150", "v151", "v152", "v153", "v154", "v155", "v156", "v157", "v158", "v159", "v160", "v161", "v162", "v163", "v164", "v165", "v166", "v167", "v168", "v169", "v170", "v171", "v172", "v173", "v174", "v175", "v176", "v177", "v178", "v179", "v180", "v181", "v182", "v183", "v184", "v185", "v186", "v187", "v188", "v189", "v190", "v191", "v192", "v193", "v194", "v195", "v196", "v197", "v198", "v199", "v200", "v201", "v202", "v203", "v204", "v205", "v206", "v207", "v208", "v209", "v210", "v211", "v212", "v213", "v214", "v215", "v216", "v217", "v218", "v219", "v220", "v221", "v222", "v223", "v224", "v225", "v226", "v227", "v228", "v229", "v230", "v231", "v232", "v233", "v234", "v235", "v236", "v237", "v238", "v239", "v240", "v241", "v242", "v243", "v244", "v245", "v246", "v247", "v248", "v249", "v250", "v251", "v252", "v253", "v254", "v255", "a0", "a1", "a2", "a3", "a4", "a5", "a6", "a7", "a8", "a9", "a10", "a11", "a12", "a13", "a14", "a15", "a16", "a17", "a18", "a19", "a20", "a21", "a22", "a23", "a24", "a25", "a26", "a27", "a28", "a29", "a30", "a31", "a32", "a33", "a34", "a35", "a36", "a37", "a38", "a39", "a40", "a41", "a42", "a43", "a44", "a45", "a46", "a47", "a48", "a49", "a50", "a51", "a52", "a53", "a54", "a55", "a56", "a57", "a58", "a59", "a60", "a61", "a62", "a63", "a64", "a65", "a66", "a67", "a68", "a69", "a70", "a71", "a72", "a73", "a74", "a75", "a76", "a77", "a78", "a79", "a80", "a81", "a82", "a83", "a84", "a85", "a86", "a87", "a88", "a89", "a90", "a91", "a92", "a93", "a94", "a95", "a96", "a97", "a98", "a99", "a100", "a101", "a102", "a103", "a104", "a105", "a106", "a107", "a108", "a109", "a110", "a111", "a112", "a113", "a114", "a115", "a116", "a117", "a118", "a119", "a120", "a121", "a122", "a123", "a124", "a125", "a126", "a127", "a128", "a129", "a130", "a131", "a132", "a133", "a134", "a135", "a136", "a137", "a138", "a139", "a140", "a141", "a142", "a143", "a144", "a145", "a146", "a147", "a148", "a149", "a150", "a151", "a152", "a153", "a154", "a155", "a156", "a157", "a158", "a159", "a160", "a161", "a162", "a163", "a164", "a165", "a166", "a167", "a168", "a169", "a170", "a171", "a172", "a173", "a174", "a175", "a176", "a177", "a178", "a179", "a180", "a181", "a182", "a183", "a184", "a185", "a186", "a187", "a188", "a189", "a190", "a191", "a192", "a193", "a194", "a195", "a196", "a197", "a198", "a199", "a200", "a201", "a202", "a203", "a204", "a205", "a206", "a207", "a208", "a209", "a210", "a211", "a212", "a213", "a214", "a215", "a216", "a217", "a218", "a219", "a220", "a221", "a222", "a223", "a224", "a225", "a226", "a227", "a228", "a229", "a230", "a231", "a232", "a233", "a234", "a235", "a236", "a237", "a238", "a239", "a240", "a241", "a242", "a243", "a244", "a245", "a246", "a247", "a248", "a249", "a250", "a251", "a252", "a253", "a254", "a255");
}
__device__ __forceinline__ void bulk_block(const Params& p, rsrc_t r, unsigned char* lds, const int wv, const int j) {
    proj_block(p, lds, wv, (j & 6) * 4 + (j >> 4), ((j >> 2) & 2) | (j & 1));
    const int un = ((j & 7) >> 1) * 32 + (j >> 3) * 2 + (j & 1);
    gate_block(p, r, j, wv);
    conv_prep(p, r, wv, un >> 2, un & 3);
    const int rp = ((j & 7) >> 1) + (j & 1) * 4, nt = (j >> 3) ^ 2, ro = (nt + 2 * rp) & 7;
    if (!conv_core(p, r, lds, wv, un, nt, ro)) return;
    if (!wait_word(r, F_CONV, 128u, 4096u)) return;
    out_block(p, lds, wv, rp, ro, nt);
}

__device__ __forceinline__ void rec_block(const Params& p, rsrc_t r, unsigned char* lds, const int wv, const int b) {
    const int xq = (b & 7) >> 1, t = (b >> 3) * 2 + (b & 1);
    const unsigned lb = __builtin_amdgcn_readfirstlane((unsigned)(size_t)lds);
    if (t < 8) rec_a(p, lb, wv, xq * 8 + t, xq);
    else if (t < 24) rec_b(p, lb, wv, (xq & 1) * 16 + t - 8, xq >> 1, xq);
    else {
        const int hi = xq * 8 + t - 24;
        rec_helper(p, r, lb, wv, hi);
    }
}

__global__ void __launch_bounds__(256, 1) k_main(Params p) {
    extern __shared__ __attribute__((aligned(16))) unsigned char lds[];
    rsrc_t r = __builtin_amdgcn_make_buffer_rsrc(p.ws, 0, W_TOTAL, 0x00020000);
    const int wv = __builtin_amdgcn_readfirstlane(threadIdx.x >> 6);
    {
        unsigned long long pc; int ea, eb, eg;
        asm volatile("s_getpc_b64 %0\n.Lk_begin:\n\ts_mov_b32 %1, .Lk_end_A-.Lk_begin\n\ts_mov_b32 %2, .Lk_end_B-.Lk_begin\n\ts_mov_b32 %3, .Lk_end_P-.Lk_begin" : "=s"(pc), "=s"(ea), "=s"(eb), "=s"(eg));
        const int o = ((blockIdx.x >> 3) * 64 + (threadIdx.x >> 2)) * 128;
        if ((threadIdx.x & 3) == 0 && o < max(ea, max(eb, eg)) - 4) touch_l2((const unsigned char*)pc + o, __builtin_amdgcn_readfirstlane((unsigned)(size_t)lds) + 1024);
    }
    if (wv == 3) {
        unsigned xc; asm volatile("s_getreg_b32 %0, hwreg(HW_REG_XCC_ID)" : "=s"(xc));
        if (xc & 4u) touch_l2(p.Aq + ((blockIdx.x >> 3) * 64 + (threadIdx.x & 63)) * 64, __builtin_amdgcn_readfirstlane((unsigned)(size_t)lds) + 2048);
    }
    if (blockIdx.x == 0 && wv == 1) {
        const int l = threadIdx.x & 63; const v4u z = {0u, 0u, 0u, 0u};
#pragma unroll
        for (int i = 0; i < 6; ++i) st16(r, F_CONV + i * 1024 + l * 16, z);
        DRAIN();
        for (unsigned spin = 0; spin < (1u << 20); ++spin) {
            if (l < 8) __hip_atomic_fetch_add((unsigned*)(p.ws + F_BEAT + l * 128), 1u, __ATOMIC_RELAXED, __HIP_MEMORY_SCOPE_AGENT);
            unsigned c = ld4(r, l < 8 ? F_TKT + l * 64 : OOB);
            c += __shfl_xor(c, 1); c += __shfl_xor(c, 2); c += __shfl_xor(c, 4);
            if (__builtin_amdgcn_readfirstlane(c) >= 256u) break; }
    }
    if (threadIdx.x == 0) {
        const unsigned bo = F_BEAT + (blockIdx.x & 7) * 128, b0 = ld4(r, bo);
        for (unsigned spin = 0; spin < (1u << 22); ++spin) { MEMBAR(); if (ld4(r, bo) != b0) break; }
        MEMBAR();
        unsigned xcc; asm volatile("s_getreg_b32 %0, hwreg(HW_REG_XCC_ID)" : "=s"(xcc)); xcc &= 7u;
        unsigned* tk = (unsigned*)(p.ws + F_TKT);
        unsigned t = __hip_atomic_fetch_add(tk + xcc * 16, 1u, __ATOMIC_RELAXED, __HIP_MEMORY_SCOPE_AGENT);
        if (t >= 32u) {
            unsigned o = __hip_atomic_fetch_add(tk + 128, 1u, __ATOMIC_RELAXED, __HIP_MEMORY_SCOPE_AGENT);
            for (unsigned spin = 0; spin < (1u << 22); ++spin) { unsigned tot = 0u;
                for (int x = 0; x < 8; ++x) tot += __hip_atomic_load(tk + x * 16, __ATOMIC_RELAXED, __HIP_MEMORY_SCOPE_AGENT);
                if (tot >= 256u) break; }
            for (int x = 0; x < 8; ++x) { const unsigned cnt = __hip_atomic_load(tk + x * 16, __ATOMIC_RELAXED, __HIP_MEMORY_SCOPE_AGENT), fr = cnt < 32u ? 32u - cnt : 0u;
                if (o < fr) { xcc = (unsigned)x; t = cnt + o; break; }
                o -= fr; }
        }
        *(volatile int*)lds = (int)((xcc >> 2) * 128u + (t >> 1) * 8u + (xcc & 3u) * 2u + (t & 1u));
    }
    DRAIN();
    __syncthreads();
    const int role = __builtin_amdgcn_readfirstlane(*(volatile int*)lds) & 255;
    __syncthreads();
    if (role >= 128) bulk_block(p, r, lds, wv, role - 128); else rec_block(p, r, lds, wv, role);
}

extern "C" void kernel_launch(void* const* d_in, const int* in_sizes, int n_in, void* d_out, int out_size, void* d_ws, size_t ws_size, hipStream_t stream) {
    Params p;
    p.Aq = (const bf16_t*)d_in[0]; p.Ak = (const bf16_t*)d_in[1]; p.Av = (const bf16_t*)d_in[2];
    p.shift_k = (const bf16_t*)d_in[3]; p.ssm_k = (const bf16_t*)d_in[4]; p.ssm_D = (const bf16_t*)d_in[5];
    p.mem0 = (const bf16_t*)d_in[7]; p.Wq = (const bf16_t*)d_in[8]; p.Wk = (const bf16_t*)d_in[9];
    p.Wv = (const bf16_t*)d_in[10]; p.Wo = (const bf16_t*)d_in[11]; p.gate_w = (const bf16_t*)d_in[13];
    p.out = (bf16_t*)d_out; p.ws = (unsigned char*)d_ws;
    (void)hipFuncSetAttribute((const void*)k_main, hipFuncAttributeMaxDynamicSharedMemorySize, L_TOTAL);
    k_main<<<256, 256, L_TOTAL, stream>>>(p);
}
```

```cpp
#include <hip/hip_runtime.h>
#include <stdint.h>
typedef unsigned short bf16_t;
using f32x16 = __attribute__((ext_vector_type(16))) float;
typedef unsigned v4u __attribute__((ext_vector_type(4)));
typedef unsigned v2u __attribute__((ext_vector_type(2)));
typedef __amdgpu_buffer_rsrc_t rsrc_t;
typedef __bf16 hbf2 __attribute__((ext_vector_type(2)));
typedef float f32x2 __attribute__((ext_vector_type(2)));
__device__ __forceinline__ unsigned pack2(float a, float b) { f32x2 v = {a, b}; return __builtin_bit_cast(unsigned, __builtin_convertvector(v, hbf2)); }
__device__ __forceinline__ float rbfh(float f) { return __uint_as_float(pack2(f, 0.f) << 16); }
__device__ __forceinline__ float blo(unsigned x) { return __uint_as_float(x << 16); }
__device__ __forceinline__ float bhi(unsigned x) { return __uint_as_float(x & 0xFFFF0000u); }

constexpr int S = 2048, D = 1024;

constexpr unsigned F_KVQ = 0;
constexpr unsigned F_CONV = 32768;
constexpr unsigned F_ERR = 33024;
constexpr unsigned F_CAL = 33792;
constexpr unsigned F_RDY0 = 34304;
constexpr unsigned F_TKT = 34816;
constexpr unsigned F_GO = 36864;
constexpr unsigned F_BEAT = 40960;
constexpr unsigned F_PROG = 49152;
constexpr unsigned F_CVY = 57344;
constexpr unsigned W_A = 8568832;
constexpr unsigned W_GATE = 65536;
constexpr unsigned W_QX = W_A + S * D * 2 + 8192;
constexpr unsigned W_KX = W_QX + S * 4096 * 2;
constexpr unsigned W_VF = W_KX + S * 4096 * 2;
constexpr unsigned W_OX = W_VF + S * 4096 * 2;
constexpr unsigned W_Y = W_OX + S * 4096 * 2;
constexpr unsigned W_U = 115531776;
constexpr unsigned W_TOTAL = W_U + S * D * 2;
constexpr unsigned OOB = 0xF0000000u;
constexpr int L_TOTAL = 163840;

__device__ __forceinline__ v4u ld16(rsrc_t r, unsigned off) { return __builtin_amdgcn_raw_buffer_load_b128(r, off, 0, 16); }
__device__ __forceinline__ v2u ld8(rsrc_t r, unsigned off) { return __builtin_amdgcn_raw_buffer_load_b64(r, off, 0, 16); }
__device__ __forceinline__ unsigned ld4(rsrc_t r, unsigned off) { return __builtin_amdgcn_raw_buffer_load_b32(r, off, 0, 16); }
__device__ __forceinline__ void st16(rsrc_t r, unsigned off, v4u v) { __builtin_amdgcn_raw_buffer_store_b128(v, r, off, 0, 16); }
__device__ __forceinline__ void st4(rsrc_t r, unsigned off, unsigned v) { __builtin_amdgcn_raw_buffer_store_b32(v, r, off, 0, 16); }
#define D1 128
#define D2 77
#define LT_A 1
#define PEN 48
#define CAL_T 450u
#define XSLP 1
#define T2U 2
#define CT2 1
#define TPRE 48
#define MEMBAR() asm volatile("" ::: "memory")
#define DRAIN() asm volatile("s_waitcnt vmcnt(0)" ::: "memory")
__device__ __forceinline__ int otid_(int wv) { int t; asm volatile("v_mbcnt_lo_u32_b32 %0, -1, 0\n\tv_mbcnt_hi_u32_b32 %0, -1, %0" : "=v"(t)); return wv * 64 + t; }
#define otid() otid_(wv)
__device__ __forceinline__ bool wait_word(rsrc_t r, unsigned off, unsigned want, unsigned code) {
    for (unsigned spin = 0;; ++spin) { MEMBAR();
        const unsigned v = __builtin_amdgcn_readfirstlane(ld4(r, off));
        if (v >= want) { MEMBAR(); return true; }
        if (spin > (1u << 20)) { st4(r, F_ERR, code); return false; }
        __builtin_amdgcn_s_sleep(8); }
}
__device__ __forceinline__ void touch_l2(const void* q, unsigned ldsdst) { asm volatile("s_mov_b32 m0, %1\n\ts_nop 0\n\tglobal_load_lds_dword %0, off" :: "v"(q), "s"(ldsdst) : "m0", "memory"); }
__device__ __forceinline__ unsigned rtnow() { return (unsigned)__builtin_amdgcn_s_memrealtime(); }

struct Params {
    const bf16_t *Aq, *Ak, *Av, *shift_k, *ssm_k, *ssm_D, *mem0, *Wq, *Wk, *Wv, *Wo, *gate_w;
    bf16_t* out; unsigned char* ws;
};
__device__ __forceinline__ unsigned ho_base(const void* ws) { const size_t a = (size_t)ws; return (unsigned)((((a + W_TOTAL + 0x7FFFFFu) >> 23) << 23) - a); }
__device__ __forceinline__ v4u mk_srd(const void* p, unsigned bytes) { const size_t a = (size_t)p; v4u d;
    d.x = __builtin_amdgcn_readfirstlane((unsigned)a); d.y = __builtin_amdgcn_readfirstlane((unsigned)(a >> 32) & 0xFFFFu); d.z = __builtin_amdgcn_readfirstlane(bytes); d.w = 0x00020000u; return d; }
constexpr unsigned HO_BYTES = 2097152;
constexpr int L_KV = 90112, L_MS = 139264, L_IMG = 155648, IMG_PS = 1056, IMG_MS = 2112, LB_R = 8192, LB_STG = 16384, LB_STGP = 8704;
static_assert(F_KVQ == 0 && F_ERR == 33024 && F_CONV == 32768 && F_CAL == 33792 && F_RDY0 == 34304 && F_GO == 36864 && F_PROG == 49152 && W_QX == 12771328 && W_KX == 29548544 && W_OX == 63102976 && W_VF == 46325760 && L_KV == 90112 && L_MS == 139264 && L_IMG == 155648 && IMG_PS == 1056 && IMG_MS == 2112 && LB_R == 8192 && LB_STG == 16384 && LB_STGP == 8704, "constants of the generated code");
#define REC_S_IN "{s[8:11]}"(sh), "{s[12:15]}"(sw), "{s[16:19]}"(s1), "{s[20:23]}"(s2), "{s29}"(D1), "{s30}"(D2), "{s43}"(w), "{s44}"(id), "{s49}"(rb), "{s50}"(PEN), "{s51}"(LT_A), "{s45}"(F_GO + (cp * 4 + w) * 128)
__device__ __forceinline__ void rec_a(const Params& p, unsigned lb, const int wv, const int id, const int cp) {
    const int w = wv, l = otid() & 63, n = l & 15, g = l >> 4, h = id, rb = id;
    const v4u sh = mk_srd(p.ws + ho_base(p.ws), HO_BYTES - 8192), sw = mk_srd(p.ws, W_TOTAL), s1 = mk_srd(w < 2 ? p.Wq : p.Wk, 1024 * 4096 * 2), s2 = s1;
    const int r8 = l >> 3, pc = l & 7, pp = n >> 3, bb = (n >> 2) & 1, f = (g & 1) | (pp << 1);
    const unsigned stg = lb + 131072 + w * 8192;
    const unsigned g_w = r8 * 8192 + h * 256 + (w & 1) * 128 + pc * 16;
    const unsigned wb0 = stg + r8 * 128 + (((pc >> 1) ^ (r8 >> 2)) << 5) + (pc & 1) * 16, wb1 = wb0 ^ 64u;
    const unsigned rb0 = stg + (16 * (g >> 1) + 8 * pp + 4 * (g & 1) + bb) * 128 + (f << 5) + (n & 3) * 8;
    const unsigned a_mem = cp * 32768 + (w >> 1) * 16384 + (w & 1) * 4096 + l * 16;
    const unsigned l_ms = lb + L_MS + l * 16;
    const unsigned l_iw = lb + L_IMG + (w >> 1) * IMG_MS + (g >> 1) * IMG_PS + (64 * (w & 1) + n) * 8 + (g & 1) * 4;
    const unsigned l_iq = lb + L_IMG + (n & 1) * IMG_PS + (8 * g + (n >> 2)) * 8;
    const unsigned a_vm = (16 + (h >> 2)) * 16384 + (h & 3) * 2048 + w * 512 + (l & 31) * 16;
    const unsigned a_o = n < 8 ? (8 + (n >> 2) * 4 + (h >> 3)) * 16384 + (h & 7) * 1024 + w * 256 + (n & 3) * 64 + g * 16 : OOB;
    const unsigned a_ox = (((w & 1) * 32 + h) * 4 + w) * 1024 + l * 16;
    asm volatile(
        "s_nop 4\n\t"
        "s_lshl_b32 s34, s43, 2\n\t"
        "s_add_u32 s34, s34, 0\n\t"
        "s_and_b32 s34, s34, 15\n\t"
        "s_lshl_b32 s34, s34, 19\n\t"
        "buffer_load_dwordx4 v[224:227], v96, s[16:19], s34 offen nt\n\t"
        "s_add_u32 s34, s34, 0x10000\n\t"
        "buffer_load_dwordx4 v[228:231], v96, s[16:19], s34 offen nt\n\t"
        "s_add_u32 s34, s34, 0x10000\n\t"
        "buffer_load_dwordx4 v[232:235], v96, s[16:19], s34 offen nt\n\t"
        "s_add_u32 s34, s34, 0x10000\n\t"
        "buffer_load_dwordx4 v[236:239], v96, s[16:19], s34 offen nt\n\t"
        "s_add_u32 s34, s34, 0x10000\n\t"
        "buffer_load_dwordx4 v[240:243], v96, s[16:19], s34 offen nt\n\t"
        "s_add_u32 s34, s34, 0x10000\n\t"
        "buffer_load_dwordx4 v[244:247], v96, s[16:19], s34 offen nt\n\t"
        "s_add_u32 s34, s34, 0x10000\n\t"
        "buffer_load_dwordx4 v[248:251], v96, s[16:19], s34 offen nt\n\t"
        "s_add_u32 s34, s34, 0x10000\n\t"
        "buffer_load_dwordx4 v[252:255], v96, s[16:19], s34 offen nt\n\t"
        "s_lshl_b32 s34, s43, 2\n\t"
        "s_add_u32 s34, s34, 1\n\t"
        "s_and_b32 s34, s34, 15\n\t"
        "s_lshl_b32 s34, s34, 19\n\t"
        "buffer_load_dwordx4 v[0:3], v96, s[16:19], s34 offen nt\n\t"
        "s_add_u32 s34, s34, 0x10000\n\t"
        "buffer_load_dwordx4 v[4:7], v96, s[16:19], s34 offen nt\n\t"
        "s_add_u32 s34, s34, 0x10000\n\t"
        "buffer_load_dwordx4 v[8:11], v96, s[16:19], s34 offen nt\n\t"
        "s_add_u32 s34, s34, 0x10000\n\t"
        "buffer_load_dwordx4 v[12:15], v96, s[16:19], s34 offen nt\n\t"
        "s_add_u32 s34, s34, 0x10000\n\t"
        "buffer_load_dwordx4 v[16:19], v96, s[16:19], s34 offen nt\n\t"
        "s_add_u32 s34, s34, 0x10000\n\t"
        "buffer_load_dwordx4 v[20:23], v96, s[16:19], s34 offen nt\n\t"
        "s_add_u32 s34, s34, 0x10000\n\t"
        "buffer_load_dwordx4 v[24:27], v96, s[16:19], s34 offen nt\n\t"
        "s_add_u32 s34, s34, 0x10000\n\t"
        "buffer_load_dwordx4 v[28:31], v96, s[16:19], s34 offen nt\n\t"
        "s_lshl_b32 s34, s43, 2\n\t"
        "s_add_u32 s34, s34, 2\n\t"
        "s_and_b32 s34, s34, 15\n\t"
        "s_lshl_b32 s34, s34, 19\n\t"
        "buffer_load_dwordx4 v[32:35], v96, s[16:19], s34 offen nt\n\t"
        "s_add_u32 s34, s34, 0x10000\n\t"
        "buffer_load_dwordx4 v[36:39], v96, s[16:19], s34 offen nt\n\t"
        "s_add_u32 s34, s34, 0x10000\n\t"
        "buffer_load_dwordx4 v[40:43], v96, s[16:19], s34 offen nt\n\t"
        "s_add_u32 s34, s34, 0x10000\n\t"
        "buffer_load_dwordx4 v[44:47], v96, s[16:19], s34 offen nt\n\t"
        "s_add_u32 s34, s34, 0x10000\n\t"
        "buffer_load_dwordx4 v[48:51], v96, s[16:19], s34 offen nt\n\t"
        "s_add_u32 s34, s34, 0x10000\n\t"
        "buffer_load_dwordx4 v[52:55], v96, s[16:19], s34 offen nt\n\t"
        "s_add_u32 s34, s34, 0x10000\n\t"
        "buffer_load_dwordx4 v[56:59], v96, s[16:19], s34 offen nt\n\t"
        "s_add_u32 s34, s34, 0x10000\n\t"
        "buffer_load_dwordx4 v[60:63], v96, s[16:19], s34 offen nt\n\t"
        "v_cmp_le_i32_e64 s[60:61], 0, v126\n\t"
        "v_cmp_le_i32_e64 s[62:63], 1, v126\n\t"
        "v_cmp_le_i32_e64 s[64:65], 2, v126\n\t"
        "v_cmp_le_i32_e64 s[66:67], 3, v126\n\t"
        "v_cmp_le_i32_e64 s[68:69], 16, v126\n\t"
        "v_cmp_le_i32_e64 s[70:71], 17, v126\n\t"
        "v_cmp_le_i32_e64 s[72:73], 18, v126\n\t"
        "v_cmp_le_i32_e64 s[74:75], 19, v126\n\t"
        "s_mul_i32 s35, s43, 10240\n\t"
        "v_add_u32_e32 v126, s35, v124\n\t"
        "s_waitcnt vmcnt(16)\n\t"
        "ds_write_b128 v97, v[224:227] offset:0\n\t"
        "ds_write_b128 v98, v[228:231] offset:1024\n\t"
        "ds_write_b128 v97, v[232:235] offset:2048\n\t"
        "ds_write_b128 v98, v[236:239] offset:3072\n\t"
        "ds_write_b128 v97, v[240:243] offset:4096\n\t"
        "ds_write_b128 v98, v[244:247] offset:5120\n\t"
        "ds_write_b128 v97, v[248:251] offset:6144\n\t"
        "ds_write_b128 v98, v[252:255] offset:7168\n\t"
        "ds_read_b64_tr_b16 a[0:1], v99 offset:0\n\t"
        "ds_read_b64_tr_b16 a[2:3], v99 offset:256\n\t"
        "ds_read_b64_tr_b16 a[4:5], v99 offset:4096\n\t"
        "ds_read_b64_tr_b16 a[6:7], v99 offset:4352\n\t"
        "s_waitcnt lgkmcnt(8)\n\t"
        "ds_read_b64_tr_b16 a[8:9], v100 offset:0\n\t"
        "ds_read_b64_tr_b16 a[10:11], v100 offset:256\n\t"
        "ds_read_b64_tr_b16 a[12:13], v100 offset:4096\n\t"
        "ds_read_b64_tr_b16 a[14:15], v100 offset:4352\n\t"
        "s_waitcnt lgkmcnt(8)\n\t"
        "ds_read_b64_tr_b16 a[16:17], v101 offset:0\n\t"
        "ds_read_b64_tr_b16 a[18:19], v101 offset:256\n\t"
        "ds_read_b64_tr_b16 a[20:21], v101 offset:4096\n\t"
        "ds_read_b64_tr_b16 a[22:23], v101 offset:4352\n\t"
        "s_waitcnt lgkmcnt(8)\n\t"
        "ds_read_b64_tr_b16 v[104:105], v102 offset:0\n\t"
        "ds_read_b64_tr_b16 v[106:107], v102 offset:256\n\t"
        "ds_read_b64_tr_b16 v[108:109], v102 offset:4096\n\t"
        "ds_read_b64_tr_b16 v[110:111], v102 offset:4352\n\t"
        "s_lshl_b32 s34, s43, 2\n\t"
        "s_add_u32 s34, s34, 3\n\t"
        "s_and_b32 s34, s34, 15\n\t"
        "s_lshl_b32 s34, s34, 19\n\t"
        "buffer_load_dwordx4 v[64:67], v96, s[16:19], s34 offen nt\n\t"
        "s_add_u32 s34, s34, 0x10000\n\t"
        "buffer_load_dwordx4 v[68:71], v96, s[16:19], s34 offen nt\n\t"
        "s_add_u32 s34, s34, 0x10000\n\t"
        "buffer_load_dwordx4 v[72:75], v96, s[16:19], s34 offen nt\n\t"
        "s_add_u32 s34, s34, 0x10000\n\t"
        "buffer_load_dwordx4 v[76:79], v96, s[16:19], s34 offen nt\n\t"
        "s_add_u32 s34, s34, 0x10000\n\t"
        "buffer_load_dwordx4 v[80:83], v96, s[16:19], s34 offen nt\n\t"
        "s_add_u32 s34, s34, 0x10000\n\t"
        "buffer_load_dwordx4 v[84:87], v96, s[16:19], s34 offen nt\n\t"
        "s_add_u32 s34, s34, 0x10000\n\t"
        "buffer_load_dwordx4 v[88:91], v96, s[16:19], s34 offen nt\n\t"
        "s_add_u32 s34, s34, 0x10000\n\t"
        "buffer_load_dwordx4 v[92:95], v96, s[16:19], s34 offen nt\n\t"
        "s_waitcnt lgkmcnt(0)\n\t"
        "ds_write_b128 v117, v[104:107] offset:0\n\t"
        "ds_write_b128 v117, v[108:111] offset:1024\n\t"
        "s_waitcnt vmcnt(16)\n\t"
        "ds_write_b128 v97, v[0:3] offset:0\n\t"
        "ds_write_b128 v98, v[4:7] offset:1024\n\t"
        "ds_write_b128 v97, v[8:11] offset:2048\n\t"
        "ds_write_b128 v98, v[12:15] offset:3072\n\t"
        "ds_write_b128 v97, v[16:19] offset:4096\n\t"
        "ds_write_b128 v98, v[20:23] offset:5120\n\t"
        "ds_write_b128 v97, v[24:27] offset:6144\n\t"
        "ds_write_b128 v98, v[28:31] offset:7168\n\t"
        "ds_read_b64_tr_b16 a[24:25], v99 offset:0\n\t"
        "ds_read_b64_tr_b16 a[26:27], v99 offset:256\n\t"
        "ds_read_b64_tr_b16 a[28:29], v99 offset:4096\n\t"
        "ds_read_b64_tr_b16 a[30:31], v99 offset:4352\n\t"
        "s_waitcnt lgkmcnt(8)\n\t"
        "ds_read_b64_tr_b16 a[32:33], v100 offset:0\n\t"
        "ds_read_b64_tr_b16 a[34:35], v100 offset:256\n\t"
        "ds_read_b64_tr_b16 a[36:37], v100 offset:4096\n\t"
        "ds_read_b64_tr_b16 a[38:39], v100 offset:4352\n\t"
        "s_waitcnt lgkmcnt(8)\n\t"
        "ds_read_b64_tr_b16 a[40:41], v101 offset:0\n\t"
        "ds_read_b64_tr_b16 a[42:43], v101 offset:256\n\t"
        "ds_read_b64_tr_b16 a[44:45], v101 offset:4096\n\t"
        "ds_read_b64_tr_b16 a[46:47], v101 offset:4352\n\t"
        "s_waitcnt lgkmcnt(8)\n\t"
        "ds_read_b64_tr_b16 v[104:105], v102 offset:0\n\t"
        "ds_read_b64_tr_b16 v[106:107], v102 offset:256\n\t"
        "ds_read_b64_tr_b16 v[108:109], v102 offset:4096\n\t"
        "ds_read_b64_tr_b16 v[110:111], v102 offset:4352\n\t"
        "s_lshl_b32 s34, s43, 2\n\t"
        "s_add_u32 s34, s34, 4\n\t"
        "s_and_b32 s34, s34, 15\n\t"
        "s_lshl_b32 s34, s34, 19\n\t"
        "buffer_load_dwordx4 a[96:99], v96, s[16:19], s34 offen nt\n\t"
        "s_add_u32 s34, s34, 0x10000\n\t"
        "buffer_load_dwordx4 a[100:103], v96, s[16:19], s34 offen nt\n\t"
        "s_add_u32 s34, s34, 0x10000\n\t"
        "buffer_load_dwordx4 a[104:107], v96, s[16:19], s34 offen nt\n\t"
        "s_add_u32 s34, s34, 0x10000\n\t"
        "buffer_load_dwordx4 a[108:111], v96, s[16:19], s34 offen nt\n\t"
        "s_add_u32 s34, s34, 0x10000\n\t"
        "buffer_load_dwordx4 a[112:115], v96, s[16:19], s34 offen nt\n\t"
        "s_add_u32 s34, s34, 0x10000\n\t"
        "buffer_load_dwordx4 a[116:119], v96, s[16:19], s34 offen nt\n\t"
        "s_add_u32 s34, s34, 0x10000\n\t"
        "buffer_load_dwordx4 a[120:123], v96, s[16:19], s34 offen nt\n\t"
        "s_add_u32 s34, s34, 0x10000\n\t"
        "buffer_load_dwordx4 a[124:127], v96, s[16:19], s34 offen nt\n\t"
        "s_waitcnt lgkmcnt(0)\n\t"
        "ds_write_b128 v117, v[104:107] offset:2048\n\t"
        "ds_write_b128 v117, v[108:111] offset:3072\n\t"
        "s_waitcnt vmcnt(16)\n\t"
        "ds_write_b128 v97, v[32:35] offset:0\n\t"
        "ds_write_b128 v98, v[36:39] offset:1024\n\t"
        "ds_write_b128 v97, v[40:43] offset:2048\n\t"
        "ds_write_b128 v98, v[44:47] offset:3072\n\t"
        "ds_write_b128 v97, v[48:51] offset:4096\n\t"
        "ds_write_b128 v98, v[52:55] offset:5120\n\t"
        "ds_write_b128 v97, v[56:59] offset:6144\n\t"
        "ds_write_b128 v98, v[60:63] offset:7168\n\t"
        "ds_read_b64_tr_b16 a[48:49], v99 offset:0\n\t"
        "ds_read_b64_tr_b16 a[50:51], v99 offset:256\n\t"
        "ds_read_b64_tr_b16 a[52:53], v99 offset:4096\n\t"
        "ds_read_b64_tr_b16 a[54:55], v99 offset:4352\n\t"
        "s_waitcnt lgkmcnt(8)\n\t"
        "ds_read_b64_tr_b16 a[56:57], v100 offset:0\n\t"
        "ds_read_b64_tr_b16 a[58:59], v100 offset:256\n\t"
        "ds_read_b64_tr_b16 a[60:61], v100 offset:4096\n\t"
        "ds_read_b64_tr_b16 a[62:63], v100 offset:4352\n\t"
        "s_waitcnt lgkmcnt(8)\n\t"
        "ds_read_b64_tr_b16 a[64:65], v101 offset:0\n\t"
        "ds_read_b64_tr_b16 a[66:67], v101 offset:256\n\t"
        "ds_read_b64_tr_b16 a[68:69], v101 offset:4096\n\t"
        "ds_read_b64_tr_b16 a[70:71], v101 offset:4352\n\t"
        "s_waitcnt lgkmcnt(8)\n\t"
        "ds_read_b64_tr_b16 v[104:105], v102 offset:0\n\t"
        "ds_read_b64_tr_b16 v[106:107], v102 offset:256\n\t"
        "ds_read_b64_tr_b16 v[108:109], v102 offset:4096\n\t"
        "ds_read_b64_tr_b16 v[110:111], v102 offset:4352\n\t"
        "s_lshl_b32 s34, s43, 2\n\t"
        "s_add_u32 s34, s34, 5\n\t"
        "s_and_b32 s34, s34, 15\n\t"
        "s_lshl_b32 s34, s34, 19\n\t"
        "buffer_load_dwordx4 a[128:131], v96, s[16:19], s34 offen nt\n\t"
        "s_add_u32 s34, s34, 0x10000\n\t"
        "buffer_load_dwordx4 a[132:135], v96, s[16:19], s34 offen nt\n\t"
        "s_add_u32 s34, s34, 0x10000\n\t"
        "buffer_load_dwordx4 a[136:139], v96, s[16:19], s34 offen nt\n\t"
        "s_add_u32 s34, s34, 0x10000\n\t"
        "buffer_load_dwordx4 a[140:143], v96, s[16:19], s34 offen nt\n\t"
        "s_add_u32 s34, s34, 0x10000\n\t"
        "buffer_load_dwordx4 a[144:147], v96, s[16:19], s34 offen nt\n\t"
        "s_add_u32 s34, s34, 0x10000\n\t"
        "buffer_load_dwordx4 a[148:151], v96, s[16:19], s34 offen nt\n\t"
        "s_add_u32 s34, s34, 0x10000\n\t"
        "buffer_load_dwordx4 a[152:155], v96, s[16:19], s34 offen nt\n\t"
        "s_add_u32 s34, s34, 0x10000\n\t"
        "buffer_load_dwordx4 a[156:159], v96, s[16:19], s34 offen nt\n\t"
        "s_waitcnt lgkmcnt(0)\n\t"
        "ds_write_b128 v117, v[104:107] offset:4096\n\t"
        "ds_write_b128 v117, v[108:111] offset:5120\n\t"
        "s_waitcnt vmcnt(16)\n\t"
        "ds_write_b128 v97, v[64:67] offset:0\n\t"
        "ds_write_b128 v98, v[68:71] offset:1024\n\t"
        "ds_write_b128 v97, v[72:75] offset:2048\n\t"
        "ds_write_b128 v98, v[76:79] offset:3072\n\t"
        "ds_write_b128 v97, v[80:83] offset:4096\n\t"
        "ds_write_b128 v98, v[84:87] offset:5120\n\t"
        "ds_write_b128 v97, v[88:91] offset:6144\n\t"
        "ds_write_b128 v98, v[92:95] offset:7168\n\t"
        "ds_read_b64_tr_b16 a[72:73], v99 offset:0\n\t"
        "ds_read_b64_tr_b16 a[74:75], v99 offset:256\n\t"
        "ds_read_b64_tr_b16 a[76:77], v99 offset:4096\n\t"
        "ds_read_b64_tr_b16 a[78:79], v99 offset:4352\n\t"
        "s_waitcnt lgkmcnt(8)\n\t"
        "ds_read_b64_tr_b16 a[80:81], v100 offset:0\n\t"
        "ds_read_b64_tr_b16 a[82:83], v100 offset:256\n\t"
        "ds_read_b64_tr_b16 a[84:85], v100 offset:4096\n\t"
        "ds_read_b64_tr_b16 a[86:87], v100 offset:4352\n\t"
        "s_waitcnt lgkmcnt(8)\n\t"
        "ds_read_b64_tr_b16 a[88:89], v101 offset:0\n\t"
        "ds_read_b64_tr_b16 a[90:91], v101 offset:256\n\t"
        "ds_read_b64_tr_b16 a[92:93], v101 offset:4096\n\t"
        "ds_read_b64_tr_b16 a[94:95], v101 offset:4352\n\t"
        "s_waitcnt lgkmcnt(8)\n\t"
        "ds_read_b64_tr_b16 v[104:105], v102 offset:0\n\t"
        "ds_read_b64_tr_b16 v[106:107], v102 offset:256\n\t"
        "ds_read_b64_tr_b16 v[108:109], v102 offset:4096\n\t"
        "ds_read_b64_tr_b16 v[110:111], v102 offset:4352\n\t"
        "s_lshl_b32 s34, s43, 2\n\t"
        "s_add_u32 s34, s34, 6\n\t"
        "s_and_b32 s34, s34, 15\n\t"
        "s_lshl_b32 s34, s34, 19\n\t"
        "buffer_load_dwordx4 a[160:163], v96, s[16:19], s34 offen nt\n\t"
        "s_add_u32 s34, s34, 0x10000\n\t"
        "buffer_load_dwordx4 a[164:167], v96, s[16:19], s34 offen nt\n\t"
        "s_add_u32 s34, s34, 0x10000\n\t"
        "buffer_load_dwordx4 a[168:171], v96, s[16:19], s34 offen nt\n\t"
        "s_add_u32 s34, s34, 0x10000\n\t"
        "buffer_load_dwordx4 a[172:175], v96, s[16:19], s34 offen nt\n\t"
        "s_add_u32 s34, s34, 0x10000\n\t"
        "buffer_load_dwordx4 a[176:179], v96, s[16:19], s34 offen nt\n\t"
        "s_add_u32 s34, s34, 0x10000\n\t"
        "buffer_load_dwordx4 a[180:183], v96, s[16:19], s34 offen nt\n\t"
        "s_add_u32 s34, s34, 0x10000\n\t"
        "buffer_load_dwordx4 a[184:187], v96, s[16:19], s34 offen nt\n\t"
        "s_add_u32 s34, s34, 0x10000\n\t"
        "buffer_load_dwordx4 a[188:191], v96, s[16:19], s34 offen nt\n\t"
        "s_waitcnt lgkmcnt(0)\n\t"
        "ds_write_b128 v117, v[104:107] offset:6144\n\t"
        "ds_write_b128 v117, v[108:111] offset:7168\n\t"
        "s_waitcnt vmcnt(16)\n\t"
        "ds_write_b128 v97, a[96:99] offset:0\n\t"
        "ds_write_b128 v98, a[100:103] offset:1024\n\t"
        "ds_write_b128 v97, a[104:107] offset:2048\n\t"
        "ds_write_b128 v98, a[108:111] offset:3072\n\t"
        "ds_write_b128 v97, a[112:115] offset:4096\n\t"
        "ds_write_b128 v98, a[116:119] offset:5120\n\t"
        "ds_write_b128 v97, a[120:123] offset:6144\n\t"
        "ds_write_b128 v98, a[124:127] offset:7168\n\t"
        "ds_read_b64_tr_b16 a[96:97], v99 offset:0\n\t"
        "ds_read_b64_tr_b16 a[98:99], v99 offset:256\n\t"
        "ds_read_b64_tr_b16 a[100:101], v99 offset:4096\n\t"
        "ds_read_b64_tr_b16 a[102:103], v99 offset:4352\n\t"
        "s_waitcnt lgkmcnt(8)\n\t"
        "ds_read_b64_tr_b16 a[104:105], v100 offset:0\n\t"
        "ds_read_b64_tr_b16 a[106:107], v100 offset:256\n\t"
        "ds_read_b64_tr_b16 a[108:109], v100 offset:4096\n\t"
        "ds_read_b64_tr_b16 a[110:111], v100 offset:4352\n\t"
        "s_waitcnt lgkmcnt(8)\n\t"
        "ds_read_b64_tr_b16 a[112:113], v101 offset:0\n\t"
        "ds_read_b64_tr_b16 a[114:115], v101 offset:256\n\t"
        "ds_read_b64_tr_b16 a[116:117], v101 offset:4096\n\t"
        "ds_read_b64_tr_b16 a[118:119], v101 offset:4352\n\t"
        "s_waitcnt lgkmcnt(8)\n\t"
        "ds_read_b64_tr_b16 v[104:105], v102 offset:0\n\t"
        "ds_read_b64_tr_b16 v[106:107], v102 offset:256\n\t"
        "ds_read_b64_tr_b16 v[108:109], v102 offset:4096\n\t"
        "ds_read_b64_tr_b16 v[110:111], v102 offset:4352\n\t"
        "s_lshl_b32 s34, s43, 2\n\t"
        "s_add_u32 s34, s34, 7\n\t"
        "s_and_b32 s34, s34, 15\n\t"
        "s_lshl_b32 s34, s34, 19\n\t"
        "buffer_load_dwordx4 a[192:195], v96, s[16:19], s34 offen nt\n\t"
        "s_add_u32 s34, s34, 0x10000\n\t"
        "buffer_load_dwordx4 a[196:199], v96, s[16:19], s34 offen nt\n\t"
        "s_add_u32 s34, s34, 0x10000\n\t"
        "buffer_load_dwordx4 a[200:203], v96, s[16:19], s34 offen nt\n\t"
        "s_add_u32 s34, s34, 0x10000\n\t"
        "buffer_load_dwordx4 a[204:207], v96, s[16:19], s34 offen nt\n\t"
        "s_add_u32 s34, s34, 0x10000\n\t"
        "buffer_load_dwordx4 a[208:211], v96, s[16:19], s34 offen nt\n\t"
        "s_add_u32 s34, s34, 0x10000\n\t"
        "buffer_load_dwordx4 a[212:215], v96, s[16:19], s34 offen nt\n\t"
        "s_add_u32 s34, s34, 0x10000\n\t"
        "buffer_load_dwordx4 a[216:219], v96, s[16:19], s34 offen nt\n\t"
        "s_add_u32 s34, s34, 0x10000\n\t"
        "buffer_load_dwordx4 a[220:223], v96, s[16:19], s34 offen nt\n\t"
        "s_waitcnt lgkmcnt(0)\n\t"
        "ds_write_b128 v126, v[104:107] offset:0\n\t"
        "ds_write_b128 v126, v[108:111] offset:1024\n\t"
        "s_waitcnt vmcnt(16)\n\t"
        "ds_write_b128 v97, a[128:131] offset:0\n\t"
        "ds_write_b128 v98, a[132:135] offset:1024\n\t"
        "ds_write_b128 v97, a[136:139] offset:2048\n\t"
        "ds_write_b128 v98, a[140:143] offset:3072\n\t"
        "ds_write_b128 v97, a[144:147] offset:4096\n\t"
        "ds_write_b128 v98, a[148:151] offset:5120\n\t"
        "ds_write_b128 v97, a[152:155] offset:6144\n\t"
        "ds_write_b128 v98, a[156:159] offset:7168\n\t"
        "ds_read_b64_tr_b16 a[120:121], v99 offset:0\n\t"
        "ds_read_b64_tr_b16 a[122:123], v99 offset:256\n\t"
        "ds_read_b64_tr_b16 a[124:125], v99 offset:4096\n\t"
        "ds_read_b64_tr_b16 a[126:127], v99 offset:4352\n\t"
        "s_waitcnt lgkmcnt(8)\n\t"
        "ds_read_b64_tr_b16 a[128:129], v100 offset:0\n\t"
        "ds_read_b64_tr_b16 a[130:131], v100 offset:256\n\t"
        "ds_read_b64_tr_b16 a[132:133], v100 offset:4096\n\t"
        "ds_read_b64_tr_b16 a[134:135], v100 offset:4352\n\t"
        "s_waitcnt lgkmcnt(8)\n\t"
        "ds_read_b64_tr_b16 a[136:137], v101 offset:0\n\t"
        "ds_read_b64_tr_b16 a[138:139], v101 offset:256\n\t"
        "ds_read_b64_tr_b16 a[140:141], v101 offset:4096\n\t"
        "ds_read_b64_tr_b16 a[142:143], v101 offset:4352\n\t"
        "s_waitcnt lgkmcnt(8)\n\t"
        "ds_read_b64_tr_b16 v[104:105], v102 offset:0\n\t"
        "ds_read_b64_tr_b16 v[106:107], v102 offset:256\n\t"
        "ds_read_b64_tr_b16 v[108:109], v102 offset:4096\n\t"
        "ds_read_b64_tr_b16 v[110:111], v102 offset:4352\n\t"
        "s_lshl_b32 s34, s43, 2\n\t"
        "s_add_u32 s34, s34, 8\n\t"
        "s_and_b32 s34, s34, 15\n\t"
        "s_lshl_b32 s34, s34, 19\n\t"
        "buffer_load_dwordx4 a[224:227], v96, s[16:19], s34 offen nt\n\t"
        "s_add_u32 s34, s34, 0x10000\n\t"
        "buffer_load_dwordx4 a[228:231], v96, s[16:19], s34 offen nt\n\t"
        "s_add_u32 s34, s34, 0x10000\n\t"
        "buffer_load_dwordx4 a[232:235], v96, s[16:19], s34 offen nt\n\t"
        "s_add_u32 s34, s34, 0x10000\n\t"
        "buffer_load_dwordx4 a[236:239], v96, s[16:19], s34 offen nt\n\t"
        "s_add_u32 s34, s34, 0x10000\n\t"
        "buffer_load_dwordx4 a[240:243], v96, s[16:19], s34 offen nt\n\t"
        "s_add_u32 s34, s34, 0x10000\n\t"
        "buffer_load_dwordx4 a[244:247], v96, s[16:19], s34 offen nt\n\t"
        "s_add_u32 s34, s34, 0x10000\n\t"
        "buffer_load_dwordx4 a[248:251], v96, s[16:19], s34 offen nt\n\t"
        "s_add_u32 s34, s34, 0x10000\n\t"
        "buffer_load_dwordx4 a[252:255], v96, s[16:19], s34 offen nt\n\t"
        "s_waitcnt lgkmcnt(0)\n\t"
        "ds_write_b128 v126, v[104:107] offset:2048\n\t"
        "ds_write_b128 v126, v[108:111] offset:3072\n\t"
        "s_waitcnt vmcnt(16)\n\t"
        "ds_write_b128 v97, a[160:163] offset:0\n\t"
        "ds_write_b128 v98, a[164:167] offset:1024\n\t"
        "ds_write_b128 v97, a[168:171] offset:2048\n\t"
        "ds_write_b128 v98, a[172:175] offset:3072\n\t"
        "ds_write_b128 v97, a[176:179] offset:4096\n\t"
        "ds_write_b128 v98, a[180:183] offset:5120\n\t"
        "ds_write_b128 v97, a[184:187] offset:6144\n\t"
        "ds_write_b128 v98, a[188:191] offset:7168\n\t"
        "ds_read_b64_tr_b16 a[144:145], v99 offset:0\n\t"
        "ds_read_b64_tr_b16 a[146:147], v99 offset:256\n\t"
        "ds_read_b64_tr_b16 a[148:149], v99 offset:4096\n\t"
        "ds_read_b64_tr_b16 a[150:151], v99 offset:4352\n\t"
        "s_waitcnt lgkmcnt(8)\n\t"
        "ds_read_b64_tr_b16 a[152:153], v100 offset:0\n\t"
        "ds_read_b64_tr_b16 a[154:155], v100 offset:256\n\t"
        "ds_read_b64_tr_b16 a[156:157], v100 offset:4096\n\t"
        "ds_read_b64_tr_b16 a[158:159], v100 offset:4352\n\t"
        "s_waitcnt lgkmcnt(8)\n\t"
        "ds_read_b64_tr_b16 a[160:161], v101 offset:0\n\t"
        "ds_read_b64_tr_b16 a[162:163], v101 offset:256\n\t"
        "ds_read_b64_tr_b16 a[164:165], v101 offset:4096\n\t"
        "ds_read_b64_tr_b16 a[166:167], v101 offset:4352\n\t"
        "s_waitcnt lgkmcnt(8)\n\t"
        "ds_read_b64_tr_b16 v[104:105], v102 offset:0\n\t"
        "ds_read_b64_tr_b16 v[106:107], v102 offset:256\n\t"
        "ds_read_b64_tr_b16 v[108:109], v102 offset:4096\n\t"
        "ds_read_b64_tr_b16 v[110:111], v102 offset:4352\n\t"
        "s_lshl_b32 s34, s43, 2\n\t"
        "s_add_u32 s34, s34, 9\n\t"
        "s_and_b32 s34, s34, 15\n\t"
        "s_lshl_b32 s34, s34, 19\n\t"
        "buffer_load_dwordx4 v[128:131], v96, s[16:19], s34 offen nt\n\t"
        "s_add_u32 s34, s34, 0x10000\n\t"
        "buffer_load_dwordx4 v[132:135], v96, s[16:19], s34 offen nt\n\t"
        "s_add_u32 s34, s34, 0x10000\n\t"
        "buffer_load_dwordx4 v[136:139], v96, s[16:19], s34 offen nt\n\t"
        "s_add_u32 s34, s34, 0x10000\n\t"
        "buffer_load_dwordx4 v[140:143], v96, s[16:19], s34 offen nt\n\t"
        "s_add_u32 s34, s34, 0x10000\n\t"
        "buffer_load_dwordx4 v[144:147], v96, s[16:19], s34 offen nt\n\t"
        "s_add_u32 s34, s34, 0x10000\n\t"
        "buffer_load_dwordx4 v[148:151], v96, s[16:19], s34 offen nt\n\t"
        "s_add_u32 s34, s34, 0x10000\n\t"
        "buffer_load_dwordx4 v[152:155], v96, s[16:19], s34 offen nt\n\t"
        "s_add_u32 s34, s34, 0x10000\n\t"
        "buffer_load_dwordx4 v[156:159], v96, s[16:19], s34 offen nt\n\t"
        "s_waitcnt lgkmcnt(0)\n\t"
        "ds_write_b128 v126, v[104:107] offset:4096\n\t"
        "ds_write_b128 v126, v[108:111] offset:5120\n\t"
        "s_waitcnt vmcnt(16)\n\t"
        "ds_write_b128 v97, a[192:195] offset:0\n\t"
        "ds_write_b128 v98, a[196:199] offset:1024\n\t"
        "ds_write_b128 v97, a[200:203] offset:2048\n\t"
        "ds_write_b128 v98, a[204:207] offset:3072\n\t"
        "ds_write_b128 v97, a[208:211] offset:4096\n\t"
        "ds_write_b128 v98, a[212:215] offset:5120\n\t"
        "ds_write_b128 v97, a[216:219] offset:6144\n\t"
        "ds_write_b128 v98, a[220:223] offset:7168\n\t"
        "ds_read_b64_tr_b16 a[168:169], v99 offset:0\n\t"
        "ds_read_b64_tr_b16 a[170:171], v99 offset:256\n\t"
        "ds_read_b64_tr_b16 a[172:173], v99 offset:4096\n\t"
        "ds_read_b64_tr_b16 a[174:175], v99 offset:4352\n\t"
        "s_waitcnt lgkmcnt(8)\n\t"
        "ds_read_b64_tr_b16 a[176:177], v100 offset:0\n\t"
        "ds_read_b64_tr_b16 a[178:179], v100 offset:256\n\t"
        "ds_read_b64_tr_b16 a[180:181], v100 offset:4096\n\t"
        "ds_read_b64_tr_b16 a[182:183], v100 offset:4352\n\t"
        "s_waitcnt lgkmcnt(8)\n\t"
        "ds_read_b64_tr_b16 a[184:185], v101 offset:0\n\t"
        "ds_read_b64_tr_b16 a[186:187], v101 offset:256\n\t"
        "ds_read_b64_tr_b16 a[188:189], v101 offset:4096\n\t"
        "ds_read_b64_tr_b16 a[190:191], v101 offset:4352\n\t"
        "s_waitcnt lgkmcnt(8)\n\t"
        "ds_read_b64_tr_b16 v[104:105], v102 offset:0\n\t"
        "ds_read_b64_tr_b16 v[106:107], v102 offset:256\n\t"
        "ds_read_b64_tr_b16 v[108:109], v102 offset:4096\n\t"
        "ds_read_b64_tr_b16 v[110:111], v102 offset:4352\n\t"
        "s_lshl_b32 s34, s43, 2\n\t"
        "s_add_u32 s34, s34, 10\n\t"
        "s_and_b32 s34, s34, 15\n\t"
        "s_lshl_b32 s34, s34, 19\n\t"
        "buffer_load_dwordx4 v[160:163], v96, s[16:19], s34 offen nt\n\t"
        "s_add_u32 s34, s34, 0x10000\n\t"
        "buffer_load_dwordx4 v[164:167], v96, s[16:19], s34 offen nt\n\t"
        "s_add_u32 s34, s34, 0x10000\n\t"
        "buffer_load_dwordx4 v[168:171], v96, s[16:19], s34 offen nt\n\t"
        "s_add_u32 s34, s34, 0x10000\n\t"
        "buffer_load_dwordx4 v[172:175], v96, s[16:19], s34 offen nt\n\t"
        "s_add_u32 s34, s34, 0x10000\n\t"
        "buffer_load_dwordx4 v[176:179], v96, s[16:19], s34 offen nt\n\t"
        "s_add_u32 s34, s34, 0x10000\n\t"
        "buffer_load_dwordx4 v[180:183], v96, s[16:19], s34 offen nt\n\t"
        "s_add_u32 s34, s34, 0x10000\n\t"
        "buffer_load_dwordx4 v[184:187], v96, s[16:19], s34 offen nt\n\t"
        "s_add_u32 s34, s34, 0x10000\n\t"
        "buffer_load_dwordx4 v[188:191], v96, s[16:19], s34 offen nt\n\t"
        "s_waitcnt lgkmcnt(0)\n\t"
        "ds_write_b128 v126, v[104:107] offset:6144\n\t"
        "ds_write_b128 v126, v[108:111] offset:7168\n\t"
        "s_waitcnt vmcnt(16)\n\t"
        "ds_write_b128 v97, a[224:227] offset:0\n\t"
        "ds_write_b128 v98, a[228:231] offset:1024\n\t"
        "ds_write_b128 v97, a[232:235] offset:2048\n\t"
        "ds_write_b128 v98, a[236:239] offset:3072\n\t"
        "ds_write_b128 v97, a[240:243] offset:4096\n\t"
        "ds_write_b128 v98, a[244:247] offset:5120\n\t"
        "ds_write_b128 v97, a[248:251] offset:6144\n\t"
        "ds_write_b128 v98, a[252:255] offset:7168\n\t"
        "ds_read_b64_tr_b16 a[192:193], v99 offset:0\n\t"
        "ds_read_b64_tr_b16 a[194:195], v99 offset:256\n\t"
        "ds_read_b64_tr_b16 a[196:197], v99 offset:4096\n\t"
        "ds_read_b64_tr_b16 a[198:199], v99 offset:4352\n\t"
        "s_waitcnt lgkmcnt(8)\n\t"
        "ds_read_b64_tr_b16 a[200:201], v100 offset:0\n\t"
        "ds_read_b64_tr_b16 a[202:203], v100 offset:256\n\t"
        "ds_read_b64_tr_b16 a[204:205], v100 offset:4096\n\t"
        "ds_read_b64_tr_b16 a[206:207], v100 offset:4352\n\t"
        "s_waitcnt lgkmcnt(8)\n\t"
        "ds_read_b64_tr_b16 a[208:209], v101 offset:0\n\t"
        "ds_read_b64_tr_b16 a[210:211], v101 offset:256\n\t"
        "ds_read_b64_tr_b16 a[212:213], v101 offset:4096\n\t"
        "ds_read_b64_tr_b16 a[214:215], v101 offset:4352\n\t"
        "s_waitcnt lgkmcnt(8)\n\t"
        "ds_read_b64_tr_b16 v[104:105], v102 offset:0\n\t"
        "ds_read_b64_tr_b16 v[106:107], v102 offset:256\n\t"
        "ds_read_b64_tr_b16 v[108:109], v102 offset:4096\n\t"
        "ds_read_b64_tr_b16 v[110:111], v102 offset:4352\n\t"
        "s_lshl_b32 s34, s43, 2\n\t"
        "s_add_u32 s34, s34, 11\n\t"
        "s_and_b32 s34, s34, 15\n\t"
        "s_lshl_b32 s34, s34, 19\n\t"
        "buffer_load_dwordx4 v[192:195], v96, s[16:19], s34 offen nt\n\t"
        "s_add_u32 s34, s34, 0x10000\n\t"
        "buffer_load_dwordx4 v[196:199], v96, s[16:19], s34 offen nt\n\t"
        "s_add_u32 s34, s34, 0x10000\n\t"
        "buffer_load_dwordx4 v[200:203], v96, s[16:19], s34 offen nt\n\t"
        "s_add_u32 s34, s34, 0x10000\n\t"
        "buffer_load_dwordx4 v[204:207], v96, s[16:19], s34 offen nt\n\t"
        "s_add_u32 s34, s34, 0x10000\n\t"
        "buffer_load_dwordx4 v[208:211], v96, s[16:19], s34 offen nt\n\t"
        "s_add_u32 s34, s34, 0x10000\n\t"
        "buffer_load_dwordx4 v[212:215], v96, s[16:19], s34 offen nt\n\t"
        "s_add_u32 s34, s34, 0x10000\n\t"
        "buffer_load_dwordx4 v[216:219], v96, s[16:19], s34 offen nt\n\t"
        "s_add_u32 s34, s34, 0x10000\n\t"
        "buffer_load_dwordx4 v[220:223], v96, s[16:19], s34 offen nt\n\t"
        "s_waitcnt lgkmcnt(0)\n\t"
        "ds_write_b128 v117, v[104:107] offset:8192\n\t"
        "ds_write_b128 v117, v[108:111] offset:9216\n\t"
        "s_waitcnt vmcnt(16)\n\t"
        "ds_write_b128 v97, v[128:131] offset:0\n\t"
        "ds_write_b128 v98, v[132:135] offset:1024\n\t"
        "ds_write_b128 v97, v[136:139] offset:2048\n\t"
        "ds_write_b128 v98, v[140:143] offset:3072\n\t"
        "ds_write_b128 v97, v[144:147] offset:4096\n\t"
        "ds_write_b128 v98, v[148:151] offset:5120\n\t"
        "ds_write_b128 v97, v[152:155] offset:6144\n\t"
        "ds_write_b128 v98, v[156:159] offset:7168\n\t"
        "ds_read_b64_tr_b16 a[216:217], v99 offset:0\n\t"
        "ds_read_b64_tr_b16 a[218:219], v99 offset:256\n\t"
        "ds_read_b64_tr_b16 a[220:221], v99 offset:4096\n\t"
        "ds_read_b64_tr_b16 a[222:223], v99 offset:4352\n\t"
        "s_waitcnt lgkmcnt(8)\n\t"
        "ds_read_b64_tr_b16 a[224:225], v100 offset:0\n\t"
        "ds_read_b64_tr_b16 a[226:227], v100 offset:256\n\t"
        "ds_read_b64_tr_b16 a[228:229], v100 offset:4096\n\t"
        "ds_read_b64_tr_b16 a[230:231], v100 offset:4352\n\t"
        "s_waitcnt lgkmcnt(8)\n\t"
        "ds_read_b64_tr_b16 a[232:233], v101 offset:0\n\t"
        "ds_read_b64_tr_b16 a[234:235], v101 offset:256\n\t"
        "ds_read_b64_tr_b16 a[236:237], v101 offset:4096\n\t"
        "ds_read_b64_tr_b16 a[238:239], v101 offset:4352\n\t"
        "s_waitcnt lgkmcnt(8)\n\t"
        "ds_read_b64_tr_b16 v[104:105], v102 offset:0\n\t"
        "ds_read_b64_tr_b16 v[106:107], v102 offset:256\n\t"
        "ds_read_b64_tr_b16 v[108:109], v102 offset:4096\n\t"
        "ds_read_b64_tr_b16 v[110:111], v102 offset:4352\n\t"
        "s_lshl_b32 s34, s43, 2\n\t"
        "s_add_u32 s34, s34, 12\n\t"
        "s_and_b32 s34, s34, 15\n\t"
        "s_lshl_b32 s34, s34, 19\n\t"
        "buffer_load_dwordx4 v[224:227], v96, s[16:19], s34 offen nt\n\t"
        "s_add_u32 s34, s34, 0x10000\n\t"
        "buffer_load_dwordx4 v[228:231], v96, s[16:19], s34 offen nt\n\t"
        "s_add_u32 s34, s34, 0x10000\n\t"
        "buffer_load_dwordx4 v[232:235], v96, s[16:19], s34 offen nt\n\t"
        "s_add_u32 s34, s34, 0x10000\n\t"
        "buffer_load_dwordx4 v[236:239], v96, s[16:19], s34 offen nt\n\t"
        "s_add_u32 s34, s34, 0x10000\n\t"
        "buffer_load_dwordx4 v[240:243], v96, s[16:19], s34 offen nt\n\t"
        "s_add_u32 s34, s34, 0x10000\n\t"
        "buffer_load_dwordx4 v[244:247], v96, s[16:19], s34 offen nt\n\t"
        "s_add_u32 s34, s34, 0x10000\n\t"
        "buffer_load_dwordx4 v[248:251], v96, s[16:19], s34 offen nt\n\t"
        "s_add_u32 s34, s34, 0x10000\n\t"
        "buffer_load_dwordx4 v[252:255], v96, s[16:19], s34 offen nt\n\t"
        "s_waitcnt lgkmcnt(0)\n\t"
        "ds_write_b128 v117, v[104:107] offset:10240\n\t"
        "ds_write_b128 v117, v[108:111] offset:11264\n\t"
        "s_waitcnt vmcnt(16)\n\t"
        "ds_write_b128 v97, v[160:163] offset:0\n\t"
        "ds_write_b128 v98, v[164:167] offset:1024\n\t"
        "ds_write_b128 v97, v[168:171] offset:2048\n\t"
        "ds_write_b128 v98, v[172:175] offset:3072\n\t"
        "ds_write_b128 v97, v[176:179] offset:4096\n\t"
        "ds_write_b128 v98, v[180:183] offset:5120\n\t"
        "ds_write_b128 v97, v[184:187] offset:6144\n\t"
        "ds_write_b128 v98, v[188:191] offset:7168\n\t"
        "ds_read_b64_tr_b16 a[240:241], v99 offset:0\n\t"
        "ds_read_b64_tr_b16 a[242:243], v99 offset:256\n\t"
        "ds_read_b64_tr_b16 a[244:245], v99 offset:4096\n\t"
        "ds_read_b64_tr_b16 a[246:247], v99 offset:4352\n\t"
        "s_waitcnt lgkmcnt(8)\n\t"
        "ds_read_b64_tr_b16 a[248:249], v100 offset:0\n\t"
        "ds_read_b64_tr_b16 a[250:251], v100 offset:256\n\t"
        "ds_read_b64_tr_b16 a[252:253], v100 offset:4096\n\t"
        "ds_read_b64_tr_b16 a[254:255], v100 offset:4352\n\t"
        "s_waitcnt lgkmcnt(8)\n\t"
        "ds_read_b64_tr_b16 v[128:129], v101 offset:0\n\t"
        "ds_read_b64_tr_b16 v[130:131], v101 offset:256\n\t"
        "ds_read_b64_tr_b16 v[132:133], v101 offset:4096\n\t"
        "ds_read_b64_tr_b16 v[134:135], v101 offset:4352\n\t"
        "s_waitcnt lgkmcnt(8)\n\t"
        "ds_read_b64_tr_b16 v[104:105], v102 offset:0\n\t"
        "ds_read_b64_tr_b16 v[106:107], v102 offset:256\n\t"
        "ds_read_b64_tr_b16 v[108:109], v102 offset:4096\n\t"
        "ds_read_b64_tr_b16 v[110:111], v102 offset:4352\n\t"
        "s_lshl_b32 s34, s43, 2\n\t"
        "s_add_u32 s34, s34, 13\n\t"
        "s_and_b32 s34, s34, 15\n\t"
        "s_lshl_b32 s34, s34, 19\n\t"
        "buffer_load_dwordx4 v[0:3], v96, s[16:19], s34 offen nt\n\t"
        "s_add_u32 s34, s34, 0x10000\n\t"
        "buffer_load_dwordx4 v[4:7], v96, s[16:19], s34 offen nt\n\t"
        "s_add_u32 s34, s34, 0x10000\n\t"
        "buffer_load_dwordx4 v[8:11], v96, s[16:19], s34 offen nt\n\t"
        "s_add_u32 s34, s34, 0x10000\n\t"
        "buffer_load_dwordx4 v[12:15], v96, s[16:19], s34 offen nt\n\t"
        "s_add_u32 s34, s34, 0x10000\n\t"
        "buffer_load_dwordx4 v[16:19], v96, s[16:19], s34 offen nt\n\t"
        "s_add_u32 s34, s34, 0x10000\n\t"
        "buffer_load_dwordx4 v[20:23], v96, s[16:19], s34 offen nt\n\t"
        "s_add_u32 s34, s34, 0x10000\n\t"
        "buffer_load_dwordx4 v[24:27], v96, s[16:19], s34 offen nt\n\t"
        "s_add_u32 s34, s34, 0x10000\n\t"
        "buffer_load_dwordx4 v[28:31], v96, s[16:19], s34 offen nt\n\t"
        "s_waitcnt lgkmcnt(0)\n\t"
        "ds_write_b128 v117, v[104:107] offset:12288\n\t"
        "ds_write_b128 v117, v[108:111] offset:13312\n\t"
        "s_waitcnt vmcnt(16)\n\t"
        "ds_write_b128 v97, v[192:195] offset:0\n\t"
        "ds_write_b128 v98, v[196:199] offset:1024\n\t"
        "ds_write_b128 v97, v[200:203] offset:2048\n\t"
        "ds_write_b128 v98, v[204:207] offset:3072\n\t"
        "ds_write_b128 v97, v[208:211] offset:4096\n\t"
        "ds_write_b128 v98, v[212:215] offset:5120\n\t"
        "ds_write_b128 v97, v[216:219] offset:6144\n\t"
        "ds_write_b128 v98, v[220:223] offset:7168\n\t"
        "ds_read_b64_tr_b16 v[136:137], v99 offset:0\n\t"
        "ds_read_b64_tr_b16 v[138:139], v99 offset:256\n\t"
        "ds_read_b64_tr_b16 v[140:141], v99 offset:4096\n\t"
        "ds_read_b64_tr_b16 v[142:143], v99 offset:4352\n\t"
        "s_waitcnt lgkmcnt(8)\n\t"
        "ds_read_b64_tr_b16 v[144:145], v100 offset:0\n\t"
        "ds_read_b64_tr_b16 v[146:147], v100 offset:256\n\t"
        "ds_read_b64_tr_b16 v[148:149], v100 offset:4096\n\t"
        "ds_read_b64_tr_b16 v[150:151], v100 offset:4352\n\t"
        "s_waitcnt lgkmcnt(8)\n\t"
        "ds_read_b64_tr_b16 v[152:153], v101 offset:0\n\t"
        "ds_read_b64_tr_b16 v[154:155], v101 offset:256\n\t"
        "ds_read_b64_tr_b16 v[156:157], v101 offset:4096\n\t"
        "ds_read_b64_tr_b16 v[158:159], v101 offset:4352\n\t"
        "s_waitcnt lgkmcnt(8)\n\t"
        "ds_read_b64_tr_b16 v[104:105], v102 offset:0\n\t"
        "ds_read_b64_tr_b16 v[106:107], v102 offset:256\n\t"
        "ds_read_b64_tr_b16 v[108:109], v102 offset:4096\n\t"
        "ds_read_b64_tr_b16 v[110:111], v102 offset:4352\n\t"
        "s_lshl_b32 s34, s43, 2\n\t"
        "s_add_u32 s34, s34, 14\n\t"
        "s_and_b32 s34, s34, 15\n\t"
        "s_lshl_b32 s34, s34, 19\n\t"
        "buffer_load_dwordx4 v[32:35], v96, s[16:19], s34 offen nt\n\t"
        "s_add_u32 s34, s34, 0x10000\n\t"
        "buffer_load_dwordx4 v[36:39], v96, s[16:19], s34 offen nt\n\t"
        "s_add_u32 s34, s34, 0x10000\n\t"
        "buffer_load_dwordx4 v[40:43], v96, s[16:19], s34 offen nt\n\t"
        "s_add_u32 s34, s34, 0x10000\n\t"
        "buffer_load_dwordx4 v[44:47], v96, s[16:19], s34 offen nt\n\t"
        "s_add_u32 s34, s34, 0x10000\n\t"
        "buffer_load_dwordx4 v[48:51], v96, s[16:19], s34 offen nt\n\t"
        "s_add_u32 s34, s34, 0x10000\n\t"
        "buffer_load_dwordx4 v[52:55], v96, s[16:19], s34 offen nt\n\t"
        "s_add_u32 s34, s34, 0x10000\n\t"
        "buffer_load_dwordx4 v[56:59], v96, s[16:19], s34 offen nt\n\t"
        "s_add_u32 s34, s34, 0x10000\n\t"
        "buffer_load_dwordx4 v[60:63], v96, s[16:19], s34 offen nt\n\t"
        "s_waitcnt lgkmcnt(0)\n\t"
        "ds_write_b128 v117, v[104:107] offset:14336\n\t"
        "ds_write_b128 v117, v[108:111] offset:15360\n\t"
        "s_waitcnt vmcnt(16)\n\t"
        "ds_write_b128 v97, v[224:227] offset:0\n\t"
        "ds_write_b128 v98, v[228:231] offset:1024\n\t"
        "ds_write_b128 v97, v[232:235] offset:2048\n\t"
        "ds_write_b128 v98, v[236:239] offset:3072\n\t"
        "ds_write_b128 v97, v[240:243] offset:4096\n\t"
        "ds_write_b128 v98, v[244:247] offset:5120\n\t"
        "ds_write_b128 v97, v[248:251] offset:6144\n\t"
        "ds_write_b128 v98, v[252:255] offset:7168\n\t"
        "ds_read_b64_tr_b16 v[160:161], v99 offset:0\n\t"
        "ds_read_b64_tr_b16 v[162:163], v99 offset:256\n\t"
        "ds_read_b64_tr_b16 v[164:165], v99 offset:4096\n\t"
        "ds_read_b64_tr_b16 v[166:167], v99 offset:4352\n\t"
        "s_waitcnt lgkmcnt(8)\n\t"
        "ds_read_b64_tr_b16 v[168:169], v100 offset:0\n\t"
        "ds_read_b64_tr_b16 v[170:171], v100 offset:256\n\t"
        "ds_read_b64_tr_b16 v[172:173], v100 offset:4096\n\t"
        "ds_read_b64_tr_b16 v[174:175], v100 offset:4352\n\t"
        "s_waitcnt lgkmcnt(8)\n\t"
        "ds_read_b64_tr_b16 v[176:177], v101 offset:0\n\t"
        "ds_read_b64_tr_b16 v[178:179], v101 offset:256\n\t"
        "ds_read_b64_tr_b16 v[180:181], v101 offset:4096\n\t"
        "ds_read_b64_tr_b16 v[182:183], v101 offset:4352\n\t"
        "s_waitcnt lgkmcnt(8)\n\t"
        "ds_read_b64_tr_b16 v[104:105], v102 offset:0\n\t"
        "ds_read_b64_tr_b16 v[106:107], v102 offset:256\n\t"
        "ds_read_b64_tr_b16 v[108:109], v102 offset:4096\n\t"
        "ds_read_b64_tr_b16 v[110:111], v102 offset:4352\n\t"
        "s_lshl_b32 s34, s43, 2\n\t"
        "s_add_u32 s34, s34, 15\n\t"
        "s_and_b32 s34, s34, 15\n\t"
        "s_lshl_b32 s34, s34, 19\n\t"
        "buffer_load_dwordx4 v[64:67], v96, s[16:19], s34 offen nt\n\t"
        "s_add_u32 s34, s34, 0x10000\n\t"
        "buffer_load_dwordx4 v[68:71], v96, s[16:19], s34 offen nt\n\t"
        "s_add_u32 s34, s34, 0x10000\n\t"
        "buffer_load_dwordx4 v[72:75], v96, s[16:19], s34 offen nt\n\t"
        "s_add_u32 s34, s34, 0x10000\n\t"
        "buffer_load_dwordx4 v[76:79], v96, s[16:19], s34 offen nt\n\t"
        "s_add_u32 s34, s34, 0x10000\n\t"
        "buffer_load_dwordx4 v[80:83], v96, s[16:19], s34 offen nt\n\t"
        "s_add_u32 s34, s34, 0x10000\n\t"
        "buffer_load_dwordx4 v[84:87], v96, s[16:19], s34 offen nt\n\t"
        "s_add_u32 s34, s34, 0x10000\n\t"
        "buffer_load_dwordx4 v[88:91], v96, s[16:19], s34 offen nt\n\t"
        "s_add_u32 s34, s34, 0x10000\n\t"
        "buffer_load_dwordx4 v[92:95], v96, s[16:19], s34 offen nt\n\t"
        "s_waitcnt lgkmcnt(0)\n\t"
        "ds_write_b128 v117, v[104:107] offset:16384\n\t"
        "ds_write_b128 v117, v[108:111] offset:17408\n\t"
        "s_waitcnt vmcnt(16)\n\t"
        "ds_write_b128 v97, v[0:3] offset:0\n\t"
        "ds_write_b128 v98, v[4:7] offset:1024\n\t"
        "ds_write_b128 v97, v[8:11] offset:2048\n\t"
        "ds_write_b128 v98, v[12:15] offset:3072\n\t"
        "ds_write_b128 v97, v[16:19] offset:4096\n\t"
        "ds_write_b128 v98, v[20:23] offset:5120\n\t"
        "ds_write_b128 v97, v[24:27] offset:6144\n\t"
        "ds_write_b128 v98, v[28:31] offset:7168\n\t"
        "ds_read_b64_tr_b16 v[184:185], v99 offset:0\n\t"
        "ds_read_b64_tr_b16 v[186:187], v99 offset:256\n\t"
        "ds_read_b64_tr_b16 v[188:189], v99 offset:4096\n\t"
        "ds_read_b64_tr_b16 v[190:191], v99 offset:4352\n\t"
        "s_waitcnt lgkmcnt(8)\n\t"
        "ds_read_b64_tr_b16 v[192:193], v100 offset:0\n\t"
        "ds_read_b64_tr_b16 v[194:195], v100 offset:256\n\t"
        "ds_read_b64_tr_b16 v[196:197], v100 offset:4096\n\t"
        "ds_read_b64_tr_b16 v[198:199], v100 offset:4352\n\t"
        "s_waitcnt lgkmcnt(8)\n\t"
        "ds_read_b64_tr_b16 v[200:201], v101 offset:0\n\t"
        "ds_read_b64_tr_b16 v[202:203], v101 offset:256\n\t"
        "ds_read_b64_tr_b16 v[204:205], v101 offset:4096\n\t"
        "ds_read_b64_tr_b16 v[206:207], v101 offset:4352\n\t"
        "s_waitcnt lgkmcnt(8)\n\t"
        "ds_read_b64_tr_b16 v[104:105], v102 offset:0\n\t"
        "ds_read_b64_tr_b16 v[106:107], v102 offset:256\n\t"
        "ds_read_b64_tr_b16 v[108:109], v102 offset:4096\n\t"
        "ds_read_b64_tr_b16 v[110:111], v102 offset:4352\n\t"
        "s_waitcnt lgkmcnt(0)\n\t"
        "ds_write_b128 v117, v[104:107] offset:18432\n\t"
        "ds_write_b128 v117, v[108:111] offset:19456\n\t"
        "s_waitcnt vmcnt(8)\n\t"
        "ds_write_b128 v97, v[32:35] offset:0\n\t"
        "ds_write_b128 v98, v[36:39] offset:1024\n\t"
        "ds_write_b128 v97, v[40:43] offset:2048\n\t"
        "ds_write_b128 v98, v[44:47] offset:3072\n\t"
        "ds_write_b128 v97, v[48:51] offset:4096\n\t"
        "ds_write_b128 v98, v[52:55] offset:5120\n\t"
        "ds_write_b128 v97, v[56:59] offset:6144\n\t"
        "ds_write_b128 v98, v[60:63] offset:7168\n\t"
        "ds_read_b64_tr_b16 v[208:209], v99 offset:0\n\t"
        "ds_read_b64_tr_b16 v[210:211], v99 offset:256\n\t"
        "ds_read_b64_tr_b16 v[212:213], v99 offset:4096\n\t"
        "ds_read_b64_tr_b16 v[214:215], v99 offset:4352\n\t"
        "s_waitcnt lgkmcnt(8)\n\t"
        "ds_read_b64_tr_b16 v[216:217], v100 offset:0\n\t"
        "ds_read_b64_tr_b16 v[218:219], v100 offset:256\n\t"
        "ds_read_b64_tr_b16 v[220:221], v100 offset:4096\n\t"
        "ds_read_b64_tr_b16 v[222:223], v100 offset:4352\n\t"
        "s_waitcnt lgkmcnt(8)\n\t"
        "ds_read_b64_tr_b16 v[224:225], v101 offset:0\n\t"
        "ds_read_b64_tr_b16 v[226:227], v101 offset:256\n\t"
        "ds_read_b64_tr_b16 v[228:229], v101 offset:4096\n\t"
        "ds_read_b64_tr_b16 v[230:231], v101 offset:4352\n\t"
        "s_waitcnt lgkmcnt(8)\n\t"
        "ds_read_b64_tr_b16 v[104:105], v102 offset:0\n\t"
        "ds_read_b64_tr_b16 v[106:107], v102 offset:256\n\t"
        "ds_read_b64_tr_b16 v[108:109], v102 offset:4096\n\t"
        "ds_read_b64_tr_b16 v[110:111], v102 offset:4352\n\t"
        "s_mov_b32 s35, 33792\n\t"
        "buffer_load_dword v0, off, s[12:15], s35 sc1\n\t"
        "s_waitcnt lgkmcnt(0)\n\t"
        "ds_write_b128 v117, v[104:107] offset:20480\n\t"
        "ds_write_b128 v117, v[108:111] offset:21504\n\t"
        "s_waitcnt vmcnt(0)\n\t"
        "ds_write_b128 v97, v[64:67] offset:0\n\t"
        "ds_write_b128 v98, v[68:71] offset:1024\n\t"
        "ds_write_b128 v97, v[72:75] offset:2048\n\t"
        "ds_write_b128 v98, v[76:79] offset:3072\n\t"
        "ds_write_b128 v97, v[80:83] offset:4096\n\t"
        "ds_write_b128 v98, v[84:87] offset:5120\n\t"
        "ds_write_b128 v97, v[88:91] offset:6144\n\t"
        "ds_write_b128 v98, v[92:95] offset:7168\n\t"
        "ds_read_b64_tr_b16 v[232:233], v99 offset:0\n\t"
        "ds_read_b64_tr_b16 v[234:235], v99 offset:256\n\t"
        "ds_read_b64_tr_b16 v[236:237], v99 offset:4096\n\t"
        "ds_read_b64_tr_b16 v[238:239], v99 offset:4352\n\t"
        "s_waitcnt lgkmcnt(8)\n\t"
        "ds_read_b64_tr_b16 v[240:241], v100 offset:0\n\t"
        "ds_read_b64_tr_b16 v[242:243], v100 offset:256\n\t"
        "ds_read_b64_tr_b16 v[244:245], v100 offset:4096\n\t"
        "ds_read_b64_tr_b16 v[246:247], v100 offset:4352\n\t"
        "s_waitcnt lgkmcnt(8)\n\t"
        "ds_read_b64_tr_b16 v[248:249], v101 offset:0\n\t"
        "ds_read_b64_tr_b16 v[250:251], v101 offset:256\n\t"
        "ds_read_b64_tr_b16 v[252:253], v101 offset:4096\n\t"
        "ds_read_b64_tr_b16 v[254:255], v101 offset:4352\n\t"
        "s_waitcnt lgkmcnt(8)\n\t"
        "ds_read_b64_tr_b16 v[104:105], v102 offset:0\n\t"
        "ds_read_b64_tr_b16 v[106:107], v102 offset:256\n\t"
        "ds_read_b64_tr_b16 v[108:109], v102 offset:4096\n\t"
        "ds_read_b64_tr_b16 v[110:111], v102 offset:4352\n\t"
        "s_mov_b32 s42, 0\n\t"
        "LA_cal%=:\n\t"
        "v_readfirstlane_b32 s34, v0\n\t"
        "s_nop 0\n\t"
        "s_cmp_lg_u32 s34, 0\n\t"
        "s_cbranch_scc1 LA_calok%=\n\t"
        "s_sleep 4\n\t"
        "s_mov_b32 s35, 33792\n\t"
        "buffer_load_dword v0, off, s[12:15], s35 sc1\n\t"
        "s_waitcnt vmcnt(0)\n\t"
        "s_add_u32 s42, s42, 1\n\t"
        "s_cmp_lt_u32 s42, 0x100000\n\t"
        "s_cbranch_scc1 LA_cal%=\n\t"
        "s_movk_i32 s34, 8\n\t"
        "s_branch LA_die%=\n\t"
        "LA_calok%=:\n\t"
        "s_lshl_b32 s35, s44, 10\n\t"
        "v_lshlrev_b32_e32 v1, 2, v122\n\t"
        "v_and_b32_e32 v1, 0x3c0, v1\n\t"
        "buffer_load_dword v1, v1, s[12:15], s35 offen offset:0 sc1\n\t"
        "s_waitcnt lgkmcnt(0)\n\t"
        "ds_write_b128 v126, v[104:107] offset:8192\n\t"
        "ds_write_b128 v126, v[108:111] offset:9216\n\t"
        "s_waitcnt lgkmcnt(0)\n\t"
        "v_mov_b32_e32 v110, v103\n\t"
        "ds_read_b128 v[64:67], v126 offset:0\n\t"
        "ds_read_b128 v[68:71], v126 offset:1024\n\t"
        "ds_read_b128 v[72:75], v126 offset:2048\n\t"
        "ds_read_b128 v[76:79], v126 offset:3072\n\t"
        "ds_read_b128 v[80:83], v126 offset:4096\n\t"
        "ds_read_b128 v[84:87], v126 offset:5120\n\t"
        "ds_read_b128 v[88:91], v126 offset:6144\n\t"
        "ds_read_b128 v[92:95], v126 offset:7168\n\t"
        "ds_read_b128 v[96:99], v126 offset:8192\n\t"
        "ds_read_b128 v[100:103], v126 offset:9216\n\t"
        "s_waitcnt lgkmcnt(0)\n\t"
        "v_mov_b32_e32 v126, 0\n\t"
        "s_barrier\n\t"
        "s_mov_b32 s48, 0\n\t"
        "s_mov_b32 s54, 0\n\t"
        "s_mov_b32 s0, 0x3f803f80\n\t"
        "s_mov_b32 s1, s0\n\t"
        "s_movk_i32 s56, 24576\n\t"
        "s_and_b32 s58, s43, 1\n\t"
        "s_lshl_b32 s58, s58, 12\n\t"
        "s_lshl_b32 s57, s44, 5\n\t"
        "s_add_u32 s57, s57, s43\n\t"
        "s_lshl_b32 s57, s57, 2\n\t"
        "s_add_u32 s57, s57, 458752\n\t"
        "s_waitcnt vmcnt(0)\n\t"
        "v_mov_b32_e32 v111, v1\n\t"
        "v_readfirstlane_b32 s34, v0\n\t"
        "s_nop 0\n\t"
        "s_and_b32 s34, s34, 8192\n\t"
        "s_add_u32 s8, s8, s34\n\t"
        "s_addc_u32 s9, s9, 0\n\t"
        "s_barrier\n\t"
        "s_cmp_lg_u32 s43, 0\n\t"
        "s_cbranch_scc1 LA_st%=\n\t"
        "v_mov_b32_e32 v2, 1\n\t"
        "s_mov_b32 s35, 34304\n\t"
        "s_mov_b64 exec, 1\n\t"
        "buffer_atomic_add v2, off, s[12:15], s35 sc0\n\t"
        "s_mov_b64 exec, -1\n\t"
        "s_waitcnt vmcnt(0)\n\t"
        "v_readfirstlane_b32 s34, v2\n\t"
        "s_nop 0\n\t"
        "s_cmp_lg_u32 s34, 95\n\t"
        "s_cbranch_scc1 LA_st%=\n\t"
        "s_memrealtime s[32:33]\n\t"
        "v_mbcnt_lo_u32_b32 v3, -1, 0\n\t"
        "v_lshlrev_b32_e32 v3, 7, v3\n\t"
        "s_waitcnt lgkmcnt(0)\n\t"
        "s_add_u32 s34, s32, 50\n\t"
        "s_or_b32 s34, s34, 1\n\t"
        "v_mov_b32_e32 v2, s34\n\t"
        "s_mov_b32 s35, 36864\n\t"
        "s_mov_b64 exec, 0xffff\n\t"
        "buffer_store_dword v2, v3, s[12:15], s35 offen sc1\n\t"
        "s_mov_b64 exec, -1\n\t"
        "LA_st%=:\n\t"
        "s_mov_b32 s24, 0\n\t"
        "s_mov_b32 s26, 0\n\t"
        "s_mov_b32 s27, 0x80000\n\t"
        "s_mov_b32 s28, 0x180000\n\t"
        "s_mov_b32 s31, 0\n\t"
        "s_mov_b32 s47, 0\n\t"
        "s_mov_b32 s38, 0xff00ff00\n\t"
        "s_mov_b32 s39, 0xff00ff00\n\t"
        "s_mov_b32 s40, 0\n\t"
        "s_mov_b32 s41, -1\n\t"
        "ds_read_b128 v[32:35], v117 offset:0\n\t"
        "ds_read_b128 v[36:39], v117 offset:1024\n\t"
        "ds_read_b128 v[40:43], v117 offset:2048\n\t"
        "ds_read_b128 v[44:47], v117 offset:3072\n\t"
        "ds_read_b128 v[48:51], v117 offset:4096\n\t"
        "ds_read_b128 v[52:55], v117 offset:5120\n\t"
        "ds_read_b128 v[56:59], v117 offset:6144\n\t"
        "ds_read_b128 v[60:63], v117 offset:7168\n\t"
        "v_mov_b64_e32 v[0:1], 0\n\t"
        "v_mov_b64_e32 v[2:3], 0\n\t"
        "v_mov_b64_e32 v[4:5], 0\n\t"
        "v_mov_b64_e32 v[6:7], 0\n\t"
        "v_mov_b64_e32 v[8:9], 0\n\t"
        "v_mov_b64_e32 v[10:11], 0\n\t"
        "v_mov_b64_e32 v[12:13], 0\n\t"
        "v_mov_b64_e32 v[14:15], 0\n\t"
        "s_lshr_b32 s59, s48, 1\n\t"
        "s_cmp_gt_u32 s54, s59\n\t"
        "s_cbranch_scc1 LA_kv0ok%=\n\t"
        "s_mov_b32 s42, 0\n\t"
        "s_waitcnt vmcnt(0)\n\t"
        "s_branch LA_kv0w%=\n\t"
        "LA_kv0%=:\n\t"
        "s_waitcnt vmcnt(0)\n\t"
        "LA_kv0w%=:\n\t"
        "v_min_u32_dpp v111, v111, v111 quad_perm:[1,0,3,2] row_mask:0xf bank_mask:0xf\n\t"
        "s_nop 1\n\t"
        "v_min_u32_dpp v111, v111, v111 quad_perm:[2,3,0,1] row_mask:0xf bank_mask:0xf\n\t"
        "s_nop 1\n\t"
        "v_min_u32_dpp v111, v111, v111 row_ror:4 row_mask:0xf bank_mask:0xf\n\t"
        "s_nop 1\n\t"
        "v_min_u32_dpp v111, v111, v111 row_ror:8 row_mask:0xf bank_mask:0xf\n\t"
        "s_nop 0\n\t"
        "v_readfirstlane_b32 s54, v111\n\t"
        "s_nop 0\n\t"
        "s_cmp_gt_u32 s54, s59\n\t"
        "s_cbranch_scc1 LA_kv0ok%=\n\t"
        "s_sleep 4\n\t"
        "s_lshl_b32 s35, s44, 10\n\t"
        "v_lshlrev_b32_e32 v111, 2, v122\n\t"
        "v_and_b32_e32 v111, 0x3c0, v111\n\t"
        "buffer_load_dword v111, v111, s[12:15], s35 offen offset:0 sc1\n\t"
        "s_add_u32 s42, s42, 1\n\t"
        "s_cmp_lt_u32 s42, 0x100000\n\t"
        "s_cbranch_scc1 LA_kv0%=\n\t"
        "s_movk_i32 s34, 16\n\t"
        "s_branch LA_die%=\n\t"
        "LA_kv0ok%=:\n\t"
        "s_and_b32 s36, s48, 1\n\t"
        "s_mul_i32 s36, s36, 24576\n\t"
        "s_add_u32 s36, s36, s55\n\t"
        "s_lshl_b32 s37, s48, 18\n\t"
        "s_lshl_b32 s34, s43, 11\n\t"
        "s_add_u32 s37, s37, s34\n\t"
        "s_add_u32 m0, s36, 0\n\t"
        "s_add_u32 s34, s37, 29548544\n\t"
        "buffer_load_dwordx4 v122, s[12:15], s34 offen sc1 lds\n\t"
        "s_add_u32 m0, s36, 1024\n\t"
        "s_add_u32 s34, s37, 29549568\n\t"
        "buffer_load_dwordx4 v122, s[12:15], s34 offen sc1 lds\n\t"
        "s_add_u32 m0, s36, 8192\n\t"
        "s_add_u32 s34, s37, 12771328\n\t"
        "buffer_load_dwordx4 v122, s[12:15], s34 offen sc1 lds\n\t"
        "s_add_u32 m0, s36, 9216\n\t"
        "s_add_u32 s34, s37, 12772352\n\t"
        "buffer_load_dwordx4 v122, s[12:15], s34 offen sc1 lds\n\t"
        "s_add_u32 m0, s36, 16384\n\t"
        "s_add_u32 s34, s37, 46325760\n\t"
        "buffer_load_dwordx4 v122, s[12:15], s34 offen sc1 lds\n\t"
        "s_add_u32 m0, s36, 17408\n\t"
        "s_add_u32 s34, s37, 46326784\n\t"
        "buffer_load_dwordx4 v122, s[12:15], s34 offen sc1 lds\n\t"
        "s_mov_b32 s42, 0\n\t"
        "LA_gw%=:\n\t"
        "buffer_load_dword v16, off, s[12:15], s45 sc1\n\t"
        "s_waitcnt vmcnt(0)\n\t"
        "v_readfirstlane_b32 s25, v16\n\t"
        "s_nop 0\n\t"
        "s_cmp_lg_u32 s25, 0\n\t"
        "s_cbranch_scc1 LA_loop%=\n\t"
        "s_sleep 2\n\t"
        "s_add_u32 s42, s42, 1\n\t"
        "s_cmp_lt_u32 s42, 0x100000\n\t"
        "s_cbranch_scc1 LA_gw%=\n\t"
        "s_movk_i32 s34, 9\n\t"
        "s_branch LA_die%=\n\t"
        ".p2align 8\n\t"
        "LA_loop%=:\n\t"
        "s_mov_b32 s42, 0\n\t"
        "s_sub_u32 s35, s25, 8\n\t"
        "s_waitcnt lgkmcnt(0)\n\t"
        "s_memrealtime s[2:3]\n\t"
        "s_memrealtime s[4:5]\n\t"
        "s_memrealtime s[6:7]\n\t"
        "LA_wa%=:\n\t"
        "s_memrealtime s[32:33]\n\t"
        "s_waitcnt lgkmcnt(6)\n\t"
        "s_sub_u32 s34, s35, s2\n\t"
        "s_cmpk_lt_u32 s34, 0x2000\n\t"
        "s_cbranch_scc0 LA_wa%=x\n\t"
        "s_memrealtime s[2:3]\n\t"
        "s_waitcnt lgkmcnt(6)\n\t"
        "s_sub_u32 s34, s35, s4\n\t"
        "s_cmpk_lt_u32 s34, 0x2000\n\t"
        "s_cbranch_scc0 LA_wa%=x\n\t"
        "s_memrealtime s[4:5]\n\t"
        "s_waitcnt lgkmcnt(6)\n\t"
        "s_sub_u32 s34, s35, s6\n\t"
        "s_cmpk_lt_u32 s34, 0x2000\n\t"
        "s_cbranch_scc0 LA_wa%=x\n\t"
        "s_memrealtime s[6:7]\n\t"
        "s_waitcnt lgkmcnt(6)\n\t"
        "s_sub_u32 s34, s35, s32\n\t"
        "s_cmpk_lt_u32 s34, 0x2000\n\t"
        "s_cbranch_scc1 LA_wa%=\n\t"
        "s_nop 3\n\t"
        "LA_wa%=x:\n\t"
        "s_add_i32 s34, s34, 4\n\t"
        "s_max_i32 s34, s34, 0\n\t"
        "s_bitcmp1_b32 s34, 1\n\t"
        "s_cbranch_scc0 LA_wa%=y\n\t"
        "s_nop 15\n\t"
        "LA_wa%=y:\n\t"
        "s_bitcmp1_b32 s34, 0\n\t"
        "s_cbranch_scc0 LA_wa%=z\n\t"
        "s_nop 9\n\t"
        "LA_wa%=z:\n\t"
        "LA_rd%=:\n\t"
        "buffer_load_dwordx4 v[16:19], v112, s[8:11], s26 offen offset:0 sc1\n\t"
        "s_nop 12\n\t"
        "buffer_load_dwordx4 v[20:23], v112, s[8:11], s26 offen offset:1024 sc1\n\t"
        "s_nop 12\n\t"
        "buffer_load_dwordx4 v[24:27], v112, s[8:11], s26 offen offset:2048 sc1\n\t"
        "s_nop 12\n\t"
        "buffer_load_dwordx4 v[28:31], v112, s[8:11], s26 offen offset:3072 sc1\n\t"
        "LA_go%=:\n\t"
        "s_waitcnt vmcnt(3)\n\t"
        "v_smfmac_f32_16x16x64_bf16 v[0:3], v[16:19], a[0:7], v110\n\t"
        "ds_write_b128 v113, v[16:19] offset:0\n\t"
        "v_smfmac_f32_16x16x64_bf16 v[4:7], v[16:19], a[8:15], v110\n\t"
        "v_max3_u32 v111, v16, v17, v18\n\t"
        "v_max_u32_e32 v111, v19, v111\n\t"
        "v_smfmac_f32_16x16x64_bf16 v[8:11], v[16:19], a[16:23], v110\n\t"
        "v_smfmac_f32_16x16x64_bf16 v[12:15], v[16:19], v[32:39], v110\n\t"
        "s_waitcnt vmcnt(2)\n\t"
        "v_smfmac_f32_16x16x64_bf16 v[0:3], v[20:23], a[24:31], v110\n\t"
        "ds_write_b128 v113, v[20:23] offset:1024\n\t"
        "v_smfmac_f32_16x16x64_bf16 v[4:7], v[20:23], a[32:39], v110\n\t"
        "v_max3_u32 v111, v20, v21, v111\n\t"
        "v_max3_u32 v111, v22, v23, v111\n\t"
        "v_smfmac_f32_16x16x64_bf16 v[8:11], v[20:23], a[40:47], v110\n\t"
        "s_waitcnt vmcnt(1)\n\t"
        "ds_write_b128 v113, v[24:27] offset:2048\n\t"
        "v_smfmac_f32_16x16x64_bf16 v[12:15], v[20:23], v[40:47], v110\n\t"
        "v_max3_u32 v111, v24, v25, v111\n\t"
        "v_max3_u32 v111, v26, v27, v111\n\t"
        "v_smfmac_f32_16x16x64_bf16 v[0:3], v[24:27], a[48:55], v110\n\t"
        "v_smfmac_f32_16x16x64_bf16 v[4:7], v[24:27], a[56:63], v110\n\t"
        "s_waitcnt vmcnt(0)\n\t"
        "ds_write_b128 v113, v[28:31] offset:3072\n\t"
        "v_max3_u32 v111, v28, v29, v111\n\t"
        "v_max3_u32 v111, v30, v31, v111\n\t"
        "v_smfmac_f32_16x16x64_bf16 v[8:11], v[24:27], a[64:71], v110\n\t"
        "v_cmp_eq_u32_e32 vcc, -1, v111\n\t"
        "v_smfmac_f32_16x16x64_bf16 v[12:15], v[24:27], v[48:55], v110\n\t"
        "s_waitcnt lgkmcnt(0)\n\t"
        "s_cbranch_vccnz LA_rdfail%=\n\t"
        "s_barrier\n\t"
        "ds_read_b128 v[16:19], v114 offset:0\n\t"
        "ds_read_b128 v[20:23], v114 offset:1024\n\t"
        "ds_read_b128 v[24:27], v114 offset:2048\n\t"
        "v_smfmac_f32_16x16x64_bf16 v[0:3], v[28:31], a[72:79], v110\n\t"
        "v_smfmac_f32_16x16x64_bf16 v[4:7], v[28:31], a[80:87], v110\n\t"
        "v_smfmac_f32_16x16x64_bf16 v[8:11], v[28:31], a[88:95], v110\n\t"
        "v_smfmac_f32_16x16x64_bf16 v[12:15], v[28:31], v[56:63], v110\n\t"
        "ds_read_b128 v[28:31], v114 offset:3072\n\t"
        "s_waitcnt lgkmcnt(3)\n\t"
        "v_smfmac_f32_16x16x64_bf16 v[12:15], v[16:19], v[64:71], v110\n\t"
        "ds_read_b128 v[32:35], v117 offset:8192\n\t"
        "v_smfmac_f32_16x16x64_bf16 v[0:3], v[16:19], a[96:103], v110\n\t"
        "ds_read_b128 v[36:39], v117 offset:9216\n\t"
        "v_smfmac_f32_16x16x64_bf16 v[4:7], v[16:19], a[104:111], v110\n\t"
        "s_waitcnt lgkmcnt(4)\n\t"
        "v_smfmac_f32_16x16x64_bf16 v[8:11], v[16:19], a[112:119], v110\n\t"
        "ds_read_b128 v[16:19], v115 offset:0\n\t"
        "v_smfmac_f32_16x16x64_bf16 v[12:15], v[20:23], v[72:79], v110\n\t"
        "ds_read_b128 v[40:43], v117 offset:10240\n\t"
        "v_smfmac_f32_16x16x64_bf16 v[0:3], v[20:23], a[120:127], v110\n\t"
        "ds_read_b128 v[44:47], v117 offset:11264\n\t"
        "v_smfmac_f32_16x16x64_bf16 v[4:7], v[20:23], a[128:135], v110\n\t"
        "s_waitcnt lgkmcnt(6)\n\t"
        "v_smfmac_f32_16x16x64_bf16 v[8:11], v[20:23], a[136:143], v110\n\t"
        "ds_read_b128 v[20:23], v115 offset:1024\n\t"
        "v_smfmac_f32_16x16x64_bf16 v[12:15], v[24:27], v[80:87], v110\n\t"
        "ds_read_b128 v[48:51], v117 offset:12288\n\t"
        "v_smfmac_f32_16x16x64_bf16 v[0:3], v[24:27], a[144:151], v110\n\t"
        "ds_read_b128 v[52:55], v117 offset:13312\n\t"
        "v_smfmac_f32_16x16x64_bf16 v[4:7], v[24:27], a[152:159], v110\n\t"
        "s_waitcnt lgkmcnt(8)\n\t"
        "v_smfmac_f32_16x16x64_bf16 v[8:11], v[24:27], a[160:167], v110\n\t"
        "ds_read_b128 v[24:27], v115 offset:2048\n\t"
        "v_smfmac_f32_16x16x64_bf16 v[12:15], v[28:31], v[88:95], v110\n\t"
        "ds_read_b128 v[56:59], v117 offset:14336\n\t"
        "v_smfmac_f32_16x16x64_bf16 v[0:3], v[28:31], a[168:175], v110\n\t"
        "ds_read_b128 v[60:63], v117 offset:15360\n\t"
        "v_smfmac_f32_16x16x64_bf16 v[4:7], v[28:31], a[176:183], v110\n\t"
        "s_waitcnt lgkmcnt(8)\n\t"
        "v_smfmac_f32_16x16x64_bf16 v[8:11], v[28:31], a[184:191], v110\n\t"
        "ds_read_b128 v[28:31], v115 offset:3072\n\t"
        "v_smfmac_f32_16x16x64_bf16 v[12:15], v[16:19], v[32:39], v110\n\t"
        "ds_read_b128 v[32:35], v117 offset:16384\n\t"
        "v_smfmac_f32_16x16x64_bf16 v[0:3], v[16:19], a[192:199], v110\n\t"
        "ds_read_b128 v[36:39], v117 offset:17408\n\t"
        "v_smfmac_f32_16x16x64_bf16 v[4:7], v[16:19], a[200:207], v110\n\t"
        "s_waitcnt lgkmcnt(8)\n\t"
        "v_smfmac_f32_16x16x64_bf16 v[8:11], v[16:19], a[208:215], v110\n\t"
        "ds_read_b128 v[16:19], v116 offset:0\n\t"
        "v_smfmac_f32_16x16x64_bf16 v[12:15], v[20:23], v[40:47], v110\n\t"
        "ds_read_b128 v[40:43], v117 offset:18432\n\t"
        "v_smfmac_f32_16x16x64_bf16 v[0:3], v[20:23], a[216:223], v110\n\t"
        "ds_read_b128 v[44:47], v117 offset:19456\n\t"
        "v_smfmac_f32_16x16x64_bf16 v[4:7], v[20:23], a[224:231], v110\n\t"
        "s_waitcnt lgkmcnt(8)\n\t"
        "v_smfmac_f32_16x16x64_bf16 v[8:11], v[20:23], a[232:239], v110\n\t"
        "ds_read_b128 v[20:23], v116 offset:1024\n\t"
        "v_smfmac_f32_16x16x64_bf16 v[12:15], v[24:27], v[48:55], v110\n\t"
        "ds_read_b128 v[48:51], v117 offset:20480\n\t"
        "v_smfmac_f32_16x16x64_bf16 v[0:3], v[24:27], a[240:247], v110\n\t"
        "ds_read_b128 v[52:55], v117 offset:21504\n\t"
        "v_smfmac_f32_16x16x64_bf16 v[4:7], v[24:27], a[248:255], v110\n\t"
        "s_waitcnt lgkmcnt(8)\n\t"
        "v_smfmac_f32_16x16x64_bf16 v[8:11], v[24:27], v[128:135], v110\n\t"
        "ds_read_b128 v[24:27], v116 offset:2048\n\t"
        "v_smfmac_f32_16x16x64_bf16 v[12:15], v[28:31], v[56:63], v110\n\t"
        "ds_read_b128 v[56:59], v124 offset:3072\n\t"
        "v_smfmac_f32_16x16x64_bf16 v[0:3], v[28:31], v[136:143], v110\n\t"
        "ds_read_b128 v[60:63], v124 offset:7168\n\t"
        "v_smfmac_f32_16x16x64_bf16 v[4:7], v[28:31], v[144:151], v110\n\t"
        "s_waitcnt lgkmcnt(8)\n\t"
        "v_smfmac_f32_16x16x64_bf16 v[8:11], v[28:31], v[152:159], v110\n\t"
        "ds_read_b128 v[28:31], v116 offset:3072\n\t"
        "v_smfmac_f32_16x16x64_bf16 v[12:15], v[16:19], v[32:39], v110\n\t"
        "ds_read_b128 v[32:35], v124 offset:0\n\t"
        "v_smfmac_f32_16x16x64_bf16 v[0:3], v[16:19], v[160:167], v110\n\t"
        "ds_read_b128 v[36:39], v124 offset:4096\n\t"
        "v_smfmac_f32_16x16x64_bf16 v[4:7], v[16:19], v[168:175], v110\n\t"
        "s_waitcnt lgkmcnt(8)\n\t"
        "v_smfmac_f32_16x16x64_bf16 v[8:11], v[16:19], v[176:183], v110\n\t"
        "v_smfmac_f32_16x16x64_bf16 v[12:15], v[20:23], v[40:47], v110\n\t"
        "ds_read_b128 v[40:43], v124 offset:1024\n\t"
        "v_smfmac_f32_16x16x64_bf16 v[8:11], v[20:23], v[200:207], v110\n\t"
        "ds_read_b128 v[44:47], v124 offset:5120\n\t"
        "s_waitcnt lgkmcnt(7)\n\t"
        "v_smfmac_f32_16x16x64_bf16 v[12:15], v[24:27], v[48:55], v110\n\t"
        "ds_read_b128 v[48:51], v124 offset:2048\n\t"
        "v_smfmac_f32_16x16x64_bf16 v[8:11], v[24:27], v[224:231], v110\n\t"
        "ds_read_b128 v[52:55], v124 offset:6144\n\t"
        "s_waitcnt lgkmcnt(6)\n\t"
        "v_smfmac_f32_16x16x64_bf16 v[12:15], v[28:31], v[96:103], v110\n\t"
        "v_smfmac_f32_16x16x64_bf16 v[8:11], v[28:31], v[248:255], v110\n\t"
        "v_smfmac_f32_16x16x64_bf16 v[0:3], v[20:23], v[184:191], v110\n\t"
        "s_lshl_b32 s34, s43, 11\n\t"
        "v_add_u32_e32 v108, s34, v124\n\t"
        "v_smfmac_f32_16x16x64_bf16 v[0:3], v[24:27], v[208:215], v110\n\t"
        "v_add_u32_e32 v109, s58, v124\n\t"
        "v_smfmac_f32_16x16x64_bf16 v[4:7], v[20:23], v[192:199], v110\n\t"
        "v_smfmac_f32_16x16x64_bf16 v[0:3], v[28:31], v[232:239], v110\n\t"
        "v_smfmac_f32_16x16x64_bf16 v[4:7], v[24:27], v[216:223], v110\n\t"
        "buffer_load_dwordx4 v[104:107], v120, s[8:11], s26 offen sc1\n\t"
        "v_smfmac_f32_16x16x64_bf16 v[4:7], v[28:31], v[240:247], v110\n\t"
        "v_add_f32_e32 v12, v12, v13\n\t"
        "v_add_f32_e32 v14, v14, v15\n\t"
        "v_cvt_pk_bf16_f32 v12, v12, v14\n\t"
        "v_add_f32_e32 v8, v8, v9\n\t"
        "v_add_f32_e32 v10, v10, v11\n\t"
        "v_cvt_pk_bf16_f32 v8, v8, v10\n\t"
        "ds_write2_b32 v118, v8, v12 offset0:64 offset1:96\n\t"
        "v_add_f32_e32 v0, v0, v1\n\t"
        "v_add_f32_e32 v2, v2, v3\n\t"
        "v_cvt_pk_bf16_f32 v0, v0, v2\n\t"
        "v_add_f32_e32 v4, v4, v5\n\t"
        "v_add_f32_e32 v6, v6, v7\n\t"
        "v_cvt_pk_bf16_f32 v4, v4, v6\n\t"
        "ds_write2_b32 v118, v0, v4 offset0:0 offset1:32\n\t"
        "s_cmp_eq_u32 s24, 63\n\t"
        "s_waitcnt lgkmcnt(0)\n\t"
        "s_barrier\n\t"
        "s_cbranch_scc1 LA_skip%=\n\t"
        "ds_read_b64_tr_b16 v[16:17], v119 offset:0\n\t"
        "ds_read_b64_tr_b16 v[18:19], v119 offset:32\n\t"
        "ds_read_b64_tr_b16 v[20:21], v119 offset:256\n\t"
        "ds_read_b64_tr_b16 v[22:23], v119 offset:288\n\t"
        "ds_read_b64_tr_b16 v[24:25], v119 offset:512\n\t"
        "ds_read_b64_tr_b16 v[26:27], v119 offset:544\n\t"
        "ds_read_b64_tr_b16 v[28:29], v119 offset:768\n\t"
        "ds_read_b64_tr_b16 v[30:31], v119 offset:800\n\t"
        "buffer_store_dword v126, v127, s[12:15], 0 offen sc1\n\t"
        "s_waitcnt lgkmcnt(6)\n\t"
        "v_mfma_f32_16x16x32_bf16 v[0:3], v[32:35], v[16:19], 0\n\t"
        "ds_read_b64_tr_b16 v[32:33], v119 offset:2112\n\t"
        "ds_read_b64_tr_b16 v[34:35], v119 offset:2144\n\t"
        "v_mfma_f32_16x16x32_bf16 v[4:7], v[36:39], v[16:19], 0\n\t"
        "ds_read_b64_tr_b16 v[36:37], v119 offset:2368\n\t"
        "ds_read_b64_tr_b16 v[38:39], v119 offset:2400\n\t"
        "s_waitcnt lgkmcnt(8)\n\t"
        "v_mfma_f32_16x16x32_bf16 v[0:3], v[40:43], v[20:23], v[0:3]\n\t"
        "ds_read_b64_tr_b16 v[40:41], v119 offset:2624\n\t"
        "ds_read_b64_tr_b16 v[42:43], v119 offset:2656\n\t"
        "v_mfma_f32_16x16x32_bf16 v[4:7], v[44:47], v[20:23], v[4:7]\n\t"
        "ds_read_b64_tr_b16 v[44:45], v119 offset:2880\n\t"
        "ds_read_b64_tr_b16 v[46:47], v119 offset:2912\n\t"
        "s_waitcnt lgkmcnt(10)\n\t"
        "v_mfma_f32_16x16x32_bf16 v[0:3], v[48:51], v[24:27], v[0:3]\n\t"
        "ds_read_b128 v[48:51], v108 offset:16384\n\t"
        "v_mfma_f32_16x16x32_bf16 v[4:7], v[52:55], v[24:27], v[4:7]\n\t"
        "ds_read_b128 v[52:55], v108 offset:17408\n\t"
        "s_waitcnt lgkmcnt(10)\n\t"
        "v_mfma_f32_16x16x32_bf16 v[0:3], v[56:59], v[28:31], v[0:3]\n\t"
        "v_mfma_f32_16x16x32_bf16 v[4:7], v[60:63], v[28:31], v[4:7]\n\t"
        "s_waitcnt lgkmcnt(2)\n\t"
        "v_mfma_f32_16x16x32_bf16 v[8:11], v[32:35], v[16:19], 0\n\t"
        "s_nop 1\n\t"
        "v_mfma_f32_16x16x32_bf16 v[8:11], v[36:39], v[20:23], v[8:11]\n\t"
        "v_mov_b64_e32 v[16:17], s[0:1]\n\t"
        "v_mov_b64_e32 v[18:19], s[0:1]\n\t"
        "v_mfma_f32_16x16x32_bf16 v[8:11], v[40:43], v[24:27], v[8:11]\n\t"
        "v_max3_f32 v59, v0, v1, v2\n\t"
        "v_max3_f32 v59, v59, v3, v4\n\t"
        "v_mfma_f32_16x16x32_bf16 v[8:11], v[44:47], v[28:31], v[8:11]\n\t"
        "v_max3_f32 v59, v59, v5, v6\n\t"
        "v_max_f32_e32 v60, v59, v7\n\t"
        "v_max_f32_e32 v59, v59, v7\n\t"
        "v_cndmask_b32_e64 v0, v0, v4, s[38:39]\n\t"
        "v_cndmask_b32_e64 v1, v1, v5, s[38:39]\n\t"
        "v_permlane32_swap_b32_e32 v59, v60\n\t"
        "v_cndmask_b32_e64 v2, v2, v6, s[38:39]\n\t"
        "v_cndmask_b32_e64 v3, v3, v7, s[38:39]\n\t"
        "v_max3_f32 v59, v59, v60, v8\n\t"
        "v_max3_f32 v59, v59, v9, v10\n\t"
        "v_max_f32_e32 v60, v59, v11\n\t"
        "v_max_f32_e32 v59, v59, v11\n\t"
        "v_cndmask_b32_e64 v8, v8, v9, s[38:39]\n\t"
        "v_cndmask_b32_e64 v10, v10, v11, s[38:39]\n\t"
        "v_permlane16_swap_b32_e32 v59, v60\n\t"
        "v_max_f32_e32 v59, v59, v60\n\t"
        "v_mul_f32_e32 v59, 0xbe02940c, v59\n\t"
        "v_cndmask_b32_e64 v58, v8, v10, s[40:41]\n\t"
        "v_fmamk_f32 v0, v0, 0x3e02940c, v59\n\t"
        "v_exp_f32_e32 v0, v0\n\t"
        "v_fmamk_f32 v1, v1, 0x3e02940c, v59\n\t"
        "v_exp_f32_e32 v1, v1\n\t"
        "v_fmamk_f32 v2, v2, 0x3e02940c, v59\n\t"
        "v_exp_f32_e32 v2, v2\n\t"
        "v_fmamk_f32 v3, v3, 0x3e02940c, v59\n\t"
        "v_exp_f32_e32 v3, v3\n\t"
        "v_fmamk_f32 v58, v58, 0x3e02940c, v59\n\t"
        "v_cvt_pk_bf16_f32 v12, v0, v1\n\t"
        "v_cvt_pk_bf16_f32 v13, v2, v3\n\t"
        "v_exp_f32_e32 v58, v58\n\t"
        "s_waitcnt lgkmcnt(0)\n\t"
        "v_mov_b32_dpp v14, v12 row_ror:8 row_mask:0xf bank_mask:0xf\n\t"
        "v_mov_b32_dpp v15, v13 row_ror:8 row_mask:0xf bank_mask:0xf\n\t"
        "v_mov_b32_dpp v59, v58 row_ror:8 row_mask:0xf bank_mask:0xf\n\t"
        "v_cvt_pk_bf16_f32 v59, v58, v59\n\t"
        "v_mfma_f32_16x16x32_bf16 v[20:23], v[16:19], v[12:15], 0\n\t"
        "v_cndmask_b32_e64 v56, v59, 0, s[40:41]\n\t"
        "v_cndmask_b32_e64 v57, 0, v59, s[40:41]\n\t"
        "v_mfma_f32_16x16x32_bf16 v[4:7], v[48:51], v[12:15], 0\n\t"
        "s_waitcnt vmcnt(1)\n\t"
        "v_max3_u32 v111, v104, v105, v106\n\t"
        "v_mfma_f32_16x16x32_bf16 v[8:11], v[52:55], v[12:15], 0\n\t"
        "v_max_u32_e32 v111, v111, v107\n\t"
        "v_mfma_f32_16x16x16_bf16 v[20:23], v[16:17], v[56:57], v[20:23]\n\t"
        "v_cmp_eq_u32_e32 vcc, -1, v111\n\t"
        "v_mfma_f32_16x16x16_bf16 v[4:7], v[104:105], v[56:57], v[4:7]\n\t"
        "s_nop 1\n\t"
        "v_mfma_f32_16x16x16_bf16 v[8:11], v[106:107], v[56:57], v[8:11]\n\t"
        "s_cbranch_vccnz LA_vmfail%=\n\t"
        "LA_vmok%=:\n\t"
        "s_nop 1\n\t"
        "v_rcp_f32_e32 v59, v20\n\t"
        "s_nop 0\n\t"
        "v_pk_mul_f32 v[4:5], v[4:5], v[58:59] op_sel:[0,1] op_sel_hi:[1,1]\n\t"
        "v_cvt_pk_bf16_f32 v0, v4, v5\n\t"
        "v_pk_mul_f32 v[6:7], v[6:7], v[58:59] op_sel:[0,1] op_sel_hi:[1,1]\n\t"
        "v_cvt_pk_bf16_f32 v1, v6, v7\n\t"
        "v_pk_mul_f32 v[8:9], v[8:9], v[58:59] op_sel:[0,1] op_sel_hi:[1,1]\n\t"
        "v_cvt_pk_bf16_f32 v2, v8, v9\n\t"
        "v_pk_mul_f32 v[10:11], v[10:11], v[58:59] op_sel:[0,1] op_sel_hi:[1,1]\n\t"
        "v_cvt_pk_bf16_f32 v3, v10, v11\n\t"
        "buffer_store_dwordx4 v[0:3], v121, s[8:11], s26 offen sc1\n\t"
        "LA_x%=:\n\t"
        "s_memrealtime s[52:53]\n\t"
        "ds_read_b128 v[16:19], v109 offset:8192\n\t"
        "ds_read_b128 v[20:23], v109 offset:9216\n\t"
        "ds_read_b128 v[24:27], v109 offset:10240\n\t"
        "ds_read_b128 v[28:31], v109 offset:11264\n\t"
        "ds_read_b128 v[56:59], v124 offset:0\n\t"
        "ds_read_b128 v[60:63], v124 offset:4096\n\t"
        "ds_read_b128 v[12:15], v124 offset:1024\n\t"
        "v_xor_b32_e32 v126, 0x800, v108\n\t"
        "s_add_u32 s48, s24, 1\n\t"
        "s_min_u32 s48, s48, 63\n\t"
        "buffer_load_dwordx2 v[108:109], v125, s[8:11], s26 offen sc1\n\t"
        "s_lshl_b32 s35, s44, 10\n\t"
        "s_lshr_b32 s34, s48, 1\n\t"
        "s_add_u32 s34, s34, 1\n\t"
        "s_min_u32 s34, s34, 31\n\t"
        "s_cmp_gt_u32 s54, s34\n\t"
        "s_cselect_b32 s35, 0xf0000000, s35\n\t"
        "v_lshlrev_b32_e32 v111, 2, v122\n\t"
        "v_and_b32_e32 v111, 0x3c0, v111\n\t"
        "buffer_load_dword v111, v111, s[12:15], s35 offen offset:0 sc1\n\t"
        "s_waitcnt lgkmcnt(6)\n\t"
        "v_mfma_f32_16x16x32_bf16 v[8:11], v[32:35], v[16:19], 0\n\t"
        "ds_read_b128 v[32:35], v124 offset:5120\n\t"
        "s_waitcnt lgkmcnt(6)\n\t"
        "v_mfma_f32_16x16x32_bf16 v[8:11], v[36:39], v[20:23], v[8:11]\n\t"
        "ds_read_b128 v[36:39], v124 offset:2048\n\t"
        "s_waitcnt lgkmcnt(6)\n\t"
        "v_mfma_f32_16x16x32_bf16 v[8:11], v[40:43], v[24:27], v[8:11]\n\t"
        "ds_read_b128 v[40:43], v124 offset:6144\n\t"
        "s_waitcnt lgkmcnt(6)\n\t"
        "v_mfma_f32_16x16x32_bf16 v[8:11], v[44:47], v[28:31], v[8:11]\n\t"
        "ds_read_b128 v[44:47], v124 offset:3072\n\t"
        "s_add_u32 s35, s24, 1\n\t"
        "s_waitcnt lgkmcnt(6)\n\t"
        "v_mfma_f32_16x16x32_bf16 v[0:3], v[56:59], v[16:19], 0\n\t"
        "ds_read_b128 v[56:59], v124 offset:7168\n\t"
        "s_waitcnt lgkmcnt(6)\n\t"
        "v_mfma_f32_16x16x32_bf16 v[4:7], v[60:63], v[16:19], 0\n\t"
        "s_and_b32 s36, s35, 1\n\t"
        "s_mul_i32 s36, s36, 24576\n\t"
        "s_waitcnt lgkmcnt(5)\n\t"
        "v_mfma_f32_16x16x32_bf16 v[0:3], v[12:15], v[20:23], v[0:3]\n\t"
        "s_add_u32 s36, s36, s55\n\t"
        "s_lshl_b32 s37, s48, 18\n\t"
        "s_waitcnt lgkmcnt(4)\n\t"
        "v_mfma_f32_16x16x32_bf16 v[4:7], v[32:35], v[20:23], v[4:7]\n\t"
        "v_xor_b32_e32 v32, 0x200, v120\n\t"
        "buffer_load_dwordx4 v[12:15], v32, s[8:11], s26 offen sc1\n\t"
        "s_waitcnt lgkmcnt(3)\n\t"
        "v_mfma_f32_16x16x32_bf16 v[0:3], v[36:39], v[24:27], v[0:3]\n\t"
        "s_lshl_b32 s34, s43, 11\n\t"
        "s_add_u32 s37, s37, s34\n\t"
        "s_waitcnt lgkmcnt(2)\n\t"
        "v_mfma_f32_16x16x32_bf16 v[4:7], v[40:43], v[24:27], v[4:7]\n\t"
        "v_cvt_pk_bf16_f32 v8, 0, v8\n\t"
        "v_cvt_pk_bf16_f32 v9, 0, v9\n\t"
        "v_cvt_pk_bf16_f32 v10, 0, v10\n\t"
        "v_cvt_pk_bf16_f32 v11, 0, v11\n\t"
        "s_waitcnt lgkmcnt(1)\n\t"
        "v_mfma_f32_16x16x32_bf16 v[0:3], v[44:47], v[28:31], v[0:3]\n\t"
        "v_mul_f32_e32 v8, 0x3db504f3, v8\n\t"
        "v_mul_f32_e32 v9, 0x3db504f3, v9\n\t"
        "v_mul_f32_e32 v10, 0x3db504f3, v10\n\t"
        "v_mul_f32_e32 v11, 0x3db504f3, v11\n\t"
        "s_waitcnt lgkmcnt(0)\n\t"
        "v_mfma_f32_16x16x32_bf16 v[4:7], v[56:59], v[28:31], v[4:7]\n\t"
        "v_cvt_pk_bf16_f32 v8, 0, v8\n\t"
        "v_cvt_pk_bf16_f32 v9, 0, v9\n\t"
        "v_cvt_pk_bf16_f32 v10, 0, v10\n\t"
        "v_cvt_pk_bf16_f32 v11, 0, v11\n\t"
        "ds_read_b128 v[56:59], v126 offset:16384\n\t"
        "ds_read_b128 v[60:63], v126 offset:17408\n\t"
        "v_mov_b64_e32 v[36:37], -1\n\t"
        "v_mov_b64_e32 v[38:39], -1\n\t"
        "v_mov_b32_e32 v40, 0xce6e6b28\n\t"
        "v_mov_b32_e32 v46, 0x3db504f3\n\t"
        "v_mov_b32_e32 v47, 0x3fb8aa3b\n\t"
        "v_cvt_pk_bf16_f32 v0, 0, v0\n\t"
        "v_cvt_pk_bf16_f32 v1, 0, v1\n\t"
        "v_cvt_pk_bf16_f32 v2, 0, v2\n\t"
        "v_cvt_pk_bf16_f32 v3, 0, v3\n\t"
        "v_cvt_pk_bf16_f32 v4, 0, v4\n\t"
        "v_cvt_pk_bf16_f32 v5, 0, v5\n\t"
        "v_cvt_pk_bf16_f32 v6, 0, v6\n\t"
        "v_cvt_pk_bf16_f32 v7, 0, v7\n\t"
        "v_pk_mul_f32 v[0:1], v[0:1], v[46:47] op_sel_hi:[1,0]\n\t"
        "v_pk_mul_f32 v[2:3], v[2:3], v[46:47] op_sel_hi:[1,0]\n\t"
        "v_pk_mul_f32 v[4:5], v[4:5], v[46:47] op_sel_hi:[1,0]\n\t"
        "v_pk_mul_f32 v[6:7], v[6:7], v[46:47] op_sel_hi:[1,0]\n\t"
        "s_lshr_b32 s59, s48, 1\n\t"
        "s_cmp_gt_u32 s54, s59\n\t"
        "s_cbranch_scc1 LA_kvok%=\n\t"
        "s_mov_b32 s42, 0\n\t"
        "s_waitcnt vmcnt(1)\n\t"
        "s_branch LA_kvw%=\n\t"
        "LA_kv%=:\n\t"
        "s_waitcnt vmcnt(0)\n\t"
        "LA_kvw%=:\n\t"
        "v_min_u32_dpp v111, v111, v111 quad_perm:[1,0,3,2] row_mask:0xf bank_mask:0xf\n\t"
        "s_nop 1\n\t"
        "v_min_u32_dpp v111, v111, v111 quad_perm:[2,3,0,1] row_mask:0xf bank_mask:0xf\n\t"
        "s_nop 1\n\t"
        "v_min_u32_dpp v111, v111, v111 row_ror:4 row_mask:0xf bank_mask:0xf\n\t"
        "s_nop 1\n\t"
        "v_min_u32_dpp v111, v111, v111 row_ror:8 row_mask:0xf bank_mask:0xf\n\t"
        "s_nop 0\n\t"
        "v_readfirstlane_b32 s54, v111\n\t"
        "s_nop 0\n\t"
        "s_cmp_gt_u32 s54, s59\n\t"
        "s_cbranch_scc1 LA_kvok%=\n\t"
        "s_sleep 4\n\t"
        "s_lshl_b32 s35, s44, 10\n\t"
        "v_lshlrev_b32_e32 v111, 2, v122\n\t"
        "v_and_b32_e32 v111, 0x3c0, v111\n\t"
        "buffer_load_dword v111, v111, s[12:15], s35 offen offset:0 sc1\n\t"
        "s_add_u32 s42, s42, 1\n\t"
        "s_cmp_lt_u32 s42, 0x100000\n\t"
        "s_cbranch_scc1 LA_kv%=\n\t"
        "s_movk_i32 s34, 16\n\t"
        "s_branch LA_die%=\n\t"
        "LA_kvok%=:\n\t"
        "s_add_u32 m0, s36, 0\n\t"
        "s_add_u32 s34, s37, 29548544\n\t"
        "buffer_load_dwordx4 v122, s[12:15], s34 offen sc1 lds\n\t"
        "v_cvt_pk_bf16_f32 v0, 0, v0\n\t"
        "v_cvt_pk_bf16_f32 v1, 0, v1\n\t"
        "v_cvt_pk_bf16_f32 v2, 0, v2\n\t"
        "v_cvt_pk_bf16_f32 v3, 0, v3\n\t"
        "v_cvt_pk_bf16_f32 v4, 0, v4\n\t"
        "v_cvt_pk_bf16_f32 v5, 0, v5\n\t"
        "v_cvt_pk_bf16_f32 v6, 0, v6\n\t"
        "v_cvt_pk_bf16_f32 v7, 0, v7\n\t"
        "s_add_u32 m0, s36, 1024\n\t"
        "s_add_u32 s34, s37, 29549568\n\t"
        "buffer_load_dwordx4 v122, s[12:15], s34 offen sc1 lds\n\t"
        "v_cndmask_b32_e64 v0, v40, v0, s[60:61]\n\t"
        "v_cndmask_b32_e64 v1, v40, v1, s[62:63]\n\t"
        "v_cndmask_b32_e64 v2, v40, v2, s[64:65]\n\t"
        "v_cndmask_b32_e64 v3, v40, v3, s[66:67]\n\t"
        "v_cndmask_b32_e64 v4, v40, v4, s[68:69]\n\t"
        "v_cndmask_b32_e64 v5, v40, v5, s[70:71]\n\t"
        "v_cndmask_b32_e64 v6, v40, v6, s[72:73]\n\t"
        "v_cndmask_b32_e64 v7, v40, v7, s[74:75]\n\t"
        "s_add_u32 m0, s36, 8192\n\t"
        "s_add_u32 s34, s37, 12771328\n\t"
        "buffer_load_dwordx4 v122, s[12:15], s34 offen sc1 lds\n\t"
        "v_max3_f32 v42, v0, v1, v2\n\t"
        "v_max3_f32 v42, v42, v3, v4\n\t"
        "v_max3_f32 v42, v42, v5, v6\n\t"
        "v_max3_f32 v42, v42, v7, v8\n\t"
        "v_max3_f32 v42, v42, v9, v10\n\t"
        "v_mov_b32_e32 v43, v11\n\t"
        "v_max_f32_e32 v44, v42, v43\n\t"
        "v_max_f32_e32 v42, v42, v43\n\t"
        "s_pack_ll_b32_b16 s34, s52, s24\n\t"
        "v_mov_b32_e32 v40, s34\n\t"
        "s_add_u32 s35, s26, s57\n\t"
        "v_permlane16_swap_b32_e32 v42, v44\n\t"
        "v_max_f32_e32 v45, v42, v44\n\t"
        "v_max_f32_e32 v42, v42, v44\n\t"
        "s_mov_b64 exec, 1\n\t"
        "buffer_store_dword v40, off, s[8:11], s35 sc1\n\t"
        "s_mov_b64 exec, -1\n\t"
        "v_permlane32_swap_b32_e32 v42, v45\n\t"
        "v_max_f32_e32 v42, v42, v45\n\t"
        "v_mul_f32_e32 v42, 0xbfb8aa3b, v42\n\t"
        "v_pk_fma_f32 v[0:1], v[0:1], v[46:47], v[42:43] op_sel:[0,1,0] op_sel_hi:[1,1,0]\n\t"
        "s_add_u32 m0, s36, 9216\n\t"
        "s_add_u32 s34, s37, 12772352\n\t"
        "buffer_load_dwordx4 v122, s[12:15], s34 offen sc1 lds\n\t"
        "v_exp_f32_e32 v0, v0\n\t"
        "v_exp_f32_e32 v1, v1\n\t"
        "v_pk_fma_f32 v[2:3], v[2:3], v[46:47], v[42:43] op_sel:[0,1,0] op_sel_hi:[1,1,0]\n\t"
        "v_exp_f32_e32 v2, v2\n\t"
        "v_exp_f32_e32 v3, v3\n\t"
        "v_pk_fma_f32 v[4:5], v[4:5], v[46:47], v[42:43] op_sel:[0,1,0] op_sel_hi:[1,1,0]\n\t"
        "v_exp_f32_e32 v4, v4\n\t"
        "s_add_u32 m0, s36, 16384\n\t"
        "s_add_u32 s34, s37, 46325760\n\t"
        "buffer_load_dwordx4 v122, s[12:15], s34 offen sc1 lds\n\t"
        "v_exp_f32_e32 v5, v5\n\t"
        "v_pk_fma_f32 v[6:7], v[6:7], v[46:47], v[42:43] op_sel:[0,1,0] op_sel_hi:[1,1,0]\n\t"
        "v_exp_f32_e32 v6, v6\n\t"
        "v_exp_f32_e32 v7, v7\n\t"
        "v_pk_fma_f32 v[8:9], v[8:9], v[46:47], v[42:43] op_sel:[0,1,0] op_sel_hi:[1,1,0]\n\t"
        "v_exp_f32_e32 v8, v8\n\t"
        "v_exp_f32_e32 v9, v9\n\t"
        "s_add_u32 m0, s36, 17408\n\t"
        "s_add_u32 s34, s37, 46326784\n\t"
        "buffer_load_dwordx4 v122, s[12:15], s34 offen sc1 lds\n\t"
        "v_pk_fma_f32 v[10:11], v[10:11], v[46:47], v[42:43] op_sel:[0,1,0] op_sel_hi:[1,1,0]\n\t"
        "v_exp_f32_e32 v10, v10\n\t"
        "v_exp_f32_e32 v11, v11\n\t"
        "v_pk_add_f32 v[42:43], v[0:1], v[2:3]\n\t"
        "v_cndmask_b32_e64 v8, v8, 0, s[40:41]\n\t"
        "v_cndmask_b32_e64 v9, v9, 0, s[40:41]\n\t"
        "v_cndmask_b32_e64 v10, v10, 0, s[40:41]\n\t"
        "buffer_store_dwordx4 v[36:39], v121, s[8:11], s28 offen sc1\n\t"
        "v_cndmask_b32_e64 v11, v11, 0, s[40:41]\n\t"
        "v_pk_add_f32 v[42:43], v[42:43], v[4:5]\n\t"
        "v_pk_add_f32 v[42:43], v[42:43], v[6:7]\n\t"
        "v_pk_add_f32 v[42:43], v[42:43], v[8:9]\n\t"
        "v_pk_add_f32 v[42:43], v[42:43], v[10:11]\n\t"
        "v_add_f32_e32 v44, v42, v43\n\t"
        "v_add_f32_e32 v42, v42, v43\n\t"
        "s_lshl_b32 s34, s24, 18\n\t"
        "s_add_u32 s34, s34, 63102976\n\t"
        "v_permlane16_swap_b32_e32 v42, v44\n\t"
        "v_add_f32_e32 v45, v42, v44\n\t"
        "v_add_f32_e32 v42, v42, v44\n\t"
        "s_add_u32 s35, s24, 1\n\t"
        "v_mov_b32_e32 v126, s35\n\t"
        "v_permlane32_swap_b32_e32 v42, v45\n\t"
        "v_add_f32_e32 v42, v42, v45\n\t"
        "v_rcp_f32_e32 v42, v42\n\t"
        "s_waitcnt vmcnt(8) lgkmcnt(0)\n\t"
        "v_max3_u32 v45, v12, v13, v14\n\t"
        "v_max3_u32 v45, v45, v15, v104\n\t"
        "v_max3_u32 v45, v45, v105, v106\n\t"
        "v_max_u32_e32 v45, v45, v107\n\t"
        "v_cmp_eq_u32_e32 vcc, -1, v45\n\t"
        "s_nop 0\n\t"
        "s_cbranch_vccnz LA_v2fail%=\n\t"
        "LA_v2ok%=:\n\t"
        "v_and_b32_e32 v45, 0xff, v109\n\t"
        "v_cmp_ne_u32_e32 vcc, s24, v45\n\t"
        "s_nop 0\n\t"
        "s_cbranch_vccnz LA_stre%=\n\t"
        "LA_stok%=:\n\t"
        "v_pk_mul_f32 v[0:1], v[0:1], v[42:43] op_sel_hi:[1,0]\n\t"
        "v_subrev_u32_e32 v108, s25, v108\n\t"
        "v_min_u32_dpp v111, v111, v111 quad_perm:[1,0,3,2] row_mask:0xf bank_mask:0xf\n\t"
        "v_cvt_pk_bf16_f32 v32, v0, v1\n\t"
        "v_pk_mul_f32 v[2:3], v[2:3], v[42:43] op_sel_hi:[1,0]\n\t"
        "v_cvt_pk_bf16_f32 v33, v2, v3\n\t"
        "v_max_i32_dpp v108, v108, v108 quad_perm:[1,0,3,2] row_mask:0xf bank_mask:0xf\n\t"
        "v_min_u32_dpp v111, v111, v111 quad_perm:[2,3,0,1] row_mask:0xf bank_mask:0xf\n\t"
        "v_pk_mul_f32 v[4:5], v[4:5], v[42:43] op_sel_hi:[1,0]\n\t"
        "v_cvt_pk_bf16_f32 v34, v4, v5\n\t"
        "v_pk_mul_f32 v[6:7], v[6:7], v[42:43] op_sel_hi:[1,0]\n\t"
        "v_max_i32_dpp v108, v108, v108 quad_perm:[2,3,0,1] row_mask:0xf bank_mask:0xf\n\t"
        "v_min_u32_dpp v111, v111, v111 row_ror:4 row_mask:0xf bank_mask:0xf\n\t"
        "v_cvt_pk_bf16_f32 v35, v6, v7\n\t"
        "v_pk_mul_f32 v[8:9], v[8:9], v[42:43] op_sel_hi:[1,0]\n\t"
        "v_cvt_pk_bf16_f32 v36, v8, v9\n\t"
        "v_max_i32_dpp v108, v108, v108 row_ror:4 row_mask:0xf bank_mask:0xf\n\t"
        "v_min_u32_dpp v111, v111, v111 row_ror:8 row_mask:0xf bank_mask:0xf\n\t"
        "v_pk_mul_f32 v[10:11], v[10:11], v[42:43] op_sel_hi:[1,0]\n\t"
        "v_cvt_pk_bf16_f32 v37, v10, v11\n\t"
        "v_mfma_f32_16x16x32_bf16 v[16:19], v[48:51], v[32:35], 0\n\t"
        "v_max_i32_dpp v108, v108, v108 row_ror:8 row_mask:0xf bank_mask:0xf\n\t"
        "v_mfma_f32_16x16x32_bf16 v[20:23], v[52:55], v[32:35], 0\n\t"
        "v_mfma_f32_16x16x32_bf16 v[24:27], v[56:59], v[32:35], 0\n\t"
        "v_mfma_f32_16x16x32_bf16 v[28:31], v[60:63], v[32:35], 0\n\t"
        "v_max_i32_dpp v108, v108, v108 row_bcast:15 row_mask:0xa bank_mask:0xf\n\t"
        "v_xor_b32_e32 v45, 0x400, v123\n\t"
        "v_add_u32_e32 v124, s56, v124\n\t"
        "v_mfma_f32_16x16x16_bf16 v[16:19], v[104:105], v[36:37], v[16:19]\n\t"
        "v_max_i32_dpp v108, v108, v108 row_bcast:31 row_mask:0xc bank_mask:0xf\n\t"
        "v_mfma_f32_16x16x16_bf16 v[20:23], v[106:107], v[36:37], v[20:23]\n\t"
        "v_mfma_f32_16x16x16_bf16 v[24:27], v[12:13], v[36:37], v[24:27]\n\t"
        "v_mfma_f32_16x16x16_bf16 v[28:31], v[14:15], v[36:37], v[28:31]\n\t"
        "ds_read_b128 v[48:51], v117 offset:4096\n\t"
        "ds_read_b128 v[52:55], v117 offset:5120\n\t"
        "ds_read_b128 v[56:59], v117 offset:6144\n\t"
        "ds_read_b128 v[60:63], v117 offset:7168\n\t"
        "s_sub_u32 s56, 0, s56\n\t"
        "v_cvt_pk_bf16_f32 v0, v16, v17\n\t"
        "v_cvt_pk_bf16_f32 v1, v18, v19\n\t"
        "v_cvt_pk_bf16_f32 v2, v20, v21\n\t"
        "v_cvt_pk_bf16_f32 v3, v22, v23\n\t"
        "buffer_store_dwordx4 v[0:3], v123, s[12:15], s34 offen sc1\n\t"
        "v_cvt_pk_bf16_f32 v4, v24, v25\n\t"
        "v_cvt_pk_bf16_f32 v5, v26, v27\n\t"
        "v_cvt_pk_bf16_f32 v6, v28, v29\n\t"
        "v_cvt_pk_bf16_f32 v7, v30, v31\n\t"
        "buffer_store_dwordx4 v[4:7], v45, s[12:15], s34 offen sc1\n\t"
        "s_cmp_eq_u32 s24, 63\n\t"
        "s_cbranch_scc1 LA_last%=\n\t"
        "ds_read_b128 v[32:35], v117 offset:0\n\t"
        "ds_read_b128 v[36:39], v117 offset:1024\n\t"
        "ds_read_b128 v[40:43], v117 offset:2048\n\t"
        "ds_read_b128 v[44:47], v117 offset:3072\n\t"
        "v_mov_b64_e32 v[14:15], 0\n\t"
        "v_mov_b64_e32 v[12:13], 0\n\t"
        "v_mov_b64_e32 v[10:11], 0\n\t"
        "v_mov_b64_e32 v[8:9], 0\n\t"
        "v_mov_b64_e32 v[6:7], 0\n\t"
        "v_mov_b64_e32 v[4:5], 0\n\t"
        "v_mov_b64_e32 v[2:3], 0\n\t"
        "v_mov_b64_e32 v[0:1], 0\n\t"
        "v_readfirstlane_b32 s34, v111\n\t"
        "s_nop 0\n\t"
        "s_max_u32 s54, s54, s34\n\t"
        "v_readfirstlane_b32 s35, v109\n\t"
        "v_readlane_b32 s36, v108, 63\n\t"
        "s_bfe_u32 s29, s35, 0xc0008\n\t"
        "s_lshr_b32 s30, s35, 20\n\t"
        "s_add_u32 s35, s31, s50\n\t"
        "s_mul_i32 s37, s50, 3\n\t"
        "s_min_u32 s35, s35, s37\n\t"
        "s_add_i32 s34, s36, s51\n\t"
        "s_cmp_gt_i32 s34, 0\n\t"
        "s_cselect_b32 s31, s35, 0\n\t"
        "s_cselect_b32 s34, s34, 0\n\t"
        "s_add_u32 s34, s34, s31\n\t"
        "s_add_u32 s37, s25, s34\n\t"
        "s_add_u32 s45, s37, s29\n\t"
        "s_add_u32 s25, s45, s30\n\t"
        "s_add_u32 s24, s24, 1\n\t"
        "s_mov_b32 s28, s26\n\t"
        "s_mov_b32 s26, s27\n\t"
        "s_add_u32 s27, s27, 0x80000\n\t"
        "s_and_b32 s27, s27, 0x1fffff\n\t"
        "s_branch LA_loop%=\n\t"
        "LA_last%=:\n\t"
        "s_waitcnt vmcnt(0)\n\t"
        "s_barrier\n\t"
        "buffer_store_dword v126, v127, s[12:15], 0 offen sc1\n\t"
        "s_endpgm\n\t"
        "LA_skip%=:\n\t"
        "buffer_store_dword v126, v127, s[12:15], 0 offen sc1\n\t"
        "ds_read_b64_tr_b16 v[32:33], v119 offset:2112\n\t"
        "ds_read_b64_tr_b16 v[34:35], v119 offset:2144\n\t"
        "ds_read_b64_tr_b16 v[36:37], v119 offset:2368\n\t"
        "ds_read_b64_tr_b16 v[38:39], v119 offset:2400\n\t"
        "ds_read_b64_tr_b16 v[40:41], v119 offset:2624\n\t"
        "ds_read_b64_tr_b16 v[42:43], v119 offset:2656\n\t"
        "ds_read_b64_tr_b16 v[44:45], v119 offset:2880\n\t"
        "ds_read_b64_tr_b16 v[46:47], v119 offset:2912\n\t"
        "ds_read_b128 v[48:51], v108 offset:16384\n\t"
        "ds_read_b128 v[52:55], v108 offset:17408\n\t"
        "s_waitcnt lgkmcnt(0)\n\t"
        "s_branch LA_x%=\n\t"
        "LA_stre%=:\n\t"
        "s_mov_b32 s42, 0\n\t"
        "LA_str2%=:\n\t"
        "buffer_load_dwordx2 v[108:109], v125, s[8:11], s26 offen sc1\n\t"
        "s_waitcnt vmcnt(0)\n\t"
        "v_and_b32_e32 v45, 0xff, v109\n\t"
        "v_cmp_ne_u32_e32 vcc, s24, v45\n\t"
        "s_nop 0\n\t"
        "s_cbranch_vccz LA_stok%=\n\t"
        "s_add_u32 s42, s42, 1\n\t"
        "s_cmp_lt_u32 s42, 0x100000\n\t"
        "s_cbranch_scc1 LA_str2%=\n\t"
        "s_movk_i32 s34, 5\n\t"
        "s_branch LA_die%=\n\t"
        "LA_v2fail%=:\n\t"
        "s_mov_b32 s42, 0\n\t"
        "LA_v2re%=:\n\t"
        "v_xor_b32_e32 v45, 0x200, v120\n\t"
        "buffer_load_dwordx4 v[12:15], v45, s[8:11], s26 offen sc1\n\t"
        "buffer_load_dwordx4 v[104:107], v120, s[8:11], s26 offen sc1\n\t"
        "s_waitcnt vmcnt(0)\n\t"
        "v_max3_u32 v45, v12, v13, v14\n\t"
        "v_max3_u32 v45, v45, v15, v104\n\t"
        "v_max3_u32 v45, v45, v105, v106\n\t"
        "v_max_u32_e32 v45, v45, v107\n\t"
        "v_cmp_eq_u32_e32 vcc, -1, v45\n\t"
        "s_nop 0\n\t"
        "s_cbranch_vccz LA_v2ok%=\n\t"
        "s_add_u32 s42, s42, 1\n\t"
        "s_cmp_lt_u32 s42, 0x100000\n\t"
        "s_cbranch_scc1 LA_v2re%=\n\t"
        "s_movk_i32 s34, 6\n\t"
        "s_branch LA_die%=\n\t"
        "LA_rdfail%=:\n\t"
        "s_or_b32 s47, s47, 1\n\t"
        "buffer_load_dwordx4 v[16:19], v112, s[8:11], s26 offen offset:0 sc1\n\t"
        "buffer_load_dwordx4 v[20:23], v112, s[8:11], s26 offen offset:1024 sc1\n\t"
        "buffer_load_dwordx4 v[24:27], v112, s[8:11], s26 offen offset:2048 sc1\n\t"
        "buffer_load_dwordx4 v[28:31], v112, s[8:11], s26 offen offset:3072 sc1\n\t"
        "s_nop 7\n\t"
        "v_mov_b64_e32 v[0:1], 0\n\t"
        "v_mov_b64_e32 v[2:3], 0\n\t"
        "v_mov_b64_e32 v[4:5], 0\n\t"
        "v_mov_b64_e32 v[6:7], 0\n\t"
        "v_mov_b64_e32 v[8:9], 0\n\t"
        "v_mov_b64_e32 v[10:11], 0\n\t"
        "v_mov_b64_e32 v[12:13], 0\n\t"
        "v_mov_b64_e32 v[14:15], 0\n\t"
        "s_branch LA_rdre%=w\n\t"
        "LA_rdre%=:\n\t"
        "buffer_load_dwordx4 v[16:19], v112, s[8:11], s26 offen offset:0 sc1\n\t"
        "buffer_load_dwordx4 v[20:23], v112, s[8:11], s26 offen offset:1024 sc1\n\t"
        "buffer_load_dwordx4 v[24:27], v112, s[8:11], s26 offen offset:2048 sc1\n\t"
        "buffer_load_dwordx4 v[28:31], v112, s[8:11], s26 offen offset:3072 sc1\n\t"
        "LA_rdre%=w:\n\t"
        "s_waitcnt vmcnt(0) lgkmcnt(0)\n\t"
        "v_max3_u32 v111, v16, v17, v18\n\t"
        "v_max3_u32 v111, v111, v19, v20\n\t"
        "v_max3_u32 v111, v111, v21, v22\n\t"
        "v_max3_u32 v111, v111, v23, v24\n\t"
        "v_max3_u32 v111, v111, v25, v26\n\t"
        "v_max3_u32 v111, v111, v27, v28\n\t"
        "v_max3_u32 v111, v111, v29, v30\n\t"
        "v_max_u32_e32 v111, v111, v31\n\t"
        "v_cmp_eq_u32_e32 vcc, -1, v111\n\t"
        "s_nop 0\n\t"
        "s_cbranch_vccz LA_go%=\n\t"
        "s_add_u32 s42, s42, 1\n\t"
        "s_cmp_lt_u32 s42, 0x100000\n\t"
        "s_cbranch_scc1 LA_rdre%=\n\t"
        "s_movk_i32 s34, 1\n\t"
        "s_branch LA_die%=\n\t"
        "LA_vmfail%=:\n\t"
        "s_or_b32 s47, s47, 2\n\t"
        "s_mov_b32 s42, 0\n\t"
        "LA_vmre%=:\n\t"
        "buffer_load_dwordx4 v[104:107], v120, s[8:11], s26 offen sc1\n\t"
        "v_mfma_f32_16x16x32_bf16 v[4:7], v[48:51], v[12:15], 0\n\t"
        "v_mfma_f32_16x16x32_bf16 v[8:11], v[52:55], v[12:15], 0\n\t"
        "s_nop 7\n\t"
        "s_waitcnt vmcnt(0)\n\t"
        "v_mfma_f32_16x16x16_bf16 v[4:7], v[104:105], v[56:57], v[4:7]\n\t"
        "v_mfma_f32_16x16x16_bf16 v[8:11], v[106:107], v[56:57], v[8:11]\n\t"
        "v_max3_u32 v111, v104, v105, v106\n\t"
        "v_max_u32_e32 v111, v111, v107\n\t"
        "v_cmp_eq_u32_e32 vcc, -1, v111\n\t"
        "s_nop 7\n\t"
        "s_cbranch_vccz LA_vmok%=\n\t"
        "s_add_u32 s42, s42, 1\n\t"
        "s_cmp_lt_u32 s42, 0x100000\n\t"
        "s_cbranch_scc1 LA_vmre%=\n\t"
        "s_movk_i32 s34, 2\n\t"
        "s_branch LA_die%=\n\t"
        "LA_die%=:\n\t"
        "v_mov_b32_e32 v30, s34\n\t"
        "s_mov_b32 s35, 33024\n\t"
        "s_mov_b64 exec, 1\n\t"
        "buffer_store_dword v30, off, s[12:15], s35 sc1\n\t"
        "s_endpgm\n\t"
        ".Lk_end_A:\n\t"
        :: REC_S_IN, "{v96}"(g_w), "{v97}"(wb0), "{v98}"(wb1), "{v99}"(rb0), "{v100}"(rb0 ^ 32u), "{v101}"(rb0 ^ 64u), "{v102}"(rb0 ^ 96u),
           "{v112}"(a_mem), "{v113}"(l_ms + w * 4096), "{v114}"(l_ms + ((w + 1) & 3) * 4096), "{v115}"(l_ms + ((w + 2) & 3) * 4096), "{v116}"(l_ms + ((w + 3) & 3) * 4096),
           "{v117}"(lb + w * 22528 + l * 16), "{v118}"(l_iw), "{v119}"(l_iq), "{v120}"(a_vm), "{v121}"(a_o), "{v122}"(h * 8192 + l * 16), "{v123}"(a_ox), "{v124}"(lb + L_KV + l * 16), "{s55}"(lb + L_KV + w * 2048),
           "{v125}"((24 + cp) * 16384 + l * 32), "{v126}"(16 * (w & 1) + n - 4 * g), "{v103}"((l & 1) ? 0xEEEEEEEEu : 0x44444444u),
           "{v127}"((l == 0 && w == 0) ? F_PROG + h * 128 : OOB)
        : "memory", "vcc", "scc", "v0", "v255", "a0", "a255", "s101");
    __builtin_unreachable();
}
__device__ __forceinline__ void rec_b(const Params& p, unsigned lb, const int wv, const int id, const int rg, const int cp) {
    const int w = wv, l = otid() & 63, n = l & 15, g = l >> 4, cs = id, rb = 32 + id * 2 + rg;
    const v4u sh = mk_srd(p.ws + ho_base(p.ws), HO_BYTES - 8192), sw = mk_srd(p.ws, W_TOTAL), s1 = mk_srd(p.Wv, 1024 * 4096 * 2), s2 = mk_srd(p.Wo, 1024 * 4096 * 2);
    const int r8 = l >> 3, pc = l & 7, pp = n >> 3, bb = (n >> 2) & 1, f = (g & 1) | (pp << 1);
    const unsigned stg = lb + LB_STG + w * LB_STGP;
    const unsigned g_v = r8 * 8192 + cs * 256 + rg * 128 + pc * 16, g_o = (l >> 2) * 2048 + cs * 64 + (l & 3) * 16;
    const unsigned wb0 = stg + r8 * 128 + (((pc >> 1) ^ (r8 >> 2)) << 5) + (pc & 1) * 16, wb1 = wb0 ^ 64u;
    const unsigned rb0 = stg + (16 * (g >> 1) + 8 * pp + 4 * (g & 1) + bb) * 128 + (f << 5) + (n & 3) * 8;
    const unsigned o_r = stg + (g >> 1) * (2048 + 128) + (g & 1) * (1024 + 32) + (bb + 4 * pp) * 64 + (n & 3) * 8;
    const unsigned a_mem = cp * 32768 + (w >> 1) * 16384 + (w & 1) * 4096 + l * 16;
    const unsigned a_vmw = w < 2 ? (16 + (cs >> 2)) * 16384 + (cs & 3) * 2048 + (2 * rg + w) * 512 + (l & 31) * 16 + (g >> 1) * 8 : OOB;
    const unsigned a_or = (8 + rg * 4 + w) * 16384 + l * 16;
    const unsigned a_mw = w * 32768 + (cs >> 4) * 16384 + ((cs >> 1) & 7) * 1024 + (cs & 1) * 512 + (l >> 5) * 256 + rg * 128 + (l & 31) * 4;
    const unsigned a_sw = (w == 0 && l < 4) ? (24 + l) * 16384 + (id * 2 + rg) * 32 : OOB;
    const bf16_t* m0p = p.mem0 + (4 * rg + ((l >> 3) & 3)) * 1024 + cs * 32 + (l >> 5) * 16 + (l & 7) * 2;
    asm volatile(
        "s_nop 4\n\t"
        "s_memtime s[70:71]\n\t"
        "s_memrealtime s[72:73]\n\t"
        "global_load_dword v109, v[108:109], off\n\t"
        "s_lshl_b32 s34, s43, 21\n\t"
        "s_add_u32 s34, s34, 0\n\t"
        "buffer_load_dwordx4 v[128:131], v96, s[16:19], s34 offen nt\n\t"
        "s_add_u32 s34, s34, 0x10000\n\t"
        "buffer_load_dwordx4 v[132:135], v96, s[16:19], s34 offen nt\n\t"
        "s_add_u32 s34, s34, 0x10000\n\t"
        "buffer_load_dwordx4 v[136:139], v96, s[16:19], s34 offen nt\n\t"
        "s_add_u32 s34, s34, 0x10000\n\t"
        "buffer_load_dwordx4 v[140:143], v96, s[16:19], s34 offen nt\n\t"
        "s_add_u32 s34, s34, 0x10000\n\t"
        "buffer_load_dwordx4 v[144:147], v96, s[16:19], s34 offen nt\n\t"
        "s_add_u32 s34, s34, 0x10000\n\t"
        "buffer_load_dwordx4 v[148:151], v96, s[16:19], s34 offen nt\n\t"
        "s_add_u32 s34, s34, 0x10000\n\t"
        "buffer_load_dwordx4 v[152:155], v96, s[16:19], s34 offen nt\n\t"
        "s_add_u32 s34, s34, 0x10000\n\t"
        "buffer_load_dwordx4 v[156:159], v96, s[16:19], s34 offen nt\n\t"
        "s_lshl_b32 s34, s43, 21\n\t"
        "s_add_u32 s34, s34, 524288\n\t"
        "buffer_load_dwordx4 v[160:163], v96, s[16:19], s34 offen nt\n\t"
        "s_add_u32 s34, s34, 0x10000\n\t"
        "buffer_load_dwordx4 v[164:167], v96, s[16:19], s34 offen nt\n\t"
        "s_add_u32 s34, s34, 0x10000\n\t"
        "buffer_load_dwordx4 v[168:171], v96, s[16:19], s34 offen nt\n\t"
        "s_add_u32 s34, s34, 0x10000\n\t"
        "buffer_load_dwordx4 v[172:175], v96, s[16:19], s34 offen nt\n\t"
        "s_add_u32 s34, s34, 0x10000\n\t"
        "buffer_load_dwordx4 v[176:179], v96, s[16:19], s34 offen nt\n\t"
        "s_add_u32 s34, s34, 0x10000\n\t"
        "buffer_load_dwordx4 v[180:183], v96, s[16:19], s34 offen nt\n\t"
        "s_add_u32 s34, s34, 0x10000\n\t"
        "buffer_load_dwordx4 v[184:187], v96, s[16:19], s34 offen nt\n\t"
        "s_add_u32 s34, s34, 0x10000\n\t"
        "buffer_load_dwordx4 v[188:191], v96, s[16:19], s34 offen nt\n\t"
        "s_lshl_b32 s34, s43, 21\n\t"
        "s_add_u32 s34, s34, 1048576\n\t"
        "buffer_load_dwordx4 v[192:195], v96, s[16:19], s34 offen nt\n\t"
        "s_add_u32 s34, s34, 0x10000\n\t"
        "buffer_load_dwordx4 v[196:199], v96, s[16:19], s34 offen nt\n\t"
        "s_add_u32 s34, s34, 0x10000\n\t"
        "buffer_load_dwordx4 v[200:203], v96, s[16:19], s34 offen nt\n\t"
        "s_add_u32 s34, s34, 0x10000\n\t"
        "buffer_load_dwordx4 v[204:207], v96, s[16:19], s34 offen nt\n\t"
        "s_add_u32 s34, s34, 0x10000\n\t"
        "buffer_load_dwordx4 v[208:211], v96, s[16:19], s34 offen nt\n\t"
        "s_add_u32 s34, s34, 0x10000\n\t"
        "buffer_load_dwordx4 v[212:215], v96, s[16:19], s34 offen nt\n\t"
        "s_add_u32 s34, s34, 0x10000\n\t"
        "buffer_load_dwordx4 v[216:219], v96, s[16:19], s34 offen nt\n\t"
        "s_add_u32 s34, s34, 0x10000\n\t"
        "buffer_load_dwordx4 v[220:223], v96, s[16:19], s34 offen nt\n\t"
        "s_waitcnt vmcnt(16)\n\t"
        "ds_write_b128 v98, v[128:131] offset:0\n\t"
        "ds_write_b128 v99, v[132:135] offset:1024\n\t"
        "ds_write_b128 v98, v[136:139] offset:2048\n\t"
        "ds_write_b128 v99, v[140:143] offset:3072\n\t"
        "ds_write_b128 v98, v[144:147] offset:4096\n\t"
        "ds_write_b128 v99, v[148:151] offset:5120\n\t"
        "ds_write_b128 v98, v[152:155] offset:6144\n\t"
        "ds_write_b128 v99, v[156:159] offset:7168\n\t"
        "ds_read_b64_tr_b16 v[128:129], v100 offset:0\n\t"
        "ds_read_b64_tr_b16 v[130:131], v100 offset:256\n\t"
        "ds_read_b64_tr_b16 v[132:133], v100 offset:4096\n\t"
        "ds_read_b64_tr_b16 v[134:135], v100 offset:4352\n\t"
        "s_waitcnt lgkmcnt(8)\n\t"
        "ds_read_b64_tr_b16 v[136:137], v101 offset:0\n\t"
        "ds_read_b64_tr_b16 v[138:139], v101 offset:256\n\t"
        "ds_read_b64_tr_b16 v[140:141], v101 offset:4096\n\t"
        "ds_read_b64_tr_b16 v[142:143], v101 offset:4352\n\t"
        "s_waitcnt lgkmcnt(8)\n\t"
        "ds_read_b64_tr_b16 v[144:145], v102 offset:0\n\t"
        "ds_read_b64_tr_b16 v[146:147], v102 offset:256\n\t"
        "ds_read_b64_tr_b16 v[148:149], v102 offset:4096\n\t"
        "ds_read_b64_tr_b16 v[150:151], v102 offset:4352\n\t"
        "s_waitcnt lgkmcnt(8)\n\t"
        "ds_read_b64_tr_b16 v[152:153], v103 offset:0\n\t"
        "ds_read_b64_tr_b16 v[154:155], v103 offset:256\n\t"
        "ds_read_b64_tr_b16 v[156:157], v103 offset:4096\n\t"
        "ds_read_b64_tr_b16 v[158:159], v103 offset:4352\n\t"
        "s_lshl_b32 s34, s43, 21\n\t"
        "s_add_u32 s34, s34, 1572864\n\t"
        "buffer_load_dwordx4 v[224:227], v96, s[16:19], s34 offen nt\n\t"
        "s_add_u32 s34, s34, 0x10000\n\t"
        "buffer_load_dwordx4 v[228:231], v96, s[16:19], s34 offen nt\n\t"
        "s_add_u32 s34, s34, 0x10000\n\t"
        "buffer_load_dwordx4 v[232:235], v96, s[16:19], s34 offen nt\n\t"
        "s_add_u32 s34, s34, 0x10000\n\t"
        "buffer_load_dwordx4 v[236:239], v96, s[16:19], s34 offen nt\n\t"
        "s_add_u32 s34, s34, 0x10000\n\t"
        "buffer_load_dwordx4 v[240:243], v96, s[16:19], s34 offen nt\n\t"
        "s_add_u32 s34, s34, 0x10000\n\t"
        "buffer_load_dwordx4 v[244:247], v96, s[16:19], s34 offen nt\n\t"
        "s_add_u32 s34, s34, 0x10000\n\t"
        "buffer_load_dwordx4 v[248:251], v96, s[16:19], s34 offen nt\n\t"
        "s_add_u32 s34, s34, 0x10000\n\t"
        "buffer_load_dwordx4 v[252:255], v96, s[16:19], s34 offen nt\n\t"
        "s_waitcnt lgkmcnt(0)\n\t"
        "s_waitcnt vmcnt(16)\n\t"
        "ds_write_b128 v98, v[160:163] offset:0\n\t"
        "ds_write_b128 v99, v[164:167] offset:1024\n\t"
        "ds_write_b128 v98, v[168:171] offset:2048\n\t"
        "ds_write_b128 v99, v[172:175] offset:3072\n\t"
        "ds_write_b128 v98, v[176:179] offset:4096\n\t"
        "ds_write_b128 v99, v[180:183] offset:5120\n\t"
        "ds_write_b128 v98, v[184:187] offset:6144\n\t"
        "ds_write_b128 v99, v[188:191] offset:7168\n\t"
        "ds_read_b64_tr_b16 v[160:161], v100 offset:0\n\t"
        "ds_read_b64_tr_b16 v[162:163], v100 offset:256\n\t"
        "ds_read_b64_tr_b16 v[164:165], v100 offset:4096\n\t"
        "ds_read_b64_tr_b16 v[166:167], v100 offset:4352\n\t"
        "s_waitcnt lgkmcnt(8)\n\t"
        "ds_read_b64_tr_b16 v[168:169], v101 offset:0\n\t"
        "ds_read_b64_tr_b16 v[170:171], v101 offset:256\n\t"
        "ds_read_b64_tr_b16 v[172:173], v101 offset:4096\n\t"
        "ds_read_b64_tr_b16 v[174:175], v101 offset:4352\n\t"
        "s_waitcnt lgkmcnt(8)\n\t"
        "ds_read_b64_tr_b16 v[176:177], v102 offset:0\n\t"
        "ds_read_b64_tr_b16 v[178:179], v102 offset:256\n\t"
        "ds_read_b64_tr_b16 v[180:181], v102 offset:4096\n\t"
        "ds_read_b64_tr_b16 v[182:183], v102 offset:4352\n\t"
        "s_waitcnt lgkmcnt(8)\n\t"
        "ds_read_b64_tr_b16 v[184:185], v103 offset:0\n\t"
        "ds_read_b64_tr_b16 v[186:187], v103 offset:256\n\t"
        "ds_read_b64_tr_b16 v[188:189], v103 offset:4096\n\t"
        "ds_read_b64_tr_b16 v[190:191], v103 offset:4352\n\t"
        "s_lshl_b32 s34, s43, 21\n\t"
        "s_add_u32 s34, s34, 0\n\t"
        "buffer_load_dwordx4 a[0:3], v97, s[20:23], s34 offen\n\t"
        "s_add_u32 s34, s34, 0x8000\n\t"
        "buffer_load_dwordx4 a[4:7], v97, s[20:23], s34 offen\n\t"
        "s_add_u32 s34, s34, 0x8000\n\t"
        "buffer_load_dwordx4 a[8:11], v97, s[20:23], s34 offen\n\t"
        "s_add_u32 s34, s34, 0x8000\n\t"
        "buffer_load_dwordx4 a[12:15], v97, s[20:23], s34 offen\n\t"
        "s_add_u32 s34, s34, 0x8000\n\t"
        "buffer_load_dwordx4 a[16:19], v97, s[20:23], s34 offen\n\t"
        "s_add_u32 s34, s34, 0x8000\n\t"
        "buffer_load_dwordx4 a[20:23], v97, s[20:23], s34 offen\n\t"
        "s_add_u32 s34, s34, 0x8000\n\t"
        "buffer_load_dwordx4 a[24:27], v97, s[20:23], s34 offen\n\t"
        "s_add_u32 s34, s34, 0x8000\n\t"
        "buffer_load_dwordx4 a[28:31], v97, s[20:23], s34 offen\n\t"
        "s_waitcnt lgkmcnt(0)\n\t"
        "s_waitcnt vmcnt(16)\n\t"
        "ds_write_b128 v98, v[192:195] offset:0\n\t"
        "ds_write_b128 v99, v[196:199] offset:1024\n\t"
        "ds_write_b128 v98, v[200:203] offset:2048\n\t"
        "ds_write_b128 v99, v[204:207] offset:3072\n\t"
        "ds_write_b128 v98, v[208:211] offset:4096\n\t"
        "ds_write_b128 v99, v[212:215] offset:5120\n\t"
        "ds_write_b128 v98, v[216:219] offset:6144\n\t"
        "ds_write_b128 v99, v[220:223] offset:7168\n\t"
        "ds_read_b64_tr_b16 v[192:193], v100 offset:0\n\t"
        "ds_read_b64_tr_b16 v[194:195], v100 offset:256\n\t"
        "ds_read_b64_tr_b16 v[196:197], v100 offset:4096\n\t"
        "ds_read_b64_tr_b16 v[198:199], v100 offset:4352\n\t"
        "s_waitcnt lgkmcnt(8)\n\t"
        "ds_read_b64_tr_b16 v[200:201], v101 offset:0\n\t"
        "ds_read_b64_tr_b16 v[202:203], v101 offset:256\n\t"
        "ds_read_b64_tr_b16 v[204:205], v101 offset:4096\n\t"
        "ds_read_b64_tr_b16 v[206:207], v101 offset:4352\n\t"
        "s_waitcnt lgkmcnt(8)\n\t"
        "ds_read_b64_tr_b16 v[208:209], v102 offset:0\n\t"
        "ds_read_b64_tr_b16 v[210:211], v102 offset:256\n\t"
        "ds_read_b64_tr_b16 v[212:213], v102 offset:4096\n\t"
        "ds_read_b64_tr_b16 v[214:215], v102 offset:4352\n\t"
        "s_waitcnt lgkmcnt(8)\n\t"
        "ds_read_b64_tr_b16 v[216:217], v103 offset:0\n\t"
        "ds_read_b64_tr_b16 v[218:219], v103 offset:256\n\t"
        "ds_read_b64_tr_b16 v[220:221], v103 offset:4096\n\t"
        "ds_read_b64_tr_b16 v[222:223], v103 offset:4352\n\t"
        "s_lshl_b32 s34, s43, 21\n\t"
        "s_add_u32 s34, s34, 262144\n\t"
        "buffer_load_dwordx4 a[32:35], v97, s[20:23], s34 offen\n\t"
        "s_add_u32 s34, s34, 0x8000\n\t"
        "buffer_load_dwordx4 a[36:39], v97, s[20:23], s34 offen\n\t"
        "s_add_u32 s34, s34, 0x8000\n\t"
        "buffer_load_dwordx4 a[40:43], v97, s[20:23], s34 offen\n\t"
        "s_add_u32 s34, s34, 0x8000\n\t"
        "buffer_load_dwordx4 a[44:47], v97, s[20:23], s34 offen\n\t"
        "s_add_u32 s34, s34, 0x8000\n\t"
        "buffer_load_dwordx4 a[48:51], v97, s[20:23], s34 offen\n\t"
        "s_add_u32 s34, s34, 0x8000\n\t"
        "buffer_load_dwordx4 a[52:55], v97, s[20:23], s34 offen\n\t"
        "s_add_u32 s34, s34, 0x8000\n\t"
        "buffer_load_dwordx4 a[56:59], v97, s[20:23], s34 offen\n\t"
        "s_add_u32 s34, s34, 0x8000\n\t"
        "buffer_load_dwordx4 a[60:63], v97, s[20:23], s34 offen\n\t"
        "s_waitcnt lgkmcnt(0)\n\t"
        "s_waitcnt vmcnt(16)\n\t"
        "ds_write_b128 v98, v[224:227] offset:0\n\t"
        "ds_write_b128 v99, v[228:231] offset:1024\n\t"
        "ds_write_b128 v98, v[232:235] offset:2048\n\t"
        "ds_write_b128 v99, v[236:239] offset:3072\n\t"
        "ds_write_b128 v98, v[240:243] offset:4096\n\t"
        "ds_write_b128 v99, v[244:247] offset:5120\n\t"
        "ds_write_b128 v98, v[248:251] offset:6144\n\t"
        "ds_write_b128 v99, v[252:255] offset:7168\n\t"
        "ds_read_b64_tr_b16 v[224:225], v100 offset:0\n\t"
        "ds_read_b64_tr_b16 v[226:227], v100 offset:256\n\t"
        "ds_read_b64_tr_b16 v[228:229], v100 offset:4096\n\t"
        "ds_read_b64_tr_b16 v[230:231], v100 offset:4352\n\t"
        "s_waitcnt lgkmcnt(8)\n\t"
        "ds_read_b64_tr_b16 v[232:233], v101 offset:0\n\t"
        "ds_read_b64_tr_b16 v[234:235], v101 offset:256\n\t"
        "ds_read_b64_tr_b16 v[236:237], v101 offset:4096\n\t"
        "ds_read_b64_tr_b16 v[238:239], v101 offset:4352\n\t"
        "s_waitcnt lgkmcnt(8)\n\t"
        "ds_read_b64_tr_b16 v[240:241], v102 offset:0\n\t"
        "ds_read_b64_tr_b16 v[242:243], v102 offset:256\n\t"
        "ds_read_b64_tr_b16 v[244:245], v102 offset:4096\n\t"
        "ds_read_b64_tr_b16 v[246:247], v102 offset:4352\n\t"
        "s_waitcnt lgkmcnt(8)\n\t"
        "ds_read_b64_tr_b16 v[248:249], v103 offset:0\n\t"
        "ds_read_b64_tr_b16 v[250:251], v103 offset:256\n\t"
        "ds_read_b64_tr_b16 v[252:253], v103 offset:4096\n\t"
        "ds_read_b64_tr_b16 v[254:255], v103 offset:4352\n\t"
        "s_lshl_b32 s34, s43, 21\n\t"
        "s_add_u32 s34, s34, 524288\n\t"
        "buffer_load_dwordx4 a[64:67], v97, s[20:23], s34 offen\n\t"
        "s_add_u32 s34, s34, 0x8000\n\t"
        "buffer_load_dwordx4 a[68:71], v97, s[20:23], s34 offen\n\t"
        "s_add_u32 s34, s34, 0x8000\n\t"
        "buffer_load_dwordx4 a[72:75], v97, s[20:23], s34 offen\n\t"
        "s_add_u32 s34, s34, 0x8000\n\t"
        "buffer_load_dwordx4 a[76:79], v97, s[20:23], s34 offen\n\t"
        "s_add_u32 s34, s34, 0x8000\n\t"
        "buffer_load_dwordx4 a[80:83], v97, s[20:23], s34 offen\n\t"
        "s_add_u32 s34, s34, 0x8000\n\t"
        "buffer_load_dwordx4 a[84:87], v97, s[20:23], s34 offen\n\t"
        "s_add_u32 s34, s34, 0x8000\n\t"
        "buffer_load_dwordx4 a[88:91], v97, s[20:23], s34 offen\n\t"
        "s_add_u32 s34, s34, 0x8000\n\t"
        "buffer_load_dwordx4 a[92:95], v97, s[20:23], s34 offen\n\t"
        "s_waitcnt lgkmcnt(0)\n\t"
        "s_waitcnt vmcnt(16)\n\t"
        "ds_write_b128 v122, a[0:3] offset:0\n\t"
        "ds_write_b128 v122, a[4:7] offset:1056\n\t"
        "ds_write_b128 v122, a[8:11] offset:2176\n\t"
        "ds_write_b128 v122, a[12:15] offset:3232\n\t"
        "ds_write_b128 v122, a[16:19] offset:4352\n\t"
        "ds_write_b128 v122, a[20:23] offset:5408\n\t"
        "ds_write_b128 v122, a[24:27] offset:6528\n\t"
        "ds_write_b128 v122, a[28:31] offset:7584\n\t"
        "ds_read_b64_tr_b16 a[0:1], v123 offset:0\n\t"
        "ds_read_b64_tr_b16 a[2:3], v123 offset:128\n\t"
        "ds_read_b64_tr_b16 a[4:5], v123 offset:4352\n\t"
        "ds_read_b64_tr_b16 a[6:7], v123 offset:4480\n\t"
        "s_waitcnt lgkmcnt(8)\n\t"
        "ds_read_b64_tr_b16 a[8:9], v123 offset:32\n\t"
        "ds_read_b64_tr_b16 a[10:11], v123 offset:160\n\t"
        "ds_read_b64_tr_b16 a[12:13], v123 offset:4384\n\t"
        "ds_read_b64_tr_b16 a[14:15], v123 offset:4512\n\t"
        "s_waitcnt lgkmcnt(8)\n\t"
        "ds_read_b64_tr_b16 a[16:17], v123 offset:512\n\t"
        "ds_read_b64_tr_b16 a[18:19], v123 offset:640\n\t"
        "ds_read_b64_tr_b16 a[20:21], v123 offset:4864\n\t"
        "ds_read_b64_tr_b16 a[22:23], v123 offset:4992\n\t"
        "s_waitcnt lgkmcnt(8)\n\t"
        "ds_read_b64_tr_b16 a[24:25], v123 offset:544\n\t"
        "ds_read_b64_tr_b16 a[26:27], v123 offset:672\n\t"
        "ds_read_b64_tr_b16 a[28:29], v123 offset:4896\n\t"
        "ds_read_b64_tr_b16 a[30:31], v123 offset:5024\n\t"
        "s_waitcnt lgkmcnt(8)\n\t"
        "s_lshl_b32 s34, s43, 21\n\t"
        "s_add_u32 s34, s34, 786432\n\t"
        "buffer_load_dwordx4 a[96:99], v97, s[20:23], s34 offen\n\t"
        "s_add_u32 s34, s34, 0x8000\n\t"
        "buffer_load_dwordx4 a[100:103], v97, s[20:23], s34 offen\n\t"
        "s_add_u32 s34, s34, 0x8000\n\t"
        "buffer_load_dwordx4 a[104:107], v97, s[20:23], s34 offen\n\t"
        "s_add_u32 s34, s34, 0x8000\n\t"
        "buffer_load_dwordx4 a[108:111], v97, s[20:23], s34 offen\n\t"
        "s_add_u32 s34, s34, 0x8000\n\t"
        "buffer_load_dwordx4 a[112:115], v97, s[20:23], s34 offen\n\t"
        "s_add_u32 s34, s34, 0x8000\n\t"
        "buffer_load_dwordx4 a[116:119], v97, s[20:23], s34 offen\n\t"
        "s_add_u32 s34, s34, 0x8000\n\t"
        "buffer_load_dwordx4 a[120:123], v97, s[20:23], s34 offen\n\t"
        "s_add_u32 s34, s34, 0x8000\n\t"
        "buffer_load_dwordx4 a[124:127], v97, s[20:23], s34 offen\n\t"
        "s_waitcnt lgkmcnt(0)\n\t"
        "s_waitcnt vmcnt(16)\n\t"
        "ds_write_b128 v122, a[32:35] offset:0\n\t"
        "ds_write_b128 v122, a[36:39] offset:1056\n\t"
        "ds_write_b128 v122, a[40:43] offset:2176\n\t"
        "ds_write_b128 v122, a[44:47] offset:3232\n\t"
        "ds_write_b128 v122, a[48:51] offset:4352\n\t"
        "ds_write_b128 v122, a[52:55] offset:5408\n\t"
        "ds_write_b128 v122, a[56:59] offset:6528\n\t"
        "ds_write_b128 v122, a[60:63] offset:7584\n\t"
        "ds_read_b64_tr_b16 a[32:33], v123 offset:0\n\t"
        "ds_read_b64_tr_b16 a[34:35], v123 offset:128\n\t"
        "ds_read_b64_tr_b16 a[36:37], v123 offset:4352\n\t"
        "ds_read_b64_tr_b16 a[38:39], v123 offset:4480\n\t"
        "s_waitcnt lgkmcnt(8)\n\t"
        "ds_read_b64_tr_b16 a[40:41], v123 offset:32\n\t"
        "ds_read_b64_tr_b16 a[42:43], v123 offset:160\n\t"
        "ds_read_b64_tr_b16 a[44:45], v123 offset:4384\n\t"
        "ds_read_b64_tr_b16 a[46:47], v123 offset:4512\n\t"
        "s_waitcnt lgkmcnt(8)\n\t"
        "ds_read_b64_tr_b16 a[48:49], v123 offset:512\n\t"
        "ds_read_b64_tr_b16 a[50:51], v123 offset:640\n\t"
        "ds_read_b64_tr_b16 a[52:53], v123 offset:4864\n\t"
        "ds_read_b64_tr_b16 a[54:55], v123 offset:4992\n\t"
        "s_waitcnt lgkmcnt(8)\n\t"
        "ds_read_b64_tr_b16 a[56:57], v123 offset:544\n\t"
        "ds_read_b64_tr_b16 a[58:59], v123 offset:672\n\t"
        "ds_read_b64_tr_b16 a[60:61], v123 offset:4896\n\t"
        "ds_read_b64_tr_b16 a[62:63], v123 offset:5024\n\t"
        "s_waitcnt lgkmcnt(8)\n\t"
        "s_lshl_b32 s34, s43, 21\n\t"
        "s_add_u32 s34, s34, 1048576\n\t"
        "buffer_load_dwordx4 a[128:131], v97, s[20:23], s34 offen\n\t"
        "s_add_u32 s34, s34, 0x8000\n\t"
        "buffer_load_dwordx4 a[132:135], v97, s[20:23], s34 offen\n\t"
        "s_add_u32 s34, s34, 0x8000\n\t"
        "buffer_load_dwordx4 a[136:139], v97, s[20:23], s34 offen\n\t"
        "s_add_u32 s34, s34, 0x8000\n\t"
        "buffer_load_dwordx4 a[140:143], v97, s[20:23], s34 offen\n\t"
        "s_add_u32 s34, s34, 0x8000\n\t"
        "buffer_load_dwordx4 a[144:147], v97, s[20:23], s34 offen\n\t"
        "s_add_u32 s34, s34, 0x8000\n\t"
        "buffer_load_dwordx4 a[148:151], v97, s[20:23], s34 offen\n\t"
        "s_add_u32 s34, s34, 0x8000\n\t"
        "buffer_load_dwordx4 a[152:155], v97, s[20:23], s34 offen\n\t"
        "s_add_u32 s34, s34, 0x8000\n\t"
        "buffer_load_dwordx4 a[156:159], v97, s[20:23], s34 offen\n\t"
        "s_waitcnt lgkmcnt(0)\n\t"
        "s_waitcnt vmcnt(16)\n\t"
        "ds_write_b128 v122, a[64:67] offset:0\n\t"
        "ds_write_b128 v122, a[68:71] offset:1056\n\t"
        "ds_write_b128 v122, a[72:75] offset:2176\n\t"
        "ds_write_b128 v122, a[76:79] offset:3232\n\t"
        "ds_write_b128 v122, a[80:83] offset:4352\n\t"
        "ds_write_b128 v122, a[84:87] offset:5408\n\t"
        "ds_write_b128 v122, a[88:91] offset:6528\n\t"
        "ds_write_b128 v122, a[92:95] offset:7584\n\t"
        "ds_read_b64_tr_b16 a[64:65], v123 offset:0\n\t"
        "ds_read_b64_tr_b16 a[66:67], v123 offset:128\n\t"
        "ds_read_b64_tr_b16 a[68:69], v123 offset:4352\n\t"
        "ds_read_b64_tr_b16 a[70:71], v123 offset:4480\n\t"
        "s_waitcnt lgkmcnt(8)\n\t"
        "ds_read_b64_tr_b16 a[72:73], v123 offset:32\n\t"
        "ds_read_b64_tr_b16 a[74:75], v123 offset:160\n\t"
        "ds_read_b64_tr_b16 a[76:77], v123 offset:4384\n\t"
        "ds_read_b64_tr_b16 a[78:79], v123 offset:4512\n\t"
        "s_waitcnt lgkmcnt(8)\n\t"
        "ds_read_b64_tr_b16 a[80:81], v123 offset:512\n\t"
        "ds_read_b64_tr_b16 a[82:83], v123 offset:640\n\t"
        "ds_read_b64_tr_b16 a[84:85], v123 offset:4864\n\t"
        "ds_read_b64_tr_b16 a[86:87], v123 offset:4992\n\t"
        "s_waitcnt lgkmcnt(8)\n\t"
        "ds_read_b64_tr_b16 a[88:89], v123 offset:544\n\t"
        "ds_read_b64_tr_b16 a[90:91], v123 offset:672\n\t"
        "ds_read_b64_tr_b16 a[92:93], v123 offset:4896\n\t"
        "ds_read_b64_tr_b16 a[94:95], v123 offset:5024\n\t"
        "s_waitcnt lgkmcnt(8)\n\t"
        "s_lshl_b32 s34, s43, 21\n\t"
        "s_add_u32 s34, s34, 1310720\n\t"
        "buffer_load_dwordx4 a[160:163], v97, s[20:23], s34 offen\n\t"
        "s_add_u32 s34, s34, 0x8000\n\t"
        "buffer_load_dwordx4 a[164:167], v97, s[20:23], s34 offen\n\t"
        "s_add_u32 s34, s34, 0x8000\n\t"
        "buffer_load_dwordx4 a[168:171], v97, s[20:23], s34 offen\n\t"
        "s_add_u32 s34, s34, 0x8000\n\t"
        "buffer_load_dwordx4 a[172:175], v97, s[20:23], s34 offen\n\t"
        "s_add_u32 s34, s34, 0x8000\n\t"
        "buffer_load_dwordx4 a[176:179], v97, s[20:23], s34 offen\n\t"
        "s_add_u32 s34, s34, 0x8000\n\t"
        "buffer_load_dwordx4 a[180:183], v97, s[20:23], s34 offen\n\t"
        "s_add_u32 s34, s34, 0x8000\n\t"
        "buffer_load_dwordx4 a[184:187], v97, s[20:23], s34 offen\n\t"
        "s_add_u32 s34, s34, 0x8000\n\t"
        "buffer_load_dwordx4 a[188:191], v97, s[20:23], s34 offen\n\t"
        "s_waitcnt lgkmcnt(0)\n\t"
        "s_waitcnt vmcnt(16)\n\t"
        "ds_write_b128 v122, a[96:99] offset:0\n\t"
        "ds_write_b128 v122, a[100:103] offset:1056\n\t"
        "ds_write_b128 v122, a[104:107] offset:2176\n\t"
        "ds_write_b128 v122, a[108:111] offset:3232\n\t"
        "ds_write_b128 v122, a[112:115] offset:4352\n\t"
        "ds_write_b128 v122, a[116:119] offset:5408\n\t"
        "ds_write_b128 v122, a[120:123] offset:6528\n\t"
        "ds_write_b128 v122, a[124:127] offset:7584\n\t"
        "ds_read_b64_tr_b16 a[96:97], v123 offset:0\n\t"
        "ds_read_b64_tr_b16 a[98:99], v123 offset:128\n\t"
        "ds_read_b64_tr_b16 a[100:101], v123 offset:4352\n\t"
        "ds_read_b64_tr_b16 a[102:103], v123 offset:4480\n\t"
        "s_waitcnt lgkmcnt(8)\n\t"
        "ds_read_b64_tr_b16 a[104:105], v123 offset:32\n\t"
        "ds_read_b64_tr_b16 a[106:107], v123 offset:160\n\t"
        "ds_read_b64_tr_b16 a[108:109], v123 offset:4384\n\t"
        "ds_read_b64_tr_b16 a[110:111], v123 offset:4512\n\t"
        "s_waitcnt lgkmcnt(8)\n\t"
        "ds_read_b64_tr_b16 a[112:113], v123 offset:512\n\t"
        "ds_read_b64_tr_b16 a[114:115], v123 offset:640\n\t"
        "ds_read_b64_tr_b16 a[116:117], v123 offset:4864\n\t"
        "ds_read_b64_tr_b16 a[118:119], v123 offset:4992\n\t"
        "s_waitcnt lgkmcnt(8)\n\t"
        "ds_read_b64_tr_b16 a[120:121], v123 offset:544\n\t"
        "ds_read_b64_tr_b16 a[122:123], v123 offset:672\n\t"
        "ds_read_b64_tr_b16 a[124:125], v123 offset:4896\n\t"
        "ds_read_b64_tr_b16 a[126:127], v123 offset:5024\n\t"
        "s_waitcnt lgkmcnt(8)\n\t"
        "s_lshl_b32 s34, s43, 21\n\t"
        "s_add_u32 s34, s34, 1572864\n\t"
        "buffer_load_dwordx4 a[192:195], v97, s[20:23], s34 offen\n\t"
        "s_add_u32 s34, s34, 0x8000\n\t"
        "buffer_load_dwordx4 a[196:199], v97, s[20:23], s34 offen\n\t"
        "s_add_u32 s34, s34, 0x8000\n\t"
        "buffer_load_dwordx4 a[200:203], v97, s[20:23], s34 offen\n\t"
        "s_add_u32 s34, s34, 0x8000\n\t"
        "buffer_load_dwordx4 a[204:207], v97, s[20:23], s34 offen\n\t"
        "s_add_u32 s34, s34, 0x8000\n\t"
        "buffer_load_dwordx4 a[208:211], v97, s[20:23], s34 offen\n\t"
        "s_add_u32 s34, s34, 0x8000\n\t"
        "buffer_load_dwordx4 a[212:215], v97, s[20:23], s34 offen\n\t"
        "s_add_u32 s34, s34, 0x8000\n\t"
        "buffer_load_dwordx4 a[216:219], v97, s[20:23], s34 offen\n\t"
        "s_add_u32 s34, s34, 0x8000\n\t"
        "buffer_load_dwordx4 a[220:223], v97, s[20:23], s34 offen\n\t"
        "s_waitcnt lgkmcnt(0)\n\t"
        "s_waitcnt vmcnt(16)\n\t"
        "ds_write_b128 v122, a[128:131] offset:0\n\t"
        "ds_write_b128 v122, a[132:135] offset:1056\n\t"
        "ds_write_b128 v122, a[136:139] offset:2176\n\t"
        "ds_write_b128 v122, a[140:143] offset:3232\n\t"
        "ds_write_b128 v122, a[144:147] offset:4352\n\t"
        "ds_write_b128 v122, a[148:151] offset:5408\n\t"
        "ds_write_b128 v122, a[152:155] offset:6528\n\t"
        "ds_write_b128 v122, a[156:159] offset:7584\n\t"
        "ds_read_b64_tr_b16 a[128:129], v123 offset:0\n\t"
        "ds_read_b64_tr_b16 a[130:131], v123 offset:128\n\t"
        "ds_read_b64_tr_b16 a[132:133], v123 offset:4352\n\t"
        "ds_read_b64_tr_b16 a[134:135], v123 offset:4480\n\t"
        "s_waitcnt lgkmcnt(8)\n\t"
        "ds_read_b64_tr_b16 a[136:137], v123 offset:32\n\t"
        "ds_read_b64_tr_b16 a[138:139], v123 offset:160\n\t"
        "ds_read_b64_tr_b16 a[140:141], v123 offset:4384\n\t"
        "ds_read_b64_tr_b16 a[142:143], v123 offset:4512\n\t"
        "s_waitcnt lgkmcnt(8)\n\t"
        "ds_read_b64_tr_b16 a[144:145], v123 offset:512\n\t"
        "ds_read_b64_tr_b16 a[146:147], v123 offset:640\n\t"
        "ds_read_b64_tr_b16 a[148:149], v123 offset:4864\n\t"
        "ds_read_b64_tr_b16 a[150:151], v123 offset:4992\n\t"
        "s_waitcnt lgkmcnt(8)\n\t"
        "ds_read_b64_tr_b16 a[152:153], v123 offset:544\n\t"
        "ds_read_b64_tr_b16 a[154:155], v123 offset:672\n\t"
        "ds_read_b64_tr_b16 a[156:157], v123 offset:4896\n\t"
        "ds_read_b64_tr_b16 a[158:159], v123 offset:5024\n\t"
        "s_waitcnt lgkmcnt(8)\n\t"
        "s_lshl_b32 s34, s43, 21\n\t"
        "s_add_u32 s34, s34, 1835008\n\t"
        "buffer_load_dwordx4 a[224:227], v97, s[20:23], s34 offen\n\t"
        "s_add_u32 s34, s34, 0x8000\n\t"
        "buffer_load_dwordx4 a[228:231], v97, s[20:23], s34 offen\n\t"
        "s_add_u32 s34, s34, 0x8000\n\t"
        "buffer_load_dwordx4 a[232:235], v97, s[20:23], s34 offen\n\t"
        "s_add_u32 s34, s34, 0x8000\n\t"
        "buffer_load_dwordx4 a[236:239], v97, s[20:23], s34 offen\n\t"
        "s_add_u32 s34, s34, 0x8000\n\t"
        "buffer_load_dwordx4 a[240:243], v97, s[20:23], s34 offen\n\t"
        "s_add_u32 s34, s34, 0x8000\n\t"
        "buffer_load_dwordx4 a[244:247], v97, s[20:23], s34 offen\n\t"
        "s_add_u32 s34, s34, 0x8000\n\t"
        "buffer_load_dwordx4 a[248:251], v97, s[20:23], s34 offen\n\t"
        "s_add_u32 s34, s34, 0x8000\n\t"
        "buffer_load_dwordx4 a[252:255], v97, s[20:23], s34 offen\n\t"
        "s_waitcnt lgkmcnt(0)\n\t"
        "s_waitcnt vmcnt(16)\n\t"
        "ds_write_b128 v122, a[160:163] offset:0\n\t"
        "ds_write_b128 v122, a[164:167] offset:1056\n\t"
        "ds_write_b128 v122, a[168:171] offset:2176\n\t"
        "ds_write_b128 v122, a[172:175] offset:3232\n\t"
        "ds_write_b128 v122, a[176:179] offset:4352\n\t"
        "ds_write_b128 v122, a[180:183] offset:5408\n\t"
        "ds_write_b128 v122, a[184:187] offset:6528\n\t"
        "ds_write_b128 v122, a[188:191] offset:7584\n\t"
        "ds_read_b64_tr_b16 a[160:161], v123 offset:0\n\t"
        "ds_read_b64_tr_b16 a[162:163], v123 offset:128\n\t"
        "ds_read_b64_tr_b16 a[164:165], v123 offset:4352\n\t"
        "ds_read_b64_tr_b16 a[166:167], v123 offset:4480\n\t"
        "s_waitcnt lgkmcnt(8)\n\t"
        "ds_read_b64_tr_b16 a[168:169], v123 offset:32\n\t"
        "ds_read_b64_tr_b16 a[170:171], v123 offset:160\n\t"
        "ds_read_b64_tr_b16 a[172:173], v123 offset:4384\n\t"
        "ds_read_b64_tr_b16 a[174:175], v123 offset:4512\n\t"
        "s_waitcnt lgkmcnt(8)\n\t"
        "ds_read_b64_tr_b16 a[176:177], v123 offset:512\n\t"
        "ds_read_b64_tr_b16 a[178:179], v123 offset:640\n\t"
        "ds_read_b64_tr_b16 a[180:181], v123 offset:4864\n\t"
        "ds_read_b64_tr_b16 a[182:183], v123 offset:4992\n\t"
        "s_waitcnt lgkmcnt(8)\n\t"
        "ds_read_b64_tr_b16 a[184:185], v123 offset:544\n\t"
        "ds_read_b64_tr_b16 a[186:187], v123 offset:672\n\t"
        "ds_read_b64_tr_b16 a[188:189], v123 offset:4896\n\t"
        "ds_read_b64_tr_b16 a[190:191], v123 offset:5024\n\t"
        "s_waitcnt lgkmcnt(8)\n\t"
        "s_waitcnt lgkmcnt(0)\n\t"
        "s_waitcnt vmcnt(8)\n\t"
        "ds_write_b128 v122, a[192:195] offset:0\n\t"
        "ds_write_b128 v122, a[196:199] offset:1056\n\t"
        "ds_write_b128 v122, a[200:203] offset:2176\n\t"
        "ds_write_b128 v122, a[204:207] offset:3232\n\t"
        "ds_write_b128 v122, a[208:211] offset:4352\n\t"
        "ds_write_b128 v122, a[212:215] offset:5408\n\t"
        "ds_write_b128 v122, a[216:219] offset:6528\n\t"
        "ds_write_b128 v122, a[220:223] offset:7584\n\t"
        "ds_read_b64_tr_b16 a[192:193], v123 offset:0\n\t"
        "ds_read_b64_tr_b16 a[194:195], v123 offset:128\n\t"
        "ds_read_b64_tr_b16 a[196:197], v123 offset:4352\n\t"
        "ds_read_b64_tr_b16 a[198:199], v123 offset:4480\n\t"
        "s_waitcnt lgkmcnt(8)\n\t"
        "ds_read_b64_tr_b16 a[200:201], v123 offset:32\n\t"
        "ds_read_b64_tr_b16 a[202:203], v123 offset:160\n\t"
        "ds_read_b64_tr_b16 a[204:205], v123 offset:4384\n\t"
        "ds_read_b64_tr_b16 a[206:207], v123 offset:4512\n\t"
        "s_waitcnt lgkmcnt(8)\n\t"
        "ds_read_b64_tr_b16 a[208:209], v123 offset:512\n\t"
        "ds_read_b64_tr_b16 a[210:211], v123 offset:640\n\t"
        "ds_read_b64_tr_b16 a[212:213], v123 offset:4864\n\t"
        "ds_read_b64_tr_b16 a[214:215], v123 offset:4992\n\t"
        "s_waitcnt lgkmcnt(8)\n\t"
        "ds_read_b64_tr_b16 a[216:217], v123 offset:544\n\t"
        "ds_read_b64_tr_b16 a[218:219], v123 offset:672\n\t"
        "ds_read_b64_tr_b16 a[220:221], v123 offset:4896\n\t"
        "ds_read_b64_tr_b16 a[222:223], v123 offset:5024\n\t"
        "s_waitcnt lgkmcnt(8)\n\t"
        "s_waitcnt lgkmcnt(0)\n\t"
        "s_waitcnt vmcnt(0)\n\t"
        "ds_write_b128 v122, a[224:227] offset:0\n\t"
        "ds_write_b128 v122, a[228:231] offset:1056\n\t"
        "ds_write_b128 v122, a[232:235] offset:2176\n\t"
        "ds_write_b128 v122, a[236:239] offset:3232\n\t"
        "ds_write_b128 v122, a[240:243] offset:4352\n\t"
        "ds_write_b128 v122, a[244:247] offset:5408\n\t"
        "ds_write_b128 v122, a[248:251] offset:6528\n\t"
        "ds_write_b128 v122, a[252:255] offset:7584\n\t"
        "ds_read_b64_tr_b16 a[224:225], v123 offset:0\n\t"
        "ds_read_b64_tr_b16 a[226:227], v123 offset:128\n\t"
        "ds_read_b64_tr_b16 a[228:229], v123 offset:4352\n\t"
        "ds_read_b64_tr_b16 a[230:231], v123 offset:4480\n\t"
        "s_waitcnt lgkmcnt(8)\n\t"
        "ds_read_b64_tr_b16 a[232:233], v123 offset:32\n\t"
        "ds_read_b64_tr_b16 a[234:235], v123 offset:160\n\t"
        "ds_read_b64_tr_b16 a[236:237], v123 offset:4384\n\t"
        "ds_read_b64_tr_b16 a[238:239], v123 offset:4512\n\t"
        "s_waitcnt lgkmcnt(8)\n\t"
        "ds_read_b64_tr_b16 a[240:241], v123 offset:512\n\t"
        "ds_read_b64_tr_b16 a[242:243], v123 offset:640\n\t"
        "ds_read_b64_tr_b16 a[244:245], v123 offset:4864\n\t"
        "ds_read_b64_tr_b16 a[246:247], v123 offset:4992\n\t"
        "s_waitcnt lgkmcnt(8)\n\t"
        "ds_read_b64_tr_b16 a[248:249], v123 offset:544\n\t"
        "ds_read_b64_tr_b16 a[250:251], v123 offset:672\n\t"
        "ds_read_b64_tr_b16 a[252:253], v123 offset:4896\n\t"
        "ds_read_b64_tr_b16 a[254:255], v123 offset:5024\n\t"
        "s_waitcnt lgkmcnt(8)\n\t"
        "s_mov_b32 s35, 33792\n\t"
        "buffer_load_dword v0, off, s[12:15], s35 sc1\n\t"
        "s_waitcnt lgkmcnt(0)\n\t"
        "s_waitcnt vmcnt(0)\n\t"
        "s_mov_b32 s42, 0\n\t"
        "LB_cal%=:\n\t"
        "v_readfirstlane_b32 s34, v0\n\t"
        "s_nop 0\n\t"
        "s_cmp_lg_u32 s34, 0\n\t"
        "s_cbranch_scc1 LB_calok%=\n\t"
        "s_sleep 4\n\t"
        "s_mov_b32 s35, 33792\n\t"
        "buffer_load_dword v0, off, s[12:15], s35 sc1\n\t"
        "s_waitcnt vmcnt(0)\n\t"
        "s_add_u32 s42, s42, 1\n\t"
        "s_cmp_lt_u32 s42, 0x100000\n\t"
        "s_cbranch_scc1 LB_cal%=\n\t"
        "s_movk_i32 s34, 8\n\t"
        "s_branch LB_die%=\n\t"
        "LB_calok%=:\n\t"
        "v_readfirstlane_b32 s34, v0\n\t"
        "s_nop 0\n\t"
        "s_and_b32 s34, s34, 8192\n\t"
        "s_add_u32 s8, s8, s34\n\t"
        "s_addc_u32 s9, s9, 0\n\t"
        "v_min_u32_e32 v109, -2, v109\n\t"
        "buffer_store_dword v109, v116, s[8:11], 0 offen sc1\n\t"
        "s_memtime s[74:75]\n\t"
        "s_memrealtime s[32:33]\n\t"
        "s_waitcnt lgkmcnt(0)\n\t"
        "v_mov_b32_e32 v110, s32\n\t"
        "s_sub_u32 s34, s74, s70\n\t"
        "s_sub_u32 s35, s32, s72\n\t"
        "v_cvt_f32_u32_e32 v2, s34\n\t"
        "v_cvt_f32_u32_e32 v3, s35\n\t"
        "v_rcp_f32_e32 v2, v2\n\t"
        "s_nop 0\n\t"
        "v_mul_f32_e32 v2, v2, v3\n\t"
        "v_mul_f32_e32 v3, 0x45354000, v2\n\t"
        "v_cvt_u32_f32_e32 v3, v3\n\t"
        "s_nop 0\n\t"
        "v_readfirstlane_b32 s34, v3\n\t"
        "s_add_u32 s35, s29, 40\n\t"
        "s_min_u32 s34, s34, s35\n\t"
        "s_sub_u32 s35, s29, 10\n\t"
        "s_max_u32 s29, s34, s35\n\t"
        "v_mul_f32_e32 v3, 0x44d70000, v2\n\t"
        "v_cvt_u32_f32_e32 v3, v3\n\t"
        "s_nop 0\n\t"
        "v_readfirstlane_b32 s34, v3\n\t"
        "s_add_u32 s35, s30, 40\n\t"
        "s_min_u32 s34, s34, s35\n\t"
        "s_sub_u32 s35, s30, 10\n\t"
        "s_max_u32 s30, s34, s35\n\t"
        "s_lshl_b32 s34, s29, 8\n\t"
        "s_lshl_b32 s35, s30, 20\n\t"
        "s_or_b32 s54, s34, s35\n\t"
        "s_add_u32 s34, s54, 8392192\n\t"
        "v_mov_b32_e32 v111, s34\n\t"
        "s_barrier\n\t"
        "s_cmp_lg_u32 s43, 0\n\t"
        "s_cbranch_scc1 LB_st%=\n\t"
        "v_mov_b32_e32 v2, 1\n\t"
        "s_mov_b32 s35, 34304\n\t"
        "s_mov_b64 exec, 1\n\t"
        "buffer_atomic_add v2, off, s[12:15], s35 sc0\n\t"
        "s_mov_b64 exec, -1\n\t"
        "s_waitcnt vmcnt(0)\n\t"
        "v_readfirstlane_b32 s34, v2\n\t"
        "s_nop 0\n\t"
        "s_cmp_lg_u32 s34, 95\n\t"
        "s_cbranch_scc1 LB_st%=\n\t"
        "s_memrealtime s[32:33]\n\t"
        "v_mbcnt_lo_u32_b32 v3, -1, 0\n\t"
        "v_lshlrev_b32_e32 v3, 7, v3\n\t"
        "s_waitcnt lgkmcnt(0)\n\t"
        "s_add_u32 s34, s32, 50\n\t"
        "s_or_b32 s34, s34, 1\n\t"
        "v_mov_b32_e32 v2, s34\n\t"
        "s_mov_b32 s35, 36864\n\t"
        "s_mov_b64 exec, 0xffff\n\t"
        "buffer_store_dword v2, v3, s[12:15], s35 offen sc1\n\t"
        "s_mov_b64 exec, -1\n\t"
        "LB_st%=:\n\t"
        "s_mov_b32 s24, 0\n\t"
        "s_mov_b32 s26, 0\n\t"
        "s_mov_b32 s27, 0x80000\n\t"
        "s_mov_b32 s28, 0x180000\n\t"
        "s_mov_b32 s31, 0\n\t"
        "s_mov_b32 s47, 0\n\t"
        "s_mov_b32 s38, 0xff00ff00\n\t"
        "s_mov_b32 s39, 0xff00ff00\n\t"
        "s_mov_b32 s40, 0\n\t"
        "s_mov_b32 s41, -1\n\t"
        "v_mov_b64_e32 v[0:1], 0\n\t"
        "v_mov_b64_e32 v[2:3], 0\n\t"
        "v_mov_b64_e32 v[4:5], 0\n\t"
        "v_mov_b64_e32 v[6:7], 0\n\t"
        "v_mov_b64_e32 v[8:9], 0\n\t"
        "v_mov_b64_e32 v[10:11], 0\n\t"
        "v_mov_b64_e32 v[12:13], 0\n\t"
        "v_mov_b64_e32 v[14:15], 0\n\t"
        "s_mov_b32 s42, 0\n\t"
        "LB_gw%=:\n\t"
        "buffer_load_dword v16, off, s[12:15], s45 sc1\n\t"
        "s_waitcnt vmcnt(0)\n\t"
        "v_readfirstlane_b32 s25, v16\n\t"
        "s_nop 0\n\t"
        "s_cmp_lg_u32 s25, 0\n\t"
        "s_cbranch_scc1 LB_loop%=\n\t"
        "s_sleep 2\n\t"
        "s_add_u32 s42, s42, 1\n\t"
        "s_cmp_lt_u32 s42, 0x100000\n\t"
        "s_cbranch_scc1 LB_gw%=\n\t"
        "s_movk_i32 s34, 9\n\t"
        "s_branch LB_die%=\n\t"
        ".p2align 8\n\t"
        "LB_loop%=:\n\t"
        "s_mov_b32 s42, 0\n\t"
        "s_sub_u32 s35, s25, 8\n\t"
        "s_waitcnt lgkmcnt(0)\n\t"
        "s_memrealtime s[2:3]\n\t"
        "s_memrealtime s[4:5]\n\t"
        "s_memrealtime s[6:7]\n\t"
        "LB_wa%=:\n\t"
        "s_memrealtime s[32:33]\n\t"
        "s_waitcnt lgkmcnt(6)\n\t"
        "s_sub_u32 s34, s35, s2\n\t"
        "s_cmpk_lt_u32 s34, 0x2000\n\t"
        "s_cbranch_scc0 LB_wa%=x\n\t"
        "s_memrealtime s[2:3]\n\t"
        "s_waitcnt lgkmcnt(6)\n\t"
        "s_sub_u32 s34, s35, s4\n\t"
        "s_cmpk_lt_u32 s34, 0x2000\n\t"
        "s_cbranch_scc0 LB_wa%=x\n\t"
        "s_memrealtime s[4:5]\n\t"
        "s_waitcnt lgkmcnt(6)\n\t"
        "s_sub_u32 s34, s35, s6\n\t"
        "s_cmpk_lt_u32 s34, 0x2000\n\t"
        "s_cbranch_scc0 LB_wa%=x\n\t"
        "s_memrealtime s[6:7]\n\t"
        "s_waitcnt lgkmcnt(6)\n\t"
        "s_sub_u32 s34, s35, s32\n\t"
        "s_cmpk_lt_u32 s34, 0x2000\n\t"
        "s_cbranch_scc1 LB_wa%=\n\t"
        "s_nop 3\n\t"
        "LB_wa%=x:\n\t"
        "s_add_i32 s34, s34, 4\n\t"
        "s_max_i32 s34, s34, 0\n\t"
        "s_bitcmp1_b32 s34, 1\n\t"
        "s_cbranch_scc0 LB_wa%=y\n\t"
        "s_nop 15\n\t"
        "LB_wa%=y:\n\t"
        "s_bitcmp1_b32 s34, 0\n\t"
        "s_cbranch_scc0 LB_wa%=z\n\t"
        "s_nop 9\n\t"
        "LB_wa%=z:\n\t"
        "LB_rd%=:\n\t"
        "buffer_load_dwordx4 v[16:19], v112, s[8:11], s26 offen offset:0 sc1\n\t"
        "s_nop 9\n\t"
        "buffer_load_dwordx4 v[20:23], v112, s[8:11], s26 offen offset:1024 sc1\n\t"
        "s_nop 9\n\t"
        "buffer_load_dwordx4 v[24:27], v112, s[8:11], s26 offen offset:2048 sc1\n\t"
        "s_nop 9\n\t"
        "buffer_load_dwordx4 v[28:31], v112, s[8:11], s26 offen offset:3072 sc1\n\t"
        "LB_go%=:\n\t"
        "s_waitcnt vmcnt(3)\n\t"
        "v_smfmac_f32_16x16x64_bf16 v[0:3], v[16:19], v[128:135], v106\n\t"
        "v_max3_u32 v108, v16, v17, v18\n\t"
        "v_max_u32_e32 v108, v19, v108\n\t"
        "v_smfmac_f32_16x16x64_bf16 v[4:7], v[16:19], v[136:143], v106\n\t"
        "v_smfmac_f32_16x16x64_bf16 v[8:11], v[16:19], v[144:151], v106\n\t"
        "v_smfmac_f32_16x16x64_bf16 v[12:15], v[16:19], v[152:159], v106\n\t"
        "s_waitcnt vmcnt(2)\n\t"
        "v_smfmac_f32_16x16x64_bf16 v[0:3], v[20:23], v[160:167], v106\n\t"
        "v_max3_u32 v108, v20, v21, v108\n\t"
        "v_max3_u32 v108, v22, v23, v108\n\t"
        "v_smfmac_f32_16x16x64_bf16 v[4:7], v[20:23], v[168:175], v106\n\t"
        "v_smfmac_f32_16x16x64_bf16 v[8:11], v[20:23], v[176:183], v106\n\t"
        "v_smfmac_f32_16x16x64_bf16 v[12:15], v[20:23], v[184:191], v106\n\t"
        "s_waitcnt vmcnt(1)\n\t"
        "v_smfmac_f32_16x16x64_bf16 v[0:3], v[24:27], v[192:199], v106\n\t"
        "v_max3_u32 v108, v24, v25, v108\n\t"
        "v_max3_u32 v108, v26, v27, v108\n\t"
        "v_smfmac_f32_16x16x64_bf16 v[4:7], v[24:27], v[200:207], v106\n\t"
        "v_smfmac_f32_16x16x64_bf16 v[8:11], v[24:27], v[208:215], v106\n\t"
        "v_smfmac_f32_16x16x64_bf16 v[12:15], v[24:27], v[216:223], v106\n\t"
        "s_waitcnt vmcnt(0)\n\t"
        "v_smfmac_f32_16x16x64_bf16 v[0:3], v[28:31], v[224:231], v106\n\t"
        "v_max3_u32 v108, v28, v29, v108\n\t"
        "v_max3_u32 v108, v30, v31, v108\n\t"
        "v_smfmac_f32_16x16x64_bf16 v[4:7], v[28:31], v[232:239], v106\n\t"
        "v_cmp_eq_u32_e32 vcc, -1, v108\n\t"
        "v_smfmac_f32_16x16x64_bf16 v[8:11], v[28:31], v[240:247], v106\n\t"
        "v_smfmac_f32_16x16x64_bf16 v[12:15], v[28:31], v[248:255], v106\n\t"
        "s_cbranch_vccnz LB_rdfail%=\n\t"
        "s_nop 0\n\t"
        "v_add_f32_e32 v0, v0, v1\n\t"
        "v_add_f32_e32 v1, v2, v3\n\t"
        "ds_write_b64 v118, v[0:1] offset:0\n\t"
        "v_add_f32_e32 v4, v4, v5\n\t"
        "v_add_f32_e32 v5, v6, v7\n\t"
        "ds_write_b64 v118, v[4:5] offset:512\n\t"
        "v_add_f32_e32 v8, v8, v9\n\t"
        "v_add_f32_e32 v9, v10, v11\n\t"
        "ds_write_b64 v118, v[8:9] offset:1024\n\t"
        "v_add_f32_e32 v12, v12, v13\n\t"
        "v_add_f32_e32 v13, v14, v15\n\t"
        "ds_write_b64 v118, v[12:13] offset:1536\n\t"
        "buffer_store_dwordx2 v[110:111], v117, s[8:11], s26 offen sc1\n\t"
        "s_waitcnt lgkmcnt(0)\n\t"
        "s_barrier\n\t"
        "ds_read_b128 v[16:19], v119 offset:0\n\t"
        "ds_read_b128 v[20:23], v119 offset:2048\n\t"
        "ds_read_b128 v[24:27], v119 offset:4096\n\t"
        "ds_read_b128 v[28:31], v119 offset:6144\n\t"
        "buffer_load_dwordx4 v[64:67], v124, s[8:11], s28 offen sc1\n\t"
        "s_waitcnt lgkmcnt(2)\n\t"
        "v_pk_add_f32 v[16:17], v[16:17], v[20:21]\n\t"
        "v_pk_add_f32 v[18:19], v[18:19], v[22:23]\n\t"
        "s_waitcnt lgkmcnt(0)\n\t"
        "v_pk_add_f32 v[24:25], v[24:25], v[28:29]\n\t"
        "v_pk_add_f32 v[26:27], v[26:27], v[30:31]\n\t"
        "v_pk_add_f32 v[16:17], v[16:17], v[24:25]\n\t"
        "v_pk_add_f32 v[18:19], v[18:19], v[26:27]\n\t"
        "v_cvt_pk_bf16_f32 v20, v16, v17\n\t"
        "v_cvt_pk_bf16_f32 v21, v18, v19\n\t"
        "buffer_store_dwordx2 v[20:21], v114, s[8:11], s26 offen sc1\n\t"
        "s_cmp_eq_u32 s24, 63\n\t"
        "s_cbranch_scc1 LB_end%=\n\t"
        "s_memrealtime s[52:53]\n\t"
        "v_mov_b64_e32 v[24:25], -1\n\t"
        "v_mov_b64_e32 v[32:33], 0\n\t"
        "v_mov_b64_e32 v[34:35], 0\n\t"
        "v_mov_b64_e32 v[36:37], 0\n\t"
        "v_mov_b64_e32 v[38:39], 0\n\t"
        "v_mov_b64_e32 v[40:41], 0\n\t"
        "v_mov_b64_e32 v[42:43], 0\n\t"
        "v_mov_b64_e32 v[44:45], 0\n\t"
        "v_mov_b64_e32 v[46:47], 0\n\t"
        "v_mov_b64_e32 v[0:1], 0\n\t"
        "v_mov_b64_e32 v[2:3], 0\n\t"
        "v_mov_b64_e32 v[4:5], 0\n\t"
        "v_mov_b64_e32 v[6:7], 0\n\t"
        "v_mov_b64_e32 v[8:9], 0\n\t"
        "v_mov_b64_e32 v[10:11], 0\n\t"
        "v_mov_b64_e32 v[12:13], 0\n\t"
        "v_mov_b64_e32 v[14:15], 0\n\t"
        "s_add_u32 s46, s26, 4096\n\t"
        "s_sleep 1\n\t"
        "buffer_load_dwordx2 v[104:105], v113, s[8:11], s26 offen sc1\n\t"
        "buffer_store_dword v24, v116, s[8:11], s28 offen sc1\n\t"
        "s_xor_b32 s34, s26, 0x100000\n\t"
        "buffer_store_dwordx2 v[24:25], v114, s[8:11], s34 offen sc1\n\t"
        "s_add_u32 s66, s49, s43\n\t"
        "s_cmp_lg_u32 s66, 32\n\t"
        "s_cbranch_scc1 LB_norep%=\n\t"
        "s_waitcnt vmcnt(4)\n\t"
        "s_sub_u32 s66, s24, 1\n\t"
        "v_lshrrev_b32_e32 v68, 16, v64\n\t"
        "v_cmp_eq_u32_e32 vcc, s66, v68\n\t"
        "v_subrev_u32_e32 v64, s59, v64\n\t"
        "v_bfe_i32 v64, v64, 0, 16\n\t"
        "v_cndmask_b32_e32 v64, 0, v64, vcc\n\t"
        "v_lshrrev_b32_e32 v68, 16, v65\n\t"
        "v_cmp_eq_u32_e32 vcc, s66, v68\n\t"
        "v_subrev_u32_e32 v65, s59, v65\n\t"
        "v_bfe_i32 v65, v65, 0, 16\n\t"
        "v_cndmask_b32_e32 v65, 0, v65, vcc\n\t"
        "v_lshrrev_b32_e32 v68, 16, v66\n\t"
        "v_cmp_eq_u32_e32 vcc, s66, v68\n\t"
        "v_subrev_u32_e32 v66, s59, v66\n\t"
        "v_bfe_i32 v66, v66, 0, 16\n\t"
        "v_cndmask_b32_e32 v66, 0, v66, vcc\n\t"
        "v_lshrrev_b32_e32 v68, 16, v67\n\t"
        "v_cmp_eq_u32_e32 vcc, s66, v68\n\t"
        "v_subrev_u32_e32 v67, s59, v67\n\t"
        "v_bfe_i32 v67, v67, 0, 16\n\t"
        "v_cndmask_b32_e32 v67, 0, v67, vcc\n\t"
        "v_max3_i32 v64, v64, v65, v66\n\t"
        "v_max_i32_e32 v64, v64, v67\n\t"
        "s_nop 1\n\t"
        "v_max_i32_dpp v64, v64, v64 quad_perm:[1,0,3,2] row_mask:0xf bank_mask:0xf\n\t"
        "s_nop 1\n\t"
        "v_max_i32_dpp v64, v64, v64 quad_perm:[2,3,0,1] row_mask:0xf bank_mask:0xf\n\t"
        "s_nop 1\n\t"
        "v_max_i32_dpp v64, v64, v64 row_ror:4 row_mask:0xf bank_mask:0xf\n\t"
        "s_nop 1\n\t"
        "v_max_i32_dpp v64, v64, v64 row_ror:8 row_mask:0xf bank_mask:0xf\n\t"
        "v_mov_b32_e32 v65, v64\n\t"
        "s_nop 1\n\t"
        "v_permlane16_swap_b32_e32 v64, v65\n\t"
        "v_max_i32_e32 v64, v64, v65\n\t"
        "v_mov_b32_e32 v65, v64\n\t"
        "s_nop 1\n\t"
        "v_permlane32_swap_b32_e32 v64, v65\n\t"
        "v_max_i32_e32 v64, v64, v65\n\t"
        "s_nop 0\n\t"
        "v_readfirstlane_b32 s64, v64\n\t"
        "LB_norep%=:\n\t"
        "s_waitcnt vmcnt(2) lgkmcnt(0)\n\t"
        "s_mov_b32 s61, s29\n\t"
        "s_mov_b32 s62, s30\n\t"
        "s_mov_b32 s63, s31\n\t"
        "v_and_b32_e32 v30, 0xff, v105\n\t"
        "v_cmp_ne_u32_e32 vcc, s24, v30\n\t"
        "s_nop 0\n\t"
        "s_cbranch_vccnz LB_stre%=\n\t"
        "LB_stok%=:\n\t"
        "v_subrev_u32_e32 v104, s25, v104\n\t"
        "s_nop 1\n\t"
        "v_max_i32_dpp v104, v104, v104 quad_perm:[1,0,3,2] row_mask:0xf bank_mask:0xf\n\t"
        "s_nop 1\n\t"
        "v_max_i32_dpp v104, v104, v104 quad_perm:[2,3,0,1] row_mask:0xf bank_mask:0xf\n\t"
        "s_nop 1\n\t"
        "v_max_i32_dpp v104, v104, v104 row_ror:4 row_mask:0xf bank_mask:0xf\n\t"
        "s_nop 1\n\t"
        "v_max_i32_dpp v104, v104, v104 row_ror:8 row_mask:0xf bank_mask:0xf\n\t"
        "s_nop 1\n\t"
        "v_max_i32_dpp v104, v104, v104 row_bcast:15 row_mask:0xa bank_mask:0xf\n\t"
        "s_nop 1\n\t"
        "v_max_i32_dpp v104, v104, v104 row_bcast:31 row_mask:0xc bank_mask:0xf\n\t"
        "v_readfirstlane_b32 s35, v105\n\t"
        "v_readlane_b32 s36, v104, 63\n\t"
        "s_bfe_u32 s29, s35, 0xc0008\n\t"
        "s_lshr_b32 s30, s35, 20\n\t"
        "s_add_u32 s35, s31, s50\n\t"
        "s_mul_i32 s37, s50, 3\n\t"
        "s_min_u32 s35, s35, s37\n\t"
        "s_add_i32 s34, s36, s51\n\t"
        "s_cmp_gt_i32 s34, 0\n\t"
        "s_cselect_b32 s31, s35, 0\n\t"
        "s_cselect_b32 s34, s34, 0\n\t"
        "s_add_u32 s34, s34, s31\n\t"
        "s_add_u32 s37, s25, s34\n\t"
        "s_add_u32 s45, s37, s29\n\t"
        "s_add_u32 s25, s45, s30\n\t"
        "s_add_u32 s66, s49, s43\n\t"
        "s_cmp_lg_u32 s66, 32\n\t"
        "s_cbranch_scc1 LB_noctl%=\n\t"
        "s_cmp_eq_u32 s24, 0\n\t"
        "s_cbranch_scc1 LB_ctl0%=\n\t"
        "s_cmp_lt_u32 s24, 6\n\t"
        "s_cselect_b32 s68, 32, 8\n\t"
        "s_add_i32 s65, s36, s62\n\t"
        "s_cmp_eq_u32 s24, 1\n\t"
        "s_cselect_b32 s34, 6, 0\n\t"
        "s_cselect_b32 s35, 3, 0\n\t"
        "s_sub_u32 s64, s64, s34\n\t"
        "s_sub_u32 s65, s65, s35\n\t"
        "s_lshl_b32 s66, s65, 1\n\t"
        "s_sub_i32 s66, s66, s56\n\t"
        "s_cmp_gt_i32 s66, 56\n\t"
        "s_cbranch_scc1 LB_c1%=\n\t"
        "s_lshl_b32 s66, s65, 1\n\t"
        "s_add_u32 s34, s56, s68\n\t"
        "s_min_i32 s34, s34, s66\n\t"
        "s_sub_u32 s35, s56, s66\n\t"
        "s_ashr_i32 s35, s35, 1\n\t"
        "s_max_i32 s35, s35, 1\n\t"
        "s_sub_u32 s35, s56, s35\n\t"
        "s_max_i32 s56, s34, s35\n\t"
        "s_branch LB_c2%=\n\t"
        "LB_c1%=:\n\t"
        "s_add_u32 s56, s56, 2\n\t"
        "s_add_i32 s66, s64, s57\n\t"
        "s_cmp_gt_i32 s66, s61\n\t"
        "s_cbranch_scc1 LB_c2%=\n\t"
        "s_add_u32 s57, s57, 4\n\t"
        "s_min_u32 s57, s57, 24\n\t"
        "LB_c2%=:\n\t"
        "s_cmp_lg_u32 s63, 0\n\t"
        "s_cbranch_scc1 LB_c4%=\n\t"
        "s_lshl_b32 s66, s64, 1\n\t"
        "s_sub_i32 s66, s66, s55\n\t"
        "s_cmp_gt_i32 s66, 56\n\t"
        "s_cbranch_scc1 LB_c3%=\n\t"
        "s_lshl_b32 s66, s64, 1\n\t"
        "s_add_u32 s34, s55, s68\n\t"
        "s_min_i32 s34, s34, s66\n\t"
        "s_sub_u32 s35, s55, s66\n\t"
        "s_ashr_i32 s35, s35, 1\n\t"
        "s_max_i32 s35, s35, 1\n\t"
        "s_sub_u32 s35, s55, s35\n\t"
        "s_max_i32 s55, s34, s35\n\t"
        "s_branch LB_c4%=\n\t"
        "LB_c3%=:\n\t"
        "s_add_u32 s55, s55, 2\n\t"
        "s_cmp_lt_i32 s60, s58\n\t"
        "s_cbranch_scc1 LB_c4%=\n\t"
        "s_add_u32 s66, s58, 8\n\t"
        "s_cmp_gt_i32 s60, s66\n\t"
        "s_cbranch_scc1 LB_c4%=\n\t"
        "s_add_u32 s58, s58, 4\n\t"
        "s_min_u32 s58, s58, 22\n\t"
        "LB_c4%=:\n\t"
        "s_and_b32 s66, s24, 3\n\t"
        "s_cmp_eq_u32 s66, 0\n\t"
        "s_cselect_b32 s66, 1, 0\n\t"
        "s_cmp_lt_u32 s24, 30\n\t"
        "s_cselect_b32 s34, 8, 8\n\t"
        "s_cselect_b32 s35, 6, 6\n\t"
        "s_sub_u32 s57, s57, s66\n\t"
        "s_max_i32 s57, s57, s34\n\t"
        "s_sub_u32 s58, s58, s66\n\t"
        "s_max_i32 s58, s58, s35\n\t"
        "s_add_u32 s34, s55, 1\n\t"
        "s_lshr_b32 s34, s34, 1\n\t"
        "s_add_u32 s34, s34, s57\n\t"
        "s_min_u32 s34, s34, 1000\n\t"
        "s_lshl_b32 s34, s34, 8\n\t"
        "s_add_u32 s35, s56, 1\n\t"
        "s_lshr_b32 s35, s35, 1\n\t"
        "s_add_u32 s35, s35, s58\n\t"
        "s_min_u32 s35, s35, 1000\n\t"
        "s_lshl_b32 s35, s35, 20\n\t"
        "s_or_b32 s54, s34, s35\n\t"
        "s_branch LB_ctl1%=\n\t"
        "LB_ctl0%=:\n\t"
        "s_movk_i32 s57, 8\n\t"
        "s_movk_i32 s58, 6\n\t"
        "s_sub_u32 s55, s29, 22\n\t"
        "s_lshl_b32 s55, s55, 1\n\t"
        "s_sub_u32 s56, s30, 14\n\t"
        "s_lshl_b32 s56, s56, 1\n\t"
        "LB_ctl1%=:\n\t"
        "s_mov_b32 s59, s37\n\t"
        "s_sub_u32 s60, 0, s36\n\t"
        "LB_noctl%=:\n\t"
        "s_cmp_eq_u32 s24, 0\n\t"
        "s_cselect_b32 s34, 1537, 1\n\t"
        "s_add_u32 s34, s34, s24\n\t"
        "s_add_u32 s34, s34, s54\n\t"
        "v_mov_b32_e32 v111, s34\n\t"
        "s_mov_b32 s42, 0\n\t"
        "s_sub_u32 s35, s45, 8\n\t"
        "s_waitcnt lgkmcnt(0)\n\t"
        "s_memrealtime s[2:3]\n\t"
        "s_memrealtime s[4:5]\n\t"
        "s_memrealtime s[6:7]\n\t"
        "LB_wo%=:\n\t"
        "s_memrealtime s[32:33]\n\t"
        "s_waitcnt lgkmcnt(6)\n\t"
        "s_sub_u32 s34, s35, s2\n\t"
        "s_cmpk_lt_u32 s34, 0x2000\n\t"
        "s_cbranch_scc0 LB_wo%=x\n\t"
        "s_memrealtime s[2:3]\n\t"
        "s_waitcnt lgkmcnt(6)\n\t"
        "s_sub_u32 s34, s35, s4\n\t"
        "s_cmpk_lt_u32 s34, 0x2000\n\t"
        "s_cbranch_scc0 LB_wo%=x\n\t"
        "s_memrealtime s[4:5]\n\t"
        "s_waitcnt lgkmcnt(6)\n\t"
        "s_sub_u32 s34, s35, s6\n\t"
        "s_cmpk_lt_u32 s34, 0x2000\n\t"
        "s_cbranch_scc0 LB_wo%=x\n\t"
        "s_memrealtime s[6:7]\n\t"
        "s_waitcnt lgkmcnt(6)\n\t"
        "s_sub_u32 s34, s35, s32\n\t"
        "s_cmpk_lt_u32 s34, 0x2000\n\t"
        "s_cbranch_scc1 LB_wo%=\n\t"
        "s_nop 3\n\t"
        "LB_wo%=x:\n\t"
        "s_add_i32 s34, s34, 4\n\t"
        "s_max_i32 s34, s34, 0\n\t"
        "s_bitcmp1_b32 s34, 1\n\t"
        "s_cbranch_scc0 LB_wo%=y\n\t"
        "s_nop 15\n\t"
        "LB_wo%=y:\n\t"
        "s_bitcmp1_b32 s34, 0\n\t"
        "s_cbranch_scc0 LB_wo%=z\n\t"
        "s_nop 9\n\t"
        "LB_wo%=z:\n\t"
        "LB_ro%=:\n\t"
        "buffer_load_dwordx4 v[64:67], v115, s[8:11], s26 offen offset:0 sc1\n\t"
        "s_nop 12\n\t"
        "buffer_load_dwordx4 v[68:71], v115, s[8:11], s26 offen offset:1024 sc1\n\t"
        "s_nop 12\n\t"
        "buffer_load_dwordx4 v[72:75], v115, s[8:11], s26 offen offset:2048 sc1\n\t"
        "s_nop 12\n\t"
        "buffer_load_dwordx4 v[76:79], v115, s[8:11], s26 offen offset:3072 sc1\n\t"
        "s_nop 12\n\t"
        "buffer_load_dwordx4 v[80:83], v115, s[8:11], s46 offen offset:0 sc1\n\t"
        "s_nop 12\n\t"
        "buffer_load_dwordx4 v[84:87], v115, s[8:11], s46 offen offset:1024 sc1\n\t"
        "s_nop 12\n\t"
        "buffer_load_dwordx4 v[88:91], v115, s[8:11], s46 offen offset:2048 sc1\n\t"
        "s_nop 12\n\t"
        "buffer_load_dwordx4 v[92:95], v115, s[8:11], s46 offen offset:3072 sc1\n\t"
        "LB_go2%=:\n\t"
        "s_waitcnt vmcnt(7)\n\t"
        "v_smfmac_f32_16x16x64_bf16 v[32:35], v[64:67], a[0:7], v106\n\t"
        "v_max3_u32 v108, v64, v65, v66\n\t"
        "v_max_u32_e32 v108, v67, v108\n\t"
        "v_smfmac_f32_16x16x64_bf16 v[40:43], v[64:67], a[16:23], v106\n\t"
        "v_smfmac_f32_16x16x64_bf16 v[36:39], v[64:67], a[8:15], v106\n\t"
        "v_smfmac_f32_16x16x64_bf16 v[44:47], v[64:67], a[24:31], v106\n\t"
        "s_waitcnt vmcnt(6)\n\t"
        "v_smfmac_f32_16x16x64_bf16 v[32:35], v[68:71], a[32:39], v106\n\t"
        "v_max3_u32 v108, v68, v69, v108\n\t"
        "v_max3_u32 v108, v70, v71, v108\n\t"
        "v_smfmac_f32_16x16x64_bf16 v[40:43], v[68:71], a[48:55], v106\n\t"
        "v_smfmac_f32_16x16x64_bf16 v[36:39], v[68:71], a[40:47], v106\n\t"
        "v_smfmac_f32_16x16x64_bf16 v[44:47], v[68:71], a[56:63], v106\n\t"
        "s_waitcnt vmcnt(5)\n\t"
        "v_smfmac_f32_16x16x64_bf16 v[32:35], v[72:75], a[64:71], v106\n\t"
        "v_max3_u32 v108, v72, v73, v108\n\t"
        "v_max3_u32 v108, v74, v75, v108\n\t"
        "v_smfmac_f32_16x16x64_bf16 v[40:43], v[72:75], a[80:87], v106\n\t"
        "v_smfmac_f32_16x16x64_bf16 v[36:39], v[72:75], a[72:79], v106\n\t"
        "v_smfmac_f32_16x16x64_bf16 v[44:47], v[72:75], a[88:95], v106\n\t"
        "s_waitcnt vmcnt(4)\n\t"
        "v_smfmac_f32_16x16x64_bf16 v[32:35], v[76:79], a[96:103], v106\n\t"
        "v_max3_u32 v108, v76, v77, v108\n\t"
        "v_max3_u32 v108, v78, v79, v108\n\t"
        "v_smfmac_f32_16x16x64_bf16 v[40:43], v[76:79], a[112:119], v106\n\t"
        "v_smfmac_f32_16x16x64_bf16 v[36:39], v[76:79], a[104:111], v106\n\t"
        "v_smfmac_f32_16x16x64_bf16 v[44:47], v[76:79], a[120:127], v106\n\t"
        "s_waitcnt vmcnt(3)\n\t"
        "v_smfmac_f32_16x16x64_bf16 v[32:35], v[80:83], a[128:135], v106\n\t"
        "v_max3_u32 v108, v80, v81, v108\n\t"
        "v_max3_u32 v108, v82, v83, v108\n\t"
        "v_smfmac_f32_16x16x64_bf16 v[40:43], v[80:83], a[144:151], v106\n\t"
        "v_smfmac_f32_16x16x64_bf16 v[36:39], v[80:83], a[136:143], v106\n\t"
        "v_smfmac_f32_16x16x64_bf16 v[44:47], v[80:83], a[152:159], v106\n\t"
        "s_waitcnt vmcnt(2)\n\t"
        "v_smfmac_f32_16x16x64_bf16 v[32:35], v[84:87], a[160:167], v106\n\t"
        "v_max3_u32 v108, v84, v85, v108\n\t"
        "v_max3_u32 v108, v86, v87, v108\n\t"
        "v_smfmac_f32_16x16x64_bf16 v[40:43], v[84:87], a[176:183], v106\n\t"
        "v_smfmac_f32_16x16x64_bf16 v[36:39], v[84:87], a[168:175], v106\n\t"
        "v_smfmac_f32_16x16x64_bf16 v[44:47], v[84:87], a[184:191], v106\n\t"
        "s_waitcnt vmcnt(1)\n\t"
        "v_smfmac_f32_16x16x64_bf16 v[32:35], v[88:91], a[192:199], v106\n\t"
        "v_max3_u32 v108, v88, v89, v108\n\t"
        "v_max3_u32 v108, v90, v91, v108\n\t"
        "v_smfmac_f32_16x16x64_bf16 v[40:43], v[88:91], a[208:215], v106\n\t"
        "v_smfmac_f32_16x16x64_bf16 v[36:39], v[88:91], a[200:207], v106\n\t"
        "v_smfmac_f32_16x16x64_bf16 v[44:47], v[88:91], a[216:223], v106\n\t"
        "s_waitcnt vmcnt(0)\n\t"
        "v_smfmac_f32_16x16x64_bf16 v[32:35], v[92:95], a[224:231], v106\n\t"
        "v_max3_u32 v108, v92, v93, v108\n\t"
        "v_max3_u32 v108, v94, v95, v108\n\t"
        "v_smfmac_f32_16x16x64_bf16 v[40:43], v[92:95], a[240:247], v106\n\t"
        "v_cmp_eq_u32_e32 vcc, -1, v108\n\t"
        "v_smfmac_f32_16x16x64_bf16 v[36:39], v[92:95], a[232:239], v106\n\t"
        "v_smfmac_f32_16x16x64_bf16 v[44:47], v[92:95], a[248:255], v106\n\t"
        "s_cbranch_vccnz LB_rofail%=\n\t"
        "s_nop 0\n\t"
        "v_add_f32_e32 v32, v32, v33\n\t"
        "s_nop 1\n\t"
        "v_add_f32_e32 v40, v42, v43\n\t"
        "v_add_f32_e32 v32, v32, v40\n\t"
        "v_add_f32_e32 v36, v36, v37\n\t"
        "v_add_f32_e32 v44, v46, v47\n\t"
        "v_add_f32_e32 v36, v36, v44\n\t"
        "ds_write2_b32 v120, v32, v36 offset0:0 offset1:64\n\t"
        "s_waitcnt lgkmcnt(0)\n\t"
        "s_barrier\n\t"
        "ds_read_b64 v[64:65], v121 offset:0\n\t"
        "ds_read_b64 v[66:67], v121 offset:512\n\t"
        "ds_read_b64 v[68:69], v121 offset:1024\n\t"
        "ds_read_b64 v[70:71], v121 offset:1536\n\t"
        "s_waitcnt lgkmcnt(2)\n\t"
        "v_pk_add_f32 v[64:65], v[64:65], v[66:67]\n\t"
        "s_waitcnt lgkmcnt(0)\n\t"
        "v_pk_add_f32 v[68:69], v[68:69], v[70:71]\n\t"
        "v_pk_add_f32 v[64:65], v[64:65], v[68:69]\n\t"
        "v_cvt_pk_bf16_f32 v64, v64, v65\n\t"
        "buffer_store_dword v64, v116, s[8:11], s27 offen sc1\n\t"
        "s_memrealtime s[32:33]\n\t"
        "s_waitcnt lgkmcnt(0)\n\t"
        "v_mov_b32_e32 v110, s32\n\t"
        "s_add_u32 s24, s24, 1\n\t"
        "s_mov_b32 s28, s26\n\t"
        "s_mov_b32 s26, s27\n\t"
        "s_add_u32 s27, s27, 0x80000\n\t"
        "s_and_b32 s27, s27, 0x1fffff\n\t"
        "s_branch LB_loop%=\n\t"
        "LB_end%=:\n\t"
        "s_endpgm\n\t"
        "LB_stre%=:\n\t"
        "s_mov_b32 s42, 0\n\t"
        "LB_str2%=:\n\t"
        "buffer_load_dwordx2 v[104:105], v113, s[8:11], s26 offen sc1\n\t"
        "s_waitcnt vmcnt(0)\n\t"
        "v_and_b32_e32 v30, 0xff, v105\n\t"
        "v_cmp_ne_u32_e32 vcc, s24, v30\n\t"
        "s_nop 0\n\t"
        "s_cbranch_vccz LB_stok%=\n\t"
        "s_add_u32 s42, s42, 1\n\t"
        "s_cmp_lt_u32 s42, 0x100000\n\t"
        "s_cbranch_scc1 LB_str2%=\n\t"
        "s_movk_i32 s34, 5\n\t"
        "s_branch LB_die%=\n\t"
        "LB_rdfail%=:\n\t"
        "s_or_b32 s47, s47, 1\n\t"
        "buffer_load_dwordx4 v[16:19], v112, s[8:11], s26 offen offset:0 sc1\n\t"
        "buffer_load_dwordx4 v[20:23], v112, s[8:11], s26 offen offset:1024 sc1\n\t"
        "buffer_load_dwordx4 v[24:27], v112, s[8:11], s26 offen offset:2048 sc1\n\t"
        "buffer_load_dwordx4 v[28:31], v112, s[8:11], s26 offen offset:3072 sc1\n\t"
        "s_nop 7\n\t"
        "v_mov_b64_e32 v[0:1], 0\n\t"
        "v_mov_b64_e32 v[2:3], 0\n\t"
        "v_mov_b64_e32 v[4:5], 0\n\t"
        "v_mov_b64_e32 v[6:7], 0\n\t"
        "v_mov_b64_e32 v[8:9], 0\n\t"
        "v_mov_b64_e32 v[10:11], 0\n\t"
        "v_mov_b64_e32 v[12:13], 0\n\t"
        "v_mov_b64_e32 v[14:15], 0\n\t"
        "s_branch LB_rdre%=w\n\t"
        "LB_rdre%=:\n\t"
        "buffer_load_dwordx4 v[16:19], v112, s[8:11], s26 offen offset:0 sc1\n\t"
        "buffer_load_dwordx4 v[20:23], v112, s[8:11], s26 offen offset:1024 sc1\n\t"
        "buffer_load_dwordx4 v[24:27], v112, s[8:11], s26 offen offset:2048 sc1\n\t"
        "buffer_load_dwordx4 v[28:31], v112, s[8:11], s26 offen offset:3072 sc1\n\t"
        "LB_rdre%=w:\n\t"
        "s_waitcnt vmcnt(0) lgkmcnt(0)\n\t"
        "v_max3_u32 v108, v16, v17, v18\n\t"
        "v_max3_u32 v108, v108, v19, v20\n\t"
        "v_max3_u32 v108, v108, v21, v22\n\t"
        "v_max3_u32 v108, v108, v23, v24\n\t"
        "v_max3_u32 v108, v108, v25, v26\n\t"
        "v_max3_u32 v108, v108, v27, v28\n\t"
        "v_max3_u32 v108, v108, v29, v30\n\t"
        "v_max_u32_e32 v108, v108, v31\n\t"
        "v_cmp_eq_u32_e32 vcc, -1, v108\n\t"
        "s_nop 0\n\t"
        "s_cbranch_vccz LB_go%=\n\t"
        "s_add_u32 s42, s42, 1\n\t"
        "s_cmp_lt_u32 s42, 0x100000\n\t"
        "s_cbranch_scc1 LB_rdre%=\n\t"
        "s_movk_i32 s34, 3\n\t"
        "s_branch LB_die%=\n\t"
        "LB_rofail%=:\n\t"
        "s_or_b32 s47, s47, 4\n\t"
        "buffer_load_dwordx4 v[64:67], v115, s[8:11], s26 offen offset:0 sc1\n\t"
        "buffer_load_dwordx4 v[68:71], v115, s[8:11], s26 offen offset:1024 sc1\n\t"
        "buffer_load_dwordx4 v[72:75], v115, s[8:11], s26 offen offset:2048 sc1\n\t"
        "buffer_load_dwordx4 v[76:79], v115, s[8:11], s26 offen offset:3072 sc1\n\t"
        "buffer_load_dwordx4 v[80:83], v115, s[8:11], s46 offen offset:0 sc1\n\t"
        "buffer_load_dwordx4 v[84:87], v115, s[8:11], s46 offen offset:1024 sc1\n\t"
        "buffer_load_dwordx4 v[88:91], v115, s[8:11], s46 offen offset:2048 sc1\n\t"
        "buffer_load_dwordx4 v[92:95], v115, s[8:11], s46 offen offset:3072 sc1\n\t"
        "s_nop 7\n\t"
        "v_mov_b64_e32 v[32:33], 0\n\t"
        "v_mov_b64_e32 v[34:35], 0\n\t"
        "v_mov_b64_e32 v[36:37], 0\n\t"
        "v_mov_b64_e32 v[38:39], 0\n\t"
        "v_mov_b64_e32 v[40:41], 0\n\t"
        "v_mov_b64_e32 v[42:43], 0\n\t"
        "v_mov_b64_e32 v[44:45], 0\n\t"
        "v_mov_b64_e32 v[46:47], 0\n\t"
        "s_branch LB_rore%=w\n\t"
        "LB_rore%=:\n\t"
        "buffer_load_dwordx4 v[64:67], v115, s[8:11], s26 offen offset:0 sc1\n\t"
        "buffer_load_dwordx4 v[68:71], v115, s[8:11], s26 offen offset:1024 sc1\n\t"
        "buffer_load_dwordx4 v[72:75], v115, s[8:11], s26 offen offset:2048 sc1\n\t"
        "buffer_load_dwordx4 v[76:79], v115, s[8:11], s26 offen offset:3072 sc1\n\t"
        "buffer_load_dwordx4 v[80:83], v115, s[8:11], s46 offen offset:0 sc1\n\t"
        "buffer_load_dwordx4 v[84:87], v115, s[8:11], s46 offen offset:1024 sc1\n\t"
        "buffer_load_dwordx4 v[88:91], v115, s[8:11], s46 offen offset:2048 sc1\n\t"
        "buffer_load_dwordx4 v[92:95], v115, s[8:11], s46 offen offset:3072 sc1\n\t"
        "LB_rore%=w:\n\t"
        "s_waitcnt vmcnt(0) lgkmcnt(0)\n\t"
        "v_max3_u32 v108, v64, v65, v66\n\t"
        "v_max3_u32 v108, v108, v67, v68\n\t"
        "v_max3_u32 v108, v108, v69, v70\n\t"
        "v_max3_u32 v108, v108, v71, v72\n\t"
        "v_max3_u32 v108, v108, v73, v74\n\t"
        "v_max3_u32 v108, v108, v75, v76\n\t"
        "v_max3_u32 v108, v108, v77, v78\n\t"
        "v_max3_u32 v108, v108, v79, v80\n\t"
        "v_max3_u32 v108, v108, v81, v82\n\t"
        "v_max3_u32 v108, v108, v83, v84\n\t"
        "v_max3_u32 v108, v108, v85, v86\n\t"
        "v_max3_u32 v108, v108, v87, v88\n\t"
        "v_max3_u32 v108, v108, v89, v90\n\t"
        "v_max3_u32 v108, v108, v91, v92\n\t"
        "v_max3_u32 v108, v108, v93, v94\n\t"
        "v_max_u32_e32 v108, v108, v95\n\t"
        "v_cmp_eq_u32_e32 vcc, -1, v108\n\t"
        "s_nop 0\n\t"
        "s_cbranch_vccz LB_go2%=\n\t"
        "s_add_u32 s42, s42, 1\n\t"
        "s_cmp_lt_u32 s42, 0x100000\n\t"
        "s_cbranch_scc1 LB_rore%=\n\t"
        "s_movk_i32 s34, 4\n\t"
        "s_branch LB_die%=\n\t"
        "LB_die%=:\n\t"
        "v_mov_b32_e32 v30, s34\n\t"
        "s_mov_b32 s35, 33024\n\t"
        "s_mov_b64 exec, 1\n\t"
        "buffer_store_dword v30, off, s[12:15], s35 sc1\n\t"
        "s_endpgm\n\t"
        ".Lk_end_B:\n\t"
        :: REC_S_IN, "{v96}"(g_v), "{v97}"(g_o), "{v98}"(wb0), "{v99}"(wb1), "{v100}"(rb0), "{v101}"(rb0 ^ 32u), "{v102}"(rb0 ^ 64u), "{v103}"(rb0 ^ 96u),
           "{v106}"((l & 1) ? 0xEEEEEEEEu : 0x44444444u), "{v[108:109]}"(m0p),
           "{v112}"(a_mem), "{v113}"((24 + cp) * 16384 + l * 32), "{v114}"(a_vmw), "{v115}"(a_or), "{v116}"(a_mw), "{v117}"(a_sw),
           "{v118}"(lb + w * 2048 + (g >> 1) * 256 + n * 16 + (g & 1) * 8), "{v119}"(lb + (w & 1) * 1024 + l * 16), "{v120}"(lb + LB_R + w * 512 + l * 4),
           "{v121}"(lb + LB_R + l * 8), "{v122}"(stg + l * 16), "{v123}"(o_r), "{v124}"(rb + w == 32 ? 28 * 16384 + (l & 31) * 128 : OOB)
        : "memory", "vcc", "scc", "v0", "v255", "a0", "a255", "s101");
    __builtin_unreachable();
}
__device__ __forceinline__ void rec_helper(const Params& p, rsrc_t r, unsigned lb, const int wv, const int hi) {
    const int tid = otid(), l = tid & 63, xq = hi >> 3;
    const rsrc_t rh = __builtin_amdgcn_make_buffer_rsrc(p.ws + ho_base(p.ws), 0, HO_BYTES, 0x00020000);
    const v4u ones = {0xFFFFFFFFu, 0xFFFFFFFFu, 0xFFFFFFFFu, 0xFFFFFFFFu};
    const bool meas = (hi & 7) < 2;
    if (meas) {
        const unsigned ca = l >= 8 ? OOB : (31 + wv * 32) * 16384 + hi * 256 + l * 16, tend = rtnow() + CAL_T;
        unsigned best0 = ~0u, best1 = ~0u;
#pragma unroll 1
        for (int i = 0; i < 64 && (i < 3 || (int)(rtnow() - tend) < 0); ++i) {
            const unsigned t0 = (unsigned)__builtin_amdgcn_s_memtime(); __builtin_amdgcn_s_waitcnt(0);
            st16(rh, ca, ones); DRAIN();
            const unsigned t1 = (unsigned)__builtin_amdgcn_s_memtime(); __builtin_amdgcn_s_waitcnt(0);
            st16(rh, ca + 8192, ones); DRAIN();
            const unsigned t2 = (unsigned)__builtin_amdgcn_s_memtime(); __builtin_amdgcn_s_waitcnt(0);
            best0 = min(best0, t1 - t0); best1 = min(best1, t2 - t1); }
        if (l < 2) __hip_atomic_fetch_max((unsigned*)(p.ws + F_CAL + 128) + l, 65535u - min(l ? best1 : best0, 65535u), __ATOMIC_RELAXED, __HIP_MEMORY_SCOPE_AGENT);
    }
    auto touch = [&](int z0, int z1) {
#pragma unroll 1
        for (int z = z0 + ((hi & 7) - 2) * 4 + wv; z < z1; z += 24) { const bf16_t* q;
            if (z < 48) q = ((z & 1) ? p.Wk : p.Wq) + (size_t)(((((z & 1) * 2 + 1) * 4 + 4 + (z >> 2)) & 15) * 64 + l) * 4096 + (xq * 8 + 1 + (z & 2) * 2) * 128 + 64;
            else if (z < 112) { const int y = z - 48; q = p.Wo + (size_t)(((y & 3) * 8 + (y >> 3)) * 128 + ((y >> 2) & 1) * 64 + l) * 1024 + (xq & 1) * 512 + 192; }
            else { const int s7 = (z - 112) % 7, y = (z - 112) / 7; q = p.Wo + (size_t)(((y & 3) * 8 + 8 - T2U + (y >> 3)) * 128 + ((y >> 2) & 1) * 64 + l) * 1024 + (xq & 1) * 512 + (s7 + (s7 >= 3)) * 64; }
            touch_l2(q, lb + 1024); } };
    if (!meas) touch(0, TPRE);
    st16(r, tid < 64 ? F_KVQ + hi * 1024 + tid * 16 : tid < 72 ? F_PROG + hi * 128 + (tid - 64) * 16 : tid < 80 ? F_CVY + hi * 128 + (tid - 72) * 16 : OOB, (v4u){0u, 0u, 0u, 0u});
#pragma unroll
    for (int i = 0; i < 2; ++i) st16(r, W_OX + 63 * 262144 + hi * 8192 + i * 4096 + tid * 16, ones);
#pragma unroll
    for (int i = 0; i < 16; ++i) st16(rh, hi * 65536 + i * 4096 + tid * 16, ones);
    DRAIN();
    __syncthreads();
    if (tid == 0 && __hip_atomic_fetch_add((unsigned*)(p.ws + F_CAL + 256), 1u, __ATOMIC_RELAXED, __HIP_MEMORY_SCOPE_AGENT) == 31u) {
        const v2u c = ld8(r, F_CAL + 128); st4(r, F_CAL, c.x < c.y ? 8193u : 1u); }
    if (!meas) touch(TPRE, 112 + 56 * T2U);
    DRAIN();
}
constexpr int CV_RP = 80;
constexpr int CV_U = 96 * CV_RP;
constexpr int CV_KP = 4416;
constexpr int CV_K0 = 8 * CV_U;
#define NAP() do { __builtin_amdgcn_sched_barrier(0); __builtin_amdgcn_s_sleep(XSLP); __builtin_amdgcn_sched_barrier(0); } while (0)
__device__ __forceinline__ void gate_begin(const Params& p, const int jb, const int wv, v4u (&xg)[4][2], v4u (&gw)[2]) {
    const int l = otid() & 63, t0 = (jb * 4 + wv) * 4;
#pragma unroll
    for (int q = 0; q < 2; ++q) gw[q] = *(const v4u*)(p.gate_w + q * 512 + l * 8);
#pragma unroll
    for (int i = 0; i < 4; ++i)
#pragma unroll
        for (int q = 0; q < 2; ++q) { xg[i][q] = *(const v4u*)(p.Aq + (size_t)(t0 + i) * D + q * 512 + l * 8); NAP(); }
}
__device__ __forceinline__ void gate_end(rsrc_t r, const int jb, const int wv, const v4u (&xg)[4][2], const v4u (&gw)[2]) {
    const int l = otid() & 63;
    float mine = 0.f;
#pragma unroll
    for (int i = 0; i < 4; ++i) { float acc = 0.f;
#pragma unroll
        for (int q = 0; q < 2; ++q)
#pragma unroll
            for (int e = 0; e < 4; ++e) { const unsigned pr = pack2(blo(xg[i][q][e]) * blo(gw[q][e]), bhi(xg[i][q][e]) * bhi(gw[q][e])); acc += blo(pr); acc += bhi(pr); }
        for (int o = 32; o > 0; o >>= 1) acc += __shfl_xor(acc, o);
        if ((l & 3) == i) mine = acc; }
    if (l < 32) { const float z = rbfh(mine); st4(r, W_GATE + (jb * 4 + wv) * 128 + l * 4, __float_as_uint(rbfh(1.f / (1.f + expf(-z))))); }
}
__device__ __forceinline__ void conv_prep(const Params& p, rsrc_t r, const int wv, const int hi, const int G, const int jb) {
    const int tid = otid(), Q = tid >> 2, cg = tid & 3, ch0 = hi * 32 + cg * 8;
    v4u sk[4]; float sv[8][4];
#pragma unroll
    for (int q = 0; q < 4; ++q) sk[q] = *(const v4u*)(p.shift_k + ch0 * 4 + q * 8);
#pragma unroll
    for (int c8 = 0; c8 < 8; ++c8)
#pragma unroll
        for (int tp = 0; tp < 4; ++tp) { const unsigned sw = sk[c8 >> 1][(c8 & 1) * 2 + (tp >> 1)]; sv[c8][tp] = (tp & 1) ? bhi(sw) : blo(sw); }
    const rsrc_t rk = __builtin_amdgcn_make_buffer_rsrc((void*)p.Ak, 0, S * D * 2, 0x00020000), rv = __builtin_amdgcn_make_buffer_rsrc((void*)p.Av, 0, S * D * 2, 0x00020000),
                 rq = __builtin_amdgcn_make_buffer_rsrc((void*)p.Aq, 0, S * D * 2, 0x00020000);
    {
        const unsigned ro = (unsigned)((Q * 32 + G * 8) * D + ch0) * 2u, uo = hi * 131072 + tid * 16;
        v4u kr[11], u[8], qv[8], xg[4][2], gw[2];
        gate_begin(p, jb, wv, xg, gw);
#pragma unroll
        for (int i = 0; i < 11; ++i) { kr[i] = __builtin_amdgcn_raw_buffer_load_b128(rk, (i < 3 && Q + G == 0) ? OOB : ro + (i - 3) * 2048, 0, 2); NAP(); }
#pragma unroll
        for (int i = 0; i < 8; ++i) { u[i] = __builtin_amdgcn_raw_buffer_load_b128(rv, ro + i * 2048, 0, 2); NAP(); }
#pragma unroll
        for (int i = 0; i < 8; ++i) { qv[i] = __builtin_amdgcn_raw_buffer_load_b128(rq, ro + i * 2048, 0, 0); NAP(); }
        gate_end(r, jb, wv, xg, gw);
        __builtin_amdgcn_sched_barrier(0);
#pragma unroll
        for (int q = 0; q < 4; ++q) {
#pragma unroll
            for (int i = 0; i < 8; ++i) {
                unsigned ks = pack2(blo(kr[i][q]) * sv[2 * q][3], bhi(kr[i][q]) * sv[2 * q + 1][3]), pr;
#pragma unroll
                for (int d = 1; d < 4; ++d) { pr = pack2(blo(kr[i + d][q]) * sv[2 * q][3 - d], bhi(kr[i + d][q]) * sv[2 * q + 1][3 - d]); ks = pack2(blo(ks) + blo(pr), bhi(ks) + bhi(pr)); }
                u[i][q] = pack2(blo(ks) * blo(u[i][q]), bhi(ks) * bhi(u[i][q])); }
            v4u lo, hh;
#pragma unroll
            for (int z = 0; z < 4; ++z) { const unsigned x = u[7 - 2 * z][q], y = u[6 - 2 * z][q]; lo[z] = __builtin_amdgcn_perm(y, x, 0x05040100u); hh[z] = __builtin_amdgcn_perm(y, x, 0x07060302u); }
            st16(r, W_U + uo + q * 32768 + G * 4096, lo); NAP(); st16(r, W_U + uo + q * 32768 + 16384 + G * 4096, hh); NAP();
#pragma unroll
            for (int z = 0; z < 2; ++z) { st16(r, W_Y + uo + q * 32768 + (2 * G + z) * 4096, (v4u){qv[4 * z][q], qv[4 * z + 1][q], qv[4 * z + 2][q], qv[4 * z + 3][q]}); NAP(); }
        }
    }
    DRAIN(); __syncthreads();
    if (tid == 0) __hip_atomic_fetch_add((unsigned*)(p.ws + F_CONV), 1u, __ATOMIC_RELAXED, __HIP_MEMORY_SCOPE_AGENT);
    if (tid == 0) __hip_atomic_fetch_add((unsigned*)(p.ws + F_CVY + hi * 128 + 64), 1u, __ATOMIC_RELAXED, __HIP_MEMORY_SCOPE_AGENT);
}
__device__ __forceinline__ bool conv_core(const Params& p, rsrc_t r, unsigned char* lds, const int wv, const int un, const int nt, const int ro) {
    const int tid = otid(), w = wv, l = tid & 63, Q = tid >> 2, cg = tid & 3;
    const rsrc_t rs = __builtin_amdgcn_make_buffer_rsrc((void*)p.ssm_k, 0, S * D * 2, 0x00020000);
    v4u kraw[4]; v2u kext[4];
    auto kload = [&](int n) { const unsigned o = (unsigned)(((un >> 2) * 32 + w * 8 + (un & 3) * 2 + n) * S + l * 8) * 2u;
#pragma unroll
        for (int i = 0; i < 4; ++i) { kraw[i] = __builtin_amdgcn_raw_buffer_load_b128(rs, o + i * 1024, 0, 2);
            kext[i] = __builtin_amdgcn_raw_buffer_load_b64(rs, (i == 3 && l == 63) ? OOB : o + i * 1024 + 16, 0, 2); } };
    kload(0);
    for (int i = tid; i < 8 * 160; i += 256) *(v4u*)(lds + (i / 160) * CV_U + (i % 160) * 16) = (v4u){0u, 0u, 0u, 0u};
    unsigned char* kc = lds + CV_K0 + w * 4 * CV_KP;
    if (l < 32) {
#pragma unroll
        for (int q = 0; q < 4; ++q) *(unsigned*)(kc + q * CV_KP + l * 4) = 0u; }
    if (!wait_word(r, F_CVY + (un >> 2) * 128 + 64, 4u, 2048u)) return false;
    v4u ur[8], qr[8];
#pragma unroll
    for (int i = 0; i < 8; ++i) ur[i] = ld16(r, W_U + un * 32768 + i * 4096 + tid * 16);
#pragma unroll
    for (int i = 0; i < 8; ++i) qr[i] = ld16(r, W_Y + un * 32768 + i * 4096 + tid * 16);
    const unsigned dv = *(const unsigned*)(p.ssm_D + (un >> 2) * 32 + cg * 8 + (un & 3) * 2);
    const int b = l & 31, kg = l >> 5;
    const unsigned kb = (unsigned)(size_t)kc + (b & 3) * CV_KP + ((b & 28) - 8 * kg + 56 - 16) * 2, ub0 = (unsigned)(size_t)lds + (1 + b) * CV_RP + kg * 16;
    unsigned char* const urow = lds + 2 * cg * CV_U + (32 + Q) * CV_RP;
#pragma unroll
    for (int i = 0; i < 8; ++i) *(v4u*)(urow + (i >> 2) * CV_U + (i & 3) * 16) = ur[i];
    __syncthreads();
    const v4u so = mk_srd(p.Wo, 1024 * 4096 * 2); const unsigned go = (w * 1024 + (l >> 3)) * 2048 + nt * 128 + (l & 7) * 16; unsigned t0, t1;
#pragma unroll
        for (int ci = 0; ci < 2; ++ci) {
            const int c8 = 2 * w + ci;
#pragma unroll
            for (int i = 0; i < 4; ++i) { const v4u e = kraw[i]; const v2u f = kext[i]; unsigned char* q = kc + 128 + (i * 64 + l) * 16;
                const unsigned s0 = __builtin_amdgcn_alignbit(e.y, e.x, 16), s1 = __builtin_amdgcn_alignbit(e.z, e.y, 16), s2 = __builtin_amdgcn_alignbit(e.w, e.z, 16),
                               s3 = __builtin_amdgcn_alignbit(f.x, e.w, 16), s4 = __builtin_amdgcn_alignbit(f.y, f.x, 16);
                *(v4u*)q = (v4u){s0, s1, s2, s3}; *(v4u*)(q + CV_KP) = (v4u){e.y, e.z, e.w, f.x}; *(v4u*)(q + 2 * CV_KP) = (v4u){s1, s2, s3, s4}; *(v4u*)(q + 3 * CV_KP) = (v4u){e.z, e.w, f.x, f.y};
                if (i == 0 && l == 0) {
                    *(v2u*)(q - 8) = (v2u){0u, e.x << 16}; *(v2u*)(q + CV_KP - 8) = (v2u){0u, e.x}; *(v2u*)(q + 2 * CV_KP - 8) = (v2u){e.x << 16, s0}; *(v2u*)(q + 3 * CV_KP - 8) = (v2u){e.x, e.y}; } }
            if (ci == 0) kload(1);
            __builtin_amdgcn_wave_barrier();
            f32x16 acc0, acc1;
            if (ci == 0) asm volatile(
                "ds_read_b64 v[192:193], %[kb] offset:32\n\t"
                "ds_read_b64 v[194:195], %[kb] offset:40\n\t"
                "ds_read_b128 v[196:199], %[ub] offset:5040\n\t"
                "ds_read_b128 v[200:203], %[ub] offset:2480\n\t"
                "ds_read_b64 v[204:205], %[kb] offset:2080\n\t"
                "ds_read_b64 v[206:207], %[kb] offset:2088\n\t"
                "ds_read_b64 v[208:209], %[kb] offset:0\n\t"
                "ds_read_b64 v[210:211], %[kb] offset:8\n\t"
                "ds_read_b128 v[212:215], %[ub] offset:5072\n\t"
                "ds_read_b128 v[216:219], %[ub] offset:2512\n\t"
                "ds_read_b64 v[220:221], %[kb] offset:2048\n\t"
                "ds_read_b64 v[222:223], %[kb] offset:2056\n\t"
                "s_waitcnt lgkmcnt(9)\n\t"
                "v_mfma_f32_32x32x16_bf16 %[a1], v[192:195], v[196:199], 0\n\t"
                "ds_read_b64 v[224:225], %[kb] offset:96\n\t"
                "ds_read_b64 v[226:227], %[kb] offset:104\n\t"
                "ds_read_b128 v[228:231], %[ub] offset:4960\n\t"
                "s_waitcnt lgkmcnt(11)\n\t"
                "v_mfma_f32_32x32x16_bf16 %[a0], v[192:195], v[200:203], 0\n\t"
                "ds_read_b128 v[232:235], %[ub] offset:2400\n\t"
                "s_add_u32 %[t1], %[ro], 0\n\t"
                "s_and_b32 %[t1], %[t1], 7\n\t"
                "s_lshl_b32 %[t1], %[t1], 18\n\t"
                "s_add_u32 %[t0], %[t1], 0\n\t"
                "buffer_load_dwordx4 a[0:3], %[go], %[so], %[t0] offen\n\t"
                "s_waitcnt lgkmcnt(10)\n\t"
                "v_mfma_f32_32x32x16_bf16 %[a1], v[204:207], v[200:203], %[a1]\n\t"
                "ds_read_b64 v[236:237], %[kb] offset:2144\n\t"
                "ds_read_b64 v[238:239], %[kb] offset:2152\n\t"
                "s_waitcnt lgkmcnt(9)\n\t"
                "v_mfma_f32_32x32x16_bf16 %[a1], v[208:211], v[212:215], %[a1]\n\t"
                "ds_read_b64 v[240:241], %[kb] offset:64\n\t"
                "ds_read_b64 v[242:243], %[kb] offset:72\n\t"
                "ds_read_b128 v[244:247], %[ub] offset:4992\n\t"
                "s_waitcnt lgkmcnt(11)\n\t"
                "v_mfma_f32_32x32x16_bf16 %[a0], v[208:211], v[216:219], %[a0]\n\t"
                "ds_read_b128 v[248:251], %[ub] offset:2432\n\t"
                "s_waitcnt lgkmcnt(10)\n\t"
                "v_mfma_f32_32x32x16_bf16 %[a1], v[220:223], v[216:219], %[a1]\n\t"
                "ds_read_b64 v[252:253], %[kb] offset:2112\n\t"
                "ds_read_b64 v[254:255], %[kb] offset:2120\n\t"
                "s_waitcnt lgkmcnt(9)\n\t"
                "v_mfma_f32_32x32x16_bf16 %[a1], v[224:227], v[228:231], %[a1]\n\t"
                "ds_read_b64 v[192:193], %[kb] offset:160\n\t"
                "ds_read_b64 v[194:195], %[kb] offset:168\n\t"
                "ds_read_b128 v[196:199], %[ub] offset:4880\n\t"
                "s_waitcnt lgkmcnt(11)\n\t"
                "v_mfma_f32_32x32x16_bf16 %[a0], v[224:227], v[232:235], %[a0]\n\t"
                "ds_read_b128 v[200:203], %[ub] offset:2320\n\t"
                "s_add_u32 %[t0], %[t1], 16384\n\t"
                "buffer_load_dwordx4 a[4:7], %[go], %[so], %[t0] offen\n\t"
                "s_waitcnt lgkmcnt(10)\n\t"
                "v_mfma_f32_32x32x16_bf16 %[a1], v[236:239], v[232:235], %[a1]\n\t"
                "ds_read_b64 v[204:205], %[kb] offset:2208\n\t"
                "ds_read_b64 v[206:207], %[kb] offset:2216\n\t"
                "s_waitcnt lgkmcnt(9)\n\t"
                "v_mfma_f32_32x32x16_bf16 %[a1], v[240:243], v[244:247], %[a1]\n\t"
                "ds_read_b64 v[208:209], %[kb] offset:128\n\t"
                "ds_read_b64 v[210:211], %[kb] offset:136\n\t"
                "ds_read_b128 v[212:215], %[ub] offset:4912\n\t"
                "s_waitcnt lgkmcnt(11)\n\t"
                "v_mfma_f32_32x32x16_bf16 %[a0], v[240:243], v[248:251], %[a0]\n\t"
                "ds_read_b128 v[216:219], %[ub] offset:2352\n\t"
                "s_waitcnt lgkmcnt(10)\n\t"
                "v_mfma_f32_32x32x16_bf16 %[a1], v[252:255], v[248:251], %[a1]\n\t"
                "ds_read_b64 v[220:221], %[kb] offset:2176\n\t"
                "ds_read_b64 v[222:223], %[kb] offset:2184\n\t"
                "s_waitcnt lgkmcnt(9)\n\t"
                "v_mfma_f32_32x32x16_bf16 %[a1], v[192:195], v[196:199], %[a1]\n\t"
                "ds_read_b64 v[224:225], %[kb] offset:224\n\t"
                "ds_read_b64 v[226:227], %[kb] offset:232\n\t"
                "ds_read_b128 v[228:231], %[ub] offset:4800\n\t"
                "s_waitcnt lgkmcnt(11)\n\t"
                "v_mfma_f32_32x32x16_bf16 %[a0], v[192:195], v[200:203], %[a0]\n\t"
                "ds_read_b128 v[232:235], %[ub] offset:2240\n\t"
                "s_add_u32 %[t0], %[t1], 32768\n\t"
                "buffer_load_dwordx4 a[8:11], %[go], %[so], %[t0] offen\n\t"
                "s_waitcnt lgkmcnt(10)\n\t"
                "v_mfma_f32_32x32x16_bf16 %[a1], v[204:207], v[200:203], %[a1]\n\t"
                "ds_read_b64 v[236:237], %[kb] offset:2272\n\t"
                "ds_read_b64 v[238:239], %[kb] offset:2280\n\t"
                "s_waitcnt lgkmcnt(9)\n\t"
                "v_mfma_f32_32x32x16_bf16 %[a1], v[208:211], v[212:215], %[a1]\n\t"
                "ds_read_b64 v[240:241], %[kb] offset:192\n\t"
                "ds_read_b64 v[242:243], %[kb] offset:200\n\t"
                "ds_read_b128 v[244:247], %[ub] offset:4832\n\t"
                "s_waitcnt lgkmcnt(11)\n\t"
                "v_mfma_f32_32x32x16_bf16 %[a0], v[208:211], v[216:219], %[a0]\n\t"
                "ds_read_b128 v[248:251], %[ub] offset:2272\n\t"
                "s_waitcnt lgkmcnt(10)\n\t"
                "v_mfma_f32_32x32x16_bf16 %[a1], v[220:223], v[216:219], %[a1]\n\t"
                "ds_read_b64 v[252:253], %[kb] offset:2240\n\t"
                "ds_read_b64 v[254:255], %[kb] offset:2248\n\t"
                "s_waitcnt lgkmcnt(9)\n\t"
                "v_mfma_f32_32x32x16_bf16 %[a1], v[224:227], v[228:231], %[a1]\n\t"
                "ds_read_b64 v[192:193], %[kb] offset:288\n\t"
                "ds_read_b64 v[194:195], %[kb] offset:296\n\t"
                "ds_read_b128 v[196:199], %[ub] offset:4720\n\t"
                "s_waitcnt lgkmcnt(11)\n\t"
                "v_mfma_f32_32x32x16_bf16 %[a0], v[224:227], v[232:235], %[a0]\n\t"
                "ds_read_b128 v[200:203], %[ub] offset:2160\n\t"
                "s_add_u32 %[t0], %[t1], 49152\n\t"
                "buffer_load_dwordx4 a[12:15], %[go], %[so], %[t0] offen\n\t"
                "s_waitcnt lgkmcnt(10)\n\t"
                "v_mfma_f32_32x32x16_bf16 %[a1], v[236:239], v[232:235], %[a1]\n\t"
                "ds_read_b64 v[204:205], %[kb] offset:2336\n\t"
                "ds_read_b64 v[206:207], %[kb] offset:2344\n\t"
                "s_waitcnt lgkmcnt(9)\n\t"
                "v_mfma_f32_32x32x16_bf16 %[a1], v[240:243], v[244:247], %[a1]\n\t"
                "ds_read_b64 v[208:209], %[kb] offset:256\n\t"
                "ds_read_b64 v[210:211], %[kb] offset:264\n\t"
                "ds_read_b128 v[212:215], %[ub] offset:4752\n\t"
                "s_waitcnt lgkmcnt(11)\n\t"
                "v_mfma_f32_32x32x16_bf16 %[a0], v[240:243], v[248:251], %[a0]\n\t"
                "ds_read_b128 v[216:219], %[ub] offset:2192\n\t"
                "s_add_u32 %[t0], %[t1], 65536\n\t"
                "buffer_load_dwordx4 a[16:19], %[go], %[so], %[t0] offen\n\t"
                "s_waitcnt lgkmcnt(10)\n\t"
                "v_mfma_f32_32x32x16_bf16 %[a1], v[252:255], v[248:251], %[a1]\n\t"
                "ds_read_b64 v[220:221], %[kb] offset:2304\n\t"
                "ds_read_b64 v[222:223], %[kb] offset:2312\n\t"
                "s_waitcnt lgkmcnt(9)\n\t"
                "v_mfma_f32_32x32x16_bf16 %[a1], v[192:195], v[196:199], %[a1]\n\t"
                "ds_read_b64 v[224:225], %[kb] offset:352\n\t"
                "ds_read_b64 v[226:227], %[kb] offset:360\n\t"
                "ds_read_b128 v[228:231], %[ub] offset:4640\n\t"
                "s_waitcnt lgkmcnt(11)\n\t"
                "v_mfma_f32_32x32x16_bf16 %[a0], v[192:195], v[200:203], %[a0]\n\t"
                "ds_read_b128 v[232:235], %[ub] offset:2080\n\t"
                "s_waitcnt lgkmcnt(10)\n\t"
                "v_mfma_f32_32x32x16_bf16 %[a1], v[204:207], v[200:203], %[a1]\n\t"
                "ds_read_b64 v[236:237], %[kb] offset:2400\n\t"
                "ds_read_b64 v[238:239], %[kb] offset:2408\n\t"
                "s_waitcnt lgkmcnt(9)\n\t"
                "v_mfma_f32_32x32x16_bf16 %[a1], v[208:211], v[212:215], %[a1]\n\t"
                "ds_read_b64 v[240:241], %[kb] offset:320\n\t"
                "ds_read_b64 v[242:243], %[kb] offset:328\n\t"
                "ds_read_b128 v[244:247], %[ub] offset:4672\n\t"
                "s_waitcnt lgkmcnt(11)\n\t"
                "v_mfma_f32_32x32x16_bf16 %[a0], v[208:211], v[216:219], %[a0]\n\t"
                "ds_read_b128 v[248:251], %[ub] offset:2112\n\t"
                "s_add_u32 %[t0], %[t1], 81920\n\t"
                "buffer_load_dwordx4 a[20:23], %[go], %[so], %[t0] offen\n\t"
                "s_waitcnt lgkmcnt(10)\n\t"
                "v_mfma_f32_32x32x16_bf16 %[a1], v[220:223], v[216:219], %[a1]\n\t"
                "ds_read_b64 v[252:253], %[kb] offset:2368\n\t"
                "ds_read_b64 v[254:255], %[kb] offset:2376\n\t"
                "s_waitcnt lgkmcnt(9)\n\t"
                "v_mfma_f32_32x32x16_bf16 %[a1], v[224:227], v[228:231], %[a1]\n\t"
                "ds_read_b64 v[192:193], %[kb] offset:416\n\t"
                "ds_read_b64 v[194:195], %[kb] offset:424\n\t"
                "ds_read_b128 v[196:199], %[ub] offset:4560\n\t"
                "s_waitcnt lgkmcnt(11)\n\t"
                "v_mfma_f32_32x32x16_bf16 %[a0], v[224:227], v[232:235], %[a0]\n\t"
                "ds_read_b128 v[200:203], %[ub] offset:2000\n\t"
                "s_waitcnt lgkmcnt(10)\n\t"
                "v_mfma_f32_32x32x16_bf16 %[a1], v[236:239], v[232:235], %[a1]\n\t"
                "ds_read_b64 v[204:205], %[kb] offset:2464\n\t"
                "ds_read_b64 v[206:207], %[kb] offset:2472\n\t"
                "s_waitcnt lgkmcnt(9)\n\t"
                "v_mfma_f32_32x32x16_bf16 %[a1], v[240:243], v[244:247], %[a1]\n\t"
                "ds_read_b64 v[208:209], %[kb] offset:384\n\t"
                "ds_read_b64 v[210:211], %[kb] offset:392\n\t"
                "ds_read_b128 v[212:215], %[ub] offset:4592\n\t"
                "s_waitcnt lgkmcnt(11)\n\t"
                "v_mfma_f32_32x32x16_bf16 %[a0], v[240:243], v[248:251], %[a0]\n\t"
                "ds_read_b128 v[216:219], %[ub] offset:2032\n\t"
                "s_add_u32 %[t0], %[t1], 98304\n\t"
                "buffer_load_dwordx4 a[24:27], %[go], %[so], %[t0] offen\n\t"
                "s_waitcnt lgkmcnt(10)\n\t"
                "v_mfma_f32_32x32x16_bf16 %[a1], v[252:255], v[248:251], %[a1]\n\t"
                "ds_read_b64 v[220:221], %[kb] offset:2432\n\t"
                "ds_read_b64 v[222:223], %[kb] offset:2440\n\t"
                "s_waitcnt lgkmcnt(9)\n\t"
                "v_mfma_f32_32x32x16_bf16 %[a1], v[192:195], v[196:199], %[a1]\n\t"
                "ds_read_b64 v[224:225], %[kb] offset:480\n\t"
                "ds_read_b64 v[226:227], %[kb] offset:488\n\t"
                "ds_read_b128 v[228:231], %[ub] offset:4480\n\t"
                "s_waitcnt lgkmcnt(11)\n\t"
                "v_mfma_f32_32x32x16_bf16 %[a0], v[192:195], v[200:203], %[a0]\n\t"
                "ds_read_b128 v[232:235], %[ub] offset:1920\n\t"
                "s_waitcnt lgkmcnt(10)\n\t"
                "v_mfma_f32_32x32x16_bf16 %[a1], v[204:207], v[200:203], %[a1]\n\t"
                "ds_read_b64 v[236:237], %[kb] offset:2528\n\t"
                "ds_read_b64 v[238:239], %[kb] offset:2536\n\t"
                "s_waitcnt lgkmcnt(9)\n\t"
                "v_mfma_f32_32x32x16_bf16 %[a1], v[208:211], v[212:215], %[a1]\n\t"
                "ds_read_b64 v[240:241], %[kb] offset:448\n\t"
                "ds_read_b64 v[242:243], %[kb] offset:456\n\t"
                "ds_read_b128 v[244:247], %[ub] offset:4512\n\t"
                "s_waitcnt lgkmcnt(11)\n\t"
                "v_mfma_f32_32x32x16_bf16 %[a0], v[208:211], v[216:219], %[a0]\n\t"
                "ds_read_b128 v[248:251], %[ub] offset:1952\n\t"
                "s_add_u32 %[t0], %[t1], 114688\n\t"
                "buffer_load_dwordx4 a[28:31], %[go], %[so], %[t0] offen\n\t"
                "s_waitcnt lgkmcnt(10)\n\t"
                "v_mfma_f32_32x32x16_bf16 %[a1], v[220:223], v[216:219], %[a1]\n\t"
                "ds_read_b64 v[252:253], %[kb] offset:2496\n\t"
                "ds_read_b64 v[254:255], %[kb] offset:2504\n\t"
                "s_waitcnt lgkmcnt(9)\n\t"
                "v_mfma_f32_32x32x16_bf16 %[a1], v[224:227], v[228:231], %[a1]\n\t"
                "ds_read_b64 v[192:193], %[kb] offset:544\n\t"
                "ds_read_b64 v[194:195], %[kb] offset:552\n\t"
                "ds_read_b128 v[196:199], %[ub] offset:4400\n\t"
                "s_waitcnt lgkmcnt(11)\n\t"
                "v_mfma_f32_32x32x16_bf16 %[a0], v[224:227], v[232:235], %[a0]\n\t"
                "ds_read_b128 v[200:203], %[ub] offset:1840\n\t"
                "s_add_u32 %[t1], %[ro], 0\n\t"
                "s_and_b32 %[t1], %[t1], 7\n\t"
                "s_lshl_b32 %[t1], %[t1], 18\n\t"
                "s_add_u32 %[t0], %[t1], 131072\n\t"
                "buffer_load_dwordx4 a[32:35], %[go], %[so], %[t0] offen\n\t"
                "s_waitcnt lgkmcnt(10)\n\t"
                "v_mfma_f32_32x32x16_bf16 %[a1], v[236:239], v[232:235], %[a1]\n\t"
                "ds_read_b64 v[204:205], %[kb] offset:2592\n\t"
                "ds_read_b64 v[206:207], %[kb] offset:2600\n\t"
                "s_waitcnt lgkmcnt(9)\n\t"
                "v_mfma_f32_32x32x16_bf16 %[a1], v[240:243], v[244:247], %[a1]\n\t"
                "ds_read_b64 v[208:209], %[kb] offset:512\n\t"
                "ds_read_b64 v[210:211], %[kb] offset:520\n\t"
                "ds_read_b128 v[212:215], %[ub] offset:4432\n\t"
                "s_waitcnt lgkmcnt(11)\n\t"
                "v_mfma_f32_32x32x16_bf16 %[a0], v[240:243], v[248:251], %[a0]\n\t"
                "ds_read_b128 v[216:219], %[ub] offset:1872\n\t"
                "s_waitcnt lgkmcnt(10)\n\t"
                "v_mfma_f32_32x32x16_bf16 %[a1], v[252:255], v[248:251], %[a1]\n\t"
                "ds_read_b64 v[220:221], %[kb] offset:2560\n\t"
                "ds_read_b64 v[222:223], %[kb] offset:2568\n\t"
                "s_waitcnt lgkmcnt(9)\n\t"
                "v_mfma_f32_32x32x16_bf16 %[a1], v[192:195], v[196:199], %[a1]\n\t"
                "ds_read_b64 v[224:225], %[kb] offset:608\n\t"
                "ds_read_b64 v[226:227], %[kb] offset:616\n\t"
                "ds_read_b128 v[228:231], %[ub] offset:4320\n\t"
                "s_waitcnt lgkmcnt(11)\n\t"
                "v_mfma_f32_32x32x16_bf16 %[a0], v[192:195], v[200:203], %[a0]\n\t"
                "ds_read_b128 v[232:235], %[ub] offset:1760\n\t"
                "s_add_u32 %[t0], %[t1], 147456\n\t"
                "buffer_load_dwordx4 a[36:39], %[go], %[so], %[t0] offen\n\t"
                "s_waitcnt lgkmcnt(10)\n\t"
                "v_mfma_f32_32x32x16_bf16 %[a1], v[204:207], v[200:203], %[a1]\n\t"
                "ds_read_b64 v[236:237], %[kb] offset:2656\n\t"
                "ds_read_b64 v[238:239], %[kb] offset:2664\n\t"
                "s_waitcnt lgkmcnt(9)\n\t"
                "v_mfma_f32_32x32x16_bf16 %[a1], v[208:211], v[212:215], %[a1]\n\t"
                "ds_read_b64 v[240:241], %[kb] offset:576\n\t"
                "ds_read_b64 v[242:243], %[kb] offset:584\n\t"
                "ds_read_b128 v[244:247], %[ub] offset:4352\n\t"
                "s_waitcnt lgkmcnt(11)\n\t"
                "v_mfma_f32_32x32x16_bf16 %[a0], v[208:211], v[216:219], %[a0]\n\t"
                "ds_read_b128 v[248:251], %[ub] offset:1792\n\t"
                "s_waitcnt lgkmcnt(10)\n\t"
                "v_mfma_f32_32x32x16_bf16 %[a1], v[220:223], v[216:219], %[a1]\n\t"
                "ds_read_b64 v[252:253], %[kb] offset:2624\n\t"
                "ds_read_b64 v[254:255], %[kb] offset:2632\n\t"
                "s_waitcnt lgkmcnt(9)\n\t"
                "v_mfma_f32_32x32x16_bf16 %[a1], v[224:227], v[228:231], %[a1]\n\t"
                "ds_read_b64 v[192:193], %[kb] offset:672\n\t"
                "ds_read_b64 v[194:195], %[kb] offset:680\n\t"
                "ds_read_b128 v[196:199], %[ub] offset:4240\n\t"
                "s_waitcnt lgkmcnt(11)\n\t"
                "v_mfma_f32_32x32x16_bf16 %[a0], v[224:227], v[232:235], %[a0]\n\t"
                "ds_read_b128 v[200:203], %[ub] offset:1680\n\t"
                "s_add_u32 %[t0], %[t1], 163840\n\t"
                "buffer_load_dwordx4 a[40:43], %[go], %[so], %[t0] offen\n\t"
                "s_waitcnt lgkmcnt(10)\n\t"
                "v_mfma_f32_32x32x16_bf16 %[a1], v[236:239], v[232:235], %[a1]\n\t"
                "ds_read_b64 v[204:205], %[kb] offset:2720\n\t"
                "ds_read_b64 v[206:207], %[kb] offset:2728\n\t"
                "s_waitcnt lgkmcnt(9)\n\t"
                "v_mfma_f32_32x32x16_bf16 %[a1], v[240:243], v[244:247], %[a1]\n\t"
                "ds_read_b64 v[208:209], %[kb] offset:640\n\t"
                "ds_read_b64 v[210:211], %[kb] offset:648\n\t"
                "ds_read_b128 v[212:215], %[ub] offset:4272\n\t"
                "s_waitcnt lgkmcnt(11)\n\t"
                "v_mfma_f32_32x32x16_bf16 %[a0], v[240:243], v[248:251], %[a0]\n\t"
                "ds_read_b128 v[216:219], %[ub] offset:1712\n\t"
                "s_waitcnt lgkmcnt(10)\n\t"
                "v_mfma_f32_32x32x16_bf16 %[a1], v[252:255], v[248:251], %[a1]\n\t"
                "ds_read_b64 v[220:221], %[kb] offset:2688\n\t"
                "ds_read_b64 v[222:223], %[kb] offset:2696\n\t"
                "s_waitcnt lgkmcnt(9)\n\t"
                "v_mfma_f32_32x32x16_bf16 %[a1], v[192:195], v[196:199], %[a1]\n\t"
                "ds_read_b64 v[224:225], %[kb] offset:736\n\t"
                "ds_read_b64 v[226:227], %[kb] offset:744\n\t"
                "ds_read_b128 v[228:231], %[ub] offset:4160\n\t"
                "s_waitcnt lgkmcnt(11)\n\t"
                "v_mfma_f32_32x32x16_bf16 %[a0], v[192:195], v[200:203], %[a0]\n\t"
                "ds_read_b128 v[232:235], %[ub] offset:1600\n\t"
                "s_add_u32 %[t0], %[t1], 180224\n\t"
                "buffer_load_dwordx4 a[44:47], %[go], %[so], %[t0] offen\n\t"
                "s_waitcnt lgkmcnt(10)\n\t"
                "v_mfma_f32_32x32x16_bf16 %[a1], v[204:207], v[200:203], %[a1]\n\t"
                "ds_read_b64 v[236:237], %[kb] offset:2784\n\t"
                "ds_read_b64 v[238:239], %[kb] offset:2792\n\t"
                "s_waitcnt lgkmcnt(9)\n\t"
                "v_mfma_f32_32x32x16_bf16 %[a1], v[208:211], v[212:215], %[a1]\n\t"
                "ds_read_b64 v[240:241], %[kb] offset:704\n\t"
                "ds_read_b64 v[242:243], %[kb] offset:712\n\t"
                "ds_read_b128 v[244:247], %[ub] offset:4192\n\t"
                "s_waitcnt lgkmcnt(11)\n\t"
                "v_mfma_f32_32x32x16_bf16 %[a0], v[208:211], v[216:219], %[a0]\n\t"
                "ds_read_b128 v[248:251], %[ub] offset:1632\n\t"
                "s_add_u32 %[t0], %[t1], 196608\n\t"
                "buffer_load_dwordx4 a[48:51], %[go], %[so], %[t0] offen\n\t"
                "s_waitcnt lgkmcnt(10)\n\t"
                "v_mfma_f32_32x32x16_bf16 %[a1], v[220:223], v[216:219], %[a1]\n\t"
                "ds_read_b64 v[252:253], %[kb] offset:2752\n\t"
                "ds_read_b64 v[254:255], %[kb] offset:2760\n\t"
                "s_waitcnt lgkmcnt(9)\n\t"
                "v_mfma_f32_32x32x16_bf16 %[a1], v[224:227], v[228:231], %[a1]\n\t"
                "ds_read_b64 v[192:193], %[kb] offset:800\n\t"
                "ds_read_b64 v[194:195], %[kb] offset:808\n\t"
                "ds_read_b128 v[196:199], %[ub] offset:4080\n\t"
                "s_waitcnt lgkmcnt(11)\n\t"
                "v_mfma_f32_32x32x16_bf16 %[a0], v[224:227], v[232:235], %[a0]\n\t"
                "ds_read_b128 v[200:203], %[ub] offset:1520\n\t"
                "s_waitcnt lgkmcnt(10)\n\t"
                "v_mfma_f32_32x32x16_bf16 %[a1], v[236:239], v[232:235], %[a1]\n\t"
                "ds_read_b64 v[204:205], %[kb] offset:2848\n\t"
                "ds_read_b64 v[206:207], %[kb] offset:2856\n\t"
                "s_waitcnt lgkmcnt(9)\n\t"
                "v_mfma_f32_32x32x16_bf16 %[a1], v[240:243], v[244:247], %[a1]\n\t"
                "ds_read_b64 v[208:209], %[kb] offset:768\n\t"
                "ds_read_b64 v[210:211], %[kb] offset:776\n\t"
                "ds_read_b128 v[212:215], %[ub] offset:4112\n\t"
                "s_waitcnt lgkmcnt(11)\n\t"
                "v_mfma_f32_32x32x16_bf16 %[a0], v[240:243], v[248:251], %[a0]\n\t"
                "ds_read_b128 v[216:219], %[ub] offset:1552\n\t"
                "s_add_u32 %[t0], %[t1], 212992\n\t"
                "buffer_load_dwordx4 a[52:55], %[go], %[so], %[t0] offen\n\t"
                "s_waitcnt lgkmcnt(10)\n\t"
                "v_mfma_f32_32x32x16_bf16 %[a1], v[252:255], v[248:251], %[a1]\n\t"
                "ds_read_b64 v[220:221], %[kb] offset:2816\n\t"
                "ds_read_b64 v[222:223], %[kb] offset:2824\n\t"
                "s_waitcnt lgkmcnt(9)\n\t"
                "v_mfma_f32_32x32x16_bf16 %[a1], v[192:195], v[196:199], %[a1]\n\t"
                "ds_read_b64 v[224:225], %[kb] offset:864\n\t"
                "ds_read_b64 v[226:227], %[kb] offset:872\n\t"
                "ds_read_b128 v[228:231], %[ub] offset:4000\n\t"
                "s_waitcnt lgkmcnt(11)\n\t"
                "v_mfma_f32_32x32x16_bf16 %[a0], v[192:195], v[200:203], %[a0]\n\t"
                "ds_read_b128 v[232:235], %[ub] offset:1440\n\t"
                "s_waitcnt lgkmcnt(10)\n\t"
                "v_mfma_f32_32x32x16_bf16 %[a1], v[204:207], v[200:203], %[a1]\n\t"
                "ds_read_b64 v[236:237], %[kb] offset:2912\n\t"
                "ds_read_b64 v[238:239], %[kb] offset:2920\n\t"
                "s_waitcnt lgkmcnt(9)\n\t"
                "v_mfma_f32_32x32x16_bf16 %[a1], v[208:211], v[212:215], %[a1]\n\t"
                "ds_read_b64 v[240:241], %[kb] offset:832\n\t"
                "ds_read_b64 v[242:243], %[kb] offset:840\n\t"
                "ds_read_b128 v[244:247], %[ub] offset:4032\n\t"
                "s_waitcnt lgkmcnt(11)\n\t"
                "v_mfma_f32_32x32x16_bf16 %[a0], v[208:211], v[216:219], %[a0]\n\t"
                "ds_read_b128 v[248:251], %[ub] offset:1472\n\t"
                "s_add_u32 %[t0], %[t1], 229376\n\t"
                "buffer_load_dwordx4 a[56:59], %[go], %[so], %[t0] offen\n\t"
                "s_waitcnt lgkmcnt(10)\n\t"
                "v_mfma_f32_32x32x16_bf16 %[a1], v[220:223], v[216:219], %[a1]\n\t"
                "ds_read_b64 v[252:253], %[kb] offset:2880\n\t"
                "ds_read_b64 v[254:255], %[kb] offset:2888\n\t"
                "s_waitcnt lgkmcnt(9)\n\t"
                "v_mfma_f32_32x32x16_bf16 %[a1], v[224:227], v[228:231], %[a1]\n\t"
                "ds_read_b64 v[192:193], %[kb] offset:928\n\t"
                "ds_read_b64 v[194:195], %[kb] offset:936\n\t"
                "ds_read_b128 v[196:199], %[ub] offset:3920\n\t"
                "s_waitcnt lgkmcnt(11)\n\t"
                "v_mfma_f32_32x32x16_bf16 %[a0], v[224:227], v[232:235], %[a0]\n\t"
                "ds_read_b128 v[200:203], %[ub] offset:1360\n\t"
                "s_waitcnt lgkmcnt(10)\n\t"
                "v_mfma_f32_32x32x16_bf16 %[a1], v[236:239], v[232:235], %[a1]\n\t"
                "ds_read_b64 v[204:205], %[kb] offset:2976\n\t"
                "ds_read_b64 v[206:207], %[kb] offset:2984\n\t"
                "s_waitcnt lgkmcnt(9)\n\t"
                "v_mfma_f32_32x32x16_bf16 %[a1], v[240:243], v[244:247], %[a1]\n\t"
                "ds_read_b64 v[208:209], %[kb] offset:896\n\t"
                "ds_read_b64 v[210:211], %[kb] offset:904\n\t"
                "ds_read_b128 v[212:215], %[ub] offset:3952\n\t"
                "s_waitcnt lgkmcnt(11)\n\t"
                "v_mfma_f32_32x32x16_bf16 %[a0], v[240:243], v[248:251], %[a0]\n\t"
                "ds_read_b128 v[216:219], %[ub] offset:1392\n\t"
                "s_add_u32 %[t0], %[t1], 245760\n\t"
                "buffer_load_dwordx4 a[60:63], %[go], %[so], %[t0] offen\n\t"
                "s_waitcnt lgkmcnt(10)\n\t"
                "v_mfma_f32_32x32x16_bf16 %[a1], v[252:255], v[248:251], %[a1]\n\t"
                "ds_read_b64 v[220:221], %[kb] offset:2944\n\t"
                "ds_read_b64 v[222:223], %[kb] offset:2952\n\t"
                "s_waitcnt lgkmcnt(9)\n\t"
                "v_mfma_f32_32x32x16_bf16 %[a1], v[192:195], v[196:199], %[a1]\n\t"
                "ds_read_b64 v[224:225], %[kb] offset:992\n\t"
                "ds_read_b64 v[226:227], %[kb] offset:1000\n\t"
                "ds_read_b128 v[228:231], %[ub] offset:3840\n\t"
                "s_waitcnt lgkmcnt(11)\n\t"
                "v_mfma_f32_32x32x16_bf16 %[a0], v[192:195], v[200:203], %[a0]\n\t"
                "ds_read_b128 v[232:235], %[ub] offset:1280\n\t"
                "s_add_u32 %[t1], %[ro], 1\n\t"
                "s_and_b32 %[t1], %[t1], 7\n\t"
                "s_lshl_b32 %[t1], %[t1], 18\n\t"
                "s_add_u32 %[t0], %[t1], 0\n\t"
                "buffer_load_dwordx4 a[64:67], %[go], %[so], %[t0] offen\n\t"
                "s_waitcnt lgkmcnt(10)\n\t"
                "v_mfma_f32_32x32x16_bf16 %[a1], v[204:207], v[200:203], %[a1]\n\t"
                "ds_read_b64 v[236:237], %[kb] offset:3040\n\t"
                "ds_read_b64 v[238:239], %[kb] offset:3048\n\t"
                "s_waitcnt lgkmcnt(9)\n\t"
                "v_mfma_f32_32x32x16_bf16 %[a1], v[208:211], v[212:215], %[a1]\n\t"
                "ds_read_b64 v[240:241], %[kb] offset:960\n\t"
                "ds_read_b64 v[242:243], %[kb] offset:968\n\t"
                "ds_read_b128 v[244:247], %[ub] offset:3872\n\t"
                "s_waitcnt lgkmcnt(11)\n\t"
                "v_mfma_f32_32x32x16_bf16 %[a0], v[208:211], v[216:219], %[a0]\n\t"
                "ds_read_b128 v[248:251], %[ub] offset:1312\n\t"
                "s_waitcnt lgkmcnt(10)\n\t"
                "v_mfma_f32_32x32x16_bf16 %[a1], v[220:223], v[216:219], %[a1]\n\t"
                "ds_read_b64 v[252:253], %[kb] offset:3008\n\t"
                "ds_read_b64 v[254:255], %[kb] offset:3016\n\t"
                "s_waitcnt lgkmcnt(9)\n\t"
                "v_mfma_f32_32x32x16_bf16 %[a1], v[224:227], v[228:231], %[a1]\n\t"
                "ds_read_b64 v[192:193], %[kb] offset:1056\n\t"
                "ds_read_b64 v[194:195], %[kb] offset:1064\n\t"
                "ds_read_b128 v[196:199], %[ub] offset:3760\n\t"
                "s_waitcnt lgkmcnt(11)\n\t"
                "v_mfma_f32_32x32x16_bf16 %[a0], v[224:227], v[232:235], %[a0]\n\t"
                "ds_read_b128 v[200:203], %[ub] offset:1200\n\t"
                "s_add_u32 %[t0], %[t1], 16384\n\t"
                "buffer_load_dwordx4 a[68:71], %[go], %[so], %[t0] offen\n\t"
                "s_waitcnt lgkmcnt(10)\n\t"
                "v_mfma_f32_32x32x16_bf16 %[a1], v[236:239], v[232:235], %[a1]\n\t"
                "ds_read_b64 v[204:205], %[kb] offset:3104\n\t"
                "ds_read_b64 v[206:207], %[kb] offset:3112\n\t"
                "s_waitcnt lgkmcnt(9)\n\t"
                "v_mfma_f32_32x32x16_bf16 %[a1], v[240:243], v[244:247], %[a1]\n\t"
                "ds_read_b64 v[208:209], %[kb] offset:1024\n\t"
                "ds_read_b64 v[210:211], %[kb] offset:1032\n\t"
                "ds_read_b128 v[212:215], %[ub] offset:3792\n\t"
                "s_waitcnt lgkmcnt(11)\n\t"
                "v_mfma_f32_32x32x16_bf16 %[a0], v[240:243], v[248:251], %[a0]\n\t"
                "ds_read_b128 v[216:219], %[ub] offset:1232\n\t"
                "s_waitcnt lgkmcnt(10)\n\t"
                "v_mfma_f32_32x32x16_bf16 %[a1], v[252:255], v[248:251], %[a1]\n\t"
                "ds_read_b64 v[220:221], %[kb] offset:3072\n\t"
                "ds_read_b64 v[222:223], %[kb] offset:3080\n\t"
                "s_waitcnt lgkmcnt(9)\n\t"
                "v_mfma_f32_32x32x16_bf16 %[a1], v[192:195], v[196:199], %[a1]\n\t"
                "ds_read_b64 v[224:225], %[kb] offset:1120\n\t"
                "ds_read_b64 v[226:227], %[kb] offset:1128\n\t"
                "ds_read_b128 v[228:231], %[ub] offset:3680\n\t"
                "s_waitcnt lgkmcnt(11)\n\t"
                "v_mfma_f32_32x32x16_bf16 %[a0], v[192:195], v[200:203], %[a0]\n\t"
                "ds_read_b128 v[232:235], %[ub] offset:1120\n\t"
                "s_add_u32 %[t0], %[t1], 32768\n\t"
                "buffer_load_dwordx4 a[72:75], %[go], %[so], %[t0] offen\n\t"
                "s_waitcnt lgkmcnt(10)\n\t"
                "v_mfma_f32_32x32x16_bf16 %[a1], v[204:207], v[200:203], %[a1]\n\t"
                "ds_read_b64 v[236:237], %[kb] offset:3168\n\t"
                "ds_read_b64 v[238:239], %[kb] offset:3176\n\t"
                "s_waitcnt lgkmcnt(9)\n\t"
                "v_mfma_f32_32x32x16_bf16 %[a1], v[208:211], v[212:215], %[a1]\n\t"
                "ds_read_b64 v[240:241], %[kb] offset:1088\n\t"
                "ds_read_b64 v[242:243], %[kb] offset:1096\n\t"
                "ds_read_b128 v[244:247], %[ub] offset:3712\n\t"
                "s_waitcnt lgkmcnt(11)\n\t"
                "v_mfma_f32_32x32x16_bf16 %[a0], v[208:211], v[216:219], %[a0]\n\t"
                "ds_read_b128 v[248:251], %[ub] offset:1152\n\t"
                "s_waitcnt lgkmcnt(10)\n\t"
                "v_mfma_f32_32x32x16_bf16 %[a1], v[220:223], v[216:219], %[a1]\n\t"
                "ds_read_b64 v[252:253], %[kb] offset:3136\n\t"
                "ds_read_b64 v[254:255], %[kb] offset:3144\n\t"
                "s_waitcnt lgkmcnt(9)\n\t"
                "v_mfma_f32_32x32x16_bf16 %[a1], v[224:227], v[228:231], %[a1]\n\t"
                "ds_read_b64 v[192:193], %[kb] offset:1184\n\t"
                "ds_read_b64 v[194:195], %[kb] offset:1192\n\t"
                "ds_read_b128 v[196:199], %[ub] offset:3600\n\t"
                "s_waitcnt lgkmcnt(11)\n\t"
                "v_mfma_f32_32x32x16_bf16 %[a0], v[224:227], v[232:235], %[a0]\n\t"
                "ds_read_b128 v[200:203], %[ub] offset:1040\n\t"
                "s_add_u32 %[t0], %[t1], 49152\n\t"
                "buffer_load_dwordx4 a[76:79], %[go], %[so], %[t0] offen\n\t"
                "s_waitcnt lgkmcnt(10)\n\t"
                "v_mfma_f32_32x32x16_bf16 %[a1], v[236:239], v[232:235], %[a1]\n\t"
                "ds_read_b64 v[204:205], %[kb] offset:3232\n\t"
                "ds_read_b64 v[206:207], %[kb] offset:3240\n\t"
                "s_waitcnt lgkmcnt(9)\n\t"
                "v_mfma_f32_32x32x16_bf16 %[a1], v[240:243], v[244:247], %[a1]\n\t"
                "ds_read_b64 v[208:209], %[kb] offset:1152\n\t"
                "ds_read_b64 v[210:211], %[kb] offset:1160\n\t"
                "ds_read_b128 v[212:215], %[ub] offset:3632\n\t"
                "s_waitcnt lgkmcnt(11)\n\t"
                "v_mfma_f32_32x32x16_bf16 %[a0], v[240:243], v[248:251], %[a0]\n\t"
                "ds_read_b128 v[216:219], %[ub] offset:1072\n\t"
                "s_add_u32 %[t0], %[t1], 65536\n\t"
                "buffer_load_dwordx4 a[80:83], %[go], %[so], %[t0] offen\n\t"
                "s_waitcnt lgkmcnt(10)\n\t"
                "v_mfma_f32_32x32x16_bf16 %[a1], v[252:255], v[248:251], %[a1]\n\t"
                "ds_read_b64 v[220:221], %[kb] offset:3200\n\t"
                "ds_read_b64 v[222:223], %[kb] offset:3208\n\t"
                "s_waitcnt lgkmcnt(9)\n\t"
                "v_mfma_f32_32x32x16_bf16 %[a1], v[192:195], v[196:199], %[a1]\n\t"
                "ds_read_b64 v[224:225], %[kb] offset:1248\n\t"
                "ds_read_b64 v[226:227], %[kb] offset:1256\n\t"
                "ds_read_b128 v[228:231], %[ub] offset:3520\n\t"
                "s_waitcnt lgkmcnt(11)\n\t"
                "v_mfma_f32_32x32x16_bf16 %[a0], v[192:195], v[200:203], %[a0]\n\t"
                "ds_read_b128 v[232:235], %[ub] offset:960\n\t"
                "s_waitcnt lgkmcnt(10)\n\t"
                "v_mfma_f32_32x32x16_bf16 %[a1], v[204:207], v[200:203], %[a1]\n\t"
                "ds_read_b64 v[236:237], %[kb] offset:3296\n\t"
                "ds_read_b64 v[238:239], %[kb] offset:3304\n\t"
                "s_waitcnt lgkmcnt(9)\n\t"
                "v_mfma_f32_32x32x16_bf16 %[a1], v[208:211], v[212:215], %[a1]\n\t"
                "ds_read_b64 v[240:241], %[kb] offset:1216\n\t"
                "ds_read_b64 v[242:243], %[kb] offset:1224\n\t"
                "ds_read_b128 v[244:247], %[ub] offset:3552\n\t"
                "s_waitcnt lgkmcnt(11)\n\t"
                "v_mfma_f32_32x32x16_bf16 %[a0], v[208:211], v[216:219], %[a0]\n\t"
                "ds_read_b128 v[248:251], %[ub] offset:992\n\t"
                "s_add_u32 %[t0], %[t1], 81920\n\t"
                "buffer_load_dwordx4 a[84:87], %[go], %[so], %[t0] offen\n\t"
                "s_waitcnt lgkmcnt(10)\n\t"
                "v_mfma_f32_32x32x16_bf16 %[a1], v[220:223], v[216:219], %[a1]\n\t"
                "ds_read_b64 v[252:253], %[kb] offset:3264\n\t"
                "ds_read_b64 v[254:255], %[kb] offset:3272\n\t"
                "s_waitcnt lgkmcnt(9)\n\t"
                "v_mfma_f32_32x32x16_bf16 %[a1], v[224:227], v[228:231], %[a1]\n\t"
                "ds_read_b64 v[192:193], %[kb] offset:1312\n\t"
                "ds_read_b64 v[194:195], %[kb] offset:1320\n\t"
                "ds_read_b128 v[196:199], %[ub] offset:3440\n\t"
                "s_waitcnt lgkmcnt(11)\n\t"
                "v_mfma_f32_32x32x16_bf16 %[a0], v[224:227], v[232:235], %[a0]\n\t"
                "ds_read_b128 v[200:203], %[ub] offset:880\n\t"
                "s_waitcnt lgkmcnt(10)\n\t"
                "v_mfma_f32_32x32x16_bf16 %[a1], v[236:239], v[232:235], %[a1]\n\t"
                "ds_read_b64 v[204:205], %[kb] offset:3360\n\t"
                "ds_read_b64 v[206:207], %[kb] offset:3368\n\t"
                "s_waitcnt lgkmcnt(9)\n\t"
                "v_mfma_f32_32x32x16_bf16 %[a1], v[240:243], v[244:247], %[a1]\n\t"
                "ds_read_b64 v[208:209], %[kb] offset:1280\n\t"
                "ds_read_b64 v[210:211], %[kb] offset:1288\n\t"
                "ds_read_b128 v[212:215], %[ub] offset:3472\n\t"
                "s_waitcnt lgkmcnt(11)\n\t"
                "v_mfma_f32_32x32x16_bf16 %[a0], v[240:243], v[248:251], %[a0]\n\t"
                "ds_read_b128 v[216:219], %[ub] offset:912\n\t"
                "s_add_u32 %[t0], %[t1], 98304\n\t"
                "buffer_load_dwordx4 a[88:91], %[go], %[so], %[t0] offen\n\t"
                "s_waitcnt lgkmcnt(10)\n\t"
                "v_mfma_f32_32x32x16_bf16 %[a1], v[252:255], v[248:251], %[a1]\n\t"
                "ds_read_b64 v[220:221], %[kb] offset:3328\n\t"
                "ds_read_b64 v[222:223], %[kb] offset:3336\n\t"
                "s_waitcnt lgkmcnt(9)\n\t"
                "v_mfma_f32_32x32x16_bf16 %[a1], v[192:195], v[196:199], %[a1]\n\t"
                "ds_read_b64 v[224:225], %[kb] offset:1376\n\t"
                "ds_read_b64 v[226:227], %[kb] offset:1384\n\t"
                "ds_read_b128 v[228:231], %[ub] offset:3360\n\t"
                "s_waitcnt lgkmcnt(11)\n\t"
                "v_mfma_f32_32x32x16_bf16 %[a0], v[192:195], v[200:203], %[a0]\n\t"
                "ds_read_b128 v[232:235], %[ub] offset:800\n\t"
                "s_waitcnt lgkmcnt(10)\n\t"
                "v_mfma_f32_32x32x16_bf16 %[a1], v[204:207], v[200:203], %[a1]\n\t"
                "ds_read_b64 v[236:237], %[kb] offset:3424\n\t"
                "ds_read_b64 v[238:239], %[kb] offset:3432\n\t"
                "s_waitcnt lgkmcnt(9)\n\t"
                "v_mfma_f32_32x32x16_bf16 %[a1], v[208:211], v[212:215], %[a1]\n\t"
                "ds_read_b64 v[240:241], %[kb] offset:1344\n\t"
                "ds_read_b64 v[242:243], %[kb] offset:1352\n\t"
                "ds_read_b128 v[244:247], %[ub] offset:3392\n\t"
                "s_waitcnt lgkmcnt(11)\n\t"
                "v_mfma_f32_32x32x16_bf16 %[a0], v[208:211], v[216:219], %[a0]\n\t"
                "ds_read_b128 v[248:251], %[ub] offset:832\n\t"
                "s_add_u32 %[t0], %[t1], 114688\n\t"
                "buffer_load_dwordx4 a[92:95], %[go], %[so], %[t0] offen\n\t"
                "s_waitcnt lgkmcnt(10)\n\t"
                "v_mfma_f32_32x32x16_bf16 %[a1], v[220:223], v[216:219], %[a1]\n\t"
                "ds_read_b64 v[252:253], %[kb] offset:3392\n\t"
                "ds_read_b64 v[254:255], %[kb] offset:3400\n\t"
                "s_waitcnt lgkmcnt(9)\n\t"
                "v_mfma_f32_32x32x16_bf16 %[a1], v[224:227], v[228:231], %[a1]\n\t"
                "ds_read_b64 v[192:193], %[kb] offset:1440\n\t"
                "ds_read_b64 v[194:195], %[kb] offset:1448\n\t"
                "ds_read_b128 v[196:199], %[ub] offset:3280\n\t"
                "s_waitcnt lgkmcnt(11)\n\t"
                "v_mfma_f32_32x32x16_bf16 %[a0], v[224:227], v[232:235], %[a0]\n\t"
                "ds_read_b128 v[200:203], %[ub] offset:720\n\t"
                "s_add_u32 %[t1], %[ro], 1\n\t"
                "s_and_b32 %[t1], %[t1], 7\n\t"
                "s_lshl_b32 %[t1], %[t1], 18\n\t"
                "s_add_u32 %[t0], %[t1], 131072\n\t"
                "buffer_load_dwordx4 a[96:99], %[go], %[so], %[t0] offen\n\t"
                "s_waitcnt lgkmcnt(10)\n\t"
                "v_mfma_f32_32x32x16_bf16 %[a1], v[236:239], v[232:235], %[a1]\n\t"
                "ds_read_b64 v[204:205], %[kb] offset:3488\n\t"
                "ds_read_b64 v[206:207], %[kb] offset:3496\n\t"
                "s_waitcnt lgkmcnt(9)\n\t"
                "v_mfma_f32_32x32x16_bf16 %[a1], v[240:243], v[244:247], %[a1]\n\t"
                "ds_read_b64 v[208:209], %[kb] offset:1408\n\t"
                "ds_read_b64 v[210:211], %[kb] offset:1416\n\t"
                "ds_read_b128 v[212:215], %[ub] offset:3312\n\t"
                "s_waitcnt lgkmcnt(11)\n\t"
                "v_mfma_f32_32x32x16_bf16 %[a0], v[240:243], v[248:251], %[a0]\n\t"
                "ds_read_b128 v[216:219], %[ub] offset:752\n\t"
                "s_waitcnt lgkmcnt(10)\n\t"
                "v_mfma_f32_32x32x16_bf16 %[a1], v[252:255], v[248:251], %[a1]\n\t"
                "ds_read_b64 v[220:221], %[kb] offset:3456\n\t"
                "ds_read_b64 v[222:223], %[kb] offset:3464\n\t"
                "s_waitcnt lgkmcnt(9)\n\t"
                "v_mfma_f32_32x32x16_bf16 %[a1], v[192:195], v[196:199], %[a1]\n\t"
                "ds_read_b64 v[224:225], %[kb] offset:1504\n\t"
                "ds_read_b64 v[226:227], %[kb] offset:1512\n\t"
                "ds_read_b128 v[228:231], %[ub] offset:3200\n\t"
                "s_waitcnt lgkmcnt(11)\n\t"
                "v_mfma_f32_32x32x16_bf16 %[a0], v[192:195], v[200:203], %[a0]\n\t"
                "ds_read_b128 v[232:235], %[ub] offset:640\n\t"
                "s_add_u32 %[t0], %[t1], 147456\n\t"
                "buffer_load_dwordx4 a[100:103], %[go], %[so], %[t0] offen\n\t"
                "s_waitcnt lgkmcnt(10)\n\t"
                "v_mfma_f32_32x32x16_bf16 %[a1], v[204:207], v[200:203], %[a1]\n\t"
                "ds_read_b64 v[236:237], %[kb] offset:3552\n\t"
                "ds_read_b64 v[238:239], %[kb] offset:3560\n\t"
                "s_waitcnt lgkmcnt(9)\n\t"
                "v_mfma_f32_32x32x16_bf16 %[a1], v[208:211], v[212:215], %[a1]\n\t"
                "ds_read_b64 v[240:241], %[kb] offset:1472\n\t"
                "ds_read_b64 v[242:243], %[kb] offset:1480\n\t"
                "ds_read_b128 v[244:247], %[ub] offset:3232\n\t"
                "s_waitcnt lgkmcnt(11)\n\t"
                "v_mfma_f32_32x32x16_bf16 %[a0], v[208:211], v[216:219], %[a0]\n\t"
                "ds_read_b128 v[248:251], %[ub] offset:672\n\t"
                "s_waitcnt lgkmcnt(10)\n\t"
                "v_mfma_f32_32x32x16_bf16 %[a1], v[220:223], v[216:219], %[a1]\n\t"
                "ds_read_b64 v[252:253], %[kb] offset:3520\n\t"
                "ds_read_b64 v[254:255], %[kb] offset:3528\n\t"
                "s_waitcnt lgkmcnt(9)\n\t"
                "v_mfma_f32_32x32x16_bf16 %[a1], v[224:227], v[228:231], %[a1]\n\t"
                "ds_read_b64 v[192:193], %[kb] offset:1568\n\t"
                "ds_read_b64 v[194:195], %[kb] offset:1576\n\t"
                "ds_read_b128 v[196:199], %[ub] offset:3120\n\t"
                "s_waitcnt lgkmcnt(11)\n\t"
                "v_mfma_f32_32x32x16_bf16 %[a0], v[224:227], v[232:235], %[a0]\n\t"
                "ds_read_b128 v[200:203], %[ub] offset:560\n\t"
                "s_add_u32 %[t0], %[t1], 163840\n\t"
                "buffer_load_dwordx4 a[104:107], %[go], %[so], %[t0] offen\n\t"
                "s_waitcnt lgkmcnt(10)\n\t"
                "v_mfma_f32_32x32x16_bf16 %[a1], v[236:239], v[232:235], %[a1]\n\t"
                "ds_read_b64 v[204:205], %[kb] offset:3616\n\t"
                "ds_read_b64 v[206:207], %[kb] offset:3624\n\t"
                "s_waitcnt lgkmcnt(9)\n\t"
                "v_mfma_f32_32x32x16_bf16 %[a1], v[240:243], v[244:247], %[a1]\n\t"
                "ds_read_b64 v[208:209], %[kb] offset:1536\n\t"
                "ds_read_b64 v[210:211], %[kb] offset:1544\n\t"
                "ds_read_b128 v[212:215], %[ub] offset:3152\n\t"
                "s_waitcnt lgkmcnt(11)\n\t"
                "v_mfma_f32_32x32x16_bf16 %[a0], v[240:243], v[248:251], %[a0]\n\t"
                "ds_read_b128 v[216:219], %[ub] offset:592\n\t"
                "s_waitcnt lgkmcnt(10)\n\t"
                "v_mfma_f32_32x32x16_bf16 %[a1], v[252:255], v[248:251], %[a1]\n\t"
                "ds_read_b64 v[220:221], %[kb] offset:3584\n\t"
                "ds_read_b64 v[222:223], %[kb] offset:3592\n\t"
                "s_waitcnt lgkmcnt(9)\n\t"
                "v_mfma_f32_32x32x16_bf16 %[a1], v[192:195], v[196:199], %[a1]\n\t"
                "ds_read_b64 v[224:225], %[kb] offset:1632\n\t"
                "ds_read_b64 v[226:227], %[kb] offset:1640\n\t"
                "ds_read_b128 v[228:231], %[ub] offset:3040\n\t"
                "s_waitcnt lgkmcnt(11)\n\t"
                "v_mfma_f32_32x32x16_bf16 %[a0], v[192:195], v[200:203], %[a0]\n\t"
                "ds_read_b128 v[232:235], %[ub] offset:480\n\t"
                "s_add_u32 %[t0], %[t1], 180224\n\t"
                "buffer_load_dwordx4 a[108:111], %[go], %[so], %[t0] offen\n\t"
                "s_waitcnt lgkmcnt(10)\n\t"
                "v_mfma_f32_32x32x16_bf16 %[a1], v[204:207], v[200:203], %[a1]\n\t"
                "ds_read_b64 v[236:237], %[kb] offset:3680\n\t"
                "ds_read_b64 v[238:239], %[kb] offset:3688\n\t"
                "s_waitcnt lgkmcnt(9)\n\t"
                "v_mfma_f32_32x32x16_bf16 %[a1], v[208:211], v[212:215], %[a1]\n\t"
                "ds_read_b64 v[240:241], %[kb] offset:1600\n\t"
                "ds_read_b64 v[242:243], %[kb] offset:1608\n\t"
                "ds_read_b128 v[244:247], %[ub] offset:3072\n\t"
                "s_waitcnt lgkmcnt(11)\n\t"
                "v_mfma_f32_32x32x16_bf16 %[a0], v[208:211], v[216:219], %[a0]\n\t"
                "ds_read_b128 v[248:251], %[ub] offset:512\n\t"
                "s_add_u32 %[t0], %[t1], 196608\n\t"
                "buffer_load_dwordx4 a[112:115], %[go], %[so], %[t0] offen\n\t"
                "s_waitcnt lgkmcnt(10)\n\t"
                "v_mfma_f32_32x32x16_bf16 %[a1], v[220:223], v[216:219], %[a1]\n\t"
                "ds_read_b64 v[252:253], %[kb] offset:3648\n\t"
                "ds_read_b64 v[254:255], %[kb] offset:3656\n\t"
                "s_waitcnt lgkmcnt(9)\n\t"
                "v_mfma_f32_32x32x16_bf16 %[a1], v[224:227], v[228:231], %[a1]\n\t"
                "ds_read_b64 v[192:193], %[kb] offset:1696\n\t"
                "ds_read_b64 v[194:195], %[kb] offset:1704\n\t"
                "ds_read_b128 v[196:199], %[ub] offset:2960\n\t"
                "s_waitcnt lgkmcnt(11)\n\t"
                "v_mfma_f32_32x32x16_bf16 %[a0], v[224:227], v[232:235], %[a0]\n\t"
                "ds_read_b128 v[200:203], %[ub] offset:400\n\t"
                "s_waitcnt lgkmcnt(10)\n\t"
                "v_mfma_f32_32x32x16_bf16 %[a1], v[236:239], v[232:235], %[a1]\n\t"
                "ds_read_b64 v[204:205], %[kb] offset:3744\n\t"
                "ds_read_b64 v[206:207], %[kb] offset:3752\n\t"
                "s_waitcnt lgkmcnt(9)\n\t"
                "v_mfma_f32_32x32x16_bf16 %[a1], v[240:243], v[244:247], %[a1]\n\t"
                "ds_read_b64 v[208:209], %[kb] offset:1664\n\t"
                "ds_read_b64 v[210:211], %[kb] offset:1672\n\t"
                "ds_read_b128 v[212:215], %[ub] offset:2992\n\t"
                "s_waitcnt lgkmcnt(11)\n\t"
                "v_mfma_f32_32x32x16_bf16 %[a0], v[240:243], v[248:251], %[a0]\n\t"
                "ds_read_b128 v[216:219], %[ub] offset:432\n\t"
                "s_add_u32 %[t0], %[t1], 212992\n\t"
                "buffer_load_dwordx4 a[116:119], %[go], %[so], %[t0] offen\n\t"
                "s_waitcnt lgkmcnt(10)\n\t"
                "v_mfma_f32_32x32x16_bf16 %[a1], v[252:255], v[248:251], %[a1]\n\t"
                "ds_read_b64 v[220:221], %[kb] offset:3712\n\t"
                "ds_read_b64 v[222:223], %[kb] offset:3720\n\t"
                "s_waitcnt lgkmcnt(9)\n\t"
                "v_mfma_f32_32x32x16_bf16 %[a1], v[192:195], v[196:199], %[a1]\n\t"
                "ds_read_b64 v[224:225], %[kb] offset:1760\n\t"
                "ds_read_b64 v[226:227], %[kb] offset:1768\n\t"
                "ds_read_b128 v[228:231], %[ub] offset:2880\n\t"
                "s_waitcnt lgkmcnt(11)\n\t"
                "v_mfma_f32_32x32x16_bf16 %[a0], v[192:195], v[200:203], %[a0]\n\t"
                "ds_read_b128 v[232:235], %[ub] offset:320\n\t"
                "s_waitcnt lgkmcnt(10)\n\t"
                "v_mfma_f32_32x32x16_bf16 %[a1], v[204:207], v[200:203], %[a1]\n\t"
                "ds_read_b64 v[236:237], %[kb] offset:3808\n\t"
                "ds_read_b64 v[238:239], %[kb] offset:3816\n\t"
                "s_waitcnt lgkmcnt(9)\n\t"
                "v_mfma_f32_32x32x16_bf16 %[a1], v[208:211], v[212:215], %[a1]\n\t"
                "ds_read_b64 v[240:241], %[kb] offset:1728\n\t"
                "ds_read_b64 v[242:243], %[kb] offset:1736\n\t"
                "ds_read_b128 v[244:247], %[ub] offset:2912\n\t"
                "s_waitcnt lgkmcnt(11)\n\t"
                "v_mfma_f32_32x32x16_bf16 %[a0], v[208:211], v[216:219], %[a0]\n\t"
                "ds_read_b128 v[248:251], %[ub] offset:352\n\t"
                "s_add_u32 %[t0], %[t1], 229376\n\t"
                "buffer_load_dwordx4 a[120:123], %[go], %[so], %[t0] offen\n\t"
                "s_waitcnt lgkmcnt(10)\n\t"
                "v_mfma_f32_32x32x16_bf16 %[a1], v[220:223], v[216:219], %[a1]\n\t"
                "ds_read_b64 v[252:253], %[kb] offset:3776\n\t"
                "ds_read_b64 v[254:255], %[kb] offset:3784\n\t"
                "s_waitcnt lgkmcnt(9)\n\t"
                "v_mfma_f32_32x32x16_bf16 %[a1], v[224:227], v[228:231], %[a1]\n\t"
                "ds_read_b64 v[192:193], %[kb] offset:1824\n\t"
                "ds_read_b64 v[194:195], %[kb] offset:1832\n\t"
                "ds_read_b128 v[196:199], %[ub] offset:2800\n\t"
                "s_waitcnt lgkmcnt(11)\n\t"
                "v_mfma_f32_32x32x16_bf16 %[a0], v[224:227], v[232:235], %[a0]\n\t"
                "ds_read_b128 v[200:203], %[ub] offset:240\n\t"
                "s_waitcnt lgkmcnt(10)\n\t"
                "v_mfma_f32_32x32x16_bf16 %[a1], v[236:239], v[232:235], %[a1]\n\t"
                "ds_read_b64 v[204:205], %[kb] offset:3872\n\t"
                "ds_read_b64 v[206:207], %[kb] offset:3880\n\t"
                "s_waitcnt lgkmcnt(9)\n\t"
                "v_mfma_f32_32x32x16_bf16 %[a1], v[240:243], v[244:247], %[a1]\n\t"
                "ds_read_b64 v[208:209], %[kb] offset:1792\n\t"
                "ds_read_b64 v[210:211], %[kb] offset:1800\n\t"
                "ds_read_b128 v[212:215], %[ub] offset:2832\n\t"
                "s_waitcnt lgkmcnt(11)\n\t"
                "v_mfma_f32_32x32x16_bf16 %[a0], v[240:243], v[248:251], %[a0]\n\t"
                "ds_read_b128 v[216:219], %[ub] offset:272\n\t"
                "s_add_u32 %[t0], %[t1], 245760\n\t"
                "buffer_load_dwordx4 a[124:127], %[go], %[so], %[t0] offen\n\t"
                "s_waitcnt lgkmcnt(10)\n\t"
                "v_mfma_f32_32x32x16_bf16 %[a1], v[252:255], v[248:251], %[a1]\n\t"
                "ds_read_b64 v[220:221], %[kb] offset:3840\n\t"
                "ds_read_b64 v[222:223], %[kb] offset:3848\n\t"
                "s_waitcnt lgkmcnt(9)\n\t"
                "v_mfma_f32_32x32x16_bf16 %[a1], v[192:195], v[196:199], %[a1]\n\t"
                "ds_read_b64 v[224:225], %[kb] offset:1888\n\t"
                "ds_read_b64 v[226:227], %[kb] offset:1896\n\t"
                "ds_read_b128 v[228:231], %[ub] offset:2720\n\t"
                "s_waitcnt lgkmcnt(11)\n\t"
                "v_mfma_f32_32x32x16_bf16 %[a0], v[192:195], v[200:203], %[a0]\n\t"
                "ds_read_b128 v[232:235], %[ub] offset:160\n\t"
                "s_waitcnt lgkmcnt(10)\n\t"
                "v_mfma_f32_32x32x16_bf16 %[a1], v[204:207], v[200:203], %[a1]\n\t"
                "ds_read_b64 v[236:237], %[kb] offset:3936\n\t"
                "ds_read_b64 v[238:239], %[kb] offset:3944\n\t"
                "s_waitcnt lgkmcnt(9)\n\t"
                "v_mfma_f32_32x32x16_bf16 %[a1], v[208:211], v[212:215], %[a1]\n\t"
                "ds_read_b64 v[240:241], %[kb] offset:1856\n\t"
                "ds_read_b64 v[242:243], %[kb] offset:1864\n\t"
                "ds_read_b128 v[244:247], %[ub] offset:2752\n\t"
                "s_waitcnt lgkmcnt(11)\n\t"
                "v_mfma_f32_32x32x16_bf16 %[a0], v[208:211], v[216:219], %[a0]\n\t"
                "ds_read_b128 v[248:251], %[ub] offset:192\n\t"
                "s_waitcnt lgkmcnt(10)\n\t"
                "v_mfma_f32_32x32x16_bf16 %[a1], v[220:223], v[216:219], %[a1]\n\t"
                "ds_read_b64 v[252:253], %[kb] offset:3904\n\t"
                "ds_read_b64 v[254:255], %[kb] offset:3912\n\t"
                "s_waitcnt lgkmcnt(9)\n\t"
                "v_mfma_f32_32x32x16_bf16 %[a1], v[224:227], v[228:231], %[a1]\n\t"
                "ds_read_b64 v[192:193], %[kb] offset:1952\n\t"
                "ds_read_b64 v[194:195], %[kb] offset:1960\n\t"
                "ds_read_b128 v[196:199], %[ub] offset:2640\n\t"
                "s_waitcnt lgkmcnt(11)\n\t"
                "v_mfma_f32_32x32x16_bf16 %[a0], v[224:227], v[232:235], %[a0]\n\t"
                "ds_read_b128 v[200:203], %[ub] offset:80\n\t"
                "s_waitcnt lgkmcnt(10)\n\t"
                "v_mfma_f32_32x32x16_bf16 %[a1], v[236:239], v[232:235], %[a1]\n\t"
                "ds_read_b64 v[204:205], %[kb] offset:4000\n\t"
                "ds_read_b64 v[206:207], %[kb] offset:4008\n\t"
                "s_waitcnt lgkmcnt(9)\n\t"
                "v_mfma_f32_32x32x16_bf16 %[a1], v[240:243], v[244:247], %[a1]\n\t"
                "ds_read_b64 v[208:209], %[kb] offset:1920\n\t"
                "ds_read_b64 v[210:211], %[kb] offset:1928\n\t"
                "ds_read_b128 v[212:215], %[ub] offset:2672\n\t"
                "s_waitcnt lgkmcnt(11)\n\t"
                "v_mfma_f32_32x32x16_bf16 %[a0], v[240:243], v[248:251], %[a0]\n\t"
                "ds_read_b128 v[216:219], %[ub] offset:112\n\t"
                "s_waitcnt lgkmcnt(10)\n\t"
                "v_mfma_f32_32x32x16_bf16 %[a1], v[252:255], v[248:251], %[a1]\n\t"
                "ds_read_b64 v[220:221], %[kb] offset:3968\n\t"
                "ds_read_b64 v[222:223], %[kb] offset:3976\n\t"
                "s_waitcnt lgkmcnt(9)\n\t"
                "v_mfma_f32_32x32x16_bf16 %[a1], v[192:195], v[196:199], %[a1]\n\t"
                "ds_read_b64 v[224:225], %[kb] offset:2016\n\t"
                "ds_read_b64 v[226:227], %[kb] offset:2024\n\t"
                "ds_read_b128 v[228:231], %[ub] offset:2560\n\t"
                "s_waitcnt lgkmcnt(11)\n\t"
                "v_mfma_f32_32x32x16_bf16 %[a0], v[192:195], v[200:203], %[a0]\n\t"
                "ds_read_b128 v[232:235], %[ub] offset:0\n\t"
                "s_waitcnt lgkmcnt(10)\n\t"
                "v_mfma_f32_32x32x16_bf16 %[a1], v[204:207], v[200:203], %[a1]\n\t"
                "ds_read_b64 v[236:237], %[kb] offset:4064\n\t"
                "ds_read_b64 v[238:239], %[kb] offset:4072\n\t"
                "s_waitcnt lgkmcnt(9)\n\t"
                "v_mfma_f32_32x32x16_bf16 %[a1], v[208:211], v[212:215], %[a1]\n\t"
                "ds_read_b64 v[240:241], %[kb] offset:1984\n\t"
                "ds_read_b64 v[242:243], %[kb] offset:1992\n\t"
                "ds_read_b128 v[244:247], %[ub] offset:2592\n\t"
                "s_waitcnt lgkmcnt(11)\n\t"
                "v_mfma_f32_32x32x16_bf16 %[a0], v[208:211], v[216:219], %[a0]\n\t"
                "ds_read_b128 v[248:251], %[ub] offset:32\n\t"
                "s_waitcnt lgkmcnt(10)\n\t"
                "v_mfma_f32_32x32x16_bf16 %[a1], v[220:223], v[216:219], %[a1]\n\t"
                "ds_read_b64 v[252:253], %[kb] offset:4032\n\t"
                "ds_read_b64 v[254:255], %[kb] offset:4040\n\t"
                "s_waitcnt lgkmcnt(9)\n\t"
                "v_mfma_f32_32x32x16_bf16 %[a1], v[224:227], v[228:231], %[a1]\n\t"
                "s_waitcnt lgkmcnt(8)\n\t"
                "v_mfma_f32_32x32x16_bf16 %[a0], v[224:227], v[232:235], %[a0]\n\t"
                "s_waitcnt lgkmcnt(6)\n\t"
                "v_mfma_f32_32x32x16_bf16 %[a1], v[236:239], v[232:235], %[a1]\n\t"
                "s_waitcnt lgkmcnt(3)\n\t"
                "v_mfma_f32_32x32x16_bf16 %[a1], v[240:243], v[244:247], %[a1]\n\t"
                "s_waitcnt lgkmcnt(2)\n\t"
                "v_mfma_f32_32x32x16_bf16 %[a0], v[240:243], v[248:251], %[a0]\n\t"
                "s_waitcnt lgkmcnt(0)\n\t"
                "v_mfma_f32_32x32x16_bf16 %[a1], v[252:255], v[248:251], %[a1]\n\t"
                "s_waitcnt vmcnt(0)\n\t"
                "s_nop 7\n\t"
                "s_nop 3\n\t"
                : [a0] "=&v"(acc0), [a1] "=&v"(acc1), [t0] "=&s"(t0), [t1] "=&s"(t1)
                : [kb] "v"(kb), [ub] "v"(ub0 + c8 * CV_U), [go] "v"(go), [so] "s"(so), [ro] "s"(ro) : "memory", "scc", "v192", "v193", "v194", "v195", "v196", "v197", "v198", "v199", "v200", "v201", "v202", "v203", "v204", "v205", "v206", "v207", "v208", "v209", "v210", "v211", "v212", "v213", "v214", "v215", "v216", "v217", "v218", "v219", "v220", "v221", "v222", "v223", "v224", "v225", "v226", "v227", "v228", "v229", "v230", "v231", "v232", "v233", "v234", "v235", "v236", "v237", "v238", "v239", "v240", "v241", "v242", "v243", "v244", "v245", "v246", "v247", "v248", "v249", "v250", "v251", "v252", "v253", "v254", "v255", "a0", "a1", "a2", "a3", "a4", "a5", "a6", "a7", "a8", "a9", "a10", "a11", "a12", "a13", "a14", "a15", "a16", "a17", "a18", "a19", "a20", "a21", "a22", "a23", "a24", "a25", "a26", "a27", "a28", "a29", "a30", "a31", "a32", "a33", "a34", "a35", "a36", "a37", "a38", "a39", "a40", "a41", "a42", "a43", "a44", "a45", "a46", "a47", "a48", "a49", "a50", "a51", "a52", "a53", "a54", "a55", "a56", "a57", "a58", "a59", "a60", "a61", "a62", "a63", "a64", "a65", "a66", "a67", "a68", "a69", "a70", "a71", "a72", "a73", "a74", "a75", "a76", "a77", "a78", "a79", "a80", "a81", "a82", "a83", "a84", "a85", "a86", "a87", "a88", "a89", "a90", "a91", "a92", "a93", "a94", "a95", "a96", "a97", "a98", "a99", "a100", "a101", "a102", "a103", "a104", "a105", "a106", "a107", "a108", "a109", "a110", "a111", "a112", "a113", "a114", "a115", "a116", "a117", "a118", "a119", "a120", "a121", "a122", "a123", "a124", "a125", "a126", "a127", "a128", "a129", "a130", "a131", "a132", "a133", "a134", "a135", "a136", "a137", "a138", "a139", "a140", "a141", "a142", "a143", "a144", "a145", "a146", "a147", "a148", "a149", "a150", "a151", "a152", "a153", "a154", "a155", "a156", "a157", "a158", "a159", "a160", "a161", "a162", "a163", "a164", "a165", "a166", "a167", "a168", "a169", "a170", "a171", "a172", "a173", "a174", "a175", "a176", "a177", "a178", "a179", "a180", "a181", "a182", "a183", "a184", "a185", "a186", "a187", "a188", "a189", "a190", "a191", "a192", "a193", "a194", "a195", "a196", "a197", "a198", "a199", "a200", "a201", "a202", "a203", "a204", "a205", "a206", "a207", "a208", "a209", "a210", "a211", "a212", "a213", "a214", "a215", "a216", "a217", "a218", "a219", "a220", "a221", "a222", "a223", "a224", "a225", "a226", "a227", "a228", "a229", "a230", "a231", "a232", "a233", "a234", "a235", "a236", "a237", "a238", "a239", "a240", "a241", "a242", "a243", "a244", "a245", "a246", "a247", "a248", "a249", "a250", "a251", "a252", "a253", "a254", "a255");
            else asm volatile(
                "ds_read_b64 v[192:193], %[kb] offset:32\n\t"
                "ds_read_b64 v[194:195], %[kb] offset:40\n\t"
                "ds_read_b128 v[196:199], %[ub] offset:5040\n\t"
                "ds_read_b128 v[200:203], %[ub] offset:2480\n\t"
                "ds_read_b64 v[204:205], %[kb] offset:2080\n\t"
                "ds_read_b64 v[206:207], %[kb] offset:2088\n\t"
                "ds_read_b64 v[208:209], %[kb] offset:0\n\t"
                "ds_read_b64 v[210:211], %[kb] offset:8\n\t"
                "ds_read_b128 v[212:215], %[ub] offset:5072\n\t"
                "ds_read_b128 v[216:219], %[ub] offset:2512\n\t"
                "ds_read_b64 v[220:221], %[kb] offset:2048\n\t"
                "ds_read_b64 v[222:223], %[kb] offset:2056\n\t"
                "s_waitcnt lgkmcnt(9)\n\t"
                "v_mfma_f32_32x32x16_bf16 %[a1], v[192:195], v[196:199], 0\n\t"
                "ds_read_b64 v[224:225], %[kb] offset:96\n\t"
                "ds_read_b64 v[226:227], %[kb] offset:104\n\t"
                "ds_read_b128 v[228:231], %[ub] offset:4960\n\t"
                "s_waitcnt lgkmcnt(11)\n\t"
                "v_mfma_f32_32x32x16_bf16 %[a0], v[192:195], v[200:203], 0\n\t"
                "ds_read_b128 v[232:235], %[ub] offset:2400\n\t"
                "s_add_u32 %[t1], %[ro], 2\n\t"
                "s_and_b32 %[t1], %[t1], 7\n\t"
                "s_lshl_b32 %[t1], %[t1], 18\n\t"
                "s_add_u32 %[t0], %[t1], 0\n\t"
                "buffer_load_dwordx4 a[128:131], %[go], %[so], %[t0] offen\n\t"
                "s_waitcnt lgkmcnt(10)\n\t"
                "v_mfma_f32_32x32x16_bf16 %[a1], v[204:207], v[200:203], %[a1]\n\t"
                "ds_read_b64 v[236:237], %[kb] offset:2144\n\t"
                "ds_read_b64 v[238:239], %[kb] offset:2152\n\t"
                "s_waitcnt lgkmcnt(9)\n\t"
                "v_mfma_f32_32x32x16_bf16 %[a1], v[208:211], v[212:215], %[a1]\n\t"
                "ds_read_b64 v[240:241], %[kb] offset:64\n\t"
                "ds_read_b64 v[242:243], %[kb] offset:72\n\t"
                "ds_read_b128 v[244:247], %[ub] offset:4992\n\t"
                "s_waitcnt lgkmcnt(11)\n\t"
                "v_mfma_f32_32x32x16_bf16 %[a0], v[208:211], v[216:219], %[a0]\n\t"
                "ds_read_b128 v[248:251], %[ub] offset:2432\n\t"
                "s_waitcnt lgkmcnt(10)\n\t"
                "v_mfma_f32_32x32x16_bf16 %[a1], v[220:223], v[216:219], %[a1]\n\t"
                "ds_read_b64 v[252:253], %[kb] offset:2112\n\t"
                "ds_read_b64 v[254:255], %[kb] offset:2120\n\t"
                "s_waitcnt lgkmcnt(9)\n\t"
                "v_mfma_f32_32x32x16_bf16 %[a1], v[224:227], v[228:231], %[a1]\n\t"
                "ds_read_b64 v[192:193], %[kb] offset:160\n\t"
                "ds_read_b64 v[194:195], %[kb] offset:168\n\t"
                "ds_read_b128 v[196:199], %[ub] offset:4880\n\t"
                "s_waitcnt lgkmcnt(11)\n\t"
                "v_mfma_f32_32x32x16_bf16 %[a0], v[224:227], v[232:235], %[a0]\n\t"
                "ds_read_b128 v[200:203], %[ub] offset:2320\n\t"
                "s_add_u32 %[t0], %[t1], 16384\n\t"
                "buffer_load_dwordx4 a[132:135], %[go], %[so], %[t0] offen\n\t"
                "s_waitcnt lgkmcnt(10)\n\t"
                "v_mfma_f32_32x32x16_bf16 %[a1], v[236:239], v[232:235], %[a1]\n\t"
                "ds_read_b64 v[204:205], %[kb] offset:2208\n\t"
                "ds_read_b64 v[206:207], %[kb] offset:2216\n\t"
                "s_waitcnt lgkmcnt(9)\n\t"
                "v_mfma_f32_32x32x16_bf16 %[a1], v[240:243], v[244:247], %[a1]\n\t"
                "ds_read_b64 v[208:209], %[kb] offset:128\n\t"
                "ds_read_b64 v[210:211], %[kb] offset:136\n\t"
                "ds_read_b128 v[212:215], %[ub] offset:4912\n\t"
                "s_waitcnt lgkmcnt(11)\n\t"
                "v_mfma_f32_32x32x16_bf16 %[a0], v[240:243], v[248:251], %[a0]\n\t"
                "ds_read_b128 v[216:219], %[ub] offset:2352\n\t"
                "s_waitcnt lgkmcnt(10)\n\t"
                "v_mfma_f32_32x32x16_bf16 %[a1], v[252:255], v[248:251], %[a1]\n\t"
                "ds_read_b64 v[220:221], %[kb] offset:2176\n\t"
                "ds_read_b64 v[222:223], %[kb] offset:2184\n\t"
                "s_waitcnt lgkmcnt(9)\n\t"
                "v_mfma_f32_32x32x16_bf16 %[a1], v[192:195], v[196:199], %[a1]\n\t"
                "ds_read_b64 v[224:225], %[kb] offset:224\n\t"
                "ds_read_b64 v[226:227], %[kb] offset:232\n\t"
                "ds_read_b128 v[228:231], %[ub] offset:4800\n\t"
                "s_waitcnt lgkmcnt(11)\n\t"
                "v_mfma_f32_32x32x16_bf16 %[a0], v[192:195], v[200:203], %[a0]\n\t"
                "ds_read_b128 v[232:235], %[ub] offset:2240\n\t"
                "s_add_u32 %[t0], %[t1], 32768\n\t"
                "buffer_load_dwordx4 a[136:139], %[go], %[so], %[t0] offen\n\t"
                "s_waitcnt lgkmcnt(10)\n\t"
                "v_mfma_f32_32x32x16_bf16 %[a1], v[204:207], v[200:203], %[a1]\n\t"
                "ds_read_b64 v[236:237], %[kb] offset:2272\n\t"
                "ds_read_b64 v[238:239], %[kb] offset:2280\n\t"
                "s_waitcnt lgkmcnt(9)\n\t"
                "v_mfma_f32_32x32x16_bf16 %[a1], v[208:211], v[212:215], %[a1]\n\t"
                "ds_read_b64 v[240:241], %[kb] offset:192\n\t"
                "ds_read_b64 v[242:243], %[kb] offset:200\n\t"
                "ds_read_b128 v[244:247], %[ub] offset:4832\n\t"
                "s_waitcnt lgkmcnt(11)\n\t"
                "v_mfma_f32_32x32x16_bf16 %[a0], v[208:211], v[216:219], %[a0]\n\t"
                "ds_read_b128 v[248:251], %[ub] offset:2272\n\t"
                "s_waitcnt lgkmcnt(10)\n\t"
                "v_mfma_f32_32x32x16_bf16 %[a1], v[220:223], v[216:219], %[a1]\n\t"
                "ds_read_b64 v[252:253], %[kb] offset:2240\n\t"
                "ds_read_b64 v[254:255], %[kb] offset:2248\n\t"
                "s_waitcnt lgkmcnt(9)\n\t"
                "v_mfma_f32_32x32x16_bf16 %[a1], v[224:227], v[228:231], %[a1]\n\t"
                "ds_read_b64 v[192:193], %[kb] offset:288\n\t"
                "ds_read_b64 v[194:195], %[kb] offset:296\n\t"
                "ds_read_b128 v[196:199], %[ub] offset:4720\n\t"
                "s_waitcnt lgkmcnt(11)\n\t"
                "v_mfma_f32_32x32x16_bf16 %[a0], v[224:227], v[232:235], %[a0]\n\t"
                "ds_read_b128 v[200:203], %[ub] offset:2160\n\t"
                "s_add_u32 %[t0], %[t1], 49152\n\t"
                "buffer_load_dwordx4 a[140:143], %[go], %[so], %[t0] offen\n\t"
                "s_waitcnt lgkmcnt(10)\n\t"
                "v_mfma_f32_32x32x16_bf16 %[a1], v[236:239], v[232:235], %[a1]\n\t"
                "ds_read_b64 v[204:205], %[kb] offset:2336\n\t"
                "ds_read_b64 v[206:207], %[kb] offset:2344\n\t"
                "s_waitcnt lgkmcnt(9)\n\t"
                "v_mfma_f32_32x32x16_bf16 %[a1], v[240:243], v[244:247], %[a1]\n\t"
                "ds_read_b64 v[208:209], %[kb] offset:256\n\t"
                "ds_read_b64 v[210:211], %[kb] offset:264\n\t"
                "ds_read_b128 v[212:215], %[ub] offset:4752\n\t"
                "s_waitcnt lgkmcnt(11)\n\t"
                "v_mfma_f32_32x32x16_bf16 %[a0], v[240:243], v[248:251], %[a0]\n\t"
                "ds_read_b128 v[216:219], %[ub] offset:2192\n\t"
                "s_add_u32 %[t0], %[t1], 65536\n\t"
                "buffer_load_dwordx4 a[144:147], %[go], %[so], %[t0] offen\n\t"
                "s_waitcnt lgkmcnt(10)\n\t"
                "v_mfma_f32_32x32x16_bf16 %[a1], v[252:255], v[248:251], %[a1]\n\t"
                "ds_read_b64 v[220:221], %[kb] offset:2304\n\t"
                "ds_read_b64 v[222:223], %[kb] offset:2312\n\t"
                "s_waitcnt lgkmcnt(9)\n\t"
                "v_mfma_f32_32x32x16_bf16 %[a1], v[192:195], v[196:199], %[a1]\n\t"
                "ds_read_b64 v[224:225], %[kb] offset:352\n\t"
                "ds_read_b64 v[226:227], %[kb] offset:360\n\t"
                "ds_read_b128 v[228:231], %[ub] offset:4640\n\t"
                "s_waitcnt lgkmcnt(11)\n\t"
                "v_mfma_f32_32x32x16_bf16 %[a0], v[192:195], v[200:203], %[a0]\n\t"
                "ds_read_b128 v[232:235], %[ub] offset:2080\n\t"
                "s_waitcnt lgkmcnt(10)\n\t"
                "v_mfma_f32_32x32x16_bf16 %[a1], v[204:207], v[200:203], %[a1]\n\t"
                "ds_read_b64 v[236:237], %[kb] offset:2400\n\t"
                "ds_read_b64 v[238:239], %[kb] offset:2408\n\t"
                "s_waitcnt lgkmcnt(9)\n\t"
                "v_mfma_f32_32x32x16_bf16 %[a1], v[208:211], v[212:215], %[a1]\n\t"
                "ds_read_b64 v[240:241], %[kb] offset:320\n\t"
                "ds_read_b64 v[242:243], %[kb] offset:328\n\t"
                "ds_read_b128 v[244:247], %[ub] offset:4672\n\t"
                "s_waitcnt lgkmcnt(11)\n\t"
                "v_mfma_f32_32x32x16_bf16 %[a0], v[208:211], v[216:219], %[a0]\n\t"
                "ds_read_b128 v[248:251], %[ub] offset:2112\n\t"
                "s_add_u32 %[t0], %[t1], 81920\n\t"
                "buffer_load_dwordx4 a[148:151], %[go], %[so], %[t0] offen\n\t"
                "s_waitcnt lgkmcnt(10)\n\t"
                "v_mfma_f32_32x32x16_bf16 %[a1], v[220:223], v[216:219], %[a1]\n\t"
                "ds_read_b64 v[252:253], %[kb] offset:2368\n\t"
                "ds_read_b64 v[254:255], %[kb] offset:2376\n\t"
                "s_waitcnt lgkmcnt(9)\n\t"
                "v_mfma_f32_32x32x16_bf16 %[a1], v[224:227], v[228:231], %[a1]\n\t"
                "ds_read_b64 v[192:193], %[kb] offset:416\n\t"
                "ds_read_b64 v[194:195], %[kb] offset:424\n\t"
                "ds_read_b128 v[196:199], %[ub] offset:4560\n\t"
                "s_waitcnt lgkmcnt(11)\n\t"
                "v_mfma_f32_32x32x16_bf16 %[a0], v[224:227], v[232:235], %[a0]\n\t"
                "ds_read_b128 v[200:203], %[ub] offset:2000\n\t"
                "s_waitcnt lgkmcnt(10)\n\t"
                "v_mfma_f32_32x32x16_bf16 %[a1], v[236:239], v[232:235], %[a1]\n\t"
                "ds_read_b64 v[204:205], %[kb] offset:2464\n\t"
                "ds_read_b64 v[206:207], %[kb] offset:2472\n\t"
                "s_waitcnt lgkmcnt(9)\n\t"
                "v_mfma_f32_32x32x16_bf16 %[a1], v[240:243], v[244:247], %[a1]\n\t"
                "ds_read_b64 v[208:209], %[kb] offset:384\n\t"
                "ds_read_b64 v[210:211], %[kb] offset:392\n\t"
                "ds_read_b128 v[212:215], %[ub] offset:4592\n\t"
                "s_waitcnt lgkmcnt(11)\n\t"
                "v_mfma_f32_32x32x16_bf16 %[a0], v[240:243], v[248:251], %[a0]\n\t"
                "ds_read_b128 v[216:219], %[ub] offset:2032\n\t"
                "s_add_u32 %[t0], %[t1], 98304\n\t"
                "buffer_load_dwordx4 a[152:155], %[go], %[so], %[t0] offen\n\t"
                "s_waitcnt lgkmcnt(10)\n\t"
                "v_mfma_f32_32x32x16_bf16 %[a1], v[252:255], v[248:251], %[a1]\n\t"
                "ds_read_b64 v[220:221], %[kb] offset:2432\n\t"
                "ds_read_b64 v[222:223], %[kb] offset:2440\n\t"
                "s_waitcnt lgkmcnt(9)\n\t"
                "v_mfma_f32_32x32x16_bf16 %[a1], v[192:195], v[196:199], %[a1]\n\t"
                "ds_read_b64 v[224:225], %[kb] offset:480\n\t"
                "ds_read_b64 v[226:227], %[kb] offset:488\n\t"
                "ds_read_b128 v[228:231], %[ub] offset:4480\n\t"
                "s_waitcnt lgkmcnt(11)\n\t"
                "v_mfma_f32_32x32x16_bf16 %[a0], v[192:195], v[200:203], %[a0]\n\t"
                "ds_read_b128 v[232:235], %[ub] offset:1920\n\t"
                "s_waitcnt lgkmcnt(10)\n\t"
                "v_mfma_f32_32x32x16_bf16 %[a1], v[204:207], v[200:203], %[a1]\n\t"
                "ds_read_b64 v[236:237], %[kb] offset:2528\n\t"
                "ds_read_b64 v[238:239], %[kb] offset:2536\n\t"
                "s_waitcnt lgkmcnt(9)\n\t"
                "v_mfma_f32_32x32x16_bf16 %[a1], v[208:211], v[212:215], %[a1]\n\t"
                "ds_read_b64 v[240:241], %[kb] offset:448\n\t"
                "ds_read_b64 v[242:243], %[kb] offset:456\n\t"
                "ds_read_b128 v[244:247], %[ub] offset:4512\n\t"
                "s_waitcnt lgkmcnt(11)\n\t"
                "v_mfma_f32_32x32x16_bf16 %[a0], v[208:211], v[216:219], %[a0]\n\t"
                "ds_read_b128 v[248:251], %[ub] offset:1952\n\t"
                "s_add_u32 %[t0], %[t1], 114688\n\t"
                "buffer_load_dwordx4 a[156:159], %[go], %[so], %[t0] offen\n\t"
                "s_waitcnt lgkmcnt(10)\n\t"
                "v_mfma_f32_32x32x16_bf16 %[a1], v[220:223], v[216:219], %[a1]\n\t"
                "ds_read_b64 v[252:253], %[kb] offset:2496\n\t"
                "ds_read_b64 v[254:255], %[kb] offset:2504\n\t"
                "s_waitcnt lgkmcnt(9)\n\t"
                "v_mfma_f32_32x32x16_bf16 %[a1], v[224:227], v[228:231], %[a1]\n\t"
                "ds_read_b64 v[192:193], %[kb] offset:544\n\t"
                "ds_read_b64 v[194:195], %[kb] offset:552\n\t"
                "ds_read_b128 v[196:199], %[ub] offset:4400\n\t"
                "s_waitcnt lgkmcnt(11)\n\t"
                "v_mfma_f32_32x32x16_bf16 %[a0], v[224:227], v[232:235], %[a0]\n\t"
                "ds_read_b128 v[200:203], %[ub] offset:1840\n\t"
                "s_add_u32 %[t1], %[ro], 2\n\t"
                "s_and_b32 %[t1], %[t1], 7\n\t"
                "s_lshl_b32 %[t1], %[t1], 18\n\t"
                "s_add_u32 %[t0], %[t1], 131072\n\t"
                "buffer_load_dwordx4 a[160:163], %[go], %[so], %[t0] offen\n\t"
                "s_waitcnt lgkmcnt(10)\n\t"
                "v_mfma_f32_32x32x16_bf16 %[a1], v[236:239], v[232:235], %[a1]\n\t"
                "ds_read_b64 v[204:205], %[kb] offset:2592\n\t"
                "ds_read_b64 v[206:207], %[kb] offset:2600\n\t"
                "s_waitcnt lgkmcnt(9)\n\t"
                "v_mfma_f32_32x32x16_bf16 %[a1], v[240:243], v[244:247], %[a1]\n\t"
                "ds_read_b64 v[208:209], %[kb] offset:512\n\t"
                "ds_read_b64 v[210:211], %[kb] offset:520\n\t"
                "ds_read_b128 v[212:215], %[ub] offset:4432\n\t"
                "s_waitcnt lgkmcnt(11)\n\t"
                "v_mfma_f32_32x32x16_bf16 %[a0], v[240:243], v[248:251], %[a0]\n\t"
                "ds_read_b128 v[216:219], %[ub] offset:1872\n\t"
                "s_waitcnt lgkmcnt(10)\n\t"
                "v_mfma_f32_32x32x16_bf16 %[a1], v[252:255], v[248:251], %[a1]\n\t"
                "ds_read_b64 v[220:221], %[kb] offset:2560\n\t"
                "ds_read_b64 v[222:223], %[kb] offset:2568\n\t"
                "s_waitcnt lgkmcnt(9)\n\t"
                "v_mfma_f32_32x32x16_bf16 %[a1], v[192:195], v[196:199], %[a1]\n\t"
                "ds_read_b64 v[224:225], %[kb] offset:608\n\t"
                "ds_read_b64 v[226:227], %[kb] offset:616\n\t"
                "ds_read_b128 v[228:231], %[ub] offset:4320\n\t"
                "s_waitcnt lgkmcnt(11)\n\t"
                "v_mfma_f32_32x32x16_bf16 %[a0], v[192:195], v[200:203], %[a0]\n\t"
                "ds_read_b128 v[232:235], %[ub] offset:1760\n\t"
                "s_add_u32 %[t0], %[t1], 147456\n\t"
                "buffer_load_dwordx4 a[164:167], %[go], %[so], %[t0] offen\n\t"
                "s_waitcnt lgkmcnt(10)\n\t"
                "v_mfma_f32_32x32x16_bf16 %[a1], v[204:207], v[200:203], %[a1]\n\t"
                "ds_read_b64 v[236:237], %[kb] offset:2656\n\t"
                "ds_read_b64 v[238:239], %[kb] offset:2664\n\t"
                "s_waitcnt lgkmcnt(9)\n\t"
                "v_mfma_f32_32x32x16_bf16 %[a1], v[208:211], v[212:215], %[a1]\n\t"
                "ds_read_b64 v[240:241], %[kb] offset:576\n\t"
                "ds_read_b64 v[242:243], %[kb] offset:584\n\t"
                "ds_read_b128 v[244:247], %[ub] offset:4352\n\t"
                "s_waitcnt lgkmcnt(11)\n\t"
                "v_mfma_f32_32x32x16_bf16 %[a0], v[208:211], v[216:219], %[a0]\n\t"
                "ds_read_b128 v[248:251], %[ub] offset:1792\n\t"
                "s_waitcnt lgkmcnt(10)\n\t"
                "v_mfma_f32_32x32x16_bf16 %[a1], v[220:223], v[216:219], %[a1]\n\t"
                "ds_read_b64 v[252:253], %[kb] offset:2624\n\t"
                "ds_read_b64 v[254:255], %[kb] offset:2632\n\t"
                "s_waitcnt lgkmcnt(9)\n\t"
                "v_mfma_f32_32x32x16_bf16 %[a1], v[224:227], v[228:231], %[a1]\n\t"
                "ds_read_b64 v[192:193], %[kb] offset:672\n\t"
                "ds_read_b64 v[194:195], %[kb] offset:680\n\t"
                "ds_read_b128 v[196:199], %[ub] offset:4240\n\t"
                "s_waitcnt lgkmcnt(11)\n\t"
                "v_mfma_f32_32x32x16_bf16 %[a0], v[224:227], v[232:235], %[a0]\n\t"
                "ds_read_b128 v[200:203], %[ub] offset:1680\n\t"
                "s_add_u32 %[t0], %[t1], 163840\n\t"
                "buffer_load_dwordx4 a[168:171], %[go], %[so], %[t0] offen\n\t"
                "s_waitcnt lgkmcnt(10)\n\t"
                "v_mfma_f32_32x32x16_bf16 %[a1], v[236:239], v[232:235], %[a1]\n\t"
                "ds_read_b64 v[204:205], %[kb] offset:2720\n\t"
                "ds_read_b64 v[206:207], %[kb] offset:2728\n\t"
                "s_waitcnt lgkmcnt(9)\n\t"
                "v_mfma_f32_32x32x16_bf16 %[a1], v[240:243], v[244:247], %[a1]\n\t"
                "ds_read_b64 v[208:209], %[kb] offset:640\n\t"
                "ds_read_b64 v[210:211], %[kb] offset:648\n\t"
                "ds_read_b128 v[212:215], %[ub] offset:4272\n\t"
                "s_waitcnt lgkmcnt(11)\n\t"
                "v_mfma_f32_32x32x16_bf16 %[a0], v[240:243], v[248:251], %[a0]\n\t"
                "ds_read_b128 v[216:219], %[ub] offset:1712\n\t"
                "s_waitcnt lgkmcnt(10)\n\t"
                "v_mfma_f32_32x32x16_bf16 %[a1], v[252:255], v[248:251], %[a1]\n\t"
                "ds_read_b64 v[220:221], %[kb] offset:2688\n\t"
                "ds_read_b64 v[222:223], %[kb] offset:2696\n\t"
                "s_waitcnt lgkmcnt(9)\n\t"
                "v_mfma_f32_32x32x16_bf16 %[a1], v[192:195], v[196:199], %[a1]\n\t"
                "ds_read_b64 v[224:225], %[kb] offset:736\n\t"
                "ds_read_b64 v[226:227], %[kb] offset:744\n\t"
                "ds_read_b128 v[228:231], %[ub] offset:4160\n\t"
                "s_waitcnt lgkmcnt(11)\n\t"
                "v_mfma_f32_32x32x16_bf16 %[a0], v[192:195], v[200:203], %[a0]\n\t"
                "ds_read_b128 v[232:235], %[ub] offset:1600\n\t"
                "s_add_u32 %[t0], %[t1], 180224\n\t"
                "buffer_load_dwordx4 a[172:175], %[go], %[so], %[t0] offen\n\t"
                "s_waitcnt lgkmcnt(10)\n\t"
                "v_mfma_f32_32x32x16_bf16 %[a1], v[204:207], v[200:203], %[a1]\n\t"
                "ds_read_b64 v[236:237], %[kb] offset:2784\n\t"
                "ds_read_b64 v[238:239], %[kb] offset:2792\n\t"
                "s_waitcnt lgkmcnt(9)\n\t"
                "v_mfma_f32_32x32x16_bf16 %[a1], v[208:211], v[212:215], %[a1]\n\t"
                "ds_read_b64 v[240:241], %[kb] offset:704\n\t"
                "ds_read_b64 v[242:243], %[kb] offset:712\n\t"
                "ds_read_b128 v[244:247], %[ub] offset:4192\n\t"
                "s_waitcnt lgkmcnt(11)\n\t"
                "v_mfma_f32_32x32x16_bf16 %[a0], v[208:211], v[216:219], %[a0]\n\t"
                "ds_read_b128 v[248:251], %[ub] offset:1632\n\t"
                "s_add_u32 %[t0], %[t1], 196608\n\t"
                "buffer_load_dwordx4 a[176:179], %[go], %[so], %[t0] offen\n\t"
                "s_waitcnt lgkmcnt(10)\n\t"
                "v_mfma_f32_32x32x16_bf16 %[a1], v[220:223], v[216:219], %[a1]\n\t"
                "ds_read_b64 v[252:253], %[kb] offset:2752\n\t"
                "ds_read_b64 v[254:255], %[kb] offset:2760\n\t"
                "s_waitcnt lgkmcnt(9)\n\t"
                "v_mfma_f32_32x32x16_bf16 %[a1], v[224:227], v[228:231], %[a1]\n\t"
                "ds_read_b64 v[192:193], %[kb] offset:800\n\t"
                "ds_read_b64 v[194:195], %[kb] offset:808\n\t"
                "ds_read_b128 v[196:199], %[ub] offset:4080\n\t"
                "s_waitcnt lgkmcnt(11)\n\t"
                "v_mfma_f32_32x32x16_bf16 %[a0], v[224:227], v[232:235], %[a0]\n\t"
                "ds_read_b128 v[200:203], %[ub] offset:1520\n\t"
                "s_waitcnt lgkmcnt(10)\n\t"
                "v_mfma_f32_32x32x16_bf16 %[a1], v[236:239], v[232:235], %[a1]\n\t"
                "ds_read_b64 v[204:205], %[kb] offset:2848\n\t"
                "ds_read_b64 v[206:207], %[kb] offset:2856\n\t"
                "s_waitcnt lgkmcnt(9)\n\t"
                "v_mfma_f32_32x32x16_bf16 %[a1], v[240:243], v[244:247], %[a1]\n\t"
                "ds_read_b64 v[208:209], %[kb] offset:768\n\t"
                "ds_read_b64 v[210:211], %[kb] offset:776\n\t"
                "ds_read_b128 v[212:215], %[ub] offset:4112\n\t"
                "s_waitcnt lgkmcnt(11)\n\t"
                "v_mfma_f32_32x32x16_bf16 %[a0], v[240:243], v[248:251], %[a0]\n\t"
                "ds_read_b128 v[216:219], %[ub] offset:1552\n\t"
                "s_add_u32 %[t0], %[t1], 212992\n\t"
                "buffer_load_dwordx4 a[180:183], %[go], %[so], %[t0] offen\n\t"
                "s_waitcnt lgkmcnt(10)\n\t"
                "v_mfma_f32_32x32x16_bf16 %[a1], v[252:255], v[248:251], %[a1]\n\t"
                "ds_read_b64 v[220:221], %[kb] offset:2816\n\t"
                "ds_read_b64 v[222:223], %[kb] offset:2824\n\t"
                "s_waitcnt lgkmcnt(9)\n\t"
                "v_mfma_f32_32x32x16_bf16 %[a1], v[192:195], v[196:199], %[a1]\n\t"
                "ds_read_b64 v[224:225], %[kb] offset:864\n\t"
                "ds_read_b64 v[226:227], %[kb] offset:872\n\t"
                "ds_read_b128 v[228:231], %[ub] offset:4000\n\t"
                "s_waitcnt lgkmcnt(11)\n\t"
                "v_mfma_f32_32x32x16_bf16 %[a0], v[192:195], v[200:203], %[a0]\n\t"
                "ds_read_b128 v[232:235], %[ub] offset:1440\n\t"
                "s_waitcnt lgkmcnt(10)\n\t"
                "v_mfma_f32_32x32x16_bf16 %[a1], v[204:207], v[200:203], %[a1]\n\t"
                "ds_read_b64 v[236:237], %[kb] offset:2912\n\t"
                "ds_read_b64 v[238:239], %[kb] offset:2920\n\t"
                "s_waitcnt lgkmcnt(9)\n\t"
                "v_mfma_f32_32x32x16_bf16 %[a1], v[208:211], v[212:215], %[a1]\n\t"
                "ds_read_b64 v[240:241], %[kb] offset:832\n\t"
                "ds_read_b64 v[242:243], %[kb] offset:840\n\t"
                "ds_read_b128 v[244:247], %[ub] offset:4032\n\t"
                "s_waitcnt lgkmcnt(11)\n\t"
                "v_mfma_f32_32x32x16_bf16 %[a0], v[208:211], v[216:219], %[a0]\n\t"
                "ds_read_b128 v[248:251], %[ub] offset:1472\n\t"
                "s_add_u32 %[t0], %[t1], 229376\n\t"
                "buffer_load_dwordx4 a[184:187], %[go], %[so], %[t0] offen\n\t"
                "s_waitcnt lgkmcnt(10)\n\t"
                "v_mfma_f32_32x32x16_bf16 %[a1], v[220:223], v[216:219], %[a1]\n\t"
                "ds_read_b64 v[252:253], %[kb] offset:2880\n\t"
                "ds_read_b64 v[254:255], %[kb] offset:2888\n\t"
                "s_waitcnt lgkmcnt(9)\n\t"
                "v_mfma_f32_32x32x16_bf16 %[a1], v[224:227], v[228:231], %[a1]\n\t"
                "ds_read_b64 v[192:193], %[kb] offset:928\n\t"
                "ds_read_b64 v[194:195], %[kb] offset:936\n\t"
                "ds_read_b128 v[196:199], %[ub] offset:3920\n\t"
                "s_waitcnt lgkmcnt(11)\n\t"
                "v_mfma_f32_32x32x16_bf16 %[a0], v[224:227], v[232:235], %[a0]\n\t"
                "ds_read_b128 v[200:203], %[ub] offset:1360\n\t"
                "s_waitcnt lgkmcnt(10)\n\t"
                "v_mfma_f32_32x32x16_bf16 %[a1], v[236:239], v[232:235], %[a1]\n\t"
                "ds_read_b64 v[204:205], %[kb] offset:2976\n\t"
                "ds_read_b64 v[206:207], %[kb] offset:2984\n\t"
                "s_waitcnt lgkmcnt(9)\n\t"
                "v_mfma_f32_32x32x16_bf16 %[a1], v[240:243], v[244:247], %[a1]\n\t"
                "ds_read_b64 v[208:209], %[kb] offset:896\n\t"
                "ds_read_b64 v[210:211], %[kb] offset:904\n\t"
                "ds_read_b128 v[212:215], %[ub] offset:3952\n\t"
                "s_waitcnt lgkmcnt(11)\n\t"
                "v_mfma_f32_32x32x16_bf16 %[a0], v[240:243], v[248:251], %[a0]\n\t"
                "ds_read_b128 v[216:219], %[ub] offset:1392\n\t"
                "s_add_u32 %[t0], %[t1], 245760\n\t"
                "buffer_load_dwordx4 a[188:191], %[go], %[so], %[t0] offen\n\t"
                "s_waitcnt lgkmcnt(10)\n\t"
                "v_mfma_f32_32x32x16_bf16 %[a1], v[252:255], v[248:251], %[a1]\n\t"
                "ds_read_b64 v[220:221], %[kb] offset:2944\n\t"
                "ds_read_b64 v[222:223], %[kb] offset:2952\n\t"
                "s_waitcnt lgkmcnt(9)\n\t"
                "v_mfma_f32_32x32x16_bf16 %[a1], v[192:195], v[196:199], %[a1]\n\t"
                "ds_read_b64 v[224:225], %[kb] offset:992\n\t"
                "ds_read_b64 v[226:227], %[kb] offset:1000\n\t"
                "ds_read_b128 v[228:231], %[ub] offset:3840\n\t"
                "s_waitcnt lgkmcnt(11)\n\t"
                "v_mfma_f32_32x32x16_bf16 %[a0], v[192:195], v[200:203], %[a0]\n\t"
                "ds_read_b128 v[232:235], %[ub] offset:1280\n\t"
                "s_add_u32 %[t1], %[ro], 3\n\t"
                "s_and_b32 %[t1], %[t1], 7\n\t"
                "s_lshl_b32 %[t1], %[t1], 18\n\t"
                "s_add_u32 %[t0], %[t1], 0\n\t"
                "buffer_load_dwordx4 a[192:195], %[go], %[so], %[t0] offen\n\t"
                "s_waitcnt lgkmcnt(10)\n\t"
                "v_mfma_f32_32x32x16_bf16 %[a1], v[204:207], v[200:203], %[a1]\n\t"
                "ds_read_b64 v[236:237], %[kb] offset:3040\n\t"
                "ds_read_b64 v[238:239], %[kb] offset:3048\n\t"
                "s_waitcnt lgkmcnt(9)\n\t"
                "v_mfma_f32_32x32x16_bf16 %[a1], v[208:211], v[212:215], %[a1]\n\t"
                "ds_read_b64 v[240:241], %[kb] offset:960\n\t"
                "ds_read_b64 v[242:243], %[kb] offset:968\n\t"
                "ds_read_b128 v[244:247], %[ub] offset:3872\n\t"
                "s_waitcnt lgkmcnt(11)\n\t"
                "v_mfma_f32_32x32x16_bf16 %[a0], v[208:211], v[216:219], %[a0]\n\t"
                "ds_read_b128 v[248:251], %[ub] offset:1312\n\t"
                "s_waitcnt lgkmcnt(10)\n\t"
                "v_mfma_f32_32x32x16_bf16 %[a1], v[220:223], v[216:219], %[a1]\n\t"
                "ds_read_b64 v[252:253], %[kb] offset:3008\n\t"
                "ds_read_b64 v[254:255], %[kb] offset:3016\n\t"
                "s_waitcnt lgkmcnt(9)\n\t"
                "v_mfma_f32_32x32x16_bf16 %[a1], v[224:227], v[228:231], %[a1]\n\t"
                "ds_read_b64 v[192:193], %[kb] offset:1056\n\t"
                "ds_read_b64 v[194:195], %[kb] offset:1064\n\t"
                "ds_read_b128 v[196:199], %[ub] offset:3760\n\t"
                "s_waitcnt lgkmcnt(11)\n\t"
                "v_mfma_f32_32x32x16_bf16 %[a0], v[224:227], v[232:235], %[a0]\n\t"
                "ds_read_b128 v[200:203], %[ub] offset:1200\n\t"
                "s_add_u32 %[t0], %[t1], 16384\n\t"
                "buffer_load_dwordx4 a[196:199], %[go], %[so], %[t0] offen\n\t"
                "s_waitcnt lgkmcnt(10)\n\t"
                "v_mfma_f32_32x32x16_bf16 %[a1], v[236:239], v[232:235], %[a1]\n\t"
                "ds_read_b64 v[204:205], %[kb] offset:3104\n\t"
                "ds_read_b64 v[206:207], %[kb] offset:3112\n\t"
                "s_waitcnt lgkmcnt(9)\n\t"
                "v_mfma_f32_32x32x16_bf16 %[a1], v[240:243], v[244:247], %[a1]\n\t"
                "ds_read_b64 v[208:209], %[kb] offset:1024\n\t"
                "ds_read_b64 v[210:211], %[kb] offset:1032\n\t"
                "ds_read_b128 v[212:215], %[ub] offset:3792\n\t"
                "s_waitcnt lgkmcnt(11)\n\t"
                "v_mfma_f32_32x32x16_bf16 %[a0], v[240:243], v[248:251], %[a0]\n\t"
                "ds_read_b128 v[216:219], %[ub] offset:1232\n\t"
                "s_waitcnt lgkmcnt(10)\n\t"
                "v_mfma_f32_32x32x16_bf16 %[a1], v[252:255], v[248:251], %[a1]\n\t"
                "ds_read_b64 v[220:221], %[kb] offset:3072\n\t"
                "ds_read_b64 v[222:223], %[kb] offset:3080\n\t"
                "s_waitcnt lgkmcnt(9)\n\t"
                "v_mfma_f32_32x32x16_bf16 %[a1], v[192:195], v[196:199], %[a1]\n\t"
                "ds_read_b64 v[224:225], %[kb] offset:1120\n\t"
                "ds_read_b64 v[226:227], %[kb] offset:1128\n\t"
                "ds_read_b128 v[228:231], %[ub] offset:3680\n\t"
                "s_waitcnt lgkmcnt(11)\n\t"
                "v_mfma_f32_32x32x16_bf16 %[a0], v[192:195], v[200:203], %[a0]\n\t"
                "ds_read_b128 v[232:235], %[ub] offset:1120\n\t"
                "s_add_u32 %[t0], %[t1], 32768\n\t"
                "buffer_load_dwordx4 a[200:203], %[go], %[so], %[t0] offen\n\t"
                "s_waitcnt lgkmcnt(10)\n\t"
                "v_mfma_f32_32x32x16_bf16 %[a1], v[204:207], v[200:203], %[a1]\n\t"
                "ds_read_b64 v[236:237], %[kb] offset:3168\n\t"
                "ds_read_b64 v[238:239], %[kb] offset:3176\n\t"
                "s_waitcnt lgkmcnt(9)\n\t"
                "v_mfma_f32_32x32x16_bf16 %[a1], v[208:211], v[212:215], %[a1]\n\t"
                "ds_read_b64 v[240:241], %[kb] offset:1088\n\t"
                "ds_read_b64 v[242:243], %[kb] offset:1096\n\t"
                "ds_read_b128 v[244:247], %[ub] offset:3712\n\t"
                "s_waitcnt lgkmcnt(11)\n\t"
                "v_mfma_f32_32x32x16_bf16 %[a0], v[208:211], v[216:219], %[a0]\n\t"
                "ds_read_b128 v[248:251], %[ub] offset:1152\n\t"
                "s_waitcnt lgkmcnt(10)\n\t"
                "v_mfma_f32_32x32x16_bf16 %[a1], v[220:223], v[216:219], %[a1]\n\t"
                "ds_read_b64 v[252:253], %[kb] offset:3136\n\t"
                "ds_read_b64 v[254:255], %[kb] offset:3144\n\t"
                "s_waitcnt lgkmcnt(9)\n\t"
                "v_mfma_f32_32x32x16_bf16 %[a1], v[224:227], v[228:231], %[a1]\n\t"
                "ds_read_b64 v[192:193], %[kb] offset:1184\n\t"
                "ds_read_b64 v[194:195], %[kb] offset:1192\n\t"
                "ds_read_b128 v[196:199], %[ub] offset:3600\n\t"
                "s_waitcnt lgkmcnt(11)\n\t"
                "v_mfma_f32_32x32x16_bf16 %[a0], v[224:227], v[232:235], %[a0]\n\t"
                "ds_read_b128 v[200:203], %[ub] offset:1040\n\t"
                "s_add_u32 %[t0], %[t1], 49152\n\t"
                "buffer_load_dwordx4 a[204:207], %[go], %[so], %[t0] offen\n\t"
                "s_waitcnt lgkmcnt(10)\n\t"
                "v_mfma_f32_32x32x16_bf16 %[a1], v[236:239], v[232:235], %[a1]\n\t"
                "ds_read_b64 v[204:205], %[kb] offset:3232\n\t"
                "ds_read_b64 v[206:207], %[kb] offset:3240\n\t"
                "s_waitcnt lgkmcnt(9)\n\t"
                "v_mfma_f32_32x32x16_bf16 %[a1], v[240:243], v[244:247], %[a1]\n\t"
                "ds_read_b64 v[208:209], %[kb] offset:1152\n\t"
                "ds_read_b64 v[210:211], %[kb] offset:1160\n\t"
                "ds_read_b128 v[212:215], %[ub] offset:3632\n\t"
                "s_waitcnt lgkmcnt(11)\n\t"
                "v_mfma_f32_32x32x16_bf16 %[a0], v[240:243], v[248:251], %[a0]\n\t"
                "ds_read_b128 v[216:219], %[ub] offset:1072\n\t"
                "s_add_u32 %[t0], %[t1], 65536\n\t"
                "buffer_load_dwordx4 a[208:211], %[go], %[so], %[t0] offen\n\t"
                "s_waitcnt lgkmcnt(10)\n\t"
                "v_mfma_f32_32x32x16_bf16 %[a1], v[252:255], v[248:251], %[a1]\n\t"
                "ds_read_b64 v[220:221], %[kb] offset:3200\n\t"
                "ds_read_b64 v[222:223], %[kb] offset:3208\n\t"
                "s_waitcnt lgkmcnt(9)\n\t"
                "v_mfma_f32_32x32x16_bf16 %[a1], v[192:195], v[196:199], %[a1]\n\t"
                "ds_read_b64 v[224:225], %[kb] offset:1248\n\t"
                "ds_read_b64 v[226:227], %[kb] offset:1256\n\t"
                "ds_read_b128 v[228:231], %[ub] offset:3520\n\t"
                "s_waitcnt lgkmcnt(11)\n\t"
                "v_mfma_f32_32x32x16_bf16 %[a0], v[192:195], v[200:203], %[a0]\n\t"
                "ds_read_b128 v[232:235], %[ub] offset:960\n\t"
                "s_waitcnt lgkmcnt(10)\n\t"
                "v_mfma_f32_32x32x16_bf16 %[a1], v[204:207], v[200:203], %[a1]\n\t"
                "ds_read_b64 v[236:237], %[kb] offset:3296\n\t"
                "ds_read_b64 v[238:239], %[kb] offset:3304\n\t"
                "s_waitcnt lgkmcnt(9)\n\t"
                "v_mfma_f32_32x32x16_bf16 %[a1], v[208:211], v[212:215], %[a1]\n\t"
                "ds_read_b64 v[240:241], %[kb] offset:1216\n\t"
                "ds_read_b64 v[242:243], %[kb] offset:1224\n\t"
                "ds_read_b128 v[244:247], %[ub] offset:3552\n\t"
                "s_waitcnt lgkmcnt(11)\n\t"
                "v_mfma_f32_32x32x16_bf16 %[a0], v[208:211], v[216:219], %[a0]\n\t"
                "ds_read_b128 v[248:251], %[ub] offset:992\n\t"
                "s_add_u32 %[t0], %[t1], 81920\n\t"
                "buffer_load_dwordx4 a[212:215], %[go], %[so], %[t0] offen\n\t"
                "s_waitcnt lgkmcnt(10)\n\t"
                "v_mfma_f32_32x32x16_bf16 %[a1], v[220:223], v[216:219], %[a1]\n\t"
                "ds_read_b64 v[252:253], %[kb] offset:3264\n\t"
                "ds_read_b64 v[254:255], %[kb] offset:3272\n\t"
                "s_waitcnt lgkmcnt(9)\n\t"
                "v_mfma_f32_32x32x16_bf16 %[a1], v[224:227], v[228:231], %[a1]\n\t"
                "ds_read_b64 v[192:193], %[kb] offset:1312\n\t"
                "ds_read_b64 v[194:195], %[kb] offset:1320\n\t"
                "ds_read_b128 v[196:199], %[ub] offset:3440\n\t"
                "s_waitcnt lgkmcnt(11)\n\t"
                "v_mfma_f32_32x32x16_bf16 %[a0], v[224:227], v[232:235], %[a0]\n\t"
                "ds_read_b128 v[200:203], %[ub] offset:880\n\t"
                "s_waitcnt lgkmcnt(10)\n\t"
                "v_mfma_f32_32x32x16_bf16 %[a1], v[236:239], v[232:235], %[a1]\n\t"
                "ds_read_b64 v[204:205], %[kb] offset:3360\n\t"
                "ds_read_b64 v[206:207], %[kb] offset:3368\n\t"
                "s_waitcnt lgkmcnt(9)\n\t"
                "v_mfma_f32_32x32x16_bf16 %[a1], v[240:243], v[244:247], %[a1]\n\t"
                "ds_read_b64 v[208:209], %[kb] offset:1280\n\t"
                "ds_read_b64 v[210:211], %[kb] offset:1288\n\t"
                "ds_read_b128 v[212:215], %[ub] offset:3472\n\t"
                "s_waitcnt lgkmcnt(11)\n\t"
                "v_mfma_f32_32x32x16_bf16 %[a0], v[240:243], v[248:251], %[a0]\n\t"
                "ds_read_b128 v[216:219], %[ub] offset:912\n\t"
                "s_add_u32 %[t0], %[t1], 98304\n\t"
                "buffer_load_dwordx4 a[216:219], %[go], %[so], %[t0] offen\n\t"
                "s_waitcnt lgkmcnt(10)\n\t"
                "v_mfma_f32_32x32x16_bf16 %[a1], v[252:255], v[248:251], %[a1]\n\t"
                "ds_read_b64 v[220:221], %[kb] offset:3328\n\t"
                "ds_read_b64 v[222:223], %[kb] offset:3336\n\t"
                "s_waitcnt lgkmcnt(9)\n\t"
                "v_mfma_f32_32x32x16_bf16 %[a1], v[192:195], v[196:199], %[a1]\n\t"
                "ds_read_b64 v[224:225], %[kb] offset:1376\n\t"
                "ds_read_b64 v[226:227], %[kb] offset:1384\n\t"
                "ds_read_b128 v[228:231], %[ub] offset:3360\n\t"
                "s_waitcnt lgkmcnt(11)\n\t"
                "v_mfma_f32_32x32x16_bf16 %[a0], v[192:195], v[200:203], %[a0]\n\t"
                "ds_read_b128 v[232:235], %[ub] offset:800\n\t"
                "s_waitcnt lgkmcnt(10)\n\t"
                "v_mfma_f32_32x32x16_bf16 %[a1], v[204:207], v[200:203], %[a1]\n\t"
                "ds_read_b64 v[236:237], %[kb] offset:3424\n\t"
                "ds_read_b64 v[238:239], %[kb] offset:3432\n\t"
                "s_waitcnt lgkmcnt(9)\n\t"
                "v_mfma_f32_32x32x16_bf16 %[a1], v[208:211], v[212:215], %[a1]\n\t"
                "ds_read_b64 v[240:241], %[kb] offset:1344\n\t"
                "ds_read_b64 v[242:243], %[kb] offset:1352\n\t"
                "ds_read_b128 v[244:247], %[ub] offset:3392\n\t"
                "s_waitcnt lgkmcnt(11)\n\t"
                "v_mfma_f32_32x32x16_bf16 %[a0], v[208:211], v[216:219], %[a0]\n\t"
                "ds_read_b128 v[248:251], %[ub] offset:832\n\t"
                "s_add_u32 %[t0], %[t1], 114688\n\t"
                "buffer_load_dwordx4 a[220:223], %[go], %[so], %[t0] offen\n\t"
                "s_waitcnt lgkmcnt(10)\n\t"
                "v_mfma_f32_32x32x16_bf16 %[a1], v[220:223], v[216:219], %[a1]\n\t"
                "ds_read_b64 v[252:253], %[kb] offset:3392\n\t"
                "ds_read_b64 v[254:255], %[kb] offset:3400\n\t"
                "s_waitcnt lgkmcnt(9)\n\t"
                "v_mfma_f32_32x32x16_bf16 %[a1], v[224:227], v[228:231], %[a1]\n\t"
                "ds_read_b64 v[192:193], %[kb] offset:1440\n\t"
                "ds_read_b64 v[194:195], %[kb] offset:1448\n\t"
                "ds_read_b128 v[196:199], %[ub] offset:3280\n\t"
                "s_waitcnt lgkmcnt(11)\n\t"
                "v_mfma_f32_32x32x16_bf16 %[a0], v[224:227], v[232:235], %[a0]\n\t"
                "ds_read_b128 v[200:203], %[ub] offset:720\n\t"
                "s_add_u32 %[t1], %[ro], 3\n\t"
                "s_and_b32 %[t1], %[t1], 7\n\t"
                "s_lshl_b32 %[t1], %[t1], 18\n\t"
                "s_add_u32 %[t0], %[t1], 131072\n\t"
                "buffer_load_dwordx4 a[224:227], %[go], %[so], %[t0] offen\n\t"
                "s_waitcnt lgkmcnt(10)\n\t"
                "v_mfma_f32_32x32x16_bf16 %[a1], v[236:239], v[232:235], %[a1]\n\t"
                "ds_read_b64 v[204:205], %[kb] offset:3488\n\t"
                "ds_read_b64 v[206:207], %[kb] offset:3496\n\t"
                "s_waitcnt lgkmcnt(9)\n\t"
                "v_mfma_f32_32x32x16_bf16 %[a1], v[240:243], v[244:247], %[a1]\n\t"
                "ds_read_b64 v[208:209], %[kb] offset:1408\n\t"
                "ds_read_b64 v[210:211], %[kb] offset:1416\n\t"
                "ds_read_b128 v[212:215], %[ub] offset:3312\n\t"
                "s_waitcnt lgkmcnt(11)\n\t"
                "v_mfma_f32_32x32x16_bf16 %[a0], v[240:243], v[248:251], %[a0]\n\t"
                "ds_read_b128 v[216:219], %[ub] offset:752\n\t"
                "s_waitcnt lgkmcnt(10)\n\t"
                "v_mfma_f32_32x32x16_bf16 %[a1], v[252:255], v[248:251], %[a1]\n\t"
                "ds_read_b64 v[220:221], %[kb] offset:3456\n\t"
                "ds_read_b64 v[222:223], %[kb] offset:3464\n\t"
                "s_waitcnt lgkmcnt(9)\n\t"
                "v_mfma_f32_32x32x16_bf16 %[a1], v[192:195], v[196:199], %[a1]\n\t"
                "ds_read_b64 v[224:225], %[kb] offset:1504\n\t"
                "ds_read_b64 v[226:227], %[kb] offset:1512\n\t"
                "ds_read_b128 v[228:231], %[ub] offset:3200\n\t"
                "s_waitcnt lgkmcnt(11)\n\t"
                "v_mfma_f32_32x32x16_bf16 %[a0], v[192:195], v[200:203], %[a0]\n\t"
                "ds_read_b128 v[232:235], %[ub] offset:640\n\t"
                "s_add_u32 %[t0], %[t1], 147456\n\t"
                "buffer_load_dwordx4 a[228:231], %[go], %[so], %[t0] offen\n\t"
                "s_waitcnt lgkmcnt(10)\n\t"
                "v_mfma_f32_32x32x16_bf16 %[a1], v[204:207], v[200:203], %[a1]\n\t"
                "ds_read_b64 v[236:237], %[kb] offset:3552\n\t"
                "ds_read_b64 v[238:239], %[kb] offset:3560\n\t"
                "s_waitcnt lgkmcnt(9)\n\t"
                "v_mfma_f32_32x32x16_bf16 %[a1], v[208:211], v[212:215], %[a1]\n\t"
                "ds_read_b64 v[240:241], %[kb] offset:1472\n\t"
                "ds_read_b64 v[242:243], %[kb] offset:1480\n\t"
                "ds_read_b128 v[244:247], %[ub] offset:3232\n\t"
                "s_waitcnt lgkmcnt(11)\n\t"
                "v_mfma_f32_32x32x16_bf16 %[a0], v[208:211], v[216:219], %[a0]\n\t"
                "ds_read_b128 v[248:251], %[ub] offset:672\n\t"
                "s_waitcnt lgkmcnt(10)\n\t"
                "v_mfma_f32_32x32x16_bf16 %[a1], v[220:223], v[216:219], %[a1]\n\t"
                "ds_read_b64 v[252:253], %[kb] offset:3520\n\t"
                "ds_read_b64 v[254:255], %[kb] offset:3528\n\t"
                "s_waitcnt lgkmcnt(9)\n\t"
                "v_mfma_f32_32x32x16_bf16 %[a1], v[224:227], v[228:231], %[a1]\n\t"
                "ds_read_b64 v[192:193], %[kb] offset:1568\n\t"
                "ds_read_b64 v[194:195], %[kb] offset:1576\n\t"
                "ds_read_b128 v[196:199], %[ub] offset:3120\n\t"
                "s_waitcnt lgkmcnt(11)\n\t"
                "v_mfma_f32_32x32x16_bf16 %[a0], v[224:227], v[232:235], %[a0]\n\t"
                "ds_read_b128 v[200:203], %[ub] offset:560\n\t"
                "s_add_u32 %[t0], %[t1], 163840\n\t"
                "buffer_load_dwordx4 a[232:235], %[go], %[so], %[t0] offen\n\t"
                "s_waitcnt lgkmcnt(10)\n\t"
                "v_mfma_f32_32x32x16_bf16 %[a1], v[236:239], v[232:235], %[a1]\n\t"
                "ds_read_b64 v[204:205], %[kb] offset:3616\n\t"
                "ds_read_b64 v[206:207], %[kb] offset:3624\n\t"
                "s_waitcnt lgkmcnt(9)\n\t"
                "v_mfma_f32_32x32x16_bf16 %[a1], v[240:243], v[244:247], %[a1]\n\t"
                "ds_read_b64 v[208:209], %[kb] offset:1536\n\t"
                "ds_read_b64 v[210:211], %[kb] offset:1544\n\t"
                "ds_read_b128 v[212:215], %[ub] offset:3152\n\t"
                "s_waitcnt lgkmcnt(11)\n\t"
                "v_mfma_f32_32x32x16_bf16 %[a0], v[240:243], v[248:251], %[a0]\n\t"
                "ds_read_b128 v[216:219], %[ub] offset:592\n\t"
                "s_waitcnt lgkmcnt(10)\n\t"
                "v_mfma_f32_32x32x16_bf16 %[a1], v[252:255], v[248:251], %[a1]\n\t"
                "ds_read_b64 v[220:221], %[kb] offset:3584\n\t"
                "ds_read_b64 v[222:223], %[kb] offset:3592\n\t"
                "s_waitcnt lgkmcnt(9)\n\t"
                "v_mfma_f32_32x32x16_bf16 %[a1], v[192:195], v[196:199], %[a1]\n\t"
                "ds_read_b64 v[224:225], %[kb] offset:1632\n\t"
                "ds_read_b64 v[226:227], %[kb] offset:1640\n\t"
                "ds_read_b128 v[228:231], %[ub] offset:3040\n\t"
                "s_waitcnt lgkmcnt(11)\n\t"
                "v_mfma_f32_32x32x16_bf16 %[a0], v[192:195], v[200:203], %[a0]\n\t"
                "ds_read_b128 v[232:235], %[ub] offset:480\n\t"
                "s_add_u32 %[t0], %[t1], 180224\n\t"
                "buffer_load_dwordx4 a[236:239], %[go], %[so], %[t0] offen\n\t"
                "s_waitcnt lgkmcnt(10)\n\t"
                "v_mfma_f32_32x32x16_bf16 %[a1], v[204:207], v[200:203], %[a1]\n\t"
                "ds_read_b64 v[236:237], %[kb] offset:3680\n\t"
                "ds_read_b64 v[238:239], %[kb] offset:3688\n\t"
                "s_waitcnt lgkmcnt(9)\n\t"
                "v_mfma_f32_32x32x16_bf16 %[a1], v[208:211], v[212:215], %[a1]\n\t"
                "ds_read_b64 v[240:241], %[kb] offset:1600\n\t"
                "ds_read_b64 v[242:243], %[kb] offset:1608\n\t"
                "ds_read_b128 v[244:247], %[ub] offset:3072\n\t"
                "s_waitcnt lgkmcnt(11)\n\t"
                "v_mfma_f32_32x32x16_bf16 %[a0], v[208:211], v[216:219], %[a0]\n\t"
                "ds_read_b128 v[248:251], %[ub] offset:512\n\t"
                "s_add_u32 %[t0], %[t1], 196608\n\t"
                "buffer_load_dwordx4 a[240:243], %[go], %[so], %[t0] offen\n\t"
                "s_waitcnt lgkmcnt(10)\n\t"
                "v_mfma_f32_32x32x16_bf16 %[a1], v[220:223], v[216:219], %[a1]\n\t"
                "ds_read_b64 v[252:253], %[kb] offset:3648\n\t"
                "ds_read_b64 v[254:255], %[kb] offset:3656\n\t"
                "s_waitcnt lgkmcnt(9)\n\t"
                "v_mfma_f32_32x32x16_bf16 %[a1], v[224:227], v[228:231], %[a1]\n\t"
                "ds_read_b64 v[192:193], %[kb] offset:1696\n\t"
                "ds_read_b64 v[194:195], %[kb] offset:1704\n\t"
                "ds_read_b128 v[196:199], %[ub] offset:2960\n\t"
                "s_waitcnt lgkmcnt(11)\n\t"
                "v_mfma_f32_32x32x16_bf16 %[a0], v[224:227], v[232:235], %[a0]\n\t"
                "ds_read_b128 v[200:203], %[ub] offset:400\n\t"
                "s_waitcnt lgkmcnt(10)\n\t"
                "v_mfma_f32_32x32x16_bf16 %[a1], v[236:239], v[232:235], %[a1]\n\t"
                "ds_read_b64 v[204:205], %[kb] offset:3744\n\t"
                "ds_read_b64 v[206:207], %[kb] offset:3752\n\t"
                "s_waitcnt lgkmcnt(9)\n\t"
                "v_mfma_f32_32x32x16_bf16 %[a1], v[240:243], v[244:247], %[a1]\n\t"
                "ds_read_b64 v[208:209], %[kb] offset:1664\n\t"
                "ds_read_b64 v[210:211], %[kb] offset:1672\n\t"
                "ds_read_b128 v[212:215], %[ub] offset:2992\n\t"
                "s_waitcnt lgkmcnt(11)\n\t"
                "v_mfma_f32_32x32x16_bf16 %[a0], v[240:243], v[248:251], %[a0]\n\t"
                "ds_read_b128 v[216:219], %[ub] offset:432\n\t"
                "s_add_u32 %[t0], %[t1], 212992\n\t"
                "buffer_load_dwordx4 a[244:247], %[go], %[so], %[t0] offen\n\t"
                "s_waitcnt lgkmcnt(10)\n\t"
                "v_mfma_f32_32x32x16_bf16 %[a1], v[252:255], v[248:251], %[a1]\n\t"
                "ds_read_b64 v[220:221], %[kb] offset:3712\n\t"
                "ds_read_b64 v[222:223], %[kb] offset:3720\n\t"
                "s_waitcnt lgkmcnt(9)\n\t"
                "v_mfma_f32_32x32x16_bf16 %[a1], v[192:195], v[196:199], %[a1]\n\t"
                "ds_read_b64 v[224:225], %[kb] offset:1760\n\t"
                "ds_read_b64 v[226:227], %[kb] offset:1768\n\t"
                "ds_read_b128 v[228:231], %[ub] offset:2880\n\t"
                "s_waitcnt lgkmcnt(11)\n\t"
                "v_mfma_f32_32x32x16_bf16 %[a0], v[192:195], v[200:203], %[a0]\n\t"
                "ds_read_b128 v[232:235], %[ub] offset:320\n\t"
                "s_waitcnt lgkmcnt(10)\n\t"
                "v_mfma_f32_32x32x16_bf16 %[a1], v[204:207], v[200:203], %[a1]\n\t"
                "ds_read_b64 v[236:237], %[kb] offset:3808\n\t"
                "ds_read_b64 v[238:239], %[kb] offset:3816\n\t"
                "s_waitcnt lgkmcnt(9)\n\t"
                "v_mfma_f32_32x32x16_bf16 %[a1], v[208:211], v[212:215], %[a1]\n\t"
                "ds_read_b64 v[240:241], %[kb] offset:1728\n\t"
                "ds_read_b64 v[242:243], %[kb] offset:1736\n\t"
                "ds_read_b128 v[244:247], %[ub] offset:2912\n\t"
                "s_waitcnt lgkmcnt(11)\n\t"
                "v_mfma_f32_32x32x16_bf16 %[a0], v[208:211], v[216:219], %[a0]\n\t"
                "ds_read_b128 v[248:251], %[ub] offset:352\n\t"
                "s_add_u32 %[t0], %[t1], 229376\n\t"
                "buffer_load_dwordx4 a[248:251], %[go], %[so], %[t0] offen\n\t"
                "s_waitcnt lgkmcnt(10)\n\t"
                "v_mfma_f32_32x32x16_bf16 %[a1], v[220:223], v[216:219], %[a1]\n\t"
                "ds_read_b64 v[252:253], %[kb] offset:3776\n\t"
                "ds_read_b64 v[254:255], %[kb] offset:3784\n\t"
                "s_waitcnt lgkmcnt(9)\n\t"
                "v_mfma_f32_32x32x16_bf16 %[a1], v[224:227], v[228:231], %[a1]\n\t"
                "ds_read_b64 v[192:193], %[kb] offset:1824\n\t"
                "ds_read_b64 v[194:195], %[kb] offset:1832\n\t"
                "ds_read_b128 v[196:199], %[ub] offset:2800\n\t"
                "s_waitcnt lgkmcnt(11)\n\t"
                "v_mfma_f32_32x32x16_bf16 %[a0], v[224:227], v[232:235], %[a0]\n\t"
                "ds_read_b128 v[200:203], %[ub] offset:240\n\t"
                "s_waitcnt lgkmcnt(10)\n\t"
                "v_mfma_f32_32x32x16_bf16 %[a1], v[236:239], v[232:235], %[a1]\n\t"
                "ds_read_b64 v[204:205], %[kb] offset:3872\n\t"
                "ds_read_b64 v[206:207], %[kb] offset:3880\n\t"
                "s_waitcnt lgkmcnt(9)\n\t"
                "v_mfma_f32_32x32x16_bf16 %[a1], v[240:243], v[244:247], %[a1]\n\t"
                "ds_read_b64 v[208:209], %[kb] offset:1792\n\t"
                "ds_read_b64 v[210:211], %[kb] offset:1800\n\t"
                "ds_read_b128 v[212:215], %[ub] offset:2832\n\t"
                "s_waitcnt lgkmcnt(11)\n\t"
                "v_mfma_f32_32x32x16_bf16 %[a0], v[240:243], v[248:251], %[a0]\n\t"
                "ds_read_b128 v[216:219], %[ub] offset:272\n\t"
                "s_add_u32 %[t0], %[t1], 245760\n\t"
                "buffer_load_dwordx4 a[252:255], %[go], %[so], %[t0] offen\n\t"
                "s_waitcnt lgkmcnt(10)\n\t"
                "v_mfma_f32_32x32x16_bf16 %[a1], v[252:255], v[248:251], %[a1]\n\t"
                "ds_read_b64 v[220:221], %[kb] offset:3840\n\t"
                "ds_read_b64 v[222:223], %[kb] offset:3848\n\t"
                "s_waitcnt lgkmcnt(9)\n\t"
                "v_mfma_f32_32x32x16_bf16 %[a1], v[192:195], v[196:199], %[a1]\n\t"
                "ds_read_b64 v[224:225], %[kb] offset:1888\n\t"
                "ds_read_b64 v[226:227], %[kb] offset:1896\n\t"
                "ds_read_b128 v[228:231], %[ub] offset:2720\n\t"
                "s_waitcnt lgkmcnt(11)\n\t"
                "v_mfma_f32_32x32x16_bf16 %[a0], v[192:195], v[200:203], %[a0]\n\t"
                "ds_read_b128 v[232:235], %[ub] offset:160\n\t"
                "s_waitcnt lgkmcnt(10)\n\t"
                "v_mfma_f32_32x32x16_bf16 %[a1], v[204:207], v[200:203], %[a1]\n\t"
                "ds_read_b64 v[236:237], %[kb] offset:3936\n\t"
                "ds_read_b64 v[238:239], %[kb] offset:3944\n\t"
                "s_waitcnt lgkmcnt(9)\n\t"
                "v_mfma_f32_32x32x16_bf16 %[a1], v[208:211], v[212:215], %[a1]\n\t"
                "ds_read_b64 v[240:241], %[kb] offset:1856\n\t"
                "ds_read_b64 v[242:243], %[kb] offset:1864\n\t"
                "ds_read_b128 v[244:247], %[ub] offset:2752\n\t"
                "s_waitcnt lgkmcnt(11)\n\t"
                "v_mfma_f32_32x32x16_bf16 %[a0], v[208:211], v[216:219], %[a0]\n\t"
                "ds_read_b128 v[248:251], %[ub] offset:192\n\t"
                "s_waitcnt lgkmcnt(10)\n\t"
                "v_mfma_f32_32x32x16_bf16 %[a1], v[220:223], v[216:219], %[a1]\n\t"
                "ds_read_b64 v[252:253], %[kb] offset:3904\n\t"
                "ds_read_b64 v[254:255], %[kb] offset:3912\n\t"
                "s_waitcnt lgkmcnt(9)\n\t"
                "v_mfma_f32_32x32x16_bf16 %[a1], v[224:227], v[228:231], %[a1]\n\t"
                "ds_read_b64 v[192:193], %[kb] offset:1952\n\t"
                "ds_read_b64 v[194:195], %[kb] offset:1960\n\t"
                "ds_read_b128 v[196:199], %[ub] offset:2640\n\t"
                "s_waitcnt lgkmcnt(11)\n\t"
                "v_mfma_f32_32x32x16_bf16 %[a0], v[224:227], v[232:235], %[a0]\n\t"
                "ds_read_b128 v[200:203], %[ub] offset:80\n\t"
                "s_waitcnt lgkmcnt(10)\n\t"
                "v_mfma_f32_32x32x16_bf16 %[a1], v[236:239], v[232:235], %[a1]\n\t"
                "ds_read_b64 v[204:205], %[kb] offset:4000\n\t"
                "ds_read_b64 v[206:207], %[kb] offset:4008\n\t"
                "s_waitcnt lgkmcnt(9)\n\t"
                "v_mfma_f32_32x32x16_bf16 %[a1], v[240:243], v[244:247], %[a1]\n\t"
                "ds_read_b64 v[208:209], %[kb] offset:1920\n\t"
                "ds_read_b64 v[210:211], %[kb] offset:1928\n\t"
                "ds_read_b128 v[212:215], %[ub] offset:2672\n\t"
                "s_waitcnt lgkmcnt(11)\n\t"
                "v_mfma_f32_32x32x16_bf16 %[a0], v[240:243], v[248:251], %[a0]\n\t"
                "ds_read_b128 v[216:219], %[ub] offset:112\n\t"
                "s_waitcnt lgkmcnt(10)\n\t"
                "v_mfma_f32_32x32x16_bf16 %[a1], v[252:255], v[248:251], %[a1]\n\t"
                "ds_read_b64 v[220:221], %[kb] offset:3968\n\t"
                "ds_read_b64 v[222:223], %[kb] offset:3976\n\t"
                "s_waitcnt lgkmcnt(9)\n\t"
                "v_mfma_f32_32x32x16_bf16 %[a1], v[192:195], v[196:199], %[a1]\n\t"
                "ds_read_b64 v[224:225], %[kb] offset:2016\n\t"
                "ds_read_b64 v[226:227], %[kb] offset:2024\n\t"
                "ds_read_b128 v[228:231], %[ub] offset:2560\n\t"
                "s_waitcnt lgkmcnt(11)\n\t"
                "v_mfma_f32_32x32x16_bf16 %[a0], v[192:195], v[200:203], %[a0]\n\t"
                "ds_read_b128 v[232:235], %[ub] offset:0\n\t"
                "s_waitcnt lgkmcnt(10)\n\t"
                "v_mfma_f32_32x32x16_bf16 %[a1], v[204:207], v[200:203], %[a1]\n\t"
                "ds_read_b64 v[236:237], %[kb] offset:4064\n\t"
                "ds_read_b64 v[238:239], %[kb] offset:4072\n\t"
                "s_waitcnt lgkmcnt(9)\n\t"
                "v_mfma_f32_32x32x16_bf16 %[a1], v[208:211], v[212:215], %[a1]\n\t"
                "ds_read_b64 v[240:241], %[kb] offset:1984\n\t"
                "ds_read_b64 v[242:243], %[kb] offset:1992\n\t"
                "ds_read_b128 v[244:247], %[ub] offset:2592\n\t"
                "s_waitcnt lgkmcnt(11)\n\t"
                "v_mfma_f32_32x32x16_bf16 %[a0], v[208:211], v[216:219], %[a0]\n\t"
                "ds_read_b128 v[248:251], %[ub] offset:32\n\t"
                "s_waitcnt lgkmcnt(10)\n\t"
                "v_mfma_f32_32x32x16_bf16 %[a1], v[220:223], v[216:219], %[a1]\n\t"
                "ds_read_b64 v[252:253], %[kb] offset:4032\n\t"
                "ds_read_b64 v[254:255], %[kb] offset:4040\n\t"
                "s_waitcnt lgkmcnt(9)\n\t"
                "v_mfma_f32_32x32x16_bf16 %[a1], v[224:227], v[228:231], %[a1]\n\t"
                "s_waitcnt lgkmcnt(8)\n\t"
                "v_mfma_f32_32x32x16_bf16 %[a0], v[224:227], v[232:235], %[a0]\n\t"
                "s_waitcnt lgkmcnt(6)\n\t"
                "v_mfma_f32_32x32x16_bf16 %[a1], v[236:239], v[232:235], %[a1]\n\t"
                "s_waitcnt lgkmcnt(3)\n\t"
                "v_mfma_f32_32x32x16_bf16 %[a1], v[240:243], v[244:247], %[a1]\n\t"
                "s_waitcnt lgkmcnt(2)\n\t"
                "v_mfma_f32_32x32x16_bf16 %[a0], v[240:243], v[248:251], %[a0]\n\t"
                "s_waitcnt lgkmcnt(0)\n\t"
                "v_mfma_f32_32x32x16_bf16 %[a1], v[252:255], v[248:251], %[a1]\n\t"
                "s_waitcnt vmcnt(0)\n\t"
                "s_nop 7\n\t"
                "s_nop 3\n\t"
                : [a0] "=&v"(acc0), [a1] "=&v"(acc1), [t0] "=&s"(t0), [t1] "=&s"(t1)
                : [kb] "v"(kb), [ub] "v"(ub0 + c8 * CV_U), [go] "v"(go), [so] "s"(so), [ro] "s"(ro) : "memory", "scc", "v192", "v193", "v194", "v195", "v196", "v197", "v198", "v199", "v200", "v201", "v202", "v203", "v204", "v205", "v206", "v207", "v208", "v209", "v210", "v211", "v212", "v213", "v214", "v215", "v216", "v217", "v218", "v219", "v220", "v221", "v222", "v223", "v224", "v225", "v226", "v227", "v228", "v229", "v230", "v231", "v232", "v233", "v234", "v235", "v236", "v237", "v238", "v239", "v240", "v241", "v242", "v243", "v244", "v245", "v246", "v247", "v248", "v249", "v250", "v251", "v252", "v253", "v254", "v255", "a0", "a1", "a2", "a3", "a4", "a5", "a6", "a7", "a8", "a9", "a10", "a11", "a12", "a13", "a14", "a15", "a16", "a17", "a18", "a19", "a20", "a21", "a22", "a23", "a24", "a25", "a26", "a27", "a28", "a29", "a30", "a31", "a32", "a33", "a34", "a35", "a36", "a37", "a38", "a39", "a40", "a41", "a42", "a43", "a44", "a45", "a46", "a47", "a48", "a49", "a50", "a51", "a52", "a53", "a54", "a55", "a56", "a57", "a58", "a59", "a60", "a61", "a62", "a63", "a64", "a65", "a66", "a67", "a68", "a69", "a70", "a71", "a72", "a73", "a74", "a75", "a76", "a77", "a78", "a79", "a80", "a81", "a82", "a83", "a84", "a85", "a86", "a87", "a88", "a89", "a90", "a91", "a92", "a93", "a94", "a95", "a96", "a97", "a98", "a99", "a100", "a101", "a102", "a103", "a104", "a105", "a106", "a107", "a108", "a109", "a110", "a111", "a112", "a113", "a114", "a115", "a116", "a117", "a118", "a119", "a120", "a121", "a122", "a123", "a124", "a125", "a126", "a127", "a128", "a129", "a130", "a131", "a132", "a133", "a134", "a135", "a136", "a137", "a138", "a139", "a140", "a141", "a142", "a143", "a144", "a145", "a146", "a147", "a148", "a149", "a150", "a151", "a152", "a153", "a154", "a155", "a156", "a157", "a158", "a159", "a160", "a161", "a162", "a163", "a164", "a165", "a166", "a167", "a168", "a169", "a170", "a171", "a172", "a173", "a174", "a175", "a176", "a177", "a178", "a179", "a180", "a181", "a182", "a183", "a184", "a185", "a186", "a187", "a188", "a189", "a190", "a191", "a192", "a193", "a194", "a195", "a196", "a197", "a198", "a199", "a200", "a201", "a202", "a203", "a204", "a205", "a206", "a207", "a208", "a209", "a210", "a211", "a212", "a213", "a214", "a215", "a216", "a217", "a218", "a219", "a220", "a221", "a222", "a223", "a224", "a225", "a226", "a227", "a228", "a229", "a230", "a231", "a232", "a233", "a234", "a235", "a236", "a237", "a238", "a239", "a240", "a241", "a242", "a243", "a244", "a245", "a246", "a247", "a248", "a249", "a250", "a251", "a252", "a253", "a254", "a255");
            unsigned char* uc = lds + c8 * CV_U + (32 + b) * CV_RP + kg * 8;
#pragma unroll
            for (int q = 0; q < 4; ++q) {
                *(v2u*)(uc + q * 16) = (v2u){pack2(acc0[4 * q], acc0[4 * q + 1]), pack2(acc0[4 * q + 2], acc0[4 * q + 3])};
                *(v2u*)(uc + 32 * CV_RP + q * 16) = (v2u){pack2(acc1[4 * q], acc1[4 * q + 1]), pack2(acc1[4 * q + 2], acc1[4 * q + 3])}; }
            __builtin_amdgcn_wave_barrier();
        }
    __syncthreads();
#pragma unroll
    for (int G = 0; G < 4; ++G) { const v4u lo = *(const v4u*)(urow + G * 16), hh = *(const v4u*)(urow + CV_U + G * 16); v4u o[2];
#pragma unroll
        for (int i = 0; i < 8; ++i) { const unsigned u0 = ur[G][(7 - i) >> 1], u1 = ur[4 + G][(7 - i) >> 1], qq = qr[2 * G + (i >> 2)][i & 3];
            const unsigned ud = pack2(((i & 1) ? blo(u0) : bhi(u0)) * blo(dv), ((i & 1) ? blo(u1) : bhi(u1)) * bhi(dv));
            const unsigned y = pack2(((i & 1) ? bhi(lo[i >> 1]) : blo(lo[i >> 1])) + blo(ud), ((i & 1) ? bhi(hh[i >> 1]) : blo(hh[i >> 1])) + bhi(ud));
            o[i >> 2][i & 3] = pack2(blo(qq) * blo(y), bhi(qq) * bhi(y)); }
        st16(r, W_A + un * 32768 + 2 * G * 4096 + tid * 16, o[0]); st16(r, W_A + un * 32768 + (2 * G + 1) * 4096 + tid * 16, o[1]); }
    DRAIN(); __syncthreads();
    if (tid == 0) __hip_atomic_fetch_add((unsigned*)(p.ws + F_CVY + (un >> 3) * 128), 1u, __ATOMIC_RELAXED, __HIP_MEMORY_SCOPE_AGENT);
    return true;
}

__device__ __forceinline__ void proj_block(const Params& p, unsigned char* lds, const int wv, const int h, const int c) {
    const int w = wv, l = otid() & 63, n = l & 15, g = l >> 4, tm = w >> 1, tn = w & 1;
    const unsigned lb = __builtin_amdgcn_readfirstlane((unsigned)(size_t)lds);
    const v4u sx = mk_srd(p.Aq, S * D * 2), sq = mk_srd(c < 2 ? p.Wq : p.Wk, 1024 * 4096 * 2), sv = mk_srd(p.Wv, 1024 * 4096 * 2), sw = mk_srd(p.ws, W_TOTAL);
    const unsigned lx = lb + (32 * tm + n) * 128 + ((g ^ (n & 7)) << 4), lr = lb + 131072, rr = 8 * g + (n >> 2);
    const int cv = ((c & 1) ^ 1) * 2 + (c >> 1);
    const unsigned aq = (c < 2 ? W_QX : W_KX) + (tm * 32 + h) * 8192 + (2 * (c & 1) + tn) * 1024 + l * 16;
    unsigned t0, xo, so, cn;
    asm volatile(
        "s_nop 4\n\t"
        "s_mov_b32 %[xo], 0\n\t"
        "s_mov_b32 %[so], 0\n\t"
        "s_mov_b32 %[cn], 1\n\t"
        "s_add_u32 m0, %[ld], 0\n\t"
        "s_add_u32 %[t0], %[xo], 0\n\t"
        "buffer_load_dwordx4 v100, %[sx], %[t0] offen lds\n\t"
        "s_add_u32 m0, %[ld], 1024\n\t"
        "s_add_u32 %[t0], %[xo], 16384\n\t"
        "buffer_load_dwordx4 v100, %[sx], %[t0] offen lds\n\t"
        "s_add_u32 m0, %[ld], 8192\n\t"
        "s_add_u32 %[t0], %[xo], 128\n\t"
        "buffer_load_dwordx4 v100, %[sx], %[t0] offen lds\n\t"
        "s_add_u32 m0, %[ld], 9216\n\t"
        "s_add_u32 %[t0], %[xo], 16512\n\t"
        "buffer_load_dwordx4 v100, %[sx], %[t0] offen lds\n\t"
        "s_add_u32 m0, %[ld], 16384\n\t"
        "s_add_u32 %[t0], %[xo], 256\n\t"
        "buffer_load_dwordx4 v100, %[sx], %[t0] offen lds\n\t"
        "s_add_u32 m0, %[ld], 17408\n\t"
        "s_add_u32 %[t0], %[xo], 16640\n\t"
        "buffer_load_dwordx4 v100, %[sx], %[t0] offen lds\n\t"
        "s_add_u32 m0, %[ld], 24576\n\t"
        "s_add_u32 %[t0], %[xo], 384\n\t"
        "buffer_load_dwordx4 v100, %[sx], %[t0] offen lds\n\t"
        "s_add_u32 m0, %[ld], 25600\n\t"
        "s_add_u32 %[t0], %[xo], 16768\n\t"
        "buffer_load_dwordx4 v100, %[sx], %[t0] offen lds\n\t"
        "s_mov_b32 %[t0], 0\n\t"
        "buffer_load_dwordx4 a[192:195], v101, %[sq], %[t0] offen\n\t"
        "buffer_load_dwordx4 a[200:203], v112, %[sv], %[t0] offen\n\t"
        "s_mov_b32 %[t0], 262144\n\t"
        "buffer_load_dwordx4 a[196:199], v101, %[sq], %[t0] offen\n\t"
        "s_mov_b32 %[t0], 524288\n\t"
        "buffer_load_dwordx4 a[204:207], v101, %[sq], %[t0] offen\n\t"
        "buffer_load_dwordx4 a[212:215], v112, %[sv], %[t0] offen\n\t"
        "s_mov_b32 %[t0], 786432\n\t"
        "buffer_load_dwordx4 a[208:211], v101, %[sq], %[t0] offen\n\t"
        "s_mov_b32 %[t0], 1048576\n\t"
        "buffer_load_dwordx4 a[216:219], v101, %[sq], %[t0] offen\n\t"
        "buffer_load_dwordx4 a[224:227], v112, %[sv], %[t0] offen\n\t"
        "s_mov_b32 %[t0], 1310720\n\t"
        "buffer_load_dwordx4 a[220:223], v101, %[sq], %[t0] offen\n\t"
        "s_mov_b32 %[t0], 1572864\n\t"
        "buffer_load_dwordx4 a[228:231], v101, %[sq], %[t0] offen\n\t"
        "buffer_load_dwordx4 a[236:239], v112, %[sv], %[t0] offen\n\t"
        "s_mov_b32 %[t0], 1835008\n\t"
        "buffer_load_dwordx4 a[232:235], v101, %[sq], %[t0] offen\n\t"
        "s_waitcnt vmcnt(9)\n\t"
        "ds_write_b128 v102, a[192:195] offset:0\n\t"
        "ds_write_b128 v102, a[196:199] offset:4096\n\t"
        "ds_write_b128 v116, a[200:203] offset:8192\n\t"
        "s_waitcnt lgkmcnt(0)\n\t"
        "s_barrier\n\t"
        "ds_read_b64_tr_b16 a[0:1], v103 offset:0\n\t"
        "ds_read_b64_tr_b16 a[2:3], v103 offset:512\n\t"
        "ds_read_b64_tr_b16 a[4:5], v103 offset:8\n\t"
        "ds_read_b64_tr_b16 a[6:7], v103 offset:520\n\t"
        "ds_read_b64_tr_b16 a[8:9], v104 offset:0\n\t"
        "ds_read_b64_tr_b16 a[10:11], v104 offset:256\n\t"
        "s_waitcnt lgkmcnt(5)\n\t"
        "ds_read_b64_tr_b16 a[12:13], v103 offset:4096\n\t"
        "ds_read_b64_tr_b16 a[14:15], v103 offset:4608\n\t"
        "ds_read_b64_tr_b16 a[16:17], v103 offset:4104\n\t"
        "ds_read_b64_tr_b16 a[18:19], v103 offset:4616\n\t"
        "ds_read_b64_tr_b16 a[20:21], v104 offset:2048\n\t"
        "ds_read_b64_tr_b16 a[22:23], v104 offset:2304\n\t"
        "ds_read_b128 v[56:59], v105 offset:0\n\t"
        "ds_read_b128 v[60:63], v105 offset:2048\n\t"
        "ds_read_b128 v[64:67], v106 offset:0\n\t"
        "ds_read_b128 v[68:71], v106 offset:2048\n\t"
        "s_add_u32 m0, %[ld], 32768\n\t"
        "s_add_u32 %[t0], %[xo], 512\n\t"
        "buffer_load_dwordx4 v100, %[sx], %[t0] offen lds\n\t"
        "s_add_u32 m0, %[ld], 33792\n\t"
        "s_add_u32 %[t0], %[xo], 16896\n\t"
        "buffer_load_dwordx4 v100, %[sx], %[t0] offen lds\n\t"
        "s_add_u32 m0, %[ld], 40960\n\t"
        "s_add_u32 %[t0], %[xo], 640\n\t"
        "buffer_load_dwordx4 v100, %[sx], %[t0] offen lds\n\t"
        "s_add_u32 m0, %[ld], 41984\n\t"
        "s_add_u32 %[t0], %[xo], 17024\n\t"
        "buffer_load_dwordx4 v100, %[sx], %[t0] offen lds\n\t"
        "s_add_u32 m0, %[ld], 49152\n\t"
        "s_add_u32 %[t0], %[xo], 768\n\t"
        "buffer_load_dwordx4 v100, %[sx], %[t0] offen lds\n\t"
        "s_add_u32 m0, %[ld], 50176\n\t"
        "s_add_u32 %[t0], %[xo], 17152\n\t"
        "buffer_load_dwordx4 v100, %[sx], %[t0] offen lds\n\t"
        "s_add_u32 m0, %[ld], 57344\n\t"
        "s_add_u32 %[t0], %[xo], 896\n\t"
        "buffer_load_dwordx4 v100, %[sx], %[t0] offen lds\n\t"
        "s_add_u32 m0, %[ld], 58368\n\t"
        "s_add_u32 %[t0], %[xo], 17280\n\t"
        "buffer_load_dwordx4 v100, %[sx], %[t0] offen lds\n\t"
        "s_mov_b32 %[t0], 2097152\n\t"
        "buffer_load_dwordx4 a[240:243], v101, %[sq], %[t0] offen\n\t"
        "buffer_load_dwordx4 a[248:251], v112, %[sv], %[t0] offen\n\t"
        "s_mov_b32 %[t0], 2359296\n\t"
        "buffer_load_dwordx4 a[244:247], v101, %[sq], %[t0] offen\n\t"
        "s_waitcnt vmcnt(17)\n\t"
        "ds_write_b128 v102, a[204:207] offset:16384\n\t"
        "ds_write_b128 v102, a[208:211] offset:20480\n\t"
        "ds_write_b128 v116, a[212:215] offset:24576\n\t"
        "s_waitcnt lgkmcnt(0)\n\t"
        "s_barrier\n\t"
        "v_mfma_f32_16x16x32_bf16 v[8:11], a[0:3], v[56:59], 0\n\t"
        "ds_read_b64_tr_b16 a[24:25], v103 offset:16384\n\t"
        "ds_read_b64_tr_b16 a[26:27], v103 offset:16896\n\t"
        "v_mfma_f32_16x16x32_bf16 v[12:15], a[4:7], v[56:59], 0\n\t"
        "ds_read_b64_tr_b16 a[28:29], v103 offset:16392\n\t"
        "ds_read_b64_tr_b16 a[30:31], v103 offset:16904\n\t"
        "v_mfma_f32_16x16x32_bf16 v[16:19], v[56:59], a[8:11], 0\n\t"
        "ds_read_b64_tr_b16 a[32:33], v104 offset:16384\n\t"
        "ds_read_b64_tr_b16 a[34:35], v104 offset:16640\n\t"
        "v_mfma_f32_16x16x32_bf16 v[20:23], a[0:3], v[60:63], 0\n\t"
        "s_waitcnt lgkmcnt(5)\n\t"
        "ds_read_b64_tr_b16 a[36:37], v103 offset:20480\n\t"
        "v_mfma_f32_16x16x32_bf16 v[24:27], a[4:7], v[60:63], 0\n\t"
        "ds_read_b64_tr_b16 a[38:39], v103 offset:20992\n\t"
        "ds_read_b64_tr_b16 a[40:41], v103 offset:20488\n\t"
        "v_mfma_f32_16x16x32_bf16 v[28:31], v[60:63], a[8:11], 0\n\t"
        "ds_read_b64_tr_b16 a[42:43], v103 offset:21000\n\t"
        "ds_read_b64_tr_b16 a[44:45], v104 offset:18432\n\t"
        "v_mfma_f32_16x16x32_bf16 v[8:11], a[12:15], v[64:67], v[8:11]\n\t"
        "ds_read_b64_tr_b16 a[46:47], v104 offset:18688\n\t"
        "ds_read_b128 v[72:75], v105 offset:8192\n\t"
        "v_mfma_f32_16x16x32_bf16 v[12:15], a[16:19], v[64:67], v[12:15]\n\t"
        "ds_read_b128 v[76:79], v105 offset:10240\n\t"
        "ds_read_b128 v[80:83], v106 offset:8192\n\t"
        "v_mfma_f32_16x16x32_bf16 v[16:19], v[64:67], a[20:23], v[16:19]\n\t"
        "ds_read_b128 v[84:87], v106 offset:10240\n\t"
        "v_mfma_f32_16x16x32_bf16 v[20:23], a[12:15], v[68:71], v[20:23]\n\t"
        "v_mfma_f32_16x16x32_bf16 v[24:27], a[16:19], v[68:71], v[24:27]\n\t"
        "v_mfma_f32_16x16x32_bf16 v[28:31], v[68:71], a[20:23], v[28:31]\n\t"
        "s_mov_b32 %[t0], 2621440\n\t"
        "buffer_load_dwordx4 a[252:255], v101, %[sq], %[t0] offen\n\t"
        "buffer_load_dwordx4 v[132:135], v112, %[sv], %[t0] offen\n\t"
        "s_mov_b32 %[t0], 2883584\n\t"
        "buffer_load_dwordx4 v[128:131], v101, %[sq], %[t0] offen\n\t"
        "s_waitcnt vmcnt(17)\n\t"
        "ds_write_b128 v102, a[216:219] offset:0\n\t"
        "ds_write_b128 v102, a[220:223] offset:4096\n\t"
        "ds_write_b128 v116, a[224:227] offset:8192\n\t"
        "s_waitcnt lgkmcnt(0)\n\t"
        "s_barrier\n\t"
        "v_mfma_f32_16x16x32_bf16 v[8:11], a[24:27], v[72:75], v[8:11]\n\t"
        "ds_read_b64_tr_b16 a[48:49], v103 offset:0\n\t"
        "ds_read_b64_tr_b16 a[50:51], v103 offset:512\n\t"
        "v_mfma_f32_16x16x32_bf16 v[12:15], a[28:31], v[72:75], v[12:15]\n\t"
        "ds_read_b64_tr_b16 a[52:53], v103 offset:8\n\t"
        "ds_read_b64_tr_b16 a[54:55], v103 offset:520\n\t"
        "v_mfma_f32_16x16x32_bf16 v[16:19], v[72:75], a[32:35], v[16:19]\n\t"
        "ds_read_b64_tr_b16 a[56:57], v104 offset:0\n\t"
        "ds_read_b64_tr_b16 a[58:59], v104 offset:256\n\t"
        "v_mfma_f32_16x16x32_bf16 v[20:23], a[24:27], v[76:79], v[20:23]\n\t"
        "s_waitcnt lgkmcnt(5)\n\t"
        "ds_read_b64_tr_b16 a[60:61], v103 offset:4096\n\t"
        "v_mfma_f32_16x16x32_bf16 v[24:27], a[28:31], v[76:79], v[24:27]\n\t"
        "ds_read_b64_tr_b16 a[62:63], v103 offset:4608\n\t"
        "ds_read_b64_tr_b16 a[64:65], v103 offset:4104\n\t"
        "v_mfma_f32_16x16x32_bf16 v[28:31], v[76:79], a[32:35], v[28:31]\n\t"
        "ds_read_b64_tr_b16 a[66:67], v103 offset:4616\n\t"
        "ds_read_b64_tr_b16 a[68:69], v104 offset:2048\n\t"
        "v_mfma_f32_16x16x32_bf16 v[8:11], a[36:39], v[80:83], v[8:11]\n\t"
        "ds_read_b64_tr_b16 a[70:71], v104 offset:2304\n\t"
        "ds_read_b128 v[56:59], v105 offset:16384\n\t"
        "v_mfma_f32_16x16x32_bf16 v[12:15], a[40:43], v[80:83], v[12:15]\n\t"
        "ds_read_b128 v[60:63], v105 offset:18432\n\t"
        "ds_read_b128 v[64:67], v106 offset:16384\n\t"
        "v_mfma_f32_16x16x32_bf16 v[16:19], v[80:83], a[44:47], v[16:19]\n\t"
        "ds_read_b128 v[68:71], v106 offset:18432\n\t"
        "v_mfma_f32_16x16x32_bf16 v[20:23], a[36:39], v[84:87], v[20:23]\n\t"
        "v_mfma_f32_16x16x32_bf16 v[24:27], a[40:43], v[84:87], v[24:27]\n\t"
        "v_mfma_f32_16x16x32_bf16 v[28:31], v[84:87], a[44:47], v[28:31]\n\t"
        "s_mov_b32 %[t0], 3145728\n\t"
        "buffer_load_dwordx4 v[136:139], v101, %[sq], %[t0] offen\n\t"
        "buffer_load_dwordx4 v[144:147], v112, %[sv], %[t0] offen\n\t"
        "s_mov_b32 %[t0], 3407872\n\t"
        "buffer_load_dwordx4 v[140:143], v101, %[sq], %[t0] offen\n\t"
        "s_waitcnt vmcnt(17)\n\t"
        "ds_write_b128 v102, a[228:231] offset:16384\n\t"
        "ds_write_b128 v102, a[232:235] offset:20480\n\t"
        "ds_write_b128 v116, a[236:239] offset:24576\n\t"
        "s_waitcnt lgkmcnt(0)\n\t"
        "s_barrier\n\t"
        "v_mfma_f32_16x16x32_bf16 v[8:11], a[48:51], v[56:59], v[8:11]\n\t"
        "ds_read_b64_tr_b16 a[72:73], v103 offset:16384\n\t"
        "ds_read_b64_tr_b16 a[74:75], v103 offset:16896\n\t"
        "v_mfma_f32_16x16x32_bf16 v[12:15], a[52:55], v[56:59], v[12:15]\n\t"
        "ds_read_b64_tr_b16 a[76:77], v103 offset:16392\n\t"
        "ds_read_b64_tr_b16 a[78:79], v103 offset:16904\n\t"
        "v_mfma_f32_16x16x32_bf16 v[16:19], v[56:59], a[56:59], v[16:19]\n\t"
        "ds_read_b64_tr_b16 a[80:81], v104 offset:16384\n\t"
        "ds_read_b64_tr_b16 a[82:83], v104 offset:16640\n\t"
        "v_mfma_f32_16x16x32_bf16 v[20:23], a[48:51], v[60:63], v[20:23]\n\t"
        "s_waitcnt lgkmcnt(5)\n\t"
        "ds_read_b64_tr_b16 a[84:85], v103 offset:20480\n\t"
        "v_mfma_f32_16x16x32_bf16 v[24:27], a[52:55], v[60:63], v[24:27]\n\t"
        "ds_read_b64_tr_b16 a[86:87], v103 offset:20992\n\t"
        "ds_read_b64_tr_b16 a[88:89], v103 offset:20488\n\t"
        "v_mfma_f32_16x16x32_bf16 v[28:31], v[60:63], a[56:59], v[28:31]\n\t"
        "ds_read_b64_tr_b16 a[90:91], v103 offset:21000\n\t"
        "ds_read_b64_tr_b16 a[92:93], v104 offset:18432\n\t"
        "v_mfma_f32_16x16x32_bf16 v[8:11], a[60:63], v[64:67], v[8:11]\n\t"
        "ds_read_b64_tr_b16 a[94:95], v104 offset:18688\n\t"
        "ds_read_b128 v[72:75], v105 offset:24576\n\t"
        "v_mfma_f32_16x16x32_bf16 v[12:15], a[64:67], v[64:67], v[12:15]\n\t"
        "ds_read_b128 v[76:79], v105 offset:26624\n\t"
        "ds_read_b128 v[80:83], v106 offset:24576\n\t"
        "v_mfma_f32_16x16x32_bf16 v[16:19], v[64:67], a[68:71], v[16:19]\n\t"
        "ds_read_b128 v[84:87], v106 offset:26624\n\t"
        "v_mfma_f32_16x16x32_bf16 v[20:23], a[60:63], v[68:71], v[20:23]\n\t"
        "v_mfma_f32_16x16x32_bf16 v[24:27], a[64:67], v[68:71], v[24:27]\n\t"
        "v_mfma_f32_16x16x32_bf16 v[28:31], v[68:71], a[68:71], v[28:31]\n\t"
        "s_mov_b32 %[t0], 3670016\n\t"
        "buffer_load_dwordx4 v[148:151], v101, %[sq], %[t0] offen\n\t"
        "buffer_load_dwordx4 v[156:159], v112, %[sv], %[t0] offen\n\t"
        "s_mov_b32 %[t0], 3932160\n\t"
        "buffer_load_dwordx4 v[152:155], v101, %[sq], %[t0] offen\n\t"
        "s_waitcnt vmcnt(9)\n\t"
        "ds_write_b128 v102, a[240:243] offset:0\n\t"
        "ds_write_b128 v102, a[244:247] offset:4096\n\t"
        "ds_write_b128 v116, a[248:251] offset:8192\n\t"
        "s_waitcnt lgkmcnt(0)\n\t"
        "s_barrier\n\t"
        "v_mfma_f32_16x16x32_bf16 v[8:11], a[72:75], v[72:75], v[8:11]\n\t"
        "ds_read_b64_tr_b16 a[96:97], v103 offset:0\n\t"
        "ds_read_b64_tr_b16 a[98:99], v103 offset:512\n\t"
        "v_mfma_f32_16x16x32_bf16 v[12:15], a[76:79], v[72:75], v[12:15]\n\t"
        "ds_read_b64_tr_b16 a[100:101], v103 offset:8\n\t"
        "ds_read_b64_tr_b16 a[102:103], v103 offset:520\n\t"
        "v_mfma_f32_16x16x32_bf16 v[16:19], v[72:75], a[80:83], v[16:19]\n\t"
        "ds_read_b64_tr_b16 a[104:105], v104 offset:0\n\t"
        "ds_read_b64_tr_b16 a[106:107], v104 offset:256\n\t"
        "v_mfma_f32_16x16x32_bf16 v[20:23], a[72:75], v[76:79], v[20:23]\n\t"
        "s_waitcnt lgkmcnt(5)\n\t"
        "ds_read_b64_tr_b16 a[108:109], v103 offset:4096\n\t"
        "v_mfma_f32_16x16x32_bf16 v[24:27], a[76:79], v[76:79], v[24:27]\n\t"
        "ds_read_b64_tr_b16 a[110:111], v103 offset:4608\n\t"
        "ds_read_b64_tr_b16 a[112:113], v103 offset:4104\n\t"
        "v_mfma_f32_16x16x32_bf16 v[28:31], v[76:79], a[80:83], v[28:31]\n\t"
        "ds_read_b64_tr_b16 a[114:115], v103 offset:4616\n\t"
        "ds_read_b64_tr_b16 a[116:117], v104 offset:2048\n\t"
        "v_mfma_f32_16x16x32_bf16 v[8:11], a[84:87], v[80:83], v[8:11]\n\t"
        "ds_read_b64_tr_b16 a[118:119], v104 offset:2304\n\t"
        "ds_read_b128 v[56:59], v105 offset:32768\n\t"
        "v_mfma_f32_16x16x32_bf16 v[12:15], a[88:91], v[80:83], v[12:15]\n\t"
        "ds_read_b128 v[60:63], v105 offset:34816\n\t"
        "ds_read_b128 v[64:67], v106 offset:32768\n\t"
        "v_mfma_f32_16x16x32_bf16 v[16:19], v[80:83], a[92:95], v[16:19]\n\t"
        "ds_read_b128 v[68:71], v106 offset:34816\n\t"
        "v_mfma_f32_16x16x32_bf16 v[20:23], a[84:87], v[84:87], v[20:23]\n\t"
        "v_mfma_f32_16x16x32_bf16 v[24:27], a[88:91], v[84:87], v[24:27]\n\t"
        "v_mfma_f32_16x16x32_bf16 v[28:31], v[84:87], a[92:95], v[28:31]\n\t"
        "s_add_u32 m0, %[ld], 65536\n\t"
        "s_add_u32 %[t0], %[xo], 1024\n\t"
        "buffer_load_dwordx4 v100, %[sx], %[t0] offen lds\n\t"
        "s_add_u32 m0, %[ld], 66560\n\t"
        "s_add_u32 %[t0], %[xo], 17408\n\t"
        "buffer_load_dwordx4 v100, %[sx], %[t0] offen lds\n\t"
        "s_add_u32 m0, %[ld], 73728\n\t"
        "s_add_u32 %[t0], %[xo], 1152\n\t"
        "buffer_load_dwordx4 v100, %[sx], %[t0] offen lds\n\t"
        "s_add_u32 m0, %[ld], 74752\n\t"
        "s_add_u32 %[t0], %[xo], 17536\n\t"
        "buffer_load_dwordx4 v100, %[sx], %[t0] offen lds\n\t"
        "s_add_u32 m0, %[ld], 81920\n\t"
        "s_add_u32 %[t0], %[xo], 1280\n\t"
        "buffer_load_dwordx4 v100, %[sx], %[t0] offen lds\n\t"
        "s_add_u32 m0, %[ld], 82944\n\t"
        "s_add_u32 %[t0], %[xo], 17664\n\t"
        "buffer_load_dwordx4 v100, %[sx], %[t0] offen lds\n\t"
        "s_add_u32 m0, %[ld], 90112\n\t"
        "s_add_u32 %[t0], %[xo], 1408\n\t"
        "buffer_load_dwordx4 v100, %[sx], %[t0] offen lds\n\t"
        "s_add_u32 m0, %[ld], 91136\n\t"
        "s_add_u32 %[t0], %[xo], 17792\n\t"
        "buffer_load_dwordx4 v100, %[sx], %[t0] offen lds\n\t"
        "s_mov_b32 %[t0], 4194304\n\t"
        "buffer_load_dwordx4 v[160:163], v101, %[sq], %[t0] offen\n\t"
        "buffer_load_dwordx4 v[168:171], v112, %[sv], %[t0] offen\n\t"
        "s_mov_b32 %[t0], 4456448\n\t"
        "buffer_load_dwordx4 v[164:167], v101, %[sq], %[t0] offen\n\t"
        "s_waitcnt vmcnt(17)\n\t"
        "ds_write_b128 v102, a[252:255] offset:16384\n\t"
        "ds_write_b128 v102, v[128:131] offset:20480\n\t"
        "ds_write_b128 v116, v[132:135] offset:24576\n\t"
        "s_waitcnt lgkmcnt(0)\n\t"
        "s_barrier\n\t"
        "v_mfma_f32_16x16x32_bf16 v[8:11], a[96:99], v[56:59], v[8:11]\n\t"
        "ds_read_b64_tr_b16 a[120:121], v103 offset:16384\n\t"
        "ds_read_b64_tr_b16 a[122:123], v103 offset:16896\n\t"
        "v_mfma_f32_16x16x32_bf16 v[12:15], a[100:103], v[56:59], v[12:15]\n\t"
        "ds_read_b64_tr_b16 a[124:125], v103 offset:16392\n\t"
        "ds_read_b64_tr_b16 a[126:127], v103 offset:16904\n\t"
        "v_mfma_f32_16x16x32_bf16 v[16:19], v[56:59], a[104:107], v[16:19]\n\t"
        "ds_read_b64_tr_b16 a[128:129], v104 offset:16384\n\t"
        "ds_read_b64_tr_b16 a[130:131], v104 offset:16640\n\t"
        "v_mfma_f32_16x16x32_bf16 v[20:23], a[96:99], v[60:63], v[20:23]\n\t"
        "s_waitcnt lgkmcnt(5)\n\t"
        "ds_read_b64_tr_b16 a[132:133], v103 offset:20480\n\t"
        "v_mfma_f32_16x16x32_bf16 v[24:27], a[100:103], v[60:63], v[24:27]\n\t"
        "ds_read_b64_tr_b16 a[134:135], v103 offset:20992\n\t"
        "ds_read_b64_tr_b16 a[136:137], v103 offset:20488\n\t"
        "v_mfma_f32_16x16x32_bf16 v[28:31], v[60:63], a[104:107], v[28:31]\n\t"
        "ds_read_b64_tr_b16 a[138:139], v103 offset:21000\n\t"
        "ds_read_b64_tr_b16 a[140:141], v104 offset:18432\n\t"
        "v_mfma_f32_16x16x32_bf16 v[8:11], a[108:111], v[64:67], v[8:11]\n\t"
        "ds_read_b64_tr_b16 a[142:143], v104 offset:18688\n\t"
        "ds_read_b128 v[72:75], v105 offset:40960\n\t"
        "v_mfma_f32_16x16x32_bf16 v[12:15], a[112:115], v[64:67], v[12:15]\n\t"
        "ds_read_b128 v[76:79], v105 offset:43008\n\t"
        "ds_read_b128 v[80:83], v106 offset:40960\n\t"
        "v_mfma_f32_16x16x32_bf16 v[16:19], v[64:67], a[116:119], v[16:19]\n\t"
        "ds_read_b128 v[84:87], v106 offset:43008\n\t"
        "v_mfma_f32_16x16x32_bf16 v[20:23], a[108:111], v[68:71], v[20:23]\n\t"
        "v_mfma_f32_16x16x32_bf16 v[24:27], a[112:115], v[68:71], v[24:27]\n\t"
        "v_mfma_f32_16x16x32_bf16 v[28:31], v[68:71], a[116:119], v[28:31]\n\t"
        "s_mov_b32 %[t0], 4718592\n\t"
        "buffer_load_dwordx4 v[172:175], v101, %[sq], %[t0] offen\n\t"
        "buffer_load_dwordx4 v[180:183], v112, %[sv], %[t0] offen\n\t"
        "s_mov_b32 %[t0], 4980736\n\t"
        "buffer_load_dwordx4 v[176:179], v101, %[sq], %[t0] offen\n\t"
        "s_waitcnt vmcnt(17)\n\t"
        "ds_write_b128 v102, v[136:139] offset:0\n\t"
        "ds_write_b128 v102, v[140:143] offset:4096\n\t"
        "ds_write_b128 v116, v[144:147] offset:8192\n\t"
        "s_waitcnt lgkmcnt(0)\n\t"
        "s_barrier\n\t"
        "v_mfma_f32_16x16x32_bf16 v[8:11], a[120:123], v[72:75], v[8:11]\n\t"
        "ds_read_b64_tr_b16 a[144:145], v103 offset:0\n\t"
        "ds_read_b64_tr_b16 a[146:147], v103 offset:512\n\t"
        "v_mfma_f32_16x16x32_bf16 v[12:15], a[124:127], v[72:75], v[12:15]\n\t"
        "ds_read_b64_tr_b16 a[148:149], v103 offset:8\n\t"
        "ds_read_b64_tr_b16 a[150:151], v103 offset:520\n\t"
        "v_mfma_f32_16x16x32_bf16 v[16:19], v[72:75], a[128:131], v[16:19]\n\t"
        "ds_read_b64_tr_b16 a[152:153], v104 offset:0\n\t"
        "ds_read_b64_tr_b16 a[154:155], v104 offset:256\n\t"
        "v_mfma_f32_16x16x32_bf16 v[20:23], a[120:123], v[76:79], v[20:23]\n\t"
        "s_waitcnt lgkmcnt(5)\n\t"
        "ds_read_b64_tr_b16 a[156:157], v103 offset:4096\n\t"
        "v_mfma_f32_16x16x32_bf16 v[24:27], a[124:127], v[76:79], v[24:27]\n\t"
        "ds_read_b64_tr_b16 a[158:159], v103 offset:4608\n\t"
        "ds_read_b64_tr_b16 a[160:161], v103 offset:4104\n\t"
        "v_mfma_f32_16x16x32_bf16 v[28:31], v[76:79], a[128:131], v[28:31]\n\t"
        "ds_read_b64_tr_b16 a[162:163], v103 offset:4616\n\t"
        "ds_read_b64_tr_b16 a[164:165], v104 offset:2048\n\t"
        "v_mfma_f32_16x16x32_bf16 v[8:11], a[132:135], v[80:83], v[8:11]\n\t"
        "ds_read_b64_tr_b16 a[166:167], v104 offset:2304\n\t"
        "ds_read_b128 v[56:59], v105 offset:49152\n\t"
        "v_mfma_f32_16x16x32_bf16 v[12:15], a[136:139], v[80:83], v[12:15]\n\t"
        "ds_read_b128 v[60:63], v105 offset:51200\n\t"
        "ds_read_b128 v[64:67], v106 offset:49152\n\t"
        "v_mfma_f32_16x16x32_bf16 v[16:19], v[80:83], a[140:143], v[16:19]\n\t"
        "ds_read_b128 v[68:71], v106 offset:51200\n\t"
        "v_mfma_f32_16x16x32_bf16 v[20:23], a[132:135], v[84:87], v[20:23]\n\t"
        "v_mfma_f32_16x16x32_bf16 v[24:27], a[136:139], v[84:87], v[24:27]\n\t"
        "v_mfma_f32_16x16x32_bf16 v[28:31], v[84:87], a[140:143], v[28:31]\n\t"
        "s_mov_b32 %[t0], 5242880\n\t"
        "buffer_load_dwordx4 v[184:187], v101, %[sq], %[t0] offen\n\t"
        "buffer_load_dwordx4 v[192:195], v112, %[sv], %[t0] offen\n\t"
        "s_mov_b32 %[t0], 5505024\n\t"
        "buffer_load_dwordx4 v[188:191], v101, %[sq], %[t0] offen\n\t"
        "s_waitcnt vmcnt(17)\n\t"
        "ds_write_b128 v102, v[148:151] offset:16384\n\t"
        "ds_write_b128 v102, v[152:155] offset:20480\n\t"
        "ds_write_b128 v116, v[156:159] offset:24576\n\t"
        "s_waitcnt lgkmcnt(0)\n\t"
        "s_barrier\n\t"
        "v_mfma_f32_16x16x32_bf16 v[8:11], a[144:147], v[56:59], v[8:11]\n\t"
        "ds_read_b64_tr_b16 a[168:169], v103 offset:16384\n\t"
        "ds_read_b64_tr_b16 a[170:171], v103 offset:16896\n\t"
        "v_mfma_f32_16x16x32_bf16 v[12:15], a[148:151], v[56:59], v[12:15]\n\t"
        "ds_read_b64_tr_b16 a[172:173], v103 offset:16392\n\t"
        "ds_read_b64_tr_b16 a[174:175], v103 offset:16904\n\t"
        "v_mfma_f32_16x16x32_bf16 v[16:19], v[56:59], a[152:155], v[16:19]\n\t"
        "ds_read_b64_tr_b16 a[176:177], v104 offset:16384\n\t"
        "ds_read_b64_tr_b16 a[178:179], v104 offset:16640\n\t"
        "v_mfma_f32_16x16x32_bf16 v[20:23], a[144:147], v[60:63], v[20:23]\n\t"
        "s_waitcnt lgkmcnt(5)\n\t"
        "ds_read_b64_tr_b16 a[180:181], v103 offset:20480\n\t"
        "v_mfma_f32_16x16x32_bf16 v[24:27], a[148:151], v[60:63], v[24:27]\n\t"
        "ds_read_b64_tr_b16 a[182:183], v103 offset:20992\n\t"
        "ds_read_b64_tr_b16 a[184:185], v103 offset:20488\n\t"
        "v_mfma_f32_16x16x32_bf16 v[28:31], v[60:63], a[152:155], v[28:31]\n\t"
        "ds_read_b64_tr_b16 a[186:187], v103 offset:21000\n\t"
        "ds_read_b64_tr_b16 a[188:189], v104 offset:18432\n\t"
        "v_mfma_f32_16x16x32_bf16 v[8:11], a[156:159], v[64:67], v[8:11]\n\t"
        "ds_read_b64_tr_b16 a[190:191], v104 offset:18688\n\t"
        "ds_read_b128 v[72:75], v105 offset:57344\n\t"
        "v_mfma_f32_16x16x32_bf16 v[12:15], a[160:163], v[64:67], v[12:15]\n\t"
        "ds_read_b128 v[76:79], v105 offset:59392\n\t"
        "ds_read_b128 v[80:83], v106 offset:57344\n\t"
        "v_mfma_f32_16x16x32_bf16 v[16:19], v[64:67], a[164:167], v[16:19]\n\t"
        "ds_read_b128 v[84:87], v106 offset:59392\n\t"
        "v_mfma_f32_16x16x32_bf16 v[20:23], a[156:159], v[68:71], v[20:23]\n\t"
        "v_mfma_f32_16x16x32_bf16 v[24:27], a[160:163], v[68:71], v[24:27]\n\t"
        "v_mfma_f32_16x16x32_bf16 v[28:31], v[68:71], a[164:167], v[28:31]\n\t"
        "s_mov_b32 %[t0], 5767168\n\t"
        "buffer_load_dwordx4 v[196:199], v101, %[sq], %[t0] offen\n\t"
        "buffer_load_dwordx4 v[204:207], v112, %[sv], %[t0] offen\n\t"
        "s_mov_b32 %[t0], 6029312\n\t"
        "buffer_load_dwordx4 v[200:203], v101, %[sq], %[t0] offen\n\t"
        "s_waitcnt vmcnt(9)\n\t"
        "ds_write_b128 v102, v[160:163] offset:0\n\t"
        "ds_write_b128 v102, v[164:167] offset:4096\n\t"
        "ds_write_b128 v116, v[168:171] offset:8192\n\t"
        "s_waitcnt lgkmcnt(0)\n\t"
        "s_barrier\n\t"
        "v_mfma_f32_16x16x32_bf16 v[8:11], a[168:171], v[72:75], v[8:11]\n\t"
        "ds_read_b64_tr_b16 a[192:193], v103 offset:0\n\t"
        "ds_read_b64_tr_b16 a[194:195], v103 offset:512\n\t"
        "v_mfma_f32_16x16x32_bf16 v[12:15], a[172:175], v[72:75], v[12:15]\n\t"
        "ds_read_b64_tr_b16 a[196:197], v103 offset:8\n\t"
        "ds_read_b64_tr_b16 a[198:199], v103 offset:520\n\t"
        "v_mfma_f32_16x16x32_bf16 v[16:19], v[72:75], a[176:179], v[16:19]\n\t"
        "ds_read_b64_tr_b16 a[200:201], v104 offset:0\n\t"
        "ds_read_b64_tr_b16 a[202:203], v104 offset:256\n\t"
        "v_mfma_f32_16x16x32_bf16 v[20:23], a[168:171], v[76:79], v[20:23]\n\t"
        "s_waitcnt lgkmcnt(5)\n\t"
        "ds_read_b64_tr_b16 a[204:205], v103 offset:4096\n\t"
        "v_mfma_f32_16x16x32_bf16 v[24:27], a[172:175], v[76:79], v[24:27]\n\t"
        "ds_read_b64_tr_b16 a[206:207], v103 offset:4608\n\t"
        "ds_read_b64_tr_b16 a[208:209], v103 offset:4104\n\t"
        "v_mfma_f32_16x16x32_bf16 v[28:31], v[76:79], a[176:179], v[28:31]\n\t"
        "ds_read_b64_tr_b16 a[210:211], v103 offset:4616\n\t"
        "ds_read_b64_tr_b16 a[212:213], v104 offset:2048\n\t"
        "v_mfma_f32_16x16x32_bf16 v[8:11], a[180:183], v[80:83], v[8:11]\n\t"
        "ds_read_b64_tr_b16 a[214:215], v104 offset:2304\n\t"
        "ds_read_b128 v[56:59], v107 offset:0\n\t"
        "v_mfma_f32_16x16x32_bf16 v[12:15], a[184:187], v[80:83], v[12:15]\n\t"
        "ds_read_b128 v[60:63], v107 offset:2048\n\t"
        "ds_read_b128 v[64:67], v108 offset:0\n\t"
        "v_mfma_f32_16x16x32_bf16 v[16:19], v[80:83], a[188:191], v[16:19]\n\t"
        "ds_read_b128 v[68:71], v108 offset:2048\n\t"
        "v_mfma_f32_16x16x32_bf16 v[20:23], a[180:183], v[84:87], v[20:23]\n\t"
        "v_mfma_f32_16x16x32_bf16 v[24:27], a[184:187], v[84:87], v[24:27]\n\t"
        "v_mfma_f32_16x16x32_bf16 v[28:31], v[84:87], a[188:191], v[28:31]\n\t"
        "s_add_u32 m0, %[ld], 98304\n\t"
        "s_add_u32 %[t0], %[xo], 1536\n\t"
        "buffer_load_dwordx4 v100, %[sx], %[t0] offen lds\n\t"
        "s_add_u32 m0, %[ld], 99328\n\t"
        "s_add_u32 %[t0], %[xo], 17920\n\t"
        "buffer_load_dwordx4 v100, %[sx], %[t0] offen lds\n\t"
        "s_add_u32 m0, %[ld], 106496\n\t"
        "s_add_u32 %[t0], %[xo], 1664\n\t"
        "buffer_load_dwordx4 v100, %[sx], %[t0] offen lds\n\t"
        "s_add_u32 m0, %[ld], 107520\n\t"
        "s_add_u32 %[t0], %[xo], 18048\n\t"
        "buffer_load_dwordx4 v100, %[sx], %[t0] offen lds\n\t"
        "s_add_u32 m0, %[ld], 114688\n\t"
        "s_add_u32 %[t0], %[xo], 1792\n\t"
        "buffer_load_dwordx4 v100, %[sx], %[t0] offen lds\n\t"
        "s_add_u32 m0, %[ld], 115712\n\t"
        "s_add_u32 %[t0], %[xo], 18176\n\t"
        "buffer_load_dwordx4 v100, %[sx], %[t0] offen lds\n\t"
        "s_add_u32 m0, %[ld], 122880\n\t"
        "s_add_u32 %[t0], %[xo], 1920\n\t"
        "buffer_load_dwordx4 v100, %[sx], %[t0] offen lds\n\t"
        "s_add_u32 m0, %[ld], 123904\n\t"
        "s_add_u32 %[t0], %[xo], 18304\n\t"
        "buffer_load_dwordx4 v100, %[sx], %[t0] offen lds\n\t"
        "s_mov_b32 %[t0], 6291456\n\t"
        "buffer_load_dwordx4 v[208:211], v101, %[sq], %[t0] offen\n\t"
        "buffer_load_dwordx4 v[216:219], v112, %[sv], %[t0] offen\n\t"
        "s_mov_b32 %[t0], 6553600\n\t"
        "buffer_load_dwordx4 v[212:215], v101, %[sq], %[t0] offen\n\t"
        "s_waitcnt vmcnt(17)\n\t"
        "ds_write_b128 v102, v[172:175] offset:16384\n\t"
        "ds_write_b128 v102, v[176:179] offset:20480\n\t"
        "ds_write_b128 v116, v[180:183] offset:24576\n\t"
        "s_waitcnt lgkmcnt(0)\n\t"
        "s_barrier\n\t"
        "v_mfma_f32_16x16x32_bf16 v[8:11], a[192:195], v[56:59], v[8:11]\n\t"
        "ds_read_b64_tr_b16 a[216:217], v103 offset:16384\n\t"
        "ds_read_b64_tr_b16 a[218:219], v103 offset:16896\n\t"
        "v_mfma_f32_16x16x32_bf16 v[12:15], a[196:199], v[56:59], v[12:15]\n\t"
        "ds_read_b64_tr_b16 a[220:221], v103 offset:16392\n\t"
        "ds_read_b64_tr_b16 a[222:223], v103 offset:16904\n\t"
        "v_mfma_f32_16x16x32_bf16 v[16:19], v[56:59], a[200:203], v[16:19]\n\t"
        "ds_read_b64_tr_b16 a[224:225], v104 offset:16384\n\t"
        "ds_read_b64_tr_b16 a[226:227], v104 offset:16640\n\t"
        "v_mfma_f32_16x16x32_bf16 v[20:23], a[192:195], v[60:63], v[20:23]\n\t"
        "s_waitcnt lgkmcnt(5)\n\t"
        "ds_read_b64_tr_b16 a[228:229], v103 offset:20480\n\t"
        "v_mfma_f32_16x16x32_bf16 v[24:27], a[196:199], v[60:63], v[24:27]\n\t"
        "ds_read_b64_tr_b16 a[230:231], v103 offset:20992\n\t"
        "ds_read_b64_tr_b16 a[232:233], v103 offset:20488\n\t"
        "v_mfma_f32_16x16x32_bf16 v[28:31], v[60:63], a[200:203], v[28:31]\n\t"
        "ds_read_b64_tr_b16 a[234:235], v103 offset:21000\n\t"
        "ds_read_b64_tr_b16 a[236:237], v104 offset:18432\n\t"
        "v_mfma_f32_16x16x32_bf16 v[8:11], a[204:207], v[64:67], v[8:11]\n\t"
        "ds_read_b64_tr_b16 a[238:239], v104 offset:18688\n\t"
        "ds_read_b128 v[72:75], v107 offset:8192\n\t"
        "v_mfma_f32_16x16x32_bf16 v[12:15], a[208:211], v[64:67], v[12:15]\n\t"
        "ds_read_b128 v[76:79], v107 offset:10240\n\t"
        "ds_read_b128 v[80:83], v108 offset:8192\n\t"
        "v_mfma_f32_16x16x32_bf16 v[16:19], v[64:67], a[212:215], v[16:19]\n\t"
        "ds_read_b128 v[84:87], v108 offset:10240\n\t"
        "v_mfma_f32_16x16x32_bf16 v[20:23], a[204:207], v[68:71], v[20:23]\n\t"
        "v_mfma_f32_16x16x32_bf16 v[24:27], a[208:211], v[68:71], v[24:27]\n\t"
        "v_mfma_f32_16x16x32_bf16 v[28:31], v[68:71], a[212:215], v[28:31]\n\t"
        "s_mov_b32 %[t0], 6815744\n\t"
        "buffer_load_dwordx4 v[220:223], v101, %[sq], %[t0] offen\n\t"
        "buffer_load_dwordx4 v[228:231], v112, %[sv], %[t0] offen\n\t"
        "s_mov_b32 %[t0], 7077888\n\t"
        "buffer_load_dwordx4 v[224:227], v101, %[sq], %[t0] offen\n\t"
        "s_waitcnt vmcnt(17)\n\t"
        "ds_write_b128 v102, v[184:187] offset:0\n\t"
        "ds_write_b128 v102, v[188:191] offset:4096\n\t"
        "ds_write_b128 v116, v[192:195] offset:8192\n\t"
        "s_waitcnt lgkmcnt(0)\n\t"
        "s_barrier\n\t"
        "v_mfma_f32_16x16x32_bf16 v[8:11], a[216:219], v[72:75], v[8:11]\n\t"
        "ds_read_b64_tr_b16 a[240:241], v103 offset:0\n\t"
        "ds_read_b64_tr_b16 a[242:243], v103 offset:512\n\t"
        "v_mfma_f32_16x16x32_bf16 v[12:15], a[220:223], v[72:75], v[12:15]\n\t"
        "ds_read_b64_tr_b16 a[244:245], v103 offset:8\n\t"
        "ds_read_b64_tr_b16 a[246:247], v103 offset:520\n\t"
        "v_mfma_f32_16x16x32_bf16 v[16:19], v[72:75], a[224:227], v[16:19]\n\t"
        "ds_read_b64_tr_b16 a[248:249], v104 offset:0\n\t"
        "ds_read_b64_tr_b16 a[250:251], v104 offset:256\n\t"
        "v_mfma_f32_16x16x32_bf16 v[20:23], a[216:219], v[76:79], v[20:23]\n\t"
        "s_waitcnt lgkmcnt(5)\n\t"
        "ds_read_b64_tr_b16 a[252:253], v103 offset:4096\n\t"
        "v_mfma_f32_16x16x32_bf16 v[24:27], a[220:223], v[76:79], v[24:27]\n\t"
        "ds_read_b64_tr_b16 a[254:255], v103 offset:4608\n\t"
        "ds_read_b64_tr_b16 v[128:129], v103 offset:4104\n\t"
        "v_mfma_f32_16x16x32_bf16 v[28:31], v[76:79], a[224:227], v[28:31]\n\t"
        "ds_read_b64_tr_b16 v[130:131], v103 offset:4616\n\t"
        "ds_read_b64_tr_b16 v[132:133], v104 offset:2048\n\t"
        "v_mfma_f32_16x16x32_bf16 v[8:11], a[228:231], v[80:83], v[8:11]\n\t"
        "ds_read_b64_tr_b16 v[134:135], v104 offset:2304\n\t"
        "ds_read_b128 v[56:59], v107 offset:16384\n\t"
        "v_mfma_f32_16x16x32_bf16 v[12:15], a[232:235], v[80:83], v[12:15]\n\t"
        "ds_read_b128 v[60:63], v107 offset:18432\n\t"
        "ds_read_b128 v[64:67], v108 offset:16384\n\t"
        "v_mfma_f32_16x16x32_bf16 v[16:19], v[80:83], a[236:239], v[16:19]\n\t"
        "ds_read_b128 v[68:71], v108 offset:18432\n\t"
        "v_mfma_f32_16x16x32_bf16 v[20:23], a[228:231], v[84:87], v[20:23]\n\t"
        "v_mfma_f32_16x16x32_bf16 v[24:27], a[232:235], v[84:87], v[24:27]\n\t"
        "v_mfma_f32_16x16x32_bf16 v[28:31], v[84:87], a[236:239], v[28:31]\n\t"
        "s_mov_b32 %[t0], 7340032\n\t"
        "buffer_load_dwordx4 v[232:235], v101, %[sq], %[t0] offen\n\t"
        "buffer_load_dwordx4 v[240:243], v112, %[sv], %[t0] offen\n\t"
        "s_mov_b32 %[t0], 7602176\n\t"
        "buffer_load_dwordx4 v[236:239], v101, %[sq], %[t0] offen\n\t"
        "s_waitcnt vmcnt(17)\n\t"
        "ds_write_b128 v102, v[196:199] offset:16384\n\t"
        "ds_write_b128 v102, v[200:203] offset:20480\n\t"
        "ds_write_b128 v116, v[204:207] offset:24576\n\t"
        "s_waitcnt lgkmcnt(0)\n\t"
        "s_barrier\n\t"
        "v_mfma_f32_16x16x32_bf16 v[8:11], a[240:243], v[56:59], v[8:11]\n\t"
        "ds_read_b64_tr_b16 v[136:137], v103 offset:16384\n\t"
        "ds_read_b64_tr_b16 v[138:139], v103 offset:16896\n\t"
        "v_mfma_f32_16x16x32_bf16 v[12:15], a[244:247], v[56:59], v[12:15]\n\t"
        "ds_read_b64_tr_b16 v[140:141], v103 offset:16392\n\t"
        "ds_read_b64_tr_b16 v[142:143], v103 offset:16904\n\t"
        "v_mfma_f32_16x16x32_bf16 v[16:19], v[56:59], a[248:251], v[16:19]\n\t"
        "ds_read_b64_tr_b16 v[144:145], v104 offset:16384\n\t"
        "ds_read_b64_tr_b16 v[146:147], v104 offset:16640\n\t"
        "v_mfma_f32_16x16x32_bf16 v[20:23], a[240:243], v[60:63], v[20:23]\n\t"
        "s_waitcnt lgkmcnt(5)\n\t"
        "ds_read_b64_tr_b16 v[148:149], v103 offset:20480\n\t"
        "v_mfma_f32_16x16x32_bf16 v[24:27], a[244:247], v[60:63], v[24:27]\n\t"
        "ds_read_b64_tr_b16 v[150:151], v103 offset:20992\n\t"
        "ds_read_b64_tr_b16 v[152:153], v103 offset:20488\n\t"
        "v_mfma_f32_16x16x32_bf16 v[28:31], v[60:63], a[248:251], v[28:31]\n\t"
        "ds_read_b64_tr_b16 v[154:155], v103 offset:21000\n\t"
        "ds_read_b64_tr_b16 v[156:157], v104 offset:18432\n\t"
        "v_mfma_f32_16x16x32_bf16 v[8:11], a[252:255], v[64:67], v[8:11]\n\t"
        "ds_read_b64_tr_b16 v[158:159], v104 offset:18688\n\t"
        "ds_read_b128 v[72:75], v107 offset:24576\n\t"
        "v_mfma_f32_16x16x32_bf16 v[12:15], v[128:131], v[64:67], v[12:15]\n\t"
        "ds_read_b128 v[76:79], v107 offset:26624\n\t"
        "ds_read_b128 v[80:83], v108 offset:24576\n\t"
        "v_mfma_f32_16x16x32_bf16 v[16:19], v[64:67], v[132:135], v[16:19]\n\t"
        "ds_read_b128 v[84:87], v108 offset:26624\n\t"
        "v_mfma_f32_16x16x32_bf16 v[20:23], a[252:255], v[68:71], v[20:23]\n\t"
        "v_mfma_f32_16x16x32_bf16 v[24:27], v[128:131], v[68:71], v[24:27]\n\t"
        "v_mfma_f32_16x16x32_bf16 v[28:31], v[68:71], v[132:135], v[28:31]\n\t"
        "s_mov_b32 %[t0], 7864320\n\t"
        "buffer_load_dwordx4 v[244:247], v101, %[sq], %[t0] offen\n\t"
        "buffer_load_dwordx4 v[252:255], v112, %[sv], %[t0] offen\n\t"
        "s_mov_b32 %[t0], 8126464\n\t"
        "buffer_load_dwordx4 v[248:251], v101, %[sq], %[t0] offen\n\t"
        "s_waitcnt vmcnt(9)\n\t"
        "ds_write_b128 v102, v[208:211] offset:0\n\t"
        "ds_write_b128 v102, v[212:215] offset:4096\n\t"
        "ds_write_b128 v116, v[216:219] offset:8192\n\t"
        "s_waitcnt lgkmcnt(0)\n\t"
        "s_barrier\n\t"
        "v_mfma_f32_16x16x32_bf16 v[8:11], v[136:139], v[72:75], v[8:11]\n\t"
        "ds_read_b64_tr_b16 v[160:161], v103 offset:0\n\t"
        "ds_read_b64_tr_b16 v[162:163], v103 offset:512\n\t"
        "v_mfma_f32_16x16x32_bf16 v[12:15], v[140:143], v[72:75], v[12:15]\n\t"
        "ds_read_b64_tr_b16 v[164:165], v103 offset:8\n\t"
        "ds_read_b64_tr_b16 v[166:167], v103 offset:520\n\t"
        "v_mfma_f32_16x16x32_bf16 v[16:19], v[72:75], v[144:147], v[16:19]\n\t"
        "ds_read_b64_tr_b16 v[168:169], v104 offset:0\n\t"
        "ds_read_b64_tr_b16 v[170:171], v104 offset:256\n\t"
        "v_mfma_f32_16x16x32_bf16 v[20:23], v[136:139], v[76:79], v[20:23]\n\t"
        "s_waitcnt lgkmcnt(5)\n\t"
        "ds_read_b64_tr_b16 v[172:173], v103 offset:4096\n\t"
        "v_mfma_f32_16x16x32_bf16 v[24:27], v[140:143], v[76:79], v[24:27]\n\t"
        "ds_read_b64_tr_b16 v[174:175], v103 offset:4608\n\t"
        "ds_read_b64_tr_b16 v[176:177], v103 offset:4104\n\t"
        "v_mfma_f32_16x16x32_bf16 v[28:31], v[76:79], v[144:147], v[28:31]\n\t"
        "ds_read_b64_tr_b16 v[178:179], v103 offset:4616\n\t"
        "ds_read_b64_tr_b16 v[180:181], v104 offset:2048\n\t"
        "v_mfma_f32_16x16x32_bf16 v[8:11], v[148:151], v[80:83], v[8:11]\n\t"
        "ds_read_b64_tr_b16 v[182:183], v104 offset:2304\n\t"
        "ds_read_b128 v[56:59], v107 offset:32768\n\t"
        "v_mfma_f32_16x16x32_bf16 v[12:15], v[152:155], v[80:83], v[12:15]\n\t"
        "ds_read_b128 v[60:63], v107 offset:34816\n\t"
        "ds_read_b128 v[64:67], v108 offset:32768\n\t"
        "v_mfma_f32_16x16x32_bf16 v[16:19], v[80:83], v[156:159], v[16:19]\n\t"
        "ds_read_b128 v[68:71], v108 offset:34816\n\t"
        "v_mfma_f32_16x16x32_bf16 v[20:23], v[148:151], v[84:87], v[20:23]\n\t"
        "v_mfma_f32_16x16x32_bf16 v[24:27], v[152:155], v[84:87], v[24:27]\n\t"
        "v_mfma_f32_16x16x32_bf16 v[28:31], v[84:87], v[156:159], v[28:31]\n\t"
        "s_mov_b32 %[xo], 0x20000\n\t"
        "s_add_u32 m0, %[ld], 0\n\t"
        "s_add_u32 %[t0], %[xo], 0\n\t"
        "buffer_load_dwordx4 v100, %[sx], %[t0] offen lds\n\t"
        "s_add_u32 m0, %[ld], 1024\n\t"
        "s_add_u32 %[t0], %[xo], 16384\n\t"
        "buffer_load_dwordx4 v100, %[sx], %[t0] offen lds\n\t"
        "s_add_u32 m0, %[ld], 8192\n\t"
        "s_add_u32 %[t0], %[xo], 128\n\t"
        "buffer_load_dwordx4 v100, %[sx], %[t0] offen lds\n\t"
        "s_add_u32 m0, %[ld], 9216\n\t"
        "s_add_u32 %[t0], %[xo], 16512\n\t"
        "buffer_load_dwordx4 v100, %[sx], %[t0] offen lds\n\t"
        "s_add_u32 m0, %[ld], 16384\n\t"
        "s_add_u32 %[t0], %[xo], 256\n\t"
        "buffer_load_dwordx4 v100, %[sx], %[t0] offen lds\n\t"
        "s_add_u32 m0, %[ld], 17408\n\t"
        "s_add_u32 %[t0], %[xo], 16640\n\t"
        "buffer_load_dwordx4 v100, %[sx], %[t0] offen lds\n\t"
        "s_add_u32 m0, %[ld], 24576\n\t"
        "s_add_u32 %[t0], %[xo], 384\n\t"
        "buffer_load_dwordx4 v100, %[sx], %[t0] offen lds\n\t"
        "s_add_u32 m0, %[ld], 25600\n\t"
        "s_add_u32 %[t0], %[xo], 16768\n\t"
        "buffer_load_dwordx4 v100, %[sx], %[t0] offen lds\n\t"
        "s_mov_b32 %[xo], 0\n\t"
        "s_waitcnt vmcnt(14)\n\t"
        "ds_write_b128 v102, v[220:223] offset:16384\n\t"
        "ds_write_b128 v102, v[224:227] offset:20480\n\t"
        "ds_write_b128 v116, v[228:231] offset:24576\n\t"
        "s_waitcnt lgkmcnt(0)\n\t"
        "s_barrier\n\t"
        "v_mfma_f32_16x16x32_bf16 v[8:11], v[160:163], v[56:59], v[8:11]\n\t"
        "ds_read_b64_tr_b16 v[184:185], v103 offset:16384\n\t"
        "ds_read_b64_tr_b16 v[186:187], v103 offset:16896\n\t"
        "v_mfma_f32_16x16x32_bf16 v[12:15], v[164:167], v[56:59], v[12:15]\n\t"
        "ds_read_b64_tr_b16 v[188:189], v103 offset:16392\n\t"
        "ds_read_b64_tr_b16 v[190:191], v103 offset:16904\n\t"
        "v_mfma_f32_16x16x32_bf16 v[16:19], v[56:59], v[168:171], v[16:19]\n\t"
        "ds_read_b64_tr_b16 v[192:193], v104 offset:16384\n\t"
        "ds_read_b64_tr_b16 v[194:195], v104 offset:16640\n\t"
        "v_mfma_f32_16x16x32_bf16 v[20:23], v[160:163], v[60:63], v[20:23]\n\t"
        "s_waitcnt lgkmcnt(5)\n\t"
        "ds_read_b64_tr_b16 v[196:197], v103 offset:20480\n\t"
        "v_mfma_f32_16x16x32_bf16 v[24:27], v[164:167], v[60:63], v[24:27]\n\t"
        "ds_read_b64_tr_b16 v[198:199], v103 offset:20992\n\t"
        "ds_read_b64_tr_b16 v[200:201], v103 offset:20488\n\t"
        "v_mfma_f32_16x16x32_bf16 v[28:31], v[60:63], v[168:171], v[28:31]\n\t"
        "ds_read_b64_tr_b16 v[202:203], v103 offset:21000\n\t"
        "ds_read_b64_tr_b16 v[204:205], v104 offset:18432\n\t"
        "v_mfma_f32_16x16x32_bf16 v[8:11], v[172:175], v[64:67], v[8:11]\n\t"
        "ds_read_b64_tr_b16 v[206:207], v104 offset:18688\n\t"
        "ds_read_b128 v[72:75], v107 offset:40960\n\t"
        "v_mfma_f32_16x16x32_bf16 v[12:15], v[176:179], v[64:67], v[12:15]\n\t"
        "ds_read_b128 v[76:79], v107 offset:43008\n\t"
        "ds_read_b128 v[80:83], v108 offset:40960\n\t"
        "v_mfma_f32_16x16x32_bf16 v[16:19], v[64:67], v[180:183], v[16:19]\n\t"
        "ds_read_b128 v[84:87], v108 offset:43008\n\t"
        "v_mfma_f32_16x16x32_bf16 v[20:23], v[172:175], v[68:71], v[20:23]\n\t"
        "v_mfma_f32_16x16x32_bf16 v[24:27], v[176:179], v[68:71], v[24:27]\n\t"
        "v_mfma_f32_16x16x32_bf16 v[28:31], v[68:71], v[180:183], v[28:31]\n\t"
        "s_waitcnt vmcnt(11)\n\t"
        "ds_write_b128 v102, v[232:235] offset:0\n\t"
        "ds_write_b128 v102, v[236:239] offset:4096\n\t"
        "ds_write_b128 v116, v[240:243] offset:8192\n\t"
        "s_waitcnt lgkmcnt(0)\n\t"
        "s_barrier\n\t"
        "v_mfma_f32_16x16x32_bf16 v[8:11], v[184:187], v[72:75], v[8:11]\n\t"
        "ds_read_b64_tr_b16 v[208:209], v103 offset:0\n\t"
        "ds_read_b64_tr_b16 v[210:211], v103 offset:512\n\t"
        "v_mfma_f32_16x16x32_bf16 v[12:15], v[188:191], v[72:75], v[12:15]\n\t"
        "ds_read_b64_tr_b16 v[212:213], v103 offset:8\n\t"
        "ds_read_b64_tr_b16 v[214:215], v103 offset:520\n\t"
        "v_mfma_f32_16x16x32_bf16 v[16:19], v[72:75], v[192:195], v[16:19]\n\t"
        "ds_read_b64_tr_b16 v[216:217], v104 offset:0\n\t"
        "ds_read_b64_tr_b16 v[218:219], v104 offset:256\n\t"
        "v_mfma_f32_16x16x32_bf16 v[20:23], v[184:187], v[76:79], v[20:23]\n\t"
        "s_waitcnt lgkmcnt(5)\n\t"
        "ds_read_b64_tr_b16 v[220:221], v103 offset:4096\n\t"
        "v_mfma_f32_16x16x32_bf16 v[24:27], v[188:191], v[76:79], v[24:27]\n\t"
        "ds_read_b64_tr_b16 v[222:223], v103 offset:4608\n\t"
        "ds_read_b64_tr_b16 v[224:225], v103 offset:4104\n\t"
        "v_mfma_f32_16x16x32_bf16 v[28:31], v[76:79], v[192:195], v[28:31]\n\t"
        "ds_read_b64_tr_b16 v[226:227], v103 offset:4616\n\t"
        "ds_read_b64_tr_b16 v[228:229], v104 offset:2048\n\t"
        "v_mfma_f32_16x16x32_bf16 v[8:11], v[196:199], v[80:83], v[8:11]\n\t"
        "ds_read_b64_tr_b16 v[230:231], v104 offset:2304\n\t"
        "ds_read_b128 v[56:59], v107 offset:49152\n\t"
        "v_mfma_f32_16x16x32_bf16 v[12:15], v[200:203], v[80:83], v[12:15]\n\t"
        "ds_read_b128 v[60:63], v107 offset:51200\n\t"
        "ds_read_b128 v[64:67], v108 offset:49152\n\t"
        "v_mfma_f32_16x16x32_bf16 v[16:19], v[80:83], v[204:207], v[16:19]\n\t"
        "ds_read_b128 v[68:71], v108 offset:51200\n\t"
        "v_mfma_f32_16x16x32_bf16 v[20:23], v[196:199], v[84:87], v[20:23]\n\t"
        "v_mfma_f32_16x16x32_bf16 v[24:27], v[200:203], v[84:87], v[24:27]\n\t"
        "v_mfma_f32_16x16x32_bf16 v[28:31], v[84:87], v[204:207], v[28:31]\n\t"
        "s_waitcnt vmcnt(8)\n\t"
        "ds_write_b128 v102, v[244:247] offset:16384\n\t"
        "ds_write_b128 v102, v[248:251] offset:20480\n\t"
        "ds_write_b128 v116, v[252:255] offset:24576\n\t"
        "s_waitcnt lgkmcnt(0)\n\t"
        "s_barrier\n\t"
        "v_mfma_f32_16x16x32_bf16 v[8:11], v[208:211], v[56:59], v[8:11]\n\t"
        "ds_read_b64_tr_b16 v[232:233], v103 offset:16384\n\t"
        "ds_read_b64_tr_b16 v[234:235], v103 offset:16896\n\t"
        "v_mfma_f32_16x16x32_bf16 v[12:15], v[212:215], v[56:59], v[12:15]\n\t"
        "ds_read_b64_tr_b16 v[236:237], v103 offset:16392\n\t"
        "ds_read_b64_tr_b16 v[238:239], v103 offset:16904\n\t"
        "v_mfma_f32_16x16x32_bf16 v[16:19], v[56:59], v[216:219], v[16:19]\n\t"
        "ds_read_b64_tr_b16 v[240:241], v104 offset:16384\n\t"
        "ds_read_b64_tr_b16 v[242:243], v104 offset:16640\n\t"
        "v_mfma_f32_16x16x32_bf16 v[20:23], v[208:211], v[60:63], v[20:23]\n\t"
        "s_waitcnt lgkmcnt(5)\n\t"
        "ds_read_b64_tr_b16 v[244:245], v103 offset:20480\n\t"
        "v_mfma_f32_16x16x32_bf16 v[24:27], v[212:215], v[60:63], v[24:27]\n\t"
        "ds_read_b64_tr_b16 v[246:247], v103 offset:20992\n\t"
        "ds_read_b64_tr_b16 v[248:249], v103 offset:20488\n\t"
        "v_mfma_f32_16x16x32_bf16 v[28:31], v[60:63], v[216:219], v[28:31]\n\t"
        "ds_read_b64_tr_b16 v[250:251], v103 offset:21000\n\t"
        "ds_read_b64_tr_b16 v[252:253], v104 offset:18432\n\t"
        "v_mfma_f32_16x16x32_bf16 v[8:11], v[220:223], v[64:67], v[8:11]\n\t"
        "ds_read_b64_tr_b16 v[254:255], v104 offset:18688\n\t"
        "ds_read_b128 v[72:75], v107 offset:57344\n\t"
        "v_mfma_f32_16x16x32_bf16 v[12:15], v[224:227], v[64:67], v[12:15]\n\t"
        "ds_read_b128 v[76:79], v107 offset:59392\n\t"
        "ds_read_b128 v[80:83], v108 offset:57344\n\t"
        "v_mfma_f32_16x16x32_bf16 v[16:19], v[64:67], v[228:231], v[16:19]\n\t"
        "ds_read_b128 v[84:87], v108 offset:59392\n\t"
        "v_mfma_f32_16x16x32_bf16 v[20:23], v[220:223], v[68:71], v[20:23]\n\t"
        "v_mfma_f32_16x16x32_bf16 v[24:27], v[224:227], v[68:71], v[24:27]\n\t"
        "v_mfma_f32_16x16x32_bf16 v[28:31], v[68:71], v[228:231], v[28:31]\n\t"
        "s_waitcnt vmcnt(0) lgkmcnt(0)\n\t"
        "s_barrier\n\t"
        "v_mfma_f32_16x16x32_bf16 v[8:11], v[232:235], v[72:75], v[8:11]\n\t"
        "v_mfma_f32_16x16x32_bf16 v[12:15], v[236:239], v[72:75], v[12:15]\n\t"
        "v_mfma_f32_16x16x32_bf16 v[16:19], v[72:75], v[240:243], v[16:19]\n\t"
        "v_mfma_f32_16x16x32_bf16 v[20:23], v[232:235], v[76:79], v[20:23]\n\t"
        "v_mfma_f32_16x16x32_bf16 v[24:27], v[236:239], v[76:79], v[24:27]\n\t"
        "v_mfma_f32_16x16x32_bf16 v[28:31], v[76:79], v[240:243], v[28:31]\n\t"
        "v_mfma_f32_16x16x32_bf16 v[8:11], v[244:247], v[80:83], v[8:11]\n\t"
        "v_mfma_f32_16x16x32_bf16 v[12:15], v[248:251], v[80:83], v[12:15]\n\t"
        "v_mfma_f32_16x16x32_bf16 v[16:19], v[80:83], v[252:255], v[16:19]\n\t"
        "v_mfma_f32_16x16x32_bf16 v[20:23], v[244:247], v[84:87], v[20:23]\n\t"
        "v_mfma_f32_16x16x32_bf16 v[24:27], v[248:251], v[84:87], v[24:27]\n\t"
        "v_mfma_f32_16x16x32_bf16 v[28:31], v[84:87], v[252:255], v[28:31]\n\t"
        "s_mov_b32 %[xo], 0x20000\n\t"
        "ds_read_b128 v[56:59], v105 offset:0\n\t"
        "ds_read_b128 v[60:63], v105 offset:2048\n\t"
        "ds_read_b128 v[64:67], v106 offset:0\n\t"
        "ds_read_b128 v[68:71], v106 offset:2048\n\t"
        "ds_read_b128 v[72:75], v105 offset:8192\n\t"
        "ds_read_b128 v[76:79], v105 offset:10240\n\t"
        "ds_read_b128 v[80:83], v106 offset:8192\n\t"
        "v_cvt_pk_bf16_f32 v88, v8, v9\n\t"
        "v_cvt_pk_bf16_f32 v89, v10, v11\n\t"
        "v_cvt_pk_bf16_f32 v90, v12, v13\n\t"
        "v_cvt_pk_bf16_f32 v91, v14, v15\n\t"
        "buffer_store_dwordx4 v[88:91], v109, %[sw], %[so] offen sc1\n\t"
        "v_cvt_pk_bf16_f32 v92, v20, v21\n\t"
        "v_cvt_pk_bf16_f32 v93, v22, v23\n\t"
        "v_cvt_pk_bf16_f32 v94, v24, v25\n\t"
        "v_cvt_pk_bf16_f32 v95, v26, v27\n\t"
        "buffer_store_dwordx4 v[92:95], v110, %[sw], %[so] offen sc1\n\t"
        "v_cvt_pk_bf16_f32 v96, v16, v17\n\t"
        "v_cvt_pk_bf16_f32 v97, v18, v19\n\t"
        "v_cvt_pk_bf16_f32 v98, v28, v29\n\t"
        "v_cvt_pk_bf16_f32 v99, v30, v31\n\t"
        "buffer_store_dwordx4 v[96:99], v111, %[sw], %[so] offen sc1\n\t"
        "s_mov_b32 %[so], 0xf0000000\n\t"
        "s_mov_b32 %[xo], 33792\n\t"
        "s_mov_b32 %[cn], 0\n\t"
        "LG_cal%=:\n\t"
        "buffer_load_dword v118, off, %[sw], %[xo] sc1\n\t"
        "s_waitcnt vmcnt(0)\n\t"
        "v_readfirstlane_b32 %[t0], v118\n\t"
        "s_cmp_lg_u32 %[t0], 0\n\t"
        "s_cbranch_scc1 LG_calok%=\n\t"
        "s_sleep 4\n\t"
        "s_add_u32 %[cn], %[cn], 1\n\t"
        "s_cmp_lt_u32 %[cn], 0x100000\n\t"
        "s_cbranch_scc1 LG_cal%=\n\t"
        "s_endpgm\n\t"
        "LG_calok%=:\n\t"
        "s_mov_b32 %[xo], 0x20000\n\t"
        "s_mov_b32 %[cn], 1\n\t"
        "v_mov_b32_e32 v119, %[cn]\n\t"
        "buffer_store_dword v119, v113, %[sw], 0 offen sc1\n\t"
        "s_add_u32 m0, %[ld], 32768\n\t"
        "s_add_u32 %[t0], %[xo], 512\n\t"
        "buffer_load_dwordx4 v100, %[sx], %[t0] offen lds\n\t"
        "s_add_u32 m0, %[ld], 33792\n\t"
        "s_add_u32 %[t0], %[xo], 16896\n\t"
        "buffer_load_dwordx4 v100, %[sx], %[t0] offen lds\n\t"
        "s_add_u32 m0, %[ld], 40960\n\t"
        "s_add_u32 %[t0], %[xo], 640\n\t"
        "buffer_load_dwordx4 v100, %[sx], %[t0] offen lds\n\t"
        "s_add_u32 m0, %[ld], 41984\n\t"
        "s_add_u32 %[t0], %[xo], 17024\n\t"
        "buffer_load_dwordx4 v100, %[sx], %[t0] offen lds\n\t"
        "s_add_u32 m0, %[ld], 49152\n\t"
        "s_add_u32 %[t0], %[xo], 768\n\t"
        "buffer_load_dwordx4 v100, %[sx], %[t0] offen lds\n\t"
        "s_add_u32 m0, %[ld], 50176\n\t"
        "s_add_u32 %[t0], %[xo], 17152\n\t"
        "buffer_load_dwordx4 v100, %[sx], %[t0] offen lds\n\t"
        "s_add_u32 m0, %[ld], 57344\n\t"
        "s_add_u32 %[t0], %[xo], 896\n\t"
        "buffer_load_dwordx4 v100, %[sx], %[t0] offen lds\n\t"
        "s_add_u32 m0, %[ld], 58368\n\t"
        "s_add_u32 %[t0], %[xo], 17280\n\t"
        "buffer_load_dwordx4 v100, %[sx], %[t0] offen lds\n\t"
        "s_add_u32 m0, %[ld], 65536\n\t"
        "s_add_u32 %[t0], %[xo], 1024\n\t"
        "buffer_load_dwordx4 v100, %[sx], %[t0] offen lds\n\t"
        "s_add_u32 m0, %[ld], 66560\n\t"
        "s_add_u32 %[t0], %[xo], 17408\n\t"
        "buffer_load_dwordx4 v100, %[sx], %[t0] offen lds\n\t"
        "s_add_u32 m0, %[ld], 73728\n\t"
        "s_add_u32 %[t0], %[xo], 1152\n\t"
        "buffer_load_dwordx4 v100, %[sx], %[t0] offen lds\n\t"
        "s_add_u32 m0, %[ld], 74752\n\t"
        "s_add_u32 %[t0], %[xo], 17536\n\t"
        "buffer_load_dwordx4 v100, %[sx], %[t0] offen lds\n\t"
        "s_add_u32 m0, %[ld], 81920\n\t"
        "s_add_u32 %[t0], %[xo], 1280\n\t"
        "buffer_load_dwordx4 v100, %[sx], %[t0] offen lds\n\t"
        "s_add_u32 m0, %[ld], 82944\n\t"
        "s_add_u32 %[t0], %[xo], 17664\n\t"
        "buffer_load_dwordx4 v100, %[sx], %[t0] offen lds\n\t"
        "s_add_u32 m0, %[ld], 90112\n\t"
        "s_add_u32 %[t0], %[xo], 1408\n\t"
        "buffer_load_dwordx4 v100, %[sx], %[t0] offen lds\n\t"
        "s_add_u32 m0, %[ld], 91136\n\t"
        "s_add_u32 %[t0], %[xo], 17792\n\t"
        "buffer_load_dwordx4 v100, %[sx], %[t0] offen lds\n\t"
        "s_add_u32 m0, %[ld], 98304\n\t"
        "s_add_u32 %[t0], %[xo], 1536\n\t"
        "buffer_load_dwordx4 v100, %[sx], %[t0] offen lds\n\t"
        "s_add_u32 m0, %[ld], 99328\n\t"
        "s_add_u32 %[t0], %[xo], 17920\n\t"
        "buffer_load_dwordx4 v100, %[sx], %[t0] offen lds\n\t"
        "s_add_u32 m0, %[ld], 106496\n\t"
        "s_add_u32 %[t0], %[xo], 1664\n\t"
        "buffer_load_dwordx4 v100, %[sx], %[t0] offen lds\n\t"
        "s_branch LG_mid%=\n\t"
        "LG_loop%=:\n\t"
        "s_waitcnt lgkmcnt(6)\n\t"
        "v_mfma_f32_16x16x32_bf16 v[8:11], a[0:3], v[56:59], 0\n\t"
        "v_mfma_f32_16x16x32_bf16 v[12:15], a[4:7], v[56:59], 0\n\t"
        "ds_read_b128 v[84:87], v106 offset:10240\n\t"
        "v_mfma_f32_16x16x32_bf16 v[16:19], v[56:59], a[8:11], 0\n\t"
        "s_add_u32 m0, %[ld], 107520\n\t"
        "s_add_u32 %[t0], %[xo], 18048\n\t"
        "s_waitcnt lgkmcnt(6)\n\t"
        "v_mfma_f32_16x16x32_bf16 v[20:23], a[0:3], v[60:63], 0\n\t"
        "buffer_load_dwordx4 v100, %[sx], %[t0] offen lds\n\t"
        "v_mfma_f32_16x16x32_bf16 v[24:27], a[4:7], v[60:63], 0\n\t"
        "ds_read_b128 v[56:59], v105 offset:16384\n\t"
        "v_mfma_f32_16x16x32_bf16 v[28:31], v[60:63], a[8:11], 0\n\t"
        "s_waitcnt lgkmcnt(6)\n\t"
        "v_mfma_f32_16x16x32_bf16 v[8:11], a[12:15], v[64:67], v[8:11]\n\t"
        "v_cvt_pk_bf16_f32 v88, v32, v33\n\t"
        "v_cvt_pk_bf16_f32 v89, v34, v35\n\t"
        "v_mfma_f32_16x16x32_bf16 v[12:15], a[16:19], v[64:67], v[12:15]\n\t"
        "ds_read_b128 v[60:63], v105 offset:18432\n\t"
        "v_cvt_pk_bf16_f32 v90, v36, v37\n\t"
        "v_mfma_f32_16x16x32_bf16 v[16:19], v[64:67], a[20:23], v[16:19]\n\t"
        "s_add_u32 m0, %[ld], 114688\n\t"
        "s_add_u32 %[t0], %[xo], 1792\n\t"
        "s_waitcnt lgkmcnt(6)\n\t"
        "v_mfma_f32_16x16x32_bf16 v[20:23], a[12:15], v[68:71], v[20:23]\n\t"
        "buffer_load_dwordx4 v100, %[sx], %[t0] offen lds\n\t"
        "v_mfma_f32_16x16x32_bf16 v[24:27], a[16:19], v[68:71], v[24:27]\n\t"
        "ds_read_b128 v[64:67], v106 offset:16384\n\t"
        "v_cvt_pk_bf16_f32 v91, v38, v39\n\t"
        "v_mfma_f32_16x16x32_bf16 v[28:31], v[68:71], a[20:23], v[28:31]\n\t"
        "buffer_store_dwordx4 v[88:91], v109, %[sw], %[so] offen sc1\n\t"
        "s_waitcnt lgkmcnt(6)\n\t"
        "v_mfma_f32_16x16x32_bf16 v[8:11], a[24:27], v[72:75], v[8:11]\n\t"
        "v_cvt_pk_bf16_f32 v92, v44, v45\n\t"
        "v_cvt_pk_bf16_f32 v93, v46, v47\n\t"
        "v_mfma_f32_16x16x32_bf16 v[12:15], a[28:31], v[72:75], v[12:15]\n\t"
        "ds_read_b128 v[68:71], v106 offset:18432\n\t"
        "v_cvt_pk_bf16_f32 v94, v48, v49\n\t"
        "v_mfma_f32_16x16x32_bf16 v[16:19], v[72:75], a[32:35], v[16:19]\n\t"
        "s_add_u32 m0, %[ld], 115712\n\t"
        "s_add_u32 %[t0], %[xo], 18176\n\t"
        "s_waitcnt lgkmcnt(6)\n\t"
        "v_mfma_f32_16x16x32_bf16 v[20:23], a[24:27], v[76:79], v[20:23]\n\t"
        "buffer_load_dwordx4 v100, %[sx], %[t0] offen lds\n\t"
        "v_mfma_f32_16x16x32_bf16 v[24:27], a[28:31], v[76:79], v[24:27]\n\t"
        "ds_read_b128 v[72:75], v105 offset:24576\n\t"
        "v_cvt_pk_bf16_f32 v95, v50, v51\n\t"
        "v_mfma_f32_16x16x32_bf16 v[28:31], v[76:79], a[32:35], v[28:31]\n\t"
        "buffer_store_dwordx4 v[92:95], v110, %[sw], %[so] offen sc1\n\t"
        "s_waitcnt lgkmcnt(6)\n\t"
        "v_mfma_f32_16x16x32_bf16 v[8:11], a[36:39], v[80:83], v[8:11]\n\t"
        "v_cvt_pk_bf16_f32 v96, v40, v41\n\t"
        "v_cvt_pk_bf16_f32 v97, v42, v43\n\t"
        "v_mfma_f32_16x16x32_bf16 v[12:15], a[40:43], v[80:83], v[12:15]\n\t"
        "ds_read_b128 v[76:79], v105 offset:26624\n\t"
        "v_cvt_pk_bf16_f32 v98, v52, v53\n\t"
        "v_mfma_f32_16x16x32_bf16 v[16:19], v[80:83], a[44:47], v[16:19]\n\t"
        "s_add_u32 m0, %[ld], 122880\n\t"
        "s_add_u32 %[t0], %[xo], 1920\n\t"
        "s_waitcnt lgkmcnt(6)\n\t"
        "v_mfma_f32_16x16x32_bf16 v[20:23], a[36:39], v[84:87], v[20:23]\n\t"
        "buffer_load_dwordx4 v100, %[sx], %[t0] offen lds\n\t"
        "v_mfma_f32_16x16x32_bf16 v[24:27], a[40:43], v[84:87], v[24:27]\n\t"
        "ds_read_b128 v[80:83], v106 offset:24576\n\t"
        "v_cvt_pk_bf16_f32 v99, v54, v55\n\t"
        "v_mfma_f32_16x16x32_bf16 v[28:31], v[84:87], a[44:47], v[28:31]\n\t"
        "buffer_store_dwordx4 v[96:99], v111, %[sw], %[so] offen sc1\n\t"
        "s_waitcnt lgkmcnt(6)\n\t"
        "v_mfma_f32_16x16x32_bf16 v[8:11], a[48:51], v[56:59], v[8:11]\n\t"
        "s_lshl_b32 %[so], %[cn], 19\n\t"
        "s_cmp_eq_u32 %[cn], 2\n\t"
        "s_cbranch_scc1 LG_first%=\n\t"
        "LG_back%=:\n\t"
        "v_mfma_f32_16x16x32_bf16 v[12:15], a[52:55], v[56:59], v[12:15]\n\t"
        "ds_read_b128 v[84:87], v106 offset:26624\n\t"
        "v_mfma_f32_16x16x32_bf16 v[16:19], v[56:59], a[56:59], v[16:19]\n\t"
        "s_add_u32 m0, %[ld], 123904\n\t"
        "s_add_u32 %[t0], %[xo], 18304\n\t"
        "s_waitcnt lgkmcnt(6)\n\t"
        "v_mfma_f32_16x16x32_bf16 v[20:23], a[48:51], v[60:63], v[20:23]\n\t"
        "buffer_load_dwordx4 v100, %[sx], %[t0] offen lds\n\t"
        "v_mfma_f32_16x16x32_bf16 v[24:27], a[52:55], v[60:63], v[24:27]\n\t"
        "v_mfma_f32_16x16x32_bf16 v[28:31], v[60:63], a[56:59], v[28:31]\n\t"
        "s_waitcnt lgkmcnt(5)\n\t"
        "v_mfma_f32_16x16x32_bf16 v[8:11], a[60:63], v[64:67], v[8:11]\n\t"
        "s_waitcnt vmcnt(20) lgkmcnt(0)\n\t"
        "s_barrier\n\t"
        "s_add_u32 %[xo], %[xo], 0x20000\n\t"
        "v_mfma_f32_16x16x32_bf16 v[12:15], a[64:67], v[64:67], v[12:15]\n\t"
        "ds_read_b128 v[56:59], v105 offset:32768\n\t"
        "v_mfma_f32_16x16x32_bf16 v[16:19], v[64:67], a[68:71], v[16:19]\n\t"
        "ds_read_b128 v[60:63], v105 offset:34816\n\t"
        "s_add_u32 m0, %[ld], 0\n\t"
        "s_add_u32 %[t0], %[xo], 0\n\t"
        "v_mfma_f32_16x16x32_bf16 v[20:23], a[60:63], v[68:71], v[20:23]\n\t"
        "buffer_load_dwordx4 v100, %[sx], %[t0] offen lds\n\t"
        "v_mfma_f32_16x16x32_bf16 v[24:27], a[64:67], v[68:71], v[24:27]\n\t"
        "ds_read_b128 v[64:67], v106 offset:32768\n\t"
        "v_mfma_f32_16x16x32_bf16 v[28:31], v[68:71], a[68:71], v[28:31]\n\t"
        "v_mfma_f32_16x16x32_bf16 v[8:11], a[72:75], v[72:75], v[8:11]\n\t"
        "v_mfma_f32_16x16x32_bf16 v[12:15], a[76:79], v[72:75], v[12:15]\n\t"
        "ds_read_b128 v[68:71], v106 offset:34816\n\t"
        "v_mfma_f32_16x16x32_bf16 v[16:19], v[72:75], a[80:83], v[16:19]\n\t"
        "s_add_u32 m0, %[ld], 1024\n\t"
        "s_add_u32 %[t0], %[xo], 16384\n\t"
        "v_mfma_f32_16x16x32_bf16 v[20:23], a[72:75], v[76:79], v[20:23]\n\t"
        "buffer_load_dwordx4 v100, %[sx], %[t0] offen lds\n\t"
        "v_mfma_f32_16x16x32_bf16 v[24:27], a[76:79], v[76:79], v[24:27]\n\t"
        "ds_read_b128 v[72:75], v105 offset:40960\n\t"
        "v_mfma_f32_16x16x32_bf16 v[28:31], v[76:79], a[80:83], v[28:31]\n\t"
        "v_mfma_f32_16x16x32_bf16 v[8:11], a[84:87], v[80:83], v[8:11]\n\t"
        "v_mfma_f32_16x16x32_bf16 v[12:15], a[88:91], v[80:83], v[12:15]\n\t"
        "ds_read_b128 v[76:79], v105 offset:43008\n\t"
        "v_mfma_f32_16x16x32_bf16 v[16:19], v[80:83], a[92:95], v[16:19]\n\t"
        "s_add_u32 m0, %[ld], 8192\n\t"
        "s_add_u32 %[t0], %[xo], 128\n\t"
        "v_mfma_f32_16x16x32_bf16 v[20:23], a[84:87], v[84:87], v[20:23]\n\t"
        "buffer_load_dwordx4 v100, %[sx], %[t0] offen lds\n\t"
        "v_mfma_f32_16x16x32_bf16 v[24:27], a[88:91], v[84:87], v[24:27]\n\t"
        "ds_read_b128 v[80:83], v106 offset:40960\n\t"
        "v_mfma_f32_16x16x32_bf16 v[28:31], v[84:87], a[92:95], v[28:31]\n\t"
        "s_waitcnt lgkmcnt(6)\n\t"
        "v_mfma_f32_16x16x32_bf16 v[8:11], a[96:99], v[56:59], v[8:11]\n\t"
        "v_mfma_f32_16x16x32_bf16 v[12:15], a[100:103], v[56:59], v[12:15]\n\t"
        "ds_read_b128 v[84:87], v106 offset:43008\n\t"
        "v_mfma_f32_16x16x32_bf16 v[16:19], v[56:59], a[104:107], v[16:19]\n\t"
        "s_add_u32 m0, %[ld], 9216\n\t"
        "s_add_u32 %[t0], %[xo], 16512\n\t"
        "s_waitcnt lgkmcnt(6)\n\t"
        "v_mfma_f32_16x16x32_bf16 v[20:23], a[96:99], v[60:63], v[20:23]\n\t"
        "buffer_load_dwordx4 v100, %[sx], %[t0] offen lds\n\t"
        "v_mfma_f32_16x16x32_bf16 v[24:27], a[100:103], v[60:63], v[24:27]\n\t"
        "ds_read_b128 v[56:59], v105 offset:49152\n\t"
        "v_mfma_f32_16x16x32_bf16 v[28:31], v[60:63], a[104:107], v[28:31]\n\t"
        "s_waitcnt lgkmcnt(6)\n\t"
        "v_mfma_f32_16x16x32_bf16 v[8:11], a[108:111], v[64:67], v[8:11]\n\t"
        "v_mfma_f32_16x16x32_bf16 v[12:15], a[112:115], v[64:67], v[12:15]\n\t"
        "ds_read_b128 v[60:63], v105 offset:51200\n\t"
        "v_mfma_f32_16x16x32_bf16 v[16:19], v[64:67], a[116:119], v[16:19]\n\t"
        "s_add_u32 m0, %[ld], 16384\n\t"
        "s_add_u32 %[t0], %[xo], 256\n\t"
        "s_waitcnt lgkmcnt(6)\n\t"
        "v_mfma_f32_16x16x32_bf16 v[20:23], a[108:111], v[68:71], v[20:23]\n\t"
        "buffer_load_dwordx4 v100, %[sx], %[t0] offen lds\n\t"
        "v_mfma_f32_16x16x32_bf16 v[24:27], a[112:115], v[68:71], v[24:27]\n\t"
        "ds_read_b128 v[64:67], v106 offset:49152\n\t"
        "v_mfma_f32_16x16x32_bf16 v[28:31], v[68:71], a[116:119], v[28:31]\n\t"
        "s_waitcnt lgkmcnt(6)\n\t"
        "v_mfma_f32_16x16x32_bf16 v[8:11], a[120:123], v[72:75], v[8:11]\n\t"
        "v_mfma_f32_16x16x32_bf16 v[12:15], a[124:127], v[72:75], v[12:15]\n\t"
        "ds_read_b128 v[68:71], v106 offset:51200\n\t"
        "v_mfma_f32_16x16x32_bf16 v[16:19], v[72:75], a[128:131], v[16:19]\n\t"
        "s_add_u32 m0, %[ld], 17408\n\t"
        "s_add_u32 %[t0], %[xo], 16640\n\t"
        "s_waitcnt lgkmcnt(6)\n\t"
        "v_mfma_f32_16x16x32_bf16 v[20:23], a[120:123], v[76:79], v[20:23]\n\t"
        "buffer_load_dwordx4 v100, %[sx], %[t0] offen lds\n\t"
        "v_mfma_f32_16x16x32_bf16 v[24:27], a[124:127], v[76:79], v[24:27]\n\t"
        "ds_read_b128 v[72:75], v105 offset:57344\n\t"
        "v_mfma_f32_16x16x32_bf16 v[28:31], v[76:79], a[128:131], v[28:31]\n\t"
        "s_waitcnt lgkmcnt(6)\n\t"
        "v_mfma_f32_16x16x32_bf16 v[8:11], a[132:135], v[80:83], v[8:11]\n\t"
        "v_mfma_f32_16x16x32_bf16 v[12:15], a[136:139], v[80:83], v[12:15]\n\t"
        "ds_read_b128 v[76:79], v105 offset:59392\n\t"
        "v_mfma_f32_16x16x32_bf16 v[16:19], v[80:83], a[140:143], v[16:19]\n\t"
        "s_add_u32 m0, %[ld], 24576\n\t"
        "s_add_u32 %[t0], %[xo], 384\n\t"
        "s_waitcnt lgkmcnt(6)\n\t"
        "v_mfma_f32_16x16x32_bf16 v[20:23], a[132:135], v[84:87], v[20:23]\n\t"
        "buffer_load_dwordx4 v100, %[sx], %[t0] offen lds\n\t"
        "v_mfma_f32_16x16x32_bf16 v[24:27], a[136:139], v[84:87], v[24:27]\n\t"
        "ds_read_b128 v[80:83], v106 offset:57344\n\t"
        "v_mfma_f32_16x16x32_bf16 v[28:31], v[84:87], a[140:143], v[28:31]\n\t"
        "s_waitcnt lgkmcnt(6)\n\t"
        "v_mfma_f32_16x16x32_bf16 v[8:11], a[144:147], v[56:59], v[8:11]\n\t"
        "v_mfma_f32_16x16x32_bf16 v[12:15], a[148:151], v[56:59], v[12:15]\n\t"
        "ds_read_b128 v[84:87], v106 offset:59392\n\t"
        "v_mfma_f32_16x16x32_bf16 v[16:19], v[56:59], a[152:155], v[16:19]\n\t"
        "s_add_u32 m0, %[ld], 25600\n\t"
        "s_add_u32 %[t0], %[xo], 16768\n\t"
        "s_waitcnt lgkmcnt(6)\n\t"
        "v_mfma_f32_16x16x32_bf16 v[20:23], a[144:147], v[60:63], v[20:23]\n\t"
        "buffer_load_dwordx4 v100, %[sx], %[t0] offen lds\n\t"
        "v_mfma_f32_16x16x32_bf16 v[24:27], a[148:151], v[60:63], v[24:27]\n\t"
        "v_mfma_f32_16x16x32_bf16 v[28:31], v[60:63], a[152:155], v[28:31]\n\t"
        "s_waitcnt lgkmcnt(5)\n\t"
        "v_mfma_f32_16x16x32_bf16 v[8:11], a[156:159], v[64:67], v[8:11]\n\t"
        "s_waitcnt vmcnt(9) lgkmcnt(0)\n\t"
        "s_barrier\n\t"
        "v_mfma_f32_16x16x32_bf16 v[12:15], a[160:163], v[64:67], v[12:15]\n\t"
        "ds_read_b128 v[56:59], v107 offset:0\n\t"
        "v_mfma_f32_16x16x32_bf16 v[16:19], v[64:67], a[164:167], v[16:19]\n\t"
        "ds_read_b128 v[60:63], v107 offset:2048\n\t"
        "s_add_u32 m0, %[ld], 32768\n\t"
        "s_add_u32 %[t0], %[xo], 512\n\t"
        "v_mfma_f32_16x16x32_bf16 v[20:23], a[156:159], v[68:71], v[20:23]\n\t"
        "buffer_load_dwordx4 v100, %[sx], %[t0] offen lds\n\t"
        "v_mfma_f32_16x16x32_bf16 v[24:27], a[160:163], v[68:71], v[24:27]\n\t"
        "ds_read_b128 v[64:67], v108 offset:0\n\t"
        "v_mfma_f32_16x16x32_bf16 v[28:31], v[68:71], a[164:167], v[28:31]\n\t"
        "v_mfma_f32_16x16x32_bf16 v[8:11], a[168:171], v[72:75], v[8:11]\n\t"
        "v_mfma_f32_16x16x32_bf16 v[12:15], a[172:175], v[72:75], v[12:15]\n\t"
        "ds_read_b128 v[68:71], v108 offset:2048\n\t"
        "v_mov_b32_e32 v119, %[cn]\n\t"
        "buffer_store_dword v119, v113, %[sw], 0 offen sc1\n\t"
        "s_add_u32 %[cn], %[cn], 1\n\t"
        "v_mfma_f32_16x16x32_bf16 v[16:19], v[72:75], a[176:179], v[16:19]\n\t"
        "s_add_u32 m0, %[ld], 33792\n\t"
        "s_add_u32 %[t0], %[xo], 16896\n\t"
        "v_mfma_f32_16x16x32_bf16 v[20:23], a[168:171], v[76:79], v[20:23]\n\t"
        "buffer_load_dwordx4 v100, %[sx], %[t0] offen lds\n\t"
        "v_mfma_f32_16x16x32_bf16 v[24:27], a[172:175], v[76:79], v[24:27]\n\t"
        "ds_read_b128 v[72:75], v107 offset:8192\n\t"
        "v_mfma_f32_16x16x32_bf16 v[28:31], v[76:79], a[176:179], v[28:31]\n\t"
        "v_mfma_f32_16x16x32_bf16 v[8:11], a[180:183], v[80:83], v[8:11]\n\t"
        "v_mfma_f32_16x16x32_bf16 v[12:15], a[184:187], v[80:83], v[12:15]\n\t"
        "ds_read_b128 v[76:79], v107 offset:10240\n\t"
        "v_mfma_f32_16x16x32_bf16 v[16:19], v[80:83], a[188:191], v[16:19]\n\t"
        "s_add_u32 m0, %[ld], 40960\n\t"
        "s_add_u32 %[t0], %[xo], 640\n\t"
        "v_mfma_f32_16x16x32_bf16 v[20:23], a[180:183], v[84:87], v[20:23]\n\t"
        "buffer_load_dwordx4 v100, %[sx], %[t0] offen lds\n\t"
        "v_mfma_f32_16x16x32_bf16 v[24:27], a[184:187], v[84:87], v[24:27]\n\t"
        "ds_read_b128 v[80:83], v108 offset:8192\n\t"
        "v_mfma_f32_16x16x32_bf16 v[28:31], v[84:87], a[188:191], v[28:31]\n\t"
        "s_waitcnt lgkmcnt(6)\n\t"
        "v_mfma_f32_16x16x32_bf16 v[8:11], a[192:195], v[56:59], v[8:11]\n\t"
        "v_mfma_f32_16x16x32_bf16 v[12:15], a[196:199], v[56:59], v[12:15]\n\t"
        "ds_read_b128 v[84:87], v108 offset:10240\n\t"
        "v_mfma_f32_16x16x32_bf16 v[16:19], v[56:59], a[200:203], v[16:19]\n\t"
        "s_add_u32 m0, %[ld], 41984\n\t"
        "s_add_u32 %[t0], %[xo], 17024\n\t"
        "s_waitcnt lgkmcnt(6)\n\t"
        "v_mfma_f32_16x16x32_bf16 v[20:23], a[192:195], v[60:63], v[20:23]\n\t"
        "buffer_load_dwordx4 v100, %[sx], %[t0] offen lds\n\t"
        "v_mfma_f32_16x16x32_bf16 v[24:27], a[196:199], v[60:63], v[24:27]\n\t"
        "ds_read_b128 v[56:59], v107 offset:16384\n\t"
        "v_mfma_f32_16x16x32_bf16 v[28:31], v[60:63], a[200:203], v[28:31]\n\t"
        "s_waitcnt lgkmcnt(6)\n\t"
        "v_mfma_f32_16x16x32_bf16 v[8:11], a[204:207], v[64:67], v[8:11]\n\t"
        "v_mfma_f32_16x16x32_bf16 v[12:15], a[208:211], v[64:67], v[12:15]\n\t"
        "ds_read_b128 v[60:63], v107 offset:18432\n\t"
        "v_mfma_f32_16x16x32_bf16 v[16:19], v[64:67], a[212:215], v[16:19]\n\t"
        "s_add_u32 m0, %[ld], 49152\n\t"
        "s_add_u32 %[t0], %[xo], 768\n\t"
        "s_waitcnt lgkmcnt(6)\n\t"
        "v_mfma_f32_16x16x32_bf16 v[20:23], a[204:207], v[68:71], v[20:23]\n\t"
        "buffer_load_dwordx4 v100, %[sx], %[t0] offen lds\n\t"
        "v_mfma_f32_16x16x32_bf16 v[24:27], a[208:211], v[68:71], v[24:27]\n\t"
        "ds_read_b128 v[64:67], v108 offset:16384\n\t"
        "v_mfma_f32_16x16x32_bf16 v[28:31], v[68:71], a[212:215], v[28:31]\n\t"
        "s_waitcnt lgkmcnt(6)\n\t"
        "v_mfma_f32_16x16x32_bf16 v[8:11], a[216:219], v[72:75], v[8:11]\n\t"
        "v_mfma_f32_16x16x32_bf16 v[12:15], a[220:223], v[72:75], v[12:15]\n\t"
        "ds_read_b128 v[68:71], v108 offset:18432\n\t"
        "v_mfma_f32_16x16x32_bf16 v[16:19], v[72:75], a[224:227], v[16:19]\n\t"
        "s_add_u32 m0, %[ld], 50176\n\t"
        "s_add_u32 %[t0], %[xo], 17152\n\t"
        "s_waitcnt lgkmcnt(6)\n\t"
        "v_mfma_f32_16x16x32_bf16 v[20:23], a[216:219], v[76:79], v[20:23]\n\t"
        "buffer_load_dwordx4 v100, %[sx], %[t0] offen lds\n\t"
        "v_mfma_f32_16x16x32_bf16 v[24:27], a[220:223], v[76:79], v[24:27]\n\t"
        "ds_read_b128 v[72:75], v107 offset:24576\n\t"
        "v_mfma_f32_16x16x32_bf16 v[28:31], v[76:79], a[224:227], v[28:31]\n\t"
        "s_waitcnt lgkmcnt(6)\n\t"
        "v_mfma_f32_16x16x32_bf16 v[8:11], a[228:231], v[80:83], v[8:11]\n\t"
        "v_mfma_f32_16x16x32_bf16 v[12:15], a[232:235], v[80:83], v[12:15]\n\t"
        "ds_read_b128 v[76:79], v107 offset:26624\n\t"
        "v_mfma_f32_16x16x32_bf16 v[16:19], v[80:83], a[236:239], v[16:19]\n\t"
        "s_add_u32 m0, %[ld], 57344\n\t"
        "s_add_u32 %[t0], %[xo], 896\n\t"
        "s_waitcnt lgkmcnt(6)\n\t"
        "v_mfma_f32_16x16x32_bf16 v[20:23], a[228:231], v[84:87], v[20:23]\n\t"
        "buffer_load_dwordx4 v100, %[sx], %[t0] offen lds\n\t"
        "v_mfma_f32_16x16x32_bf16 v[24:27], a[232:235], v[84:87], v[24:27]\n\t"
        "ds_read_b128 v[80:83], v108 offset:24576\n\t"
        "v_mfma_f32_16x16x32_bf16 v[28:31], v[84:87], a[236:239], v[28:31]\n\t"
        "s_waitcnt lgkmcnt(6)\n\t"
        "v_mfma_f32_16x16x32_bf16 v[8:11], a[240:243], v[56:59], v[8:11]\n\t"
        "v_mfma_f32_16x16x32_bf16 v[12:15], a[244:247], v[56:59], v[12:15]\n\t"
        "ds_read_b128 v[84:87], v108 offset:26624\n\t"
        "v_mfma_f32_16x16x32_bf16 v[16:19], v[56:59], a[248:251], v[16:19]\n\t"
        "s_add_u32 m0, %[ld], 58368\n\t"
        "s_add_u32 %[t0], %[xo], 17280\n\t"
        "s_waitcnt lgkmcnt(6)\n\t"
        "v_mfma_f32_16x16x32_bf16 v[20:23], a[240:243], v[60:63], v[20:23]\n\t"
        "buffer_load_dwordx4 v100, %[sx], %[t0] offen lds\n\t"
        "v_mfma_f32_16x16x32_bf16 v[24:27], a[244:247], v[60:63], v[24:27]\n\t"
        "v_mfma_f32_16x16x32_bf16 v[28:31], v[60:63], a[248:251], v[28:31]\n\t"
        "s_waitcnt lgkmcnt(5)\n\t"
        "v_mfma_f32_16x16x32_bf16 v[8:11], a[252:255], v[64:67], v[8:11]\n\t"
        "s_waitcnt vmcnt(17) lgkmcnt(0)\n\t"
        "s_barrier\n\t"
        "v_mfma_f32_16x16x32_bf16 v[12:15], v[128:131], v[64:67], v[12:15]\n\t"
        "ds_read_b128 v[56:59], v107 offset:32768\n\t"
        "v_mfma_f32_16x16x32_bf16 v[16:19], v[64:67], v[132:135], v[16:19]\n\t"
        "ds_read_b128 v[60:63], v107 offset:34816\n\t"
        "s_add_u32 m0, %[ld], 65536\n\t"
        "s_add_u32 %[t0], %[xo], 1024\n\t"
        "v_mfma_f32_16x16x32_bf16 v[20:23], a[252:255], v[68:71], v[20:23]\n\t"
        "buffer_load_dwordx4 v100, %[sx], %[t0] offen lds\n\t"
        "v_mfma_f32_16x16x32_bf16 v[24:27], v[128:131], v[68:71], v[24:27]\n\t"
        "ds_read_b128 v[64:67], v108 offset:32768\n\t"
        "v_mfma_f32_16x16x32_bf16 v[28:31], v[68:71], v[132:135], v[28:31]\n\t"
        "v_mfma_f32_16x16x32_bf16 v[8:11], v[136:139], v[72:75], v[8:11]\n\t"
        "v_mfma_f32_16x16x32_bf16 v[12:15], v[140:143], v[72:75], v[12:15]\n\t"
        "ds_read_b128 v[68:71], v108 offset:34816\n\t"
        "v_mfma_f32_16x16x32_bf16 v[16:19], v[72:75], v[144:147], v[16:19]\n\t"
        "s_add_u32 m0, %[ld], 66560\n\t"
        "s_add_u32 %[t0], %[xo], 17408\n\t"
        "v_mfma_f32_16x16x32_bf16 v[20:23], v[136:139], v[76:79], v[20:23]\n\t"
        "buffer_load_dwordx4 v100, %[sx], %[t0] offen lds\n\t"
        "v_mfma_f32_16x16x32_bf16 v[24:27], v[140:143], v[76:79], v[24:27]\n\t"
        "ds_read_b128 v[72:75], v107 offset:40960\n\t"
        "v_mfma_f32_16x16x32_bf16 v[28:31], v[76:79], v[144:147], v[28:31]\n\t"
        "v_mfma_f32_16x16x32_bf16 v[8:11], v[148:151], v[80:83], v[8:11]\n\t"
        "v_mfma_f32_16x16x32_bf16 v[12:15], v[152:155], v[80:83], v[12:15]\n\t"
        "ds_read_b128 v[76:79], v107 offset:43008\n\t"
        "v_mfma_f32_16x16x32_bf16 v[16:19], v[80:83], v[156:159], v[16:19]\n\t"
        "s_add_u32 m0, %[ld], 73728\n\t"
        "s_add_u32 %[t0], %[xo], 1152\n\t"
        "v_mfma_f32_16x16x32_bf16 v[20:23], v[148:151], v[84:87], v[20:23]\n\t"
        "buffer_load_dwordx4 v100, %[sx], %[t0] offen lds\n\t"
        "v_mfma_f32_16x16x32_bf16 v[24:27], v[152:155], v[84:87], v[24:27]\n\t"
        "ds_read_b128 v[80:83], v108 offset:40960\n\t"
        "v_mfma_f32_16x16x32_bf16 v[28:31], v[84:87], v[156:159], v[28:31]\n\t"
        "s_waitcnt lgkmcnt(6)\n\t"
        "v_mfma_f32_16x16x32_bf16 v[8:11], v[160:163], v[56:59], v[8:11]\n\t"
        "v_mfma_f32_16x16x32_bf16 v[12:15], v[164:167], v[56:59], v[12:15]\n\t"
        "ds_read_b128 v[84:87], v108 offset:43008\n\t"
        "v_mfma_f32_16x16x32_bf16 v[16:19], v[56:59], v[168:171], v[16:19]\n\t"
        "s_add_u32 m0, %[ld], 74752\n\t"
        "s_add_u32 %[t0], %[xo], 17536\n\t"
        "s_waitcnt lgkmcnt(6)\n\t"
        "v_mfma_f32_16x16x32_bf16 v[20:23], v[160:163], v[60:63], v[20:23]\n\t"
        "buffer_load_dwordx4 v100, %[sx], %[t0] offen lds\n\t"
        "v_mfma_f32_16x16x32_bf16 v[24:27], v[164:167], v[60:63], v[24:27]\n\t"
        "ds_read_b128 v[56:59], v107 offset:49152\n\t"
        "v_mfma_f32_16x16x32_bf16 v[28:31], v[60:63], v[168:171], v[28:31]\n\t"
        "s_waitcnt lgkmcnt(6)\n\t"
        "v_mfma_f32_16x16x32_bf16 v[8:11], v[172:175], v[64:67], v[8:11]\n\t"
        "v_mfma_f32_16x16x32_bf16 v[12:15], v[176:179], v[64:67], v[12:15]\n\t"
        "ds_read_b128 v[60:63], v107 offset:51200\n\t"
        "v_mfma_f32_16x16x32_bf16 v[16:19], v[64:67], v[180:183], v[16:19]\n\t"
        "s_add_u32 m0, %[ld], 81920\n\t"
        "s_add_u32 %[t0], %[xo], 1280\n\t"
        "s_waitcnt lgkmcnt(6)\n\t"
        "v_mfma_f32_16x16x32_bf16 v[20:23], v[172:175], v[68:71], v[20:23]\n\t"
        "buffer_load_dwordx4 v100, %[sx], %[t0] offen lds\n\t"
        "v_mfma_f32_16x16x32_bf16 v[24:27], v[176:179], v[68:71], v[24:27]\n\t"
        "ds_read_b128 v[64:67], v108 offset:49152\n\t"
        "v_mfma_f32_16x16x32_bf16 v[28:31], v[68:71], v[180:183], v[28:31]\n\t"
        "s_waitcnt lgkmcnt(6)\n\t"
        "v_mfma_f32_16x16x32_bf16 v[8:11], v[184:187], v[72:75], v[8:11]\n\t"
        "v_mfma_f32_16x16x32_bf16 v[12:15], v[188:191], v[72:75], v[12:15]\n\t"
        "ds_read_b128 v[68:71], v108 offset:51200\n\t"
        "v_mfma_f32_16x16x32_bf16 v[16:19], v[72:75], v[192:195], v[16:19]\n\t"
        "s_add_u32 m0, %[ld], 82944\n\t"
        "s_add_u32 %[t0], %[xo], 17664\n\t"
        "s_waitcnt lgkmcnt(6)\n\t"
        "v_mfma_f32_16x16x32_bf16 v[20:23], v[184:187], v[76:79], v[20:23]\n\t"
        "buffer_load_dwordx4 v100, %[sx], %[t0] offen lds\n\t"
        "v_mfma_f32_16x16x32_bf16 v[24:27], v[188:191], v[76:79], v[24:27]\n\t"
        "ds_read_b128 v[72:75], v107 offset:57344\n\t"
        "v_mfma_f32_16x16x32_bf16 v[28:31], v[76:79], v[192:195], v[28:31]\n\t"
        "s_waitcnt lgkmcnt(6)\n\t"
        "v_mfma_f32_16x16x32_bf16 v[8:11], v[196:199], v[80:83], v[8:11]\n\t"
        "v_mfma_f32_16x16x32_bf16 v[12:15], v[200:203], v[80:83], v[12:15]\n\t"
        "ds_read_b128 v[76:79], v107 offset:59392\n\t"
        "v_mfma_f32_16x16x32_bf16 v[16:19], v[80:83], v[204:207], v[16:19]\n\t"
        "s_add_u32 m0, %[ld], 90112\n\t"
        "s_add_u32 %[t0], %[xo], 1408\n\t"
        "s_waitcnt lgkmcnt(6)\n\t"
        "v_mfma_f32_16x16x32_bf16 v[20:23], v[196:199], v[84:87], v[20:23]\n\t"
        "buffer_load_dwordx4 v100, %[sx], %[t0] offen lds\n\t"
        "v_mfma_f32_16x16x32_bf16 v[24:27], v[200:203], v[84:87], v[24:27]\n\t"
        "ds_read_b128 v[80:83], v108 offset:57344\n\t"
        "v_mfma_f32_16x16x32_bf16 v[28:31], v[84:87], v[204:207], v[28:31]\n\t"
        "s_waitcnt lgkmcnt(6)\n\t"
        "v_mfma_f32_16x16x32_bf16 v[8:11], v[208:211], v[56:59], v[8:11]\n\t"
        "v_mfma_f32_16x16x32_bf16 v[12:15], v[212:215], v[56:59], v[12:15]\n\t"
        "ds_read_b128 v[84:87], v108 offset:59392\n\t"
        "v_mfma_f32_16x16x32_bf16 v[16:19], v[56:59], v[216:219], v[16:19]\n\t"
        "s_add_u32 m0, %[ld], 91136\n\t"
        "s_add_u32 %[t0], %[xo], 17792\n\t"
        "s_waitcnt lgkmcnt(6)\n\t"
        "v_mfma_f32_16x16x32_bf16 v[20:23], v[208:211], v[60:63], v[20:23]\n\t"
        "buffer_load_dwordx4 v100, %[sx], %[t0] offen lds\n\t"
        "v_mfma_f32_16x16x32_bf16 v[24:27], v[212:215], v[60:63], v[24:27]\n\t"
        "v_mfma_f32_16x16x32_bf16 v[28:31], v[60:63], v[216:219], v[28:31]\n\t"
        "s_waitcnt lgkmcnt(5)\n\t"
        "v_mfma_f32_16x16x32_bf16 v[8:11], v[220:223], v[64:67], v[8:11]\n\t"
        "s_waitcnt vmcnt(17) lgkmcnt(0)\n\t"
        "s_barrier\n\t"
        "v_mfma_f32_16x16x32_bf16 v[12:15], v[224:227], v[64:67], v[12:15]\n\t"
        "ds_read_b128 v[56:59], v105 offset:0\n\t"
        "v_mfma_f32_16x16x32_bf16 v[16:19], v[64:67], v[228:231], v[16:19]\n\t"
        "ds_read_b128 v[60:63], v105 offset:2048\n\t"
        "s_add_u32 m0, %[ld], 98304\n\t"
        "s_add_u32 %[t0], %[xo], 1536\n\t"
        "v_mfma_f32_16x16x32_bf16 v[20:23], v[220:223], v[68:71], v[20:23]\n\t"
        "buffer_load_dwordx4 v100, %[sx], %[t0] offen lds\n\t"
        "v_mfma_f32_16x16x32_bf16 v[24:27], v[224:227], v[68:71], v[24:27]\n\t"
        "ds_read_b128 v[64:67], v106 offset:0\n\t"
        "v_mfma_f32_16x16x32_bf16 v[28:31], v[68:71], v[228:231], v[28:31]\n\t"
        "v_mfma_f32_16x16x32_bf16 v[8:11], v[232:235], v[72:75], v[8:11]\n\t"
        "v_mfma_f32_16x16x32_bf16 v[12:15], v[236:239], v[72:75], v[12:15]\n\t"
        "ds_read_b128 v[68:71], v106 offset:2048\n\t"
        "v_mfma_f32_16x16x32_bf16 v[16:19], v[72:75], v[240:243], v[16:19]\n\t"
        "s_add_u32 m0, %[ld], 99328\n\t"
        "s_add_u32 %[t0], %[xo], 17920\n\t"
        "v_mfma_f32_16x16x32_bf16 v[20:23], v[232:235], v[76:79], v[20:23]\n\t"
        "buffer_load_dwordx4 v100, %[sx], %[t0] offen lds\n\t"
        "v_mfma_f32_16x16x32_bf16 v[24:27], v[236:239], v[76:79], v[24:27]\n\t"
        "ds_read_b128 v[72:75], v105 offset:8192\n\t"
        "v_mfma_f32_16x16x32_bf16 v[28:31], v[76:79], v[240:243], v[28:31]\n\t"
        "v_mfma_f32_16x16x32_bf16 v[8:11], v[244:247], v[80:83], v[8:11]\n\t"
        "v_mfma_f32_16x16x32_bf16 v[12:15], v[248:251], v[80:83], v[12:15]\n\t"
        "ds_read_b128 v[76:79], v105 offset:10240\n\t"
        "v_mfma_f32_16x16x32_bf16 v[16:19], v[80:83], v[252:255], v[16:19]\n\t"
        "s_add_u32 m0, %[ld], 106496\n\t"
        "s_add_u32 %[t0], %[xo], 1664\n\t"
        "v_mfma_f32_16x16x32_bf16 v[20:23], v[244:247], v[84:87], v[20:23]\n\t"
        "buffer_load_dwordx4 v100, %[sx], %[t0] offen lds\n\t"
        "v_mfma_f32_16x16x32_bf16 v[24:27], v[248:251], v[84:87], v[24:27]\n\t"
        "ds_read_b128 v[80:83], v106 offset:8192\n\t"
        "v_mfma_f32_16x16x32_bf16 v[28:31], v[84:87], v[252:255], v[28:31]\n\t"
        "LG_mid%=:\n\t"
        "s_waitcnt lgkmcnt(6)\n\t"
        "v_mfma_f32_16x16x32_bf16 v[32:35], a[0:3], v[56:59], 0\n\t"
        "v_mfma_f32_16x16x32_bf16 v[36:39], a[4:7], v[56:59], 0\n\t"
        "ds_read_b128 v[84:87], v106 offset:10240\n\t"
        "v_mfma_f32_16x16x32_bf16 v[40:43], v[56:59], a[8:11], 0\n\t"
        "s_add_u32 m0, %[ld], 107520\n\t"
        "s_add_u32 %[t0], %[xo], 18048\n\t"
        "s_waitcnt lgkmcnt(6)\n\t"
        "v_mfma_f32_16x16x32_bf16 v[44:47], a[0:3], v[60:63], 0\n\t"
        "buffer_load_dwordx4 v100, %[sx], %[t0] offen lds\n\t"
        "v_mfma_f32_16x16x32_bf16 v[48:51], a[4:7], v[60:63], 0\n\t"
        "ds_read_b128 v[56:59], v105 offset:16384\n\t"
        "v_mfma_f32_16x16x32_bf16 v[52:55], v[60:63], a[8:11], 0\n\t"
        "s_waitcnt lgkmcnt(6)\n\t"
        "v_mfma_f32_16x16x32_bf16 v[32:35], a[12:15], v[64:67], v[32:35]\n\t"
        "v_cvt_pk_bf16_f32 v88, v8, v9\n\t"
        "v_cvt_pk_bf16_f32 v89, v10, v11\n\t"
        "v_mfma_f32_16x16x32_bf16 v[36:39], a[16:19], v[64:67], v[36:39]\n\t"
        "ds_read_b128 v[60:63], v105 offset:18432\n\t"
        "v_cvt_pk_bf16_f32 v90, v12, v13\n\t"
        "v_mfma_f32_16x16x32_bf16 v[40:43], v[64:67], a[20:23], v[40:43]\n\t"
        "s_add_u32 m0, %[ld], 114688\n\t"
        "s_add_u32 %[t0], %[xo], 1792\n\t"
        "s_waitcnt lgkmcnt(6)\n\t"
        "v_mfma_f32_16x16x32_bf16 v[44:47], a[12:15], v[68:71], v[44:47]\n\t"
        "buffer_load_dwordx4 v100, %[sx], %[t0] offen lds\n\t"
        "v_mfma_f32_16x16x32_bf16 v[48:51], a[16:19], v[68:71], v[48:51]\n\t"
        "ds_read_b128 v[64:67], v106 offset:16384\n\t"
        "v_cvt_pk_bf16_f32 v91, v14, v15\n\t"
        "v_mfma_f32_16x16x32_bf16 v[52:55], v[68:71], a[20:23], v[52:55]\n\t"
        "buffer_store_dwordx4 v[88:91], v109, %[sw], %[so] offen sc1\n\t"
        "s_waitcnt lgkmcnt(6)\n\t"
        "v_mfma_f32_16x16x32_bf16 v[32:35], a[24:27], v[72:75], v[32:35]\n\t"
        "v_cvt_pk_bf16_f32 v92, v20, v21\n\t"
        "v_cvt_pk_bf16_f32 v93, v22, v23\n\t"
        "v_mfma_f32_16x16x32_bf16 v[36:39], a[28:31], v[72:75], v[36:39]\n\t"
        "ds_read_b128 v[68:71], v106 offset:18432\n\t"
        "v_cvt_pk_bf16_f32 v94, v24, v25\n\t"
        "v_mfma_f32_16x16x32_bf16 v[40:43], v[72:75], a[32:35], v[40:43]\n\t"
        "s_add_u32 m0, %[ld], 115712\n\t"
        "s_add_u32 %[t0], %[xo], 18176\n\t"
        "s_waitcnt lgkmcnt(6)\n\t"
        "v_mfma_f32_16x16x32_bf16 v[44:47], a[24:27], v[76:79], v[44:47]\n\t"
        "buffer_load_dwordx4 v100, %[sx], %[t0] offen lds\n\t"
        "v_mfma_f32_16x16x32_bf16 v[48:51], a[28:31], v[76:79], v[48:51]\n\t"
        "ds_read_b128 v[72:75], v105 offset:24576\n\t"
        "v_cvt_pk_bf16_f32 v95, v26, v27\n\t"
        "v_mfma_f32_16x16x32_bf16 v[52:55], v[76:79], a[32:35], v[52:55]\n\t"
        "buffer_store_dwordx4 v[92:95], v110, %[sw], %[so] offen sc1\n\t"
        "s_waitcnt lgkmcnt(6)\n\t"
        "v_mfma_f32_16x16x32_bf16 v[32:35], a[36:39], v[80:83], v[32:35]\n\t"
        "v_cvt_pk_bf16_f32 v96, v16, v17\n\t"
        "v_cvt_pk_bf16_f32 v97, v18, v19\n\t"
        "v_mfma_f32_16x16x32_bf16 v[36:39], a[40:43], v[80:83], v[36:39]\n\t"
        "ds_read_b128 v[76:79], v105 offset:26624\n\t"
        "v_cvt_pk_bf16_f32 v98, v28, v29\n\t"
        "v_mfma_f32_16x16x32_bf16 v[40:43], v[80:83], a[44:47], v[40:43]\n\t"
        "s_add_u32 m0, %[ld], 122880\n\t"
        "s_add_u32 %[t0], %[xo], 1920\n\t"
        "s_waitcnt lgkmcnt(6)\n\t"
        "v_mfma_f32_16x16x32_bf16 v[44:47], a[36:39], v[84:87], v[44:47]\n\t"
        "buffer_load_dwordx4 v100, %[sx], %[t0] offen lds\n\t"
        "v_mfma_f32_16x16x32_bf16 v[48:51], a[40:43], v[84:87], v[48:51]\n\t"
        "ds_read_b128 v[80:83], v106 offset:24576\n\t"
        "v_cvt_pk_bf16_f32 v99, v30, v31\n\t"
        "v_mfma_f32_16x16x32_bf16 v[52:55], v[84:87], a[44:47], v[52:55]\n\t"
        "buffer_store_dwordx4 v[96:99], v111, %[sw], %[so] offen sc1\n\t"
        "s_waitcnt lgkmcnt(6)\n\t"
        "v_mfma_f32_16x16x32_bf16 v[32:35], a[48:51], v[56:59], v[32:35]\n\t"
        "s_lshl_b32 %[so], %[cn], 19\n\t"
        "v_mfma_f32_16x16x32_bf16 v[36:39], a[52:55], v[56:59], v[36:39]\n\t"
        "ds_read_b128 v[84:87], v106 offset:26624\n\t"
        "v_mfma_f32_16x16x32_bf16 v[40:43], v[56:59], a[56:59], v[40:43]\n\t"
        "s_add_u32 m0, %[ld], 123904\n\t"
        "s_add_u32 %[t0], %[xo], 18304\n\t"
        "s_waitcnt lgkmcnt(6)\n\t"
        "v_mfma_f32_16x16x32_bf16 v[44:47], a[48:51], v[60:63], v[44:47]\n\t"
        "buffer_load_dwordx4 v100, %[sx], %[t0] offen lds\n\t"
        "v_mfma_f32_16x16x32_bf16 v[48:51], a[52:55], v[60:63], v[48:51]\n\t"
        "v_mfma_f32_16x16x32_bf16 v[52:55], v[60:63], a[56:59], v[52:55]\n\t"
        "s_waitcnt lgkmcnt(5)\n\t"
        "v_mfma_f32_16x16x32_bf16 v[32:35], a[60:63], v[64:67], v[32:35]\n\t"
        "s_waitcnt vmcnt(19) lgkmcnt(0)\n\t"
        "s_barrier\n\t"
        "s_add_u32 %[xo], %[xo], 0x20000\n\t"
        "v_mfma_f32_16x16x32_bf16 v[36:39], a[64:67], v[64:67], v[36:39]\n\t"
        "ds_read_b128 v[56:59], v105 offset:32768\n\t"
        "v_mfma_f32_16x16x32_bf16 v[40:43], v[64:67], a[68:71], v[40:43]\n\t"
        "ds_read_b128 v[60:63], v105 offset:34816\n\t"
        "s_add_u32 m0, %[ld], 0\n\t"
        "s_add_u32 %[t0], %[xo], 0\n\t"
        "v_mfma_f32_16x16x32_bf16 v[44:47], a[60:63], v[68:71], v[44:47]\n\t"
        "buffer_load_dwordx4 v100, %[sx], %[t0] offen lds\n\t"
        "v_mfma_f32_16x16x32_bf16 v[48:51], a[64:67], v[68:71], v[48:51]\n\t"
        "ds_read_b128 v[64:67], v106 offset:32768\n\t"
        "v_mfma_f32_16x16x32_bf16 v[52:55], v[68:71], a[68:71], v[52:55]\n\t"
        "v_mfma_f32_16x16x32_bf16 v[32:35], a[72:75], v[72:75], v[32:35]\n\t"
        "v_mfma_f32_16x16x32_bf16 v[36:39], a[76:79], v[72:75], v[36:39]\n\t"
        "ds_read_b128 v[68:71], v106 offset:34816\n\t"
        "v_mfma_f32_16x16x32_bf16 v[40:43], v[72:75], a[80:83], v[40:43]\n\t"
        "s_add_u32 m0, %[ld], 1024\n\t"
        "s_add_u32 %[t0], %[xo], 16384\n\t"
        "v_mfma_f32_16x16x32_bf16 v[44:47], a[72:75], v[76:79], v[44:47]\n\t"
        "buffer_load_dwordx4 v100, %[sx], %[t0] offen lds\n\t"
        "v_mfma_f32_16x16x32_bf16 v[48:51], a[76:79], v[76:79], v[48:51]\n\t"
        "ds_read_b128 v[72:75], v105 offset:40960\n\t"
        "v_mfma_f32_16x16x32_bf16 v[52:55], v[76:79], a[80:83], v[52:55]\n\t"
        "v_mfma_f32_16x16x32_bf16 v[32:35], a[84:87], v[80:83], v[32:35]\n\t"
        "v_mfma_f32_16x16x32_bf16 v[36:39], a[88:91], v[80:83], v[36:39]\n\t"
        "ds_read_b128 v[76:79], v105 offset:43008\n\t"
        "v_mfma_f32_16x16x32_bf16 v[40:43], v[80:83], a[92:95], v[40:43]\n\t"
        "s_add_u32 m0, %[ld], 8192\n\t"
        "s_add_u32 %[t0], %[xo], 128\n\t"
        "v_mfma_f32_16x16x32_bf16 v[44:47], a[84:87], v[84:87], v[44:47]\n\t"
        "buffer_load_dwordx4 v100, %[sx], %[t0] offen lds\n\t"
        "v_mfma_f32_16x16x32_bf16 v[48:51], a[88:91], v[84:87], v[48:51]\n\t"
        "ds_read_b128 v[80:83], v106 offset:40960\n\t"
        "v_mfma_f32_16x16x32_bf16 v[52:55], v[84:87], a[92:95], v[52:55]\n\t"
        "s_waitcnt lgkmcnt(6)\n\t"
        "v_mfma_f32_16x16x32_bf16 v[32:35], a[96:99], v[56:59], v[32:35]\n\t"
        "v_mfma_f32_16x16x32_bf16 v[36:39], a[100:103], v[56:59], v[36:39]\n\t"
        "ds_read_b128 v[84:87], v106 offset:43008\n\t"
        "v_mfma_f32_16x16x32_bf16 v[40:43], v[56:59], a[104:107], v[40:43]\n\t"
        "s_add_u32 m0, %[ld], 9216\n\t"
        "s_add_u32 %[t0], %[xo], 16512\n\t"
        "s_waitcnt lgkmcnt(6)\n\t"
        "v_mfma_f32_16x16x32_bf16 v[44:47], a[96:99], v[60:63], v[44:47]\n\t"
        "buffer_load_dwordx4 v100, %[sx], %[t0] offen lds\n\t"
        "v_mfma_f32_16x16x32_bf16 v[48:51], a[100:103], v[60:63], v[48:51]\n\t"
        "ds_read_b128 v[56:59], v105 offset:49152\n\t"
        "v_mfma_f32_16x16x32_bf16 v[52:55], v[60:63], a[104:107], v[52:55]\n\t"
        "s_waitcnt lgkmcnt(6)\n\t"
        "v_mfma_f32_16x16x32_bf16 v[32:35], a[108:111], v[64:67], v[32:35]\n\t"
        "v_mfma_f32_16x16x32_bf16 v[36:39], a[112:115], v[64:67], v[36:39]\n\t"
        "ds_read_b128 v[60:63], v105 offset:51200\n\t"
        "v_mfma_f32_16x16x32_bf16 v[40:43], v[64:67], a[116:119], v[40:43]\n\t"
        "s_add_u32 m0, %[ld], 16384\n\t"
        "s_add_u32 %[t0], %[xo], 256\n\t"
        "s_waitcnt lgkmcnt(6)\n\t"
        "v_mfma_f32_16x16x32_bf16 v[44:47], a[108:111], v[68:71], v[44:47]\n\t"
        "buffer_load_dwordx4 v100, %[sx], %[t0] offen lds\n\t"
        "v_mfma_f32_16x16x32_bf16 v[48:51], a[112:115], v[68:71], v[48:51]\n\t"
        "ds_read_b128 v[64:67], v106 offset:49152\n\t"
        "v_mfma_f32_16x16x32_bf16 v[52:55], v[68:71], a[116:119], v[52:55]\n\t"
        "s_waitcnt lgkmcnt(6)\n\t"
        "v_mfma_f32_16x16x32_bf16 v[32:35], a[120:123], v[72:75], v[32:35]\n\t"
        "v_mfma_f32_16x16x32_bf16 v[36:39], a[124:127], v[72:75], v[36:39]\n\t"
        "ds_read_b128 v[68:71], v106 offset:51200\n\t"
        "v_mfma_f32_16x16x32_bf16 v[40:43], v[72:75], a[128:131], v[40:43]\n\t"
        "s_add_u32 m0, %[ld], 17408\n\t"
        "s_add_u32 %[t0], %[xo], 16640\n\t"
        "s_waitcnt lgkmcnt(6)\n\t"
        "v_mfma_f32_16x16x32_bf16 v[44:47], a[120:123], v[76:79], v[44:47]\n\t"
        "buffer_load_dwordx4 v100, %[sx], %[t0] offen lds\n\t"
        "v_mfma_f32_16x16x32_bf16 v[48:51], a[124:127], v[76:79], v[48:51]\n\t"
        "ds_read_b128 v[72:75], v105 offset:57344\n\t"
        "v_mfma_f32_16x16x32_bf16 v[52:55], v[76:79], a[128:131], v[52:55]\n\t"
        "s_waitcnt lgkmcnt(6)\n\t"
        "v_mfma_f32_16x16x32_bf16 v[32:35], a[132:135], v[80:83], v[32:35]\n\t"
        "v_mfma_f32_16x16x32_bf16 v[36:39], a[136:139], v[80:83], v[36:39]\n\t"
        "ds_read_b128 v[76:79], v105 offset:59392\n\t"
        "v_mfma_f32_16x16x32_bf16 v[40:43], v[80:83], a[140:143], v[40:43]\n\t"
        "s_add_u32 m0, %[ld], 24576\n\t"
        "s_add_u32 %[t0], %[xo], 384\n\t"
        "s_waitcnt lgkmcnt(6)\n\t"
        "v_mfma_f32_16x16x32_bf16 v[44:47], a[132:135], v[84:87], v[44:47]\n\t"
        "buffer_load_dwordx4 v100, %[sx], %[t0] offen lds\n\t"
        "v_mfma_f32_16x16x32_bf16 v[48:51], a[136:139], v[84:87], v[48:51]\n\t"
        "ds_read_b128 v[80:83], v106 offset:57344\n\t"
        "v_mfma_f32_16x16x32_bf16 v[52:55], v[84:87], a[140:143], v[52:55]\n\t"
        "s_waitcnt lgkmcnt(6)\n\t"
        "v_mfma_f32_16x16x32_bf16 v[32:35], a[144:147], v[56:59], v[32:35]\n\t"
        "v_mfma_f32_16x16x32_bf16 v[36:39], a[148:151], v[56:59], v[36:39]\n\t"
        "ds_read_b128 v[84:87], v106 offset:59392\n\t"
        "v_mfma_f32_16x16x32_bf16 v[40:43], v[56:59], a[152:155], v[40:43]\n\t"
        "s_add_u32 m0, %[ld], 25600\n\t"
        "s_add_u32 %[t0], %[xo], 16768\n\t"
        "s_waitcnt lgkmcnt(6)\n\t"
        "v_mfma_f32_16x16x32_bf16 v[44:47], a[144:147], v[60:63], v[44:47]\n\t"
        "buffer_load_dwordx4 v100, %[sx], %[t0] offen lds\n\t"
        "v_mfma_f32_16x16x32_bf16 v[48:51], a[148:151], v[60:63], v[48:51]\n\t"
        "v_mfma_f32_16x16x32_bf16 v[52:55], v[60:63], a[152:155], v[52:55]\n\t"
        "s_waitcnt lgkmcnt(5)\n\t"
        "v_mfma_f32_16x16x32_bf16 v[32:35], a[156:159], v[64:67], v[32:35]\n\t"
        "s_waitcnt vmcnt(9) lgkmcnt(0)\n\t"
        "s_barrier\n\t"
        "v_mfma_f32_16x16x32_bf16 v[36:39], a[160:163], v[64:67], v[36:39]\n\t"
        "ds_read_b128 v[56:59], v107 offset:0\n\t"
        "v_mfma_f32_16x16x32_bf16 v[40:43], v[64:67], a[164:167], v[40:43]\n\t"
        "ds_read_b128 v[60:63], v107 offset:2048\n\t"
        "s_add_u32 m0, %[ld], 32768\n\t"
        "s_add_u32 %[t0], %[xo], 512\n\t"
        "v_mfma_f32_16x16x32_bf16 v[44:47], a[156:159], v[68:71], v[44:47]\n\t"
        "buffer_load_dwordx4 v100, %[sx], %[t0] offen lds\n\t"
        "v_mfma_f32_16x16x32_bf16 v[48:51], a[160:163], v[68:71], v[48:51]\n\t"
        "ds_read_b128 v[64:67], v108 offset:0\n\t"
        "v_mfma_f32_16x16x32_bf16 v[52:55], v[68:71], a[164:167], v[52:55]\n\t"
        "v_mfma_f32_16x16x32_bf16 v[32:35], a[168:171], v[72:75], v[32:35]\n\t"
        "v_mfma_f32_16x16x32_bf16 v[36:39], a[172:175], v[72:75], v[36:39]\n\t"
        "ds_read_b128 v[68:71], v108 offset:2048\n\t"
        "v_mov_b32_e32 v119, %[cn]\n\t"
        "buffer_store_dword v119, v113, %[sw], 0 offen sc1\n\t"
        "s_add_u32 %[cn], %[cn], 1\n\t"
        "v_mfma_f32_16x16x32_bf16 v[40:43], v[72:75], a[176:179], v[40:43]\n\t"
        "s_add_u32 m0, %[ld], 33792\n\t"
        "s_add_u32 %[t0], %[xo], 16896\n\t"
        "v_mfma_f32_16x16x32_bf16 v[44:47], a[168:171], v[76:79], v[44:47]\n\t"
        "buffer_load_dwordx4 v100, %[sx], %[t0] offen lds\n\t"
        "v_mfma_f32_16x16x32_bf16 v[48:51], a[172:175], v[76:79], v[48:51]\n\t"
        "ds_read_b128 v[72:75], v107 offset:8192\n\t"
        "v_mfma_f32_16x16x32_bf16 v[52:55], v[76:79], a[176:179], v[52:55]\n\t"
        "v_mfma_f32_16x16x32_bf16 v[32:35], a[180:183], v[80:83], v[32:35]\n\t"
        "v_mfma_f32_16x16x32_bf16 v[36:39], a[184:187], v[80:83], v[36:39]\n\t"
        "ds_read_b128 v[76:79], v107 offset:10240\n\t"
        "v_mfma_f32_16x16x32_bf16 v[40:43], v[80:83], a[188:191], v[40:43]\n\t"
        "s_add_u32 m0, %[ld], 40960\n\t"
        "s_add_u32 %[t0], %[xo], 640\n\t"
        "v_mfma_f32_16x16x32_bf16 v[44:47], a[180:183], v[84:87], v[44:47]\n\t"
        "buffer_load_dwordx4 v100, %[sx], %[t0] offen lds\n\t"
        "v_mfma_f32_16x16x32_bf16 v[48:51], a[184:187], v[84:87], v[48:51]\n\t"
        "ds_read_b128 v[80:83], v108 offset:8192\n\t"
        "v_mfma_f32_16x16x32_bf16 v[52:55], v[84:87], a[188:191], v[52:55]\n\t"
        "s_waitcnt lgkmcnt(6)\n\t"
        "v_mfma_f32_16x16x32_bf16 v[32:35], a[192:195], v[56:59], v[32:35]\n\t"
        "v_mfma_f32_16x16x32_bf16 v[36:39], a[196:199], v[56:59], v[36:39]\n\t"
        "ds_read_b128 v[84:87], v108 offset:10240\n\t"
        "v_mfma_f32_16x16x32_bf16 v[40:43], v[56:59], a[200:203], v[40:43]\n\t"
        "s_add_u32 m0, %[ld], 41984\n\t"
        "s_add_u32 %[t0], %[xo], 17024\n\t"
        "s_waitcnt lgkmcnt(6)\n\t"
        "v_mfma_f32_16x16x32_bf16 v[44:47], a[192:195], v[60:63], v[44:47]\n\t"
        "buffer_load_dwordx4 v100, %[sx], %[t0] offen lds\n\t"
        "v_mfma_f32_16x16x32_bf16 v[48:51], a[196:199], v[60:63], v[48:51]\n\t"
        "ds_read_b128 v[56:59], v107 offset:16384\n\t"
        "v_mfma_f32_16x16x32_bf16 v[52:55], v[60:63], a[200:203], v[52:55]\n\t"
        "s_waitcnt lgkmcnt(6)\n\t"
        "v_mfma_f32_16x16x32_bf16 v[32:35], a[204:207], v[64:67], v[32:35]\n\t"
        "v_mfma_f32_16x16x32_bf16 v[36:39], a[208:211], v[64:67], v[36:39]\n\t"
        "ds_read_b128 v[60:63], v107 offset:18432\n\t"
        "v_mfma_f32_16x16x32_bf16 v[40:43], v[64:67], a[212:215], v[40:43]\n\t"
        "s_add_u32 m0, %[ld], 49152\n\t"
        "s_add_u32 %[t0], %[xo], 768\n\t"
        "s_waitcnt lgkmcnt(6)\n\t"
        "v_mfma_f32_16x16x32_bf16 v[44:47], a[204:207], v[68:71], v[44:47]\n\t"
        "buffer_load_dwordx4 v100, %[sx], %[t0] offen lds\n\t"
        "v_mfma_f32_16x16x32_bf16 v[48:51], a[208:211], v[68:71], v[48:51]\n\t"
        "ds_read_b128 v[64:67], v108 offset:16384\n\t"
        "v_mfma_f32_16x16x32_bf16 v[52:55], v[68:71], a[212:215], v[52:55]\n\t"
        "s_waitcnt lgkmcnt(6)\n\t"
        "v_mfma_f32_16x16x32_bf16 v[32:35], a[216:219], v[72:75], v[32:35]\n\t"
        "v_mfma_f32_16x16x32_bf16 v[36:39], a[220:223], v[72:75], v[36:39]\n\t"
        "ds_read_b128 v[68:71], v108 offset:18432\n\t"
        "v_mfma_f32_16x16x32_bf16 v[40:43], v[72:75], a[224:227], v[40:43]\n\t"
        "s_add_u32 m0, %[ld], 50176\n\t"
        "s_add_u32 %[t0], %[xo], 17152\n\t"
        "s_waitcnt lgkmcnt(6)\n\t"
        "v_mfma_f32_16x16x32_bf16 v[44:47], a[216:219], v[76:79], v[44:47]\n\t"
        "buffer_load_dwordx4 v100, %[sx], %[t0] offen lds\n\t"
        "v_mfma_f32_16x16x32_bf16 v[48:51], a[220:223], v[76:79], v[48:51]\n\t"
        "ds_read_b128 v[72:75], v107 offset:24576\n\t"
        "v_mfma_f32_16x16x32_bf16 v[52:55], v[76:79], a[224:227], v[52:55]\n\t"
        "s_waitcnt lgkmcnt(6)\n\t"
        "v_mfma_f32_16x16x32_bf16 v[32:35], a[228:231], v[80:83], v[32:35]\n\t"
        "v_mfma_f32_16x16x32_bf16 v[36:39], a[232:235], v[80:83], v[36:39]\n\t"
        "ds_read_b128 v[76:79], v107 offset:26624\n\t"
        "v_mfma_f32_16x16x32_bf16 v[40:43], v[80:83], a[236:239], v[40:43]\n\t"
        "s_add_u32 m0, %[ld], 57344\n\t"
        "s_add_u32 %[t0], %[xo], 896\n\t"
        "s_waitcnt lgkmcnt(6)\n\t"
        "v_mfma_f32_16x16x32_bf16 v[44:47], a[228:231], v[84:87], v[44:47]\n\t"
        "buffer_load_dwordx4 v100, %[sx], %[t0] offen lds\n\t"
        "v_mfma_f32_16x16x32_bf16 v[48:51], a[232:235], v[84:87], v[48:51]\n\t"
        "ds_read_b128 v[80:83], v108 offset:24576\n\t"
        "v_mfma_f32_16x16x32_bf16 v[52:55], v[84:87], a[236:239], v[52:55]\n\t"
        "s_waitcnt lgkmcnt(6)\n\t"
        "v_mfma_f32_16x16x32_bf16 v[32:35], a[240:243], v[56:59], v[32:35]\n\t"
        "v_mfma_f32_16x16x32_bf16 v[36:39], a[244:247], v[56:59], v[36:39]\n\t"
        "ds_read_b128 v[84:87], v108 offset:26624\n\t"
        "v_mfma_f32_16x16x32_bf16 v[40:43], v[56:59], a[248:251], v[40:43]\n\t"
        "s_add_u32 m0, %[ld], 58368\n\t"
        "s_add_u32 %[t0], %[xo], 17280\n\t"
        "s_waitcnt lgkmcnt(6)\n\t"
        "v_mfma_f32_16x16x32_bf16 v[44:47], a[240:243], v[60:63], v[44:47]\n\t"
        "buffer_load_dwordx4 v100, %[sx], %[t0] offen lds\n\t"
        "v_mfma_f32_16x16x32_bf16 v[48:51], a[244:247], v[60:63], v[48:51]\n\t"
        "v_mfma_f32_16x16x32_bf16 v[52:55], v[60:63], a[248:251], v[52:55]\n\t"
        "s_waitcnt lgkmcnt(5)\n\t"
        "v_mfma_f32_16x16x32_bf16 v[32:35], a[252:255], v[64:67], v[32:35]\n\t"
        "s_waitcnt vmcnt(17) lgkmcnt(0)\n\t"
        "s_barrier\n\t"
        "v_mfma_f32_16x16x32_bf16 v[36:39], v[128:131], v[64:67], v[36:39]\n\t"
        "ds_read_b128 v[56:59], v107 offset:32768\n\t"
        "s_cmp_eq_u32 %[cn], %[wv]\n\t"
        "s_cselect_b32 %[t0], 0, 0xf0000000\n\t"
        "buffer_load_dword v118, v117, %[sx], %[t0] offen\n\t"
        "v_mfma_f32_16x16x32_bf16 v[40:43], v[64:67], v[132:135], v[40:43]\n\t"
        "ds_read_b128 v[60:63], v107 offset:34816\n\t"
        "s_add_u32 m0, %[ld], 65536\n\t"
        "s_add_u32 %[t0], %[xo], 1024\n\t"
        "v_mfma_f32_16x16x32_bf16 v[44:47], a[252:255], v[68:71], v[44:47]\n\t"
        "buffer_load_dwordx4 v100, %[sx], %[t0] offen lds\n\t"
        "v_mfma_f32_16x16x32_bf16 v[48:51], v[128:131], v[68:71], v[48:51]\n\t"
        "ds_read_b128 v[64:67], v108 offset:32768\n\t"
        "v_mfma_f32_16x16x32_bf16 v[52:55], v[68:71], v[132:135], v[52:55]\n\t"
        "v_mfma_f32_16x16x32_bf16 v[32:35], v[136:139], v[72:75], v[32:35]\n\t"
        "v_mfma_f32_16x16x32_bf16 v[36:39], v[140:143], v[72:75], v[36:39]\n\t"
        "ds_read_b128 v[68:71], v108 offset:34816\n\t"
        "v_mfma_f32_16x16x32_bf16 v[40:43], v[72:75], v[144:147], v[40:43]\n\t"
        "s_add_u32 m0, %[ld], 66560\n\t"
        "s_add_u32 %[t0], %[xo], 17408\n\t"
        "v_mfma_f32_16x16x32_bf16 v[44:47], v[136:139], v[76:79], v[44:47]\n\t"
        "buffer_load_dwordx4 v100, %[sx], %[t0] offen lds\n\t"
        "v_mfma_f32_16x16x32_bf16 v[48:51], v[140:143], v[76:79], v[48:51]\n\t"
        "ds_read_b128 v[72:75], v107 offset:40960\n\t"
        "v_mfma_f32_16x16x32_bf16 v[52:55], v[76:79], v[144:147], v[52:55]\n\t"
        "v_mfma_f32_16x16x32_bf16 v[32:35], v[148:151], v[80:83], v[32:35]\n\t"
        "v_mfma_f32_16x16x32_bf16 v[36:39], v[152:155], v[80:83], v[36:39]\n\t"
        "ds_read_b128 v[76:79], v107 offset:43008\n\t"
        "v_mfma_f32_16x16x32_bf16 v[40:43], v[80:83], v[156:159], v[40:43]\n\t"
        "s_add_u32 m0, %[ld], 73728\n\t"
        "s_add_u32 %[t0], %[xo], 1152\n\t"
        "v_mfma_f32_16x16x32_bf16 v[44:47], v[148:151], v[84:87], v[44:47]\n\t"
        "buffer_load_dwordx4 v100, %[sx], %[t0] offen lds\n\t"
        "v_mfma_f32_16x16x32_bf16 v[48:51], v[152:155], v[84:87], v[48:51]\n\t"
        "ds_read_b128 v[80:83], v108 offset:40960\n\t"
        "v_mfma_f32_16x16x32_bf16 v[52:55], v[84:87], v[156:159], v[52:55]\n\t"
        "s_waitcnt lgkmcnt(6)\n\t"
        "v_mfma_f32_16x16x32_bf16 v[32:35], v[160:163], v[56:59], v[32:35]\n\t"
        "v_mfma_f32_16x16x32_bf16 v[36:39], v[164:167], v[56:59], v[36:39]\n\t"
        "ds_read_b128 v[84:87], v108 offset:43008\n\t"
        "v_mfma_f32_16x16x32_bf16 v[40:43], v[56:59], v[168:171], v[40:43]\n\t"
        "s_add_u32 m0, %[ld], 74752\n\t"
        "s_add_u32 %[t0], %[xo], 17536\n\t"
        "s_waitcnt lgkmcnt(6)\n\t"
        "v_mfma_f32_16x16x32_bf16 v[44:47], v[160:163], v[60:63], v[44:47]\n\t"
        "buffer_load_dwordx4 v100, %[sx], %[t0] offen lds\n\t"
        "v_mfma_f32_16x16x32_bf16 v[48:51], v[164:167], v[60:63], v[48:51]\n\t"
        "ds_read_b128 v[56:59], v107 offset:49152\n\t"
        "v_mfma_f32_16x16x32_bf16 v[52:55], v[60:63], v[168:171], v[52:55]\n\t"
        "s_waitcnt lgkmcnt(6)\n\t"
        "v_mfma_f32_16x16x32_bf16 v[32:35], v[172:175], v[64:67], v[32:35]\n\t"
        "v_mfma_f32_16x16x32_bf16 v[36:39], v[176:179], v[64:67], v[36:39]\n\t"
        "ds_read_b128 v[60:63], v107 offset:51200\n\t"
        "v_mfma_f32_16x16x32_bf16 v[40:43], v[64:67], v[180:183], v[40:43]\n\t"
        "s_add_u32 m0, %[ld], 81920\n\t"
        "s_add_u32 %[t0], %[xo], 1280\n\t"
        "s_waitcnt lgkmcnt(6)\n\t"
        "v_mfma_f32_16x16x32_bf16 v[44:47], v[172:175], v[68:71], v[44:47]\n\t"
        "buffer_load_dwordx4 v100, %[sx], %[t0] offen lds\n\t"
        "v_mfma_f32_16x16x32_bf16 v[48:51], v[176:179], v[68:71], v[48:51]\n\t"
        "ds_read_b128 v[64:67], v108 offset:49152\n\t"
        "v_mfma_f32_16x16x32_bf16 v[52:55], v[68:71], v[180:183], v[52:55]\n\t"
        "s_waitcnt lgkmcnt(6)\n\t"
        "v_mfma_f32_16x16x32_bf16 v[32:35], v[184:187], v[72:75], v[32:35]\n\t"
        "v_mfma_f32_16x16x32_bf16 v[36:39], v[188:191], v[72:75], v[36:39]\n\t"
        "ds_read_b128 v[68:71], v108 offset:51200\n\t"
        "v_mfma_f32_16x16x32_bf16 v[40:43], v[72:75], v[192:195], v[40:43]\n\t"
        "s_add_u32 m0, %[ld], 82944\n\t"
        "s_add_u32 %[t0], %[xo], 17664\n\t"
        "s_waitcnt lgkmcnt(6)\n\t"
        "v_mfma_f32_16x16x32_bf16 v[44:47], v[184:187], v[76:79], v[44:47]\n\t"
        "buffer_load_dwordx4 v100, %[sx], %[t0] offen lds\n\t"
        "v_mfma_f32_16x16x32_bf16 v[48:51], v[188:191], v[76:79], v[48:51]\n\t"
        "ds_read_b128 v[72:75], v107 offset:57344\n\t"
        "v_mfma_f32_16x16x32_bf16 v[52:55], v[76:79], v[192:195], v[52:55]\n\t"
        "s_waitcnt lgkmcnt(6)\n\t"
        "v_mfma_f32_16x16x32_bf16 v[32:35], v[196:199], v[80:83], v[32:35]\n\t"
        "v_mfma_f32_16x16x32_bf16 v[36:39], v[200:203], v[80:83], v[36:39]\n\t"
        "ds_read_b128 v[76:79], v107 offset:59392\n\t"
        "v_mfma_f32_16x16x32_bf16 v[40:43], v[80:83], v[204:207], v[40:43]\n\t"
        "s_add_u32 m0, %[ld], 90112\n\t"
        "s_add_u32 %[t0], %[xo], 1408\n\t"
        "s_waitcnt lgkmcnt(6)\n\t"
        "v_mfma_f32_16x16x32_bf16 v[44:47], v[196:199], v[84:87], v[44:47]\n\t"
        "buffer_load_dwordx4 v100, %[sx], %[t0] offen lds\n\t"
        "v_mfma_f32_16x16x32_bf16 v[48:51], v[200:203], v[84:87], v[48:51]\n\t"
        "ds_read_b128 v[80:83], v108 offset:57344\n\t"
        "v_mfma_f32_16x16x32_bf16 v[52:55], v[84:87], v[204:207], v[52:55]\n\t"
        "s_waitcnt lgkmcnt(6)\n\t"
        "v_mfma_f32_16x16x32_bf16 v[32:35], v[208:211], v[56:59], v[32:35]\n\t"
        "v_mfma_f32_16x16x32_bf16 v[36:39], v[212:215], v[56:59], v[36:39]\n\t"
        "ds_read_b128 v[84:87], v108 offset:59392\n\t"
        "v_mfma_f32_16x16x32_bf16 v[40:43], v[56:59], v[216:219], v[40:43]\n\t"
        "s_add_u32 m0, %[ld], 91136\n\t"
        "s_add_u32 %[t0], %[xo], 17792\n\t"
        "s_waitcnt lgkmcnt(6)\n\t"
        "v_mfma_f32_16x16x32_bf16 v[44:47], v[208:211], v[60:63], v[44:47]\n\t"
        "buffer_load_dwordx4 v100, %[sx], %[t0] offen lds\n\t"
        "v_mfma_f32_16x16x32_bf16 v[48:51], v[212:215], v[60:63], v[48:51]\n\t"
        "v_mfma_f32_16x16x32_bf16 v[52:55], v[60:63], v[216:219], v[52:55]\n\t"
        "s_waitcnt lgkmcnt(5)\n\t"
        "v_mfma_f32_16x16x32_bf16 v[32:35], v[220:223], v[64:67], v[32:35]\n\t"
        "s_waitcnt vmcnt(18) lgkmcnt(0)\n\t"
        "s_barrier\n\t"
        "v_mfma_f32_16x16x32_bf16 v[36:39], v[224:227], v[64:67], v[36:39]\n\t"
        "ds_read_b128 v[56:59], v105 offset:0\n\t"
        "v_mfma_f32_16x16x32_bf16 v[40:43], v[64:67], v[228:231], v[40:43]\n\t"
        "ds_read_b128 v[60:63], v105 offset:2048\n\t"
        "s_add_u32 m0, %[ld], 98304\n\t"
        "s_add_u32 %[t0], %[xo], 1536\n\t"
        "v_mfma_f32_16x16x32_bf16 v[44:47], v[220:223], v[68:71], v[44:47]\n\t"
        "buffer_load_dwordx4 v100, %[sx], %[t0] offen lds\n\t"
        "v_mfma_f32_16x16x32_bf16 v[48:51], v[224:227], v[68:71], v[48:51]\n\t"
        "ds_read_b128 v[64:67], v106 offset:0\n\t"
        "v_mfma_f32_16x16x32_bf16 v[52:55], v[68:71], v[228:231], v[52:55]\n\t"
        "v_mfma_f32_16x16x32_bf16 v[32:35], v[232:235], v[72:75], v[32:35]\n\t"
        "v_mfma_f32_16x16x32_bf16 v[36:39], v[236:239], v[72:75], v[36:39]\n\t"
        "ds_read_b128 v[68:71], v106 offset:2048\n\t"
        "v_mfma_f32_16x16x32_bf16 v[40:43], v[72:75], v[240:243], v[40:43]\n\t"
        "s_add_u32 m0, %[ld], 99328\n\t"
        "s_add_u32 %[t0], %[xo], 17920\n\t"
        "v_mfma_f32_16x16x32_bf16 v[44:47], v[232:235], v[76:79], v[44:47]\n\t"
        "buffer_load_dwordx4 v100, %[sx], %[t0] offen lds\n\t"
        "v_mfma_f32_16x16x32_bf16 v[48:51], v[236:239], v[76:79], v[48:51]\n\t"
        "ds_read_b128 v[72:75], v105 offset:8192\n\t"
        "v_mfma_f32_16x16x32_bf16 v[52:55], v[76:79], v[240:243], v[52:55]\n\t"
        "v_mfma_f32_16x16x32_bf16 v[32:35], v[244:247], v[80:83], v[32:35]\n\t"
        "v_mfma_f32_16x16x32_bf16 v[36:39], v[248:251], v[80:83], v[36:39]\n\t"
        "ds_read_b128 v[76:79], v105 offset:10240\n\t"
        "v_mfma_f32_16x16x32_bf16 v[40:43], v[80:83], v[252:255], v[40:43]\n\t"
        "s_add_u32 m0, %[ld], 106496\n\t"
        "s_add_u32 %[t0], %[xo], 1664\n\t"
        "v_mfma_f32_16x16x32_bf16 v[44:47], v[244:247], v[84:87], v[44:47]\n\t"
        "buffer_load_dwordx4 v100, %[sx], %[t0] offen lds\n\t"
        "v_mfma_f32_16x16x32_bf16 v[48:51], v[248:251], v[84:87], v[48:51]\n\t"
        "ds_read_b128 v[80:83], v106 offset:8192\n\t"
        "v_mfma_f32_16x16x32_bf16 v[52:55], v[84:87], v[252:255], v[52:55]\n\t"
        "s_cmp_lt_u32 %[cn], 32\n\t"
        "s_cbranch_scc1 LG_loop%=\n\t"
        "s_nop 7\n\t"
        "v_cvt_pk_bf16_f32 v88, v32, v33\n\t"
        "v_cvt_pk_bf16_f32 v89, v34, v35\n\t"
        "v_cvt_pk_bf16_f32 v90, v36, v37\n\t"
        "v_cvt_pk_bf16_f32 v91, v38, v39\n\t"
        "buffer_store_dwordx4 v[88:91], v109, %[sw], %[so] offen sc1\n\t"
        "v_cvt_pk_bf16_f32 v92, v44, v45\n\t"
        "v_cvt_pk_bf16_f32 v93, v46, v47\n\t"
        "v_cvt_pk_bf16_f32 v94, v48, v49\n\t"
        "v_cvt_pk_bf16_f32 v95, v50, v51\n\t"
        "buffer_store_dwordx4 v[92:95], v110, %[sw], %[so] offen sc1\n\t"
        "v_cvt_pk_bf16_f32 v96, v40, v41\n\t"
        "v_cvt_pk_bf16_f32 v97, v42, v43\n\t"
        "v_cvt_pk_bf16_f32 v98, v52, v53\n\t"
        "v_cvt_pk_bf16_f32 v99, v54, v55\n\t"
        "buffer_store_dwordx4 v[96:99], v111, %[sw], %[so] offen sc1\n\t"
        "s_waitcnt vmcnt(0) lgkmcnt(0)\n\t"
        "v_mov_b32_e32 v119, %[cn]\n\t"
        "buffer_store_dword v119, v113, %[sw], 0 offen sc1\n\t"
        "s_barrier\n\t"
        "s_branch LG_end%=\n\t"
        "LG_first%=:\n\t"
        "s_waitcnt vmcnt(0)\n\t"
        "v_mov_b32_e32 v119, %[cn]\n\t"
        "buffer_store_dword v119, v113, %[sw], 0 offen sc1\n\t"
        "s_branch LG_back%=\n\t"
        "LG_end%=:\n\t"
        ".Lk_end_G:\n\t"
        : [t0] "=&s"(t0), [xo] "=&s"(xo), [so] "=&s"(so), [cn] "=&s"(cn)
        : [sx] "s"(sx), [sq] "s"(sq), [sv] "s"(sv), [sw] "s"(sw), [ld] "s"(lb + w * 2048), [wv] "s"(2 * w + 2),
          "{v117}"((w * 8192 + (h * 4 + c) * 64 + l) * 128),
          "{v100}"((16 * w + (l >> 3)) * 2048 + (((l & 7) ^ (l >> 3)) << 4)), "{v101}"((8 * w + (l >> 3)) * 8192 + h * 256 + (c & 1) * 128 + (l & 7) * 16),
          "{v112}"((16 * w + (l >> 2)) * 8192 + h * 256 + cv * 64 + (l & 3) * 16), "{v102}"(lb + 131072 + w * 1024 + ((l * 16) ^ ((l & 16) << 2))), "{v116}"(lb + 131072 + w * 1024 + ((l * 16) ^ (l & 32))),
          "{v103}"(lr + rr * 128 + ((tn ^ (n >> 3)) * 64) + (n & 3) * 16), "{v104}"(lr + 8192 + rr * 64 + ((tn ^ (g & 1)) * 32) + (n & 3) * 8), "{v105}"(lx), "{v106}"(lx ^ 64u), "{v107}"(lx + 65536), "{v108}"((lx ^ 64u) + 65536),
          "{v109}"(aq), "{v110}"(aq + 4096), "{v111}"(W_VF + (tm * 32 + h) * 8192 + (2 * cv + tn) * 1024 + l * 16), "{v113}"(l == 0 ? F_KVQ + h * 1024 + (c * 4 + w) * 64 : OOB)
        : "memory", "scc", "m0", "v8", "v9", "v10", "v11", "v12", "v13", "v14", "v15", "v16", "v17", "v18", "v19", "v20", "v21", "v22", "v23", "v24", "v25", "v26", "v27", "v28", "v29", "v30", "v31", "v32", "v33", "v34", "v35", "v36", "v37", "v38", "v39", "v40", "v41", "v42", "v43", "v44", "v45", "v46", "v47", "v48", "v49", "v50", "v51", "v52", "v53", "v54", "v55", "v56", "v57", "v58", "v59", "v60", "v61", "v62", "v63", "v64", "v65", "v66", "v67", "v68", "v69", "v70", "v71", "v72", "v73", "v74", "v75", "v76", "v77", "v78", "v79", "v80", "v81", "v82", "v83", "v84", "v85", "v86", "v87", "v88", "v89", "v90", "v91", "v92", "v93", "v94", "v95", "v96", "v97", "v98", "v99", "v118", "v119", "v120", "v121", "v122", "v123", "v124", "v125", "v126", "v127", "v128", "v129", "v130", "v131", "v132", "v133", "v134", "v135", "v136", "v137", "v138", "v139", "v140", "v141", "v142", "v143", "v144", "v145", "v146", "v147", "v148", "v149", "v150", "v151", "v152", "v153", "v154", "v155", "v156", "v157", "v158", "v159", "v160", "v161", "v162", "v163", "v164", "v165", "v166", "v167", "v168", "v169", "v170", "v171", "v172", "v173", "v174", "v175", "v176", "v177", "v178", "v179", "v180", "v181", "v182", "v183", "v184", "v185", "v186", "v187", "v188", "v189", "v190", "v191", "v192", "v193", "v194", "v195", "v196", "v197", "v198", "v199", "v200", "v201", "v202", "v203", "v204", "v205", "v206", "v207", "v208", "v209", "v210", "v211", "v212", "v213", "v214", "v215", "v216", "v217", "v218", "v219", "v220", "v221", "v222", "v223", "v224", "v225", "v226", "v227", "v228", "v229", "v230", "v231", "v232", "v233", "v234", "v235", "v236", "v237", "v238", "v239", "v240", "v241", "v242", "v243", "v244", "v245", "v246", "v247", "v248", "v249", "v250", "v251", "v252", "v253", "v254", "v255", "a0", "a1", "a2", "a3", "a4", "a5", "a6", "a7", "a8", "a9", "a10", "a11", "a12", "a13", "a14", "a15", "a16", "a17", "a18", "a19", "a20", "a21", "a22", "a23", "a24", "a25", "a26", "a27", "a28", "a29", "a30", "a31", "a32", "a33", "a34", "a35", "a36", "a37", "a38", "a39", "a40", "a41", "a42", "a43", "a44", "a45", "a46", "a47", "a48", "a49", "a50", "a51", "a52", "a53", "a54", "a55", "a56", "a57", "a58", "a59", "a60", "a61", "a62", "a63", "a64", "a65", "a66", "a67", "a68", "a69", "a70", "a71", "a72", "a73", "a74", "a75", "a76", "a77", "a78", "a79", "a80", "a81", "a82", "a83", "a84", "a85", "a86", "a87", "a88", "a89", "a90", "a91", "a92", "a93", "a94", "a95", "a96", "a97", "a98", "a99", "a100", "a101", "a102", "a103", "a104", "a105", "a106", "a107", "a108", "a109", "a110", "a111", "a112", "a113", "a114", "a115", "a116", "a117", "a118", "a119", "a120", "a121", "a122", "a123", "a124", "a125", "a126", "a127", "a128", "a129", "a130", "a131", "a132", "a133", "a134", "a135", "a136", "a137", "a138", "a139", "a140", "a141", "a142", "a143", "a144", "a145", "a146", "a147", "a148", "a149", "a150", "a151", "a152", "a153", "a154", "a155", "a156", "a157", "a158", "a159", "a160", "a161", "a162", "a163", "a164", "a165", "a166", "a167", "a168", "a169", "a170", "a171", "a172", "a173", "a174", "a175", "a176", "a177", "a178", "a179", "a180", "a181", "a182", "a183", "a184", "a185", "a186", "a187", "a188", "a189", "a190", "a191", "a192", "a193", "a194", "a195", "a196", "a197", "a198", "a199", "a200", "a201", "a202", "a203", "a204", "a205", "a206", "a207", "a208", "a209", "a210", "a211", "a212", "a213", "a214", "a215", "a216", "a217", "a218", "a219", "a220", "a221", "a222", "a223", "a224", "a225", "a226", "a227", "a228", "a229", "a230", "a231", "a232", "a233", "a234", "a235", "a236", "a237", "a238", "a239", "a240", "a241", "a242", "a243", "a244", "a245", "a246", "a247", "a248", "a249", "a250", "a251", "a252", "a253", "a254", "a255");
}

__device__ __forceinline__ void out_block(const Params& p, unsigned char* lds, const int wv, const int rp, const int ro, const int nt) {
    const int w = wv, l = otid() & 63, n = l & 15, g = l >> 4;
    const unsigned lb = __builtin_amdgcn_readfirstlane((unsigned)(size_t)lds), stg = lb + 131072 + w * 8192;
    const v4u so = mk_srd(p.Wo, 1024 * 4096 * 2), sw = mk_srd(p.ws, W_TOTAL), su = mk_srd(p.out, S * D * 2);
    const unsigned lr = stg + (4 * g + (n >> 2)) * 128 + ((2 * (g & 1) + (n >> 3)) << 5) + (n & 3) * 8;
    unsigned t0, t1, tl = rp, pg, cu, nx, sp, re;
    asm volatile(
        "s_nop 4\n\t"
        "ds_write_b128 v111, a[0:3] offset:0\n\t"
        "ds_write_b128 v111, a[4:7] offset:1024\n\t"
        "ds_write_b128 v111, a[8:11] offset:2048\n\t"
        "ds_write_b128 v111, a[12:15] offset:3072\n\t"
        "ds_write_b128 v111, a[16:19] offset:4096\n\t"
        "ds_write_b128 v111, a[20:23] offset:5120\n\t"
        "ds_write_b128 v111, a[24:27] offset:6144\n\t"
        "ds_write_b128 v111, a[28:31] offset:7168\n\t"
        "s_add_u32 %[t1], %[ro], 7\n\t"
        "s_and_b32 %[t1], %[t1], 7\n\t"
        "s_lshl_b32 %[t1], %[t1], 18\n\t"
        "s_add_u32 %[t0], %[t1], 131072\n\t"
        "buffer_load_dwordx4 v[232:235], v110, %[so], %[t0] offen\n\t"
        "ds_read_b64_tr_b16 a[0:1], v112 offset:0\n\t"
        "ds_read_b64_tr_b16 a[2:3], v112 offset:2048\n\t"
        "ds_read_b64_tr_b16 a[12:13], v112 offset:4096\n\t"
        "ds_read_b64_tr_b16 a[14:15], v112 offset:6144\n\t"
        "s_add_u32 %[t0], %[t1], 147456\n\t"
        "buffer_load_dwordx4 v[236:239], v110, %[so], %[t0] offen\n\t"
        "s_waitcnt lgkmcnt(8)\n\t"
        "ds_read_b64_tr_b16 a[4:5], v113 offset:0\n\t"
        "ds_read_b64_tr_b16 a[6:7], v113 offset:2048\n\t"
        "ds_read_b64_tr_b16 a[16:17], v113 offset:4096\n\t"
        "ds_read_b64_tr_b16 a[18:19], v113 offset:6144\n\t"
        "s_add_u32 %[t0], %[t1], 163840\n\t"
        "buffer_load_dwordx4 v[240:243], v110, %[so], %[t0] offen\n\t"
        "s_waitcnt lgkmcnt(8)\n\t"
        "ds_read_b64_tr_b16 a[8:9], v114 offset:0\n\t"
        "ds_read_b64_tr_b16 a[10:11], v114 offset:2048\n\t"
        "ds_read_b64_tr_b16 a[20:21], v114 offset:4096\n\t"
        "ds_read_b64_tr_b16 a[22:23], v114 offset:6144\n\t"
        "s_add_u32 %[t0], %[t1], 180224\n\t"
        "buffer_load_dwordx4 v[244:247], v110, %[so], %[t0] offen\n\t"
        "s_waitcnt lgkmcnt(8)\n\t"
        "ds_read_b64_tr_b16 v[80:81], v115 offset:0\n\t"
        "ds_read_b64_tr_b16 v[82:83], v115 offset:2048\n\t"
        "ds_read_b64_tr_b16 v[84:85], v115 offset:4096\n\t"
        "ds_read_b64_tr_b16 v[86:87], v115 offset:6144\n\t"
        "s_add_u32 %[t0], %[t1], 196608\n\t"
        "buffer_load_dwordx4 v[248:251], v110, %[so], %[t0] offen\n\t"
        "s_waitcnt lgkmcnt(0)\n\t"
        "ds_write_b128 v116, v[80:83] offset:0\n\t"
        "ds_write_b128 v116, v[84:87] offset:1024\n\t"
        "ds_write_b128 v111, a[32:35] offset:0\n\t"
        "ds_write_b128 v111, a[36:39] offset:1024\n\t"
        "ds_write_b128 v111, a[40:43] offset:2048\n\t"
        "ds_write_b128 v111, a[44:47] offset:3072\n\t"
        "ds_write_b128 v111, a[48:51] offset:4096\n\t"
        "ds_write_b128 v111, a[52:55] offset:5120\n\t"
        "ds_write_b128 v111, a[56:59] offset:6144\n\t"
        "ds_write_b128 v111, a[60:63] offset:7168\n\t"
        "s_add_u32 %[t0], %[t1], 212992\n\t"
        "buffer_load_dwordx4 v[252:255], v110, %[so], %[t0] offen\n\t"
        "ds_read_b64_tr_b16 a[24:25], v112 offset:0\n\t"
        "ds_read_b64_tr_b16 a[26:27], v112 offset:2048\n\t"
        "ds_read_b64_tr_b16 a[36:37], v112 offset:4096\n\t"
        "ds_read_b64_tr_b16 a[38:39], v112 offset:6144\n\t"
        "s_add_u32 %[t0], %[t1], 229376\n\t"
        "buffer_load_dwordx4 v[24:27], v110, %[so], %[t0] offen\n\t"
        "s_waitcnt lgkmcnt(8)\n\t"
        "ds_read_b64_tr_b16 a[28:29], v113 offset:0\n\t"
        "ds_read_b64_tr_b16 a[30:31], v113 offset:2048\n\t"
        "ds_read_b64_tr_b16 a[40:41], v113 offset:4096\n\t"
        "ds_read_b64_tr_b16 a[42:43], v113 offset:6144\n\t"
        "s_add_u32 %[t0], %[t1], 245760\n\t"
        "buffer_load_dwordx4 v[28:31], v110, %[so], %[t0] offen\n\t"
        "s_waitcnt lgkmcnt(8)\n\t"
        "ds_read_b64_tr_b16 a[32:33], v114 offset:0\n\t"
        "ds_read_b64_tr_b16 a[34:35], v114 offset:2048\n\t"
        "ds_read_b64_tr_b16 a[44:45], v114 offset:4096\n\t"
        "ds_read_b64_tr_b16 a[46:47], v114 offset:6144\n\t"
        "s_add_u32 %[t1], %[ro], 7\n\t"
        "s_and_b32 %[t1], %[t1], 7\n\t"
        "s_lshl_b32 %[t1], %[t1], 18\n\t"
        "s_add_u32 %[t0], %[t1], 0\n\t"
        "buffer_load_dwordx4 v[208:211], v110, %[so], %[t0] offen\n\t"
        "s_waitcnt lgkmcnt(8)\n\t"
        "ds_read_b64_tr_b16 v[80:81], v115 offset:0\n\t"
        "ds_read_b64_tr_b16 v[82:83], v115 offset:2048\n\t"
        "ds_read_b64_tr_b16 v[84:85], v115 offset:4096\n\t"
        "ds_read_b64_tr_b16 v[86:87], v115 offset:6144\n\t"
        "s_add_u32 %[t0], %[t1], 16384\n\t"
        "buffer_load_dwordx4 v[212:215], v110, %[so], %[t0] offen\n\t"
        "s_waitcnt lgkmcnt(0)\n\t"
        "ds_write_b128 v116, v[80:83] offset:2048\n\t"
        "ds_write_b128 v116, v[84:87] offset:3072\n\t"
        "ds_write_b128 v111, a[64:67] offset:0\n\t"
        "ds_write_b128 v111, a[68:71] offset:1024\n\t"
        "ds_write_b128 v111, a[72:75] offset:2048\n\t"
        "ds_write_b128 v111, a[76:79] offset:3072\n\t"
        "ds_write_b128 v111, a[80:83] offset:4096\n\t"
        "ds_write_b128 v111, a[84:87] offset:5120\n\t"
        "ds_write_b128 v111, a[88:91] offset:6144\n\t"
        "ds_write_b128 v111, a[92:95] offset:7168\n\t"
        "s_add_u32 %[t0], %[t1], 32768\n\t"
        "buffer_load_dwordx4 v[216:219], v110, %[so], %[t0] offen\n\t"
        "ds_read_b64_tr_b16 a[48:49], v112 offset:0\n\t"
        "ds_read_b64_tr_b16 a[50:51], v112 offset:2048\n\t"
        "ds_read_b64_tr_b16 a[60:61], v112 offset:4096\n\t"
        "ds_read_b64_tr_b16 a[62:63], v112 offset:6144\n\t"
        "s_add_u32 %[t0], %[t1], 49152\n\t"
        "buffer_load_dwordx4 v[220:223], v110, %[so], %[t0] offen\n\t"
        "s_waitcnt lgkmcnt(8)\n\t"
        "ds_read_b64_tr_b16 a[52:53], v113 offset:0\n\t"
        "ds_read_b64_tr_b16 a[54:55], v113 offset:2048\n\t"
        "ds_read_b64_tr_b16 a[64:65], v113 offset:4096\n\t"
        "ds_read_b64_tr_b16 a[66:67], v113 offset:6144\n\t"
        "s_add_u32 %[t0], %[t1], 65536\n\t"
        "buffer_load_dwordx4 v[224:227], v110, %[so], %[t0] offen\n\t"
        "s_waitcnt lgkmcnt(8)\n\t"
        "ds_read_b64_tr_b16 a[56:57], v114 offset:0\n\t"
        "ds_read_b64_tr_b16 a[58:59], v114 offset:2048\n\t"
        "ds_read_b64_tr_b16 a[68:69], v114 offset:4096\n\t"
        "ds_read_b64_tr_b16 a[70:71], v114 offset:6144\n\t"
        "s_add_u32 %[t0], %[t1], 81920\n\t"
        "buffer_load_dwordx4 v[228:231], v110, %[so], %[t0] offen\n\t"
        "s_waitcnt lgkmcnt(8)\n\t"
        "ds_read_b64_tr_b16 v[80:81], v115 offset:0\n\t"
        "ds_read_b64_tr_b16 v[82:83], v115 offset:2048\n\t"
        "ds_read_b64_tr_b16 v[84:85], v115 offset:4096\n\t"
        "ds_read_b64_tr_b16 v[86:87], v115 offset:6144\n\t"
        "s_add_u32 %[t0], %[t1], 98304\n\t"
        "buffer_load_dwordx4 v[32:35], v110, %[so], %[t0] offen\n\t"
        "s_waitcnt lgkmcnt(0)\n\t"
        "ds_write_b128 v116, v[80:83] offset:4096\n\t"
        "ds_write_b128 v116, v[84:87] offset:5120\n\t"
        "ds_write_b128 v111, a[96:99] offset:0\n\t"
        "ds_write_b128 v111, a[100:103] offset:1024\n\t"
        "ds_write_b128 v111, a[104:107] offset:2048\n\t"
        "ds_write_b128 v111, a[108:111] offset:3072\n\t"
        "ds_write_b128 v111, a[112:115] offset:4096\n\t"
        "ds_write_b128 v111, a[116:119] offset:5120\n\t"
        "ds_write_b128 v111, a[120:123] offset:6144\n\t"
        "ds_write_b128 v111, a[124:127] offset:7168\n\t"
        "s_add_u32 %[t0], %[t1], 114688\n\t"
        "buffer_load_dwordx4 v[36:39], v110, %[so], %[t0] offen\n\t"
        "ds_read_b64_tr_b16 a[72:73], v112 offset:0\n\t"
        "ds_read_b64_tr_b16 a[74:75], v112 offset:2048\n\t"
        "ds_read_b64_tr_b16 a[84:85], v112 offset:4096\n\t"
        "ds_read_b64_tr_b16 a[86:87], v112 offset:6144\n\t"
        "s_add_u32 %[t1], %[ro], 6\n\t"
        "s_and_b32 %[t1], %[t1], 7\n\t"
        "s_lshl_b32 %[t1], %[t1], 18\n\t"
        "s_add_u32 %[t0], %[t1], 131072\n\t"
        "buffer_load_dwordx4 v[184:187], v110, %[so], %[t0] offen\n\t"
        "s_waitcnt lgkmcnt(8)\n\t"
        "ds_read_b64_tr_b16 a[76:77], v113 offset:0\n\t"
        "ds_read_b64_tr_b16 a[78:79], v113 offset:2048\n\t"
        "ds_read_b64_tr_b16 a[88:89], v113 offset:4096\n\t"
        "ds_read_b64_tr_b16 a[90:91], v113 offset:6144\n\t"
        "s_add_u32 %[t0], %[t1], 147456\n\t"
        "buffer_load_dwordx4 v[188:191], v110, %[so], %[t0] offen\n\t"
        "s_waitcnt lgkmcnt(8)\n\t"
        "ds_read_b64_tr_b16 a[80:81], v114 offset:0\n\t"
        "ds_read_b64_tr_b16 a[82:83], v114 offset:2048\n\t"
        "ds_read_b64_tr_b16 a[92:93], v114 offset:4096\n\t"
        "ds_read_b64_tr_b16 a[94:95], v114 offset:6144\n\t"
        "s_add_u32 %[t0], %[t1], 163840\n\t"
        "buffer_load_dwordx4 v[192:195], v110, %[so], %[t0] offen\n\t"
        "s_waitcnt lgkmcnt(8)\n\t"
        "ds_read_b64_tr_b16 v[80:81], v115 offset:0\n\t"
        "ds_read_b64_tr_b16 v[82:83], v115 offset:2048\n\t"
        "ds_read_b64_tr_b16 v[84:85], v115 offset:4096\n\t"
        "ds_read_b64_tr_b16 v[86:87], v115 offset:6144\n\t"
        "s_add_u32 %[t0], %[t1], 180224\n\t"
        "buffer_load_dwordx4 v[196:199], v110, %[so], %[t0] offen\n\t"
        "s_waitcnt lgkmcnt(0)\n\t"
        "ds_write_b128 v116, v[80:83] offset:6144\n\t"
        "ds_write_b128 v116, v[84:87] offset:7168\n\t"
        "ds_write_b128 v111, a[128:131] offset:0\n\t"
        "ds_write_b128 v111, a[132:135] offset:1024\n\t"
        "ds_write_b128 v111, a[136:139] offset:2048\n\t"
        "ds_write_b128 v111, a[140:143] offset:3072\n\t"
        "ds_write_b128 v111, a[144:147] offset:4096\n\t"
        "ds_write_b128 v111, a[148:151] offset:5120\n\t"
        "ds_write_b128 v111, a[152:155] offset:6144\n\t"
        "ds_write_b128 v111, a[156:159] offset:7168\n\t"
        "s_add_u32 %[t0], %[t1], 196608\n\t"
        "buffer_load_dwordx4 v[200:203], v110, %[so], %[t0] offen\n\t"
        "ds_read_b64_tr_b16 a[96:97], v112 offset:0\n\t"
        "ds_read_b64_tr_b16 a[98:99], v112 offset:2048\n\t"
        "ds_read_b64_tr_b16 a[108:109], v112 offset:4096\n\t"
        "ds_read_b64_tr_b16 a[110:111], v112 offset:6144\n\t"
        "s_add_u32 %[t0], %[t1], 212992\n\t"
        "buffer_load_dwordx4 v[204:207], v110, %[so], %[t0] offen\n\t"
        "s_waitcnt lgkmcnt(8)\n\t"
        "ds_read_b64_tr_b16 a[100:101], v113 offset:0\n\t"
        "ds_read_b64_tr_b16 a[102:103], v113 offset:2048\n\t"
        "ds_read_b64_tr_b16 a[112:113], v113 offset:4096\n\t"
        "ds_read_b64_tr_b16 a[114:115], v113 offset:6144\n\t"
        "s_add_u32 %[t0], %[t1], 229376\n\t"
        "buffer_load_dwordx4 v[40:43], v110, %[so], %[t0] offen\n\t"
        "s_waitcnt lgkmcnt(8)\n\t"
        "ds_read_b64_tr_b16 a[104:105], v114 offset:0\n\t"
        "ds_read_b64_tr_b16 a[106:107], v114 offset:2048\n\t"
        "ds_read_b64_tr_b16 a[116:117], v114 offset:4096\n\t"
        "ds_read_b64_tr_b16 a[118:119], v114 offset:6144\n\t"
        "s_add_u32 %[t0], %[t1], 245760\n\t"
        "buffer_load_dwordx4 v[44:47], v110, %[so], %[t0] offen\n\t"
        "s_waitcnt lgkmcnt(8)\n\t"
        "ds_read_b64_tr_b16 v[80:81], v115 offset:0\n\t"
        "ds_read_b64_tr_b16 v[82:83], v115 offset:2048\n\t"
        "ds_read_b64_tr_b16 v[84:85], v115 offset:4096\n\t"
        "ds_read_b64_tr_b16 v[86:87], v115 offset:6144\n\t"
        "s_add_u32 %[t1], %[ro], 6\n\t"
        "s_and_b32 %[t1], %[t1], 7\n\t"
        "s_lshl_b32 %[t1], %[t1], 18\n\t"
        "s_add_u32 %[t0], %[t1], 0\n\t"
        "buffer_load_dwordx4 v[160:163], v110, %[so], %[t0] offen\n\t"
        "s_waitcnt lgkmcnt(0)\n\t"
        "ds_write_b128 v116, v[80:83] offset:8192\n\t"
        "ds_write_b128 v116, v[84:87] offset:9216\n\t"
        "ds_write_b128 v111, a[160:163] offset:0\n\t"
        "ds_write_b128 v111, a[164:167] offset:1024\n\t"
        "ds_write_b128 v111, a[168:171] offset:2048\n\t"
        "ds_write_b128 v111, a[172:175] offset:3072\n\t"
        "ds_write_b128 v111, a[176:179] offset:4096\n\t"
        "ds_write_b128 v111, a[180:183] offset:5120\n\t"
        "ds_write_b128 v111, a[184:187] offset:6144\n\t"
        "ds_write_b128 v111, a[188:191] offset:7168\n\t"
        "s_add_u32 %[t0], %[t1], 16384\n\t"
        "buffer_load_dwordx4 v[164:167], v110, %[so], %[t0] offen\n\t"
        "ds_read_b64_tr_b16 a[120:121], v112 offset:0\n\t"
        "ds_read_b64_tr_b16 a[122:123], v112 offset:2048\n\t"
        "ds_read_b64_tr_b16 a[132:133], v112 offset:4096\n\t"
        "ds_read_b64_tr_b16 a[134:135], v112 offset:6144\n\t"
        "s_add_u32 %[t0], %[t1], 32768\n\t"
        "buffer_load_dwordx4 v[168:171], v110, %[so], %[t0] offen\n\t"
        "s_waitcnt lgkmcnt(8)\n\t"
        "ds_read_b64_tr_b16 a[124:125], v113 offset:0\n\t"
        "ds_read_b64_tr_b16 a[126:127], v113 offset:2048\n\t"
        "ds_read_b64_tr_b16 a[136:137], v113 offset:4096\n\t"
        "ds_read_b64_tr_b16 a[138:139], v113 offset:6144\n\t"
        "s_add_u32 %[t0], %[t1], 49152\n\t"
        "buffer_load_dwordx4 v[172:175], v110, %[so], %[t0] offen\n\t"
        "s_waitcnt lgkmcnt(8)\n\t"
        "ds_read_b64_tr_b16 a[128:129], v114 offset:0\n\t"
        "ds_read_b64_tr_b16 a[130:131], v114 offset:2048\n\t"
        "ds_read_b64_tr_b16 a[140:141], v114 offset:4096\n\t"
        "ds_read_b64_tr_b16 a[142:143], v114 offset:6144\n\t"
        "s_add_u32 %[t0], %[t1], 65536\n\t"
        "buffer_load_dwordx4 v[176:179], v110, %[so], %[t0] offen\n\t"
        "s_waitcnt lgkmcnt(8)\n\t"
        "ds_read_b64_tr_b16 v[80:81], v115 offset:0\n\t"
        "ds_read_b64_tr_b16 v[82:83], v115 offset:2048\n\t"
        "ds_read_b64_tr_b16 v[84:85], v115 offset:4096\n\t"
        "ds_read_b64_tr_b16 v[86:87], v115 offset:6144\n\t"
        "s_add_u32 %[t0], %[t1], 81920\n\t"
        "buffer_load_dwordx4 v[180:183], v110, %[so], %[t0] offen\n\t"
        "s_waitcnt lgkmcnt(0)\n\t"
        "ds_write_b128 v116, v[80:83] offset:10240\n\t"
        "ds_write_b128 v116, v[84:87] offset:11264\n\t"
        "ds_write_b128 v111, a[192:195] offset:0\n\t"
        "ds_write_b128 v111, a[196:199] offset:1024\n\t"
        "ds_write_b128 v111, a[200:203] offset:2048\n\t"
        "ds_write_b128 v111, a[204:207] offset:3072\n\t"
        "ds_write_b128 v111, a[208:211] offset:4096\n\t"
        "ds_write_b128 v111, a[212:215] offset:5120\n\t"
        "ds_write_b128 v111, a[216:219] offset:6144\n\t"
        "ds_write_b128 v111, a[220:223] offset:7168\n\t"
        "s_add_u32 %[t0], %[t1], 98304\n\t"
        "buffer_load_dwordx4 v[48:51], v110, %[so], %[t0] offen\n\t"
        "ds_read_b64_tr_b16 a[144:145], v112 offset:0\n\t"
        "ds_read_b64_tr_b16 a[146:147], v112 offset:2048\n\t"
        "ds_read_b64_tr_b16 a[156:157], v112 offset:4096\n\t"
        "ds_read_b64_tr_b16 a[158:159], v112 offset:6144\n\t"
        "s_add_u32 %[t0], %[t1], 114688\n\t"
        "buffer_load_dwordx4 v[52:55], v110, %[so], %[t0] offen\n\t"
        "s_waitcnt lgkmcnt(8)\n\t"
        "ds_read_b64_tr_b16 a[148:149], v113 offset:0\n\t"
        "ds_read_b64_tr_b16 a[150:151], v113 offset:2048\n\t"
        "ds_read_b64_tr_b16 a[160:161], v113 offset:4096\n\t"
        "ds_read_b64_tr_b16 a[162:163], v113 offset:6144\n\t"
        "s_add_u32 %[t1], %[ro], 5\n\t"
        "s_and_b32 %[t1], %[t1], 7\n\t"
        "s_lshl_b32 %[t1], %[t1], 18\n\t"
        "s_add_u32 %[t0], %[t1], 131072\n\t"
        "buffer_load_dwordx4 v[136:139], v110, %[so], %[t0] offen\n\t"
        "s_waitcnt lgkmcnt(8)\n\t"
        "ds_read_b64_tr_b16 a[152:153], v114 offset:0\n\t"
        "ds_read_b64_tr_b16 a[154:155], v114 offset:2048\n\t"
        "ds_read_b64_tr_b16 a[164:165], v114 offset:4096\n\t"
        "ds_read_b64_tr_b16 a[166:167], v114 offset:6144\n\t"
        "s_add_u32 %[t0], %[t1], 147456\n\t"
        "buffer_load_dwordx4 v[140:143], v110, %[so], %[t0] offen\n\t"
        "s_waitcnt lgkmcnt(8)\n\t"
        "ds_read_b64_tr_b16 v[80:81], v115 offset:0\n\t"
        "ds_read_b64_tr_b16 v[82:83], v115 offset:2048\n\t"
        "ds_read_b64_tr_b16 v[84:85], v115 offset:4096\n\t"
        "ds_read_b64_tr_b16 v[86:87], v115 offset:6144\n\t"
        "s_add_u32 %[t0], %[t1], 163840\n\t"
        "buffer_load_dwordx4 v[144:147], v110, %[so], %[t0] offen\n\t"
        "s_waitcnt lgkmcnt(0)\n\t"
        "ds_write_b128 v116, v[80:83] offset:12288\n\t"
        "ds_write_b128 v116, v[84:87] offset:13312\n\t"
        "ds_write_b128 v111, a[224:227] offset:0\n\t"
        "ds_write_b128 v111, a[228:231] offset:1024\n\t"
        "ds_write_b128 v111, a[232:235] offset:2048\n\t"
        "ds_write_b128 v111, a[236:239] offset:3072\n\t"
        "ds_write_b128 v111, a[240:243] offset:4096\n\t"
        "ds_write_b128 v111, a[244:247] offset:5120\n\t"
        "ds_write_b128 v111, a[248:251] offset:6144\n\t"
        "ds_write_b128 v111, a[252:255] offset:7168\n\t"
        "s_add_u32 %[t0], %[t1], 180224\n\t"
        "buffer_load_dwordx4 v[148:151], v110, %[so], %[t0] offen\n\t"
        "ds_read_b64_tr_b16 a[168:169], v112 offset:0\n\t"
        "ds_read_b64_tr_b16 a[170:171], v112 offset:2048\n\t"
        "ds_read_b64_tr_b16 a[180:181], v112 offset:4096\n\t"
        "ds_read_b64_tr_b16 a[182:183], v112 offset:6144\n\t"
        "s_add_u32 %[t0], %[t1], 196608\n\t"
        "buffer_load_dwordx4 v[152:155], v110, %[so], %[t0] offen\n\t"
        "s_waitcnt lgkmcnt(8)\n\t"
        "ds_read_b64_tr_b16 a[172:173], v113 offset:0\n\t"
        "ds_read_b64_tr_b16 a[174:175], v113 offset:2048\n\t"
        "ds_read_b64_tr_b16 a[184:185], v113 offset:4096\n\t"
        "ds_read_b64_tr_b16 a[186:187], v113 offset:6144\n\t"
        "s_add_u32 %[t0], %[t1], 212992\n\t"
        "buffer_load_dwordx4 v[156:159], v110, %[so], %[t0] offen\n\t"
        "s_waitcnt lgkmcnt(8)\n\t"
        "ds_read_b64_tr_b16 a[176:177], v114 offset:0\n\t"
        "ds_read_b64_tr_b16 a[178:179], v114 offset:2048\n\t"
        "ds_read_b64_tr_b16 a[188:189], v114 offset:4096\n\t"
        "ds_read_b64_tr_b16 a[190:191], v114 offset:6144\n\t"
        "s_add_u32 %[t0], %[t1], 229376\n\t"
        "buffer_load_dwordx4 v[56:59], v110, %[so], %[t0] offen\n\t"
        "s_waitcnt lgkmcnt(8)\n\t"
        "ds_read_b64_tr_b16 v[80:81], v115 offset:0\n\t"
        "ds_read_b64_tr_b16 v[82:83], v115 offset:2048\n\t"
        "ds_read_b64_tr_b16 v[84:85], v115 offset:4096\n\t"
        "ds_read_b64_tr_b16 v[86:87], v115 offset:6144\n\t"
        "s_add_u32 %[t0], %[t1], 245760\n\t"
        "buffer_load_dwordx4 v[60:63], v110, %[so], %[t0] offen\n\t"
        "s_waitcnt lgkmcnt(0)\n\t"
        "ds_write_b128 v116, v[80:83] offset:14336\n\t"
        "ds_write_b128 v116, v[84:87] offset:15360\n\t"
        "s_add_u32 %[t1], %[ro], 5\n\t"
        "s_and_b32 %[t1], %[t1], 7\n\t"
        "s_lshl_b32 %[t1], %[t1], 18\n\t"
        "s_add_u32 %[t0], %[t1], 0\n\t"
        "buffer_load_dwordx4 a[240:243], v110, %[so], %[t0] offen\n\t"
        "s_waitcnt vmcnt(33)\n\t"
        "ds_write_b128 v111, v[232:235] offset:0\n\t"
        "ds_write_b128 v111, v[236:239] offset:1024\n\t"
        "ds_write_b128 v111, v[240:243] offset:2048\n\t"
        "ds_write_b128 v111, v[244:247] offset:3072\n\t"
        "ds_write_b128 v111, v[248:251] offset:4096\n\t"
        "ds_write_b128 v111, v[252:255] offset:5120\n\t"
        "ds_write_b128 v111, v[24:27] offset:6144\n\t"
        "ds_write_b128 v111, v[28:31] offset:7168\n\t"
        "s_add_u32 %[t0], %[t1], 16384\n\t"
        "buffer_load_dwordx4 a[244:247], v110, %[so], %[t0] offen\n\t"
        "ds_read_b64_tr_b16 v[232:233], v112 offset:0\n\t"
        "ds_read_b64_tr_b16 v[234:235], v112 offset:2048\n\t"
        "ds_read_b64_tr_b16 v[244:245], v112 offset:4096\n\t"
        "ds_read_b64_tr_b16 v[246:247], v112 offset:6144\n\t"
        "s_add_u32 %[t0], %[t1], 32768\n\t"
        "buffer_load_dwordx4 a[248:251], v110, %[so], %[t0] offen\n\t"
        "s_waitcnt lgkmcnt(8)\n\t"
        "ds_read_b64_tr_b16 v[236:237], v113 offset:0\n\t"
        "ds_read_b64_tr_b16 v[238:239], v113 offset:2048\n\t"
        "ds_read_b64_tr_b16 v[248:249], v113 offset:4096\n\t"
        "ds_read_b64_tr_b16 v[250:251], v113 offset:6144\n\t"
        "s_add_u32 %[t0], %[t1], 49152\n\t"
        "buffer_load_dwordx4 a[252:255], v110, %[so], %[t0] offen\n\t"
        "s_waitcnt lgkmcnt(8)\n\t"
        "ds_read_b64_tr_b16 v[240:241], v114 offset:0\n\t"
        "ds_read_b64_tr_b16 v[242:243], v114 offset:2048\n\t"
        "ds_read_b64_tr_b16 v[252:253], v114 offset:4096\n\t"
        "ds_read_b64_tr_b16 v[254:255], v114 offset:6144\n\t"
        "s_add_u32 %[t0], %[t1], 65536\n\t"
        "buffer_load_dwordx4 v[128:131], v110, %[so], %[t0] offen\n\t"
        "s_waitcnt lgkmcnt(8)\n\t"
        "ds_read_b64_tr_b16 v[80:81], v115 offset:0\n\t"
        "ds_read_b64_tr_b16 v[82:83], v115 offset:2048\n\t"
        "ds_read_b64_tr_b16 v[84:85], v115 offset:4096\n\t"
        "ds_read_b64_tr_b16 v[86:87], v115 offset:6144\n\t"
        "s_add_u32 %[t0], %[t1], 81920\n\t"
        "buffer_load_dwordx4 v[132:135], v110, %[so], %[t0] offen\n\t"
        "s_waitcnt lgkmcnt(0)\n\t"
        "ds_write_b128 v116, v[80:83] offset:30720\n\t"
        "ds_write_b128 v116, v[84:87] offset:31744\n\t"
        "s_add_u32 %[t0], %[t1], 98304\n\t"
        "buffer_load_dwordx4 v[64:67], v110, %[so], %[t0] offen\n\t"
        "s_waitcnt vmcnt(31)\n\t"
        "ds_write_b128 v111, v[208:211] offset:0\n\t"
        "ds_write_b128 v111, v[212:215] offset:1024\n\t"
        "ds_write_b128 v111, v[216:219] offset:2048\n\t"
        "ds_write_b128 v111, v[220:223] offset:3072\n\t"
        "ds_write_b128 v111, v[224:227] offset:4096\n\t"
        "ds_write_b128 v111, v[228:231] offset:5120\n\t"
        "ds_write_b128 v111, v[32:35] offset:6144\n\t"
        "ds_write_b128 v111, v[36:39] offset:7168\n\t"
        "s_add_u32 %[t0], %[t1], 114688\n\t"
        "buffer_load_dwordx4 v[68:71], v110, %[so], %[t0] offen\n\t"
        "ds_read_b64_tr_b16 v[208:209], v112 offset:0\n\t"
        "ds_read_b64_tr_b16 v[210:211], v112 offset:2048\n\t"
        "ds_read_b64_tr_b16 v[220:221], v112 offset:4096\n\t"
        "ds_read_b64_tr_b16 v[222:223], v112 offset:6144\n\t"
        "s_add_u32 %[t1], %[ro], 4\n\t"
        "s_and_b32 %[t1], %[t1], 7\n\t"
        "s_lshl_b32 %[t1], %[t1], 18\n\t"
        "s_add_u32 %[t0], %[t1], 131072\n\t"
        "buffer_load_dwordx4 a[216:219], v110, %[so], %[t0] offen\n\t"
        "s_waitcnt lgkmcnt(8)\n\t"
        "ds_read_b64_tr_b16 v[212:213], v113 offset:0\n\t"
        "ds_read_b64_tr_b16 v[214:215], v113 offset:2048\n\t"
        "ds_read_b64_tr_b16 v[224:225], v113 offset:4096\n\t"
        "ds_read_b64_tr_b16 v[226:227], v113 offset:6144\n\t"
        "s_add_u32 %[t0], %[t1], 147456\n\t"
        "buffer_load_dwordx4 a[220:223], v110, %[so], %[t0] offen\n\t"
        "s_waitcnt lgkmcnt(8)\n\t"
        "ds_read_b64_tr_b16 v[216:217], v114 offset:0\n\t"
        "ds_read_b64_tr_b16 v[218:219], v114 offset:2048\n\t"
        "ds_read_b64_tr_b16 v[228:229], v114 offset:4096\n\t"
        "ds_read_b64_tr_b16 v[230:231], v114 offset:6144\n\t"
        "s_add_u32 %[t0], %[t1], 163840\n\t"
        "buffer_load_dwordx4 a[224:227], v110, %[so], %[t0] offen\n\t"
        "s_waitcnt lgkmcnt(8)\n\t"
        "ds_read_b64_tr_b16 v[80:81], v115 offset:0\n\t"
        "ds_read_b64_tr_b16 v[82:83], v115 offset:2048\n\t"
        "ds_read_b64_tr_b16 v[84:85], v115 offset:4096\n\t"
        "ds_read_b64_tr_b16 v[86:87], v115 offset:6144\n\t"
        "s_add_u32 %[t0], %[t1], 180224\n\t"
        "buffer_load_dwordx4 a[228:231], v110, %[so], %[t0] offen\n\t"
        "s_waitcnt lgkmcnt(0)\n\t"
        "ds_write_b128 v116, v[80:83] offset:28672\n\t"
        "ds_write_b128 v116, v[84:87] offset:29696\n\t"
        "s_add_u32 %[t0], %[t1], 196608\n\t"
        "buffer_load_dwordx4 a[232:235], v110, %[so], %[t0] offen\n\t"
        "s_waitcnt vmcnt(29)\n\t"
        "ds_write_b128 v111, v[184:187] offset:0\n\t"
        "ds_write_b128 v111, v[188:191] offset:1024\n\t"
        "ds_write_b128 v111, v[192:195] offset:2048\n\t"
        "ds_write_b128 v111, v[196:199] offset:3072\n\t"
        "ds_write_b128 v111, v[200:203] offset:4096\n\t"
        "ds_write_b128 v111, v[204:207] offset:5120\n\t"
        "ds_write_b128 v111, v[40:43] offset:6144\n\t"
        "ds_write_b128 v111, v[44:47] offset:7168\n\t"
        "s_add_u32 %[t0], %[t1], 212992\n\t"
        "buffer_load_dwordx4 a[236:239], v110, %[so], %[t0] offen\n\t"
        "ds_read_b64_tr_b16 v[184:185], v112 offset:0\n\t"
        "ds_read_b64_tr_b16 v[186:187], v112 offset:2048\n\t"
        "ds_read_b64_tr_b16 v[196:197], v112 offset:4096\n\t"
        "ds_read_b64_tr_b16 v[198:199], v112 offset:6144\n\t"
        "s_add_u32 %[t0], %[t1], 229376\n\t"
        "buffer_load_dwordx4 v[72:75], v110, %[so], %[t0] offen\n\t"
        "s_waitcnt lgkmcnt(8)\n\t"
        "ds_read_b64_tr_b16 v[188:189], v113 offset:0\n\t"
        "ds_read_b64_tr_b16 v[190:191], v113 offset:2048\n\t"
        "ds_read_b64_tr_b16 v[200:201], v113 offset:4096\n\t"
        "ds_read_b64_tr_b16 v[202:203], v113 offset:6144\n\t"
        "s_add_u32 %[t0], %[t1], 245760\n\t"
        "buffer_load_dwordx4 v[76:79], v110, %[so], %[t0] offen\n\t"
        "s_waitcnt lgkmcnt(8)\n\t"
        "ds_read_b64_tr_b16 v[192:193], v114 offset:0\n\t"
        "ds_read_b64_tr_b16 v[194:195], v114 offset:2048\n\t"
        "ds_read_b64_tr_b16 v[204:205], v114 offset:4096\n\t"
        "ds_read_b64_tr_b16 v[206:207], v114 offset:6144\n\t"
        "s_add_u32 %[t1], %[ro], 4\n\t"
        "s_and_b32 %[t1], %[t1], 7\n\t"
        "s_lshl_b32 %[t1], %[t1], 18\n\t"
        "s_add_u32 %[t0], %[t1], 0\n\t"
        "buffer_load_dwordx4 a[192:195], v110, %[so], %[t0] offen\n\t"
        "s_waitcnt lgkmcnt(8)\n\t"
        "ds_read_b64_tr_b16 v[80:81], v115 offset:0\n\t"
        "ds_read_b64_tr_b16 v[82:83], v115 offset:2048\n\t"
        "ds_read_b64_tr_b16 v[84:85], v115 offset:4096\n\t"
        "ds_read_b64_tr_b16 v[86:87], v115 offset:6144\n\t"
        "s_add_u32 %[t0], %[t1], 16384\n\t"
        "buffer_load_dwordx4 a[196:199], v110, %[so], %[t0] offen\n\t"
        "s_waitcnt lgkmcnt(0)\n\t"
        "ds_write_b128 v116, v[80:83] offset:26624\n\t"
        "ds_write_b128 v116, v[84:87] offset:27648\n\t"
        "s_add_u32 %[t0], %[t1], 32768\n\t"
        "buffer_load_dwordx4 a[200:203], v110, %[so], %[t0] offen\n\t"
        "s_waitcnt vmcnt(27)\n\t"
        "ds_write_b128 v111, v[160:163] offset:0\n\t"
        "ds_write_b128 v111, v[164:167] offset:1024\n\t"
        "ds_write_b128 v111, v[168:171] offset:2048\n\t"
        "ds_write_b128 v111, v[172:175] offset:3072\n\t"
        "ds_write_b128 v111, v[176:179] offset:4096\n\t"
        "ds_write_b128 v111, v[180:183] offset:5120\n\t"
        "ds_write_b128 v111, v[48:51] offset:6144\n\t"
        "ds_write_b128 v111, v[52:55] offset:7168\n\t"
        "s_add_u32 %[t0], %[t1], 49152\n\t"
        "buffer_load_dwordx4 a[204:207], v110, %[so], %[t0] offen\n\t"
        "ds_read_b64_tr_b16 v[160:161], v112 offset:0\n\t"
        "ds_read_b64_tr_b16 v[162:163], v112 offset:2048\n\t"
        "ds_read_b64_tr_b16 v[172:173], v112 offset:4096\n\t"
        "ds_read_b64_tr_b16 v[174:175], v112 offset:6144\n\t"
        "s_add_u32 %[t0], %[t1], 65536\n\t"
        "buffer_load_dwordx4 a[208:211], v110, %[so], %[t0] offen\n\t"
        "s_waitcnt lgkmcnt(8)\n\t"
        "ds_read_b64_tr_b16 v[164:165], v113 offset:0\n\t"
        "ds_read_b64_tr_b16 v[166:167], v113 offset:2048\n\t"
        "ds_read_b64_tr_b16 v[176:177], v113 offset:4096\n\t"
        "ds_read_b64_tr_b16 v[178:179], v113 offset:6144\n\t"
        "s_add_u32 %[t0], %[t1], 81920\n\t"
        "buffer_load_dwordx4 a[212:215], v110, %[so], %[t0] offen\n\t"
        "s_waitcnt lgkmcnt(8)\n\t"
        "ds_read_b64_tr_b16 v[168:169], v114 offset:0\n\t"
        "ds_read_b64_tr_b16 v[170:171], v114 offset:2048\n\t"
        "ds_read_b64_tr_b16 v[180:181], v114 offset:4096\n\t"
        "ds_read_b64_tr_b16 v[182:183], v114 offset:6144\n\t"
        "s_add_u32 %[t0], %[t1], 98304\n\t"
        "buffer_load_dwordx4 v[24:27], v110, %[so], %[t0] offen\n\t"
        "s_waitcnt lgkmcnt(8)\n\t"
        "ds_read_b64_tr_b16 v[80:81], v115 offset:0\n\t"
        "ds_read_b64_tr_b16 v[82:83], v115 offset:2048\n\t"
        "ds_read_b64_tr_b16 v[84:85], v115 offset:4096\n\t"
        "ds_read_b64_tr_b16 v[86:87], v115 offset:6144\n\t"
        "s_add_u32 %[t0], %[t1], 114688\n\t"
        "buffer_load_dwordx4 v[28:31], v110, %[so], %[t0] offen\n\t"
        "s_waitcnt lgkmcnt(0)\n\t"
        "ds_write_b128 v116, v[80:83] offset:24576\n\t"
        "ds_write_b128 v116, v[84:87] offset:25600\n\t"
        "s_waitcnt vmcnt(24)\n\t"
        "ds_write_b128 v111, v[136:139] offset:0\n\t"
        "ds_write_b128 v111, v[140:143] offset:1024\n\t"
        "ds_write_b128 v111, v[144:147] offset:2048\n\t"
        "ds_write_b128 v111, v[148:151] offset:3072\n\t"
        "ds_write_b128 v111, v[152:155] offset:4096\n\t"
        "ds_write_b128 v111, v[156:159] offset:5120\n\t"
        "ds_write_b128 v111, v[56:59] offset:6144\n\t"
        "ds_write_b128 v111, v[60:63] offset:7168\n\t"
        "ds_read_b64_tr_b16 v[136:137], v112 offset:0\n\t"
        "ds_read_b64_tr_b16 v[138:139], v112 offset:2048\n\t"
        "ds_read_b64_tr_b16 v[148:149], v112 offset:4096\n\t"
        "ds_read_b64_tr_b16 v[150:151], v112 offset:6144\n\t"
        "s_waitcnt lgkmcnt(8)\n\t"
        "ds_read_b64_tr_b16 v[140:141], v113 offset:0\n\t"
        "ds_read_b64_tr_b16 v[142:143], v113 offset:2048\n\t"
        "ds_read_b64_tr_b16 v[152:153], v113 offset:4096\n\t"
        "ds_read_b64_tr_b16 v[154:155], v113 offset:6144\n\t"
        "s_waitcnt lgkmcnt(8)\n\t"
        "ds_read_b64_tr_b16 v[144:145], v114 offset:0\n\t"
        "ds_read_b64_tr_b16 v[146:147], v114 offset:2048\n\t"
        "ds_read_b64_tr_b16 v[156:157], v114 offset:4096\n\t"
        "ds_read_b64_tr_b16 v[158:159], v114 offset:6144\n\t"
        "s_waitcnt lgkmcnt(8)\n\t"
        "ds_read_b64_tr_b16 v[80:81], v115 offset:0\n\t"
        "ds_read_b64_tr_b16 v[82:83], v115 offset:2048\n\t"
        "ds_read_b64_tr_b16 v[84:85], v115 offset:4096\n\t"
        "ds_read_b64_tr_b16 v[86:87], v115 offset:6144\n\t"
        "s_waitcnt lgkmcnt(0)\n\t"
        "ds_write_b128 v116, v[80:83] offset:22528\n\t"
        "ds_write_b128 v116, v[84:87] offset:23552\n\t"
        "s_waitcnt vmcnt(16)\n\t"
        "ds_write_b128 v111, a[240:243] offset:0\n\t"
        "ds_write_b128 v111, a[244:247] offset:1024\n\t"
        "ds_write_b128 v111, a[248:251] offset:2048\n\t"
        "ds_write_b128 v111, a[252:255] offset:3072\n\t"
        "ds_write_b128 v111, v[128:131] offset:4096\n\t"
        "ds_write_b128 v111, v[132:135] offset:5120\n\t"
        "ds_write_b128 v111, v[64:67] offset:6144\n\t"
        "ds_write_b128 v111, v[68:71] offset:7168\n\t"
        "ds_read_b64_tr_b16 a[240:241], v112 offset:0\n\t"
        "ds_read_b64_tr_b16 a[242:243], v112 offset:2048\n\t"
        "ds_read_b64_tr_b16 a[252:253], v112 offset:4096\n\t"
        "ds_read_b64_tr_b16 a[254:255], v112 offset:6144\n\t"
        "s_waitcnt lgkmcnt(8)\n\t"
        "ds_read_b64_tr_b16 a[244:245], v113 offset:0\n\t"
        "ds_read_b64_tr_b16 a[246:247], v113 offset:2048\n\t"
        "ds_read_b64_tr_b16 v[128:129], v113 offset:4096\n\t"
        "ds_read_b64_tr_b16 v[130:131], v113 offset:6144\n\t"
        "s_waitcnt lgkmcnt(8)\n\t"
        "ds_read_b64_tr_b16 a[248:249], v114 offset:0\n\t"
        "ds_read_b64_tr_b16 a[250:251], v114 offset:2048\n\t"
        "ds_read_b64_tr_b16 v[132:133], v114 offset:4096\n\t"
        "ds_read_b64_tr_b16 v[134:135], v114 offset:6144\n\t"
        "s_waitcnt lgkmcnt(8)\n\t"
        "ds_read_b64_tr_b16 v[80:81], v115 offset:0\n\t"
        "ds_read_b64_tr_b16 v[82:83], v115 offset:2048\n\t"
        "ds_read_b64_tr_b16 v[84:85], v115 offset:4096\n\t"
        "ds_read_b64_tr_b16 v[86:87], v115 offset:6144\n\t"
        "s_waitcnt lgkmcnt(0)\n\t"
        "ds_write_b128 v116, v[80:83] offset:20480\n\t"
        "ds_write_b128 v116, v[84:87] offset:21504\n\t"
        "s_waitcnt vmcnt(8)\n\t"
        "ds_write_b128 v111, a[216:219] offset:0\n\t"
        "ds_write_b128 v111, a[220:223] offset:1024\n\t"
        "ds_write_b128 v111, a[224:227] offset:2048\n\t"
        "ds_write_b128 v111, a[228:231] offset:3072\n\t"
        "ds_write_b128 v111, a[232:235] offset:4096\n\t"
        "ds_write_b128 v111, a[236:239] offset:5120\n\t"
        "ds_write_b128 v111, v[72:75] offset:6144\n\t"
        "ds_write_b128 v111, v[76:79] offset:7168\n\t"
        "ds_read_b64_tr_b16 a[216:217], v112 offset:0\n\t"
        "ds_read_b64_tr_b16 a[218:219], v112 offset:2048\n\t"
        "ds_read_b64_tr_b16 a[228:229], v112 offset:4096\n\t"
        "ds_read_b64_tr_b16 a[230:231], v112 offset:6144\n\t"
        "s_waitcnt lgkmcnt(8)\n\t"
        "ds_read_b64_tr_b16 a[220:221], v113 offset:0\n\t"
        "ds_read_b64_tr_b16 a[222:223], v113 offset:2048\n\t"
        "ds_read_b64_tr_b16 a[232:233], v113 offset:4096\n\t"
        "ds_read_b64_tr_b16 a[234:235], v113 offset:6144\n\t"
        "s_waitcnt lgkmcnt(8)\n\t"
        "ds_read_b64_tr_b16 a[224:225], v114 offset:0\n\t"
        "ds_read_b64_tr_b16 a[226:227], v114 offset:2048\n\t"
        "ds_read_b64_tr_b16 a[236:237], v114 offset:4096\n\t"
        "ds_read_b64_tr_b16 a[238:239], v114 offset:6144\n\t"
        "s_waitcnt lgkmcnt(8)\n\t"
        "ds_read_b64_tr_b16 v[80:81], v115 offset:0\n\t"
        "ds_read_b64_tr_b16 v[82:83], v115 offset:2048\n\t"
        "ds_read_b64_tr_b16 v[84:85], v115 offset:4096\n\t"
        "ds_read_b64_tr_b16 v[86:87], v115 offset:6144\n\t"
        "s_waitcnt lgkmcnt(0)\n\t"
        "ds_write_b128 v116, v[80:83] offset:18432\n\t"
        "ds_write_b128 v116, v[84:87] offset:19456\n\t"
        "s_waitcnt vmcnt(0)\n\t"
        "ds_write_b128 v111, a[192:195] offset:0\n\t"
        "ds_write_b128 v111, a[196:199] offset:1024\n\t"
        "ds_write_b128 v111, a[200:203] offset:2048\n\t"
        "ds_write_b128 v111, a[204:207] offset:3072\n\t"
        "ds_write_b128 v111, a[208:211] offset:4096\n\t"
        "ds_write_b128 v111, a[212:215] offset:5120\n\t"
        "ds_write_b128 v111, v[24:27] offset:6144\n\t"
        "ds_write_b128 v111, v[28:31] offset:7168\n\t"
        "ds_read_b64_tr_b16 a[192:193], v112 offset:0\n\t"
        "ds_read_b64_tr_b16 a[194:195], v112 offset:2048\n\t"
        "ds_read_b64_tr_b16 a[204:205], v112 offset:4096\n\t"
        "ds_read_b64_tr_b16 a[206:207], v112 offset:6144\n\t"
        "s_waitcnt lgkmcnt(8)\n\t"
        "ds_read_b64_tr_b16 a[196:197], v113 offset:0\n\t"
        "ds_read_b64_tr_b16 a[198:199], v113 offset:2048\n\t"
        "ds_read_b64_tr_b16 a[208:209], v113 offset:4096\n\t"
        "ds_read_b64_tr_b16 a[210:211], v113 offset:6144\n\t"
        "s_waitcnt lgkmcnt(8)\n\t"
        "ds_read_b64_tr_b16 a[200:201], v114 offset:0\n\t"
        "ds_read_b64_tr_b16 a[202:203], v114 offset:2048\n\t"
        "ds_read_b64_tr_b16 a[212:213], v114 offset:4096\n\t"
        "ds_read_b64_tr_b16 a[214:215], v114 offset:6144\n\t"
        "s_waitcnt lgkmcnt(8)\n\t"
        "ds_read_b64_tr_b16 v[80:81], v115 offset:0\n\t"
        "ds_read_b64_tr_b16 v[82:83], v115 offset:2048\n\t"
        "ds_read_b64_tr_b16 v[84:85], v115 offset:4096\n\t"
        "ds_read_b64_tr_b16 v[86:87], v115 offset:6144\n\t"
        "s_waitcnt lgkmcnt(0)\n\t"
        "ds_write_b128 v116, v[80:83] offset:16384\n\t"
        "ds_write_b128 v116, v[84:87] offset:17408\n\t"
        "ds_read_b128 v[88:91], v116 offset:0\n\t"
        "ds_read_b128 v[92:95], v116 offset:1024\n\t"
        "ds_read_b128 v[96:99], v116 offset:2048\n\t"
        "s_mov_b32 %[sp], 0\n\t"
        "LP_cv%=:\n\t"
        "buffer_load_dword v104, off, %[sw], %[cv] sc1\n\t"
        "s_waitcnt vmcnt(0) lgkmcnt(0)\n\t"
        "v_readfirstlane_b32 %[t0], v104\n\t"
        "s_cmp_ge_u32 %[t0], 8\n\t"
        "s_cbranch_scc1 LP_wait%=\n\t"
        "s_sleep 8\n\t"
        "s_add_u32 %[sp], %[sp], 1\n\t"
        "s_cmp_lt_u32 %[sp], 0x100000\n\t"
        "s_cbranch_scc1 LP_cv%=\n\t"
        "v_mov_b32_e32 v104, 256\n\t"
        "s_mov_b32 %[t0], 33024\n\t"
        "buffer_store_dword v104, off, %[sw], %[t0] sc1\n\t"
        "s_endpgm\n\t"
        "LP_wait%=:\n\t"
        "s_mov_b32 %[re], 0\n\t"
        "LP_again%=:\n\t"
        "s_mov_b32 %[sp], 0\n\t"
        "LP_poll%=:\n\t"
        "buffer_load_dword v103, v118, %[sw], 0 offen sc1\n\t"
        "s_lshr_b32 %[t1], %[tl], 1\n\t"
        "s_waitcnt vmcnt(0)\n\t"
        "v_min_u32_dpp v103, v103, v103 quad_perm:[1,0,3,2] row_mask:0xf bank_mask:0xf\n\t"
        "s_nop 1\n\t"
        "v_min_u32_dpp v103, v103, v103 quad_perm:[2,3,0,1] row_mask:0xf bank_mask:0xf\n\t"
        "s_nop 1\n\t"
        "v_min_u32_dpp v103, v103, v103 row_ror:4 row_mask:0xf bank_mask:0xf\n\t"
        "s_nop 0\n\t"
        "v_readfirstlane_b32 %[pg], v103\n\t"
        "s_cmp_gt_u32 %[pg], %[t1]\n\t"
        "s_cbranch_scc1 LP_have%=\n\t"
        "s_add_u32 %[t0], %[pg], %[re]\n\t"
        "s_cmp_eq_u32 %[t0], 63\n\t"
        "s_cbranch_scc0 LP_no%=\n\t"
        "s_sleep 16\n\t"
        "s_branch LP_have%=\n\t"
        "LP_no%=:\n\t"
        "s_min_u32 %[t1], %[t1], 62\n\t"
        "s_cmp_ge_u32 %[pg], %[t1]\n\t"
        "s_cbranch_scc1 LP_near%=\n\t"
        "s_sleep 24\n\t"
        "LP_near%=:\n\t"
        "s_sleep 2\n\t"
        "s_add_u32 %[sp], %[sp], 1\n\t"
        "s_cmp_lt_u32 %[sp], 0x100000\n\t"
        "s_cbranch_scc1 LP_poll%=\n\t"
        "v_mov_b32_e32 v104, 128\n\t"
        "s_mov_b32 %[t0], 33024\n\t"
        "buffer_store_dword v104, off, %[sw], %[t0] sc1\n\t"
        "s_endpgm\n\t"
        "LP_have%=:\n\t"
        "s_lshl_b32 %[nx], %[tl], 17\n\t"
        "s_add_u32 %[t1], %[t1], 4\n\t"
        "s_cmp_gt_u32 %[pg], %[t1]\n\t"
        "s_cbranch_scc1 LP_run%=\n\t"
        "s_add_u32 %[t0], %[ro], 0\n\t"
        "s_and_b32 %[t0], %[t0], 7\n\t"
        "s_lshl_b32 %[t0], %[t0], 12\n\t"
        "s_add_u32 %[t0], %[t0], %[nx]\n\t"
        "buffer_load_dwordx4 v[24:27], v117, %[sw], %[t0] offen offset:0 sc1\n\t"
        "buffer_load_dwordx4 v[28:31], v117, %[sw], %[t0] offen offset:1024 sc1\n\t"
        "buffer_load_dwordx4 v[32:35], v117, %[sw], %[t0] offen offset:2048 sc1\n\t"
        "buffer_load_dwordx4 v[36:39], v117, %[sw], %[t0] offen offset:3072 sc1\n\t"
        "s_add_u32 %[t0], %[ro], 1\n\t"
        "s_and_b32 %[t0], %[t0], 7\n\t"
        "s_lshl_b32 %[t0], %[t0], 12\n\t"
        "s_add_u32 %[t0], %[t0], %[nx]\n\t"
        "buffer_load_dwordx4 v[40:43], v117, %[sw], %[t0] offen offset:0 sc1\n\t"
        "buffer_load_dwordx4 v[44:47], v117, %[sw], %[t0] offen offset:1024 sc1\n\t"
        "s_waitcnt vmcnt(5) lgkmcnt(2)\n\t"
        "v_mfma_f32_16x16x32_bf16 v[20:23], v[88:91], v[24:27], 0\n\t"
        "ds_read_b128 v[88:91], v116 offset:3072\n\t"
        "v_mfma_f32_16x16x32_bf16 v[8:11], a[0:3], v[24:27], 0\n\t"
        "v_max3_u32 v125, v24, v25, v26\n\t"
        "buffer_load_dwordx4 v[48:51], v117, %[sw], %[t0] offen offset:2048 sc1\n\t"
        "v_mfma_f32_16x16x32_bf16 v[12:15], a[4:7], v[24:27], 0\n\t"
        "v_max_u32_e32 v125, v27, v125\n\t"
        "v_mfma_f32_16x16x32_bf16 v[16:19], a[8:11], v[24:27], 0\n\t"
        "buffer_load_dwordx4 v[52:55], v117, %[sw], %[t0] offen offset:3072 sc1\n\t"
        "s_waitcnt vmcnt(6) lgkmcnt(2)\n\t"
        "v_mfma_f32_16x16x32_bf16 v[20:23], v[92:95], v[28:31], v[20:23]\n\t"
        "ds_read_b128 v[92:95], v116 offset:4096\n\t"
        "v_mfma_f32_16x16x32_bf16 v[8:11], a[12:15], v[28:31], v[8:11]\n\t"
        "v_max3_u32 v125, v28, v29, v125\n\t"
        "s_add_u32 %[t0], %[ro], 2\n\t"
        "s_and_b32 %[t0], %[t0], 7\n\t"
        "s_lshl_b32 %[t0], %[t0], 12\n\t"
        "s_add_u32 %[t0], %[t0], %[nx]\n\t"
        "buffer_load_dwordx4 v[56:59], v117, %[sw], %[t0] offen offset:0 sc1\n\t"
        "v_mfma_f32_16x16x32_bf16 v[12:15], a[16:19], v[28:31], v[12:15]\n\t"
        "v_max3_u32 v125, v30, v31, v125\n\t"
        "v_mfma_f32_16x16x32_bf16 v[16:19], a[20:23], v[28:31], v[16:19]\n\t"
        "buffer_load_dwordx4 v[60:63], v117, %[sw], %[t0] offen offset:1024 sc1\n\t"
        "s_and_b32 %[sp], %[tl], 1\n\t"
        "s_lshl_b32 %[sp], %[sp], 14\n\t"
        "s_lshr_b32 %[t1], %[tl], 1\n\t"
        "s_lshl_b32 %[t1], %[t1], 6\n\t"
        "s_add_u32 %[t1], %[sp], %[t1]\n\t"
        "buffer_load_dword v100, v119, %[sw], %[t1] offen sc1\n\t"
        "s_add_u32 %[t1], %[t1], 32768\n\t"
        "buffer_load_dword v101, v119, %[sw], %[t1] offen sc1\n\t"
        "s_lshl_b32 %[t1], %[tl], 9\n\t"
        "buffer_load_dword v102, v120, %[sw], %[t1] offen sc1\n\t"
        "buffer_load_dword v103, v118, %[sw], 0 offen sc1\n\t"
        "s_waitcnt vmcnt(11) lgkmcnt(2)\n\t"
        "v_mfma_f32_16x16x32_bf16 v[20:23], v[96:99], v[32:35], v[20:23]\n\t"
        "ds_read_b128 v[96:99], v116 offset:5120\n\t"
        "v_mfma_f32_16x16x32_bf16 v[8:11], a[24:27], v[32:35], v[8:11]\n\t"
        "v_max3_u32 v125, v32, v33, v125\n\t"
        "buffer_load_dwordx4 v[64:67], v117, %[sw], %[t0] offen offset:2048 sc1\n\t"
        "v_mfma_f32_16x16x32_bf16 v[12:15], a[28:31], v[32:35], v[12:15]\n\t"
        "v_max3_u32 v125, v34, v35, v125\n\t"
        "v_mfma_f32_16x16x32_bf16 v[16:19], a[32:35], v[32:35], v[16:19]\n\t"
        "buffer_load_dwordx4 v[68:71], v117, %[sw], %[t0] offen offset:3072 sc1\n\t"
        "s_waitcnt vmcnt(12) lgkmcnt(2)\n\t"
        "v_mfma_f32_16x16x32_bf16 v[20:23], v[88:91], v[36:39], v[20:23]\n\t"
        "ds_read_b128 v[88:91], v116 offset:6144\n\t"
        "v_mfma_f32_16x16x32_bf16 v[8:11], a[36:39], v[36:39], v[8:11]\n\t"
        "v_max3_u32 v125, v36, v37, v125\n\t"
        "s_add_u32 %[t0], %[ro], 3\n\t"
        "s_and_b32 %[t0], %[t0], 7\n\t"
        "s_lshl_b32 %[t0], %[t0], 12\n\t"
        "s_add_u32 %[t0], %[t0], %[nx]\n\t"
        "buffer_load_dwordx4 v[72:75], v117, %[sw], %[t0] offen offset:0 sc1\n\t"
        "v_mfma_f32_16x16x32_bf16 v[12:15], a[40:43], v[36:39], v[12:15]\n\t"
        "v_max3_u32 v125, v38, v39, v125\n\t"
        "v_mfma_f32_16x16x32_bf16 v[16:19], a[44:47], v[36:39], v[16:19]\n\t"
        "buffer_load_dwordx4 v[76:79], v117, %[sw], %[t0] offen offset:1024 sc1\n\t"
        "s_waitcnt vmcnt(13) lgkmcnt(2)\n\t"
        "v_mfma_f32_16x16x32_bf16 v[20:23], v[92:95], v[40:43], v[20:23]\n\t"
        "ds_read_b128 v[92:95], v116 offset:7168\n\t"
        "v_mfma_f32_16x16x32_bf16 v[8:11], a[48:51], v[40:43], v[8:11]\n\t"
        "v_max3_u32 v125, v40, v41, v125\n\t"
        "buffer_load_dwordx4 v[80:83], v117, %[sw], %[t0] offen offset:2048 sc1\n\t"
        "v_mfma_f32_16x16x32_bf16 v[12:15], a[52:55], v[40:43], v[12:15]\n\t"
        "v_max3_u32 v125, v42, v43, v125\n\t"
        "v_mfma_f32_16x16x32_bf16 v[16:19], a[56:59], v[40:43], v[16:19]\n\t"
        "buffer_load_dwordx4 v[84:87], v117, %[sw], %[t0] offen offset:3072 sc1\n\t"
        "s_waitcnt vmcnt(14) lgkmcnt(2)\n\t"
        "v_mfma_f32_16x16x32_bf16 v[20:23], v[96:99], v[44:47], v[20:23]\n\t"
        "ds_read_b128 v[96:99], v116 offset:8192\n\t"
        "v_mfma_f32_16x16x32_bf16 v[8:11], a[60:63], v[44:47], v[8:11]\n\t"
        "v_max3_u32 v125, v44, v45, v125\n\t"
        "s_add_u32 %[t0], %[ro], 4\n\t"
        "s_and_b32 %[t0], %[t0], 7\n\t"
        "s_lshl_b32 %[t0], %[t0], 12\n\t"
        "s_add_u32 %[t0], %[t0], %[nx]\n\t"
        "buffer_load_dwordx4 v[24:27], v117, %[sw], %[t0] offen offset:0 sc1\n\t"
        "v_mfma_f32_16x16x32_bf16 v[12:15], a[64:67], v[44:47], v[12:15]\n\t"
        "v_max3_u32 v125, v46, v47, v125\n\t"
        "v_mfma_f32_16x16x32_bf16 v[16:19], a[68:71], v[44:47], v[16:19]\n\t"
        "buffer_load_dwordx4 v[28:31], v117, %[sw], %[t0] offen offset:1024 sc1\n\t"
        "s_waitcnt vmcnt(15) lgkmcnt(2)\n\t"
        "v_mfma_f32_16x16x32_bf16 v[20:23], v[88:91], v[48:51], v[20:23]\n\t"
        "ds_read_b128 v[88:91], v116 offset:9216\n\t"
        "v_mfma_f32_16x16x32_bf16 v[8:11], a[72:75], v[48:51], v[8:11]\n\t"
        "v_max3_u32 v125, v48, v49, v125\n\t"
        "v_mfma_f32_16x16x32_bf16 v[12:15], a[76:79], v[48:51], v[12:15]\n\t"
        "v_max3_u32 v125, v50, v51, v125\n\t"
        "v_mfma_f32_16x16x32_bf16 v[16:19], a[80:83], v[48:51], v[16:19]\n\t"
        "buffer_load_dwordx4 v[32:35], v117, %[sw], %[t0] offen offset:2048 sc1\n\t"
        "s_waitcnt vmcnt(15) lgkmcnt(2)\n\t"
        "v_mfma_f32_16x16x32_bf16 v[20:23], v[92:95], v[52:55], v[20:23]\n\t"
        "ds_read_b128 v[92:95], v116 offset:10240\n\t"
        "v_mfma_f32_16x16x32_bf16 v[8:11], a[84:87], v[52:55], v[8:11]\n\t"
        "v_max3_u32 v125, v52, v53, v125\n\t"
        "v_mfma_f32_16x16x32_bf16 v[12:15], a[88:91], v[52:55], v[12:15]\n\t"
        "v_max3_u32 v125, v54, v55, v125\n\t"
        "v_mfma_f32_16x16x32_bf16 v[16:19], a[92:95], v[52:55], v[16:19]\n\t"
        "buffer_load_dwordx4 v[36:39], v117, %[sw], %[t0] offen offset:3072 sc1\n\t"
        "s_waitcnt vmcnt(15) lgkmcnt(2)\n\t"
        "s_cmp_lg_u32 %[re], 0\n\t"
        "s_cbranch_scc1 LP_nb%=\n\t"
        "s_barrier\n\t"
        "LP_nb%=:\n\t"
        "v_mfma_f32_16x16x32_bf16 v[20:23], v[96:99], v[56:59], v[20:23]\n\t"
        "ds_read_b128 v[96:99], v116 offset:11264\n\t"
        "v_mfma_f32_16x16x32_bf16 v[8:11], a[96:99], v[56:59], v[8:11]\n\t"
        "v_max3_u32 v125, v56, v57, v125\n\t"
        "v_mfma_f32_16x16x32_bf16 v[12:15], a[100:103], v[56:59], v[12:15]\n\t"
        "v_max3_u32 v125, v58, v59, v125\n\t"
        "v_mfma_f32_16x16x32_bf16 v[16:19], a[104:107], v[56:59], v[16:19]\n\t"
        "s_add_u32 %[t0], %[ro], 5\n\t"
        "s_and_b32 %[t0], %[t0], 7\n\t"
        "s_lshl_b32 %[t0], %[t0], 12\n\t"
        "s_add_u32 %[t0], %[t0], %[nx]\n\t"
        "buffer_load_dwordx4 v[40:43], v117, %[sw], %[t0] offen offset:0 sc1\n\t"
        "s_waitcnt vmcnt(15) lgkmcnt(2)\n\t"
        "v_mfma_f32_16x16x32_bf16 v[20:23], v[88:91], v[60:63], v[20:23]\n\t"
        "ds_read_b128 v[88:91], v116 offset:12288\n\t"
        "v_mfma_f32_16x16x32_bf16 v[8:11], a[108:111], v[60:63], v[8:11]\n\t"
        "v_max3_u32 v125, v60, v61, v125\n\t"
        "v_mfma_f32_16x16x32_bf16 v[12:15], a[112:115], v[60:63], v[12:15]\n\t"
        "v_max3_u32 v125, v62, v63, v125\n\t"
        "v_mfma_f32_16x16x32_bf16 v[16:19], a[116:119], v[60:63], v[16:19]\n\t"
        "buffer_load_dwordx4 v[44:47], v117, %[sw], %[t0] offen offset:1024 sc1\n\t"
        "s_waitcnt vmcnt(11) lgkmcnt(2)\n\t"
        "v_mfma_f32_16x16x32_bf16 v[20:23], v[92:95], v[64:67], v[20:23]\n\t"
        "ds_read_b128 v[92:95], v116 offset:13312\n\t"
        "v_mfma_f32_16x16x32_bf16 v[8:11], a[120:123], v[64:67], v[8:11]\n\t"
        "v_max3_u32 v125, v64, v65, v125\n\t"
        "v_mfma_f32_16x16x32_bf16 v[12:15], a[124:127], v[64:67], v[12:15]\n\t"
        "v_max3_u32 v125, v66, v67, v125\n\t"
        "v_mfma_f32_16x16x32_bf16 v[16:19], a[128:131], v[64:67], v[16:19]\n\t"
        "buffer_load_dwordx4 v[48:51], v117, %[sw], %[t0] offen offset:2048 sc1\n\t"
        "s_waitcnt vmcnt(11) lgkmcnt(2)\n\t"
        "v_mfma_f32_16x16x32_bf16 v[20:23], v[96:99], v[68:71], v[20:23]\n\t"
        "ds_read_b128 v[96:99], v116 offset:14336\n\t"
        "v_mfma_f32_16x16x32_bf16 v[8:11], a[132:135], v[68:71], v[8:11]\n\t"
        "v_max3_u32 v125, v68, v69, v125\n\t"
        "v_mfma_f32_16x16x32_bf16 v[12:15], a[136:139], v[68:71], v[12:15]\n\t"
        "v_max3_u32 v125, v70, v71, v125\n\t"
        "v_mfma_f32_16x16x32_bf16 v[16:19], a[140:143], v[68:71], v[16:19]\n\t"
        "buffer_load_dwordx4 v[52:55], v117, %[sw], %[t0] offen offset:3072 sc1\n\t"
        "s_waitcnt vmcnt(11) lgkmcnt(2)\n\t"
        "v_mfma_f32_16x16x32_bf16 v[20:23], v[88:91], v[72:75], v[20:23]\n\t"
        "ds_read_b128 v[88:91], v116 offset:15360\n\t"
        "v_mfma_f32_16x16x32_bf16 v[8:11], a[144:147], v[72:75], v[8:11]\n\t"
        "v_max3_u32 v125, v72, v73, v125\n\t"
        "v_mfma_f32_16x16x32_bf16 v[12:15], a[148:151], v[72:75], v[12:15]\n\t"
        "v_max3_u32 v125, v74, v75, v125\n\t"
        "v_mfma_f32_16x16x32_bf16 v[16:19], a[152:155], v[72:75], v[16:19]\n\t"
        "s_add_u32 %[t0], %[ro], 6\n\t"
        "s_and_b32 %[t0], %[t0], 7\n\t"
        "s_lshl_b32 %[t0], %[t0], 12\n\t"
        "s_add_u32 %[t0], %[t0], %[nx]\n\t"
        "buffer_load_dwordx4 v[56:59], v117, %[sw], %[t0] offen offset:0 sc1\n\t"
        "s_waitcnt vmcnt(11) lgkmcnt(2)\n\t"
        "v_mfma_f32_16x16x32_bf16 v[20:23], v[92:95], v[76:79], v[20:23]\n\t"
        "ds_read_b128 v[92:95], v116 offset:16384\n\t"
        "v_mfma_f32_16x16x32_bf16 v[8:11], a[156:159], v[76:79], v[8:11]\n\t"
        "v_max3_u32 v125, v76, v77, v125\n\t"
        "v_mfma_f32_16x16x32_bf16 v[12:15], a[160:163], v[76:79], v[12:15]\n\t"
        "v_max3_u32 v125, v78, v79, v125\n\t"
        "v_mfma_f32_16x16x32_bf16 v[16:19], a[164:167], v[76:79], v[16:19]\n\t"
        "buffer_load_dwordx4 v[60:63], v117, %[sw], %[t0] offen offset:1024 sc1\n\t"
        "s_waitcnt vmcnt(11) lgkmcnt(2)\n\t"
        "v_mfma_f32_16x16x32_bf16 v[20:23], v[96:99], v[80:83], v[20:23]\n\t"
        "ds_read_b128 v[96:99], v116 offset:17408\n\t"
        "v_mfma_f32_16x16x32_bf16 v[8:11], a[168:171], v[80:83], v[8:11]\n\t"
        "v_max3_u32 v125, v80, v81, v125\n\t"
        "v_mfma_f32_16x16x32_bf16 v[12:15], a[172:175], v[80:83], v[12:15]\n\t"
        "v_max3_u32 v125, v82, v83, v125\n\t"
        "v_mfma_f32_16x16x32_bf16 v[16:19], a[176:179], v[80:83], v[16:19]\n\t"
        "buffer_load_dwordx4 v[64:67], v117, %[sw], %[t0] offen offset:2048 sc1\n\t"
        "s_waitcnt vmcnt(11) lgkmcnt(2)\n\t"
        "v_mfma_f32_16x16x32_bf16 v[20:23], v[88:91], v[84:87], v[20:23]\n\t"
        "ds_read_b128 v[88:91], v116 offset:18432\n\t"
        "v_mfma_f32_16x16x32_bf16 v[8:11], a[180:183], v[84:87], v[8:11]\n\t"
        "v_max3_u32 v125, v84, v85, v125\n\t"
        "v_mfma_f32_16x16x32_bf16 v[12:15], a[184:187], v[84:87], v[12:15]\n\t"
        "v_max3_u32 v125, v86, v87, v125\n\t"
        "v_mfma_f32_16x16x32_bf16 v[16:19], a[188:191], v[84:87], v[16:19]\n\t"
        "buffer_load_dwordx4 v[68:71], v117, %[sw], %[t0] offen offset:3072 sc1\n\t"
        "s_waitcnt vmcnt(11) lgkmcnt(2)\n\t"
        "v_mfma_f32_16x16x32_bf16 v[20:23], v[92:95], v[24:27], v[20:23]\n\t"
        "ds_read_b128 v[92:95], v116 offset:19456\n\t"
        "v_mfma_f32_16x16x32_bf16 v[8:11], a[192:195], v[24:27], v[8:11]\n\t"
        "v_max3_u32 v125, v24, v25, v125\n\t"
        "v_mfma_f32_16x16x32_bf16 v[12:15], a[196:199], v[24:27], v[12:15]\n\t"
        "v_max3_u32 v125, v26, v27, v125\n\t"
        "v_mfma_f32_16x16x32_bf16 v[16:19], a[200:203], v[24:27], v[16:19]\n\t"
        "s_add_u32 %[t0], %[ro], 7\n\t"
        "s_and_b32 %[t0], %[t0], 7\n\t"
        "s_lshl_b32 %[t0], %[t0], 12\n\t"
        "s_add_u32 %[t0], %[t0], %[nx]\n\t"
        "buffer_load_dwordx4 v[72:75], v117, %[sw], %[t0] offen offset:0 sc1\n\t"
        "s_waitcnt vmcnt(11) lgkmcnt(2)\n\t"
        "v_mfma_f32_16x16x32_bf16 v[20:23], v[96:99], v[28:31], v[20:23]\n\t"
        "ds_read_b128 v[96:99], v116 offset:20480\n\t"
        "v_mfma_f32_16x16x32_bf16 v[8:11], a[204:207], v[28:31], v[8:11]\n\t"
        "v_max3_u32 v125, v28, v29, v125\n\t"
        "v_mfma_f32_16x16x32_bf16 v[12:15], a[208:211], v[28:31], v[12:15]\n\t"
        "v_max3_u32 v125, v30, v31, v125\n\t"
        "v_sub_f32_e32 v104, 1.0, v102\n\t"
        "v_mfma_f32_16x16x32_bf16 v[16:19], a[212:215], v[28:31], v[16:19]\n\t"
        "buffer_load_dwordx4 v[76:79], v117, %[sw], %[t0] offen offset:1024 sc1\n\t"
        "s_waitcnt vmcnt(11) lgkmcnt(2)\n\t"
        "v_mfma_f32_16x16x32_bf16 v[20:23], v[88:91], v[32:35], v[20:23]\n\t"
        "ds_read_b128 v[88:91], v116 offset:21504\n\t"
        "v_mfma_f32_16x16x32_bf16 v[8:11], a[216:219], v[32:35], v[8:11]\n\t"
        "v_max3_u32 v125, v32, v33, v125\n\t"
        "v_mfma_f32_16x16x32_bf16 v[12:15], a[220:223], v[32:35], v[12:15]\n\t"
        "v_max3_u32 v125, v34, v35, v125\n\t"
        "v_cvt_pk_bf16_f32 v104, 0, v104\n\t"
        "v_mfma_f32_16x16x32_bf16 v[16:19], a[224:227], v[32:35], v[16:19]\n\t"
        "buffer_load_dwordx4 v[80:83], v117, %[sw], %[t0] offen offset:2048 sc1\n\t"
        "s_waitcnt vmcnt(11) lgkmcnt(2)\n\t"
        "v_mfma_f32_16x16x32_bf16 v[20:23], v[92:95], v[36:39], v[20:23]\n\t"
        "ds_read_b128 v[92:95], v116 offset:22528\n\t"
        "v_mfma_f32_16x16x32_bf16 v[8:11], a[228:231], v[36:39], v[8:11]\n\t"
        "v_max3_u32 v125, v36, v37, v125\n\t"
        "v_mfma_f32_16x16x32_bf16 v[12:15], a[232:235], v[36:39], v[12:15]\n\t"
        "v_max3_u32 v125, v38, v39, v125\n\t"
        "v_lshlrev_b32_e32 v106, 16, v100\n\t"
        "v_mfma_f32_16x16x32_bf16 v[16:19], a[236:239], v[36:39], v[16:19]\n\t"
        "buffer_load_dwordx4 v[84:87], v117, %[sw], %[t0] offen offset:3072 sc1\n\t"
        "s_waitcnt vmcnt(11) lgkmcnt(2)\n\t"
        "v_mfma_f32_16x16x32_bf16 v[20:23], v[96:99], v[40:43], v[20:23]\n\t"
        "ds_read_b128 v[96:99], v116 offset:23552\n\t"
        "v_mfma_f32_16x16x32_bf16 v[8:11], a[240:243], v[40:43], v[8:11]\n\t"
        "v_max3_u32 v125, v40, v41, v125\n\t"
        "v_mfma_f32_16x16x32_bf16 v[12:15], a[244:247], v[40:43], v[12:15]\n\t"
        "v_max3_u32 v125, v42, v43, v125\n\t"
        "v_mul_f32_e32 v106, v102, v106\n\t"
        "v_mfma_f32_16x16x32_bf16 v[16:19], a[248:251], v[40:43], v[16:19]\n\t"
        "s_waitcnt vmcnt(10) lgkmcnt(2)\n\t"
        "v_mfma_f32_16x16x32_bf16 v[20:23], v[88:91], v[44:47], v[20:23]\n\t"
        "ds_read_b128 v[88:91], v116 offset:24576\n\t"
        "v_mfma_f32_16x16x32_bf16 v[8:11], a[252:255], v[44:47], v[8:11]\n\t"
        "v_max3_u32 v125, v44, v45, v125\n\t"
        "v_mfma_f32_16x16x32_bf16 v[12:15], v[128:131], v[44:47], v[12:15]\n\t"
        "v_max3_u32 v125, v46, v47, v125\n\t"
        "v_cvt_pk_bf16_f32 v106, 0, v106\n\t"
        "v_mfma_f32_16x16x32_bf16 v[16:19], v[132:135], v[44:47], v[16:19]\n\t"
        "s_waitcnt vmcnt(9) lgkmcnt(2)\n\t"
        "v_mfma_f32_16x16x32_bf16 v[20:23], v[92:95], v[48:51], v[20:23]\n\t"
        "ds_read_b128 v[92:95], v116 offset:25600\n\t"
        "v_mfma_f32_16x16x32_bf16 v[8:11], v[136:139], v[48:51], v[8:11]\n\t"
        "v_max3_u32 v125, v48, v49, v125\n\t"
        "v_mfma_f32_16x16x32_bf16 v[12:15], v[140:143], v[48:51], v[12:15]\n\t"
        "v_max3_u32 v125, v50, v51, v125\n\t"
        "v_and_b32_e32 v107, 0xffff0000, v100\n\t"
        "v_mfma_f32_16x16x32_bf16 v[16:19], v[144:147], v[48:51], v[16:19]\n\t"
        "s_waitcnt vmcnt(8) lgkmcnt(2)\n\t"
        "v_mfma_f32_16x16x32_bf16 v[20:23], v[96:99], v[52:55], v[20:23]\n\t"
        "ds_read_b128 v[96:99], v116 offset:26624\n\t"
        "v_mfma_f32_16x16x32_bf16 v[8:11], v[148:151], v[52:55], v[8:11]\n\t"
        "v_max3_u32 v125, v52, v53, v125\n\t"
        "v_mfma_f32_16x16x32_bf16 v[12:15], v[152:155], v[52:55], v[12:15]\n\t"
        "v_max3_u32 v125, v54, v55, v125\n\t"
        "v_mul_f32_e32 v107, v102, v107\n\t"
        "v_mfma_f32_16x16x32_bf16 v[16:19], v[156:159], v[52:55], v[16:19]\n\t"
        "s_waitcnt vmcnt(7) lgkmcnt(2)\n\t"
        "v_mfma_f32_16x16x32_bf16 v[20:23], v[88:91], v[56:59], v[20:23]\n\t"
        "ds_read_b128 v[88:91], v116 offset:27648\n\t"
        "v_mfma_f32_16x16x32_bf16 v[8:11], v[160:163], v[56:59], v[8:11]\n\t"
        "v_max3_u32 v125, v56, v57, v125\n\t"
        "v_mfma_f32_16x16x32_bf16 v[12:15], v[164:167], v[56:59], v[12:15]\n\t"
        "v_max3_u32 v125, v58, v59, v125\n\t"
        "v_cvt_pk_bf16_f32 v107, 0, v107\n\t"
        "v_mfma_f32_16x16x32_bf16 v[16:19], v[168:171], v[56:59], v[16:19]\n\t"
        "s_waitcnt vmcnt(6) lgkmcnt(2)\n\t"
        "v_mfma_f32_16x16x32_bf16 v[20:23], v[92:95], v[60:63], v[20:23]\n\t"
        "ds_read_b128 v[92:95], v116 offset:28672\n\t"
        "v_mfma_f32_16x16x32_bf16 v[8:11], v[172:175], v[60:63], v[8:11]\n\t"
        "v_max3_u32 v125, v60, v61, v125\n\t"
        "v_mfma_f32_16x16x32_bf16 v[12:15], v[176:179], v[60:63], v[12:15]\n\t"
        "v_max3_u32 v125, v62, v63, v125\n\t"
        "v_lshlrev_b32_e32 v108, 16, v101\n\t"
        "v_mfma_f32_16x16x32_bf16 v[16:19], v[180:183], v[60:63], v[16:19]\n\t"
        "s_waitcnt vmcnt(5) lgkmcnt(2)\n\t"
        "v_mfma_f32_16x16x32_bf16 v[20:23], v[96:99], v[64:67], v[20:23]\n\t"
        "ds_read_b128 v[96:99], v116 offset:29696\n\t"
        "v_mfma_f32_16x16x32_bf16 v[8:11], v[184:187], v[64:67], v[8:11]\n\t"
        "v_max3_u32 v125, v64, v65, v125\n\t"
        "v_mfma_f32_16x16x32_bf16 v[12:15], v[188:191], v[64:67], v[12:15]\n\t"
        "v_max3_u32 v125, v66, v67, v125\n\t"
        "v_mul_f32_e32 v108, v102, v108\n\t"
        "v_mfma_f32_16x16x32_bf16 v[16:19], v[192:195], v[64:67], v[16:19]\n\t"
        "s_waitcnt vmcnt(4) lgkmcnt(2)\n\t"
        "v_mfma_f32_16x16x32_bf16 v[20:23], v[88:91], v[68:71], v[20:23]\n\t"
        "ds_read_b128 v[88:91], v116 offset:30720\n\t"
        "v_mfma_f32_16x16x32_bf16 v[8:11], v[196:199], v[68:71], v[8:11]\n\t"
        "v_max3_u32 v125, v68, v69, v125\n\t"
        "v_mfma_f32_16x16x32_bf16 v[12:15], v[200:203], v[68:71], v[12:15]\n\t"
        "v_max3_u32 v125, v70, v71, v125\n\t"
        "v_cvt_pk_bf16_f32 v108, 0, v108\n\t"
        "v_mfma_f32_16x16x32_bf16 v[16:19], v[204:207], v[68:71], v[16:19]\n\t"
        "s_waitcnt vmcnt(3) lgkmcnt(2)\n\t"
        "v_mfma_f32_16x16x32_bf16 v[20:23], v[92:95], v[72:75], v[20:23]\n\t"
        "ds_read_b128 v[92:95], v116 offset:31744\n\t"
        "v_mfma_f32_16x16x32_bf16 v[8:11], v[208:211], v[72:75], v[8:11]\n\t"
        "v_max3_u32 v125, v72, v73, v125\n\t"
        "v_mfma_f32_16x16x32_bf16 v[12:15], v[212:215], v[72:75], v[12:15]\n\t"
        "v_max3_u32 v125, v74, v75, v125\n\t"
        "v_and_b32_e32 v109, 0xffff0000, v101\n\t"
        "v_mfma_f32_16x16x32_bf16 v[16:19], v[216:219], v[72:75], v[16:19]\n\t"
        "s_waitcnt vmcnt(2) lgkmcnt(2)\n\t"
        "v_mfma_f32_16x16x32_bf16 v[20:23], v[96:99], v[76:79], v[20:23]\n\t"
        "v_mfma_f32_16x16x32_bf16 v[8:11], v[220:223], v[76:79], v[8:11]\n\t"
        "v_max3_u32 v125, v76, v77, v125\n\t"
        "v_mfma_f32_16x16x32_bf16 v[12:15], v[224:227], v[76:79], v[12:15]\n\t"
        "v_max3_u32 v125, v78, v79, v125\n\t"
        "v_mul_f32_e32 v109, v102, v109\n\t"
        "v_mfma_f32_16x16x32_bf16 v[16:19], v[228:231], v[76:79], v[16:19]\n\t"
        "s_waitcnt vmcnt(1) lgkmcnt(1)\n\t"
        "v_mfma_f32_16x16x32_bf16 v[20:23], v[88:91], v[80:83], v[20:23]\n\t"
        "ds_read_b128 v[88:91], v116 offset:0\n\t"
        "v_mfma_f32_16x16x32_bf16 v[8:11], v[232:235], v[80:83], v[8:11]\n\t"
        "v_max3_u32 v125, v80, v81, v125\n\t"
        "v_mfma_f32_16x16x32_bf16 v[12:15], v[236:239], v[80:83], v[12:15]\n\t"
        "v_max3_u32 v125, v82, v83, v125\n\t"
        "v_cvt_pk_bf16_f32 v109, 0, v109\n\t"
        "v_mfma_f32_16x16x32_bf16 v[16:19], v[240:243], v[80:83], v[16:19]\n\t"
        "s_waitcnt vmcnt(0) lgkmcnt(1)\n\t"
        "v_mfma_f32_16x16x32_bf16 v[20:23], v[92:95], v[84:87], v[20:23]\n\t"
        "ds_read_b128 v[92:95], v116 offset:1024\n\t"
        "v_mfma_f32_16x16x32_bf16 v[8:11], v[244:247], v[84:87], v[8:11]\n\t"
        "ds_read_b128 v[96:99], v116 offset:2048\n\t"
        "v_max3_u32 v125, v84, v85, v125\n\t"
        "v_mfma_f32_16x16x32_bf16 v[12:15], v[248:251], v[84:87], v[12:15]\n\t"
        "v_max3_u32 v125, v86, v87, v125\n\t"
        "v_mfma_f32_16x16x32_bf16 v[16:19], v[252:255], v[84:87], v[16:19]\n\t"
        "v_cmp_eq_u32_e32 vcc, -1, v125\n\t"
        "s_mov_b32 %[nx], 0xf0000000\n\t"
        "s_mov_b32 %[re], 64\n\t"
        "s_cbranch_vccz LP_epi%=\n\t"
        "s_waitcnt lgkmcnt(0)\n\t"
        "s_branch LP_again%=\n\t"
        "LP_run%=:\n\t"
        "s_add_u32 %[t0], %[ro], 0\n\t"
        "s_and_b32 %[t0], %[t0], 7\n\t"
        "s_lshl_b32 %[t0], %[t0], 12\n\t"
        "s_add_u32 %[t0], %[t0], %[nx]\n\t"
        "buffer_load_dwordx4 v[24:27], v117, %[sw], %[t0] offen offset:0 sc1\n\t"
        "buffer_load_dwordx4 v[28:31], v117, %[sw], %[t0] offen offset:1024 sc1\n\t"
        "buffer_load_dwordx4 v[32:35], v117, %[sw], %[t0] offen offset:2048 sc1\n\t"
        "buffer_load_dwordx4 v[36:39], v117, %[sw], %[t0] offen offset:3072 sc1\n\t"
        "s_add_u32 %[t0], %[ro], 1\n\t"
        "s_and_b32 %[t0], %[t0], 7\n\t"
        "s_lshl_b32 %[t0], %[t0], 12\n\t"
        "s_add_u32 %[t0], %[t0], %[nx]\n\t"
        "buffer_load_dwordx4 v[40:43], v117, %[sw], %[t0] offen offset:0 sc1\n\t"
        "buffer_load_dwordx4 v[44:47], v117, %[sw], %[t0] offen offset:1024 sc1\n\t"
        "buffer_load_dwordx4 v[48:51], v117, %[sw], %[t0] offen offset:2048 sc1\n\t"
        "buffer_load_dwordx4 v[52:55], v117, %[sw], %[t0] offen offset:3072 sc1\n\t"
        "s_add_u32 %[t0], %[ro], 2\n\t"
        "s_and_b32 %[t0], %[t0], 7\n\t"
        "s_lshl_b32 %[t0], %[t0], 12\n\t"
        "s_add_u32 %[t0], %[t0], %[nx]\n\t"
        "buffer_load_dwordx4 v[56:59], v117, %[sw], %[t0] offen offset:0 sc1\n\t"
        "buffer_load_dwordx4 v[60:63], v117, %[sw], %[t0] offen offset:1024 sc1\n\t"
        "buffer_load_dwordx4 v[64:67], v117, %[sw], %[t0] offen offset:2048 sc1\n\t"
        "buffer_load_dwordx4 v[68:71], v117, %[sw], %[t0] offen offset:3072 sc1\n\t"
        "s_add_u32 %[t0], %[ro], 3\n\t"
        "s_and_b32 %[t0], %[t0], 7\n\t"
        "s_lshl_b32 %[t0], %[t0], 12\n\t"
        "s_add_u32 %[t0], %[t0], %[nx]\n\t"
        "buffer_load_dwordx4 v[72:75], v117, %[sw], %[t0] offen offset:0 sc1\n\t"
        "buffer_load_dwordx4 v[76:79], v117, %[sw], %[t0] offen offset:1024 sc1\n\t"
        "buffer_load_dwordx4 v[80:83], v117, %[sw], %[t0] offen offset:2048 sc1\n\t"
        "buffer_load_dwordx4 v[84:87], v117, %[sw], %[t0] offen offset:3072 sc1\n\t"
        "buffer_load_dword v103, v118, %[sw], 0 offen sc1\n\t"
        "LP_tile%=:\n\t"
        "s_and_b32 %[sp], %[tl], 1\n\t"
        "s_lshl_b32 %[sp], %[sp], 14\n\t"
        "s_lshr_b32 %[t1], %[tl], 1\n\t"
        "s_lshl_b32 %[t1], %[t1], 6\n\t"
        "s_add_u32 %[t1], %[sp], %[t1]\n\t"
        "buffer_load_dword v100, v119, %[sw], %[t1] offen sc1\n\t"
        "s_add_u32 %[t1], %[t1], 32768\n\t"
        "buffer_load_dword v101, v119, %[sw], %[t1] offen sc1\n\t"
        "s_lshl_b32 %[t1], %[tl], 9\n\t"
        "buffer_load_dword v102, v120, %[sw], %[t1] offen sc1\n\t"
        "buffer_load_dword v103, v118, %[sw], 0 offen sc1\n\t"
        "s_mov_b32 %[cu], %[nx]\n\t"
        "s_waitcnt vmcnt(20) lgkmcnt(2)\n\t"
        "v_mfma_f32_16x16x32_bf16 v[20:23], v[88:91], v[24:27], 0\n\t"
        "ds_read_b128 v[88:91], v116 offset:3072\n\t"
        "v_mfma_f32_16x16x32_bf16 v[8:11], a[0:3], v[24:27], 0\n\t"
        "v_mfma_f32_16x16x32_bf16 v[12:15], a[4:7], v[24:27], 0\n\t"
        "v_mfma_f32_16x16x32_bf16 v[16:19], a[8:11], v[24:27], 0\n\t"
        "s_add_u32 %[t0], %[ro], 4\n\t"
        "s_and_b32 %[t0], %[t0], 7\n\t"
        "s_lshl_b32 %[t0], %[t0], 12\n\t"
        "s_add_u32 %[t0], %[t0], %[cu]\n\t"
        "buffer_load_dwordx4 v[24:27], v117, %[sw], %[t0] offen offset:0 sc1\n\t"
        "s_waitcnt vmcnt(20) lgkmcnt(2)\n\t"
        "v_mfma_f32_16x16x32_bf16 v[20:23], v[92:95], v[28:31], v[20:23]\n\t"
        "ds_read_b128 v[92:95], v116 offset:4096\n\t"
        "v_mfma_f32_16x16x32_bf16 v[8:11], a[12:15], v[28:31], v[8:11]\n\t"
        "v_mfma_f32_16x16x32_bf16 v[12:15], a[16:19], v[28:31], v[12:15]\n\t"
        "v_mfma_f32_16x16x32_bf16 v[16:19], a[20:23], v[28:31], v[16:19]\n\t"
        "buffer_load_dwordx4 v[28:31], v117, %[sw], %[t0] offen offset:1024 sc1\n\t"
        "s_waitcnt vmcnt(20) lgkmcnt(2)\n\t"
        "v_mfma_f32_16x16x32_bf16 v[20:23], v[96:99], v[32:35], v[20:23]\n\t"
        "ds_read_b128 v[96:99], v116 offset:5120\n\t"
        "v_mfma_f32_16x16x32_bf16 v[8:11], a[24:27], v[32:35], v[8:11]\n\t"
        "v_mfma_f32_16x16x32_bf16 v[12:15], a[28:31], v[32:35], v[12:15]\n\t"
        "v_mfma_f32_16x16x32_bf16 v[16:19], a[32:35], v[32:35], v[16:19]\n\t"
        "buffer_load_dwordx4 v[32:35], v117, %[sw], %[t0] offen offset:2048 sc1\n\t"
        "s_waitcnt vmcnt(20) lgkmcnt(2)\n\t"
        "v_mfma_f32_16x16x32_bf16 v[20:23], v[88:91], v[36:39], v[20:23]\n\t"
        "ds_read_b128 v[88:91], v116 offset:6144\n\t"
        "v_mfma_f32_16x16x32_bf16 v[8:11], a[36:39], v[36:39], v[8:11]\n\t"
        "v_mfma_f32_16x16x32_bf16 v[12:15], a[40:43], v[36:39], v[12:15]\n\t"
        "v_mfma_f32_16x16x32_bf16 v[16:19], a[44:47], v[36:39], v[16:19]\n\t"
        "buffer_load_dwordx4 v[36:39], v117, %[sw], %[t0] offen offset:3072 sc1\n\t"
        "s_waitcnt vmcnt(20) lgkmcnt(2)\n\t"
        "v_mfma_f32_16x16x32_bf16 v[20:23], v[92:95], v[40:43], v[20:23]\n\t"
        "ds_read_b128 v[92:95], v116 offset:7168\n\t"
        "v_mfma_f32_16x16x32_bf16 v[8:11], a[48:51], v[40:43], v[8:11]\n\t"
        "v_mfma_f32_16x16x32_bf16 v[12:15], a[52:55], v[40:43], v[12:15]\n\t"
        "v_mfma_f32_16x16x32_bf16 v[16:19], a[56:59], v[40:43], v[16:19]\n\t"
        "s_add_u32 %[t0], %[ro], 5\n\t"
        "s_and_b32 %[t0], %[t0], 7\n\t"
        "s_lshl_b32 %[t0], %[t0], 12\n\t"
        "s_add_u32 %[t0], %[t0], %[cu]\n\t"
        "buffer_load_dwordx4 v[40:43], v117, %[sw], %[t0] offen offset:0 sc1\n\t"
        "s_waitcnt vmcnt(20) lgkmcnt(2)\n\t"
        "v_mfma_f32_16x16x32_bf16 v[20:23], v[96:99], v[44:47], v[20:23]\n\t"
        "ds_read_b128 v[96:99], v116 offset:8192\n\t"
        "v_mfma_f32_16x16x32_bf16 v[8:11], a[60:63], v[44:47], v[8:11]\n\t"
        "v_mfma_f32_16x16x32_bf16 v[12:15], a[64:67], v[44:47], v[12:15]\n\t"
        "v_mfma_f32_16x16x32_bf16 v[16:19], a[68:71], v[44:47], v[16:19]\n\t"
        "buffer_load_dwordx4 v[44:47], v117, %[sw], %[t0] offen offset:1024 sc1\n\t"
        "s_waitcnt vmcnt(20) lgkmcnt(2)\n\t"
        "v_mfma_f32_16x16x32_bf16 v[20:23], v[88:91], v[48:51], v[20:23]\n\t"
        "ds_read_b128 v[88:91], v116 offset:9216\n\t"
        "v_mfma_f32_16x16x32_bf16 v[8:11], a[72:75], v[48:51], v[8:11]\n\t"
        "v_mfma_f32_16x16x32_bf16 v[12:15], a[76:79], v[48:51], v[12:15]\n\t"
        "v_mfma_f32_16x16x32_bf16 v[16:19], a[80:83], v[48:51], v[16:19]\n\t"
        "buffer_load_dwordx4 v[48:51], v117, %[sw], %[t0] offen offset:2048 sc1\n\t"
        "s_waitcnt vmcnt(20) lgkmcnt(2)\n\t"
        "v_mfma_f32_16x16x32_bf16 v[20:23], v[92:95], v[52:55], v[20:23]\n\t"
        "ds_read_b128 v[92:95], v116 offset:10240\n\t"
        "v_mfma_f32_16x16x32_bf16 v[8:11], a[84:87], v[52:55], v[8:11]\n\t"
        "v_mfma_f32_16x16x32_bf16 v[12:15], a[88:91], v[52:55], v[12:15]\n\t"
        "v_mfma_f32_16x16x32_bf16 v[16:19], a[92:95], v[52:55], v[16:19]\n\t"
        "buffer_load_dwordx4 v[52:55], v117, %[sw], %[t0] offen offset:3072 sc1\n\t"
        "s_waitcnt vmcnt(20) lgkmcnt(2)\n\t"
        "s_barrier\n\t"
        "v_mfma_f32_16x16x32_bf16 v[20:23], v[96:99], v[56:59], v[20:23]\n\t"
        "ds_read_b128 v[96:99], v116 offset:11264\n\t"
        "v_mfma_f32_16x16x32_bf16 v[8:11], a[96:99], v[56:59], v[8:11]\n\t"
        "v_mfma_f32_16x16x32_bf16 v[12:15], a[100:103], v[56:59], v[12:15]\n\t"
        "v_mfma_f32_16x16x32_bf16 v[16:19], a[104:107], v[56:59], v[16:19]\n\t"
        "s_add_u32 %[t0], %[ro], 6\n\t"
        "s_and_b32 %[t0], %[t0], 7\n\t"
        "s_lshl_b32 %[t0], %[t0], 12\n\t"
        "s_add_u32 %[t0], %[t0], %[cu]\n\t"
        "buffer_load_dwordx4 v[56:59], v117, %[sw], %[t0] offen offset:0 sc1\n\t"
        "s_waitcnt vmcnt(20) lgkmcnt(2)\n\t"
        "v_mfma_f32_16x16x32_bf16 v[20:23], v[88:91], v[60:63], v[20:23]\n\t"
        "ds_read_b128 v[88:91], v116 offset:12288\n\t"
        "v_mfma_f32_16x16x32_bf16 v[8:11], a[108:111], v[60:63], v[8:11]\n\t"
        "v_mfma_f32_16x16x32_bf16 v[12:15], a[112:115], v[60:63], v[12:15]\n\t"
        "v_mfma_f32_16x16x32_bf16 v[16:19], a[116:119], v[60:63], v[16:19]\n\t"
        "buffer_load_dwordx4 v[60:63], v117, %[sw], %[t0] offen offset:1024 sc1\n\t"
        "s_waitcnt vmcnt(20) lgkmcnt(2)\n\t"
        "v_mfma_f32_16x16x32_bf16 v[20:23], v[92:95], v[64:67], v[20:23]\n\t"
        "ds_read_b128 v[92:95], v116 offset:13312\n\t"
        "v_mfma_f32_16x16x32_bf16 v[8:11], a[120:123], v[64:67], v[8:11]\n\t"
        "v_mfma_f32_16x16x32_bf16 v[12:15], a[124:127], v[64:67], v[12:15]\n\t"
        "v_mfma_f32_16x16x32_bf16 v[16:19], a[128:131], v[64:67], v[16:19]\n\t"
        "buffer_load_dwordx4 v[64:67], v117, %[sw], %[t0] offen offset:2048 sc1\n\t"
        "s_waitcnt vmcnt(20) lgkmcnt(2)\n\t"
        "v_mfma_f32_16x16x32_bf16 v[20:23], v[96:99], v[68:71], v[20:23]\n\t"
        "ds_read_b128 v[96:99], v116 offset:14336\n\t"
        "v_mfma_f32_16x16x32_bf16 v[8:11], a[132:135], v[68:71], v[8:11]\n\t"
        "v_mfma_f32_16x16x32_bf16 v[12:15], a[136:139], v[68:71], v[12:15]\n\t"
        "v_mfma_f32_16x16x32_bf16 v[16:19], a[140:143], v[68:71], v[16:19]\n\t"
        "buffer_load_dwordx4 v[68:71], v117, %[sw], %[t0] offen offset:3072 sc1\n\t"
        "s_waitcnt vmcnt(20) lgkmcnt(2)\n\t"
        "v_mfma_f32_16x16x32_bf16 v[20:23], v[88:91], v[72:75], v[20:23]\n\t"
        "ds_read_b128 v[88:91], v116 offset:15360\n\t"
        "v_mfma_f32_16x16x32_bf16 v[8:11], a[144:147], v[72:75], v[8:11]\n\t"
        "v_mfma_f32_16x16x32_bf16 v[12:15], a[148:151], v[72:75], v[12:15]\n\t"
        "v_mfma_f32_16x16x32_bf16 v[16:19], a[152:155], v[72:75], v[16:19]\n\t"
        "s_add_u32 %[t0], %[ro], 7\n\t"
        "s_and_b32 %[t0], %[t0], 7\n\t"
        "s_lshl_b32 %[t0], %[t0], 12\n\t"
        "s_add_u32 %[t0], %[t0], %[cu]\n\t"
        "buffer_load_dwordx4 v[72:75], v117, %[sw], %[t0] offen offset:0 sc1\n\t"
        "s_waitcnt vmcnt(13) lgkmcnt(2)\n\t"
        "v_mfma_f32_16x16x32_bf16 v[20:23], v[92:95], v[76:79], v[20:23]\n\t"
        "ds_read_b128 v[92:95], v116 offset:16384\n\t"
        "v_min_u32_dpp v103, v103, v103 quad_perm:[1,0,3,2] row_mask:0xf bank_mask:0xf\n\t"
        "s_nop 0\n\t"
        "v_mfma_f32_16x16x32_bf16 v[8:11], a[156:159], v[76:79], v[8:11]\n\t"
        "v_min_u32_dpp v103, v103, v103 quad_perm:[2,3,0,1] row_mask:0xf bank_mask:0xf\n\t"
        "s_nop 0\n\t"
        "v_mfma_f32_16x16x32_bf16 v[12:15], a[160:163], v[76:79], v[12:15]\n\t"
        "v_min_u32_dpp v103, v103, v103 row_ror:4 row_mask:0xf bank_mask:0xf\n\t"
        "s_nop 0\n\t"
        "v_mfma_f32_16x16x32_bf16 v[16:19], a[164:167], v[76:79], v[16:19]\n\t"
        "buffer_load_dwordx4 v[76:79], v117, %[sw], %[t0] offen offset:1024 sc1\n\t"
        "s_waitcnt vmcnt(20) lgkmcnt(2)\n\t"
        "v_mfma_f32_16x16x32_bf16 v[20:23], v[96:99], v[80:83], v[20:23]\n\t"
        "ds_read_b128 v[96:99], v116 offset:17408\n\t"
        "v_readfirstlane_b32 %[pg], v103\n\t"
        "s_add_u32 %[t1], %[tl], 8\n\t"
        "s_lshl_b32 %[nx], %[t1], 17\n\t"
        "s_lshr_b32 %[t1], %[t1], 1\n\t"
        "v_mfma_f32_16x16x32_bf16 v[8:11], a[168:171], v[80:83], v[8:11]\n\t"
        "s_cmp_gt_u32 %[pg], %[t1]\n\t"
        "s_cselect_b32 %[nx], %[nx], 0xf0000000\n\t"
        "v_mfma_f32_16x16x32_bf16 v[12:15], a[172:175], v[80:83], v[12:15]\n\t"
        "v_mfma_f32_16x16x32_bf16 v[16:19], a[176:179], v[80:83], v[16:19]\n\t"
        "buffer_load_dwordx4 v[80:83], v117, %[sw], %[t0] offen offset:2048 sc1\n\t"
        "s_waitcnt vmcnt(20) lgkmcnt(2)\n\t"
        "v_mfma_f32_16x16x32_bf16 v[20:23], v[88:91], v[84:87], v[20:23]\n\t"
        "ds_read_b128 v[88:91], v116 offset:18432\n\t"
        "v_mfma_f32_16x16x32_bf16 v[8:11], a[180:183], v[84:87], v[8:11]\n\t"
        "v_mfma_f32_16x16x32_bf16 v[12:15], a[184:187], v[84:87], v[12:15]\n\t"
        "v_mfma_f32_16x16x32_bf16 v[16:19], a[188:191], v[84:87], v[16:19]\n\t"
        "buffer_load_dwordx4 v[84:87], v117, %[sw], %[t0] offen offset:3072 sc1\n\t"
        "s_waitcnt vmcnt(15) lgkmcnt(2)\n\t"
        "v_mfma_f32_16x16x32_bf16 v[20:23], v[92:95], v[24:27], v[20:23]\n\t"
        "ds_read_b128 v[92:95], v116 offset:19456\n\t"
        "v_mfma_f32_16x16x32_bf16 v[8:11], a[192:195], v[24:27], v[8:11]\n\t"
        "v_mfma_f32_16x16x32_bf16 v[12:15], a[196:199], v[24:27], v[12:15]\n\t"
        "v_mfma_f32_16x16x32_bf16 v[16:19], a[200:203], v[24:27], v[16:19]\n\t"
        "s_add_u32 %[t0], %[ro], 0\n\t"
        "s_and_b32 %[t0], %[t0], 7\n\t"
        "s_lshl_b32 %[t0], %[t0], 12\n\t"
        "s_add_u32 %[t0], %[t0], %[nx]\n\t"
        "buffer_load_dwordx4 v[24:27], v117, %[sw], %[t0] offen offset:0 sc1\n\t"
        "s_waitcnt vmcnt(15) lgkmcnt(2)\n\t"
        "v_mfma_f32_16x16x32_bf16 v[20:23], v[96:99], v[28:31], v[20:23]\n\t"
        "ds_read_b128 v[96:99], v116 offset:20480\n\t"
        "v_mfma_f32_16x16x32_bf16 v[8:11], a[204:207], v[28:31], v[8:11]\n\t"
        "v_mfma_f32_16x16x32_bf16 v[12:15], a[208:211], v[28:31], v[12:15]\n\t"
        "v_sub_f32_e32 v104, 1.0, v102\n\t"
        "v_mfma_f32_16x16x32_bf16 v[16:19], a[212:215], v[28:31], v[16:19]\n\t"
        "buffer_load_dwordx4 v[28:31], v117, %[sw], %[t0] offen offset:1024 sc1\n\t"
        "s_waitcnt vmcnt(15) lgkmcnt(2)\n\t"
        "v_mfma_f32_16x16x32_bf16 v[20:23], v[88:91], v[32:35], v[20:23]\n\t"
        "ds_read_b128 v[88:91], v116 offset:21504\n\t"
        "v_mfma_f32_16x16x32_bf16 v[8:11], a[216:219], v[32:35], v[8:11]\n\t"
        "v_mfma_f32_16x16x32_bf16 v[12:15], a[220:223], v[32:35], v[12:15]\n\t"
        "v_cvt_pk_bf16_f32 v104, 0, v104\n\t"
        "v_mfma_f32_16x16x32_bf16 v[16:19], a[224:227], v[32:35], v[16:19]\n\t"
        "buffer_load_dwordx4 v[32:35], v117, %[sw], %[t0] offen offset:2048 sc1\n\t"
        "s_waitcnt vmcnt(15) lgkmcnt(2)\n\t"
        "v_mfma_f32_16x16x32_bf16 v[20:23], v[92:95], v[36:39], v[20:23]\n\t"
        "ds_read_b128 v[92:95], v116 offset:22528\n\t"
        "v_mfma_f32_16x16x32_bf16 v[8:11], a[228:231], v[36:39], v[8:11]\n\t"
        "v_mfma_f32_16x16x32_bf16 v[12:15], a[232:235], v[36:39], v[12:15]\n\t"
        "v_lshlrev_b32_e32 v106, 16, v100\n\t"
        "v_mfma_f32_16x16x32_bf16 v[16:19], a[236:239], v[36:39], v[16:19]\n\t"
        "buffer_load_dwordx4 v[36:39], v117, %[sw], %[t0] offen offset:3072 sc1\n\t"
        "s_waitcnt vmcnt(15) lgkmcnt(2)\n\t"
        "v_mfma_f32_16x16x32_bf16 v[20:23], v[96:99], v[40:43], v[20:23]\n\t"
        "ds_read_b128 v[96:99], v116 offset:23552\n\t"
        "v_mfma_f32_16x16x32_bf16 v[8:11], a[240:243], v[40:43], v[8:11]\n\t"
        "v_mfma_f32_16x16x32_bf16 v[12:15], a[244:247], v[40:43], v[12:15]\n\t"
        "v_mul_f32_e32 v106, v102, v106\n\t"
        "v_mfma_f32_16x16x32_bf16 v[16:19], a[248:251], v[40:43], v[16:19]\n\t"
        "s_add_u32 %[t0], %[ro], 1\n\t"
        "s_and_b32 %[t0], %[t0], 7\n\t"
        "s_lshl_b32 %[t0], %[t0], 12\n\t"
        "s_add_u32 %[t0], %[t0], %[nx]\n\t"
        "buffer_load_dwordx4 v[40:43], v117, %[sw], %[t0] offen offset:0 sc1\n\t"
        "s_waitcnt vmcnt(15) lgkmcnt(2)\n\t"
        "v_mfma_f32_16x16x32_bf16 v[20:23], v[88:91], v[44:47], v[20:23]\n\t"
        "ds_read_b128 v[88:91], v116 offset:24576\n\t"
        "v_mfma_f32_16x16x32_bf16 v[8:11], a[252:255], v[44:47], v[8:11]\n\t"
        "v_mfma_f32_16x16x32_bf16 v[12:15], v[128:131], v[44:47], v[12:15]\n\t"
        "v_cvt_pk_bf16_f32 v106, 0, v106\n\t"
        "v_mfma_f32_16x16x32_bf16 v[16:19], v[132:135], v[44:47], v[16:19]\n\t"
        "buffer_load_dwordx4 v[44:47], v117, %[sw], %[t0] offen offset:1024 sc1\n\t"
        "s_waitcnt vmcnt(15) lgkmcnt(2)\n\t"
        "v_mfma_f32_16x16x32_bf16 v[20:23], v[92:95], v[48:51], v[20:23]\n\t"
        "ds_read_b128 v[92:95], v116 offset:25600\n\t"
        "v_mfma_f32_16x16x32_bf16 v[8:11], v[136:139], v[48:51], v[8:11]\n\t"
        "v_mfma_f32_16x16x32_bf16 v[12:15], v[140:143], v[48:51], v[12:15]\n\t"
        "v_and_b32_e32 v107, 0xffff0000, v100\n\t"
        "v_mfma_f32_16x16x32_bf16 v[16:19], v[144:147], v[48:51], v[16:19]\n\t"
        "buffer_load_dwordx4 v[48:51], v117, %[sw], %[t0] offen offset:2048 sc1\n\t"
        "s_waitcnt vmcnt(15) lgkmcnt(2)\n\t"
        "v_mfma_f32_16x16x32_bf16 v[20:23], v[96:99], v[52:55], v[20:23]\n\t"
        "ds_read_b128 v[96:99], v116 offset:26624\n\t"
        "v_mfma_f32_16x16x32_bf16 v[8:11], v[148:151], v[52:55], v[8:11]\n\t"
        "v_mfma_f32_16x16x32_bf16 v[12:15], v[152:155], v[52:55], v[12:15]\n\t"
        "v_mul_f32_e32 v107, v102, v107\n\t"
        "v_mfma_f32_16x16x32_bf16 v[16:19], v[156:159], v[52:55], v[16:19]\n\t"
        "buffer_load_dwordx4 v[52:55], v117, %[sw], %[t0] offen offset:3072 sc1\n\t"
        "s_waitcnt vmcnt(15) lgkmcnt(2)\n\t"
        "v_mfma_f32_16x16x32_bf16 v[20:23], v[88:91], v[56:59], v[20:23]\n\t"
        "ds_read_b128 v[88:91], v116 offset:27648\n\t"
        "v_mfma_f32_16x16x32_bf16 v[8:11], v[160:163], v[56:59], v[8:11]\n\t"
        "v_mfma_f32_16x16x32_bf16 v[12:15], v[164:167], v[56:59], v[12:15]\n\t"
        "v_cvt_pk_bf16_f32 v107, 0, v107\n\t"
        "v_mfma_f32_16x16x32_bf16 v[16:19], v[168:171], v[56:59], v[16:19]\n\t"
        "s_add_u32 %[t0], %[ro], 2\n\t"
        "s_and_b32 %[t0], %[t0], 7\n\t"
        "s_lshl_b32 %[t0], %[t0], 12\n\t"
        "s_add_u32 %[t0], %[t0], %[nx]\n\t"
        "buffer_load_dwordx4 v[56:59], v117, %[sw], %[t0] offen offset:0 sc1\n\t"
        "s_waitcnt vmcnt(15) lgkmcnt(2)\n\t"
        "v_mfma_f32_16x16x32_bf16 v[20:23], v[92:95], v[60:63], v[20:23]\n\t"
        "ds_read_b128 v[92:95], v116 offset:28672\n\t"
        "v_mfma_f32_16x16x32_bf16 v[8:11], v[172:175], v[60:63], v[8:11]\n\t"
        "v_mfma_f32_16x16x32_bf16 v[12:15], v[176:179], v[60:63], v[12:15]\n\t"
        "v_lshlrev_b32_e32 v108, 16, v101\n\t"
        "v_mfma_f32_16x16x32_bf16 v[16:19], v[180:183], v[60:63], v[16:19]\n\t"
        "buffer_load_dwordx4 v[60:63], v117, %[sw], %[t0] offen offset:1024 sc1\n\t"
        "s_waitcnt vmcnt(15) lgkmcnt(2)\n\t"
        "v_mfma_f32_16x16x32_bf16 v[20:23], v[96:99], v[64:67], v[20:23]\n\t"
        "ds_read_b128 v[96:99], v116 offset:29696\n\t"
        "v_mfma_f32_16x16x32_bf16 v[8:11], v[184:187], v[64:67], v[8:11]\n\t"
        "v_mfma_f32_16x16x32_bf16 v[12:15], v[188:191], v[64:67], v[12:15]\n\t"
        "v_mul_f32_e32 v108, v102, v108\n\t"
        "v_mfma_f32_16x16x32_bf16 v[16:19], v[192:195], v[64:67], v[16:19]\n\t"
        "buffer_load_dwordx4 v[64:67], v117, %[sw], %[t0] offen offset:2048 sc1\n\t"
        "s_waitcnt vmcnt(15) lgkmcnt(2)\n\t"
        "v_mfma_f32_16x16x32_bf16 v[20:23], v[88:91], v[68:71], v[20:23]\n\t"
        "ds_read_b128 v[88:91], v116 offset:30720\n\t"
        "v_mfma_f32_16x16x32_bf16 v[8:11], v[196:199], v[68:71], v[8:11]\n\t"
        "v_mfma_f32_16x16x32_bf16 v[12:15], v[200:203], v[68:71], v[12:15]\n\t"
        "v_cvt_pk_bf16_f32 v108, 0, v108\n\t"
        "v_mfma_f32_16x16x32_bf16 v[16:19], v[204:207], v[68:71], v[16:19]\n\t"
        "buffer_load_dwordx4 v[68:71], v117, %[sw], %[t0] offen offset:3072 sc1\n\t"
        "s_waitcnt vmcnt(15) lgkmcnt(2)\n\t"
        "v_mfma_f32_16x16x32_bf16 v[20:23], v[92:95], v[72:75], v[20:23]\n\t"
        "ds_read_b128 v[92:95], v116 offset:31744\n\t"
        "v_mfma_f32_16x16x32_bf16 v[8:11], v[208:211], v[72:75], v[8:11]\n\t"
        "v_mfma_f32_16x16x32_bf16 v[12:15], v[212:215], v[72:75], v[12:15]\n\t"
        "v_and_b32_e32 v109, 0xffff0000, v101\n\t"
        "v_mfma_f32_16x16x32_bf16 v[16:19], v[216:219], v[72:75], v[16:19]\n\t"
        "s_add_u32 %[t0], %[ro], 3\n\t"
        "s_and_b32 %[t0], %[t0], 7\n\t"
        "s_lshl_b32 %[t0], %[t0], 12\n\t"
        "s_add_u32 %[t0], %[t0], %[nx]\n\t"
        "buffer_load_dwordx4 v[72:75], v117, %[sw], %[t0] offen offset:0 sc1\n\t"
        "s_waitcnt vmcnt(15) lgkmcnt(2)\n\t"
        "v_mfma_f32_16x16x32_bf16 v[20:23], v[96:99], v[76:79], v[20:23]\n\t"
        "v_mfma_f32_16x16x32_bf16 v[8:11], v[220:223], v[76:79], v[8:11]\n\t"
        "v_mfma_f32_16x16x32_bf16 v[12:15], v[224:227], v[76:79], v[12:15]\n\t"
        "v_mul_f32_e32 v109, v102, v109\n\t"
        "v_mfma_f32_16x16x32_bf16 v[16:19], v[228:231], v[76:79], v[16:19]\n\t"
        "buffer_load_dwordx4 v[76:79], v117, %[sw], %[t0] offen offset:1024 sc1\n\t"
        "s_waitcnt vmcnt(15) lgkmcnt(1)\n\t"
        "v_mfma_f32_16x16x32_bf16 v[20:23], v[88:91], v[80:83], v[20:23]\n\t"
        "ds_read_b128 v[88:91], v116 offset:0\n\t"
        "v_mfma_f32_16x16x32_bf16 v[8:11], v[232:235], v[80:83], v[8:11]\n\t"
        "v_mfma_f32_16x16x32_bf16 v[12:15], v[236:239], v[80:83], v[12:15]\n\t"
        "v_cvt_pk_bf16_f32 v109, 0, v109\n\t"
        "v_mfma_f32_16x16x32_bf16 v[16:19], v[240:243], v[80:83], v[16:19]\n\t"
        "buffer_load_dwordx4 v[80:83], v117, %[sw], %[t0] offen offset:2048 sc1\n\t"
        "s_waitcnt vmcnt(15) lgkmcnt(1)\n\t"
        "v_mfma_f32_16x16x32_bf16 v[20:23], v[92:95], v[84:87], v[20:23]\n\t"
        "ds_read_b128 v[92:95], v116 offset:1024\n\t"
        "v_mfma_f32_16x16x32_bf16 v[8:11], v[244:247], v[84:87], v[8:11]\n\t"
        "ds_read_b128 v[96:99], v116 offset:2048\n\t"
        "v_mfma_f32_16x16x32_bf16 v[12:15], v[248:251], v[84:87], v[12:15]\n\t"
        "v_mfma_f32_16x16x32_bf16 v[16:19], v[252:255], v[84:87], v[16:19]\n\t"
        "buffer_load_dwordx4 v[84:87], v117, %[sw], %[t0] offen offset:3072 sc1\n\t"
        "LP_epi%=:\n\t"
        "s_lshl_b32 %[t0], %[tl], 15\n\t"
        "s_nop 0\n\t"
        "ds_write_b128 v122, v[20:23] offset:192\n\t"
        "s_nop 0\n\t"
        "ds_write_b128 v122, v[8:11] offset:0\n\t"
        "s_nop 0\n\t"
        "ds_write_b128 v122, v[12:15] offset:64\n\t"
        "ds_write_b128 v122, v[16:19] offset:128\n\t"
        "s_waitcnt lgkmcnt(0)\n\t"
        "s_barrier\n\t"
        "ds_read_b128 v[8:11], v123 offset:0\n\t"
        "ds_read_b128 v[12:15], v123 offset:4352\n\t"
        "ds_read_b128 v[16:19], v123 offset:8704\n\t"
        "ds_read_b128 v[20:23], v123 offset:13056\n\t"
        "s_waitcnt lgkmcnt(2)\n\t"
        "v_pk_add_f32 v[8:9], v[8:9], v[12:13]\n\t"
        "v_pk_add_f32 v[10:11], v[10:11], v[14:15]\n\t"
        "s_waitcnt lgkmcnt(0)\n\t"
        "v_pk_add_f32 v[16:17], v[16:17], v[20:21]\n\t"
        "v_pk_add_f32 v[18:19], v[18:19], v[22:23]\n\t"
        "v_pk_add_f32 v[8:9], v[8:9], v[16:17]\n\t"
        "v_pk_add_f32 v[10:11], v[10:11], v[18:19]\n\t"
        "v_cvt_pk_bf16_f32 v8, 0, v8\n\t"
        "v_cvt_pk_bf16_f32 v9, 0, v9\n\t"
        "v_cvt_pk_bf16_f32 v10, 0, v10\n\t"
        "v_cvt_pk_bf16_f32 v11, 0, v11\n\t"
        "v_mul_f32_e32 v8, v104, v8\n\t"
        "v_mul_f32_e32 v9, v104, v9\n\t"
        "v_mul_f32_e32 v10, v104, v10\n\t"
        "v_mul_f32_e32 v11, v104, v11\n\t"
        "v_cvt_pk_bf16_f32 v8, 0, v8\n\t"
        "v_cvt_pk_bf16_f32 v9, 0, v9\n\t"
        "v_cvt_pk_bf16_f32 v10, 0, v10\n\t"
        "v_cvt_pk_bf16_f32 v11, 0, v11\n\t"
        "v_add_f32_e32 v8, v106, v8\n\t"
        "v_add_f32_e32 v9, v107, v9\n\t"
        "v_add_f32_e32 v10, v108, v10\n\t"
        "v_add_f32_e32 v11, v109, v11\n\t"
        "v_cvt_pk_bf16_f32 v104, v8, v9\n\t"
        "v_cvt_pk_bf16_f32 v105, v10, v11\n\t"
        "buffer_store_dwordx2 v[104:105], v121, %[su], %[t0] offen sc1\n\t"
        "s_add_u32 %[tl], %[tl], 8\n\t"
        "s_cmp_ge_u32 %[tl], 128\n\t"
        "s_cbranch_scc1 LP_end%=\n\t"
        "s_cmp_lg_u32 %[nx], 0xf0000000\n\t"
        "s_cbranch_scc1 LP_tile%=\n\t"
        "s_branch LP_wait%=\n\t"
        "LP_end%=:\n\t"
        "s_waitcnt vmcnt(0) lgkmcnt(0)\n\t"
        ".Lk_end_P:\n\t"
        : [t0] "=&s"(t0), [t1] "=&s"(t1), [tl] "+s"(tl), [pg] "=&s"(pg), [cu] "=&s"(cu), [nx] "=&s"(nx), [sp] "=&s"(sp), [re] "=&s"(re)
        : [so] "s"(so), [sw] "s"(sw), [su] "s"(su), [cv] "s"(F_CVY + nt * 128), [ro] "s"(ro),
          "{v110}"((w * 1024 + (l >> 3)) * 2048 + nt * 128 + (l & 7) * 16), "{v111}"(stg + ((l * 16) ^ ((l & 48) << 1))), "{v112}"(lr), "{v113}"(lr ^ 32u), "{v114}"(lr ^ 64u), "{v115}"(lr ^ 96u),
          "{v116}"(lb + w * 32768 + l * 16), "{v117}"(W_OX + w * 32768 + l * 16), "{v118}"(F_PROG + (w * 8 + (l & 7)) * 128),
          "{v119}"(W_A + ((nt * 2 + (n >> 3)) * 4 + 2 * (n & 1)) * 32768 + w * 4096 + ((n & 7) >> 1) * 16 + g * 4), "{v120}"(W_GATE + w * 128 + g * 4), "{v121}"((4 * w + g) * 2048 + nt * 128 + n * 8),
          "{v122}"(lb + 131072 + w * 4352 + n * 272 + g * 16), "{v123}"(lb + 131072 + (4 * w + g) * 272 + n * 16)
        : "memory", "scc", "vcc", "v8", "v9", "v10", "v11", "v12", "v13", "v14", "v15", "v16", "v17", "v18", "v19", "v20", "v21", "v22", "v23", "v24", "v25", "v26", "v27", "v28", "v29", "v30", "v31", "v32", "v33", "v34", "v35", "v36", "v37", "v38", "v39", "v40", "v41", "v42", "v43", "v44", "v45", "v46", "v47", "v48", "v49", "v50", "v51", "v52", "v53", "v54", "v55", "v56", "v57", "v58", "v59", "v60", "v61", "v62", "v63", "v64", "v65", "v66", "v67", "v68", "v69", "v70", "v71", "v72", "v73", "v74", "v75", "v76", "v77", "v78", "v79", "v80", "v81", "v82", "v83", "v84", "v85", "v86", "v87", "v88", "v89", "v90", "v91", "v92", "v93", "v94", "v95", "v96", "v97", "v98", "v99", "v100", "v101", "v102", "v103", "v104", "v105", "v106", "v107", "v108", "v109", "v124", "v125", "v126", "v127", "v128", "v129", "v130", "v131", "v132", "v133", "v134", "v135", "v136", "v137", "v138", "v139", "v140", "v141", "v142", "v143", "v144", "v145", "v146", "v147", "v148", "v149", "v150", "v151", "v152", "v153", "v154", "v155", "v156", "v157", "v158", "v159", "v160", "v161", "v162", "v163", "v164", "v165", "v166", "v167", "v168", "v169", "v170", "v171", "v172", "v173", "v174", "v175", "v176", "v177", "v178", "v179", "v180", "v181", "v182", "v183", "v184", "v185", "v186", "v187", "v188", "v189", "v190", "v191", "v192", "v193", "v194", "v195", "v196", "v197", "v198", "v199", "v200", "v201", "v202", "v203", "v204", "v205", "v206", "v207", "v208", "v209", "v210", "v211", "v212", "v213", "v214", "v215", "v216", "v217", "v218", "v219", "v220", "v221", "v222", "v223", "v224", "v225", "v226", "v227", "v228", "v229", "v230", "v231", "v232", "v233", "v234", "v235", "v236", "v237", "v238", "v239", "v240", "v241", "v242", "v243", "v244", "v245", "v246", "v247", "v248", "v249", "v250", "v251", "v252", "v253", "v254", "v255", "a0", "a1", "a2", "a3", "a4", "a5", "a6", "a7", "a8", "a9", "a10", "a11", "a12", "a13", "a14", "a15", "a16", "a17", "a18", "a19", "a20", "a21", "a22", "a23", "a24", "a25", "a26", "a27", "a28", "a29", "a30", "a31", "a32", "a33", "a34", "a35", "a36", "a37", "a38", "a39", "a40", "a41", "a42", "a43", "a44", "a45", "a46", "a47", "a48", "a49", "a50", "a51", "a52", "a53", "a54", "a55", "a56", "a57", "a58", "a59", "a60", "a61", "a62", "a63", "a64", "a65", "a66", "a67", "a68", "a69", "a70", "a71", "a72", "a73", "a74", "a75", "a76", "a77", "a78", "a79", "a80", "a81", "a82", "a83", "a84", "a85", "a86", "a87", "a88", "a89", "a90", "a91", "a92", "a93", "a94", "a95", "a96", "a97", "a98", "a99", "a100", "a101", "a102", "a103", "a104", "a105", "a106", "a107", "a108", "a109", "a110", "a111", "a112", "a113", "a114", "a115", "a116", "a117", "a118", "a119", "a120", "a121", "a122", "a123", "a124", "a125", "a126", "a127", "a128", "a129", "a130", "a131", "a132", "a133", "a134", "a135", "a136", "a137", "a138", "a139", "a140", "a141", "a142", "a143", "a144", "a145", "a146", "a147", "a148", "a149", "a150", "a151", "a152", "a153", "a154", "a155", "a156", "a157", "a158", "a159", "a160", "a161", "a162", "a163", "a164", "a165", "a166", "a167", "a168", "a169", "a170", "a171", "a172", "a173", "a174", "a175", "a176", "a177", "a178", "a179", "a180", "a181", "a182", "a183", "a184", "a185", "a186", "a187", "a188", "a189", "a190", "a191", "a192", "a193", "a194", "a195", "a196", "a197", "a198", "a199", "a200", "a201", "a202", "a203", "a204", "a205", "a206", "a207", "a208", "a209", "a210", "a211", "a212", "a213", "a214", "a215", "a216", "a217", "a218", "a219", "a220", "a221", "a222", "a223", "a224", "a225", "a226", "a227", "a228", "a229", "a230", "a231", "a232", "a233", "a234", "a235", "a236", "a237", "a238", "a239", "a240", "a241", "a242", "a243", "a244", "a245", "a246", "a247", "a248", "a249", "a250", "a251", "a252", "a253", "a254", "a255");
}
__device__ __forceinline__ void bulk_block(const Params& p, rsrc_t r, unsigned char* lds, const int wv, const int j, const unsigned char* code, const int clen) {
    proj_block(p, lds, wv, (j & 6) * 4 + (j >> 4), ((j >> 2) & 2) | (j & 1));
    const int un = ((j & 7) >> 1) * 32 + (j >> 3) * 2 + (j & 1);
    if (CT2) { const int o = ((un & 3) * 256 + otid()) * 128; if (o < clen) touch_l2(code + o, __builtin_amdgcn_readfirstlane((unsigned)(size_t)lds) + 161792); }
    conv_prep(p, r, wv, un >> 2, un & 3, j);
    const int rp = ((j & 7) >> 1) + (j & 1) * 4, nt = (j >> 3) ^ 2, ro = (nt + 2 * rp) & 7;
    if (!conv_core(p, r, lds, wv, un, nt, ro)) return;
    if (!wait_word(r, F_CONV, 128u, 4096u)) return;
    out_block(p, lds, wv, rp, ro, nt);
}

__device__ __forceinline__ void rec_block(const Params& p, rsrc_t r, unsigned char* lds, const int wv, const int b) {
    const int xq = (b & 7) >> 1, t = (b >> 3) * 2 + (b & 1);
    const unsigned lb = __builtin_amdgcn_readfirstlane((unsigned)(size_t)lds);
    if (t < 8) rec_a(p, lb, wv, xq * 8 + t, xq);
    else if (t < 24) rec_b(p, lb, wv, (xq & 1) * 16 + t - 8, xq >> 1, xq);
    else {
        const int hi = xq * 8 + t - 24;
        rec_helper(p, r, lb, wv, hi);
    }
}

__global__ void __launch_bounds__(256, 1) k_main(Params p) {
    extern __shared__ __attribute__((aligned(16))) unsigned char lds[];
    rsrc_t r = __builtin_amdgcn_make_buffer_rsrc(p.ws, 0, W_TOTAL, 0x00020000);
    const int wv = __builtin_amdgcn_readfirstlane(threadIdx.x >> 6);
    unsigned long long pc; int clen;
    {
        int ea, eb, eg;
        asm volatile("s_getpc_b64 %0\n.Lk_begin:\n\ts_mov_b32 %1, .Lk_end_A-.Lk_begin\n\ts_mov_b32 %2, .Lk_end_B-.Lk_begin\n\ts_mov_b32 %3, .Lk_end_P-.Lk_begin" : "=s"(pc), "=s"(ea), "=s"(eb), "=s"(eg));
        const int o = ((blockIdx.x >> 3) * 64 + (threadIdx.x >> 2)) * 128;
        clen = __builtin_amdgcn_readfirstlane(max(ea, max(eb, eg)) - 4);
        if ((threadIdx.x & 3) == 0 && o < clen) touch_l2((const unsigned char*)pc + o, __builtin_amdgcn_readfirstlane((unsigned)(size_t)lds) + 1024);
    }
    if (wv == 3) {
        unsigned xc; asm volatile("s_getreg_b32 %0, hwreg(HW_REG_XCC_ID)" : "=s"(xc));
        if (xc & 4u) touch_l2(p.Aq + ((blockIdx.x >> 3) * 64 + (threadIdx.x & 63)) * 64, __builtin_amdgcn_readfirstlane((unsigned)(size_t)lds) + 2048);
    }
    if (blockIdx.x == 0 && wv == 1) {
        const int l = threadIdx.x & 63; const v4u z = {0u, 0u, 0u, 0u};
#pragma unroll
        for (int i = 0; i < 6; ++i) st16(r, F_CONV + i * 1024 + l * 16, z);
        DRAIN();
        for (unsigned spin = 0; spin < (1u << 20); ++spin) {
            if (l < 8) __hip_atomic_fetch_add((unsigned*)(p.ws + F_BEAT + l * 128), 1u, __ATOMIC_RELAXED, __HIP_MEMORY_SCOPE_AGENT);
            unsigned c = ld4(r, l < 8 ? F_TKT + l * 64 : OOB);
            c += __shfl_xor(c, 1); c += __shfl_xor(c, 2); c += __shfl_xor(c, 4);
            if (__builtin_amdgcn_readfirstlane(c) >= 256u) break; }
    }
    if (threadIdx.x == 0) {
        const unsigned bo = F_BEAT + (blockIdx.x & 7) * 128, b0 = ld4(r, bo);
        for (unsigned spin = 0; spin < (1u << 22); ++spin) { MEMBAR(); if (ld4(r, bo) != b0) break; }
        MEMBAR();
        unsigned xcc; asm volatile("s_getreg_b32 %0, hwreg(HW_REG_XCC_ID)" : "=s"(xcc)); xcc &= 7u;
        unsigned* tk = (unsigned*)(p.ws + F_TKT);
        unsigned t = __hip_atomic_fetch_add(tk + xcc * 16, 1u, __ATOMIC_RELAXED, __HIP_MEMORY_SCOPE_AGENT);
        if (t >= 32u) {
            unsigned o = __hip_atomic_fetch_add(tk + 128, 1u, __ATOMIC_RELAXED, __HIP_MEMORY_SCOPE_AGENT);
            for (unsigned spin = 0; spin < (1u << 22); ++spin) { unsigned tot = 0u;
                for (int x = 0; x < 8; ++x) tot += __hip_atomic_load(tk + x * 16, __ATOMIC_RELAXED, __HIP_MEMORY_SCOPE_AGENT);
                if (tot >= 256u) break; }
            for (int x = 0; x < 8; ++x) { const unsigned cnt = __hip_atomic_load(tk + x * 16, __ATOMIC_RELAXED, __HIP_MEMORY_SCOPE_AGENT), fr = cnt < 32u ? 32u - cnt : 0u;
                if (o < fr) { xcc = (unsigned)x; t = cnt + o; break; }
                o -= fr; }
        }
        *(volatile int*)lds = (int)((xcc >> 2) * 128u + (t >> 1) * 8u + (xcc & 3u) * 2u + (t & 1u));
    }
    DRAIN();
    __syncthreads();
    const int role = __builtin_amdgcn_readfirstlane(*(volatile int*)lds) & 255;
    __syncthreads();
    if (role >= 128) bulk_block(p, r, lds, wv, role - 128, (const unsigned char*)pc, clen); else rec_block(p, r, lds, wv, role);
}

extern "C" void kernel_launch(void* const* d_in, const int* in_sizes, int n_in, void* d_out, int out_size, void* d_ws, size_t ws_size, hipStream_t stream) {
    Params p;
    p.Aq = (const bf16_t*)d_in[0]; p.Ak = (const bf16_t*)d_in[1]; p.Av = (const bf16_t*)d_in[2];
    p.shift_k = (const bf16_t*)d_in[3]; p.ssm_k = (const bf16_t*)d_in[4]; p.ssm_D = (const bf16_t*)d_in[5];
    p.mem0 = (const bf16_t*)d_in[7]; p.Wq = (const bf16_t*)d_in[8]; p.Wk = (const bf16_t*)d_in[9];
    p.Wv = (const bf16_t*)d_in[10]; p.Wo = (const bf16_t*)d_in[11]; p.gate_w = (const bf16_t*)d_in[13];
    p.out = (bf16_t*)d_out; p.ws = (unsigned char*)d_ws;
    (void)hipFuncSetAttribute((const void*)k_main, hipFuncAttributeMaxDynamicSharedMemorySize, L_TOTAL);
    k_main<<<256, 256, L_TOTAL, stream>>>(p);
}
```

```cpp
#include <hip/hip_runtime.h>
#include <stdint.h>
#include <type_traits>
typedef unsigned short bf16_t;
using bf16x8 = __attribute__((ext_vector_type(8))) short;
using f32x4 = __attribute__((ext_vector_type(4))) float;
typedef unsigned v4u __attribute__((ext_vector_type(4)));
typedef unsigned v2u __attribute__((ext_vector_type(2)));
typedef __amdgpu_buffer_rsrc_t rsrc_t;

__device__ __forceinline__ float bf2f(bf16_t b) { return __uint_as_float(((unsigned)b) << 16); }
typedef __bf16 hbf2 __attribute__((ext_vector_type(2)));
typedef float f32x2 __attribute__((ext_vector_type(2)));
__device__ __forceinline__ unsigned pack2(float a, float b) { f32x2 v = {a, b}; return __builtin_bit_cast(unsigned, __builtin_convertvector(v, hbf2)); }
__device__ __forceinline__ float rbfh(float f) { return __uint_as_float(pack2(f, 0.f) << 16); }
__device__ __forceinline__ unsigned nosent(unsigned x) {
    typedef unsigned short us2 __attribute__((ext_vector_type(2)));
    const us2 a = __builtin_bit_cast(us2, x), c = {(unsigned short)0xFFFE, (unsigned short)0xFFFE}; return __builtin_bit_cast(unsigned, __builtin_elementwise_min(a, c)); }

constexpr int S = 2048, D = 1024, H = 32, DH = 128, M = 8, SEG = 32, NSEG = 64;

constexpr unsigned W_FLG = 0;
constexpr unsigned F_KVQ = W_FLG;
constexpr unsigned F_CONV = W_FLG + 32768;
constexpr unsigned F_ERR = W_FLG + 32768 + 256;
constexpr unsigned F_PREP = W_FLG + 32768 + 512;
constexpr unsigned F_CAL = W_FLG + 33792;
constexpr unsigned F_TKT = W_FLG + 34816;
constexpr unsigned F_BEAT = W_FLG + 40960;
constexpr unsigned F_PROG = W_FLG + 49152;
constexpr unsigned F_CVU = W_FLG + 53248, F_CVY = W_FLG + 57344;
constexpr unsigned F_OP = W_FLG + 65536;
constexpr unsigned F_ATT = W_FLG + 131072;
constexpr unsigned F_RDY = W_FLG + 147456;
constexpr unsigned FLG_BYTES = 180224;
constexpr unsigned HO_KVM = W_FLG + FLG_BYTES;
constexpr unsigned W_A = HO_KVM + 64 * 32 * 4096;
constexpr unsigned W_GATE = W_A + S * D * 2;
constexpr unsigned W_QX = W_GATE + 8192;
constexpr unsigned W_KX = W_QX + S * 4096 * 2;
constexpr unsigned W_VF = W_KX + S * 4096 * 2;
constexpr unsigned W_OX = W_VF + 64 * 32 * 8192;
constexpr unsigned W_PART = W_OX + S * 4096 * 2;
constexpr unsigned W_Y = W_PART;
constexpr unsigned W_DBG = W_PART + 32 * 4 * 64 * 1024 * 4;
constexpr unsigned W_U = W_DBG + 256 * 64 * 16 * 8;
constexpr unsigned W_TOTAL = W_U + S * D * 2;

__device__ __forceinline__ v4u ld16(rsrc_t r, unsigned off) { return __builtin_amdgcn_raw_buffer_load_b128(r, off, 0, 16); }
__device__ __forceinline__ v2u ld8(rsrc_t r, unsigned off) { return __builtin_amdgcn_raw_buffer_load_b64(r, off, 0, 16); }
__device__ __forceinline__ unsigned ld4(rsrc_t r, unsigned off) { return __builtin_amdgcn_raw_buffer_load_b32(r, off, 0, 16); }
__device__ __forceinline__ void st16(rsrc_t r, unsigned off, v4u v) { __builtin_amdgcn_raw_buffer_store_b128(v, r, off, 0, 16); }
__device__ __forceinline__ void st8(rsrc_t r, unsigned off, v2u v) { __builtin_amdgcn_raw_buffer_store_b64(v, r, off, 0, 16); }
__device__ __forceinline__ void st4(rsrc_t r, unsigned off, unsigned v) { __builtin_amdgcn_raw_buffer_store_b32(v, r, off, 0, 16); }
__device__ __forceinline__ bool okdw(unsigned x) { return ((x & 0xFFFFu) != 0xFFFFu) & ((x >> 16) != 0xFFFFu); }
typedef unsigned short us2_t __attribute__((ext_vector_type(2)));
__device__ __forceinline__ unsigned pkmax(unsigned a, unsigned b) { return __builtin_bit_cast(unsigned, __builtin_elementwise_max(__builtin_bit_cast(us2_t, a), __builtin_bit_cast(us2_t, b))); }
__device__ __forceinline__ unsigned pkmax4(v4u v) { return pkmax(pkmax(v.x, v.y), pkmax(v.z, v.w)); }
__device__ __forceinline__ bool ok16(v4u v) { return okdw(pkmax4(v)); }
__device__ __forceinline__ bool ok8(v2u v) { return okdw(v.x) & okdw(v.y); }
__device__ __forceinline__ bf16x8 as_frag(v4u v) { return __builtin_bit_cast(bf16x8, v); }
#define MFMA(a, b, c) __builtin_amdgcn_mfma_f32_16x16x32_bf16((a), (b), (c), 0, 0, 0)
#define D1 128
#define D2 77
#define D3 76
#define LT_A 1
#define LB0 12
#define LC0 20
#define LA0 24
#define TR_UP 4
#define TR_DN 2
#define L_DEC 3
#define WARM1 0
#define KROT 0
#define STAG 1
#define UP_SH 2
#define EPI_SLP 0
#define SITA 1
#define PAIRX 4
#define DUE_P 1
#define LATE 1
#define EARLY_FROM 56
#define SKEW 0
#define OUT_WARM 1
#define ADAPT 1
#define RPL 0
#define CV_POL 2
#define POLL_SLP 4
#define TOUCH 1
#define ROT 0
#define CODE_T 1
#define CAL_T 450u
#define GTOUCH 0
#define XTOUCH 32
#define ST_P 32
#define CV_NAP 4000u
#define XSLP 3
#define XDLY 0
#define GDLY 0
#define YDLY 0
#define PROT 3
#define XSTG 0
#define XG 1
#define MS_BYTES 65536
#define BEAT_SLP 0
#define T2U 2
#define T2A 0
#define T2F 0
#define TPRE 48
#define XWAIT 1
#define PEN 48
#define MEMBAR() asm volatile("" ::: "memory")
#define FT(i) do { } while (0)
#define ENDPGM() asm volatile("s_endpgm" ::: "memory")
#define DRAIN() asm volatile("s_waitcnt vmcnt(0)" ::: "memory")

__device__ __forceinline__ float xor16_max(float x) { auto r = __builtin_amdgcn_permlane16_swap(__float_as_uint(x), __float_as_uint(x), false, false); return fmaxf(__uint_as_float(r[0]), __uint_as_float(r[1])); }
__device__ __forceinline__ float xor32_max(float x) { auto r = __builtin_amdgcn_permlane32_swap(__float_as_uint(x), __float_as_uint(x), false, false); return fmaxf(__uint_as_float(r[0]), __uint_as_float(r[1])); }
__device__ __forceinline__ float xor16_add(float x) { auto r = __builtin_amdgcn_permlane16_swap(__float_as_uint(x), __float_as_uint(x), false, false); return __uint_as_float(r[0]) + __uint_as_float(r[1]); }
__device__ __forceinline__ float xor32_add(float x) { auto r = __builtin_amdgcn_permlane32_swap(__float_as_uint(x), __float_as_uint(x), false, false); return __uint_as_float(r[0]) + __uint_as_float(r[1]); }
__device__ __forceinline__ v2u tr_read(unsigned lds_addr) { v2u r; asm volatile("ds_read_b64_tr_b16 %0, %1\n\ts_waitcnt lgkmcnt(0)" : "=&v"(r) : "v"(lds_addr) : "memory"); return r; }

typedef short v4i16_t __attribute__((ext_vector_type(4)));
__device__ __forceinline__ v2u trb(unsigned lds_addr) { return __builtin_bit_cast(v2u, __builtin_amdgcn_ds_read_tr16_b64_v4i16((__attribute__((address_space(3))) v4i16_t*)(size_t)lds_addr)); }
__device__ __forceinline__ int otid_(int wv) { int t; asm volatile("v_mbcnt_lo_u32_b32 %0, -1, 0\n\tv_mbcnt_hi_u32_b32 %0, -1, %0" : "=v"(t)); return wv * 64 + t; }
#define otid() otid_(wv)
__device__ __forceinline__ bool wait_word(rsrc_t r, unsigned off, unsigned want, unsigned code) {
    for (unsigned spin = 0;; ++spin) { MEMBAR();
        const unsigned v = __builtin_amdgcn_readfirstlane(ld4(r, off));
        if (v >= want) return true;
        if (spin > (1u << 20)) { st4(r, F_ERR, code); return false; }
        __builtin_amdgcn_s_sleep(8); }
}

__device__ __forceinline__ unsigned rtnow() { return (unsigned)__builtin_amdgcn_s_memrealtime(); }
__device__ __forceinline__ void wait_until(unsigned target) { for (int i = 0; i < 4096; ++i) { if ((int)(rtnow() - target) >= 0) break; } }
__device__ __forceinline__ void wait_long(unsigned target) { for (int i = 0; i < 4096; ++i) { if ((int)(rtnow() - target) >= 0) break; __builtin_amdgcn_s_sleep(8); } }
constexpr int RED_G = 320;
constexpr int RED_T = 4 * RED_G;
constexpr int L_RED = 0;
constexpr int L_KX = 65536;
constexpr int KXP = 272;
constexpr int L_VF = L_KX + 2 * 32 * KXP;
constexpr int L_QKV = L_VF + 16384;
constexpr int L_OFF = L_QKV + 6144;
constexpr int L_STMP = L_OFF + 256;
constexpr int L_TOTAL = 163840;
constexpr unsigned U_A = 0, U_S = 32, U_B = 36, U_INIT = 113, U_C = 128, U_END = 512;
#define PLACE 1
#define CAL_N 5
#define STEP_MIN 190u

struct Params {
    const bf16_t *Aq, *Ak, *Av, *shift_k, *ssm_k, *ssm_D, *mem0, *Wq, *Wk, *Wv, *Wo, *gate_w;
    bf16_t* out; unsigned char* ws;
};
__host__ __device__ __forceinline__ unsigned hx_base(const void* ws) { const size_t a = (size_t)ws; return (unsigned)((((a + W_TOTAL + 0x7FFFFFu) >> 23) << 23) - a); }

#define STAMP(i) do { } while (0)
#define BSTAMP(i) do { } while (0)
#define EV(code) do { } while (0)
#define SPINS(i) do { } while (0)
typedef short v4s __attribute__((ext_vector_type(4)));
typedef unsigned v32u __attribute__((ext_vector_type(32)));
typedef unsigned v16u __attribute__((ext_vector_type(16)));
static_assert(L_VF - L_KX == 16384 + 1024, "fragment read offsets");
#define MFMA16(a, b, c) __builtin_amdgcn_mfma_f32_16x16x16bf16_1k(__builtin_bit_cast(v4s, (a)), __builtin_bit_cast(v4s, (b)), (c), 0, 0, 0)
__device__ __forceinline__ unsigned ror8(unsigned x) { return (unsigned)__builtin_amdgcn_update_dpp(0, (int)x, 0x128, 0xF, 0xF, false); }
__device__ __forceinline__ unsigned qx1(unsigned x) { return (unsigned)__builtin_amdgcn_update_dpp(0, (int)x, 0xB1, 0xF, 0xF, false); }
__device__ __forceinline__ unsigned wave_pkmax(unsigned d) {
    d = pkmax(d, (unsigned)__builtin_amdgcn_update_dpp((int)d, (int)d, 0xB1, 0xF, 0xF, false)); d = pkmax(d, (unsigned)__builtin_amdgcn_update_dpp((int)d, (int)d, 0x4E, 0xF, 0xF, false));
    d = pkmax(d, (unsigned)__builtin_amdgcn_update_dpp((int)d, (int)d, 0x124, 0xF, 0xF, false)); d = pkmax(d, (unsigned)__builtin_amdgcn_update_dpp((int)d, (int)d, 0x128, 0xF, 0xF, false));
    { auto q = __builtin_amdgcn_permlane16_swap(d, d, false, false); d = pkmax(q[0], q[1]); }
    { auto q = __builtin_amdgcn_permlane32_swap(d, d, false, false); d = pkmax(q[0], q[1]); }
    return d; }
__device__ __forceinline__ int wave_max(int d) {
    d = max(d, __builtin_amdgcn_update_dpp(d, d, 0xB1, 0xF, 0xF, false)); d = max(d, __builtin_amdgcn_update_dpp(d, d, 0x4E, 0xF, 0xF, false));
    d = max(d, __builtin_amdgcn_update_dpp(d, d, 0x124, 0xF, 0xF, false)); d = max(d, __builtin_amdgcn_update_dpp(d, d, 0x128, 0xF, 0xF, false));
    { auto q = __builtin_amdgcn_permlane16_swap((unsigned)d, (unsigned)d, false, false); d = max((int)q[0], (int)q[1]); }
    { auto q = __builtin_amdgcn_permlane32_swap((unsigned)d, (unsigned)d, false, false); d = max((int)q[0], (int)q[1]); }
    return d; }

__device__ __forceinline__ v4u mk_srd(const void* p, unsigned bytes) { const size_t a = (size_t)p; v4u d;
    d.x = __builtin_amdgcn_readfirstlane((unsigned)a); d.y = __builtin_amdgcn_readfirstlane((unsigned)(a >> 32) & 0xFFFFu); d.z = __builtin_amdgcn_readfirstlane(bytes); d.w = 0x00020000u; return d; }
__device__ __forceinline__ bf16x8 ldsa(unsigned addr) { return *(const __attribute__((address_space(3))) bf16x8*)(size_t)addr; }
constexpr unsigned HX_BYTES = 2097152, OOB = 0xF0000000u;
constexpr unsigned F_RDY0 = W_FLG + 34304;
constexpr unsigned F_GO = W_FLG + 36864;
constexpr int L_KV = 90112, L_MS = 139264, L_IMG = 155648, IMG_PS = 1056, IMG_MS = 2112, LB_R = 8192, LB_STG = 16384, LB_STGP = 8704;
static_assert(F_KVQ == 0 && F_ERR == 33024 && F_CONV == 32768 && F_CAL == 33792 && F_RDY0 == 34304 && F_GO == 36864 && F_PROG == 49152 && W_QX == 12771328 && W_KX == 29548544 && W_OX == 63102976 && W_VF == 46325760 && W_DBG == 113434624 && L_KV == 90112 && L_MS == 139264 && L_IMG == 155648 && IMG_PS == 1056 && IMG_MS == 2112 && LB_R == 8192 && LB_STG == 16384 && LB_STGP == 8704, "constants of the generated code");
#define REC_S_IN "{s[8:11]}"(sh), "{s[12:15]}"(sw), "{s[16:19]}"(s1), "{s[20:23]}"(s2), "{s29}"(D1), "{s30}"(D2), "{s43}"(w), "{s44}"(id), "{s49}"(rb), "{s50}"(PEN), "{s51}"(LT_A), "{s45}"(F_GO + (cp * 4 + w) * 128)
__device__ __forceinline__ void rec_a(const Params& p, unsigned lb, const int wv, const int id, const int cp) {
    const int w = wv, l = otid() & 63, n = l & 15, g = l >> 4, h = id, rb = id;
    const v4u sh = mk_srd(p.ws + hx_base(p.ws), HX_BYTES - 8192), sw = mk_srd(p.ws, W_TOTAL), s1 = mk_srd(w < 2 ? p.Wq : p.Wk, 1024 * 4096 * 2), s2 = s1;
    const int r8 = l >> 3, pc = l & 7, pp = n >> 3, bb = (n >> 2) & 1, f = (g & 1) | (pp << 1);
    const unsigned stg = lb + 131072 + w * 8192;
    const unsigned g_w = r8 * 8192 + h * 256 + (w & 1) * 128 + pc * 16;
    const unsigned wb0 = stg + r8 * 128 + (((pc >> 1) ^ (r8 >> 2)) << 5) + (pc & 1) * 16, wb1 = wb0 ^ 64u;
    const unsigned rb0 = stg + (16 * (g >> 1) + 8 * pp + 4 * (g & 1) + bb) * 128 + (f << 5) + (n & 3) * 8;
    const unsigned a_mem = cp * 32768 + (w >> 1) * 16384 + (w & 1) * 4096 + l * 16;
    const unsigned l_ms = lb + L_MS + l * 16;
    const unsigned l_iw = lb + L_IMG + (w >> 1) * IMG_MS + (g >> 1) * IMG_PS + (64 * (w & 1) + n) * 8 + (g & 1) * 4;
    const unsigned l_iq = lb + L_IMG + (n & 1) * IMG_PS + (8 * g + (n >> 2)) * 8;
    const unsigned a_vm = (16 + (h >> 2)) * 16384 + (h & 3) * 2048 + w * 512 + (l & 31) * 16;
    const unsigned a_o = n < 8 ? (8 + (n >> 2) * 4 + (h >> 3)) * 16384 + (h & 7) * 1024 + w * 256 + (n & 3) * 64 + g * 16 : OOB;
    const unsigned a_ox = (((w & 1) * 32 + h) * 4 + w) * 1024 + l * 16;
    asm volatile(
        "s_lshl_b32 s34, s43, 2\n\t"
        "s_add_u32 s34, s34, 0\n\t"
        "s_and_b32 s34, s34, 15\n\t"
        "s_lshl_b32 s34, s34, 19\n\t"
        "buffer_load_dwordx4 v[224:227], v96, s[16:19], s34 offen nt\n\t"
        "s_add_u32 s34, s34, 0x10000\n\t"
        "buffer_load_dwordx4 v[228:231], v96, s[16:19], s34 offen nt\n\t"
        "s_add_u32 s34, s34, 0x10000\n\t"
        "buffer_load_dwordx4 v[232:235], v96, s[16:19], s34 offen nt\n\t"
        "s_add_u32 s34, s34, 0x10000\n\t"
        "buffer_load_dwordx4 v[236:239], v96, s[16:19], s34 offen nt\n\t"
        "s_add_u32 s34, s34, 0x10000\n\t"
        "buffer_load_dwordx4 v[240:243], v96, s[16:19], s34 offen nt\n\t"
        "s_add_u32 s34, s34, 0x10000\n\t"
        "buffer_load_dwordx4 v[244:247], v96, s[16:19], s34 offen nt\n\t"
        "s_add_u32 s34, s34, 0x10000\n\t"
        "buffer_load_dwordx4 v[248:251], v96, s[16:19], s34 offen nt\n\t"
        "s_add_u32 s34, s34, 0x10000\n\t"
        "buffer_load_dwordx4 v[252:255], v96, s[16:19], s34 offen nt\n\t"
        "s_lshl_b32 s34, s43, 2\n\t"
        "s_add_u32 s34, s34, 1\n\t"
        "s_and_b32 s34, s34, 15\n\t"
        "s_lshl_b32 s34, s34, 19\n\t"
        "buffer_load_dwordx4 v[0:3], v96, s[16:19], s34 offen nt\n\t"
        "s_add_u32 s34, s34, 0x10000\n\t"
        "buffer_load_dwordx4 v[4:7], v96, s[16:19], s34 offen nt\n\t"
        "s_add_u32 s34, s34, 0x10000\n\t"
        "buffer_load_dwordx4 v[8:11], v96, s[16:19], s34 offen nt\n\t"
        "s_add_u32 s34, s34, 0x10000\n\t"
        "buffer_load_dwordx4 v[12:15], v96, s[16:19], s34 offen nt\n\t"
        "s_add_u32 s34, s34, 0x10000\n\t"
        "buffer_load_dwordx4 v[16:19], v96, s[16:19], s34 offen nt\n\t"
        "s_add_u32 s34, s34, 0x10000\n\t"
        "buffer_load_dwordx4 v[20:23], v96, s[16:19], s34 offen nt\n\t"
        "s_add_u32 s34, s34, 0x10000\n\t"
        "buffer_load_dwordx4 v[24:27], v96, s[16:19], s34 offen nt\n\t"
        "s_add_u32 s34, s34, 0x10000\n\t"
        "buffer_load_dwordx4 v[28:31], v96, s[16:19], s34 offen nt\n\t"
        "s_lshl_b32 s34, s43, 2\n\t"
        "s_add_u32 s34, s34, 2\n\t"
        "s_and_b32 s34, s34, 15\n\t"
        "s_lshl_b32 s34, s34, 19\n\t"
        "buffer_load_dwordx4 v[32:35], v96, s[16:19], s34 offen nt\n\t"
        "s_add_u32 s34, s34, 0x10000\n\t"
        "buffer_load_dwordx4 v[36:39], v96, s[16:19], s34 offen nt\n\t"
        "s_add_u32 s34, s34, 0x10000\n\t"
        "buffer_load_dwordx4 v[40:43], v96, s[16:19], s34 offen nt\n\t"
        "s_add_u32 s34, s34, 0x10000\n\t"
        "buffer_load_dwordx4 v[44:47], v96, s[16:19], s34 offen nt\n\t"
        "s_add_u32 s34, s34, 0x10000\n\t"
        "buffer_load_dwordx4 v[48:51], v96, s[16:19], s34 offen nt\n\t"
        "s_add_u32 s34, s34, 0x10000\n\t"
        "buffer_load_dwordx4 v[52:55], v96, s[16:19], s34 offen nt\n\t"
        "s_add_u32 s34, s34, 0x10000\n\t"
        "buffer_load_dwordx4 v[56:59], v96, s[16:19], s34 offen nt\n\t"
        "s_add_u32 s34, s34, 0x10000\n\t"
        "buffer_load_dwordx4 v[60:63], v96, s[16:19], s34 offen nt\n\t"
        "v_cmp_le_i32_e64 s[60:61], 0, v126\n\t"
        "v_cmp_le_i32_e64 s[62:63], 1, v126\n\t"
        "v_cmp_le_i32_e64 s[64:65], 2, v126\n\t"
        "v_cmp_le_i32_e64 s[66:67], 3, v126\n\t"
        "v_cmp_le_i32_e64 s[68:69], 16, v126\n\t"
        "v_cmp_le_i32_e64 s[70:71], 17, v126\n\t"
        "v_cmp_le_i32_e64 s[72:73], 18, v126\n\t"
        "v_cmp_le_i32_e64 s[74:75], 19, v126\n\t"
        "s_mul_i32 s35, s43, 10240\n\t"
        "v_add_u32_e32 v126, s35, v124\n\t"
        "s_waitcnt vmcnt(16)\n\t"
        "ds_write_b128 v97, v[224:227] offset:0\n\t"
        "ds_write_b128 v98, v[228:231] offset:1024\n\t"
        "ds_write_b128 v97, v[232:235] offset:2048\n\t"
        "ds_write_b128 v98, v[236:239] offset:3072\n\t"
        "ds_write_b128 v97, v[240:243] offset:4096\n\t"
        "ds_write_b128 v98, v[244:247] offset:5120\n\t"
        "ds_write_b128 v97, v[248:251] offset:6144\n\t"
        "ds_write_b128 v98, v[252:255] offset:7168\n\t"
        "ds_read_b64_tr_b16 a[0:1], v99 offset:0\n\t"
        "ds_read_b64_tr_b16 a[2:3], v99 offset:256\n\t"
        "ds_read_b64_tr_b16 a[4:5], v99 offset:4096\n\t"
        "ds_read_b64_tr_b16 a[6:7], v99 offset:4352\n\t"
        "s_waitcnt lgkmcnt(8)\n\t"
        "ds_read_b64_tr_b16 a[8:9], v100 offset:0\n\t"
        "ds_read_b64_tr_b16 a[10:11], v100 offset:256\n\t"
        "ds_read_b64_tr_b16 a[12:13], v100 offset:4096\n\t"
        "ds_read_b64_tr_b16 a[14:15], v100 offset:4352\n\t"
        "s_waitcnt lgkmcnt(8)\n\t"
        "ds_read_b64_tr_b16 a[16:17], v101 offset:0\n\t"
        "ds_read_b64_tr_b16 a[18:19], v101 offset:256\n\t"
        "ds_read_b64_tr_b16 a[20:21], v101 offset:4096\n\t"
        "ds_read_b64_tr_b16 a[22:23], v101 offset:4352\n\t"
        "s_waitcnt lgkmcnt(8)\n\t"
        "ds_read_b64_tr_b16 v[104:105], v102 offset:0\n\t"
        "ds_read_b64_tr_b16 v[106:107], v102 offset:256\n\t"
        "ds_read_b64_tr_b16 v[108:109], v102 offset:4096\n\t"
        "ds_read_b64_tr_b16 v[110:111], v102 offset:4352\n\t"
        "s_lshl_b32 s34, s43, 2\n\t"
        "s_add_u32 s34, s34, 3\n\t"
        "s_and_b32 s34, s34, 15\n\t"
        "s_lshl_b32 s34, s34, 19\n\t"
        "buffer_load_dwordx4 v[64:67], v96, s[16:19], s34 offen nt\n\t"
        "s_add_u32 s34, s34, 0x10000\n\t"
        "buffer_load_dwordx4 v[68:71], v96, s[16:19], s34 offen nt\n\t"
        "s_add_u32 s34, s34, 0x10000\n\t"
        "buffer_load_dwordx4 v[72:75], v96, s[16:19], s34 offen nt\n\t"
        "s_add_u32 s34, s34, 0x10000\n\t"
        "buffer_load_dwordx4 v[76:79], v96, s[16:19], s34 offen nt\n\t"
        "s_add_u32 s34, s34, 0x10000\n\t"
        "buffer_load_dwordx4 v[80:83], v96, s[16:19], s34 offen nt\n\t"
        "s_add_u32 s34, s34, 0x10000\n\t"
        "buffer_load_dwordx4 v[84:87], v96, s[16:19], s34 offen nt\n\t"
        "s_add_u32 s34, s34, 0x10000\n\t"
        "buffer_load_dwordx4 v[88:91], v96, s[16:19], s34 offen nt\n\t"
        "s_add_u32 s34, s34, 0x10000\n\t"
        "buffer_load_dwordx4 v[92:95], v96, s[16:19], s34 offen nt\n\t"
        "s_waitcnt lgkmcnt(0)\n\t"
        "ds_write_b128 v117, v[104:107] offset:0\n\t"
        "ds_write_b128 v117, v[108:111] offset:1024\n\t"
        "s_waitcnt vmcnt(16)\n\t"
        "ds_write_b128 v97, v[0:3] offset:0\n\t"
        "ds_write_b128 v98, v[4:7] offset:1024\n\t"
        "ds_write_b128 v97, v[8:11] offset:2048\n\t"
        "ds_write_b128 v98, v[12:15] offset:3072\n\t"
        "ds_write_b128 v97, v[16:19] offset:4096\n\t"
        "ds_write_b128 v98, v[20:23] offset:5120\n\t"
        "ds_write_b128 v97, v[24:27] offset:6144\n\t"
        "ds_write_b128 v98, v[28:31] offset:7168\n\t"
        "ds_read_b64_tr_b16 a[24:25], v99 offset:0\n\t"
        "ds_read_b64_tr_b16 a[26:27], v99 offset:256\n\t"
        "ds_read_b64_tr_b16 a[28:29], v99 offset:4096\n\t"
        "ds_read_b64_tr_b16 a[30:31], v99 offset:4352\n\t"
        "s_waitcnt lgkmcnt(8)\n\t"
        "ds_read_b64_tr_b16 a[32:33], v100 offset:0\n\t"
        "ds_read_b64_tr_b16 a[34:35], v100 offset:256\n\t"
        "ds_read_b64_tr_b16 a[36:37], v100 offset:4096\n\t"
        "ds_read_b64_tr_b16 a[38:39], v100 offset:4352\n\t"
        "s_waitcnt lgkmcnt(8)\n\t"
        "ds_read_b64_tr_b16 a[40:41], v101 offset:0\n\t"
        "ds_read_b64_tr_b16 a[42:43], v101 offset:256\n\t"
        "ds_read_b64_tr_b16 a[44:45], v101 offset:4096\n\t"
        "ds_read_b64_tr_b16 a[46:47], v101 offset:4352\n\t"
        "s_waitcnt lgkmcnt(8)\n\t"
        "ds_read_b64_tr_b16 v[104:105], v102 offset:0\n\t"
        "ds_read_b64_tr_b16 v[106:107], v102 offset:256\n\t"
        "ds_read_b64_tr_b16 v[108:109], v102 offset:4096\n\t"
        "ds_read_b64_tr_b16 v[110:111], v102 offset:4352\n\t"
        "s_lshl_b32 s34, s43, 2\n\t"
        "s_add_u32 s34, s34, 4\n\t"
        "s_and_b32 s34, s34, 15\n\t"
        "s_lshl_b32 s34, s34, 19\n\t"
        "buffer_load_dwordx4 a[96:99], v96, s[16:19], s34 offen nt\n\t"
        "s_add_u32 s34, s34, 0x10000\n\t"
        "buffer_load_dwordx4 a[100:103], v96, s[16:19], s34 offen nt\n\t"
        "s_add_u32 s34, s34, 0x10000\n\t"
        "buffer_load_dwordx4 a[104:107], v96, s[16:19], s34 offen nt\n\t"
        "s_add_u32 s34, s34, 0x10000\n\t"
        "buffer_load_dwordx4 a[108:111], v96, s[16:19], s34 offen nt\n\t"
        "s_add_u32 s34, s34, 0x10000\n\t"
        "buffer_load_dwordx4 a[112:115], v96, s[16:19], s34 offen nt\n\t"
        "s_add_u32 s34, s34, 0x10000\n\t"
        "buffer_load_dwordx4 a[116:119], v96, s[16:19], s34 offen nt\n\t"
        "s_add_u32 s34, s34, 0x10000\n\t"
        "buffer_load_dwordx4 a[120:123], v96, s[16:19], s34 offen nt\n\t"
        "s_add_u32 s34, s34, 0x10000\n\t"
        "buffer_load_dwordx4 a[124:127], v96, s[16:19], s34 offen nt\n\t"
        "s_waitcnt lgkmcnt(0)\n\t"
        "ds_write_b128 v117, v[104:107] offset:2048\n\t"
        "ds_write_b128 v117, v[108:111] offset:3072\n\t"
        "s_waitcnt vmcnt(16)\n\t"
        "ds_write_b128 v97, v[32:35] offset:0\n\t"
        "ds_write_b128 v98, v[36:39] offset:1024\n\t"
        "ds_write_b128 v97, v[40:43] offset:2048\n\t"
        "ds_write_b128 v98, v[44:47] offset:3072\n\t"
        "ds_write_b128 v97, v[48:51] offset:4096\n\t"
        "ds_write_b128 v98, v[52:55] offset:5120\n\t"
        "ds_write_b128 v97, v[56:59] offset:6144\n\t"
        "ds_write_b128 v98, v[60:63] offset:7168\n\t"
        "ds_read_b64_tr_b16 a[48:49], v99 offset:0\n\t"
        "ds_read_b64_tr_b16 a[50:51], v99 offset:256\n\t"
        "ds_read_b64_tr_b16 a[52:53], v99 offset:4096\n\t"
        "ds_read_b64_tr_b16 a[54:55], v99 offset:4352\n\t"
        "s_waitcnt lgkmcnt(8)\n\t"
        "ds_read_b64_tr_b16 a[56:57], v100 offset:0\n\t"
        "ds_read_b64_tr_b16 a[58:59], v100 offset:256\n\t"
        "ds_read_b64_tr_b16 a[60:61], v100 offset:4096\n\t"
        "ds_read_b64_tr_b16 a[62:63], v100 offset:4352\n\t"
        "s_waitcnt lgkmcnt(8)\n\t"
        "ds_read_b64_tr_b16 a[64:65], v101 offset:0\n\t"
        "ds_read_b64_tr_b16 a[66:67], v101 offset:256\n\t"
        "ds_read_b64_tr_b16 a[68:69], v101 offset:4096\n\t"
        "ds_read_b64_tr_b16 a[70:71], v101 offset:4352\n\t"
        "s_waitcnt lgkmcnt(8)\n\t"
        "ds_read_b64_tr_b16 v[104:105], v102 offset:0\n\t"
        "ds_read_b64_tr_b16 v[106:107], v102 offset:256\n\t"
        "ds_read_b64_tr_b16 v[108:109], v102 offset:4096\n\t"
        "ds_read_b64_tr_b16 v[110:111], v102 offset:4352\n\t"
        "s_lshl_b32 s34, s43, 2\n\t"
        "s_add_u32 s34, s34, 5\n\t"
        "s_and_b32 s34, s34, 15\n\t"
        "s_lshl_b32 s34, s34, 19\n\t"
        "buffer_load_dwordx4 a[128:131], v96, s[16:19], s34 offen nt\n\t"
        "s_add_u32 s34, s34, 0x10000\n\t"
        "buffer_load_dwordx4 a[132:135], v96, s[16:19], s34 offen nt\n\t"
        "s_add_u32 s34, s34, 0x10000\n\t"
        "buffer_load_dwordx4 a[136:139], v96, s[16:19], s34 offen nt\n\t"
        "s_add_u32 s34, s34, 0x10000\n\t"
        "buffer_load_dwordx4 a[140:143], v96, s[16:19], s34 offen nt\n\t"
        "s_add_u32 s34, s34, 0x10000\n\t"
        "buffer_load_dwordx4 a[144:147], v96, s[16:19], s34 offen nt\n\t"
        "s_add_u32 s34, s34, 0x10000\n\t"
        "buffer_load_dwordx4 a[148:151], v96, s[16:19], s34 offen nt\n\t"
        "s_add_u32 s34, s34, 0x10000\n\t"
        "buffer_load_dwordx4 a[152:155], v96, s[16:19], s34 offen nt\n\t"
        "s_add_u32 s34, s34, 0x10000\n\t"
        "buffer_load_dwordx4 a[156:159], v96, s[16:19], s34 offen nt\n\t"
        "s_waitcnt lgkmcnt(0)\n\t"
        "ds_write_b128 v117, v[104:107] offset:4096\n\t"
        "ds_write_b128 v117, v[108:111] offset:5120\n\t"
        "s_waitcnt vmcnt(16)\n\t"
        "ds_write_b128 v97, v[64:67] offset:0\n\t"
        "ds_write_b128 v98, v[68:71] offset:1024\n\t"
        "ds_write_b128 v97, v[72:75] offset:2048\n\t"
        "ds_write_b128 v98, v[76:79] offset:3072\n\t"
        "ds_write_b128 v97, v[80:83] offset:4096\n\t"
        "ds_write_b128 v98, v[84:87] offset:5120\n\t"
        "ds_write_b128 v97, v[88:91] offset:6144\n\t"
        "ds_write_b128 v98, v[92:95] offset:7168\n\t"
        "ds_read_b64_tr_b16 a[72:73], v99 offset:0\n\t"
        "ds_read_b64_tr_b16 a[74:75], v99 offset:256\n\t"
        "ds_read_b64_tr_b16 a[76:77], v99 offset:4096\n\t"
        "ds_read_b64_tr_b16 a[78:79], v99 offset:4352\n\t"
        "s_waitcnt lgkmcnt(8)\n\t"
        "ds_read_b64_tr_b16 a[80:81], v100 offset:0\n\t"
        "ds_read_b64_tr_b16 a[82:83], v100 offset:256\n\t"
        "ds_read_b64_tr_b16 a[84:85], v100 offset:4096\n\t"
        "ds_read_b64_tr_b16 a[86:87], v100 offset:4352\n\t"
        "s_waitcnt lgkmcnt(8)\n\t"
        "ds_read_b64_tr_b16 a[88:89], v101 offset:0\n\t"
        "ds_read_b64_tr_b16 a[90:91], v101 offset:256\n\t"
        "ds_read_b64_tr_b16 a[92:93], v101 offset:4096\n\t"
        "ds_read_b64_tr_b16 a[94:95], v101 offset:4352\n\t"
        "s_waitcnt lgkmcnt(8)\n\t"
        "ds_read_b64_tr_b16 v[104:105], v102 offset:0\n\t"
        "ds_read_b64_tr_b16 v[106:107], v102 offset:256\n\t"
        "ds_read_b64_tr_b16 v[108:109], v102 offset:4096\n\t"
        "ds_read_b64_tr_b16 v[110:111], v102 offset:4352\n\t"
        "s_lshl_b32 s34, s43, 2\n\t"
        "s_add_u32 s34, s34, 6\n\t"
        "s_and_b32 s34, s34, 15\n\t"
        "s_lshl_b32 s34, s34, 19\n\t"
        "buffer_load_dwordx4 a[160:163], v96, s[16:19], s34 offen nt\n\t"
        "s_add_u32 s34, s34, 0x10000\n\t"
        "buffer_load_dwordx4 a[164:167], v96, s[16:19], s34 offen nt\n\t"
        "s_add_u32 s34, s34, 0x10000\n\t"
        "buffer_load_dwordx4 a[168:171], v96, s[16:19], s34 offen nt\n\t"
        "s_add_u32 s34, s34, 0x10000\n\t"
        "buffer_load_dwordx4 a[172:175], v96, s[16:19], s34 offen nt\n\t"
        "s_add_u32 s34, s34, 0x10000\n\t"
        "buffer_load_dwordx4 a[176:179], v96, s[16:19], s34 offen nt\n\t"
        "s_add_u32 s34, s34, 0x10000\n\t"
        "buffer_load_dwordx4 a[180:183], v96, s[16:19], s34 offen nt\n\t"
        "s_add_u32 s34, s34, 0x10000\n\t"
        "buffer_load_dwordx4 a[184:187], v96, s[16:19], s34 offen nt\n\t"
        "s_add_u32 s34, s34, 0x10000\n\t"
        "buffer_load_dwordx4 a[188:191], v96, s[16:19], s34 offen nt\n\t"
        "s_waitcnt lgkmcnt(0)\n\t"
        "ds_write_b128 v117, v[104:107] offset:6144\n\t"
        "ds_write_b128 v117, v[108:111] offset:7168\n\t"
        "s_waitcnt vmcnt(16)\n\t"
        "ds_write_b128 v97, a[96:99] offset:0\n\t"
        "ds_write_b128 v98, a[100:103] offset:1024\n\t"
        "ds_write_b128 v97, a[104:107] offset:2048\n\t"
        "ds_write_b128 v98, a[108:111] offset:3072\n\t"
        "ds_write_b128 v97, a[112:115] offset:4096\n\t"
        "ds_write_b128 v98, a[116:119] offset:5120\n\t"
        "ds_write_b128 v97, a[120:123] offset:6144\n\t"
        "ds_write_b128 v98, a[124:127] offset:7168\n\t"
        "ds_read_b64_tr_b16 a[96:97], v99 offset:0\n\t"
        "ds_read_b64_tr_b16 a[98:99], v99 offset:256\n\t"
        "ds_read_b64_tr_b16 a[100:101], v99 offset:4096\n\t"
        "ds_read_b64_tr_b16 a[102:103], v99 offset:4352\n\t"
        "s_waitcnt lgkmcnt(8)\n\t"
        "ds_read_b64_tr_b16 a[104:105], v100 offset:0\n\t"
        "ds_read_b64_tr_b16 a[106:107], v100 offset:256\n\t"
        "ds_read_b64_tr_b16 a[108:109], v100 offset:4096\n\t"
        "ds_read_b64_tr_b16 a[110:111], v100 offset:4352\n\t"
        "s_waitcnt lgkmcnt(8)\n\t"
        "ds_read_b64_tr_b16 a[112:113], v101 offset:0\n\t"
        "ds_read_b64_tr_b16 a[114:115], v101 offset:256\n\t"
        "ds_read_b64_tr_b16 a[116:117], v101 offset:4096\n\t"
        "ds_read_b64_tr_b16 a[118:119], v101 offset:4352\n\t"
        "s_waitcnt lgkmcnt(8)\n\t"
        "ds_read_b64_tr_b16 v[104:105], v102 offset:0\n\t"
        "ds_read_b64_tr_b16 v[106:107], v102 offset:256\n\t"
        "ds_read_b64_tr_b16 v[108:109], v102 offset:4096\n\t"
        "ds_read_b64_tr_b16 v[110:111], v102 offset:4352\n\t"
        "s_lshl_b32 s34, s43, 2\n\t"
        "s_add_u32 s34, s34, 7\n\t"
        "s_and_b32 s34, s34, 15\n\t"
        "s_lshl_b32 s34, s34, 19\n\t"
        "buffer_load_dwordx4 a[192:195], v96, s[16:19], s34 offen nt\n\t"
        "s_add_u32 s34, s34, 0x10000\n\t"
        "buffer_load_dwordx4 a[196:199], v96, s[16:19], s34 offen nt\n\t"
        "s_add_u32 s34, s34, 0x10000\n\t"
        "buffer_load_dwordx4 a[200:203], v96, s[16:19], s34 offen nt\n\t"
        "s_add_u32 s34, s34, 0x10000\n\t"
        "buffer_load_dwordx4 a[204:207], v96, s[16:19], s34 offen nt\n\t"
        "s_add_u32 s34, s34, 0x10000\n\t"
        "buffer_load_dwordx4 a[208:211], v96, s[16:19], s34 offen nt\n\t"
        "s_add_u32 s34, s34, 0x10000\n\t"
        "buffer_load_dwordx4 a[212:215], v96, s[16:19], s34 offen nt\n\t"
        "s_add_u32 s34, s34, 0x10000\n\t"
        "buffer_load_dwordx4 a[216:219], v96, s[16:19], s34 offen nt\n\t"
        "s_add_u32 s34, s34, 0x10000\n\t"
        "buffer_load_dwordx4 a[220:223], v96, s[16:19], s34 offen nt\n\t"
        "s_waitcnt lgkmcnt(0)\n\t"
        "ds_write_b128 v126, v[104:107] offset:0\n\t"
        "ds_write_b128 v126, v[108:111] offset:1024\n\t"
        "s_waitcnt vmcnt(16)\n\t"
        "ds_write_b128 v97, a[128:131] offset:0\n\t"
        "ds_write_b128 v98, a[132:135] offset:1024\n\t"
        "ds_write_b128 v97, a[136:139] offset:2048\n\t"
        "ds_write_b128 v98, a[140:143] offset:3072\n\t"
        "ds_write_b128 v97, a[144:147] offset:4096\n\t"
        "ds_write_b128 v98, a[148:151] offset:5120\n\t"
        "ds_write_b128 v97, a[152:155] offset:6144\n\t"
        "ds_write_b128 v98, a[156:159] offset:7168\n\t"
        "ds_read_b64_tr_b16 a[120:121], v99 offset:0\n\t"
        "ds_read_b64_tr_b16 a[122:123], v99 offset:256\n\t"
        "ds_read_b64_tr_b16 a[124:125], v99 offset:4096\n\t"
        "ds_read_b64_tr_b16 a[126:127], v99 offset:4352\n\t"
        "s_waitcnt lgkmcnt(8)\n\t"
        "ds_read_b64_tr_b16 a[128:129], v100 offset:0\n\t"
        "ds_read_b64_tr_b16 a[130:131], v100 offset:256\n\t"
        "ds_read_b64_tr_b16 a[132:133], v100 offset:4096\n\t"
        "ds_read_b64_tr_b16 a[134:135], v100 offset:4352\n\t"
        "s_waitcnt lgkmcnt(8)\n\t"
        "ds_read_b64_tr_b16 a[136:137], v101 offset:0\n\t"
        "ds_read_b64_tr_b16 a[138:139], v101 offset:256\n\t"
        "ds_read_b64_tr_b16 a[140:141], v101 offset:4096\n\t"
        "ds_read_b64_tr_b16 a[142:143], v101 offset:4352\n\t"
        "s_waitcnt lgkmcnt(8)\n\t"
        "ds_read_b64_tr_b16 v[104:105], v102 offset:0\n\t"
        "ds_read_b64_tr_b16 v[106:107], v102 offset:256\n\t"
        "ds_read_b64_tr_b16 v[108:109], v102 offset:4096\n\t"
        "ds_read_b64_tr_b16 v[110:111], v102 offset:4352\n\t"
        "s_lshl_b32 s34, s43, 2\n\t"
        "s_add_u32 s34, s34, 8\n\t"
        "s_and_b32 s34, s34, 15\n\t"
        "s_lshl_b32 s34, s34, 19\n\t"
        "buffer_load_dwordx4 a[224:227], v96, s[16:19], s34 offen nt\n\t"
        "s_add_u32 s34, s34, 0x10000\n\t"
        "buffer_load_dwordx4 a[228:231], v96, s[16:19], s34 offen nt\n\t"
        "s_add_u32 s34, s34, 0x10000\n\t"
        "buffer_load_dwordx4 a[232:235], v96, s[16:19], s34 offen nt\n\t"
        "s_add_u32 s34, s34, 0x10000\n\t"
        "buffer_load_dwordx4 a[236:239], v96, s[16:19], s34 offen nt\n\t"
        "s_add_u32 s34, s34, 0x10000\n\t"
        "buffer_load_dwordx4 a[240:243], v96, s[16:19], s34 offen nt\n\t"
        "s_add_u32 s34, s34, 0x10000\n\t"
        "buffer_load_dwordx4 a[244:247], v96, s[16:19], s34 offen nt\n\t"
        "s_add_u32 s34, s34, 0x10000\n\t"
        "buffer_load_dwordx4 a[248:251], v96, s[16:19], s34 offen nt\n\t"
        "s_add_u32 s34, s34, 0x10000\n\t"
        "buffer_load_dwordx4 a[252:255], v96, s[16:19], s34 offen nt\n\t"
        "s_waitcnt lgkmcnt(0)\n\t"
        "ds_write_b128 v126, v[104:107] offset:2048\n\t"
        "ds_write_b128 v126, v[108:111] offset:3072\n\t"
        "s_waitcnt vmcnt(16)\n\t"
        "ds_write_b128 v97, a[160:163] offset:0\n\t"
        "ds_write_b128 v98, a[164:167] offset:1024\n\t"
        "ds_write_b128 v97, a[168:171] offset:2048\n\t"
        "ds_write_b128 v98, a[172:175] offset:3072\n\t"
        "ds_write_b128 v97, a[176:179] offset:4096\n\t"
        "ds_write_b128 v98, a[180:183] offset:5120\n\t"
        "ds_write_b128 v97, a[184:187] offset:6144\n\t"
        "ds_write_b128 v98, a[188:191] offset:7168\n\t"
        "ds_read_b64_tr_b16 a[144:145], v99 offset:0\n\t"
        "ds_read_b64_tr_b16 a[146:147], v99 offset:256\n\t"
        "ds_read_b64_tr_b16 a[148:149], v99 offset:4096\n\t"
        "ds_read_b64_tr_b16 a[150:151], v99 offset:4352\n\t"
        "s_waitcnt lgkmcnt(8)\n\t"
        "ds_read_b64_tr_b16 a[152:153], v100 offset:0\n\t"
        "ds_read_b64_tr_b16 a[154:155], v100 offset:256\n\t"
        "ds_read_b64_tr_b16 a[156:157], v100 offset:4096\n\t"
        "ds_read_b64_tr_b16 a[158:159], v100 offset:4352\n\t"
        "s_waitcnt lgkmcnt(8)\n\t"
        "ds_read_b64_tr_b16 a[160:161], v101 offset:0\n\t"
        "ds_read_b64_tr_b16 a[162:163], v101 offset:256\n\t"
        "ds_read_b64_tr_b16 a[164:165], v101 offset:4096\n\t"
        "ds_read_b64_tr_b16 a[166:167], v101 offset:4352\n\t"
        "s_waitcnt lgkmcnt(8)\n\t"
        "ds_read_b64_tr_b16 v[104:105], v102 offset:0\n\t"
        "ds_read_b64_tr_b16 v[106:107], v102 offset:256\n\t"
        "ds_read_b64_tr_b16 v[108:109], v102 offset:4096\n\t"
        "ds_read_b64_tr_b16 v[110:111], v102 offset:4352\n\t"
        "s_lshl_b32 s34, s43, 2\n\t"
        "s_add_u32 s34, s34, 9\n\t"
        "s_and_b32 s34, s34, 15\n\t"
        "s_lshl_b32 s34, s34, 19\n\t"
        "buffer_load_dwordx4 v[128:131], v96, s[16:19], s34 offen nt\n\t"
        "s_add_u32 s34, s34, 0x10000\n\t"
        "buffer_load_dwordx4 v[132:135], v96, s[16:19], s34 offen nt\n\t"
        "s_add_u32 s34, s34, 0x10000\n\t"
        "buffer_load_dwordx4 v[136:139], v96, s[16:19], s34 offen nt\n\t"
        "s_add_u32 s34, s34, 0x10000\n\t"
        "buffer_load_dwordx4 v[140:143], v96, s[16:19], s34 offen nt\n\t"
        "s_add_u32 s34, s34, 0x10000\n\t"
        "buffer_load_dwordx4 v[144:147], v96, s[16:19], s34 offen nt\n\t"
        "s_add_u32 s34, s34, 0x10000\n\t"
        "buffer_load_dwordx4 v[148:151], v96, s[16:19], s34 offen nt\n\t"
        "s_add_u32 s34, s34, 0x10000\n\t"
        "buffer_load_dwordx4 v[152:155], v96, s[16:19], s34 offen nt\n\t"
        "s_add_u32 s34, s34, 0x10000\n\t"
        "buffer_load_dwordx4 v[156:159], v96, s[16:19], s34 offen nt\n\t"
        "s_waitcnt lgkmcnt(0)\n\t"
        "ds_write_b128 v126, v[104:107] offset:4096\n\t"
        "ds_write_b128 v126, v[108:111] offset:5120\n\t"
        "s_waitcnt vmcnt(16)\n\t"
        "ds_write_b128 v97, a[192:195] offset:0\n\t"
        "ds_write_b128 v98, a[196:199] offset:1024\n\t"
        "ds_write_b128 v97, a[200:203] offset:2048\n\t"
        "ds_write_b128 v98, a[204:207] offset:3072\n\t"
        "ds_write_b128 v97, a[208:211] offset:4096\n\t"
        "ds_write_b128 v98, a[212:215] offset:5120\n\t"
        "ds_write_b128 v97, a[216:219] offset:6144\n\t"
        "ds_write_b128 v98, a[220:223] offset:7168\n\t"
        "ds_read_b64_tr_b16 a[168:169], v99 offset:0\n\t"
        "ds_read_b64_tr_b16 a[170:171], v99 offset:256\n\t"
        "ds_read_b64_tr_b16 a[172:173], v99 offset:4096\n\t"
        "ds_read_b64_tr_b16 a[174:175], v99 offset:4352\n\t"
        "s_waitcnt lgkmcnt(8)\n\t"
        "ds_read_b64_tr_b16 a[176:177], v100 offset:0\n\t"
        "ds_read_b64_tr_b16 a[178:179], v100 offset:256\n\t"
        "ds_read_b64_tr_b16 a[180:181], v100 offset:4096\n\t"
        "ds_read_b64_tr_b16 a[182:183], v100 offset:4352\n\t"
        "s_waitcnt lgkmcnt(8)\n\t"
        "ds_read_b64_tr_b16 a[184:185], v101 offset:0\n\t"
        "ds_read_b64_tr_b16 a[186:187], v101 offset:256\n\t"
        "ds_read_b64_tr_b16 a[188:189], v101 offset:4096\n\t"
        "ds_read_b64_tr_b16 a[190:191], v101 offset:4352\n\t"
        "s_waitcnt lgkmcnt(8)\n\t"
        "ds_read_b64_tr_b16 v[104:105], v102 offset:0\n\t"
        "ds_read_b64_tr_b16 v[106:107], v102 offset:256\n\t"
        "ds_read_b64_tr_b16 v[108:109], v102 offset:4096\n\t"
        "ds_read_b64_tr_b16 v[110:111], v102 offset:4352\n\t"
        "s_lshl_b32 s34, s43, 2\n\t"
        "s_add_u32 s34, s34, 10\n\t"
        "s_and_b32 s34, s34, 15\n\t"
        "s_lshl_b32 s34, s34, 19\n\t"
        "buffer_load_dwordx4 v[160:163], v96, s[16:19], s34 offen nt\n\t"
        "s_add_u32 s34, s34, 0x10000\n\t"
        "buffer_load_dwordx4 v[164:167], v96, s[16:19], s34 offen nt\n\t"
        "s_add_u32 s34, s34, 0x10000\n\t"
        "buffer_load_dwordx4 v[168:171], v96, s[16:19], s34 offen nt\n\t"
        "s_add_u32 s34, s34, 0x10000\n\t"
        "buffer_load_dwordx4 v[172:175], v96, s[16:19], s34 offen nt\n\t"
        "s_add_u32 s34, s34, 0x10000\n\t"
        "buffer_load_dwordx4 v[176:179], v96, s[16:19], s34 offen nt\n\t"
        "s_add_u32 s34, s34, 0x10000\n\t"
        "buffer_load_dwordx4 v[180:183], v96, s[16:19], s34 offen nt\n\t"
        "s_add_u32 s34, s34, 0x10000\n\t"
        "buffer_load_dwordx4 v[184:187], v96, s[16:19], s34 offen nt\n\t"
        "s_add_u32 s34, s34, 0x10000\n\t"
        "buffer_load_dwordx4 v[188:191], v96, s[16:19], s34 offen nt\n\t"
        "s_waitcnt lgkmcnt(0)\n\t"
        "ds_write_b128 v126, v[104:107] offset:6144\n\t"
        "ds_write_b128 v126, v[108:111] offset:7168\n\t"
        "s_waitcnt vmcnt(16)\n\t"
        "ds_write_b128 v97, a[224:227] offset:0\n\t"
        "ds_write_b128 v98, a[228:231] offset:1024\n\t"
        "ds_write_b128 v97, a[232:235] offset:2048\n\t"
        "ds_write_b128 v98, a[236:239] offset:3072\n\t"
        "ds_write_b128 v97, a[240:243] offset:4096\n\t"
        "ds_write_b128 v98, a[244:247] offset:5120\n\t"
        "ds_write_b128 v97, a[248:251] offset:6144\n\t"
        "ds_write_b128 v98, a[252:255] offset:7168\n\t"
        "ds_read_b64_tr_b16 a[192:193], v99 offset:0\n\t"
        "ds_read_b64_tr_b16 a[194:195], v99 offset:256\n\t"
        "ds_read_b64_tr_b16 a[196:197], v99 offset:4096\n\t"
        "ds_read_b64_tr_b16 a[198:199], v99 offset:4352\n\t"
        "s_waitcnt lgkmcnt(8)\n\t"
        "ds_read_b64_tr_b16 a[200:201], v100 offset:0\n\t"
        "ds_read_b64_tr_b16 a[202:203], v100 offset:256\n\t"
        "ds_read_b64_tr_b16 a[204:205], v100 offset:4096\n\t"
        "ds_read_b64_tr_b16 a[206:207], v100 offset:4352\n\t"
        "s_waitcnt lgkmcnt(8)\n\t"
        "ds_read_b64_tr_b16 a[208:209], v101 offset:0\n\t"
        "ds_read_b64_tr_b16 a[210:211], v101 offset:256\n\t"
        "ds_read_b64_tr_b16 a[212:213], v101 offset:4096\n\t"
        "ds_read_b64_tr_b16 a[214:215], v101 offset:4352\n\t"
        "s_waitcnt lgkmcnt(8)\n\t"
        "ds_read_b64_tr_b16 v[104:105], v102 offset:0\n\t"
        "ds_read_b64_tr_b16 v[106:107], v102 offset:256\n\t"
        "ds_read_b64_tr_b16 v[108:109], v102 offset:4096\n\t"
        "ds_read_b64_tr_b16 v[110:111], v102 offset:4352\n\t"
        "s_lshl_b32 s34, s43, 2\n\t"
        "s_add_u32 s34, s34, 11\n\t"
        "s_and_b32 s34, s34, 15\n\t"
        "s_lshl_b32 s34, s34, 19\n\t"
        "buffer_load_dwordx4 v[192:195], v96, s[16:19], s34 offen nt\n\t"
        "s_add_u32 s34, s34, 0x10000\n\t"
        "buffer_load_dwordx4 v[196:199], v96, s[16:19], s34 offen nt\n\t"
        "s_add_u32 s34, s34, 0x10000\n\t"
        "buffer_load_dwordx4 v[200:203], v96, s[16:19], s34 offen nt\n\t"
        "s_add_u32 s34, s34, 0x10000\n\t"
        "buffer_load_dwordx4 v[204:207], v96, s[16:19], s34 offen nt\n\t"
        "s_add_u32 s34, s34, 0x10000\n\t"
        "buffer_load_dwordx4 v[208:211], v96, s[16:19], s34 offen nt\n\t"
        "s_add_u32 s34, s34, 0x10000\n\t"
        "buffer_load_dwordx4 v[212:215], v96, s[16:19], s34 offen nt\n\t"
        "s_add_u32 s34, s34, 0x10000\n\t"
        "buffer_load_dwordx4 v[216:219], v96, s[16:19], s34 offen nt\n\t"
        "s_add_u32 s34, s34, 0x10000\n\t"
        "buffer_load_dwordx4 v[220:223], v96, s[16:19], s34 offen nt\n\t"
        "s_waitcnt lgkmcnt(0)\n\t"
        "ds_write_b128 v117, v[104:107] offset:8192\n\t"
        "ds_write_b128 v117, v[108:111] offset:9216\n\t"
        "s_waitcnt vmcnt(16)\n\t"
        "ds_write_b128 v97, v[128:131] offset:0\n\t"
        "ds_write_b128 v98, v[132:135] offset:1024\n\t"
        "ds_write_b128 v97, v[136:139] offset:2048\n\t"
        "ds_write_b128 v98, v[140:143] offset:3072\n\t"
        "ds_write_b128 v97, v[144:147] offset:4096\n\t"
        "ds_write_b128 v98, v[148:151] offset:5120\n\t"
        "ds_write_b128 v97, v[152:155] offset:6144\n\t"
        "ds_write_b128 v98, v[156:159] offset:7168\n\t"
        "ds_read_b64_tr_b16 a[216:217], v99 offset:0\n\t"
        "ds_read_b64_tr_b16 a[218:219], v99 offset:256\n\t"
        "ds_read_b64_tr_b16 a[220:221], v99 offset:4096\n\t"
        "ds_read_b64_tr_b16 a[222:223], v99 offset:4352\n\t"
        "s_waitcnt lgkmcnt(8)\n\t"
        "ds_read_b64_tr_b16 a[224:225], v100 offset:0\n\t"
        "ds_read_b64_tr_b16 a[226:227], v100 offset:256\n\t"
        "ds_read_b64_tr_b16 a[228:229], v100 offset:4096\n\t"
        "ds_read_b64_tr_b16 a[230:231], v100 offset:4352\n\t"
        "s_waitcnt lgkmcnt(8)\n\t"
        "ds_read_b64_tr_b16 a[232:233], v101 offset:0\n\t"
        "ds_read_b64_tr_b16 a[234:235], v101 offset:256\n\t"
        "ds_read_b64_tr_b16 a[236:237], v101 offset:4096\n\t"
        "ds_read_b64_tr_b16 a[238:239], v101 offset:4352\n\t"
        "s_waitcnt lgkmcnt(8)\n\t"
        "ds_read_b64_tr_b16 v[104:105], v102 offset:0\n\t"
        "ds_read_b64_tr_b16 v[106:107], v102 offset:256\n\t"
        "ds_read_b64_tr_b16 v[108:109], v102 offset:4096\n\t"
        "ds_read_b64_tr_b16 v[110:111], v102 offset:4352\n\t"
        "s_lshl_b32 s34, s43, 2\n\t"
        "s_add_u32 s34, s34, 12\n\t"
        "s_and_b32 s34, s34, 15\n\t"
        "s_lshl_b32 s34, s34, 19\n\t"
        "buffer_load_dwordx4 v[224:227], v96, s[16:19], s34 offen nt\n\t"
        "s_add_u32 s34, s34, 0x10000\n\t"
        "buffer_load_dwordx4 v[228:231], v96, s[16:19], s34 offen nt\n\t"
        "s_add_u32 s34, s34, 0x10000\n\t"
        "buffer_load_dwordx4 v[232:235], v96, s[16:19], s34 offen nt\n\t"
        "s_add_u32 s34, s34, 0x10000\n\t"
        "buffer_load_dwordx4 v[236:239], v96, s[16:19], s34 offen nt\n\t"
        "s_add_u32 s34, s34, 0x10000\n\t"
        "buffer_load_dwordx4 v[240:243], v96, s[16:19], s34 offen nt\n\t"
        "s_add_u32 s34, s34, 0x10000\n\t"
        "buffer_load_dwordx4 v[244:247], v96, s[16:19], s34 offen nt\n\t"
        "s_add_u32 s34, s34, 0x10000\n\t"
        "buffer_load_dwordx4 v[248:251], v96, s[16:19], s34 offen nt\n\t"
        "s_add_u32 s34, s34, 0x10000\n\t"
        "buffer_load_dwordx4 v[252:255], v96, s[16:19], s34 offen nt\n\t"
        "s_waitcnt lgkmcnt(0)\n\t"
        "ds_write_b128 v117, v[104:107] offset:10240\n\t"
        "ds_write_b128 v117, v[108:111] offset:11264\n\t"
        "s_waitcnt vmcnt(16)\n\t"
        "ds_write_b128 v97, v[160:163] offset:0\n\t"
        "ds_write_b128 v98, v[164:167] offset:1024\n\t"
        "ds_write_b128 v97, v[168:171] offset:2048\n\t"
        "ds_write_b128 v98, v[172:175] offset:3072\n\t"
        "ds_write_b128 v97, v[176:179] offset:4096\n\t"
        "ds_write_b128 v98, v[180:183] offset:5120\n\t"
        "ds_write_b128 v97, v[184:187] offset:6144\n\t"
        "ds_write_b128 v98, v[188:191] offset:7168\n\t"
        "ds_read_b64_tr_b16 a[240:241], v99 offset:0\n\t"
        "ds_read_b64_tr_b16 a[242:243], v99 offset:256\n\t"
        "ds_read_b64_tr_b16 a[244:245], v99 offset:4096\n\t"
        "ds_read_b64_tr_b16 a[246:247], v99 offset:4352\n\t"
        "s_waitcnt lgkmcnt(8)\n\t"
        "ds_read_b64_tr_b16 a[248:249], v100 offset:0\n\t"
        "ds_read_b64_tr_b16 a[250:251], v100 offset:256\n\t"
        "ds_read_b64_tr_b16 a[252:253], v100 offset:4096\n\t"
        "ds_read_b64_tr_b16 a[254:255], v100 offset:4352\n\t"
        "s_waitcnt lgkmcnt(8)\n\t"
        "ds_read_b64_tr_b16 v[128:129], v101 offset:0\n\t"
        "ds_read_b64_tr_b16 v[130:131], v101 offset:256\n\t"
        "ds_read_b64_tr_b16 v[132:133], v101 offset:4096\n\t"
        "ds_read_b64_tr_b16 v[134:135], v101 offset:4352\n\t"
        "s_waitcnt lgkmcnt(8)\n\t"
        "ds_read_b64_tr_b16 v[104:105], v102 offset:0\n\t"
        "ds_read_b64_tr_b16 v[106:107], v102 offset:256\n\t"
        "ds_read_b64_tr_b16 v[108:109], v102 offset:4096\n\t"
        "ds_read_b64_tr_b16 v[110:111], v102 offset:4352\n\t"
        "s_lshl_b32 s34, s43, 2\n\t"
        "s_add_u32 s34, s34, 13\n\t"
        "s_and_b32 s34, s34, 15\n\t"
        "s_lshl_b32 s34, s34, 19\n\t"
        "buffer_load_dwordx4 v[0:3], v96, s[16:19], s34 offen nt\n\t"
        "s_add_u32 s34, s34, 0x10000\n\t"
        "buffer_load_dwordx4 v[4:7], v96, s[16:19], s34 offen nt\n\t"
        "s_add_u32 s34, s34, 0x10000\n\t"
        "buffer_load_dwordx4 v[8:11], v96, s[16:19], s34 offen nt\n\t"
        "s_add_u32 s34, s34, 0x10000\n\t"
        "buffer_load_dwordx4 v[12:15], v96, s[16:19], s34 offen nt\n\t"
        "s_add_u32 s34, s34, 0x10000\n\t"
        "buffer_load_dwordx4 v[16:19], v96, s[16:19], s34 offen nt\n\t"
        "s_add_u32 s34, s34, 0x10000\n\t"
        "buffer_load_dwordx4 v[20:23], v96, s[16:19], s34 offen nt\n\t"
        "s_add_u32 s34, s34, 0x10000\n\t"
        "buffer_load_dwordx4 v[24:27], v96, s[16:19], s34 offen nt\n\t"
        "s_add_u32 s34, s34, 0x10000\n\t"
        "buffer_load_dwordx4 v[28:31], v96, s[16:19], s34 offen nt\n\t"
        "s_waitcnt lgkmcnt(0)\n\t"
        "ds_write_b128 v117, v[104:107] offset:12288\n\t"
        "ds_write_b128 v117, v[108:111] offset:13312\n\t"
        "s_waitcnt vmcnt(16)\n\t"
        "ds_write_b128 v97, v[192:195] offset:0\n\t"
        "ds_write_b128 v98, v[196:199] offset:1024\n\t"
        "ds_write_b128 v97, v[200:203] offset:2048\n\t"
        "ds_write_b128 v98, v[204:207] offset:3072\n\t"
        "ds_write_b128 v97, v[208:211] offset:4096\n\t"
        "ds_write_b128 v98, v[212:215] offset:5120\n\t"
        "ds_write_b128 v97, v[216:219] offset:6144\n\t"
        "ds_write_b128 v98, v[220:223] offset:7168\n\t"
        "ds_read_b64_tr_b16 v[136:137], v99 offset:0\n\t"
        "ds_read_b64_tr_b16 v[138:139], v99 offset:256\n\t"
        "ds_read_b64_tr_b16 v[140:141], v99 offset:4096\n\t"
        "ds_read_b64_tr_b16 v[142:143], v99 offset:4352\n\t"
        "s_waitcnt lgkmcnt(8)\n\t"
        "ds_read_b64_tr_b16 v[144:145], v100 offset:0\n\t"
        "ds_read_b64_tr_b16 v[146:147], v100 offset:256\n\t"
        "ds_read_b64_tr_b16 v[148:149], v100 offset:4096\n\t"
        "ds_read_b64_tr_b16 v[150:151], v100 offset:4352\n\t"
        "s_waitcnt lgkmcnt(8)\n\t"
        "ds_read_b64_tr_b16 v[152:153], v101 offset:0\n\t"
        "ds_read_b64_tr_b16 v[154:155], v101 offset:256\n\t"
        "ds_read_b64_tr_b16 v[156:157], v101 offset:4096\n\t"
        "ds_read_b64_tr_b16 v[158:159], v101 offset:4352\n\t"
        "s_waitcnt lgkmcnt(8)\n\t"
        "ds_read_b64_tr_b16 v[104:105], v102 offset:0\n\t"
        "ds_read_b64_tr_b16 v[106:107], v102 offset:256\n\t"
        "ds_read_b64_tr_b16 v[108:109], v102 offset:4096\n\t"
        "ds_read_b64_tr_b16 v[110:111], v102 offset:4352\n\t"
        "s_lshl_b32 s34, s43, 2\n\t"
        "s_add_u32 s34, s34, 14\n\t"
        "s_and_b32 s34, s34, 15\n\t"
        "s_lshl_b32 s34, s34, 19\n\t"
        "buffer_load_dwordx4 v[32:35], v96, s[16:19], s34 offen nt\n\t"
        "s_add_u32 s34, s34, 0x10000\n\t"
        "buffer_load_dwordx4 v[36:39], v96, s[16:19], s34 offen nt\n\t"
        "s_add_u32 s34, s34, 0x10000\n\t"
        "buffer_load_dwordx4 v[40:43], v96, s[16:19], s34 offen nt\n\t"
        "s_add_u32 s34, s34, 0x10000\n\t"
        "buffer_load_dwordx4 v[44:47], v96, s[16:19], s34 offen nt\n\t"
        "s_add_u32 s34, s34, 0x10000\n\t"
        "buffer_load_dwordx4 v[48:51], v96, s[16:19], s34 offen nt\n\t"
        "s_add_u32 s34, s34, 0x10000\n\t"
        "buffer_load_dwordx4 v[52:55], v96, s[16:19], s34 offen nt\n\t"
        "s_add_u32 s34, s34, 0x10000\n\t"
        "buffer_load_dwordx4 v[56:59], v96, s[16:19], s34 offen nt\n\t"
        "s_add_u32 s34, s34, 0x10000\n\t"
        "buffer_load_dwordx4 v[60:63], v96, s[16:19], s34 offen nt\n\t"
        "s_waitcnt lgkmcnt(0)\n\t"
        "ds_write_b128 v117, v[104:107] offset:14336\n\t"
        "ds_write_b128 v117, v[108:111] offset:15360\n\t"
        "s_waitcnt vmcnt(16)\n\t"
        "ds_write_b128 v97, v[224:227] offset:0\n\t"
        "ds_write_b128 v98, v[228:231] offset:1024\n\t"
        "ds_write_b128 v97, v[232:235] offset:2048\n\t"
        "ds_write_b128 v98, v[236:239] offset:3072\n\t"
        "ds_write_b128 v97, v[240:243] offset:4096\n\t"
        "ds_write_b128 v98, v[244:247] offset:5120\n\t"
        "ds_write_b128 v97, v[248:251] offset:6144\n\t"
        "ds_write_b128 v98, v[252:255] offset:7168\n\t"
        "ds_read_b64_tr_b16 v[160:161], v99 offset:0\n\t"
        "ds_read_b64_tr_b16 v[162:163], v99 offset:256\n\t"
        "ds_read_b64_tr_b16 v[164:165], v99 offset:4096\n\t"
        "ds_read_b64_tr_b16 v[166:167], v99 offset:4352\n\t"
        "s_waitcnt lgkmcnt(8)\n\t"
        "ds_read_b64_tr_b16 v[168:169], v100 offset:0\n\t"
        "ds_read_b64_tr_b16 v[170:171], v100 offset:256\n\t"
        "ds_read_b64_tr_b16 v[172:173], v100 offset:4096\n\t"
        "ds_read_b64_tr_b16 v[174:175], v100 offset:4352\n\t"
        "s_waitcnt lgkmcnt(8)\n\t"
        "ds_read_b64_tr_b16 v[176:177], v101 offset:0\n\t"
        "ds_read_b64_tr_b16 v[178:179], v101 offset:256\n\t"
        "ds_read_b64_tr_b16 v[180:181], v101 offset:4096\n\t"
        "ds_read_b64_tr_b16 v[182:183], v101 offset:4352\n\t"
        "s_waitcnt lgkmcnt(8)\n\t"
        "ds_read_b64_tr_b16 v[104:105], v102 offset:0\n\t"
        "ds_read_b64_tr_b16 v[106:107], v102 offset:256\n\t"
        "ds_read_b64_tr_b16 v[108:109], v102 offset:4096\n\t"
        "ds_read_b64_tr_b16 v[110:111], v102 offset:4352\n\t"
        "s_lshl_b32 s34, s43, 2\n\t"
        "s_add_u32 s34, s34, 15\n\t"
        "s_and_b32 s34, s34, 15\n\t"
        "s_lshl_b32 s34, s34, 19\n\t"
        "buffer_load_dwordx4 v[64:67], v96, s[16:19], s34 offen nt\n\t"
        "s_add_u32 s34, s34, 0x10000\n\t"
        "buffer_load_dwordx4 v[68:71], v96, s[16:19], s34 offen nt\n\t"
        "s_add_u32 s34, s34, 0x10000\n\t"
        "buffer_load_dwordx4 v[72:75], v96, s[16:19], s34 offen nt\n\t"
        "s_add_u32 s34, s34, 0x10000\n\t"
        "buffer_load_dwordx4 v[76:79], v96, s[16:19], s34 offen nt\n\t"
        "s_add_u32 s34, s34, 0x10000\n\t"
        "buffer_load_dwordx4 v[80:83], v96, s[16:19], s34 offen nt\n\t"
        "s_add_u32 s34, s34, 0x10000\n\t"
        "buffer_load_dwordx4 v[84:87], v96, s[16:19], s34 offen nt\n\t"
        "s_add_u32 s34, s34, 0x10000\n\t"
        "buffer_load_dwordx4 v[88:91], v96, s[16:19], s34 offen nt\n\t"
        "s_add_u32 s34, s34, 0x10000\n\t"
        "buffer_load_dwordx4 v[92:95], v96, s[16:19], s34 offen nt\n\t"
        "s_waitcnt lgkmcnt(0)\n\t"
        "ds_write_b128 v117, v[104:107] offset:16384\n\t"
        "ds_write_b128 v117, v[108:111] offset:17408\n\t"
        "s_waitcnt vmcnt(16)\n\t"
        "ds_write_b128 v97, v[0:3] offset:0\n\t"
        "ds_write_b128 v98, v[4:7] offset:1024\n\t"
        "ds_write_b128 v97, v[8:11] offset:2048\n\t"
        "ds_write_b128 v98, v[12:15] offset:3072\n\t"
        "ds_write_b128 v97, v[16:19] offset:4096\n\t"
        "ds_write_b128 v98, v[20:23] offset:5120\n\t"
        "ds_write_b128 v97, v[24:27] offset:6144\n\t"
        "ds_write_b128 v98, v[28:31] offset:7168\n\t"
        "ds_read_b64_tr_b16 v[184:185], v99 offset:0\n\t"
        "ds_read_b64_tr_b16 v[186:187], v99 offset:256\n\t"
        "ds_read_b64_tr_b16 v[188:189], v99 offset:4096\n\t"
        "ds_read_b64_tr_b16 v[190:191], v99 offset:4352\n\t"
        "s_waitcnt lgkmcnt(8)\n\t"
        "ds_read_b64_tr_b16 v[192:193], v100 offset:0\n\t"
        "ds_read_b64_tr_b16 v[194:195], v100 offset:256\n\t"
        "ds_read_b64_tr_b16 v[196:197], v100 offset:4096\n\t"
        "ds_read_b64_tr_b16 v[198:199], v100 offset:4352\n\t"
        "s_waitcnt lgkmcnt(8)\n\t"
        "ds_read_b64_tr_b16 v[200:201], v101 offset:0\n\t"
        "ds_read_b64_tr_b16 v[202:203], v101 offset:256\n\t"
        "ds_read_b64_tr_b16 v[204:205], v101 offset:4096\n\t"
        "ds_read_b64_tr_b16 v[206:207], v101 offset:4352\n\t"
        "s_waitcnt lgkmcnt(8)\n\t"
        "ds_read_b64_tr_b16 v[104:105], v102 offset:0\n\t"
        "ds_read_b64_tr_b16 v[106:107], v102 offset:256\n\t"
        "ds_read_b64_tr_b16 v[108:109], v102 offset:4096\n\t"
        "ds_read_b64_tr_b16 v[110:111], v102 offset:4352\n\t"
        "s_waitcnt lgkmcnt(0)\n\t"
        "ds_write_b128 v117, v[104:107] offset:18432\n\t"
        "ds_write_b128 v117, v[108:111] offset:19456\n\t"
        "s_waitcnt vmcnt(8)\n\t"
        "ds_write_b128 v97, v[32:35] offset:0\n\t"
        "ds_write_b128 v98, v[36:39] offset:1024\n\t"
        "ds_write_b128 v97, v[40:43] offset:2048\n\t"
        "ds_write_b128 v98, v[44:47] offset:3072\n\t"
        "ds_write_b128 v97, v[48:51] offset:4096\n\t"
        "ds_write_b128 v98, v[52:55] offset:5120\n\t"
        "ds_write_b128 v97, v[56:59] offset:6144\n\t"
        "ds_write_b128 v98, v[60:63] offset:7168\n\t"
        "ds_read_b64_tr_b16 v[208:209], v99 offset:0\n\t"
        "ds_read_b64_tr_b16 v[210:211], v99 offset:256\n\t"
        "ds_read_b64_tr_b16 v[212:213], v99 offset:4096\n\t"
        "ds_read_b64_tr_b16 v[214:215], v99 offset:4352\n\t"
        "s_waitcnt lgkmcnt(8)\n\t"
        "ds_read_b64_tr_b16 v[216:217], v100 offset:0\n\t"
        "ds_read_b64_tr_b16 v[218:219], v100 offset:256\n\t"
        "ds_read_b64_tr_b16 v[220:221], v100 offset:4096\n\t"
        "ds_read_b64_tr_b16 v[222:223], v100 offset:4352\n\t"
        "s_waitcnt lgkmcnt(8)\n\t"
        "ds_read_b64_tr_b16 v[224:225], v101 offset:0\n\t"
        "ds_read_b64_tr_b16 v[226:227], v101 offset:256\n\t"
        "ds_read_b64_tr_b16 v[228:229], v101 offset:4096\n\t"
        "ds_read_b64_tr_b16 v[230:231], v101 offset:4352\n\t"
        "s_waitcnt lgkmcnt(8)\n\t"
        "ds_read_b64_tr_b16 v[104:105], v102 offset:0\n\t"
        "ds_read_b64_tr_b16 v[106:107], v102 offset:256\n\t"
        "ds_read_b64_tr_b16 v[108:109], v102 offset:4096\n\t"
        "ds_read_b64_tr_b16 v[110:111], v102 offset:4352\n\t"
        "s_mov_b32 s35, 33792\n\t"
        "buffer_load_dword v0, off, s[12:15], s35 sc1\n\t"
        "s_waitcnt lgkmcnt(0)\n\t"
        "ds_write_b128 v117, v[104:107] offset:20480\n\t"
        "ds_write_b128 v117, v[108:111] offset:21504\n\t"
        "s_waitcnt vmcnt(0)\n\t"
        "ds_write_b128 v97, v[64:67] offset:0\n\t"
        "ds_write_b128 v98, v[68:71] offset:1024\n\t"
        "ds_write_b128 v97, v[72:75] offset:2048\n\t"
        "ds_write_b128 v98, v[76:79] offset:3072\n\t"
        "ds_write_b128 v97, v[80:83] offset:4096\n\t"
        "ds_write_b128 v98, v[84:87] offset:5120\n\t"
        "ds_write_b128 v97, v[88:91] offset:6144\n\t"
        "ds_write_b128 v98, v[92:95] offset:7168\n\t"
        "ds_read_b64_tr_b16 v[232:233], v99 offset:0\n\t"
        "ds_read_b64_tr_b16 v[234:235], v99 offset:256\n\t"
        "ds_read_b64_tr_b16 v[236:237], v99 offset:4096\n\t"
        "ds_read_b64_tr_b16 v[238:239], v99 offset:4352\n\t"
        "s_waitcnt lgkmcnt(8)\n\t"
        "ds_read_b64_tr_b16 v[240:241], v100 offset:0\n\t"
        "ds_read_b64_tr_b16 v[242:243], v100 offset:256\n\t"
        "ds_read_b64_tr_b16 v[244:245], v100 offset:4096\n\t"
        "ds_read_b64_tr_b16 v[246:247], v100 offset:4352\n\t"
        "s_waitcnt lgkmcnt(8)\n\t"
        "ds_read_b64_tr_b16 v[248:249], v101 offset:0\n\t"
        "ds_read_b64_tr_b16 v[250:251], v101 offset:256\n\t"
        "ds_read_b64_tr_b16 v[252:253], v101 offset:4096\n\t"
        "ds_read_b64_tr_b16 v[254:255], v101 offset:4352\n\t"
        "s_waitcnt lgkmcnt(8)\n\t"
        "ds_read_b64_tr_b16 v[104:105], v102 offset:0\n\t"
        "ds_read_b64_tr_b16 v[106:107], v102 offset:256\n\t"
        "ds_read_b64_tr_b16 v[108:109], v102 offset:4096\n\t"
        "ds_read_b64_tr_b16 v[110:111], v102 offset:4352\n\t"
        "s_mov_b32 s42, 0\n\t"
        "LA_cal%=:\n\t"
        "v_readfirstlane_b32 s34, v0\n\t"
        "s_nop 0\n\t"
        "s_cmp_lg_u32 s34, 0\n\t"
        "s_cbranch_scc1 LA_calok%=\n\t"
        "s_sleep 4\n\t"
        "s_mov_b32 s35, 33792\n\t"
        "buffer_load_dword v0, off, s[12:15], s35 sc1\n\t"
        "s_waitcnt vmcnt(0)\n\t"
        "s_add_u32 s42, s42, 1\n\t"
        "s_cmp_lt_u32 s42, 0x100000\n\t"
        "s_cbranch_scc1 LA_cal%=\n\t"
        "s_movk_i32 s34, 8\n\t"
        "s_branch LA_die%=\n\t"
        "LA_calok%=:\n\t"
        "s_lshl_b32 s35, s44, 10\n\t"
        "v_lshlrev_b32_e32 v1, 2, v122\n\t"
        "v_and_b32_e32 v1, 0x3c0, v1\n\t"
        "buffer_load_dword v1, v1, s[12:15], s35 offen offset:0 sc1\n\t"
        "s_waitcnt lgkmcnt(0)\n\t"
        "ds_write_b128 v126, v[104:107] offset:8192\n\t"
        "ds_write_b128 v126, v[108:111] offset:9216\n\t"
        "s_waitcnt lgkmcnt(0)\n\t"
        "v_mov_b32_e32 v110, v103\n\t"
        "ds_read_b128 v[64:67], v126 offset:0\n\t"
        "ds_read_b128 v[68:71], v126 offset:1024\n\t"
        "ds_read_b128 v[72:75], v126 offset:2048\n\t"
        "ds_read_b128 v[76:79], v126 offset:3072\n\t"
        "ds_read_b128 v[80:83], v126 offset:4096\n\t"
        "ds_read_b128 v[84:87], v126 offset:5120\n\t"
        "ds_read_b128 v[88:91], v126 offset:6144\n\t"
        "ds_read_b128 v[92:95], v126 offset:7168\n\t"
        "ds_read_b128 v[96:99], v126 offset:8192\n\t"
        "ds_read_b128 v[100:103], v126 offset:9216\n\t"
        "s_waitcnt lgkmcnt(0)\n\t"
        "v_mov_b32_e32 v126, 0\n\t"
        "s_barrier\n\t"
        "s_mov_b32 s48, 0\n\t"
        "s_mov_b32 s54, 0\n\t"
        "s_mov_b32 s0, 0x3f803f80\n\t"
        "s_mov_b32 s1, s0\n\t"
        "s_movk_i32 s56, 24576\n\t"
        "s_and_b32 s58, s43, 1\n\t"
        "s_lshl_b32 s58, s58, 12\n\t"
        "s_lshl_b32 s57, s44, 5\n\t"
        "s_add_u32 s57, s57, s43\n\t"
        "s_lshl_b32 s57, s57, 2\n\t"
        "s_add_u32 s57, s57, 458752\n\t"
        "s_waitcnt vmcnt(0)\n\t"
        "v_mov_b32_e32 v111, v1\n\t"
        "s_lshr_b32 s59, s48, 1\n\t"
        "s_cmp_gt_u32 s54, s59\n\t"
        "s_cbranch_scc1 LA_kv0ok%=\n\t"
        "s_mov_b32 s42, 0\n\t"
        "s_waitcnt vmcnt(0)\n\t"
        "s_branch LA_kv0w%=\n\t"
        "LA_kv0%=:\n\t"
        "s_waitcnt vmcnt(0)\n\t"
        "LA_kv0w%=:\n\t"
        "v_min_u32_dpp v111, v111, v111 quad_perm:[1,0,3,2] row_mask:0xf bank_mask:0xf\n\t"
        "s_nop 1\n\t"
        "v_min_u32_dpp v111, v111, v111 quad_perm:[2,3,0,1] row_mask:0xf bank_mask:0xf\n\t"
        "s_nop 1\n\t"
        "v_min_u32_dpp v111, v111, v111 row_ror:4 row_mask:0xf bank_mask:0xf\n\t"
        "s_nop 1\n\t"
        "v_min_u32_dpp v111, v111, v111 row_ror:8 row_mask:0xf bank_mask:0xf\n\t"
        "s_nop 0\n\t"
        "v_readfirstlane_b32 s54, v111\n\t"
        "s_nop 0\n\t"
        "s_cmp_gt_u32 s54, s59\n\t"
        "s_cbranch_scc1 LA_kv0ok%=\n\t"
        "s_sleep 4\n\t"
        "s_lshl_b32 s35, s44, 10\n\t"
        "v_lshlrev_b32_e32 v111, 2, v122\n\t"
        "v_and_b32_e32 v111, 0x3c0, v111\n\t"
        "buffer_load_dword v111, v111, s[12:15], s35 offen offset:0 sc1\n\t"
        "s_add_u32 s42, s42, 1\n\t"
        "s_cmp_lt_u32 s42, 0x100000\n\t"
        "s_cbranch_scc1 LA_kv0%=\n\t"
        "s_movk_i32 s34, 16\n\t"
        "s_branch LA_die%=\n\t"
        "LA_kv0ok%=:\n\t"
        "s_and_b32 s36, s48, 1\n\t"
        "s_mul_i32 s36, s36, 24576\n\t"
        "s_add_u32 s36, s36, s55\n\t"
        "s_lshl_b32 s37, s48, 18\n\t"
        "s_lshl_b32 s34, s43, 11\n\t"
        "s_add_u32 s37, s37, s34\n\t"
        "s_add_u32 m0, s36, 0\n\t"
        "s_add_u32 s34, s37, 29548544\n\t"
        "buffer_load_dwordx4 v122, s[12:15], s34 offen sc1 lds\n\t"
        "s_add_u32 m0, s36, 1024\n\t"
        "s_add_u32 s34, s37, 29549568\n\t"
        "buffer_load_dwordx4 v122, s[12:15], s34 offen sc1 lds\n\t"
        "s_add_u32 m0, s36, 8192\n\t"
        "s_add_u32 s34, s37, 12771328\n\t"
        "buffer_load_dwordx4 v122, s[12:15], s34 offen sc1 lds\n\t"
        "s_add_u32 m0, s36, 9216\n\t"
        "s_add_u32 s34, s37, 12772352\n\t"
        "buffer_load_dwordx4 v122, s[12:15], s34 offen sc1 lds\n\t"
        "s_add_u32 m0, s36, 16384\n\t"
        "s_add_u32 s34, s37, 46325760\n\t"
        "buffer_load_dwordx4 v122, s[12:15], s34 offen sc1 lds\n\t"
        "s_add_u32 m0, s36, 17408\n\t"
        "s_add_u32 s34, s37, 46326784\n\t"
        "buffer_load_dwordx4 v122, s[12:15], s34 offen sc1 lds\n\t"
        "v_readfirstlane_b32 s34, v0\n\t"
        "s_nop 0\n\t"
        "s_and_b32 s34, s34, 8192\n\t"
        "s_add_u32 s8, s8, s34\n\t"
        "s_addc_u32 s9, s9, 0\n\t"
        "s_barrier\n\t"
        "s_cmp_lg_u32 s43, 0\n\t"
        "s_cbranch_scc1 LA_st%=\n\t"
        "v_mov_b32_e32 v2, 1\n\t"
        "s_mov_b32 s35, 34304\n\t"
        "s_mov_b64 exec, 1\n\t"
        "buffer_atomic_add v2, off, s[12:15], s35 sc0\n\t"
        "s_mov_b64 exec, -1\n\t"
        "s_waitcnt vmcnt(0)\n\t"
        "v_readfirstlane_b32 s34, v2\n\t"
        "s_nop 0\n\t"
        "s_cmp_lg_u32 s34, 95\n\t"
        "s_cbranch_scc1 LA_st%=\n\t"
        "s_memrealtime s[32:33]\n\t"
        "v_mbcnt_lo_u32_b32 v3, -1, 0\n\t"
        "v_lshlrev_b32_e32 v3, 7, v3\n\t"
        "s_waitcnt lgkmcnt(0)\n\t"
        "s_add_u32 s34, s32, 50\n\t"
        "s_or_b32 s34, s34, 1\n\t"
        "v_mov_b32_e32 v2, s34\n\t"
        "s_mov_b32 s35, 36864\n\t"
        "s_mov_b64 exec, 0xffff\n\t"
        "buffer_store_dword v2, v3, s[12:15], s35 offen sc1\n\t"
        "s_mov_b64 exec, -1\n\t"
        "LA_st%=:\n\t"
        "s_mov_b32 s24, 0\n\t"
        "s_mov_b32 s26, 0\n\t"
        "s_mov_b32 s27, 0x80000\n\t"
        "s_mov_b32 s28, 0x180000\n\t"
        "s_mov_b32 s31, 0\n\t"
        "s_mov_b32 s47, 0\n\t"
        "s_mov_b32 s38, 0xff00ff00\n\t"
        "s_mov_b32 s39, 0xff00ff00\n\t"
        "s_mov_b32 s40, 0\n\t"
        "s_mov_b32 s41, -1\n\t"
        "ds_read_b128 v[32:35], v117 offset:0\n\t"
        "ds_read_b128 v[36:39], v117 offset:1024\n\t"
        "ds_read_b128 v[40:43], v117 offset:2048\n\t"
        "ds_read_b128 v[44:47], v117 offset:3072\n\t"
        "ds_read_b128 v[48:51], v117 offset:4096\n\t"
        "ds_read_b128 v[52:55], v117 offset:5120\n\t"
        "ds_read_b128 v[56:59], v117 offset:6144\n\t"
        "ds_read_b128 v[60:63], v117 offset:7168\n\t"
        "v_mov_b64_e32 v[0:1], 0\n\t"
        "v_mov_b64_e32 v[2:3], 0\n\t"
        "v_mov_b64_e32 v[4:5], 0\n\t"
        "v_mov_b64_e32 v[6:7], 0\n\t"
        "v_mov_b64_e32 v[8:9], 0\n\t"
        "v_mov_b64_e32 v[10:11], 0\n\t"
        "v_mov_b64_e32 v[12:13], 0\n\t"
        "v_mov_b64_e32 v[14:15], 0\n\t"
        "s_mov_b32 s42, 0\n\t"
        "LA_gw%=:\n\t"
        "buffer_load_dword v16, off, s[12:15], s45 sc1\n\t"
        "s_waitcnt vmcnt(0)\n\t"
        "v_readfirstlane_b32 s25, v16\n\t"
        "s_nop 0\n\t"
        "s_cmp_lg_u32 s25, 0\n\t"
        "s_cbranch_scc1 LA_loop%=\n\t"
        "s_sleep 2\n\t"
        "s_add_u32 s42, s42, 1\n\t"
        "s_cmp_lt_u32 s42, 0x100000\n\t"
        "s_cbranch_scc1 LA_gw%=\n\t"
        "s_movk_i32 s34, 9\n\t"
        "s_branch LA_die%=\n\t"
        ".p2align 8\n\t"
        "LA_loop%=:\n\t"
        "s_mov_b32 s42, 0\n\t"
        "s_sub_u32 s35, s25, 1\n\t"
        "LA_wa%=:\n\t"
        "s_memrealtime s[32:33]\n\t"
        "s_waitcnt lgkmcnt(0)\n\t"
        "s_sub_u32 s34, s35, s32\n\t"
        "s_cmpk_lt_u32 s34, 0x2000\n\t"
        "s_cbranch_scc1 LA_wa%=\n\t"
        "LA_rd%=:\n\t"
        "buffer_load_dwordx4 v[16:19], v112, s[8:11], s26 offen offset:0 sc1\n\t"
        "s_nop 12\n\t"
        "buffer_load_dwordx4 v[20:23], v112, s[8:11], s26 offen offset:1024 sc1\n\t"
        "s_nop 12\n\t"
        "buffer_load_dwordx4 v[24:27], v112, s[8:11], s26 offen offset:2048 sc1\n\t"
        "s_nop 12\n\t"
        "buffer_load_dwordx4 v[28:31], v112, s[8:11], s26 offen offset:3072 sc1\n\t"
        "LA_go%=:\n\t"
        "s_waitcnt vmcnt(3)\n\t"
        "v_smfmac_f32_16x16x64_bf16 v[0:3], v[16:19], a[0:7], v110\n\t"
        "ds_write_b128 v113, v[16:19] offset:0\n\t"
        "v_smfmac_f32_16x16x64_bf16 v[4:7], v[16:19], a[8:15], v110\n\t"
        "v_max3_u32 v111, v16, v17, v18\n\t"
        "v_max_u32_e32 v111, v19, v111\n\t"
        "v_smfmac_f32_16x16x64_bf16 v[8:11], v[16:19], a[16:23], v110\n\t"
        "v_smfmac_f32_16x16x64_bf16 v[12:15], v[16:19], v[32:39], v110\n\t"
        "s_waitcnt vmcnt(2)\n\t"
        "v_smfmac_f32_16x16x64_bf16 v[0:3], v[20:23], a[24:31], v110\n\t"
        "ds_write_b128 v113, v[20:23] offset:1024\n\t"
        "v_smfmac_f32_16x16x64_bf16 v[4:7], v[20:23], a[32:39], v110\n\t"
        "v_max3_u32 v111, v20, v21, v111\n\t"
        "v_max3_u32 v111, v22, v23, v111\n\t"
        "v_smfmac_f32_16x16x64_bf16 v[8:11], v[20:23], a[40:47], v110\n\t"
        "s_waitcnt vmcnt(1)\n\t"
        "ds_write_b128 v113, v[24:27] offset:2048\n\t"
        "v_smfmac_f32_16x16x64_bf16 v[12:15], v[20:23], v[40:47], v110\n\t"
        "v_max3_u32 v111, v24, v25, v111\n\t"
        "v_max3_u32 v111, v26, v27, v111\n\t"
        "v_smfmac_f32_16x16x64_bf16 v[0:3], v[24:27], a[48:55], v110\n\t"
        "v_smfmac_f32_16x16x64_bf16 v[4:7], v[24:27], a[56:63], v110\n\t"
        "s_waitcnt vmcnt(0)\n\t"
        "ds_write_b128 v113, v[28:31] offset:3072\n\t"
        "v_max3_u32 v111, v28, v29, v111\n\t"
        "v_max3_u32 v111, v30, v31, v111\n\t"
        "v_smfmac_f32_16x16x64_bf16 v[8:11], v[24:27], a[64:71], v110\n\t"
        "v_cmp_eq_u32_e32 vcc, -1, v111\n\t"
        "v_smfmac_f32_16x16x64_bf16 v[12:15], v[24:27], v[48:55], v110\n\t"
        "s_waitcnt lgkmcnt(0)\n\t"
        "s_cbranch_vccnz LA_rdfail%=\n\t"
        "s_barrier\n\t"
        "ds_read_b128 v[16:19], v114 offset:0\n\t"
        "ds_read_b128 v[20:23], v114 offset:1024\n\t"
        "ds_read_b128 v[24:27], v114 offset:2048\n\t"
        "v_smfmac_f32_16x16x64_bf16 v[0:3], v[28:31], a[72:79], v110\n\t"
        "v_smfmac_f32_16x16x64_bf16 v[4:7], v[28:31], a[80:87], v110\n\t"
        "v_smfmac_f32_16x16x64_bf16 v[8:11], v[28:31], a[88:95], v110\n\t"
        "v_smfmac_f32_16x16x64_bf16 v[12:15], v[28:31], v[56:63], v110\n\t"
        "ds_read_b128 v[28:31], v114 offset:3072\n\t"
        "s_waitcnt lgkmcnt(3)\n\t"
        "v_smfmac_f32_16x16x64_bf16 v[12:15], v[16:19], v[64:71], v110\n\t"
        "ds_read_b128 v[32:35], v117 offset:8192\n\t"
        "v_smfmac_f32_16x16x64_bf16 v[0:3], v[16:19], a[96:103], v110\n\t"
        "ds_read_b128 v[36:39], v117 offset:9216\n\t"
        "v_smfmac_f32_16x16x64_bf16 v[4:7], v[16:19], a[104:111], v110\n\t"
        "s_waitcnt lgkmcnt(4)\n\t"
        "v_smfmac_f32_16x16x64_bf16 v[8:11], v[16:19], a[112:119], v110\n\t"
        "ds_read_b128 v[16:19], v115 offset:0\n\t"
        "v_smfmac_f32_16x16x64_bf16 v[12:15], v[20:23], v[72:79], v110\n\t"
        "ds_read_b128 v[40:43], v117 offset:10240\n\t"
        "v_smfmac_f32_16x16x64_bf16 v[0:3], v[20:23], a[120:127], v110\n\t"
        "ds_read_b128 v[44:47], v117 offset:11264\n\t"
        "v_smfmac_f32_16x16x64_bf16 v[4:7], v[20:23], a[128:135], v110\n\t"
        "s_waitcnt lgkmcnt(6)\n\t"
        "v_smfmac_f32_16x16x64_bf16 v[8:11], v[20:23], a[136:143], v110\n\t"
        "ds_read_b128 v[20:23], v115 offset:1024\n\t"
        "v_smfmac_f32_16x16x64_bf16 v[12:15], v[24:27], v[80:87], v110\n\t"
        "ds_read_b128 v[48:51], v117 offset:12288\n\t"
        "v_smfmac_f32_16x16x64_bf16 v[0:3], v[24:27], a[144:151], v110\n\t"
        "ds_read_b128 v[52:55], v117 offset:13312\n\t"
        "v_smfmac_f32_16x16x64_bf16 v[4:7], v[24:27], a[152:159], v110\n\t"
        "s_waitcnt lgkmcnt(8)\n\t"
        "v_smfmac_f32_16x16x64_bf16 v[8:11], v[24:27], a[160:167], v110\n\t"
        "ds_read_b128 v[24:27], v115 offset:2048\n\t"
        "v_smfmac_f32_16x16x64_bf16 v[12:15], v[28:31], v[88:95], v110\n\t"
        "ds_read_b128 v[56:59], v117 offset:14336\n\t"
        "v_smfmac_f32_16x16x64_bf16 v[0:3], v[28:31], a[168:175], v110\n\t"
        "ds_read_b128 v[60:63], v117 offset:15360\n\t"
        "v_smfmac_f32_16x16x64_bf16 v[4:7], v[28:31], a[176:183], v110\n\t"
        "s_waitcnt lgkmcnt(8)\n\t"
        "v_smfmac_f32_16x16x64_bf16 v[8:11], v[28:31], a[184:191], v110\n\t"
        "ds_read_b128 v[28:31], v115 offset:3072\n\t"
        "v_smfmac_f32_16x16x64_bf16 v[12:15], v[16:19], v[32:39], v110\n\t"
        "ds_read_b128 v[32:35], v117 offset:16384\n\t"
        "v_smfmac_f32_16x16x64_bf16 v[0:3], v[16:19], a[192:199], v110\n\t"
        "ds_read_b128 v[36:39], v117 offset:17408\n\t"
        "v_smfmac_f32_16x16x64_bf16 v[4:7], v[16:19], a[200:207], v110\n\t"
        "s_waitcnt lgkmcnt(8)\n\t"
        "v_smfmac_f32_16x16x64_bf16 v[8:11], v[16:19], a[208:215], v110\n\t"
        "ds_read_b128 v[16:19], v116 offset:0\n\t"
        "v_smfmac_f32_16x16x64_bf16 v[12:15], v[20:23], v[40:47], v110\n\t"
        "ds_read_b128 v[40:43], v117 offset:18432\n\t"
        "v_smfmac_f32_16x16x64_bf16 v[0:3], v[20:23], a[216:223], v110\n\t"
        "ds_read_b128 v[44:47], v117 offset:19456\n\t"
        "v_smfmac_f32_16x16x64_bf16 v[4:7], v[20:23], a[224:231], v110\n\t"
        "s_waitcnt lgkmcnt(8)\n\t"
        "v_smfmac_f32_16x16x64_bf16 v[8:11], v[20:23], a[232:239], v110\n\t"
        "ds_read_b128 v[20:23], v116 offset:1024\n\t"
        "v_smfmac_f32_16x16x64_bf16 v[12:15], v[24:27], v[48:55], v110\n\t"
        "ds_read_b128 v[48:51], v117 offset:20480\n\t"
        "v_smfmac_f32_16x16x64_bf16 v[0:3], v[24:27], a[240:247], v110\n\t"
        "ds_read_b128 v[52:55], v117 offset:21504\n\t"
        "v_smfmac_f32_16x16x64_bf16 v[4:7], v[24:27], a[248:255], v110\n\t"
        "s_waitcnt lgkmcnt(8)\n\t"
        "v_smfmac_f32_16x16x64_bf16 v[8:11], v[24:27], v[128:135], v110\n\t"
        "ds_read_b128 v[24:27], v116 offset:2048\n\t"
        "v_smfmac_f32_16x16x64_bf16 v[12:15], v[28:31], v[56:63], v110\n\t"
        "ds_read_b128 v[56:59], v124 offset:3072\n\t"
        "v_smfmac_f32_16x16x64_bf16 v[0:3], v[28:31], v[136:143], v110\n\t"
        "ds_read_b128 v[60:63], v124 offset:7168\n\t"
        "v_smfmac_f32_16x16x64_bf16 v[4:7], v[28:31], v[144:151], v110\n\t"
        "s_waitcnt lgkmcnt(8)\n\t"
        "v_smfmac_f32_16x16x64_bf16 v[8:11], v[28:31], v[152:159], v110\n\t"
        "ds_read_b128 v[28:31], v116 offset:3072\n\t"
        "v_smfmac_f32_16x16x64_bf16 v[12:15], v[16:19], v[32:39], v110\n\t"
        "ds_read_b128 v[32:35], v124 offset:0\n\t"
        "v_smfmac_f32_16x16x64_bf16 v[0:3], v[16:19], v[160:167], v110\n\t"
        "ds_read_b128 v[36:39], v124 offset:4096\n\t"
        "v_smfmac_f32_16x16x64_bf16 v[4:7], v[16:19], v[168:175], v110\n\t"
        "s_waitcnt lgkmcnt(8)\n\t"
        "v_smfmac_f32_16x16x64_bf16 v[8:11], v[16:19], v[176:183], v110\n\t"
        "v_smfmac_f32_16x16x64_bf16 v[12:15], v[20:23], v[40:47], v110\n\t"
        "ds_read_b128 v[40:43], v124 offset:1024\n\t"
        "v_smfmac_f32_16x16x64_bf16 v[8:11], v[20:23], v[200:207], v110\n\t"
        "ds_read_b128 v[44:47], v124 offset:5120\n\t"
        "s_waitcnt lgkmcnt(7)\n\t"
        "v_smfmac_f32_16x16x64_bf16 v[12:15], v[24:27], v[48:55], v110\n\t"
        "ds_read_b128 v[48:51], v124 offset:2048\n\t"
        "v_smfmac_f32_16x16x64_bf16 v[8:11], v[24:27], v[224:231], v110\n\t"
        "ds_read_b128 v[52:55], v124 offset:6144\n\t"
        "s_waitcnt lgkmcnt(6)\n\t"
        "v_smfmac_f32_16x16x64_bf16 v[12:15], v[28:31], v[96:103], v110\n\t"
        "v_smfmac_f32_16x16x64_bf16 v[8:11], v[28:31], v[248:255], v110\n\t"
        "v_smfmac_f32_16x16x64_bf16 v[0:3], v[20:23], v[184:191], v110\n\t"
        "s_lshl_b32 s34, s43, 11\n\t"
        "v_add_u32_e32 v108, s34, v124\n\t"
        "v_smfmac_f32_16x16x64_bf16 v[0:3], v[24:27], v[208:215], v110\n\t"
        "v_add_u32_e32 v109, s58, v124\n\t"
        "v_smfmac_f32_16x16x64_bf16 v[4:7], v[20:23], v[192:199], v110\n\t"
        "v_smfmac_f32_16x16x64_bf16 v[0:3], v[28:31], v[232:239], v110\n\t"
        "v_smfmac_f32_16x16x64_bf16 v[4:7], v[24:27], v[216:223], v110\n\t"
        "buffer_load_dwordx4 v[104:107], v120, s[8:11], s26 offen sc1\n\t"
        "v_smfmac_f32_16x16x64_bf16 v[4:7], v[28:31], v[240:247], v110\n\t"
        "v_add_f32_e32 v12, v12, v13\n\t"
        "v_add_f32_e32 v14, v14, v15\n\t"
        "v_cvt_pk_bf16_f32 v12, v12, v14\n\t"
        "v_add_f32_e32 v8, v8, v9\n\t"
        "v_add_f32_e32 v10, v10, v11\n\t"
        "v_cvt_pk_bf16_f32 v8, v8, v10\n\t"
        "ds_write2_b32 v118, v8, v12 offset0:64 offset1:96\n\t"
        "v_add_f32_e32 v0, v0, v1\n\t"
        "v_add_f32_e32 v2, v2, v3\n\t"
        "v_cvt_pk_bf16_f32 v0, v0, v2\n\t"
        "v_add_f32_e32 v4, v4, v5\n\t"
        "v_add_f32_e32 v6, v6, v7\n\t"
        "v_cvt_pk_bf16_f32 v4, v4, v6\n\t"
        "ds_write2_b32 v118, v0, v4 offset0:0 offset1:32\n\t"
        "s_cmp_eq_u32 s24, 63\n\t"
        "s_waitcnt lgkmcnt(0)\n\t"
        "s_barrier\n\t"
        "s_cbranch_scc1 LA_skip%=\n\t"
        "ds_read_b64_tr_b16 v[16:17], v119 offset:0\n\t"
        "ds_read_b64_tr_b16 v[18:19], v119 offset:32\n\t"
        "ds_read_b64_tr_b16 v[20:21], v119 offset:256\n\t"
        "ds_read_b64_tr_b16 v[22:23], v119 offset:288\n\t"
        "ds_read_b64_tr_b16 v[24:25], v119 offset:512\n\t"
        "ds_read_b64_tr_b16 v[26:27], v119 offset:544\n\t"
        "ds_read_b64_tr_b16 v[28:29], v119 offset:768\n\t"
        "ds_read_b64_tr_b16 v[30:31], v119 offset:800\n\t"
        "buffer_store_dword v126, v127, s[12:15], 0 offen sc1\n\t"
        "s_waitcnt lgkmcnt(6)\n\t"
        "v_mfma_f32_16x16x32_bf16 v[0:3], v[32:35], v[16:19], 0\n\t"
        "ds_read_b64_tr_b16 v[32:33], v119 offset:2112\n\t"
        "ds_read_b64_tr_b16 v[34:35], v119 offset:2144\n\t"
        "v_mfma_f32_16x16x32_bf16 v[4:7], v[36:39], v[16:19], 0\n\t"
        "ds_read_b64_tr_b16 v[36:37], v119 offset:2368\n\t"
        "ds_read_b64_tr_b16 v[38:39], v119 offset:2400\n\t"
        "s_waitcnt lgkmcnt(8)\n\t"
        "v_mfma_f32_16x16x32_bf16 v[0:3], v[40:43], v[20:23], v[0:3]\n\t"
        "ds_read_b64_tr_b16 v[40:41], v119 offset:2624\n\t"
        "ds_read_b64_tr_b16 v[42:43], v119 offset:2656\n\t"
        "v_mfma_f32_16x16x32_bf16 v[4:7], v[44:47], v[20:23], v[4:7]\n\t"
        "ds_read_b64_tr_b16 v[44:45], v119 offset:2880\n\t"
        "ds_read_b64_tr_b16 v[46:47], v119 offset:2912\n\t"
        "s_waitcnt lgkmcnt(10)\n\t"
        "v_mfma_f32_16x16x32_bf16 v[0:3], v[48:51], v[24:27], v[0:3]\n\t"
        "ds_read_b128 v[48:51], v108 offset:16384\n\t"
        "v_mfma_f32_16x16x32_bf16 v[4:7], v[52:55], v[24:27], v[4:7]\n\t"
        "ds_read_b128 v[52:55], v108 offset:17408\n\t"
        "s_waitcnt lgkmcnt(10)\n\t"
        "v_mfma_f32_16x16x32_bf16 v[0:3], v[56:59], v[28:31], v[0:3]\n\t"
        "v_mfma_f32_16x16x32_bf16 v[4:7], v[60:63], v[28:31], v[4:7]\n\t"
        "s_waitcnt lgkmcnt(2)\n\t"
        "v_mfma_f32_16x16x32_bf16 v[8:11], v[32:35], v[16:19], 0\n\t"
        "s_nop 1\n\t"
        "v_mfma_f32_16x16x32_bf16 v[8:11], v[36:39], v[20:23], v[8:11]\n\t"
        "v_mov_b64_e32 v[16:17], s[0:1]\n\t"
        "v_mov_b64_e32 v[18:19], s[0:1]\n\t"
        "v_mfma_f32_16x16x32_bf16 v[8:11], v[40:43], v[24:27], v[8:11]\n\t"
        "v_max3_f32 v59, v0, v1, v2\n\t"
        "v_max3_f32 v59, v59, v3, v4\n\t"
        "v_mfma_f32_16x16x32_bf16 v[8:11], v[44:47], v[28:31], v[8:11]\n\t"
        "v_max3_f32 v59, v59, v5, v6\n\t"
        "v_max_f32_e32 v60, v59, v7\n\t"
        "v_max_f32_e32 v59, v59, v7\n\t"
        "v_cndmask_b32_e64 v0, v0, v4, s[38:39]\n\t"
        "v_cndmask_b32_e64 v1, v1, v5, s[38:39]\n\t"
        "v_permlane32_swap_b32_e32 v59, v60\n\t"
        "v_cndmask_b32_e64 v2, v2, v6, s[38:39]\n\t"
        "v_cndmask_b32_e64 v3, v3, v7, s[38:39]\n\t"
        "v_max3_f32 v59, v59, v60, v8\n\t"
        "v_max3_f32 v59, v59, v9, v10\n\t"
        "v_max_f32_e32 v60, v59, v11\n\t"
        "v_max_f32_e32 v59, v59, v11\n\t"
        "v_cndmask_b32_e64 v8, v8, v9, s[38:39]\n\t"
        "v_cndmask_b32_e64 v10, v10, v11, s[38:39]\n\t"
        "v_permlane16_swap_b32_e32 v59, v60\n\t"
        "v_max_f32_e32 v59, v59, v60\n\t"
        "v_mul_f32_e32 v59, 0xbe02940c, v59\n\t"
        "v_cndmask_b32_e64 v58, v8, v10, s[40:41]\n\t"
        "v_fmamk_f32 v0, v0, 0x3e02940c, v59\n\t"
        "v_exp_f32_e32 v0, v0\n\t"
        "v_fmamk_f32 v1, v1, 0x3e02940c, v59\n\t"
        "v_exp_f32_e32 v1, v1\n\t"
        "v_fmamk_f32 v2, v2, 0x3e02940c, v59\n\t"
        "v_exp_f32_e32 v2, v2\n\t"
        "v_fmamk_f32 v3, v3, 0x3e02940c, v59\n\t"
        "v_exp_f32_e32 v3, v3\n\t"
        "v_fmamk_f32 v58, v58, 0x3e02940c, v59\n\t"
        "v_cvt_pk_bf16_f32 v12, v0, v1\n\t"
        "v_cvt_pk_bf16_f32 v13, v2, v3\n\t"
        "v_exp_f32_e32 v58, v58\n\t"
        "s_waitcnt lgkmcnt(0)\n\t"
        "v_mov_b32_dpp v14, v12 row_ror:8 row_mask:0xf bank_mask:0xf\n\t"
        "v_mov_b32_dpp v15, v13 row_ror:8 row_mask:0xf bank_mask:0xf\n\t"
        "v_mov_b32_dpp v59, v58 row_ror:8 row_mask:0xf bank_mask:0xf\n\t"
        "v_cvt_pk_bf16_f32 v59, v58, v59\n\t"
        "v_mfma_f32_16x16x32_bf16 v[20:23], v[16:19], v[12:15], 0\n\t"
        "v_cndmask_b32_e64 v56, v59, 0, s[40:41]\n\t"
        "v_cndmask_b32_e64 v57, 0, v59, s[40:41]\n\t"
        "v_mfma_f32_16x16x32_bf16 v[4:7], v[48:51], v[12:15], 0\n\t"
        "s_waitcnt vmcnt(1)\n\t"
        "v_max3_u32 v111, v104, v105, v106\n\t"
        "v_mfma_f32_16x16x32_bf16 v[8:11], v[52:55], v[12:15], 0\n\t"
        "v_max_u32_e32 v111, v111, v107\n\t"
        "v_mfma_f32_16x16x16_bf16 v[20:23], v[16:17], v[56:57], v[20:23]\n\t"
        "v_cmp_eq_u32_e32 vcc, -1, v111\n\t"
        "v_mfma_f32_16x16x16_bf16 v[4:7], v[104:105], v[56:57], v[4:7]\n\t"
        "s_nop 1\n\t"
        "v_mfma_f32_16x16x16_bf16 v[8:11], v[106:107], v[56:57], v[8:11]\n\t"
        "s_cbranch_vccnz LA_vmfail%=\n\t"
        "LA_vmok%=:\n\t"
        "s_nop 1\n\t"
        "v_rcp_f32_e32 v59, v20\n\t"
        "s_nop 0\n\t"
        "v_pk_mul_f32 v[4:5], v[4:5], v[58:59] op_sel:[0,1] op_sel_hi:[1,1]\n\t"
        "v_cvt_pk_bf16_f32 v0, v4, v5\n\t"
        "v_pk_mul_f32 v[6:7], v[6:7], v[58:59] op_sel:[0,1] op_sel_hi:[1,1]\n\t"
        "v_cvt_pk_bf16_f32 v1, v6, v7\n\t"
        "v_pk_mul_f32 v[8:9], v[8:9], v[58:59] op_sel:[0,1] op_sel_hi:[1,1]\n\t"
        "v_cvt_pk_bf16_f32 v2, v8, v9\n\t"
        "v_pk_mul_f32 v[10:11], v[10:11], v[58:59] op_sel:[0,1] op_sel_hi:[1,1]\n\t"
        "v_cvt_pk_bf16_f32 v3, v10, v11\n\t"
        "buffer_store_dwordx4 v[0:3], v121, s[8:11], s26 offen sc1\n\t"
        "LA_x%=:\n\t"
        "s_memrealtime s[52:53]\n\t"
        "ds_read_b128 v[16:19], v109 offset:8192\n\t"
        "ds_read_b128 v[20:23], v109 offset:9216\n\t"
        "ds_read_b128 v[24:27], v109 offset:10240\n\t"
        "ds_read_b128 v[28:31], v109 offset:11264\n\t"
        "ds_read_b128 v[56:59], v124 offset:0\n\t"
        "ds_read_b128 v[60:63], v124 offset:4096\n\t"
        "ds_read_b128 v[12:15], v124 offset:1024\n\t"
        "v_xor_b32_e32 v126, 0x800, v108\n\t"
        "s_add_u32 s48, s24, 1\n\t"
        "s_min_u32 s48, s48, 63\n\t"
        "buffer_load_dwordx2 v[108:109], v125, s[8:11], s26 offen sc1\n\t"
        "s_lshl_b32 s35, s44, 10\n\t"
        "s_lshr_b32 s34, s48, 1\n\t"
        "s_add_u32 s34, s34, 1\n\t"
        "s_min_u32 s34, s34, 31\n\t"
        "s_cmp_gt_u32 s54, s34\n\t"
        "s_cselect_b32 s35, 0xf0000000, s35\n\t"
        "v_lshlrev_b32_e32 v111, 2, v122\n\t"
        "v_and_b32_e32 v111, 0x3c0, v111\n\t"
        "buffer_load_dword v111, v111, s[12:15], s35 offen offset:0 sc1\n\t"
        "s_waitcnt lgkmcnt(6)\n\t"
        "v_mfma_f32_16x16x32_bf16 v[8:11], v[32:35], v[16:19], 0\n\t"
        "ds_read_b128 v[32:35], v124 offset:5120\n\t"
        "s_waitcnt lgkmcnt(6)\n\t"
        "v_mfma_f32_16x16x32_bf16 v[8:11], v[36:39], v[20:23], v[8:11]\n\t"
        "ds_read_b128 v[36:39], v124 offset:2048\n\t"
        "s_waitcnt lgkmcnt(6)\n\t"
        "v_mfma_f32_16x16x32_bf16 v[8:11], v[40:43], v[24:27], v[8:11]\n\t"
        "ds_read_b128 v[40:43], v124 offset:6144\n\t"
        "s_waitcnt lgkmcnt(6)\n\t"
        "v_mfma_f32_16x16x32_bf16 v[8:11], v[44:47], v[28:31], v[8:11]\n\t"
        "ds_read_b128 v[44:47], v124 offset:3072\n\t"
        "s_add_u32 s35, s24, 1\n\t"
        "s_waitcnt lgkmcnt(6)\n\t"
        "v_mfma_f32_16x16x32_bf16 v[0:3], v[56:59], v[16:19], 0\n\t"
        "ds_read_b128 v[56:59], v124 offset:7168\n\t"
        "s_waitcnt lgkmcnt(6)\n\t"
        "v_mfma_f32_16x16x32_bf16 v[4:7], v[60:63], v[16:19], 0\n\t"
        "s_and_b32 s36, s35, 1\n\t"
        "s_mul_i32 s36, s36, 24576\n\t"
        "s_waitcnt lgkmcnt(5)\n\t"
        "v_mfma_f32_16x16x32_bf16 v[0:3], v[12:15], v[20:23], v[0:3]\n\t"
        "s_add_u32 s36, s36, s55\n\t"
        "s_lshl_b32 s37, s48, 18\n\t"
        "s_waitcnt lgkmcnt(4)\n\t"
        "v_mfma_f32_16x16x32_bf16 v[4:7], v[32:35], v[20:23], v[4:7]\n\t"
        "v_xor_b32_e32 v32, 0x200, v120\n\t"
        "buffer_load_dwordx4 v[12:15], v32, s[8:11], s26 offen sc1\n\t"
        "s_waitcnt lgkmcnt(3)\n\t"
        "v_mfma_f32_16x16x32_bf16 v[0:3], v[36:39], v[24:27], v[0:3]\n\t"
        "s_lshl_b32 s34, s43, 11\n\t"
        "s_add_u32 s37, s37, s34\n\t"
        "s_waitcnt lgkmcnt(2)\n\t"
        "v_mfma_f32_16x16x32_bf16 v[4:7], v[40:43], v[24:27], v[4:7]\n\t"
        "v_cvt_pk_bf16_f32 v8, 0, v8\n\t"
        "v_cvt_pk_bf16_f32 v9, 0, v9\n\t"
        "v_cvt_pk_bf16_f32 v10, 0, v10\n\t"
        "v_cvt_pk_bf16_f32 v11, 0, v11\n\t"
        "s_waitcnt lgkmcnt(1)\n\t"
        "v_mfma_f32_16x16x32_bf16 v[0:3], v[44:47], v[28:31], v[0:3]\n\t"
        "v_mul_f32_e32 v8, 0x3db504f3, v8\n\t"
        "v_mul_f32_e32 v9, 0x3db504f3, v9\n\t"
        "v_mul_f32_e32 v10, 0x3db504f3, v10\n\t"
        "v_mul_f32_e32 v11, 0x3db504f3, v11\n\t"
        "s_waitcnt lgkmcnt(0)\n\t"
        "v_mfma_f32_16x16x32_bf16 v[4:7], v[56:59], v[28:31], v[4:7]\n\t"
        "v_cvt_pk_bf16_f32 v8, 0, v8\n\t"
        "v_cvt_pk_bf16_f32 v9, 0, v9\n\t"
        "v_cvt_pk_bf16_f32 v10, 0, v10\n\t"
        "v_cvt_pk_bf16_f32 v11, 0, v11\n\t"
        "ds_read_b128 v[56:59], v126 offset:16384\n\t"
        "ds_read_b128 v[60:63], v126 offset:17408\n\t"
        "v_mov_b64_e32 v[36:37], -1\n\t"
        "v_mov_b64_e32 v[38:39], -1\n\t"
        "v_mov_b32_e32 v40, 0xce6e6b28\n\t"
        "v_mov_b32_e32 v46, 0x3db504f3\n\t"
        "v_mov_b32_e32 v47, 0x3fb8aa3b\n\t"
        "v_cvt_pk_bf16_f32 v0, 0, v0\n\t"
        "v_cvt_pk_bf16_f32 v1, 0, v1\n\t"
        "v_cvt_pk_bf16_f32 v2, 0, v2\n\t"
        "v_cvt_pk_bf16_f32 v3, 0, v3\n\t"
        "v_cvt_pk_bf16_f32 v4, 0, v4\n\t"
        "v_cvt_pk_bf16_f32 v5, 0, v5\n\t"
        "v_cvt_pk_bf16_f32 v6, 0, v6\n\t"
        "v_cvt_pk_bf16_f32 v7, 0, v7\n\t"
        "v_pk_mul_f32 v[0:1], v[0:1], v[46:47] op_sel_hi:[1,0]\n\t"
        "v_pk_mul_f32 v[2:3], v[2:3], v[46:47] op_sel_hi:[1,0]\n\t"
        "v_pk_mul_f32 v[4:5], v[4:5], v[46:47] op_sel_hi:[1,0]\n\t"
        "v_pk_mul_f32 v[6:7], v[6:7], v[46:47] op_sel_hi:[1,0]\n\t"
        "s_lshr_b32 s59, s48, 1\n\t"
        "s_cmp_gt_u32 s54, s59\n\t"
        "s_cbranch_scc1 LA_kvok%=\n\t"
        "s_mov_b32 s42, 0\n\t"
        "s_waitcnt vmcnt(1)\n\t"
        "s_branch LA_kvw%=\n\t"
        "LA_kv%=:\n\t"
        "s_waitcnt vmcnt(0)\n\t"
        "LA_kvw%=:\n\t"
        "v_min_u32_dpp v111, v111, v111 quad_perm:[1,0,3,2] row_mask:0xf bank_mask:0xf\n\t"
        "s_nop 1\n\t"
        "v_min_u32_dpp v111, v111, v111 quad_perm:[2,3,0,1] row_mask:0xf bank_mask:0xf\n\t"
        "s_nop 1\n\t"
        "v_min_u32_dpp v111, v111, v111 row_ror:4 row_mask:0xf bank_mask:0xf\n\t"
        "s_nop 1\n\t"
        "v_min_u32_dpp v111, v111, v111 row_ror:8 row_mask:0xf bank_mask:0xf\n\t"
        "s_nop 0\n\t"
        "v_readfirstlane_b32 s54, v111\n\t"
        "s_nop 0\n\t"
        "s_cmp_gt_u32 s54, s59\n\t"
        "s_cbranch_scc1 LA_kvok%=\n\t"
        "s_sleep 4\n\t"
        "s_lshl_b32 s35, s44, 10\n\t"
        "v_lshlrev_b32_e32 v111, 2, v122\n\t"
        "v_and_b32_e32 v111, 0x3c0, v111\n\t"
        "buffer_load_dword v111, v111, s[12:15], s35 offen offset:0 sc1\n\t"
        "s_add_u32 s42, s42, 1\n\t"
        "s_cmp_lt_u32 s42, 0x100000\n\t"
        "s_cbranch_scc1 LA_kv%=\n\t"
        "s_movk_i32 s34, 16\n\t"
        "s_branch LA_die%=\n\t"
        "LA_kvok%=:\n\t"
        "s_add_u32 m0, s36, 0\n\t"
        "s_add_u32 s34, s37, 29548544\n\t"
        "buffer_load_dwordx4 v122, s[12:15], s34 offen sc1 lds\n\t"
        "v_cvt_pk_bf16_f32 v0, 0, v0\n\t"
        "v_cvt_pk_bf16_f32 v1, 0, v1\n\t"
        "v_cvt_pk_bf16_f32 v2, 0, v2\n\t"
        "v_cvt_pk_bf16_f32 v3, 0, v3\n\t"
        "v_cvt_pk_bf16_f32 v4, 0, v4\n\t"
        "v_cvt_pk_bf16_f32 v5, 0, v5\n\t"
        "v_cvt_pk_bf16_f32 v6, 0, v6\n\t"
        "v_cvt_pk_bf16_f32 v7, 0, v7\n\t"
        "s_add_u32 m0, s36, 1024\n\t"
        "s_add_u32 s34, s37, 29549568\n\t"
        "buffer_load_dwordx4 v122, s[12:15], s34 offen sc1 lds\n\t"
        "v_cndmask_b32_e64 v0, v40, v0, s[60:61]\n\t"
        "v_cndmask_b32_e64 v1, v40, v1, s[62:63]\n\t"
        "v_cndmask_b32_e64 v2, v40, v2, s[64:65]\n\t"
        "v_cndmask_b32_e64 v3, v40, v3, s[66:67]\n\t"
        "v_cndmask_b32_e64 v4, v40, v4, s[68:69]\n\t"
        "v_cndmask_b32_e64 v5, v40, v5, s[70:71]\n\t"
        "v_cndmask_b32_e64 v6, v40, v6, s[72:73]\n\t"
        "v_cndmask_b32_e64 v7, v40, v7, s[74:75]\n\t"
        "s_add_u32 m0, s36, 8192\n\t"
        "s_add_u32 s34, s37, 12771328\n\t"
        "buffer_load_dwordx4 v122, s[12:15], s34 offen sc1 lds\n\t"
        "v_max3_f32 v42, v0, v1, v2\n\t"
        "v_max3_f32 v42, v42, v3, v4\n\t"
        "v_max3_f32 v42, v42, v5, v6\n\t"
        "v_max3_f32 v42, v42, v7, v8\n\t"
        "v_max3_f32 v42, v42, v9, v10\n\t"
        "v_mov_b32_e32 v43, v11\n\t"
        "v_max_f32_e32 v44, v42, v43\n\t"
        "v_max_f32_e32 v42, v42, v43\n\t"
        "s_pack_ll_b32_b16 s34, s52, s24\n\t"
        "v_mov_b32_e32 v40, s34\n\t"
        "s_add_u32 s35, s26, s57\n\t"
        "v_permlane16_swap_b32_e32 v42, v44\n\t"
        "v_max_f32_e32 v45, v42, v44\n\t"
        "v_max_f32_e32 v42, v42, v44\n\t"
        "s_mov_b64 exec, 1\n\t"
        "buffer_store_dword v40, off, s[8:11], s35 sc1\n\t"
        "s_mov_b64 exec, -1\n\t"
        "v_permlane32_swap_b32_e32 v42, v45\n\t"
        "v_max_f32_e32 v42, v42, v45\n\t"
        "v_mul_f32_e32 v42, 0xbfb8aa3b, v42\n\t"
        "v_pk_fma_f32 v[0:1], v[0:1], v[46:47], v[42:43] op_sel:[0,1,0] op_sel_hi:[1,1,0]\n\t"
        "s_add_u32 m0, s36, 9216\n\t"
        "s_add_u32 s34, s37, 12772352\n\t"
        "buffer_load_dwordx4 v122, s[12:15], s34 offen sc1 lds\n\t"
        "v_exp_f32_e32 v0, v0\n\t"
        "v_exp_f32_e32 v1, v1\n\t"
        "v_pk_fma_f32 v[2:3], v[2:3], v[46:47], v[42:43] op_sel:[0,1,0] op_sel_hi:[1,1,0]\n\t"
        "v_exp_f32_e32 v2, v2\n\t"
        "v_exp_f32_e32 v3, v3\n\t"
        "v_pk_fma_f32 v[4:5], v[4:5], v[46:47], v[42:43] op_sel:[0,1,0] op_sel_hi:[1,1,0]\n\t"
        "v_exp_f32_e32 v4, v4\n\t"
        "s_add_u32 m0, s36, 16384\n\t"
        "s_add_u32 s34, s37, 46325760\n\t"
        "buffer_load_dwordx4 v122, s[12:15], s34 offen sc1 lds\n\t"
        "v_exp_f32_e32 v5, v5\n\t"
        "v_pk_fma_f32 v[6:7], v[6:7], v[46:47], v[42:43] op_sel:[0,1,0] op_sel_hi:[1,1,0]\n\t"
        "v_exp_f32_e32 v6, v6\n\t"
        "v_exp_f32_e32 v7, v7\n\t"
        "v_pk_fma_f32 v[8:9], v[8:9], v[46:47], v[42:43] op_sel:[0,1,0] op_sel_hi:[1,1,0]\n\t"
        "v_exp_f32_e32 v8, v8\n\t"
        "v_exp_f32_e32 v9, v9\n\t"
        "s_add_u32 m0, s36, 17408\n\t"
        "s_add_u32 s34, s37, 46326784\n\t"
        "buffer_load_dwordx4 v122, s[12:15], s34 offen sc1 lds\n\t"
        "v_pk_fma_f32 v[10:11], v[10:11], v[46:47], v[42:43] op_sel:[0,1,0] op_sel_hi:[1,1,0]\n\t"
        "v_exp_f32_e32 v10, v10\n\t"
        "v_exp_f32_e32 v11, v11\n\t"
        "v_pk_add_f32 v[42:43], v[0:1], v[2:3]\n\t"
        "v_cndmask_b32_e64 v8, v8, 0, s[40:41]\n\t"
        "v_cndmask_b32_e64 v9, v9, 0, s[40:41]\n\t"
        "v_cndmask_b32_e64 v10, v10, 0, s[40:41]\n\t"
        "buffer_store_dwordx4 v[36:39], v121, s[8:11], s28 offen sc1\n\t"
        "v_cndmask_b32_e64 v11, v11, 0, s[40:41]\n\t"
        "v_pk_add_f32 v[42:43], v[42:43], v[4:5]\n\t"
        "v_pk_add_f32 v[42:43], v[42:43], v[6:7]\n\t"
        "v_pk_add_f32 v[42:43], v[42:43], v[8:9]\n\t"
        "v_pk_add_f32 v[42:43], v[42:43], v[10:11]\n\t"
        "v_add_f32_e32 v44, v42, v43\n\t"
        "v_add_f32_e32 v42, v42, v43\n\t"
        "s_lshl_b32 s34, s24, 18\n\t"
        "s_add_u32 s34, s34, 63102976\n\t"
        "v_permlane16_swap_b32_e32 v42, v44\n\t"
        "v_add_f32_e32 v45, v42, v44\n\t"
        "v_add_f32_e32 v42, v42, v44\n\t"
        "s_add_u32 s35, s24, 1\n\t"
        "v_mov_b32_e32 v126, s35\n\t"
        "v_permlane32_swap_b32_e32 v42, v45\n\t"
        "v_add_f32_e32 v42, v42, v45\n\t"
        "v_rcp_f32_e32 v42, v42\n\t"
        "s_waitcnt vmcnt(8) lgkmcnt(0)\n\t"
        "v_max3_u32 v45, v12, v13, v14\n\t"
        "v_max3_u32 v45, v45, v15, v104\n\t"
        "v_max3_u32 v45, v45, v105, v106\n\t"
        "v_max_u32_e32 v45, v45, v107\n\t"
        "v_cmp_eq_u32_e32 vcc, -1, v45\n\t"
        "s_nop 0\n\t"
        "s_cbranch_vccnz LA_v2fail%=\n\t"
        "LA_v2ok%=:\n\t"
        "v_and_b32_e32 v45, 0xff, v109\n\t"
        "v_cmp_ne_u32_e32 vcc, s24, v45\n\t"
        "s_nop 0\n\t"
        "s_cbranch_vccnz LA_stre%=\n\t"
        "LA_stok%=:\n\t"
        "v_pk_mul_f32 v[0:1], v[0:1], v[42:43] op_sel_hi:[1,0]\n\t"
        "v_subrev_u32_e32 v108, s25, v108\n\t"
        "v_min_u32_dpp v111, v111, v111 quad_perm:[1,0,3,2] row_mask:0xf bank_mask:0xf\n\t"
        "v_cvt_pk_bf16_f32 v32, v0, v1\n\t"
        "v_pk_mul_f32 v[2:3], v[2:3], v[42:43] op_sel_hi:[1,0]\n\t"
        "v_cvt_pk_bf16_f32 v33, v2, v3\n\t"
        "v_max_i32_dpp v108, v108, v108 quad_perm:[1,0,3,2] row_mask:0xf bank_mask:0xf\n\t"
        "v_min_u32_dpp v111, v111, v111 quad_perm:[2,3,0,1] row_mask:0xf bank_mask:0xf\n\t"
        "v_pk_mul_f32 v[4:5], v[4:5], v[42:43] op_sel_hi:[1,0]\n\t"
        "v_cvt_pk_bf16_f32 v34, v4, v5\n\t"
        "v_pk_mul_f32 v[6:7], v[6:7], v[42:43] op_sel_hi:[1,0]\n\t"
        "v_max_i32_dpp v108, v108, v108 quad_perm:[2,3,0,1] row_mask:0xf bank_mask:0xf\n\t"
        "v_min_u32_dpp v111, v111, v111 row_ror:4 row_mask:0xf bank_mask:0xf\n\t"
        "v_cvt_pk_bf16_f32 v35, v6, v7\n\t"
        "v_pk_mul_f32 v[8:9], v[8:9], v[42:43] op_sel_hi:[1,0]\n\t"
        "v_cvt_pk_bf16_f32 v36, v8, v9\n\t"
        "v_max_i32_dpp v108, v108, v108 row_ror:4 row_mask:0xf bank_mask:0xf\n\t"
        "v_min_u32_dpp v111, v111, v111 row_ror:8 row_mask:0xf bank_mask:0xf\n\t"
        "v_pk_mul_f32 v[10:11], v[10:11], v[42:43] op_sel_hi:[1,0]\n\t"
        "v_cvt_pk_bf16_f32 v37, v10, v11\n\t"
        "v_mfma_f32_16x16x32_bf16 v[16:19], v[48:51], v[32:35], 0\n\t"
        "v_max_i32_dpp v108, v108, v108 row_ror:8 row_mask:0xf bank_mask:0xf\n\t"
        "v_mfma_f32_16x16x32_bf16 v[20:23], v[52:55], v[32:35], 0\n\t"
        "v_mfma_f32_16x16x32_bf16 v[24:27], v[56:59], v[32:35], 0\n\t"
        "v_mfma_f32_16x16x32_bf16 v[28:31], v[60:63], v[32:35], 0\n\t"
        "v_max_i32_dpp v108, v108, v108 row_bcast:15 row_mask:0xa bank_mask:0xf\n\t"
        "v_xor_b32_e32 v45, 0x400, v123\n\t"
        "v_add_u32_e32 v124, s56, v124\n\t"
        "v_mfma_f32_16x16x16_bf16 v[16:19], v[104:105], v[36:37], v[16:19]\n\t"
        "v_max_i32_dpp v108, v108, v108 row_bcast:31 row_mask:0xc bank_mask:0xf\n\t"
        "v_mfma_f32_16x16x16_bf16 v[20:23], v[106:107], v[36:37], v[20:23]\n\t"
        "v_mfma_f32_16x16x16_bf16 v[24:27], v[12:13], v[36:37], v[24:27]\n\t"
        "v_mfma_f32_16x16x16_bf16 v[28:31], v[14:15], v[36:37], v[28:31]\n\t"
        "ds_read_b128 v[48:51], v117 offset:4096\n\t"
        "ds_read_b128 v[52:55], v117 offset:5120\n\t"
        "ds_read_b128 v[56:59], v117 offset:6144\n\t"
        "ds_read_b128 v[60:63], v117 offset:7168\n\t"
        "s_sub_u32 s56, 0, s56\n\t"
        "v_cvt_pk_bf16_f32 v0, v16, v17\n\t"
        "v_cvt_pk_bf16_f32 v1, v18, v19\n\t"
        "v_cvt_pk_bf16_f32 v2, v20, v21\n\t"
        "v_cvt_pk_bf16_f32 v3, v22, v23\n\t"
        "buffer_store_dwordx4 v[0:3], v123, s[12:15], s34 offen sc1\n\t"
        "v_cvt_pk_bf16_f32 v4, v24, v25\n\t"
        "v_cvt_pk_bf16_f32 v5, v26, v27\n\t"
        "v_cvt_pk_bf16_f32 v6, v28, v29\n\t"
        "v_cvt_pk_bf16_f32 v7, v30, v31\n\t"
        "buffer_store_dwordx4 v[4:7], v45, s[12:15], s34 offen sc1\n\t"
        "s_cmp_eq_u32 s24, 63\n\t"
        "s_cbranch_scc1 LA_last%=\n\t"
        "ds_read_b128 v[32:35], v117 offset:0\n\t"
        "ds_read_b128 v[36:39], v117 offset:1024\n\t"
        "ds_read_b128 v[40:43], v117 offset:2048\n\t"
        "ds_read_b128 v[44:47], v117 offset:3072\n\t"
        "v_mov_b64_e32 v[14:15], 0\n\t"
        "v_mov_b64_e32 v[12:13], 0\n\t"
        "v_mov_b64_e32 v[10:11], 0\n\t"
        "v_mov_b64_e32 v[8:9], 0\n\t"
        "v_mov_b64_e32 v[6:7], 0\n\t"
        "v_mov_b64_e32 v[4:5], 0\n\t"
        "v_mov_b64_e32 v[2:3], 0\n\t"
        "v_mov_b64_e32 v[0:1], 0\n\t"
        "v_readfirstlane_b32 s34, v111\n\t"
        "s_nop 0\n\t"
        "s_max_u32 s54, s54, s34\n\t"
        "v_readfirstlane_b32 s35, v109\n\t"
        "v_readlane_b32 s36, v108, 63\n\t"
        "s_bfe_u32 s29, s35, 0xc0008\n\t"
        "s_lshr_b32 s30, s35, 20\n\t"
        "s_add_u32 s35, s31, s50\n\t"
        "s_mul_i32 s37, s50, 3\n\t"
        "s_min_u32 s35, s35, s37\n\t"
        "s_add_i32 s34, s36, s51\n\t"
        "s_cmp_gt_i32 s34, 0\n\t"
        "s_cselect_b32 s31, s35, 0\n\t"
        "s_cselect_b32 s34, s34, 0\n\t"
        "s_add_u32 s34, s34, s31\n\t"
        "s_add_u32 s37, s25, s34\n\t"
        "s_add_u32 s45, s37, s29\n\t"
        "s_add_u32 s25, s45, s30\n\t"
        "s_add_u32 s24, s24, 1\n\t"
        "s_mov_b32 s28, s26\n\t"
        "s_mov_b32 s26, s27\n\t"
        "s_add_u32 s27, s27, 0x80000\n\t"
        "s_and_b32 s27, s27, 0x1fffff\n\t"
        "s_branch LA_loop%=\n\t"
        "LA_last%=:\n\t"
        "s_waitcnt vmcnt(0)\n\t"
        "s_barrier\n\t"
        "buffer_store_dword v126, v127, s[12:15], 0 offen sc1\n\t"
        "s_endpgm\n\t"
        "LA_skip%=:\n\t"
        "buffer_store_dword v126, v127, s[12:15], 0 offen sc1\n\t"
        "ds_read_b64_tr_b16 v[32:33], v119 offset:2112\n\t"
        "ds_read_b64_tr_b16 v[34:35], v119 offset:2144\n\t"
        "ds_read_b64_tr_b16 v[36:37], v119 offset:2368\n\t"
        "ds_read_b64_tr_b16 v[38:39], v119 offset:2400\n\t"
        "ds_read_b64_tr_b16 v[40:41], v119 offset:2624\n\t"
        "ds_read_b64_tr_b16 v[42:43], v119 offset:2656\n\t"
        "ds_read_b64_tr_b16 v[44:45], v119 offset:2880\n\t"
        "ds_read_b64_tr_b16 v[46:47], v119 offset:2912\n\t"
        "ds_read_b128 v[48:51], v108 offset:16384\n\t"
        "ds_read_b128 v[52:55], v108 offset:17408\n\t"
        "s_waitcnt lgkmcnt(0)\n\t"
        "s_branch LA_x%=\n\t"
        "LA_stre%=:\n\t"
        "s_mov_b32 s42, 0\n\t"
        "LA_str2%=:\n\t"
        "buffer_load_dwordx2 v[108:109], v125, s[8:11], s26 offen sc1\n\t"
        "s_waitcnt vmcnt(0)\n\t"
        "v_and_b32_e32 v45, 0xff, v109\n\t"
        "v_cmp_ne_u32_e32 vcc, s24, v45\n\t"
        "s_nop 0\n\t"
        "s_cbranch_vccz LA_stok%=\n\t"
        "s_add_u32 s42, s42, 1\n\t"
        "s_cmp_lt_u32 s42, 0x100000\n\t"
        "s_cbranch_scc1 LA_str2%=\n\t"
        "s_movk_i32 s34, 5\n\t"
        "s_branch LA_die%=\n\t"
        "LA_v2fail%=:\n\t"
        "s_mov_b32 s42, 0\n\t"
        "LA_v2re%=:\n\t"
        "v_xor_b32_e32 v45, 0x200, v120\n\t"
        "buffer_load_dwordx4 v[12:15], v45, s[8:11], s26 offen sc1\n\t"
        "buffer_load_dwordx4 v[104:107], v120, s[8:11], s26 offen sc1\n\t"
        "s_waitcnt vmcnt(0)\n\t"
        "v_max3_u32 v45, v12, v13, v14\n\t"
        "v_max3_u32 v45, v45, v15, v104\n\t"
        "v_max3_u32 v45, v45, v105, v106\n\t"
        "v_max_u32_e32 v45, v45, v107\n\t"
        "v_cmp_eq_u32_e32 vcc, -1, v45\n\t"
        "s_nop 0\n\t"
        "s_cbranch_vccz LA_v2ok%=\n\t"
        "s_add_u32 s42, s42, 1\n\t"
        "s_cmp_lt_u32 s42, 0x100000\n\t"
        "s_cbranch_scc1 LA_v2re%=\n\t"
        "s_movk_i32 s34, 6\n\t"
        "s_branch LA_die%=\n\t"
        "LA_rdfail%=:\n\t"
        "s_or_b32 s47, s47, 1\n\t"
        "buffer_load_dwordx4 v[16:19], v112, s[8:11], s26 offen offset:0 sc1\n\t"
        "buffer_load_dwordx4 v[20:23], v112, s[8:11], s26 offen offset:1024 sc1\n\t"
        "buffer_load_dwordx4 v[24:27], v112, s[8:11], s26 offen offset:2048 sc1\n\t"
        "buffer_load_dwordx4 v[28:31], v112, s[8:11], s26 offen offset:3072 sc1\n\t"
        "s_nop 7\n\t"
        "v_mov_b64_e32 v[0:1], 0\n\t"
        "v_mov_b64_e32 v[2:3], 0\n\t"
        "v_mov_b64_e32 v[4:5], 0\n\t"
        "v_mov_b64_e32 v[6:7], 0\n\t"
        "v_mov_b64_e32 v[8:9], 0\n\t"
        "v_mov_b64_e32 v[10:11], 0\n\t"
        "v_mov_b64_e32 v[12:13], 0\n\t"
        "v_mov_b64_e32 v[14:15], 0\n\t"
        "s_branch LA_rdre%=w\n\t"
        "LA_rdre%=:\n\t"
        "buffer_load_dwordx4 v[16:19], v112, s[8:11], s26 offen offset:0 sc1\n\t"
        "buffer_load_dwordx4 v[20:23], v112, s[8:11], s26 offen offset:1024 sc1\n\t"
        "buffer_load_dwordx4 v[24:27], v112, s[8:11], s26 offen offset:2048 sc1\n\t"
        "buffer_load_dwordx4 v[28:31], v112, s[8:11], s26 offen offset:3072 sc1\n\t"
        "LA_rdre%=w:\n\t"
        "s_waitcnt vmcnt(0) lgkmcnt(0)\n\t"
        "v_max3_u32 v111, v16, v17, v18\n\t"
        "v_max3_u32 v111, v111, v19, v20\n\t"
        "v_max3_u32 v111, v111, v21, v22\n\t"
        "v_max3_u32 v111, v111, v23, v24\n\t"
        "v_max3_u32 v111, v111, v25, v26\n\t"
        "v_max3_u32 v111, v111, v27, v28\n\t"
        "v_max3_u32 v111, v111, v29, v30\n\t"
        "v_max_u32_e32 v111, v111, v31\n\t"
        "v_cmp_eq_u32_e32 vcc, -1, v111\n\t"
        "s_nop 0\n\t"
        "s_cbranch_vccz LA_go%=\n\t"
        "s_add_u32 s42, s42, 1\n\t"
        "s_cmp_lt_u32 s42, 0x100000\n\t"
        "s_cbranch_scc1 LA_rdre%=\n\t"
        "s_movk_i32 s34, 1\n\t"
        "s_branch LA_die%=\n\t"
        "LA_f2%=:\n\t"
        "s_or_b32 s47, s47, 1\n\t"
        "buffer_load_dwordx4 v[24:27], v112, s[8:11], s26 offen offset:2048 sc1\n\t"
        "s_nop 7\n\t"
        "s_branch LA_r2%=w\n\t"
        "LA_r2%=:\n\t"
        "buffer_load_dwordx4 v[24:27], v112, s[8:11], s26 offen offset:2048 sc1\n\t"
        "LA_r2%=w:\n\t"
        "s_waitcnt vmcnt(0) lgkmcnt(0)\n\t"
        "v_max3_u32 v111, v24, v25, v26\n\t"
        "v_max_u32_e32 v111, v111, v27\n\t"
        "v_cmp_eq_u32_e32 vcc, -1, v111\n\t"
        "s_nop 0\n\t"
        "s_cbranch_vccz LA_go%=\n\t"
        "s_add_u32 s42, s42, 1\n\t"
        "s_cmp_lt_u32 s42, 0x100000\n\t"
        "s_cbranch_scc1 LA_r2%=\n\t"
        "s_movk_i32 s34, 1\n\t"
        "s_branch LA_die%=\n\t"
        "LA_f3%=:\n\t"
        "s_or_b32 s47, s47, 1\n\t"
        "buffer_load_dwordx4 v[28:31], v112, s[8:11], s26 offen offset:3072 sc1\n\t"
        "s_nop 7\n\t"
        "s_branch LA_r3%=w\n\t"
        "LA_r3%=:\n\t"
        "buffer_load_dwordx4 v[28:31], v112, s[8:11], s26 offen offset:3072 sc1\n\t"
        "LA_r3%=w:\n\t"
        "s_waitcnt vmcnt(0) lgkmcnt(0)\n\t"
        "v_max3_u32 v111, v28, v29, v30\n\t"
        "v_max_u32_e32 v111, v111, v31\n\t"
        "v_cmp_eq_u32_e32 vcc, -1, v111\n\t"
        "s_nop 0\n\t"
        "s_cbranch_vccz LA_go%=\n\t"
        "s_add_u32 s42, s42, 1\n\t"
        "s_cmp_lt_u32 s42, 0x100000\n\t"
        "s_cbranch_scc1 LA_r3%=\n\t"
        "s_movk_i32 s34, 1\n\t"
        "s_branch LA_die%=\n\t"
        "LA_vmfail%=:\n\t"
        "s_or_b32 s47, s47, 2\n\t"
        "s_mov_b32 s42, 0\n\t"
        "LA_vmre%=:\n\t"
        "buffer_load_dwordx4 v[104:107], v120, s[8:11], s26 offen sc1\n\t"
        "v_mfma_f32_16x16x32_bf16 v[4:7], v[48:51], v[12:15], 0\n\t"
        "v_mfma_f32_16x16x32_bf16 v[8:11], v[52:55], v[12:15], 0\n\t"
        "s_nop 7\n\t"
        "s_waitcnt vmcnt(0)\n\t"
        "v_mfma_f32_16x16x16_bf16 v[4:7], v[104:105], v[56:57], v[4:7]\n\t"
        "v_mfma_f32_16x16x16_bf16 v[8:11], v[106:107], v[56:57], v[8:11]\n\t"
        "v_max3_u32 v111, v104, v105, v106\n\t"
        "v_max_u32_e32 v111, v111, v107\n\t"
        "v_cmp_eq_u32_e32 vcc, -1, v111\n\t"
        "s_nop 7\n\t"
        "s_cbranch_vccz LA_vmok%=\n\t"
        "s_add_u32 s42, s42, 1\n\t"
        "s_cmp_lt_u32 s42, 0x100000\n\t"
        "s_cbranch_scc1 LA_vmre%=\n\t"
        "s_movk_i32 s34, 2\n\t"
        "s_branch LA_die%=\n\t"
        "LA_die%=:\n\t"
        "v_mov_b32_e32 v30, s34\n\t"
        "s_mov_b32 s35, 33024\n\t"
        "s_mov_b64 exec, 1\n\t"
        "buffer_store_dword v30, off, s[12:15], s35 sc1\n\t"
        "s_endpgm\n\t"
        ".Lk_end_A:\n\t"
        :: REC_S_IN, "{v96}"(g_w), "{v97}"(wb0), "{v98}"(wb1), "{v99}"(rb0), "{v100}"(rb0 ^ 32u), "{v101}"(rb0 ^ 64u), "{v102}"(rb0 ^ 96u),
           "{v112}"(a_mem), "{v113}"(l_ms + w * 4096), "{v114}"(l_ms + ((w + 1) & 3) * 4096), "{v115}"(l_ms + ((w + 2) & 3) * 4096), "{v116}"(l_ms + ((w + 3) & 3) * 4096),
           "{v117}"(lb + w * 22528 + l * 16), "{v118}"(l_iw), "{v119}"(l_iq), "{v120}"(a_vm), "{v121}"(a_o), "{v122}"(h * 8192 + l * 16), "{v123}"(a_ox), "{v124}"(lb + L_KV + l * 16), "{s55}"(lb + L_KV + w * 2048),
           "{v125}"((24 + cp) * 16384 + l * ST_P), "{v126}"(16 * (w & 1) + n - 4 * g), "{v103}"((l & 1) ? 0xEEEEEEEEu : 0x44444444u),
           "{v127}"((l == 0 && w == 0) ? F_PROG + h * 128 : OOB)
        : "memory", "vcc", "scc", "v0", "v255", "a0", "a255", "s101");
    __builtin_unreachable();
}
__device__ __forceinline__ void rec_b(const Params& p, unsigned lb, const int wv, const int id, const int rg, const int cp, const int rot, const int bq) {
    const int w = wv, l = otid() & 63, n = l & 15, g = l >> 4, cs = id, rb = 32 + id * 2 + rg;
    const v4u sh = mk_srd(p.ws + hx_base(p.ws), HX_BYTES - 8192), sw = mk_srd(p.ws, W_TOTAL), s1 = mk_srd(p.Wv, 1024 * 4096 * 2), s2 = mk_srd(p.Wo, 1024 * 4096 * 2);
    const int r8 = l >> 3, pc = l & 7, pp = n >> 3, bb = (n >> 2) & 1, f = (g & 1) | (pp << 1);
    const unsigned stg = lb + LB_STG + w * LB_STGP;
    const unsigned g_v = r8 * 8192 + cs * 256 + rg * 128 + pc * 16, g_o = (l >> 2) * 2048 + cs * 64 + (l & 3) * 16;
    const unsigned wb0 = stg + r8 * 128 + (((pc >> 1) ^ (r8 >> 2)) << 5) + (pc & 1) * 16, wb1 = wb0 ^ 64u;
    const unsigned rb0 = stg + (16 * (g >> 1) + 8 * pp + 4 * (g & 1) + bb) * 128 + (f << 5) + (n & 3) * 8;
    const unsigned o_r = stg + (g >> 1) * (2048 + 128) + (g & 1) * (1024 + 32) + (bb + 4 * pp) * 64 + (n & 3) * 8;
    const unsigned a_mem = cp * 32768 + (w >> 1) * 16384 + (w & 1) * 4096 + l * 16;
    const unsigned a_vmw = w < 2 ? (16 + (cs >> 2)) * 16384 + (cs & 3) * 2048 + (2 * rg + w) * 512 + (l & 31) * 16 + (g >> 1) * 8 : OOB;
    const unsigned a_or = (8 + rg * 4 + w) * 16384 + l * 16;
    const unsigned a_mw = (RPL != 0 && RPL != 4 && w == 3) ? OOB : w * 32768 + (cs >> 4) * 16384 + ((cs >> 1) & 7) * 1024 + (cs & 1) * 512 + (l >> 5) * 256 + rg * 128 + (l & 31) * 4;
    const unsigned a_sw = (w == 0 && l < 4) ? (24 + l) * 16384 + (id * 2 + rg) * ST_P : OOB;
    const bf16_t* m0p = p.mem0 + (4 * rg + ((l >> 3) & 3)) * 1024 + cs * 32 + (l >> 5) * 16 + (l & 7) * 2;
    asm volatile(
        "global_load_dword v109, v[108:109], off\n\t"
        "s_add_u32 s35, s68, 0\n\t"
        "s_and_b32 s35, s35, 7\n\t"
        "s_lshl_b32 s69, s35, 10\n\t"
        "s_add_u32 s35, s68, 2\n\t"
        "s_and_b32 s35, s35, 7\n\t"
        "s_lshl_b32 s70, s35, 10\n\t"
        "s_add_u32 s35, s68, 4\n\t"
        "s_and_b32 s35, s35, 7\n\t"
        "s_lshl_b32 s71, s35, 10\n\t"
        "s_add_u32 s35, s68, 6\n\t"
        "s_and_b32 s35, s35, 7\n\t"
        "s_lshl_b32 s72, s35, 10\n\t"
        "s_lshl_b32 s34, s43, 21\n\t"
        "s_add_u32 s34, s34, 0\n\t"
        "buffer_load_dwordx4 v[128:131], v96, s[16:19], s34 offen nt\n\t"
        "s_add_u32 s34, s34, 0x10000\n\t"
        "buffer_load_dwordx4 v[132:135], v96, s[16:19], s34 offen nt\n\t"
        "s_add_u32 s34, s34, 0x10000\n\t"
        "buffer_load_dwordx4 v[136:139], v96, s[16:19], s34 offen nt\n\t"
        "s_add_u32 s34, s34, 0x10000\n\t"
        "buffer_load_dwordx4 v[140:143], v96, s[16:19], s34 offen nt\n\t"
        "s_add_u32 s34, s34, 0x10000\n\t"
        "buffer_load_dwordx4 v[144:147], v96, s[16:19], s34 offen nt\n\t"
        "s_add_u32 s34, s34, 0x10000\n\t"
        "buffer_load_dwordx4 v[148:151], v96, s[16:19], s34 offen nt\n\t"
        "s_add_u32 s34, s34, 0x10000\n\t"
        "buffer_load_dwordx4 v[152:155], v96, s[16:19], s34 offen nt\n\t"
        "s_add_u32 s34, s34, 0x10000\n\t"
        "buffer_load_dwordx4 v[156:159], v96, s[16:19], s34 offen nt\n\t"
        "s_lshl_b32 s34, s43, 21\n\t"
        "s_add_u32 s34, s34, 524288\n\t"
        "buffer_load_dwordx4 v[160:163], v96, s[16:19], s34 offen nt\n\t"
        "s_add_u32 s34, s34, 0x10000\n\t"
        "buffer_load_dwordx4 v[164:167], v96, s[16:19], s34 offen nt\n\t"
        "s_add_u32 s34, s34, 0x10000\n\t"
        "buffer_load_dwordx4 v[168:171], v96, s[16:19], s34 offen nt\n\t"
        "s_add_u32 s34, s34, 0x10000\n\t"
        "buffer_load_dwordx4 v[172:175], v96, s[16:19], s34 offen nt\n\t"
        "s_add_u32 s34, s34, 0x10000\n\t"
        "buffer_load_dwordx4 v[176:179], v96, s[16:19], s34 offen nt\n\t"
        "s_add_u32 s34, s34, 0x10000\n\t"
        "buffer_load_dwordx4 v[180:183], v96, s[16:19], s34 offen nt\n\t"
        "s_add_u32 s34, s34, 0x10000\n\t"
        "buffer_load_dwordx4 v[184:187], v96, s[16:19], s34 offen nt\n\t"
        "s_add_u32 s34, s34, 0x10000\n\t"
        "buffer_load_dwordx4 v[188:191], v96, s[16:19], s34 offen nt\n\t"
        "s_lshl_b32 s34, s43, 21\n\t"
        "s_add_u32 s34, s34, 1048576\n\t"
        "buffer_load_dwordx4 v[192:195], v96, s[16:19], s34 offen nt\n\t"
        "s_add_u32 s34, s34, 0x10000\n\t"
        "buffer_load_dwordx4 v[196:199], v96, s[16:19], s34 offen nt\n\t"
        "s_add_u32 s34, s34, 0x10000\n\t"
        "buffer_load_dwordx4 v[200:203], v96, s[16:19], s34 offen nt\n\t"
        "s_add_u32 s34, s34, 0x10000\n\t"
        "buffer_load_dwordx4 v[204:207], v96, s[16:19], s34 offen nt\n\t"
        "s_add_u32 s34, s34, 0x10000\n\t"
        "buffer_load_dwordx4 v[208:211], v96, s[16:19], s34 offen nt\n\t"
        "s_add_u32 s34, s34, 0x10000\n\t"
        "buffer_load_dwordx4 v[212:215], v96, s[16:19], s34 offen nt\n\t"
        "s_add_u32 s34, s34, 0x10000\n\t"
        "buffer_load_dwordx4 v[216:219], v96, s[16:19], s34 offen nt\n\t"
        "s_add_u32 s34, s34, 0x10000\n\t"
        "buffer_load_dwordx4 v[220:223], v96, s[16:19], s34 offen nt\n\t"
        "s_waitcnt vmcnt(16)\n\t"
        "ds_write_b128 v98, v[128:131] offset:0\n\t"
        "ds_write_b128 v99, v[132:135] offset:1024\n\t"
        "ds_write_b128 v98, v[136:139] offset:2048\n\t"
        "ds_write_b128 v99, v[140:143] offset:3072\n\t"
        "ds_write_b128 v98, v[144:147] offset:4096\n\t"
        "ds_write_b128 v99, v[148:151] offset:5120\n\t"
        "ds_write_b128 v98, v[152:155] offset:6144\n\t"
        "ds_write_b128 v99, v[156:159] offset:7168\n\t"
        "ds_read_b64_tr_b16 v[128:129], v100 offset:0\n\t"
        "ds_read_b64_tr_b16 v[130:131], v100 offset:256\n\t"
        "ds_read_b64_tr_b16 v[132:133], v100 offset:4096\n\t"
        "ds_read_b64_tr_b16 v[134:135], v100 offset:4352\n\t"
        "s_waitcnt lgkmcnt(8)\n\t"
        "ds_read_b64_tr_b16 v[136:137], v101 offset:0\n\t"
        "ds_read_b64_tr_b16 v[138:139], v101 offset:256\n\t"
        "ds_read_b64_tr_b16 v[140:141], v101 offset:4096\n\t"
        "ds_read_b64_tr_b16 v[142:143], v101 offset:4352\n\t"
        "s_waitcnt lgkmcnt(8)\n\t"
        "ds_read_b64_tr_b16 v[144:145], v102 offset:0\n\t"
        "ds_read_b64_tr_b16 v[146:147], v102 offset:256\n\t"
        "ds_read_b64_tr_b16 v[148:149], v102 offset:4096\n\t"
        "ds_read_b64_tr_b16 v[150:151], v102 offset:4352\n\t"
        "s_waitcnt lgkmcnt(8)\n\t"
        "ds_read_b64_tr_b16 v[152:153], v103 offset:0\n\t"
        "ds_read_b64_tr_b16 v[154:155], v103 offset:256\n\t"
        "ds_read_b64_tr_b16 v[156:157], v103 offset:4096\n\t"
        "ds_read_b64_tr_b16 v[158:159], v103 offset:4352\n\t"
        "s_lshl_b32 s34, s43, 21\n\t"
        "s_add_u32 s34, s34, 1572864\n\t"
        "buffer_load_dwordx4 v[224:227], v96, s[16:19], s34 offen nt\n\t"
        "s_add_u32 s34, s34, 0x10000\n\t"
        "buffer_load_dwordx4 v[228:231], v96, s[16:19], s34 offen nt\n\t"
        "s_add_u32 s34, s34, 0x10000\n\t"
        "buffer_load_dwordx4 v[232:235], v96, s[16:19], s34 offen nt\n\t"
        "s_add_u32 s34, s34, 0x10000\n\t"
        "buffer_load_dwordx4 v[236:239], v96, s[16:19], s34 offen nt\n\t"
        "s_add_u32 s34, s34, 0x10000\n\t"
        "buffer_load_dwordx4 v[240:243], v96, s[16:19], s34 offen nt\n\t"
        "s_add_u32 s34, s34, 0x10000\n\t"
        "buffer_load_dwordx4 v[244:247], v96, s[16:19], s34 offen nt\n\t"
        "s_add_u32 s34, s34, 0x10000\n\t"
        "buffer_load_dwordx4 v[248:251], v96, s[16:19], s34 offen nt\n\t"
        "s_add_u32 s34, s34, 0x10000\n\t"
        "buffer_load_dwordx4 v[252:255], v96, s[16:19], s34 offen nt\n\t"
        "s_waitcnt lgkmcnt(0)\n\t"
        "s_waitcnt vmcnt(16)\n\t"
        "ds_write_b128 v98, v[160:163] offset:0\n\t"
        "ds_write_b128 v99, v[164:167] offset:1024\n\t"
        "ds_write_b128 v98, v[168:171] offset:2048\n\t"
        "ds_write_b128 v99, v[172:175] offset:3072\n\t"
        "ds_write_b128 v98, v[176:179] offset:4096\n\t"
        "ds_write_b128 v99, v[180:183] offset:5120\n\t"
        "ds_write_b128 v98, v[184:187] offset:6144\n\t"
        "ds_write_b128 v99, v[188:191] offset:7168\n\t"
        "ds_read_b64_tr_b16 v[160:161], v100 offset:0\n\t"
        "ds_read_b64_tr_b16 v[162:163], v100 offset:256\n\t"
        "ds_read_b64_tr_b16 v[164:165], v100 offset:4096\n\t"
        "ds_read_b64_tr_b16 v[166:167], v100 offset:4352\n\t"
        "s_waitcnt lgkmcnt(8)\n\t"
        "ds_read_b64_tr_b16 v[168:169], v101 offset:0\n\t"
        "ds_read_b64_tr_b16 v[170:171], v101 offset:256\n\t"
        "ds_read_b64_tr_b16 v[172:173], v101 offset:4096\n\t"
        "ds_read_b64_tr_b16 v[174:175], v101 offset:4352\n\t"
        "s_waitcnt lgkmcnt(8)\n\t"
        "ds_read_b64_tr_b16 v[176:177], v102 offset:0\n\t"
        "ds_read_b64_tr_b16 v[178:179], v102 offset:256\n\t"
        "ds_read_b64_tr_b16 v[180:181], v102 offset:4096\n\t"
        "ds_read_b64_tr_b16 v[182:183], v102 offset:4352\n\t"
        "s_waitcnt lgkmcnt(8)\n\t"
        "ds_read_b64_tr_b16 v[184:185], v103 offset:0\n\t"
        "ds_read_b64_tr_b16 v[186:187], v103 offset:256\n\t"
        "ds_read_b64_tr_b16 v[188:189], v103 offset:4096\n\t"
        "ds_read_b64_tr_b16 v[190:191], v103 offset:4352\n\t"
        "s_add_u32 s35, s68, 0\n\t"
        "s_and_b32 s35, s35, 7\n\t"
        "s_lshl_b32 s35, s35, 18\n\t"
        "s_lshl_b32 s34, s43, 21\n\t"
        "s_add_u32 s34, s34, s35\n\t"
        "buffer_load_dwordx4 a[0:3], v97, s[20:23], s34 offen\n\t"
        "s_add_u32 s34, s34, 0x8000\n\t"
        "buffer_load_dwordx4 a[4:7], v97, s[20:23], s34 offen\n\t"
        "s_add_u32 s34, s34, 0x8000\n\t"
        "buffer_load_dwordx4 a[8:11], v97, s[20:23], s34 offen\n\t"
        "s_add_u32 s34, s34, 0x8000\n\t"
        "buffer_load_dwordx4 a[12:15], v97, s[20:23], s34 offen\n\t"
        "s_add_u32 s34, s34, 0x8000\n\t"
        "buffer_load_dwordx4 a[16:19], v97, s[20:23], s34 offen\n\t"
        "s_add_u32 s34, s34, 0x8000\n\t"
        "buffer_load_dwordx4 a[20:23], v97, s[20:23], s34 offen\n\t"
        "s_add_u32 s34, s34, 0x8000\n\t"
        "buffer_load_dwordx4 a[24:27], v97, s[20:23], s34 offen\n\t"
        "s_add_u32 s34, s34, 0x8000\n\t"
        "buffer_load_dwordx4 a[28:31], v97, s[20:23], s34 offen\n\t"
        "s_waitcnt lgkmcnt(0)\n\t"
        "s_waitcnt vmcnt(16)\n\t"
        "ds_write_b128 v98, v[192:195] offset:0\n\t"
        "ds_write_b128 v99, v[196:199] offset:1024\n\t"
        "ds_write_b128 v98, v[200:203] offset:2048\n\t"
        "ds_write_b128 v99, v[204:207] offset:3072\n\t"
        "ds_write_b128 v98, v[208:211] offset:4096\n\t"
        "ds_write_b128 v99, v[212:215] offset:5120\n\t"
        "ds_write_b128 v98, v[216:219] offset:6144\n\t"
        "ds_write_b128 v99, v[220:223] offset:7168\n\t"
        "ds_read_b64_tr_b16 v[192:193], v100 offset:0\n\t"
        "ds_read_b64_tr_b16 v[194:195], v100 offset:256\n\t"
        "ds_read_b64_tr_b16 v[196:197], v100 offset:4096\n\t"
        "ds_read_b64_tr_b16 v[198:199], v100 offset:4352\n\t"
        "s_waitcnt lgkmcnt(8)\n\t"
        "ds_read_b64_tr_b16 v[200:201], v101 offset:0\n\t"
        "ds_read_b64_tr_b16 v[202:203], v101 offset:256\n\t"
        "ds_read_b64_tr_b16 v[204:205], v101 offset:4096\n\t"
        "ds_read_b64_tr_b16 v[206:207], v101 offset:4352\n\t"
        "s_waitcnt lgkmcnt(8)\n\t"
        "ds_read_b64_tr_b16 v[208:209], v102 offset:0\n\t"
        "ds_read_b64_tr_b16 v[210:211], v102 offset:256\n\t"
        "ds_read_b64_tr_b16 v[212:213], v102 offset:4096\n\t"
        "ds_read_b64_tr_b16 v[214:215], v102 offset:4352\n\t"
        "s_waitcnt lgkmcnt(8)\n\t"
        "ds_read_b64_tr_b16 v[216:217], v103 offset:0\n\t"
        "ds_read_b64_tr_b16 v[218:219], v103 offset:256\n\t"
        "ds_read_b64_tr_b16 v[220:221], v103 offset:4096\n\t"
        "ds_read_b64_tr_b16 v[222:223], v103 offset:4352\n\t"
        "s_add_u32 s35, s68, 1\n\t"
        "s_and_b32 s35, s35, 7\n\t"
        "s_lshl_b32 s35, s35, 18\n\t"
        "s_lshl_b32 s34, s43, 21\n\t"
        "s_add_u32 s34, s34, s35\n\t"
        "buffer_load_dwordx4 a[32:35], v97, s[20:23], s34 offen\n\t"
        "s_add_u32 s34, s34, 0x8000\n\t"
        "buffer_load_dwordx4 a[36:39], v97, s[20:23], s34 offen\n\t"
        "s_add_u32 s34, s34, 0x8000\n\t"
        "buffer_load_dwordx4 a[40:43], v97, s[20:23], s34 offen\n\t"
        "s_add_u32 s34, s34, 0x8000\n\t"
        "buffer_load_dwordx4 a[44:47], v97, s[20:23], s34 offen\n\t"
        "s_add_u32 s34, s34, 0x8000\n\t"
        "buffer_load_dwordx4 a[48:51], v97, s[20:23], s34 offen\n\t"
        "s_add_u32 s34, s34, 0x8000\n\t"
        "buffer_load_dwordx4 a[52:55], v97, s[20:23], s34 offen\n\t"
        "s_add_u32 s34, s34, 0x8000\n\t"
        "buffer_load_dwordx4 a[56:59], v97, s[20:23], s34 offen\n\t"
        "s_add_u32 s34, s34, 0x8000\n\t"
        "buffer_load_dwordx4 a[60:63], v97, s[20:23], s34 offen\n\t"
        "s_waitcnt lgkmcnt(0)\n\t"
        "s_waitcnt vmcnt(16)\n\t"
        "ds_write_b128 v98, v[224:227] offset:0\n\t"
        "ds_write_b128 v99, v[228:231] offset:1024\n\t"
        "ds_write_b128 v98, v[232:235] offset:2048\n\t"
        "ds_write_b128 v99, v[236:239] offset:3072\n\t"
        "ds_write_b128 v98, v[240:243] offset:4096\n\t"
        "ds_write_b128 v99, v[244:247] offset:5120\n\t"
        "ds_write_b128 v98, v[248:251] offset:6144\n\t"
        "ds_write_b128 v99, v[252:255] offset:7168\n\t"
        "ds_read_b64_tr_b16 v[224:225], v100 offset:0\n\t"
        "ds_read_b64_tr_b16 v[226:227], v100 offset:256\n\t"
        "ds_read_b64_tr_b16 v[228:229], v100 offset:4096\n\t"
        "ds_read_b64_tr_b16 v[230:231], v100 offset:4352\n\t"
        "s_waitcnt lgkmcnt(8)\n\t"
        "ds_read_b64_tr_b16 v[232:233], v101 offset:0\n\t"
        "ds_read_b64_tr_b16 v[234:235], v101 offset:256\n\t"
        "ds_read_b64_tr_b16 v[236:237], v101 offset:4096\n\t"
        "ds_read_b64_tr_b16 v[238:239], v101 offset:4352\n\t"
        "s_waitcnt lgkmcnt(8)\n\t"
        "ds_read_b64_tr_b16 v[240:241], v102 offset:0\n\t"
        "ds_read_b64_tr_b16 v[242:243], v102 offset:256\n\t"
        "ds_read_b64_tr_b16 v[244:245], v102 offset:4096\n\t"
        "ds_read_b64_tr_b16 v[246:247], v102 offset:4352\n\t"
        "s_waitcnt lgkmcnt(8)\n\t"
        "ds_read_b64_tr_b16 v[248:249], v103 offset:0\n\t"
        "ds_read_b64_tr_b16 v[250:251], v103 offset:256\n\t"
        "ds_read_b64_tr_b16 v[252:253], v103 offset:4096\n\t"
        "ds_read_b64_tr_b16 v[254:255], v103 offset:4352\n\t"
        "s_add_u32 s35, s68, 2\n\t"
        "s_and_b32 s35, s35, 7\n\t"
        "s_lshl_b32 s35, s35, 18\n\t"
        "s_lshl_b32 s34, s43, 21\n\t"
        "s_add_u32 s34, s34, s35\n\t"
        "buffer_load_dwordx4 a[64:67], v97, s[20:23], s34 offen\n\t"
        "s_add_u32 s34, s34, 0x8000\n\t"
        "buffer_load_dwordx4 a[68:71], v97, s[20:23], s34 offen\n\t"
        "s_add_u32 s34, s34, 0x8000\n\t"
        "buffer_load_dwordx4 a[72:75], v97, s[20:23], s34 offen\n\t"
        "s_add_u32 s34, s34, 0x8000\n\t"
        "buffer_load_dwordx4 a[76:79], v97, s[20:23], s34 offen\n\t"
        "s_add_u32 s34, s34, 0x8000\n\t"
        "buffer_load_dwordx4 a[80:83], v97, s[20:23], s34 offen\n\t"
        "s_add_u32 s34, s34, 0x8000\n\t"
        "buffer_load_dwordx4 a[84:87], v97, s[20:23], s34 offen\n\t"
        "s_add_u32 s34, s34, 0x8000\n\t"
        "buffer_load_dwordx4 a[88:91], v97, s[20:23], s34 offen\n\t"
        "s_add_u32 s34, s34, 0x8000\n\t"
        "buffer_load_dwordx4 a[92:95], v97, s[20:23], s34 offen\n\t"
        "s_waitcnt lgkmcnt(0)\n\t"
        "s_waitcnt vmcnt(16)\n\t"
        "ds_write_b128 v122, a[0:3] offset:0\n\t"
        "ds_write_b128 v122, a[4:7] offset:1056\n\t"
        "ds_write_b128 v122, a[8:11] offset:2176\n\t"
        "ds_write_b128 v122, a[12:15] offset:3232\n\t"
        "ds_write_b128 v122, a[16:19] offset:4352\n\t"
        "ds_write_b128 v122, a[20:23] offset:5408\n\t"
        "ds_write_b128 v122, a[24:27] offset:6528\n\t"
        "ds_write_b128 v122, a[28:31] offset:7584\n\t"
        "ds_read_b64_tr_b16 a[0:1], v123 offset:0\n\t"
        "ds_read_b64_tr_b16 a[2:3], v123 offset:128\n\t"
        "ds_read_b64_tr_b16 a[4:5], v123 offset:4352\n\t"
        "ds_read_b64_tr_b16 a[6:7], v123 offset:4480\n\t"
        "s_waitcnt lgkmcnt(8)\n\t"
        "ds_read_b64_tr_b16 a[8:9], v123 offset:32\n\t"
        "ds_read_b64_tr_b16 a[10:11], v123 offset:160\n\t"
        "ds_read_b64_tr_b16 a[12:13], v123 offset:4384\n\t"
        "ds_read_b64_tr_b16 a[14:15], v123 offset:4512\n\t"
        "s_waitcnt lgkmcnt(8)\n\t"
        "ds_read_b64_tr_b16 a[16:17], v123 offset:512\n\t"
        "ds_read_b64_tr_b16 a[18:19], v123 offset:640\n\t"
        "ds_read_b64_tr_b16 a[20:21], v123 offset:4864\n\t"
        "ds_read_b64_tr_b16 a[22:23], v123 offset:4992\n\t"
        "s_waitcnt lgkmcnt(8)\n\t"
        "ds_read_b64_tr_b16 a[24:25], v123 offset:544\n\t"
        "ds_read_b64_tr_b16 a[26:27], v123 offset:672\n\t"
        "ds_read_b64_tr_b16 a[28:29], v123 offset:4896\n\t"
        "ds_read_b64_tr_b16 a[30:31], v123 offset:5024\n\t"
        "s_waitcnt lgkmcnt(8)\n\t"
        "s_add_u32 s35, s68, 3\n\t"
        "s_and_b32 s35, s35, 7\n\t"
        "s_lshl_b32 s35, s35, 18\n\t"
        "s_lshl_b32 s34, s43, 21\n\t"
        "s_add_u32 s34, s34, s35\n\t"
        "buffer_load_dwordx4 a[96:99], v97, s[20:23], s34 offen\n\t"
        "s_add_u32 s34, s34, 0x8000\n\t"
        "buffer_load_dwordx4 a[100:103], v97, s[20:23], s34 offen\n\t"
        "s_add_u32 s34, s34, 0x8000\n\t"
        "buffer_load_dwordx4 a[104:107], v97, s[20:23], s34 offen\n\t"
        "s_add_u32 s34, s34, 0x8000\n\t"
        "buffer_load_dwordx4 a[108:111], v97, s[20:23], s34 offen\n\t"
        "s_add_u32 s34, s34, 0x8000\n\t"
        "buffer_load_dwordx4 a[112:115], v97, s[20:23], s34 offen\n\t"
        "s_add_u32 s34, s34, 0x8000\n\t"
        "buffer_load_dwordx4 a[116:119], v97, s[20:23], s34 offen\n\t"
        "s_add_u32 s34, s34, 0x8000\n\t"
        "buffer_load_dwordx4 a[120:123], v97, s[20:23], s34 offen\n\t"
        "s_add_u32 s34, s34, 0x8000\n\t"
        "buffer_load_dwordx4 a[124:127], v97, s[20:23], s34 offen\n\t"
        "s_waitcnt lgkmcnt(0)\n\t"
        "s_waitcnt vmcnt(16)\n\t"
        "ds_write_b128 v122, a[32:35] offset:0\n\t"
        "ds_write_b128 v122, a[36:39] offset:1056\n\t"
        "ds_write_b128 v122, a[40:43] offset:2176\n\t"
        "ds_write_b128 v122, a[44:47] offset:3232\n\t"
        "ds_write_b128 v122, a[48:51] offset:4352\n\t"
        "ds_write_b128 v122, a[52:55] offset:5408\n\t"
        "ds_write_b128 v122, a[56:59] offset:6528\n\t"
        "ds_write_b128 v122, a[60:63] offset:7584\n\t"
        "ds_read_b64_tr_b16 a[32:33], v123 offset:0\n\t"
        "ds_read_b64_tr_b16 a[34:35], v123 offset:128\n\t"
        "ds_read_b64_tr_b16 a[36:37], v123 offset:4352\n\t"
        "ds_read_b64_tr_b16 a[38:39], v123 offset:4480\n\t"
        "s_waitcnt lgkmcnt(8)\n\t"
        "ds_read_b64_tr_b16 a[40:41], v123 offset:32\n\t"
        "ds_read_b64_tr_b16 a[42:43], v123 offset:160\n\t"
        "ds_read_b64_tr_b16 a[44:45], v123 offset:4384\n\t"
        "ds_read_b64_tr_b16 a[46:47], v123 offset:4512\n\t"
        "s_waitcnt lgkmcnt(8)\n\t"
        "ds_read_b64_tr_b16 a[48:49], v123 offset:512\n\t"
        "ds_read_b64_tr_b16 a[50:51], v123 offset:640\n\t"
        "ds_read_b64_tr_b16 a[52:53], v123 offset:4864\n\t"
        "ds_read_b64_tr_b16 a[54:55], v123 offset:4992\n\t"
        "s_waitcnt lgkmcnt(8)\n\t"
        "ds_read_b64_tr_b16 a[56:57], v123 offset:544\n\t"
        "ds_read_b64_tr_b16 a[58:59], v123 offset:672\n\t"
        "ds_read_b64_tr_b16 a[60:61], v123 offset:4896\n\t"
        "ds_read_b64_tr_b16 a[62:63], v123 offset:5024\n\t"
        "s_waitcnt lgkmcnt(8)\n\t"
        "s_add_u32 s35, s68, 4\n\t"
        "s_and_b32 s35, s35, 7\n\t"
        "s_lshl_b32 s35, s35, 18\n\t"
        "s_lshl_b32 s34, s43, 21\n\t"
        "s_add_u32 s34, s34, s35\n\t"
        "buffer_load_dwordx4 a[128:131], v97, s[20:23], s34 offen\n\t"
        "s_add_u32 s34, s34, 0x8000\n\t"
        "buffer_load_dwordx4 a[132:135], v97, s[20:23], s34 offen\n\t"
        "s_add_u32 s34, s34, 0x8000\n\t"
        "buffer_load_dwordx4 a[136:139], v97, s[20:23], s34 offen\n\t"
        "s_add_u32 s34, s34, 0x8000\n\t"
        "buffer_load_dwordx4 a[140:143], v97, s[20:23], s34 offen\n\t"
        "s_add_u32 s34, s34, 0x8000\n\t"
        "buffer_load_dwordx4 a[144:147], v97, s[20:23], s34 offen\n\t"
        "s_add_u32 s34, s34, 0x8000\n\t"
        "buffer_load_dwordx4 a[148:151], v97, s[20:23], s34 offen\n\t"
        "s_add_u32 s34, s34, 0x8000\n\t"
        "buffer_load_dwordx4 a[152:155], v97, s[20:23], s34 offen\n\t"
        "s_add_u32 s34, s34, 0x8000\n\t"
        "buffer_load_dwordx4 a[156:159], v97, s[20:23], s34 offen\n\t"
        "s_waitcnt lgkmcnt(0)\n\t"
        "s_waitcnt vmcnt(16)\n\t"
        "ds_write_b128 v122, a[64:67] offset:0\n\t"
        "ds_write_b128 v122, a[68:71] offset:1056\n\t"
        "ds_write_b128 v122, a[72:75] offset:2176\n\t"
        "ds_write_b128 v122, a[76:79] offset:3232\n\t"
        "ds_write_b128 v122, a[80:83] offset:4352\n\t"
        "ds_write_b128 v122, a[84:87] offset:5408\n\t"
        "ds_write_b128 v122, a[88:91] offset:6528\n\t"
        "ds_write_b128 v122, a[92:95] offset:7584\n\t"
        "ds_read_b64_tr_b16 a[64:65], v123 offset:0\n\t"
        "ds_read_b64_tr_b16 a[66:67], v123 offset:128\n\t"
        "ds_read_b64_tr_b16 a[68:69], v123 offset:4352\n\t"
        "ds_read_b64_tr_b16 a[70:71], v123 offset:4480\n\t"
        "s_waitcnt lgkmcnt(8)\n\t"
        "ds_read_b64_tr_b16 a[72:73], v123 offset:32\n\t"
        "ds_read_b64_tr_b16 a[74:75], v123 offset:160\n\t"
        "ds_read_b64_tr_b16 a[76:77], v123 offset:4384\n\t"
        "ds_read_b64_tr_b16 a[78:79], v123 offset:4512\n\t"
        "s_waitcnt lgkmcnt(8)\n\t"
        "ds_read_b64_tr_b16 a[80:81], v123 offset:512\n\t"
        "ds_read_b64_tr_b16 a[82:83], v123 offset:640\n\t"
        "ds_read_b64_tr_b16 a[84:85], v123 offset:4864\n\t"
        "ds_read_b64_tr_b16 a[86:87], v123 offset:4992\n\t"
        "s_waitcnt lgkmcnt(8)\n\t"
        "ds_read_b64_tr_b16 a[88:89], v123 offset:544\n\t"
        "ds_read_b64_tr_b16 a[90:91], v123 offset:672\n\t"
        "ds_read_b64_tr_b16 a[92:93], v123 offset:4896\n\t"
        "ds_read_b64_tr_b16 a[94:95], v123 offset:5024\n\t"
        "s_waitcnt lgkmcnt(8)\n\t"
        "s_add_u32 s35, s68, 5\n\t"
        "s_and_b32 s35, s35, 7\n\t"
        "s_lshl_b32 s35, s35, 18\n\t"
        "s_lshl_b32 s34, s43, 21\n\t"
        "s_add_u32 s34, s34, s35\n\t"
        "buffer_load_dwordx4 a[160:163], v97, s[20:23], s34 offen\n\t"
        "s_add_u32 s34, s34, 0x8000\n\t"
        "buffer_load_dwordx4 a[164:167], v97, s[20:23], s34 offen\n\t"
        "s_add_u32 s34, s34, 0x8000\n\t"
        "buffer_load_dwordx4 a[168:171], v97, s[20:23], s34 offen\n\t"
        "s_add_u32 s34, s34, 0x8000\n\t"
        "buffer_load_dwordx4 a[172:175], v97, s[20:23], s34 offen\n\t"
        "s_add_u32 s34, s34, 0x8000\n\t"
        "buffer_load_dwordx4 a[176:179], v97, s[20:23], s34 offen\n\t"
        "s_add_u32 s34, s34, 0x8000\n\t"
        "buffer_load_dwordx4 a[180:183], v97, s[20:23], s34 offen\n\t"
        "s_add_u32 s34, s34, 0x8000\n\t"
        "buffer_load_dwordx4 a[184:187], v97, s[20:23], s34 offen\n\t"
        "s_add_u32 s34, s34, 0x8000\n\t"
        "buffer_load_dwordx4 a[188:191], v97, s[20:23], s34 offen\n\t"
        "s_waitcnt lgkmcnt(0)\n\t"
        "s_waitcnt vmcnt(16)\n\t"
        "ds_write_b128 v122, a[96:99] offset:0\n\t"
        "ds_write_b128 v122, a[100:103] offset:1056\n\t"
        "ds_write_b128 v122, a[104:107] offset:2176\n\t"
        "ds_write_b128 v122, a[108:111] offset:3232\n\t"
        "ds_write_b128 v122, a[112:115] offset:4352\n\t"
        "ds_write_b128 v122, a[116:119] offset:5408\n\t"
        "ds_write_b128 v122, a[120:123] offset:6528\n\t"
        "ds_write_b128 v122, a[124:127] offset:7584\n\t"
        "ds_read_b64_tr_b16 a[96:97], v123 offset:0\n\t"
        "ds_read_b64_tr_b16 a[98:99], v123 offset:128\n\t"
        "ds_read_b64_tr_b16 a[100:101], v123 offset:4352\n\t"
        "ds_read_b64_tr_b16 a[102:103], v123 offset:4480\n\t"
        "s_waitcnt lgkmcnt(8)\n\t"
        "ds_read_b64_tr_b16 a[104:105], v123 offset:32\n\t"
        "ds_read_b64_tr_b16 a[106:107], v123 offset:160\n\t"
        "ds_read_b64_tr_b16 a[108:109], v123 offset:4384\n\t"
        "ds_read_b64_tr_b16 a[110:111], v123 offset:4512\n\t"
        "s_waitcnt lgkmcnt(8)\n\t"
        "ds_read_b64_tr_b16 a[112:113], v123 offset:512\n\t"
        "ds_read_b64_tr_b16 a[114:115], v123 offset:640\n\t"
        "ds_read_b64_tr_b16 a[116:117], v123 offset:4864\n\t"
        "ds_read_b64_tr_b16 a[118:119], v123 offset:4992\n\t"
        "s_waitcnt lgkmcnt(8)\n\t"
        "ds_read_b64_tr_b16 a[120:121], v123 offset:544\n\t"
        "ds_read_b64_tr_b16 a[122:123], v123 offset:672\n\t"
        "ds_read_b64_tr_b16 a[124:125], v123 offset:4896\n\t"
        "ds_read_b64_tr_b16 a[126:127], v123 offset:5024\n\t"
        "s_waitcnt lgkmcnt(8)\n\t"
        "s_add_u32 s35, s68, 6\n\t"
        "s_and_b32 s35, s35, 7\n\t"
        "s_lshl_b32 s35, s35, 18\n\t"
        "s_lshl_b32 s34, s43, 21\n\t"
        "s_add_u32 s34, s34, s35\n\t"
        "buffer_load_dwordx4 a[192:195], v97, s[20:23], s34 offen\n\t"
        "s_add_u32 s34, s34, 0x8000\n\t"
        "buffer_load_dwordx4 a[196:199], v97, s[20:23], s34 offen\n\t"
        "s_add_u32 s34, s34, 0x8000\n\t"
        "buffer_load_dwordx4 a[200:203], v97, s[20:23], s34 offen\n\t"
        "s_add_u32 s34, s34, 0x8000\n\t"
        "buffer_load_dwordx4 a[204:207], v97, s[20:23], s34 offen\n\t"
        "s_add_u32 s34, s34, 0x8000\n\t"
        "buffer_load_dwordx4 a[208:211], v97, s[20:23], s34 offen\n\t"
        "s_add_u32 s34, s34, 0x8000\n\t"
        "buffer_load_dwordx4 a[212:215], v97, s[20:23], s34 offen\n\t"
        "s_add_u32 s34, s34, 0x8000\n\t"
        "buffer_load_dwordx4 a[216:219], v97, s[20:23], s34 offen\n\t"
        "s_add_u32 s34, s34, 0x8000\n\t"
        "buffer_load_dwordx4 a[220:223], v97, s[20:23], s34 offen\n\t"
        "s_waitcnt lgkmcnt(0)\n\t"
        "s_waitcnt vmcnt(16)\n\t"
        "ds_write_b128 v122, a[128:131] offset:0\n\t"
        "ds_write_b128 v122, a[132:135] offset:1056\n\t"
        "ds_write_b128 v122, a[136:139] offset:2176\n\t"
        "ds_write_b128 v122, a[140:143] offset:3232\n\t"
        "ds_write_b128 v122, a[144:147] offset:4352\n\t"
        "ds_write_b128 v122, a[148:151] offset:5408\n\t"
        "ds_write_b128 v122, a[152:155] offset:6528\n\t"
        "ds_write_b128 v122, a[156:159] offset:7584\n\t"
        "ds_read_b64_tr_b16 a[128:129], v123 offset:0\n\t"
        "ds_read_b64_tr_b16 a[130:131], v123 offset:128\n\t"
        "ds_read_b64_tr_b16 a[132:133], v123 offset:4352\n\t"
        "ds_read_b64_tr_b16 a[134:135], v123 offset:4480\n\t"
        "s_waitcnt lgkmcnt(8)\n\t"
        "ds_read_b64_tr_b16 a[136:137], v123 offset:32\n\t"
        "ds_read_b64_tr_b16 a[138:139], v123 offset:160\n\t"
        "ds_read_b64_tr_b16 a[140:141], v123 offset:4384\n\t"
        "ds_read_b64_tr_b16 a[142:143], v123 offset:4512\n\t"
        "s_waitcnt lgkmcnt(8)\n\t"
        "ds_read_b64_tr_b16 a[144:145], v123 offset:512\n\t"
        "ds_read_b64_tr_b16 a[146:147], v123 offset:640\n\t"
        "ds_read_b64_tr_b16 a[148:149], v123 offset:4864\n\t"
        "ds_read_b64_tr_b16 a[150:151], v123 offset:4992\n\t"
        "s_waitcnt lgkmcnt(8)\n\t"
        "ds_read_b64_tr_b16 a[152:153], v123 offset:544\n\t"
        "ds_read_b64_tr_b16 a[154:155], v123 offset:672\n\t"
        "ds_read_b64_tr_b16 a[156:157], v123 offset:4896\n\t"
        "ds_read_b64_tr_b16 a[158:159], v123 offset:5024\n\t"
        "s_waitcnt lgkmcnt(8)\n\t"
        "s_add_u32 s35, s68, 7\n\t"
        "s_and_b32 s35, s35, 7\n\t"
        "s_lshl_b32 s35, s35, 18\n\t"
        "s_lshl_b32 s34, s43, 21\n\t"
        "s_add_u32 s34, s34, s35\n\t"
        "buffer_load_dwordx4 a[224:227], v97, s[20:23], s34 offen\n\t"
        "s_add_u32 s34, s34, 0x8000\n\t"
        "buffer_load_dwordx4 a[228:231], v97, s[20:23], s34 offen\n\t"
        "s_add_u32 s34, s34, 0x8000\n\t"
        "buffer_load_dwordx4 a[232:235], v97, s[20:23], s34 offen\n\t"
        "s_add_u32 s34, s34, 0x8000\n\t"
        "buffer_load_dwordx4 a[236:239], v97, s[20:23], s34 offen\n\t"
        "s_add_u32 s34, s34, 0x8000\n\t"
        "buffer_load_dwordx4 a[240:243], v97, s[20:23], s34 offen\n\t"
        "s_add_u32 s34, s34, 0x8000\n\t"
        "buffer_load_dwordx4 a[244:247], v97, s[20:23], s34 offen\n\t"
        "s_add_u32 s34, s34, 0x8000\n\t"
        "buffer_load_dwordx4 a[248:251], v97, s[20:23], s34 offen\n\t"
        "s_add_u32 s34, s34, 0x8000\n\t"
        "buffer_load_dwordx4 a[252:255], v97, s[20:23], s34 offen\n\t"
        "s_waitcnt lgkmcnt(0)\n\t"
        "s_waitcnt vmcnt(16)\n\t"
        "ds_write_b128 v122, a[160:163] offset:0\n\t"
        "ds_write_b128 v122, a[164:167] offset:1056\n\t"
        "ds_write_b128 v122, a[168:171] offset:2176\n\t"
        "ds_write_b128 v122, a[172:175] offset:3232\n\t"
        "ds_write_b128 v122, a[176:179] offset:4352\n\t"
        "ds_write_b128 v122, a[180:183] offset:5408\n\t"
        "ds_write_b128 v122, a[184:187] offset:6528\n\t"
        "ds_write_b128 v122, a[188:191] offset:7584\n\t"
        "ds_read_b64_tr_b16 a[160:161], v123 offset:0\n\t"
        "ds_read_b64_tr_b16 a[162:163], v123 offset:128\n\t"
        "ds_read_b64_tr_b16 a[164:165], v123 offset:4352\n\t"
        "ds_read_b64_tr_b16 a[166:167], v123 offset:4480\n\t"
        "s_waitcnt lgkmcnt(8)\n\t"
        "ds_read_b64_tr_b16 a[168:169], v123 offset:32\n\t"
        "ds_read_b64_tr_b16 a[170:171], v123 offset:160\n\t"
        "ds_read_b64_tr_b16 a[172:173], v123 offset:4384\n\t"
        "ds_read_b64_tr_b16 a[174:175], v123 offset:4512\n\t"
        "s_waitcnt lgkmcnt(8)\n\t"
        "ds_read_b64_tr_b16 a[176:177], v123 offset:512\n\t"
        "ds_read_b64_tr_b16 a[178:179], v123 offset:640\n\t"
        "ds_read_b64_tr_b16 a[180:181], v123 offset:4864\n\t"
        "ds_read_b64_tr_b16 a[182:183], v123 offset:4992\n\t"
        "s_waitcnt lgkmcnt(8)\n\t"
        "ds_read_b64_tr_b16 a[184:185], v123 offset:544\n\t"
        "ds_read_b64_tr_b16 a[186:187], v123 offset:672\n\t"
        "ds_read_b64_tr_b16 a[188:189], v123 offset:4896\n\t"
        "ds_read_b64_tr_b16 a[190:191], v123 offset:5024\n\t"
        "s_waitcnt lgkmcnt(8)\n\t"
        "s_waitcnt lgkmcnt(0)\n\t"
        "s_waitcnt vmcnt(8)\n\t"
        "ds_write_b128 v122, a[192:195] offset:0\n\t"
        "ds_write_b128 v122, a[196:199] offset:1056\n\t"
        "ds_write_b128 v122, a[200:203] offset:2176\n\t"
        "ds_write_b128 v122, a[204:207] offset:3232\n\t"
        "ds_write_b128 v122, a[208:211] offset:4352\n\t"
        "ds_write_b128 v122, a[212:215] offset:5408\n\t"
        "ds_write_b128 v122, a[216:219] offset:6528\n\t"
        "ds_write_b128 v122, a[220:223] offset:7584\n\t"
        "ds_read_b64_tr_b16 a[192:193], v123 offset:0\n\t"
        "ds_read_b64_tr_b16 a[194:195], v123 offset:128\n\t"
        "ds_read_b64_tr_b16 a[196:197], v123 offset:4352\n\t"
        "ds_read_b64_tr_b16 a[198:199], v123 offset:4480\n\t"
        "s_waitcnt lgkmcnt(8)\n\t"
        "ds_read_b64_tr_b16 a[200:201], v123 offset:32\n\t"
        "ds_read_b64_tr_b16 a[202:203], v123 offset:160\n\t"
        "ds_read_b64_tr_b16 a[204:205], v123 offset:4384\n\t"
        "ds_read_b64_tr_b16 a[206:207], v123 offset:4512\n\t"
        "s_waitcnt lgkmcnt(8)\n\t"
        "ds_read_b64_tr_b16 a[208:209], v123 offset:512\n\t"
        "ds_read_b64_tr_b16 a[210:211], v123 offset:640\n\t"
        "ds_read_b64_tr_b16 a[212:213], v123 offset:4864\n\t"
        "ds_read_b64_tr_b16 a[214:215], v123 offset:4992\n\t"
        "s_waitcnt lgkmcnt(8)\n\t"
        "ds_read_b64_tr_b16 a[216:217], v123 offset:544\n\t"
        "ds_read_b64_tr_b16 a[218:219], v123 offset:672\n\t"
        "ds_read_b64_tr_b16 a[220:221], v123 offset:4896\n\t"
        "ds_read_b64_tr_b16 a[222:223], v123 offset:5024\n\t"
        "s_waitcnt lgkmcnt(8)\n\t"
        "s_waitcnt lgkmcnt(0)\n\t"
        "s_waitcnt vmcnt(0)\n\t"
        "ds_write_b128 v122, a[224:227] offset:0\n\t"
        "ds_write_b128 v122, a[228:231] offset:1056\n\t"
        "ds_write_b128 v122, a[232:235] offset:2176\n\t"
        "ds_write_b128 v122, a[236:239] offset:3232\n\t"
        "ds_write_b128 v122, a[240:243] offset:4352\n\t"
        "ds_write_b128 v122, a[244:247] offset:5408\n\t"
        "ds_write_b128 v122, a[248:251] offset:6528\n\t"
        "ds_write_b128 v122, a[252:255] offset:7584\n\t"
        "ds_read_b64_tr_b16 a[224:225], v123 offset:0\n\t"
        "ds_read_b64_tr_b16 a[226:227], v123 offset:128\n\t"
        "ds_read_b64_tr_b16 a[228:229], v123 offset:4352\n\t"
        "ds_read_b64_tr_b16 a[230:231], v123 offset:4480\n\t"
        "s_waitcnt lgkmcnt(8)\n\t"
        "ds_read_b64_tr_b16 a[232:233], v123 offset:32\n\t"
        "ds_read_b64_tr_b16 a[234:235], v123 offset:160\n\t"
        "ds_read_b64_tr_b16 a[236:237], v123 offset:4384\n\t"
        "ds_read_b64_tr_b16 a[238:239], v123 offset:4512\n\t"
        "s_waitcnt lgkmcnt(8)\n\t"
        "ds_read_b64_tr_b16 a[240:241], v123 offset:512\n\t"
        "ds_read_b64_tr_b16 a[242:243], v123 offset:640\n\t"
        "ds_read_b64_tr_b16 a[244:245], v123 offset:4864\n\t"
        "ds_read_b64_tr_b16 a[246:247], v123 offset:4992\n\t"
        "s_waitcnt lgkmcnt(8)\n\t"
        "ds_read_b64_tr_b16 a[248:249], v123 offset:544\n\t"
        "ds_read_b64_tr_b16 a[250:251], v123 offset:672\n\t"
        "ds_read_b64_tr_b16 a[252:253], v123 offset:4896\n\t"
        "ds_read_b64_tr_b16 a[254:255], v123 offset:5024\n\t"
        "s_waitcnt lgkmcnt(8)\n\t"
        "s_mov_b32 s35, 33792\n\t"
        "buffer_load_dword v0, off, s[12:15], s35 sc1\n\t"
        "s_waitcnt lgkmcnt(0)\n\t"
        "s_waitcnt vmcnt(0)\n\t"
        "s_mov_b32 s42, 0\n\t"
        "LB_cal%=:\n\t"
        "v_readfirstlane_b32 s34, v0\n\t"
        "s_nop 0\n\t"
        "s_cmp_lg_u32 s34, 0\n\t"
        "s_cbranch_scc1 LB_calok%=\n\t"
        "s_sleep 4\n\t"
        "s_mov_b32 s35, 33792\n\t"
        "buffer_load_dword v0, off, s[12:15], s35 sc1\n\t"
        "s_waitcnt vmcnt(0)\n\t"
        "s_add_u32 s42, s42, 1\n\t"
        "s_cmp_lt_u32 s42, 0x100000\n\t"
        "s_cbranch_scc1 LB_cal%=\n\t"
        "s_movk_i32 s34, 8\n\t"
        "s_branch LB_die%=\n\t"
        "LB_calok%=:\n\t"
        "v_readfirstlane_b32 s34, v0\n\t"
        "s_nop 0\n\t"
        "s_and_b32 s34, s34, 8192\n\t"
        "s_add_u32 s8, s8, s34\n\t"
        "s_addc_u32 s9, s9, 0\n\t"
        "v_min_u32_e32 v109, -2, v109\n\t"
        "buffer_store_dword v109, v116, s[8:11], 0 offen sc1\n\t"
        "s_memrealtime s[32:33]\n\t"
        "s_waitcnt lgkmcnt(0)\n\t"
        "v_mov_b32_e32 v110, s32\n\t"
        "s_lshl_b32 s34, s29, 8\n\t"
        "s_lshl_b32 s35, s30, 20\n\t"
        "s_or_b32 s54, s34, s35\n\t"
        "s_add_u32 s34, s54, 12589056\n\t"
        "v_mov_b32_e32 v111, s34\n\t"
        "s_barrier\n\t"
        "s_cmp_lg_u32 s43, 0\n\t"
        "s_cbranch_scc1 LB_st%=\n\t"
        "v_mov_b32_e32 v2, 1\n\t"
        "s_mov_b32 s35, 34304\n\t"
        "s_mov_b64 exec, 1\n\t"
        "buffer_atomic_add v2, off, s[12:15], s35 sc0\n\t"
        "s_mov_b64 exec, -1\n\t"
        "s_waitcnt vmcnt(0)\n\t"
        "v_readfirstlane_b32 s34, v2\n\t"
        "s_nop 0\n\t"
        "s_cmp_lg_u32 s34, 95\n\t"
        "s_cbranch_scc1 LB_st%=\n\t"
        "s_memrealtime s[32:33]\n\t"
        "v_mbcnt_lo_u32_b32 v3, -1, 0\n\t"
        "v_lshlrev_b32_e32 v3, 7, v3\n\t"
        "s_waitcnt lgkmcnt(0)\n\t"
        "s_add_u32 s34, s32, 50\n\t"
        "s_or_b32 s34, s34, 1\n\t"
        "v_mov_b32_e32 v2, s34\n\t"
        "s_mov_b32 s35, 36864\n\t"
        "s_mov_b64 exec, 0xffff\n\t"
        "buffer_store_dword v2, v3, s[12:15], s35 offen sc1\n\t"
        "s_mov_b64 exec, -1\n\t"
        "LB_st%=:\n\t"
        "s_mov_b32 s24, 0\n\t"
        "s_mov_b32 s26, 0\n\t"
        "s_mov_b32 s27, 0x80000\n\t"
        "s_mov_b32 s28, 0x180000\n\t"
        "s_mov_b32 s31, 0\n\t"
        "s_mov_b32 s47, 0\n\t"
        "s_mov_b32 s38, 0xff00ff00\n\t"
        "s_mov_b32 s39, 0xff00ff00\n\t"
        "s_mov_b32 s40, 0\n\t"
        "s_mov_b32 s41, -1\n\t"
        "v_mov_b64_e32 v[0:1], 0\n\t"
        "v_mov_b64_e32 v[2:3], 0\n\t"
        "v_mov_b64_e32 v[4:5], 0\n\t"
        "v_mov_b64_e32 v[6:7], 0\n\t"
        "v_mov_b64_e32 v[8:9], 0\n\t"
        "v_mov_b64_e32 v[10:11], 0\n\t"
        "v_mov_b64_e32 v[12:13], 0\n\t"
        "v_mov_b64_e32 v[14:15], 0\n\t"
        "s_mov_b32 s42, 0\n\t"
        "LB_gw%=:\n\t"
        "buffer_load_dword v16, off, s[12:15], s45 sc1\n\t"
        "s_waitcnt vmcnt(0)\n\t"
        "v_readfirstlane_b32 s25, v16\n\t"
        "s_nop 0\n\t"
        "s_cmp_lg_u32 s25, 0\n\t"
        "s_cbranch_scc1 LB_loop%=\n\t"
        "s_sleep 2\n\t"
        "s_add_u32 s42, s42, 1\n\t"
        "s_cmp_lt_u32 s42, 0x100000\n\t"
        "s_cbranch_scc1 LB_gw%=\n\t"
        "s_movk_i32 s34, 9\n\t"
        "s_branch LB_die%=\n\t"
        ".p2align 8\n\t"
        "LB_loop%=:\n\t"
        "s_mov_b32 s42, 0\n\t"
        "s_sub_u32 s35, s25, 1\n\t"
        "LB_wa%=:\n\t"
        "s_memrealtime s[32:33]\n\t"
        "s_waitcnt lgkmcnt(0)\n\t"
        "s_sub_u32 s34, s35, s32\n\t"
        "s_cmpk_lt_u32 s34, 0x2000\n\t"
        "s_cbranch_scc1 LB_wa%=\n\t"
        "LB_rd%=:\n\t"
        "buffer_load_dwordx4 v[16:19], v112, s[8:11], s26 offen offset:0 sc1\n\t"
        "s_nop 9\n\t"
        "buffer_load_dwordx4 v[20:23], v112, s[8:11], s26 offen offset:1024 sc1\n\t"
        "s_nop 9\n\t"
        "buffer_load_dwordx4 v[24:27], v112, s[8:11], s26 offen offset:2048 sc1\n\t"
        "s_nop 9\n\t"
        "buffer_load_dwordx4 v[28:31], v112, s[8:11], s26 offen offset:3072 sc1\n\t"
        "LB_go%=:\n\t"
        "s_waitcnt vmcnt(3)\n\t"
        "v_smfmac_f32_16x16x64_bf16 v[0:3], v[16:19], v[128:135], v106\n\t"
        "v_max3_u32 v108, v16, v17, v18\n\t"
        "v_max_u32_e32 v108, v19, v108\n\t"
        "v_smfmac_f32_16x16x64_bf16 v[4:7], v[16:19], v[136:143], v106\n\t"
        "v_smfmac_f32_16x16x64_bf16 v[8:11], v[16:19], v[144:151], v106\n\t"
        "v_smfmac_f32_16x16x64_bf16 v[12:15], v[16:19], v[152:159], v106\n\t"
        "s_waitcnt vmcnt(2)\n\t"
        "v_smfmac_f32_16x16x64_bf16 v[0:3], v[20:23], v[160:167], v106\n\t"
        "v_max3_u32 v108, v20, v21, v108\n\t"
        "v_max3_u32 v108, v22, v23, v108\n\t"
        "v_smfmac_f32_16x16x64_bf16 v[4:7], v[20:23], v[168:175], v106\n\t"
        "v_smfmac_f32_16x16x64_bf16 v[8:11], v[20:23], v[176:183], v106\n\t"
        "v_smfmac_f32_16x16x64_bf16 v[12:15], v[20:23], v[184:191], v106\n\t"
        "s_waitcnt vmcnt(1)\n\t"
        "v_smfmac_f32_16x16x64_bf16 v[0:3], v[24:27], v[192:199], v106\n\t"
        "v_max3_u32 v108, v24, v25, v108\n\t"
        "v_max3_u32 v108, v26, v27, v108\n\t"
        "v_smfmac_f32_16x16x64_bf16 v[4:7], v[24:27], v[200:207], v106\n\t"
        "v_smfmac_f32_16x16x64_bf16 v[8:11], v[24:27], v[208:215], v106\n\t"
        "v_smfmac_f32_16x16x64_bf16 v[12:15], v[24:27], v[216:223], v106\n\t"
        "s_waitcnt vmcnt(0)\n\t"
        "v_smfmac_f32_16x16x64_bf16 v[0:3], v[28:31], v[224:231], v106\n\t"
        "v_max3_u32 v108, v28, v29, v108\n\t"
        "v_max3_u32 v108, v30, v31, v108\n\t"
        "v_smfmac_f32_16x16x64_bf16 v[4:7], v[28:31], v[232:239], v106\n\t"
        "v_cmp_eq_u32_e32 vcc, -1, v108\n\t"
        "v_smfmac_f32_16x16x64_bf16 v[8:11], v[28:31], v[240:247], v106\n\t"
        "v_smfmac_f32_16x16x64_bf16 v[12:15], v[28:31], v[248:255], v106\n\t"
        "s_cbranch_vccnz LB_rdfail%=\n\t"
        "s_nop 0\n\t"
        "v_add_f32_e32 v0, v0, v1\n\t"
        "v_add_f32_e32 v1, v2, v3\n\t"
        "ds_write_b64 v118, v[0:1] offset:0\n\t"
        "v_add_f32_e32 v4, v4, v5\n\t"
        "v_add_f32_e32 v5, v6, v7\n\t"
        "ds_write_b64 v118, v[4:5] offset:512\n\t"
        "v_add_f32_e32 v8, v8, v9\n\t"
        "v_add_f32_e32 v9, v10, v11\n\t"
        "ds_write_b64 v118, v[8:9] offset:1024\n\t"
        "v_add_f32_e32 v12, v12, v13\n\t"
        "v_add_f32_e32 v13, v14, v15\n\t"
        "ds_write_b64 v118, v[12:13] offset:1536\n\t"
        "buffer_store_dwordx2 v[110:111], v117, s[8:11], s26 offen sc1\n\t"
        "s_waitcnt lgkmcnt(0)\n\t"
        "s_barrier\n\t"
        "ds_read_b128 v[16:19], v119 offset:0\n\t"
        "ds_read_b128 v[20:23], v119 offset:2048\n\t"
        "ds_read_b128 v[24:27], v119 offset:4096\n\t"
        "ds_read_b128 v[28:31], v119 offset:6144\n\t"
        "buffer_load_dwordx4 v[64:67], v124, s[8:11], s28 offen sc1\n\t"
        "s_waitcnt lgkmcnt(2)\n\t"
        "v_pk_add_f32 v[16:17], v[16:17], v[20:21]\n\t"
        "v_pk_add_f32 v[18:19], v[18:19], v[22:23]\n\t"
        "s_waitcnt lgkmcnt(0)\n\t"
        "v_pk_add_f32 v[24:25], v[24:25], v[28:29]\n\t"
        "v_pk_add_f32 v[26:27], v[26:27], v[30:31]\n\t"
        "v_pk_add_f32 v[16:17], v[16:17], v[24:25]\n\t"
        "v_pk_add_f32 v[18:19], v[18:19], v[26:27]\n\t"
        "v_cvt_pk_bf16_f32 v20, v16, v17\n\t"
        "v_cvt_pk_bf16_f32 v21, v18, v19\n\t"
        "buffer_store_dwordx2 v[20:21], v114, s[8:11], s26 offen sc1\n\t"
        "s_cmp_eq_u32 s24, 63\n\t"
        "s_cbranch_scc1 LB_end%=\n\t"
        "s_memrealtime s[52:53]\n\t"
        "v_mov_b64_e32 v[24:25], -1\n\t"
        "v_mov_b64_e32 v[32:33], 0\n\t"
        "v_mov_b64_e32 v[34:35], 0\n\t"
        "v_mov_b64_e32 v[36:37], 0\n\t"
        "v_mov_b64_e32 v[38:39], 0\n\t"
        "v_mov_b64_e32 v[40:41], 0\n\t"
        "v_mov_b64_e32 v[42:43], 0\n\t"
        "v_mov_b64_e32 v[44:45], 0\n\t"
        "v_mov_b64_e32 v[46:47], 0\n\t"
        "v_mov_b64_e32 v[0:1], 0\n\t"
        "v_mov_b64_e32 v[2:3], 0\n\t"
        "v_mov_b64_e32 v[4:5], 0\n\t"
        "v_mov_b64_e32 v[6:7], 0\n\t"
        "v_mov_b64_e32 v[8:9], 0\n\t"
        "v_mov_b64_e32 v[10:11], 0\n\t"
        "v_mov_b64_e32 v[12:13], 0\n\t"
        "v_mov_b64_e32 v[14:15], 0\n\t"
        "s_add_u32 s46, s26, s69\n\t"
        "s_add_u32 s48, s26, s70\n\t"
        "s_add_u32 s73, s26, s71\n\t"
        "s_add_u32 s74, s26, s72\n\t"
        "s_sleep 1\n\t"
        "buffer_load_dwordx2 v[104:105], v113, s[8:11], s26 offen sc1\n\t"
        "buffer_store_dword v24, v116, s[8:11], s28 offen sc1\n\t"
        "s_xor_b32 s34, s26, 0x100000\n\t"
        "buffer_store_dwordx2 v[24:25], v114, s[8:11], s34 offen sc1\n\t"
        "s_add_u32 s66, s49, s43\n\t"
        "s_cmp_lg_u32 s66, 32\n\t"
        "s_cbranch_scc1 LB_norep%=\n\t"
        "s_waitcnt vmcnt(4)\n\t"
        "s_sub_u32 s66, s24, 1\n\t"
        "v_lshrrev_b32_e32 v68, 16, v64\n\t"
        "v_cmp_eq_u32_e32 vcc, s66, v68\n\t"
        "v_subrev_u32_e32 v64, s59, v64\n\t"
        "v_bfe_i32 v64, v64, 0, 16\n\t"
        "v_cndmask_b32_e32 v64, 0, v64, vcc\n\t"
        "v_lshrrev_b32_e32 v68, 16, v65\n\t"
        "v_cmp_eq_u32_e32 vcc, s66, v68\n\t"
        "v_subrev_u32_e32 v65, s59, v65\n\t"
        "v_bfe_i32 v65, v65, 0, 16\n\t"
        "v_cndmask_b32_e32 v65, 0, v65, vcc\n\t"
        "v_lshrrev_b32_e32 v68, 16, v66\n\t"
        "v_cmp_eq_u32_e32 vcc, s66, v68\n\t"
        "v_subrev_u32_e32 v66, s59, v66\n\t"
        "v_bfe_i32 v66, v66, 0, 16\n\t"
        "v_cndmask_b32_e32 v66, 0, v66, vcc\n\t"
        "v_lshrrev_b32_e32 v68, 16, v67\n\t"
        "v_cmp_eq_u32_e32 vcc, s66, v68\n\t"
        "v_subrev_u32_e32 v67, s59, v67\n\t"
        "v_bfe_i32 v67, v67, 0, 16\n\t"
        "v_cndmask_b32_e32 v67, 0, v67, vcc\n\t"
        "v_max3_i32 v64, v64, v65, v66\n\t"
        "v_max_i32_e32 v64, v64, v67\n\t"
        "s_nop 1\n\t"
        "v_max_i32_dpp v64, v64, v64 quad_perm:[1,0,3,2] row_mask:0xf bank_mask:0xf\n\t"
        "s_nop 1\n\t"
        "v_max_i32_dpp v64, v64, v64 quad_perm:[2,3,0,1] row_mask:0xf bank_mask:0xf\n\t"
        "s_nop 1\n\t"
        "v_max_i32_dpp v64, v64, v64 row_ror:4 row_mask:0xf bank_mask:0xf\n\t"
        "s_nop 1\n\t"
        "v_max_i32_dpp v64, v64, v64 row_ror:8 row_mask:0xf bank_mask:0xf\n\t"
        "v_mov_b32_e32 v65, v64\n\t"
        "s_nop 1\n\t"
        "v_permlane16_swap_b32_e32 v64, v65\n\t"
        "v_max_i32_e32 v64, v64, v65\n\t"
        "v_mov_b32_e32 v65, v64\n\t"
        "s_nop 1\n\t"
        "v_permlane32_swap_b32_e32 v64, v65\n\t"
        "v_max_i32_e32 v64, v64, v65\n\t"
        "s_nop 0\n\t"
        "v_readfirstlane_b32 s64, v64\n\t"
        "LB_norep%=:\n\t"
        "s_waitcnt vmcnt(2) lgkmcnt(0)\n\t"
        "s_mov_b32 s61, s29\n\t"
        "s_mov_b32 s62, s30\n\t"
        "s_mov_b32 s63, s31\n\t"
        "v_and_b32_e32 v30, 0xff, v105\n\t"
        "v_cmp_ne_u32_e32 vcc, s24, v30\n\t"
        "s_nop 0\n\t"
        "s_cbranch_vccnz LB_stre%=\n\t"
        "LB_stok%=:\n\t"
        "v_subrev_u32_e32 v104, s25, v104\n\t"
        "s_nop 1\n\t"
        "v_max_i32_dpp v104, v104, v104 quad_perm:[1,0,3,2] row_mask:0xf bank_mask:0xf\n\t"
        "s_nop 1\n\t"
        "v_max_i32_dpp v104, v104, v104 quad_perm:[2,3,0,1] row_mask:0xf bank_mask:0xf\n\t"
        "s_nop 1\n\t"
        "v_max_i32_dpp v104, v104, v104 row_ror:4 row_mask:0xf bank_mask:0xf\n\t"
        "s_nop 1\n\t"
        "v_max_i32_dpp v104, v104, v104 row_ror:8 row_mask:0xf bank_mask:0xf\n\t"
        "s_nop 1\n\t"
        "v_max_i32_dpp v104, v104, v104 row_bcast:15 row_mask:0xa bank_mask:0xf\n\t"
        "s_nop 1\n\t"
        "v_max_i32_dpp v104, v104, v104 row_bcast:31 row_mask:0xc bank_mask:0xf\n\t"
        "v_readfirstlane_b32 s35, v105\n\t"
        "v_readlane_b32 s36, v104, 63\n\t"
        "s_bfe_u32 s29, s35, 0xc0008\n\t"
        "s_lshr_b32 s30, s35, 20\n\t"
        "s_add_u32 s35, s31, s50\n\t"
        "s_mul_i32 s37, s50, 3\n\t"
        "s_min_u32 s35, s35, s37\n\t"
        "s_add_i32 s34, s36, s51\n\t"
        "s_cmp_gt_i32 s34, 0\n\t"
        "s_cselect_b32 s31, s35, 0\n\t"
        "s_cselect_b32 s34, s34, 0\n\t"
        "s_add_u32 s34, s34, s31\n\t"
        "s_add_u32 s37, s25, s34\n\t"
        "s_add_u32 s45, s37, s29\n\t"
        "s_add_u32 s25, s45, s30\n\t"
        "s_add_u32 s66, s49, s43\n\t"
        "s_cmp_lg_u32 s66, 32\n\t"
        "s_cbranch_scc1 LB_noctl%=\n\t"
        "s_cmp_eq_u32 s24, 0\n\t"
        "s_cbranch_scc1 LB_ctl0%=\n\t"
        "s_cmp_lt_u32 s24, 6\n\t"
        "s_cselect_b32 s68, 32, 8\n\t"
        "s_add_i32 s65, s36, s62\n\t"
        "s_cmp_eq_u32 s24, 1\n\t"
        "s_cselect_b32 s34, 6, 0\n\t"
        "s_cselect_b32 s35, 3, 0\n\t"
        "s_sub_u32 s64, s64, s34\n\t"
        "s_sub_u32 s65, s65, s35\n\t"
        "s_lshl_b32 s66, s65, 1\n\t"
        "s_sub_i32 s66, s66, s56\n\t"
        "s_cmp_gt_i32 s66, 56\n\t"
        "s_cbranch_scc1 LB_c1%=\n\t"
        "s_lshl_b32 s66, s65, 1\n\t"
        "s_add_u32 s34, s56, s68\n\t"
        "s_min_i32 s34, s34, s66\n\t"
        "s_sub_u32 s35, s56, s66\n\t"
        "s_ashr_i32 s35, s35, 1\n\t"
        "s_max_i32 s35, s35, 1\n\t"
        "s_sub_u32 s35, s56, s35\n\t"
        "s_max_i32 s56, s34, s35\n\t"
        "s_branch LB_c2%=\n\t"
        "LB_c1%=:\n\t"
        "s_add_u32 s56, s56, 2\n\t"
        "s_add_i32 s66, s64, s57\n\t"
        "s_cmp_gt_i32 s66, s61\n\t"
        "s_cbranch_scc1 LB_c2%=\n\t"
        "s_add_u32 s57, s57, 4\n\t"
        "s_min_u32 s57, s57, 23\n\t"
        "LB_c2%=:\n\t"
        "s_cmp_lg_u32 s63, 0\n\t"
        "s_cbranch_scc1 LB_c4%=\n\t"
        "s_lshl_b32 s66, s64, 1\n\t"
        "s_sub_i32 s66, s66, s55\n\t"
        "s_cmp_gt_i32 s66, 56\n\t"
        "s_cbranch_scc1 LB_c3%=\n\t"
        "s_lshl_b32 s66, s64, 1\n\t"
        "s_add_u32 s34, s55, s68\n\t"
        "s_min_i32 s34, s34, s66\n\t"
        "s_sub_u32 s35, s55, s66\n\t"
        "s_ashr_i32 s35, s35, 1\n\t"
        "s_max_i32 s35, s35, 1\n\t"
        "s_sub_u32 s35, s55, s35\n\t"
        "s_max_i32 s55, s34, s35\n\t"
        "s_branch LB_c4%=\n\t"
        "LB_c3%=:\n\t"
        "s_add_u32 s55, s55, 2\n\t"
        "s_cmp_lt_i32 s60, s58\n\t"
        "s_cbranch_scc1 LB_c4%=\n\t"
        "s_add_u32 s66, s58, 8\n\t"
        "s_cmp_gt_i32 s60, s66\n\t"
        "s_cbranch_scc1 LB_c4%=\n\t"
        "s_add_u32 s58, s58, 4\n\t"
        "s_min_u32 s58, s58, 20\n\t"
        "LB_c4%=:\n\t"
        "s_and_b32 s66, s24, 3\n\t"
        "s_cmp_eq_u32 s66, 0\n\t"
        "s_cselect_b32 s66, 1, 0\n\t"
        "s_cmp_lt_u32 s24, 30\n\t"
        "s_cselect_b32 s34, 7, 7\n\t"
        "s_cselect_b32 s35, 4, 4\n\t"
        "s_sub_u32 s57, s57, s66\n\t"
        "s_max_i32 s57, s57, s34\n\t"
        "s_sub_u32 s58, s58, s66\n\t"
        "s_max_i32 s58, s58, s35\n\t"
        "s_add_u32 s34, s55, 1\n\t"
        "s_lshr_b32 s34, s34, 1\n\t"
        "s_add_u32 s34, s34, s57\n\t"
        "s_min_u32 s34, s34, 1000\n\t"
        "s_lshl_b32 s34, s34, 8\n\t"
        "s_add_u32 s35, s56, 1\n\t"
        "s_lshr_b32 s35, s35, 1\n\t"
        "s_add_u32 s35, s35, s58\n\t"
        "s_min_u32 s35, s35, 1000\n\t"
        "s_lshl_b32 s35, s35, 20\n\t"
        "s_or_b32 s54, s34, s35\n\t"
        "s_branch LB_ctl1%=\n\t"
        "LB_ctl0%=:\n\t"
        "s_movk_i32 s57, 7\n\t"
        "s_movk_i32 s58, 4\n\t"
        "s_sub_u32 s55, s29, 31\n\t"
        "s_lshl_b32 s55, s55, 1\n\t"
        "s_sub_u32 s56, s30, 16\n\t"
        "s_lshl_b32 s56, s56, 1\n\t"
        "LB_ctl1%=:\n\t"
        "s_mov_b32 s59, s37\n\t"
        "s_sub_u32 s60, 0, s36\n\t"
        "LB_noctl%=:\n\t"
        "s_cmp_eq_u32 s24, 0\n\t"
        "s_cselect_b32 s34, 1537, 1\n\t"
        "s_add_u32 s34, s34, s24\n\t"
        "s_add_u32 s34, s34, s54\n\t"
        "v_mov_b32_e32 v111, s34\n\t"
        "s_mov_b32 s42, 0\n\t"
        "s_sub_u32 s35, s45, 1\n\t"
        "LB_wo%=:\n\t"
        "s_memrealtime s[32:33]\n\t"
        "s_waitcnt lgkmcnt(0)\n\t"
        "s_sub_u32 s34, s35, s32\n\t"
        "s_cmpk_lt_u32 s34, 0x2000\n\t"
        "s_cbranch_scc1 LB_wo%=\n\t"
        "LB_ro%=:\n\t"
        "buffer_load_dwordx4 v[64:67], v115, s[8:11], s46 offen offset:0 sc1\n\t"
        "s_nop 12\n\t"
        "buffer_load_dwordx4 v[68:71], v115, s[8:11], s46 offen offset:1024 sc1\n\t"
        "s_nop 12\n\t"
        "buffer_load_dwordx4 v[72:75], v115, s[8:11], s48 offen offset:0 sc1\n\t"
        "s_nop 12\n\t"
        "buffer_load_dwordx4 v[76:79], v115, s[8:11], s48 offen offset:1024 sc1\n\t"
        "s_nop 12\n\t"
        "buffer_load_dwordx4 v[80:83], v115, s[8:11], s73 offen offset:0 sc1\n\t"
        "s_nop 12\n\t"
        "buffer_load_dwordx4 v[84:87], v115, s[8:11], s73 offen offset:1024 sc1\n\t"
        "s_nop 12\n\t"
        "buffer_load_dwordx4 v[88:91], v115, s[8:11], s74 offen offset:0 sc1\n\t"
        "s_nop 12\n\t"
        "buffer_load_dwordx4 v[92:95], v115, s[8:11], s74 offen offset:1024 sc1\n\t"
        "LB_go2%=:\n\t"
        "s_waitcnt vmcnt(7)\n\t"
        "v_smfmac_f32_16x16x64_bf16 v[32:35], v[64:67], a[0:7], v106\n\t"
        "v_max3_u32 v108, v64, v65, v66\n\t"
        "v_max_u32_e32 v108, v67, v108\n\t"
        "v_smfmac_f32_16x16x64_bf16 v[40:43], v[64:67], a[16:23], v106\n\t"
        "v_smfmac_f32_16x16x64_bf16 v[36:39], v[64:67], a[8:15], v106\n\t"
        "v_smfmac_f32_16x16x64_bf16 v[44:47], v[64:67], a[24:31], v106\n\t"
        "s_waitcnt vmcnt(6)\n\t"
        "v_smfmac_f32_16x16x64_bf16 v[32:35], v[68:71], a[32:39], v106\n\t"
        "v_max3_u32 v108, v68, v69, v108\n\t"
        "v_max3_u32 v108, v70, v71, v108\n\t"
        "v_smfmac_f32_16x16x64_bf16 v[40:43], v[68:71], a[48:55], v106\n\t"
        "v_smfmac_f32_16x16x64_bf16 v[36:39], v[68:71], a[40:47], v106\n\t"
        "v_smfmac_f32_16x16x64_bf16 v[44:47], v[68:71], a[56:63], v106\n\t"
        "s_waitcnt vmcnt(5)\n\t"
        "v_smfmac_f32_16x16x64_bf16 v[32:35], v[72:75], a[64:71], v106\n\t"
        "v_max3_u32 v108, v72, v73, v108\n\t"
        "v_max3_u32 v108, v74, v75, v108\n\t"
        "v_smfmac_f32_16x16x64_bf16 v[40:43], v[72:75], a[80:87], v106\n\t"
        "v_smfmac_f32_16x16x64_bf16 v[36:39], v[72:75], a[72:79], v106\n\t"
        "v_smfmac_f32_16x16x64_bf16 v[44:47], v[72:75], a[88:95], v106\n\t"
        "s_waitcnt vmcnt(4)\n\t"
        "v_smfmac_f32_16x16x64_bf16 v[32:35], v[76:79], a[96:103], v106\n\t"
        "v_max3_u32 v108, v76, v77, v108\n\t"
        "v_max3_u32 v108, v78, v79, v108\n\t"
        "v_smfmac_f32_16x16x64_bf16 v[40:43], v[76:79], a[112:119], v106\n\t"
        "v_smfmac_f32_16x16x64_bf16 v[36:39], v[76:79], a[104:111], v106\n\t"
        "v_smfmac_f32_16x16x64_bf16 v[44:47], v[76:79], a[120:127], v106\n\t"
        "s_waitcnt vmcnt(3)\n\t"
        "v_smfmac_f32_16x16x64_bf16 v[32:35], v[80:83], a[128:135], v106\n\t"
        "v_max3_u32 v108, v80, v81, v108\n\t"
        "v_max3_u32 v108, v82, v83, v108\n\t"
        "v_smfmac_f32_16x16x64_bf16 v[40:43], v[80:83], a[144:151], v106\n\t"
        "v_smfmac_f32_16x16x64_bf16 v[36:39], v[80:83], a[136:143], v106\n\t"
        "v_smfmac_f32_16x16x64_bf16 v[44:47], v[80:83], a[152:159], v106\n\t"
        "s_waitcnt vmcnt(2)\n\t"
        "v_smfmac_f32_16x16x64_bf16 v[32:35], v[84:87], a[160:167], v106\n\t"
        "v_max3_u32 v108, v84, v85, v108\n\t"
        "v_max3_u32 v108, v86, v87, v108\n\t"
        "v_smfmac_f32_16x16x64_bf16 v[40:43], v[84:87], a[176:183], v106\n\t"
        "v_smfmac_f32_16x16x64_bf16 v[36:39], v[84:87], a[168:175], v106\n\t"
        "v_smfmac_f32_16x16x64_bf16 v[44:47], v[84:87], a[184:191], v106\n\t"
        "s_waitcnt vmcnt(1)\n\t"
        "v_smfmac_f32_16x16x64_bf16 v[32:35], v[88:91], a[192:199], v106\n\t"
        "v_max3_u32 v108, v88, v89, v108\n\t"
        "v_max3_u32 v108, v90, v91, v108\n\t"
        "v_smfmac_f32_16x16x64_bf16 v[40:43], v[88:91], a[208:215], v106\n\t"
        "v_smfmac_f32_16x16x64_bf16 v[36:39], v[88:91], a[200:207], v106\n\t"
        "v_smfmac_f32_16x16x64_bf16 v[44:47], v[88:91], a[216:223], v106\n\t"
        "s_waitcnt vmcnt(0)\n\t"
        "v_smfmac_f32_16x16x64_bf16 v[32:35], v[92:95], a[224:231], v106\n\t"
        "v_max3_u32 v108, v92, v93, v108\n\t"
        "v_max3_u32 v108, v94, v95, v108\n\t"
        "v_smfmac_f32_16x16x64_bf16 v[40:43], v[92:95], a[240:247], v106\n\t"
        "v_cmp_eq_u32_e32 vcc, -1, v108\n\t"
        "v_smfmac_f32_16x16x64_bf16 v[36:39], v[92:95], a[232:239], v106\n\t"
        "v_smfmac_f32_16x16x64_bf16 v[44:47], v[92:95], a[248:255], v106\n\t"
        "s_cbranch_vccnz LB_rofail%=\n\t"
        "s_nop 0\n\t"
        "v_add_f32_e32 v32, v32, v33\n\t"
        "s_nop 1\n\t"
        "v_add_f32_e32 v40, v42, v43\n\t"
        "v_add_f32_e32 v32, v32, v40\n\t"
        "v_add_f32_e32 v36, v36, v37\n\t"
        "v_add_f32_e32 v44, v46, v47\n\t"
        "v_add_f32_e32 v36, v36, v44\n\t"
        "ds_write2_b32 v120, v32, v36 offset0:0 offset1:64\n\t"
        "s_waitcnt lgkmcnt(0)\n\t"
        "s_barrier\n\t"
        "ds_read_b64 v[64:65], v121 offset:0\n\t"
        "ds_read_b64 v[66:67], v121 offset:512\n\t"
        "ds_read_b64 v[68:69], v121 offset:1024\n\t"
        "ds_read_b64 v[70:71], v121 offset:1536\n\t"
        "s_waitcnt lgkmcnt(2)\n\t"
        "v_pk_add_f32 v[64:65], v[64:65], v[66:67]\n\t"
        "s_waitcnt lgkmcnt(0)\n\t"
        "v_pk_add_f32 v[68:69], v[68:69], v[70:71]\n\t"
        "v_pk_add_f32 v[64:65], v[64:65], v[68:69]\n\t"
        "v_cvt_pk_bf16_f32 v64, v64, v65\n\t"
        "buffer_store_dword v64, v116, s[8:11], s27 offen sc1\n\t"
        "s_memrealtime s[32:33]\n\t"
        "s_waitcnt lgkmcnt(0)\n\t"
        "v_mov_b32_e32 v110, s32\n\t"
        "s_add_u32 s24, s24, 1\n\t"
        "s_mov_b32 s28, s26\n\t"
        "s_mov_b32 s26, s27\n\t"
        "s_add_u32 s27, s27, 0x80000\n\t"
        "s_and_b32 s27, s27, 0x1fffff\n\t"
        "s_branch LB_loop%=\n\t"
        "LB_end%=:\n\t"
        "s_endpgm\n\t"
        "LB_stre%=:\n\t"
        "s_mov_b32 s42, 0\n\t"
        "LB_str2%=:\n\t"
        "buffer_load_dwordx2 v[104:105], v113, s[8:11], s26 offen sc1\n\t"
        "s_waitcnt vmcnt(0)\n\t"
        "v_and_b32_e32 v30, 0xff, v105\n\t"
        "v_cmp_ne_u32_e32 vcc, s24, v30\n\t"
        "s_nop 0\n\t"
        "s_cbranch_vccz LB_stok%=\n\t"
        "s_add_u32 s42, s42, 1\n\t"
        "s_cmp_lt_u32 s42, 0x100000\n\t"
        "s_cbranch_scc1 LB_str2%=\n\t"
        "s_movk_i32 s34, 5\n\t"
        "s_branch LB_die%=\n\t"
        "LB_rdfail%=:\n\t"
        "s_or_b32 s47, s47, 1\n\t"
        "buffer_load_dwordx4 v[16:19], v112, s[8:11], s26 offen offset:0 sc1\n\t"
        "buffer_load_dwordx4 v[20:23], v112, s[8:11], s26 offen offset:1024 sc1\n\t"
        "buffer_load_dwordx4 v[24:27], v112, s[8:11], s26 offen offset:2048 sc1\n\t"
        "buffer_load_dwordx4 v[28:31], v112, s[8:11], s26 offen offset:3072 sc1\n\t"
        "s_nop 7\n\t"
        "v_mov_b64_e32 v[0:1], 0\n\t"
        "v_mov_b64_e32 v[2:3], 0\n\t"
        "v_mov_b64_e32 v[4:5], 0\n\t"
        "v_mov_b64_e32 v[6:7], 0\n\t"
        "v_mov_b64_e32 v[8:9], 0\n\t"
        "v_mov_b64_e32 v[10:11], 0\n\t"
        "v_mov_b64_e32 v[12:13], 0\n\t"
        "v_mov_b64_e32 v[14:15], 0\n\t"
        "s_branch LB_rdre%=w\n\t"
        "LB_rdre%=:\n\t"
        "buffer_load_dwordx4 v[16:19], v112, s[8:11], s26 offen offset:0 sc1\n\t"
        "buffer_load_dwordx4 v[20:23], v112, s[8:11], s26 offen offset:1024 sc1\n\t"
        "buffer_load_dwordx4 v[24:27], v112, s[8:11], s26 offen offset:2048 sc1\n\t"
        "buffer_load_dwordx4 v[28:31], v112, s[8:11], s26 offen offset:3072 sc1\n\t"
        "LB_rdre%=w:\n\t"
        "s_waitcnt vmcnt(0) lgkmcnt(0)\n\t"
        "v_max3_u32 v108, v16, v17, v18\n\t"
        "v_max3_u32 v108, v108, v19, v20\n\t"
        "v_max3_u32 v108, v108, v21, v22\n\t"
        "v_max3_u32 v108, v108, v23, v24\n\t"
        "v_max3_u32 v108, v108, v25, v26\n\t"
        "v_max3_u32 v108, v108, v27, v28\n\t"
        "v_max3_u32 v108, v108, v29, v30\n\t"
        "v_max_u32_e32 v108, v108, v31\n\t"
        "v_cmp_eq_u32_e32 vcc, -1, v108\n\t"
        "s_nop 0\n\t"
        "s_cbranch_vccz LB_go%=\n\t"
        "s_add_u32 s42, s42, 1\n\t"
        "s_cmp_lt_u32 s42, 0x100000\n\t"
        "s_cbranch_scc1 LB_rdre%=\n\t"
        "s_movk_i32 s34, 3\n\t"
        "s_branch LB_die%=\n\t"
        "LB_rofail%=:\n\t"
        "s_or_b32 s47, s47, 4\n\t"
        "buffer_load_dwordx4 v[64:67], v115, s[8:11], s46 offen offset:0 sc1\n\t"
        "buffer_load_dwordx4 v[68:71], v115, s[8:11], s46 offen offset:1024 sc1\n\t"
        "buffer_load_dwordx4 v[72:75], v115, s[8:11], s48 offen offset:0 sc1\n\t"
        "buffer_load_dwordx4 v[76:79], v115, s[8:11], s48 offen offset:1024 sc1\n\t"
        "buffer_load_dwordx4 v[80:83], v115, s[8:11], s73 offen offset:0 sc1\n\t"
        "buffer_load_dwordx4 v[84:87], v115, s[8:11], s73 offen offset:1024 sc1\n\t"
        "buffer_load_dwordx4 v[88:91], v115, s[8:11], s74 offen offset:0 sc1\n\t"
        "buffer_load_dwordx4 v[92:95], v115, s[8:11], s74 offen offset:1024 sc1\n\t"
        "s_nop 7\n\t"
        "v_mov_b64_e32 v[32:33], 0\n\t"
        "v_mov_b64_e32 v[34:35], 0\n\t"
        "v_mov_b64_e32 v[36:37], 0\n\t"
        "v_mov_b64_e32 v[38:39], 0\n\t"
        "v_mov_b64_e32 v[40:41], 0\n\t"
        "v_mov_b64_e32 v[42:43], 0\n\t"
        "v_mov_b64_e32 v[44:45], 0\n\t"
        "v_mov_b64_e32 v[46:47], 0\n\t"
        "s_branch LB_rore%=w\n\t"
        "LB_rore%=:\n\t"
        "buffer_load_dwordx4 v[64:67], v115, s[8:11], s46 offen offset:0 sc1\n\t"
        "buffer_load_dwordx4 v[68:71], v115, s[8:11], s46 offen offset:1024 sc1\n\t"
        "buffer_load_dwordx4 v[72:75], v115, s[8:11], s48 offen offset:0 sc1\n\t"
        "buffer_load_dwordx4 v[76:79], v115, s[8:11], s48 offen offset:1024 sc1\n\t"
        "buffer_load_dwordx4 v[80:83], v115, s[8:11], s73 offen offset:0 sc1\n\t"
        "buffer_load_dwordx4 v[84:87], v115, s[8:11], s73 offen offset:1024 sc1\n\t"
        "buffer_load_dwordx4 v[88:91], v115, s[8:11], s74 offen offset:0 sc1\n\t"
        "buffer_load_dwordx4 v[92:95], v115, s[8:11], s74 offen offset:1024 sc1\n\t"
        "LB_rore%=w:\n\t"
        "s_waitcnt vmcnt(0) lgkmcnt(0)\n\t"
        "v_max3_u32 v108, v64, v65, v66\n\t"
        "v_max3_u32 v108, v108, v67, v68\n\t"
        "v_max3_u32 v108, v108, v69, v70\n\t"
        "v_max3_u32 v108, v108, v71, v72\n\t"
        "v_max3_u32 v108, v108, v73, v74\n\t"
        "v_max3_u32 v108, v108, v75, v76\n\t"
        "v_max3_u32 v108, v108, v77, v78\n\t"
        "v_max3_u32 v108, v108, v79, v80\n\t"
        "v_max3_u32 v108, v108, v81, v82\n\t"
        "v_max3_u32 v108, v108, v83, v84\n\t"
        "v_max3_u32 v108, v108, v85, v86\n\t"
        "v_max3_u32 v108, v108, v87, v88\n\t"
        "v_max3_u32 v108, v108, v89, v90\n\t"
        "v_max3_u32 v108, v108, v91, v92\n\t"
        "v_max3_u32 v108, v108, v93, v94\n\t"
        "v_max_u32_e32 v108, v108, v95\n\t"
        "v_cmp_eq_u32_e32 vcc, -1, v108\n\t"
        "s_nop 0\n\t"
        "s_cbranch_vccz LB_go2%=\n\t"
        "s_add_u32 s42, s42, 1\n\t"
        "s_cmp_lt_u32 s42, 0x100000\n\t"
        "s_cbranch_scc1 LB_rore%=\n\t"
        "s_movk_i32 s34, 4\n\t"
        "s_branch LB_die%=\n\t"
        "LB_die%=:\n\t"
        "v_mov_b32_e32 v30, s34\n\t"
        "s_mov_b32 s35, 33024\n\t"
        "s_mov_b64 exec, 1\n\t"
        "buffer_store_dword v30, off, s[12:15], s35 sc1\n\t"
        "s_endpgm\n\t"
        ".Lk_end_B:\n\t"
        :: REC_S_IN, "{v96}"(g_v), "{v97}"(g_o), "{v98}"(wb0), "{v99}"(wb1), "{v100}"(rb0), "{v101}"(rb0 ^ 32u), "{v102}"(rb0 ^ 64u), "{v103}"(rb0 ^ 96u),
           "{v106}"((l & 1) ? 0xEEEEEEEEu : 0x44444444u), "{v[108:109]}"(m0p), "{s68}"(rot),
           "{v112}"(a_mem), "{v113}"((24 + cp) * 16384 + l * ST_P), "{v114}"(a_vmw), "{v115}"(a_or), "{v116}"(a_mw), "{v117}"(a_sw),
           "{v118}"(lb + w * 2048 + (g >> 1) * 256 + n * 16 + (g & 1) * 8), "{v119}"(lb + (w & 1) * 1024 + l * 16), "{v120}"(lb + LB_R + w * 512 + l * 4),
           "{v121}"(lb + LB_R + l * 8), "{v122}"(stg + l * 16), "{v123}"(o_r), "{v124}"(rb + w == 32 ? 28 * 16384 + (l & 31) * 128 : OOB)
        : "memory", "vcc", "scc", "v0", "v255", "a0", "a255", "s101");
    __builtin_unreachable();
}
__device__ __forceinline__ void rec_helper(const Params& p, rsrc_t r, const int wv, const int hi) {
    const int tid = otid(), l = tid & 63, xq = hi >> 3;
    const rsrc_t rh = __builtin_amdgcn_make_buffer_rsrc(p.ws + hx_base(p.ws), 0, HX_BYTES, 0x00020000);
    const v4u ones = {0xFFFFFFFFu, 0xFFFFFFFFu, 0xFFFFFFFFu, 0xFFFFFFFFu};
    const bool meas = (hi & 7) < 2;
    if (meas) {
        const unsigned ca = l >= 8 ? OOB : (31 + wv * 32) * 16384 + hi * 256 + l * 16, tend = rtnow() + CAL_T;
        unsigned best0 = ~0u, best1 = ~0u;
#pragma unroll 1
        for (int i = 0; i < 64 && (i < 3 || (int)(rtnow() - tend) < 0); ++i) {
            const unsigned t0 = (unsigned)__builtin_amdgcn_s_memtime(); __builtin_amdgcn_s_waitcnt(0);
            st16(rh, ca, ones); DRAIN();
            const unsigned t1 = (unsigned)__builtin_amdgcn_s_memtime(); __builtin_amdgcn_s_waitcnt(0);
            st16(rh, ca + 8192, ones); DRAIN();
            const unsigned t2 = (unsigned)__builtin_amdgcn_s_memtime(); __builtin_amdgcn_s_waitcnt(0);
            best0 = min(best0, t1 - t0); best1 = min(best1, t2 - t1); }
        if (l < 2) __hip_atomic_fetch_max((unsigned*)(p.ws + F_CAL + 128) + l, 65535u - min(l ? best1 : best0, 65535u), __ATOMIC_RELAXED, __HIP_MEMORY_SCOPE_AGENT);
    }
    unsigned d = 0u;
    auto touch = [&](int z0, int z1) {
#pragma unroll 1
        for (int z = z0 + ((hi & 7) - 2) * 4 + wv; z < z1; z += 24) { const bf16_t* q;
            if (z < 48) q = ((z & 1) ? p.Wk : p.Wq) + (size_t)(((((z & 1) * 2 + 1) * 4 + 4 + (z >> 2)) & 15) * 64 + l) * 4096 + (xq * 8 + 1 + (z & 2) * 2) * 128 + 64;
            else if (z < 112) { const int y = z - 48; q = p.Wo + (size_t)(((y & 3) * 8 + (y >> 3)) * 128 + ((y >> 2) & 1) * 64 + l) * 1024 + (xq & 1) * 512 + 192; }
            else { const int s7 = (z - 112) % 7, y = (z - 112) / 7; q = p.Wo + (size_t)(((y & 3) * 8 + 8 - T2U + (y >> 3)) * 128 + ((y >> 2) & 1) * 64 + l) * 1024 + (xq & 1) * 512 + (s7 + (s7 >= 3)) * 64; }
            asm volatile("global_load_dword %0, %1, off" : "+v"(d) : "v"(q) : "memory"); } };
    if (!meas) touch(0, TPRE);
    st16(r, tid < 64 ? F_KVQ + hi * 1024 + tid * 16 : tid < 72 ? F_PROG + hi * 128 + (tid - 64) * 16 : tid < 80 ? F_CVY + hi * 128 + (tid - 72) * 16 : OOB, (v4u){0u, 0u, 0u, 0u});
#pragma unroll
    for (int i = 0; i < 2; ++i) st16(r, W_OX + 63 * 262144 + hi * 8192 + i * 4096 + tid * 16, ones);
#pragma unroll
    for (int i = 0; i < 16; ++i) st16(rh, hi * 65536 + i * 4096 + tid * 16, ones);
    asm volatile("s_waitcnt vmcnt(0)" : "+v"(d) :: "memory");
    __syncthreads();
    if (tid == 0 && __hip_atomic_fetch_add((unsigned*)(p.ws + F_CAL + 256), 1u, __ATOMIC_RELAXED, __HIP_MEMORY_SCOPE_AGENT) == 31u) {
        const v2u c = ld8(r, F_CAL + 128); st4(r, F_CAL, c.x < c.y ? 8193u : 1u); }
    if (!meas) touch(TPRE, 112 + 56 * T2U);
    asm volatile("s_waitcnt vmcnt(0)" : "+v"(d) :: "memory");
}
using f32x16 = __attribute__((ext_vector_type(16))) float;
constexpr int CV_RP = 80;
constexpr int CV_U = 96 * CV_RP;
constexpr int CV_KP = 4416;
constexpr int CV_K0 = 8 * CV_U;
__device__ __forceinline__ float blo(unsigned x) { return __uint_as_float(x << 16); }
__device__ __forceinline__ float bhi(unsigned x) { return __uint_as_float(x & 0xFFFF0000u); }
#define NAP() do { __builtin_amdgcn_sched_barrier(0); __builtin_amdgcn_s_sleep(XSLP); __builtin_amdgcn_sched_barrier(0); } while (0)
template <int NT>
__device__ __forceinline__ void gate_block(const Params& p, rsrc_t r, const int hi, const int wv) {
    const int l = otid() & 63, t0 = (hi * 4 + wv) * NT;
    v4u xg[NT][2], gw[2];
#pragma unroll
    for (int q = 0; q < 2; ++q) gw[q] = *(const v4u*)(p.gate_w + q * 512 + l * 8);
#pragma unroll
    for (int i = 0; i < NT; ++i)
#pragma unroll
        for (int q = 0; q < 2; ++q) { xg[i][q] = *(const v4u*)(p.Aq + (size_t)(t0 + i) * D + q * 512 + l * 8); NAP(); }
    float mine = 0.f;
#pragma unroll
    for (int i = 0; i < NT; ++i) { float acc = 0.f;
#pragma unroll
        for (int q = 0; q < 2; ++q)
#pragma unroll
            for (int e = 0; e < 4; ++e) { const unsigned pr = pack2(blo(xg[i][q][e]) * blo(gw[q][e]), bhi(xg[i][q][e]) * bhi(gw[q][e])); acc += blo(pr); acc += bhi(pr); }
        for (int o = 32; o > 0; o >>= 1) acc += __shfl_xor(acc, o);
        if (l == i) mine = acc; }
    if (l < NT) { const float z = rbfh(mine); st4(r, W_GATE + (t0 + l) * 4, __float_as_uint(rbfh(1.f / (1.f + expf(-z))))); }
}
__device__ __forceinline__ void conv_prep(const Params& p, rsrc_t r, const int wv, const int hi, const int G0, const int G1) {
    const int tid = otid(), Q = tid >> 2, cg = tid & 3, ch0 = hi * 32 + cg * 8;
    v4u sk[4]; float sv[8][4];
#pragma unroll
    for (int q = 0; q < 4; ++q) sk[q] = *(const v4u*)(p.shift_k + ch0 * 4 + q * 8);
#pragma unroll
    for (int c8 = 0; c8 < 8; ++c8)
#pragma unroll
        for (int tp = 0; tp < 4; ++tp) { const unsigned sw = sk[c8 >> 1][(c8 & 1) * 2 + (tp >> 1)]; sv[c8][tp] = (tp & 1) ? bhi(sw) : blo(sw); }
    const rsrc_t rk = __builtin_amdgcn_make_buffer_rsrc((void*)p.Ak, 0, S * D * 2, 0x00020000), rv = __builtin_amdgcn_make_buffer_rsrc((void*)p.Av, 0, S * D * 2, 0x00020000),
                 rq = __builtin_amdgcn_make_buffer_rsrc((void*)p.Aq, 0, S * D * 2, 0x00020000);
#pragma unroll 1
    for (int G = G0; G < G1; ++G) {
        const unsigned ro = (unsigned)((Q * 32 + G * 8) * D + ch0) * 2u, uo = hi * 131072 + tid * 16;
        v4u kr[11], u[8], qv[8];
#pragma unroll
        for (int i = 0; i < 11; ++i) { kr[i] = __builtin_amdgcn_raw_buffer_load_b128(rk, (i < 3 && Q + G == 0) ? OOB : ro + (i - 3) * 2048, 0, 0); NAP(); }
#pragma unroll
        for (int i = 0; i < 8; ++i) { u[i] = __builtin_amdgcn_raw_buffer_load_b128(rv, ro + i * 2048, 0, 0); NAP(); }
#pragma unroll
        for (int i = 0; i < 8; ++i) { qv[i] = __builtin_amdgcn_raw_buffer_load_b128(rq, ro + i * 2048, 0, 0); NAP(); }
#pragma unroll
        for (int q = 0; q < 4; ++q) {
#pragma unroll
            for (int i = 0; i < 8; ++i) {
                unsigned ks = pack2(blo(kr[i][q]) * sv[2 * q][3], bhi(kr[i][q]) * sv[2 * q + 1][3]), pr;
#pragma unroll
                for (int d = 1; d < 4; ++d) { pr = pack2(blo(kr[i + d][q]) * sv[2 * q][3 - d], bhi(kr[i + d][q]) * sv[2 * q + 1][3 - d]); ks = pack2(blo(ks) + blo(pr), bhi(ks) + bhi(pr)); }
                u[i][q] = pack2(blo(ks) * blo(u[i][q]), bhi(ks) * bhi(u[i][q])); }
            v4u lo, hh;
#pragma unroll
            for (int z = 0; z < 4; ++z) { const unsigned x = u[7 - 2 * z][q], y = u[6 - 2 * z][q]; lo[z] = __builtin_amdgcn_perm(y, x, 0x05040100u); hh[z] = __builtin_amdgcn_perm(y, x, 0x07060302u); }
            st16(r, W_U + uo + q * 32768 + G * 4096, lo); NAP(); st16(r, W_U + uo + q * 32768 + 16384 + G * 4096, hh); NAP();
#pragma unroll
            for (int z = 0; z < 2; ++z) { st16(r, W_Y + uo + q * 32768 + (2 * G + z) * 4096, (v4u){qv[4 * z][q], qv[4 * z + 1][q], qv[4 * z + 2][q], qv[4 * z + 3][q]}); NAP(); }
        }
    }
    DRAIN(); __syncthreads();
    if (tid == 0) __hip_atomic_fetch_add((unsigned*)(p.ws + F_CONV), 1u, __ATOMIC_RELAXED, __HIP_MEMORY_SCOPE_AGENT);
    if (XG && tid == 0) __hip_atomic_fetch_add((unsigned*)(p.ws + F_CVY + hi * 128 + 64), 1u, __ATOMIC_RELAXED, __HIP_MEMORY_SCOPE_AGENT);
}
__device__ __forceinline__ bool conv_core(const Params& p, rsrc_t r, unsigned char* lds, const int wv, const int un) {
    const int tid = otid(), w = wv, l = tid & 63, Q = tid >> 2, cg = tid & 3;
    const rsrc_t rs = __builtin_amdgcn_make_buffer_rsrc((void*)p.ssm_k, 0, S * D * 2, 0x00020000);
    v4u kraw[4]; v2u kext[4];
    auto kload = [&](int n) { const unsigned o = (unsigned)(((un >> 2) * 32 + w * 8 + (un & 3) * 2 + n) * S + l * 8) * 2u;
#pragma unroll
        for (int i = 0; i < 4; ++i) { kraw[i] = __builtin_amdgcn_raw_buffer_load_b128(rs, o + i * 1024, 0, CV_POL);
            kext[i] = __builtin_amdgcn_raw_buffer_load_b64(rs, (i == 3 && l == 63) ? OOB : o + i * 1024 + 16, 0, 0); } };
    kload(0);
    if (!wait_word(r, XG ? F_CVY + (un >> 2) * 128 + 64 : F_CONV, XG ? 4u : 32u, 2048u)) return false;
    v4u ur[8], qr[8];
#pragma unroll
    for (int i = 0; i < 8; ++i) ur[i] = ld16(r, W_U + un * 32768 + i * 4096 + tid * 16);
#pragma unroll
    for (int i = 0; i < 8; ++i) qr[i] = ld16(r, W_Y + un * 32768 + i * 4096 + tid * 16);
    const unsigned dv = *(const unsigned*)(p.ssm_D + (un >> 2) * 32 + cg * 8 + (un & 3) * 2);
    for (int i = tid; i < 8 * 160; i += 256) *(v4u*)(lds + (i / 160) * CV_U + (i % 160) * 16) = (v4u){0u, 0u, 0u, 0u};
    unsigned char* kc = lds + CV_K0 + w * 4 * CV_KP;
    if (l < 32) {
#pragma unroll
        for (int q = 0; q < 4; ++q) *(unsigned*)(kc + q * CV_KP + l * 4) = 0u; }
    const int b = l & 31, kg = l >> 5;
    const unsigned kb = (unsigned)(size_t)kc + (b & 3) * CV_KP + ((b & 28) - 8 * kg + 56 - 16) * 2, ub0 = (unsigned)(size_t)lds + (1 + b) * CV_RP + kg * 16;
    unsigned char* const urow = lds + 2 * cg * CV_U + (32 + Q) * CV_RP;
#pragma unroll
    for (int i = 0; i < 8; ++i) *(v4u*)(urow + (i >> 2) * CV_U + (i & 3) * 16) = ur[i];
    __syncthreads();
#pragma unroll 1
        for (int ci = 0; ci < 2; ++ci) {
            const int c8 = 2 * w + ci;
#pragma unroll
            for (int i = 0; i < 4; ++i) { const v4u e = kraw[i]; const v2u f = kext[i]; unsigned char* q = kc + 128 + (i * 64 + l) * 16;
                const unsigned s0 = __builtin_amdgcn_alignbit(e.y, e.x, 16), s1 = __builtin_amdgcn_alignbit(e.z, e.y, 16), s2 = __builtin_amdgcn_alignbit(e.w, e.z, 16),
                               s3 = __builtin_amdgcn_alignbit(f.x, e.w, 16), s4 = __builtin_amdgcn_alignbit(f.y, f.x, 16);
                *(v4u*)q = (v4u){s0, s1, s2, s3}; *(v4u*)(q + CV_KP) = (v4u){e.y, e.z, e.w, f.x}; *(v4u*)(q + 2 * CV_KP) = (v4u){s1, s2, s3, s4}; *(v4u*)(q + 3 * CV_KP) = (v4u){e.z, e.w, f.x, f.y};
                if (i == 0 && l == 0) {
                    *(v2u*)(q - 8) = (v2u){0u, e.x << 16}; *(v2u*)(q + CV_KP - 8) = (v2u){0u, e.x}; *(v2u*)(q + 2 * CV_KP - 8) = (v2u){e.x << 16, s0}; *(v2u*)(q + 3 * CV_KP - 8) = (v2u){e.x, e.y}; } }
            if (ci == 0) kload(1);
            __builtin_amdgcn_wave_barrier();
            f32x16 acc0, acc1;
            asm volatile(
                "ds_read_b64 v[192:193], %[kb] offset:32\n\t"
                "ds_read_b64 v[194:195], %[kb] offset:40\n\t"
                "ds_read_b128 v[196:199], %[ub] offset:5040\n\t"
                "ds_read_b128 v[200:203], %[ub] offset:2480\n\t"
                "ds_read_b64 v[204:205], %[kb] offset:2080\n\t"
                "ds_read_b64 v[206:207], %[kb] offset:2088\n\t"
                "ds_read_b64 v[208:209], %[kb] offset:0\n\t"
                "ds_read_b64 v[210:211], %[kb] offset:8\n\t"
                "ds_read_b128 v[212:215], %[ub] offset:5072\n\t"
                "ds_read_b128 v[216:219], %[ub] offset:2512\n\t"
                "ds_read_b64 v[220:221], %[kb] offset:2048\n\t"
                "ds_read_b64 v[222:223], %[kb] offset:2056\n\t"
                "s_waitcnt lgkmcnt(9)\n\t"
                "v_mfma_f32_32x32x16_bf16 %[a1], v[192:195], v[196:199], 0\n\t"
                "ds_read_b64 v[224:225], %[kb] offset:96\n\t"
                "ds_read_b64 v[226:227], %[kb] offset:104\n\t"
                "ds_read_b128 v[228:231], %[ub] offset:4960\n\t"
                "s_waitcnt lgkmcnt(11)\n\t"
                "v_mfma_f32_32x32x16_bf16 %[a0], v[192:195], v[200:203], 0\n\t"
                "ds_read_b128 v[232:235], %[ub] offset:2400\n\t"
                "s_waitcnt lgkmcnt(10)\n\t"
                "v_mfma_f32_32x32x16_bf16 %[a1], v[204:207], v[200:203], %[a1]\n\t"
                "ds_read_b64 v[236:237], %[kb] offset:2144\n\t"
                "ds_read_b64 v[238:239], %[kb] offset:2152\n\t"
                "s_waitcnt lgkmcnt(9)\n\t"
                "v_mfma_f32_32x32x16_bf16 %[a1], v[208:211], v[212:215], %[a1]\n\t"
                "ds_read_b64 v[240:241], %[kb] offset:64\n\t"
                "ds_read_b64 v[242:243], %[kb] offset:72\n\t"
                "ds_read_b128 v[244:247], %[ub] offset:4992\n\t"
                "s_waitcnt lgkmcnt(11)\n\t"
                "v_mfma_f32_32x32x16_bf16 %[a0], v[208:211], v[216:219], %[a0]\n\t"
                "ds_read_b128 v[248:251], %[ub] offset:2432\n\t"
                "s_waitcnt lgkmcnt(10)\n\t"
                "v_mfma_f32_32x32x16_bf16 %[a1], v[220:223], v[216:219], %[a1]\n\t"
                "ds_read_b64 v[252:253], %[kb] offset:2112\n\t"
                "ds_read_b64 v[254:255], %[kb] offset:2120\n\t"
                "s_waitcnt lgkmcnt(9)\n\t"
                "v_mfma_f32_32x32x16_bf16 %[a1], v[224:227], v[228:231], %[a1]\n\t"
                "ds_read_b64 v[192:193], %[kb] offset:160\n\t"
                "ds_read_b64 v[194:195], %[kb] offset:168\n\t"
                "ds_read_b128 v[196:199], %[ub] offset:4880\n\t"
                "s_waitcnt lgkmcnt(11)\n\t"
                "v_mfma_f32_32x32x16_bf16 %[a0], v[224:227], v[232:235], %[a0]\n\t"
                "ds_read_b128 v[200:203], %[ub] offset:2320\n\t"
                "s_waitcnt lgkmcnt(10)\n\t"
                "v_mfma_f32_32x32x16_bf16 %[a1], v[236:239], v[232:235], %[a1]\n\t"
                "ds_read_b64 v[204:205], %[kb] offset:2208\n\t"
                "ds_read_b64 v[206:207], %[kb] offset:2216\n\t"
                "s_waitcnt lgkmcnt(9)\n\t"
                "v_mfma_f32_32x32x16_bf16 %[a1], v[240:243], v[244:247], %[a1]\n\t"
                "ds_read_b64 v[208:209], %[kb] offset:128\n\t"
                "ds_read_b64 v[210:211], %[kb] offset:136\n\t"
                "ds_read_b128 v[212:215], %[ub] offset:4912\n\t"
                "s_waitcnt lgkmcnt(11)\n\t"
                "v_mfma_f32_32x32x16_bf16 %[a0], v[240:243], v[248:251], %[a0]\n\t"
                "ds_read_b128 v[216:219], %[ub] offset:2352\n\t"
                "s_waitcnt lgkmcnt(10)\n\t"
                "v_mfma_f32_32x32x16_bf16 %[a1], v[252:255], v[248:251], %[a1]\n\t"
                "ds_read_b64 v[220:221], %[kb] offset:2176\n\t"
                "ds_read_b64 v[222:223], %[kb] offset:2184\n\t"
                "s_waitcnt lgkmcnt(9)\n\t"
                "v_mfma_f32_32x32x16_bf16 %[a1], v[192:195], v[196:199], %[a1]\n\t"
                "ds_read_b64 v[224:225], %[kb] offset:224\n\t"
                "ds_read_b64 v[226:227], %[kb] offset:232\n\t"
                "ds_read_b128 v[228:231], %[ub] offset:4800\n\t"
                "s_waitcnt lgkmcnt(11)\n\t"
                "v_mfma_f32_32x32x16_bf16 %[a0], v[192:195], v[200:203], %[a0]\n\t"
                "ds_read_b128 v[232:235], %[ub] offset:2240\n\t"
                "s_waitcnt lgkmcnt(10)\n\t"
                "v_mfma_f32_32x32x16_bf16 %[a1], v[204:207], v[200:203], %[a1]\n\t"
                "ds_read_b64 v[236:237], %[kb] offset:2272\n\t"
                "ds_read_b64 v[238:239], %[kb] offset:2280\n\t"
                "s_waitcnt lgkmcnt(9)\n\t"
                "v_mfma_f32_32x32x16_bf16 %[a1], v[208:211], v[212:215], %[a1]\n\t"
                "ds_read_b64 v[240:241], %[kb] offset:192\n\t"
                "ds_read_b64 v[242:243], %[kb] offset:200\n\t"
                "ds_read_b128 v[244:247], %[ub] offset:4832\n\t"
                "s_waitcnt lgkmcnt(11)\n\t"
                "v_mfma_f32_32x32x16_bf16 %[a0], v[208:211], v[216:219], %[a0]\n\t"
                "ds_read_b128 v[248:251], %[ub] offset:2272\n\t"
                "s_waitcnt lgkmcnt(10)\n\t"
                "v_mfma_f32_32x32x16_bf16 %[a1], v[220:223], v[216:219], %[a1]\n\t"
                "ds_read_b64 v[252:253], %[kb] offset:2240\n\t"
                "ds_read_b64 v[254:255], %[kb] offset:2248\n\t"
                "s_waitcnt lgkmcnt(9)\n\t"
                "v_mfma_f32_32x32x16_bf16 %[a1], v[224:227], v[228:231], %[a1]\n\t"
                "ds_read_b64 v[192:193], %[kb] offset:288\n\t"
                "ds_read_b64 v[194:195], %[kb] offset:296\n\t"
                "ds_read_b128 v[196:199], %[ub] offset:4720\n\t"
                "s_waitcnt lgkmcnt(11)\n\t"
                "v_mfma_f32_32x32x16_bf16 %[a0], v[224:227], v[232:235], %[a0]\n\t"
                "ds_read_b128 v[200:203], %[ub] offset:2160\n\t"
                "s_waitcnt lgkmcnt(10)\n\t"
                "v_mfma_f32_32x32x16_bf16 %[a1], v[236:239], v[232:235], %[a1]\n\t"
                "ds_read_b64 v[204:205], %[kb] offset:2336\n\t"
                "ds_read_b64 v[206:207], %[kb] offset:2344\n\t"
                "s_waitcnt lgkmcnt(9)\n\t"
                "v_mfma_f32_32x32x16_bf16 %[a1], v[240:243], v[244:247], %[a1]\n\t"
                "ds_read_b64 v[208:209], %[kb] offset:256\n\t"
                "ds_read_b64 v[210:211], %[kb] offset:264\n\t"
                "ds_read_b128 v[212:215], %[ub] offset:4752\n\t"
                "s_waitcnt lgkmcnt(11)\n\t"
                "v_mfma_f32_32x32x16_bf16 %[a0], v[240:243], v[248:251], %[a0]\n\t"
                "ds_read_b128 v[216:219], %[ub] offset:2192\n\t"
                "s_waitcnt lgkmcnt(10)\n\t"
                "v_mfma_f32_32x32x16_bf16 %[a1], v[252:255], v[248:251], %[a1]\n\t"
                "ds_read_b64 v[220:221], %[kb] offset:2304\n\t"
                "ds_read_b64 v[222:223], %[kb] offset:2312\n\t"
                "s_waitcnt lgkmcnt(9)\n\t"
                "v_mfma_f32_32x32x16_bf16 %[a1], v[192:195], v[196:199], %[a1]\n\t"
                "ds_read_b64 v[224:225], %[kb] offset:352\n\t"
                "ds_read_b64 v[226:227], %[kb] offset:360\n\t"
                "ds_read_b128 v[228:231], %[ub] offset:4640\n\t"
                "s_waitcnt lgkmcnt(11)\n\t"
                "v_mfma_f32_32x32x16_bf16 %[a0], v[192:195], v[200:203], %[a0]\n\t"
                "ds_read_b128 v[232:235], %[ub] offset:2080\n\t"
                "s_waitcnt lgkmcnt(10)\n\t"
                "v_mfma_f32_32x32x16_bf16 %[a1], v[204:207], v[200:203], %[a1]\n\t"
                "ds_read_b64 v[236:237], %[kb] offset:2400\n\t"
                "ds_read_b64 v[238:239], %[kb] offset:2408\n\t"
                "s_waitcnt lgkmcnt(9)\n\t"
                "v_mfma_f32_32x32x16_bf16 %[a1], v[208:211], v[212:215], %[a1]\n\t"
                "ds_read_b64 v[240:241], %[kb] offset:320\n\t"
                "ds_read_b64 v[242:243], %[kb] offset:328\n\t"
                "ds_read_b128 v[244:247], %[ub] offset:4672\n\t"
                "s_waitcnt lgkmcnt(11)\n\t"
                "v_mfma_f32_32x32x16_bf16 %[a0], v[208:211], v[216:219], %[a0]\n\t"
                "ds_read_b128 v[248:251], %[ub] offset:2112\n\t"
                "s_waitcnt lgkmcnt(10)\n\t"
                "v_mfma_f32_32x32x16_bf16 %[a1], v[220:223], v[216:219], %[a1]\n\t"
                "ds_read_b64 v[252:253], %[kb] offset:2368\n\t"
                "ds_read_b64 v[254:255], %[kb] offset:2376\n\t"
                "s_waitcnt lgkmcnt(9)\n\t"
                "v_mfma_f32_32x32x16_bf16 %[a1], v[224:227], v[228:231], %[a1]\n\t"
                "ds_read_b64 v[192:193], %[kb] offset:416\n\t"
                "ds_read_b64 v[194:195], %[kb] offset:424\n\t"
                "ds_read_b128 v[196:199], %[ub] offset:4560\n\t"
                "s_waitcnt lgkmcnt(11)\n\t"
                "v_mfma_f32_32x32x16_bf16 %[a0], v[224:227], v[232:235], %[a0]\n\t"
                "ds_read_b128 v[200:203], %[ub] offset:2000\n\t"
                "s_waitcnt lgkmcnt(10)\n\t"
                "v_mfma_f32_32x32x16_bf16 %[a1], v[236:239], v[232:235], %[a1]\n\t"
                "ds_read_b64 v[204:205], %[kb] offset:2464\n\t"
                "ds_read_b64 v[206:207], %[kb] offset:2472\n\t"
                "s_waitcnt lgkmcnt(9)\n\t"
                "v_mfma_f32_32x32x16_bf16 %[a1], v[240:243], v[244:247], %[a1]\n\t"
                "ds_read_b64 v[208:209], %[kb] offset:384\n\t"
                "ds_read_b64 v[210:211], %[kb] offset:392\n\t"
                "ds_read_b128 v[212:215], %[ub] offset:4592\n\t"
                "s_waitcnt lgkmcnt(11)\n\t"
                "v_mfma_f32_32x32x16_bf16 %[a0], v[240:243], v[248:251], %[a0]\n\t"
                "ds_read_b128 v[216:219], %[ub] offset:2032\n\t"
                "s_waitcnt lgkmcnt(10)\n\t"
                "v_mfma_f32_32x32x16_bf16 %[a1], v[252:255], v[248:251], %[a1]\n\t"
                "ds_read_b64 v[220:221], %[kb] offset:2432\n\t"
                "ds_read_b64 v[222:223], %[kb] offset:2440\n\t"
                "s_waitcnt lgkmcnt(9)\n\t"
                "v_mfma_f32_32x32x16_bf16 %[a1], v[192:195], v[196:199], %[a1]\n\t"
                "ds_read_b64 v[224:225], %[kb] offset:480\n\t"
                "ds_read_b64 v[226:227], %[kb] offset:488\n\t"
                "ds_read_b128 v[228:231], %[ub] offset:4480\n\t"
                "s_waitcnt lgkmcnt(11)\n\t"
                "v_mfma_f32_32x32x16_bf16 %[a0], v[192:195], v[200:203], %[a0]\n\t"
                "ds_read_b128 v[232:235], %[ub] offset:1920\n\t"
                "s_waitcnt lgkmcnt(10)\n\t"
                "v_mfma_f32_32x32x16_bf16 %[a1], v[204:207], v[200:203], %[a1]\n\t"
                "ds_read_b64 v[236:237], %[kb] offset:2528\n\t"
                "ds_read_b64 v[238:239], %[kb] offset:2536\n\t"
                "s_waitcnt lgkmcnt(9)\n\t"
                "v_mfma_f32_32x32x16_bf16 %[a1], v[208:211], v[212:215], %[a1]\n\t"
                "ds_read_b64 v[240:241], %[kb] offset:448\n\t"
                "ds_read_b64 v[242:243], %[kb] offset:456\n\t"
                "ds_read_b128 v[244:247], %[ub] offset:4512\n\t"
                "s_waitcnt lgkmcnt(11)\n\t"
                "v_mfma_f32_32x32x16_bf16 %[a0], v[208:211], v[216:219], %[a0]\n\t"
                "ds_read_b128 v[248:251], %[ub] offset:1952\n\t"
                "s_waitcnt lgkmcnt(10)\n\t"
                "v_mfma_f32_32x32x16_bf16 %[a1], v[220:223], v[216:219], %[a1]\n\t"
                "ds_read_b64 v[252:253], %[kb] offset:2496\n\t"
                "ds_read_b64 v[254:255], %[kb] offset:2504\n\t"
                "s_waitcnt lgkmcnt(9)\n\t"
                "v_mfma_f32_32x32x16_bf16 %[a1], v[224:227], v[228:231], %[a1]\n\t"
                "ds_read_b64 v[192:193], %[kb] offset:544\n\t"
                "ds_read_b64 v[194:195], %[kb] offset:552\n\t"
                "ds_read_b128 v[196:199], %[ub] offset:4400\n\t"
                "s_waitcnt lgkmcnt(11)\n\t"
                "v_mfma_f32_32x32x16_bf16 %[a0], v[224:227], v[232:235], %[a0]\n\t"
                "ds_read_b128 v[200:203], %[ub] offset:1840\n\t"
                "s_waitcnt lgkmcnt(10)\n\t"
                "v_mfma_f32_32x32x16_bf16 %[a1], v[236:239], v[232:235], %[a1]\n\t"
                "ds_read_b64 v[204:205], %[kb] offset:2592\n\t"
                "ds_read_b64 v[206:207], %[kb] offset:2600\n\t"
                "s_waitcnt lgkmcnt(9)\n\t"
                "v_mfma_f32_32x32x16_bf16 %[a1], v[240:243], v[244:247], %[a1]\n\t"
                "ds_read_b64 v[208:209], %[kb] offset:512\n\t"
                "ds_read_b64 v[210:211], %[kb] offset:520\n\t"
                "ds_read_b128 v[212:215], %[ub] offset:4432\n\t"
                "s_waitcnt lgkmcnt(11)\n\t"
                "v_mfma_f32_32x32x16_bf16 %[a0], v[240:243], v[248:251], %[a0]\n\t"
                "ds_read_b128 v[216:219], %[ub] offset:1872\n\t"
                "s_waitcnt lgkmcnt(10)\n\t"
                "v_mfma_f32_32x32x16_bf16 %[a1], v[252:255], v[248:251], %[a1]\n\t"
                "ds_read_b64 v[220:221], %[kb] offset:2560\n\t"
                "ds_read_b64 v[222:223], %[kb] offset:2568\n\t"
                "s_waitcnt lgkmcnt(9)\n\t"
                "v_mfma_f32_32x32x16_bf16 %[a1], v[192:195], v[196:199], %[a1]\n\t"
                "ds_read_b64 v[224:225], %[kb] offset:608\n\t"
                "ds_read_b64 v[226:227], %[kb] offset:616\n\t"
                "ds_read_b128 v[228:231], %[ub] offset:4320\n\t"
                "s_waitcnt lgkmcnt(11)\n\t"
                "v_mfma_f32_32x32x16_bf16 %[a0], v[192:195], v[200:203], %[a0]\n\t"
                "ds_read_b128 v[232:235], %[ub] offset:1760\n\t"
                "s_waitcnt lgkmcnt(10)\n\t"
                "v_mfma_f32_32x32x16_bf16 %[a1], v[204:207], v[200:203], %[a1]\n\t"
                "ds_read_b64 v[236:237], %[kb] offset:2656\n\t"
                "ds_read_b64 v[238:239], %[kb] offset:2664\n\t"
                "s_waitcnt lgkmcnt(9)\n\t"
                "v_mfma_f32_32x32x16_bf16 %[a1], v[208:211], v[212:215], %[a1]\n\t"
                "ds_read_b64 v[240:241], %[kb] offset:576\n\t"
                "ds_read_b64 v[242:243], %[kb] offset:584\n\t"
                "ds_read_b128 v[244:247], %[ub] offset:4352\n\t"
                "s_waitcnt lgkmcnt(11)\n\t"
                "v_mfma_f32_32x32x16_bf16 %[a0], v[208:211], v[216:219], %[a0]\n\t"
                "ds_read_b128 v[248:251], %[ub] offset:1792\n\t"
                "s_waitcnt lgkmcnt(10)\n\t"
                "v_mfma_f32_32x32x16_bf16 %[a1], v[220:223], v[216:219], %[a1]\n\t"
                "ds_read_b64 v[252:253], %[kb] offset:2624\n\t"
                "ds_read_b64 v[254:255], %[kb] offset:2632\n\t"
                "s_waitcnt lgkmcnt(9)\n\t"
                "v_mfma_f32_32x32x16_bf16 %[a1], v[224:227], v[228:231], %[a1]\n\t"
                "ds_read_b64 v[192:193], %[kb] offset:672\n\t"
                "ds_read_b64 v[194:195], %[kb] offset:680\n\t"
                "ds_read_b128 v[196:199], %[ub] offset:4240\n\t"
                "s_waitcnt lgkmcnt(11)\n\t"
                "v_mfma_f32_32x32x16_bf16 %[a0], v[224:227], v[232:235], %[a0]\n\t"
                "ds_read_b128 v[200:203], %[ub] offset:1680\n\t"
                "s_waitcnt lgkmcnt(10)\n\t"
                "v_mfma_f32_32x32x16_bf16 %[a1], v[236:239], v[232:235], %[a1]\n\t"
                "ds_read_b64 v[204:205], %[kb] offset:2720\n\t"
                "ds_read_b64 v[206:207], %[kb] offset:2728\n\t"
                "s_waitcnt lgkmcnt(9)\n\t"
                "v_mfma_f32_32x32x16_bf16 %[a1], v[240:243], v[244:247], %[a1]\n\t"
                "ds_read_b64 v[208:209], %[kb] offset:640\n\t"
                "ds_read_b64 v[210:211], %[kb] offset:648\n\t"
                "ds_read_b128 v[212:215], %[ub] offset:4272\n\t"
                "s_waitcnt lgkmcnt(11)\n\t"
                "v_mfma_f32_32x32x16_bf16 %[a0], v[240:243], v[248:251], %[a0]\n\t"
                "ds_read_b128 v[216:219], %[ub] offset:1712\n\t"
                "s_waitcnt lgkmcnt(10)\n\t"
                "v_mfma_f32_32x32x16_bf16 %[a1], v[252:255], v[248:251], %[a1]\n\t"
                "ds_read_b64 v[220:221], %[kb] offset:2688\n\t"
                "ds_read_b64 v[222:223], %[kb] offset:2696\n\t"
                "s_waitcnt lgkmcnt(9)\n\t"
                "v_mfma_f32_32x32x16_bf16 %[a1], v[192:195], v[196:199], %[a1]\n\t"
                "ds_read_b64 v[224:225], %[kb] offset:736\n\t"
                "ds_read_b64 v[226:227], %[kb] offset:744\n\t"
                "ds_read_b128 v[228:231], %[ub] offset:4160\n\t"
                "s_waitcnt lgkmcnt(11)\n\t"
                "v_mfma_f32_32x32x16_bf16 %[a0], v[192:195], v[200:203], %[a0]\n\t"
                "ds_read_b128 v[232:235], %[ub] offset:1600\n\t"
                "s_waitcnt lgkmcnt(10)\n\t"
                "v_mfma_f32_32x32x16_bf16 %[a1], v[204:207], v[200:203], %[a1]\n\t"
                "ds_read_b64 v[236:237], %[kb] offset:2784\n\t"
                "ds_read_b64 v[238:239], %[kb] offset:2792\n\t"
                "s_waitcnt lgkmcnt(9)\n\t"
                "v_mfma_f32_32x32x16_bf16 %[a1], v[208:211], v[212:215], %[a1]\n\t"
                "ds_read_b64 v[240:241], %[kb] offset:704\n\t"
                "ds_read_b64 v[242:243], %[kb] offset:712\n\t"
                "ds_read_b128 v[244:247], %[ub] offset:4192\n\t"
                "s_waitcnt lgkmcnt(11)\n\t"
                "v_mfma_f32_32x32x16_bf16 %[a0], v[208:211], v[216:219], %[a0]\n\t"
                "ds_read_b128 v[248:251], %[ub] offset:1632\n\t"
                "s_waitcnt lgkmcnt(10)\n\t"
                "v_mfma_f32_32x32x16_bf16 %[a1], v[220:223], v[216:219], %[a1]\n\t"
                "ds_read_b64 v[252:253], %[kb] offset:2752\n\t"
                "ds_read_b64 v[254:255], %[kb] offset:2760\n\t"
                "s_waitcnt lgkmcnt(9)\n\t"
                "v_mfma_f32_32x32x16_bf16 %[a1], v[224:227], v[228:231], %[a1]\n\t"
                "ds_read_b64 v[192:193], %[kb] offset:800\n\t"
                "ds_read_b64 v[194:195], %[kb] offset:808\n\t"
                "ds_read_b128 v[196:199], %[ub] offset:4080\n\t"
                "s_waitcnt lgkmcnt(11)\n\t"
                "v_mfma_f32_32x32x16_bf16 %[a0], v[224:227], v[232:235], %[a0]\n\t"
                "ds_read_b128 v[200:203], %[ub] offset:1520\n\t"
                "s_waitcnt lgkmcnt(10)\n\t"
                "v_mfma_f32_32x32x16_bf16 %[a1], v[236:239], v[232:235], %[a1]\n\t"
                "ds_read_b64 v[204:205], %[kb] offset:2848\n\t"
                "ds_read_b64 v[206:207], %[kb] offset:2856\n\t"
                "s_waitcnt lgkmcnt(9)\n\t"
                "v_mfma_f32_32x32x16_bf16 %[a1], v[240:243], v[244:247], %[a1]\n\t"
                "ds_read_b64 v[208:209], %[kb] offset:768\n\t"
                "ds_read_b64 v[210:211], %[kb] offset:776\n\t"
                "ds_read_b128 v[212:215], %[ub] offset:4112\n\t"
                "s_waitcnt lgkmcnt(11)\n\t"
                "v_mfma_f32_32x32x16_bf16 %[a0], v[240:243], v[248:251], %[a0]\n\t"
                "ds_read_b128 v[216:219], %[ub] offset:1552\n\t"
                "s_waitcnt lgkmcnt(10)\n\t"
                "v_mfma_f32_32x32x16_bf16 %[a1], v[252:255], v[248:251], %[a1]\n\t"
                "ds_read_b64 v[220:221], %[kb] offset:2816\n\t"
                "ds_read_b64 v[222:223], %[kb] offset:2824\n\t"
                "s_waitcnt lgkmcnt(9)\n\t"
                "v_mfma_f32_32x32x16_bf16 %[a1], v[192:195], v[196:199], %[a1]\n\t"
                "ds_read_b64 v[224:225], %[kb] offset:864\n\t"
                "ds_read_b64 v[226:227], %[kb] offset:872\n\t"
                "ds_read_b128 v[228:231], %[ub] offset:4000\n\t"
                "s_waitcnt lgkmcnt(11)\n\t"
                "v_mfma_f32_32x32x16_bf16 %[a0], v[192:195], v[200:203], %[a0]\n\t"
                "ds_read_b128 v[232:235], %[ub] offset:1440\n\t"
                "s_waitcnt lgkmcnt(10)\n\t"
                "v_mfma_f32_32x32x16_bf16 %[a1], v[204:207], v[200:203], %[a1]\n\t"
                "ds_read_b64 v[236:237], %[kb] offset:2912\n\t"
                "ds_read_b64 v[238:239], %[kb] offset:2920\n\t"
                "s_waitcnt lgkmcnt(9)\n\t"
                "v_mfma_f32_32x32x16_bf16 %[a1], v[208:211], v[212:215], %[a1]\n\t"
                "ds_read_b64 v[240:241], %[kb] offset:832\n\t"
                "ds_read_b64 v[242:243], %[kb] offset:840\n\t"
                "ds_read_b128 v[244:247], %[ub] offset:4032\n\t"
                "s_waitcnt lgkmcnt(11)\n\t"
                "v_mfma_f32_32x32x16_bf16 %[a0], v[208:211], v[216:219], %[a0]\n\t"
                "ds_read_b128 v[248:251], %[ub] offset:1472\n\t"
                "s_waitcnt lgkmcnt(10)\n\t"
                "v_mfma_f32_32x32x16_bf16 %[a1], v[220:223], v[216:219], %[a1]\n\t"
                "ds_read_b64 v[252:253], %[kb] offset:2880\n\t"
                "ds_read_b64 v[254:255], %[kb] offset:2888\n\t"
                "s_waitcnt lgkmcnt(9)\n\t"
                "v_mfma_f32_32x32x16_bf16 %[a1], v[224:227], v[228:231], %[a1]\n\t"
                "ds_read_b64 v[192:193], %[kb] offset:928\n\t"
                "ds_read_b64 v[194:195], %[kb] offset:936\n\t"
                "ds_read_b128 v[196:199], %[ub] offset:3920\n\t"
                "s_waitcnt lgkmcnt(11)\n\t"
                "v_mfma_f32_32x32x16_bf16 %[a0], v[224:227], v[232:235], %[a0]\n\t"
                "ds_read_b128 v[200:203], %[ub] offset:1360\n\t"
                "s_waitcnt lgkmcnt(10)\n\t"
                "v_mfma_f32_32x32x16_bf16 %[a1], v[236:239], v[232:235], %[a1]\n\t"
                "ds_read_b64 v[204:205], %[kb] offset:2976\n\t"
                "ds_read_b64 v[206:207], %[kb] offset:2984\n\t"
                "s_waitcnt lgkmcnt(9)\n\t"
                "v_mfma_f32_32x32x16_bf16 %[a1], v[240:243], v[244:247], %[a1]\n\t"
                "ds_read_b64 v[208:209], %[kb] offset:896\n\t"
                "ds_read_b64 v[210:211], %[kb] offset:904\n\t"
                "ds_read_b128 v[212:215], %[ub] offset:3952\n\t"
                "s_waitcnt lgkmcnt(11)\n\t"
                "v_mfma_f32_32x32x16_bf16 %[a0], v[240:243], v[248:251], %[a0]\n\t"
                "ds_read_b128 v[216:219], %[ub] offset:1392\n\t"
                "s_waitcnt lgkmcnt(10)\n\t"
                "v_mfma_f32_32x32x16_bf16 %[a1], v[252:255], v[248:251], %[a1]\n\t"
                "ds_read_b64 v[220:221], %[kb] offset:2944\n\t"
                "ds_read_b64 v[222:223], %[kb] offset:2952\n\t"
                "s_waitcnt lgkmcnt(9)\n\t"
                "v_mfma_f32_32x32x16_bf16 %[a1], v[192:195], v[196:199], %[a1]\n\t"
                "ds_read_b64 v[224:225], %[kb] offset:992\n\t"
                "ds_read_b64 v[226:227], %[kb] offset:1000\n\t"
                "ds_read_b128 v[228:231], %[ub] offset:3840\n\t"
                "s_waitcnt lgkmcnt(11)\n\t"
                "v_mfma_f32_32x32x16_bf16 %[a0], v[192:195], v[200:203], %[a0]\n\t"
                "ds_read_b128 v[232:235], %[ub] offset:1280\n\t"
                "s_waitcnt lgkmcnt(10)\n\t"
                "v_mfma_f32_32x32x16_bf16 %[a1], v[204:207], v[200:203], %[a1]\n\t"
                "ds_read_b64 v[236:237], %[kb] offset:3040\n\t"
                "ds_read_b64 v[238:239], %[kb] offset:3048\n\t"
                "s_waitcnt lgkmcnt(9)\n\t"
                "v_mfma_f32_32x32x16_bf16 %[a1], v[208:211], v[212:215], %[a1]\n\t"
                "ds_read_b64 v[240:241], %[kb] offset:960\n\t"
                "ds_read_b64 v[242:243], %[kb] offset:968\n\t"
                "ds_read_b128 v[244:247], %[ub] offset:3872\n\t"
                "s_waitcnt lgkmcnt(11)\n\t"
                "v_mfma_f32_32x32x16_bf16 %[a0], v[208:211], v[216:219], %[a0]\n\t"
                "ds_read_b128 v[248:251], %[ub] offset:1312\n\t"
                "s_waitcnt lgkmcnt(10)\n\t"
                "v_mfma_f32_32x32x16_bf16 %[a1], v[220:223], v[216:219], %[a1]\n\t"
                "ds_read_b64 v[252:253], %[kb] offset:3008\n\t"
                "ds_read_b64 v[254:255], %[kb] offset:3016\n\t"
                "s_waitcnt lgkmcnt(9)\n\t"
                "v_mfma_f32_32x32x16_bf16 %[a1], v[224:227], v[228:231], %[a1]\n\t"
                "ds_read_b64 v[192:193], %[kb] offset:1056\n\t"
                "ds_read_b64 v[194:195], %[kb] offset:1064\n\t"
                "ds_read_b128 v[196:199], %[ub] offset:3760\n\t"
                "s_waitcnt lgkmcnt(11)\n\t"
                "v_mfma_f32_32x32x16_bf16 %[a0], v[224:227], v[232:235], %[a0]\n\t"
                "ds_read_b128 v[200:203], %[ub] offset:1200\n\t"
                "s_waitcnt lgkmcnt(10)\n\t"
                "v_mfma_f32_32x32x16_bf16 %[a1], v[236:239], v[232:235], %[a1]\n\t"
                "ds_read_b64 v[204:205], %[kb] offset:3104\n\t"
                "ds_read_b64 v[206:207], %[kb] offset:3112\n\t"
                "s_waitcnt lgkmcnt(9)\n\t"
                "v_mfma_f32_32x32x16_bf16 %[a1], v[240:243], v[244:247], %[a1]\n\t"
                "ds_read_b64 v[208:209], %[kb] offset:1024\n\t"
                "ds_read_b64 v[210:211], %[kb] offset:1032\n\t"
                "ds_read_b128 v[212:215], %[ub] offset:3792\n\t"
                "s_waitcnt lgkmcnt(11)\n\t"
                "v_mfma_f32_32x32x16_bf16 %[a0], v[240:243], v[248:251], %[a0]\n\t"
                "ds_read_b128 v[216:219], %[ub] offset:1232\n\t"
                "s_waitcnt lgkmcnt(10)\n\t"
                "v_mfma_f32_32x32x16_bf16 %[a1], v[252:255], v[248:251], %[a1]\n\t"
                "ds_read_b64 v[220:221], %[kb] offset:3072\n\t"
                "ds_read_b64 v[222:223], %[kb] offset:3080\n\t"
                "s_waitcnt lgkmcnt(9)\n\t"
                "v_mfma_f32_32x32x16_bf16 %[a1], v[192:195], v[196:199], %[a1]\n\t"
                "ds_read_b64 v[224:225], %[kb] offset:1120\n\t"
                "ds_read_b64 v[226:227], %[kb] offset:1128\n\t"
                "ds_read_b128 v[228:231], %[ub] offset:3680\n\t"
                "s_waitcnt lgkmcnt(11)\n\t"
                "v_mfma_f32_32x32x16_bf16 %[a0], v[192:195], v[200:203], %[a0]\n\t"
                "ds_read_b128 v[232:235], %[ub] offset:1120\n\t"
                "s_waitcnt lgkmcnt(10)\n\t"
                "v_mfma_f32_32x32x16_bf16 %[a1], v[204:207], v[200:203], %[a1]\n\t"
                "ds_read_b64 v[236:237], %[kb] offset:3168\n\t"
                "ds_read_b64 v[238:239], %[kb] offset:3176\n\t"
                "s_waitcnt lgkmcnt(9)\n\t"
                "v_mfma_f32_32x32x16_bf16 %[a1], v[208:211], v[212:215], %[a1]\n\t"
                "ds_read_b64 v[240:241], %[kb] offset:1088\n\t"
                "ds_read_b64 v[242:243], %[kb] offset:1096\n\t"
                "ds_read_b128 v[244:247], %[ub] offset:3712\n\t"
                "s_waitcnt lgkmcnt(11)\n\t"
                "v_mfma_f32_32x32x16_bf16 %[a0], v[208:211], v[216:219], %[a0]\n\t"
                "ds_read_b128 v[248:251], %[ub] offset:1152\n\t"
                "s_waitcnt lgkmcnt(10)\n\t"
                "v_mfma_f32_32x32x16_bf16 %[a1], v[220:223], v[216:219], %[a1]\n\t"
                "ds_read_b64 v[252:253], %[kb] offset:3136\n\t"
                "ds_read_b64 v[254:255], %[kb] offset:3144\n\t"
                "s_waitcnt lgkmcnt(9)\n\t"
                "v_mfma_f32_32x32x16_bf16 %[a1], v[224:227], v[228:231], %[a1]\n\t"
                "ds_read_b64 v[192:193], %[kb] offset:1184\n\t"
                "ds_read_b64 v[194:195], %[kb] offset:1192\n\t"
                "ds_read_b128 v[196:199], %[ub] offset:3600\n\t"
                "s_waitcnt lgkmcnt(11)\n\t"
                "v_mfma_f32_32x32x16_bf16 %[a0], v[224:227], v[232:235], %[a0]\n\t"
                "ds_read_b128 v[200:203], %[ub] offset:1040\n\t"
                "s_waitcnt lgkmcnt(10)\n\t"
                "v_mfma_f32_32x32x16_bf16 %[a1], v[236:239], v[232:235], %[a1]\n\t"
                "ds_read_b64 v[204:205], %[kb] offset:3232\n\t"
                "ds_read_b64 v[206:207], %[kb] offset:3240\n\t"
                "s_waitcnt lgkmcnt(9)\n\t"
                "v_mfma_f32_32x32x16_bf16 %[a1], v[240:243], v[244:247], %[a1]\n\t"
                "ds_read_b64 v[208:209], %[kb] offset:1152\n\t"
                "ds_read_b64 v[210:211], %[kb] offset:1160\n\t"
                "ds_read_b128 v[212:215], %[ub] offset:3632\n\t"
                "s_waitcnt lgkmcnt(11)\n\t"
                "v_mfma_f32_32x32x16_bf16 %[a0], v[240:243], v[248:251], %[a0]\n\t"
                "ds_read_b128 v[216:219], %[ub] offset:1072\n\t"
                "s_waitcnt lgkmcnt(10)\n\t"
                "v_mfma_f32_32x32x16_bf16 %[a1], v[252:255], v[248:251], %[a1]\n\t"
                "ds_read_b64 v[220:221], %[kb] offset:3200\n\t"
                "ds_read_b64 v[222:223], %[kb] offset:3208\n\t"
                "s_waitcnt lgkmcnt(9)\n\t"
                "v_mfma_f32_32x32x16_bf16 %[a1], v[192:195], v[196:199], %[a1]\n\t"
                "ds_read_b64 v[224:225], %[kb] offset:1248\n\t"
                "ds_read_b64 v[226:227], %[kb] offset:1256\n\t"
                "ds_read_b128 v[228:231], %[ub] offset:3520\n\t"
                "s_waitcnt lgkmcnt(11)\n\t"
                "v_mfma_f32_32x32x16_bf16 %[a0], v[192:195], v[200:203], %[a0]\n\t"
                "ds_read_b128 v[232:235], %[ub] offset:960\n\t"
                "s_waitcnt lgkmcnt(10)\n\t"
                "v_mfma_f32_32x32x16_bf16 %[a1], v[204:207], v[200:203], %[a1]\n\t"
                "ds_read_b64 v[236:237], %[kb] offset:3296\n\t"
                "ds_read_b64 v[238:239], %[kb] offset:3304\n\t"
                "s_waitcnt lgkmcnt(9)\n\t"
                "v_mfma_f32_32x32x16_bf16 %[a1], v[208:211], v[212:215], %[a1]\n\t"
                "ds_read_b64 v[240:241], %[kb] offset:1216\n\t"
                "ds_read_b64 v[242:243], %[kb] offset:1224\n\t"
                "ds_read_b128 v[244:247], %[ub] offset:3552\n\t"
                "s_waitcnt lgkmcnt(11)\n\t"
                "v_mfma_f32_32x32x16_bf16 %[a0], v[208:211], v[216:219], %[a0]\n\t"
                "ds_read_b128 v[248:251], %[ub] offset:992\n\t"
                "s_waitcnt lgkmcnt(10)\n\t"
                "v_mfma_f32_32x32x16_bf16 %[a1], v[220:223], v[216:219], %[a1]\n\t"
                "ds_read_b64 v[252:253], %[kb] offset:3264\n\t"
                "ds_read_b64 v[254:255], %[kb] offset:3272\n\t"
                "s_waitcnt lgkmcnt(9)\n\t"
                "v_mfma_f32_32x32x16_bf16 %[a1], v[224:227], v[228:231], %[a1]\n\t"
                "ds_read_b64 v[192:193], %[kb] offset:1312\n\t"
                "ds_read_b64 v[194:195], %[kb] offset:1320\n\t"
                "ds_read_b128 v[196:199], %[ub] offset:3440\n\t"
                "s_waitcnt lgkmcnt(11)\n\t"
                "v_mfma_f32_32x32x16_bf16 %[a0], v[224:227], v[232:235], %[a0]\n\t"
                "ds_read_b128 v[200:203], %[ub] offset:880\n\t"
                "s_waitcnt lgkmcnt(10)\n\t"
                "v_mfma_f32_32x32x16_bf16 %[a1], v[236:239], v[232:235], %[a1]\n\t"
                "ds_read_b64 v[204:205], %[kb] offset:3360\n\t"
                "ds_read_b64 v[206:207], %[kb] offset:3368\n\t"
                "s_waitcnt lgkmcnt(9)\n\t"
                "v_mfma_f32_32x32x16_bf16 %[a1], v[240:243], v[244:247], %[a1]\n\t"
                "ds_read_b64 v[208:209], %[kb] offset:1280\n\t"
                "ds_read_b64 v[210:211], %[kb] offset:1288\n\t"
                "ds_read_b128 v[212:215], %[ub] offset:3472\n\t"
                "s_waitcnt lgkmcnt(11)\n\t"
                "v_mfma_f32_32x32x16_bf16 %[a0], v[240:243], v[248:251], %[a0]\n\t"
                "ds_read_b128 v[216:219], %[ub] offset:912\n\t"
                "s_waitcnt lgkmcnt(10)\n\t"
                "v_mfma_f32_32x32x16_bf16 %[a1], v[252:255], v[248:251], %[a1]\n\t"
                "ds_read_b64 v[220:221], %[kb] offset:3328\n\t"
                "ds_read_b64 v[222:223], %[kb] offset:3336\n\t"
                "s_waitcnt lgkmcnt(9)\n\t"
                "v_mfma_f32_32x32x16_bf16 %[a1], v[192:195], v[196:199], %[a1]\n\t"
                "ds_read_b64 v[224:225], %[kb] offset:1376\n\t"
                "ds_read_b64 v[226:227], %[kb] offset:1384\n\t"
                "ds_read_b128 v[228:231], %[ub] offset:3360\n\t"
                "s_waitcnt lgkmcnt(11)\n\t"
                "v_mfma_f32_32x32x16_bf16 %[a0], v[192:195], v[200:203], %[a0]\n\t"
                "ds_read_b128 v[232:235], %[ub] offset:800\n\t"
                "s_waitcnt lgkmcnt(10)\n\t"
                "v_mfma_f32_32x32x16_bf16 %[a1], v[204:207], v[200:203], %[a1]\n\t"
                "ds_read_b64 v[236:237], %[kb] offset:3424\n\t"
                "ds_read_b64 v[238:239], %[kb] offset:3432\n\t"
                "s_waitcnt lgkmcnt(9)\n\t"
                "v_mfma_f32_32x32x16_bf16 %[a1], v[208:211], v[212:215], %[a1]\n\t"
                "ds_read_b64 v[240:241], %[kb] offset:1344\n\t"
                "ds_read_b64 v[242:243], %[kb] offset:1352\n\t"
                "ds_read_b128 v[244:247], %[ub] offset:3392\n\t"
                "s_waitcnt lgkmcnt(11)\n\t"
                "v_mfma_f32_32x32x16_bf16 %[a0], v[208:211], v[216:219], %[a0]\n\t"
                "ds_read_b128 v[248:251], %[ub] offset:832\n\t"
                "s_waitcnt lgkmcnt(10)\n\t"
                "v_mfma_f32_32x32x16_bf16 %[a1], v[220:223], v[216:219], %[a1]\n\t"
                "ds_read_b64 v[252:253], %[kb] offset:3392\n\t"
                "ds_read_b64 v[254:255], %[kb] offset:3400\n\t"
                "s_waitcnt lgkmcnt(9)\n\t"
                "v_mfma_f32_32x32x16_bf16 %[a1], v[224:227], v[228:231], %[a1]\n\t"
                "ds_read_b64 v[192:193], %[kb] offset:1440\n\t"
                "ds_read_b64 v[194:195], %[kb] offset:1448\n\t"
                "ds_read_b128 v[196:199], %[ub] offset:3280\n\t"
                "s_waitcnt lgkmcnt(11)\n\t"
                "v_mfma_f32_32x32x16_bf16 %[a0], v[224:227], v[232:235], %[a0]\n\t"
                "ds_read_b128 v[200:203], %[ub] offset:720\n\t"
                "s_waitcnt lgkmcnt(10)\n\t"
                "v_mfma_f32_32x32x16_bf16 %[a1], v[236:239], v[232:235], %[a1]\n\t"
                "ds_read_b64 v[204:205], %[kb] offset:3488\n\t"
                "ds_read_b64 v[206:207], %[kb] offset:3496\n\t"
                "s_waitcnt lgkmcnt(9)\n\t"
                "v_mfma_f32_32x32x16_bf16 %[a1], v[240:243], v[244:247], %[a1]\n\t"
                "ds_read_b64 v[208:209], %[kb] offset:1408\n\t"
                "ds_read_b64 v[210:211], %[kb] offset:1416\n\t"
                "ds_read_b128 v[212:215], %[ub] offset:3312\n\t"
                "s_waitcnt lgkmcnt(11)\n\t"
                "v_mfma_f32_32x32x16_bf16 %[a0], v[240:243], v[248:251], %[a0]\n\t"
                "ds_read_b128 v[216:219], %[ub] offset:752\n\t"
                "s_waitcnt lgkmcnt(10)\n\t"
                "v_mfma_f32_32x32x16_bf16 %[a1], v[252:255], v[248:251], %[a1]\n\t"
                "ds_read_b64 v[220:221], %[kb] offset:3456\n\t"
                "ds_read_b64 v[222:223], %[kb] offset:3464\n\t"
                "s_waitcnt lgkmcnt(9)\n\t"
                "v_mfma_f32_32x32x16_bf16 %[a1], v[192:195], v[196:199], %[a1]\n\t"
                "ds_read_b64 v[224:225], %[kb] offset:1504\n\t"
                "ds_read_b64 v[226:227], %[kb] offset:1512\n\t"
                "ds_read_b128 v[228:231], %[ub] offset:3200\n\t"
                "s_waitcnt lgkmcnt(11)\n\t"
                "v_mfma_f32_32x32x16_bf16 %[a0], v[192:195], v[200:203], %[a0]\n\t"
                "ds_read_b128 v[232:235], %[ub] offset:640\n\t"
                "s_waitcnt lgkmcnt(10)\n\t"
                "v_mfma_f32_32x32x16_bf16 %[a1], v[204:207], v[200:203], %[a1]\n\t"
                "ds_read_b64 v[236:237], %[kb] offset:3552\n\t"
                "ds_read_b64 v[238:239], %[kb] offset:3560\n\t"
                "s_waitcnt lgkmcnt(9)\n\t"
                "v_mfma_f32_32x32x16_bf16 %[a1], v[208:211], v[212:215], %[a1]\n\t"
                "ds_read_b64 v[240:241], %[kb] offset:1472\n\t"
                "ds_read_b64 v[242:243], %[kb] offset:1480\n\t"
                "ds_read_b128 v[244:247], %[ub] offset:3232\n\t"
                "s_waitcnt lgkmcnt(11)\n\t"
                "v_mfma_f32_32x32x16_bf16 %[a0], v[208:211], v[216:219], %[a0]\n\t"
                "ds_read_b128 v[248:251], %[ub] offset:672\n\t"
                "s_waitcnt lgkmcnt(10)\n\t"
                "v_mfma_f32_32x32x16_bf16 %[a1], v[220:223], v[216:219], %[a1]\n\t"
                "ds_read_b64 v[252:253], %[kb] offset:3520\n\t"
                "ds_read_b64 v[254:255], %[kb] offset:3528\n\t"
                "s_waitcnt lgkmcnt(9)\n\t"
                "v_mfma_f32_32x32x16_bf16 %[a1], v[224:227], v[228:231], %[a1]\n\t"
                "ds_read_b64 v[192:193], %[kb] offset:1568\n\t"
                "ds_read_b64 v[194:195], %[kb] offset:1576\n\t"
                "ds_read_b128 v[196:199], %[ub] offset:3120\n\t"
                "s_waitcnt lgkmcnt(11)\n\t"
                "v_mfma_f32_32x32x16_bf16 %[a0], v[224:227], v[232:235], %[a0]\n\t"
                "ds_read_b128 v[200:203], %[ub] offset:560\n\t"
                "s_waitcnt lgkmcnt(10)\n\t"
                "v_mfma_f32_32x32x16_bf16 %[a1], v[236:239], v[232:235], %[a1]\n\t"
                "ds_read_b64 v[204:205], %[kb] offset:3616\n\t"
                "ds_read_b64 v[206:207], %[kb] offset:3624\n\t"
                "s_waitcnt lgkmcnt(9)\n\t"
                "v_mfma_f32_32x32x16_bf16 %[a1], v[240:243], v[244:247], %[a1]\n\t"
                "ds_read_b64 v[208:209], %[kb] offset:1536\n\t"
                "ds_read_b64 v[210:211], %[kb] offset:1544\n\t"
                "ds_read_b128 v[212:215], %[ub] offset:3152\n\t"
                "s_waitcnt lgkmcnt(11)\n\t"
                "v_mfma_f32_32x32x16_bf16 %[a0], v[240:243], v[248:251], %[a0]\n\t"
                "ds_read_b128 v[216:219], %[ub] offset:592\n\t"
                "s_waitcnt lgkmcnt(10)\n\t"
                "v_mfma_f32_32x32x16_bf16 %[a1], v[252:255], v[248:251], %[a1]\n\t"
                "ds_read_b64 v[220:221], %[kb] offset:3584\n\t"
                "ds_read_b64 v[222:223], %[kb] offset:3592\n\t"
                "s_waitcnt lgkmcnt(9)\n\t"
                "v_mfma_f32_32x32x16_bf16 %[a1], v[192:195], v[196:199], %[a1]\n\t"
                "ds_read_b64 v[224:225], %[kb] offset:1632\n\t"
                "ds_read_b64 v[226:227], %[kb] offset:1640\n\t"
                "ds_read_b128 v[228:231], %[ub] offset:3040\n\t"
                "s_waitcnt lgkmcnt(11)\n\t"
                "v_mfma_f32_32x32x16_bf16 %[a0], v[192:195], v[200:203], %[a0]\n\t"
                "ds_read_b128 v[232:235], %[ub] offset:480\n\t"
                "s_waitcnt lgkmcnt(10)\n\t"
                "v_mfma_f32_32x32x16_bf16 %[a1], v[204:207], v[200:203], %[a1]\n\t"
                "ds_read_b64 v[236:237], %[kb] offset:3680\n\t"
                "ds_read_b64 v[238:239], %[kb] offset:3688\n\t"
                "s_waitcnt lgkmcnt(9)\n\t"
                "v_mfma_f32_32x32x16_bf16 %[a1], v[208:211], v[212:215], %[a1]\n\t"
                "ds_read_b64 v[240:241], %[kb] offset:1600\n\t"
                "ds_read_b64 v[242:243], %[kb] offset:1608\n\t"
                "ds_read_b128 v[244:247], %[ub] offset:3072\n\t"
                "s_waitcnt lgkmcnt(11)\n\t"
                "v_mfma_f32_32x32x16_bf16 %[a0], v[208:211], v[216:219], %[a0]\n\t"
                "ds_read_b128 v[248:251], %[ub] offset:512\n\t"
                "s_waitcnt lgkmcnt(10)\n\t"
                "v_mfma_f32_32x32x16_bf16 %[a1], v[220:223], v[216:219], %[a1]\n\t"
                "ds_read_b64 v[252:253], %[kb] offset:3648\n\t"
                "ds_read_b64 v[254:255], %[kb] offset:3656\n\t"
                "s_waitcnt lgkmcnt(9)\n\t"
                "v_mfma_f32_32x32x16_bf16 %[a1], v[224:227], v[228:231], %[a1]\n\t"
                "ds_read_b64 v[192:193], %[kb] offset:1696\n\t"
                "ds_read_b64 v[194:195], %[kb] offset:1704\n\t"
                "ds_read_b128 v[196:199], %[ub] offset:2960\n\t"
                "s_waitcnt lgkmcnt(11)\n\t"
                "v_mfma_f32_32x32x16_bf16 %[a0], v[224:227], v[232:235], %[a0]\n\t"
                "ds_read_b128 v[200:203], %[ub] offset:400\n\t"
                "s_waitcnt lgkmcnt(10)\n\t"
                "v_mfma_f32_32x32x16_bf16 %[a1], v[236:239], v[232:235], %[a1]\n\t"
                "ds_read_b64 v[204:205], %[kb] offset:3744\n\t"
                "ds_read_b64 v[206:207], %[kb] offset:3752\n\t"
                "s_waitcnt lgkmcnt(9)\n\t"
                "v_mfma_f32_32x32x16_bf16 %[a1], v[240:243], v[244:247], %[a1]\n\t"
                "ds_read_b64 v[208:209], %[kb] offset:1664\n\t"
                "ds_read_b64 v[210:211], %[kb] offset:1672\n\t"
                "ds_read_b128 v[212:215], %[ub] offset:2992\n\t"
                "s_waitcnt lgkmcnt(11)\n\t"
                "v_mfma_f32_32x32x16_bf16 %[a0], v[240:243], v[248:251], %[a0]\n\t"
                "ds_read_b128 v[216:219], %[ub] offset:432\n\t"
                "s_waitcnt lgkmcnt(10)\n\t"
                "v_mfma_f32_32x32x16_bf16 %[a1], v[252:255], v[248:251], %[a1]\n\t"
                "ds_read_b64 v[220:221], %[kb] offset:3712\n\t"
                "ds_read_b64 v[222:223], %[kb] offset:3720\n\t"
                "s_waitcnt lgkmcnt(9)\n\t"
                "v_mfma_f32_32x32x16_bf16 %[a1], v[192:195], v[196:199], %[a1]\n\t"
                "ds_read_b64 v[224:225], %[kb] offset:1760\n\t"
                "ds_read_b64 v[226:227], %[kb] offset:1768\n\t"
                "ds_read_b128 v[228:231], %[ub] offset:2880\n\t"
                "s_waitcnt lgkmcnt(11)\n\t"
                "v_mfma_f32_32x32x16_bf16 %[a0], v[192:195], v[200:203], %[a0]\n\t"
                "ds_read_b128 v[232:235], %[ub] offset:320\n\t"
                "s_waitcnt lgkmcnt(10)\n\t"
                "v_mfma_f32_32x32x16_bf16 %[a1], v[204:207], v[200:203], %[a1]\n\t"
                "ds_read_b64 v[236:237], %[kb] offset:3808\n\t"
                "ds_read_b64 v[238:239], %[kb] offset:3816\n\t"
                "s_waitcnt lgkmcnt(9)\n\t"
                "v_mfma_f32_32x32x16_bf16 %[a1], v[208:211], v[212:215], %[a1]\n\t"
                "ds_read_b64 v[240:241], %[kb] offset:1728\n\t"
                "ds_read_b64 v[242:243], %[kb] offset:1736\n\t"
                "ds_read_b128 v[244:247], %[ub] offset:2912\n\t"
                "s_waitcnt lgkmcnt(11)\n\t"
                "v_mfma_f32_32x32x16_bf16 %[a0], v[208:211], v[216:219], %[a0]\n\t"
                "ds_read_b128 v[248:251], %[ub] offset:352\n\t"
                "s_waitcnt lgkmcnt(10)\n\t"
                "v_mfma_f32_32x32x16_bf16 %[a1], v[220:223], v[216:219], %[a1]\n\t"
                "ds_read_b64 v[252:253], %[kb] offset:3776\n\t"
                "ds_read_b64 v[254:255], %[kb] offset:3784\n\t"
                "s_waitcnt lgkmcnt(9)\n\t"
                "v_mfma_f32_32x32x16_bf16 %[a1], v[224:227], v[228:231], %[a1]\n\t"
                "ds_read_b64 v[192:193], %[kb] offset:1824\n\t"
                "ds_read_b64 v[194:195], %[kb] offset:1832\n\t"
                "ds_read_b128 v[196:199], %[ub] offset:2800\n\t"
                "s_waitcnt lgkmcnt(11)\n\t"
                "v_mfma_f32_32x32x16_bf16 %[a0], v[224:227], v[232:235], %[a0]\n\t"
                "ds_read_b128 v[200:203], %[ub] offset:240\n\t"
                "s_waitcnt lgkmcnt(10)\n\t"
                "v_mfma_f32_32x32x16_bf16 %[a1], v[236:239], v[232:235], %[a1]\n\t"
                "ds_read_b64 v[204:205], %[kb] offset:3872\n\t"
                "ds_read_b64 v[206:207], %[kb] offset:3880\n\t"
                "s_waitcnt lgkmcnt(9)\n\t"
                "v_mfma_f32_32x32x16_bf16 %[a1], v[240:243], v[244:247], %[a1]\n\t"
                "ds_read_b64 v[208:209], %[kb] offset:1792\n\t"
                "ds_read_b64 v[210:211], %[kb] offset:1800\n\t"
                "ds_read_b128 v[212:215], %[ub] offset:2832\n\t"
                "s_waitcnt lgkmcnt(11)\n\t"
                "v_mfma_f32_32x32x16_bf16 %[a0], v[240:243], v[248:251], %[a0]\n\t"
                "ds_read_b128 v[216:219], %[ub] offset:272\n\t"
                "s_waitcnt lgkmcnt(10)\n\t"
                "v_mfma_f32_32x32x16_bf16 %[a1], v[252:255], v[248:251], %[a1]\n\t"
                "ds_read_b64 v[220:221], %[kb] offset:3840\n\t"
                "ds_read_b64 v[222:223], %[kb] offset:3848\n\t"
                "s_waitcnt lgkmcnt(9)\n\t"
                "v_mfma_f32_32x32x16_bf16 %[a1], v[192:195], v[196:199], %[a1]\n\t"
                "ds_read_b64 v[224:225], %[kb] offset:1888\n\t"
                "ds_read_b64 v[226:227], %[kb] offset:1896\n\t"
                "ds_read_b128 v[228:231], %[ub] offset:2720\n\t"
                "s_waitcnt lgkmcnt(11)\n\t"
                "v_mfma_f32_32x32x16_bf16 %[a0], v[192:195], v[200:203], %[a0]\n\t"
                "ds_read_b128 v[232:235], %[ub] offset:160\n\t"
                "s_waitcnt lgkmcnt(10)\n\t"
                "v_mfma_f32_32x32x16_bf16 %[a1], v[204:207], v[200:203], %[a1]\n\t"
                "ds_read_b64 v[236:237], %[kb] offset:3936\n\t"
                "ds_read_b64 v[238:239], %[kb] offset:3944\n\t"
                "s_waitcnt lgkmcnt(9)\n\t"
                "v_mfma_f32_32x32x16_bf16 %[a1], v[208:211], v[212:215], %[a1]\n\t"
                "ds_read_b64 v[240:241], %[kb] offset:1856\n\t"
                "ds_read_b64 v[242:243], %[kb] offset:1864\n\t"
                "ds_read_b128 v[244:247], %[ub] offset:2752\n\t"
                "s_waitcnt lgkmcnt(11)\n\t"
                "v_mfma_f32_32x32x16_bf16 %[a0], v[208:211], v[216:219], %[a0]\n\t"
                "ds_read_b128 v[248:251], %[ub] offset:192\n\t"
                "s_waitcnt lgkmcnt(10)\n\t"
                "v_mfma_f32_32x32x16_bf16 %[a1], v[220:223], v[216:219], %[a1]\n\t"
                "ds_read_b64 v[252:253], %[kb] offset:3904\n\t"
                "ds_read_b64 v[254:255], %[kb] offset:3912\n\t"
                "s_waitcnt lgkmcnt(9)\n\t"
                "v_mfma_f32_32x32x16_bf16 %[a1], v[224:227], v[228:231], %[a1]\n\t"
                "ds_read_b64 v[192:193], %[kb] offset:1952\n\t"
                "ds_read_b64 v[194:195], %[kb] offset:1960\n\t"
                "ds_read_b128 v[196:199], %[ub] offset:2640\n\t"
                "s_waitcnt lgkmcnt(11)\n\t"
                "v_mfma_f32_32x32x16_bf16 %[a0], v[224:227], v[232:235], %[a0]\n\t"
                "ds_read_b128 v[200:203], %[ub] offset:80\n\t"
                "s_waitcnt lgkmcnt(10)\n\t"
                "v_mfma_f32_32x32x16_bf16 %[a1], v[236:239], v[232:235], %[a1]\n\t"
                "ds_read_b64 v[204:205], %[kb] offset:4000\n\t"
                "ds_read_b64 v[206:207], %[kb] offset:4008\n\t"
                "s_waitcnt lgkmcnt(9)\n\t"
                "v_mfma_f32_32x32x16_bf16 %[a1], v[240:243], v[244:247], %[a1]\n\t"
                "ds_read_b64 v[208:209], %[kb] offset:1920\n\t"
                "ds_read_b64 v[210:211], %[kb] offset:1928\n\t"
                "ds_read_b128 v[212:215], %[ub] offset:2672\n\t"
                "s_waitcnt lgkmcnt(11)\n\t"
                "v_mfma_f32_32x32x16_bf16 %[a0], v[240:243], v[248:251], %[a0]\n\t"
                "ds_read_b128 v[216:219], %[ub] offset:112\n\t"
                "s_waitcnt lgkmcnt(10)\n\t"
                "v_mfma_f32_32x32x16_bf16 %[a1], v[252:255], v[248:251], %[a1]\n\t"
                "ds_read_b64 v[220:221], %[kb] offset:3968\n\t"
                "ds_read_b64 v[222:223], %[kb] offset:3976\n\t"
                "s_waitcnt lgkmcnt(9)\n\t"
                "v_mfma_f32_32x32x16_bf16 %[a1], v[192:195], v[196:199], %[a1]\n\t"
                "ds_read_b64 v[224:225], %[kb] offset:2016\n\t"
                "ds_read_b64 v[226:227], %[kb] offset:2024\n\t"
                "ds_read_b128 v[228:231], %[ub] offset:2560\n\t"
                "s_waitcnt lgkmcnt(11)\n\t"
                "v_mfma_f32_32x32x16_bf16 %[a0], v[192:195], v[200:203], %[a0]\n\t"
                "ds_read_b128 v[232:235], %[ub] offset:0\n\t"
                "s_waitcnt lgkmcnt(10)\n\t"
                "v_mfma_f32_32x32x16_bf16 %[a1], v[204:207], v[200:203], %[a1]\n\t"
                "ds_read_b64 v[236:237], %[kb] offset:4064\n\t"
                "ds_read_b64 v[238:239], %[kb] offset:4072\n\t"
                "s_waitcnt lgkmcnt(9)\n\t"
                "v_mfma_f32_32x32x16_bf16 %[a1], v[208:211], v[212:215], %[a1]\n\t"
                "ds_read_b64 v[240:241], %[kb] offset:1984\n\t"
                "ds_read_b64 v[242:243], %[kb] offset:1992\n\t"
                "ds_read_b128 v[244:247], %[ub] offset:2592\n\t"
                "s_waitcnt lgkmcnt(11)\n\t"
                "v_mfma_f32_32x32x16_bf16 %[a0], v[208:211], v[216:219], %[a0]\n\t"
                "ds_read_b128 v[248:251], %[ub] offset:32\n\t"
                "s_waitcnt lgkmcnt(10)\n\t"
                "v_mfma_f32_32x32x16_bf16 %[a1], v[220:223], v[216:219], %[a1]\n\t"
                "ds_read_b64 v[252:253], %[kb] offset:4032\n\t"
                "ds_read_b64 v[254:255], %[kb] offset:4040\n\t"
                "s_waitcnt lgkmcnt(9)\n\t"
                "v_mfma_f32_32x32x16_bf16 %[a1], v[224:227], v[228:231], %[a1]\n\t"
                "s_waitcnt lgkmcnt(8)\n\t"
                "v_mfma_f32_32x32x16_bf16 %[a0], v[224:227], v[232:235], %[a0]\n\t"
                "s_waitcnt lgkmcnt(6)\n\t"
                "v_mfma_f32_32x32x16_bf16 %[a1], v[236:239], v[232:235], %[a1]\n\t"
                "s_waitcnt lgkmcnt(3)\n\t"
                "v_mfma_f32_32x32x16_bf16 %[a1], v[240:243], v[244:247], %[a1]\n\t"
                "s_waitcnt lgkmcnt(2)\n\t"
                "v_mfma_f32_32x32x16_bf16 %[a0], v[240:243], v[248:251], %[a0]\n\t"
                "s_waitcnt lgkmcnt(0)\n\t"
                "v_mfma_f32_32x32x16_bf16 %[a1], v[252:255], v[248:251], %[a1]\n\t"
                "s_nop 7\n\t"
                "s_nop 3\n\t"
                : [a0] "=&v"(acc0), [a1] "=&v"(acc1) : [kb] "v"(kb), [ub] "v"(ub0 + c8 * CV_U) : "memory", "v192", "v193", "v194", "v195", "v196", "v197", "v198", "v199", "v200", "v201", "v202", "v203", "v204", "v205", "v206", "v207", "v208", "v209", "v210", "v211", "v212", "v213", "v214", "v215", "v216", "v217", "v218", "v219", "v220", "v221", "v222", "v223", "v224", "v225", "v226", "v227", "v228", "v229", "v230", "v231", "v232", "v233", "v234", "v235", "v236", "v237", "v238", "v239", "v240", "v241", "v242", "v243", "v244", "v245", "v246", "v247", "v248", "v249", "v250", "v251", "v252", "v253", "v254", "v255");
            unsigned char* uc = lds + c8 * CV_U + (32 + b) * CV_RP + kg * 8;
#pragma unroll
            for (int q = 0; q < 4; ++q) {
                *(v2u*)(uc + q * 16) = (v2u){pack2(acc0[4 * q], acc0[4 * q + 1]), pack2(acc0[4 * q + 2], acc0[4 * q + 3])};
                *(v2u*)(uc + 32 * CV_RP + q * 16) = (v2u){pack2(acc1[4 * q], acc1[4 * q + 1]), pack2(acc1[4 * q + 2], acc1[4 * q + 3])}; }
            __builtin_amdgcn_wave_barrier();
        }
    __syncthreads();
#pragma unroll
    for (int G = 0; G < 4; ++G) { const v4u lo = *(const v4u*)(urow + G * 16), hh = *(const v4u*)(urow + CV_U + G * 16); v4u o[2];
#pragma unroll
        for (int i = 0; i < 8; ++i) { const unsigned u0 = ur[G][(7 - i) >> 1], u1 = ur[4 + G][(7 - i) >> 1], qq = qr[2 * G + (i >> 2)][i & 3];
            const unsigned ud = pack2(((i & 1) ? blo(u0) : bhi(u0)) * blo(dv), ((i & 1) ? blo(u1) : bhi(u1)) * bhi(dv));
            const unsigned y = pack2(((i & 1) ? bhi(lo[i >> 1]) : blo(lo[i >> 1])) + blo(ud), ((i & 1) ? bhi(hh[i >> 1]) : blo(hh[i >> 1])) + bhi(ud));
            o[i >> 2][i & 3] = pack2(blo(qq) * blo(y), bhi(qq) * bhi(y)); }
        st16(r, W_A + un * 32768 + 2 * G * 4096 + tid * 16, o[0]); st16(r, W_A + un * 32768 + (2 * G + 1) * 4096 + tid * 16, o[1]); }
    DRAIN(); __syncthreads();
    if (tid == 0) __hip_atomic_fetch_add((unsigned*)(p.ws + F_CVY + (un >> 3) * 128), 1u, __ATOMIC_RELAXED, __HIP_MEMORY_SCOPE_AGENT);
    return true;
}

__device__ __forceinline__ void proj_block(const Params& p, unsigned char* lds, const int wv, const int h, const int c, const int j) {
    const int w = wv, l = otid() & 63, n = l & 15, g = l >> 4, tm = w >> 1, tn = w & 1;
    const unsigned lb = __builtin_amdgcn_readfirstlane((unsigned)(size_t)lds);
    const v4u sx = mk_srd(p.Aq, S * D * 2), sq = mk_srd(c < 2 ? p.Wq : p.Wk, 1024 * 4096 * 2), sv = mk_srd(p.Wv, 1024 * 4096 * 2), sw = mk_srd(p.ws, W_TOTAL);
    const unsigned lx = lb + (32 * tm + n) * 128 + ((g ^ (n & 7)) << 4), lr = lb + 131072, rr = 8 * g + (n >> 2);
    const int cv = ((c & 1) ^ 1) * 2 + (c >> 1);
    const unsigned aq = (c < 2 ? W_QX : W_KX) + (tm * 32 + h) * 8192 + (2 * (c & 1) + tn) * 1024 + l * 16;
    const int hq = h & 7, tk = ((hq - (hq > 1) - (hq > 5)) * 4 + c) * 4 + w, ts6 = tk % 6, tch = 15 - tk / 6, tx = tk - 6 * GTOUCH;
    const bf16_t* tp = (h & 3) == 1 ? p.gate_w : tx < 0 ? (ts6 % 3 == 0 ? p.Wq : ts6 % 3 == 1 ? p.Wk : p.Wv) + (size_t)(tch * 64 + l) * 4096 + ((h & 24) + 1 + (ts6 / 3) * 4) * 128 + 64
                     : tx < XTOUCH ? p.Aq + (tx * 64 + l) * 64 : p.gate_w;
    unsigned t0, xo, so, cn; unsigned long long ts, ta, tb, tc, td;
    asm volatile(
        "s_mov_b32 %[xo], 0\n\t"
        "s_mov_b32 %[so], 0\n\t"
        "s_mov_b32 %[cn], 1\n\t"
        "global_load_dword v88, v[114:115], off\n\t"
        "s_add_u32 m0, %[ld], 0\n\t"
        "s_add_u32 %[t0], %[xo], 0\n\t"
        "buffer_load_dwordx4 v100, %[sx], %[t0] offen lds\n\t"
        "s_add_u32 m0, %[ld], 1024\n\t"
        "s_add_u32 %[t0], %[xo], 16384\n\t"
        "buffer_load_dwordx4 v100, %[sx], %[t0] offen lds\n\t"
        "s_add_u32 m0, %[ld], 8192\n\t"
        "s_add_u32 %[t0], %[xo], 128\n\t"
        "buffer_load_dwordx4 v100, %[sx], %[t0] offen lds\n\t"
        "s_add_u32 m0, %[ld], 9216\n\t"
        "s_add_u32 %[t0], %[xo], 16512\n\t"
        "buffer_load_dwordx4 v100, %[sx], %[t0] offen lds\n\t"
        "s_add_u32 m0, %[ld], 16384\n\t"
        "s_add_u32 %[t0], %[xo], 256\n\t"
        "buffer_load_dwordx4 v100, %[sx], %[t0] offen lds\n\t"
        "s_add_u32 m0, %[ld], 17408\n\t"
        "s_add_u32 %[t0], %[xo], 16640\n\t"
        "buffer_load_dwordx4 v100, %[sx], %[t0] offen lds\n\t"
        "s_add_u32 m0, %[ld], 24576\n\t"
        "s_add_u32 %[t0], %[xo], 384\n\t"
        "buffer_load_dwordx4 v100, %[sx], %[t0] offen lds\n\t"
        "s_add_u32 m0, %[ld], 25600\n\t"
        "s_add_u32 %[t0], %[xo], 16768\n\t"
        "buffer_load_dwordx4 v100, %[sx], %[t0] offen lds\n\t"
        "s_mov_b32 %[t0], 0\n\t"
        "buffer_load_dwordx4 a[192:195], v101, %[sq], %[t0] offen\n\t"
        "buffer_load_dwordx4 a[200:203], v112, %[sv], %[t0] offen\n\t"
        "s_mov_b32 %[t0], 262144\n\t"
        "buffer_load_dwordx4 a[196:199], v101, %[sq], %[t0] offen\n\t"
        "s_mov_b32 %[t0], 524288\n\t"
        "buffer_load_dwordx4 a[204:207], v101, %[sq], %[t0] offen\n\t"
        "buffer_load_dwordx4 a[212:215], v112, %[sv], %[t0] offen\n\t"
        "s_mov_b32 %[t0], 786432\n\t"
        "buffer_load_dwordx4 a[208:211], v101, %[sq], %[t0] offen\n\t"
        "s_mov_b32 %[t0], 1048576\n\t"
        "buffer_load_dwordx4 a[216:219], v101, %[sq], %[t0] offen\n\t"
        "buffer_load_dwordx4 a[224:227], v112, %[sv], %[t0] offen\n\t"
        "s_mov_b32 %[t0], 1310720\n\t"
        "buffer_load_dwordx4 a[220:223], v101, %[sq], %[t0] offen\n\t"
        "s_mov_b32 %[t0], 1572864\n\t"
        "buffer_load_dwordx4 a[228:231], v101, %[sq], %[t0] offen\n\t"
        "buffer_load_dwordx4 a[236:239], v112, %[sv], %[t0] offen\n\t"
        "s_mov_b32 %[t0], 1835008\n\t"
        "buffer_load_dwordx4 a[232:235], v101, %[sq], %[t0] offen\n\t"
        "s_waitcnt vmcnt(9)\n\t"
        "ds_write_b128 v102, a[192:195] offset:0\n\t"
        "ds_write_b128 v102, a[196:199] offset:4096\n\t"
        "ds_write_b128 v116, a[200:203] offset:8192\n\t"
        "s_waitcnt lgkmcnt(0)\n\t"
        "s_barrier\n\t"
        "ds_read_b64_tr_b16 a[0:1], v103 offset:0\n\t"
        "ds_read_b64_tr_b16 a[2:3], v103 offset:512\n\t"
        "ds_read_b64_tr_b16 a[4:5], v103 offset:8\n\t"
        "ds_read_b64_tr_b16 a[6:7], v103 offset:520\n\t"
        "ds_read_b64_tr_b16 a[8:9], v104 offset:0\n\t"
        "ds_read_b64_tr_b16 a[10:11], v104 offset:256\n\t"
        "s_waitcnt lgkmcnt(5)\n\t"
        "ds_read_b64_tr_b16 a[12:13], v103 offset:4096\n\t"
        "ds_read_b64_tr_b16 a[14:15], v103 offset:4608\n\t"
        "ds_read_b64_tr_b16 a[16:17], v103 offset:4104\n\t"
        "ds_read_b64_tr_b16 a[18:19], v103 offset:4616\n\t"
        "ds_read_b64_tr_b16 a[20:21], v104 offset:2048\n\t"
        "ds_read_b64_tr_b16 a[22:23], v104 offset:2304\n\t"
        "ds_read_b128 v[56:59], v105 offset:0\n\t"
        "ds_read_b128 v[60:63], v105 offset:2048\n\t"
        "ds_read_b128 v[64:67], v106 offset:0\n\t"
        "ds_read_b128 v[68:71], v106 offset:2048\n\t"
        "s_add_u32 m0, %[ld], 32768\n\t"
        "s_add_u32 %[t0], %[xo], 512\n\t"
        "buffer_load_dwordx4 v100, %[sx], %[t0] offen lds\n\t"
        "s_add_u32 m0, %[ld], 33792\n\t"
        "s_add_u32 %[t0], %[xo], 16896\n\t"
        "buffer_load_dwordx4 v100, %[sx], %[t0] offen lds\n\t"
        "s_add_u32 m0, %[ld], 40960\n\t"
        "s_add_u32 %[t0], %[xo], 640\n\t"
        "buffer_load_dwordx4 v100, %[sx], %[t0] offen lds\n\t"
        "s_add_u32 m0, %[ld], 41984\n\t"
        "s_add_u32 %[t0], %[xo], 17024\n\t"
        "buffer_load_dwordx4 v100, %[sx], %[t0] offen lds\n\t"
        "s_add_u32 m0, %[ld], 49152\n\t"
        "s_add_u32 %[t0], %[xo], 768\n\t"
        "buffer_load_dwordx4 v100, %[sx], %[t0] offen lds\n\t"
        "s_add_u32 m0, %[ld], 50176\n\t"
        "s_add_u32 %[t0], %[xo], 17152\n\t"
        "buffer_load_dwordx4 v100, %[sx], %[t0] offen lds\n\t"
        "s_add_u32 m0, %[ld], 57344\n\t"
        "s_add_u32 %[t0], %[xo], 896\n\t"
        "buffer_load_dwordx4 v100, %[sx], %[t0] offen lds\n\t"
        "s_add_u32 m0, %[ld], 58368\n\t"
        "s_add_u32 %[t0], %[xo], 17280\n\t"
        "buffer_load_dwordx4 v100, %[sx], %[t0] offen lds\n\t"
        "s_mov_b32 %[t0], 2097152\n\t"
        "buffer_load_dwordx4 a[240:243], v101, %[sq], %[t0] offen\n\t"
        "buffer_load_dwordx4 a[248:251], v112, %[sv], %[t0] offen\n\t"
        "s_mov_b32 %[t0], 2359296\n\t"
        "buffer_load_dwordx4 a[244:247], v101, %[sq], %[t0] offen\n\t"
        "s_waitcnt vmcnt(17)\n\t"
        "ds_write_b128 v102, a[204:207] offset:16384\n\t"
        "ds_write_b128 v102, a[208:211] offset:20480\n\t"
        "ds_write_b128 v116, a[212:215] offset:24576\n\t"
        "s_waitcnt lgkmcnt(0)\n\t"
        "s_barrier\n\t"
        "v_mfma_f32_16x16x32_bf16 v[8:11], a[0:3], v[56:59], 0\n\t"
        "ds_read_b64_tr_b16 a[24:25], v103 offset:16384\n\t"
        "ds_read_b64_tr_b16 a[26:27], v103 offset:16896\n\t"
        "v_mfma_f32_16x16x32_bf16 v[12:15], a[4:7], v[56:59], 0\n\t"
        "ds_read_b64_tr_b16 a[28:29], v103 offset:16392\n\t"
        "ds_read_b64_tr_b16 a[30:31], v103 offset:16904\n\t"
        "v_mfma_f32_16x16x32_bf16 v[16:19], v[56:59], a[8:11], 0\n\t"
        "ds_read_b64_tr_b16 a[32:33], v104 offset:16384\n\t"
        "ds_read_b64_tr_b16 a[34:35], v104 offset:16640\n\t"
        "v_mfma_f32_16x16x32_bf16 v[20:23], a[0:3], v[60:63], 0\n\t"
        "s_waitcnt lgkmcnt(5)\n\t"
        "ds_read_b64_tr_b16 a[36:37], v103 offset:20480\n\t"
        "v_mfma_f32_16x16x32_bf16 v[24:27], a[4:7], v[60:63], 0\n\t"
        "ds_read_b64_tr_b16 a[38:39], v103 offset:20992\n\t"
        "ds_read_b64_tr_b16 a[40:41], v103 offset:20488\n\t"
        "v_mfma_f32_16x16x32_bf16 v[28:31], v[60:63], a[8:11], 0\n\t"
        "ds_read_b64_tr_b16 a[42:43], v103 offset:21000\n\t"
        "ds_read_b64_tr_b16 a[44:45], v104 offset:18432\n\t"
        "v_mfma_f32_16x16x32_bf16 v[8:11], a[12:15], v[64:67], v[8:11]\n\t"
        "ds_read_b64_tr_b16 a[46:47], v104 offset:18688\n\t"
        "ds_read_b128 v[72:75], v105 offset:8192\n\t"
        "v_mfma_f32_16x16x32_bf16 v[12:15], a[16:19], v[64:67], v[12:15]\n\t"
        "ds_read_b128 v[76:79], v105 offset:10240\n\t"
        "ds_read_b128 v[80:83], v106 offset:8192\n\t"
        "v_mfma_f32_16x16x32_bf16 v[16:19], v[64:67], a[20:23], v[16:19]\n\t"
        "ds_read_b128 v[84:87], v106 offset:10240\n\t"
        "v_mfma_f32_16x16x32_bf16 v[20:23], a[12:15], v[68:71], v[20:23]\n\t"
        "v_mfma_f32_16x16x32_bf16 v[24:27], a[16:19], v[68:71], v[24:27]\n\t"
        "v_mfma_f32_16x16x32_bf16 v[28:31], v[68:71], a[20:23], v[28:31]\n\t"
        "s_mov_b32 %[t0], 2621440\n\t"
        "buffer_load_dwordx4 a[252:255], v101, %[sq], %[t0] offen\n\t"
        "buffer_load_dwordx4 v[132:135], v112, %[sv], %[t0] offen\n\t"
        "s_mov_b32 %[t0], 2883584\n\t"
        "buffer_load_dwordx4 v[128:131], v101, %[sq], %[t0] offen\n\t"
        "s_waitcnt vmcnt(17)\n\t"
        "ds_write_b128 v102, a[216:219] offset:0\n\t"
        "ds_write_b128 v102, a[220:223] offset:4096\n\t"
        "ds_write_b128 v116, a[224:227] offset:8192\n\t"
        "s_waitcnt lgkmcnt(0)\n\t"
        "s_barrier\n\t"
        "v_mfma_f32_16x16x32_bf16 v[8:11], a[24:27], v[72:75], v[8:11]\n\t"
        "ds_read_b64_tr_b16 a[48:49], v103 offset:0\n\t"
        "ds_read_b64_tr_b16 a[50:51], v103 offset:512\n\t"
        "v_mfma_f32_16x16x32_bf16 v[12:15], a[28:31], v[72:75], v[12:15]\n\t"
        "ds_read_b64_tr_b16 a[52:53], v103 offset:8\n\t"
        "ds_read_b64_tr_b16 a[54:55], v103 offset:520\n\t"
        "v_mfma_f32_16x16x32_bf16 v[16:19], v[72:75], a[32:35], v[16:19]\n\t"
        "ds_read_b64_tr_b16 a[56:57], v104 offset:0\n\t"
        "ds_read_b64_tr_b16 a[58:59], v104 offset:256\n\t"
        "v_mfma_f32_16x16x32_bf16 v[20:23], a[24:27], v[76:79], v[20:23]\n\t"
        "s_waitcnt lgkmcnt(5)\n\t"
        "ds_read_b64_tr_b16 a[60:61], v103 offset:4096\n\t"
        "v_mfma_f32_16x16x32_bf16 v[24:27], a[28:31], v[76:79], v[24:27]\n\t"
        "ds_read_b64_tr_b16 a[62:63], v103 offset:4608\n\t"
        "ds_read_b64_tr_b16 a[64:65], v103 offset:4104\n\t"
        "v_mfma_f32_16x16x32_bf16 v[28:31], v[76:79], a[32:35], v[28:31]\n\t"
        "ds_read_b64_tr_b16 a[66:67], v103 offset:4616\n\t"
        "ds_read_b64_tr_b16 a[68:69], v104 offset:2048\n\t"
        "v_mfma_f32_16x16x32_bf16 v[8:11], a[36:39], v[80:83], v[8:11]\n\t"
        "ds_read_b64_tr_b16 a[70:71], v104 offset:2304\n\t"
        "ds_read_b128 v[56:59], v105 offset:16384\n\t"
        "v_mfma_f32_16x16x32_bf16 v[12:15], a[40:43], v[80:83], v[12:15]\n\t"
        "ds_read_b128 v[60:63], v105 offset:18432\n\t"
        "ds_read_b128 v[64:67], v106 offset:16384\n\t"
        "v_mfma_f32_16x16x32_bf16 v[16:19], v[80:83], a[44:47], v[16:19]\n\t"
        "ds_read_b128 v[68:71], v106 offset:18432\n\t"
        "v_mfma_f32_16x16x32_bf16 v[20:23], a[36:39], v[84:87], v[20:23]\n\t"
        "v_mfma_f32_16x16x32_bf16 v[24:27], a[40:43], v[84:87], v[24:27]\n\t"
        "v_mfma_f32_16x16x32_bf16 v[28:31], v[84:87], a[44:47], v[28:31]\n\t"
        "s_mov_b32 %[t0], 3145728\n\t"
        "buffer_load_dwordx4 v[136:139], v101, %[sq], %[t0] offen\n\t"
        "buffer_load_dwordx4 v[144:147], v112, %[sv], %[t0] offen\n\t"
        "s_mov_b32 %[t0], 3407872\n\t"
        "buffer_load_dwordx4 v[140:143], v101, %[sq], %[t0] offen\n\t"
        "s_waitcnt vmcnt(17)\n\t"
        "ds_write_b128 v102, a[228:231] offset:16384\n\t"
        "ds_write_b128 v102, a[232:235] offset:20480\n\t"
        "ds_write_b128 v116, a[236:239] offset:24576\n\t"
        "s_waitcnt lgkmcnt(0)\n\t"
        "s_barrier\n\t"
        "v_mfma_f32_16x16x32_bf16 v[8:11], a[48:51], v[56:59], v[8:11]\n\t"
        "ds_read_b64_tr_b16 a[72:73], v103 offset:16384\n\t"
        "ds_read_b64_tr_b16 a[74:75], v103 offset:16896\n\t"
        "v_mfma_f32_16x16x32_bf16 v[12:15], a[52:55], v[56:59], v[12:15]\n\t"
        "ds_read_b64_tr_b16 a[76:77], v103 offset:16392\n\t"
        "ds_read_b64_tr_b16 a[78:79], v103 offset:16904\n\t"
        "v_mfma_f32_16x16x32_bf16 v[16:19], v[56:59], a[56:59], v[16:19]\n\t"
        "ds_read_b64_tr_b16 a[80:81], v104 offset:16384\n\t"
        "ds_read_b64_tr_b16 a[82:83], v104 offset:16640\n\t"
        "v_mfma_f32_16x16x32_bf16 v[20:23], a[48:51], v[60:63], v[20:23]\n\t"
        "s_waitcnt lgkmcnt(5)\n\t"
        "ds_read_b64_tr_b16 a[84:85], v103 offset:20480\n\t"
        "v_mfma_f32_16x16x32_bf16 v[24:27], a[52:55], v[60:63], v[24:27]\n\t"
        "ds_read_b64_tr_b16 a[86:87], v103 offset:20992\n\t"
        "ds_read_b64_tr_b16 a[88:89], v103 offset:20488\n\t"
        "v_mfma_f32_16x16x32_bf16 v[28:31], v[60:63], a[56:59], v[28:31]\n\t"
        "ds_read_b64_tr_b16 a[90:91], v103 offset:21000\n\t"
        "ds_read_b64_tr_b16 a[92:93], v104 offset:18432\n\t"
        "v_mfma_f32_16x16x32_bf16 v[8:11], a[60:63], v[64:67], v[8:11]\n\t"
        "ds_read_b64_tr_b16 a[94:95], v104 offset:18688\n\t"
        "ds_read_b128 v[72:75], v105 offset:24576\n\t"
        "v_mfma_f32_16x16x32_bf16 v[12:15], a[64:67], v[64:67], v[12:15]\n\t"
        "ds_read_b128 v[76:79], v105 offset:26624\n\t"
        "ds_read_b128 v[80:83], v106 offset:24576\n\t"
        "v_mfma_f32_16x16x32_bf16 v[16:19], v[64:67], a[68:71], v[16:19]\n\t"
        "ds_read_b128 v[84:87], v106 offset:26624\n\t"
        "v_mfma_f32_16x16x32_bf16 v[20:23], a[60:63], v[68:71], v[20:23]\n\t"
        "v_mfma_f32_16x16x32_bf16 v[24:27], a[64:67], v[68:71], v[24:27]\n\t"
        "v_mfma_f32_16x16x32_bf16 v[28:31], v[68:71], a[68:71], v[28:31]\n\t"
        "s_mov_b32 %[t0], 3670016\n\t"
        "buffer_load_dwordx4 v[148:151], v101, %[sq], %[t0] offen\n\t"
        "buffer_load_dwordx4 v[156:159], v112, %[sv], %[t0] offen\n\t"
        "s_mov_b32 %[t0], 3932160\n\t"
        "buffer_load_dwordx4 v[152:155], v101, %[sq], %[t0] offen\n\t"
        "s_waitcnt vmcnt(9)\n\t"
        "ds_write_b128 v102, a[240:243] offset:0\n\t"
        "ds_write_b128 v102, a[244:247] offset:4096\n\t"
        "ds_write_b128 v116, a[248:251] offset:8192\n\t"
        "s_waitcnt lgkmcnt(0)\n\t"
        "s_barrier\n\t"
        "v_mfma_f32_16x16x32_bf16 v[8:11], a[72:75], v[72:75], v[8:11]\n\t"
        "ds_read_b64_tr_b16 a[96:97], v103 offset:0\n\t"
        "ds_read_b64_tr_b16 a[98:99], v103 offset:512\n\t"
        "v_mfma_f32_16x16x32_bf16 v[12:15], a[76:79], v[72:75], v[12:15]\n\t"
        "ds_read_b64_tr_b16 a[100:101], v103 offset:8\n\t"
        "ds_read_b64_tr_b16 a[102:103], v103 offset:520\n\t"
        "v_mfma_f32_16x16x32_bf16 v[16:19], v[72:75], a[80:83], v[16:19]\n\t"
        "ds_read_b64_tr_b16 a[104:105], v104 offset:0\n\t"
        "ds_read_b64_tr_b16 a[106:107], v104 offset:256\n\t"
        "v_mfma_f32_16x16x32_bf16 v[20:23], a[72:75], v[76:79], v[20:23]\n\t"
        "s_waitcnt lgkmcnt(5)\n\t"
        "ds_read_b64_tr_b16 a[108:109], v103 offset:4096\n\t"
        "v_mfma_f32_16x16x32_bf16 v[24:27], a[76:79], v[76:79], v[24:27]\n\t"
        "ds_read_b64_tr_b16 a[110:111], v103 offset:4608\n\t"
        "ds_read_b64_tr_b16 a[112:113], v103 offset:4104\n\t"
        "v_mfma_f32_16x16x32_bf16 v[28:31], v[76:79], a[80:83], v[28:31]\n\t"
        "ds_read_b64_tr_b16 a[114:115], v103 offset:4616\n\t"
        "ds_read_b64_tr_b16 a[116:117], v104 offset:2048\n\t"
        "v_mfma_f32_16x16x32_bf16 v[8:11], a[84:87], v[80:83], v[8:11]\n\t"
        "ds_read_b64_tr_b16 a[118:119], v104 offset:2304\n\t"
        "ds_read_b128 v[56:59], v105 offset:32768\n\t"
        "v_mfma_f32_16x16x32_bf16 v[12:15], a[88:91], v[80:83], v[12:15]\n\t"
        "ds_read_b128 v[60:63], v105 offset:34816\n\t"
        "ds_read_b128 v[64:67], v106 offset:32768\n\t"
        "v_mfma_f32_16x16x32_bf16 v[16:19], v[80:83], a[92:95], v[16:19]\n\t"
        "ds_read_b128 v[68:71], v106 offset:34816\n\t"
        "v_mfma_f32_16x16x32_bf16 v[20:23], a[84:87], v[84:87], v[20:23]\n\t"
        "v_mfma_f32_16x16x32_bf16 v[24:27], a[88:91], v[84:87], v[24:27]\n\t"
        "v_mfma_f32_16x16x32_bf16 v[28:31], v[84:87], a[92:95], v[28:31]\n\t"
        "s_add_u32 m0, %[ld], 65536\n\t"
        "s_add_u32 %[t0], %[xo], 1024\n\t"
        "buffer_load_dwordx4 v100, %[sx], %[t0] offen lds\n\t"
        "s_add_u32 m0, %[ld], 66560\n\t"
        "s_add_u32 %[t0], %[xo], 17408\n\t"
        "buffer_load_dwordx4 v100, %[sx], %[t0] offen lds\n\t"
        "s_add_u32 m0, %[ld], 73728\n\t"
        "s_add_u32 %[t0], %[xo], 1152\n\t"
        "buffer_load_dwordx4 v100, %[sx], %[t0] offen lds\n\t"
        "s_add_u32 m0, %[ld], 74752\n\t"
        "s_add_u32 %[t0], %[xo], 17536\n\t"
        "buffer_load_dwordx4 v100, %[sx], %[t0] offen lds\n\t"
        "s_add_u32 m0, %[ld], 81920\n\t"
        "s_add_u32 %[t0], %[xo], 1280\n\t"
        "buffer_load_dwordx4 v100, %[sx], %[t0] offen lds\n\t"
        "s_add_u32 m0, %[ld], 82944\n\t"
        "s_add_u32 %[t0], %[xo], 17664\n\t"
        "buffer_load_dwordx4 v100, %[sx], %[t0] offen lds\n\t"
        "s_add_u32 m0, %[ld], 90112\n\t"
        "s_add_u32 %[t0], %[xo], 1408\n\t"
        "buffer_load_dwordx4 v100, %[sx], %[t0] offen lds\n\t"
        "s_add_u32 m0, %[ld], 91136\n\t"
        "s_add_u32 %[t0], %[xo], 17792\n\t"
        "buffer_load_dwordx4 v100, %[sx], %[t0] offen lds\n\t"
        "s_mov_b32 %[t0], 4194304\n\t"
        "buffer_load_dwordx4 v[160:163], v101, %[sq], %[t0] offen\n\t"
        "buffer_load_dwordx4 v[168:171], v112, %[sv], %[t0] offen\n\t"
        "s_mov_b32 %[t0], 4456448\n\t"
        "buffer_load_dwordx4 v[164:167], v101, %[sq], %[t0] offen\n\t"
        "s_waitcnt vmcnt(17)\n\t"
        "ds_write_b128 v102, a[252:255] offset:16384\n\t"
        "ds_write_b128 v102, v[128:131] offset:20480\n\t"
        "ds_write_b128 v116, v[132:135] offset:24576\n\t"
        "s_waitcnt lgkmcnt(0)\n\t"
        "s_barrier\n\t"
        "v_mfma_f32_16x16x32_bf16 v[8:11], a[96:99], v[56:59], v[8:11]\n\t"
        "ds_read_b64_tr_b16 a[120:121], v103 offset:16384\n\t"
        "ds_read_b64_tr_b16 a[122:123], v103 offset:16896\n\t"
        "v_mfma_f32_16x16x32_bf16 v[12:15], a[100:103], v[56:59], v[12:15]\n\t"
        "ds_read_b64_tr_b16 a[124:125], v103 offset:16392\n\t"
        "ds_read_b64_tr_b16 a[126:127], v103 offset:16904\n\t"
        "v_mfma_f32_16x16x32_bf16 v[16:19], v[56:59], a[104:107], v[16:19]\n\t"
        "ds_read_b64_tr_b16 a[128:129], v104 offset:16384\n\t"
        "ds_read_b64_tr_b16 a[130:131], v104 offset:16640\n\t"
        "v_mfma_f32_16x16x32_bf16 v[20:23], a[96:99], v[60:63], v[20:23]\n\t"
        "s_waitcnt lgkmcnt(5)\n\t"
        "ds_read_b64_tr_b16 a[132:133], v103 offset:20480\n\t"
        "v_mfma_f32_16x16x32_bf16 v[24:27], a[100:103], v[60:63], v[24:27]\n\t"
        "ds_read_b64_tr_b16 a[134:135], v103 offset:20992\n\t"
        "ds_read_b64_tr_b16 a[136:137], v103 offset:20488\n\t"
        "v_mfma_f32_16x16x32_bf16 v[28:31], v[60:63], a[104:107], v[28:31]\n\t"
        "ds_read_b64_tr_b16 a[138:139], v103 offset:21000\n\t"
        "ds_read_b64_tr_b16 a[140:141], v104 offset:18432\n\t"
        "v_mfma_f32_16x16x32_bf16 v[8:11], a[108:111], v[64:67], v[8:11]\n\t"
        "ds_read_b64_tr_b16 a[142:143], v104 offset:18688\n\t"
        "ds_read_b128 v[72:75], v105 offset:40960\n\t"
        "v_mfma_f32_16x16x32_bf16 v[12:15], a[112:115], v[64:67], v[12:15]\n\t"
        "ds_read_b128 v[76:79], v105 offset:43008\n\t"
        "ds_read_b128 v[80:83], v106 offset:40960\n\t"
        "v_mfma_f32_16x16x32_bf16 v[16:19], v[64:67], a[116:119], v[16:19]\n\t"
        "ds_read_b128 v[84:87], v106 offset:43008\n\t"
        "v_mfma_f32_16x16x32_bf16 v[20:23], a[108:111], v[68:71], v[20:23]\n\t"
        "v_mfma_f32_16x16x32_bf16 v[24:27], a[112:115], v[68:71], v[24:27]\n\t"
        "v_mfma_f32_16x16x32_bf16 v[28:31], v[68:71], a[116:119], v[28:31]\n\t"
        "s_mov_b32 %[t0], 4718592\n\t"
        "buffer_load_dwordx4 v[172:175], v101, %[sq], %[t0] offen\n\t"
        "buffer_load_dwordx4 v[180:183], v112, %[sv], %[t0] offen\n\t"
        "s_mov_b32 %[t0], 4980736\n\t"
        "buffer_load_dwordx4 v[176:179], v101, %[sq], %[t0] offen\n\t"
        "s_waitcnt vmcnt(17)\n\t"
        "ds_write_b128 v102, v[136:139] offset:0\n\t"
        "ds_write_b128 v102, v[140:143] offset:4096\n\t"
        "ds_write_b128 v116, v[144:147] offset:8192\n\t"
        "s_waitcnt lgkmcnt(0)\n\t"
        "s_barrier\n\t"
        "v_mfma_f32_16x16x32_bf16 v[8:11], a[120:123], v[72:75], v[8:11]\n\t"
        "ds_read_b64_tr_b16 a[144:145], v103 offset:0\n\t"
        "ds_read_b64_tr_b16 a[146:147], v103 offset:512\n\t"
        "v_mfma_f32_16x16x32_bf16 v[12:15], a[124:127], v[72:75], v[12:15]\n\t"
        "ds_read_b64_tr_b16 a[148:149], v103 offset:8\n\t"
        "ds_read_b64_tr_b16 a[150:151], v103 offset:520\n\t"
        "v_mfma_f32_16x16x32_bf16 v[16:19], v[72:75], a[128:131], v[16:19]\n\t"
        "ds_read_b64_tr_b16 a[152:153], v104 offset:0\n\t"
        "ds_read_b64_tr_b16 a[154:155], v104 offset:256\n\t"
        "v_mfma_f32_16x16x32_bf16 v[20:23], a[120:123], v[76:79], v[20:23]\n\t"
        "s_waitcnt lgkmcnt(5)\n\t"
        "ds_read_b64_tr_b16 a[156:157], v103 offset:4096\n\t"
        "v_mfma_f32_16x16x32_bf16 v[24:27], a[124:127], v[76:79], v[24:27]\n\t"
        "ds_read_b64_tr_b16 a[158:159], v103 offset:4608\n\t"
        "ds_read_b64_tr_b16 a[160:161], v103 offset:4104\n\t"
        "v_mfma_f32_16x16x32_bf16 v[28:31], v[76:79], a[128:131], v[28:31]\n\t"
        "ds_read_b64_tr_b16 a[162:163], v103 offset:4616\n\t"
        "ds_read_b64_tr_b16 a[164:165], v104 offset:2048\n\t"
        "v_mfma_f32_16x16x32_bf16 v[8:11], a[132:135], v[80:83], v[8:11]\n\t"
        "ds_read_b64_tr_b16 a[166:167], v104 offset:2304\n\t"
        "ds_read_b128 v[56:59], v105 offset:49152\n\t"
        "v_mfma_f32_16x16x32_bf16 v[12:15], a[136:139], v[80:83], v[12:15]\n\t"
        "ds_read_b128 v[60:63], v105 offset:51200\n\t"
        "ds_read_b128 v[64:67], v106 offset:49152\n\t"
        "v_mfma_f32_16x16x32_bf16 v[16:19], v[80:83], a[140:143], v[16:19]\n\t"
        "ds_read_b128 v[68:71], v106 offset:51200\n\t"
        "v_mfma_f32_16x16x32_bf16 v[20:23], a[132:135], v[84:87], v[20:23]\n\t"
        "v_mfma_f32_16x16x32_bf16 v[24:27], a[136:139], v[84:87], v[24:27]\n\t"
        "v_mfma_f32_16x16x32_bf16 v[28:31], v[84:87], a[140:143], v[28:31]\n\t"
        "s_mov_b32 %[t0], 5242880\n\t"
        "buffer_load_dwordx4 v[184:187], v101, %[sq], %[t0] offen\n\t"
        "buffer_load_dwordx4 v[192:195], v112, %[sv], %[t0] offen\n\t"
        "s_mov_b32 %[t0], 5505024\n\t"
        "buffer_load_dwordx4 v[188:191], v101, %[sq], %[t0] offen\n\t"
        "s_waitcnt vmcnt(17)\n\t"
        "ds_write_b128 v102, v[148:151] offset:16384\n\t"
        "ds_write_b128 v102, v[152:155] offset:20480\n\t"
        "ds_write_b128 v116, v[156:159] offset:24576\n\t"
        "s_waitcnt lgkmcnt(0)\n\t"
        "s_barrier\n\t"
        "v_mfma_f32_16x16x32_bf16 v[8:11], a[144:147], v[56:59], v[8:11]\n\t"
        "ds_read_b64_tr_b16 a[168:169], v103 offset:16384\n\t"
        "ds_read_b64_tr_b16 a[170:171], v103 offset:16896\n\t"
        "v_mfma_f32_16x16x32_bf16 v[12:15], a[148:151], v[56:59], v[12:15]\n\t"
        "ds_read_b64_tr_b16 a[172:173], v103 offset:16392\n\t"
        "ds_read_b64_tr_b16 a[174:175], v103 offset:16904\n\t"
        "v_mfma_f32_16x16x32_bf16 v[16:19], v[56:59], a[152:155], v[16:19]\n\t"
        "ds_read_b64_tr_b16 a[176:177], v104 offset:16384\n\t"
        "ds_read_b64_tr_b16 a[178:179], v104 offset:16640\n\t"
        "v_mfma_f32_16x16x32_bf16 v[20:23], a[144:147], v[60:63], v[20:23]\n\t"
        "s_waitcnt lgkmcnt(5)\n\t"
        "ds_read_b64_tr_b16 a[180:181], v103 offset:20480\n\t"
        "v_mfma_f32_16x16x32_bf16 v[24:27], a[148:151], v[60:63], v[24:27]\n\t"
        "ds_read_b64_tr_b16 a[182:183], v103 offset:20992\n\t"
        "ds_read_b64_tr_b16 a[184:185], v103 offset:20488\n\t"
        "v_mfma_f32_16x16x32_bf16 v[28:31], v[60:63], a[152:155], v[28:31]\n\t"
        "ds_read_b64_tr_b16 a[186:187], v103 offset:21000\n\t"
        "ds_read_b64_tr_b16 a[188:189], v104 offset:18432\n\t"
        "v_mfma_f32_16x16x32_bf16 v[8:11], a[156:159], v[64:67], v[8:11]\n\t"
        "ds_read_b64_tr_b16 a[190:191], v104 offset:18688\n\t"
        "ds_read_b128 v[72:75], v105 offset:57344\n\t"
        "v_mfma_f32_16x16x32_bf16 v[12:15], a[160:163], v[64:67], v[12:15]\n\t"
        "ds_read_b128 v[76:79], v105 offset:59392\n\t"
        "ds_read_b128 v[80:83], v106 offset:57344\n\t"
        "v_mfma_f32_16x16x32_bf16 v[16:19], v[64:67], a[164:167], v[16:19]\n\t"
        "ds_read_b128 v[84:87], v106 offset:59392\n\t"
        "v_mfma_f32_16x16x32_bf16 v[20:23], a[156:159], v[68:71], v[20:23]\n\t"
        "v_mfma_f32_16x16x32_bf16 v[24:27], a[160:163], v[68:71], v[24:27]\n\t"
        "v_mfma_f32_16x16x32_bf16 v[28:31], v[68:71], a[164:167], v[28:31]\n\t"
        "s_mov_b32 %[t0], 5767168\n\t"
        "buffer_load_dwordx4 v[196:199], v101, %[sq], %[t0] offen\n\t"
        "buffer_load_dwordx4 v[204:207], v112, %[sv], %[t0] offen\n\t"
        "s_mov_b32 %[t0], 6029312\n\t"
        "buffer_load_dwordx4 v[200:203], v101, %[sq], %[t0] offen\n\t"
        "s_waitcnt vmcnt(9)\n\t"
        "ds_write_b128 v102, v[160:163] offset:0\n\t"
        "ds_write_b128 v102, v[164:167] offset:4096\n\t"
        "ds_write_b128 v116, v[168:171] offset:8192\n\t"
        "s_waitcnt lgkmcnt(0)\n\t"
        "s_barrier\n\t"
        "v_mfma_f32_16x16x32_bf16 v[8:11], a[168:171], v[72:75], v[8:11]\n\t"
        "ds_read_b64_tr_b16 a[192:193], v103 offset:0\n\t"
        "ds_read_b64_tr_b16 a[194:195], v103 offset:512\n\t"
        "v_mfma_f32_16x16x32_bf16 v[12:15], a[172:175], v[72:75], v[12:15]\n\t"
        "ds_read_b64_tr_b16 a[196:197], v103 offset:8\n\t"
        "ds_read_b64_tr_b16 a[198:199], v103 offset:520\n\t"
        "v_mfma_f32_16x16x32_bf16 v[16:19], v[72:75], a[176:179], v[16:19]\n\t"
        "ds_read_b64_tr_b16 a[200:201], v104 offset:0\n\t"
        "ds_read_b64_tr_b16 a[202:203], v104 offset:256\n\t"
        "v_mfma_f32_16x16x32_bf16 v[20:23], a[168:171], v[76:79], v[20:23]\n\t"
        "s_waitcnt lgkmcnt(5)\n\t"
        "ds_read_b64_tr_b16 a[204:205], v103 offset:4096\n\t"
        "v_mfma_f32_16x16x32_bf16 v[24:27], a[172:175], v[76:79], v[24:27]\n\t"
        "ds_read_b64_tr_b16 a[206:207], v103 offset:4608\n\t"
        "ds_read_b64_tr_b16 a[208:209], v103 offset:4104\n\t"
        "v_mfma_f32_16x16x32_bf16 v[28:31], v[76:79], a[176:179], v[28:31]\n\t"
        "ds_read_b64_tr_b16 a[210:211], v103 offset:4616\n\t"
        "ds_read_b64_tr_b16 a[212:213], v104 offset:2048\n\t"
        "v_mfma_f32_16x16x32_bf16 v[8:11], a[180:183], v[80:83], v[8:11]\n\t"
        "ds_read_b64_tr_b16 a[214:215], v104 offset:2304\n\t"
        "ds_read_b128 v[56:59], v107 offset:0\n\t"
        "v_mfma_f32_16x16x32_bf16 v[12:15], a[184:187], v[80:83], v[12:15]\n\t"
        "ds_read_b128 v[60:63], v107 offset:2048\n\t"
        "ds_read_b128 v[64:67], v108 offset:0\n\t"
        "v_mfma_f32_16x16x32_bf16 v[16:19], v[80:83], a[188:191], v[16:19]\n\t"
        "ds_read_b128 v[68:71], v108 offset:2048\n\t"
        "v_mfma_f32_16x16x32_bf16 v[20:23], a[180:183], v[84:87], v[20:23]\n\t"
        "v_mfma_f32_16x16x32_bf16 v[24:27], a[184:187], v[84:87], v[24:27]\n\t"
        "v_mfma_f32_16x16x32_bf16 v[28:31], v[84:87], a[188:191], v[28:31]\n\t"
        "s_add_u32 m0, %[ld], 98304\n\t"
        "s_add_u32 %[t0], %[xo], 1536\n\t"
        "buffer_load_dwordx4 v100, %[sx], %[t0] offen lds\n\t"
        "s_add_u32 m0, %[ld], 99328\n\t"
        "s_add_u32 %[t0], %[xo], 17920\n\t"
        "buffer_load_dwordx4 v100, %[sx], %[t0] offen lds\n\t"
        "s_add_u32 m0, %[ld], 106496\n\t"
        "s_add_u32 %[t0], %[xo], 1664\n\t"
        "buffer_load_dwordx4 v100, %[sx], %[t0] offen lds\n\t"
        "s_add_u32 m0, %[ld], 107520\n\t"
        "s_add_u32 %[t0], %[xo], 18048\n\t"
        "buffer_load_dwordx4 v100, %[sx], %[t0] offen lds\n\t"
        "s_add_u32 m0, %[ld], 114688\n\t"
        "s_add_u32 %[t0], %[xo], 1792\n\t"
        "buffer_load_dwordx4 v100, %[sx], %[t0] offen lds\n\t"
        "s_add_u32 m0, %[ld], 115712\n\t"
        "s_add_u32 %[t0], %[xo], 18176\n\t"
        "buffer_load_dwordx4 v100, %[sx], %[t0] offen lds\n\t"
        "s_add_u32 m0, %[ld], 122880\n\t"
        "s_add_u32 %[t0], %[xo], 1920\n\t"
        "buffer_load_dwordx4 v100, %[sx], %[t0] offen lds\n\t"
        "s_add_u32 m0, %[ld], 123904\n\t"
        "s_add_u32 %[t0], %[xo], 18304\n\t"
        "buffer_load_dwordx4 v100, %[sx], %[t0] offen lds\n\t"
        "s_mov_b32 %[t0], 6291456\n\t"
        "buffer_load_dwordx4 v[208:211], v101, %[sq], %[t0] offen\n\t"
        "buffer_load_dwordx4 v[216:219], v112, %[sv], %[t0] offen\n\t"
        "s_mov_b32 %[t0], 6553600\n\t"
        "buffer_load_dwordx4 v[212:215], v101, %[sq], %[t0] offen\n\t"
        "s_waitcnt vmcnt(17)\n\t"
        "ds_write_b128 v102, v[172:175] offset:16384\n\t"
        "ds_write_b128 v102, v[176:179] offset:20480\n\t"
        "ds_write_b128 v116, v[180:183] offset:24576\n\t"
        "s_waitcnt lgkmcnt(0)\n\t"
        "s_barrier\n\t"
        "v_mfma_f32_16x16x32_bf16 v[8:11], a[192:195], v[56:59], v[8:11]\n\t"
        "ds_read_b64_tr_b16 a[216:217], v103 offset:16384\n\t"
        "ds_read_b64_tr_b16 a[218:219], v103 offset:16896\n\t"
        "v_mfma_f32_16x16x32_bf16 v[12:15], a[196:199], v[56:59], v[12:15]\n\t"
        "ds_read_b64_tr_b16 a[220:221], v103 offset:16392\n\t"
        "ds_read_b64_tr_b16 a[222:223], v103 offset:16904\n\t"
        "v_mfma_f32_16x16x32_bf16 v[16:19], v[56:59], a[200:203], v[16:19]\n\t"
        "ds_read_b64_tr_b16 a[224:225], v104 offset:16384\n\t"
        "ds_read_b64_tr_b16 a[226:227], v104 offset:16640\n\t"
        "v_mfma_f32_16x16x32_bf16 v[20:23], a[192:195], v[60:63], v[20:23]\n\t"
        "s_waitcnt lgkmcnt(5)\n\t"
        "ds_read_b64_tr_b16 a[228:229], v103 offset:20480\n\t"
        "v_mfma_f32_16x16x32_bf16 v[24:27], a[196:199], v[60:63], v[24:27]\n\t"
        "ds_read_b64_tr_b16 a[230:231], v103 offset:20992\n\t"
        "ds_read_b64_tr_b16 a[232:233], v103 offset:20488\n\t"
        "v_mfma_f32_16x16x32_bf16 v[28:31], v[60:63], a[200:203], v[28:31]\n\t"
        "ds_read_b64_tr_b16 a[234:235], v103 offset:21000\n\t"
        "ds_read_b64_tr_b16 a[236:237], v104 offset:18432\n\t"
        "v_mfma_f32_16x16x32_bf16 v[8:11], a[204:207], v[64:67], v[8:11]\n\t"
        "ds_read_b64_tr_b16 a[238:239], v104 offset:18688\n\t"
        "ds_read_b128 v[72:75], v107 offset:8192\n\t"
        "v_mfma_f32_16x16x32_bf16 v[12:15], a[208:211], v[64:67], v[12:15]\n\t"
        "ds_read_b128 v[76:79], v107 offset:10240\n\t"
        "ds_read_b128 v[80:83], v108 offset:8192\n\t"
        "v_mfma_f32_16x16x32_bf16 v[16:19], v[64:67], a[212:215], v[16:19]\n\t"
        "ds_read_b128 v[84:87], v108 offset:10240\n\t"
        "v_mfma_f32_16x16x32_bf16 v[20:23], a[204:207], v[68:71], v[20:23]\n\t"
        "v_mfma_f32_16x16x32_bf16 v[24:27], a[208:211], v[68:71], v[24:27]\n\t"
        "v_mfma_f32_16x16x32_bf16 v[28:31], v[68:71], a[212:215], v[28:31]\n\t"
        "s_mov_b32 %[t0], 6815744\n\t"
        "buffer_load_dwordx4 v[220:223], v101, %[sq], %[t0] offen\n\t"
        "buffer_load_dwordx4 v[228:231], v112, %[sv], %[t0] offen\n\t"
        "s_mov_b32 %[t0], 7077888\n\t"
        "buffer_load_dwordx4 v[224:227], v101, %[sq], %[t0] offen\n\t"
        "s_waitcnt vmcnt(17)\n\t"
        "ds_write_b128 v102, v[184:187] offset:0\n\t"
        "ds_write_b128 v102, v[188:191] offset:4096\n\t"
        "ds_write_b128 v116, v[192:195] offset:8192\n\t"
        "s_waitcnt lgkmcnt(0)\n\t"
        "s_barrier\n\t"
        "v_mfma_f32_16x16x32_bf16 v[8:11], a[216:219], v[72:75], v[8:11]\n\t"
        "ds_read_b64_tr_b16 a[240:241], v103 offset:0\n\t"
        "ds_read_b64_tr_b16 a[242:243], v103 offset:512\n\t"
        "v_mfma_f32_16x16x32_bf16 v[12:15], a[220:223], v[72:75], v[12:15]\n\t"
        "ds_read_b64_tr_b16 a[244:245], v103 offset:8\n\t"
        "ds_read_b64_tr_b16 a[246:247], v103 offset:520\n\t"
        "v_mfma_f32_16x16x32_bf16 v[16:19], v[72:75], a[224:227], v[16:19]\n\t"
        "ds_read_b64_tr_b16 a[248:249], v104 offset:0\n\t"
        "ds_read_b64_tr_b16 a[250:251], v104 offset:256\n\t"
        "v_mfma_f32_16x16x32_bf16 v[20:23], a[216:219], v[76:79], v[20:23]\n\t"
        "s_waitcnt lgkmcnt(5)\n\t"
        "ds_read_b64_tr_b16 a[252:253], v103 offset:4096\n\t"
        "v_mfma_f32_16x16x32_bf16 v[24:27], a[220:223], v[76:79], v[24:27]\n\t"
        "ds_read_b64_tr_b16 a[254:255], v103 offset:4608\n\t"
        "ds_read_b64_tr_b16 v[128:129], v103 offset:4104\n\t"
        "v_mfma_f32_16x16x32_bf16 v[28:31], v[76:79], a[224:227], v[28:31]\n\t"
        "ds_read_b64_tr_b16 v[130:131], v103 offset:4616\n\t"
        "ds_read_b64_tr_b16 v[132:133], v104 offset:2048\n\t"
        "v_mfma_f32_16x16x32_bf16 v[8:11], a[228:231], v[80:83], v[8:11]\n\t"
        "ds_read_b64_tr_b16 v[134:135], v104 offset:2304\n\t"
        "ds_read_b128 v[56:59], v107 offset:16384\n\t"
        "v_mfma_f32_16x16x32_bf16 v[12:15], a[232:235], v[80:83], v[12:15]\n\t"
        "ds_read_b128 v[60:63], v107 offset:18432\n\t"
        "ds_read_b128 v[64:67], v108 offset:16384\n\t"
        "v_mfma_f32_16x16x32_bf16 v[16:19], v[80:83], a[236:239], v[16:19]\n\t"
        "ds_read_b128 v[68:71], v108 offset:18432\n\t"
        "v_mfma_f32_16x16x32_bf16 v[20:23], a[228:231], v[84:87], v[20:23]\n\t"
        "v_mfma_f32_16x16x32_bf16 v[24:27], a[232:235], v[84:87], v[24:27]\n\t"
        "v_mfma_f32_16x16x32_bf16 v[28:31], v[84:87], a[236:239], v[28:31]\n\t"
        "s_mov_b32 %[t0], 7340032\n\t"
        "buffer_load_dwordx4 v[232:235], v101, %[sq], %[t0] offen\n\t"
        "buffer_load_dwordx4 v[240:243], v112, %[sv], %[t0] offen\n\t"
        "s_mov_b32 %[t0], 7602176\n\t"
        "buffer_load_dwordx4 v[236:239], v101, %[sq], %[t0] offen\n\t"
        "s_waitcnt vmcnt(17)\n\t"
        "ds_write_b128 v102, v[196:199] offset:16384\n\t"
        "ds_write_b128 v102, v[200:203] offset:20480\n\t"
        "ds_write_b128 v116, v[204:207] offset:24576\n\t"
        "s_waitcnt lgkmcnt(0)\n\t"
        "s_barrier\n\t"
        "v_mfma_f32_16x16x32_bf16 v[8:11], a[240:243], v[56:59], v[8:11]\n\t"
        "ds_read_b64_tr_b16 v[136:137], v103 offset:16384\n\t"
        "ds_read_b64_tr_b16 v[138:139], v103 offset:16896\n\t"
        "v_mfma_f32_16x16x32_bf16 v[12:15], a[244:247], v[56:59], v[12:15]\n\t"
        "ds_read_b64_tr_b16 v[140:141], v103 offset:16392\n\t"
        "ds_read_b64_tr_b16 v[142:143], v103 offset:16904\n\t"
        "v_mfma_f32_16x16x32_bf16 v[16:19], v[56:59], a[248:251], v[16:19]\n\t"
        "ds_read_b64_tr_b16 v[144:145], v104 offset:16384\n\t"
        "ds_read_b64_tr_b16 v[146:147], v104 offset:16640\n\t"
        "v_mfma_f32_16x16x32_bf16 v[20:23], a[240:243], v[60:63], v[20:23]\n\t"
        "s_waitcnt lgkmcnt(5)\n\t"
        "ds_read_b64_tr_b16 v[148:149], v103 offset:20480\n\t"
        "v_mfma_f32_16x16x32_bf16 v[24:27], a[244:247], v[60:63], v[24:27]\n\t"
        "ds_read_b64_tr_b16 v[150:151], v103 offset:20992\n\t"
        "ds_read_b64_tr_b16 v[152:153], v103 offset:20488\n\t"
        "v_mfma_f32_16x16x32_bf16 v[28:31], v[60:63], a[248:251], v[28:31]\n\t"
        "ds_read_b64_tr_b16 v[154:155], v103 offset:21000\n\t"
        "ds_read_b64_tr_b16 v[156:157], v104 offset:18432\n\t"
        "v_mfma_f32_16x16x32_bf16 v[8:11], a[252:255], v[64:67], v[8:11]\n\t"
        "ds_read_b64_tr_b16 v[158:159], v104 offset:18688\n\t"
        "ds_read_b128 v[72:75], v107 offset:24576\n\t"
        "v_mfma_f32_16x16x32_bf16 v[12:15], v[128:131], v[64:67], v[12:15]\n\t"
        "ds_read_b128 v[76:79], v107 offset:26624\n\t"
        "ds_read_b128 v[80:83], v108 offset:24576\n\t"
        "v_mfma_f32_16x16x32_bf16 v[16:19], v[64:67], v[132:135], v[16:19]\n\t"
        "ds_read_b128 v[84:87], v108 offset:26624\n\t"
        "v_mfma_f32_16x16x32_bf16 v[20:23], a[252:255], v[68:71], v[20:23]\n\t"
        "v_mfma_f32_16x16x32_bf16 v[24:27], v[128:131], v[68:71], v[24:27]\n\t"
        "v_mfma_f32_16x16x32_bf16 v[28:31], v[68:71], v[132:135], v[28:31]\n\t"
        "s_mov_b32 %[t0], 7864320\n\t"
        "buffer_load_dwordx4 v[244:247], v101, %[sq], %[t0] offen\n\t"
        "buffer_load_dwordx4 v[252:255], v112, %[sv], %[t0] offen\n\t"
        "s_mov_b32 %[t0], 8126464\n\t"
        "buffer_load_dwordx4 v[248:251], v101, %[sq], %[t0] offen\n\t"
        "s_waitcnt vmcnt(9)\n\t"
        "ds_write_b128 v102, v[208:211] offset:0\n\t"
        "ds_write_b128 v102, v[212:215] offset:4096\n\t"
        "ds_write_b128 v116, v[216:219] offset:8192\n\t"
        "s_waitcnt lgkmcnt(0)\n\t"
        "s_barrier\n\t"
        "v_mfma_f32_16x16x32_bf16 v[8:11], v[136:139], v[72:75], v[8:11]\n\t"
        "ds_read_b64_tr_b16 v[160:161], v103 offset:0\n\t"
        "ds_read_b64_tr_b16 v[162:163], v103 offset:512\n\t"
        "v_mfma_f32_16x16x32_bf16 v[12:15], v[140:143], v[72:75], v[12:15]\n\t"
        "ds_read_b64_tr_b16 v[164:165], v103 offset:8\n\t"
        "ds_read_b64_tr_b16 v[166:167], v103 offset:520\n\t"
        "v_mfma_f32_16x16x32_bf16 v[16:19], v[72:75], v[144:147], v[16:19]\n\t"
        "ds_read_b64_tr_b16 v[168:169], v104 offset:0\n\t"
        "ds_read_b64_tr_b16 v[170:171], v104 offset:256\n\t"
        "v_mfma_f32_16x16x32_bf16 v[20:23], v[136:139], v[76:79], v[20:23]\n\t"
        "s_waitcnt lgkmcnt(5)\n\t"
        "ds_read_b64_tr_b16 v[172:173], v103 offset:4096\n\t"
        "v_mfma_f32_16x16x32_bf16 v[24:27], v[140:143], v[76:79], v[24:27]\n\t"
        "ds_read_b64_tr_b16 v[174:175], v103 offset:4608\n\t"
        "ds_read_b64_tr_b16 v[176:177], v103 offset:4104\n\t"
        "v_mfma_f32_16x16x32_bf16 v[28:31], v[76:79], v[144:147], v[28:31]\n\t"
        "ds_read_b64_tr_b16 v[178:179], v103 offset:4616\n\t"
        "ds_read_b64_tr_b16 v[180:181], v104 offset:2048\n\t"
        "v_mfma_f32_16x16x32_bf16 v[8:11], v[148:151], v[80:83], v[8:11]\n\t"
        "ds_read_b64_tr_b16 v[182:183], v104 offset:2304\n\t"
        "ds_read_b128 v[56:59], v107 offset:32768\n\t"
        "v_mfma_f32_16x16x32_bf16 v[12:15], v[152:155], v[80:83], v[12:15]\n\t"
        "ds_read_b128 v[60:63], v107 offset:34816\n\t"
        "ds_read_b128 v[64:67], v108 offset:32768\n\t"
        "v_mfma_f32_16x16x32_bf16 v[16:19], v[80:83], v[156:159], v[16:19]\n\t"
        "ds_read_b128 v[68:71], v108 offset:34816\n\t"
        "v_mfma_f32_16x16x32_bf16 v[20:23], v[148:151], v[84:87], v[20:23]\n\t"
        "v_mfma_f32_16x16x32_bf16 v[24:27], v[152:155], v[84:87], v[24:27]\n\t"
        "v_mfma_f32_16x16x32_bf16 v[28:31], v[84:87], v[156:159], v[28:31]\n\t"
        "s_mov_b32 %[xo], 0x20000\n\t"
        "s_add_u32 m0, %[ld], 0\n\t"
        "s_add_u32 %[t0], %[xo], 0\n\t"
        "buffer_load_dwordx4 v100, %[sx], %[t0] offen lds\n\t"
        "s_add_u32 m0, %[ld], 1024\n\t"
        "s_add_u32 %[t0], %[xo], 16384\n\t"
        "buffer_load_dwordx4 v100, %[sx], %[t0] offen lds\n\t"
        "s_add_u32 m0, %[ld], 8192\n\t"
        "s_add_u32 %[t0], %[xo], 128\n\t"
        "buffer_load_dwordx4 v100, %[sx], %[t0] offen lds\n\t"
        "s_add_u32 m0, %[ld], 9216\n\t"
        "s_add_u32 %[t0], %[xo], 16512\n\t"
        "buffer_load_dwordx4 v100, %[sx], %[t0] offen lds\n\t"
        "s_add_u32 m0, %[ld], 16384\n\t"
        "s_add_u32 %[t0], %[xo], 256\n\t"
        "buffer_load_dwordx4 v100, %[sx], %[t0] offen lds\n\t"
        "s_add_u32 m0, %[ld], 17408\n\t"
        "s_add_u32 %[t0], %[xo], 16640\n\t"
        "buffer_load_dwordx4 v100, %[sx], %[t0] offen lds\n\t"
        "s_add_u32 m0, %[ld], 24576\n\t"
        "s_add_u32 %[t0], %[xo], 384\n\t"
        "buffer_load_dwordx4 v100, %[sx], %[t0] offen lds\n\t"
        "s_add_u32 m0, %[ld], 25600\n\t"
        "s_add_u32 %[t0], %[xo], 16768\n\t"
        "buffer_load_dwordx4 v100, %[sx], %[t0] offen lds\n\t"
        "s_mov_b32 %[xo], 0\n\t"
        "s_waitcnt vmcnt(14)\n\t"
        "ds_write_b128 v102, v[220:223] offset:16384\n\t"
        "ds_write_b128 v102, v[224:227] offset:20480\n\t"
        "ds_write_b128 v116, v[228:231] offset:24576\n\t"
        "s_waitcnt lgkmcnt(0)\n\t"
        "s_barrier\n\t"
        "v_mfma_f32_16x16x32_bf16 v[8:11], v[160:163], v[56:59], v[8:11]\n\t"
        "ds_read_b64_tr_b16 v[184:185], v103 offset:16384\n\t"
        "ds_read_b64_tr_b16 v[186:187], v103 offset:16896\n\t"
        "v_mfma_f32_16x16x32_bf16 v[12:15], v[164:167], v[56:59], v[12:15]\n\t"
        "ds_read_b64_tr_b16 v[188:189], v103 offset:16392\n\t"
        "ds_read_b64_tr_b16 v[190:191], v103 offset:16904\n\t"
        "v_mfma_f32_16x16x32_bf16 v[16:19], v[56:59], v[168:171], v[16:19]\n\t"
        "ds_read_b64_tr_b16 v[192:193], v104 offset:16384\n\t"
        "ds_read_b64_tr_b16 v[194:195], v104 offset:16640\n\t"
        "v_mfma_f32_16x16x32_bf16 v[20:23], v[160:163], v[60:63], v[20:23]\n\t"
        "s_waitcnt lgkmcnt(5)\n\t"
        "ds_read_b64_tr_b16 v[196:197], v103 offset:20480\n\t"
        "v_mfma_f32_16x16x32_bf16 v[24:27], v[164:167], v[60:63], v[24:27]\n\t"
        "ds_read_b64_tr_b16 v[198:199], v103 offset:20992\n\t"
        "ds_read_b64_tr_b16 v[200:201], v103 offset:20488\n\t"
        "v_mfma_f32_16x16x32_bf16 v[28:31], v[60:63], v[168:171], v[28:31]\n\t"
        "ds_read_b64_tr_b16 v[202:203], v103 offset:21000\n\t"
        "ds_read_b64_tr_b16 v[204:205], v104 offset:18432\n\t"
        "v_mfma_f32_16x16x32_bf16 v[8:11], v[172:175], v[64:67], v[8:11]\n\t"
        "ds_read_b64_tr_b16 v[206:207], v104 offset:18688\n\t"
        "ds_read_b128 v[72:75], v107 offset:40960\n\t"
        "v_mfma_f32_16x16x32_bf16 v[12:15], v[176:179], v[64:67], v[12:15]\n\t"
        "ds_read_b128 v[76:79], v107 offset:43008\n\t"
        "ds_read_b128 v[80:83], v108 offset:40960\n\t"
        "v_mfma_f32_16x16x32_bf16 v[16:19], v[64:67], v[180:183], v[16:19]\n\t"
        "ds_read_b128 v[84:87], v108 offset:43008\n\t"
        "v_mfma_f32_16x16x32_bf16 v[20:23], v[172:175], v[68:71], v[20:23]\n\t"
        "v_mfma_f32_16x16x32_bf16 v[24:27], v[176:179], v[68:71], v[24:27]\n\t"
        "v_mfma_f32_16x16x32_bf16 v[28:31], v[68:71], v[180:183], v[28:31]\n\t"
        "s_waitcnt vmcnt(11)\n\t"
        "ds_write_b128 v102, v[232:235] offset:0\n\t"
        "ds_write_b128 v102, v[236:239] offset:4096\n\t"
        "ds_write_b128 v116, v[240:243] offset:8192\n\t"
        "s_waitcnt lgkmcnt(0)\n\t"
        "s_barrier\n\t"
        "v_mfma_f32_16x16x32_bf16 v[8:11], v[184:187], v[72:75], v[8:11]\n\t"
        "ds_read_b64_tr_b16 v[208:209], v103 offset:0\n\t"
        "ds_read_b64_tr_b16 v[210:211], v103 offset:512\n\t"
        "v_mfma_f32_16x16x32_bf16 v[12:15], v[188:191], v[72:75], v[12:15]\n\t"
        "ds_read_b64_tr_b16 v[212:213], v103 offset:8\n\t"
        "ds_read_b64_tr_b16 v[214:215], v103 offset:520\n\t"
        "v_mfma_f32_16x16x32_bf16 v[16:19], v[72:75], v[192:195], v[16:19]\n\t"
        "ds_read_b64_tr_b16 v[216:217], v104 offset:0\n\t"
        "ds_read_b64_tr_b16 v[218:219], v104 offset:256\n\t"
        "v_mfma_f32_16x16x32_bf16 v[20:23], v[184:187], v[76:79], v[20:23]\n\t"
        "s_waitcnt lgkmcnt(5)\n\t"
        "ds_read_b64_tr_b16 v[220:221], v103 offset:4096\n\t"
        "v_mfma_f32_16x16x32_bf16 v[24:27], v[188:191], v[76:79], v[24:27]\n\t"
        "ds_read_b64_tr_b16 v[222:223], v103 offset:4608\n\t"
        "ds_read_b64_tr_b16 v[224:225], v103 offset:4104\n\t"
        "v_mfma_f32_16x16x32_bf16 v[28:31], v[76:79], v[192:195], v[28:31]\n\t"
        "ds_read_b64_tr_b16 v[226:227], v103 offset:4616\n\t"
        "ds_read_b64_tr_b16 v[228:229], v104 offset:2048\n\t"
        "v_mfma_f32_16x16x32_bf16 v[8:11], v[196:199], v[80:83], v[8:11]\n\t"
        "ds_read_b64_tr_b16 v[230:231], v104 offset:2304\n\t"
        "ds_read_b128 v[56:59], v107 offset:49152\n\t"
        "v_mfma_f32_16x16x32_bf16 v[12:15], v[200:203], v[80:83], v[12:15]\n\t"
        "ds_read_b128 v[60:63], v107 offset:51200\n\t"
        "ds_read_b128 v[64:67], v108 offset:49152\n\t"
        "v_mfma_f32_16x16x32_bf16 v[16:19], v[80:83], v[204:207], v[16:19]\n\t"
        "ds_read_b128 v[68:71], v108 offset:51200\n\t"
        "v_mfma_f32_16x16x32_bf16 v[20:23], v[196:199], v[84:87], v[20:23]\n\t"
        "v_mfma_f32_16x16x32_bf16 v[24:27], v[200:203], v[84:87], v[24:27]\n\t"
        "v_mfma_f32_16x16x32_bf16 v[28:31], v[84:87], v[204:207], v[28:31]\n\t"
        "s_waitcnt vmcnt(8)\n\t"
        "ds_write_b128 v102, v[244:247] offset:16384\n\t"
        "ds_write_b128 v102, v[248:251] offset:20480\n\t"
        "ds_write_b128 v116, v[252:255] offset:24576\n\t"
        "s_waitcnt lgkmcnt(0)\n\t"
        "s_barrier\n\t"
        "v_mfma_f32_16x16x32_bf16 v[8:11], v[208:211], v[56:59], v[8:11]\n\t"
        "ds_read_b64_tr_b16 v[232:233], v103 offset:16384\n\t"
        "ds_read_b64_tr_b16 v[234:235], v103 offset:16896\n\t"
        "v_mfma_f32_16x16x32_bf16 v[12:15], v[212:215], v[56:59], v[12:15]\n\t"
        "ds_read_b64_tr_b16 v[236:237], v103 offset:16392\n\t"
        "ds_read_b64_tr_b16 v[238:239], v103 offset:16904\n\t"
        "v_mfma_f32_16x16x32_bf16 v[16:19], v[56:59], v[216:219], v[16:19]\n\t"
        "ds_read_b64_tr_b16 v[240:241], v104 offset:16384\n\t"
        "ds_read_b64_tr_b16 v[242:243], v104 offset:16640\n\t"
        "v_mfma_f32_16x16x32_bf16 v[20:23], v[208:211], v[60:63], v[20:23]\n\t"
        "s_waitcnt lgkmcnt(5)\n\t"
        "ds_read_b64_tr_b16 v[244:245], v103 offset:20480\n\t"
        "v_mfma_f32_16x16x32_bf16 v[24:27], v[212:215], v[60:63], v[24:27]\n\t"
        "ds_read_b64_tr_b16 v[246:247], v103 offset:20992\n\t"
        "ds_read_b64_tr_b16 v[248:249], v103 offset:20488\n\t"
        "v_mfma_f32_16x16x32_bf16 v[28:31], v[60:63], v[216:219], v[28:31]\n\t"
        "ds_read_b64_tr_b16 v[250:251], v103 offset:21000\n\t"
        "ds_read_b64_tr_b16 v[252:253], v104 offset:18432\n\t"
        "v_mfma_f32_16x16x32_bf16 v[8:11], v[220:223], v[64:67], v[8:11]\n\t"
        "ds_read_b64_tr_b16 v[254:255], v104 offset:18688\n\t"
        "ds_read_b128 v[72:75], v107 offset:57344\n\t"
        "v_mfma_f32_16x16x32_bf16 v[12:15], v[224:227], v[64:67], v[12:15]\n\t"
        "ds_read_b128 v[76:79], v107 offset:59392\n\t"
        "ds_read_b128 v[80:83], v108 offset:57344\n\t"
        "v_mfma_f32_16x16x32_bf16 v[16:19], v[64:67], v[228:231], v[16:19]\n\t"
        "ds_read_b128 v[84:87], v108 offset:59392\n\t"
        "v_mfma_f32_16x16x32_bf16 v[20:23], v[220:223], v[68:71], v[20:23]\n\t"
        "v_mfma_f32_16x16x32_bf16 v[24:27], v[224:227], v[68:71], v[24:27]\n\t"
        "v_mfma_f32_16x16x32_bf16 v[28:31], v[68:71], v[228:231], v[28:31]\n\t"
        "s_waitcnt vmcnt(0) lgkmcnt(0)\n\t"
        "s_barrier\n\t"
        "v_mfma_f32_16x16x32_bf16 v[8:11], v[232:235], v[72:75], v[8:11]\n\t"
        "v_mfma_f32_16x16x32_bf16 v[12:15], v[236:239], v[72:75], v[12:15]\n\t"
        "v_mfma_f32_16x16x32_bf16 v[16:19], v[72:75], v[240:243], v[16:19]\n\t"
        "v_mfma_f32_16x16x32_bf16 v[20:23], v[232:235], v[76:79], v[20:23]\n\t"
        "v_mfma_f32_16x16x32_bf16 v[24:27], v[236:239], v[76:79], v[24:27]\n\t"
        "v_mfma_f32_16x16x32_bf16 v[28:31], v[76:79], v[240:243], v[28:31]\n\t"
        "v_mfma_f32_16x16x32_bf16 v[8:11], v[244:247], v[80:83], v[8:11]\n\t"
        "v_mfma_f32_16x16x32_bf16 v[12:15], v[248:251], v[80:83], v[12:15]\n\t"
        "v_mfma_f32_16x16x32_bf16 v[16:19], v[80:83], v[252:255], v[16:19]\n\t"
        "v_mfma_f32_16x16x32_bf16 v[20:23], v[244:247], v[84:87], v[20:23]\n\t"
        "v_mfma_f32_16x16x32_bf16 v[24:27], v[248:251], v[84:87], v[24:27]\n\t"
        "v_mfma_f32_16x16x32_bf16 v[28:31], v[84:87], v[252:255], v[28:31]\n\t"
        "s_mov_b32 %[xo], 0x20000\n\t"
        "ds_read_b128 v[56:59], v105 offset:0\n\t"
        "ds_read_b128 v[60:63], v105 offset:2048\n\t"
        "ds_read_b128 v[64:67], v106 offset:0\n\t"
        "ds_read_b128 v[68:71], v106 offset:2048\n\t"
        "ds_read_b128 v[72:75], v105 offset:8192\n\t"
        "ds_read_b128 v[76:79], v105 offset:10240\n\t"
        "ds_read_b128 v[80:83], v106 offset:8192\n\t"
        "v_cvt_pk_bf16_f32 v88, v8, v9\n\t"
        "v_cvt_pk_bf16_f32 v89, v10, v11\n\t"
        "v_cvt_pk_bf16_f32 v90, v12, v13\n\t"
        "v_cvt_pk_bf16_f32 v91, v14, v15\n\t"
        "buffer_store_dwordx4 v[88:91], v109, %[sw], %[so] offen sc1\n\t"
        "v_cvt_pk_bf16_f32 v92, v20, v21\n\t"
        "v_cvt_pk_bf16_f32 v93, v22, v23\n\t"
        "v_cvt_pk_bf16_f32 v94, v24, v25\n\t"
        "v_cvt_pk_bf16_f32 v95, v26, v27\n\t"
        "buffer_store_dwordx4 v[92:95], v110, %[sw], %[so] offen sc1\n\t"
        "v_cvt_pk_bf16_f32 v96, v16, v17\n\t"
        "v_cvt_pk_bf16_f32 v97, v18, v19\n\t"
        "v_cvt_pk_bf16_f32 v98, v28, v29\n\t"
        "v_cvt_pk_bf16_f32 v99, v30, v31\n\t"
        "buffer_store_dwordx4 v[96:99], v111, %[sw], %[so] offen sc1\n\t"
        "s_mov_b32 %[so], 0xf0000000\n\t"
        "s_mov_b32 %[xo], 33792\n\t"
        "s_mov_b32 %[cn], 0\n\t"
        "LG_cal%=:\n\t"
        "buffer_load_dword v118, off, %[sw], %[xo] sc1\n\t"
        "s_waitcnt vmcnt(0)\n\t"
        "v_readfirstlane_b32 %[t0], v118\n\t"
        "s_cmp_lg_u32 %[t0], 0\n\t"
        "s_cbranch_scc1 LG_calok%=\n\t"
        "s_sleep 4\n\t"
        "s_add_u32 %[cn], %[cn], 1\n\t"
        "s_cmp_lt_u32 %[cn], 0x100000\n\t"
        "s_cbranch_scc1 LG_cal%=\n\t"
        "s_endpgm\n\t"
        "LG_calok%=:\n\t"
        "s_mov_b32 %[xo], 0x20000\n\t"
        "s_mov_b32 %[cn], 1\n\t"
        "v_mov_b32_e32 v117, %[cn]\n\t"
        "buffer_store_dword v117, v113, %[sw], 0 offen sc1\n\t"
        "s_add_u32 m0, %[ld], 32768\n\t"
        "s_add_u32 %[t0], %[xo], 512\n\t"
        "buffer_load_dwordx4 v100, %[sx], %[t0] offen lds\n\t"
        "s_add_u32 m0, %[ld], 33792\n\t"
        "s_add_u32 %[t0], %[xo], 16896\n\t"
        "buffer_load_dwordx4 v100, %[sx], %[t0] offen lds\n\t"
        "s_add_u32 m0, %[ld], 40960\n\t"
        "s_add_u32 %[t0], %[xo], 640\n\t"
        "buffer_load_dwordx4 v100, %[sx], %[t0] offen lds\n\t"
        "s_add_u32 m0, %[ld], 41984\n\t"
        "s_add_u32 %[t0], %[xo], 17024\n\t"
        "buffer_load_dwordx4 v100, %[sx], %[t0] offen lds\n\t"
        "s_add_u32 m0, %[ld], 49152\n\t"
        "s_add_u32 %[t0], %[xo], 768\n\t"
        "buffer_load_dwordx4 v100, %[sx], %[t0] offen lds\n\t"
        "s_add_u32 m0, %[ld], 50176\n\t"
        "s_add_u32 %[t0], %[xo], 17152\n\t"
        "buffer_load_dwordx4 v100, %[sx], %[t0] offen lds\n\t"
        "s_add_u32 m0, %[ld], 57344\n\t"
        "s_add_u32 %[t0], %[xo], 896\n\t"
        "buffer_load_dwordx4 v100, %[sx], %[t0] offen lds\n\t"
        "s_add_u32 m0, %[ld], 58368\n\t"
        "s_add_u32 %[t0], %[xo], 17280\n\t"
        "buffer_load_dwordx4 v100, %[sx], %[t0] offen lds\n\t"
        "s_add_u32 m0, %[ld], 65536\n\t"
        "s_add_u32 %[t0], %[xo], 1024\n\t"
        "buffer_load_dwordx4 v100, %[sx], %[t0] offen lds\n\t"
        "s_add_u32 m0, %[ld], 66560\n\t"
        "s_add_u32 %[t0], %[xo], 17408\n\t"
        "buffer_load_dwordx4 v100, %[sx], %[t0] offen lds\n\t"
        "s_add_u32 m0, %[ld], 73728\n\t"
        "s_add_u32 %[t0], %[xo], 1152\n\t"
        "buffer_load_dwordx4 v100, %[sx], %[t0] offen lds\n\t"
        "s_add_u32 m0, %[ld], 74752\n\t"
        "s_add_u32 %[t0], %[xo], 17536\n\t"
        "buffer_load_dwordx4 v100, %[sx], %[t0] offen lds\n\t"
        "s_add_u32 m0, %[ld], 81920\n\t"
        "s_add_u32 %[t0], %[xo], 1280\n\t"
        "buffer_load_dwordx4 v100, %[sx], %[t0] offen lds\n\t"
        "s_add_u32 m0, %[ld], 82944\n\t"
        "s_add_u32 %[t0], %[xo], 17664\n\t"
        "buffer_load_dwordx4 v100, %[sx], %[t0] offen lds\n\t"
        "s_add_u32 m0, %[ld], 90112\n\t"
        "s_add_u32 %[t0], %[xo], 1408\n\t"
        "buffer_load_dwordx4 v100, %[sx], %[t0] offen lds\n\t"
        "s_add_u32 m0, %[ld], 91136\n\t"
        "s_add_u32 %[t0], %[xo], 17792\n\t"
        "buffer_load_dwordx4 v100, %[sx], %[t0] offen lds\n\t"
        "s_add_u32 m0, %[ld], 98304\n\t"
        "s_add_u32 %[t0], %[xo], 1536\n\t"
        "buffer_load_dwordx4 v100, %[sx], %[t0] offen lds\n\t"
        "s_add_u32 m0, %[ld], 99328\n\t"
        "s_add_u32 %[t0], %[xo], 17920\n\t"
        "buffer_load_dwordx4 v100, %[sx], %[t0] offen lds\n\t"
        "s_add_u32 m0, %[ld], 106496\n\t"
        "s_add_u32 %[t0], %[xo], 1664\n\t"
        "buffer_load_dwordx4 v100, %[sx], %[t0] offen lds\n\t"
        "s_branch LG_mid%=\n\t"
        "LG_loop%=:\n\t"
        "s_waitcnt lgkmcnt(6)\n\t"
        "v_mfma_f32_16x16x32_bf16 v[8:11], a[0:3], v[56:59], 0\n\t"
        "v_mfma_f32_16x16x32_bf16 v[12:15], a[4:7], v[56:59], 0\n\t"
        "ds_read_b128 v[84:87], v106 offset:10240\n\t"
        "v_mfma_f32_16x16x32_bf16 v[16:19], v[56:59], a[8:11], 0\n\t"
        "s_add_u32 m0, %[ld], 107520\n\t"
        "s_add_u32 %[t0], %[xo], 18048\n\t"
        "s_waitcnt lgkmcnt(6)\n\t"
        "v_mfma_f32_16x16x32_bf16 v[20:23], a[0:3], v[60:63], 0\n\t"
        "buffer_load_dwordx4 v100, %[sx], %[t0] offen lds\n\t"
        "v_mfma_f32_16x16x32_bf16 v[24:27], a[4:7], v[60:63], 0\n\t"
        "ds_read_b128 v[56:59], v105 offset:16384\n\t"
        "v_mfma_f32_16x16x32_bf16 v[28:31], v[60:63], a[8:11], 0\n\t"
        "s_waitcnt lgkmcnt(6)\n\t"
        "v_mfma_f32_16x16x32_bf16 v[8:11], a[12:15], v[64:67], v[8:11]\n\t"
        "v_cvt_pk_bf16_f32 v88, v32, v33\n\t"
        "v_cvt_pk_bf16_f32 v89, v34, v35\n\t"
        "v_mfma_f32_16x16x32_bf16 v[12:15], a[16:19], v[64:67], v[12:15]\n\t"
        "ds_read_b128 v[60:63], v105 offset:18432\n\t"
        "v_cvt_pk_bf16_f32 v90, v36, v37\n\t"
        "v_mfma_f32_16x16x32_bf16 v[16:19], v[64:67], a[20:23], v[16:19]\n\t"
        "s_add_u32 m0, %[ld], 114688\n\t"
        "s_add_u32 %[t0], %[xo], 1792\n\t"
        "s_waitcnt lgkmcnt(6)\n\t"
        "v_mfma_f32_16x16x32_bf16 v[20:23], a[12:15], v[68:71], v[20:23]\n\t"
        "buffer_load_dwordx4 v100, %[sx], %[t0] offen lds\n\t"
        "v_mfma_f32_16x16x32_bf16 v[24:27], a[16:19], v[68:71], v[24:27]\n\t"
        "ds_read_b128 v[64:67], v106 offset:16384\n\t"
        "v_cvt_pk_bf16_f32 v91, v38, v39\n\t"
        "v_mfma_f32_16x16x32_bf16 v[28:31], v[68:71], a[20:23], v[28:31]\n\t"
        "buffer_store_dwordx4 v[88:91], v109, %[sw], %[so] offen sc1\n\t"
        "s_waitcnt lgkmcnt(6)\n\t"
        "v_mfma_f32_16x16x32_bf16 v[8:11], a[24:27], v[72:75], v[8:11]\n\t"
        "v_cvt_pk_bf16_f32 v92, v44, v45\n\t"
        "v_cvt_pk_bf16_f32 v93, v46, v47\n\t"
        "v_mfma_f32_16x16x32_bf16 v[12:15], a[28:31], v[72:75], v[12:15]\n\t"
        "ds_read_b128 v[68:71], v106 offset:18432\n\t"
        "v_cvt_pk_bf16_f32 v94, v48, v49\n\t"
        "v_mfma_f32_16x16x32_bf16 v[16:19], v[72:75], a[32:35], v[16:19]\n\t"
        "s_add_u32 m0, %[ld], 115712\n\t"
        "s_add_u32 %[t0], %[xo], 18176\n\t"
        "s_waitcnt lgkmcnt(6)\n\t"
        "v_mfma_f32_16x16x32_bf16 v[20:23], a[24:27], v[76:79], v[20:23]\n\t"
        "buffer_load_dwordx4 v100, %[sx], %[t0] offen lds\n\t"
        "v_mfma_f32_16x16x32_bf16 v[24:27], a[28:31], v[76:79], v[24:27]\n\t"
        "ds_read_b128 v[72:75], v105 offset:24576\n\t"
        "v_cvt_pk_bf16_f32 v95, v50, v51\n\t"
        "v_mfma_f32_16x16x32_bf16 v[28:31], v[76:79], a[32:35], v[28:31]\n\t"
        "buffer_store_dwordx4 v[92:95], v110, %[sw], %[so] offen sc1\n\t"
        "s_waitcnt lgkmcnt(6)\n\t"
        "v_mfma_f32_16x16x32_bf16 v[8:11], a[36:39], v[80:83], v[8:11]\n\t"
        "v_cvt_pk_bf16_f32 v96, v40, v41\n\t"
        "v_cvt_pk_bf16_f32 v97, v42, v43\n\t"
        "v_mfma_f32_16x16x32_bf16 v[12:15], a[40:43], v[80:83], v[12:15]\n\t"
        "ds_read_b128 v[76:79], v105 offset:26624\n\t"
        "v_cvt_pk_bf16_f32 v98, v52, v53\n\t"
        "v_mfma_f32_16x16x32_bf16 v[16:19], v[80:83], a[44:47], v[16:19]\n\t"
        "s_add_u32 m0, %[ld], 122880\n\t"
        "s_add_u32 %[t0], %[xo], 1920\n\t"
        "s_waitcnt lgkmcnt(6)\n\t"
        "v_mfma_f32_16x16x32_bf16 v[20:23], a[36:39], v[84:87], v[20:23]\n\t"
        "buffer_load_dwordx4 v100, %[sx], %[t0] offen lds\n\t"
        "v_mfma_f32_16x16x32_bf16 v[24:27], a[40:43], v[84:87], v[24:27]\n\t"
        "ds_read_b128 v[80:83], v106 offset:24576\n\t"
        "v_cvt_pk_bf16_f32 v99, v54, v55\n\t"
        "v_mfma_f32_16x16x32_bf16 v[28:31], v[84:87], a[44:47], v[28:31]\n\t"
        "buffer_store_dwordx4 v[96:99], v111, %[sw], %[so] offen sc1\n\t"
        "s_waitcnt lgkmcnt(6)\n\t"
        "v_mfma_f32_16x16x32_bf16 v[8:11], a[48:51], v[56:59], v[8:11]\n\t"
        "s_lshl_b32 %[so], %[cn], 19\n\t"
        "s_cmp_eq_u32 %[cn], 2\n\t"
        "s_cbranch_scc1 LG_first%=\n\t"
        "LG_back%=:\n\t"
        "v_mfma_f32_16x16x32_bf16 v[12:15], a[52:55], v[56:59], v[12:15]\n\t"
        "ds_read_b128 v[84:87], v106 offset:26624\n\t"
        "v_mfma_f32_16x16x32_bf16 v[16:19], v[56:59], a[56:59], v[16:19]\n\t"
        "s_add_u32 m0, %[ld], 123904\n\t"
        "s_add_u32 %[t0], %[xo], 18304\n\t"
        "s_waitcnt lgkmcnt(6)\n\t"
        "v_mfma_f32_16x16x32_bf16 v[20:23], a[48:51], v[60:63], v[20:23]\n\t"
        "buffer_load_dwordx4 v100, %[sx], %[t0] offen lds\n\t"
        "v_mfma_f32_16x16x32_bf16 v[24:27], a[52:55], v[60:63], v[24:27]\n\t"
        "v_mfma_f32_16x16x32_bf16 v[28:31], v[60:63], a[56:59], v[28:31]\n\t"
        "s_waitcnt lgkmcnt(5)\n\t"
        "v_mfma_f32_16x16x32_bf16 v[8:11], a[60:63], v[64:67], v[8:11]\n\t"
        "s_waitcnt vmcnt(19) lgkmcnt(0)\n\t"
        "s_barrier\n\t"
        "s_add_u32 %[xo], %[xo], 0x20000\n\t"
        "v_mfma_f32_16x16x32_bf16 v[12:15], a[64:67], v[64:67], v[12:15]\n\t"
        "ds_read_b128 v[56:59], v105 offset:32768\n\t"
        "v_mfma_f32_16x16x32_bf16 v[16:19], v[64:67], a[68:71], v[16:19]\n\t"
        "ds_read_b128 v[60:63], v105 offset:34816\n\t"
        "s_add_u32 m0, %[ld], 0\n\t"
        "s_add_u32 %[t0], %[xo], 0\n\t"
        "v_mfma_f32_16x16x32_bf16 v[20:23], a[60:63], v[68:71], v[20:23]\n\t"
        "buffer_load_dwordx4 v100, %[sx], %[t0] offen lds\n\t"
        "v_mfma_f32_16x16x32_bf16 v[24:27], a[64:67], v[68:71], v[24:27]\n\t"
        "ds_read_b128 v[64:67], v106 offset:32768\n\t"
        "v_mfma_f32_16x16x32_bf16 v[28:31], v[68:71], a[68:71], v[28:31]\n\t"
        "v_mfma_f32_16x16x32_bf16 v[8:11], a[72:75], v[72:75], v[8:11]\n\t"
        "v_mfma_f32_16x16x32_bf16 v[12:15], a[76:79], v[72:75], v[12:15]\n\t"
        "ds_read_b128 v[68:71], v106 offset:34816\n\t"
        "v_mfma_f32_16x16x32_bf16 v[16:19], v[72:75], a[80:83], v[16:19]\n\t"
        "s_add_u32 m0, %[ld], 1024\n\t"
        "s_add_u32 %[t0], %[xo], 16384\n\t"
        "v_mfma_f32_16x16x32_bf16 v[20:23], a[72:75], v[76:79], v[20:23]\n\t"
        "buffer_load_dwordx4 v100, %[sx], %[t0] offen lds\n\t"
        "v_mfma_f32_16x16x32_bf16 v[24:27], a[76:79], v[76:79], v[24:27]\n\t"
        "ds_read_b128 v[72:75], v105 offset:40960\n\t"
        "v_mfma_f32_16x16x32_bf16 v[28:31], v[76:79], a[80:83], v[28:31]\n\t"
        "v_mfma_f32_16x16x32_bf16 v[8:11], a[84:87], v[80:83], v[8:11]\n\t"
        "v_mfma_f32_16x16x32_bf16 v[12:15], a[88:91], v[80:83], v[12:15]\n\t"
        "ds_read_b128 v[76:79], v105 offset:43008\n\t"
        "v_mfma_f32_16x16x32_bf16 v[16:19], v[80:83], a[92:95], v[16:19]\n\t"
        "s_add_u32 m0, %[ld], 8192\n\t"
        "s_add_u32 %[t0], %[xo], 128\n\t"
        "v_mfma_f32_16x16x32_bf16 v[20:23], a[84:87], v[84:87], v[20:23]\n\t"
        "buffer_load_dwordx4 v100, %[sx], %[t0] offen lds\n\t"
        "v_mfma_f32_16x16x32_bf16 v[24:27], a[88:91], v[84:87], v[24:27]\n\t"
        "ds_read_b128 v[80:83], v106 offset:40960\n\t"
        "v_mfma_f32_16x16x32_bf16 v[28:31], v[84:87], a[92:95], v[28:31]\n\t"
        "s_waitcnt lgkmcnt(6)\n\t"
        "v_mfma_f32_16x16x32_bf16 v[8:11], a[96:99], v[56:59], v[8:11]\n\t"
        "v_mfma_f32_16x16x32_bf16 v[12:15], a[100:103], v[56:59], v[12:15]\n\t"
        "ds_read_b128 v[84:87], v106 offset:43008\n\t"
        "v_mfma_f32_16x16x32_bf16 v[16:19], v[56:59], a[104:107], v[16:19]\n\t"
        "s_add_u32 m0, %[ld], 9216\n\t"
        "s_add_u32 %[t0], %[xo], 16512\n\t"
        "s_waitcnt lgkmcnt(6)\n\t"
        "v_mfma_f32_16x16x32_bf16 v[20:23], a[96:99], v[60:63], v[20:23]\n\t"
        "buffer_load_dwordx4 v100, %[sx], %[t0] offen lds\n\t"
        "v_mfma_f32_16x16x32_bf16 v[24:27], a[100:103], v[60:63], v[24:27]\n\t"
        "ds_read_b128 v[56:59], v105 offset:49152\n\t"
        "v_mfma_f32_16x16x32_bf16 v[28:31], v[60:63], a[104:107], v[28:31]\n\t"
        "s_waitcnt lgkmcnt(6)\n\t"
        "v_mfma_f32_16x16x32_bf16 v[8:11], a[108:111], v[64:67], v[8:11]\n\t"
        "v_mfma_f32_16x16x32_bf16 v[12:15], a[112:115], v[64:67], v[12:15]\n\t"
        "ds_read_b128 v[60:63], v105 offset:51200\n\t"
        "v_mfma_f32_16x16x32_bf16 v[16:19], v[64:67], a[116:119], v[16:19]\n\t"
        "s_add_u32 m0, %[ld], 16384\n\t"
        "s_add_u32 %[t0], %[xo], 256\n\t"
        "s_waitcnt lgkmcnt(6)\n\t"
        "v_mfma_f32_16x16x32_bf16 v[20:23], a[108:111], v[68:71], v[20:23]\n\t"
        "buffer_load_dwordx4 v100, %[sx], %[t0] offen lds\n\t"
        "v_mfma_f32_16x16x32_bf16 v[24:27], a[112:115], v[68:71], v[24:27]\n\t"
        "ds_read_b128 v[64:67], v106 offset:49152\n\t"
        "v_mfma_f32_16x16x32_bf16 v[28:31], v[68:71], a[116:119], v[28:31]\n\t"
        "s_waitcnt lgkmcnt(6)\n\t"
        "v_mfma_f32_16x16x32_bf16 v[8:11], a[120:123], v[72:75], v[8:11]\n\t"
        "v_mfma_f32_16x16x32_bf16 v[12:15], a[124:127], v[72:75], v[12:15]\n\t"
        "ds_read_b128 v[68:71], v106 offset:51200\n\t"
        "v_mfma_f32_16x16x32_bf16 v[16:19], v[72:75], a[128:131], v[16:19]\n\t"
        "s_add_u32 m0, %[ld], 17408\n\t"
        "s_add_u32 %[t0], %[xo], 16640\n\t"
        "s_waitcnt lgkmcnt(6)\n\t"
        "v_mfma_f32_16x16x32_bf16 v[20:23], a[120:123], v[76:79], v[20:23]\n\t"
        "buffer_load_dwordx4 v100, %[sx], %[t0] offen lds\n\t"
        "v_mfma_f32_16x16x32_bf16 v[24:27], a[124:127], v[76:79], v[24:27]\n\t"
        "ds_read_b128 v[72:75], v105 offset:57344\n\t"
        "v_mfma_f32_16x16x32_bf16 v[28:31], v[76:79], a[128:131], v[28:31]\n\t"
        "s_waitcnt lgkmcnt(6)\n\t"
        "v_mfma_f32_16x16x32_bf16 v[8:11], a[132:135], v[80:83], v[8:11]\n\t"
        "v_mfma_f32_16x16x32_bf16 v[12:15], a[136:139], v[80:83], v[12:15]\n\t"
        "ds_read_b128 v[76:79], v105 offset:59392\n\t"
        "v_mfma_f32_16x16x32_bf16 v[16:19], v[80:83], a[140:143], v[16:19]\n\t"
        "s_add_u32 m0, %[ld], 24576\n\t"
        "s_add_u32 %[t0], %[xo], 384\n\t"
        "s_waitcnt lgkmcnt(6)\n\t"
        "v_mfma_f32_16x16x32_bf16 v[20:23], a[132:135], v[84:87], v[20:23]\n\t"
        "buffer_load_dwordx4 v100, %[sx], %[t0] offen lds\n\t"
        "v_mfma_f32_16x16x32_bf16 v[24:27], a[136:139], v[84:87], v[24:27]\n\t"
        "ds_read_b128 v[80:83], v106 offset:57344\n\t"
        "v_mfma_f32_16x16x32_bf16 v[28:31], v[84:87], a[140:143], v[28:31]\n\t"
        "s_waitcnt lgkmcnt(6)\n\t"
        "v_mfma_f32_16x16x32_bf16 v[8:11], a[144:147], v[56:59], v[8:11]\n\t"
        "v_mfma_f32_16x16x32_bf16 v[12:15], a[148:151], v[56:59], v[12:15]\n\t"
        "ds_read_b128 v[84:87], v106 offset:59392\n\t"
        "v_mfma_f32_16x16x32_bf16 v[16:19], v[56:59], a[152:155], v[16:19]\n\t"
        "s_add_u32 m0, %[ld], 25600\n\t"
        "s_add_u32 %[t0], %[xo], 16768\n\t"
        "s_waitcnt lgkmcnt(6)\n\t"
        "v_mfma_f32_16x16x32_bf16 v[20:23], a[144:147], v[60:63], v[20:23]\n\t"
        "buffer_load_dwordx4 v100, %[sx], %[t0] offen lds\n\t"
        "v_mfma_f32_16x16x32_bf16 v[24:27], a[148:151], v[60:63], v[24:27]\n\t"
        "v_mfma_f32_16x16x32_bf16 v[28:31], v[60:63], a[152:155], v[28:31]\n\t"
        "s_waitcnt lgkmcnt(5)\n\t"
        "v_mfma_f32_16x16x32_bf16 v[8:11], a[156:159], v[64:67], v[8:11]\n\t"
        "s_waitcnt vmcnt(9) lgkmcnt(0)\n\t"
        "s_barrier\n\t"
        "v_mfma_f32_16x16x32_bf16 v[12:15], a[160:163], v[64:67], v[12:15]\n\t"
        "ds_read_b128 v[56:59], v107 offset:0\n\t"
        "v_mfma_f32_16x16x32_bf16 v[16:19], v[64:67], a[164:167], v[16:19]\n\t"
        "ds_read_b128 v[60:63], v107 offset:2048\n\t"
        "s_add_u32 m0, %[ld], 32768\n\t"
        "s_add_u32 %[t0], %[xo], 512\n\t"
        "v_mfma_f32_16x16x32_bf16 v[20:23], a[156:159], v[68:71], v[20:23]\n\t"
        "buffer_load_dwordx4 v100, %[sx], %[t0] offen lds\n\t"
        "v_mfma_f32_16x16x32_bf16 v[24:27], a[160:163], v[68:71], v[24:27]\n\t"
        "ds_read_b128 v[64:67], v108 offset:0\n\t"
        "v_mfma_f32_16x16x32_bf16 v[28:31], v[68:71], a[164:167], v[28:31]\n\t"
        "v_mfma_f32_16x16x32_bf16 v[8:11], a[168:171], v[72:75], v[8:11]\n\t"
        "v_mfma_f32_16x16x32_bf16 v[12:15], a[172:175], v[72:75], v[12:15]\n\t"
        "ds_read_b128 v[68:71], v108 offset:2048\n\t"
        "v_mov_b32_e32 v117, %[cn]\n\t"
        "buffer_store_dword v117, v113, %[sw], 0 offen sc1\n\t"
        "s_add_u32 %[cn], %[cn], 1\n\t"
        "v_mfma_f32_16x16x32_bf16 v[16:19], v[72:75], a[176:179], v[16:19]\n\t"
        "s_add_u32 m0, %[ld], 33792\n\t"
        "s_add_u32 %[t0], %[xo], 16896\n\t"
        "v_mfma_f32_16x16x32_bf16 v[20:23], a[168:171], v[76:79], v[20:23]\n\t"
        "buffer_load_dwordx4 v100, %[sx], %[t0] offen lds\n\t"
        "v_mfma_f32_16x16x32_bf16 v[24:27], a[172:175], v[76:79], v[24:27]\n\t"
        "ds_read_b128 v[72:75], v107 offset:8192\n\t"
        "v_mfma_f32_16x16x32_bf16 v[28:31], v[76:79], a[176:179], v[28:31]\n\t"
        "v_mfma_f32_16x16x32_bf16 v[8:11], a[180:183], v[80:83], v[8:11]\n\t"
        "v_mfma_f32_16x16x32_bf16 v[12:15], a[184:187], v[80:83], v[12:15]\n\t"
        "ds_read_b128 v[76:79], v107 offset:10240\n\t"
        "v_mfma_f32_16x16x32_bf16 v[16:19], v[80:83], a[188:191], v[16:19]\n\t"
        "s_add_u32 m0, %[ld], 40960\n\t"
        "s_add_u32 %[t0], %[xo], 640\n\t"
        "v_mfma_f32_16x16x32_bf16 v[20:23], a[180:183], v[84:87], v[20:23]\n\t"
        "buffer_load_dwordx4 v100, %[sx], %[t0] offen lds\n\t"
        "v_mfma_f32_16x16x32_bf16 v[24:27], a[184:187], v[84:87], v[24:27]\n\t"
        "ds_read_b128 v[80:83], v108 offset:8192\n\t"
        "v_mfma_f32_16x16x32_bf16 v[28:31], v[84:87], a[188:191], v[28:31]\n\t"
        "s_waitcnt lgkmcnt(6)\n\t"
        "v_mfma_f32_16x16x32_bf16 v[8:11], a[192:195], v[56:59], v[8:11]\n\t"
        "v_mfma_f32_16x16x32_bf16 v[12:15], a[196:199], v[56:59], v[12:15]\n\t"
        "ds_read_b128 v[84:87], v108 offset:10240\n\t"
        "v_mfma_f32_16x16x32_bf16 v[16:19], v[56:59], a[200:203], v[16:19]\n\t"
        "s_add_u32 m0, %[ld], 41984\n\t"
        "s_add_u32 %[t0], %[xo], 17024\n\t"
        "s_waitcnt lgkmcnt(6)\n\t"
        "v_mfma_f32_16x16x32_bf16 v[20:23], a[192:195], v[60:63], v[20:23]\n\t"
        "buffer_load_dwordx4 v100, %[sx], %[t0] offen lds\n\t"
        "v_mfma_f32_16x16x32_bf16 v[24:27], a[196:199], v[60:63], v[24:27]\n\t"
        "ds_read_b128 v[56:59], v107 offset:16384\n\t"
        "v_mfma_f32_16x16x32_bf16 v[28:31], v[60:63], a[200:203], v[28:31]\n\t"
        "s_waitcnt lgkmcnt(6)\n\t"
        "v_mfma_f32_16x16x32_bf16 v[8:11], a[204:207], v[64:67], v[8:11]\n\t"
        "v_mfma_f32_16x16x32_bf16 v[12:15], a[208:211], v[64:67], v[12:15]\n\t"
        "ds_read_b128 v[60:63], v107 offset:18432\n\t"
        "v_mfma_f32_16x16x32_bf16 v[16:19], v[64:67], a[212:215], v[16:19]\n\t"
        "s_add_u32 m0, %[ld], 49152\n\t"
        "s_add_u32 %[t0], %[xo], 768\n\t"
        "s_waitcnt lgkmcnt(6)\n\t"
        "v_mfma_f32_16x16x32_bf16 v[20:23], a[204:207], v[68:71], v[20:23]\n\t"
        "buffer_load_dwordx4 v100, %[sx], %[t0] offen lds\n\t"
        "v_mfma_f32_16x16x32_bf16 v[24:27], a[208:211], v[68:71], v[24:27]\n\t"
        "ds_read_b128 v[64:67], v108 offset:16384\n\t"
        "v_mfma_f32_16x16x32_bf16 v[28:31], v[68:71], a[212:215], v[28:31]\n\t"
        "s_waitcnt lgkmcnt(6)\n\t"
        "v_mfma_f32_16x16x32_bf16 v[8:11], a[216:219], v[72:75], v[8:11]\n\t"
        "v_mfma_f32_16x16x32_bf16 v[12:15], a[220:223], v[72:75], v[12:15]\n\t"
        "ds_read_b128 v[68:71], v108 offset:18432\n\t"
        "v_mfma_f32_16x16x32_bf16 v[16:19], v[72:75], a[224:227], v[16:19]\n\t"
        "s_add_u32 m0, %[ld], 50176\n\t"
        "s_add_u32 %[t0], %[xo], 17152\n\t"
        "s_waitcnt lgkmcnt(6)\n\t"
        "v_mfma_f32_16x16x32_bf16 v[20:23], a[216:219], v[76:79], v[20:23]\n\t"
        "buffer_load_dwordx4 v100, %[sx], %[t0] offen lds\n\t"
        "v_mfma_f32_16x16x32_bf16 v[24:27], a[220:223], v[76:79], v[24:27]\n\t"
        "ds_read_b128 v[72:75], v107 offset:24576\n\t"
        "v_mfma_f32_16x16x32_bf16 v[28:31], v[76:79], a[224:227], v[28:31]\n\t"
        "s_waitcnt lgkmcnt(6)\n\t"
        "v_mfma_f32_16x16x32_bf16 v[8:11], a[228:231], v[80:83], v[8:11]\n\t"
        "v_mfma_f32_16x16x32_bf16 v[12:15], a[232:235], v[80:83], v[12:15]\n\t"
        "ds_read_b128 v[76:79], v107 offset:26624\n\t"
        "v_mfma_f32_16x16x32_bf16 v[16:19], v[80:83], a[236:239], v[16:19]\n\t"
        "s_add_u32 m0, %[ld], 57344\n\t"
        "s_add_u32 %[t0], %[xo], 896\n\t"
        "s_waitcnt lgkmcnt(6)\n\t"
        "v_mfma_f32_16x16x32_bf16 v[20:23], a[228:231], v[84:87], v[20:23]\n\t"
        "buffer_load_dwordx4 v100, %[sx], %[t0] offen lds\n\t"
        "v_mfma_f32_16x16x32_bf16 v[24:27], a[232:235], v[84:87], v[24:27]\n\t"
        "ds_read_b128 v[80:83], v108 offset:24576\n\t"
        "v_mfma_f32_16x16x32_bf16 v[28:31], v[84:87], a[236:239], v[28:31]\n\t"
        "s_waitcnt lgkmcnt(6)\n\t"
        "v_mfma_f32_16x16x32_bf16 v[8:11], a[240:243], v[56:59], v[8:11]\n\t"
        "v_mfma_f32_16x16x32_bf16 v[12:15], a[244:247], v[56:59], v[12:15]\n\t"
        "ds_read_b128 v[84:87], v108 offset:26624\n\t"
        "v_mfma_f32_16x16x32_bf16 v[16:19], v[56:59], a[248:251], v[16:19]\n\t"
        "s_add_u32 m0, %[ld], 58368\n\t"
        "s_add_u32 %[t0], %[xo], 17280\n\t"
        "s_waitcnt lgkmcnt(6)\n\t"
        "v_mfma_f32_16x16x32_bf16 v[20:23], a[240:243], v[60:63], v[20:23]\n\t"
        "buffer_load_dwordx4 v100, %[sx], %[t0] offen lds\n\t"
        "v_mfma_f32_16x16x32_bf16 v[24:27], a[244:247], v[60:63], v[24:27]\n\t"
        "v_mfma_f32_16x16x32_bf16 v[28:31], v[60:63], a[248:251], v[28:31]\n\t"
        "s_waitcnt lgkmcnt(5)\n\t"
        "v_mfma_f32_16x16x32_bf16 v[8:11], a[252:255], v[64:67], v[8:11]\n\t"
        "s_waitcnt vmcnt(17) lgkmcnt(0)\n\t"
        "s_barrier\n\t"
        "v_mfma_f32_16x16x32_bf16 v[12:15], v[128:131], v[64:67], v[12:15]\n\t"
        "ds_read_b128 v[56:59], v107 offset:32768\n\t"
        "v_mfma_f32_16x16x32_bf16 v[16:19], v[64:67], v[132:135], v[16:19]\n\t"
        "ds_read_b128 v[60:63], v107 offset:34816\n\t"
        "s_add_u32 m0, %[ld], 65536\n\t"
        "s_add_u32 %[t0], %[xo], 1024\n\t"
        "v_mfma_f32_16x16x32_bf16 v[20:23], a[252:255], v[68:71], v[20:23]\n\t"
        "buffer_load_dwordx4 v100, %[sx], %[t0] offen lds\n\t"
        "v_mfma_f32_16x16x32_bf16 v[24:27], v[128:131], v[68:71], v[24:27]\n\t"
        "ds_read_b128 v[64:67], v108 offset:32768\n\t"
        "v_mfma_f32_16x16x32_bf16 v[28:31], v[68:71], v[132:135], v[28:31]\n\t"
        "v_mfma_f32_16x16x32_bf16 v[8:11], v[136:139], v[72:75], v[8:11]\n\t"
        "v_mfma_f32_16x16x32_bf16 v[12:15], v[140:143], v[72:75], v[12:15]\n\t"
        "ds_read_b128 v[68:71], v108 offset:34816\n\t"
        "v_mfma_f32_16x16x32_bf16 v[16:19], v[72:75], v[144:147], v[16:19]\n\t"
        "s_add_u32 m0, %[ld], 66560\n\t"
        "s_add_u32 %[t0], %[xo], 17408\n\t"
        "v_mfma_f32_16x16x32_bf16 v[20:23], v[136:139], v[76:79], v[20:23]\n\t"
        "buffer_load_dwordx4 v100, %[sx], %[t0] offen lds\n\t"
        "v_mfma_f32_16x16x32_bf16 v[24:27], v[140:143], v[76:79], v[24:27]\n\t"
        "ds_read_b128 v[72:75], v107 offset:40960\n\t"
        "v_mfma_f32_16x16x32_bf16 v[28:31], v[76:79], v[144:147], v[28:31]\n\t"
        "v_mfma_f32_16x16x32_bf16 v[8:11], v[148:151], v[80:83], v[8:11]\n\t"
        "v_mfma_f32_16x16x32_bf16 v[12:15], v[152:155], v[80:83], v[12:15]\n\t"
        "ds_read_b128 v[76:79], v107 offset:43008\n\t"
        "v_mfma_f32_16x16x32_bf16 v[16:19], v[80:83], v[156:159], v[16:19]\n\t"
        "s_add_u32 m0, %[ld], 73728\n\t"
        "s_add_u32 %[t0], %[xo], 1152\n\t"
        "v_mfma_f32_16x16x32_bf16 v[20:23], v[148:151], v[84:87], v[20:23]\n\t"
        "buffer_load_dwordx4 v100, %[sx], %[t0] offen lds\n\t"
        "v_mfma_f32_16x16x32_bf16 v[24:27], v[152:155], v[84:87], v[24:27]\n\t"
        "ds_read_b128 v[80:83], v108 offset:40960\n\t"
        "v_mfma_f32_16x16x32_bf16 v[28:31], v[84:87], v[156:159], v[28:31]\n\t"
        "s_waitcnt lgkmcnt(6)\n\t"
        "v_mfma_f32_16x16x32_bf16 v[8:11], v[160:163], v[56:59], v[8:11]\n\t"
        "v_mfma_f32_16x16x32_bf16 v[12:15], v[164:167], v[56:59], v[12:15]\n\t"
        "ds_read_b128 v[84:87], v108 offset:43008\n\t"
        "v_mfma_f32_16x16x32_bf16 v[16:19], v[56:59], v[168:171], v[16:19]\n\t"
        "s_add_u32 m0, %[ld], 74752\n\t"
        "s_add_u32 %[t0], %[xo], 17536\n\t"
        "s_waitcnt lgkmcnt(6)\n\t"
        "v_mfma_f32_16x16x32_bf16 v[20:23], v[160:163], v[60:63], v[20:23]\n\t"
        "buffer_load_dwordx4 v100, %[sx], %[t0] offen lds\n\t"
        "v_mfma_f32_16x16x32_bf16 v[24:27], v[164:167], v[60:63], v[24:27]\n\t"
        "ds_read_b128 v[56:59], v107 offset:49152\n\t"
        "v_mfma_f32_16x16x32_bf16 v[28:31], v[60:63], v[168:171], v[28:31]\n\t"
        "s_waitcnt lgkmcnt(6)\n\t"
        "v_mfma_f32_16x16x32_bf16 v[8:11], v[172:175], v[64:67], v[8:11]\n\t"
        "v_mfma_f32_16x16x32_bf16 v[12:15], v[176:179], v[64:67], v[12:15]\n\t"
        "ds_read_b128 v[60:63], v107 offset:51200\n\t"
        "v_mfma_f32_16x16x32_bf16 v[16:19], v[64:67], v[180:183], v[16:19]\n\t"
        "s_add_u32 m0, %[ld], 81920\n\t"
        "s_add_u32 %[t0], %[xo], 1280\n\t"
        "s_waitcnt lgkmcnt(6)\n\t"
        "v_mfma_f32_16x16x32_bf16 v[20:23], v[172:175], v[68:71], v[20:23]\n\t"
        "buffer_load_dwordx4 v100, %[sx], %[t0] offen lds\n\t"
        "v_mfma_f32_16x16x32_bf16 v[24:27], v[176:179], v[68:71], v[24:27]\n\t"
        "ds_read_b128 v[64:67], v108 offset:49152\n\t"
        "v_mfma_f32_16x16x32_bf16 v[28:31], v[68:71], v[180:183], v[28:31]\n\t"
        "s_waitcnt lgkmcnt(6)\n\t"
        "v_mfma_f32_16x16x32_bf16 v[8:11], v[184:187], v[72:75], v[8:11]\n\t"
        "v_mfma_f32_16x16x32_bf16 v[12:15], v[188:191], v[72:75], v[12:15]\n\t"
        "ds_read_b128 v[68:71], v108 offset:51200\n\t"
        "v_mfma_f32_16x16x32_bf16 v[16:19], v[72:75], v[192:195], v[16:19]\n\t"
        "s_add_u32 m0, %[ld], 82944\n\t"
        "s_add_u32 %[t0], %[xo], 17664\n\t"
        "s_waitcnt lgkmcnt(6)\n\t"
        "v_mfma_f32_16x16x32_bf16 v[20:23], v[184:187], v[76:79], v[20:23]\n\t"
        "buffer_load_dwordx4 v100, %[sx], %[t0] offen lds\n\t"
        "v_mfma_f32_16x16x32_bf16 v[24:27], v[188:191], v[76:79], v[24:27]\n\t"
        "ds_read_b128 v[72:75], v107 offset:57344\n\t"
        "v_mfma_f32_16x16x32_bf16 v[28:31], v[76:79], v[192:195], v[28:31]\n\t"
        "s_waitcnt lgkmcnt(6)\n\t"
        "v_mfma_f32_16x16x32_bf16 v[8:11], v[196:199], v[80:83], v[8:11]\n\t"
        "v_mfma_f32_16x16x32_bf16 v[12:15], v[200:203], v[80:83], v[12:15]\n\t"
        "ds_read_b128 v[76:79], v107 offset:59392\n\t"
        "v_mfma_f32_16x16x32_bf16 v[16:19], v[80:83], v[204:207], v[16:19]\n\t"
        "s_add_u32 m0, %[ld], 90112\n\t"
        "s_add_u32 %[t0], %[xo], 1408\n\t"
        "s_waitcnt lgkmcnt(6)\n\t"
        "v_mfma_f32_16x16x32_bf16 v[20:23], v[196:199], v[84:87], v[20:23]\n\t"
        "buffer_load_dwordx4 v100, %[sx], %[t0] offen lds\n\t"
        "v_mfma_f32_16x16x32_bf16 v[24:27], v[200:203], v[84:87], v[24:27]\n\t"
        "ds_read_b128 v[80:83], v108 offset:57344\n\t"
        "v_mfma_f32_16x16x32_bf16 v[28:31], v[84:87], v[204:207], v[28:31]\n\t"
        "s_waitcnt lgkmcnt(6)\n\t"
        "v_mfma_f32_16x16x32_bf16 v[8:11], v[208:211], v[56:59], v[8:11]\n\t"
        "v_mfma_f32_16x16x32_bf16 v[12:15], v[212:215], v[56:59], v[12:15]\n\t"
        "ds_read_b128 v[84:87], v108 offset:59392\n\t"
        "v_mfma_f32_16x16x32_bf16 v[16:19], v[56:59], v[216:219], v[16:19]\n\t"
        "s_add_u32 m0, %[ld], 91136\n\t"
        "s_add_u32 %[t0], %[xo], 17792\n\t"
        "s_waitcnt lgkmcnt(6)\n\t"
        "v_mfma_f32_16x16x32_bf16 v[20:23], v[208:211], v[60:63], v[20:23]\n\t"
        "buffer_load_dwordx4 v100, %[sx], %[t0] offen lds\n\t"
        "v_mfma_f32_16x16x32_bf16 v[24:27], v[212:215], v[60:63], v[24:27]\n\t"
        "v_mfma_f32_16x16x32_bf16 v[28:31], v[60:63], v[216:219], v[28:31]\n\t"
        "s_waitcnt lgkmcnt(5)\n\t"
        "v_mfma_f32_16x16x32_bf16 v[8:11], v[220:223], v[64:67], v[8:11]\n\t"
        "s_waitcnt vmcnt(17) lgkmcnt(0)\n\t"
        "s_barrier\n\t"
        "v_mfma_f32_16x16x32_bf16 v[12:15], v[224:227], v[64:67], v[12:15]\n\t"
        "ds_read_b128 v[56:59], v105 offset:0\n\t"
        "v_mfma_f32_16x16x32_bf16 v[16:19], v[64:67], v[228:231], v[16:19]\n\t"
        "ds_read_b128 v[60:63], v105 offset:2048\n\t"
        "s_add_u32 m0, %[ld], 98304\n\t"
        "s_add_u32 %[t0], %[xo], 1536\n\t"
        "v_mfma_f32_16x16x32_bf16 v[20:23], v[220:223], v[68:71], v[20:23]\n\t"
        "buffer_load_dwordx4 v100, %[sx], %[t0] offen lds\n\t"
        "v_mfma_f32_16x16x32_bf16 v[24:27], v[224:227], v[68:71], v[24:27]\n\t"
        "ds_read_b128 v[64:67], v106 offset:0\n\t"
        "v_mfma_f32_16x16x32_bf16 v[28:31], v[68:71], v[228:231], v[28:31]\n\t"
        "v_mfma_f32_16x16x32_bf16 v[8:11], v[232:235], v[72:75], v[8:11]\n\t"
        "v_mfma_f32_16x16x32_bf16 v[12:15], v[236:239], v[72:75], v[12:15]\n\t"
        "ds_read_b128 v[68:71], v106 offset:2048\n\t"
        "v_mfma_f32_16x16x32_bf16 v[16:19], v[72:75], v[240:243], v[16:19]\n\t"
        "s_add_u32 m0, %[ld], 99328\n\t"
        "s_add_u32 %[t0], %[xo], 17920\n\t"
        "v_mfma_f32_16x16x32_bf16 v[20:23], v[232:235], v[76:79], v[20:23]\n\t"
        "buffer_load_dwordx4 v100, %[sx], %[t0] offen lds\n\t"
        "v_mfma_f32_16x16x32_bf16 v[24:27], v[236:239], v[76:79], v[24:27]\n\t"
        "ds_read_b128 v[72:75], v105 offset:8192\n\t"
        "v_mfma_f32_16x16x32_bf16 v[28:31], v[76:79], v[240:243], v[28:31]\n\t"
        "v_mfma_f32_16x16x32_bf16 v[8:11], v[244:247], v[80:83], v[8:11]\n\t"
        "v_mfma_f32_16x16x32_bf16 v[12:15], v[248:251], v[80:83], v[12:15]\n\t"
        "ds_read_b128 v[76:79], v105 offset:10240\n\t"
        "v_mfma_f32_16x16x32_bf16 v[16:19], v[80:83], v[252:255], v[16:19]\n\t"
        "s_add_u32 m0, %[ld], 106496\n\t"
        "s_add_u32 %[t0], %[xo], 1664\n\t"
        "v_mfma_f32_16x16x32_bf16 v[20:23], v[244:247], v[84:87], v[20:23]\n\t"
        "buffer_load_dwordx4 v100, %[sx], %[t0] offen lds\n\t"
        "v_mfma_f32_16x16x32_bf16 v[24:27], v[248:251], v[84:87], v[24:27]\n\t"
        "ds_read_b128 v[80:83], v106 offset:8192\n\t"
        "v_mfma_f32_16x16x32_bf16 v[28:31], v[84:87], v[252:255], v[28:31]\n\t"
        "LG_mid%=:\n\t"
        "s_waitcnt lgkmcnt(6)\n\t"
        "v_mfma_f32_16x16x32_bf16 v[32:35], a[0:3], v[56:59], 0\n\t"
        "v_mfma_f32_16x16x32_bf16 v[36:39], a[4:7], v[56:59], 0\n\t"
        "ds_read_b128 v[84:87], v106 offset:10240\n\t"
        "v_mfma_f32_16x16x32_bf16 v[40:43], v[56:59], a[8:11], 0\n\t"
        "s_add_u32 m0, %[ld], 107520\n\t"
        "s_add_u32 %[t0], %[xo], 18048\n\t"
        "s_waitcnt lgkmcnt(6)\n\t"
        "v_mfma_f32_16x16x32_bf16 v[44:47], a[0:3], v[60:63], 0\n\t"
        "buffer_load_dwordx4 v100, %[sx], %[t0] offen lds\n\t"
        "v_mfma_f32_16x16x32_bf16 v[48:51], a[4:7], v[60:63], 0\n\t"
        "ds_read_b128 v[56:59], v105 offset:16384\n\t"
        "v_mfma_f32_16x16x32_bf16 v[52:55], v[60:63], a[8:11], 0\n\t"
        "s_waitcnt lgkmcnt(6)\n\t"
        "v_mfma_f32_16x16x32_bf16 v[32:35], a[12:15], v[64:67], v[32:35]\n\t"
        "v_cvt_pk_bf16_f32 v88, v8, v9\n\t"
        "v_cvt_pk_bf16_f32 v89, v10, v11\n\t"
        "v_mfma_f32_16x16x32_bf16 v[36:39], a[16:19], v[64:67], v[36:39]\n\t"
        "ds_read_b128 v[60:63], v105 offset:18432\n\t"
        "v_cvt_pk_bf16_f32 v90, v12, v13\n\t"
        "v_mfma_f32_16x16x32_bf16 v[40:43], v[64:67], a[20:23], v[40:43]\n\t"
        "s_add_u32 m0, %[ld], 114688\n\t"
        "s_add_u32 %[t0], %[xo], 1792\n\t"
        "s_waitcnt lgkmcnt(6)\n\t"
        "v_mfma_f32_16x16x32_bf16 v[44:47], a[12:15], v[68:71], v[44:47]\n\t"
        "buffer_load_dwordx4 v100, %[sx], %[t0] offen lds\n\t"
        "v_mfma_f32_16x16x32_bf16 v[48:51], a[16:19], v[68:71], v[48:51]\n\t"
        "ds_read_b128 v[64:67], v106 offset:16384\n\t"
        "v_cvt_pk_bf16_f32 v91, v14, v15\n\t"
        "v_mfma_f32_16x16x32_bf16 v[52:55], v[68:71], a[20:23], v[52:55]\n\t"
        "buffer_store_dwordx4 v[88:91], v109, %[sw], %[so] offen sc1\n\t"
        "s_waitcnt lgkmcnt(6)\n\t"
        "v_mfma_f32_16x16x32_bf16 v[32:35], a[24:27], v[72:75], v[32:35]\n\t"
        "v_cvt_pk_bf16_f32 v92, v20, v21\n\t"
        "v_cvt_pk_bf16_f32 v93, v22, v23\n\t"
        "v_mfma_f32_16x16x32_bf16 v[36:39], a[28:31], v[72:75], v[36:39]\n\t"
        "ds_read_b128 v[68:71], v106 offset:18432\n\t"
        "v_cvt_pk_bf16_f32 v94, v24, v25\n\t"
        "v_mfma_f32_16x16x32_bf16 v[40:43], v[72:75], a[32:35], v[40:43]\n\t"
        "s_add_u32 m0, %[ld], 115712\n\t"
        "s_add_u32 %[t0], %[xo], 18176\n\t"
        "s_waitcnt lgkmcnt(6)\n\t"
        "v_mfma_f32_16x16x32_bf16 v[44:47], a[24:27], v[76:79], v[44:47]\n\t"
        "buffer_load_dwordx4 v100, %[sx], %[t0] offen lds\n\t"
        "v_mfma_f32_16x16x32_bf16 v[48:51], a[28:31], v[76:79], v[48:51]\n\t"
        "ds_read_b128 v[72:75], v105 offset:24576\n\t"
        "v_cvt_pk_bf16_f32 v95, v26, v27\n\t"
        "v_mfma_f32_16x16x32_bf16 v[52:55], v[76:79], a[32:35], v[52:55]\n\t"
        "buffer_store_dwordx4 v[92:95], v110, %[sw], %[so] offen sc1\n\t"
        "s_waitcnt lgkmcnt(6)\n\t"
        "v_mfma_f32_16x16x32_bf16 v[32:35], a[36:39], v[80:83], v[32:35]\n\t"
        "v_cvt_pk_bf16_f32 v96, v16, v17\n\t"
        "v_cvt_pk_bf16_f32 v97, v18, v19\n\t"
        "v_mfma_f32_16x16x32_bf16 v[36:39], a[40:43], v[80:83], v[36:39]\n\t"
        "ds_read_b128 v[76:79], v105 offset:26624\n\t"
        "v_cvt_pk_bf16_f32 v98, v28, v29\n\t"
        "v_mfma_f32_16x16x32_bf16 v[40:43], v[80:83], a[44:47], v[40:43]\n\t"
        "s_add_u32 m0, %[ld], 122880\n\t"
        "s_add_u32 %[t0], %[xo], 1920\n\t"
        "s_waitcnt lgkmcnt(6)\n\t"
        "v_mfma_f32_16x16x32_bf16 v[44:47], a[36:39], v[84:87], v[44:47]\n\t"
        "buffer_load_dwordx4 v100, %[sx], %[t0] offen lds\n\t"
        "v_mfma_f32_16x16x32_bf16 v[48:51], a[40:43], v[84:87], v[48:51]\n\t"
        "ds_read_b128 v[80:83], v106 offset:24576\n\t"
        "v_cvt_pk_bf16_f32 v99, v30, v31\n\t"
        "v_mfma_f32_16x16x32_bf16 v[52:55], v[84:87], a[44:47], v[52:55]\n\t"
        "buffer_store_dwordx4 v[96:99], v111, %[sw], %[so] offen sc1\n\t"
        "s_waitcnt lgkmcnt(6)\n\t"
        "v_mfma_f32_16x16x32_bf16 v[32:35], a[48:51], v[56:59], v[32:35]\n\t"
        "s_lshl_b32 %[so], %[cn], 19\n\t"
        "v_mfma_f32_16x16x32_bf16 v[36:39], a[52:55], v[56:59], v[36:39]\n\t"
        "ds_read_b128 v[84:87], v106 offset:26624\n\t"
        "v_mfma_f32_16x16x32_bf16 v[40:43], v[56:59], a[56:59], v[40:43]\n\t"
        "s_add_u32 m0, %[ld], 123904\n\t"
        "s_add_u32 %[t0], %[xo], 18304\n\t"
        "s_waitcnt lgkmcnt(6)\n\t"
        "v_mfma_f32_16x16x32_bf16 v[44:47], a[48:51], v[60:63], v[44:47]\n\t"
        "buffer_load_dwordx4 v100, %[sx], %[t0] offen lds\n\t"
        "v_mfma_f32_16x16x32_bf16 v[48:51], a[52:55], v[60:63], v[48:51]\n\t"
        "v_mfma_f32_16x16x32_bf16 v[52:55], v[60:63], a[56:59], v[52:55]\n\t"
        "s_waitcnt lgkmcnt(5)\n\t"
        "v_mfma_f32_16x16x32_bf16 v[32:35], a[60:63], v[64:67], v[32:35]\n\t"
        "s_waitcnt vmcnt(19) lgkmcnt(0)\n\t"
        "s_barrier\n\t"
        "s_add_u32 %[xo], %[xo], 0x20000\n\t"
        "v_mfma_f32_16x16x32_bf16 v[36:39], a[64:67], v[64:67], v[36:39]\n\t"
        "ds_read_b128 v[56:59], v105 offset:32768\n\t"
        "v_mfma_f32_16x16x32_bf16 v[40:43], v[64:67], a[68:71], v[40:43]\n\t"
        "ds_read_b128 v[60:63], v105 offset:34816\n\t"
        "s_add_u32 m0, %[ld], 0\n\t"
        "s_add_u32 %[t0], %[xo], 0\n\t"
        "v_mfma_f32_16x16x32_bf16 v[44:47], a[60:63], v[68:71], v[44:47]\n\t"
        "buffer_load_dwordx4 v100, %[sx], %[t0] offen lds\n\t"
        "v_mfma_f32_16x16x32_bf16 v[48:51], a[64:67], v[68:71], v[48:51]\n\t"
        "ds_read_b128 v[64:67], v106 offset:32768\n\t"
        "v_mfma_f32_16x16x32_bf16 v[52:55], v[68:71], a[68:71], v[52:55]\n\t"
        "v_mfma_f32_16x16x32_bf16 v[32:35], a[72:75], v[72:75], v[32:35]\n\t"
        "v_mfma_f32_16x16x32_bf16 v[36:39], a[76:79], v[72:75], v[36:39]\n\t"
        "ds_read_b128 v[68:71], v106 offset:34816\n\t"
        "v_mfma_f32_16x16x32_bf16 v[40:43], v[72:75], a[80:83], v[40:43]\n\t"
        "s_add_u32 m0, %[ld], 1024\n\t"
        "s_add_u32 %[t0], %[xo], 16384\n\t"
        "v_mfma_f32_16x16x32_bf16 v[44:47], a[72:75], v[76:79], v[44:47]\n\t"
        "buffer_load_dwordx4 v100, %[sx], %[t0] offen lds\n\t"
        "v_mfma_f32_16x16x32_bf16 v[48:51], a[76:79], v[76:79], v[48:51]\n\t"
        "ds_read_b128 v[72:75], v105 offset:40960\n\t"
        "v_mfma_f32_16x16x32_bf16 v[52:55], v[76:79], a[80:83], v[52:55]\n\t"
        "v_mfma_f32_16x16x32_bf16 v[32:35], a[84:87], v[80:83], v[32:35]\n\t"
        "v_mfma_f32_16x16x32_bf16 v[36:39], a[88:91], v[80:83], v[36:39]\n\t"
        "ds_read_b128 v[76:79], v105 offset:43008\n\t"
        "v_mfma_f32_16x16x32_bf16 v[40:43], v[80:83], a[92:95], v[40:43]\n\t"
        "s_add_u32 m0, %[ld], 8192\n\t"
        "s_add_u32 %[t0], %[xo], 128\n\t"
        "v_mfma_f32_16x16x32_bf16 v[44:47], a[84:87], v[84:87], v[44:47]\n\t"
        "buffer_load_dwordx4 v100, %[sx], %[t0] offen lds\n\t"
        "v_mfma_f32_16x16x32_bf16 v[48:51], a[88:91], v[84:87], v[48:51]\n\t"
        "ds_read_b128 v[80:83], v106 offset:40960\n\t"
        "v_mfma_f32_16x16x32_bf16 v[52:55], v[84:87], a[92:95], v[52:55]\n\t"
        "s_waitcnt lgkmcnt(6)\n\t"
        "v_mfma_f32_16x16x32_bf16 v[32:35], a[96:99], v[56:59], v[32:35]\n\t"
        "v_mfma_f32_16x16x32_bf16 v[36:39], a[100:103], v[56:59], v[36:39]\n\t"
        "ds_read_b128 v[84:87], v106 offset:43008\n\t"
        "v_mfma_f32_16x16x32_bf16 v[40:43], v[56:59], a[104:107], v[40:43]\n\t"
        "s_add_u32 m0, %[ld], 9216\n\t"
        "s_add_u32 %[t0], %[xo], 16512\n\t"
        "s_waitcnt lgkmcnt(6)\n\t"
        "v_mfma_f32_16x16x32_bf16 v[44:47], a[96:99], v[60:63], v[44:47]\n\t"
        "buffer_load_dwordx4 v100, %[sx], %[t0] offen lds\n\t"
        "v_mfma_f32_16x16x32_bf16 v[48:51], a[100:103], v[60:63], v[48:51]\n\t"
        "ds_read_b128 v[56:59], v105 offset:49152\n\t"
        "v_mfma_f32_16x16x32_bf16 v[52:55], v[60:63], a[104:107], v[52:55]\n\t"
        "s_waitcnt lgkmcnt(6)\n\t"
        "v_mfma_f32_16x16x32_bf16 v[32:35], a[108:111], v[64:67], v[32:35]\n\t"
        "v_mfma_f32_16x16x32_bf16 v[36:39], a[112:115], v[64:67], v[36:39]\n\t"
        "ds_read_b128 v[60:63], v105 offset:51200\n\t"
        "v_mfma_f32_16x16x32_bf16 v[40:43], v[64:67], a[116:119], v[40:43]\n\t"
        "s_add_u32 m0, %[ld], 16384\n\t"
        "s_add_u32 %[t0], %[xo], 256\n\t"
        "s_waitcnt lgkmcnt(6)\n\t"
        "v_mfma_f32_16x16x32_bf16 v[44:47], a[108:111], v[68:71], v[44:47]\n\t"
        "buffer_load_dwordx4 v100, %[sx], %[t0] offen lds\n\t"
        "v_mfma_f32_16x16x32_bf16 v[48:51], a[112:115], v[68:71], v[48:51]\n\t"
        "ds_read_b128 v[64:67], v106 offset:49152\n\t"
        "v_mfma_f32_16x16x32_bf16 v[52:55], v[68:71], a[116:119], v[52:55]\n\t"
        "s_waitcnt lgkmcnt(6)\n\t"
        "v_mfma_f32_16x16x32_bf16 v[32:35], a[120:123], v[72:75], v[32:35]\n\t"
        "v_mfma_f32_16x16x32_bf16 v[36:39], a[124:127], v[72:75], v[36:39]\n\t"
        "ds_read_b128 v[68:71], v106 offset:51200\n\t"
        "v_mfma_f32_16x16x32_bf16 v[40:43], v[72:75], a[128:131], v[40:43]\n\t"
        "s_add_u32 m0, %[ld], 17408\n\t"
        "s_add_u32 %[t0], %[xo], 16640\n\t"
        "s_waitcnt lgkmcnt(6)\n\t"
        "v_mfma_f32_16x16x32_bf16 v[44:47], a[120:123], v[76:79], v[44:47]\n\t"
        "buffer_load_dwordx4 v100, %[sx], %[t0] offen lds\n\t"
        "v_mfma_f32_16x16x32_bf16 v[48:51], a[124:127], v[76:79], v[48:51]\n\t"
        "ds_read_b128 v[72:75], v105 offset:57344\n\t"
        "v_mfma_f32_16x16x32_bf16 v[52:55], v[76:79], a[128:131], v[52:55]\n\t"
        "s_waitcnt lgkmcnt(6)\n\t"
        "v_mfma_f32_16x16x32_bf16 v[32:35], a[132:135], v[80:83], v[32:35]\n\t"
        "v_mfma_f32_16x16x32_bf16 v[36:39], a[136:139], v[80:83], v[36:39]\n\t"
        "ds_read_b128 v[76:79], v105 offset:59392\n\t"
        "v_mfma_f32_16x16x32_bf16 v[40:43], v[80:83], a[140:143], v[40:43]\n\t"
        "s_add_u32 m0, %[ld], 24576\n\t"
        "s_add_u32 %[t0], %[xo], 384\n\t"
        "s_waitcnt lgkmcnt(6)\n\t"
        "v_mfma_f32_16x16x32_bf16 v[44:47], a[132:135], v[84:87], v[44:47]\n\t"
        "buffer_load_dwordx4 v100, %[sx], %[t0] offen lds\n\t"
        "v_mfma_f32_16x16x32_bf16 v[48:51], a[136:139], v[84:87], v[48:51]\n\t"
        "ds_read_b128 v[80:83], v106 offset:57344\n\t"
        "v_mfma_f32_16x16x32_bf16 v[52:55], v[84:87], a[140:143], v[52:55]\n\t"
        "s_waitcnt lgkmcnt(6)\n\t"
        "v_mfma_f32_16x16x32_bf16 v[32:35], a[144:147], v[56:59], v[32:35]\n\t"
        "v_mfma_f32_16x16x32_bf16 v[36:39], a[148:151], v[56:59], v[36:39]\n\t"
        "ds_read_b128 v[84:87], v106 offset:59392\n\t"
        "v_mfma_f32_16x16x32_bf16 v[40:43], v[56:59], a[152:155], v[40:43]\n\t"
        "s_add_u32 m0, %[ld], 25600\n\t"
        "s_add_u32 %[t0], %[xo], 16768\n\t"
        "s_waitcnt lgkmcnt(6)\n\t"
        "v_mfma_f32_16x16x32_bf16 v[44:47], a[144:147], v[60:63], v[44:47]\n\t"
        "buffer_load_dwordx4 v100, %[sx], %[t0] offen lds\n\t"
        "v_mfma_f32_16x16x32_bf16 v[48:51], a[148:151], v[60:63], v[48:51]\n\t"
        "v_mfma_f32_16x16x32_bf16 v[52:55], v[60:63], a[152:155], v[52:55]\n\t"
        "s_waitcnt lgkmcnt(5)\n\t"
        "v_mfma_f32_16x16x32_bf16 v[32:35], a[156:159], v[64:67], v[32:35]\n\t"
        "s_waitcnt vmcnt(9) lgkmcnt(0)\n\t"
        "s_barrier\n\t"
        "v_mfma_f32_16x16x32_bf16 v[36:39], a[160:163], v[64:67], v[36:39]\n\t"
        "ds_read_b128 v[56:59], v107 offset:0\n\t"
        "v_mfma_f32_16x16x32_bf16 v[40:43], v[64:67], a[164:167], v[40:43]\n\t"
        "ds_read_b128 v[60:63], v107 offset:2048\n\t"
        "s_add_u32 m0, %[ld], 32768\n\t"
        "s_add_u32 %[t0], %[xo], 512\n\t"
        "v_mfma_f32_16x16x32_bf16 v[44:47], a[156:159], v[68:71], v[44:47]\n\t"
        "buffer_load_dwordx4 v100, %[sx], %[t0] offen lds\n\t"
        "v_mfma_f32_16x16x32_bf16 v[48:51], a[160:163], v[68:71], v[48:51]\n\t"
        "ds_read_b128 v[64:67], v108 offset:0\n\t"
        "v_mfma_f32_16x16x32_bf16 v[52:55], v[68:71], a[164:167], v[52:55]\n\t"
        "v_mfma_f32_16x16x32_bf16 v[32:35], a[168:171], v[72:75], v[32:35]\n\t"
        "v_mfma_f32_16x16x32_bf16 v[36:39], a[172:175], v[72:75], v[36:39]\n\t"
        "ds_read_b128 v[68:71], v108 offset:2048\n\t"
        "v_mov_b32_e32 v117, %[cn]\n\t"
        "buffer_store_dword v117, v113, %[sw], 0 offen sc1\n\t"
        "s_add_u32 %[cn], %[cn], 1\n\t"
        "v_mfma_f32_16x16x32_bf16 v[40:43], v[72:75], a[176:179], v[40:43]\n\t"
        "s_add_u32 m0, %[ld], 33792\n\t"
        "s_add_u32 %[t0], %[xo], 16896\n\t"
        "v_mfma_f32_16x16x32_bf16 v[44:47], a[168:171], v[76:79], v[44:47]\n\t"
        "buffer_load_dwordx4 v100, %[sx], %[t0] offen lds\n\t"
        "v_mfma_f32_16x16x32_bf16 v[48:51], a[172:175], v[76:79], v[48:51]\n\t"
        "ds_read_b128 v[72:75], v107 offset:8192\n\t"
        "v_mfma_f32_16x16x32_bf16 v[52:55], v[76:79], a[176:179], v[52:55]\n\t"
        "v_mfma_f32_16x16x32_bf16 v[32:35], a[180:183], v[80:83], v[32:35]\n\t"
        "v_mfma_f32_16x16x32_bf16 v[36:39], a[184:187], v[80:83], v[36:39]\n\t"
        "ds_read_b128 v[76:79], v107 offset:10240\n\t"
        "v_mfma_f32_16x16x32_bf16 v[40:43], v[80:83], a[188:191], v[40:43]\n\t"
        "s_add_u32 m0, %[ld], 40960\n\t"
        "s_add_u32 %[t0], %[xo], 640\n\t"
        "v_mfma_f32_16x16x32_bf16 v[44:47], a[180:183], v[84:87], v[44:47]\n\t"
        "buffer_load_dwordx4 v100, %[sx], %[t0] offen lds\n\t"
        "v_mfma_f32_16x16x32_bf16 v[48:51], a[184:187], v[84:87], v[48:51]\n\t"
        "ds_read_b128 v[80:83], v108 offset:8192\n\t"
        "v_mfma_f32_16x16x32_bf16 v[52:55], v[84:87], a[188:191], v[52:55]\n\t"
        "s_waitcnt lgkmcnt(6)\n\t"
        "v_mfma_f32_16x16x32_bf16 v[32:35], a[192:195], v[56:59], v[32:35]\n\t"
        "v_mfma_f32_16x16x32_bf16 v[36:39], a[196:199], v[56:59], v[36:39]\n\t"
        "ds_read_b128 v[84:87], v108 offset:10240\n\t"
        "v_mfma_f32_16x16x32_bf16 v[40:43], v[56:59], a[200:203], v[40:43]\n\t"
        "s_add_u32 m0, %[ld], 41984\n\t"
        "s_add_u32 %[t0], %[xo], 17024\n\t"
        "s_waitcnt lgkmcnt(6)\n\t"
        "v_mfma_f32_16x16x32_bf16 v[44:47], a[192:195], v[60:63], v[44:47]\n\t"
        "buffer_load_dwordx4 v100, %[sx], %[t0] offen lds\n\t"
        "v_mfma_f32_16x16x32_bf16 v[48:51], a[196:199], v[60:63], v[48:51]\n\t"
        "ds_read_b128 v[56:59], v107 offset:16384\n\t"
        "v_mfma_f32_16x16x32_bf16 v[52:55], v[60:63], a[200:203], v[52:55]\n\t"
        "s_waitcnt lgkmcnt(6)\n\t"
        "v_mfma_f32_16x16x32_bf16 v[32:35], a[204:207], v[64:67], v[32:35]\n\t"
        "v_mfma_f32_16x16x32_bf16 v[36:39], a[208:211], v[64:67], v[36:39]\n\t"
        "ds_read_b128 v[60:63], v107 offset:18432\n\t"
        "v_mfma_f32_16x16x32_bf16 v[40:43], v[64:67], a[212:215], v[40:43]\n\t"
        "s_add_u32 m0, %[ld], 49152\n\t"
        "s_add_u32 %[t0], %[xo], 768\n\t"
        "s_waitcnt lgkmcnt(6)\n\t"
        "v_mfma_f32_16x16x32_bf16 v[44:47], a[204:207], v[68:71], v[44:47]\n\t"
        "buffer_load_dwordx4 v100, %[sx], %[t0] offen lds\n\t"
        "v_mfma_f32_16x16x32_bf16 v[48:51], a[208:211], v[68:71], v[48:51]\n\t"
        "ds_read_b128 v[64:67], v108 offset:16384\n\t"
        "v_mfma_f32_16x16x32_bf16 v[52:55], v[68:71], a[212:215], v[52:55]\n\t"
        "s_waitcnt lgkmcnt(6)\n\t"
        "v_mfma_f32_16x16x32_bf16 v[32:35], a[216:219], v[72:75], v[32:35]\n\t"
        "v_mfma_f32_16x16x32_bf16 v[36:39], a[220:223], v[72:75], v[36:39]\n\t"
        "ds_read_b128 v[68:71], v108 offset:18432\n\t"
        "v_mfma_f32_16x16x32_bf16 v[40:43], v[72:75], a[224:227], v[40:43]\n\t"
        "s_add_u32 m0, %[ld], 50176\n\t"
        "s_add_u32 %[t0], %[xo], 17152\n\t"
        "s_waitcnt lgkmcnt(6)\n\t"
        "v_mfma_f32_16x16x32_bf16 v[44:47], a[216:219], v[76:79], v[44:47]\n\t"
        "buffer_load_dwordx4 v100, %[sx], %[t0] offen lds\n\t"
        "v_mfma_f32_16x16x32_bf16 v[48:51], a[220:223], v[76:79], v[48:51]\n\t"
        "ds_read_b128 v[72:75], v107 offset:24576\n\t"
        "v_mfma_f32_16x16x32_bf16 v[52:55], v[76:79], a[224:227], v[52:55]\n\t"
        "s_waitcnt lgkmcnt(6)\n\t"
        "v_mfma_f32_16x16x32_bf16 v[32:35], a[228:231], v[80:83], v[32:35]\n\t"
        "v_mfma_f32_16x16x32_bf16 v[36:39], a[232:235], v[80:83], v[36:39]\n\t"
        "ds_read_b128 v[76:79], v107 offset:26624\n\t"
        "v_mfma_f32_16x16x32_bf16 v[40:43], v[80:83], a[236:239], v[40:43]\n\t"
        "s_add_u32 m0, %[ld], 57344\n\t"
        "s_add_u32 %[t0], %[xo], 896\n\t"
        "s_waitcnt lgkmcnt(6)\n\t"
        "v_mfma_f32_16x16x32_bf16 v[44:47], a[228:231], v[84:87], v[44:47]\n\t"
        "buffer_load_dwordx4 v100, %[sx], %[t0] offen lds\n\t"
        "v_mfma_f32_16x16x32_bf16 v[48:51], a[232:235], v[84:87], v[48:51]\n\t"
        "ds_read_b128 v[80:83], v108 offset:24576\n\t"
        "v_mfma_f32_16x16x32_bf16 v[52:55], v[84:87], a[236:239], v[52:55]\n\t"
        "s_waitcnt lgkmcnt(6)\n\t"
        "v_mfma_f32_16x16x32_bf16 v[32:35], a[240:243], v[56:59], v[32:35]\n\t"
        "v_mfma_f32_16x16x32_bf16 v[36:39], a[244:247], v[56:59], v[36:39]\n\t"
        "ds_read_b128 v[84:87], v108 offset:26624\n\t"
        "v_mfma_f32_16x16x32_bf16 v[40:43], v[56:59], a[248:251], v[40:43]\n\t"
        "s_add_u32 m0, %[ld], 58368\n\t"
        "s_add_u32 %[t0], %[xo], 17280\n\t"
        "s_waitcnt lgkmcnt(6)\n\t"
        "v_mfma_f32_16x16x32_bf16 v[44:47], a[240:243], v[60:63], v[44:47]\n\t"
        "buffer_load_dwordx4 v100, %[sx], %[t0] offen lds\n\t"
        "v_mfma_f32_16x16x32_bf16 v[48:51], a[244:247], v[60:63], v[48:51]\n\t"
        "v_mfma_f32_16x16x32_bf16 v[52:55], v[60:63], a[248:251], v[52:55]\n\t"
        "s_waitcnt lgkmcnt(5)\n\t"
        "v_mfma_f32_16x16x32_bf16 v[32:35], a[252:255], v[64:67], v[32:35]\n\t"
        "s_waitcnt vmcnt(17) lgkmcnt(0)\n\t"
        "s_barrier\n\t"
        "v_mfma_f32_16x16x32_bf16 v[36:39], v[128:131], v[64:67], v[36:39]\n\t"
        "ds_read_b128 v[56:59], v107 offset:32768\n\t"
        "v_mfma_f32_16x16x32_bf16 v[40:43], v[64:67], v[132:135], v[40:43]\n\t"
        "ds_read_b128 v[60:63], v107 offset:34816\n\t"
        "s_add_u32 m0, %[ld], 65536\n\t"
        "s_add_u32 %[t0], %[xo], 1024\n\t"
        "v_mfma_f32_16x16x32_bf16 v[44:47], a[252:255], v[68:71], v[44:47]\n\t"
        "buffer_load_dwordx4 v100, %[sx], %[t0] offen lds\n\t"
        "v_mfma_f32_16x16x32_bf16 v[48:51], v[128:131], v[68:71], v[48:51]\n\t"
        "ds_read_b128 v[64:67], v108 offset:32768\n\t"
        "v_mfma_f32_16x16x32_bf16 v[52:55], v[68:71], v[132:135], v[52:55]\n\t"
        "v_mfma_f32_16x16x32_bf16 v[32:35], v[136:139], v[72:75], v[32:35]\n\t"
        "v_mfma_f32_16x16x32_bf16 v[36:39], v[140:143], v[72:75], v[36:39]\n\t"
        "ds_read_b128 v[68:71], v108 offset:34816\n\t"
        "v_mfma_f32_16x16x32_bf16 v[40:43], v[72:75], v[144:147], v[40:43]\n\t"
        "s_add_u32 m0, %[ld], 66560\n\t"
        "s_add_u32 %[t0], %[xo], 17408\n\t"
        "v_mfma_f32_16x16x32_bf16 v[44:47], v[136:139], v[76:79], v[44:47]\n\t"
        "buffer_load_dwordx4 v100, %[sx], %[t0] offen lds\n\t"
        "v_mfma_f32_16x16x32_bf16 v[48:51], v[140:143], v[76:79], v[48:51]\n\t"
        "ds_read_b128 v[72:75], v107 offset:40960\n\t"
        "v_mfma_f32_16x16x32_bf16 v[52:55], v[76:79], v[144:147], v[52:55]\n\t"
        "v_mfma_f32_16x16x32_bf16 v[32:35], v[148:151], v[80:83], v[32:35]\n\t"
        "v_mfma_f32_16x16x32_bf16 v[36:39], v[152:155], v[80:83], v[36:39]\n\t"
        "ds_read_b128 v[76:79], v107 offset:43008\n\t"
        "v_mfma_f32_16x16x32_bf16 v[40:43], v[80:83], v[156:159], v[40:43]\n\t"
        "s_add_u32 m0, %[ld], 73728\n\t"
        "s_add_u32 %[t0], %[xo], 1152\n\t"
        "v_mfma_f32_16x16x32_bf16 v[44:47], v[148:151], v[84:87], v[44:47]\n\t"
        "buffer_load_dwordx4 v100, %[sx], %[t0] offen lds\n\t"
        "v_mfma_f32_16x16x32_bf16 v[48:51], v[152:155], v[84:87], v[48:51]\n\t"
        "ds_read_b128 v[80:83], v108 offset:40960\n\t"
        "v_mfma_f32_16x16x32_bf16 v[52:55], v[84:87], v[156:159], v[52:55]\n\t"
        "s_waitcnt lgkmcnt(6)\n\t"
        "v_mfma_f32_16x16x32_bf16 v[32:35], v[160:163], v[56:59], v[32:35]\n\t"
        "v_mfma_f32_16x16x32_bf16 v[36:39], v[164:167], v[56:59], v[36:39]\n\t"
        "ds_read_b128 v[84:87], v108 offset:43008\n\t"
        "v_mfma_f32_16x16x32_bf16 v[40:43], v[56:59], v[168:171], v[40:43]\n\t"
        "s_add_u32 m0, %[ld], 74752\n\t"
        "s_add_u32 %[t0], %[xo], 17536\n\t"
        "s_waitcnt lgkmcnt(6)\n\t"
        "v_mfma_f32_16x16x32_bf16 v[44:47], v[160:163], v[60:63], v[44:47]\n\t"
        "buffer_load_dwordx4 v100, %[sx], %[t0] offen lds\n\t"
        "v_mfma_f32_16x16x32_bf16 v[48:51], v[164:167], v[60:63], v[48:51]\n\t"
        "ds_read_b128 v[56:59], v107 offset:49152\n\t"
        "v_mfma_f32_16x16x32_bf16 v[52:55], v[60:63], v[168:171], v[52:55]\n\t"
        "s_waitcnt lgkmcnt(6)\n\t"
        "v_mfma_f32_16x16x32_bf16 v[32:35], v[172:175], v[64:67], v[32:35]\n\t"
        "v_mfma_f32_16x16x32_bf16 v[36:39], v[176:179], v[64:67], v[36:39]\n\t"
        "ds_read_b128 v[60:63], v107 offset:51200\n\t"
        "v_mfma_f32_16x16x32_bf16 v[40:43], v[64:67], v[180:183], v[40:43]\n\t"
        "s_add_u32 m0, %[ld], 81920\n\t"
        "s_add_u32 %[t0], %[xo], 1280\n\t"
        "s_waitcnt lgkmcnt(6)\n\t"
        "v_mfma_f32_16x16x32_bf16 v[44:47], v[172:175], v[68:71], v[44:47]\n\t"
        "buffer_load_dwordx4 v100, %[sx], %[t0] offen lds\n\t"
        "v_mfma_f32_16x16x32_bf16 v[48:51], v[176:179], v[68:71], v[48:51]\n\t"
        "ds_read_b128 v[64:67], v108 offset:49152\n\t"
        "v_mfma_f32_16x16x32_bf16 v[52:55], v[68:71], v[180:183], v[52:55]\n\t"
        "s_waitcnt lgkmcnt(6)\n\t"
        "v_mfma_f32_16x16x32_bf16 v[32:35], v[184:187], v[72:75], v[32:35]\n\t"
        "v_mfma_f32_16x16x32_bf16 v[36:39], v[188:191], v[72:75], v[36:39]\n\t"
        "ds_read_b128 v[68:71], v108 offset:51200\n\t"
        "v_mfma_f32_16x16x32_bf16 v[40:43], v[72:75], v[192:195], v[40:43]\n\t"
        "s_add_u32 m0, %[ld], 82944\n\t"
        "s_add_u32 %[t0], %[xo], 17664\n\t"
        "s_waitcnt lgkmcnt(6)\n\t"
        "v_mfma_f32_16x16x32_bf16 v[44:47], v[184:187], v[76:79], v[44:47]\n\t"
        "buffer_load_dwordx4 v100, %[sx], %[t0] offen lds\n\t"
        "v_mfma_f32_16x16x32_bf16 v[48:51], v[188:191], v[76:79], v[48:51]\n\t"
        "ds_read_b128 v[72:75], v107 offset:57344\n\t"
        "v_mfma_f32_16x16x32_bf16 v[52:55], v[76:79], v[192:195], v[52:55]\n\t"
        "s_waitcnt lgkmcnt(6)\n\t"
        "v_mfma_f32_16x16x32_bf16 v[32:35], v[196:199], v[80:83], v[32:35]\n\t"
        "v_mfma_f32_16x16x32_bf16 v[36:39], v[200:203], v[80:83], v[36:39]\n\t"
        "ds_read_b128 v[76:79], v107 offset:59392\n\t"
        "v_mfma_f32_16x16x32_bf16 v[40:43], v[80:83], v[204:207], v[40:43]\n\t"
        "s_add_u32 m0, %[ld], 90112\n\t"
        "s_add_u32 %[t0], %[xo], 1408\n\t"
        "s_waitcnt lgkmcnt(6)\n\t"
        "v_mfma_f32_16x16x32_bf16 v[44:47], v[196:199], v[84:87], v[44:47]\n\t"
        "buffer_load_dwordx4 v100, %[sx], %[t0] offen lds\n\t"
        "v_mfma_f32_16x16x32_bf16 v[48:51], v[200:203], v[84:87], v[48:51]\n\t"
        "ds_read_b128 v[80:83], v108 offset:57344\n\t"
        "v_mfma_f32_16x16x32_bf16 v[52:55], v[84:87], v[204:207], v[52:55]\n\t"
        "s_waitcnt lgkmcnt(6)\n\t"
        "v_mfma_f32_16x16x32_bf16 v[32:35], v[208:211], v[56:59], v[32:35]\n\t"
        "v_mfma_f32_16x16x32_bf16 v[36:39], v[212:215], v[56:59], v[36:39]\n\t"
        "ds_read_b128 v[84:87], v108 offset:59392\n\t"
        "v_mfma_f32_16x16x32_bf16 v[40:43], v[56:59], v[216:219], v[40:43]\n\t"
        "s_add_u32 m0, %[ld], 91136\n\t"
        "s_add_u32 %[t0], %[xo], 17792\n\t"
        "s_waitcnt lgkmcnt(6)\n\t"
        "v_mfma_f32_16x16x32_bf16 v[44:47], v[208:211], v[60:63], v[44:47]\n\t"
        "buffer_load_dwordx4 v100, %[sx], %[t0] offen lds\n\t"
        "v_mfma_f32_16x16x32_bf16 v[48:51], v[212:215], v[60:63], v[48:51]\n\t"
        "v_mfma_f32_16x16x32_bf16 v[52:55], v[60:63], v[216:219], v[52:55]\n\t"
        "s_waitcnt lgkmcnt(5)\n\t"
        "v_mfma_f32_16x16x32_bf16 v[32:35], v[220:223], v[64:67], v[32:35]\n\t"
        "s_waitcnt vmcnt(17) lgkmcnt(0)\n\t"
        "s_barrier\n\t"
        "v_mfma_f32_16x16x32_bf16 v[36:39], v[224:227], v[64:67], v[36:39]\n\t"
        "ds_read_b128 v[56:59], v105 offset:0\n\t"
        "v_mfma_f32_16x16x32_bf16 v[40:43], v[64:67], v[228:231], v[40:43]\n\t"
        "ds_read_b128 v[60:63], v105 offset:2048\n\t"
        "s_add_u32 m0, %[ld], 98304\n\t"
        "s_add_u32 %[t0], %[xo], 1536\n\t"
        "v_mfma_f32_16x16x32_bf16 v[44:47], v[220:223], v[68:71], v[44:47]\n\t"
        "buffer_load_dwordx4 v100, %[sx], %[t0] offen lds\n\t"
        "v_mfma_f32_16x16x32_bf16 v[48:51], v[224:227], v[68:71], v[48:51]\n\t"
        "ds_read_b128 v[64:67], v106 offset:0\n\t"
        "v_mfma_f32_16x16x32_bf16 v[52:55], v[68:71], v[228:231], v[52:55]\n\t"
        "v_mfma_f32_16x16x32_bf16 v[32:35], v[232:235], v[72:75], v[32:35]\n\t"
        "v_mfma_f32_16x16x32_bf16 v[36:39], v[236:239], v[72:75], v[36:39]\n\t"
        "ds_read_b128 v[68:71], v106 offset:2048\n\t"
        "v_mfma_f32_16x16x32_bf16 v[40:43], v[72:75], v[240:243], v[40:43]\n\t"
        "s_add_u32 m0, %[ld], 99328\n\t"
        "s_add_u32 %[t0], %[xo], 17920\n\t"
        "v_mfma_f32_16x16x32_bf16 v[44:47], v[232:235], v[76:79], v[44:47]\n\t"
        "buffer_load_dwordx4 v100, %[sx], %[t0] offen lds\n\t"
        "v_mfma_f32_16x16x32_bf16 v[48:51], v[236:239], v[76:79], v[48:51]\n\t"
        "ds_read_b128 v[72:75], v105 offset:8192\n\t"
        "v_mfma_f32_16x16x32_bf16 v[52:55], v[76:79], v[240:243], v[52:55]\n\t"
        "v_mfma_f32_16x16x32_bf16 v[32:35], v[244:247], v[80:83], v[32:35]\n\t"
        "v_mfma_f32_16x16x32_bf16 v[36:39], v[248:251], v[80:83], v[36:39]\n\t"
        "ds_read_b128 v[76:79], v105 offset:10240\n\t"
        "v_mfma_f32_16x16x32_bf16 v[40:43], v[80:83], v[252:255], v[40:43]\n\t"
        "s_add_u32 m0, %[ld], 106496\n\t"
        "s_add_u32 %[t0], %[xo], 1664\n\t"
        "v_mfma_f32_16x16x32_bf16 v[44:47], v[244:247], v[84:87], v[44:47]\n\t"
        "buffer_load_dwordx4 v100, %[sx], %[t0] offen lds\n\t"
        "v_mfma_f32_16x16x32_bf16 v[48:51], v[248:251], v[84:87], v[48:51]\n\t"
        "ds_read_b128 v[80:83], v106 offset:8192\n\t"
        "v_mfma_f32_16x16x32_bf16 v[52:55], v[84:87], v[252:255], v[52:55]\n\t"
        "s_cmp_lt_u32 %[cn], 32\n\t"
        "s_cbranch_scc1 LG_loop%=\n\t"
        "s_nop 7\n\t"
        "v_cvt_pk_bf16_f32 v88, v32, v33\n\t"
        "v_cvt_pk_bf16_f32 v89, v34, v35\n\t"
        "v_cvt_pk_bf16_f32 v90, v36, v37\n\t"
        "v_cvt_pk_bf16_f32 v91, v38, v39\n\t"
        "buffer_store_dwordx4 v[88:91], v109, %[sw], %[so] offen sc1\n\t"
        "v_cvt_pk_bf16_f32 v92, v44, v45\n\t"
        "v_cvt_pk_bf16_f32 v93, v46, v47\n\t"
        "v_cvt_pk_bf16_f32 v94, v48, v49\n\t"
        "v_cvt_pk_bf16_f32 v95, v50, v51\n\t"
        "buffer_store_dwordx4 v[92:95], v110, %[sw], %[so] offen sc1\n\t"
        "v_cvt_pk_bf16_f32 v96, v40, v41\n\t"
        "v_cvt_pk_bf16_f32 v97, v42, v43\n\t"
        "v_cvt_pk_bf16_f32 v98, v52, v53\n\t"
        "v_cvt_pk_bf16_f32 v99, v54, v55\n\t"
        "buffer_store_dwordx4 v[96:99], v111, %[sw], %[so] offen sc1\n\t"
        "s_waitcnt vmcnt(0) lgkmcnt(0)\n\t"
        "v_mov_b32_e32 v117, %[cn]\n\t"
        "buffer_store_dword v117, v113, %[sw], 0 offen sc1\n\t"
        "s_barrier\n\t"
        "s_branch LG_end%=\n\t"
        "LG_first%=:\n\t"
        "s_waitcnt vmcnt(0)\n\t"
        "v_mov_b32_e32 v117, %[cn]\n\t"
        "buffer_store_dword v117, v113, %[sw], 0 offen sc1\n\t"
        "s_branch LG_back%=\n\t"
        "LG_end%=:\n\t"
        ".Lk_end_G:\n\t"
        : [t0] "=&s"(t0), [xo] "=&s"(xo), [so] "=&s"(so), [cn] "=&s"(cn), [ts] "=&s"(ts), [ta] "=&s"(ta), [tb] "=&s"(tb), [tc] "=&s"(tc), [td] "=&s"(td)
        : [sx] "s"(sx), [sq] "s"(sq), [sv] "s"(sv), [sw] "s"(sw), [ld] "s"(lb + w * 2048),
          "{v100}"((16 * w + (l >> 3)) * 2048 + (((l & 7) ^ (l >> 3)) << 4)), "{v101}"((8 * w + (l >> 3)) * 8192 + h * 256 + (c & 1) * 128 + (l & 7) * 16),
          "{v112}"((16 * w + (l >> 2)) * 8192 + h * 256 + cv * 64 + (l & 3) * 16), "{v102}"(lb + 131072 + w * 1024 + ((l * 16) ^ ((l & 16) << 2))), "{v116}"(lb + 131072 + w * 1024 + ((l * 16) ^ (l & 32))),
          "{v103}"(lr + rr * 128 + ((tn ^ (n >> 3)) * 64) + (n & 3) * 16), "{v104}"(lr + 8192 + rr * 64 + ((tn ^ (g & 1)) * 32) + (n & 3) * 8), "{v105}"(lx), "{v106}"(lx ^ 64u), "{v107}"(lx + 65536), "{v108}"((lx ^ 64u) + 65536),
          "{v109}"(aq), "{v110}"(aq + 4096), "{v111}"(W_VF + (tm * 32 + h) * 8192 + (2 * cv + tn) * 1024 + l * 16), "{v113}"(l == 0 ? F_KVQ + h * 1024 + (c * 4 + w) * 64 : OOB), "{v[114:115]}"(tp)
        : "memory", "scc", "m0", "v8", "v9", "v10", "v11", "v12", "v13", "v14", "v15", "v16", "v17", "v18", "v19", "v20", "v21", "v22", "v23", "v24", "v25", "v26", "v27", "v28", "v29", "v30", "v31", "v32", "v33", "v34", "v35", "v36", "v37", "v38", "v39", "v40", "v41", "v42", "v43", "v44", "v45", "v46", "v47", "v48", "v49", "v50", "v51", "v52", "v53", "v54", "v55", "v56", "v57", "v58", "v59", "v60", "v61", "v62", "v63", "v64", "v65", "v66", "v67", "v68", "v69", "v70", "v71", "v72", "v73", "v74", "v75", "v76", "v77", "v78", "v79", "v80", "v81", "v82", "v83", "v84", "v85", "v86", "v87", "v88", "v89", "v90", "v91", "v92", "v93", "v94", "v95", "v96", "v97", "v98", "v99", "v117", "v118", "v119", "v120", "v121", "v122", "v123", "v124", "v125", "v126", "v127", "v128", "v129", "v130", "v131", "v132", "v133", "v134", "v135", "v136", "v137", "v138", "v139", "v140", "v141", "v142", "v143", "v144", "v145", "v146", "v147", "v148", "v149", "v150", "v151", "v152", "v153", "v154", "v155", "v156", "v157", "v158", "v159", "v160", "v161", "v162", "v163", "v164", "v165", "v166", "v167", "v168", "v169", "v170", "v171", "v172", "v173", "v174", "v175", "v176", "v177", "v178", "v179", "v180", "v181", "v182", "v183", "v184", "v185", "v186", "v187", "v188", "v189", "v190", "v191", "v192", "v193", "v194", "v195", "v196", "v197", "v198", "v199", "v200", "v201", "v202", "v203", "v204", "v205", "v206", "v207", "v208", "v209", "v210", "v211", "v212", "v213", "v214", "v215", "v216", "v217", "v218", "v219", "v220", "v221", "v222", "v223", "v224", "v225", "v226", "v227", "v228", "v229", "v230", "v231", "v232", "v233", "v234", "v235", "v236", "v237", "v238", "v239", "v240", "v241", "v242", "v243", "v244", "v245", "v246", "v247", "v248", "v249", "v250", "v251", "v252", "v253", "v254", "v255", "a0", "a1", "a2", "a3", "a4", "a5", "a6", "a7", "a8", "a9", "a10", "a11", "a12", "a13", "a14", "a15", "a16", "a17", "a18", "a19", "a20", "a21", "a22", "a23", "a24", "a25", "a26", "a27", "a28", "a29", "a30", "a31", "a32", "a33", "a34", "a35", "a36", "a37", "a38", "a39", "a40", "a41", "a42", "a43", "a44", "a45", "a46", "a47", "a48", "a49", "a50", "a51", "a52", "a53", "a54", "a55", "a56", "a57", "a58", "a59", "a60", "a61", "a62", "a63", "a64", "a65", "a66", "a67", "a68", "a69", "a70", "a71", "a72", "a73", "a74", "a75", "a76", "a77", "a78", "a79", "a80", "a81", "a82", "a83", "a84", "a85", "a86", "a87", "a88", "a89", "a90", "a91", "a92", "a93", "a94", "a95", "a96", "a97", "a98", "a99", "a100", "a101", "a102", "a103", "a104", "a105", "a106", "a107", "a108", "a109", "a110", "a111", "a112", "a113", "a114", "a115", "a116", "a117", "a118", "a119", "a120", "a121", "a122", "a123", "a124", "a125", "a126", "a127", "a128", "a129", "a130", "a131", "a132", "a133", "a134", "a135", "a136", "a137", "a138", "a139", "a140", "a141", "a142", "a143", "a144", "a145", "a146", "a147", "a148", "a149", "a150", "a151", "a152", "a153", "a154", "a155", "a156", "a157", "a158", "a159", "a160", "a161", "a162", "a163", "a164", "a165", "a166", "a167", "a168", "a169", "a170", "a171", "a172", "a173", "a174", "a175", "a176", "a177", "a178", "a179", "a180", "a181", "a182", "a183", "a184", "a185", "a186", "a187", "a188", "a189", "a190", "a191", "a192", "a193", "a194", "a195", "a196", "a197", "a198", "a199", "a200", "a201", "a202", "a203", "a204", "a205", "a206", "a207", "a208", "a209", "a210", "a211", "a212", "a213", "a214", "a215", "a216", "a217", "a218", "a219", "a220", "a221", "a222", "a223", "a224", "a225", "a226", "a227", "a228", "a229", "a230", "a231", "a232", "a233", "a234", "a235", "a236", "a237", "a238", "a239", "a240", "a241", "a242", "a243", "a244", "a245", "a246", "a247", "a248", "a249", "a250", "a251", "a252", "a253", "a254", "a255");
}

__device__ __forceinline__ void out_block(const Params& p, unsigned char* lds, const int wv, const int rp, const int nt, const int j) {
    const int w = wv, l = otid() & 63, n = l & 15, g = l >> 4;
    const unsigned lb = __builtin_amdgcn_readfirstlane((unsigned)(size_t)lds), stg = lb + 131072 + w * 8192;
    const v4u so = mk_srd(p.Wo, 1024 * 4096 * 2), sw = mk_srd(p.ws, W_TOTAL), su = mk_srd(p.out, S * D * 2);
    const unsigned lr = stg + (4 * g + (n >> 2)) * 128 + ((2 * (g & 1) + (n >> 3)) << 5) + (n & 3) * 8;
    unsigned t0, t1, tl = rp, pg, cu, nx, sp, re; unsigned long long tu;
    asm volatile(
        "s_add_u32 %[t1], %[ro], 0\n\t"
        "s_and_b32 %[t1], %[t1], 7\n\t"
        "s_lshl_b32 %[t1], %[t1], 18\n\t"
        "s_add_u32 %[t0], %[t1], 0\n\t"
        "buffer_load_dwordx4 a[0:3], v110, %[so], %[t0] offen\n\t"
        "s_sleep 2\n\t"
        "s_add_u32 %[t0], %[t1], 16384\n\t"
        "buffer_load_dwordx4 a[4:7], v110, %[so], %[t0] offen\n\t"
        "s_sleep 2\n\t"
        "s_add_u32 %[t0], %[t1], 32768\n\t"
        "buffer_load_dwordx4 a[8:11], v110, %[so], %[t0] offen\n\t"
        "s_sleep 2\n\t"
        "s_add_u32 %[t0], %[t1], 49152\n\t"
        "buffer_load_dwordx4 a[12:15], v110, %[so], %[t0] offen\n\t"
        "s_sleep 2\n\t"
        "s_add_u32 %[t0], %[t1], 65536\n\t"
        "buffer_load_dwordx4 a[16:19], v110, %[so], %[t0] offen\n\t"
        "s_sleep 2\n\t"
        "s_add_u32 %[t0], %[t1], 81920\n\t"
        "buffer_load_dwordx4 a[20:23], v110, %[so], %[t0] offen\n\t"
        "s_sleep 2\n\t"
        "s_add_u32 %[t0], %[t1], 98304\n\t"
        "buffer_load_dwordx4 v[24:27], v110, %[so], %[t0] offen\n\t"
        "s_sleep 2\n\t"
        "s_add_u32 %[t0], %[t1], 114688\n\t"
        "buffer_load_dwordx4 v[28:31], v110, %[so], %[t0] offen\n\t"
        "s_sleep 2\n\t"
        "s_add_u32 %[t1], %[ro], 0\n\t"
        "s_and_b32 %[t1], %[t1], 7\n\t"
        "s_lshl_b32 %[t1], %[t1], 18\n\t"
        "s_add_u32 %[t0], %[t1], 131072\n\t"
        "buffer_load_dwordx4 a[24:27], v110, %[so], %[t0] offen\n\t"
        "s_sleep 2\n\t"
        "s_add_u32 %[t0], %[t1], 147456\n\t"
        "buffer_load_dwordx4 a[28:31], v110, %[so], %[t0] offen\n\t"
        "s_sleep 2\n\t"
        "s_add_u32 %[t0], %[t1], 163840\n\t"
        "buffer_load_dwordx4 a[32:35], v110, %[so], %[t0] offen\n\t"
        "s_sleep 2\n\t"
        "s_add_u32 %[t0], %[t1], 180224\n\t"
        "buffer_load_dwordx4 a[36:39], v110, %[so], %[t0] offen\n\t"
        "s_sleep 2\n\t"
        "s_add_u32 %[t0], %[t1], 196608\n\t"
        "buffer_load_dwordx4 a[40:43], v110, %[so], %[t0] offen\n\t"
        "s_sleep 2\n\t"
        "s_add_u32 %[t0], %[t1], 212992\n\t"
        "buffer_load_dwordx4 a[44:47], v110, %[so], %[t0] offen\n\t"
        "s_sleep 2\n\t"
        "s_add_u32 %[t0], %[t1], 229376\n\t"
        "buffer_load_dwordx4 v[32:35], v110, %[so], %[t0] offen\n\t"
        "s_sleep 2\n\t"
        "s_add_u32 %[t0], %[t1], 245760\n\t"
        "buffer_load_dwordx4 v[36:39], v110, %[so], %[t0] offen\n\t"
        "s_sleep 2\n\t"
        "s_waitcnt vmcnt(8)\n\t"
        "ds_write_b128 v111, a[0:3] offset:0\n\t"
        "ds_write_b128 v111, a[4:7] offset:1024\n\t"
        "ds_write_b128 v111, a[8:11] offset:2048\n\t"
        "ds_write_b128 v111, a[12:15] offset:3072\n\t"
        "ds_write_b128 v111, a[16:19] offset:4096\n\t"
        "ds_write_b128 v111, a[20:23] offset:5120\n\t"
        "ds_write_b128 v111, v[24:27] offset:6144\n\t"
        "ds_write_b128 v111, v[28:31] offset:7168\n\t"
        "ds_read_b64_tr_b16 a[0:1], v112 offset:0\n\t"
        "ds_read_b64_tr_b16 a[2:3], v112 offset:2048\n\t"
        "ds_read_b64_tr_b16 a[12:13], v112 offset:4096\n\t"
        "ds_read_b64_tr_b16 a[14:15], v112 offset:6144\n\t"
        "s_waitcnt lgkmcnt(8)\n\t"
        "ds_read_b64_tr_b16 a[4:5], v113 offset:0\n\t"
        "ds_read_b64_tr_b16 a[6:7], v113 offset:2048\n\t"
        "ds_read_b64_tr_b16 a[16:17], v113 offset:4096\n\t"
        "ds_read_b64_tr_b16 a[18:19], v113 offset:6144\n\t"
        "s_waitcnt lgkmcnt(8)\n\t"
        "ds_read_b64_tr_b16 a[8:9], v114 offset:0\n\t"
        "ds_read_b64_tr_b16 a[10:11], v114 offset:2048\n\t"
        "ds_read_b64_tr_b16 a[20:21], v114 offset:4096\n\t"
        "ds_read_b64_tr_b16 a[22:23], v114 offset:6144\n\t"
        "s_waitcnt lgkmcnt(8)\n\t"
        "ds_read_b64_tr_b16 v[56:57], v115 offset:0\n\t"
        "ds_read_b64_tr_b16 v[58:59], v115 offset:2048\n\t"
        "ds_read_b64_tr_b16 v[60:61], v115 offset:4096\n\t"
        "ds_read_b64_tr_b16 v[62:63], v115 offset:6144\n\t"
        "s_add_u32 %[t1], %[ro], 1\n\t"
        "s_and_b32 %[t1], %[t1], 7\n\t"
        "s_lshl_b32 %[t1], %[t1], 18\n\t"
        "s_add_u32 %[t0], %[t1], 0\n\t"
        "buffer_load_dwordx4 a[48:51], v110, %[so], %[t0] offen\n\t"
        "s_sleep 2\n\t"
        "s_add_u32 %[t0], %[t1], 16384\n\t"
        "buffer_load_dwordx4 a[52:55], v110, %[so], %[t0] offen\n\t"
        "s_sleep 2\n\t"
        "s_add_u32 %[t0], %[t1], 32768\n\t"
        "buffer_load_dwordx4 a[56:59], v110, %[so], %[t0] offen\n\t"
        "s_sleep 2\n\t"
        "s_add_u32 %[t0], %[t1], 49152\n\t"
        "buffer_load_dwordx4 a[60:63], v110, %[so], %[t0] offen\n\t"
        "s_sleep 2\n\t"
        "s_add_u32 %[t0], %[t1], 65536\n\t"
        "buffer_load_dwordx4 a[64:67], v110, %[so], %[t0] offen\n\t"
        "s_sleep 2\n\t"
        "s_add_u32 %[t0], %[t1], 81920\n\t"
        "buffer_load_dwordx4 a[68:71], v110, %[so], %[t0] offen\n\t"
        "s_sleep 2\n\t"
        "s_add_u32 %[t0], %[t1], 98304\n\t"
        "buffer_load_dwordx4 v[40:43], v110, %[so], %[t0] offen\n\t"
        "s_sleep 2\n\t"
        "s_add_u32 %[t0], %[t1], 114688\n\t"
        "buffer_load_dwordx4 v[44:47], v110, %[so], %[t0] offen\n\t"
        "s_sleep 2\n\t"
        "s_waitcnt lgkmcnt(0)\n\t"
        "ds_write_b128 v116, v[56:59] offset:0\n\t"
        "ds_write_b128 v116, v[60:63] offset:1024\n\t"
        "s_waitcnt vmcnt(8)\n\t"
        "ds_write_b128 v111, a[24:27] offset:0\n\t"
        "ds_write_b128 v111, a[28:31] offset:1024\n\t"
        "ds_write_b128 v111, a[32:35] offset:2048\n\t"
        "ds_write_b128 v111, a[36:39] offset:3072\n\t"
        "ds_write_b128 v111, a[40:43] offset:4096\n\t"
        "ds_write_b128 v111, a[44:47] offset:5120\n\t"
        "ds_write_b128 v111, v[32:35] offset:6144\n\t"
        "ds_write_b128 v111, v[36:39] offset:7168\n\t"
        "ds_read_b64_tr_b16 a[24:25], v112 offset:0\n\t"
        "ds_read_b64_tr_b16 a[26:27], v112 offset:2048\n\t"
        "ds_read_b64_tr_b16 a[36:37], v112 offset:4096\n\t"
        "ds_read_b64_tr_b16 a[38:39], v112 offset:6144\n\t"
        "s_waitcnt lgkmcnt(8)\n\t"
        "ds_read_b64_tr_b16 a[28:29], v113 offset:0\n\t"
        "ds_read_b64_tr_b16 a[30:31], v113 offset:2048\n\t"
        "ds_read_b64_tr_b16 a[40:41], v113 offset:4096\n\t"
        "ds_read_b64_tr_b16 a[42:43], v113 offset:6144\n\t"
        "s_waitcnt lgkmcnt(8)\n\t"
        "ds_read_b64_tr_b16 a[32:33], v114 offset:0\n\t"
        "ds_read_b64_tr_b16 a[34:35], v114 offset:2048\n\t"
        "ds_read_b64_tr_b16 a[44:45], v114 offset:4096\n\t"
        "ds_read_b64_tr_b16 a[46:47], v114 offset:6144\n\t"
        "s_waitcnt lgkmcnt(8)\n\t"
        "ds_read_b64_tr_b16 v[56:57], v115 offset:0\n\t"
        "ds_read_b64_tr_b16 v[58:59], v115 offset:2048\n\t"
        "ds_read_b64_tr_b16 v[60:61], v115 offset:4096\n\t"
        "ds_read_b64_tr_b16 v[62:63], v115 offset:6144\n\t"
        "s_add_u32 %[t1], %[ro], 1\n\t"
        "s_and_b32 %[t1], %[t1], 7\n\t"
        "s_lshl_b32 %[t1], %[t1], 18\n\t"
        "s_add_u32 %[t0], %[t1], 131072\n\t"
        "buffer_load_dwordx4 a[72:75], v110, %[so], %[t0] offen\n\t"
        "s_sleep 2\n\t"
        "s_add_u32 %[t0], %[t1], 147456\n\t"
        "buffer_load_dwordx4 a[76:79], v110, %[so], %[t0] offen\n\t"
        "s_sleep 2\n\t"
        "s_add_u32 %[t0], %[t1], 163840\n\t"
        "buffer_load_dwordx4 a[80:83], v110, %[so], %[t0] offen\n\t"
        "s_sleep 2\n\t"
        "s_add_u32 %[t0], %[t1], 180224\n\t"
        "buffer_load_dwordx4 a[84:87], v110, %[so], %[t0] offen\n\t"
        "s_sleep 2\n\t"
        "s_add_u32 %[t0], %[t1], 196608\n\t"
        "buffer_load_dwordx4 a[88:91], v110, %[so], %[t0] offen\n\t"
        "s_sleep 2\n\t"
        "s_add_u32 %[t0], %[t1], 212992\n\t"
        "buffer_load_dwordx4 a[92:95], v110, %[so], %[t0] offen\n\t"
        "s_sleep 2\n\t"
        "s_add_u32 %[t0], %[t1], 229376\n\t"
        "buffer_load_dwordx4 v[48:51], v110, %[so], %[t0] offen\n\t"
        "s_sleep 2\n\t"
        "s_add_u32 %[t0], %[t1], 245760\n\t"
        "buffer_load_dwordx4 v[52:55], v110, %[so], %[t0] offen\n\t"
        "s_sleep 2\n\t"
        "s_waitcnt lgkmcnt(0)\n\t"
        "ds_write_b128 v116, v[56:59] offset:2048\n\t"
        "ds_write_b128 v116, v[60:63] offset:3072\n\t"
        "s_waitcnt vmcnt(8)\n\t"
        "ds_write_b128 v111, a[48:51] offset:0\n\t"
        "ds_write_b128 v111, a[52:55] offset:1024\n\t"
        "ds_write_b128 v111, a[56:59] offset:2048\n\t"
        "ds_write_b128 v111, a[60:63] offset:3072\n\t"
        "ds_write_b128 v111, a[64:67] offset:4096\n\t"
        "ds_write_b128 v111, a[68:71] offset:5120\n\t"
        "ds_write_b128 v111, v[40:43] offset:6144\n\t"
        "ds_write_b128 v111, v[44:47] offset:7168\n\t"
        "ds_read_b64_tr_b16 a[48:49], v112 offset:0\n\t"
        "ds_read_b64_tr_b16 a[50:51], v112 offset:2048\n\t"
        "ds_read_b64_tr_b16 a[60:61], v112 offset:4096\n\t"
        "ds_read_b64_tr_b16 a[62:63], v112 offset:6144\n\t"
        "s_waitcnt lgkmcnt(8)\n\t"
        "ds_read_b64_tr_b16 a[52:53], v113 offset:0\n\t"
        "ds_read_b64_tr_b16 a[54:55], v113 offset:2048\n\t"
        "ds_read_b64_tr_b16 a[64:65], v113 offset:4096\n\t"
        "ds_read_b64_tr_b16 a[66:67], v113 offset:6144\n\t"
        "s_waitcnt lgkmcnt(8)\n\t"
        "ds_read_b64_tr_b16 a[56:57], v114 offset:0\n\t"
        "ds_read_b64_tr_b16 a[58:59], v114 offset:2048\n\t"
        "ds_read_b64_tr_b16 a[68:69], v114 offset:4096\n\t"
        "ds_read_b64_tr_b16 a[70:71], v114 offset:6144\n\t"
        "s_waitcnt lgkmcnt(8)\n\t"
        "ds_read_b64_tr_b16 v[56:57], v115 offset:0\n\t"
        "ds_read_b64_tr_b16 v[58:59], v115 offset:2048\n\t"
        "ds_read_b64_tr_b16 v[60:61], v115 offset:4096\n\t"
        "ds_read_b64_tr_b16 v[62:63], v115 offset:6144\n\t"
        "s_add_u32 %[t1], %[ro], 2\n\t"
        "s_and_b32 %[t1], %[t1], 7\n\t"
        "s_lshl_b32 %[t1], %[t1], 18\n\t"
        "s_add_u32 %[t0], %[t1], 0\n\t"
        "buffer_load_dwordx4 a[96:99], v110, %[so], %[t0] offen\n\t"
        "s_sleep 2\n\t"
        "s_add_u32 %[t0], %[t1], 16384\n\t"
        "buffer_load_dwordx4 a[100:103], v110, %[so], %[t0] offen\n\t"
        "s_sleep 2\n\t"
        "s_add_u32 %[t0], %[t1], 32768\n\t"
        "buffer_load_dwordx4 a[104:107], v110, %[so], %[t0] offen\n\t"
        "s_sleep 2\n\t"
        "s_add_u32 %[t0], %[t1], 49152\n\t"
        "buffer_load_dwordx4 a[108:111], v110, %[so], %[t0] offen\n\t"
        "s_sleep 2\n\t"
        "s_add_u32 %[t0], %[t1], 65536\n\t"
        "buffer_load_dwordx4 a[112:115], v110, %[so], %[t0] offen\n\t"
        "s_sleep 2\n\t"
        "s_add_u32 %[t0], %[t1], 81920\n\t"
        "buffer_load_dwordx4 a[116:119], v110, %[so], %[t0] offen\n\t"
        "s_sleep 2\n\t"
        "s_add_u32 %[t0], %[t1], 98304\n\t"
        "buffer_load_dwordx4 v[24:27], v110, %[so], %[t0] offen\n\t"
        "s_sleep 2\n\t"
        "s_add_u32 %[t0], %[t1], 114688\n\t"
        "buffer_load_dwordx4 v[28:31], v110, %[so], %[t0] offen\n\t"
        "s_sleep 2\n\t"
        "s_waitcnt lgkmcnt(0)\n\t"
        "ds_write_b128 v116, v[56:59] offset:4096\n\t"
        "ds_write_b128 v116, v[60:63] offset:5120\n\t"
        "s_waitcnt vmcnt(8)\n\t"
        "ds_write_b128 v111, a[72:75] offset:0\n\t"
        "ds_write_b128 v111, a[76:79] offset:1024\n\t"
        "ds_write_b128 v111, a[80:83] offset:2048\n\t"
        "ds_write_b128 v111, a[84:87] offset:3072\n\t"
        "ds_write_b128 v111, a[88:91] offset:4096\n\t"
        "ds_write_b128 v111, a[92:95] offset:5120\n\t"
        "ds_write_b128 v111, v[48:51] offset:6144\n\t"
        "ds_write_b128 v111, v[52:55] offset:7168\n\t"
        "ds_read_b64_tr_b16 a[72:73], v112 offset:0\n\t"
        "ds_read_b64_tr_b16 a[74:75], v112 offset:2048\n\t"
        "ds_read_b64_tr_b16 a[84:85], v112 offset:4096\n\t"
        "ds_read_b64_tr_b16 a[86:87], v112 offset:6144\n\t"
        "s_waitcnt lgkmcnt(8)\n\t"
        "ds_read_b64_tr_b16 a[76:77], v113 offset:0\n\t"
        "ds_read_b64_tr_b16 a[78:79], v113 offset:2048\n\t"
        "ds_read_b64_tr_b16 a[88:89], v113 offset:4096\n\t"
        "ds_read_b64_tr_b16 a[90:91], v113 offset:6144\n\t"
        "s_waitcnt lgkmcnt(8)\n\t"
        "ds_read_b64_tr_b16 a[80:81], v114 offset:0\n\t"
        "ds_read_b64_tr_b16 a[82:83], v114 offset:2048\n\t"
        "ds_read_b64_tr_b16 a[92:93], v114 offset:4096\n\t"
        "ds_read_b64_tr_b16 a[94:95], v114 offset:6144\n\t"
        "s_waitcnt lgkmcnt(8)\n\t"
        "ds_read_b64_tr_b16 v[56:57], v115 offset:0\n\t"
        "ds_read_b64_tr_b16 v[58:59], v115 offset:2048\n\t"
        "ds_read_b64_tr_b16 v[60:61], v115 offset:4096\n\t"
        "ds_read_b64_tr_b16 v[62:63], v115 offset:6144\n\t"
        "s_add_u32 %[t1], %[ro], 2\n\t"
        "s_and_b32 %[t1], %[t1], 7\n\t"
        "s_lshl_b32 %[t1], %[t1], 18\n\t"
        "s_add_u32 %[t0], %[t1], 131072\n\t"
        "buffer_load_dwordx4 a[120:123], v110, %[so], %[t0] offen\n\t"
        "s_sleep 2\n\t"
        "s_add_u32 %[t0], %[t1], 147456\n\t"
        "buffer_load_dwordx4 a[124:127], v110, %[so], %[t0] offen\n\t"
        "s_sleep 2\n\t"
        "s_add_u32 %[t0], %[t1], 163840\n\t"
        "buffer_load_dwordx4 a[128:131], v110, %[so], %[t0] offen\n\t"
        "s_sleep 2\n\t"
        "s_add_u32 %[t0], %[t1], 180224\n\t"
        "buffer_load_dwordx4 a[132:135], v110, %[so], %[t0] offen\n\t"
        "s_sleep 2\n\t"
        "s_add_u32 %[t0], %[t1], 196608\n\t"
        "buffer_load_dwordx4 a[136:139], v110, %[so], %[t0] offen\n\t"
        "s_sleep 2\n\t"
        "s_add_u32 %[t0], %[t1], 212992\n\t"
        "buffer_load_dwordx4 a[140:143], v110, %[so], %[t0] offen\n\t"
        "s_sleep 2\n\t"
        "s_add_u32 %[t0], %[t1], 229376\n\t"
        "buffer_load_dwordx4 v[32:35], v110, %[so], %[t0] offen\n\t"
        "s_sleep 2\n\t"
        "s_add_u32 %[t0], %[t1], 245760\n\t"
        "buffer_load_dwordx4 v[36:39], v110, %[so], %[t0] offen\n\t"
        "s_sleep 2\n\t"
        "s_waitcnt lgkmcnt(0)\n\t"
        "ds_write_b128 v116, v[56:59] offset:6144\n\t"
        "ds_write_b128 v116, v[60:63] offset:7168\n\t"
        "s_waitcnt vmcnt(8)\n\t"
        "ds_write_b128 v111, a[96:99] offset:0\n\t"
        "ds_write_b128 v111, a[100:103] offset:1024\n\t"
        "ds_write_b128 v111, a[104:107] offset:2048\n\t"
        "ds_write_b128 v111, a[108:111] offset:3072\n\t"
        "ds_write_b128 v111, a[112:115] offset:4096\n\t"
        "ds_write_b128 v111, a[116:119] offset:5120\n\t"
        "ds_write_b128 v111, v[24:27] offset:6144\n\t"
        "ds_write_b128 v111, v[28:31] offset:7168\n\t"
        "ds_read_b64_tr_b16 a[96:97], v112 offset:0\n\t"
        "ds_read_b64_tr_b16 a[98:99], v112 offset:2048\n\t"
        "ds_read_b64_tr_b16 a[108:109], v112 offset:4096\n\t"
        "ds_read_b64_tr_b16 a[110:111], v112 offset:6144\n\t"
        "s_waitcnt lgkmcnt(8)\n\t"
        "ds_read_b64_tr_b16 a[100:101], v113 offset:0\n\t"
        "ds_read_b64_tr_b16 a[102:103], v113 offset:2048\n\t"
        "ds_read_b64_tr_b16 a[112:113], v113 offset:4096\n\t"
        "ds_read_b64_tr_b16 a[114:115], v113 offset:6144\n\t"
        "s_waitcnt lgkmcnt(8)\n\t"
        "ds_read_b64_tr_b16 a[104:105], v114 offset:0\n\t"
        "ds_read_b64_tr_b16 a[106:107], v114 offset:2048\n\t"
        "ds_read_b64_tr_b16 a[116:117], v114 offset:4096\n\t"
        "ds_read_b64_tr_b16 a[118:119], v114 offset:6144\n\t"
        "s_waitcnt lgkmcnt(8)\n\t"
        "ds_read_b64_tr_b16 v[56:57], v115 offset:0\n\t"
        "ds_read_b64_tr_b16 v[58:59], v115 offset:2048\n\t"
        "ds_read_b64_tr_b16 v[60:61], v115 offset:4096\n\t"
        "ds_read_b64_tr_b16 v[62:63], v115 offset:6144\n\t"
        "s_add_u32 %[t1], %[ro], 3\n\t"
        "s_and_b32 %[t1], %[t1], 7\n\t"
        "s_lshl_b32 %[t1], %[t1], 18\n\t"
        "s_add_u32 %[t0], %[t1], 0\n\t"
        "buffer_load_dwordx4 a[144:147], v110, %[so], %[t0] offen\n\t"
        "s_sleep 2\n\t"
        "s_add_u32 %[t0], %[t1], 16384\n\t"
        "buffer_load_dwordx4 a[148:151], v110, %[so], %[t0] offen\n\t"
        "s_sleep 2\n\t"
        "s_add_u32 %[t0], %[t1], 32768\n\t"
        "buffer_load_dwordx4 a[152:155], v110, %[so], %[t0] offen\n\t"
        "s_sleep 2\n\t"
        "s_add_u32 %[t0], %[t1], 49152\n\t"
        "buffer_load_dwordx4 a[156:159], v110, %[so], %[t0] offen\n\t"
        "s_sleep 2\n\t"
        "s_add_u32 %[t0], %[t1], 65536\n\t"
        "buffer_load_dwordx4 a[160:163], v110, %[so], %[t0] offen\n\t"
        "s_sleep 2\n\t"
        "s_add_u32 %[t0], %[t1], 81920\n\t"
        "buffer_load_dwordx4 a[164:167], v110, %[so], %[t0] offen\n\t"
        "s_sleep 2\n\t"
        "s_add_u32 %[t0], %[t1], 98304\n\t"
        "buffer_load_dwordx4 v[40:43], v110, %[so], %[t0] offen\n\t"
        "s_sleep 2\n\t"
        "s_add_u32 %[t0], %[t1], 114688\n\t"
        "buffer_load_dwordx4 v[44:47], v110, %[so], %[t0] offen\n\t"
        "s_sleep 2\n\t"
        "s_waitcnt lgkmcnt(0)\n\t"
        "ds_write_b128 v116, v[56:59] offset:8192\n\t"
        "ds_write_b128 v116, v[60:63] offset:9216\n\t"
        "s_waitcnt vmcnt(8)\n\t"
        "ds_write_b128 v111, a[120:123] offset:0\n\t"
        "ds_write_b128 v111, a[124:127] offset:1024\n\t"
        "ds_write_b128 v111, a[128:131] offset:2048\n\t"
        "ds_write_b128 v111, a[132:135] offset:3072\n\t"
        "ds_write_b128 v111, a[136:139] offset:4096\n\t"
        "ds_write_b128 v111, a[140:143] offset:5120\n\t"
        "ds_write_b128 v111, v[32:35] offset:6144\n\t"
        "ds_write_b128 v111, v[36:39] offset:7168\n\t"
        "ds_read_b64_tr_b16 a[120:121], v112 offset:0\n\t"
        "ds_read_b64_tr_b16 a[122:123], v112 offset:2048\n\t"
        "ds_read_b64_tr_b16 a[132:133], v112 offset:4096\n\t"
        "ds_read_b64_tr_b16 a[134:135], v112 offset:6144\n\t"
        "s_waitcnt lgkmcnt(8)\n\t"
        "ds_read_b64_tr_b16 a[124:125], v113 offset:0\n\t"
        "ds_read_b64_tr_b16 a[126:127], v113 offset:2048\n\t"
        "ds_read_b64_tr_b16 a[136:137], v113 offset:4096\n\t"
        "ds_read_b64_tr_b16 a[138:139], v113 offset:6144\n\t"
        "s_waitcnt lgkmcnt(8)\n\t"
        "ds_read_b64_tr_b16 a[128:129], v114 offset:0\n\t"
        "ds_read_b64_tr_b16 a[130:131], v114 offset:2048\n\t"
        "ds_read_b64_tr_b16 a[140:141], v114 offset:4096\n\t"
        "ds_read_b64_tr_b16 a[142:143], v114 offset:6144\n\t"
        "s_waitcnt lgkmcnt(8)\n\t"
        "ds_read_b64_tr_b16 v[56:57], v115 offset:0\n\t"
        "ds_read_b64_tr_b16 v[58:59], v115 offset:2048\n\t"
        "ds_read_b64_tr_b16 v[60:61], v115 offset:4096\n\t"
        "ds_read_b64_tr_b16 v[62:63], v115 offset:6144\n\t"
        "s_add_u32 %[t1], %[ro], 3\n\t"
        "s_and_b32 %[t1], %[t1], 7\n\t"
        "s_lshl_b32 %[t1], %[t1], 18\n\t"
        "s_add_u32 %[t0], %[t1], 131072\n\t"
        "buffer_load_dwordx4 a[168:171], v110, %[so], %[t0] offen\n\t"
        "s_sleep 2\n\t"
        "s_add_u32 %[t0], %[t1], 147456\n\t"
        "buffer_load_dwordx4 a[172:175], v110, %[so], %[t0] offen\n\t"
        "s_sleep 2\n\t"
        "s_add_u32 %[t0], %[t1], 163840\n\t"
        "buffer_load_dwordx4 a[176:179], v110, %[so], %[t0] offen\n\t"
        "s_sleep 2\n\t"
        "s_add_u32 %[t0], %[t1], 180224\n\t"
        "buffer_load_dwordx4 a[180:183], v110, %[so], %[t0] offen\n\t"
        "s_sleep 2\n\t"
        "s_add_u32 %[t0], %[t1], 196608\n\t"
        "buffer_load_dwordx4 a[184:187], v110, %[so], %[t0] offen\n\t"
        "s_sleep 2\n\t"
        "s_add_u32 %[t0], %[t1], 212992\n\t"
        "buffer_load_dwordx4 a[188:191], v110, %[so], %[t0] offen\n\t"
        "s_sleep 2\n\t"
        "s_add_u32 %[t0], %[t1], 229376\n\t"
        "buffer_load_dwordx4 v[48:51], v110, %[so], %[t0] offen\n\t"
        "s_sleep 2\n\t"
        "s_add_u32 %[t0], %[t1], 245760\n\t"
        "buffer_load_dwordx4 v[52:55], v110, %[so], %[t0] offen\n\t"
        "s_sleep 2\n\t"
        "s_waitcnt lgkmcnt(0)\n\t"
        "ds_write_b128 v116, v[56:59] offset:10240\n\t"
        "ds_write_b128 v116, v[60:63] offset:11264\n\t"
        "s_waitcnt vmcnt(8)\n\t"
        "ds_write_b128 v111, a[144:147] offset:0\n\t"
        "ds_write_b128 v111, a[148:151] offset:1024\n\t"
        "ds_write_b128 v111, a[152:155] offset:2048\n\t"
        "ds_write_b128 v111, a[156:159] offset:3072\n\t"
        "ds_write_b128 v111, a[160:163] offset:4096\n\t"
        "ds_write_b128 v111, a[164:167] offset:5120\n\t"
        "ds_write_b128 v111, v[40:43] offset:6144\n\t"
        "ds_write_b128 v111, v[44:47] offset:7168\n\t"
        "ds_read_b64_tr_b16 a[144:145], v112 offset:0\n\t"
        "ds_read_b64_tr_b16 a[146:147], v112 offset:2048\n\t"
        "ds_read_b64_tr_b16 a[156:157], v112 offset:4096\n\t"
        "ds_read_b64_tr_b16 a[158:159], v112 offset:6144\n\t"
        "s_waitcnt lgkmcnt(8)\n\t"
        "ds_read_b64_tr_b16 a[148:149], v113 offset:0\n\t"
        "ds_read_b64_tr_b16 a[150:151], v113 offset:2048\n\t"
        "ds_read_b64_tr_b16 a[160:161], v113 offset:4096\n\t"
        "ds_read_b64_tr_b16 a[162:163], v113 offset:6144\n\t"
        "s_waitcnt lgkmcnt(8)\n\t"
        "ds_read_b64_tr_b16 a[152:153], v114 offset:0\n\t"
        "ds_read_b64_tr_b16 a[154:155], v114 offset:2048\n\t"
        "ds_read_b64_tr_b16 a[164:165], v114 offset:4096\n\t"
        "ds_read_b64_tr_b16 a[166:167], v114 offset:6144\n\t"
        "s_waitcnt lgkmcnt(8)\n\t"
        "ds_read_b64_tr_b16 v[56:57], v115 offset:0\n\t"
        "ds_read_b64_tr_b16 v[58:59], v115 offset:2048\n\t"
        "ds_read_b64_tr_b16 v[60:61], v115 offset:4096\n\t"
        "ds_read_b64_tr_b16 v[62:63], v115 offset:6144\n\t"
        "s_add_u32 %[t1], %[ro], 4\n\t"
        "s_and_b32 %[t1], %[t1], 7\n\t"
        "s_lshl_b32 %[t1], %[t1], 18\n\t"
        "s_add_u32 %[t0], %[t1], 0\n\t"
        "buffer_load_dwordx4 a[192:195], v110, %[so], %[t0] offen\n\t"
        "s_sleep 2\n\t"
        "s_add_u32 %[t0], %[t1], 16384\n\t"
        "buffer_load_dwordx4 a[196:199], v110, %[so], %[t0] offen\n\t"
        "s_sleep 2\n\t"
        "s_add_u32 %[t0], %[t1], 32768\n\t"
        "buffer_load_dwordx4 a[200:203], v110, %[so], %[t0] offen\n\t"
        "s_sleep 2\n\t"
        "s_add_u32 %[t0], %[t1], 49152\n\t"
        "buffer_load_dwordx4 a[204:207], v110, %[so], %[t0] offen\n\t"
        "s_sleep 2\n\t"
        "s_add_u32 %[t0], %[t1], 65536\n\t"
        "buffer_load_dwordx4 a[208:211], v110, %[so], %[t0] offen\n\t"
        "s_sleep 2\n\t"
        "s_add_u32 %[t0], %[t1], 81920\n\t"
        "buffer_load_dwordx4 a[212:215], v110, %[so], %[t0] offen\n\t"
        "s_sleep 2\n\t"
        "s_add_u32 %[t0], %[t1], 98304\n\t"
        "buffer_load_dwordx4 v[24:27], v110, %[so], %[t0] offen\n\t"
        "s_sleep 2\n\t"
        "s_add_u32 %[t0], %[t1], 114688\n\t"
        "buffer_load_dwordx4 v[28:31], v110, %[so], %[t0] offen\n\t"
        "s_sleep 2\n\t"
        "s_waitcnt lgkmcnt(0)\n\t"
        "ds_write_b128 v116, v[56:59] offset:12288\n\t"
        "ds_write_b128 v116, v[60:63] offset:13312\n\t"
        "s_waitcnt vmcnt(8)\n\t"
        "ds_write_b128 v111, a[168:171] offset:0\n\t"
        "ds_write_b128 v111, a[172:175] offset:1024\n\t"
        "ds_write_b128 v111, a[176:179] offset:2048\n\t"
        "ds_write_b128 v111, a[180:183] offset:3072\n\t"
        "ds_write_b128 v111, a[184:187] offset:4096\n\t"
        "ds_write_b128 v111, a[188:191] offset:5120\n\t"
        "ds_write_b128 v111, v[48:51] offset:6144\n\t"
        "ds_write_b128 v111, v[52:55] offset:7168\n\t"
        "ds_read_b64_tr_b16 a[168:169], v112 offset:0\n\t"
        "ds_read_b64_tr_b16 a[170:171], v112 offset:2048\n\t"
        "ds_read_b64_tr_b16 a[180:181], v112 offset:4096\n\t"
        "ds_read_b64_tr_b16 a[182:183], v112 offset:6144\n\t"
        "s_waitcnt lgkmcnt(8)\n\t"
        "ds_read_b64_tr_b16 a[172:173], v113 offset:0\n\t"
        "ds_read_b64_tr_b16 a[174:175], v113 offset:2048\n\t"
        "ds_read_b64_tr_b16 a[184:185], v113 offset:4096\n\t"
        "ds_read_b64_tr_b16 a[186:187], v113 offset:6144\n\t"
        "s_waitcnt lgkmcnt(8)\n\t"
        "ds_read_b64_tr_b16 a[176:177], v114 offset:0\n\t"
        "ds_read_b64_tr_b16 a[178:179], v114 offset:2048\n\t"
        "ds_read_b64_tr_b16 a[188:189], v114 offset:4096\n\t"
        "ds_read_b64_tr_b16 a[190:191], v114 offset:6144\n\t"
        "s_waitcnt lgkmcnt(8)\n\t"
        "ds_read_b64_tr_b16 v[56:57], v115 offset:0\n\t"
        "ds_read_b64_tr_b16 v[58:59], v115 offset:2048\n\t"
        "ds_read_b64_tr_b16 v[60:61], v115 offset:4096\n\t"
        "ds_read_b64_tr_b16 v[62:63], v115 offset:6144\n\t"
        "s_add_u32 %[t1], %[ro], 4\n\t"
        "s_and_b32 %[t1], %[t1], 7\n\t"
        "s_lshl_b32 %[t1], %[t1], 18\n\t"
        "s_add_u32 %[t0], %[t1], 131072\n\t"
        "buffer_load_dwordx4 a[216:219], v110, %[so], %[t0] offen\n\t"
        "s_sleep 2\n\t"
        "s_add_u32 %[t0], %[t1], 147456\n\t"
        "buffer_load_dwordx4 a[220:223], v110, %[so], %[t0] offen\n\t"
        "s_sleep 2\n\t"
        "s_add_u32 %[t0], %[t1], 163840\n\t"
        "buffer_load_dwordx4 a[224:227], v110, %[so], %[t0] offen\n\t"
        "s_sleep 2\n\t"
        "s_add_u32 %[t0], %[t1], 180224\n\t"
        "buffer_load_dwordx4 a[228:231], v110, %[so], %[t0] offen\n\t"
        "s_sleep 2\n\t"
        "s_add_u32 %[t0], %[t1], 196608\n\t"
        "buffer_load_dwordx4 a[232:235], v110, %[so], %[t0] offen\n\t"
        "s_sleep 2\n\t"
        "s_add_u32 %[t0], %[t1], 212992\n\t"
        "buffer_load_dwordx4 a[236:239], v110, %[so], %[t0] offen\n\t"
        "s_sleep 2\n\t"
        "s_add_u32 %[t0], %[t1], 229376\n\t"
        "buffer_load_dwordx4 v[32:35], v110, %[so], %[t0] offen\n\t"
        "s_sleep 2\n\t"
        "s_add_u32 %[t0], %[t1], 245760\n\t"
        "buffer_load_dwordx4 v[36:39], v110, %[so], %[t0] offen\n\t"
        "s_sleep 2\n\t"
        "s_waitcnt lgkmcnt(0)\n\t"
        "ds_write_b128 v116, v[56:59] offset:14336\n\t"
        "ds_write_b128 v116, v[60:63] offset:15360\n\t"
        "s_waitcnt vmcnt(8)\n\t"
        "ds_write_b128 v111, a[192:195] offset:0\n\t"
        "ds_write_b128 v111, a[196:199] offset:1024\n\t"
        "ds_write_b128 v111, a[200:203] offset:2048\n\t"
        "ds_write_b128 v111, a[204:207] offset:3072\n\t"
        "ds_write_b128 v111, a[208:211] offset:4096\n\t"
        "ds_write_b128 v111, a[212:215] offset:5120\n\t"
        "ds_write_b128 v111, v[24:27] offset:6144\n\t"
        "ds_write_b128 v111, v[28:31] offset:7168\n\t"
        "ds_read_b64_tr_b16 a[192:193], v112 offset:0\n\t"
        "ds_read_b64_tr_b16 a[194:195], v112 offset:2048\n\t"
        "ds_read_b64_tr_b16 a[204:205], v112 offset:4096\n\t"
        "ds_read_b64_tr_b16 a[206:207], v112 offset:6144\n\t"
        "s_waitcnt lgkmcnt(8)\n\t"
        "ds_read_b64_tr_b16 a[196:197], v113 offset:0\n\t"
        "ds_read_b64_tr_b16 a[198:199], v113 offset:2048\n\t"
        "ds_read_b64_tr_b16 a[208:209], v113 offset:4096\n\t"
        "ds_read_b64_tr_b16 a[210:211], v113 offset:6144\n\t"
        "s_waitcnt lgkmcnt(8)\n\t"
        "ds_read_b64_tr_b16 a[200:201], v114 offset:0\n\t"
        "ds_read_b64_tr_b16 a[202:203], v114 offset:2048\n\t"
        "ds_read_b64_tr_b16 a[212:213], v114 offset:4096\n\t"
        "ds_read_b64_tr_b16 a[214:215], v114 offset:6144\n\t"
        "s_waitcnt lgkmcnt(8)\n\t"
        "ds_read_b64_tr_b16 v[56:57], v115 offset:0\n\t"
        "ds_read_b64_tr_b16 v[58:59], v115 offset:2048\n\t"
        "ds_read_b64_tr_b16 v[60:61], v115 offset:4096\n\t"
        "ds_read_b64_tr_b16 v[62:63], v115 offset:6144\n\t"
        "s_add_u32 %[t1], %[ro], 5\n\t"
        "s_and_b32 %[t1], %[t1], 7\n\t"
        "s_lshl_b32 %[t1], %[t1], 18\n\t"
        "s_add_u32 %[t0], %[t1], 0\n\t"
        "buffer_load_dwordx4 a[240:243], v110, %[so], %[t0] offen\n\t"
        "s_sleep 2\n\t"
        "s_add_u32 %[t0], %[t1], 16384\n\t"
        "buffer_load_dwordx4 a[244:247], v110, %[so], %[t0] offen\n\t"
        "s_sleep 2\n\t"
        "s_add_u32 %[t0], %[t1], 32768\n\t"
        "buffer_load_dwordx4 a[248:251], v110, %[so], %[t0] offen\n\t"
        "s_sleep 2\n\t"
        "s_add_u32 %[t0], %[t1], 49152\n\t"
        "buffer_load_dwordx4 a[252:255], v110, %[so], %[t0] offen\n\t"
        "s_sleep 2\n\t"
        "s_add_u32 %[t0], %[t1], 65536\n\t"
        "buffer_load_dwordx4 v[128:131], v110, %[so], %[t0] offen\n\t"
        "s_sleep 2\n\t"
        "s_add_u32 %[t0], %[t1], 81920\n\t"
        "buffer_load_dwordx4 v[132:135], v110, %[so], %[t0] offen\n\t"
        "s_sleep 2\n\t"
        "s_add_u32 %[t0], %[t1], 98304\n\t"
        "buffer_load_dwordx4 v[40:43], v110, %[so], %[t0] offen\n\t"
        "s_sleep 2\n\t"
        "s_add_u32 %[t0], %[t1], 114688\n\t"
        "buffer_load_dwordx4 v[44:47], v110, %[so], %[t0] offen\n\t"
        "s_sleep 2\n\t"
        "s_waitcnt lgkmcnt(0)\n\t"
        "ds_write_b128 v116, v[56:59] offset:16384\n\t"
        "ds_write_b128 v116, v[60:63] offset:17408\n\t"
        "s_waitcnt vmcnt(8)\n\t"
        "ds_write_b128 v111, a[216:219] offset:0\n\t"
        "ds_write_b128 v111, a[220:223] offset:1024\n\t"
        "ds_write_b128 v111, a[224:227] offset:2048\n\t"
        "ds_write_b128 v111, a[228:231] offset:3072\n\t"
        "ds_write_b128 v111, a[232:235] offset:4096\n\t"
        "ds_write_b128 v111, a[236:239] offset:5120\n\t"
        "ds_write_b128 v111, v[32:35] offset:6144\n\t"
        "ds_write_b128 v111, v[36:39] offset:7168\n\t"
        "ds_read_b64_tr_b16 a[216:217], v112 offset:0\n\t"
        "ds_read_b64_tr_b16 a[218:219], v112 offset:2048\n\t"
        "ds_read_b64_tr_b16 a[228:229], v112 offset:4096\n\t"
        "ds_read_b64_tr_b16 a[230:231], v112 offset:6144\n\t"
        "s_waitcnt lgkmcnt(8)\n\t"
        "ds_read_b64_tr_b16 a[220:221], v113 offset:0\n\t"
        "ds_read_b64_tr_b16 a[222:223], v113 offset:2048\n\t"
        "ds_read_b64_tr_b16 a[232:233], v113 offset:4096\n\t"
        "ds_read_b64_tr_b16 a[234:235], v113 offset:6144\n\t"
        "s_waitcnt lgkmcnt(8)\n\t"
        "ds_read_b64_tr_b16 a[224:225], v114 offset:0\n\t"
        "ds_read_b64_tr_b16 a[226:227], v114 offset:2048\n\t"
        "ds_read_b64_tr_b16 a[236:237], v114 offset:4096\n\t"
        "ds_read_b64_tr_b16 a[238:239], v114 offset:6144\n\t"
        "s_waitcnt lgkmcnt(8)\n\t"
        "ds_read_b64_tr_b16 v[56:57], v115 offset:0\n\t"
        "ds_read_b64_tr_b16 v[58:59], v115 offset:2048\n\t"
        "ds_read_b64_tr_b16 v[60:61], v115 offset:4096\n\t"
        "ds_read_b64_tr_b16 v[62:63], v115 offset:6144\n\t"
        "s_add_u32 %[t1], %[ro], 5\n\t"
        "s_and_b32 %[t1], %[t1], 7\n\t"
        "s_lshl_b32 %[t1], %[t1], 18\n\t"
        "s_add_u32 %[t0], %[t1], 131072\n\t"
        "buffer_load_dwordx4 v[136:139], v110, %[so], %[t0] offen\n\t"
        "s_sleep 2\n\t"
        "s_add_u32 %[t0], %[t1], 147456\n\t"
        "buffer_load_dwordx4 v[140:143], v110, %[so], %[t0] offen\n\t"
        "s_sleep 2\n\t"
        "s_add_u32 %[t0], %[t1], 163840\n\t"
        "buffer_load_dwordx4 v[144:147], v110, %[so], %[t0] offen\n\t"
        "s_sleep 2\n\t"
        "s_add_u32 %[t0], %[t1], 180224\n\t"
        "buffer_load_dwordx4 v[148:151], v110, %[so], %[t0] offen\n\t"
        "s_sleep 2\n\t"
        "s_add_u32 %[t0], %[t1], 196608\n\t"
        "buffer_load_dwordx4 v[152:155], v110, %[so], %[t0] offen\n\t"
        "s_sleep 2\n\t"
        "s_add_u32 %[t0], %[t1], 212992\n\t"
        "buffer_load_dwordx4 v[156:159], v110, %[so], %[t0] offen\n\t"
        "s_sleep 2\n\t"
        "s_add_u32 %[t0], %[t1], 229376\n\t"
        "buffer_load_dwordx4 v[48:51], v110, %[so], %[t0] offen\n\t"
        "s_sleep 2\n\t"
        "s_add_u32 %[t0], %[t1], 245760\n\t"
        "buffer_load_dwordx4 v[52:55], v110, %[so], %[t0] offen\n\t"
        "s_sleep 2\n\t"
        "s_waitcnt lgkmcnt(0)\n\t"
        "ds_write_b128 v116, v[56:59] offset:18432\n\t"
        "ds_write_b128 v116, v[60:63] offset:19456\n\t"
        "s_waitcnt vmcnt(8)\n\t"
        "ds_write_b128 v111, a[240:243] offset:0\n\t"
        "ds_write_b128 v111, a[244:247] offset:1024\n\t"
        "ds_write_b128 v111, a[248:251] offset:2048\n\t"
        "ds_write_b128 v111, a[252:255] offset:3072\n\t"
        "ds_write_b128 v111, v[128:131] offset:4096\n\t"
        "ds_write_b128 v111, v[132:135] offset:5120\n\t"
        "ds_write_b128 v111, v[40:43] offset:6144\n\t"
        "ds_write_b128 v111, v[44:47] offset:7168\n\t"
        "ds_read_b64_tr_b16 a[240:241], v112 offset:0\n\t"
        "ds_read_b64_tr_b16 a[242:243], v112 offset:2048\n\t"
        "ds_read_b64_tr_b16 a[252:253], v112 offset:4096\n\t"
        "ds_read_b64_tr_b16 a[254:255], v112 offset:6144\n\t"
        "s_waitcnt lgkmcnt(8)\n\t"
        "ds_read_b64_tr_b16 a[244:245], v113 offset:0\n\t"
        "ds_read_b64_tr_b16 a[246:247], v113 offset:2048\n\t"
        "ds_read_b64_tr_b16 v[128:129], v113 offset:4096\n\t"
        "ds_read_b64_tr_b16 v[130:131], v113 offset:6144\n\t"
        "s_waitcnt lgkmcnt(8)\n\t"
        "ds_read_b64_tr_b16 a[248:249], v114 offset:0\n\t"
        "ds_read_b64_tr_b16 a[250:251], v114 offset:2048\n\t"
        "ds_read_b64_tr_b16 v[132:133], v114 offset:4096\n\t"
        "ds_read_b64_tr_b16 v[134:135], v114 offset:6144\n\t"
        "s_waitcnt lgkmcnt(8)\n\t"
        "ds_read_b64_tr_b16 v[56:57], v115 offset:0\n\t"
        "ds_read_b64_tr_b16 v[58:59], v115 offset:2048\n\t"
        "ds_read_b64_tr_b16 v[60:61], v115 offset:4096\n\t"
        "ds_read_b64_tr_b16 v[62:63], v115 offset:6144\n\t"
        "s_add_u32 %[t1], %[ro], 6\n\t"
        "s_and_b32 %[t1], %[t1], 7\n\t"
        "s_lshl_b32 %[t1], %[t1], 18\n\t"
        "s_add_u32 %[t0], %[t1], 0\n\t"
        "buffer_load_dwordx4 v[160:163], v110, %[so], %[t0] offen\n\t"
        "s_sleep 2\n\t"
        "s_add_u32 %[t0], %[t1], 16384\n\t"
        "buffer_load_dwordx4 v[164:167], v110, %[so], %[t0] offen\n\t"
        "s_sleep 2\n\t"
        "s_add_u32 %[t0], %[t1], 32768\n\t"
        "buffer_load_dwordx4 v[168:171], v110, %[so], %[t0] offen\n\t"
        "s_sleep 2\n\t"
        "s_add_u32 %[t0], %[t1], 49152\n\t"
        "buffer_load_dwordx4 v[172:175], v110, %[so], %[t0] offen\n\t"
        "s_sleep 2\n\t"
        "s_add_u32 %[t0], %[t1], 65536\n\t"
        "buffer_load_dwordx4 v[176:179], v110, %[so], %[t0] offen\n\t"
        "s_sleep 2\n\t"
        "s_add_u32 %[t0], %[t1], 81920\n\t"
        "buffer_load_dwordx4 v[180:183], v110, %[so], %[t0] offen\n\t"
        "s_sleep 2\n\t"
        "s_add_u32 %[t0], %[t1], 98304\n\t"
        "buffer_load_dwordx4 v[24:27], v110, %[so], %[t0] offen\n\t"
        "s_sleep 2\n\t"
        "s_add_u32 %[t0], %[t1], 114688\n\t"
        "buffer_load_dwordx4 v[28:31], v110, %[so], %[t0] offen\n\t"
        "s_sleep 2\n\t"
        "s_waitcnt lgkmcnt(0)\n\t"
        "ds_write_b128 v116, v[56:59] offset:20480\n\t"
        "ds_write_b128 v116, v[60:63] offset:21504\n\t"
        "s_waitcnt vmcnt(8)\n\t"
        "ds_write_b128 v111, v[136:139] offset:0\n\t"
        "ds_write_b128 v111, v[140:143] offset:1024\n\t"
        "ds_write_b128 v111, v[144:147] offset:2048\n\t"
        "ds_write_b128 v111, v[148:151] offset:3072\n\t"
        "ds_write_b128 v111, v[152:155] offset:4096\n\t"
        "ds_write_b128 v111, v[156:159] offset:5120\n\t"
        "ds_write_b128 v111, v[48:51] offset:6144\n\t"
        "ds_write_b128 v111, v[52:55] offset:7168\n\t"
        "ds_read_b64_tr_b16 v[136:137], v112 offset:0\n\t"
        "ds_read_b64_tr_b16 v[138:139], v112 offset:2048\n\t"
        "ds_read_b64_tr_b16 v[148:149], v112 offset:4096\n\t"
        "ds_read_b64_tr_b16 v[150:151], v112 offset:6144\n\t"
        "s_waitcnt lgkmcnt(8)\n\t"
        "ds_read_b64_tr_b16 v[140:141], v113 offset:0\n\t"
        "ds_read_b64_tr_b16 v[142:143], v113 offset:2048\n\t"
        "ds_read_b64_tr_b16 v[152:153], v113 offset:4096\n\t"
        "ds_read_b64_tr_b16 v[154:155], v113 offset:6144\n\t"
        "s_waitcnt lgkmcnt(8)\n\t"
        "ds_read_b64_tr_b16 v[144:145], v114 offset:0\n\t"
        "ds_read_b64_tr_b16 v[146:147], v114 offset:2048\n\t"
        "ds_read_b64_tr_b16 v[156:157], v114 offset:4096\n\t"
        "ds_read_b64_tr_b16 v[158:159], v114 offset:6144\n\t"
        "s_waitcnt lgkmcnt(8)\n\t"
        "ds_read_b64_tr_b16 v[56:57], v115 offset:0\n\t"
        "ds_read_b64_tr_b16 v[58:59], v115 offset:2048\n\t"
        "ds_read_b64_tr_b16 v[60:61], v115 offset:4096\n\t"
        "ds_read_b64_tr_b16 v[62:63], v115 offset:6144\n\t"
        "s_add_u32 %[t1], %[ro], 6\n\t"
        "s_and_b32 %[t1], %[t1], 7\n\t"
        "s_lshl_b32 %[t1], %[t1], 18\n\t"
        "s_add_u32 %[t0], %[t1], 131072\n\t"
        "buffer_load_dwordx4 v[184:187], v110, %[so], %[t0] offen\n\t"
        "s_sleep 2\n\t"
        "s_add_u32 %[t0], %[t1], 147456\n\t"
        "buffer_load_dwordx4 v[188:191], v110, %[so], %[t0] offen\n\t"
        "s_sleep 2\n\t"
        "s_add_u32 %[t0], %[t1], 163840\n\t"
        "buffer_load_dwordx4 v[192:195], v110, %[so], %[t0] offen\n\t"
        "s_sleep 2\n\t"
        "s_add_u32 %[t0], %[t1], 180224\n\t"
        "buffer_load_dwordx4 v[196:199], v110, %[so], %[t0] offen\n\t"
        "s_sleep 2\n\t"
        "s_add_u32 %[t0], %[t1], 196608\n\t"
        "buffer_load_dwordx4 v[200:203], v110, %[so], %[t0] offen\n\t"
        "s_sleep 2\n\t"
        "s_add_u32 %[t0], %[t1], 212992\n\t"
        "buffer_load_dwordx4 v[204:207], v110, %[so], %[t0] offen\n\t"
        "s_sleep 2\n\t"
        "s_add_u32 %[t0], %[t1], 229376\n\t"
        "buffer_load_dwordx4 v[32:35], v110, %[so], %[t0] offen\n\t"
        "s_sleep 2\n\t"
        "s_add_u32 %[t0], %[t1], 245760\n\t"
        "buffer_load_dwordx4 v[36:39], v110, %[so], %[t0] offen\n\t"
        "s_sleep 2\n\t"
        "s_waitcnt lgkmcnt(0)\n\t"
        "ds_write_b128 v116, v[56:59] offset:22528\n\t"
        "ds_write_b128 v116, v[60:63] offset:23552\n\t"
        "s_waitcnt vmcnt(8)\n\t"
        "ds_write_b128 v111, v[160:163] offset:0\n\t"
        "ds_write_b128 v111, v[164:167] offset:1024\n\t"
        "ds_write_b128 v111, v[168:171] offset:2048\n\t"
        "ds_write_b128 v111, v[172:175] offset:3072\n\t"
        "ds_write_b128 v111, v[176:179] offset:4096\n\t"
        "ds_write_b128 v111, v[180:183] offset:5120\n\t"
        "ds_write_b128 v111, v[24:27] offset:6144\n\t"
        "ds_write_b128 v111, v[28:31] offset:7168\n\t"
        "ds_read_b64_tr_b16 v[160:161], v112 offset:0\n\t"
        "ds_read_b64_tr_b16 v[162:163], v112 offset:2048\n\t"
        "ds_read_b64_tr_b16 v[172:173], v112 offset:4096\n\t"
        "ds_read_b64_tr_b16 v[174:175], v112 offset:6144\n\t"
        "s_waitcnt lgkmcnt(8)\n\t"
        "ds_read_b64_tr_b16 v[164:165], v113 offset:0\n\t"
        "ds_read_b64_tr_b16 v[166:167], v113 offset:2048\n\t"
        "ds_read_b64_tr_b16 v[176:177], v113 offset:4096\n\t"
        "ds_read_b64_tr_b16 v[178:179], v113 offset:6144\n\t"
        "s_waitcnt lgkmcnt(8)\n\t"
        "ds_read_b64_tr_b16 v[168:169], v114 offset:0\n\t"
        "ds_read_b64_tr_b16 v[170:171], v114 offset:2048\n\t"
        "ds_read_b64_tr_b16 v[180:181], v114 offset:4096\n\t"
        "ds_read_b64_tr_b16 v[182:183], v114 offset:6144\n\t"
        "s_waitcnt lgkmcnt(8)\n\t"
        "ds_read_b64_tr_b16 v[56:57], v115 offset:0\n\t"
        "ds_read_b64_tr_b16 v[58:59], v115 offset:2048\n\t"
        "ds_read_b64_tr_b16 v[60:61], v115 offset:4096\n\t"
        "ds_read_b64_tr_b16 v[62:63], v115 offset:6144\n\t"
        "s_add_u32 %[t1], %[ro], 7\n\t"
        "s_and_b32 %[t1], %[t1], 7\n\t"
        "s_lshl_b32 %[t1], %[t1], 18\n\t"
        "s_add_u32 %[t0], %[t1], 0\n\t"
        "buffer_load_dwordx4 v[208:211], v110, %[so], %[t0] offen\n\t"
        "s_sleep 2\n\t"
        "s_add_u32 %[t0], %[t1], 16384\n\t"
        "buffer_load_dwordx4 v[212:215], v110, %[so], %[t0] offen\n\t"
        "s_sleep 2\n\t"
        "s_add_u32 %[t0], %[t1], 32768\n\t"
        "buffer_load_dwordx4 v[216:219], v110, %[so], %[t0] offen\n\t"
        "s_sleep 2\n\t"
        "s_add_u32 %[t0], %[t1], 49152\n\t"
        "buffer_load_dwordx4 v[220:223], v110, %[so], %[t0] offen\n\t"
        "s_sleep 2\n\t"
        "s_add_u32 %[t0], %[t1], 65536\n\t"
        "buffer_load_dwordx4 v[224:227], v110, %[so], %[t0] offen\n\t"
        "s_sleep 2\n\t"
        "s_add_u32 %[t0], %[t1], 81920\n\t"
        "buffer_load_dwordx4 v[228:231], v110, %[so], %[t0] offen\n\t"
        "s_sleep 2\n\t"
        "s_add_u32 %[t0], %[t1], 98304\n\t"
        "buffer_load_dwordx4 v[40:43], v110, %[so], %[t0] offen\n\t"
        "s_sleep 2\n\t"
        "s_add_u32 %[t0], %[t1], 114688\n\t"
        "buffer_load_dwordx4 v[44:47], v110, %[so], %[t0] offen\n\t"
        "s_sleep 2\n\t"
        "s_waitcnt lgkmcnt(0)\n\t"
        "ds_write_b128 v116, v[56:59] offset:24576\n\t"
        "ds_write_b128 v116, v[60:63] offset:25600\n\t"
        "s_waitcnt vmcnt(8)\n\t"
        "ds_write_b128 v111, v[184:187] offset:0\n\t"
        "ds_write_b128 v111, v[188:191] offset:1024\n\t"
        "ds_write_b128 v111, v[192:195] offset:2048\n\t"
        "ds_write_b128 v111, v[196:199] offset:3072\n\t"
        "ds_write_b128 v111, v[200:203] offset:4096\n\t"
        "ds_write_b128 v111, v[204:207] offset:5120\n\t"
        "ds_write_b128 v111, v[32:35] offset:6144\n\t"
        "ds_write_b128 v111, v[36:39] offset:7168\n\t"
        "ds_read_b64_tr_b16 v[184:185], v112 offset:0\n\t"
        "ds_read_b64_tr_b16 v[186:187], v112 offset:2048\n\t"
        "ds_read_b64_tr_b16 v[196:197], v112 offset:4096\n\t"
        "ds_read_b64_tr_b16 v[198:199], v112 offset:6144\n\t"
        "s_waitcnt lgkmcnt(8)\n\t"
        "ds_read_b64_tr_b16 v[188:189], v113 offset:0\n\t"
        "ds_read_b64_tr_b16 v[190:191], v113 offset:2048\n\t"
        "ds_read_b64_tr_b16 v[200:201], v113 offset:4096\n\t"
        "ds_read_b64_tr_b16 v[202:203], v113 offset:6144\n\t"
        "s_waitcnt lgkmcnt(8)\n\t"
        "ds_read_b64_tr_b16 v[192:193], v114 offset:0\n\t"
        "ds_read_b64_tr_b16 v[194:195], v114 offset:2048\n\t"
        "ds_read_b64_tr_b16 v[204:205], v114 offset:4096\n\t"
        "ds_read_b64_tr_b16 v[206:207], v114 offset:6144\n\t"
        "s_waitcnt lgkmcnt(8)\n\t"
        "ds_read_b64_tr_b16 v[56:57], v115 offset:0\n\t"
        "ds_read_b64_tr_b16 v[58:59], v115 offset:2048\n\t"
        "ds_read_b64_tr_b16 v[60:61], v115 offset:4096\n\t"
        "ds_read_b64_tr_b16 v[62:63], v115 offset:6144\n\t"
        "s_add_u32 %[t1], %[ro], 7\n\t"
        "s_and_b32 %[t1], %[t1], 7\n\t"
        "s_lshl_b32 %[t1], %[t1], 18\n\t"
        "s_add_u32 %[t0], %[t1], 131072\n\t"
        "buffer_load_dwordx4 v[232:235], v110, %[so], %[t0] offen\n\t"
        "s_sleep 2\n\t"
        "s_add_u32 %[t0], %[t1], 147456\n\t"
        "buffer_load_dwordx4 v[236:239], v110, %[so], %[t0] offen\n\t"
        "s_sleep 2\n\t"
        "s_add_u32 %[t0], %[t1], 163840\n\t"
        "buffer_load_dwordx4 v[240:243], v110, %[so], %[t0] offen\n\t"
        "s_sleep 2\n\t"
        "s_add_u32 %[t0], %[t1], 180224\n\t"
        "buffer_load_dwordx4 v[244:247], v110, %[so], %[t0] offen\n\t"
        "s_sleep 2\n\t"
        "s_add_u32 %[t0], %[t1], 196608\n\t"
        "buffer_load_dwordx4 v[248:251], v110, %[so], %[t0] offen\n\t"
        "s_sleep 2\n\t"
        "s_add_u32 %[t0], %[t1], 212992\n\t"
        "buffer_load_dwordx4 v[252:255], v110, %[so], %[t0] offen\n\t"
        "s_sleep 2\n\t"
        "s_add_u32 %[t0], %[t1], 229376\n\t"
        "buffer_load_dwordx4 v[48:51], v110, %[so], %[t0] offen\n\t"
        "s_sleep 2\n\t"
        "s_add_u32 %[t0], %[t1], 245760\n\t"
        "buffer_load_dwordx4 v[52:55], v110, %[so], %[t0] offen\n\t"
        "s_sleep 2\n\t"
        "s_waitcnt lgkmcnt(0)\n\t"
        "ds_write_b128 v116, v[56:59] offset:26624\n\t"
        "ds_write_b128 v116, v[60:63] offset:27648\n\t"
        "s_waitcnt vmcnt(8)\n\t"
        "ds_write_b128 v111, v[208:211] offset:0\n\t"
        "ds_write_b128 v111, v[212:215] offset:1024\n\t"
        "ds_write_b128 v111, v[216:219] offset:2048\n\t"
        "ds_write_b128 v111, v[220:223] offset:3072\n\t"
        "ds_write_b128 v111, v[224:227] offset:4096\n\t"
        "ds_write_b128 v111, v[228:231] offset:5120\n\t"
        "ds_write_b128 v111, v[40:43] offset:6144\n\t"
        "ds_write_b128 v111, v[44:47] offset:7168\n\t"
        "ds_read_b64_tr_b16 v[208:209], v112 offset:0\n\t"
        "ds_read_b64_tr_b16 v[210:211], v112 offset:2048\n\t"
        "ds_read_b64_tr_b16 v[220:221], v112 offset:4096\n\t"
        "ds_read_b64_tr_b16 v[222:223], v112 offset:6144\n\t"
        "s_waitcnt lgkmcnt(8)\n\t"
        "ds_read_b64_tr_b16 v[212:213], v113 offset:0\n\t"
        "ds_read_b64_tr_b16 v[214:215], v113 offset:2048\n\t"
        "ds_read_b64_tr_b16 v[224:225], v113 offset:4096\n\t"
        "ds_read_b64_tr_b16 v[226:227], v113 offset:6144\n\t"
        "s_waitcnt lgkmcnt(8)\n\t"
        "ds_read_b64_tr_b16 v[216:217], v114 offset:0\n\t"
        "ds_read_b64_tr_b16 v[218:219], v114 offset:2048\n\t"
        "ds_read_b64_tr_b16 v[228:229], v114 offset:4096\n\t"
        "ds_read_b64_tr_b16 v[230:231], v114 offset:6144\n\t"
        "s_waitcnt lgkmcnt(8)\n\t"
        "ds_read_b64_tr_b16 v[56:57], v115 offset:0\n\t"
        "ds_read_b64_tr_b16 v[58:59], v115 offset:2048\n\t"
        "ds_read_b64_tr_b16 v[60:61], v115 offset:4096\n\t"
        "ds_read_b64_tr_b16 v[62:63], v115 offset:6144\n\t"
        "s_waitcnt lgkmcnt(0)\n\t"
        "ds_write_b128 v116, v[56:59] offset:28672\n\t"
        "ds_write_b128 v116, v[60:63] offset:29696\n\t"
        "s_waitcnt vmcnt(0)\n\t"
        "ds_write_b128 v111, v[232:235] offset:0\n\t"
        "ds_write_b128 v111, v[236:239] offset:1024\n\t"
        "ds_write_b128 v111, v[240:243] offset:2048\n\t"
        "ds_write_b128 v111, v[244:247] offset:3072\n\t"
        "ds_write_b128 v111, v[248:251] offset:4096\n\t"
        "ds_write_b128 v111, v[252:255] offset:5120\n\t"
        "ds_write_b128 v111, v[48:51] offset:6144\n\t"
        "ds_write_b128 v111, v[52:55] offset:7168\n\t"
        "ds_read_b64_tr_b16 v[232:233], v112 offset:0\n\t"
        "ds_read_b64_tr_b16 v[234:235], v112 offset:2048\n\t"
        "ds_read_b64_tr_b16 v[244:245], v112 offset:4096\n\t"
        "ds_read_b64_tr_b16 v[246:247], v112 offset:6144\n\t"
        "s_waitcnt lgkmcnt(8)\n\t"
        "ds_read_b64_tr_b16 v[236:237], v113 offset:0\n\t"
        "ds_read_b64_tr_b16 v[238:239], v113 offset:2048\n\t"
        "ds_read_b64_tr_b16 v[248:249], v113 offset:4096\n\t"
        "ds_read_b64_tr_b16 v[250:251], v113 offset:6144\n\t"
        "s_waitcnt lgkmcnt(8)\n\t"
        "ds_read_b64_tr_b16 v[240:241], v114 offset:0\n\t"
        "ds_read_b64_tr_b16 v[242:243], v114 offset:2048\n\t"
        "ds_read_b64_tr_b16 v[252:253], v114 offset:4096\n\t"
        "ds_read_b64_tr_b16 v[254:255], v114 offset:6144\n\t"
        "s_waitcnt lgkmcnt(8)\n\t"
        "ds_read_b64_tr_b16 v[56:57], v115 offset:0\n\t"
        "ds_read_b64_tr_b16 v[58:59], v115 offset:2048\n\t"
        "ds_read_b64_tr_b16 v[60:61], v115 offset:4096\n\t"
        "ds_read_b64_tr_b16 v[62:63], v115 offset:6144\n\t"
        "s_waitcnt lgkmcnt(0)\n\t"
        "ds_write_b128 v116, v[56:59] offset:30720\n\t"
        "ds_write_b128 v116, v[60:63] offset:31744\n\t"
        "ds_read_b128 v[88:91], v116 offset:0\n\t"
        "ds_read_b128 v[92:95], v116 offset:1024\n\t"
        "ds_read_b128 v[96:99], v116 offset:2048\n\t"
        "s_mov_b32 %[sp], 0\n\t"
        "LP_cv%=:\n\t"
        "buffer_load_dword v104, off, %[sw], %[cv] sc1\n\t"
        "s_waitcnt vmcnt(0) lgkmcnt(0)\n\t"
        "v_readfirstlane_b32 %[t0], v104\n\t"
        "s_cmp_ge_u32 %[t0], 8\n\t"
        "s_cbranch_scc1 LP_wait%=\n\t"
        "s_sleep 8\n\t"
        "s_add_u32 %[sp], %[sp], 1\n\t"
        "s_cmp_lt_u32 %[sp], 0x100000\n\t"
        "s_cbranch_scc1 LP_cv%=\n\t"
        "v_mov_b32_e32 v104, 256\n\t"
        "s_mov_b32 %[t0], 33024\n\t"
        "buffer_store_dword v104, off, %[sw], %[t0] sc1\n\t"
        "s_endpgm\n\t"
        "LP_wait%=:\n\t"
        "s_mov_b32 %[re], 0\n\t"
        "LP_again%=:\n\t"
        "s_mov_b32 %[sp], 0\n\t"
        "LP_poll%=:\n\t"
        "buffer_load_dword v103, v118, %[sw], 0 offen sc1\n\t"
        "s_lshr_b32 %[t1], %[tl], 1\n\t"
        "s_waitcnt vmcnt(0)\n\t"
        "v_min_u32_dpp v103, v103, v103 quad_perm:[1,0,3,2] row_mask:0xf bank_mask:0xf\n\t"
        "s_nop 1\n\t"
        "v_min_u32_dpp v103, v103, v103 quad_perm:[2,3,0,1] row_mask:0xf bank_mask:0xf\n\t"
        "s_nop 1\n\t"
        "v_min_u32_dpp v103, v103, v103 row_ror:4 row_mask:0xf bank_mask:0xf\n\t"
        "s_nop 0\n\t"
        "v_readfirstlane_b32 %[pg], v103\n\t"
        "s_cmp_gt_u32 %[pg], %[t1]\n\t"
        "s_cbranch_scc1 LP_have%=\n\t"
        "s_add_u32 %[t0], %[pg], %[re]\n\t"
        "s_cmp_eq_u32 %[t0], 63\n\t"
        "s_cbranch_scc0 LP_no%=\n\t"
        "s_sleep 16\n\t"
        "s_branch LP_have%=\n\t"
        "LP_no%=:\n\t"
        "s_min_u32 %[t1], %[t1], 62\n\t"
        "s_cmp_ge_u32 %[pg], %[t1]\n\t"
        "s_cbranch_scc1 LP_near%=\n\t"
        "s_sleep 24\n\t"
        "LP_near%=:\n\t"
        "s_sleep 2\n\t"
        "s_add_u32 %[sp], %[sp], 1\n\t"
        "s_cmp_lt_u32 %[sp], 0x100000\n\t"
        "s_cbranch_scc1 LP_poll%=\n\t"
        "v_mov_b32_e32 v104, 128\n\t"
        "s_mov_b32 %[t0], 33024\n\t"
        "buffer_store_dword v104, off, %[sw], %[t0] sc1\n\t"
        "s_endpgm\n\t"
        "LP_have%=:\n\t"
        "s_lshl_b32 %[nx], %[tl], 17\n\t"
        "s_add_u32 %[t1], %[t1], 4\n\t"
        "s_cmp_gt_u32 %[pg], %[t1]\n\t"
        "s_cbranch_scc1 LP_run%=\n\t"
        "s_add_u32 %[t0], %[ro], 0\n\t"
        "s_and_b32 %[t0], %[t0], 7\n\t"
        "s_lshl_b32 %[t0], %[t0], 12\n\t"
        "s_add_u32 %[t0], %[t0], %[nx]\n\t"
        "buffer_load_dwordx4 v[24:27], v117, %[sw], %[t0] offen offset:0 sc1\n\t"
        "buffer_load_dwordx4 v[28:31], v117, %[sw], %[t0] offen offset:1024 sc1\n\t"
        "buffer_load_dwordx4 v[32:35], v117, %[sw], %[t0] offen offset:2048 sc1\n\t"
        "buffer_load_dwordx4 v[36:39], v117, %[sw], %[t0] offen offset:3072 sc1\n\t"
        "s_add_u32 %[t0], %[ro], 1\n\t"
        "s_and_b32 %[t0], %[t0], 7\n\t"
        "s_lshl_b32 %[t0], %[t0], 12\n\t"
        "s_add_u32 %[t0], %[t0], %[nx]\n\t"
        "buffer_load_dwordx4 v[40:43], v117, %[sw], %[t0] offen offset:0 sc1\n\t"
        "buffer_load_dwordx4 v[44:47], v117, %[sw], %[t0] offen offset:1024 sc1\n\t"
        "s_waitcnt vmcnt(5) lgkmcnt(2)\n\t"
        "v_mfma_f32_16x16x32_bf16 v[20:23], v[88:91], v[24:27], 0\n\t"
        "ds_read_b128 v[88:91], v116 offset:3072\n\t"
        "v_mfma_f32_16x16x32_bf16 v[8:11], a[0:3], v[24:27], 0\n\t"
        "v_max3_u32 v125, v24, v25, v26\n\t"
        "buffer_load_dwordx4 v[48:51], v117, %[sw], %[t0] offen offset:2048 sc1\n\t"
        "v_mfma_f32_16x16x32_bf16 v[12:15], a[4:7], v[24:27], 0\n\t"
        "v_max_u32_e32 v125, v27, v125\n\t"
        "v_mfma_f32_16x16x32_bf16 v[16:19], a[8:11], v[24:27], 0\n\t"
        "buffer_load_dwordx4 v[52:55], v117, %[sw], %[t0] offen offset:3072 sc1\n\t"
        "s_waitcnt vmcnt(6) lgkmcnt(2)\n\t"
        "v_mfma_f32_16x16x32_bf16 v[20:23], v[92:95], v[28:31], v[20:23]\n\t"
        "ds_read_b128 v[92:95], v116 offset:4096\n\t"
        "v_mfma_f32_16x16x32_bf16 v[8:11], a[12:15], v[28:31], v[8:11]\n\t"
        "v_max3_u32 v125, v28, v29, v125\n\t"
        "s_add_u32 %[t0], %[ro], 2\n\t"
        "s_and_b32 %[t0], %[t0], 7\n\t"
        "s_lshl_b32 %[t0], %[t0], 12\n\t"
        "s_add_u32 %[t0], %[t0], %[nx]\n\t"
        "buffer_load_dwordx4 v[56:59], v117, %[sw], %[t0] offen offset:0 sc1\n\t"
        "v_mfma_f32_16x16x32_bf16 v[12:15], a[16:19], v[28:31], v[12:15]\n\t"
        "v_max3_u32 v125, v30, v31, v125\n\t"
        "v_mfma_f32_16x16x32_bf16 v[16:19], a[20:23], v[28:31], v[16:19]\n\t"
        "buffer_load_dwordx4 v[60:63], v117, %[sw], %[t0] offen offset:1024 sc1\n\t"
        "s_and_b32 %[sp], %[tl], 1\n\t"
        "s_lshl_b32 %[sp], %[sp], 14\n\t"
        "s_lshr_b32 %[t1], %[tl], 1\n\t"
        "s_lshl_b32 %[t1], %[t1], 6\n\t"
        "s_add_u32 %[t1], %[sp], %[t1]\n\t"
        "buffer_load_dword v100, v119, %[sw], %[t1] offen sc1\n\t"
        "s_add_u32 %[t1], %[t1], 32768\n\t"
        "buffer_load_dword v101, v119, %[sw], %[t1] offen sc1\n\t"
        "s_lshl_b32 %[t1], %[tl], 6\n\t"
        "buffer_load_dword v102, v120, %[sw], %[t1] offen sc1\n\t"
        "buffer_load_dword v103, v118, %[sw], 0 offen sc1\n\t"
        "s_waitcnt vmcnt(11) lgkmcnt(2)\n\t"
        "v_mfma_f32_16x16x32_bf16 v[20:23], v[96:99], v[32:35], v[20:23]\n\t"
        "ds_read_b128 v[96:99], v116 offset:5120\n\t"
        "v_mfma_f32_16x16x32_bf16 v[8:11], a[24:27], v[32:35], v[8:11]\n\t"
        "v_max3_u32 v125, v32, v33, v125\n\t"
        "buffer_load_dwordx4 v[64:67], v117, %[sw], %[t0] offen offset:2048 sc1\n\t"
        "v_mfma_f32_16x16x32_bf16 v[12:15], a[28:31], v[32:35], v[12:15]\n\t"
        "v_max3_u32 v125, v34, v35, v125\n\t"
        "v_mfma_f32_16x16x32_bf16 v[16:19], a[32:35], v[32:35], v[16:19]\n\t"
        "buffer_load_dwordx4 v[68:71], v117, %[sw], %[t0] offen offset:3072 sc1\n\t"
        "s_waitcnt vmcnt(12) lgkmcnt(2)\n\t"
        "v_mfma_f32_16x16x32_bf16 v[20:23], v[88:91], v[36:39], v[20:23]\n\t"
        "ds_read_b128 v[88:91], v116 offset:6144\n\t"
        "v_mfma_f32_16x16x32_bf16 v[8:11], a[36:39], v[36:39], v[8:11]\n\t"
        "v_max3_u32 v125, v36, v37, v125\n\t"
        "s_add_u32 %[t0], %[ro], 3\n\t"
        "s_and_b32 %[t0], %[t0], 7\n\t"
        "s_lshl_b32 %[t0], %[t0], 12\n\t"
        "s_add_u32 %[t0], %[t0], %[nx]\n\t"
        "buffer_load_dwordx4 v[72:75], v117, %[sw], %[t0] offen offset:0 sc1\n\t"
        "v_mfma_f32_16x16x32_bf16 v[12:15], a[40:43], v[36:39], v[12:15]\n\t"
        "v_max3_u32 v125, v38, v39, v125\n\t"
        "v_mfma_f32_16x16x32_bf16 v[16:19], a[44:47], v[36:39], v[16:19]\n\t"
        "buffer_load_dwordx4 v[76:79], v117, %[sw], %[t0] offen offset:1024 sc1\n\t"
        "s_waitcnt vmcnt(13) lgkmcnt(2)\n\t"
        "v_mfma_f32_16x16x32_bf16 v[20:23], v[92:95], v[40:43], v[20:23]\n\t"
        "ds_read_b128 v[92:95], v116 offset:7168\n\t"
        "v_mfma_f32_16x16x32_bf16 v[8:11], a[48:51], v[40:43], v[8:11]\n\t"
        "v_max3_u32 v125, v40, v41, v125\n\t"
        "buffer_load_dwordx4 v[80:83], v117, %[sw], %[t0] offen offset:2048 sc1\n\t"
        "v_mfma_f32_16x16x32_bf16 v[12:15], a[52:55], v[40:43], v[12:15]\n\t"
        "v_max3_u32 v125, v42, v43, v125\n\t"
        "v_mfma_f32_16x16x32_bf16 v[16:19], a[56:59], v[40:43], v[16:19]\n\t"
        "buffer_load_dwordx4 v[84:87], v117, %[sw], %[t0] offen offset:3072 sc1\n\t"
        "s_waitcnt vmcnt(14) lgkmcnt(2)\n\t"
        "v_mfma_f32_16x16x32_bf16 v[20:23], v[96:99], v[44:47], v[20:23]\n\t"
        "ds_read_b128 v[96:99], v116 offset:8192\n\t"
        "v_mfma_f32_16x16x32_bf16 v[8:11], a[60:63], v[44:47], v[8:11]\n\t"
        "v_max3_u32 v125, v44, v45, v125\n\t"
        "s_add_u32 %[t0], %[ro], 4\n\t"
        "s_and_b32 %[t0], %[t0], 7\n\t"
        "s_lshl_b32 %[t0], %[t0], 12\n\t"
        "s_add_u32 %[t0], %[t0], %[nx]\n\t"
        "buffer_load_dwordx4 v[24:27], v117, %[sw], %[t0] offen offset:0 sc1\n\t"
        "v_mfma_f32_16x16x32_bf16 v[12:15], a[64:67], v[44:47], v[12:15]\n\t"
        "v_max3_u32 v125, v46, v47, v125\n\t"
        "v_mfma_f32_16x16x32_bf16 v[16:19], a[68:71], v[44:47], v[16:19]\n\t"
        "buffer_load_dwordx4 v[28:31], v117, %[sw], %[t0] offen offset:1024 sc1\n\t"
        "s_waitcnt vmcnt(15) lgkmcnt(2)\n\t"
        "v_mfma_f32_16x16x32_bf16 v[20:23], v[88:91], v[48:51], v[20:23]\n\t"
        "ds_read_b128 v[88:91], v116 offset:9216\n\t"
        "v_mfma_f32_16x16x32_bf16 v[8:11], a[72:75], v[48:51], v[8:11]\n\t"
        "v_max3_u32 v125, v48, v49, v125\n\t"
        "v_mfma_f32_16x16x32_bf16 v[12:15], a[76:79], v[48:51], v[12:15]\n\t"
        "v_max3_u32 v125, v50, v51, v125\n\t"
        "v_mfma_f32_16x16x32_bf16 v[16:19], a[80:83], v[48:51], v[16:19]\n\t"
        "buffer_load_dwordx4 v[32:35], v117, %[sw], %[t0] offen offset:2048 sc1\n\t"
        "s_waitcnt vmcnt(15) lgkmcnt(2)\n\t"
        "v_mfma_f32_16x16x32_bf16 v[20:23], v[92:95], v[52:55], v[20:23]\n\t"
        "ds_read_b128 v[92:95], v116 offset:10240\n\t"
        "v_mfma_f32_16x16x32_bf16 v[8:11], a[84:87], v[52:55], v[8:11]\n\t"
        "v_max3_u32 v125, v52, v53, v125\n\t"
        "v_mfma_f32_16x16x32_bf16 v[12:15], a[88:91], v[52:55], v[12:15]\n\t"
        "v_max3_u32 v125, v54, v55, v125\n\t"
        "v_mfma_f32_16x16x32_bf16 v[16:19], a[92:95], v[52:55], v[16:19]\n\t"
        "buffer_load_dwordx4 v[36:39], v117, %[sw], %[t0] offen offset:3072 sc1\n\t"
        "s_waitcnt vmcnt(15) lgkmcnt(2)\n\t"
        "s_cmp_lg_u32 %[re], 0\n\t"
        "s_cbranch_scc1 LP_nb%=\n\t"
        "s_barrier\n\t"
        "LP_nb%=:\n\t"
        "v_mfma_f32_16x16x32_bf16 v[20:23], v[96:99], v[56:59], v[20:23]\n\t"
        "ds_read_b128 v[96:99], v116 offset:11264\n\t"
        "v_mfma_f32_16x16x32_bf16 v[8:11], a[96:99], v[56:59], v[8:11]\n\t"
        "v_max3_u32 v125, v56, v57, v125\n\t"
        "v_mfma_f32_16x16x32_bf16 v[12:15], a[100:103], v[56:59], v[12:15]\n\t"
        "v_max3_u32 v125, v58, v59, v125\n\t"
        "v_mfma_f32_16x16x32_bf16 v[16:19], a[104:107], v[56:59], v[16:19]\n\t"
        "s_add_u32 %[t0], %[ro], 5\n\t"
        "s_and_b32 %[t0], %[t0], 7\n\t"
        "s_lshl_b32 %[t0], %[t0], 12\n\t"
        "s_add_u32 %[t0], %[t0], %[nx]\n\t"
        "buffer_load_dwordx4 v[40:43], v117, %[sw], %[t0] offen offset:0 sc1\n\t"
        "s_waitcnt vmcnt(15) lgkmcnt(2)\n\t"
        "v_mfma_f32_16x16x32_bf16 v[20:23], v[88:91], v[60:63], v[20:23]\n\t"
        "ds_read_b128 v[88:91], v116 offset:12288\n\t"
        "v_mfma_f32_16x16x32_bf16 v[8:11], a[108:111], v[60:63], v[8:11]\n\t"
        "v_max3_u32 v125, v60, v61, v125\n\t"
        "v_mfma_f32_16x16x32_bf16 v[12:15], a[112:115], v[60:63], v[12:15]\n\t"
        "v_max3_u32 v125, v62, v63, v125\n\t"
        "v_mfma_f32_16x16x32_bf16 v[16:19], a[116:119], v[60:63], v[16:19]\n\t"
        "buffer_load_dwordx4 v[44:47], v117, %[sw], %[t0] offen offset:1024 sc1\n\t"
        "s_waitcnt vmcnt(11) lgkmcnt(2)\n\t"
        "v_mfma_f32_16x16x32_bf16 v[20:23], v[92:95], v[64:67], v[20:23]\n\t"
        "ds_read_b128 v[92:95], v116 offset:13312\n\t"
        "v_mfma_f32_16x16x32_bf16 v[8:11], a[120:123], v[64:67], v[8:11]\n\t"
        "v_max3_u32 v125, v64, v65, v125\n\t"
        "v_mfma_f32_16x16x32_bf16 v[12:15], a[124:127], v[64:67], v[12:15]\n\t"
        "v_max3_u32 v125, v66, v67, v125\n\t"
        "v_mfma_f32_16x16x32_bf16 v[16:19], a[128:131], v[64:67], v[16:19]\n\t"
        "buffer_load_dwordx4 v[48:51], v117, %[sw], %[t0] offen offset:2048 sc1\n\t"
        "s_waitcnt vmcnt(11) lgkmcnt(2)\n\t"
        "v_mfma_f32_16x16x32_bf16 v[20:23], v[96:99], v[68:71], v[20:23]\n\t"
        "ds_read_b128 v[96:99], v116 offset:14336\n\t"
        "v_mfma_f32_16x16x32_bf16 v[8:11], a[132:135], v[68:71], v[8:11]\n\t"
        "v_max3_u32 v125, v68, v69, v125\n\t"
        "v_mfma_f32_16x16x32_bf16 v[12:15], a[136:139], v[68:71], v[12:15]\n\t"
        "v_max3_u32 v125, v70, v71, v125\n\t"
        "v_mfma_f32_16x16x32_bf16 v[16:19], a[140:143], v[68:71], v[16:19]\n\t"
        "buffer_load_dwordx4 v[52:55], v117, %[sw], %[t0] offen offset:3072 sc1\n\t"
        "s_waitcnt vmcnt(11) lgkmcnt(2)\n\t"
        "v_mfma_f32_16x16x32_bf16 v[20:23], v[88:91], v[72:75], v[20:23]\n\t"
        "ds_read_b128 v[88:91], v116 offset:15360\n\t"
        "v_mfma_f32_16x16x32_bf16 v[8:11], a[144:147], v[72:75], v[8:11]\n\t"
        "v_max3_u32 v125, v72, v73, v125\n\t"
        "v_mfma_f32_16x16x32_bf16 v[12:15], a[148:151], v[72:75], v[12:15]\n\t"
        "v_max3_u32 v125, v74, v75, v125\n\t"
        "v_mfma_f32_16x16x32_bf16 v[16:19], a[152:155], v[72:75], v[16:19]\n\t"
        "s_add_u32 %[t0], %[ro], 6\n\t"
        "s_and_b32 %[t0], %[t0], 7\n\t"
        "s_lshl_b32 %[t0], %[t0], 12\n\t"
        "s_add_u32 %[t0], %[t0], %[nx]\n\t"
        "buffer_load_dwordx4 v[56:59], v117, %[sw], %[t0] offen offset:0 sc1\n\t"
        "s_waitcnt vmcnt(11) lgkmcnt(2)\n\t"
        "v_mfma_f32_16x16x32_bf16 v[20:23], v[92:95], v[76:79], v[20:23]\n\t"
        "ds_read_b128 v[92:95], v116 offset:16384\n\t"
        "v_mfma_f32_16x16x32_bf16 v[8:11], a[156:159], v[76:79], v[8:11]\n\t"
        "v_max3_u32 v125, v76, v77, v125\n\t"
        "v_mfma_f32_16x16x32_bf16 v[12:15], a[160:163], v[76:79], v[12:15]\n\t"
        "v_max3_u32 v125, v78, v79, v125\n\t"
        "v_mfma_f32_16x16x32_bf16 v[16:19], a[164:167], v[76:79], v[16:19]\n\t"
        "buffer_load_dwordx4 v[60:63], v117, %[sw], %[t0] offen offset:1024 sc1\n\t"
        "s_waitcnt vmcnt(11) lgkmcnt(2)\n\t"
        "v_mfma_f32_16x16x32_bf16 v[20:23], v[96:99], v[80:83], v[20:23]\n\t"
        "ds_read_b128 v[96:99], v116 offset:17408\n\t"
        "v_mfma_f32_16x16x32_bf16 v[8:11], a[168:171], v[80:83], v[8:11]\n\t"
        "v_max3_u32 v125, v80, v81, v125\n\t"
        "v_mfma_f32_16x16x32_bf16 v[12:15], a[172:175], v[80:83], v[12:15]\n\t"
        "v_max3_u32 v125, v82, v83, v125\n\t"
        "v_mfma_f32_16x16x32_bf16 v[16:19], a[176:179], v[80:83], v[16:19]\n\t"
        "buffer_load_dwordx4 v[64:67], v117, %[sw], %[t0] offen offset:2048 sc1\n\t"
        "s_waitcnt vmcnt(11) lgkmcnt(2)\n\t"
        "v_mfma_f32_16x16x32_bf16 v[20:23], v[88:91], v[84:87], v[20:23]\n\t"
        "ds_read_b128 v[88:91], v116 offset:18432\n\t"
        "v_mfma_f32_16x16x32_bf16 v[8:11], a[180:183], v[84:87], v[8:11]\n\t"
        "v_max3_u32 v125, v84, v85, v125\n\t"
        "v_mfma_f32_16x16x32_bf16 v[12:15], a[184:187], v[84:87], v[12:15]\n\t"
        "v_max3_u32 v125, v86, v87, v125\n\t"
        "v_mfma_f32_16x16x32_bf16 v[16:19], a[188:191], v[84:87], v[16:19]\n\t"
        "buffer_load_dwordx4 v[68:71], v117, %[sw], %[t0] offen offset:3072 sc1\n\t"
        "s_waitcnt vmcnt(11) lgkmcnt(2)\n\t"
        "v_mfma_f32_16x16x32_bf16 v[20:23], v[92:95], v[24:27], v[20:23]\n\t"
        "ds_read_b128 v[92:95], v116 offset:19456\n\t"
        "v_mfma_f32_16x16x32_bf16 v[8:11], a[192:195], v[24:27], v[8:11]\n\t"
        "v_max3_u32 v125, v24, v25, v125\n\t"
        "v_mfma_f32_16x16x32_bf16 v[12:15], a[196:199], v[24:27], v[12:15]\n\t"
        "v_max3_u32 v125, v26, v27, v125\n\t"
        "v_mfma_f32_16x16x32_bf16 v[16:19], a[200:203], v[24:27], v[16:19]\n\t"
        "s_add_u32 %[t0], %[ro], 7\n\t"
        "s_and_b32 %[t0], %[t0], 7\n\t"
        "s_lshl_b32 %[t0], %[t0], 12\n\t"
        "s_add_u32 %[t0], %[t0], %[nx]\n\t"
        "buffer_load_dwordx4 v[72:75], v117, %[sw], %[t0] offen offset:0 sc1\n\t"
        "s_waitcnt vmcnt(11) lgkmcnt(2)\n\t"
        "v_mfma_f32_16x16x32_bf16 v[20:23], v[96:99], v[28:31], v[20:23]\n\t"
        "ds_read_b128 v[96:99], v116 offset:20480\n\t"
        "v_mfma_f32_16x16x32_bf16 v[8:11], a[204:207], v[28:31], v[8:11]\n\t"
        "v_max3_u32 v125, v28, v29, v125\n\t"
        "v_mfma_f32_16x16x32_bf16 v[12:15], a[208:211], v[28:31], v[12:15]\n\t"
        "v_max3_u32 v125, v30, v31, v125\n\t"
        "v_sub_f32_e32 v104, 1.0, v102\n\t"
        "v_mfma_f32_16x16x32_bf16 v[16:19], a[212:215], v[28:31], v[16:19]\n\t"
        "buffer_load_dwordx4 v[76:79], v117, %[sw], %[t0] offen offset:1024 sc1\n\t"
        "s_waitcnt vmcnt(11) lgkmcnt(2)\n\t"
        "v_mfma_f32_16x16x32_bf16 v[20:23], v[88:91], v[32:35], v[20:23]\n\t"
        "ds_read_b128 v[88:91], v116 offset:21504\n\t"
        "v_mfma_f32_16x16x32_bf16 v[8:11], a[216:219], v[32:35], v[8:11]\n\t"
        "v_max3_u32 v125, v32, v33, v125\n\t"
        "v_mfma_f32_16x16x32_bf16 v[12:15], a[220:223], v[32:35], v[12:15]\n\t"
        "v_max3_u32 v125, v34, v35, v125\n\t"
        "v_cvt_pk_bf16_f32 v104, 0, v104\n\t"
        "v_mfma_f32_16x16x32_bf16 v[16:19], a[224:227], v[32:35], v[16:19]\n\t"
        "buffer_load_dwordx4 v[80:83], v117, %[sw], %[t0] offen offset:2048 sc1\n\t"
        "s_waitcnt vmcnt(11) lgkmcnt(2)\n\t"
        "v_mfma_f32_16x16x32_bf16 v[20:23], v[92:95], v[36:39], v[20:23]\n\t"
        "ds_read_b128 v[92:95], v116 offset:22528\n\t"
        "v_mfma_f32_16x16x32_bf16 v[8:11], a[228:231], v[36:39], v[8:11]\n\t"
        "v_max3_u32 v125, v36, v37, v125\n\t"
        "v_mfma_f32_16x16x32_bf16 v[12:15], a[232:235], v[36:39], v[12:15]\n\t"
        "v_max3_u32 v125, v38, v39, v125\n\t"
        "v_lshlrev_b32_e32 v106, 16, v100\n\t"
        "v_mfma_f32_16x16x32_bf16 v[16:19], a[236:239], v[36:39], v[16:19]\n\t"
        "buffer_load_dwordx4 v[84:87], v117, %[sw], %[t0] offen offset:3072 sc1\n\t"
        "s_waitcnt vmcnt(11) lgkmcnt(2)\n\t"
        "v_mfma_f32_16x16x32_bf16 v[20:23], v[96:99], v[40:43], v[20:23]\n\t"
        "ds_read_b128 v[96:99], v116 offset:23552\n\t"
        "v_mfma_f32_16x16x32_bf16 v[8:11], a[240:243], v[40:43], v[8:11]\n\t"
        "v_max3_u32 v125, v40, v41, v125\n\t"
        "v_mfma_f32_16x16x32_bf16 v[12:15], a[244:247], v[40:43], v[12:15]\n\t"
        "v_max3_u32 v125, v42, v43, v125\n\t"
        "v_mul_f32_e32 v106, v102, v106\n\t"
        "v_mfma_f32_16x16x32_bf16 v[16:19], a[248:251], v[40:43], v[16:19]\n\t"
        "s_waitcnt vmcnt(10) lgkmcnt(2)\n\t"
        "v_mfma_f32_16x16x32_bf16 v[20:23], v[88:91], v[44:47], v[20:23]\n\t"
        "ds_read_b128 v[88:91], v116 offset:24576\n\t"
        "v_mfma_f32_16x16x32_bf16 v[8:11], a[252:255], v[44:47], v[8:11]\n\t"
        "v_max3_u32 v125, v44, v45, v125\n\t"
        "v_mfma_f32_16x16x32_bf16 v[12:15], v[128:131], v[44:47], v[12:15]\n\t"
        "v_max3_u32 v125, v46, v47, v125\n\t"
        "v_cvt_pk_bf16_f32 v106, 0, v106\n\t"
        "v_mfma_f32_16x16x32_bf16 v[16:19], v[132:135], v[44:47], v[16:19]\n\t"
        "s_waitcnt vmcnt(9) lgkmcnt(2)\n\t"
        "v_mfma_f32_16x16x32_bf16 v[20:23], v[92:95], v[48:51], v[20:23]\n\t"
        "ds_read_b128 v[92:95], v116 offset:25600\n\t"
        "v_mfma_f32_16x16x32_bf16 v[8:11], v[136:139], v[48:51], v[8:11]\n\t"
        "v_max3_u32 v125, v48, v49, v125\n\t"
        "v_mfma_f32_16x16x32_bf16 v[12:15], v[140:143], v[48:51], v[12:15]\n\t"
        "v_max3_u32 v125, v50, v51, v125\n\t"
        "v_and_b32_e32 v107, 0xffff0000, v100\n\t"
        "v_mfma_f32_16x16x32_bf16 v[16:19], v[144:147], v[48:51], v[16:19]\n\t"
        "s_waitcnt vmcnt(8) lgkmcnt(2)\n\t"
        "v_mfma_f32_16x16x32_bf16 v[20:23], v[96:99], v[52:55], v[20:23]\n\t"
        "ds_read_b128 v[96:99], v116 offset:26624\n\t"
        "v_mfma_f32_16x16x32_bf16 v[8:11], v[148:151], v[52:55], v[8:11]\n\t"
        "v_max3_u32 v125, v52, v53, v125\n\t"
        "v_mfma_f32_16x16x32_bf16 v[12:15], v[152:155], v[52:55], v[12:15]\n\t"
        "v_max3_u32 v125, v54, v55, v125\n\t"
        "v_mul_f32_e32 v107, v102, v107\n\t"
        "v_mfma_f32_16x16x32_bf16 v[16:19], v[156:159], v[52:55], v[16:19]\n\t"
        "s_waitcnt vmcnt(7) lgkmcnt(2)\n\t"
        "v_mfma_f32_16x16x32_bf16 v[20:23], v[88:91], v[56:59], v[20:23]\n\t"
        "ds_read_b128 v[88:91], v116 offset:27648\n\t"
        "v_mfma_f32_16x16x32_bf16 v[8:11], v[160:163], v[56:59], v[8:11]\n\t"
        "v_max3_u32 v125, v56, v57, v125\n\t"
        "v_mfma_f32_16x16x32_bf16 v[12:15], v[164:167], v[56:59], v[12:15]\n\t"
        "v_max3_u32 v125, v58, v59, v125\n\t"
        "v_cvt_pk_bf16_f32 v107, 0, v107\n\t"
        "v_mfma_f32_16x16x32_bf16 v[16:19], v[168:171], v[56:59], v[16:19]\n\t"
        "s_waitcnt vmcnt(6) lgkmcnt(2)\n\t"
        "v_mfma_f32_16x16x32_bf16 v[20:23], v[92:95], v[60:63], v[20:23]\n\t"
        "ds_read_b128 v[92:95], v116 offset:28672\n\t"
        "v_mfma_f32_16x16x32_bf16 v[8:11], v[172:175], v[60:63], v[8:11]\n\t"
        "v_max3_u32 v125, v60, v61, v125\n\t"
        "v_mfma_f32_16x16x32_bf16 v[12:15], v[176:179], v[60:63], v[12:15]\n\t"
        "v_max3_u32 v125, v62, v63, v125\n\t"
        "v_lshlrev_b32_e32 v108, 16, v101\n\t"
        "v_mfma_f32_16x16x32_bf16 v[16:19], v[180:183], v[60:63], v[16:19]\n\t"
        "s_waitcnt vmcnt(5) lgkmcnt(2)\n\t"
        "v_mfma_f32_16x16x32_bf16 v[20:23], v[96:99], v[64:67], v[20:23]\n\t"
        "ds_read_b128 v[96:99], v116 offset:29696\n\t"
        "v_mfma_f32_16x16x32_bf16 v[8:11], v[184:187], v[64:67], v[8:11]\n\t"
        "v_max3_u32 v125, v64, v65, v125\n\t"
        "v_mfma_f32_16x16x32_bf16 v[12:15], v[188:191], v[64:67], v[12:15]\n\t"
        "v_max3_u32 v125, v66, v67, v125\n\t"
        "v_mul_f32_e32 v108, v102, v108\n\t"
        "v_mfma_f32_16x16x32_bf16 v[16:19], v[192:195], v[64:67], v[16:19]\n\t"
        "s_waitcnt vmcnt(4) lgkmcnt(2)\n\t"
        "v_mfma_f32_16x16x32_bf16 v[20:23], v[88:91], v[68:71], v[20:23]\n\t"
        "ds_read_b128 v[88:91], v116 offset:30720\n\t"
        "v_mfma_f32_16x16x32_bf16 v[8:11], v[196:199], v[68:71], v[8:11]\n\t"
        "v_max3_u32 v125, v68, v69, v125\n\t"
        "v_mfma_f32_16x16x32_bf16 v[12:15], v[200:203], v[68:71], v[12:15]\n\t"
        "v_max3_u32 v125, v70, v71, v125\n\t"
        "v_cvt_pk_bf16_f32 v108, 0, v108\n\t"
        "v_mfma_f32_16x16x32_bf16 v[16:19], v[204:207], v[68:71], v[16:19]\n\t"
        "s_waitcnt vmcnt(3) lgkmcnt(2)\n\t"
        "v_mfma_f32_16x16x32_bf16 v[20:23], v[92:95], v[72:75], v[20:23]\n\t"
        "ds_read_b128 v[92:95], v116 offset:31744\n\t"
        "v_mfma_f32_16x16x32_bf16 v[8:11], v[208:211], v[72:75], v[8:11]\n\t"
        "v_max3_u32 v125, v72, v73, v125\n\t"
        "v_mfma_f32_16x16x32_bf16 v[12:15], v[212:215], v[72:75], v[12:15]\n\t"
        "v_max3_u32 v125, v74, v75, v125\n\t"
        "v_and_b32_e32 v109, 0xffff0000, v101\n\t"
        "v_mfma_f32_16x16x32_bf16 v[16:19], v[216:219], v[72:75], v[16:19]\n\t"
        "s_waitcnt vmcnt(2) lgkmcnt(2)\n\t"
        "v_mfma_f32_16x16x32_bf16 v[20:23], v[96:99], v[76:79], v[20:23]\n\t"
        "v_mfma_f32_16x16x32_bf16 v[8:11], v[220:223], v[76:79], v[8:11]\n\t"
        "v_max3_u32 v125, v76, v77, v125\n\t"
        "v_mfma_f32_16x16x32_bf16 v[12:15], v[224:227], v[76:79], v[12:15]\n\t"
        "v_max3_u32 v125, v78, v79, v125\n\t"
        "v_mul_f32_e32 v109, v102, v109\n\t"
        "v_mfma_f32_16x16x32_bf16 v[16:19], v[228:231], v[76:79], v[16:19]\n\t"
        "s_waitcnt vmcnt(1) lgkmcnt(1)\n\t"
        "v_mfma_f32_16x16x32_bf16 v[20:23], v[88:91], v[80:83], v[20:23]\n\t"
        "ds_read_b128 v[88:91], v116 offset:0\n\t"
        "v_mfma_f32_16x16x32_bf16 v[8:11], v[232:235], v[80:83], v[8:11]\n\t"
        "v_max3_u32 v125, v80, v81, v125\n\t"
        "v_mfma_f32_16x16x32_bf16 v[12:15], v[236:239], v[80:83], v[12:15]\n\t"
        "v_max3_u32 v125, v82, v83, v125\n\t"
        "v_cvt_pk_bf16_f32 v109, 0, v109\n\t"
        "v_mfma_f32_16x16x32_bf16 v[16:19], v[240:243], v[80:83], v[16:19]\n\t"
        "s_waitcnt vmcnt(0) lgkmcnt(1)\n\t"
        "v_mfma_f32_16x16x32_bf16 v[20:23], v[92:95], v[84:87], v[20:23]\n\t"
        "ds_read_b128 v[92:95], v116 offset:1024\n\t"
        "v_mfma_f32_16x16x32_bf16 v[8:11], v[244:247], v[84:87], v[8:11]\n\t"
        "ds_read_b128 v[96:99], v116 offset:2048\n\t"
        "v_max3_u32 v125, v84, v85, v125\n\t"
        "v_mfma_f32_16x16x32_bf16 v[12:15], v[248:251], v[84:87], v[12:15]\n\t"
        "v_max3_u32 v125, v86, v87, v125\n\t"
        "v_mfma_f32_16x16x32_bf16 v[16:19], v[252:255], v[84:87], v[16:19]\n\t"
        "v_cmp_eq_u32_e32 vcc, -1, v125\n\t"
        "s_mov_b32 %[nx], 0xf0000000\n\t"
        "s_mov_b32 %[re], 64\n\t"
        "s_cbranch_vccz LP_epi%=\n\t"
        "s_waitcnt lgkmcnt(0)\n\t"
        "s_branch LP_again%=\n\t"
        "LP_run%=:\n\t"
        "s_add_u32 %[t0], %[ro], 0\n\t"
        "s_and_b32 %[t0], %[t0], 7\n\t"
        "s_lshl_b32 %[t0], %[t0], 12\n\t"
        "s_add_u32 %[t0], %[t0], %[nx]\n\t"
        "buffer_load_dwordx4 v[24:27], v117, %[sw], %[t0] offen offset:0 sc1\n\t"
        "buffer_load_dwordx4 v[28:31], v117, %[sw], %[t0] offen offset:1024 sc1\n\t"
        "buffer_load_dwordx4 v[32:35], v117, %[sw], %[t0] offen offset:2048 sc1\n\t"
        "buffer_load_dwordx4 v[36:39], v117, %[sw], %[t0] offen offset:3072 sc1\n\t"
        "s_add_u32 %[t0], %[ro], 1\n\t"
        "s_and_b32 %[t0], %[t0], 7\n\t"
        "s_lshl_b32 %[t0], %[t0], 12\n\t"
        "s_add_u32 %[t0], %[t0], %[nx]\n\t"
        "buffer_load_dwordx4 v[40:43], v117, %[sw], %[t0] offen offset:0 sc1\n\t"
        "buffer_load_dwordx4 v[44:47], v117, %[sw], %[t0] offen offset:1024 sc1\n\t"
        "buffer_load_dwordx4 v[48:51], v117, %[sw], %[t0] offen offset:2048 sc1\n\t"
        "buffer_load_dwordx4 v[52:55], v117, %[sw], %[t0] offen offset:3072 sc1\n\t"
        "s_add_u32 %[t0], %[ro], 2\n\t"
        "s_and_b32 %[t0], %[t0], 7\n\t"
        "s_lshl_b32 %[t0], %[t0], 12\n\t"
        "s_add_u32 %[t0], %[t0], %[nx]\n\t"
        "buffer_load_dwordx4 v[56:59], v117, %[sw], %[t0] offen offset:0 sc1\n\t"
        "buffer_load_dwordx4 v[60:63], v117, %[sw], %[t0] offen offset:1024 sc1\n\t"
        "buffer_load_dwordx4 v[64:67], v117, %[sw], %[t0] offen offset:2048 sc1\n\t"
        "buffer_load_dwordx4 v[68:71], v117, %[sw], %[t0] offen offset:3072 sc1\n\t"
        "s_add_u32 %[t0], %[ro], 3\n\t"
        "s_and_b32 %[t0], %[t0], 7\n\t"
        "s_lshl_b32 %[t0], %[t0], 12\n\t"
        "s_add_u32 %[t0], %[t0], %[nx]\n\t"
        "buffer_load_dwordx4 v[72:75], v117, %[sw], %[t0] offen offset:0 sc1\n\t"
        "buffer_load_dwordx4 v[76:79], v117, %[sw], %[t0] offen offset:1024 sc1\n\t"
        "buffer_load_dwordx4 v[80:83], v117, %[sw], %[t0] offen offset:2048 sc1\n\t"
        "buffer_load_dwordx4 v[84:87], v117, %[sw], %[t0] offen offset:3072 sc1\n\t"
        "buffer_load_dword v103, v118, %[sw], 0 offen sc1\n\t"
        "LP_tile%=:\n\t"
        "s_and_b32 %[sp], %[tl], 1\n\t"
        "s_lshl_b32 %[sp], %[sp], 14\n\t"
        "s_lshr_b32 %[t1], %[tl], 1\n\t"
        "s_lshl_b32 %[t1], %[t1], 6\n\t"
        "s_add_u32 %[t1], %[sp], %[t1]\n\t"
        "buffer_load_dword v100, v119, %[sw], %[t1] offen sc1\n\t"
        "s_add_u32 %[t1], %[t1], 32768\n\t"
        "buffer_load_dword v101, v119, %[sw], %[t1] offen sc1\n\t"
        "s_lshl_b32 %[t1], %[tl], 6\n\t"
        "buffer_load_dword v102, v120, %[sw], %[t1] offen sc1\n\t"
        "buffer_load_dword v103, v118, %[sw], 0 offen sc1\n\t"
        "s_mov_b32 %[cu], %[nx]\n\t"
        "s_waitcnt vmcnt(20) lgkmcnt(2)\n\t"
        "v_mfma_f32_16x16x32_bf16 v[20:23], v[88:91], v[24:27], 0\n\t"
        "ds_read_b128 v[88:91], v116 offset:3072\n\t"
        "v_mfma_f32_16x16x32_bf16 v[8:11], a[0:3], v[24:27], 0\n\t"
        "v_mfma_f32_16x16x32_bf16 v[12:15], a[4:7], v[24:27], 0\n\t"
        "v_mfma_f32_16x16x32_bf16 v[16:19], a[8:11], v[24:27], 0\n\t"
        "s_add_u32 %[t0], %[ro], 4\n\t"
        "s_and_b32 %[t0], %[t0], 7\n\t"
        "s_lshl_b32 %[t0], %[t0], 12\n\t"
        "s_add_u32 %[t0], %[t0], %[cu]\n\t"
        "buffer_load_dwordx4 v[24:27], v117, %[sw], %[t0] offen offset:0 sc1\n\t"
        "s_waitcnt vmcnt(20) lgkmcnt(2)\n\t"
        "v_mfma_f32_16x16x32_bf16 v[20:23], v[92:95], v[28:31], v[20:23]\n\t"
        "ds_read_b128 v[92:95], v116 offset:4096\n\t"
        "v_mfma_f32_16x16x32_bf16 v[8:11], a[12:15], v[28:31], v[8:11]\n\t"
        "v_mfma_f32_16x16x32_bf16 v[12:15], a[16:19], v[28:31], v[12:15]\n\t"
        "v_mfma_f32_16x16x32_bf16 v[16:19], a[20:23], v[28:31], v[16:19]\n\t"
        "buffer_load_dwordx4 v[28:31], v117, %[sw], %[t0] offen offset:1024 sc1\n\t"
        "s_waitcnt vmcnt(20) lgkmcnt(2)\n\t"
        "v_mfma_f32_16x16x32_bf16 v[20:23], v[96:99], v[32:35], v[20:23]\n\t"
        "ds_read_b128 v[96:99], v116 offset:5120\n\t"
        "v_mfma_f32_16x16x32_bf16 v[8:11], a[24:27], v[32:35], v[8:11]\n\t"
        "v_mfma_f32_16x16x32_bf16 v[12:15], a[28:31], v[32:35], v[12:15]\n\t"
        "v_mfma_f32_16x16x32_bf16 v[16:19], a[32:35], v[32:35], v[16:19]\n\t"
        "buffer_load_dwordx4 v[32:35], v117, %[sw], %[t0] offen offset:2048 sc1\n\t"
        "s_waitcnt vmcnt(20) lgkmcnt(2)\n\t"
        "v_mfma_f32_16x16x32_bf16 v[20:23], v[88:91], v[36:39], v[20:23]\n\t"
        "ds_read_b128 v[88:91], v116 offset:6144\n\t"
        "v_mfma_f32_16x16x32_bf16 v[8:11], a[36:39], v[36:39], v[8:11]\n\t"
        "v_mfma_f32_16x16x32_bf16 v[12:15], a[40:43], v[36:39], v[12:15]\n\t"
        "v_mfma_f32_16x16x32_bf16 v[16:19], a[44:47], v[36:39], v[16:19]\n\t"
        "buffer_load_dwordx4 v[36:39], v117, %[sw], %[t0] offen offset:3072 sc1\n\t"
        "s_waitcnt vmcnt(20) lgkmcnt(2)\n\t"
        "v_mfma_f32_16x16x32_bf16 v[20:23], v[92:95], v[40:43], v[20:23]\n\t"
        "ds_read_b128 v[92:95], v116 offset:7168\n\t"
        "v_mfma_f32_16x16x32_bf16 v[8:11], a[48:51], v[40:43], v[8:11]\n\t"
        "v_mfma_f32_16x16x32_bf16 v[12:15], a[52:55], v[40:43], v[12:15]\n\t"
        "v_mfma_f32_16x16x32_bf16 v[16:19], a[56:59], v[40:43], v[16:19]\n\t"
        "s_add_u32 %[t0], %[ro], 5\n\t"
        "s_and_b32 %[t0], %[t0], 7\n\t"
        "s_lshl_b32 %[t0], %[t0], 12\n\t"
        "s_add_u32 %[t0], %[t0], %[cu]\n\t"
        "buffer_load_dwordx4 v[40:43], v117, %[sw], %[t0] offen offset:0 sc1\n\t"
        "s_waitcnt vmcnt(20) lgkmcnt(2)\n\t"
        "v_mfma_f32_16x16x32_bf16 v[20:23], v[96:99], v[44:47], v[20:23]\n\t"
        "ds_read_b128 v[96:99], v116 offset:8192\n\t"
        "v_mfma_f32_16x16x32_bf16 v[8:11], a[60:63], v[44:47], v[8:11]\n\t"
        "v_mfma_f32_16x16x32_bf16 v[12:15], a[64:67], v[44:47], v[12:15]\n\t"
        "v_mfma_f32_16x16x32_bf16 v[16:19], a[68:71], v[44:47], v[16:19]\n\t"
        "buffer_load_dwordx4 v[44:47], v117, %[sw], %[t0] offen offset:1024 sc1\n\t"
        "s_waitcnt vmcnt(20) lgkmcnt(2)\n\t"
        "v_mfma_f32_16x16x32_bf16 v[20:23], v[88:91], v[48:51], v[20:23]\n\t"
        "ds_read_b128 v[88:91], v116 offset:9216\n\t"
        "v_mfma_f32_16x16x32_bf16 v[8:11], a[72:75], v[48:51], v[8:11]\n\t"
        "v_mfma_f32_16x16x32_bf16 v[12:15], a[76:79], v[48:51], v[12:15]\n\t"
        "v_mfma_f32_16x16x32_bf16 v[16:19], a[80:83], v[48:51], v[16:19]\n\t"
        "buffer_load_dwordx4 v[48:51], v117, %[sw], %[t0] offen offset:2048 sc1\n\t"
        "s_waitcnt vmcnt(20) lgkmcnt(2)\n\t"
        "v_mfma_f32_16x16x32_bf16 v[20:23], v[92:95], v[52:55], v[20:23]\n\t"
        "ds_read_b128 v[92:95], v116 offset:10240\n\t"
        "v_mfma_f32_16x16x32_bf16 v[8:11], a[84:87], v[52:55], v[8:11]\n\t"
        "v_mfma_f32_16x16x32_bf16 v[12:15], a[88:91], v[52:55], v[12:15]\n\t"
        "v_mfma_f32_16x16x32_bf16 v[16:19], a[92:95], v[52:55], v[16:19]\n\t"
        "buffer_load_dwordx4 v[52:55], v117, %[sw], %[t0] offen offset:3072 sc1\n\t"
        "s_waitcnt vmcnt(20) lgkmcnt(2)\n\t"
        "s_barrier\n\t"
        "v_mfma_f32_16x16x32_bf16 v[20:23], v[96:99], v[56:59], v[20:23]\n\t"
        "ds_read_b128 v[96:99], v116 offset:11264\n\t"
        "v_mfma_f32_16x16x32_bf16 v[8:11], a[96:99], v[56:59], v[8:11]\n\t"
        "v_mfma_f32_16x16x32_bf16 v[12:15], a[100:103], v[56:59], v[12:15]\n\t"
        "v_mfma_f32_16x16x32_bf16 v[16:19], a[104:107], v[56:59], v[16:19]\n\t"
        "s_add_u32 %[t0], %[ro], 6\n\t"
        "s_and_b32 %[t0], %[t0], 7\n\t"
        "s_lshl_b32 %[t0], %[t0], 12\n\t"
        "s_add_u32 %[t0], %[t0], %[cu]\n\t"
        "buffer_load_dwordx4 v[56:59], v117, %[sw], %[t0] offen offset:0 sc1\n\t"
        "s_waitcnt vmcnt(20) lgkmcnt(2)\n\t"
        "v_mfma_f32_16x16x32_bf16 v[20:23], v[88:91], v[60:63], v[20:23]\n\t"
        "ds_read_b128 v[88:91], v116 offset:12288\n\t"
        "v_mfma_f32_16x16x32_bf16 v[8:11], a[108:111], v[60:63], v[8:11]\n\t"
        "v_mfma_f32_16x16x32_bf16 v[12:15], a[112:115], v[60:63], v[12:15]\n\t"
        "v_mfma_f32_16x16x32_bf16 v[16:19], a[116:119], v[60:63], v[16:19]\n\t"
        "buffer_load_dwordx4 v[60:63], v117, %[sw], %[t0] offen offset:1024 sc1\n\t"
        "s_waitcnt vmcnt(20) lgkmcnt(2)\n\t"
        "v_mfma_f32_16x16x32_bf16 v[20:23], v[92:95], v[64:67], v[20:23]\n\t"
        "ds_read_b128 v[92:95], v116 offset:13312\n\t"
        "v_mfma_f32_16x16x32_bf16 v[8:11], a[120:123], v[64:67], v[8:11]\n\t"
        "v_mfma_f32_16x16x32_bf16 v[12:15], a[124:127], v[64:67], v[12:15]\n\t"
        "v_mfma_f32_16x16x32_bf16 v[16:19], a[128:131], v[64:67], v[16:19]\n\t"
        "buffer_load_dwordx4 v[64:67], v117, %[sw], %[t0] offen offset:2048 sc1\n\t"
        "s_waitcnt vmcnt(20) lgkmcnt(2)\n\t"
        "v_mfma_f32_16x16x32_bf16 v[20:23], v[96:99], v[68:71], v[20:23]\n\t"
        "ds_read_b128 v[96:99], v116 offset:14336\n\t"
        "v_mfma_f32_16x16x32_bf16 v[8:11], a[132:135], v[68:71], v[8:11]\n\t"
        "v_mfma_f32_16x16x32_bf16 v[12:15], a[136:139], v[68:71], v[12:15]\n\t"
        "v_mfma_f32_16x16x32_bf16 v[16:19], a[140:143], v[68:71], v[16:19]\n\t"
        "buffer_load_dwordx4 v[68:71], v117, %[sw], %[t0] offen offset:3072 sc1\n\t"
        "s_waitcnt vmcnt(20) lgkmcnt(2)\n\t"
        "v_mfma_f32_16x16x32_bf16 v[20:23], v[88:91], v[72:75], v[20:23]\n\t"
        "ds_read_b128 v[88:91], v116 offset:15360\n\t"
        "v_mfma_f32_16x16x32_bf16 v[8:11], a[144:147], v[72:75], v[8:11]\n\t"
        "v_mfma_f32_16x16x32_bf16 v[12:15], a[148:151], v[72:75], v[12:15]\n\t"
        "v_mfma_f32_16x16x32_bf16 v[16:19], a[152:155], v[72:75], v[16:19]\n\t"
        "s_add_u32 %[t0], %[ro], 7\n\t"
        "s_and_b32 %[t0], %[t0], 7\n\t"
        "s_lshl_b32 %[t0], %[t0], 12\n\t"
        "s_add_u32 %[t0], %[t0], %[cu]\n\t"
        "buffer_load_dwordx4 v[72:75], v117, %[sw], %[t0] offen offset:0 sc1\n\t"
        "s_waitcnt vmcnt(13) lgkmcnt(2)\n\t"
        "v_mfma_f32_16x16x32_bf16 v[20:23], v[92:95], v[76:79], v[20:23]\n\t"
        "ds_read_b128 v[92:95], v116 offset:16384\n\t"
        "v_min_u32_dpp v103, v103, v103 quad_perm:[1,0,3,2] row_mask:0xf bank_mask:0xf\n\t"
        "s_nop 0\n\t"
        "v_mfma_f32_16x16x32_bf16 v[8:11], a[156:159], v[76:79], v[8:11]\n\t"
        "v_min_u32_dpp v103, v103, v103 quad_perm:[2,3,0,1] row_mask:0xf bank_mask:0xf\n\t"
        "s_nop 0\n\t"
        "v_mfma_f32_16x16x32_bf16 v[12:15], a[160:163], v[76:79], v[12:15]\n\t"
        "v_min_u32_dpp v103, v103, v103 row_ror:4 row_mask:0xf bank_mask:0xf\n\t"
        "s_nop 0\n\t"
        "v_mfma_f32_16x16x32_bf16 v[16:19], a[164:167], v[76:79], v[16:19]\n\t"
        "buffer_load_dwordx4 v[76:79], v117, %[sw], %[t0] offen offset:1024 sc1\n\t"
        "s_waitcnt vmcnt(20) lgkmcnt(2)\n\t"
        "v_mfma_f32_16x16x32_bf16 v[20:23], v[96:99], v[80:83], v[20:23]\n\t"
        "ds_read_b128 v[96:99], v116 offset:17408\n\t"
        "v_readfirstlane_b32 %[pg], v103\n\t"
        "s_add_u32 %[t1], %[tl], 8\n\t"
        "s_lshl_b32 %[nx], %[t1], 17\n\t"
        "s_lshr_b32 %[t1], %[t1], 1\n\t"
        "v_mfma_f32_16x16x32_bf16 v[8:11], a[168:171], v[80:83], v[8:11]\n\t"
        "s_cmp_gt_u32 %[pg], %[t1]\n\t"
        "s_cselect_b32 %[nx], %[nx], 0xf0000000\n\t"
        "v_mfma_f32_16x16x32_bf16 v[12:15], a[172:175], v[80:83], v[12:15]\n\t"
        "v_mfma_f32_16x16x32_bf16 v[16:19], a[176:179], v[80:83], v[16:19]\n\t"
        "buffer_load_dwordx4 v[80:83], v117, %[sw], %[t0] offen offset:2048 sc1\n\t"
        "s_waitcnt vmcnt(20) lgkmcnt(2)\n\t"
        "v_mfma_f32_16x16x32_bf16 v[20:23], v[88:91], v[84:87], v[20:23]\n\t"
        "ds_read_b128 v[88:91], v116 offset:18432\n\t"
        "v_mfma_f32_16x16x32_bf16 v[8:11], a[180:183], v[84:87], v[8:11]\n\t"
        "v_mfma_f32_16x16x32_bf16 v[12:15], a[184:187], v[84:87], v[12:15]\n\t"
        "v_mfma_f32_16x16x32_bf16 v[16:19], a[188:191], v[84:87], v[16:19]\n\t"
        "buffer_load_dwordx4 v[84:87], v117, %[sw], %[t0] offen offset:3072 sc1\n\t"
        "s_waitcnt vmcnt(15) lgkmcnt(2)\n\t"
        "v_mfma_f32_16x16x32_bf16 v[20:23], v[92:95], v[24:27], v[20:23]\n\t"
        "ds_read_b128 v[92:95], v116 offset:19456\n\t"
        "v_mfma_f32_16x16x32_bf16 v[8:11], a[192:195], v[24:27], v[8:11]\n\t"
        "v_mfma_f32_16x16x32_bf16 v[12:15], a[196:199], v[24:27], v[12:15]\n\t"
        "v_mfma_f32_16x16x32_bf16 v[16:19], a[200:203], v[24:27], v[16:19]\n\t"
        "s_add_u32 %[t0], %[ro], 0\n\t"
        "s_and_b32 %[t0], %[t0], 7\n\t"
        "s_lshl_b32 %[t0], %[t0], 12\n\t"
        "s_add_u32 %[t0], %[t0], %[nx]\n\t"
        "buffer_load_dwordx4 v[24:27], v117, %[sw], %[t0] offen offset:0 sc1\n\t"
        "s_waitcnt vmcnt(15) lgkmcnt(2)\n\t"
        "v_mfma_f32_16x16x32_bf16 v[20:23], v[96:99], v[28:31], v[20:23]\n\t"
        "ds_read_b128 v[96:99], v116 offset:20480\n\t"
        "v_mfma_f32_16x16x32_bf16 v[8:11], a[204:207], v[28:31], v[8:11]\n\t"
        "v_mfma_f32_16x16x32_bf16 v[12:15], a[208:211], v[28:31], v[12:15]\n\t"
        "v_sub_f32_e32 v104, 1.0, v102\n\t"
        "v_mfma_f32_16x16x32_bf16 v[16:19], a[212:215], v[28:31], v[16:19]\n\t"
        "buffer_load_dwordx4 v[28:31], v117, %[sw], %[t0] offen offset:1024 sc1\n\t"
        "s_waitcnt vmcnt(15) lgkmcnt(2)\n\t"
        "v_mfma_f32_16x16x32_bf16 v[20:23], v[88:91], v[32:35], v[20:23]\n\t"
        "ds_read_b128 v[88:91], v116 offset:21504\n\t"
        "v_mfma_f32_16x16x32_bf16 v[8:11], a[216:219], v[32:35], v[8:11]\n\t"
        "v_mfma_f32_16x16x32_bf16 v[12:15], a[220:223], v[32:35], v[12:15]\n\t"
        "v_cvt_pk_bf16_f32 v104, 0, v104\n\t"
        "v_mfma_f32_16x16x32_bf16 v[16:19], a[224:227], v[32:35], v[16:19]\n\t"
        "buffer_load_dwordx4 v[32:35], v117, %[sw], %[t0] offen offset:2048 sc1\n\t"
        "s_waitcnt vmcnt(15) lgkmcnt(2)\n\t"
        "v_mfma_f32_16x16x32_bf16 v[20:23], v[92:95], v[36:39], v[20:23]\n\t"
        "ds_read_b128 v[92:95], v116 offset:22528\n\t"
        "v_mfma_f32_16x16x32_bf16 v[8:11], a[228:231], v[36:39], v[8:11]\n\t"
        "v_mfma_f32_16x16x32_bf16 v[12:15], a[232:235], v[36:39], v[12:15]\n\t"
        "v_lshlrev_b32_e32 v106, 16, v100\n\t"
        "v_mfma_f32_16x16x32_bf16 v[16:19], a[236:239], v[36:39], v[16:19]\n\t"
        "buffer_load_dwordx4 v[36:39], v117, %[sw], %[t0] offen offset:3072 sc1\n\t"
        "s_waitcnt vmcnt(15) lgkmcnt(2)\n\t"
        "v_mfma_f32_16x16x32_bf16 v[20:23], v[96:99], v[40:43], v[20:23]\n\t"
        "ds_read_b128 v[96:99], v116 offset:23552\n\t"
        "v_mfma_f32_16x16x32_bf16 v[8:11], a[240:243], v[40:43], v[8:11]\n\t"
        "v_mfma_f32_16x16x32_bf16 v[12:15], a[244:247], v[40:43], v[12:15]\n\t"
        "v_mul_f32_e32 v106, v102, v106\n\t"
        "v_mfma_f32_16x16x32_bf16 v[16:19], a[248:251], v[40:43], v[16:19]\n\t"
        "s_add_u32 %[t0], %[ro], 1\n\t"
        "s_and_b32 %[t0], %[t0], 7\n\t"
        "s_lshl_b32 %[t0], %[t0], 12\n\t"
        "s_add_u32 %[t0], %[t0], %[nx]\n\t"
        "buffer_load_dwordx4 v[40:43], v117, %[sw], %[t0] offen offset:0 sc1\n\t"
        "s_waitcnt vmcnt(15) lgkmcnt(2)\n\t"
        "v_mfma_f32_16x16x32_bf16 v[20:23], v[88:91], v[44:47], v[20:23]\n\t"
        "ds_read_b128 v[88:91], v116 offset:24576\n\t"
        "v_mfma_f32_16x16x32_bf16 v[8:11], a[252:255], v[44:47], v[8:11]\n\t"
        "v_mfma_f32_16x16x32_bf16 v[12:15], v[128:131], v[44:47], v[12:15]\n\t"
        "v_cvt_pk_bf16_f32 v106, 0, v106\n\t"
        "v_mfma_f32_16x16x32_bf16 v[16:19], v[132:135], v[44:47], v[16:19]\n\t"
        "buffer_load_dwordx4 v[44:47], v117, %[sw], %[t0] offen offset:1024 sc1\n\t"
        "s_waitcnt vmcnt(15) lgkmcnt(2)\n\t"
        "v_mfma_f32_16x16x32_bf16 v[20:23], v[92:95], v[48:51], v[20:23]\n\t"
        "ds_read_b128 v[92:95], v116 offset:25600\n\t"
        "v_mfma_f32_16x16x32_bf16 v[8:11], v[136:139], v[48:51], v[8:11]\n\t"
        "v_mfma_f32_16x16x32_bf16 v[12:15], v[140:143], v[48:51], v[12:15]\n\t"
        "v_and_b32_e32 v107, 0xffff0000, v100\n\t"
        "v_mfma_f32_16x16x32_bf16 v[16:19], v[144:147], v[48:51], v[16:19]\n\t"
        "buffer_load_dwordx4 v[48:51], v117, %[sw], %[t0] offen offset:2048 sc1\n\t"
        "s_waitcnt vmcnt(15) lgkmcnt(2)\n\t"
        "v_mfma_f32_16x16x32_bf16 v[20:23], v[96:99], v[52:55], v[20:23]\n\t"
        "ds_read_b128 v[96:99], v116 offset:26624\n\t"
        "v_mfma_f32_16x16x32_bf16 v[8:11], v[148:151], v[52:55], v[8:11]\n\t"
        "v_mfma_f32_16x16x32_bf16 v[12:15], v[152:155], v[52:55], v[12:15]\n\t"
        "v_mul_f32_e32 v107, v102, v107\n\t"
        "v_mfma_f32_16x16x32_bf16 v[16:19], v[156:159], v[52:55], v[16:19]\n\t"
        "buffer_load_dwordx4 v[52:55], v117, %[sw], %[t0] offen offset:3072 sc1\n\t"
        "s_waitcnt vmcnt(15) lgkmcnt(2)\n\t"
        "v_mfma_f32_16x16x32_bf16 v[20:23], v[88:91], v[56:59], v[20:23]\n\t"
        "ds_read_b128 v[88:91], v116 offset:27648\n\t"
        "v_mfma_f32_16x16x32_bf16 v[8:11], v[160:163], v[56:59], v[8:11]\n\t"
        "v_mfma_f32_16x16x32_bf16 v[12:15], v[164:167], v[56:59], v[12:15]\n\t"
        "v_cvt_pk_bf16_f32 v107, 0, v107\n\t"
        "v_mfma_f32_16x16x32_bf16 v[16:19], v[168:171], v[56:59], v[16:19]\n\t"
        "s_add_u32 %[t0], %[ro], 2\n\t"
        "s_and_b32 %[t0], %[t0], 7\n\t"
        "s_lshl_b32 %[t0], %[t0], 12\n\t"
        "s_add_u32 %[t0], %[t0], %[nx]\n\t"
        "buffer_load_dwordx4 v[56:59], v117, %[sw], %[t0] offen offset:0 sc1\n\t"
        "s_waitcnt vmcnt(15) lgkmcnt(2)\n\t"
        "v_mfma_f32_16x16x32_bf16 v[20:23], v[92:95], v[60:63], v[20:23]\n\t"
        "ds_read_b128 v[92:95], v116 offset:28672\n\t"
        "v_mfma_f32_16x16x32_bf16 v[8:11], v[172:175], v[60:63], v[8:11]\n\t"
        "v_mfma_f32_16x16x32_bf16 v[12:15], v[176:179], v[60:63], v[12:15]\n\t"
        "v_lshlrev_b32_e32 v108, 16, v101\n\t"
        "v_mfma_f32_16x16x32_bf16 v[16:19], v[180:183], v[60:63], v[16:19]\n\t"
        "buffer_load_dwordx4 v[60:63], v117, %[sw], %[t0] offen offset:1024 sc1\n\t"
        "s_waitcnt vmcnt(15) lgkmcnt(2)\n\t"
        "v_mfma_f32_16x16x32_bf16 v[20:23], v[96:99], v[64:67], v[20:23]\n\t"
        "ds_read_b128 v[96:99], v116 offset:29696\n\t"
        "v_mfma_f32_16x16x32_bf16 v[8:11], v[184:187], v[64:67], v[8:11]\n\t"
        "v_mfma_f32_16x16x32_bf16 v[12:15], v[188:191], v[64:67], v[12:15]\n\t"
        "v_mul_f32_e32 v108, v102, v108\n\t"
        "v_mfma_f32_16x16x32_bf16 v[16:19], v[192:195], v[64:67], v[16:19]\n\t"
        "buffer_load_dwordx4 v[64:67], v117, %[sw], %[t0] offen offset:2048 sc1\n\t"
        "s_waitcnt vmcnt(15) lgkmcnt(2)\n\t"
        "v_mfma_f32_16x16x32_bf16 v[20:23], v[88:91], v[68:71], v[20:23]\n\t"
        "ds_read_b128 v[88:91], v116 offset:30720\n\t"
        "v_mfma_f32_16x16x32_bf16 v[8:11], v[196:199], v[68:71], v[8:11]\n\t"
        "v_mfma_f32_16x16x32_bf16 v[12:15], v[200:203], v[68:71], v[12:15]\n\t"
        "v_cvt_pk_bf16_f32 v108, 0, v108\n\t"
        "v_mfma_f32_16x16x32_bf16 v[16:19], v[204:207], v[68:71], v[16:19]\n\t"
        "buffer_load_dwordx4 v[68:71], v117, %[sw], %[t0] offen offset:3072 sc1\n\t"
        "s_waitcnt vmcnt(15) lgkmcnt(2)\n\t"
        "v_mfma_f32_16x16x32_bf16 v[20:23], v[92:95], v[72:75], v[20:23]\n\t"
        "ds_read_b128 v[92:95], v116 offset:31744\n\t"
        "v_mfma_f32_16x16x32_bf16 v[8:11], v[208:211], v[72:75], v[8:11]\n\t"
        "v_mfma_f32_16x16x32_bf16 v[12:15], v[212:215], v[72:75], v[12:15]\n\t"
        "v_and_b32_e32 v109, 0xffff0000, v101\n\t"
        "v_mfma_f32_16x16x32_bf16 v[16:19], v[216:219], v[72:75], v[16:19]\n\t"
        "s_add_u32 %[t0], %[ro], 3\n\t"
        "s_and_b32 %[t0], %[t0], 7\n\t"
        "s_lshl_b32 %[t0], %[t0], 12\n\t"
        "s_add_u32 %[t0], %[t0], %[nx]\n\t"
        "buffer_load_dwordx4 v[72:75], v117, %[sw], %[t0] offen offset:0 sc1\n\t"
        "s_waitcnt vmcnt(15) lgkmcnt(2)\n\t"
        "v_mfma_f32_16x16x32_bf16 v[20:23], v[96:99], v[76:79], v[20:23]\n\t"
        "v_mfma_f32_16x16x32_bf16 v[8:11], v[220:223], v[76:79], v[8:11]\n\t"
        "v_mfma_f32_16x16x32_bf16 v[12:15], v[224:227], v[76:79], v[12:15]\n\t"
        "v_mul_f32_e32 v109, v102, v109\n\t"
        "v_mfma_f32_16x16x32_bf16 v[16:19], v[228:231], v[76:79], v[16:19]\n\t"
        "buffer_load_dwordx4 v[76:79], v117, %[sw], %[t0] offen offset:1024 sc1\n\t"
        "s_waitcnt vmcnt(15) lgkmcnt(1)\n\t"
        "v_mfma_f32_16x16x32_bf16 v[20:23], v[88:91], v[80:83], v[20:23]\n\t"
        "ds_read_b128 v[88:91], v116 offset:0\n\t"
        "v_mfma_f32_16x16x32_bf16 v[8:11], v[232:235], v[80:83], v[8:11]\n\t"
        "v_mfma_f32_16x16x32_bf16 v[12:15], v[236:239], v[80:83], v[12:15]\n\t"
        "v_cvt_pk_bf16_f32 v109, 0, v109\n\t"
        "v_mfma_f32_16x16x32_bf16 v[16:19], v[240:243], v[80:83], v[16:19]\n\t"
        "buffer_load_dwordx4 v[80:83], v117, %[sw], %[t0] offen offset:2048 sc1\n\t"
        "s_waitcnt vmcnt(15) lgkmcnt(1)\n\t"
        "v_mfma_f32_16x16x32_bf16 v[20:23], v[92:95], v[84:87], v[20:23]\n\t"
        "ds_read_b128 v[92:95], v116 offset:1024\n\t"
        "v_mfma_f32_16x16x32_bf16 v[8:11], v[244:247], v[84:87], v[8:11]\n\t"
        "ds_read_b128 v[96:99], v116 offset:2048\n\t"
        "v_mfma_f32_16x16x32_bf16 v[12:15], v[248:251], v[84:87], v[12:15]\n\t"
        "v_mfma_f32_16x16x32_bf16 v[16:19], v[252:255], v[84:87], v[16:19]\n\t"
        "buffer_load_dwordx4 v[84:87], v117, %[sw], %[t0] offen offset:3072 sc1\n\t"
        "LP_epi%=:\n\t"
        "s_lshl_b32 %[t0], %[tl], 15\n\t"
        "s_nop 0\n\t"
        "ds_write_b128 v122, v[20:23] offset:192\n\t"
        "s_nop 0\n\t"
        "ds_write_b128 v122, v[8:11] offset:0\n\t"
        "s_nop 0\n\t"
        "ds_write_b128 v122, v[12:15] offset:64\n\t"
        "ds_write_b128 v122, v[16:19] offset:128\n\t"
        "s_waitcnt lgkmcnt(0)\n\t"
        "s_barrier\n\t"
        "ds_read_b128 v[8:11], v123 offset:0\n\t"
        "ds_read_b128 v[12:15], v123 offset:4352\n\t"
        "ds_read_b128 v[16:19], v123 offset:8704\n\t"
        "ds_read_b128 v[20:23], v123 offset:13056\n\t"
        "s_waitcnt lgkmcnt(2)\n\t"
        "v_pk_add_f32 v[8:9], v[8:9], v[12:13]\n\t"
        "v_pk_add_f32 v[10:11], v[10:11], v[14:15]\n\t"
        "s_waitcnt lgkmcnt(0)\n\t"
        "v_pk_add_f32 v[16:17], v[16:17], v[20:21]\n\t"
        "v_pk_add_f32 v[18:19], v[18:19], v[22:23]\n\t"
        "v_pk_add_f32 v[8:9], v[8:9], v[16:17]\n\t"
        "v_pk_add_f32 v[10:11], v[10:11], v[18:19]\n\t"
        "v_cvt_pk_bf16_f32 v8, 0, v8\n\t"
        "v_cvt_pk_bf16_f32 v9, 0, v9\n\t"
        "v_cvt_pk_bf16_f32 v10, 0, v10\n\t"
        "v_cvt_pk_bf16_f32 v11, 0, v11\n\t"
        "v_mul_f32_e32 v8, v104, v8\n\t"
        "v_mul_f32_e32 v9, v104, v9\n\t"
        "v_mul_f32_e32 v10, v104, v10\n\t"
        "v_mul_f32_e32 v11, v104, v11\n\t"
        "v_cvt_pk_bf16_f32 v8, 0, v8\n\t"
        "v_cvt_pk_bf16_f32 v9, 0, v9\n\t"
        "v_cvt_pk_bf16_f32 v10, 0, v10\n\t"
        "v_cvt_pk_bf16_f32 v11, 0, v11\n\t"
        "v_add_f32_e32 v8, v106, v8\n\t"
        "v_add_f32_e32 v9, v107, v9\n\t"
        "v_add_f32_e32 v10, v108, v10\n\t"
        "v_add_f32_e32 v11, v109, v11\n\t"
        "v_cvt_pk_bf16_f32 v104, v8, v9\n\t"
        "v_cvt_pk_bf16_f32 v105, v10, v11\n\t"
        "buffer_store_dwordx2 v[104:105], v121, %[su], %[t0] offen sc1\n\t"
        "s_add_u32 %[tl], %[tl], 8\n\t"
        "s_cmp_ge_u32 %[tl], 128\n\t"
        "s_cbranch_scc1 LP_end%=\n\t"
        "s_cmp_lg_u32 %[nx], 0xf0000000\n\t"
        "s_cbranch_scc1 LP_tile%=\n\t"
        "s_branch LP_wait%=\n\t"
        "LP_end%=:\n\t"
        "s_waitcnt vmcnt(0) lgkmcnt(0)\n\t"
        ".Lk_end_P:\n\t"
        : [t0] "=&s"(t0), [t1] "=&s"(t1), [tl] "+s"(tl), [pg] "=&s"(pg), [cu] "=&s"(cu), [nx] "=&s"(nx), [sp] "=&s"(sp), [re] "=&s"(re), [tu] "=&s"(tu)
        : [so] "s"(so), [sw] "s"(sw), [su] "s"(su), [cv] "s"(F_CVY + nt * 128), [ro] "s"(PROT ? (nt + 2 * rp) & 7 : 0), [db] "s"(W_DBG + (128 + j) * 8192 + 512),
          "{v110}"((w * 1024 + (l >> 3)) * 2048 + nt * 128 + (l & 7) * 16), "{v111}"(stg + ((l * 16) ^ ((l & 48) << 1))), "{v112}"(lr), "{v113}"(lr ^ 32u), "{v114}"(lr ^ 64u), "{v115}"(lr ^ 96u),
          "{v116}"(lb + w * 32768 + l * 16), "{v117}"(W_OX + w * 32768 + l * 16), "{v118}"(F_PROG + (w * 8 + (l & 7)) * 128),
          "{v119}"(W_A + ((nt * 2 + (n >> 3)) * 4 + 2 * (n & 1)) * 32768 + w * 4096 + ((n & 7) >> 1) * 16 + g * 4), "{v120}"(W_GATE + (4 * w + g) * 4), "{v121}"((4 * w + g) * 2048 + nt * 128 + n * 8),
          "{v122}"(lb + 131072 + w * 4352 + n * 272 + g * 16), "{v123}"(lb + 131072 + (4 * w + g) * 272 + n * 16)
        : "memory", "scc", "vcc", "v8", "v9", "v10", "v11", "v12", "v13", "v14", "v15", "v16", "v17", "v18", "v19", "v20", "v21", "v22", "v23", "v24", "v25", "v26", "v27", "v28", "v29", "v30", "v31", "v32", "v33", "v34", "v35", "v36", "v37", "v38", "v39", "v40", "v41", "v42", "v43", "v44", "v45", "v46", "v47", "v48", "v49", "v50", "v51", "v52", "v53", "v54", "v55", "v56", "v57", "v58", "v59", "v60", "v61", "v62", "v63", "v64", "v65", "v66", "v67", "v68", "v69", "v70", "v71", "v72", "v73", "v74", "v75", "v76", "v77", "v78", "v79", "v80", "v81", "v82", "v83", "v84", "v85", "v86", "v87", "v88", "v89", "v90", "v91", "v92", "v93", "v94", "v95", "v96", "v97", "v98", "v99", "v100", "v101", "v102", "v103", "v104", "v105", "v106", "v107", "v108", "v109", "v124", "v125", "v126", "v127", "v128", "v129", "v130", "v131", "v132", "v133", "v134", "v135", "v136", "v137", "v138", "v139", "v140", "v141", "v142", "v143", "v144", "v145", "v146", "v147", "v148", "v149", "v150", "v151", "v152", "v153", "v154", "v155", "v156", "v157", "v158", "v159", "v160", "v161", "v162", "v163", "v164", "v165", "v166", "v167", "v168", "v169", "v170", "v171", "v172", "v173", "v174", "v175", "v176", "v177", "v178", "v179", "v180", "v181", "v182", "v183", "v184", "v185", "v186", "v187", "v188", "v189", "v190", "v191", "v192", "v193", "v194", "v195", "v196", "v197", "v198", "v199", "v200", "v201", "v202", "v203", "v204", "v205", "v206", "v207", "v208", "v209", "v210", "v211", "v212", "v213", "v214", "v215", "v216", "v217", "v218", "v219", "v220", "v221", "v222", "v223", "v224", "v225", "v226", "v227", "v228", "v229", "v230", "v231", "v232", "v233", "v234", "v235", "v236", "v237", "v238", "v239", "v240", "v241", "v242", "v243", "v244", "v245", "v246", "v247", "v248", "v249", "v250", "v251", "v252", "v253", "v254", "v255", "a0", "a1", "a2", "a3", "a4", "a5", "a6", "a7", "a8", "a9", "a10", "a11", "a12", "a13", "a14", "a15", "a16", "a17", "a18", "a19", "a20", "a21", "a22", "a23", "a24", "a25", "a26", "a27", "a28", "a29", "a30", "a31", "a32", "a33", "a34", "a35", "a36", "a37", "a38", "a39", "a40", "a41", "a42", "a43", "a44", "a45", "a46", "a47", "a48", "a49", "a50", "a51", "a52", "a53", "a54", "a55", "a56", "a57", "a58", "a59", "a60", "a61", "a62", "a63", "a64", "a65", "a66", "a67", "a68", "a69", "a70", "a71", "a72", "a73", "a74", "a75", "a76", "a77", "a78", "a79", "a80", "a81", "a82", "a83", "a84", "a85", "a86", "a87", "a88", "a89", "a90", "a91", "a92", "a93", "a94", "a95", "a96", "a97", "a98", "a99", "a100", "a101", "a102", "a103", "a104", "a105", "a106", "a107", "a108", "a109", "a110", "a111", "a112", "a113", "a114", "a115", "a116", "a117", "a118", "a119", "a120", "a121", "a122", "a123", "a124", "a125", "a126", "a127", "a128", "a129", "a130", "a131", "a132", "a133", "a134", "a135", "a136", "a137", "a138", "a139", "a140", "a141", "a142", "a143", "a144", "a145", "a146", "a147", "a148", "a149", "a150", "a151", "a152", "a153", "a154", "a155", "a156", "a157", "a158", "a159", "a160", "a161", "a162", "a163", "a164", "a165", "a166", "a167", "a168", "a169", "a170", "a171", "a172", "a173", "a174", "a175", "a176", "a177", "a178", "a179", "a180", "a181", "a182", "a183", "a184", "a185", "a186", "a187", "a188", "a189", "a190", "a191", "a192", "a193", "a194", "a195", "a196", "a197", "a198", "a199", "a200", "a201", "a202", "a203", "a204", "a205", "a206", "a207", "a208", "a209", "a210", "a211", "a212", "a213", "a214", "a215", "a216", "a217", "a218", "a219", "a220", "a221", "a222", "a223", "a224", "a225", "a226", "a227", "a228", "a229", "a230", "a231", "a232", "a233", "a234", "a235", "a236", "a237", "a238", "a239", "a240", "a241", "a242", "a243", "a244", "a245", "a246", "a247", "a248", "a249", "a250", "a251", "a252", "a253", "a254", "a255");
}
__device__ __forceinline__ void bulk_block(const Params& p, rsrc_t r, unsigned char* lds, const int wv, const int j, const unsigned te) {
    BSTAMP(0);
    proj_block(p, lds, wv, (j & 6) * 4 + (j >> 4), ((j >> 2) & 2) | (j & 1), j);
    BSTAMP(2);
    const int un = ((j & 7) >> 1) * 32 + (j >> 3) * 2 + (j & 1);
    if (XG) { gate_block<4>(p, r, j, wv); conv_prep(p, r, wv, un >> 2, un & 3, (un & 3) + 1); }
    BSTAMP(5);
    if (!conv_core(p, r, lds, wv, un)) return;
    if (XG) { if (!wait_word(r, F_CONV, 128u, 4096u)) return; }
    BSTAMP(3);
    if (GDLY) wait_long(rtnow() + GDLY);
    out_block(p, lds, wv, ((j & 7) >> 1) + (j & 1) * 4, j >> 3, j);
    BSTAMP(7);
}

__device__ __forceinline__ void rec_block(const Params& p, rsrc_t r, unsigned char* lds, const int wv, const int b) {
    const int xq = (b & 7) >> 1, t = (b >> 3) * 2 + (b & 1);
    const unsigned lb = __builtin_amdgcn_readfirstlane((unsigned)(size_t)lds);
    int kind, id, rg = 0, rot = 0, bq = 99;
    if (RPL == 0) { kind = t < 8 ? 0 : t < 24 ? 1 : 2; id = t < 8 ? xq * 8 + t : t < 24 ? (xq & 1) * 16 + t - 8 : xq * 8 + t - 24; rg = xq >> 1; }
    else if (RPL == 4) { kind = t < 8 ? 0 : t < 24 ? 1 : 2; id = t < 8 ? xq * 8 + t : t < 24 ? xq * 8 + ((t - 8) >> 1) : xq * 8 + t - 24; rg = t & 1; }
    else if (RPL == 1) { kind = xq == 0 ? 0 : xq < 3 ? 1 : 2; id = t; rg = xq - 1; }
    else if (RPL == 2) { const int na = xq == 2 ? 10 : 11, q = xq * 21 + t - na; kind = xq == 3 ? 2 : t < na ? 0 : 1; id = xq == 3 ? t : t < na ? xq * 11 + t : q >> 1; rg = q & 1; }
    else { const int na = xq == 2 ? 12 : 10, q = t - na; kind = xq == 3 ? 2 : t < na ? 0 : 1; rg = xq < 2 ? xq : q & 1; bq = q;
        const int c2 = q >> 1, cs = xq < 2 ? (q < 6 ? q : q < 20 ? q + 2 : q + 4) : (c2 < 2 ? 6 + c2 : c2 < 4 ? 20 + c2 : 22 + c2);
        id = xq == 3 ? t : t < na ? xq * 10 + t : cs; rot = !ROT ? 0 : xq < 2 ? (cs & 1) * 4 : ((cs & 1) * 2 + rg) * 2; }
    if (kind == 0) rec_a(p, lb, wv, id, xq);
    else if (kind == 1) rec_b(p, lb, wv, id, rg, xq, rot, bq);
    else {
        const int hi = id, j = hi - 32; (void)j;
        BSTAMP(0);
        rec_helper(p, r, wv, hi);
        BSTAMP(6);
        if (XG) return;
        if (XWAIT == 1) { if (!wait_word(r, F_GO + (hi >> 1) * 128, 1u, 1024u)) return; }
        if (XSTG) wait_long(rtnow() + (hi & 7) * XSTG);
        gate_block<16>(p, r, hi, wv);
        BSTAMP(2);
        if (XWAIT == 2) { if (!wait_word(r, F_GO + (hi >> 1) * 128, 1u, 1024u)) return; }
        conv_prep(p, r, wv, hi, 0, 4);
        BSTAMP(3);
    }
}

__global__ void __launch_bounds__(256, 1) k_main(Params p) {
    extern __shared__ __attribute__((aligned(16))) unsigned char lds[];
    rsrc_t r = __builtin_amdgcn_make_buffer_rsrc(p.ws, 0, W_TOTAL, 0x00020000);
    const int wv = __builtin_amdgcn_readfirstlane(threadIdx.x >> 6);
    int role = blockIdx.x;
    unsigned ctouch = 0u;
    const unsigned te = 0u;
    if (CODE_T) {
        unsigned long long pc; int ea, eb, eg;
        asm volatile("s_getpc_b64 %0\n.Lk_begin:\n\ts_mov_b32 %1, .Lk_end_A-.Lk_begin\n\ts_mov_b32 %2, .Lk_end_B-.Lk_begin\n\ts_mov_b32 %3, .Lk_end_P-.Lk_begin" : "=s"(pc), "=s"(ea), "=s"(eb), "=s"(eg));
        const int o = ((blockIdx.x >> 3) * 64 + (threadIdx.x >> 2)) * 128;
        if ((threadIdx.x & 3) == 0 && o < max(ea, max(eb, eg)) - 4) asm volatile("global_load_dword %0, %1, off" : "+v"(ctouch) : "v"((const unsigned char*)pc + o) : "memory");
    }
    if (blockIdx.x == 0 && wv == 1) {
        const int l = threadIdx.x & 63; const v4u z = {0u, 0u, 0u, 0u};
#pragma unroll
        for (int i = 0; i < 6; ++i) st16(r, F_CONV + i * 1024 + l * 16, z);
        DRAIN();
        for (unsigned spin = 0; spin < (1u << 20); ++spin) {
            if (l < 8) __hip_atomic_fetch_add((unsigned*)(p.ws + F_BEAT + l * 128), 1u, __ATOMIC_RELAXED, __HIP_MEMORY_SCOPE_AGENT);
            __builtin_amdgcn_s_sleep(BEAT_SLP);
            unsigned c = ld4(r, l < 8 ? F_TKT + l * 64 : OOB);
            c += __shfl_xor(c, 1); c += __shfl_xor(c, 2); c += __shfl_xor(c, 4);
            if (__builtin_amdgcn_readfirstlane(c) >= 256u) break; }
    }
    if (threadIdx.x == 0) {
        const unsigned bo = F_BEAT + (blockIdx.x & 7) * 128, b0 = ld4(r, bo);
        for (unsigned spin = 0; spin < (1u << 22); ++spin) { MEMBAR(); if (ld4(r, bo) != b0) break; }
        MEMBAR();
        unsigned xcc; asm volatile("s_getreg_b32 %0, hwreg(HW_REG_XCC_ID)" : "=s"(xcc)); xcc &= 7u;
        unsigned* tk = (unsigned*)(p.ws + F_TKT);
        unsigned t = __hip_atomic_fetch_add(tk + xcc * 16, 1u, __ATOMIC_RELAXED, __HIP_MEMORY_SCOPE_AGENT);
        if (t >= 32u) {
            unsigned o = __hip_atomic_fetch_add(tk + 128, 1u, __ATOMIC_RELAXED, __HIP_MEMORY_SCOPE_AGENT);
            for (unsigned spin = 0; spin < (1u << 22); ++spin) { unsigned tot = 0u;
                for (int x = 0; x < 8; ++x) tot += __hip_atomic_load(tk + x * 16, __ATOMIC_RELAXED, __HIP_MEMORY_SCOPE_AGENT);
                if (tot >= 256u) break; }
            for (int x = 0; x < 8; ++x) { const unsigned cnt = __hip_atomic_load(tk + x * 16, __ATOMIC_RELAXED, __HIP_MEMORY_SCOPE_AGENT), fr = cnt < 32u ? 32u - cnt : 0u;
                if (o < fr) { xcc = (unsigned)x; t = cnt + o; break; }
                o -= fr; }
        }
        *(volatile int*)lds = (int)((xcc >> 2) * 128u + (t >> 1) * 8u + (xcc & 3u) * 2u + (t & 1u));
    }
    asm volatile("s_waitcnt vmcnt(0)" : "+v"(ctouch) :: "memory");
    __syncthreads();
    role = __builtin_amdgcn_readfirstlane(*(volatile int*)lds) & 255;
    __syncthreads();
    if (role >= 128) bulk_block(p, r, lds, wv, role - 128, te); else rec_block(p, r, lds, wv, role);
}

extern "C" void kernel_launch(void* const* d_in, const int* in_sizes, int n_in, void* d_out, int out_size, void* d_ws, size_t ws_size, hipStream_t stream) {
    Params p;
    p.Aq = (const bf16_t*)d_in[0]; p.Ak = (const bf16_t*)d_in[1]; p.Av = (const bf16_t*)d_in[2];
    p.shift_k = (const bf16_t*)d_in[3]; p.ssm_k = (const bf16_t*)d_in[4]; p.ssm_D = (const bf16_t*)d_in[5];
    p.mem0 = (const bf16_t*)d_in[7]; p.Wq = (const bf16_t*)d_in[8]; p.Wk = (const bf16_t*)d_in[9];
    p.Wv = (const bf16_t*)d_in[10]; p.Wo = (const bf16_t*)d_in[11]; p.gate_w = (const bf16_t*)d_in[13];
    p.out = (bf16_t*)d_out; p.ws = (unsigned char*)d_ws;
    (void)hipFuncSetAttribute((const void*)k_main, hipFuncAttributeMaxDynamicSharedMemorySize, L_TOTAL);
    k_main<<<256, 256, L_TOTAL, stream>>>(p);
}
```

```cpp
#include <hip/hip_runtime.h>
#include <stdint.h>
typedef unsigned short bf16_t;
using f32x16 = __attribute__((ext_vector_type(16))) float;
typedef unsigned v4u __attribute__((ext_vector_type(4)));
typedef unsigned v2u __attribute__((ext_vector_type(2)));
typedef __amdgpu_buffer_rsrc_t rsrc_t;
typedef __bf16 hbf2 __attribute__((ext_vector_type(2)));
typedef float f32x2 __attribute__((ext_vector_type(2)));
__device__ __forceinline__ unsigned pack2(float a, float b) { f32x2 v = {a, b}; return __builtin_bit_cast(unsigned, __builtin_convertvector(v, hbf2)); }
__device__ __forceinline__ float rbfh(float f) { return __uint_as_float(pack2(f, 0.f) << 16); }
__device__ __forceinline__ float blo(unsigned x) { return __uint_as_float(x << 16); }
__device__ __forceinline__ float bhi(unsigned x) { return __uint_as_float(x & 0xFFFF0000u); }

constexpr int S = 2048, D = 1024;

constexpr unsigned F_KVQ = 0;
constexpr unsigned F_CONV = 32768;
constexpr unsigned F_ERR = 33024;
constexpr unsigned F_CAL = 33792;
constexpr unsigned F_RDY0 = 34304;
constexpr unsigned F_TKT = 34816;
constexpr unsigned F_GO = 36864;
constexpr unsigned F_BEAT = 40960;
constexpr unsigned F_PROG = 49152;
constexpr unsigned F_CVY = 57344;
constexpr unsigned W_A = 8568832;
constexpr unsigned W_GATE = 65536;
constexpr unsigned W_QX = W_A + S * D * 2 + 8192;
constexpr unsigned W_KX = W_QX + S * 4096 * 2;
constexpr unsigned W_VF = W_KX + S * 4096 * 2;
constexpr unsigned W_OX = W_VF + S * 4096 * 2;
constexpr unsigned W_Y = W_OX + S * 4096 * 2;
constexpr unsigned W_U = 115531776;
constexpr unsigned W_TOTAL = W_U + S * D * 2;
constexpr unsigned OOB = 0xF0000000u;
constexpr int L_TOTAL = 163840;

__device__ __forceinline__ v4u ld16(rsrc_t r, unsigned off) { return __builtin_amdgcn_raw_buffer_load_b128(r, off, 0, 16); }
__device__ __forceinline__ v2u ld8(rsrc_t r, unsigned off) { return __builtin_amdgcn_raw_buffer_load_b64(r, off, 0, 16); }
__device__ __forceinline__ unsigned ld4(rsrc_t r, unsigned off) { return __builtin_amdgcn_raw_buffer_load_b32(r, off, 0, 16); }
__device__ __forceinline__ void st16(rsrc_t r, unsigned off, v4u v) { __builtin_amdgcn_raw_buffer_store_b128(v, r, off, 0, 16); }
__device__ __forceinline__ void st4(rsrc_t r, unsigned off, unsigned v) { __builtin_amdgcn_raw_buffer_store_b32(v, r, off, 0, 16); }
#define D1 128
#define D2 77
#define LT_A 1
#define PEN 48
#define CAL_T 450u
#define XSLP 1
#define T2U 2
#define TPRE 48
#define MEMBAR() asm volatile("" ::: "memory")
#define DRAIN() asm volatile("s_waitcnt vmcnt(0)" ::: "memory")
__device__ __forceinline__ int otid_(int wv) { int t; asm volatile("v_mbcnt_lo_u32_b32 %0, -1, 0\n\tv_mbcnt_hi_u32_b32 %0, -1, %0" : "=v"(t)); return wv * 64 + t; }
#define otid() otid_(wv)
__device__ __forceinline__ bool wait_word(rsrc_t r, unsigned off, unsigned want, unsigned code) {
    for (unsigned spin = 0;; ++spin) { MEMBAR();
        const unsigned v = __builtin_amdgcn_readfirstlane(ld4(r, off));
        if (v >= want) { MEMBAR(); return true; }
        if (spin > (1u << 20)) { st4(r, F_ERR, code); return false; }
        __builtin_amdgcn_s_sleep(8); }
}
__device__ __forceinline__ void touch_l2(const void* q, unsigned ldsdst) { asm volatile("s_mov_b32 m0, %1\n\ts_nop 0\n\tglobal_load_lds_dword %0, off" :: "v"(q), "s"(ldsdst) : "m0", "memory"); }
__device__ __forceinline__ unsigned rtnow() { return (unsigned)__builtin_amdgcn_s_memrealtime(); }

struct Params {
    const bf16_t *Aq, *Ak, *Av, *shift_k, *ssm_k, *ssm_D, *mem0, *Wq, *Wk, *Wv, *Wo, *gate_w;
    bf16_t* out; unsigned char* ws;
};
__device__ __forceinline__ unsigned ho_base(const void* ws) { const size_t a = (size_t)ws; return (unsigned)((((a + W_TOTAL + 0x7FFFFFu) >> 23) << 23) - a); }
__device__ __forceinline__ v4u mk_srd(const void* p, unsigned bytes) { const size_t a = (size_t)p; v4u d;
    d.x = __builtin_amdgcn_readfirstlane((unsigned)a); d.y = __builtin_amdgcn_readfirstlane((unsigned)(a >> 32) & 0xFFFFu); d.z = __builtin_amdgcn_readfirstlane(bytes); d.w = 0x00020000u; return d; }
constexpr unsigned HO_BYTES = 2097152;
constexpr int L_KV = 90112, L_MS = 139264, L_IMG = 155648, IMG_PS = 1056, IMG_MS = 2112, LB_R = 8192, LB_STG = 16384, LB_STGP = 8704;
static_assert(F_KVQ == 0 && F_ERR == 33024 && F_CONV == 32768 && F_CAL == 33792 && F_RDY0 == 34304 && F_GO == 36864 && F_PROG == 49152 && W_QX == 12771328 && W_KX == 29548544 && W_OX == 63102976 && W_VF == 46325760 && L_KV == 90112 && L_MS == 139264 && L_IMG == 155648 && IMG_PS == 1056 && IMG_MS == 2112 && LB_R == 8192 && LB_STG == 16384 && LB_STGP == 8704, "constants of the generated code");
#define REC_S_IN "{s[8:11]}"(sh), "{s[12:15]}"(sw), "{s[16:19]}"(s1), "{s[20:23]}"(s2), "{s29}"(D1), "{s30}"(D2), "{s43}"(w), "{s44}"(id), "{s49}"(rb), "{s50}"(PEN), "{s51}"(LT_A), "{s45}"(F_GO + (cp * 4 + w) * 128)
__device__ __forceinline__ void rec_a(const Params& p, unsigned lb, const int wv, const int id, const int cp) {
    const int w = wv, l = otid() & 63, n = l & 15, g = l >> 4, h = id, rb = id;
    const v4u sh = mk_srd(p.ws + ho_base(p.ws), HO_BYTES - 8192), sw = mk_srd(p.ws, W_TOTAL), s1 = mk_srd(w < 2 ? p.Wq : p.Wk, 1024 * 4096 * 2), s2 = s1;
    const int r8 = l >> 3, pc = l & 7, pp = n >> 3, bb = (n >> 2) & 1, f = (g & 1) | (pp << 1);
    const unsigned stg = lb + 131072 + w * 8192;
    const unsigned g_w = r8 * 8192 + h * 256 + (w & 1) * 128 + pc * 16;
    const unsigned wb0 = stg + r8 * 128 + (((pc >> 1) ^ (r8 >> 2)) << 5) + (pc & 1) * 16, wb1 = wb0 ^ 64u;
    const unsigned rb0 = stg + (16 * (g >> 1) + 8 * pp + 4 * (g & 1) + bb) * 128 + (f << 5) + (n & 3) * 8;
    const unsigned a_mem = cp * 32768 + (w >> 1) * 16384 + (w & 1) * 4096 + l * 16;
    const unsigned l_ms = lb + L_MS + l * 16;
    const unsigned l_iw = lb + L_IMG + (w >> 1) * IMG_MS + (g >> 1) * IMG_PS + (64 * (w & 1) + n) * 8 + (g & 1) * 4;
    const unsigned l_iq = lb + L_IMG + (n & 1) * IMG_PS + (8 * g + (n >> 2)) * 8;
    const unsigned a_vm = (16 + (h >> 2)) * 16384 + (h & 3) * 2048 + w * 512 + (l & 31) * 16;
    const unsigned a_o = n < 8 ? (8 + (n >> 2) * 4 + (h >> 3)) * 16384 + (h & 7) * 1024 + w * 256 + (n & 3) * 64 + g * 16 : OOB;
    const unsigned a_ox = (((w & 1) * 32 + h) * 4 + w) * 1024 + l * 16;
    asm volatile(
        "s_nop 4\n\t"
        "s_lshl_b32 s34, s43, 2\n\t"
        "s_add_u32 s34, s34, 0\n\t"
        "s_and_b32 s34, s34, 15\n\t"
        "s_lshl_b32 s34, s34, 19\n\t"
        "buffer_load_dwordx4 v[224:227], v96, s[16:19], s34 offen nt\n\t"
        "s_add_u32 s34, s34, 0x10000\n\t"
        "buffer_load_dwordx4 v[228:231], v96, s[16:19], s34 offen nt\n\t"
        "s_add_u32 s34, s34, 0x10000\n\t"
        "buffer_load_dwordx4 v[232:235], v96, s[16:19], s34 offen nt\n\t"
        "s_add_u32 s34, s34, 0x10000\n\t"
        "buffer_load_dwordx4 v[236:239], v96, s[16:19], s34 offen nt\n\t"
        "s_add_u32 s34, s34, 0x10000\n\t"
        "buffer_load_dwordx4 v[240:243], v96, s[16:19], s34 offen nt\n\t"
        "s_add_u32 s34, s34, 0x10000\n\t"
        "buffer_load_dwordx4 v[244:247], v96, s[16:19], s34 offen nt\n\t"
        "s_add_u32 s34, s34, 0x10000\n\t"
        "buffer_load_dwordx4 v[248:251], v96, s[16:19], s34 offen nt\n\t"
        "s_add_u32 s34, s34, 0x10000\n\t"
        "buffer_load_dwordx4 v[252:255], v96, s[16:19], s34 offen nt\n\t"
        "s_lshl_b32 s34, s43, 2\n\t"
        "s_add_u32 s34, s34, 1\n\t"
        "s_and_b32 s34, s34, 15\n\t"
        "s_lshl_b32 s34, s34, 19\n\t"
        "buffer_load_dwordx4 v[0:3], v96, s[16:19], s34 offen nt\n\t"
        "s_add_u32 s34, s34, 0x10000\n\t"
        "buffer_load_dwordx4 v[4:7], v96, s[16:19], s34 offen nt\n\t"
        "s_add_u32 s34, s34, 0x10000\n\t"
        "buffer_load_dwordx4 v[8:11], v96, s[16:19], s34 offen nt\n\t"
        "s_add_u32 s34, s34, 0x10000\n\t"
        "buffer_load_dwordx4 v[12:15], v96, s[16:19], s34 offen nt\n\t"
        "s_add_u32 s34, s34, 0x10000\n\t"
        "buffer_load_dwordx4 v[16:19], v96, s[16:19], s34 offen nt\n\t"
        "s_add_u32 s34, s34, 0x10000\n\t"
        "buffer_load_dwordx4 v[20:23], v96, s[16:19], s34 offen nt\n\t"
        "s_add_u32 s34, s34, 0x10000\n\t"
        "buffer_load_dwordx4 v[24:27], v96, s[16:19], s34 offen nt\n\t"
        "s_add_u32 s34, s34, 0x10000\n\t"
        "buffer_load_dwordx4 v[28:31], v96, s[16:19], s34 offen nt\n\t"
        "s_lshl_b32 s34, s43, 2\n\t"
        "s_add_u32 s34, s34, 2\n\t"
        "s_and_b32 s34, s34, 15\n\t"
        "s_lshl_b32 s34, s34, 19\n\t"
        "buffer_load_dwordx4 v[32:35], v96, s[16:19], s34 offen nt\n\t"
        "s_add_u32 s34, s34, 0x10000\n\t"
        "buffer_load_dwordx4 v[36:39], v96, s[16:19], s34 offen nt\n\t"
        "s_add_u32 s34, s34, 0x10000\n\t"
        "buffer_load_dwordx4 v[40:43], v96, s[16:19], s34 offen nt\n\t"
        "s_add_u32 s34, s34, 0x10000\n\t"
        "buffer_load_dwordx4 v[44:47], v96, s[16:19], s34 offen nt\n\t"
        "s_add_u32 s34, s34, 0x10000\n\t"
        "buffer_load_dwordx4 v[48:51], v96, s[16:19], s34 offen nt\n\t"
        "s_add_u32 s34, s34, 0x10000\n\t"
        "buffer_load_dwordx4 v[52:55], v96, s[16:19], s34 offen nt\n\t"
        "s_add_u32 s34, s34, 0x10000\n\t"
        "buffer_load_dwordx4 v[56:59], v96, s[16:19], s34 offen nt\n\t"
        "s_add_u32 s34, s34, 0x10000\n\t"
        "buffer_load_dwordx4 v[60:63], v96, s[16:19], s34 offen nt\n\t"
        "v_cmp_le_i32_e64 s[60:61], 0, v126\n\t"
        "v_cmp_le_i32_e64 s[62:63], 1, v126\n\t"
        "v_cmp_le_i32_e64 s[64:65], 2, v126\n\t"
        "v_cmp_le_i32_e64 s[66:67], 3, v126\n\t"
        "v_cmp_le_i32_e64 s[68:69], 16, v126\n\t"
        "v_cmp_le_i32_e64 s[70:71], 17, v126\n\t"
        "v_cmp_le_i32_e64 s[72:73], 18, v126\n\t"
        "v_cmp_le_i32_e64 s[74:75], 19, v126\n\t"
        "s_mul_i32 s35, s43, 10240\n\t"
        "v_add_u32_e32 v126, s35, v124\n\t"
        "s_waitcnt vmcnt(16)\n\t"
        "ds_write_b128 v97, v[224:227] offset:0\n\t"
        "ds_write_b128 v98, v[228:231] offset:1024\n\t"
        "ds_write_b128 v97, v[232:235] offset:2048\n\t"
        "ds_write_b128 v98, v[236:239] offset:3072\n\t"
        "ds_write_b128 v97, v[240:243] offset:4096\n\t"
        "ds_write_b128 v98, v[244:247] offset:5120\n\t"
        "ds_write_b128 v97, v[248:251] offset:6144\n\t"
        "ds_write_b128 v98, v[252:255] offset:7168\n\t"
        "ds_read_b64_tr_b16 a[0:1], v99 offset:0\n\t"
        "ds_read_b64_tr_b16 a[2:3], v99 offset:256\n\t"
        "ds_read_b64_tr_b16 a[4:5], v99 offset:4096\n\t"
        "ds_read_b64_tr_b16 a[6:7], v99 offset:4352\n\t"
        "s_waitcnt lgkmcnt(8)\n\t"
        "ds_read_b64_tr_b16 a[8:9], v100 offset:0\n\t"
        "ds_read_b64_tr_b16 a[10:11], v100 offset:256\n\t"
        "ds_read_b64_tr_b16 a[12:13], v100 offset:4096\n\t"
        "ds_read_b64_tr_b16 a[14:15], v100 offset:4352\n\t"
        "s_waitcnt lgkmcnt(8)\n\t"
        "ds_read_b64_tr_b16 a[16:17], v101 offset:0\n\t"
        "ds_read_b64_tr_b16 a[18:19], v101 offset:256\n\t"
        "ds_read_b64_tr_b16 a[20:21], v101 offset:4096\n\t"
        "ds_read_b64_tr_b16 a[22:23], v101 offset:4352\n\t"
        "s_waitcnt lgkmcnt(8)\n\t"
        "ds_read_b64_tr_b16 v[104:105], v102 offset:0\n\t"
        "ds_read_b64_tr_b16 v[106:107], v102 offset:256\n\t"
        "ds_read_b64_tr_b16 v[108:109], v102 offset:4096\n\t"
        "ds_read_b64_tr_b16 v[110:111], v102 offset:4352\n\t"
        "s_lshl_b32 s34, s43, 2\n\t"
        "s_add_u32 s34, s34, 3\n\t"
        "s_and_b32 s34, s34, 15\n\t"
        "s_lshl_b32 s34, s34, 19\n\t"
        "buffer_load_dwordx4 v[64:67], v96, s[16:19], s34 offen nt\n\t"
        "s_add_u32 s34, s34, 0x10000\n\t"
        "buffer_load_dwordx4 v[68:71], v96, s[16:19], s34 offen nt\n\t"
        "s_add_u32 s34, s34, 0x10000\n\t"
        "buffer_load_dwordx4 v[72:75], v96, s[16:19], s34 offen nt\n\t"
        "s_add_u32 s34, s34, 0x10000\n\t"
        "buffer_load_dwordx4 v[76:79], v96, s[16:19], s34 offen nt\n\t"
        "s_add_u32 s34, s34, 0x10000\n\t"
        "buffer_load_dwordx4 v[80:83], v96, s[16:19], s34 offen nt\n\t"
        "s_add_u32 s34, s34, 0x10000\n\t"
        "buffer_load_dwordx4 v[84:87], v96, s[16:19], s34 offen nt\n\t"
        "s_add_u32 s34, s34, 0x10000\n\t"
        "buffer_load_dwordx4 v[88:91], v96, s[16:19], s34 offen nt\n\t"
        "s_add_u32 s34, s34, 0x10000\n\t"
        "buffer_load_dwordx4 v[92:95], v96, s[16:19], s34 offen nt\n\t"
        "s_waitcnt lgkmcnt(0)\n\t"
        "ds_write_b128 v117, v[104:107] offset:0\n\t"
        "ds_write_b128 v117, v[108:111] offset:1024\n\t"
        "s_waitcnt vmcnt(16)\n\t"
        "ds_write_b128 v97, v[0:3] offset:0\n\t"
        "ds_write_b128 v98, v[4:7] offset:1024\n\t"
        "ds_write_b128 v97, v[8:11] offset:2048\n\t"
        "ds_write_b128 v98, v[12:15] offset:3072\n\t"
        "ds_write_b128 v97, v[16:19] offset:4096\n\t"
        "ds_write_b128 v98, v[20:23] offset:5120\n\t"
        "ds_write_b128 v97, v[24:27] offset:6144\n\t"
        "ds_write_b128 v98, v[28:31] offset:7168\n\t"
        "ds_read_b64_tr_b16 a[24:25], v99 offset:0\n\t"
        "ds_read_b64_tr_b16 a[26:27], v99 offset:256\n\t"
        "ds_read_b64_tr_b16 a[28:29], v99 offset:4096\n\t"
        "ds_read_b64_tr_b16 a[30:31], v99 offset:4352\n\t"
        "s_waitcnt lgkmcnt(8)\n\t"
        "ds_read_b64_tr_b16 a[32:33], v100 offset:0\n\t"
        "ds_read_b64_tr_b16 a[34:35], v100 offset:256\n\t"
        "ds_read_b64_tr_b16 a[36:37], v100 offset:4096\n\t"
        "ds_read_b64_tr_b16 a[38:39], v100 offset:4352\n\t"
        "s_waitcnt lgkmcnt(8)\n\t"
        "ds_read_b64_tr_b16 a[40:41], v101 offset:0\n\t"
        "ds_read_b64_tr_b16 a[42:43], v101 offset:256\n\t"
        "ds_read_b64_tr_b16 a[44:45], v101 offset:4096\n\t"
        "ds_read_b64_tr_b16 a[46:47], v101 offset:4352\n\t"
        "s_waitcnt lgkmcnt(8)\n\t"
        "ds_read_b64_tr_b16 v[104:105], v102 offset:0\n\t"
        "ds_read_b64_tr_b16 v[106:107], v102 offset:256\n\t"
        "ds_read_b64_tr_b16 v[108:109], v102 offset:4096\n\t"
        "ds_read_b64_tr_b16 v[110:111], v102 offset:4352\n\t"
        "s_lshl_b32 s34, s43, 2\n\t"
        "s_add_u32 s34, s34, 4\n\t"
        "s_and_b32 s34, s34, 15\n\t"
        "s_lshl_b32 s34, s34, 19\n\t"
        "buffer_load_dwordx4 a[96:99], v96, s[16:19], s34 offen nt\n\t"
        "s_add_u32 s34, s34, 0x10000\n\t"
        "buffer_load_dwordx4 a[100:103], v96, s[16:19], s34 offen nt\n\t"
        "s_add_u32 s34, s34, 0x10000\n\t"
        "buffer_load_dwordx4 a[104:107], v96, s[16:19], s34 offen nt\n\t"
        "s_add_u32 s34, s34, 0x10000\n\t"
        "buffer_load_dwordx4 a[108:111], v96, s[16:19], s34 offen nt\n\t"
        "s_add_u32 s34, s34, 0x10000\n\t"
        "buffer_load_dwordx4 a[112:115], v96, s[16:19], s34 offen nt\n\t"
        "s_add_u32 s34, s34, 0x10000\n\t"
        "buffer_load_dwordx4 a[116:119], v96, s[16:19], s34 offen nt\n\t"
        "s_add_u32 s34, s34, 0x10000\n\t"
        "buffer_load_dwordx4 a[120:123], v96, s[16:19], s34 offen nt\n\t"
        "s_add_u32 s34, s34, 0x10000\n\t"
        "buffer_load_dwordx4 a[124:127], v96, s[16:19], s34 offen nt\n\t"
        "s_waitcnt lgkmcnt(0)\n\t"
        "ds_write_b128 v117, v[104:107] offset:2048\n\t"
        "ds_write_b128 v117, v[108:111] offset:3072\n\t"
        "s_waitcnt vmcnt(16)\n\t"
        "ds_write_b128 v97, v[32:35] offset:0\n\t"
        "ds_write_b128 v98, v[36:39] offset:1024\n\t"
        "ds_write_b128 v97, v[40:43] offset:2048\n\t"
        "ds_write_b128 v98, v[44:47] offset:3072\n\t"
        "ds_write_b128 v97, v[48:51] offset:4096\n\t"
        "ds_write_b128 v98, v[52:55] offset:5120\n\t"
        "ds_write_b128 v97, v[56:59] offset:6144\n\t"
        "ds_write_b128 v98, v[60:63] offset:7168\n\t"
        "ds_read_b64_tr_b16 a[48:49], v99 offset:0\n\t"
        "ds_read_b64_tr_b16 a[50:51], v99 offset:256\n\t"
        "ds_read_b64_tr_b16 a[52:53], v99 offset:4096\n\t"
        "ds_read_b64_tr_b16 a[54:55], v99 offset:4352\n\t"
        "s_waitcnt lgkmcnt(8)\n\t"
        "ds_read_b64_tr_b16 a[56:57], v100 offset:0\n\t"
        "ds_read_b64_tr_b16 a[58:59], v100 offset:256\n\t"
        "ds_read_b64_tr_b16 a[60:61], v100 offset:4096\n\t"
        "ds_read_b64_tr_b16 a[62:63], v100 offset:4352\n\t"
        "s_waitcnt lgkmcnt(8)\n\t"
        "ds_read_b64_tr_b16 a[64:65], v101 offset:0\n\t"
        "ds_read_b64_tr_b16 a[66:67], v101 offset:256\n\t"
        "ds_read_b64_tr_b16 a[68:69], v101 offset:4096\n\t"
        "ds_read_b64_tr_b16 a[70:71], v101 offset:4352\n\t"
        "s_waitcnt lgkmcnt(8)\n\t"
        "ds_read_b64_tr_b16 v[104:105], v102 offset:0\n\t"
        "ds_read_b64_tr_b16 v[106:107], v102 offset:256\n\t"
        "ds_read_b64_tr_b16 v[108:109], v102 offset:4096\n\t"
        "ds_read_b64_tr_b16 v[110:111], v102 offset:4352\n\t"
        "s_lshl_b32 s34, s43, 2\n\t"
        "s_add_u32 s34, s34, 5\n\t"
        "s_and_b32 s34, s34, 15\n\t"
        "s_lshl_b32 s34, s34, 19\n\t"
        "buffer_load_dwordx4 a[128:131], v96, s[16:19], s34 offen nt\n\t"
        "s_add_u32 s34, s34, 0x10000\n\t"
        "buffer_load_dwordx4 a[132:135], v96, s[16:19], s34 offen nt\n\t"
        "s_add_u32 s34, s34, 0x10000\n\t"
        "buffer_load_dwordx4 a[136:139], v96, s[16:19], s34 offen nt\n\t"
        "s_add_u32 s34, s34, 0x10000\n\t"
        "buffer_load_dwordx4 a[140:143], v96, s[16:19], s34 offen nt\n\t"
        "s_add_u32 s34, s34, 0x10000\n\t"
        "buffer_load_dwordx4 a[144:147], v96, s[16:19], s34 offen nt\n\t"
        "s_add_u32 s34, s34, 0x10000\n\t"
        "buffer_load_dwordx4 a[148:151], v96, s[16:19], s34 offen nt\n\t"
        "s_add_u32 s34, s34, 0x10000\n\t"
        "buffer_load_dwordx4 a[152:155], v96, s[16:19], s34 offen nt\n\t"
        "s_add_u32 s34, s34, 0x10000\n\t"
        "buffer_load_dwordx4 a[156:159], v96, s[16:19], s34 offen nt\n\t"
        "s_waitcnt lgkmcnt(0)\n\t"
        "ds_write_b128 v117, v[104:107] offset:4096\n\t"
        "ds_write_b128 v117, v[108:111] offset:5120\n\t"
        "s_waitcnt vmcnt(16)\n\t"
        "ds_write_b128 v97, v[64:67] offset:0\n\t"
        "ds_write_b128 v98, v[68:71] offset:1024\n\t"
        "ds_write_b128 v97, v[72:75] offset:2048\n\t"
        "ds_write_b128 v98, v[76:79] offset:3072\n\t"
        "ds_write_b128 v97, v[80:83] offset:4096\n\t"
        "ds_write_b128 v98, v[84:87] offset:5120\n\t"
        "ds_write_b128 v97, v[88:91] offset:6144\n\t"
        "ds_write_b128 v98, v[92:95] offset:7168\n\t"
        "ds_read_b64_tr_b16 a[72:73], v99 offset:0\n\t"
        "ds_read_b64_tr_b16 a[74:75], v99 offset:256\n\t"
        "ds_read_b64_tr_b16 a[76:77], v99 offset:4096\n\t"
        "ds_read_b64_tr_b16 a[78:79], v99 offset:4352\n\t"
        "s_waitcnt lgkmcnt(8)\n\t"
        "ds_read_b64_tr_b16 a[80:81], v100 offset:0\n\t"
        "ds_read_b64_tr_b16 a[82:83], v100 offset:256\n\t"
        "ds_read_b64_tr_b16 a[84:85], v100 offset:4096\n\t"
        "ds_read_b64_tr_b16 a[86:87], v100 offset:4352\n\t"
        "s_waitcnt lgkmcnt(8)\n\t"
        "ds_read_b64_tr_b16 a[88:89], v101 offset:0\n\t"
        "ds_read_b64_tr_b16 a[90:91], v101 offset:256\n\t"
        "ds_read_b64_tr_b16 a[92:93], v101 offset:4096\n\t"
        "ds_read_b64_tr_b16 a[94:95], v101 offset:4352\n\t"
        "s_waitcnt lgkmcnt(8)\n\t"
        "ds_read_b64_tr_b16 v[104:105], v102 offset:0\n\t"
        "ds_read_b64_tr_b16 v[106:107], v102 offset:256\n\t"
        "ds_read_b64_tr_b16 v[108:109], v102 offset:4096\n\t"
        "ds_read_b64_tr_b16 v[110:111], v102 offset:4352\n\t"
        "s_lshl_b32 s34, s43, 2\n\t"
        "s_add_u32 s34, s34, 6\n\t"
        "s_and_b32 s34, s34, 15\n\t"
        "s_lshl_b32 s34, s34, 19\n\t"
        "buffer_load_dwordx4 a[160:163], v96, s[16:19], s34 offen nt\n\t"
        "s_add_u32 s34, s34, 0x10000\n\t"
        "buffer_load_dwordx4 a[164:167], v96, s[16:19], s34 offen nt\n\t"
        "s_add_u32 s34, s34, 0x10000\n\t"
        "buffer_load_dwordx4 a[168:171], v96, s[16:19], s34 offen nt\n\t"
        "s_add_u32 s34, s34, 0x10000\n\t"
        "buffer_load_dwordx4 a[172:175], v96, s[16:19], s34 offen nt\n\t"
        "s_add_u32 s34, s34, 0x10000\n\t"
        "buffer_load_dwordx4 a[176:179], v96, s[16:19], s34 offen nt\n\t"
        "s_add_u32 s34, s34, 0x10000\n\t"
        "buffer_load_dwordx4 a[180:183], v96, s[16:19], s34 offen nt\n\t"
        "s_add_u32 s34, s34, 0x10000\n\t"
        "buffer_load_dwordx4 a[184:187], v96, s[16:19], s34 offen nt\n\t"
        "s_add_u32 s34, s34, 0x10000\n\t"
        "buffer_load_dwordx4 a[188:191], v96, s[16:19], s34 offen nt\n\t"
        "s_waitcnt lgkmcnt(0)\n\t"
        "ds_write_b128 v117, v[104:107] offset:6144\n\t"
        "ds_write_b128 v117, v[108:111] offset:7168\n\t"
        "s_waitcnt vmcnt(16)\n\t"
        "ds_write_b128 v97, a[96:99] offset:0\n\t"
        "ds_write_b128 v98, a[100:103] offset:1024\n\t"
        "ds_write_b128 v97, a[104:107] offset:2048\n\t"
        "ds_write_b128 v98, a[108:111] offset:3072\n\t"
        "ds_write_b128 v97, a[112:115] offset:4096\n\t"
        "ds_write_b128 v98, a[116:119] offset:5120\n\t"
        "ds_write_b128 v97, a[120:123] offset:6144\n\t"
        "ds_write_b128 v98, a[124:127] offset:7168\n\t"
        "ds_read_b64_tr_b16 a[96:97], v99 offset:0\n\t"
        "ds_read_b64_tr_b16 a[98:99], v99 offset:256\n\t"
        "ds_read_b64_tr_b16 a[100:101], v99 offset:4096\n\t"
        "ds_read_b64_tr_b16 a[102:103], v99 offset:4352\n\t"
        "s_waitcnt lgkmcnt(8)\n\t"
        "ds_read_b64_tr_b16 a[104:105], v100 offset:0\n\t"
        "ds_read_b64_tr_b16 a[106:107], v100 offset:256\n\t"
        "ds_read_b64_tr_b16 a[108:109], v100 offset:4096\n\t"
        "ds_read_b64_tr_b16 a[110:111], v100 offset:4352\n\t"
        "s_waitcnt lgkmcnt(8)\n\t"
        "ds_read_b64_tr_b16 a[112:113], v101 offset:0\n\t"
        "ds_read_b64_tr_b16 a[114:115], v101 offset:256\n\t"
        "ds_read_b64_tr_b16 a[116:117], v101 offset:4096\n\t"
        "ds_read_b64_tr_b16 a[118:119], v101 offset:4352\n\t"
        "s_waitcnt lgkmcnt(8)\n\t"
        "ds_read_b64_tr_b16 v[104:105], v102 offset:0\n\t"
        "ds_read_b64_tr_b16 v[106:107], v102 offset:256\n\t"
        "ds_read_b64_tr_b16 v[108:109], v102 offset:4096\n\t"
        "ds_read_b64_tr_b16 v[110:111], v102 offset:4352\n\t"
        "s_lshl_b32 s34, s43, 2\n\t"
        "s_add_u32 s34, s34, 7\n\t"
        "s_and_b32 s34, s34, 15\n\t"
        "s_lshl_b32 s34, s34, 19\n\t"
        "buffer_load_dwordx4 a[192:195], v96, s[16:19], s34 offen nt\n\t"
        "s_add_u32 s34, s34, 0x10000\n\t"
        "buffer_load_dwordx4 a[196:199], v96, s[16:19], s34 offen nt\n\t"
        "s_add_u32 s34, s34, 0x10000\n\t"
        "buffer_load_dwordx4 a[200:203], v96, s[16:19], s34 offen nt\n\t"
        "s_add_u32 s34, s34, 0x10000\n\t"
        "buffer_load_dwordx4 a[204:207], v96, s[16:19], s34 offen nt\n\t"
        "s_add_u32 s34, s34, 0x10000\n\t"
        "buffer_load_dwordx4 a[208:211], v96, s[16:19], s34 offen nt\n\t"
        "s_add_u32 s34, s34, 0x10000\n\t"
        "buffer_load_dwordx4 a[212:215], v96, s[16:19], s34 offen nt\n\t"
        "s_add_u32 s34, s34, 0x10000\n\t"
        "buffer_load_dwordx4 a[216:219], v96, s[16:19], s34 offen nt\n\t"
        "s_add_u32 s34, s34, 0x10000\n\t"
        "buffer_load_dwordx4 a[220:223], v96, s[16:19], s34 offen nt\n\t"
        "s_waitcnt lgkmcnt(0)\n\t"
        "ds_write_b128 v126, v[104:107] offset:0\n\t"
        "ds_write_b128 v126, v[108:111] offset:1024\n\t"
        "s_waitcnt vmcnt(16)\n\t"
        "ds_write_b128 v97, a[128:131] offset:0\n\t"
        "ds_write_b128 v98, a[132:135] offset:1024\n\t"
        "ds_write_b128 v97, a[136:139] offset:2048\n\t"
        "ds_write_b128 v98, a[140:143] offset:3072\n\t"
        "ds_write_b128 v97, a[144:147] offset:4096\n\t"
        "ds_write_b128 v98, a[148:151] offset:5120\n\t"
        "ds_write_b128 v97, a[152:155] offset:6144\n\t"
        "ds_write_b128 v98, a[156:159] offset:7168\n\t"
        "ds_read_b64_tr_b16 a[120:121], v99 offset:0\n\t"
        "ds_read_b64_tr_b16 a[122:123], v99 offset:256\n\t"
        "ds_read_b64_tr_b16 a[124:125], v99 offset:4096\n\t"
        "ds_read_b64_tr_b16 a[126:127], v99 offset:4352\n\t"
        "s_waitcnt lgkmcnt(8)\n\t"
        "ds_read_b64_tr_b16 a[128:129], v100 offset:0\n\t"
        "ds_read_b64_tr_b16 a[130:131], v100 offset:256\n\t"
        "ds_read_b64_tr_b16 a[132:133], v100 offset:4096\n\t"
        "ds_read_b64_tr_b16 a[134:135], v100 offset:4352\n\t"
        "s_waitcnt lgkmcnt(8)\n\t"
        "ds_read_b64_tr_b16 a[136:137], v101 offset:0\n\t"
        "ds_read_b64_tr_b16 a[138:139], v101 offset:256\n\t"
        "ds_read_b64_tr_b16 a[140:141], v101 offset:4096\n\t"
        "ds_read_b64_tr_b16 a[142:143], v101 offset:4352\n\t"
        "s_waitcnt lgkmcnt(8)\n\t"
        "ds_read_b64_tr_b16 v[104:105], v102 offset:0\n\t"
        "ds_read_b64_tr_b16 v[106:107], v102 offset:256\n\t"
        "ds_read_b64_tr_b16 v[108:109], v102 offset:4096\n\t"
        "ds_read_b64_tr_b16 v[110:111], v102 offset:4352\n\t"
        "s_lshl_b32 s34, s43, 2\n\t"
        "s_add_u32 s34, s34, 8\n\t"
        "s_and_b32 s34, s34, 15\n\t"
        "s_lshl_b32 s34, s34, 19\n\t"
        "buffer_load_dwordx4 a[224:227], v96, s[16:19], s34 offen nt\n\t"
        "s_add_u32 s34, s34, 0x10000\n\t"
        "buffer_load_dwordx4 a[228:231], v96, s[16:19], s34 offen nt\n\t"
        "s_add_u32 s34, s34, 0x10000\n\t"
        "buffer_load_dwordx4 a[232:235], v96, s[16:19], s34 offen nt\n\t"
        "s_add_u32 s34, s34, 0x10000\n\t"
        "buffer_load_dwordx4 a[236:239], v96, s[16:19], s34 offen nt\n\t"
        "s_add_u32 s34, s34, 0x10000\n\t"
        "buffer_load_dwordx4 a[240:243], v96, s[16:19], s34 offen nt\n\t"
        "s_add_u32 s34, s34, 0x10000\n\t"
        "buffer_load_dwordx4 a[244:247], v96, s[16:19], s34 offen nt\n\t"
        "s_add_u32 s34, s34, 0x10000\n\t"
        "buffer_load_dwordx4 a[248:251], v96, s[16:19], s34 offen nt\n\t"
        "s_add_u32 s34, s34, 0x10000\n\t"
        "buffer_load_dwordx4 a[252:255], v96, s[16:19], s34 offen nt\n\t"
        "s_waitcnt lgkmcnt(0)\n\t"
        "ds_write_b128 v126, v[104:107] offset:2048\n\t"
        "ds_write_b128 v126, v[108:111] offset:3072\n\t"
        "s_waitcnt vmcnt(16)\n\t"
        "ds_write_b128 v97, a[160:163] offset:0\n\t"
        "ds_write_b128 v98, a[164:167] offset:1024\n\t"
        "ds_write_b128 v97, a[168:171] offset:2048\n\t"
        "ds_write_b128 v98, a[172:175] offset:3072\n\t"
        "ds_write_b128 v97, a[176:179] offset:4096\n\t"
        "ds_write_b128 v98, a[180:183] offset:5120\n\t"
        "ds_write_b128 v97, a[184:187] offset:6144\n\t"
        "ds_write_b128 v98, a[188:191] offset:7168\n\t"
        "ds_read_b64_tr_b16 a[144:145], v99 offset:0\n\t"
        "ds_read_b64_tr_b16 a[146:147], v99 offset:256\n\t"
        "ds_read_b64_tr_b16 a[148:149], v99 offset:4096\n\t"
        "ds_read_b64_tr_b16 a[150:151], v99 offset:4352\n\t"
        "s_waitcnt lgkmcnt(8)\n\t"
        "ds_read_b64_tr_b16 a[152:153], v100 offset:0\n\t"
        "ds_read_b64_tr_b16 a[154:155], v100 offset:256\n\t"
        "ds_read_b64_tr_b16 a[156:157], v100 offset:4096\n\t"
        "ds_read_b64_tr_b16 a[158:159], v100 offset:4352\n\t"
        "s_waitcnt lgkmcnt(8)\n\t"
        "ds_read_b64_tr_b16 a[160:161], v101 offset:0\n\t"
        "ds_read_b64_tr_b16 a[162:163], v101 offset:256\n\t"
        "ds_read_b64_tr_b16 a[164:165], v101 offset:4096\n\t"
        "ds_read_b64_tr_b16 a[166:167], v101 offset:4352\n\t"
        "s_waitcnt lgkmcnt(8)\n\t"
        "ds_read_b64_tr_b16 v[104:105], v102 offset:0\n\t"
        "ds_read_b64_tr_b16 v[106:107], v102 offset:256\n\t"
        "ds_read_b64_tr_b16 v[108:109], v102 offset:4096\n\t"
        "ds_read_b64_tr_b16 v[110:111], v102 offset:4352\n\t"
        "s_lshl_b32 s34, s43, 2\n\t"
        "s_add_u32 s34, s34, 9\n\t"
        "s_and_b32 s34, s34, 15\n\t"
        "s_lshl_b32 s34, s34, 19\n\t"
        "buffer_load_dwordx4 v[128:131], v96, s[16:19], s34 offen nt\n\t"
        "s_add_u32 s34, s34, 0x10000\n\t"
        "buffer_load_dwordx4 v[132:135], v96, s[16:19], s34 offen nt\n\t"
        "s_add_u32 s34, s34, 0x10000\n\t"
        "buffer_load_dwordx4 v[136:139], v96, s[16:19], s34 offen nt\n\t"
        "s_add_u32 s34, s34, 0x10000\n\t"
        "buffer_load_dwordx4 v[140:143], v96, s[16:19], s34 offen nt\n\t"
        "s_add_u32 s34, s34, 0x10000\n\t"
        "buffer_load_dwordx4 v[144:147], v96, s[16:19], s34 offen nt\n\t"
        "s_add_u32 s34, s34, 0x10000\n\t"
        "buffer_load_dwordx4 v[148:151], v96, s[16:19], s34 offen nt\n\t"
        "s_add_u32 s34, s34, 0x10000\n\t"
        "buffer_load_dwordx4 v[152:155], v96, s[16:19], s34 offen nt\n\t"
        "s_add_u32 s34, s34, 0x10000\n\t"
        "buffer_load_dwordx4 v[156:159], v96, s[16:19], s34 offen nt\n\t"
        "s_waitcnt lgkmcnt(0)\n\t"
        "ds_write_b128 v126, v[104:107] offset:4096\n\t"
        "ds_write_b128 v126, v[108:111] offset:5120\n\t"
        "s_waitcnt vmcnt(16)\n\t"
        "ds_write_b128 v97, a[192:195] offset:0\n\t"
        "ds_write_b128 v98, a[196:199] offset:1024\n\t"
        "ds_write_b128 v97, a[200:203] offset:2048\n\t"
        "ds_write_b128 v98, a[204:207] offset:3072\n\t"
        "ds_write_b128 v97, a[208:211] offset:4096\n\t"
        "ds_write_b128 v98, a[212:215] offset:5120\n\t"
        "ds_write_b128 v97, a[216:219] offset:6144\n\t"
        "ds_write_b128 v98, a[220:223] offset:7168\n\t"
        "ds_read_b64_tr_b16 a[168:169], v99 offset:0\n\t"
        "ds_read_b64_tr_b16 a[170:171], v99 offset:256\n\t"
        "ds_read_b64_tr_b16 a[172:173], v99 offset:4096\n\t"
        "ds_read_b64_tr_b16 a[174:175], v99 offset:4352\n\t"
        "s_waitcnt lgkmcnt(8)\n\t"
        "ds_read_b64_tr_b16 a[176:177], v100 offset:0\n\t"
        "ds_read_b64_tr_b16 a[178:179], v100 offset:256\n\t"
        "ds_read_b64_tr_b16 a[180:181], v100 offset:4096\n\t"
        "ds_read_b64_tr_b16 a[182:183], v100 offset:4352\n\t"
        "s_waitcnt lgkmcnt(8)\n\t"
        "ds_read_b64_tr_b16 a[184:185], v101 offset:0\n\t"
        "ds_read_b64_tr_b16 a[186:187], v101 offset:256\n\t"
        "ds_read_b64_tr_b16 a[188:189], v101 offset:4096\n\t"
        "ds_read_b64_tr_b16 a[190:191], v101 offset:4352\n\t"
        "s_waitcnt lgkmcnt(8)\n\t"
        "ds_read_b64_tr_b16 v[104:105], v102 offset:0\n\t"
        "ds_read_b64_tr_b16 v[106:107], v102 offset:256\n\t"
        "ds_read_b64_tr_b16 v[108:109], v102 offset:4096\n\t"
        "ds_read_b64_tr_b16 v[110:111], v102 offset:4352\n\t"
        "s_lshl_b32 s34, s43, 2\n\t"
        "s_add_u32 s34, s34, 10\n\t"
        "s_and_b32 s34, s34, 15\n\t"
        "s_lshl_b32 s34, s34, 19\n\t"
        "buffer_load_dwordx4 v[160:163], v96, s[16:19], s34 offen nt\n\t"
        "s_add_u32 s34, s34, 0x10000\n\t"
        "buffer_load_dwordx4 v[164:167], v96, s[16:19], s34 offen nt\n\t"
        "s_add_u32 s34, s34, 0x10000\n\t"
        "buffer_load_dwordx4 v[168:171], v96, s[16:19], s34 offen nt\n\t"
        "s_add_u32 s34, s34, 0x10000\n\t"
        "buffer_load_dwordx4 v[172:175], v96, s[16:19], s34 offen nt\n\t"
        "s_add_u32 s34, s34, 0x10000\n\t"
        "buffer_load_dwordx4 v[176:179], v96, s[16:19], s34 offen nt\n\t"
        "s_add_u32 s34, s34, 0x10000\n\t"
        "buffer_load_dwordx4 v[180:183], v96, s[16:19], s34 offen nt\n\t"
        "s_add_u32 s34, s34, 0x10000\n\t"
        "buffer_load_dwordx4 v[184:187], v96, s[16:19], s34 offen nt\n\t"
        "s_add_u32 s34, s34, 0x10000\n\t"
        "buffer_load_dwordx4 v[188:191], v96, s[16:19], s34 offen nt\n\t"
        "s_waitcnt lgkmcnt(0)\n\t"
        "ds_write_b128 v126, v[104:107] offset:6144\n\t"
        "ds_write_b128 v126, v[108:111] offset:7168\n\t"
        "s_waitcnt vmcnt(16)\n\t"
        "ds_write_b128 v97, a[224:227] offset:0\n\t"
        "ds_write_b128 v98, a[228:231] offset:1024\n\t"
        "ds_write_b128 v97, a[232:235] offset:2048\n\t"
        "ds_write_b128 v98, a[236:239] offset:3072\n\t"
        "ds_write_b128 v97, a[240:243] offset:4096\n\t"
        "ds_write_b128 v98, a[244:247] offset:5120\n\t"
        "ds_write_b128 v97, a[248:251] offset:6144\n\t"
        "ds_write_b128 v98, a[252:255] offset:7168\n\t"
        "ds_read_b64_tr_b16 a[192:193], v99 offset:0\n\t"
        "ds_read_b64_tr_b16 a[194:195], v99 offset:256\n\t"
        "ds_read_b64_tr_b16 a[196:197], v99 offset:4096\n\t"
        "ds_read_b64_tr_b16 a[198:199], v99 offset:4352\n\t"
        "s_waitcnt lgkmcnt(8)\n\t"
        "ds_read_b64_tr_b16 a[200:201], v100 offset:0\n\t"
        "ds_read_b64_tr_b16 a[202:203], v100 offset:256\n\t"
        "ds_read_b64_tr_b16 a[204:205], v100 offset:4096\n\t"
        "ds_read_b64_tr_b16 a[206:207], v100 offset:4352\n\t"
        "s_waitcnt lgkmcnt(8)\n\t"
        "ds_read_b64_tr_b16 a[208:209], v101 offset:0\n\t"
        "ds_read_b64_tr_b16 a[210:211], v101 offset:256\n\t"
        "ds_read_b64_tr_b16 a[212:213], v101 offset:4096\n\t"
        "ds_read_b64_tr_b16 a[214:215], v101 offset:4352\n\t"
        "s_waitcnt lgkmcnt(8)\n\t"
        "ds_read_b64_tr_b16 v[104:105], v102 offset:0\n\t"
        "ds_read_b64_tr_b16 v[106:107], v102 offset:256\n\t"
        "ds_read_b64_tr_b16 v[108:109], v102 offset:4096\n\t"
        "ds_read_b64_tr_b16 v[110:111], v102 offset:4352\n\t"
        "s_lshl_b32 s34, s43, 2\n\t"
        "s_add_u32 s34, s34, 11\n\t"
        "s_and_b32 s34, s34, 15\n\t"
        "s_lshl_b32 s34, s34, 19\n\t"
        "buffer_load_dwordx4 v[192:195], v96, s[16:19], s34 offen nt\n\t"
        "s_add_u32 s34, s34, 0x10000\n\t"
        "buffer_load_dwordx4 v[196:199], v96, s[16:19], s34 offen nt\n\t"
        "s_add_u32 s34, s34, 0x10000\n\t"
        "buffer_load_dwordx4 v[200:203], v96, s[16:19], s34 offen nt\n\t"
        "s_add_u32 s34, s34, 0x10000\n\t"
        "buffer_load_dwordx4 v[204:207], v96, s[16:19], s34 offen nt\n\t"
        "s_add_u32 s34, s34, 0x10000\n\t"
        "buffer_load_dwordx4 v[208:211], v96, s[16:19], s34 offen nt\n\t"
        "s_add_u32 s34, s34, 0x10000\n\t"
        "buffer_load_dwordx4 v[212:215], v96, s[16:19], s34 offen nt\n\t"
        "s_add_u32 s34, s34, 0x10000\n\t"
        "buffer_load_dwordx4 v[216:219], v96, s[16:19], s34 offen nt\n\t"
        "s_add_u32 s34, s34, 0x10000\n\t"
        "buffer_load_dwordx4 v[220:223], v96, s[16:19], s34 offen nt\n\t"
        "s_waitcnt lgkmcnt(0)\n\t"
        "ds_write_b128 v117, v[104:107] offset:8192\n\t"
        "ds_write_b128 v117, v[108:111] offset:9216\n\t"
        "s_waitcnt vmcnt(16)\n\t"
        "ds_write_b128 v97, v[128:131] offset:0\n\t"
        "ds_write_b128 v98, v[132:135] offset:1024\n\t"
        "ds_write_b128 v97, v[136:139] offset:2048\n\t"
        "ds_write_b128 v98, v[140:143] offset:3072\n\t"
        "ds_write_b128 v97, v[144:147] offset:4096\n\t"
        "ds_write_b128 v98, v[148:151] offset:5120\n\t"
        "ds_write_b128 v97, v[152:155] offset:6144\n\t"
        "ds_write_b128 v98, v[156:159] offset:7168\n\t"
        "ds_read_b64_tr_b16 a[216:217], v99 offset:0\n\t"
        "ds_read_b64_tr_b16 a[218:219], v99 offset:256\n\t"
        "ds_read_b64_tr_b16 a[220:221], v99 offset:4096\n\t"
        "ds_read_b64_tr_b16 a[222:223], v99 offset:4352\n\t"
        "s_waitcnt lgkmcnt(8)\n\t"
        "ds_read_b64_tr_b16 a[224:225], v100 offset:0\n\t"
        "ds_read_b64_tr_b16 a[226:227], v100 offset:256\n\t"
        "ds_read_b64_tr_b16 a[228:229], v100 offset:4096\n\t"
        "ds_read_b64_tr_b16 a[230:231], v100 offset:4352\n\t"
        "s_waitcnt lgkmcnt(8)\n\t"
        "ds_read_b64_tr_b16 a[232:233], v101 offset:0\n\t"
        "ds_read_b64_tr_b16 a[234:235], v101 offset:256\n\t"
        "ds_read_b64_tr_b16 a[236:237], v101 offset:4096\n\t"
        "ds_read_b64_tr_b16 a[238:239], v101 offset:4352\n\t"
        "s_waitcnt lgkmcnt(8)\n\t"
        "ds_read_b64_tr_b16 v[104:105], v102 offset:0\n\t"
        "ds_read_b64_tr_b16 v[106:107], v102 offset:256\n\t"
        "ds_read_b64_tr_b16 v[108:109], v102 offset:4096\n\t"
        "ds_read_b64_tr_b16 v[110:111], v102 offset:4352\n\t"
        "s_lshl_b32 s34, s43, 2\n\t"
        "s_add_u32 s34, s34, 12\n\t"
        "s_and_b32 s34, s34, 15\n\t"
        "s_lshl_b32 s34, s34, 19\n\t"
        "buffer_load_dwordx4 v[224:227], v96, s[16:19], s34 offen nt\n\t"
        "s_add_u32 s34, s34, 0x10000\n\t"
        "buffer_load_dwordx4 v[228:231], v96, s[16:19], s34 offen nt\n\t"
        "s_add_u32 s34, s34, 0x10000\n\t"
        "buffer_load_dwordx4 v[232:235], v96, s[16:19], s34 offen nt\n\t"
        "s_add_u32 s34, s34, 0x10000\n\t"
        "buffer_load_dwordx4 v[236:239], v96, s[16:19], s34 offen nt\n\t"
        "s_add_u32 s34, s34, 0x10000\n\t"
        "buffer_load_dwordx4 v[240:243], v96, s[16:19], s34 offen nt\n\t"
        "s_add_u32 s34, s34, 0x10000\n\t"
        "buffer_load_dwordx4 v[244:247], v96, s[16:19], s34 offen nt\n\t"
        "s_add_u32 s34, s34, 0x10000\n\t"
        "buffer_load_dwordx4 v[248:251], v96, s[16:19], s34 offen nt\n\t"
        "s_add_u32 s34, s34, 0x10000\n\t"
        "buffer_load_dwordx4 v[252:255], v96, s[16:19], s34 offen nt\n\t"
        "s_waitcnt lgkmcnt(0)\n\t"
        "ds_write_b128 v117, v[104:107] offset:10240\n\t"
        "ds_write_b128 v117, v[108:111] offset:11264\n\t"
        "s_waitcnt vmcnt(16)\n\t"
        "ds_write_b128 v97, v[160:163] offset:0\n\t"
        "ds_write_b128 v98, v[164:167] offset:1024\n\t"
        "ds_write_b128 v97, v[168:171] offset:2048\n\t"
        "ds_write_b128 v98, v[172:175] offset:3072\n\t"
        "ds_write_b128 v97, v[176:179] offset:4096\n\t"
        "ds_write_b128 v98, v[180:183] offset:5120\n\t"
        "ds_write_b128 v97, v[184:187] offset:6144\n\t"
        "ds_write_b128 v98, v[188:191] offset:7168\n\t"
        "ds_read_b64_tr_b16 a[240:241], v99 offset:0\n\t"
        "ds_read_b64_tr_b16 a[242:243], v99 offset:256\n\t"
        "ds_read_b64_tr_b16 a[244:245], v99 offset:4096\n\t"
        "ds_read_b64_tr_b16 a[246:247], v99 offset:4352\n\t"
        "s_waitcnt lgkmcnt(8)\n\t"
        "ds_read_b64_tr_b16 a[248:249], v100 offset:0\n\t"
        "ds_read_b64_tr_b16 a[250:251], v100 offset:256\n\t"
        "ds_read_b64_tr_b16 a[252:253], v100 offset:4096\n\t"
        "ds_read_b64_tr_b16 a[254:255], v100 offset:4352\n\t"
        "s_waitcnt lgkmcnt(8)\n\t"
        "ds_read_b64_tr_b16 v[128:129], v101 offset:0\n\t"
        "ds_read_b64_tr_b16 v[130:131], v101 offset:256\n\t"
        "ds_read_b64_tr_b16 v[132:133], v101 offset:4096\n\t"
        "ds_read_b64_tr_b16 v[134:135], v101 offset:4352\n\t"
        "s_waitcnt lgkmcnt(8)\n\t"
        "ds_read_b64_tr_b16 v[104:105], v102 offset:0\n\t"
        "ds_read_b64_tr_b16 v[106:107], v102 offset:256\n\t"
        "ds_read_b64_tr_b16 v[108:109], v102 offset:4096\n\t"
        "ds_read_b64_tr_b16 v[110:111], v102 offset:4352\n\t"
        "s_lshl_b32 s34, s43, 2\n\t"
        "s_add_u32 s34, s34, 13\n\t"
        "s_and_b32 s34, s34, 15\n\t"
        "s_lshl_b32 s34, s34, 19\n\t"
        "buffer_load_dwordx4 v[0:3], v96, s[16:19], s34 offen nt\n\t"
        "s_add_u32 s34, s34, 0x10000\n\t"
        "buffer_load_dwordx4 v[4:7], v96, s[16:19], s34 offen nt\n\t"
        "s_add_u32 s34, s34, 0x10000\n\t"
        "buffer_load_dwordx4 v[8:11], v96, s[16:19], s34 offen nt\n\t"
        "s_add_u32 s34, s34, 0x10000\n\t"
        "buffer_load_dwordx4 v[12:15], v96, s[16:19], s34 offen nt\n\t"
        "s_add_u32 s34, s34, 0x10000\n\t"
        "buffer_load_dwordx4 v[16:19], v96, s[16:19], s34 offen nt\n\t"
        "s_add_u32 s34, s34, 0x10000\n\t"
        "buffer_load_dwordx4 v[20:23], v96, s[16:19], s34 offen nt\n\t"
        "s_add_u32 s34, s34, 0x10000\n\t"
        "buffer_load_dwordx4 v[24:27], v96, s[16:19], s34 offen nt\n\t"
        "s_add_u32 s34, s34, 0x10000\n\t"
        "buffer_load_dwordx4 v[28:31], v96, s[16:19], s34 offen nt\n\t"
        "s_waitcnt lgkmcnt(0)\n\t"
        "ds_write_b128 v117, v[104:107] offset:12288\n\t"
        "ds_write_b128 v117, v[108:111] offset:13312\n\t"
        "s_waitcnt vmcnt(16)\n\t"
        "ds_write_b128 v97, v[192:195] offset:0\n\t"
        "ds_write_b128 v98, v[196:199] offset:1024\n\t"
        "ds_write_b128 v97, v[200:203] offset:2048\n\t"
        "ds_write_b128 v98, v[204:207] offset:3072\n\t"
        "ds_write_b128 v97, v[208:211] offset:4096\n\t"
        "ds_write_b128 v98, v[212:215] offset:5120\n\t"
        "ds_write_b128 v97, v[216:219] offset:6144\n\t"
        "ds_write_b128 v98, v[220:223] offset:7168\n\t"
        "ds_read_b64_tr_b16 v[136:137], v99 offset:0\n\t"
        "ds_read_b64_tr_b16 v[138:139], v99 offset:256\n\t"
        "ds_read_b64_tr_b16 v[140:141], v99 offset:4096\n\t"
        "ds_read_b64_tr_b16 v[142:143], v99 offset:4352\n\t"
        "s_waitcnt lgkmcnt(8)\n\t"
        "ds_read_b64_tr_b16 v[144:145], v100 offset:0\n\t"
        "ds_read_b64_tr_b16 v[146:147], v100 offset:256\n\t"
        "ds_read_b64_tr_b16 v[148:149], v100 offset:4096\n\t"
        "ds_read_b64_tr_b16 v[150:151], v100 offset:4352\n\t"
        "s_waitcnt lgkmcnt(8)\n\t"
        "ds_read_b64_tr_b16 v[152:153], v101 offset:0\n\t"
        "ds_read_b64_tr_b16 v[154:155], v101 offset:256\n\t"
        "ds_read_b64_tr_b16 v[156:157], v101 offset:4096\n\t"
        "ds_read_b64_tr_b16 v[158:159], v101 offset:4352\n\t"
        "s_waitcnt lgkmcnt(8)\n\t"
        "ds_read_b64_tr_b16 v[104:105], v102 offset:0\n\t"
        "ds_read_b64_tr_b16 v[106:107], v102 offset:256\n\t"
        "ds_read_b64_tr_b16 v[108:109], v102 offset:4096\n\t"
        "ds_read_b64_tr_b16 v[110:111], v102 offset:4352\n\t"
        "s_lshl_b32 s34, s43, 2\n\t"
        "s_add_u32 s34, s34, 14\n\t"
        "s_and_b32 s34, s34, 15\n\t"
        "s_lshl_b32 s34, s34, 19\n\t"
        "buffer_load_dwordx4 v[32:35], v96, s[16:19], s34 offen nt\n\t"
        "s_add_u32 s34, s34, 0x10000\n\t"
        "buffer_load_dwordx4 v[36:39], v96, s[16:19], s34 offen nt\n\t"
        "s_add_u32 s34, s34, 0x10000\n\t"
        "buffer_load_dwordx4 v[40:43], v96, s[16:19], s34 offen nt\n\t"
        "s_add_u32 s34, s34, 0x10000\n\t"
        "buffer_load_dwordx4 v[44:47], v96, s[16:19], s34 offen nt\n\t"
        "s_add_u32 s34, s34, 0x10000\n\t"
        "buffer_load_dwordx4 v[48:51], v96, s[16:19], s34 offen nt\n\t"
        "s_add_u32 s34, s34, 0x10000\n\t"
        "buffer_load_dwordx4 v[52:55], v96, s[16:19], s34 offen nt\n\t"
        "s_add_u32 s34, s34, 0x10000\n\t"
        "buffer_load_dwordx4 v[56:59], v96, s[16:19], s34 offen nt\n\t"
        "s_add_u32 s34, s34, 0x10000\n\t"
        "buffer_load_dwordx4 v[60:63], v96, s[16:19], s34 offen nt\n\t"
        "s_waitcnt lgkmcnt(0)\n\t"
        "ds_write_b128 v117, v[104:107] offset:14336\n\t"
        "ds_write_b128 v117, v[108:111] offset:15360\n\t"
        "s_waitcnt vmcnt(16)\n\t"
        "ds_write_b128 v97, v[224:227] offset:0\n\t"
        "ds_write_b128 v98, v[228:231] offset:1024\n\t"
        "ds_write_b128 v97, v[232:235] offset:2048\n\t"
        "ds_write_b128 v98, v[236:239] offset:3072\n\t"
        "ds_write_b128 v97, v[240:243] offset:4096\n\t"
        "ds_write_b128 v98, v[244:247] offset:5120\n\t"
        "ds_write_b128 v97, v[248:251] offset:6144\n\t"
        "ds_write_b128 v98, v[252:255] offset:7168\n\t"
        "ds_read_b64_tr_b16 v[160:161], v99 offset:0\n\t"
        "ds_read_b64_tr_b16 v[162:163], v99 offset:256\n\t"
        "ds_read_b64_tr_b16 v[164:165], v99 offset:4096\n\t"
        "ds_read_b64_tr_b16 v[166:167], v99 offset:4352\n\t"
        "s_waitcnt lgkmcnt(8)\n\t"
        "ds_read_b64_tr_b16 v[168:169], v100 offset:0\n\t"
        "ds_read_b64_tr_b16 v[170:171], v100 offset:256\n\t"
        "ds_read_b64_tr_b16 v[172:173], v100 offset:4096\n\t"
        "ds_read_b64_tr_b16 v[174:175], v100 offset:4352\n\t"
        "s_waitcnt lgkmcnt(8)\n\t"
        "ds_read_b64_tr_b16 v[176:177], v101 offset:0\n\t"
        "ds_read_b64_tr_b16 v[178:179], v101 offset:256\n\t"
        "ds_read_b64_tr_b16 v[180:181], v101 offset:4096\n\t"
        "ds_read_b64_tr_b16 v[182:183], v101 offset:4352\n\t"
        "s_waitcnt lgkmcnt(8)\n\t"
        "ds_read_b64_tr_b16 v[104:105], v102 offset:0\n\t"
        "ds_read_b64_tr_b16 v[106:107], v102 offset:256\n\t"
        "ds_read_b64_tr_b16 v[108:109], v102 offset:4096\n\t"
        "ds_read_b64_tr_b16 v[110:111], v102 offset:4352\n\t"
        "s_lshl_b32 s34, s43, 2\n\t"
        "s_add_u32 s34, s34, 15\n\t"
        "s_and_b32 s34, s34, 15\n\t"
        "s_lshl_b32 s34, s34, 19\n\t"
        "buffer_load_dwordx4 v[64:67], v96, s[16:19], s34 offen nt\n\t"
        "s_add_u32 s34, s34, 0x10000\n\t"
        "buffer_load_dwordx4 v[68:71], v96, s[16:19], s34 offen nt\n\t"
        "s_add_u32 s34, s34, 0x10000\n\t"
        "buffer_load_dwordx4 v[72:75], v96, s[16:19], s34 offen nt\n\t"
        "s_add_u32 s34, s34, 0x10000\n\t"
        "buffer_load_dwordx4 v[76:79], v96, s[16:19], s34 offen nt\n\t"
        "s_add_u32 s34, s34, 0x10000\n\t"
        "buffer_load_dwordx4 v[80:83], v96, s[16:19], s34 offen nt\n\t"
        "s_add_u32 s34, s34, 0x10000\n\t"
        "buffer_load_dwordx4 v[84:87], v96, s[16:19], s34 offen nt\n\t"
        "s_add_u32 s34, s34, 0x10000\n\t"
        "buffer_load_dwordx4 v[88:91], v96, s[16:19], s34 offen nt\n\t"
        "s_add_u32 s34, s34, 0x10000\n\t"
        "buffer_load_dwordx4 v[92:95], v96, s[16:19], s34 offen nt\n\t"
        "s_waitcnt lgkmcnt(0)\n\t"
        "ds_write_b128 v117, v[104:107] offset:16384\n\t"
        "ds_write_b128 v117, v[108:111] offset:17408\n\t"
        "s_waitcnt vmcnt(16)\n\t"
        "ds_write_b128 v97, v[0:3] offset:0\n\t"
        "ds_write_b128 v98, v[4:7] offset:1024\n\t"
        "ds_write_b128 v97, v[8:11] offset:2048\n\t"
        "ds_write_b128 v98, v[12:15] offset:3072\n\t"
        "ds_write_b128 v97, v[16:19] offset:4096\n\t"
        "ds_write_b128 v98, v[20:23] offset:5120\n\t"
        "ds_write_b128 v97, v[24:27] offset:6144\n\t"
        "ds_write_b128 v98, v[28:31] offset:7168\n\t"
        "ds_read_b64_tr_b16 v[184:185], v99 offset:0\n\t"
        "ds_read_b64_tr_b16 v[186:187], v99 offset:256\n\t"
        "ds_read_b64_tr_b16 v[188:189], v99 offset:4096\n\t"
        "ds_read_b64_tr_b16 v[190:191], v99 offset:4352\n\t"
        "s_waitcnt lgkmcnt(8)\n\t"
        "ds_read_b64_tr_b16 v[192:193], v100 offset:0\n\t"
        "ds_read_b64_tr_b16 v[194:195], v100 offset:256\n\t"
        "ds_read_b64_tr_b16 v[196:197], v100 offset:4096\n\t"
        "ds_read_b64_tr_b16 v[198:199], v100 offset:4352\n\t"
        "s_waitcnt lgkmcnt(8)\n\t"
        "ds_read_b64_tr_b16 v[200:201], v101 offset:0\n\t"
        "ds_read_b64_tr_b16 v[202:203], v101 offset:256\n\t"
        "ds_read_b64_tr_b16 v[204:205], v101 offset:4096\n\t"
        "ds_read_b64_tr_b16 v[206:207], v101 offset:4352\n\t"
        "s_waitcnt lgkmcnt(8)\n\t"
        "ds_read_b64_tr_b16 v[104:105], v102 offset:0\n\t"
        "ds_read_b64_tr_b16 v[106:107], v102 offset:256\n\t"
        "ds_read_b64_tr_b16 v[108:109], v102 offset:4096\n\t"
        "ds_read_b64_tr_b16 v[110:111], v102 offset:4352\n\t"
        "s_waitcnt lgkmcnt(0)\n\t"
        "ds_write_b128 v117, v[104:107] offset:18432\n\t"
        "ds_write_b128 v117, v[108:111] offset:19456\n\t"
        "s_waitcnt vmcnt(8)\n\t"
        "ds_write_b128 v97, v[32:35] offset:0\n\t"
        "ds_write_b128 v98, v[36:39] offset:1024\n\t"
        "ds_write_b128 v97, v[40:43] offset:2048\n\t"
        "ds_write_b128 v98, v[44:47] offset:3072\n\t"
        "ds_write_b128 v97, v[48:51] offset:4096\n\t"
        "ds_write_b128 v98, v[52:55] offset:5120\n\t"
        "ds_write_b128 v97, v[56:59] offset:6144\n\t"
        "ds_write_b128 v98, v[60:63] offset:7168\n\t"
        "ds_read_b64_tr_b16 v[208:209], v99 offset:0\n\t"
        "ds_read_b64_tr_b16 v[210:211], v99 offset:256\n\t"
        "ds_read_b64_tr_b16 v[212:213], v99 offset:4096\n\t"
        "ds_read_b64_tr_b16 v[214:215], v99 offset:4352\n\t"
        "s_waitcnt lgkmcnt(8)\n\t"
        "ds_read_b64_tr_b16 v[216:217], v100 offset:0\n\t"
        "ds_read_b64_tr_b16 v[218:219], v100 offset:256\n\t"
        "ds_read_b64_tr_b16 v[220:221], v100 offset:4096\n\t"
        "ds_read_b64_tr_b16 v[222:223], v100 offset:4352\n\t"
        "s_waitcnt lgkmcnt(8)\n\t"
        "ds_read_b64_tr_b16 v[224:225], v101 offset:0\n\t"
        "ds_read_b64_tr_b16 v[226:227], v101 offset:256\n\t"
        "ds_read_b64_tr_b16 v[228:229], v101 offset:4096\n\t"
        "ds_read_b64_tr_b16 v[230:231], v101 offset:4352\n\t"
        "s_waitcnt lgkmcnt(8)\n\t"
        "ds_read_b64_tr_b16 v[104:105], v102 offset:0\n\t"
        "ds_read_b64_tr_b16 v[106:107], v102 offset:256\n\t"
        "ds_read_b64_tr_b16 v[108:109], v102 offset:4096\n\t"
        "ds_read_b64_tr_b16 v[110:111], v102 offset:4352\n\t"
        "s_mov_b32 s35, 33792\n\t"
        "buffer_load_dword v0, off, s[12:15], s35 sc1\n\t"
        "s_waitcnt lgkmcnt(0)\n\t"
        "ds_write_b128 v117, v[104:107] offset:20480\n\t"
        "ds_write_b128 v117, v[108:111] offset:21504\n\t"
        "s_waitcnt vmcnt(0)\n\t"
        "ds_write_b128 v97, v[64:67] offset:0\n\t"
        "ds_write_b128 v98, v[68:71] offset:1024\n\t"
        "ds_write_b128 v97, v[72:75] offset:2048\n\t"
        "ds_write_b128 v98, v[76:79] offset:3072\n\t"
        "ds_write_b128 v97, v[80:83] offset:4096\n\t"
        "ds_write_b128 v98, v[84:87] offset:5120\n\t"
        "ds_write_b128 v97, v[88:91] offset:6144\n\t"
        "ds_write_b128 v98, v[92:95] offset:7168\n\t"
        "ds_read_b64_tr_b16 v[232:233], v99 offset:0\n\t"
        "ds_read_b64_tr_b16 v[234:235], v99 offset:256\n\t"
        "ds_read_b64_tr_b16 v[236:237], v99 offset:4096\n\t"
        "ds_read_b64_tr_b16 v[238:239], v99 offset:4352\n\t"
        "s_waitcnt lgkmcnt(8)\n\t"
        "ds_read_b64_tr_b16 v[240:241], v100 offset:0\n\t"
        "ds_read_b64_tr_b16 v[242:243], v100 offset:256\n\t"
        "ds_read_b64_tr_b16 v[244:245], v100 offset:4096\n\t"
        "ds_read_b64_tr_b16 v[246:247], v100 offset:4352\n\t"
        "s_waitcnt lgkmcnt(8)\n\t"
        "ds_read_b64_tr_b16 v[248:249], v101 offset:0\n\t"
        "ds_read_b64_tr_b16 v[250:251], v101 offset:256\n\t"
        "ds_read_b64_tr_b16 v[252:253], v101 offset:4096\n\t"
        "ds_read_b64_tr_b16 v[254:255], v101 offset:4352\n\t"
        "s_waitcnt lgkmcnt(8)\n\t"
        "ds_read_b64_tr_b16 v[104:105], v102 offset:0\n\t"
        "ds_read_b64_tr_b16 v[106:107], v102 offset:256\n\t"
        "ds_read_b64_tr_b16 v[108:109], v102 offset:4096\n\t"
        "ds_read_b64_tr_b16 v[110:111], v102 offset:4352\n\t"
        "s_mov_b32 s42, 0\n\t"
        "LA_cal%=:\n\t"
        "v_readfirstlane_b32 s34, v0\n\t"
        "s_nop 0\n\t"
        "s_cmp_lg_u32 s34, 0\n\t"
        "s_cbranch_scc1 LA_calok%=\n\t"
        "s_sleep 4\n\t"
        "s_mov_b32 s35, 33792\n\t"
        "buffer_load_dword v0, off, s[12:15], s35 sc1\n\t"
        "s_waitcnt vmcnt(0)\n\t"
        "s_add_u32 s42, s42, 1\n\t"
        "s_cmp_lt_u32 s42, 0x100000\n\t"
        "s_cbranch_scc1 LA_cal%=\n\t"
        "s_movk_i32 s34, 8\n\t"
        "s_branch LA_die%=\n\t"
        "LA_calok%=:\n\t"
        "s_lshl_b32 s35, s44, 10\n\t"
        "v_lshlrev_b32_e32 v1, 2, v122\n\t"
        "v_and_b32_e32 v1, 0x3c0, v1\n\t"
        "buffer_load_dword v1, v1, s[12:15], s35 offen offset:0 sc1\n\t"
        "s_waitcnt lgkmcnt(0)\n\t"
        "ds_write_b128 v126, v[104:107] offset:8192\n\t"
        "ds_write_b128 v126, v[108:111] offset:9216\n\t"
        "s_waitcnt lgkmcnt(0)\n\t"
        "v_mov_b32_e32 v110, v103\n\t"
        "ds_read_b128 v[64:67], v126 offset:0\n\t"
        "ds_read_b128 v[68:71], v126 offset:1024\n\t"
        "ds_read_b128 v[72:75], v126 offset:2048\n\t"
        "ds_read_b128 v[76:79], v126 offset:3072\n\t"
        "ds_read_b128 v[80:83], v126 offset:4096\n\t"
        "ds_read_b128 v[84:87], v126 offset:5120\n\t"
        "ds_read_b128 v[88:91], v126 offset:6144\n\t"
        "ds_read_b128 v[92:95], v126 offset:7168\n\t"
        "ds_read_b128 v[96:99], v126 offset:8192\n\t"
        "ds_read_b128 v[100:103], v126 offset:9216\n\t"
        "s_waitcnt lgkmcnt(0)\n\t"
        "v_mov_b32_e32 v126, 0\n\t"
        "s_barrier\n\t"
        "s_mov_b32 s48, 0\n\t"
        "s_mov_b32 s54, 0\n\t"
        "s_mov_b32 s0, 0x3f803f80\n\t"
        "s_mov_b32 s1, s0\n\t"
        "s_movk_i32 s56, 24576\n\t"
        "s_and_b32 s58, s43, 1\n\t"
        "s_lshl_b32 s58, s58, 12\n\t"
        "s_lshl_b32 s57, s44, 5\n\t"
        "s_add_u32 s57, s57, s43\n\t"
        "s_lshl_b32 s57, s57, 2\n\t"
        "s_add_u32 s57, s57, 458752\n\t"
        "s_waitcnt vmcnt(0)\n\t"
        "v_mov_b32_e32 v111, v1\n\t"
        "v_readfirstlane_b32 s34, v0\n\t"
        "s_nop 0\n\t"
        "s_and_b32 s34, s34, 8192\n\t"
        "s_add_u32 s8, s8, s34\n\t"
        "s_addc_u32 s9, s9, 0\n\t"
        "s_barrier\n\t"
        "s_cmp_lg_u32 s43, 0\n\t"
        "s_cbranch_scc1 LA_st%=\n\t"
        "v_mov_b32_e32 v2, 1\n\t"
        "s_mov_b32 s35, 34304\n\t"
        "s_mov_b64 exec, 1\n\t"
        "buffer_atomic_add v2, off, s[12:15], s35 sc0\n\t"
        "s_mov_b64 exec, -1\n\t"
        "s_waitcnt vmcnt(0)\n\t"
        "v_readfirstlane_b32 s34, v2\n\t"
        "s_nop 0\n\t"
        "s_cmp_lg_u32 s34, 95\n\t"
        "s_cbranch_scc1 LA_st%=\n\t"
        "s_memrealtime s[32:33]\n\t"
        "v_mbcnt_lo_u32_b32 v3, -1, 0\n\t"
        "v_lshlrev_b32_e32 v3, 7, v3\n\t"
        "s_waitcnt lgkmcnt(0)\n\t"
        "s_add_u32 s34, s32, 50\n\t"
        "s_or_b32 s34, s34, 1\n\t"
        "v_mov_b32_e32 v2, s34\n\t"
        "s_mov_b32 s35, 36864\n\t"
        "s_mov_b64 exec, 0xffff\n\t"
        "buffer_store_dword v2, v3, s[12:15], s35 offen sc1\n\t"
        "s_mov_b64 exec, -1\n\t"
        "LA_st%=:\n\t"
        "s_mov_b32 s24, 0\n\t"
        "s_mov_b32 s26, 0\n\t"
        "s_mov_b32 s27, 0x80000\n\t"
        "s_mov_b32 s28, 0x180000\n\t"
        "s_mov_b32 s31, 0\n\t"
        "s_mov_b32 s47, 0\n\t"
        "s_mov_b32 s38, 0xff00ff00\n\t"
        "s_mov_b32 s39, 0xff00ff00\n\t"
        "s_mov_b32 s40, 0\n\t"
        "s_mov_b32 s41, -1\n\t"
        "ds_read_b128 v[32:35], v117 offset:0\n\t"
        "ds_read_b128 v[36:39], v117 offset:1024\n\t"
        "ds_read_b128 v[40:43], v117 offset:2048\n\t"
        "ds_read_b128 v[44:47], v117 offset:3072\n\t"
        "ds_read_b128 v[48:51], v117 offset:4096\n\t"
        "ds_read_b128 v[52:55], v117 offset:5120\n\t"
        "ds_read_b128 v[56:59], v117 offset:6144\n\t"
        "ds_read_b128 v[60:63], v117 offset:7168\n\t"
        "v_mov_b64_e32 v[0:1], 0\n\t"
        "v_mov_b64_e32 v[2:3], 0\n\t"
        "v_mov_b64_e32 v[4:5], 0\n\t"
        "v_mov_b64_e32 v[6:7], 0\n\t"
        "v_mov_b64_e32 v[8:9], 0\n\t"
        "v_mov_b64_e32 v[10:11], 0\n\t"
        "v_mov_b64_e32 v[12:13], 0\n\t"
        "v_mov_b64_e32 v[14:15], 0\n\t"
        "s_lshr_b32 s59, s48, 1\n\t"
        "s_cmp_gt_u32 s54, s59\n\t"
        "s_cbranch_scc1 LA_kv0ok%=\n\t"
        "s_mov_b32 s42, 0\n\t"
        "s_waitcnt vmcnt(0)\n\t"
        "s_branch LA_kv0w%=\n\t"
        "LA_kv0%=:\n\t"
        "s_waitcnt vmcnt(0)\n\t"
        "LA_kv0w%=:\n\t"
        "v_min_u32_dpp v111, v111, v111 quad_perm:[1,0,3,2] row_mask:0xf bank_mask:0xf\n\t"
        "s_nop 1\n\t"
        "v_min_u32_dpp v111, v111, v111 quad_perm:[2,3,0,1] row_mask:0xf bank_mask:0xf\n\t"
        "s_nop 1\n\t"
        "v_min_u32_dpp v111, v111, v111 row_ror:4 row_mask:0xf bank_mask:0xf\n\t"
        "s_nop 1\n\t"
        "v_min_u32_dpp v111, v111, v111 row_ror:8 row_mask:0xf bank_mask:0xf\n\t"
        "s_nop 0\n\t"
        "v_readfirstlane_b32 s54, v111\n\t"
        "s_nop 0\n\t"
        "s_cmp_gt_u32 s54, s59\n\t"
        "s_cbranch_scc1 LA_kv0ok%=\n\t"
        "s_sleep 4\n\t"
        "s_lshl_b32 s35, s44, 10\n\t"
        "v_lshlrev_b32_e32 v111, 2, v122\n\t"
        "v_and_b32_e32 v111, 0x3c0, v111\n\t"
        "buffer_load_dword v111, v111, s[12:15], s35 offen offset:0 sc1\n\t"
        "s_add_u32 s42, s42, 1\n\t"
        "s_cmp_lt_u32 s42, 0x100000\n\t"
        "s_cbranch_scc1 LA_kv0%=\n\t"
        "s_movk_i32 s34, 16\n\t"
        "s_branch LA_die%=\n\t"
        "LA_kv0ok%=:\n\t"
        "s_and_b32 s36, s48, 1\n\t"
        "s_mul_i32 s36, s36, 24576\n\t"
        "s_add_u32 s36, s36, s55\n\t"
        "s_lshl_b32 s37, s48, 18\n\t"
        "s_lshl_b32 s34, s43, 11\n\t"
        "s_add_u32 s37, s37, s34\n\t"
        "s_add_u32 m0, s36, 0\n\t"
        "s_add_u32 s34, s37, 29548544\n\t"
        "buffer_load_dwordx4 v122, s[12:15], s34 offen sc1 lds\n\t"
        "s_add_u32 m0, s36, 1024\n\t"
        "s_add_u32 s34, s37, 29549568\n\t"
        "buffer_load_dwordx4 v122, s[12:15], s34 offen sc1 lds\n\t"
        "s_add_u32 m0, s36, 8192\n\t"
        "s_add_u32 s34, s37, 12771328\n\t"
        "buffer_load_dwordx4 v122, s[12:15], s34 offen sc1 lds\n\t"
        "s_add_u32 m0, s36, 9216\n\t"
        "s_add_u32 s34, s37, 12772352\n\t"
        "buffer_load_dwordx4 v122, s[12:15], s34 offen sc1 lds\n\t"
        "s_add_u32 m0, s36, 16384\n\t"
        "s_add_u32 s34, s37, 46325760\n\t"
        "buffer_load_dwordx4 v122, s[12:15], s34 offen sc1 lds\n\t"
        "s_add_u32 m0, s36, 17408\n\t"
        "s_add_u32 s34, s37, 46326784\n\t"
        "buffer_load_dwordx4 v122, s[12:15], s34 offen sc1 lds\n\t"
        "s_mov_b32 s42, 0\n\t"
        "LA_gw%=:\n\t"
        "buffer_load_dword v16, off, s[12:15], s45 sc1\n\t"
        "s_waitcnt vmcnt(0)\n\t"
        "v_readfirstlane_b32 s25, v16\n\t"
        "s_nop 0\n\t"
        "s_cmp_lg_u32 s25, 0\n\t"
        "s_cbranch_scc1 LA_loop%=\n\t"
        "s_sleep 2\n\t"
        "s_add_u32 s42, s42, 1\n\t"
        "s_cmp_lt_u32 s42, 0x100000\n\t"
        "s_cbranch_scc1 LA_gw%=\n\t"
        "s_movk_i32 s34, 9\n\t"
        "s_branch LA_die%=\n\t"
        ".p2align 8\n\t"
        "LA_loop%=:\n\t"
        "s_mov_b32 s42, 0\n\t"
        "s_sub_u32 s35, s25, 8\n\t"
        "s_waitcnt lgkmcnt(0)\n\t"
        "s_memrealtime s[2:3]\n\t"
        "s_memrealtime s[4:5]\n\t"
        "s_memrealtime s[6:7]\n\t"
        "LA_wa%=:\n\t"
        "s_memrealtime s[32:33]\n\t"
        "s_waitcnt lgkmcnt(6)\n\t"
        "s_sub_u32 s34, s35, s2\n\t"
        "s_cmpk_lt_u32 s34, 0x2000\n\t"
        "s_cbranch_scc0 LA_wa%=x\n\t"
        "s_memrealtime s[2:3]\n\t"
        "s_waitcnt lgkmcnt(6)\n\t"
        "s_sub_u32 s34, s35, s4\n\t"
        "s_cmpk_lt_u32 s34, 0x2000\n\t"
        "s_cbranch_scc0 LA_wa%=x\n\t"
        "s_memrealtime s[4:5]\n\t"
        "s_waitcnt lgkmcnt(6)\n\t"
        "s_sub_u32 s34, s35, s6\n\t"
        "s_cmpk_lt_u32 s34, 0x2000\n\t"
        "s_cbranch_scc0 LA_wa%=x\n\t"
        "s_memrealtime s[6:7]\n\t"
        "s_waitcnt lgkmcnt(6)\n\t"
        "s_sub_u32 s34, s35, s32\n\t"
        "s_cmpk_lt_u32 s34, 0x2000\n\t"
        "s_cbranch_scc1 LA_wa%=\n\t"
        "s_nop 3\n\t"
        "LA_wa%=x:\n\t"
        "s_add_i32 s34, s34, 4\n\t"
        "s_max_i32 s34, s34, 0\n\t"
        "s_bitcmp1_b32 s34, 1\n\t"
        "s_cbranch_scc0 LA_wa%=y\n\t"
        "s_nop 15\n\t"
        "LA_wa%=y:\n\t"
        "s_bitcmp1_b32 s34, 0\n\t"
        "s_cbranch_scc0 LA_wa%=z\n\t"
        "s_nop 9\n\t"
        "LA_wa%=z:\n\t"
        "LA_rd%=:\n\t"
        "buffer_load_dwordx4 v[16:19], v112, s[8:11], s26 offen offset:0 sc1\n\t"
        "s_nop 12\n\t"
        "buffer_load_dwordx4 v[20:23], v112, s[8:11], s26 offen offset:1024 sc1\n\t"
        "s_nop 12\n\t"
        "buffer_load_dwordx4 v[24:27], v112, s[8:11], s26 offen offset:2048 sc1\n\t"
        "s_nop 12\n\t"
        "buffer_load_dwordx4 v[28:31], v112, s[8:11], s26 offen offset:3072 sc1\n\t"
        "LA_go%=:\n\t"
        "s_waitcnt vmcnt(3)\n\t"
        "v_smfmac_f32_16x16x64_bf16 v[0:3], v[16:19], a[0:7], v110\n\t"
        "ds_write_b128 v113, v[16:19] offset:0\n\t"
        "v_smfmac_f32_16x16x64_bf16 v[4:7], v[16:19], a[8:15], v110\n\t"
        "v_max3_u32 v111, v16, v17, v18\n\t"
        "v_max_u32_e32 v111, v19, v111\n\t"
        "v_smfmac_f32_16x16x64_bf16 v[8:11], v[16:19], a[16:23], v110\n\t"
        "v_smfmac_f32_16x16x64_bf16 v[12:15], v[16:19], v[32:39], v110\n\t"
        "s_waitcnt vmcnt(2)\n\t"
        "v_smfmac_f32_16x16x64_bf16 v[0:3], v[20:23], a[24:31], v110\n\t"
        "ds_write_b128 v113, v[20:23] offset:1024\n\t"
        "v_smfmac_f32_16x16x64_bf16 v[4:7], v[20:23], a[32:39], v110\n\t"
        "v_max3_u32 v111, v20, v21, v111\n\t"
        "v_max3_u32 v111, v22, v23, v111\n\t"
        "v_smfmac_f32_16x16x64_bf16 v[8:11], v[20:23], a[40:47], v110\n\t"
        "s_waitcnt vmcnt(1)\n\t"
        "ds_write_b128 v113, v[24:27] offset:2048\n\t"
        "v_smfmac_f32_16x16x64_bf16 v[12:15], v[20:23], v[40:47], v110\n\t"
        "v_max3_u32 v111, v24, v25, v111\n\t"
        "v_max3_u32 v111, v26, v27, v111\n\t"
        "v_smfmac_f32_16x16x64_bf16 v[0:3], v[24:27], a[48:55], v110\n\t"
        "v_smfmac_f32_16x16x64_bf16 v[4:7], v[24:27], a[56:63], v110\n\t"
        "s_waitcnt vmcnt(0)\n\t"
        "ds_write_b128 v113, v[28:31] offset:3072\n\t"
        "v_max3_u32 v111, v28, v29, v111\n\t"
        "v_max3_u32 v111, v30, v31, v111\n\t"
        "v_smfmac_f32_16x16x64_bf16 v[8:11], v[24:27], a[64:71], v110\n\t"
        "v_cmp_eq_u32_e32 vcc, -1, v111\n\t"
        "v_smfmac_f32_16x16x64_bf16 v[12:15], v[24:27], v[48:55], v110\n\t"
        "s_waitcnt lgkmcnt(0)\n\t"
        "s_cbranch_vccnz LA_rdfail%=\n\t"
        "s_barrier\n\t"
        "ds_read_b128 v[16:19], v114 offset:0\n\t"
        "ds_read_b128 v[20:23], v114 offset:1024\n\t"
        "ds_read_b128 v[24:27], v114 offset:2048\n\t"
        "v_smfmac_f32_16x16x64_bf16 v[0:3], v[28:31], a[72:79], v110\n\t"
        "v_smfmac_f32_16x16x64_bf16 v[4:7], v[28:31], a[80:87], v110\n\t"
        "v_smfmac_f32_16x16x64_bf16 v[8:11], v[28:31], a[88:95], v110\n\t"
        "v_smfmac_f32_16x16x64_bf16 v[12:15], v[28:31], v[56:63], v110\n\t"
        "ds_read_b128 v[28:31], v114 offset:3072\n\t"
        "s_waitcnt lgkmcnt(3)\n\t"
        "v_smfmac_f32_16x16x64_bf16 v[12:15], v[16:19], v[64:71], v110\n\t"
        "ds_read_b128 v[32:35], v117 offset:8192\n\t"
        "v_smfmac_f32_16x16x64_bf16 v[0:3], v[16:19], a[96:103], v110\n\t"
        "ds_read_b128 v[36:39], v117 offset:9216\n\t"
        "v_smfmac_f32_16x16x64_bf16 v[4:7], v[16:19], a[104:111], v110\n\t"
        "s_waitcnt lgkmcnt(4)\n\t"
        "v_smfmac_f32_16x16x64_bf16 v[8:11], v[16:19], a[112:119], v110\n\t"
        "ds_read_b128 v[16:19], v115 offset:0\n\t"
        "v_smfmac_f32_16x16x64_bf16 v[12:15], v[20:23], v[72:79], v110\n\t"
        "ds_read_b128 v[40:43], v117 offset:10240\n\t"
        "v_smfmac_f32_16x16x64_bf16 v[0:3], v[20:23], a[120:127], v110\n\t"
        "ds_read_b128 v[44:47], v117 offset:11264\n\t"
        "v_smfmac_f32_16x16x64_bf16 v[4:7], v[20:23], a[128:135], v110\n\t"
        "s_waitcnt lgkmcnt(6)\n\t"
        "v_smfmac_f32_16x16x64_bf16 v[8:11], v[20:23], a[136:143], v110\n\t"
        "ds_read_b128 v[20:23], v115 offset:1024\n\t"
        "v_smfmac_f32_16x16x64_bf16 v[12:15], v[24:27], v[80:87], v110\n\t"
        "ds_read_b128 v[48:51], v117 offset:12288\n\t"
        "v_smfmac_f32_16x16x64_bf16 v[0:3], v[24:27], a[144:151], v110\n\t"
        "ds_read_b128 v[52:55], v117 offset:13312\n\t"
        "v_smfmac_f32_16x16x64_bf16 v[4:7], v[24:27], a[152:159], v110\n\t"
        "s_waitcnt lgkmcnt(8)\n\t"
        "v_smfmac_f32_16x16x64_bf16 v[8:11], v[24:27], a[160:167], v110\n\t"
        "ds_read_b128 v[24:27], v115 offset:2048\n\t"
        "v_smfmac_f32_16x16x64_bf16 v[12:15], v[28:31], v[88:95], v110\n\t"
        "ds_read_b128 v[56:59], v117 offset:14336\n\t"
        "v_smfmac_f32_16x16x64_bf16 v[0:3], v[28:31], a[168:175], v110\n\t"
        "ds_read_b128 v[60:63], v117 offset:15360\n\t"
        "v_smfmac_f32_16x16x64_bf16 v[4:7], v[28:31], a[176:183], v110\n\t"
        "s_waitcnt lgkmcnt(8)\n\t"
        "v_smfmac_f32_16x16x64_bf16 v[8:11], v[28:31], a[184:191], v110\n\t"
        "ds_read_b128 v[28:31], v115 offset:3072\n\t"
        "v_smfmac_f32_16x16x64_bf16 v[12:15], v[16:19], v[32:39], v110\n\t"
        "ds_read_b128 v[32:35], v117 offset:16384\n\t"
        "v_smfmac_f32_16x16x64_bf16 v[0:3], v[16:19], a[192:199], v110\n\t"
        "ds_read_b128 v[36:39], v117 offset:17408\n\t"
        "v_smfmac_f32_16x16x64_bf16 v[4:7], v[16:19], a[200:207], v110\n\t"
        "s_waitcnt lgkmcnt(8)\n\t"
        "v_smfmac_f32_16x16x64_bf16 v[8:11], v[16:19], a[208:215], v110\n\t"
        "ds_read_b128 v[16:19], v116 offset:0\n\t"
        "v_smfmac_f32_16x16x64_bf16 v[12:15], v[20:23], v[40:47], v110\n\t"
        "ds_read_b128 v[40:43], v117 offset:18432\n\t"
        "v_smfmac_f32_16x16x64_bf16 v[0:3], v[20:23], a[216:223], v110\n\t"
        "ds_read_b128 v[44:47], v117 offset:19456\n\t"
        "v_smfmac_f32_16x16x64_bf16 v[4:7], v[20:23], a[224:231], v110\n\t"
        "s_waitcnt lgkmcnt(8)\n\t"
        "v_smfmac_f32_16x16x64_bf16 v[8:11], v[20:23], a[232:239], v110\n\t"
        "ds_read_b128 v[20:23], v116 offset:1024\n\t"
        "v_smfmac_f32_16x16x64_bf16 v[12:15], v[24:27], v[48:55], v110\n\t"
        "ds_read_b128 v[48:51], v117 offset:20480\n\t"
        "v_smfmac_f32_16x16x64_bf16 v[0:3], v[24:27], a[240:247], v110\n\t"
        "ds_read_b128 v[52:55], v117 offset:21504\n\t"
        "v_smfmac_f32_16x16x64_bf16 v[4:7], v[24:27], a[248:255], v110\n\t"
        "s_waitcnt lgkmcnt(8)\n\t"
        "v_smfmac_f32_16x16x64_bf16 v[8:11], v[24:27], v[128:135], v110\n\t"
        "ds_read_b128 v[24:27], v116 offset:2048\n\t"
        "v_smfmac_f32_16x16x64_bf16 v[12:15], v[28:31], v[56:63], v110\n\t"
        "ds_read_b128 v[56:59], v124 offset:3072\n\t"
        "v_smfmac_f32_16x16x64_bf16 v[0:3], v[28:31], v[136:143], v110\n\t"
        "ds_read_b128 v[60:63], v124 offset:7168\n\t"
        "v_smfmac_f32_16x16x64_bf16 v[4:7], v[28:31], v[144:151], v110\n\t"
        "s_waitcnt lgkmcnt(8)\n\t"
        "v_smfmac_f32_16x16x64_bf16 v[8:11], v[28:31], v[152:159], v110\n\t"
        "ds_read_b128 v[28:31], v116 offset:3072\n\t"
        "v_smfmac_f32_16x16x64_bf16 v[12:15], v[16:19], v[32:39], v110\n\t"
        "ds_read_b128 v[32:35], v124 offset:0\n\t"
        "v_smfmac_f32_16x16x64_bf16 v[0:3], v[16:19], v[160:167], v110\n\t"
        "ds_read_b128 v[36:39], v124 offset:4096\n\t"
        "v_smfmac_f32_16x16x64_bf16 v[4:7], v[16:19], v[168:175], v110\n\t"
        "s_waitcnt lgkmcnt(8)\n\t"
        "v_smfmac_f32_16x16x64_bf16 v[8:11], v[16:19], v[176:183], v110\n\t"
        "v_smfmac_f32_16x16x64_bf16 v[12:15], v[20:23], v[40:47], v110\n\t"
        "ds_read_b128 v[40:43], v124 offset:1024\n\t"
        "v_smfmac_f32_16x16x64_bf16 v[8:11], v[20:23], v[200:207], v110\n\t"
        "ds_read_b128 v[44:47], v124 offset:5120\n\t"
        "s_waitcnt lgkmcnt(7)\n\t"
        "v_smfmac_f32_16x16x64_bf16 v[12:15], v[24:27], v[48:55], v110\n\t"
        "ds_read_b128 v[48:51], v124 offset:2048\n\t"
        "v_smfmac_f32_16x16x64_bf16 v[8:11], v[24:27], v[224:231], v110\n\t"
        "ds_read_b128 v[52:55], v124 offset:6144\n\t"
        "s_waitcnt lgkmcnt(6)\n\t"
        "v_smfmac_f32_16x16x64_bf16 v[12:15], v[28:31], v[96:103], v110\n\t"
        "v_smfmac_f32_16x16x64_bf16 v[8:11], v[28:31], v[248:255], v110\n\t"
        "v_smfmac_f32_16x16x64_bf16 v[0:3], v[20:23], v[184:191], v110\n\t"
        "s_lshl_b32 s34, s43, 11\n\t"
        "v_add_u32_e32 v108, s34, v124\n\t"
        "v_smfmac_f32_16x16x64_bf16 v[0:3], v[24:27], v[208:215], v110\n\t"
        "v_add_u32_e32 v109, s58, v124\n\t"
        "v_smfmac_f32_16x16x64_bf16 v[4:7], v[20:23], v[192:199], v110\n\t"
        "v_smfmac_f32_16x16x64_bf16 v[0:3], v[28:31], v[232:239], v110\n\t"
        "v_smfmac_f32_16x16x64_bf16 v[4:7], v[24:27], v[216:223], v110\n\t"
        "buffer_load_dwordx4 v[104:107], v120, s[8:11], s26 offen sc1\n\t"
        "v_smfmac_f32_16x16x64_bf16 v[4:7], v[28:31], v[240:247], v110\n\t"
        "v_add_f32_e32 v12, v12, v13\n\t"
        "v_add_f32_e32 v14, v14, v15\n\t"
        "v_cvt_pk_bf16_f32 v12, v12, v14\n\t"
        "v_add_f32_e32 v8, v8, v9\n\t"
        "v_add_f32_e32 v10, v10, v11\n\t"
        "v_cvt_pk_bf16_f32 v8, v8, v10\n\t"
        "ds_write2_b32 v118, v8, v12 offset0:64 offset1:96\n\t"
        "v_add_f32_e32 v0, v0, v1\n\t"
        "v_add_f32_e32 v2, v2, v3\n\t"
        "v_cvt_pk_bf16_f32 v0, v0, v2\n\t"
        "v_add_f32_e32 v4, v4, v5\n\t"
        "v_add_f32_e32 v6, v6, v7\n\t"
        "v_cvt_pk_bf16_f32 v4, v4, v6\n\t"
        "ds_write2_b32 v118, v0, v4 offset0:0 offset1:32\n\t"
        "s_cmp_eq_u32 s24, 63\n\t"
        "s_waitcnt lgkmcnt(0)\n\t"
        "s_barrier\n\t"
        "s_cbranch_scc1 LA_skip%=\n\t"
        "ds_read_b64_tr_b16 v[16:17], v119 offset:0\n\t"
        "ds_read_b64_tr_b16 v[18:19], v119 offset:32\n\t"
        "ds_read_b64_tr_b16 v[20:21], v119 offset:256\n\t"
        "ds_read_b64_tr_b16 v[22:23], v119 offset:288\n\t"
        "ds_read_b64_tr_b16 v[24:25], v119 offset:512\n\t"
        "ds_read_b64_tr_b16 v[26:27], v119 offset:544\n\t"
        "ds_read_b64_tr_b16 v[28:29], v119 offset:768\n\t"
        "ds_read_b64_tr_b16 v[30:31], v119 offset:800\n\t"
        "buffer_store_dword v126, v127, s[12:15], 0 offen sc1\n\t"
        "s_waitcnt lgkmcnt(6)\n\t"
        "v_mfma_f32_16x16x32_bf16 v[0:3], v[32:35], v[16:19], 0\n\t"
        "ds_read_b64_tr_b16 v[32:33], v119 offset:2112\n\t"
        "ds_read_b64_tr_b16 v[34:35], v119 offset:2144\n\t"
        "v_mfma_f32_16x16x32_bf16 v[4:7], v[36:39], v[16:19], 0\n\t"
        "ds_read_b64_tr_b16 v[36:37], v119 offset:2368\n\t"
        "ds_read_b64_tr_b16 v[38:39], v119 offset:2400\n\t"
        "s_waitcnt lgkmcnt(8)\n\t"
        "v_mfma_f32_16x16x32_bf16 v[0:3], v[40:43], v[20:23], v[0:3]\n\t"
        "ds_read_b64_tr_b16 v[40:41], v119 offset:2624\n\t"
        "ds_read_b64_tr_b16 v[42:43], v119 offset:2656\n\t"
        "v_mfma_f32_16x16x32_bf16 v[4:7], v[44:47], v[20:23], v[4:7]\n\t"
        "ds_read_b64_tr_b16 v[44:45], v119 offset:2880\n\t"
        "ds_read_b64_tr_b16 v[46:47], v119 offset:2912\n\t"
        "s_waitcnt lgkmcnt(10)\n\t"
        "v_mfma_f32_16x16x32_bf16 v[0:3], v[48:51], v[24:27], v[0:3]\n\t"
        "ds_read_b128 v[48:51], v108 offset:16384\n\t"
        "v_mfma_f32_16x16x32_bf16 v[4:7], v[52:55], v[24:27], v[4:7]\n\t"
        "ds_read_b128 v[52:55], v108 offset:17408\n\t"
        "s_waitcnt lgkmcnt(10)\n\t"
        "v_mfma_f32_16x16x32_bf16 v[0:3], v[56:59], v[28:31], v[0:3]\n\t"
        "v_mfma_f32_16x16x32_bf16 v[4:7], v[60:63], v[28:31], v[4:7]\n\t"
        "s_waitcnt lgkmcnt(2)\n\t"
        "v_mfma_f32_16x16x32_bf16 v[8:11], v[32:35], v[16:19], 0\n\t"
        "s_nop 1\n\t"
        "v_mfma_f32_16x16x32_bf16 v[8:11], v[36:39], v[20:23], v[8:11]\n\t"
        "v_mov_b64_e32 v[16:17], s[0:1]\n\t"
        "v_mov_b64_e32 v[18:19], s[0:1]\n\t"
        "v_mfma_f32_16x16x32_bf16 v[8:11], v[40:43], v[24:27], v[8:11]\n\t"
        "v_max3_f32 v59, v0, v1, v2\n\t"
        "v_max3_f32 v59, v59, v3, v4\n\t"
        "v_mfma_f32_16x16x32_bf16 v[8:11], v[44:47], v[28:31], v[8:11]\n\t"
        "v_max3_f32 v59, v59, v5, v6\n\t"
        "v_max_f32_e32 v60, v59, v7\n\t"
        "v_max_f32_e32 v59, v59, v7\n\t"
        "v_cndmask_b32_e64 v0, v0, v4, s[38:39]\n\t"
        "v_cndmask_b32_e64 v1, v1, v5, s[38:39]\n\t"
        "v_permlane32_swap_b32_e32 v59, v60\n\t"
        "v_cndmask_b32_e64 v2, v2, v6, s[38:39]\n\t"
        "v_cndmask_b32_e64 v3, v3, v7, s[38:39]\n\t"
        "v_max3_f32 v59, v59, v60, v8\n\t"
        "v_max3_f32 v59, v59, v9, v10\n\t"
        "v_max_f32_e32 v60, v59, v11\n\t"
        "v_max_f32_e32 v59, v59, v11\n\t"
        "v_cndmask_b32_e64 v8, v8, v9, s[38:39]\n\t"
        "v_cndmask_b32_e64 v10, v10, v11, s[38:39]\n\t"
        "v_permlane16_swap_b32_e32 v59, v60\n\t"
        "v_max_f32_e32 v59, v59, v60\n\t"
        "v_mul_f32_e32 v59, 0xbe02940c, v59\n\t"
        "v_cndmask_b32_e64 v58, v8, v10, s[40:41]\n\t"
        "v_fmamk_f32 v0, v0, 0x3e02940c, v59\n\t"
        "v_exp_f32_e32 v0, v0\n\t"
        "v_fmamk_f32 v1, v1, 0x3e02940c, v59\n\t"
        "v_exp_f32_e32 v1, v1\n\t"
        "v_fmamk_f32 v2, v2, 0x3e02940c, v59\n\t"
        "v_exp_f32_e32 v2, v2\n\t"
        "v_fmamk_f32 v3, v3, 0x3e02940c, v59\n\t"
        "v_exp_f32_e32 v3, v3\n\t"
        "v_fmamk_f32 v58, v58, 0x3e02940c, v59\n\t"
        "v_cvt_pk_bf16_f32 v12, v0, v1\n\t"
        "v_cvt_pk_bf16_f32 v13, v2, v3\n\t"
        "v_exp_f32_e32 v58, v58\n\t"
        "s_waitcnt lgkmcnt(0)\n\t"
        "v_mov_b32_dpp v14, v12 row_ror:8 row_mask:0xf bank_mask:0xf\n\t"
        "v_mov_b32_dpp v15, v13 row_ror:8 row_mask:0xf bank_mask:0xf\n\t"
        "v_mov_b32_dpp v59, v58 row_ror:8 row_mask:0xf bank_mask:0xf\n\t"
        "v_cvt_pk_bf16_f32 v59, v58, v59\n\t"
        "v_mfma_f32_16x16x32_bf16 v[20:23], v[16:19], v[12:15], 0\n\t"
        "v_cndmask_b32_e64 v56, v59, 0, s[40:41]\n\t"
        "v_cndmask_b32_e64 v57, 0, v59, s[40:41]\n\t"
        "v_mfma_f32_16x16x32_bf16 v[4:7], v[48:51], v[12:15], 0\n\t"
        "s_waitcnt vmcnt(1)\n\t"
        "v_max3_u32 v111, v104, v105, v106\n\t"
        "v_mfma_f32_16x16x32_bf16 v[8:11], v[52:55], v[12:15], 0\n\t"
        "v_max_u32_e32 v111, v111, v107\n\t"
        "v_mfma_f32_16x16x16_bf16 v[20:23], v[16:17], v[56:57], v[20:23]\n\t"
        "v_cmp_eq_u32_e32 vcc, -1, v111\n\t"
        "v_mfma_f32_16x16x16_bf16 v[4:7], v[104:105], v[56:57], v[4:7]\n\t"
        "s_nop 1\n\t"
        "v_mfma_f32_16x16x16_bf16 v[8:11], v[106:107], v[56:57], v[8:11]\n\t"
        "s_cbranch_vccnz LA_vmfail%=\n\t"
        "LA_vmok%=:\n\t"
        "s_nop 1\n\t"
        "v_rcp_f32_e32 v59, v20\n\t"
        "s_nop 0\n\t"
        "v_pk_mul_f32 v[4:5], v[4:5], v[58:59] op_sel:[0,1] op_sel_hi:[1,1]\n\t"
        "v_cvt_pk_bf16_f32 v0, v4, v5\n\t"
        "v_pk_mul_f32 v[6:7], v[6:7], v[58:59] op_sel:[0,1] op_sel_hi:[1,1]\n\t"
        "v_cvt_pk_bf16_f32 v1, v6, v7\n\t"
        "v_pk_mul_f32 v[8:9], v[8:9], v[58:59] op_sel:[0,1] op_sel_hi:[1,1]\n\t"
        "v_cvt_pk_bf16_f32 v2, v8, v9\n\t"
        "v_pk_mul_f32 v[10:11], v[10:11], v[58:59] op_sel:[0,1] op_sel_hi:[1,1]\n\t"
        "v_cvt_pk_bf16_f32 v3, v10, v11\n\t"
        "buffer_store_dwordx4 v[0:3], v121, s[8:11], s26 offen sc1\n\t"
        "LA_x%=:\n\t"
        "s_memrealtime s[52:53]\n\t"
        "ds_read_b128 v[16:19], v109 offset:8192\n\t"
        "ds_read_b128 v[20:23], v109 offset:9216\n\t"
        "ds_read_b128 v[24:27], v109 offset:10240\n\t"
        "ds_read_b128 v[28:31], v109 offset:11264\n\t"
        "ds_read_b128 v[56:59], v124 offset:0\n\t"
        "ds_read_b128 v[60:63], v124 offset:4096\n\t"
        "ds_read_b128 v[12:15], v124 offset:1024\n\t"
        "v_xor_b32_e32 v126, 0x800, v108\n\t"
        "s_add_u32 s48, s24, 1\n\t"
        "s_min_u32 s48, s48, 63\n\t"
        "buffer_load_dwordx2 v[108:109], v125, s[8:11], s26 offen sc1\n\t"
        "s_lshl_b32 s35, s44, 10\n\t"
        "s_lshr_b32 s34, s48, 1\n\t"
        "s_add_u32 s34, s34, 1\n\t"
        "s_min_u32 s34, s34, 31\n\t"
        "s_cmp_gt_u32 s54, s34\n\t"
        "s_cselect_b32 s35, 0xf0000000, s35\n\t"
        "v_lshlrev_b32_e32 v111, 2, v122\n\t"
        "v_and_b32_e32 v111, 0x3c0, v111\n\t"
        "buffer_load_dword v111, v111, s[12:15], s35 offen offset:0 sc1\n\t"
        "s_waitcnt lgkmcnt(6)\n\t"
        "v_mfma_f32_16x16x32_bf16 v[8:11], v[32:35], v[16:19], 0\n\t"
        "ds_read_b128 v[32:35], v124 offset:5120\n\t"
        "s_waitcnt lgkmcnt(6)\n\t"
        "v_mfma_f32_16x16x32_bf16 v[8:11], v[36:39], v[20:23], v[8:11]\n\t"
        "ds_read_b128 v[36:39], v124 offset:2048\n\t"
        "s_waitcnt lgkmcnt(6)\n\t"
        "v_mfma_f32_16x16x32_bf16 v[8:11], v[40:43], v[24:27], v[8:11]\n\t"
        "ds_read_b128 v[40:43], v124 offset:6144\n\t"
        "s_waitcnt lgkmcnt(6)\n\t"
        "v_mfma_f32_16x16x32_bf16 v[8:11], v[44:47], v[28:31], v[8:11]\n\t"
        "ds_read_b128 v[44:47], v124 offset:3072\n\t"
        "s_add_u32 s35, s24, 1\n\t"
        "s_waitcnt lgkmcnt(6)\n\t"
        "v_mfma_f32_16x16x32_bf16 v[0:3], v[56:59], v[16:19], 0\n\t"
        "ds_read_b128 v[56:59], v124 offset:7168\n\t"
        "s_waitcnt lgkmcnt(6)\n\t"
        "v_mfma_f32_16x16x32_bf16 v[4:7], v[60:63], v[16:19], 0\n\t"
        "s_and_b32 s36, s35, 1\n\t"
        "s_mul_i32 s36, s36, 24576\n\t"
        "s_waitcnt lgkmcnt(5)\n\t"
        "v_mfma_f32_16x16x32_bf16 v[0:3], v[12:15], v[20:23], v[0:3]\n\t"
        "s_add_u32 s36, s36, s55\n\t"
        "s_lshl_b32 s37, s48, 18\n\t"
        "s_waitcnt lgkmcnt(4)\n\t"
        "v_mfma_f32_16x16x32_bf16 v[4:7], v[32:35], v[20:23], v[4:7]\n\t"
        "v_xor_b32_e32 v32, 0x200, v120\n\t"
        "buffer_load_dwordx4 v[12:15], v32, s[8:11], s26 offen sc1\n\t"
        "s_waitcnt lgkmcnt(3)\n\t"
        "v_mfma_f32_16x16x32_bf16 v[0:3], v[36:39], v[24:27], v[0:3]\n\t"
        "s_lshl_b32 s34, s43, 11\n\t"
        "s_add_u32 s37, s37, s34\n\t"
        "s_waitcnt lgkmcnt(2)\n\t"
        "v_mfma_f32_16x16x32_bf16 v[4:7], v[40:43], v[24:27], v[4:7]\n\t"
        "v_cvt_pk_bf16_f32 v8, 0, v8\n\t"
        "v_cvt_pk_bf16_f32 v9, 0, v9\n\t"
        "v_cvt_pk_bf16_f32 v10, 0, v10\n\t"
        "v_cvt_pk_bf16_f32 v11, 0, v11\n\t"
        "s_waitcnt lgkmcnt(1)\n\t"
        "v_mfma_f32_16x16x32_bf16 v[0:3], v[44:47], v[28:31], v[0:3]\n\t"
        "v_mul_f32_e32 v8, 0x3db504f3, v8\n\t"
        "v_mul_f32_e32 v9, 0x3db504f3, v9\n\t"
        "v_mul_f32_e32 v10, 0x3db504f3, v10\n\t"
        "v_mul_f32_e32 v11, 0x3db504f3, v11\n\t"
        "s_waitcnt lgkmcnt(0)\n\t"
        "v_mfma_f32_16x16x32_bf16 v[4:7], v[56:59], v[28:31], v[4:7]\n\t"
        "v_cvt_pk_bf16_f32 v8, 0, v8\n\t"
        "v_cvt_pk_bf16_f32 v9, 0, v9\n\t"
        "v_cvt_pk_bf16_f32 v10, 0, v10\n\t"
        "v_cvt_pk_bf16_f32 v11, 0, v11\n\t"
        "ds_read_b128 v[56:59], v126 offset:16384\n\t"
        "ds_read_b128 v[60:63], v126 offset:17408\n\t"
        "v_mov_b64_e32 v[36:37], -1\n\t"
        "v_mov_b64_e32 v[38:39], -1\n\t"
        "v_mov_b32_e32 v40, 0xce6e6b28\n\t"
        "v_mov_b32_e32 v46, 0x3db504f3\n\t"
        "v_mov_b32_e32 v47, 0x3fb8aa3b\n\t"
        "v_cvt_pk_bf16_f32 v0, 0, v0\n\t"
        "v_cvt_pk_bf16_f32 v1, 0, v1\n\t"
        "v_cvt_pk_bf16_f32 v2, 0, v2\n\t"
        "v_cvt_pk_bf16_f32 v3, 0, v3\n\t"
        "v_cvt_pk_bf16_f32 v4, 0, v4\n\t"
        "v_cvt_pk_bf16_f32 v5, 0, v5\n\t"
        "v_cvt_pk_bf16_f32 v6, 0, v6\n\t"
        "v_cvt_pk_bf16_f32 v7, 0, v7\n\t"
        "v_pk_mul_f32 v[0:1], v[0:1], v[46:47] op_sel_hi:[1,0]\n\t"
        "v_pk_mul_f32 v[2:3], v[2:3], v[46:47] op_sel_hi:[1,0]\n\t"
        "v_pk_mul_f32 v[4:5], v[4:5], v[46:47] op_sel_hi:[1,0]\n\t"
        "v_pk_mul_f32 v[6:7], v[6:7], v[46:47] op_sel_hi:[1,0]\n\t"
        "s_lshr_b32 s59, s48, 1\n\t"
        "s_cmp_gt_u32 s54, s59\n\t"
        "s_cbranch_scc1 LA_kvok%=\n\t"
        "s_mov_b32 s42, 0\n\t"
        "s_waitcnt vmcnt(1)\n\t"
        "s_branch LA_kvw%=\n\t"
        "LA_kv%=:\n\t"
        "s_waitcnt vmcnt(0)\n\t"
        "LA_kvw%=:\n\t"
        "v_min_u32_dpp v111, v111, v111 quad_perm:[1,0,3,2] row_mask:0xf bank_mask:0xf\n\t"
        "s_nop 1\n\t"
        "v_min_u32_dpp v111, v111, v111 quad_perm:[2,3,0,1] row_mask:0xf bank_mask:0xf\n\t"
        "s_nop 1\n\t"
        "v_min_u32_dpp v111, v111, v111 row_ror:4 row_mask:0xf bank_mask:0xf\n\t"
        "s_nop 1\n\t"
        "v_min_u32_dpp v111, v111, v111 row_ror:8 row_mask:0xf bank_mask:0xf\n\t"
        "s_nop 0\n\t"
        "v_readfirstlane_b32 s54, v111\n\t"
        "s_nop 0\n\t"
        "s_cmp_gt_u32 s54, s59\n\t"
        "s_cbranch_scc1 LA_kvok%=\n\t"
        "s_sleep 4\n\t"
        "s_lshl_b32 s35, s44, 10\n\t"
        "v_lshlrev_b32_e32 v111, 2, v122\n\t"
        "v_and_b32_e32 v111, 0x3c0, v111\n\t"
        "buffer_load_dword v111, v111, s[12:15], s35 offen offset:0 sc1\n\t"
        "s_add_u32 s42, s42, 1\n\t"
        "s_cmp_lt_u32 s42, 0x100000\n\t"
        "s_cbranch_scc1 LA_kv%=\n\t"
        "s_movk_i32 s34, 16\n\t"
        "s_branch LA_die%=\n\t"
        "LA_kvok%=:\n\t"
        "s_add_u32 m0, s36, 0\n\t"
        "s_add_u32 s34, s37, 29548544\n\t"
        "buffer_load_dwordx4 v122, s[12:15], s34 offen sc1 lds\n\t"
        "v_cvt_pk_bf16_f32 v0, 0, v0\n\t"
        "v_cvt_pk_bf16_f32 v1, 0, v1\n\t"
        "v_cvt_pk_bf16_f32 v2, 0, v2\n\t"
        "v_cvt_pk_bf16_f32 v3, 0, v3\n\t"
        "v_cvt_pk_bf16_f32 v4, 0, v4\n\t"
        "v_cvt_pk_bf16_f32 v5, 0, v5\n\t"
        "v_cvt_pk_bf16_f32 v6, 0, v6\n\t"
        "v_cvt_pk_bf16_f32 v7, 0, v7\n\t"
        "s_add_u32 m0, s36, 1024\n\t"
        "s_add_u32 s34, s37, 29549568\n\t"
        "buffer_load_dwordx4 v122, s[12:15], s34 offen sc1 lds\n\t"
        "v_cndmask_b32_e64 v0, v40, v0, s[60:61]\n\t"
        "v_cndmask_b32_e64 v1, v40, v1, s[62:63]\n\t"
        "v_cndmask_b32_e64 v2, v40, v2, s[64:65]\n\t"
        "v_cndmask_b32_e64 v3, v40, v3, s[66:67]\n\t"
        "v_cndmask_b32_e64 v4, v40, v4, s[68:69]\n\t"
        "v_cndmask_b32_e64 v5, v40, v5, s[70:71]\n\t"
        "v_cndmask_b32_e64 v6, v40, v6, s[72:73]\n\t"
        "v_cndmask_b32_e64 v7, v40, v7, s[74:75]\n\t"
        "s_add_u32 m0, s36, 8192\n\t"
        "s_add_u32 s34, s37, 12771328\n\t"
        "buffer_load_dwordx4 v122, s[12:15], s34 offen sc1 lds\n\t"
        "v_max3_f32 v42, v0, v1, v2\n\t"
        "v_max3_f32 v42, v42, v3, v4\n\t"
        "v_max3_f32 v42, v42, v5, v6\n\t"
        "v_max3_f32 v42, v42, v7, v8\n\t"
        "v_max3_f32 v42, v42, v9, v10\n\t"
        "v_mov_b32_e32 v43, v11\n\t"
        "v_max_f32_e32 v44, v42, v43\n\t"
        "v_max_f32_e32 v42, v42, v43\n\t"
        "s_pack_ll_b32_b16 s34, s52, s24\n\t"
        "v_mov_b32_e32 v40, s34\n\t"
        "s_add_u32 s35, s26, s57\n\t"
        "v_permlane16_swap_b32_e32 v42, v44\n\t"
        "v_max_f32_e32 v45, v42, v44\n\t"
        "v_max_f32_e32 v42, v42, v44\n\t"
        "s_mov_b64 exec, 1\n\t"
        "buffer_store_dword v40, off, s[8:11], s35 sc1\n\t"
        "s_mov_b64 exec, -1\n\t"
        "v_permlane32_swap_b32_e32 v42, v45\n\t"
        "v_max_f32_e32 v42, v42, v45\n\t"
        "v_mul_f32_e32 v42, 0xbfb8aa3b, v42\n\t"
        "v_pk_fma_f32 v[0:1], v[0:1], v[46:47], v[42:43] op_sel:[0,1,0] op_sel_hi:[1,1,0]\n\t"
        "s_add_u32 m0, s36, 9216\n\t"
        "s_add_u32 s34, s37, 12772352\n\t"
        "buffer_load_dwordx4 v122, s[12:15], s34 offen sc1 lds\n\t"
        "v_exp_f32_e32 v0, v0\n\t"
        "v_exp_f32_e32 v1, v1\n\t"
        "v_pk_fma_f32 v[2:3], v[2:3], v[46:47], v[42:43] op_sel:[0,1,0] op_sel_hi:[1,1,0]\n\t"
        "v_exp_f32_e32 v2, v2\n\t"
        "v_exp_f32_e32 v3, v3\n\t"
        "v_pk_fma_f32 v[4:5], v[4:5], v[46:47], v[42:43] op_sel:[0,1,0] op_sel_hi:[1,1,0]\n\t"
        "v_exp_f32_e32 v4, v4\n\t"
        "s_add_u32 m0, s36, 16384\n\t"
        "s_add_u32 s34, s37, 46325760\n\t"
        "buffer_load_dwordx4 v122, s[12:15], s34 offen sc1 lds\n\t"
        "v_exp_f32_e32 v5, v5\n\t"
        "v_pk_fma_f32 v[6:7], v[6:7], v[46:47], v[42:43] op_sel:[0,1,0] op_sel_hi:[1,1,0]\n\t"
        "v_exp_f32_e32 v6, v6\n\t"
        "v_exp_f32_e32 v7, v7\n\t"
        "v_pk_fma_f32 v[8:9], v[8:9], v[46:47], v[42:43] op_sel:[0,1,0] op_sel_hi:[1,1,0]\n\t"
        "v_exp_f32_e32 v8, v8\n\t"
        "v_exp_f32_e32 v9, v9\n\t"
        "s_add_u32 m0, s36, 17408\n\t"
        "s_add_u32 s34, s37, 46326784\n\t"
        "buffer_load_dwordx4 v122, s[12:15], s34 offen sc1 lds\n\t"
        "v_pk_fma_f32 v[10:11], v[10:11], v[46:47], v[42:43] op_sel:[0,1,0] op_sel_hi:[1,1,0]\n\t"
        "v_exp_f32_e32 v10, v10\n\t"
        "v_exp_f32_e32 v11, v11\n\t"
        "v_pk_add_f32 v[42:43], v[0:1], v[2:3]\n\t"
        "v_cndmask_b32_e64 v8, v8, 0, s[40:41]\n\t"
        "v_cndmask_b32_e64 v9, v9, 0, s[40:41]\n\t"
        "v_cndmask_b32_e64 v10, v10, 0, s[40:41]\n\t"
        "buffer_store_dwordx4 v[36:39], v121, s[8:11], s28 offen sc1\n\t"
        "v_cndmask_b32_e64 v11, v11, 0, s[40:41]\n\t"
        "v_pk_add_f32 v[42:43], v[42:43], v[4:5]\n\t"
        "v_pk_add_f32 v[42:43], v[42:43], v[6:7]\n\t"
        "v_pk_add_f32 v[42:43], v[42:43], v[8:9]\n\t"
        "v_pk_add_f32 v[42:43], v[42:43], v[10:11]\n\t"
        "v_add_f32_e32 v44, v42, v43\n\t"
        "v_add_f32_e32 v42, v42, v43\n\t"
        "s_lshl_b32 s34, s24, 18\n\t"
        "s_add_u32 s34, s34, 63102976\n\t"
        "v_permlane16_swap_b32_e32 v42, v44\n\t"
        "v_add_f32_e32 v45, v42, v44\n\t"
        "v_add_f32_e32 v42, v42, v44\n\t"
        "s_add_u32 s35, s24, 1\n\t"
        "v_mov_b32_e32 v126, s35\n\t"
        "v_permlane32_swap_b32_e32 v42, v45\n\t"
        "v_add_f32_e32 v42, v42, v45\n\t"
        "v_rcp_f32_e32 v42, v42\n\t"
        "s_waitcnt vmcnt(8) lgkmcnt(0)\n\t"
        "v_max3_u32 v45, v12, v13, v14\n\t"
        "v_max3_u32 v45, v45, v15, v104\n\t"
        "v_max3_u32 v45, v45, v105, v106\n\t"
        "v_max_u32_e32 v45, v45, v107\n\t"
        "v_cmp_eq_u32_e32 vcc, -1, v45\n\t"
        "s_nop 0\n\t"
        "s_cbranch_vccnz LA_v2fail%=\n\t"
        "LA_v2ok%=:\n\t"
        "v_and_b32_e32 v45, 0xff, v109\n\t"
        "v_cmp_ne_u32_e32 vcc, s24, v45\n\t"
        "s_nop 0\n\t"
        "s_cbranch_vccnz LA_stre%=\n\t"
        "LA_stok%=:\n\t"
        "v_pk_mul_f32 v[0:1], v[0:1], v[42:43] op_sel_hi:[1,0]\n\t"
        "v_subrev_u32_e32 v108, s25, v108\n\t"
        "v_min_u32_dpp v111, v111, v111 quad_perm:[1,0,3,2] row_mask:0xf bank_mask:0xf\n\t"
        "v_cvt_pk_bf16_f32 v32, v0, v1\n\t"
        "v_pk_mul_f32 v[2:3], v[2:3], v[42:43] op_sel_hi:[1,0]\n\t"
        "v_cvt_pk_bf16_f32 v33, v2, v3\n\t"
        "v_max_i32_dpp v108, v108, v108 quad_perm:[1,0,3,2] row_mask:0xf bank_mask:0xf\n\t"
        "v_min_u32_dpp v111, v111, v111 quad_perm:[2,3,0,1] row_mask:0xf bank_mask:0xf\n\t"
        "v_pk_mul_f32 v[4:5], v[4:5], v[42:43] op_sel_hi:[1,0]\n\t"
        "v_cvt_pk_bf16_f32 v34, v4, v5\n\t"
        "v_pk_mul_f32 v[6:7], v[6:7], v[42:43] op_sel_hi:[1,0]\n\t"
        "v_max_i32_dpp v108, v108, v108 quad_perm:[2,3,0,1] row_mask:0xf bank_mask:0xf\n\t"
        "v_min_u32_dpp v111, v111, v111 row_ror:4 row_mask:0xf bank_mask:0xf\n\t"
        "v_cvt_pk_bf16_f32 v35, v6, v7\n\t"
        "v_pk_mul_f32 v[8:9], v[8:9], v[42:43] op_sel_hi:[1,0]\n\t"
        "v_cvt_pk_bf16_f32 v36, v8, v9\n\t"
        "v_max_i32_dpp v108, v108, v108 row_ror:4 row_mask:0xf bank_mask:0xf\n\t"
        "v_min_u32_dpp v111, v111, v111 row_ror:8 row_mask:0xf bank_mask:0xf\n\t"
        "v_pk_mul_f32 v[10:11], v[10:11], v[42:43] op_sel_hi:[1,0]\n\t"
        "v_cvt_pk_bf16_f32 v37, v10, v11\n\t"
        "v_mfma_f32_16x16x32_bf16 v[16:19], v[48:51], v[32:35], 0\n\t"
        "v_max_i32_dpp v108, v108, v108 row_ror:8 row_mask:0xf bank_mask:0xf\n\t"
        "v_mfma_f32_16x16x32_bf16 v[20:23], v[52:55], v[32:35], 0\n\t"
        "v_mfma_f32_16x16x32_bf16 v[24:27], v[56:59], v[32:35], 0\n\t"
        "v_mfma_f32_16x16x32_bf16 v[28:31], v[60:63], v[32:35], 0\n\t"
        "v_max_i32_dpp v108, v108, v108 row_bcast:15 row_mask:0xa bank_mask:0xf\n\t"
        "v_xor_b32_e32 v45, 0x400, v123\n\t"
        "v_add_u32_e32 v124, s56, v124\n\t"
        "v_mfma_f32_16x16x16_bf16 v[16:19], v[104:105], v[36:37], v[16:19]\n\t"
        "v_max_i32_dpp v108, v108, v108 row_bcast:31 row_mask:0xc bank_mask:0xf\n\t"
        "v_mfma_f32_16x16x16_bf16 v[20:23], v[106:107], v[36:37], v[20:23]\n\t"
        "v_mfma_f32_16x16x16_bf16 v[24:27], v[12:13], v[36:37], v[24:27]\n\t"
        "v_mfma_f32_16x16x16_bf16 v[28:31], v[14:15], v[36:37], v[28:31]\n\t"
        "ds_read_b128 v[48:51], v117 offset:4096\n\t"
        "ds_read_b128 v[52:55], v117 offset:5120\n\t"
        "ds_read_b128 v[56:59], v117 offset:6144\n\t"
        "ds_read_b128 v[60:63], v117 offset:7168\n\t"
        "s_sub_u32 s56, 0, s56\n\t"
        "v_cvt_pk_bf16_f32 v0, v16, v17\n\t"
        "v_cvt_pk_bf16_f32 v1, v18, v19\n\t"
        "v_cvt_pk_bf16_f32 v2, v20, v21\n\t"
        "v_cvt_pk_bf16_f32 v3, v22, v23\n\t"
        "buffer_store_dwordx4 v[0:3], v123, s[12:15], s34 offen sc1\n\t"
        "v_cvt_pk_bf16_f32 v4, v24, v25\n\t"
        "v_cvt_pk_bf16_f32 v5, v26, v27\n\t"
        "v_cvt_pk_bf16_f32 v6, v28, v29\n\t"
        "v_cvt_pk_bf16_f32 v7, v30, v31\n\t"
        "buffer_store_dwordx4 v[4:7], v45, s[12:15], s34 offen sc1\n\t"
        "s_cmp_eq_u32 s24, 63\n\t"
        "s_cbranch_scc1 LA_last%=\n\t"
        "ds_read_b128 v[32:35], v117 offset:0\n\t"
        "ds_read_b128 v[36:39], v117 offset:1024\n\t"
        "ds_read_b128 v[40:43], v117 offset:2048\n\t"
        "ds_read_b128 v[44:47], v117 offset:3072\n\t"
        "v_mov_b64_e32 v[14:15], 0\n\t"
        "v_mov_b64_e32 v[12:13], 0\n\t"
        "v_mov_b64_e32 v[10:11], 0\n\t"
        "v_mov_b64_e32 v[8:9], 0\n\t"
        "v_mov_b64_e32 v[6:7], 0\n\t"
        "v_mov_b64_e32 v[4:5], 0\n\t"
        "v_mov_b64_e32 v[2:3], 0\n\t"
        "v_mov_b64_e32 v[0:1], 0\n\t"
        "v_readfirstlane_b32 s34, v111\n\t"
        "s_nop 0\n\t"
        "s_max_u32 s54, s54, s34\n\t"
        "v_readfirstlane_b32 s35, v109\n\t"
        "v_readlane_b32 s36, v108, 63\n\t"
        "s_bfe_u32 s29, s35, 0xc0008\n\t"
        "s_lshr_b32 s30, s35, 20\n\t"
        "s_add_u32 s35, s31, s50\n\t"
        "s_mul_i32 s37, s50, 3\n\t"
        "s_min_u32 s35, s35, s37\n\t"
        "s_add_i32 s34, s36, s51\n\t"
        "s_cmp_gt_i32 s34, 0\n\t"
        "s_cselect_b32 s31, s35, 0\n\t"
        "s_cselect_b32 s34, s34, 0\n\t"
        "s_add_u32 s34, s34, s31\n\t"
        "s_add_u32 s37, s25, s34\n\t"
        "s_add_u32 s45, s37, s29\n\t"
        "s_add_u32 s25, s45, s30\n\t"
        "s_add_u32 s24, s24, 1\n\t"
        "s_mov_b32 s28, s26\n\t"
        "s_mov_b32 s26, s27\n\t"
        "s_add_u32 s27, s27, 0x80000\n\t"
        "s_and_b32 s27, s27, 0x1fffff\n\t"
        "s_branch LA_loop%=\n\t"
        "LA_last%=:\n\t"
        "s_waitcnt vmcnt(0)\n\t"
        "s_barrier\n\t"
        "buffer_store_dword v126, v127, s[12:15], 0 offen sc1\n\t"
        "s_endpgm\n\t"
        "LA_skip%=:\n\t"
        "buffer_store_dword v126, v127, s[12:15], 0 offen sc1\n\t"
        "ds_read_b64_tr_b16 v[32:33], v119 offset:2112\n\t"
        "ds_read_b64_tr_b16 v[34:35], v119 offset:2144\n\t"
        "ds_read_b64_tr_b16 v[36:37], v119 offset:2368\n\t"
        "ds_read_b64_tr_b16 v[38:39], v119 offset:2400\n\t"
        "ds_read_b64_tr_b16 v[40:41], v119 offset:2624\n\t"
        "ds_read_b64_tr_b16 v[42:43], v119 offset:2656\n\t"
        "ds_read_b64_tr_b16 v[44:45], v119 offset:2880\n\t"
        "ds_read_b64_tr_b16 v[46:47], v119 offset:2912\n\t"
        "ds_read_b128 v[48:51], v108 offset:16384\n\t"
        "ds_read_b128 v[52:55], v108 offset:17408\n\t"
        "s_waitcnt lgkmcnt(0)\n\t"
        "s_branch LA_x%=\n\t"
        "LA_stre%=:\n\t"
        "s_mov_b32 s42, 0\n\t"
        "LA_str2%=:\n\t"
        "buffer_load_dwordx2 v[108:109], v125, s[8:11], s26 offen sc1\n\t"
        "s_waitcnt vmcnt(0)\n\t"
        "v_and_b32_e32 v45, 0xff, v109\n\t"
        "v_cmp_ne_u32_e32 vcc, s24, v45\n\t"
        "s_nop 0\n\t"
        "s_cbranch_vccz LA_stok%=\n\t"
        "s_add_u32 s42, s42, 1\n\t"
        "s_cmp_lt_u32 s42, 0x100000\n\t"
        "s_cbranch_scc1 LA_str2%=\n\t"
        "s_movk_i32 s34, 5\n\t"
        "s_branch LA_die%=\n\t"
        "LA_v2fail%=:\n\t"
        "s_mov_b32 s42, 0\n\t"
        "LA_v2re%=:\n\t"
        "v_xor_b32_e32 v45, 0x200, v120\n\t"
        "buffer_load_dwordx4 v[12:15], v45, s[8:11], s26 offen sc1\n\t"
        "buffer_load_dwordx4 v[104:107], v120, s[8:11], s26 offen sc1\n\t"
        "s_waitcnt vmcnt(0)\n\t"
        "v_max3_u32 v45, v12, v13, v14\n\t"
        "v_max3_u32 v45, v45, v15, v104\n\t"
        "v_max3_u32 v45, v45, v105, v106\n\t"
        "v_max_u32_e32 v45, v45, v107\n\t"
        "v_cmp_eq_u32_e32 vcc, -1, v45\n\t"
        "s_nop 0\n\t"
        "s_cbranch_vccz LA_v2ok%=\n\t"
        "s_add_u32 s42, s42, 1\n\t"
        "s_cmp_lt_u32 s42, 0x100000\n\t"
        "s_cbranch_scc1 LA_v2re%=\n\t"
        "s_movk_i32 s34, 6\n\t"
        "s_branch LA_die%=\n\t"
        "LA_rdfail%=:\n\t"
        "s_or_b32 s47, s47, 1\n\t"
        "buffer_load_dwordx4 v[16:19], v112, s[8:11], s26 offen offset:0 sc1\n\t"
        "buffer_load_dwordx4 v[20:23], v112, s[8:11], s26 offen offset:1024 sc1\n\t"
        "buffer_load_dwordx4 v[24:27], v112, s[8:11], s26 offen offset:2048 sc1\n\t"
        "buffer_load_dwordx4 v[28:31], v112, s[8:11], s26 offen offset:3072 sc1\n\t"
        "s_nop 7\n\t"
        "v_mov_b64_e32 v[0:1], 0\n\t"
        "v_mov_b64_e32 v[2:3], 0\n\t"
        "v_mov_b64_e32 v[4:5], 0\n\t"
        "v_mov_b64_e32 v[6:7], 0\n\t"
        "v_mov_b64_e32 v[8:9], 0\n\t"
        "v_mov_b64_e32 v[10:11], 0\n\t"
        "v_mov_b64_e32 v[12:13], 0\n\t"
        "v_mov_b64_e32 v[14:15], 0\n\t"
        "s_branch LA_rdre%=w\n\t"
        "LA_rdre%=:\n\t"
        "buffer_load_dwordx4 v[16:19], v112, s[8:11], s26 offen offset:0 sc1\n\t"
        "buffer_load_dwordx4 v[20:23], v112, s[8:11], s26 offen offset:1024 sc1\n\t"
        "buffer_load_dwordx4 v[24:27], v112, s[8:11], s26 offen offset:2048 sc1\n\t"
        "buffer_load_dwordx4 v[28:31], v112, s[8:11], s26 offen offset:3072 sc1\n\t"
        "LA_rdre%=w:\n\t"
        "s_waitcnt vmcnt(0) lgkmcnt(0)\n\t"
        "v_max3_u32 v111, v16, v17, v18\n\t"
        "v_max3_u32 v111, v111, v19, v20\n\t"
        "v_max3_u32 v111, v111, v21, v22\n\t"
        "v_max3_u32 v111, v111, v23, v24\n\t"
        "v_max3_u32 v111, v111, v25, v26\n\t"
        "v_max3_u32 v111, v111, v27, v28\n\t"
        "v_max3_u32 v111, v111, v29, v30\n\t"
        "v_max_u32_e32 v111, v111, v31\n\t"
        "v_cmp_eq_u32_e32 vcc, -1, v111\n\t"
        "s_nop 0\n\t"
        "s_cbranch_vccz LA_go%=\n\t"
        "s_add_u32 s42, s42, 1\n\t"
        "s_cmp_lt_u32 s42, 0x100000\n\t"
        "s_cbranch_scc1 LA_rdre%=\n\t"
        "s_movk_i32 s34, 1\n\t"
        "s_branch LA_die%=\n\t"
        "LA_vmfail%=:\n\t"
        "s_or_b32 s47, s47, 2\n\t"
        "s_mov_b32 s42, 0\n\t"
        "LA_vmre%=:\n\t"
        "buffer_load_dwordx4 v[104:107], v120, s[8:11], s26 offen sc1\n\t"
        "v_mfma_f32_16x16x32_bf16 v[4:7], v[48:51], v[12:15], 0\n\t"
        "v_mfma_f32_16x16x32_bf16 v[8:11], v[52:55], v[12:15], 0\n\t"
        "s_nop 7\n\t"
        "s_waitcnt vmcnt(0)\n\t"
        "v_mfma_f32_16x16x16_bf16 v[4:7], v[104:105], v[56:57], v[4:7]\n\t"
        "v_mfma_f32_16x16x16_bf16 v[8:11], v[106:107], v[56:57], v[8:11]\n\t"
        "v_max3_u32 v111, v104, v105, v106\n\t"
        "v_max_u32_e32 v111, v111, v107\n\t"
        "v_cmp_eq_u32_e32 vcc, -1, v111\n\t"
        "s_nop 7\n\t"
        "s_cbranch_vccz LA_vmok%=\n\t"
        "s_add_u32 s42, s42, 1\n\t"
        "s_cmp_lt_u32 s42, 0x100000\n\t"
        "s_cbranch_scc1 LA_vmre%=\n\t"
        "s_movk_i32 s34, 2\n\t"
        "s_branch LA_die%=\n\t"
        "LA_die%=:\n\t"
        "v_mov_b32_e32 v30, s34\n\t"
        "s_mov_b32 s35, 33024\n\t"
        "s_mov_b64 exec, 1\n\t"
        "buffer_store_dword v30, off, s[12:15], s35 sc1\n\t"
        "s_endpgm\n\t"
        ".Lk_end_A:\n\t"
        :: REC_S_IN, "{v96}"(g_w), "{v97}"(wb0), "{v98}"(wb1), "{v99}"(rb0), "{v100}"(rb0 ^ 32u), "{v101}"(rb0 ^ 64u), "{v102}"(rb0 ^ 96u),
           "{v112}"(a_mem), "{v113}"(l_ms + w * 4096), "{v114}"(l_ms + ((w + 1) & 3) * 4096), "{v115}"(l_ms + ((w + 2) & 3) * 4096), "{v116}"(l_ms + ((w + 3) & 3) * 4096),
           "{v117}"(lb + w * 22528 + l * 16), "{v118}"(l_iw), "{v119}"(l_iq), "{v120}"(a_vm), "{v121}"(a_o), "{v122}"(h * 8192 + l * 16), "{v123}"(a_ox), "{v124}"(lb + L_KV + l * 16), "{s55}"(lb + L_KV + w * 2048),
           "{v125}"((24 + cp) * 16384 + l * 32), "{v126}"(16 * (w & 1) + n - 4 * g), "{v103}"((l & 1) ? 0xEEEEEEEEu : 0x44444444u),
           "{v127}"((l == 0 && w == 0) ? F_PROG + h * 128 : OOB)
        : "memory", "vcc", "scc", "v0", "v255", "a0", "a255", "s101");
    __builtin_unreachable();
}
__device__ __forceinline__ void rec_b(const Params& p, unsigned lb, const int wv, const int id, const int rg, const int cp) {
    const int w = wv, l = otid() & 63, n = l & 15, g = l >> 4, cs = id, rb = 32 + id * 2 + rg;
    const v4u sh = mk_srd(p.ws + ho_base(p.ws), HO_BYTES - 8192), sw = mk_srd(p.ws, W_TOTAL), s1 = mk_srd(p.Wv, 1024 * 4096 * 2), s2 = mk_srd(p.Wo, 1024 * 4096 * 2);
    const int r8 = l >> 3, pc = l & 7, pp = n >> 3, bb = (n >> 2) & 1, f = (g & 1) | (pp << 1);
    const unsigned stg = lb + LB_STG + w * LB_STGP;
    const unsigned g_v = r8 * 8192 + cs * 256 + rg * 128 + pc * 16, g_o = (l >> 2) * 2048 + cs * 64 + (l & 3) * 16;
    const unsigned wb0 = stg + r8 * 128 + (((pc >> 1) ^ (r8 >> 2)) << 5) + (pc & 1) * 16, wb1 = wb0 ^ 64u;
    const unsigned rb0 = stg + (16 * (g >> 1) + 8 * pp + 4 * (g & 1) + bb) * 128 + (f << 5) + (n & 3) * 8;
    const unsigned o_r = stg + (g >> 1) * (2048 + 128) + (g & 1) * (1024 + 32) + (bb + 4 * pp) * 64 + (n & 3) * 8;
    const unsigned a_mem = cp * 32768 + (w >> 1) * 16384 + (w & 1) * 4096 + l * 16;
    const unsigned a_vmw = w < 2 ? (16 + (cs >> 2)) * 16384 + (cs & 3) * 2048 + (2 * rg + w) * 512 + (l & 31) * 16 + (g >> 1) * 8 : OOB;
    const unsigned a_or = (8 + rg * 4 + w) * 16384 + l * 16;
    const unsigned a_mw = w * 32768 + (cs >> 4) * 16384 + ((cs >> 1) & 7) * 1024 + (cs & 1) * 512 + (l >> 5) * 256 + rg * 128 + (l & 31) * 4;
    const unsigned a_sw = (w == 0 && l < 4) ? (24 + l) * 16384 + (id * 2 + rg) * 32 : OOB;
    const bf16_t* m0p = p.mem0 + (4 * rg + ((l >> 3) & 3)) * 1024 + cs * 32 + (l >> 5) * 16 + (l & 7) * 2;
    asm volatile(
        "s_nop 4\n\t"
        "s_memtime s[70:71]\n\t"
        "s_memrealtime s[72:73]\n\t"
        "global_load_dword v109, v[108:109], off\n\t"
        "s_lshl_b32 s34, s43, 21\n\t"
        "s_add_u32 s34, s34, 0\n\t"
        "buffer_load_dwordx4 v[128:131], v96, s[16:19], s34 offen nt\n\t"
        "s_add_u32 s34, s34, 0x10000\n\t"
        "buffer_load_dwordx4 v[132:135], v96, s[16:19], s34 offen nt\n\t"
        "s_add_u32 s34, s34, 0x10000\n\t"
        "buffer_load_dwordx4 v[136:139], v96, s[16:19], s34 offen nt\n\t"
        "s_add_u32 s34, s34, 0x10000\n\t"
        "buffer_load_dwordx4 v[140:143], v96, s[16:19], s34 offen nt\n\t"
        "s_add_u32 s34, s34, 0x10000\n\t"
        "buffer_load_dwordx4 v[144:147], v96, s[16:19], s34 offen nt\n\t"
        "s_add_u32 s34, s34, 0x10000\n\t"
        "buffer_load_dwordx4 v[148:151], v96, s[16:19], s34 offen nt\n\t"
        "s_add_u32 s34, s34, 0x10000\n\t"
        "buffer_load_dwordx4 v[152:155], v96, s[16:19], s34 offen nt\n\t"
        "s_add_u32 s34, s34, 0x10000\n\t"
        "buffer_load_dwordx4 v[156:159], v96, s[16:19], s34 offen nt\n\t"
        "s_lshl_b32 s34, s43, 21\n\t"
        "s_add_u32 s34, s34, 524288\n\t"
        "buffer_load_dwordx4 v[160:163], v96, s[16:19], s34 offen nt\n\t"
        "s_add_u32 s34, s34, 0x10000\n\t"
        "buffer_load_dwordx4 v[164:167], v96, s[16:19], s34 offen nt\n\t"
        "s_add_u32 s34, s34, 0x10000\n\t"
        "buffer_load_dwordx4 v[168:171], v96, s[16:19], s34 offen nt\n\t"
        "s_add_u32 s34, s34, 0x10000\n\t"
        "buffer_load_dwordx4 v[172:175], v96, s[16:19], s34 offen nt\n\t"
        "s_add_u32 s34, s34, 0x10000\n\t"
        "buffer_load_dwordx4 v[176:179], v96, s[16:19], s34 offen nt\n\t"
        "s_add_u32 s34, s34, 0x10000\n\t"
        "buffer_load_dwordx4 v[180:183], v96, s[16:19], s34 offen nt\n\t"
        "s_add_u32 s34, s34, 0x10000\n\t"
        "buffer_load_dwordx4 v[184:187], v96, s[16:19], s34 offen nt\n\t"
        "s_add_u32 s34, s34, 0x10000\n\t"
        "buffer_load_dwordx4 v[188:191], v96, s[16:19], s34 offen nt\n\t"
        "s_lshl_b32 s34, s43, 21\n\t"
        "s_add_u32 s34, s34, 1048576\n\t"
        "buffer_load_dwordx4 v[192:195], v96, s[16:19], s34 offen nt\n\t"
        "s_add_u32 s34, s34, 0x10000\n\t"
        "buffer_load_dwordx4 v[196:199], v96, s[16:19], s34 offen nt\n\t"
        "s_add_u32 s34, s34, 0x10000\n\t"
        "buffer_load_dwordx4 v[200:203], v96, s[16:19], s34 offen nt\n\t"
        "s_add_u32 s34, s34, 0x10000\n\t"
        "buffer_load_dwordx4 v[204:207], v96, s[16:19], s34 offen nt\n\t"
        "s_add_u32 s34, s34, 0x10000\n\t"
        "buffer_load_dwordx4 v[208:211], v96, s[16:19], s34 offen nt\n\t"
        "s_add_u32 s34, s34, 0x10000\n\t"
        "buffer_load_dwordx4 v[212:215], v96, s[16:19], s34 offen nt\n\t"
        "s_add_u32 s34, s34, 0x10000\n\t"
        "buffer_load_dwordx4 v[216:219], v96, s[16:19], s34 offen nt\n\t"
        "s_add_u32 s34, s34, 0x10000\n\t"
        "buffer_load_dwordx4 v[220:223], v96, s[16:19], s34 offen nt\n\t"
        "s_waitcnt vmcnt(16)\n\t"
        "ds_write_b128 v98, v[128:131] offset:0\n\t"
        "ds_write_b128 v99, v[132:135] offset:1024\n\t"
        "ds_write_b128 v98, v[136:139] offset:2048\n\t"
        "ds_write_b128 v99, v[140:143] offset:3072\n\t"
        "ds_write_b128 v98, v[144:147] offset:4096\n\t"
        "ds_write_b128 v99, v[148:151] offset:5120\n\t"
        "ds_write_b128 v98, v[152:155] offset:6144\n\t"
        "ds_write_b128 v99, v[156:159] offset:7168\n\t"
        "ds_read_b64_tr_b16 v[128:129], v100 offset:0\n\t"
        "ds_read_b64_tr_b16 v[130:131], v100 offset:256\n\t"
        "ds_read_b64_tr_b16 v[132:133], v100 offset:4096\n\t"
        "ds_read_b64_tr_b16 v[134:135], v100 offset:4352\n\t"
        "s_waitcnt lgkmcnt(8)\n\t"
        "ds_read_b64_tr_b16 v[136:137], v101 offset:0\n\t"
        "ds_read_b64_tr_b16 v[138:139], v101 offset:256\n\t"
        "ds_read_b64_tr_b16 v[140:141], v101 offset:4096\n\t"
        "ds_read_b64_tr_b16 v[142:143], v101 offset:4352\n\t"
        "s_waitcnt lgkmcnt(8)\n\t"
        "ds_read_b64_tr_b16 v[144:145], v102 offset:0\n\t"
        "ds_read_b64_tr_b16 v[146:147], v102 offset:256\n\t"
        "ds_read_b64_tr_b16 v[148:149], v102 offset:4096\n\t"
        "ds_read_b64_tr_b16 v[150:151], v102 offset:4352\n\t"
        "s_waitcnt lgkmcnt(8)\n\t"
        "ds_read_b64_tr_b16 v[152:153], v103 offset:0\n\t"
        "ds_read_b64_tr_b16 v[154:155], v103 offset:256\n\t"
        "ds_read_b64_tr_b16 v[156:157], v103 offset:4096\n\t"
        "ds_read_b64_tr_b16 v[158:159], v103 offset:4352\n\t"
        "s_lshl_b32 s34, s43, 21\n\t"
        "s_add_u32 s34, s34, 1572864\n\t"
        "buffer_load_dwordx4 v[224:227], v96, s[16:19], s34 offen nt\n\t"
        "s_add_u32 s34, s34, 0x10000\n\t"
        "buffer_load_dwordx4 v[228:231], v96, s[16:19], s34 offen nt\n\t"
        "s_add_u32 s34, s34, 0x10000\n\t"
        "buffer_load_dwordx4 v[232:235], v96, s[16:19], s34 offen nt\n\t"
        "s_add_u32 s34, s34, 0x10000\n\t"
        "buffer_load_dwordx4 v[236:239], v96, s[16:19], s34 offen nt\n\t"
        "s_add_u32 s34, s34, 0x10000\n\t"
        "buffer_load_dwordx4 v[240:243], v96, s[16:19], s34 offen nt\n\t"
        "s_add_u32 s34, s34, 0x10000\n\t"
        "buffer_load_dwordx4 v[244:247], v96, s[16:19], s34 offen nt\n\t"
        "s_add_u32 s34, s34, 0x10000\n\t"
        "buffer_load_dwordx4 v[248:251], v96, s[16:19], s34 offen nt\n\t"
        "s_add_u32 s34, s34, 0x10000\n\t"
        "buffer_load_dwordx4 v[252:255], v96, s[16:19], s34 offen nt\n\t"
        "s_waitcnt lgkmcnt(0)\n\t"
        "s_waitcnt vmcnt(16)\n\t"
        "ds_write_b128 v98, v[160:163] offset:0\n\t"
        "ds_write_b128 v99, v[164:167] offset:1024\n\t"
        "ds_write_b128 v98, v[168:171] offset:2048\n\t"
        "ds_write_b128 v99, v[172:175] offset:3072\n\t"
        "ds_write_b128 v98, v[176:179] offset:4096\n\t"
        "ds_write_b128 v99, v[180:183] offset:5120\n\t"
        "ds_write_b128 v98, v[184:187] offset:6144\n\t"
        "ds_write_b128 v99, v[188:191] offset:7168\n\t"
        "ds_read_b64_tr_b16 v[160:161], v100 offset:0\n\t"
        "ds_read_b64_tr_b16 v[162:163], v100 offset:256\n\t"
        "ds_read_b64_tr_b16 v[164:165], v100 offset:4096\n\t"
        "ds_read_b64_tr_b16 v[166:167], v100 offset:4352\n\t"
        "s_waitcnt lgkmcnt(8)\n\t"
        "ds_read_b64_tr_b16 v[168:169], v101 offset:0\n\t"
        "ds_read_b64_tr_b16 v[170:171], v101 offset:256\n\t"
        "ds_read_b64_tr_b16 v[172:173], v101 offset:4096\n\t"
        "ds_read_b64_tr_b16 v[174:175], v101 offset:4352\n\t"
        "s_waitcnt lgkmcnt(8)\n\t"
        "ds_read_b64_tr_b16 v[176:177], v102 offset:0\n\t"
        "ds_read_b64_tr_b16 v[178:179], v102 offset:256\n\t"
        "ds_read_b64_tr_b16 v[180:181], v102 offset:4096\n\t"
        "ds_read_b64_tr_b16 v[182:183], v102 offset:4352\n\t"
        "s_waitcnt lgkmcnt(8)\n\t"
        "ds_read_b64_tr_b16 v[184:185], v103 offset:0\n\t"
        "ds_read_b64_tr_b16 v[186:187], v103 offset:256\n\t"
        "ds_read_b64_tr_b16 v[188:189], v103 offset:4096\n\t"
        "ds_read_b64_tr_b16 v[190:191], v103 offset:4352\n\t"
        "s_lshl_b32 s34, s43, 21\n\t"
        "s_add_u32 s34, s34, 0\n\t"
        "buffer_load_dwordx4 a[0:3], v97, s[20:23], s34 offen\n\t"
        "s_add_u32 s34, s34, 0x8000\n\t"
        "buffer_load_dwordx4 a[4:7], v97, s[20:23], s34 offen\n\t"
        "s_add_u32 s34, s34, 0x8000\n\t"
        "buffer_load_dwordx4 a[8:11], v97, s[20:23], s34 offen\n\t"
        "s_add_u32 s34, s34, 0x8000\n\t"
        "buffer_load_dwordx4 a[12:15], v97, s[20:23], s34 offen\n\t"
        "s_add_u32 s34, s34, 0x8000\n\t"
        "buffer_load_dwordx4 a[16:19], v97, s[20:23], s34 offen\n\t"
        "s_add_u32 s34, s34, 0x8000\n\t"
        "buffer_load_dwordx4 a[20:23], v97, s[20:23], s34 offen\n\t"
        "s_add_u32 s34, s34, 0x8000\n\t"
        "buffer_load_dwordx4 a[24:27], v97, s[20:23], s34 offen\n\t"
        "s_add_u32 s34, s34, 0x8000\n\t"
        "buffer_load_dwordx4 a[28:31], v97, s[20:23], s34 offen\n\t"
        "s_waitcnt lgkmcnt(0)\n\t"
        "s_waitcnt vmcnt(16)\n\t"
        "ds_write_b128 v98, v[192:195] offset:0\n\t"
        "ds_write_b128 v99, v[196:199] offset:1024\n\t"
        "ds_write_b128 v98, v[200:203] offset:2048\n\t"
        "ds_write_b128 v99, v[204:207] offset:3072\n\t"
        "ds_write_b128 v98, v[208:211] offset:4096\n\t"
        "ds_write_b128 v99, v[212:215] offset:5120\n\t"
        "ds_write_b128 v98, v[216:219] offset:6144\n\t"
        "ds_write_b128 v99, v[220:223] offset:7168\n\t"
        "ds_read_b64_tr_b16 v[192:193], v100 offset:0\n\t"
        "ds_read_b64_tr_b16 v[194:195], v100 offset:256\n\t"
        "ds_read_b64_tr_b16 v[196:197], v100 offset:4096\n\t"
        "ds_read_b64_tr_b16 v[198:199], v100 offset:4352\n\t"
        "s_waitcnt lgkmcnt(8)\n\t"
        "ds_read_b64_tr_b16 v[200:201], v101 offset:0\n\t"
        "ds_read_b64_tr_b16 v[202:203], v101 offset:256\n\t"
        "ds_read_b64_tr_b16 v[204:205], v101 offset:4096\n\t"
        "ds_read_b64_tr_b16 v[206:207], v101 offset:4352\n\t"
        "s_waitcnt lgkmcnt(8)\n\t"
        "ds_read_b64_tr_b16 v[208:209], v102 offset:0\n\t"
        "ds_read_b64_tr_b16 v[210:211], v102 offset:256\n\t"
        "ds_read_b64_tr_b16 v[212:213], v102 offset:4096\n\t"
        "ds_read_b64_tr_b16 v[214:215], v102 offset:4352\n\t"
        "s_waitcnt lgkmcnt(8)\n\t"
        "ds_read_b64_tr_b16 v[216:217], v103 offset:0\n\t"
        "ds_read_b64_tr_b16 v[218:219], v103 offset:256\n\t"
        "ds_read_b64_tr_b16 v[220:221], v103 offset:4096\n\t"
        "ds_read_b64_tr_b16 v[222:223], v103 offset:4352\n\t"
        "s_lshl_b32 s34, s43, 21\n\t"
        "s_add_u32 s34, s34, 262144\n\t"
        "buffer_load_dwordx4 a[32:35], v97, s[20:23], s34 offen\n\t"
        "s_add_u32 s34, s34, 0x8000\n\t"
        "buffer_load_dwordx4 a[36:39], v97, s[20:23], s34 offen\n\t"
        "s_add_u32 s34, s34, 0x8000\n\t"
        "buffer_load_dwordx4 a[40:43], v97, s[20:23], s34 offen\n\t"
        "s_add_u32 s34, s34, 0x8000\n\t"
        "buffer_load_dwordx4 a[44:47], v97, s[20:23], s34 offen\n\t"
        "s_add_u32 s34, s34, 0x8000\n\t"
        "buffer_load_dwordx4 a[48:51], v97, s[20:23], s34 offen\n\t"
        "s_add_u32 s34, s34, 0x8000\n\t"
        "buffer_load_dwordx4 a[52:55], v97, s[20:23], s34 offen\n\t"
        "s_add_u32 s34, s34, 0x8000\n\t"
        "buffer_load_dwordx4 a[56:59], v97, s[20:23], s34 offen\n\t"
        "s_add_u32 s34, s34, 0x8000\n\t"
        "buffer_load_dwordx4 a[60:63], v97, s[20:23], s34 offen\n\t"
        "s_waitcnt lgkmcnt(0)\n\t"
        "s_waitcnt vmcnt(16)\n\t"
        "ds_write_b128 v98, v[224:227] offset:0\n\t"
        "ds_write_b128 v99, v[228:231] offset:1024\n\t"
        "ds_write_b128 v98, v[232:235] offset:2048\n\t"
        "ds_write_b128 v99, v[236:239] offset:3072\n\t"
        "ds_write_b128 v98, v[240:243] offset:4096\n\t"
        "ds_write_b128 v99, v[244:247] offset:5120\n\t"
        "ds_write_b128 v98, v[248:251] offset:6144\n\t"
        "ds_write_b128 v99, v[252:255] offset:7168\n\t"
        "ds_read_b64_tr_b16 v[224:225], v100 offset:0\n\t"
        "ds_read_b64_tr_b16 v[226:227], v100 offset:256\n\t"
        "ds_read_b64_tr_b16 v[228:229], v100 offset:4096\n\t"
        "ds_read_b64_tr_b16 v[230:231], v100 offset:4352\n\t"
        "s_waitcnt lgkmcnt(8)\n\t"
        "ds_read_b64_tr_b16 v[232:233], v101 offset:0\n\t"
        "ds_read_b64_tr_b16 v[234:235], v101 offset:256\n\t"
        "ds_read_b64_tr_b16 v[236:237], v101 offset:4096\n\t"
        "ds_read_b64_tr_b16 v[238:239], v101 offset:4352\n\t"
        "s_waitcnt lgkmcnt(8)\n\t"
        "ds_read_b64_tr_b16 v[240:241], v102 offset:0\n\t"
        "ds_read_b64_tr_b16 v[242:243], v102 offset:256\n\t"
        "ds_read_b64_tr_b16 v[244:245], v102 offset:4096\n\t"
        "ds_read_b64_tr_b16 v[246:247], v102 offset:4352\n\t"
        "s_waitcnt lgkmcnt(8)\n\t"
        "ds_read_b64_tr_b16 v[248:249], v103 offset:0\n\t"
        "ds_read_b64_tr_b16 v[250:251], v103 offset:256\n\t"
        "ds_read_b64_tr_b16 v[252:253], v103 offset:4096\n\t"
        "ds_read_b64_tr_b16 v[254:255], v103 offset:4352\n\t"
        "s_lshl_b32 s34, s43, 21\n\t"
        "s_add_u32 s34, s34, 524288\n\t"
        "buffer_load_dwordx4 a[64:67], v97, s[20:23], s34 offen\n\t"
        "s_add_u32 s34, s34, 0x8000\n\t"
        "buffer_load_dwordx4 a[68:71], v97, s[20:23], s34 offen\n\t"
        "s_add_u32 s34, s34, 0x8000\n\t"
        "buffer_load_dwordx4 a[72:75], v97, s[20:23], s34 offen\n\t"
        "s_add_u32 s34, s34, 0x8000\n\t"
        "buffer_load_dwordx4 a[76:79], v97, s[20:23], s34 offen\n\t"
        "s_add_u32 s34, s34, 0x8000\n\t"
        "buffer_load_dwordx4 a[80:83], v97, s[20:23], s34 offen\n\t"
        "s_add_u32 s34, s34, 0x8000\n\t"
        "buffer_load_dwordx4 a[84:87], v97, s[20:23], s34 offen\n\t"
        "s_add_u32 s34, s34, 0x8000\n\t"
        "buffer_load_dwordx4 a[88:91], v97, s[20:23], s34 offen\n\t"
        "s_add_u32 s34, s34, 0x8000\n\t"
        "buffer_load_dwordx4 a[92:95], v97, s[20:23], s34 offen\n\t"
        "s_waitcnt lgkmcnt(0)\n\t"
        "s_waitcnt vmcnt(16)\n\t"
        "ds_write_b128 v122, a[0:3] offset:0\n\t"
        "ds_write_b128 v122, a[4:7] offset:1056\n\t"
        "ds_write_b128 v122, a[8:11] offset:2176\n\t"
        "ds_write_b128 v122, a[12:15] offset:3232\n\t"
        "ds_write_b128 v122, a[16:19] offset:4352\n\t"
        "ds_write_b128 v122, a[20:23] offset:5408\n\t"
        "ds_write_b128 v122, a[24:27] offset:6528\n\t"
        "ds_write_b128 v122, a[28:31] offset:7584\n\t"
        "ds_read_b64_tr_b16 a[0:1], v123 offset:0\n\t"
        "ds_read_b64_tr_b16 a[2:3], v123 offset:128\n\t"
        "ds_read_b64_tr_b16 a[4:5], v123 offset:4352\n\t"
        "ds_read_b64_tr_b16 a[6:7], v123 offset:4480\n\t"
        "s_waitcnt lgkmcnt(8)\n\t"
        "ds_read_b64_tr_b16 a[8:9], v123 offset:32\n\t"
        "ds_read_b64_tr_b16 a[10:11], v123 offset:160\n\t"
        "ds_read_b64_tr_b16 a[12:13], v123 offset:4384\n\t"
        "ds_read_b64_tr_b16 a[14:15], v123 offset:4512\n\t"
        "s_waitcnt lgkmcnt(8)\n\t"
        "ds_read_b64_tr_b16 a[16:17], v123 offset:512\n\t"
        "ds_read_b64_tr_b16 a[18:19], v123 offset:640\n\t"
        "ds_read_b64_tr_b16 a[20:21], v123 offset:4864\n\t"
        "ds_read_b64_tr_b16 a[22:23], v123 offset:4992\n\t"
        "s_waitcnt lgkmcnt(8)\n\t"
        "ds_read_b64_tr_b16 a[24:25], v123 offset:544\n\t"
        "ds_read_b64_tr_b16 a[26:27], v123 offset:672\n\t"
        "ds_read_b64_tr_b16 a[28:29], v123 offset:4896\n\t"
        "ds_read_b64_tr_b16 a[30:31], v123 offset:5024\n\t"
        "s_waitcnt lgkmcnt(8)\n\t"
        "s_lshl_b32 s34, s43, 21\n\t"
        "s_add_u32 s34, s34, 786432\n\t"
        "buffer_load_dwordx4 a[96:99], v97, s[20:23], s34 offen\n\t"
        "s_add_u32 s34, s34, 0x8000\n\t"
        "buffer_load_dwordx4 a[100:103], v97, s[20:23], s34 offen\n\t"
        "s_add_u32 s34, s34, 0x8000\n\t"
        "buffer_load_dwordx4 a[104:107], v97, s[20:23], s34 offen\n\t"
        "s_add_u32 s34, s34, 0x8000\n\t"
        "buffer_load_dwordx4 a[108:111], v97, s[20:23], s34 offen\n\t"
        "s_add_u32 s34, s34, 0x8000\n\t"
        "buffer_load_dwordx4 a[112:115], v97, s[20:23], s34 offen\n\t"
        "s_add_u32 s34, s34, 0x8000\n\t"
        "buffer_load_dwordx4 a[116:119], v97, s[20:23], s34 offen\n\t"
        "s_add_u32 s34, s34, 0x8000\n\t"
        "buffer_load_dwordx4 a[120:123], v97, s[20:23], s34 offen\n\t"
        "s_add_u32 s34, s34, 0x8000\n\t"
        "buffer_load_dwordx4 a[124:127], v97, s[20:23], s34 offen\n\t"
        "s_waitcnt lgkmcnt(0)\n\t"
        "s_waitcnt vmcnt(16)\n\t"
        "ds_write_b128 v122, a[32:35] offset:0\n\t"
        "ds_write_b128 v122, a[36:39] offset:1056\n\t"
        "ds_write_b128 v122, a[40:43] offset:2176\n\t"
        "ds_write_b128 v122, a[44:47] offset:3232\n\t"
        "ds_write_b128 v122, a[48:51] offset:4352\n\t"
        "ds_write_b128 v122, a[52:55] offset:5408\n\t"
        "ds_write_b128 v122, a[56:59] offset:6528\n\t"
        "ds_write_b128 v122, a[60:63] offset:7584\n\t"
        "ds_read_b64_tr_b16 a[32:33], v123 offset:0\n\t"
        "ds_read_b64_tr_b16 a[34:35], v123 offset:128\n\t"
        "ds_read_b64_tr_b16 a[36:37], v123 offset:4352\n\t"
        "ds_read_b64_tr_b16 a[38:39], v123 offset:4480\n\t"
        "s_waitcnt lgkmcnt(8)\n\t"
        "ds_read_b64_tr_b16 a[40:41], v123 offset:32\n\t"
        "ds_read_b64_tr_b16 a[42:43], v123 offset:160\n\t"
        "ds_read_b64_tr_b16 a[44:45], v123 offset:4384\n\t"
        "ds_read_b64_tr_b16 a[46:47], v123 offset:4512\n\t"
        "s_waitcnt lgkmcnt(8)\n\t"
        "ds_read_b64_tr_b16 a[48:49], v123 offset:512\n\t"
        "ds_read_b64_tr_b16 a[50:51], v123 offset:640\n\t"
        "ds_read_b64_tr_b16 a[52:53], v123 offset:4864\n\t"
        "ds_read_b64_tr_b16 a[54:55], v123 offset:4992\n\t"
        "s_waitcnt lgkmcnt(8)\n\t"
        "ds_read_b64_tr_b16 a[56:57], v123 offset:544\n\t"
        "ds_read_b64_tr_b16 a[58:59], v123 offset:672\n\t"
        "ds_read_b64_tr_b16 a[60:61], v123 offset:4896\n\t"
        "ds_read_b64_tr_b16 a[62:63], v123 offset:5024\n\t"
        "s_waitcnt lgkmcnt(8)\n\t"
        "s_lshl_b32 s34, s43, 21\n\t"
        "s_add_u32 s34, s34, 1048576\n\t"
        "buffer_load_dwordx4 a[128:131], v97, s[20:23], s34 offen\n\t"
        "s_add_u32 s34, s34, 0x8000\n\t"
        "buffer_load_dwordx4 a[132:135], v97, s[20:23], s34 offen\n\t"
        "s_add_u32 s34, s34, 0x8000\n\t"
        "buffer_load_dwordx4 a[136:139], v97, s[20:23], s34 offen\n\t"
        "s_add_u32 s34, s34, 0x8000\n\t"
        "buffer_load_dwordx4 a[140:143], v97, s[20:23], s34 offen\n\t"
        "s_add_u32 s34, s34, 0x8000\n\t"
        "buffer_load_dwordx4 a[144:147], v97, s[20:23], s34 offen\n\t"
        "s_add_u32 s34, s34, 0x8000\n\t"
        "buffer_load_dwordx4 a[148:151], v97, s[20:23], s34 offen\n\t"
        "s_add_u32 s34, s34, 0x8000\n\t"
        "buffer_load_dwordx4 a[152:155], v97, s[20:23], s34 offen\n\t"
        "s_add_u32 s34, s34, 0x8000\n\t"
        "buffer_load_dwordx4 a[156:159], v97, s[20:23], s34 offen\n\t"
        "s_waitcnt lgkmcnt(0)\n\t"
        "s_waitcnt vmcnt(16)\n\t"
        "ds_write_b128 v122, a[64:67] offset:0\n\t"
        "ds_write_b128 v122, a[68:71] offset:1056\n\t"
        "ds_write_b128 v122, a[72:75] offset:2176\n\t"
        "ds_write_b128 v122, a[76:79] offset:3232\n\t"
        "ds_write_b128 v122, a[80:83] offset:4352\n\t"
        "ds_write_b128 v122, a[84:87] offset:5408\n\t"
        "ds_write_b128 v122, a[88:91] offset:6528\n\t"
        "ds_write_b128 v122, a[92:95] offset:7584\n\t"
        "ds_read_b64_tr_b16 a[64:65], v123 offset:0\n\t"
        "ds_read_b64_tr_b16 a[66:67], v123 offset:128\n\t"
        "ds_read_b64_tr_b16 a[68:69], v123 offset:4352\n\t"
        "ds_read_b64_tr_b16 a[70:71], v123 offset:4480\n\t"
        "s_waitcnt lgkmcnt(8)\n\t"
        "ds_read_b64_tr_b16 a[72:73], v123 offset:32\n\t"
        "ds_read_b64_tr_b16 a[74:75], v123 offset:160\n\t"
        "ds_read_b64_tr_b16 a[76:77], v123 offset:4384\n\t"
        "ds_read_b64_tr_b16 a[78:79], v123 offset:4512\n\t"
        "s_waitcnt lgkmcnt(8)\n\t"
        "ds_read_b64_tr_b16 a[80:81], v123 offset:512\n\t"
        "ds_read_b64_tr_b16 a[82:83], v123 offset:640\n\t"
        "ds_read_b64_tr_b16 a[84:85], v123 offset:4864\n\t"
        "ds_read_b64_tr_b16 a[86:87], v123 offset:4992\n\t"
        "s_waitcnt lgkmcnt(8)\n\t"
        "ds_read_b64_tr_b16 a[88:89], v123 offset:544\n\t"
        "ds_read_b64_tr_b16 a[90:91], v123 offset:672\n\t"
        "ds_read_b64_tr_b16 a[92:93], v123 offset:4896\n\t"
        "ds_read_b64_tr_b16 a[94:95], v123 offset:5024\n\t"
        "s_waitcnt lgkmcnt(8)\n\t"
        "s_lshl_b32 s34, s43, 21\n\t"
        "s_add_u32 s34, s34, 1310720\n\t"
        "buffer_load_dwordx4 a[160:163], v97, s[20:23], s34 offen\n\t"
        "s_add_u32 s34, s34, 0x8000\n\t"
        "buffer_load_dwordx4 a[164:167], v97, s[20:23], s34 offen\n\t"
        "s_add_u32 s34, s34, 0x8000\n\t"
        "buffer_load_dwordx4 a[168:171], v97, s[20:23], s34 offen\n\t"
        "s_add_u32 s34, s34, 0x8000\n\t"
        "buffer_load_dwordx4 a[172:175], v97, s[20:23], s34 offen\n\t"
        "s_add_u32 s34, s34, 0x8000\n\t"
        "buffer_load_dwordx4 a[176:179], v97, s[20:23], s34 offen\n\t"
        "s_add_u32 s34, s34, 0x8000\n\t"
        "buffer_load_dwordx4 a[180:183], v97, s[20:23], s34 offen\n\t"
        "s_add_u32 s34, s34, 0x8000\n\t"
        "buffer_load_dwordx4 a[184:187], v97, s[20:23], s34 offen\n\t"
        "s_add_u32 s34, s34, 0x8000\n\t"
        "buffer_load_dwordx4 a[188:191], v97, s[20:23], s34 offen\n\t"
        "s_waitcnt lgkmcnt(0)\n\t"
        "s_waitcnt vmcnt(16)\n\t"
        "ds_write_b128 v122, a[96:99] offset:0\n\t"
        "ds_write_b128 v122, a[100:103] offset:1056\n\t"
        "ds_write_b128 v122, a[104:107] offset:2176\n\t"
        "ds_write_b128 v122, a[108:111] offset:3232\n\t"
        "ds_write_b128 v122, a[112:115] offset:4352\n\t"
        "ds_write_b128 v122, a[116:119] offset:5408\n\t"
        "ds_write_b128 v122, a[120:123] offset:6528\n\t"
        "ds_write_b128 v122, a[124:127] offset:7584\n\t"
        "ds_read_b64_tr_b16 a[96:97], v123 offset:0\n\t"
        "ds_read_b64_tr_b16 a[98:99], v123 offset:128\n\t"
        "ds_read_b64_tr_b16 a[100:101], v123 offset:4352\n\t"
        "ds_read_b64_tr_b16 a[102:103], v123 offset:4480\n\t"
        "s_waitcnt lgkmcnt(8)\n\t"
        "ds_read_b64_tr_b16 a[104:105], v123 offset:32\n\t"
        "ds_read_b64_tr_b16 a[106:107], v123 offset:160\n\t"
        "ds_read_b64_tr_b16 a[108:109], v123 offset:4384\n\t"
        "ds_read_b64_tr_b16 a[110:111], v123 offset:4512\n\t"
        "s_waitcnt lgkmcnt(8)\n\t"
        "ds_read_b64_tr_b16 a[112:113], v123 offset:512\n\t"
        "ds_read_b64_tr_b16 a[114:115], v123 offset:640\n\t"
        "ds_read_b64_tr_b16 a[116:117], v123 offset:4864\n\t"
        "ds_read_b64_tr_b16 a[118:119], v123 offset:4992\n\t"
        "s_waitcnt lgkmcnt(8)\n\t"
        "ds_read_b64_tr_b16 a[120:121], v123 offset:544\n\t"
        "ds_read_b64_tr_b16 a[122:123], v123 offset:672\n\t"
        "ds_read_b64_tr_b16 a[124:125], v123 offset:4896\n\t"
        "ds_read_b64_tr_b16 a[126:127], v123 offset:5024\n\t"
        "s_waitcnt lgkmcnt(8)\n\t"
        "s_lshl_b32 s34, s43, 21\n\t"
        "s_add_u32 s34, s34, 1572864\n\t"
        "buffer_load_dwordx4 a[192:195], v97, s[20:23], s34 offen\n\t"
        "s_add_u32 s34, s34, 0x8000\n\t"
        "buffer_load_dwordx4 a[196:199], v97, s[20:23], s34 offen\n\t"
        "s_add_u32 s34, s34, 0x8000\n\t"
        "buffer_load_dwordx4 a[200:203], v97, s[20:23], s34 offen\n\t"
        "s_add_u32 s34, s34, 0x8000\n\t"
        "buffer_load_dwordx4 a[204:207], v97, s[20:23], s34 offen\n\t"
        "s_add_u32 s34, s34, 0x8000\n\t"
        "buffer_load_dwordx4 a[208:211], v97, s[20:23], s34 offen\n\t"
        "s_add_u32 s34, s34, 0x8000\n\t"
        "buffer_load_dwordx4 a[212:215], v97, s[20:23], s34 offen\n\t"
        "s_add_u32 s34, s34, 0x8000\n\t"
        "buffer_load_dwordx4 a[216:219], v97, s[20:23], s34 offen\n\t"
        "s_add_u32 s34, s34, 0x8000\n\t"
        "buffer_load_dwordx4 a[220:223], v97, s[20:23], s34 offen\n\t"
        "s_waitcnt lgkmcnt(0)\n\t"
        "s_waitcnt vmcnt(16)\n\t"
        "ds_write_b128 v122, a[128:131] offset:0\n\t"
        "ds_write_b128 v122, a[132:135] offset:1056\n\t"
        "ds_write_b128 v122, a[136:139] offset:2176\n\t"
        "ds_write_b128 v122, a[140:143] offset:3232\n\t"
        "ds_write_b128 v122, a[144:147] offset:4352\n\t"
        "ds_write_b128 v122, a[148:151] offset:5408\n\t"
        "ds_write_b128 v122, a[152:155] offset:6528\n\t"
        "ds_write_b128 v122, a[156:159] offset:7584\n\t"
        "ds_read_b64_tr_b16 a[128:129], v123 offset:0\n\t"
        "ds_read_b64_tr_b16 a[130:131], v123 offset:128\n\t"
        "ds_read_b64_tr_b16 a[132:133], v123 offset:4352\n\t"
        "ds_read_b64_tr_b16 a[134:135], v123 offset:4480\n\t"
        "s_waitcnt lgkmcnt(8)\n\t"
        "ds_read_b64_tr_b16 a[136:137], v123 offset:32\n\t"
        "ds_read_b64_tr_b16 a[138:139], v123 offset:160\n\t"
        "ds_read_b64_tr_b16 a[140:141], v123 offset:4384\n\t"
        "ds_read_b64_tr_b16 a[142:143], v123 offset:4512\n\t"
        "s_waitcnt lgkmcnt(8)\n\t"
        "ds_read_b64_tr_b16 a[144:145], v123 offset:512\n\t"
        "ds_read_b64_tr_b16 a[146:147], v123 offset:640\n\t"
        "ds_read_b64_tr_b16 a[148:149], v123 offset:4864\n\t"
        "ds_read_b64_tr_b16 a[150:151], v123 offset:4992\n\t"
        "s_waitcnt lgkmcnt(8)\n\t"
        "ds_read_b64_tr_b16 a[152:153], v123 offset:544\n\t"
        "ds_read_b64_tr_b16 a[154:155], v123 offset:672\n\t"
        "ds_read_b64_tr_b16 a[156:157], v123 offset:4896\n\t"
        "ds_read_b64_tr_b16 a[158:159], v123 offset:5024\n\t"
        "s_waitcnt lgkmcnt(8)\n\t"
        "s_lshl_b32 s34, s43, 21\n\t"
        "s_add_u32 s34, s34, 1835008\n\t"
        "buffer_load_dwordx4 a[224:227], v97, s[20:23], s34 offen\n\t"
        "s_add_u32 s34, s34, 0x8000\n\t"
        "buffer_load_dwordx4 a[228:231], v97, s[20:23], s34 offen\n\t"
        "s_add_u32 s34, s34, 0x8000\n\t"
        "buffer_load_dwordx4 a[232:235], v97, s[20:23], s34 offen\n\t"
        "s_add_u32 s34, s34, 0x8000\n\t"
        "buffer_load_dwordx4 a[236:239], v97, s[20:23], s34 offen\n\t"
        "s_add_u32 s34, s34, 0x8000\n\t"
        "buffer_load_dwordx4 a[240:243], v97, s[20:23], s34 offen\n\t"
        "s_add_u32 s34, s34, 0x8000\n\t"
        "buffer_load_dwordx4 a[244:247], v97, s[20:23], s34 offen\n\t"
        "s_add_u32 s34, s34, 0x8000\n\t"
        "buffer_load_dwordx4 a[248:251], v97, s[20:23], s34 offen\n\t"
        "s_add_u32 s34, s34, 0x8000\n\t"
        "buffer_load_dwordx4 a[252:255], v97, s[20:23], s34 offen\n\t"
        "s_waitcnt lgkmcnt(0)\n\t"
        "s_waitcnt vmcnt(16)\n\t"
        "ds_write_b128 v122, a[160:163] offset:0\n\t"
        "ds_write_b128 v122, a[164:167] offset:1056\n\t"
        "ds_write_b128 v122, a[168:171] offset:2176\n\t"
        "ds_write_b128 v122, a[172:175] offset:3232\n\t"
        "ds_write_b128 v122, a[176:179] offset:4352\n\t"
        "ds_write_b128 v122, a[180:183] offset:5408\n\t"
        "ds_write_b128 v122, a[184:187] offset:6528\n\t"
        "ds_write_b128 v122, a[188:191] offset:7584\n\t"
        "ds_read_b64_tr_b16 a[160:161], v123 offset:0\n\t"
        "ds_read_b64_tr_b16 a[162:163], v123 offset:128\n\t"
        "ds_read_b64_tr_b16 a[164:165], v123 offset:4352\n\t"
        "ds_read_b64_tr_b16 a[166:167], v123 offset:4480\n\t"
        "s_waitcnt lgkmcnt(8)\n\t"
        "ds_read_b64_tr_b16 a[168:169], v123 offset:32\n\t"
        "ds_read_b64_tr_b16 a[170:171], v123 offset:160\n\t"
        "ds_read_b64_tr_b16 a[172:173], v123 offset:4384\n\t"
        "ds_read_b64_tr_b16 a[174:175], v123 offset:4512\n\t"
        "s_waitcnt lgkmcnt(8)\n\t"
        "ds_read_b64_tr_b16 a[176:177], v123 offset:512\n\t"
        "ds_read_b64_tr_b16 a[178:179], v123 offset:640\n\t"
        "ds_read_b64_tr_b16 a[180:181], v123 offset:4864\n\t"
        "ds_read_b64_tr_b16 a[182:183], v123 offset:4992\n\t"
        "s_waitcnt lgkmcnt(8)\n\t"
        "ds_read_b64_tr_b16 a[184:185], v123 offset:544\n\t"
        "ds_read_b64_tr_b16 a[186:187], v123 offset:672\n\t"
        "ds_read_b64_tr_b16 a[188:189], v123 offset:4896\n\t"
        "ds_read_b64_tr_b16 a[190:191], v123 offset:5024\n\t"
        "s_waitcnt lgkmcnt(8)\n\t"
        "s_waitcnt lgkmcnt(0)\n\t"
        "s_waitcnt vmcnt(8)\n\t"
        "ds_write_b128 v122, a[192:195] offset:0\n\t"
        "ds_write_b128 v122, a[196:199] offset:1056\n\t"
        "ds_write_b128 v122, a[200:203] offset:2176\n\t"
        "ds_write_b128 v122, a[204:207] offset:3232\n\t"
        "ds_write_b128 v122, a[208:211] offset:4352\n\t"
        "ds_write_b128 v122, a[212:215] offset:5408\n\t"
        "ds_write_b128 v122, a[216:219] offset:6528\n\t"
        "ds_write_b128 v122, a[220:223] offset:7584\n\t"
        "ds_read_b64_tr_b16 a[192:193], v123 offset:0\n\t"
        "ds_read_b64_tr_b16 a[194:195], v123 offset:128\n\t"
        "ds_read_b64_tr_b16 a[196:197], v123 offset:4352\n\t"
        "ds_read_b64_tr_b16 a[198:199], v123 offset:4480\n\t"
        "s_waitcnt lgkmcnt(8)\n\t"
        "ds_read_b64_tr_b16 a[200:201], v123 offset:32\n\t"
        "ds_read_b64_tr_b16 a[202:203], v123 offset:160\n\t"
        "ds_read_b64_tr_b16 a[204:205], v123 offset:4384\n\t"
        "ds_read_b64_tr_b16 a[206:207], v123 offset:4512\n\t"
        "s_waitcnt lgkmcnt(8)\n\t"
        "ds_read_b64_tr_b16 a[208:209], v123 offset:512\n\t"
        "ds_read_b64_tr_b16 a[210:211], v123 offset:640\n\t"
        "ds_read_b64_tr_b16 a[212:213], v123 offset:4864\n\t"
        "ds_read_b64_tr_b16 a[214:215], v123 offset:4992\n\t"
        "s_waitcnt lgkmcnt(8)\n\t"
        "ds_read_b64_tr_b16 a[216:217], v123 offset:544\n\t"
        "ds_read_b64_tr_b16 a[218:219], v123 offset:672\n\t"
        "ds_read_b64_tr_b16 a[220:221], v123 offset:4896\n\t"
        "ds_read_b64_tr_b16 a[222:223], v123 offset:5024\n\t"
        "s_waitcnt lgkmcnt(8)\n\t"
        "s_waitcnt lgkmcnt(0)\n\t"
        "s_waitcnt vmcnt(0)\n\t"
        "ds_write_b128 v122, a[224:227] offset:0\n\t"
        "ds_write_b128 v122, a[228:231] offset:1056\n\t"
        "ds_write_b128 v122, a[232:235] offset:2176\n\t"
        "ds_write_b128 v122, a[236:239] offset:3232\n\t"
        "ds_write_b128 v122, a[240:243] offset:4352\n\t"
        "ds_write_b128 v122, a[244:247] offset:5408\n\t"
        "ds_write_b128 v122, a[248:251] offset:6528\n\t"
        "ds_write_b128 v122, a[252:255] offset:7584\n\t"
        "ds_read_b64_tr_b16 a[224:225], v123 offset:0\n\t"
        "ds_read_b64_tr_b16 a[226:227], v123 offset:128\n\t"
        "ds_read_b64_tr_b16 a[228:229], v123 offset:4352\n\t"
        "ds_read_b64_tr_b16 a[230:231], v123 offset:4480\n\t"
        "s_waitcnt lgkmcnt(8)\n\t"
        "ds_read_b64_tr_b16 a[232:233], v123 offset:32\n\t"
        "ds_read_b64_tr_b16 a[234:235], v123 offset:160\n\t"
        "ds_read_b64_tr_b16 a[236:237], v123 offset:4384\n\t"
        "ds_read_b64_tr_b16 a[238:239], v123 offset:4512\n\t"
        "s_waitcnt lgkmcnt(8)\n\t"
        "ds_read_b64_tr_b16 a[240:241], v123 offset:512\n\t"
        "ds_read_b64_tr_b16 a[242:243], v123 offset:640\n\t"
        "ds_read_b64_tr_b16 a[244:245], v123 offset:4864\n\t"
        "ds_read_b64_tr_b16 a[246:247], v123 offset:4992\n\t"
        "s_waitcnt lgkmcnt(8)\n\t"
        "ds_read_b64_tr_b16 a[248:249], v123 offset:544\n\t"
        "ds_read_b64_tr_b16 a[250:251], v123 offset:672\n\t"
        "ds_read_b64_tr_b16 a[252:253], v123 offset:4896\n\t"
        "ds_read_b64_tr_b16 a[254:255], v123 offset:5024\n\t"
        "s_waitcnt lgkmcnt(8)\n\t"
        "s_mov_b32 s35, 33792\n\t"
        "buffer_load_dword v0, off, s[12:15], s35 sc1\n\t"
        "s_waitcnt lgkmcnt(0)\n\t"
        "s_waitcnt vmcnt(0)\n\t"
        "s_mov_b32 s42, 0\n\t"
        "LB_cal%=:\n\t"
        "v_readfirstlane_b32 s34, v0\n\t"
        "s_nop 0\n\t"
        "s_cmp_lg_u32 s34, 0\n\t"
        "s_cbranch_scc1 LB_calok%=\n\t"
        "s_sleep 4\n\t"
        "s_mov_b32 s35, 33792\n\t"
        "buffer_load_dword v0, off, s[12:15], s35 sc1\n\t"
        "s_waitcnt vmcnt(0)\n\t"
        "s_add_u32 s42, s42, 1\n\t"
        "s_cmp_lt_u32 s42, 0x100000\n\t"
        "s_cbranch_scc1 LB_cal%=\n\t"
        "s_movk_i32 s34, 8\n\t"
        "s_branch LB_die%=\n\t"
        "LB_calok%=:\n\t"
        "v_readfirstlane_b32 s34, v0\n\t"
        "s_nop 0\n\t"
        "s_and_b32 s34, s34, 8192\n\t"
        "s_add_u32 s8, s8, s34\n\t"
        "s_addc_u32 s9, s9, 0\n\t"
        "v_min_u32_e32 v109, -2, v109\n\t"
        "buffer_store_dword v109, v116, s[8:11], 0 offen sc1\n\t"
        "s_memtime s[74:75]\n\t"
        "s_memrealtime s[32:33]\n\t"
        "s_waitcnt lgkmcnt(0)\n\t"
        "v_mov_b32_e32 v110, s32\n\t"
        "s_sub_u32 s34, s74, s70\n\t"
        "s_sub_u32 s35, s32, s72\n\t"
        "v_cvt_f32_u32_e32 v2, s34\n\t"
        "v_cvt_f32_u32_e32 v3, s35\n\t"
        "v_rcp_f32_e32 v2, v2\n\t"
        "s_nop 0\n\t"
        "v_mul_f32_e32 v2, v2, v3\n\t"
        "v_mul_f32_e32 v3, 0x45354000, v2\n\t"
        "v_cvt_u32_f32_e32 v3, v3\n\t"
        "s_nop 0\n\t"
        "v_readfirstlane_b32 s34, v3\n\t"
        "s_add_u32 s35, s29, 40\n\t"
        "s_min_u32 s34, s34, s35\n\t"
        "s_sub_u32 s35, s29, 10\n\t"
        "s_max_u32 s29, s34, s35\n\t"
        "v_mul_f32_e32 v3, 0x44d70000, v2\n\t"
        "v_cvt_u32_f32_e32 v3, v3\n\t"
        "s_nop 0\n\t"
        "v_readfirstlane_b32 s34, v3\n\t"
        "s_add_u32 s35, s30, 40\n\t"
        "s_min_u32 s34, s34, s35\n\t"
        "s_sub_u32 s35, s30, 10\n\t"
        "s_max_u32 s30, s34, s35\n\t"
        "s_lshl_b32 s34, s29, 8\n\t"
        "s_lshl_b32 s35, s30, 20\n\t"
        "s_or_b32 s54, s34, s35\n\t"
        "s_add_u32 s34, s54, 8400128\n\t"
        "v_mov_b32_e32 v111, s34\n\t"
        "s_barrier\n\t"
        "s_cmp_lg_u32 s43, 0\n\t"
        "s_cbranch_scc1 LB_st%=\n\t"
        "v_mov_b32_e32 v2, 1\n\t"
        "s_mov_b32 s35, 34304\n\t"
        "s_mov_b64 exec, 1\n\t"
        "buffer_atomic_add v2, off, s[12:15], s35 sc0\n\t"
        "s_mov_b64 exec, -1\n\t"
        "s_waitcnt vmcnt(0)\n\t"
        "v_readfirstlane_b32 s34, v2\n\t"
        "s_nop 0\n\t"
        "s_cmp_lg_u32 s34, 95\n\t"
        "s_cbranch_scc1 LB_st%=\n\t"
        "s_memrealtime s[32:33]\n\t"
        "v_mbcnt_lo_u32_b32 v3, -1, 0\n\t"
        "v_lshlrev_b32_e32 v3, 7, v3\n\t"
        "s_waitcnt lgkmcnt(0)\n\t"
        "s_add_u32 s34, s32, 50\n\t"
        "s_or_b32 s34, s34, 1\n\t"
        "v_mov_b32_e32 v2, s34\n\t"
        "s_mov_b32 s35, 36864\n\t"
        "s_mov_b64 exec, 0xffff\n\t"
        "buffer_store_dword v2, v3, s[12:15], s35 offen sc1\n\t"
        "s_mov_b64 exec, -1\n\t"
        "LB_st%=:\n\t"
        "s_mov_b32 s24, 0\n\t"
        "s_mov_b32 s26, 0\n\t"
        "s_mov_b32 s27, 0x80000\n\t"
        "s_mov_b32 s28, 0x180000\n\t"
        "s_mov_b32 s31, 0\n\t"
        "s_mov_b32 s47, 0\n\t"
        "s_mov_b32 s38, 0xff00ff00\n\t"
        "s_mov_b32 s39, 0xff00ff00\n\t"
        "s_mov_b32 s40, 0\n\t"
        "s_mov_b32 s41, -1\n\t"
        "v_mov_b64_e32 v[0:1], 0\n\t"
        "v_mov_b64_e32 v[2:3], 0\n\t"
        "v_mov_b64_e32 v[4:5], 0\n\t"
        "v_mov_b64_e32 v[6:7], 0\n\t"
        "v_mov_b64_e32 v[8:9], 0\n\t"
        "v_mov_b64_e32 v[10:11], 0\n\t"
        "v_mov_b64_e32 v[12:13], 0\n\t"
        "v_mov_b64_e32 v[14:15], 0\n\t"
        "s_mov_b32 s42, 0\n\t"
        "LB_gw%=:\n\t"
        "buffer_load_dword v16, off, s[12:15], s45 sc1\n\t"
        "s_waitcnt vmcnt(0)\n\t"
        "v_readfirstlane_b32 s25, v16\n\t"
        "s_nop 0\n\t"
        "s_cmp_lg_u32 s25, 0\n\t"
        "s_cbranch_scc1 LB_loop%=\n\t"
        "s_sleep 2\n\t"
        "s_add_u32 s42, s42, 1\n\t"
        "s_cmp_lt_u32 s42, 0x100000\n\t"
        "s_cbranch_scc1 LB_gw%=\n\t"
        "s_movk_i32 s34, 9\n\t"
        "s_branch LB_die%=\n\t"
        ".p2align 8\n\t"
        "LB_loop%=:\n\t"
        "s_mov_b32 s42, 0\n\t"
        "s_sub_u32 s35, s25, 8\n\t"
        "s_waitcnt lgkmcnt(0)\n\t"
        "s_memrealtime s[2:3]\n\t"
        "s_memrealtime s[4:5]\n\t"
        "s_memrealtime s[6:7]\n\t"
        "LB_wa%=:\n\t"
        "s_memrealtime s[32:33]\n\t"
        "s_waitcnt lgkmcnt(6)\n\t"
        "s_sub_u32 s34, s35, s2\n\t"
        "s_cmpk_lt_u32 s34, 0x2000\n\t"
        "s_cbranch_scc0 LB_wa%=x\n\t"
        "s_memrealtime s[2:3]\n\t"
        "s_waitcnt lgkmcnt(6)\n\t"
        "s_sub_u32 s34, s35, s4\n\t"
        "s_cmpk_lt_u32 s34, 0x2000\n\t"
        "s_cbranch_scc0 LB_wa%=x\n\t"
        "s_memrealtime s[4:5]\n\t"
        "s_waitcnt lgkmcnt(6)\n\t"
        "s_sub_u32 s34, s35, s6\n\t"
        "s_cmpk_lt_u32 s34, 0x2000\n\t"
        "s_cbranch_scc0 LB_wa%=x\n\t"
        "s_memrealtime s[6:7]\n\t"
        "s_waitcnt lgkmcnt(6)\n\t"
        "s_sub_u32 s34, s35, s32\n\t"
        "s_cmpk_lt_u32 s34, 0x2000\n\t"
        "s_cbranch_scc1 LB_wa%=\n\t"
        "s_nop 3\n\t"
        "LB_wa%=x:\n\t"
        "s_add_i32 s34, s34, 4\n\t"
        "s_max_i32 s34, s34, 0\n\t"
        "s_bitcmp1_b32 s34, 1\n\t"
        "s_cbranch_scc0 LB_wa%=y\n\t"
        "s_nop 15\n\t"
        "LB_wa%=y:\n\t"
        "s_bitcmp1_b32 s34, 0\n\t"
        "s_cbranch_scc0 LB_wa%=z\n\t"
        "s_nop 9\n\t"
        "LB_wa%=z:\n\t"
        "LB_rd%=:\n\t"
        "buffer_load_dwordx4 v[16:19], v112, s[8:11], s26 offen offset:0 sc1\n\t"
        "s_nop 9\n\t"
        "buffer_load_dwordx4 v[20:23], v112, s[8:11], s26 offen offset:1024 sc1\n\t"
        "s_nop 9\n\t"
        "buffer_load_dwordx4 v[24:27], v112, s[8:11], s26 offen offset:2048 sc1\n\t"
        "s_nop 9\n\t"
        "buffer_load_dwordx4 v[28:31], v112, s[8:11], s26 offen offset:3072 sc1\n\t"
        "LB_go%=:\n\t"
        "s_waitcnt vmcnt(3)\n\t"
        "v_smfmac_f32_16x16x64_bf16 v[0:3], v[16:19], v[128:135], v106\n\t"
        "v_max3_u32 v108, v16, v17, v18\n\t"
        "v_max_u32_e32 v108, v19, v108\n\t"
        "v_smfmac_f32_16x16x64_bf16 v[4:7], v[16:19], v[136:143], v106\n\t"
        "v_smfmac_f32_16x16x64_bf16 v[8:11], v[16:19], v[144:151], v106\n\t"
        "v_smfmac_f32_16x16x64_bf16 v[12:15], v[16:19], v[152:159], v106\n\t"
        "s_waitcnt vmcnt(2)\n\t"
        "v_smfmac_f32_16x16x64_bf16 v[0:3], v[20:23], v[160:167], v106\n\t"
        "v_max3_u32 v108, v20, v21, v108\n\t"
        "v_max3_u32 v108, v22, v23, v108\n\t"
        "v_smfmac_f32_16x16x64_bf16 v[4:7], v[20:23], v[168:175], v106\n\t"
        "v_smfmac_f32_16x16x64_bf16 v[8:11], v[20:23], v[176:183], v106\n\t"
        "v_smfmac_f32_16x16x64_bf16 v[12:15], v[20:23], v[184:191], v106\n\t"
        "s_waitcnt vmcnt(1)\n\t"
        "v_smfmac_f32_16x16x64_bf16 v[0:3], v[24:27], v[192:199], v106\n\t"
        "v_max3_u32 v108, v24, v25, v108\n\t"
        "v_max3_u32 v108, v26, v27, v108\n\t"
        "v_smfmac_f32_16x16x64_bf16 v[4:7], v[24:27], v[200:207], v106\n\t"
        "v_smfmac_f32_16x16x64_bf16 v[8:11], v[24:27], v[208:215], v106\n\t"
        "v_smfmac_f32_16x16x64_bf16 v[12:15], v[24:27], v[216:223], v106\n\t"
        "s_waitcnt vmcnt(0)\n\t"
        "v_smfmac_f32_16x16x64_bf16 v[0:3], v[28:31], v[224:231], v106\n\t"
        "v_max3_u32 v108, v28, v29, v108\n\t"
        "v_max3_u32 v108, v30, v31, v108\n\t"
        "v_smfmac_f32_16x16x64_bf16 v[4:7], v[28:31], v[232:239], v106\n\t"
        "v_cmp_eq_u32_e32 vcc, -1, v108\n\t"
        "v_smfmac_f32_16x16x64_bf16 v[8:11], v[28:31], v[240:247], v106\n\t"
        "v_smfmac_f32_16x16x64_bf16 v[12:15], v[28:31], v[248:255], v106\n\t"
        "s_cbranch_vccnz LB_rdfail%=\n\t"
        "s_nop 0\n\t"
        "v_add_f32_e32 v0, v0, v1\n\t"
        "v_add_f32_e32 v1, v2, v3\n\t"
        "ds_write_b64 v118, v[0:1] offset:0\n\t"
        "v_add_f32_e32 v4, v4, v5\n\t"
        "v_add_f32_e32 v5, v6, v7\n\t"
        "ds_write_b64 v118, v[4:5] offset:512\n\t"
        "v_add_f32_e32 v8, v8, v9\n\t"
        "v_add_f32_e32 v9, v10, v11\n\t"
        "ds_write_b64 v118, v[8:9] offset:1024\n\t"
        "v_add_f32_e32 v12, v12, v13\n\t"
        "v_add_f32_e32 v13, v14, v15\n\t"
        "ds_write_b64 v118, v[12:13] offset:1536\n\t"
        "buffer_store_dwordx2 v[110:111], v117, s[8:11], s26 offen sc1\n\t"
        "s_waitcnt lgkmcnt(0)\n\t"
        "s_barrier\n\t"
        "ds_read_b128 v[16:19], v119 offset:0\n\t"
        "ds_read_b128 v[20:23], v119 offset:2048\n\t"
        "ds_read_b128 v[24:27], v119 offset:4096\n\t"
        "ds_read_b128 v[28:31], v119 offset:6144\n\t"
        "buffer_load_dwordx4 v[64:67], v124, s[8:11], s28 offen sc1\n\t"
        "s_waitcnt lgkmcnt(2)\n\t"
        "v_pk_add_f32 v[16:17], v[16:17], v[20:21]\n\t"
        "v_pk_add_f32 v[18:19], v[18:19], v[22:23]\n\t"
        "s_waitcnt lgkmcnt(0)\n\t"
        "v_pk_add_f32 v[24:25], v[24:25], v[28:29]\n\t"
        "v_pk_add_f32 v[26:27], v[26:27], v[30:31]\n\t"
        "v_pk_add_f32 v[16:17], v[16:17], v[24:25]\n\t"
        "v_pk_add_f32 v[18:19], v[18:19], v[26:27]\n\t"
        "v_cvt_pk_bf16_f32 v20, v16, v17\n\t"
        "v_cvt_pk_bf16_f32 v21, v18, v19\n\t"
        "buffer_store_dwordx2 v[20:21], v114, s[8:11], s26 offen sc1\n\t"
        "s_cmp_eq_u32 s24, 63\n\t"
        "s_cbranch_scc1 LB_end%=\n\t"
        "s_memrealtime s[52:53]\n\t"
        "v_mov_b64_e32 v[24:25], -1\n\t"
        "v_mov_b64_e32 v[32:33], 0\n\t"
        "v_mov_b64_e32 v[34:35], 0\n\t"
        "v_mov_b64_e32 v[36:37], 0\n\t"
        "v_mov_b64_e32 v[38:39], 0\n\t"
        "v_mov_b64_e32 v[40:41], 0\n\t"
        "v_mov_b64_e32 v[42:43], 0\n\t"
        "v_mov_b64_e32 v[44:45], 0\n\t"
        "v_mov_b64_e32 v[46:47], 0\n\t"
        "v_mov_b64_e32 v[0:1], 0\n\t"
        "v_mov_b64_e32 v[2:3], 0\n\t"
        "v_mov_b64_e32 v[4:5], 0\n\t"
        "v_mov_b64_e32 v[6:7], 0\n\t"
        "v_mov_b64_e32 v[8:9], 0\n\t"
        "v_mov_b64_e32 v[10:11], 0\n\t"
        "v_mov_b64_e32 v[12:13], 0\n\t"
        "v_mov_b64_e32 v[14:15], 0\n\t"
        "s_add_u32 s46, s26, 4096\n\t"
        "s_sleep 1\n\t"
        "buffer_load_dwordx2 v[104:105], v113, s[8:11], s26 offen sc1\n\t"
        "buffer_store_dword v24, v116, s[8:11], s28 offen sc1\n\t"
        "s_xor_b32 s34, s26, 0x100000\n\t"
        "buffer_store_dwordx2 v[24:25], v114, s[8:11], s34 offen sc1\n\t"
        "s_add_u32 s66, s49, s43\n\t"
        "s_cmp_lg_u32 s66, 32\n\t"
        "s_cbranch_scc1 LB_norep%=\n\t"
        "s_waitcnt vmcnt(4)\n\t"
        "s_sub_u32 s66, s24, 1\n\t"
        "v_lshrrev_b32_e32 v68, 16, v64\n\t"
        "v_cmp_eq_u32_e32 vcc, s66, v68\n\t"
        "v_subrev_u32_e32 v64, s59, v64\n\t"
        "v_bfe_i32 v64, v64, 0, 16\n\t"
        "v_cndmask_b32_e32 v64, 0, v64, vcc\n\t"
        "v_lshrrev_b32_e32 v68, 16, v65\n\t"
        "v_cmp_eq_u32_e32 vcc, s66, v68\n\t"
        "v_subrev_u32_e32 v65, s59, v65\n\t"
        "v_bfe_i32 v65, v65, 0, 16\n\t"
        "v_cndmask_b32_e32 v65, 0, v65, vcc\n\t"
        "v_lshrrev_b32_e32 v68, 16, v66\n\t"
        "v_cmp_eq_u32_e32 vcc, s66, v68\n\t"
        "v_subrev_u32_e32 v66, s59, v66\n\t"
        "v_bfe_i32 v66, v66, 0, 16\n\t"
        "v_cndmask_b32_e32 v66, 0, v66, vcc\n\t"
        "v_lshrrev_b32_e32 v68, 16, v67\n\t"
        "v_cmp_eq_u32_e32 vcc, s66, v68\n\t"
        "v_subrev_u32_e32 v67, s59, v67\n\t"
        "v_bfe_i32 v67, v67, 0, 16\n\t"
        "v_cndmask_b32_e32 v67, 0, v67, vcc\n\t"
        "v_max3_i32 v64, v64, v65, v66\n\t"
        "v_max_i32_e32 v64, v64, v67\n\t"
        "s_nop 1\n\t"
        "v_max_i32_dpp v64, v64, v64 quad_perm:[1,0,3,2] row_mask:0xf bank_mask:0xf\n\t"
        "s_nop 1\n\t"
        "v_max_i32_dpp v64, v64, v64 quad_perm:[2,3,0,1] row_mask:0xf bank_mask:0xf\n\t"
        "s_nop 1\n\t"
        "v_max_i32_dpp v64, v64, v64 row_ror:4 row_mask:0xf bank_mask:0xf\n\t"
        "s_nop 1\n\t"
        "v_max_i32_dpp v64, v64, v64 row_ror:8 row_mask:0xf bank_mask:0xf\n\t"
        "v_mov_b32_e32 v65, v64\n\t"
        "s_nop 1\n\t"
        "v_permlane16_swap_b32_e32 v64, v65\n\t"
        "v_max_i32_e32 v64, v64, v65\n\t"
        "v_mov_b32_e32 v65, v64\n\t"
        "s_nop 1\n\t"
        "v_permlane32_swap_b32_e32 v64, v65\n\t"
        "v_max_i32_e32 v64, v64, v65\n\t"
        "s_nop 0\n\t"
        "v_readfirstlane_b32 s64, v64\n\t"
        "LB_norep%=:\n\t"
        "s_waitcnt vmcnt(2) lgkmcnt(0)\n\t"
        "s_mov_b32 s61, s29\n\t"
        "s_mov_b32 s62, s30\n\t"
        "s_cmp_eq_u32 s24, 1\n\t"
        "s_cselect_b32 s63, 1, s31\n\t"
        "v_and_b32_e32 v30, 0xff, v105\n\t"
        "v_cmp_ne_u32_e32 vcc, s24, v30\n\t"
        "s_nop 0\n\t"
        "s_cbranch_vccnz LB_stre%=\n\t"
        "LB_stok%=:\n\t"
        "v_subrev_u32_e32 v104, s25, v104\n\t"
        "s_nop 1\n\t"
        "v_max_i32_dpp v104, v104, v104 quad_perm:[1,0,3,2] row_mask:0xf bank_mask:0xf\n\t"
        "s_nop 1\n\t"
        "v_max_i32_dpp v104, v104, v104 quad_perm:[2,3,0,1] row_mask:0xf bank_mask:0xf\n\t"
        "s_nop 1\n\t"
        "v_max_i32_dpp v104, v104, v104 row_ror:4 row_mask:0xf bank_mask:0xf\n\t"
        "s_nop 1\n\t"
        "v_max_i32_dpp v104, v104, v104 row_ror:8 row_mask:0xf bank_mask:0xf\n\t"
        "s_nop 1\n\t"
        "v_max_i32_dpp v104, v104, v104 row_bcast:15 row_mask:0xa bank_mask:0xf\n\t"
        "s_nop 1\n\t"
        "v_max_i32_dpp v104, v104, v104 row_bcast:31 row_mask:0xc bank_mask:0xf\n\t"
        "v_readfirstlane_b32 s35, v105\n\t"
        "v_readlane_b32 s36, v104, 63\n\t"
        "s_bfe_u32 s29, s35, 0xc0008\n\t"
        "s_lshr_b32 s30, s35, 20\n\t"
        "s_add_u32 s35, s31, s50\n\t"
        "s_mul_i32 s37, s50, 3\n\t"
        "s_min_u32 s35, s35, s37\n\t"
        "s_add_i32 s34, s36, s51\n\t"
        "s_cmp_gt_i32 s34, 0\n\t"
        "s_cselect_b32 s31, s35, 0\n\t"
        "s_cselect_b32 s34, s34, 0\n\t"
        "s_add_u32 s34, s34, s31\n\t"
        "s_add_u32 s37, s25, s34\n\t"
        "s_add_u32 s45, s37, s29\n\t"
        "s_add_u32 s25, s45, s30\n\t"
        "s_add_u32 s66, s49, s43\n\t"
        "s_cmp_lg_u32 s66, 32\n\t"
        "s_cbranch_scc1 LB_noctl%=\n\t"
        "s_cmp_eq_u32 s24, 0\n\t"
        "s_cbranch_scc1 LB_ctl0%=\n\t"
        "s_cmp_lt_u32 s24, 6\n\t"
        "s_cselect_b32 s68, 32, 8\n\t"
        "s_add_i32 s65, s36, s62\n\t"
        "s_cmp_eq_u32 s24, 1\n\t"
        "s_cselect_b32 s34, 6, 0\n\t"
        "s_cselect_b32 s35, 3, 0\n\t"
        "s_sub_u32 s64, s64, s34\n\t"
        "s_sub_u32 s65, s65, s35\n\t"
        "s_lshl_b32 s66, s65, 1\n\t"
        "s_sub_i32 s66, s66, s56\n\t"
        "s_cmp_gt_i32 s66, 56\n\t"
        "s_cbranch_scc1 LB_c1%=\n\t"
        "s_lshl_b32 s66, s65, 1\n\t"
        "s_add_u32 s34, s56, s68\n\t"
        "s_min_i32 s34, s34, s66\n\t"
        "s_sub_u32 s35, s56, s66\n\t"
        "s_ashr_i32 s35, s35, 1\n\t"
        "s_max_i32 s35, s35, 1\n\t"
        "s_sub_u32 s35, s56, s35\n\t"
        "s_max_i32 s56, s34, s35\n\t"
        "s_branch LB_c2%=\n\t"
        "LB_c1%=:\n\t"
        "s_add_u32 s56, s56, 2\n\t"
        "s_add_i32 s66, s64, s57\n\t"
        "s_cmp_gt_i32 s66, s61\n\t"
        "s_cbranch_scc1 LB_c2%=\n\t"
        "s_add_u32 s57, s57, 4\n\t"
        "s_min_u32 s57, s57, 24\n\t"
        "LB_c2%=:\n\t"
        "s_cmp_lg_u32 s63, 0\n\t"
        "s_cbranch_scc1 LB_c4%=\n\t"
        "s_lshl_b32 s66, s64, 1\n\t"
        "s_sub_i32 s66, s66, s55\n\t"
        "s_cmp_gt_i32 s66, 56\n\t"
        "s_cbranch_scc1 LB_c3%=\n\t"
        "s_lshl_b32 s66, s64, 1\n\t"
        "s_add_u32 s34, s55, s68\n\t"
        "s_min_i32 s34, s34, s66\n\t"
        "s_sub_u32 s35, s55, s66\n\t"
        "s_ashr_i32 s35, s35, 1\n\t"
        "s_max_i32 s35, s35, 1\n\t"
        "s_sub_u32 s35, s55, s35\n\t"
        "s_max_i32 s55, s34, s35\n\t"
        "s_branch LB_c4%=\n\t"
        "LB_c3%=:\n\t"
        "s_add_u32 s55, s55, 2\n\t"
        "s_cmp_lt_i32 s60, s58\n\t"
        "s_cbranch_scc1 LB_c4%=\n\t"
        "s_add_u32 s66, s58, 8\n\t"
        "s_cmp_gt_i32 s60, s66\n\t"
        "s_cbranch_scc1 LB_c4%=\n\t"
        "s_add_u32 s58, s58, 4\n\t"
        "s_min_u32 s58, s58, 22\n\t"
        "LB_c4%=:\n\t"
        "s_and_b32 s66, s24, 3\n\t"
        "s_cmp_eq_u32 s66, 0\n\t"
        "s_cselect_b32 s66, 1, 0\n\t"
        "s_cmp_lt_u32 s24, 30\n\t"
        "s_cselect_b32 s34, 8, 8\n\t"
        "s_cselect_b32 s35, 6, 6\n\t"
        "s_sub_u32 s57, s57, s66\n\t"
        "s_max_i32 s57, s57, s34\n\t"
        "s_sub_u32 s58, s58, s66\n\t"
        "s_max_i32 s58, s58, s35\n\t"
        "s_add_u32 s34, s55, 1\n\t"
        "s_lshr_b32 s34, s34, 1\n\t"
        "s_add_u32 s34, s34, s57\n\t"
        "s_min_u32 s34, s34, 1000\n\t"
        "s_lshl_b32 s34, s34, 8\n\t"
        "s_add_u32 s35, s56, 1\n\t"
        "s_lshr_b32 s35, s35, 1\n\t"
        "s_add_u32 s35, s35, s58\n\t"
        "s_min_u32 s35, s35, 1000\n\t"
        "s_lshl_b32 s35, s35, 20\n\t"
        "s_or_b32 s54, s34, s35\n\t"
        "s_branch LB_ctl1%=\n\t"
        "LB_ctl0%=:\n\t"
        "s_movk_i32 s57, 8\n\t"
        "s_movk_i32 s58, 6\n\t"
        "s_sub_u32 s55, s29, 53\n\t"
        "s_lshl_b32 s55, s55, 1\n\t"
        "s_sub_u32 s56, s30, 14\n\t"
        "s_lshl_b32 s56, s56, 1\n\t"
        "LB_ctl1%=:\n\t"
        "s_mov_b32 s59, s37\n\t"
        "s_sub_u32 s60, 0, s36\n\t"
        "LB_noctl%=:\n\t"
        "s_cmp_eq_u32 s24, 0\n\t"
        "s_cselect_b32 s34, 2561, 1\n\t"
        "s_add_u32 s34, s34, s24\n\t"
        "s_add_u32 s34, s34, s54\n\t"
        "v_mov_b32_e32 v111, s34\n\t"
        "s_mov_b32 s42, 0\n\t"
        "s_sub_u32 s35, s45, 8\n\t"
        "s_waitcnt lgkmcnt(0)\n\t"
        "s_memrealtime s[2:3]\n\t"
        "s_memrealtime s[4:5]\n\t"
        "s_memrealtime s[6:7]\n\t"
        "LB_wo%=:\n\t"
        "s_memrealtime s[32:33]\n\t"
        "s_waitcnt lgkmcnt(6)\n\t"
        "s_sub_u32 s34, s35, s2\n\t"
        "s_cmpk_lt_u32 s34, 0x2000\n\t"
        "s_cbranch_scc0 LB_wo%=x\n\t"
        "s_memrealtime s[2:3]\n\t"
        "s_waitcnt lgkmcnt(6)\n\t"
        "s_sub_u32 s34, s35, s4\n\t"
        "s_cmpk_lt_u32 s34, 0x2000\n\t"
        "s_cbranch_scc0 LB_wo%=x\n\t"
        "s_memrealtime s[4:5]\n\t"
        "s_waitcnt lgkmcnt(6)\n\t"
        "s_sub_u32 s34, s35, s6\n\t"
        "s_cmpk_lt_u32 s34, 0x2000\n\t"
        "s_cbranch_scc0 LB_wo%=x\n\t"
        "s_memrealtime s[6:7]\n\t"
        "s_waitcnt lgkmcnt(6)\n\t"
        "s_sub_u32 s34, s35, s32\n\t"
        "s_cmpk_lt_u32 s34, 0x2000\n\t"
        "s_cbranch_scc1 LB_wo%=\n\t"
        "s_nop 3\n\t"
        "LB_wo%=x:\n\t"
        "s_add_i32 s34, s34, 4\n\t"
        "s_max_i32 s34, s34, 0\n\t"
        "s_bitcmp1_b32 s34, 1\n\t"
        "s_cbranch_scc0 LB_wo%=y\n\t"
        "s_nop 15\n\t"
        "LB_wo%=y:\n\t"
        "s_bitcmp1_b32 s34, 0\n\t"
        "s_cbranch_scc0 LB_wo%=z\n\t"
        "s_nop 9\n\t"
        "LB_wo%=z:\n\t"
        "LB_ro%=:\n\t"
        "buffer_load_dwordx4 v[64:67], v115, s[8:11], s26 offen offset:0 sc1\n\t"
        "s_nop 12\n\t"
        "buffer_load_dwordx4 v[68:71], v115, s[8:11], s26 offen offset:1024 sc1\n\t"
        "s_nop 12\n\t"
        "buffer_load_dwordx4 v[72:75], v115, s[8:11], s26 offen offset:2048 sc1\n\t"
        "s_nop 12\n\t"
        "buffer_load_dwordx4 v[76:79], v115, s[8:11], s26 offen offset:3072 sc1\n\t"
        "s_nop 12\n\t"
        "buffer_load_dwordx4 v[80:83], v115, s[8:11], s46 offen offset:0 sc1\n\t"
        "s_nop 12\n\t"
        "buffer_load_dwordx4 v[84:87], v115, s[8:11], s46 offen offset:1024 sc1\n\t"
        "s_nop 12\n\t"
        "buffer_load_dwordx4 v[88:91], v115, s[8:11], s46 offen offset:2048 sc1\n\t"
        "s_nop 12\n\t"
        "buffer_load_dwordx4 v[92:95], v115, s[8:11], s46 offen offset:3072 sc1\n\t"
        "LB_go2%=:\n\t"
        "s_waitcnt vmcnt(7)\n\t"
        "v_smfmac_f32_16x16x64_bf16 v[32:35], v[64:67], a[0:7], v106\n\t"
        "v_max3_u32 v108, v64, v65, v66\n\t"
        "v_max_u32_e32 v108, v67, v108\n\t"
        "v_smfmac_f32_16x16x64_bf16 v[40:43], v[64:67], a[16:23], v106\n\t"
        "v_smfmac_f32_16x16x64_bf16 v[36:39], v[64:67], a[8:15], v106\n\t"
        "v_smfmac_f32_16x16x64_bf16 v[44:47], v[64:67], a[24:31], v106\n\t"
        "s_waitcnt vmcnt(6)\n\t"
        "v_smfmac_f32_16x16x64_bf16 v[32:35], v[68:71], a[32:39], v106\n\t"
        "v_max3_u32 v108, v68, v69, v108\n\t"
        "v_max3_u32 v108, v70, v71, v108\n\t"
        "v_smfmac_f32_16x16x64_bf16 v[40:43], v[68:71], a[48:55], v106\n\t"
        "v_smfmac_f32_16x16x64_bf16 v[36:39], v[68:71], a[40:47], v106\n\t"
        "v_smfmac_f32_16x16x64_bf16 v[44:47], v[68:71], a[56:63], v106\n\t"
        "s_waitcnt vmcnt(5)\n\t"
        "v_smfmac_f32_16x16x64_bf16 v[32:35], v[72:75], a[64:71], v106\n\t"
        "v_max3_u32 v108, v72, v73, v108\n\t"
        "v_max3_u32 v108, v74, v75, v108\n\t"
        "v_smfmac_f32_16x16x64_bf16 v[40:43], v[72:75], a[80:87], v106\n\t"
        "v_smfmac_f32_16x16x64_bf16 v[36:39], v[72:75], a[72:79], v106\n\t"
        "v_smfmac_f32_16x16x64_bf16 v[44:47], v[72:75], a[88:95], v106\n\t"
        "s_waitcnt vmcnt(4)\n\t"
        "v_smfmac_f32_16x16x64_bf16 v[32:35], v[76:79], a[96:103], v106\n\t"
        "v_max3_u32 v108, v76, v77, v108\n\t"
        "v_max3_u32 v108, v78, v79, v108\n\t"
        "v_smfmac_f32_16x16x64_bf16 v[40:43], v[76:79], a[112:119], v106\n\t"
        "v_smfmac_f32_16x16x64_bf16 v[36:39], v[76:79], a[104:111], v106\n\t"
        "v_smfmac_f32_16x16x64_bf16 v[44:47], v[76:79], a[120:127], v106\n\t"
        "s_waitcnt vmcnt(3)\n\t"
        "v_smfmac_f32_16x16x64_bf16 v[32:35], v[80:83], a[128:135], v106\n\t"
        "v_max3_u32 v108, v80, v81, v108\n\t"
        "v_max3_u32 v108, v82, v83, v108\n\t"
        "v_smfmac_f32_16x16x64_bf16 v[40:43], v[80:83], a[144:151], v106\n\t"
        "v_smfmac_f32_16x16x64_bf16 v[36:39], v[80:83], a[136:143], v106\n\t"
        "v_smfmac_f32_16x16x64_bf16 v[44:47], v[80:83], a[152:159], v106\n\t"
        "s_waitcnt vmcnt(2)\n\t"
        "v_smfmac_f32_16x16x64_bf16 v[32:35], v[84:87], a[160:167], v106\n\t"
        "v_max3_u32 v108, v84, v85, v108\n\t"
        "v_max3_u32 v108, v86, v87, v108\n\t"
        "v_smfmac_f32_16x16x64_bf16 v[40:43], v[84:87], a[176:183], v106\n\t"
        "v_smfmac_f32_16x16x64_bf16 v[36:39], v[84:87], a[168:175], v106\n\t"
        "v_smfmac_f32_16x16x64_bf16 v[44:47], v[84:87], a[184:191], v106\n\t"
        "s_waitcnt vmcnt(1)\n\t"
        "v_smfmac_f32_16x16x64_bf16 v[32:35], v[88:91], a[192:199], v106\n\t"
        "v_max3_u32 v108, v88, v89, v108\n\t"
        "v_max3_u32 v108, v90, v91, v108\n\t"
        "v_smfmac_f32_16x16x64_bf16 v[40:43], v[88:91], a[208:215], v106\n\t"
        "v_smfmac_f32_16x16x64_bf16 v[36:39], v[88:91], a[200:207], v106\n\t"
        "v_smfmac_f32_16x16x64_bf16 v[44:47], v[88:91], a[216:223], v106\n\t"
        "s_waitcnt vmcnt(0)\n\t"
        "v_smfmac_f32_16x16x64_bf16 v[32:35], v[92:95], a[224:231], v106\n\t"
        "v_max3_u32 v108, v92, v93, v108\n\t"
        "v_max3_u32 v108, v94, v95, v108\n\t"
        "v_smfmac_f32_16x16x64_bf16 v[40:43], v[92:95], a[240:247], v106\n\t"
        "v_cmp_eq_u32_e32 vcc, -1, v108\n\t"
        "v_smfmac_f32_16x16x64_bf16 v[36:39], v[92:95], a[232:239], v106\n\t"
        "v_smfmac_f32_16x16x64_bf16 v[44:47], v[92:95], a[248:255], v106\n\t"
        "s_cbranch_vccnz LB_rofail%=\n\t"
        "s_nop 0\n\t"
        "v_add_f32_e32 v32, v32, v33\n\t"
        "s_nop 1\n\t"
        "v_add_f32_e32 v40, v42, v43\n\t"
        "v_add_f32_e32 v32, v32, v40\n\t"
        "v_add_f32_e32 v36, v36, v37\n\t"
        "v_add_f32_e32 v44, v46, v47\n\t"
        "v_add_f32_e32 v36, v36, v44\n\t"
        "ds_write2_b32 v120, v32, v36 offset0:0 offset1:64\n\t"
        "s_waitcnt lgkmcnt(0)\n\t"
        "s_barrier\n\t"
        "ds_read_b64 v[64:65], v121 offset:0\n\t"
        "ds_read_b64 v[66:67], v121 offset:512\n\t"
        "ds_read_b64 v[68:69], v121 offset:1024\n\t"
        "ds_read_b64 v[70:71], v121 offset:1536\n\t"
        "s_waitcnt lgkmcnt(2)\n\t"
        "v_pk_add_f32 v[64:65], v[64:65], v[66:67]\n\t"
        "s_waitcnt lgkmcnt(0)\n\t"
        "v_pk_add_f32 v[68:69], v[68:69], v[70:71]\n\t"
        "v_pk_add_f32 v[64:65], v[64:65], v[68:69]\n\t"
        "v_cvt_pk_bf16_f32 v64, v64, v65\n\t"
        "buffer_store_dword v64, v116, s[8:11], s27 offen sc1\n\t"
        "s_memrealtime s[32:33]\n\t"
        "s_waitcnt lgkmcnt(0)\n\t"
        "v_mov_b32_e32 v110, s32\n\t"
        "s_add_u32 s24, s24, 1\n\t"
        "s_mov_b32 s28, s26\n\t"
        "s_mov_b32 s26, s27\n\t"
        "s_add_u32 s27, s27, 0x80000\n\t"
        "s_and_b32 s27, s27, 0x1fffff\n\t"
        "s_branch LB_loop%=\n\t"
        "LB_end%=:\n\t"
        "s_endpgm\n\t"
        "LB_stre%=:\n\t"
        "s_mov_b32 s42, 0\n\t"
        "LB_str2%=:\n\t"
        "buffer_load_dwordx2 v[104:105], v113, s[8:11], s26 offen sc1\n\t"
        "s_waitcnt vmcnt(0)\n\t"
        "v_and_b32_e32 v30, 0xff, v105\n\t"
        "v_cmp_ne_u32_e32 vcc, s24, v30\n\t"
        "s_nop 0\n\t"
        "s_cbranch_vccz LB_stok%=\n\t"
        "s_add_u32 s42, s42, 1\n\t"
        "s_cmp_lt_u32 s42, 0x100000\n\t"
        "s_cbranch_scc1 LB_str2%=\n\t"
        "s_movk_i32 s34, 5\n\t"
        "s_branch LB_die%=\n\t"
        "LB_rdfail%=:\n\t"
        "s_or_b32 s47, s47, 1\n\t"
        "buffer_load_dwordx4 v[16:19], v112, s[8:11], s26 offen offset:0 sc1\n\t"
        "buffer_load_dwordx4 v[20:23], v112, s[8:11], s26 offen offset:1024 sc1\n\t"
        "buffer_load_dwordx4 v[24:27], v112, s[8:11], s26 offen offset:2048 sc1\n\t"
        "buffer_load_dwordx4 v[28:31], v112, s[8:11], s26 offen offset:3072 sc1\n\t"
        "s_nop 7\n\t"
        "v_mov_b64_e32 v[0:1], 0\n\t"
        "v_mov_b64_e32 v[2:3], 0\n\t"
        "v_mov_b64_e32 v[4:5], 0\n\t"
        "v_mov_b64_e32 v[6:7], 0\n\t"
        "v_mov_b64_e32 v[8:9], 0\n\t"
        "v_mov_b64_e32 v[10:11], 0\n\t"
        "v_mov_b64_e32 v[12:13], 0\n\t"
        "v_mov_b64_e32 v[14:15], 0\n\t"
        "s_branch LB_rdre%=w\n\t"
        "LB_rdre%=:\n\t"
        "buffer_load_dwordx4 v[16:19], v112, s[8:11], s26 offen offset:0 sc1\n\t"
        "buffer_load_dwordx4 v[20:23], v112, s[8:11], s26 offen offset:1024 sc1\n\t"
        "buffer_load_dwordx4 v[24:27], v112, s[8:11], s26 offen offset:2048 sc1\n\t"
        "buffer_load_dwordx4 v[28:31], v112, s[8:11], s26 offen offset:3072 sc1\n\t"
        "LB_rdre%=w:\n\t"
        "s_waitcnt vmcnt(0) lgkmcnt(0)\n\t"
        "v_max3_u32 v108, v16, v17, v18\n\t"
        "v_max3_u32 v108, v108, v19, v20\n\t"
        "v_max3_u32 v108, v108, v21, v22\n\t"
        "v_max3_u32 v108, v108, v23, v24\n\t"
        "v_max3_u32 v108, v108, v25, v26\n\t"
        "v_max3_u32 v108, v108, v27, v28\n\t"
        "v_max3_u32 v108, v108, v29, v30\n\t"
        "v_max_u32_e32 v108, v108, v31\n\t"
        "v_cmp_eq_u32_e32 vcc, -1, v108\n\t"
        "s_nop 0\n\t"
        "s_cbranch_vccz LB_go%=\n\t"
        "s_add_u32 s42, s42, 1\n\t"
        "s_cmp_lt_u32 s42, 0x100000\n\t"
        "s_cbranch_scc1 LB_rdre%=\n\t"
        "s_movk_i32 s34, 3\n\t"
        "s_branch LB_die%=\n\t"
        "LB_rofail%=:\n\t"
        "s_or_b32 s47, s47, 4\n\t"
        "buffer_load_dwordx4 v[64:67], v115, s[8:11], s26 offen offset:0 sc1\n\t"
        "buffer_load_dwordx4 v[68:71], v115, s[8:11], s26 offen offset:1024 sc1\n\t"
        "buffer_load_dwordx4 v[72:75], v115, s[8:11], s26 offen offset:2048 sc1\n\t"
        "buffer_load_dwordx4 v[76:79], v115, s[8:11], s26 offen offset:3072 sc1\n\t"
        "buffer_load_dwordx4 v[80:83], v115, s[8:11], s46 offen offset:0 sc1\n\t"
        "buffer_load_dwordx4 v[84:87], v115, s[8:11], s46 offen offset:1024 sc1\n\t"
        "buffer_load_dwordx4 v[88:91], v115, s[8:11], s46 offen offset:2048 sc1\n\t"
        "buffer_load_dwordx4 v[92:95], v115, s[8:11], s46 offen offset:3072 sc1\n\t"
        "s_nop 7\n\t"
        "v_mov_b64_e32 v[32:33], 0\n\t"
        "v_mov_b64_e32 v[34:35], 0\n\t"
        "v_mov_b64_e32 v[36:37], 0\n\t"
        "v_mov_b64_e32 v[38:39], 0\n\t"
        "v_mov_b64_e32 v[40:41], 0\n\t"
        "v_mov_b64_e32 v[42:43], 0\n\t"
        "v_mov_b64_e32 v[44:45], 0\n\t"
        "v_mov_b64_e32 v[46:47], 0\n\t"
        "s_branch LB_rore%=w\n\t"
        "LB_rore%=:\n\t"
        "buffer_load_dwordx4 v[64:67], v115, s[8:11], s26 offen offset:0 sc1\n\t"
        "buffer_load_dwordx4 v[68:71], v115, s[8:11], s26 offen offset:1024 sc1\n\t"
        "buffer_load_dwordx4 v[72:75], v115, s[8:11], s26 offen offset:2048 sc1\n\t"
        "buffer_load_dwordx4 v[76:79], v115, s[8:11], s26 offen offset:3072 sc1\n\t"
        "buffer_load_dwordx4 v[80:83], v115, s[8:11], s46 offen offset:0 sc1\n\t"
        "buffer_load_dwordx4 v[84:87], v115, s[8:11], s46 offen offset:1024 sc1\n\t"
        "buffer_load_dwordx4 v[88:91], v115, s[8:11], s46 offen offset:2048 sc1\n\t"
        "buffer_load_dwordx4 v[92:95], v115, s[8:11], s46 offen offset:3072 sc1\n\t"
        "LB_rore%=w:\n\t"
        "s_waitcnt vmcnt(0) lgkmcnt(0)\n\t"
        "v_max3_u32 v108, v64, v65, v66\n\t"
        "v_max3_u32 v108, v108, v67, v68\n\t"
        "v_max3_u32 v108, v108, v69, v70\n\t"
        "v_max3_u32 v108, v108, v71, v72\n\t"
        "v_max3_u32 v108, v108, v73, v74\n\t"
        "v_max3_u32 v108, v108, v75, v76\n\t"
        "v_max3_u32 v108, v108, v77, v78\n\t"
        "v_max3_u32 v108, v108, v79, v80\n\t"
        "v_max3_u32 v108, v108, v81, v82\n\t"
        "v_max3_u32 v108, v108, v83, v84\n\t"
        "v_max3_u32 v108, v108, v85, v86\n\t"
        "v_max3_u32 v108, v108, v87, v88\n\t"
        "v_max3_u32 v108, v108, v89, v90\n\t"
        "v_max3_u32 v108, v108, v91, v92\n\t"
        "v_max3_u32 v108, v108, v93, v94\n\t"
        "v_max_u32_e32 v108, v108, v95\n\t"
        "v_cmp_eq_u32_e32 vcc, -1, v108\n\t"
        "s_nop 0\n\t"
        "s_cbranch_vccz LB_go2%=\n\t"
        "s_add_u32 s42, s42, 1\n\t"
        "s_cmp_lt_u32 s42, 0x100000\n\t"
        "s_cbranch_scc1 LB_rore%=\n\t"
        "s_movk_i32 s34, 4\n\t"
        "s_branch LB_die%=\n\t"
        "LB_die%=:\n\t"
        "v_mov_b32_e32 v30, s34\n\t"
        "s_mov_b32 s35, 33024\n\t"
        "s_mov_b64 exec, 1\n\t"
        "buffer_store_dword v30, off, s[12:15], s35 sc1\n\t"
        "s_endpgm\n\t"
        ".Lk_end_B:\n\t"
        :: REC_S_IN, "{v96}"(g_v), "{v97}"(g_o), "{v98}"(wb0), "{v99}"(wb1), "{v100}"(rb0), "{v101}"(rb0 ^ 32u), "{v102}"(rb0 ^ 64u), "{v103}"(rb0 ^ 96u),
           "{v106}"((l & 1) ? 0xEEEEEEEEu : 0x44444444u), "{v[108:109]}"(m0p),
           "{v112}"(a_mem), "{v113}"((24 + cp) * 16384 + l * 32), "{v114}"(a_vmw), "{v115}"(a_or), "{v116}"(a_mw), "{v117}"(a_sw),
           "{v118}"(lb + w * 2048 + (g >> 1) * 256 + n * 16 + (g & 1) * 8), "{v119}"(lb + (w & 1) * 1024 + l * 16), "{v120}"(lb + LB_R + w * 512 + l * 4),
           "{v121}"(lb + LB_R + l * 8), "{v122}"(stg + l * 16), "{v123}"(o_r), "{v124}"(rb + w == 32 ? 28 * 16384 + (l & 31) * 128 : OOB)
        : "memory", "vcc", "scc", "v0", "v255", "a0", "a255", "s101");
    __builtin_unreachable();
}
__device__ __forceinline__ void rec_helper(const Params& p, rsrc_t r, unsigned lb, const int wv, const int hi) {
    const int tid = otid(), l = tid & 63, xq = hi >> 3;
    const rsrc_t rh = __builtin_amdgcn_make_buffer_rsrc(p.ws + ho_base(p.ws), 0, HO_BYTES, 0x00020000);
    const v4u ones = {0xFFFFFFFFu, 0xFFFFFFFFu, 0xFFFFFFFFu, 0xFFFFFFFFu};
    const bool meas = (hi & 7) < 2;
    if (meas) {
        const unsigned ca = l >= 8 ? OOB : (31 + wv * 32) * 16384 + hi * 256 + l * 16, tend = rtnow() + CAL_T;
        unsigned best0 = ~0u, best1 = ~0u;
#pragma unroll 1
        for (int i = 0; i < 64 && (i < 3 || (int)(rtnow() - tend) < 0); ++i) {
            const unsigned t0 = (unsigned)__builtin_amdgcn_s_memtime(); __builtin_amdgcn_s_waitcnt(0);
            st16(rh, ca, ones); DRAIN();
            const unsigned t1 = (unsigned)__builtin_amdgcn_s_memtime(); __builtin_amdgcn_s_waitcnt(0);
            st16(rh, ca + 8192, ones); DRAIN();
            const unsigned t2 = (unsigned)__builtin_amdgcn_s_memtime(); __builtin_amdgcn_s_waitcnt(0);
            best0 = min(best0, t1 - t0); best1 = min(best1, t2 - t1); }
        if (l < 2) __hip_atomic_fetch_max((unsigned*)(p.ws + F_CAL + 128) + l, 65535u - min(l ? best1 : best0, 65535u), __ATOMIC_RELAXED, __HIP_MEMORY_SCOPE_AGENT);
    }
    auto touch = [&](int z0, int z1) {
#pragma unroll 1
        for (int z = z0 + ((hi & 7) - 2) * 4 + wv; z < z1; z += 24) { const bf16_t* q;
            if (z < 48) q = ((z & 1) ? p.Wk : p.Wq) + (size_t)(((((z & 1) * 2 + 1) * 4 + 4 + (z >> 2)) & 15) * 64 + l) * 4096 + (xq * 8 + 1 + (z & 2) * 2) * 128 + 64;
            else if (z < 112) { const int y = z - 48; q = p.Wo + (size_t)(((y & 3) * 8 + (y >> 3)) * 128 + ((y >> 2) & 1) * 64 + l) * 1024 + (xq & 1) * 512 + 192; }
            else { const int s7 = (z - 112) % 7, y = (z - 112) / 7; q = p.Wo + (size_t)(((y & 3) * 8 + 8 - T2U + (y >> 3)) * 128 + ((y >> 2) & 1) * 64 + l) * 1024 + (xq & 1) * 512 + (s7 + (s7 >= 3)) * 64; }
            touch_l2(q, lb + 1024); } };
    if (!meas) touch(0, TPRE);
    st16(r, tid < 64 ? F_KVQ + hi * 1024 + tid * 16 : tid < 72 ? F_PROG + hi * 128 + (tid - 64) * 16 : tid < 80 ? F_CVY + hi * 128 + (tid - 72) * 16 : OOB, (v4u){0u, 0u, 0u, 0u});
#pragma unroll
    for (int i = 0; i < 2; ++i) st16(r, W_OX + 63 * 262144 + hi * 8192 + i * 4096 + tid * 16, ones);
#pragma unroll
    for (int i = 0; i < 16; ++i) st16(rh, hi * 65536 + i * 4096 + tid * 16, ones);
    DRAIN();
    __syncthreads();
    if (tid == 0 && __hip_atomic_fetch_add((unsigned*)(p.ws + F_CAL + 256), 1u, __ATOMIC_RELAXED, __HIP_MEMORY_SCOPE_AGENT) == 31u) {
        const v2u c = ld8(r, F_CAL + 128); st4(r, F_CAL, c.x < c.y ? 8193u : 1u); }
    if (!meas) touch(TPRE, 112 + 56 * T2U);
    DRAIN();
}
constexpr int CV_RP = 80;
constexpr int CV_U = 96 * CV_RP;
constexpr int CV_KP = 4416;
constexpr int CV_K0 = 8 * CV_U;
#define NAP() do { __builtin_amdgcn_sched_barrier(0); __builtin_amdgcn_s_sleep(XSLP); __builtin_amdgcn_sched_barrier(0); } while (0)
__device__ __forceinline__ void gate_begin(const Params& p, const int jb, const int wv, v4u (&xg)[4][2], v4u (&gw)[2]) {
    const int l = otid() & 63, t0 = (jb * 4 + wv) * 4;
#pragma unroll
    for (int q = 0; q < 2; ++q) gw[q] = *(const v4u*)(p.gate_w + q * 512 + l * 8);
#pragma unroll
    for (int i = 0; i < 4; ++i)
#pragma unroll
        for (int q = 0; q < 2; ++q) { xg[i][q] = *(const v4u*)(p.Aq + (size_t)(t0 + i) * D + q * 512 + l * 8); NAP(); }
}
__device__ __forceinline__ void gate_end(rsrc_t r, const int jb, const int wv, const v4u (&xg)[4][2], const v4u (&gw)[2]) {
    const int l = otid() & 63;
    float mine = 0.f;
#pragma unroll
    for (int i = 0; i < 4; ++i) { float acc = 0.f;
#pragma unroll
        for (int q = 0; q < 2; ++q)
#pragma unroll
            for (int e = 0; e < 4; ++e) { const unsigned pr = pack2(blo(xg[i][q][e]) * blo(gw[q][e]), bhi(xg[i][q][e]) * bhi(gw[q][e])); acc += blo(pr); acc += bhi(pr); }
        for (int o = 32; o > 0; o >>= 1) acc += __shfl_xor(acc, o);
        if ((l & 3) == i) mine = acc; }
    if (l < 32) { const float z = rbfh(mine); st4(r, W_GATE + (jb * 4 + wv) * 128 + l * 4, __float_as_uint(rbfh(1.f / (1.f + expf(-z))))); }
}
__device__ __forceinline__ void conv_prep(const Params& p, rsrc_t r, const int wv, const int hi, const int G, const int jb) {
    const int tid = otid(), Q = tid >> 2, cg = tid & 3, ch0 = hi * 32 + cg * 8;
    v4u sk[4]; float sv[8][4];
#pragma unroll
    for (int q = 0; q < 4; ++q) sk[q] = *(const v4u*)(p.shift_k + ch0 * 4 + q * 8);
#pragma unroll
    for (int c8 = 0; c8 < 8; ++c8)
#pragma unroll
        for (int tp = 0; tp < 4; ++tp) { const unsigned sw = sk[c8 >> 1][(c8 & 1) * 2 + (tp >> 1)]; sv[c8][tp] = (tp & 1) ? bhi(sw) : blo(sw); }
    const rsrc_t rk = __builtin_amdgcn_make_buffer_rsrc((void*)p.Ak, 0, S * D * 2, 0x00020000), rv = __builtin_amdgcn_make_buffer_rsrc((void*)p.Av, 0, S * D * 2, 0x00020000),
                 rq = __builtin_amdgcn_make_buffer_rsrc((void*)p.Aq, 0, S * D * 2, 0x00020000);
    {
        const unsigned ro = (unsigned)((Q * 32 + G * 8) * D + ch0) * 2u, uo = hi * 131072 + tid * 16;
        v4u kr[11], u[8], qv[8], xg[4][2], gw[2];
        gate_begin(p, jb, wv, xg, gw);
#pragma unroll
        for (int i = 0; i < 11; ++i) { kr[i] = __builtin_amdgcn_raw_buffer_load_b128(rk, (i < 3 && Q + G == 0) ? OOB : ro + (i - 3) * 2048, 0, 2); NAP(); }
#pragma unroll
        for (int i = 0; i < 8; ++i) { u[i] = __builtin_amdgcn_raw_buffer_load_b128(rv, ro + i * 2048, 0, 2); NAP(); }
#pragma unroll
        for (int i = 0; i < 8; ++i) { qv[i] = __builtin_amdgcn_raw_buffer_load_b128(rq, ro + i * 2048, 0, 0); NAP(); }
        gate_end(r, jb, wv, xg, gw);
        __builtin_amdgcn_sched_barrier(0);
#pragma unroll
        for (int q = 0; q < 4; ++q) {
#pragma unroll
            for (int i = 0; i < 8; ++i) {
                unsigned ks = pack2(blo(kr[i][q]) * sv[2 * q][3], bhi(kr[i][q]) * sv[2 * q + 1][3]), pr;
#pragma unroll
                for (int d = 1; d < 4; ++d) { pr = pack2(blo(kr[i + d][q]) * sv[2 * q][3 - d], bhi(kr[i + d][q]) * sv[2 * q + 1][3 - d]); ks = pack2(blo(ks) + blo(pr), bhi(ks) + bhi(pr)); }
                u[i][q] = pack2(blo(ks) * blo(u[i][q]), bhi(ks) * bhi(u[i][q])); }
            v4u lo, hh;
#pragma unroll
            for (int z = 0; z < 4; ++z) { const unsigned x = u[7 - 2 * z][q], y = u[6 - 2 * z][q]; lo[z] = __builtin_amdgcn_perm(y, x, 0x05040100u); hh[z] = __builtin_amdgcn_perm(y, x, 0x07060302u); }
            st16(r, W_U + uo + q * 32768 + G * 4096, lo); NAP(); st16(r, W_U + uo + q * 32768 + 16384 + G * 4096, hh); NAP();
#pragma unroll
            for (int z = 0; z < 2; ++z) { st16(r, W_Y + uo + q * 32768 + (2 * G + z) * 4096, (v4u){qv[4 * z][q], qv[4 * z + 1][q], qv[4 * z + 2][q], qv[4 * z + 3][q]}); NAP(); }
        }
    }
    DRAIN(); __syncthreads();
    if (tid == 0) __hip_atomic_fetch_add((unsigned*)(p.ws + F_CONV), 1u, __ATOMIC_RELAXED, __HIP_MEMORY_SCOPE_AGENT);
    if (tid == 0) __hip_atomic_fetch_add((unsigned*)(p.ws + F_CVY + hi * 128 + 64), 1u, __ATOMIC_RELAXED, __HIP_MEMORY_SCOPE_AGENT);
}
__device__ __forceinline__ bool conv_core(const Params& p, rsrc_t r, unsigned char* lds, const int wv, const int un, const int nt, const int ro) {
    const int tid = otid(), w = wv, l = tid & 63, Q = tid >> 2, cg = tid & 3;
    const rsrc_t rs = __builtin_amdgcn_make_buffer_rsrc((void*)p.ssm_k, 0, S * D * 2, 0x00020000);
    v4u kraw[4]; v2u kext[4];
    auto kload = [&](int n) { const unsigned o = (unsigned)(((un >> 2) * 32 + w * 8 + (un & 3) * 2 + n) * S + l * 8) * 2u;
#pragma unroll
        for (int i = 0; i < 4; ++i) { kraw[i] = __builtin_amdgcn_raw_buffer_load_b128(rs, o + i * 1024, 0, 2);
            kext[i] = __builtin_amdgcn_raw_buffer_load_b64(rs, (i == 3 && l == 63) ? OOB : o + i * 1024 + 16, 0, 2); } };
    kload(0);
    for (int i = tid; i < 8 * 160; i += 256) *(v4u*)(lds + (i / 160) * CV_U + (i % 160) * 16) = (v4u){0u, 0u, 0u, 0u};
    unsigned char* kc = lds + CV_K0 + w * 4 * CV_KP;
    if (l < 32) {
#pragma unroll
        for (int q = 0; q < 4; ++q) *(unsigned*)(kc + q * CV_KP + l * 4) = 0u; }
    if (!wait_word(r, F_CVY + (un >> 2) * 128 + 64, 4u, 2048u)) return false;
    v4u ur[8], qr[8];
#pragma unroll
    for (int i = 0; i < 8; ++i) ur[i] = ld16(r, W_U + un * 32768 + i * 4096 + tid * 16);
#pragma unroll
    for (int i = 0; i < 8; ++i) qr[i] = ld16(r, W_Y + un * 32768 + i * 4096 + tid * 16);
    const unsigned dv = *(const unsigned*)(p.ssm_D + (un >> 2) * 32 + cg * 8 + (un & 3) * 2);
    const int b = l & 31, kg = l >> 5;
    const unsigned kb = (unsigned)(size_t)kc + (b & 3) * CV_KP + ((b & 28) - 8 * kg + 56 - 16) * 2, ub0 = (unsigned)(size_t)lds + (1 + b) * CV_RP + kg * 16;
    unsigned char* const urow = lds + 2 * cg * CV_U + (32 + Q) * CV_RP;
#pragma unroll
    for (int i = 0; i < 8; ++i) *(v4u*)(urow + (i >> 2) * CV_U + (i & 3) * 16) = ur[i];
    __syncthreads();
    const v4u so = mk_srd(p.Wo, 1024 * 4096 * 2); const unsigned go = (w * 1024 + (l >> 3)) * 2048 + nt * 128 + (l & 7) * 16; unsigned t0, t1;
#pragma unroll
        for (int ci = 0; ci < 2; ++ci) {
            const int c8 = 2 * w + ci;
#pragma unroll
            for (int i = 0; i < 4; ++i) { const v4u e = kraw[i]; const v2u f = kext[i]; unsigned char* q = kc + 128 + (i * 64 + l) * 16;
                const unsigned s0 = __builtin_amdgcn_alignbit(e.y, e.x, 16), s1 = __builtin_amdgcn_alignbit(e.z, e.y, 16), s2 = __builtin_amdgcn_alignbit(e.w, e.z, 16),
                               s3 = __builtin_amdgcn_alignbit(f.x, e.w, 16), s4 = __builtin_amdgcn_alignbit(f.y, f.x, 16);
                *(v4u*)q = (v4u){s0, s1, s2, s3}; *(v4u*)(q + CV_KP) = (v4u){e.y, e.z, e.w, f.x}; *(v4u*)(q + 2 * CV_KP) = (v4u){s1, s2, s3, s4}; *(v4u*)(q + 3 * CV_KP) = (v4u){e.z, e.w, f.x, f.y};
                if (i == 0 && l == 0) {
                    *(v2u*)(q - 8) = (v2u){0u, e.x << 16}; *(v2u*)(q + CV_KP - 8) = (v2u){0u, e.x}; *(v2u*)(q + 2 * CV_KP - 8) = (v2u){e.x << 16, s0}; *(v2u*)(q + 3 * CV_KP - 8) = (v2u){e.x, e.y}; } }
            if (ci == 0) kload(1);
            __builtin_amdgcn_wave_barrier();
            f32x16 acc0, acc1;
            if (ci == 0) asm volatile(
                "ds_read_b64 v[192:193], %[kb] offset:32\n\t"
                "ds_read_b64 v[194:195], %[kb] offset:40\n\t"
                "ds_read_b128 v[196:199], %[ub] offset:5040\n\t"
                "ds_read_b128 v[200:203], %[ub] offset:2480\n\t"
                "ds_read_b64 v[204:205], %[kb] offset:2080\n\t"
                "ds_read_b64 v[206:207], %[kb] offset:2088\n\t"
                "ds_read_b64 v[208:209], %[kb] offset:0\n\t"
                "ds_read_b64 v[210:211], %[kb] offset:8\n\t"
                "ds_read_b128 v[212:215], %[ub] offset:5072\n\t"
                "ds_read_b128 v[216:219], %[ub] offset:2512\n\t"
                "ds_read_b64 v[220:221], %[kb] offset:2048\n\t"
                "ds_read_b64 v[222:223], %[kb] offset:2056\n\t"
                "s_waitcnt lgkmcnt(9)\n\t"
                "v_mfma_f32_32x32x16_bf16 %[a1], v[192:195], v[196:199], 0\n\t"
                "ds_read_b64 v[224:225], %[kb] offset:96\n\t"
                "ds_read_b64 v[226:227], %[kb] offset:104\n\t"
                "ds_read_b128 v[228:231], %[ub] offset:4960\n\t"
                "s_waitcnt lgkmcnt(11)\n\t"
                "v_mfma_f32_32x32x16_bf16 %[a0], v[192:195], v[200:203], 0\n\t"
                "ds_read_b128 v[232:235], %[ub] offset:2400\n\t"
                "s_add_u32 %[t1], %[ro], 0\n\t"
                "s_and_b32 %[t1], %[t1], 7\n\t"
                "s_lshl_b32 %[t1], %[t1], 18\n\t"
                "s_add_u32 %[t0], %[t1], 0\n\t"
                "buffer_load_dwordx4 a[0:3], %[go], %[so], %[t0] offen\n\t"
                "s_waitcnt lgkmcnt(10)\n\t"
                "v_mfma_f32_32x32x16_bf16 %[a1], v[204:207], v[200:203], %[a1]\n\t"
                "ds_read_b64 v[236:237], %[kb] offset:2144\n\t"
                "ds_read_b64 v[238:239], %[kb] offset:2152\n\t"
                "s_waitcnt lgkmcnt(9)\n\t"
                "v_mfma_f32_32x32x16_bf16 %[a1], v[208:211], v[212:215], %[a1]\n\t"
                "ds_read_b64 v[240:241], %[kb] offset:64\n\t"
                "ds_read_b64 v[242:243], %[kb] offset:72\n\t"
                "ds_read_b128 v[244:247], %[ub] offset:4992\n\t"
                "s_waitcnt lgkmcnt(11)\n\t"
                "v_mfma_f32_32x32x16_bf16 %[a0], v[208:211], v[216:219], %[a0]\n\t"
                "ds_read_b128 v[248:251], %[ub] offset:2432\n\t"
                "s_waitcnt lgkmcnt(10)\n\t"
                "v_mfma_f32_32x32x16_bf16 %[a1], v[220:223], v[216:219], %[a1]\n\t"
                "ds_read_b64 v[252:253], %[kb] offset:2112\n\t"
                "ds_read_b64 v[254:255], %[kb] offset:2120\n\t"
                "s_waitcnt lgkmcnt(9)\n\t"
                "v_mfma_f32_32x32x16_bf16 %[a1], v[224:227], v[228:231], %[a1]\n\t"
                "ds_read_b64 v[192:193], %[kb] offset:160\n\t"
                "ds_read_b64 v[194:195], %[kb] offset:168\n\t"
                "ds_read_b128 v[196:199], %[ub] offset:4880\n\t"
                "s_waitcnt lgkmcnt(11)\n\t"
                "v_mfma_f32_32x32x16_bf16 %[a0], v[224:227], v[232:235], %[a0]\n\t"
                "ds_read_b128 v[200:203], %[ub] offset:2320\n\t"
                "s_add_u32 %[t0], %[t1], 16384\n\t"
                "buffer_load_dwordx4 a[4:7], %[go], %[so], %[t0] offen\n\t"
                "s_waitcnt lgkmcnt(10)\n\t"
                "v_mfma_f32_32x32x16_bf16 %[a1], v[236:239], v[232:235], %[a1]\n\t"
                "ds_read_b64 v[204:205], %[kb] offset:2208\n\t"
                "ds_read_b64 v[206:207], %[kb] offset:2216\n\t"
                "s_waitcnt lgkmcnt(9)\n\t"
                "v_mfma_f32_32x32x16_bf16 %[a1], v[240:243], v[244:247], %[a1]\n\t"
                "ds_read_b64 v[208:209], %[kb] offset:128\n\t"
                "ds_read_b64 v[210:211], %[kb] offset:136\n\t"
                "ds_read_b128 v[212:215], %[ub] offset:4912\n\t"
                "s_waitcnt lgkmcnt(11)\n\t"
                "v_mfma_f32_32x32x16_bf16 %[a0], v[240:243], v[248:251], %[a0]\n\t"
                "ds_read_b128 v[216:219], %[ub] offset:2352\n\t"
                "s_waitcnt lgkmcnt(10)\n\t"
                "v_mfma_f32_32x32x16_bf16 %[a1], v[252:255], v[248:251], %[a1]\n\t"
                "ds_read_b64 v[220:221], %[kb] offset:2176\n\t"
                "ds_read_b64 v[222:223], %[kb] offset:2184\n\t"
                "s_waitcnt lgkmcnt(9)\n\t"
                "v_mfma_f32_32x32x16_bf16 %[a1], v[192:195], v[196:199], %[a1]\n\t"
                "ds_read_b64 v[224:225], %[kb] offset:224\n\t"
                "ds_read_b64 v[226:227], %[kb] offset:232\n\t"
                "ds_read_b128 v[228:231], %[ub] offset:4800\n\t"
                "s_waitcnt lgkmcnt(11)\n\t"
                "v_mfma_f32_32x32x16_bf16 %[a0], v[192:195], v[200:203], %[a0]\n\t"
                "ds_read_b128 v[232:235], %[ub] offset:2240\n\t"
                "s_add_u32 %[t0], %[t1], 32768\n\t"
                "buffer_load_dwordx4 a[8:11], %[go], %[so], %[t0] offen\n\t"
                "s_waitcnt lgkmcnt(10)\n\t"
                "v_mfma_f32_32x32x16_bf16 %[a1], v[204:207], v[200:203], %[a1]\n\t"
                "ds_read_b64 v[236:237], %[kb] offset:2272\n\t"
                "ds_read_b64 v[238:239], %[kb] offset:2280\n\t"
                "s_waitcnt lgkmcnt(9)\n\t"
                "v_mfma_f32_32x32x16_bf16 %[a1], v[208:211], v[212:215], %[a1]\n\t"
                "ds_read_b64 v[240:241], %[kb] offset:192\n\t"
                "ds_read_b64 v[242:243], %[kb] offset:200\n\t"
                "ds_read_b128 v[244:247], %[ub] offset:4832\n\t"
                "s_waitcnt lgkmcnt(11)\n\t"
                "v_mfma_f32_32x32x16_bf16 %[a0], v[208:211], v[216:219], %[a0]\n\t"
                "ds_read_b128 v[248:251], %[ub] offset:2272\n\t"
                "s_waitcnt lgkmcnt(10)\n\t"
                "v_mfma_f32_32x32x16_bf16 %[a1], v[220:223], v[216:219], %[a1]\n\t"
                "ds_read_b64 v[252:253], %[kb] offset:2240\n\t"
                "ds_read_b64 v[254:255], %[kb] offset:2248\n\t"
                "s_waitcnt lgkmcnt(9)\n\t"
                "v_mfma_f32_32x32x16_bf16 %[a1], v[224:227], v[228:231], %[a1]\n\t"
                "ds_read_b64 v[192:193], %[kb] offset:288\n\t"
                "ds_read_b64 v[194:195], %[kb] offset:296\n\t"
                "ds_read_b128 v[196:199], %[ub] offset:4720\n\t"
                "s_waitcnt lgkmcnt(11)\n\t"
                "v_mfma_f32_32x32x16_bf16 %[a0], v[224:227], v[232:235], %[a0]\n\t"
                "ds_read_b128 v[200:203], %[ub] offset:2160\n\t"
                "s_add_u32 %[t0], %[t1], 49152\n\t"
                "buffer_load_dwordx4 a[12:15], %[go], %[so], %[t0] offen\n\t"
                "s_waitcnt lgkmcnt(10)\n\t"
                "v_mfma_f32_32x32x16_bf16 %[a1], v[236:239], v[232:235], %[a1]\n\t"
                "ds_read_b64 v[204:205], %[kb] offset:2336\n\t"
                "ds_read_b64 v[206:207], %[kb] offset:2344\n\t"
                "s_waitcnt lgkmcnt(9)\n\t"
                "v_mfma_f32_32x32x16_bf16 %[a1], v[240:243], v[244:247], %[a1]\n\t"
                "ds_read_b64 v[208:209], %[kb] offset:256\n\t"
                "ds_read_b64 v[210:211], %[kb] offset:264\n\t"
                "ds_read_b128 v[212:215], %[ub] offset:4752\n\t"
                "s_waitcnt lgkmcnt(11)\n\t"
                "v_mfma_f32_32x32x16_bf16 %[a0], v[240:243], v[248:251], %[a0]\n\t"
                "ds_read_b128 v[216:219], %[ub] offset:2192\n\t"
                "s_add_u32 %[t0], %[t1], 65536\n\t"
                "buffer_load_dwordx4 a[16:19], %[go], %[so], %[t0] offen\n\t"
                "s_waitcnt lgkmcnt(10)\n\t"
                "v_mfma_f32_32x32x16_bf16 %[a1], v[252:255], v[248:251], %[a1]\n\t"
                "ds_read_b64 v[220:221], %[kb] offset:2304\n\t"
                "ds_read_b64 v[222:223], %[kb] offset:2312\n\t"
                "s_waitcnt lgkmcnt(9)\n\t"
                "v_mfma_f32_32x32x16_bf16 %[a1], v[192:195], v[196:199], %[a1]\n\t"
                "ds_read_b64 v[224:225], %[kb] offset:352\n\t"
                "ds_read_b64 v[226:227], %[kb] offset:360\n\t"
                "ds_read_b128 v[228:231], %[ub] offset:4640\n\t"
                "s_waitcnt lgkmcnt(11)\n\t"
                "v_mfma_f32_32x32x16_bf16 %[a0], v[192:195], v[200:203], %[a0]\n\t"
                "ds_read_b128 v[232:235], %[ub] offset:2080\n\t"
                "s_waitcnt lgkmcnt(10)\n\t"
                "v_mfma_f32_32x32x16_bf16 %[a1], v[204:207], v[200:203], %[a1]\n\t"
                "ds_read_b64 v[236:237], %[kb] offset:2400\n\t"
                "ds_read_b64 v[238:239], %[kb] offset:2408\n\t"
                "s_waitcnt lgkmcnt(9)\n\t"
                "v_mfma_f32_32x32x16_bf16 %[a1], v[208:211], v[212:215], %[a1]\n\t"
                "ds_read_b64 v[240:241], %[kb] offset:320\n\t"
                "ds_read_b64 v[242:243], %[kb] offset:328\n\t"
                "ds_read_b128 v[244:247], %[ub] offset:4672\n\t"
                "s_waitcnt lgkmcnt(11)\n\t"
                "v_mfma_f32_32x32x16_bf16 %[a0], v[208:211], v[216:219], %[a0]\n\t"
                "ds_read_b128 v[248:251], %[ub] offset:2112\n\t"
                "s_add_u32 %[t0], %[t1], 81920\n\t"
                "buffer_load_dwordx4 a[20:23], %[go], %[so], %[t0] offen\n\t"
                "s_waitcnt lgkmcnt(10)\n\t"
                "v_mfma_f32_32x32x16_bf16 %[a1], v[220:223], v[216:219], %[a1]\n\t"
                "ds_read_b64 v[252:253], %[kb] offset:2368\n\t"
                "ds_read_b64 v[254:255], %[kb] offset:2376\n\t"
                "s_waitcnt lgkmcnt(9)\n\t"
                "v_mfma_f32_32x32x16_bf16 %[a1], v[224:227], v[228:231], %[a1]\n\t"
                "ds_read_b64 v[192:193], %[kb] offset:416\n\t"
                "ds_read_b64 v[194:195], %[kb] offset:424\n\t"
                "ds_read_b128 v[196:199], %[ub] offset:4560\n\t"
                "s_waitcnt lgkmcnt(11)\n\t"
                "v_mfma_f32_32x32x16_bf16 %[a0], v[224:227], v[232:235], %[a0]\n\t"
                "ds_read_b128 v[200:203], %[ub] offset:2000\n\t"
                "s_waitcnt lgkmcnt(10)\n\t"
                "v_mfma_f32_32x32x16_bf16 %[a1], v[236:239], v[232:235], %[a1]\n\t"
                "ds_read_b64 v[204:205], %[kb] offset:2464\n\t"
                "ds_read_b64 v[206:207], %[kb] offset:2472\n\t"
                "s_waitcnt lgkmcnt(9)\n\t"
                "v_mfma_f32_32x32x16_bf16 %[a1], v[240:243], v[244:247], %[a1]\n\t"
                "ds_read_b64 v[208:209], %[kb] offset:384\n\t"
                "ds_read_b64 v[210:211], %[kb] offset:392\n\t"
                "ds_read_b128 v[212:215], %[ub] offset:4592\n\t"
                "s_waitcnt lgkmcnt(11)\n\t"
                "v_mfma_f32_32x32x16_bf16 %[a0], v[240:243], v[248:251], %[a0]\n\t"
                "ds_read_b128 v[216:219], %[ub] offset:2032\n\t"
                "s_add_u32 %[t0], %[t1], 98304\n\t"
                "buffer_load_dwordx4 a[24:27], %[go], %[so], %[t0] offen\n\t"
                "s_waitcnt lgkmcnt(10)\n\t"
                "v_mfma_f32_32x32x16_bf16 %[a1], v[252:255], v[248:251], %[a1]\n\t"
                "ds_read_b64 v[220:221], %[kb] offset:2432\n\t"
                "ds_read_b64 v[222:223], %[kb] offset:2440\n\t"
                "s_waitcnt lgkmcnt(9)\n\t"
                "v_mfma_f32_32x32x16_bf16 %[a1], v[192:195], v[196:199], %[a1]\n\t"
                "ds_read_b64 v[224:225], %[kb] offset:480\n\t"
                "ds_read_b64 v[226:227], %[kb] offset:488\n\t"
                "ds_read_b128 v[228:231], %[ub] offset:4480\n\t"
                "s_waitcnt lgkmcnt(11)\n\t"
                "v_mfma_f32_32x32x16_bf16 %[a0], v[192:195], v[200:203], %[a0]\n\t"
                "ds_read_b128 v[232:235], %[ub] offset:1920\n\t"
                "s_waitcnt lgkmcnt(10)\n\t"
                "v_mfma_f32_32x32x16_bf16 %[a1], v[204:207], v[200:203], %[a1]\n\t"
                "ds_read_b64 v[236:237], %[kb] offset:2528\n\t"
                "ds_read_b64 v[238:239], %[kb] offset:2536\n\t"
                "s_waitcnt lgkmcnt(9)\n\t"
                "v_mfma_f32_32x32x16_bf16 %[a1], v[208:211], v[212:215], %[a1]\n\t"
                "ds_read_b64 v[240:241], %[kb] offset:448\n\t"
                "ds_read_b64 v[242:243], %[kb] offset:456\n\t"
                "ds_read_b128 v[244:247], %[ub] offset:4512\n\t"
                "s_waitcnt lgkmcnt(11)\n\t"
                "v_mfma_f32_32x32x16_bf16 %[a0], v[208:211], v[216:219], %[a0]\n\t"
                "ds_read_b128 v[248:251], %[ub] offset:1952\n\t"
                "s_add_u32 %[t0], %[t1], 114688\n\t"
                "buffer_load_dwordx4 a[28:31], %[go], %[so], %[t0] offen\n\t"
                "s_waitcnt lgkmcnt(10)\n\t"
                "v_mfma_f32_32x32x16_bf16 %[a1], v[220:223], v[216:219], %[a1]\n\t"
                "ds_read_b64 v[252:253], %[kb] offset:2496\n\t"
                "ds_read_b64 v[254:255], %[kb] offset:2504\n\t"
                "s_waitcnt lgkmcnt(9)\n\t"
                "v_mfma_f32_32x32x16_bf16 %[a1], v[224:227], v[228:231], %[a1]\n\t"
                "ds_read_b64 v[192:193], %[kb] offset:544\n\t"
                "ds_read_b64 v[194:195], %[kb] offset:552\n\t"
                "ds_read_b128 v[196:199], %[ub] offset:4400\n\t"
                "s_waitcnt lgkmcnt(11)\n\t"
                "v_mfma_f32_32x32x16_bf16 %[a0], v[224:227], v[232:235], %[a0]\n\t"
                "ds_read_b128 v[200:203], %[ub] offset:1840\n\t"
                "s_add_u32 %[t1], %[ro], 0\n\t"
                "s_and_b32 %[t1], %[t1], 7\n\t"
                "s_lshl_b32 %[t1], %[t1], 18\n\t"
                "s_add_u32 %[t0], %[t1], 131072\n\t"
                "buffer_load_dwordx4 a[32:35], %[go], %[so], %[t0] offen\n\t"
                "s_waitcnt lgkmcnt(10)\n\t"
                "v_mfma_f32_32x32x16_bf16 %[a1], v[236:239], v[232:235], %[a1]\n\t"
                "ds_read_b64 v[204:205], %[kb] offset:2592\n\t"
                "ds_read_b64 v[206:207], %[kb] offset:2600\n\t"
                "s_waitcnt lgkmcnt(9)\n\t"
                "v_mfma_f32_32x32x16_bf16 %[a1], v[240:243], v[244:247], %[a1]\n\t"
                "ds_read_b64 v[208:209], %[kb] offset:512\n\t"
                "ds_read_b64 v[210:211], %[kb] offset:520\n\t"
                "ds_read_b128 v[212:215], %[ub] offset:4432\n\t"
                "s_waitcnt lgkmcnt(11)\n\t"
                "v_mfma_f32_32x32x16_bf16 %[a0], v[240:243], v[248:251], %[a0]\n\t"
                "ds_read_b128 v[216:219], %[ub] offset:1872\n\t"
                "s_waitcnt lgkmcnt(10)\n\t"
                "v_mfma_f32_32x32x16_bf16 %[a1], v[252:255], v[248:251], %[a1]\n\t"
                "ds_read_b64 v[220:221], %[kb] offset:2560\n\t"
                "ds_read_b64 v[222:223], %[kb] offset:2568\n\t"
                "s_waitcnt lgkmcnt(9)\n\t"
                "v_mfma_f32_32x32x16_bf16 %[a1], v[192:195], v[196:199], %[a1]\n\t"
                "ds_read_b64 v[224:225], %[kb] offset:608\n\t"
                "ds_read_b64 v[226:227], %[kb] offset:616\n\t"
                "ds_read_b128 v[228:231], %[ub] offset:4320\n\t"
                "s_waitcnt lgkmcnt(11)\n\t"
                "v_mfma_f32_32x32x16_bf16 %[a0], v[192:195], v[200:203], %[a0]\n\t"
                "ds_read_b128 v[232:235], %[ub] offset:1760\n\t"
                "s_add_u32 %[t0], %[t1], 147456\n\t"
                "buffer_load_dwordx4 a[36:39], %[go], %[so], %[t0] offen\n\t"
                "s_waitcnt lgkmcnt(10)\n\t"
                "v_mfma_f32_32x32x16_bf16 %[a1], v[204:207], v[200:203], %[a1]\n\t"
                "ds_read_b64 v[236:237], %[kb] offset:2656\n\t"
                "ds_read_b64 v[238:239], %[kb] offset:2664\n\t"
                "s_waitcnt lgkmcnt(9)\n\t"
                "v_mfma_f32_32x32x16_bf16 %[a1], v[208:211], v[212:215], %[a1]\n\t"
                "ds_read_b64 v[240:241], %[kb] offset:576\n\t"
                "ds_read_b64 v[242:243], %[kb] offset:584\n\t"
                "ds_read_b128 v[244:247], %[ub] offset:4352\n\t"
                "s_waitcnt lgkmcnt(11)\n\t"
                "v_mfma_f32_32x32x16_bf16 %[a0], v[208:211], v[216:219], %[a0]\n\t"
                "ds_read_b128 v[248:251], %[ub] offset:1792\n\t"
                "s_waitcnt lgkmcnt(10)\n\t"
                "v_mfma_f32_32x32x16_bf16 %[a1], v[220:223], v[216:219], %[a1]\n\t"
                "ds_read_b64 v[252:253], %[kb] offset:2624\n\t"
                "ds_read_b64 v[254:255], %[kb] offset:2632\n\t"
                "s_waitcnt lgkmcnt(9)\n\t"
                "v_mfma_f32_32x32x16_bf16 %[a1], v[224:227], v[228:231], %[a1]\n\t"
                "ds_read_b64 v[192:193], %[kb] offset:672\n\t"
                "ds_read_b64 v[194:195], %[kb] offset:680\n\t"
                "ds_read_b128 v[196:199], %[ub] offset:4240\n\t"
                "s_waitcnt lgkmcnt(11)\n\t"
                "v_mfma_f32_32x32x16_bf16 %[a0], v[224:227], v[232:235], %[a0]\n\t"
                "ds_read_b128 v[200:203], %[ub] offset:1680\n\t"
                "s_add_u32 %[t0], %[t1], 163840\n\t"
                "buffer_load_dwordx4 a[40:43], %[go], %[so], %[t0] offen\n\t"
                "s_waitcnt lgkmcnt(10)\n\t"
                "v_mfma_f32_32x32x16_bf16 %[a1], v[236:239], v[232:235], %[a1]\n\t"
                "ds_read_b64 v[204:205], %[kb] offset:2720\n\t"
                "ds_read_b64 v[206:207], %[kb] offset:2728\n\t"
                "s_waitcnt lgkmcnt(9)\n\t"
                "v_mfma_f32_32x32x16_bf16 %[a1], v[240:243], v[244:247], %[a1]\n\t"
                "ds_read_b64 v[208:209], %[kb] offset:640\n\t"
                "ds_read_b64 v[210:211], %[kb] offset:648\n\t"
                "ds_read_b128 v[212:215], %[ub] offset:4272\n\t"
                "s_waitcnt lgkmcnt(11)\n\t"
                "v_mfma_f32_32x32x16_bf16 %[a0], v[240:243], v[248:251], %[a0]\n\t"
                "ds_read_b128 v[216:219], %[ub] offset:1712\n\t"
                "s_waitcnt lgkmcnt(10)\n\t"
                "v_mfma_f32_32x32x16_bf16 %[a1], v[252:255], v[248:251], %[a1]\n\t"
                "ds_read_b64 v[220:221], %[kb] offset:2688\n\t"
                "ds_read_b64 v[222:223], %[kb] offset:2696\n\t"
                "s_waitcnt lgkmcnt(9)\n\t"
                "v_mfma_f32_32x32x16_bf16 %[a1], v[192:195], v[196:199], %[a1]\n\t"
                "ds_read_b64 v[224:225], %[kb] offset:736\n\t"
                "ds_read_b64 v[226:227], %[kb] offset:744\n\t"
                "ds_read_b128 v[228:231], %[ub] offset:4160\n\t"
                "s_waitcnt lgkmcnt(11)\n\t"
                "v_mfma_f32_32x32x16_bf16 %[a0], v[192:195], v[200:203], %[a0]\n\t"
                "ds_read_b128 v[232:235], %[ub] offset:1600\n\t"
                "s_add_u32 %[t0], %[t1], 180224\n\t"
                "buffer_load_dwordx4 a[44:47], %[go], %[so], %[t0] offen\n\t"
                "s_waitcnt lgkmcnt(10)\n\t"
                "v_mfma_f32_32x32x16_bf16 %[a1], v[204:207], v[200:203], %[a1]\n\t"
                "ds_read_b64 v[236:237], %[kb] offset:2784\n\t"
                "ds_read_b64 v[238:239], %[kb] offset:2792\n\t"
                "s_waitcnt lgkmcnt(9)\n\t"
                "v_mfma_f32_32x32x16_bf16 %[a1], v[208:211], v[212:215], %[a1]\n\t"
                "ds_read_b64 v[240:241], %[kb] offset:704\n\t"
                "ds_read_b64 v[242:243], %[kb] offset:712\n\t"
                "ds_read_b128 v[244:247], %[ub] offset:4192\n\t"
                "s_waitcnt lgkmcnt(11)\n\t"
                "v_mfma_f32_32x32x16_bf16 %[a0], v[208:211], v[216:219], %[a0]\n\t"
                "ds_read_b128 v[248:251], %[ub] offset:1632\n\t"
                "s_add_u32 %[t0], %[t1], 196608\n\t"
                "buffer_load_dwordx4 a[48:51], %[go], %[so], %[t0] offen\n\t"
                "s_waitcnt lgkmcnt(10)\n\t"
                "v_mfma_f32_32x32x16_bf16 %[a1], v[220:223], v[216:219], %[a1]\n\t"
                "ds_read_b64 v[252:253], %[kb] offset:2752\n\t"
                "ds_read_b64 v[254:255], %[kb] offset:2760\n\t"
                "s_waitcnt lgkmcnt(9)\n\t"
                "v_mfma_f32_32x32x16_bf16 %[a1], v[224:227], v[228:231], %[a1]\n\t"
                "ds_read_b64 v[192:193], %[kb] offset:800\n\t"
                "ds_read_b64 v[194:195], %[kb] offset:808\n\t"
                "ds_read_b128 v[196:199], %[ub] offset:4080\n\t"
                "s_waitcnt lgkmcnt(11)\n\t"
                "v_mfma_f32_32x32x16_bf16 %[a0], v[224:227], v[232:235], %[a0]\n\t"
                "ds_read_b128 v[200:203], %[ub] offset:1520\n\t"
                "s_waitcnt lgkmcnt(10)\n\t"
                "v_mfma_f32_32x32x16_bf16 %[a1], v[236:239], v[232:235], %[a1]\n\t"
                "ds_read_b64 v[204:205], %[kb] offset:2848\n\t"
                "ds_read_b64 v[206:207], %[kb] offset:2856\n\t"
                "s_waitcnt lgkmcnt(9)\n\t"
                "v_mfma_f32_32x32x16_bf16 %[a1], v[240:243], v[244:247], %[a1]\n\t"
                "ds_read_b64 v[208:209], %[kb] offset:768\n\t"
                "ds_read_b64 v[210:211], %[kb] offset:776\n\t"
                "ds_read_b128 v[212:215], %[ub] offset:4112\n\t"
                "s_waitcnt lgkmcnt(11)\n\t"
                "v_mfma_f32_32x32x16_bf16 %[a0], v[240:243], v[248:251], %[a0]\n\t"
                "ds_read_b128 v[216:219], %[ub] offset:1552\n\t"
                "s_add_u32 %[t0], %[t1], 212992\n\t"
                "buffer_load_dwordx4 a[52:55], %[go], %[so], %[t0] offen\n\t"
                "s_waitcnt lgkmcnt(10)\n\t"
                "v_mfma_f32_32x32x16_bf16 %[a1], v[252:255], v[248:251], %[a1]\n\t"
                "ds_read_b64 v[220:221], %[kb] offset:2816\n\t"
                "ds_read_b64 v[222:223], %[kb] offset:2824\n\t"
                "s_waitcnt lgkmcnt(9)\n\t"
                "v_mfma_f32_32x32x16_bf16 %[a1], v[192:195], v[196:199], %[a1]\n\t"
                "ds_read_b64 v[224:225], %[kb] offset:864\n\t"
                "ds_read_b64 v[226:227], %[kb] offset:872\n\t"
                "ds_read_b128 v[228:231], %[ub] offset:4000\n\t"
                "s_waitcnt lgkmcnt(11)\n\t"
                "v_mfma_f32_32x32x16_bf16 %[a0], v[192:195], v[200:203], %[a0]\n\t"
                "ds_read_b128 v[232:235], %[ub] offset:1440\n\t"
                "s_waitcnt lgkmcnt(10)\n\t"
                "v_mfma_f32_32x32x16_bf16 %[a1], v[204:207], v[200:203], %[a1]\n\t"
                "ds_read_b64 v[236:237], %[kb] offset:2912\n\t"
                "ds_read_b64 v[238:239], %[kb] offset:2920\n\t"
                "s_waitcnt lgkmcnt(9)\n\t"
                "v_mfma_f32_32x32x16_bf16 %[a1], v[208:211], v[212:215], %[a1]\n\t"
                "ds_read_b64 v[240:241], %[kb] offset:832\n\t"
                "ds_read_b64 v[242:243], %[kb] offset:840\n\t"
                "ds_read_b128 v[244:247], %[ub] offset:4032\n\t"
                "s_waitcnt lgkmcnt(11)\n\t"
                "v_mfma_f32_32x32x16_bf16 %[a0], v[208:211], v[216:219], %[a0]\n\t"
                "ds_read_b128 v[248:251], %[ub] offset:1472\n\t"
                "s_add_u32 %[t0], %[t1], 229376\n\t"
                "buffer_load_dwordx4 a[56:59], %[go], %[so], %[t0] offen\n\t"
                "s_waitcnt lgkmcnt(10)\n\t"
                "v_mfma_f32_32x32x16_bf16 %[a1], v[220:223], v[216:219], %[a1]\n\t"
                "ds_read_b64 v[252:253], %[kb] offset:2880\n\t"
                "ds_read_b64 v[254:255], %[kb] offset:2888\n\t"
                "s_waitcnt lgkmcnt(9)\n\t"
                "v_mfma_f32_32x32x16_bf16 %[a1], v[224:227], v[228:231], %[a1]\n\t"
                "ds_read_b64 v[192:193], %[kb] offset:928\n\t"
                "ds_read_b64 v[194:195], %[kb] offset:936\n\t"
                "ds_read_b128 v[196:199], %[ub] offset:3920\n\t"
                "s_waitcnt lgkmcnt(11)\n\t"
                "v_mfma_f32_32x32x16_bf16 %[a0], v[224:227], v[232:235], %[a0]\n\t"
                "ds_read_b128 v[200:203], %[ub] offset:1360\n\t"
                "s_waitcnt lgkmcnt(10)\n\t"
                "v_mfma_f32_32x32x16_bf16 %[a1], v[236:239], v[232:235], %[a1]\n\t"
                "ds_read_b64 v[204:205], %[kb] offset:2976\n\t"
                "ds_read_b64 v[206:207], %[kb] offset:2984\n\t"
                "s_waitcnt lgkmcnt(9)\n\t"
                "v_mfma_f32_32x32x16_bf16 %[a1], v[240:243], v[244:247], %[a1]\n\t"
                "ds_read_b64 v[208:209], %[kb] offset:896\n\t"
                "ds_read_b64 v[210:211], %[kb] offset:904\n\t"
                "ds_read_b128 v[212:215], %[ub] offset:3952\n\t"
                "s_waitcnt lgkmcnt(11)\n\t"
                "v_mfma_f32_32x32x16_bf16 %[a0], v[240:243], v[248:251], %[a0]\n\t"
                "ds_read_b128 v[216:219], %[ub] offset:1392\n\t"
                "s_add_u32 %[t0], %[t1], 245760\n\t"
                "buffer_load_dwordx4 a[60:63], %[go], %[so], %[t0] offen\n\t"
                "s_waitcnt lgkmcnt(10)\n\t"
                "v_mfma_f32_32x32x16_bf16 %[a1], v[252:255], v[248:251], %[a1]\n\t"
                "ds_read_b64 v[220:221], %[kb] offset:2944\n\t"
                "ds_read_b64 v[222:223], %[kb] offset:2952\n\t"
                "s_waitcnt lgkmcnt(9)\n\t"
                "v_mfma_f32_32x32x16_bf16 %[a1], v[192:195], v[196:199], %[a1]\n\t"
                "ds_read_b64 v[224:225], %[kb] offset:992\n\t"
                "ds_read_b64 v[226:227], %[kb] offset:1000\n\t"
                "ds_read_b128 v[228:231], %[ub] offset:3840\n\t"
                "s_waitcnt lgkmcnt(11)\n\t"
                "v_mfma_f32_32x32x16_bf16 %[a0], v[192:195], v[200:203], %[a0]\n\t"
                "ds_read_b128 v[232:235], %[ub] offset:1280\n\t"
                "s_add_u32 %[t1], %[ro], 1\n\t"
                "s_and_b32 %[t1], %[t1], 7\n\t"
                "s_lshl_b32 %[t1], %[t1], 18\n\t"
                "s_add_u32 %[t0], %[t1], 0\n\t"
                "buffer_load_dwordx4 a[64:67], %[go], %[so], %[t0] offen\n\t"
                "s_waitcnt lgkmcnt(10)\n\t"
                "v_mfma_f32_32x32x16_bf16 %[a1], v[204:207], v[200:203], %[a1]\n\t"
                "ds_read_b64 v[236:237], %[kb] offset:3040\n\t"
                "ds_read_b64 v[238:239], %[kb] offset:3048\n\t"
                "s_waitcnt lgkmcnt(9)\n\t"
                "v_mfma_f32_32x32x16_bf16 %[a1], v[208:211], v[212:215], %[a1]\n\t"
                "ds_read_b64 v[240:241], %[kb] offset:960\n\t"
                "ds_read_b64 v[242:243], %[kb] offset:968\n\t"
                "ds_read_b128 v[244:247], %[ub] offset:3872\n\t"
                "s_waitcnt lgkmcnt(11)\n\t"
                "v_mfma_f32_32x32x16_bf16 %[a0], v[208:211], v[216:219], %[a0]\n\t"
                "ds_read_b128 v[248:251], %[ub] offset:1312\n\t"
                "s_waitcnt lgkmcnt(10)\n\t"
                "v_mfma_f32_32x32x16_bf16 %[a1], v[220:223], v[216:219], %[a1]\n\t"
                "ds_read_b64 v[252:253], %[kb] offset:3008\n\t"
                "ds_read_b64 v[254:255], %[kb] offset:3016\n\t"
                "s_waitcnt lgkmcnt(9)\n\t"
                "v_mfma_f32_32x32x16_bf16 %[a1], v[224:227], v[228:231], %[a1]\n\t"
                "ds_read_b64 v[192:193], %[kb] offset:1056\n\t"
                "ds_read_b64 v[194:195], %[kb] offset:1064\n\t"
                "ds_read_b128 v[196:199], %[ub] offset:3760\n\t"
                "s_waitcnt lgkmcnt(11)\n\t"
                "v_mfma_f32_32x32x16_bf16 %[a0], v[224:227], v[232:235], %[a0]\n\t"
                "ds_read_b128 v[200:203], %[ub] offset:1200\n\t"
                "s_add_u32 %[t0], %[t1], 16384\n\t"
                "buffer_load_dwordx4 a[68:71], %[go], %[so], %[t0] offen\n\t"
                "s_waitcnt lgkmcnt(10)\n\t"
                "v_mfma_f32_32x32x16_bf16 %[a1], v[236:239], v[232:235], %[a1]\n\t"
                "ds_read_b64 v[204:205], %[kb] offset:3104\n\t"
                "ds_read_b64 v[206:207], %[kb] offset:3112\n\t"
                "s_waitcnt lgkmcnt(9)\n\t"
                "v_mfma_f32_32x32x16_bf16 %[a1], v[240:243], v[244:247], %[a1]\n\t"
                "ds_read_b64 v[208:209], %[kb] offset:1024\n\t"
                "ds_read_b64 v[210:211], %[kb] offset:1032\n\t"
                "ds_read_b128 v[212:215], %[ub] offset:3792\n\t"
                "s_waitcnt lgkmcnt(11)\n\t"
                "v_mfma_f32_32x32x16_bf16 %[a0], v[240:243], v[248:251], %[a0]\n\t"
                "ds_read_b128 v[216:219], %[ub] offset:1232\n\t"
                "s_waitcnt lgkmcnt(10)\n\t"
                "v_mfma_f32_32x32x16_bf16 %[a1], v[252:255], v[248:251], %[a1]\n\t"
                "ds_read_b64 v[220:221], %[kb] offset:3072\n\t"
                "ds_read_b64 v[222:223], %[kb] offset:3080\n\t"
                "s_waitcnt lgkmcnt(9)\n\t"
                "v_mfma_f32_32x32x16_bf16 %[a1], v[192:195], v[196:199], %[a1]\n\t"
                "ds_read_b64 v[224:225], %[kb] offset:1120\n\t"
                "ds_read_b64 v[226:227], %[kb] offset:1128\n\t"
                "ds_read_b128 v[228:231], %[ub] offset:3680\n\t"
                "s_waitcnt lgkmcnt(11)\n\t"
                "v_mfma_f32_32x32x16_bf16 %[a0], v[192:195], v[200:203], %[a0]\n\t"
                "ds_read_b128 v[232:235], %[ub] offset:1120\n\t"
                "s_add_u32 %[t0], %[t1], 32768\n\t"
                "buffer_load_dwordx4 a[72:75], %[go], %[so], %[t0] offen\n\t"
                "s_waitcnt lgkmcnt(10)\n\t"
                "v_mfma_f32_32x32x16_bf16 %[a1], v[204:207], v[200:203], %[a1]\n\t"
                "ds_read_b64 v[236:237], %[kb] offset:3168\n\t"
                "ds_read_b64 v[238:239], %[kb] offset:3176\n\t"
                "s_waitcnt lgkmcnt(9)\n\t"
                "v_mfma_f32_32x32x16_bf16 %[a1], v[208:211], v[212:215], %[a1]\n\t"
                "ds_read_b64 v[240:241], %[kb] offset:1088\n\t"
                "ds_read_b64 v[242:243], %[kb] offset:1096\n\t"
                "ds_read_b128 v[244:247], %[ub] offset:3712\n\t"
                "s_waitcnt lgkmcnt(11)\n\t"
                "v_mfma_f32_32x32x16_bf16 %[a0], v[208:211], v[216:219], %[a0]\n\t"
                "ds_read_b128 v[248:251], %[ub] offset:1152\n\t"
                "s_waitcnt lgkmcnt(10)\n\t"
                "v_mfma_f32_32x32x16_bf16 %[a1], v[220:223], v[216:219], %[a1]\n\t"
                "ds_read_b64 v[252:253], %[kb] offset:3136\n\t"
                "ds_read_b64 v[254:255], %[kb] offset:3144\n\t"
                "s_waitcnt lgkmcnt(9)\n\t"
                "v_mfma_f32_32x32x16_bf16 %[a1], v[224:227], v[228:231], %[a1]\n\t"
                "ds_read_b64 v[192:193], %[kb] offset:1184\n\t"
                "ds_read_b64 v[194:195], %[kb] offset:1192\n\t"
                "ds_read_b128 v[196:199], %[ub] offset:3600\n\t"
                "s_waitcnt lgkmcnt(11)\n\t"
                "v_mfma_f32_32x32x16_bf16 %[a0], v[224:227], v[232:235], %[a0]\n\t"
                "ds_read_b128 v[200:203], %[ub] offset:1040\n\t"
                "s_add_u32 %[t0], %[t1], 49152\n\t"
                "buffer_load_dwordx4 a[76:79], %[go], %[so], %[t0] offen\n\t"
                "s_waitcnt lgkmcnt(10)\n\t"
                "v_mfma_f32_32x32x16_bf16 %[a1], v[236:239], v[232:235], %[a1]\n\t"
                "ds_read_b64 v[204:205], %[kb] offset:3232\n\t"
                "ds_read_b64 v[206:207], %[kb] offset:3240\n\t"
                "s_waitcnt lgkmcnt(9)\n\t"
                "v_mfma_f32_32x32x16_bf16 %[a1], v[240:243], v[244:247], %[a1]\n\t"
                "ds_read_b64 v[208:209], %[kb] offset:1152\n\t"
                "ds_read_b64 v[210:211], %[kb] offset:1160\n\t"
                "ds_read_b128 v[212:215], %[ub] offset:3632\n\t"
                "s_waitcnt lgkmcnt(11)\n\t"
                "v_mfma_f32_32x32x16_bf16 %[a0], v[240:243], v[248:251], %[a0]\n\t"
                "ds_read_b128 v[216:219], %[ub] offset:1072\n\t"
                "s_add_u32 %[t0], %[t1], 65536\n\t"
                "buffer_load_dwordx4 a[80:83], %[go], %[so], %[t0] offen\n\t"
                "s_waitcnt lgkmcnt(10)\n\t"
                "v_mfma_f32_32x32x16_bf16 %[a1], v[252:255], v[248:251], %[a1]\n\t"
                "ds_read_b64 v[220:221], %[kb] offset:3200\n\t"
                "ds_read_b64 v[222:223], %[kb] offset:3208\n\t"
                "s_waitcnt lgkmcnt(9)\n\t"
                "v_mfma_f32_32x32x16_bf16 %[a1], v[192:195], v[196:199], %[a1]\n\t"
                "ds_read_b64 v[224:225], %[kb] offset:1248\n\t"
                "ds_read_b64 v[226:227], %[kb] offset:1256\n\t"
                "ds_read_b128 v[228:231], %[ub] offset:3520\n\t"
                "s_waitcnt lgkmcnt(11)\n\t"
                "v_mfma_f32_32x32x16_bf16 %[a0], v[192:195], v[200:203], %[a0]\n\t"
                "ds_read_b128 v[232:235], %[ub] offset:960\n\t"
                "s_waitcnt lgkmcnt(10)\n\t"
                "v_mfma_f32_32x32x16_bf16 %[a1], v[204:207], v[200:203], %[a1]\n\t"
                "ds_read_b64 v[236:237], %[kb] offset:3296\n\t"
                "ds_read_b64 v[238:239], %[kb] offset:3304\n\t"
                "s_waitcnt lgkmcnt(9)\n\t"
                "v_mfma_f32_32x32x16_bf16 %[a1], v[208:211], v[212:215], %[a1]\n\t"
                "ds_read_b64 v[240:241], %[kb] offset:1216\n\t"
                "ds_read_b64 v[242:243], %[kb] offset:1224\n\t"
                "ds_read_b128 v[244:247], %[ub] offset:3552\n\t"
                "s_waitcnt lgkmcnt(11)\n\t"
                "v_mfma_f32_32x32x16_bf16 %[a0], v[208:211], v[216:219], %[a0]\n\t"
                "ds_read_b128 v[248:251], %[ub] offset:992\n\t"
                "s_add_u32 %[t0], %[t1], 81920\n\t"
                "buffer_load_dwordx4 a[84:87], %[go], %[so], %[t0] offen\n\t"
                "s_waitcnt lgkmcnt(10)\n\t"
                "v_mfma_f32_32x32x16_bf16 %[a1], v[220:223], v[216:219], %[a1]\n\t"
                "ds_read_b64 v[252:253], %[kb] offset:3264\n\t"
                "ds_read_b64 v[254:255], %[kb] offset:3272\n\t"
                "s_waitcnt lgkmcnt(9)\n\t"
                "v_mfma_f32_32x32x16_bf16 %[a1], v[224:227], v[228:231], %[a1]\n\t"
                "ds_read_b64 v[192:193], %[kb] offset:1312\n\t"
                "ds_read_b64 v[194:195], %[kb] offset:1320\n\t"
                "ds_read_b128 v[196:199], %[ub] offset:3440\n\t"
                "s_waitcnt lgkmcnt(11)\n\t"
                "v_mfma_f32_32x32x16_bf16 %[a0], v[224:227], v[232:235], %[a0]\n\t"
                "ds_read_b128 v[200:203], %[ub] offset:880\n\t"
                "s_waitcnt lgkmcnt(10)\n\t"
                "v_mfma_f32_32x32x16_bf16 %[a1], v[236:239], v[232:235], %[a1]\n\t"
                "ds_read_b64 v[204:205], %[kb] offset:3360\n\t"
                "ds_read_b64 v[206:207], %[kb] offset:3368\n\t"
                "s_waitcnt lgkmcnt(9)\n\t"
                "v_mfma_f32_32x32x16_bf16 %[a1], v[240:243], v[244:247], %[a1]\n\t"
                "ds_read_b64 v[208:209], %[kb] offset:1280\n\t"
                "ds_read_b64 v[210:211], %[kb] offset:1288\n\t"
                "ds_read_b128 v[212:215], %[ub] offset:3472\n\t"
                "s_waitcnt lgkmcnt(11)\n\t"
                "v_mfma_f32_32x32x16_bf16 %[a0], v[240:243], v[248:251], %[a0]\n\t"
                "ds_read_b128 v[216:219], %[ub] offset:912\n\t"
                "s_add_u32 %[t0], %[t1], 98304\n\t"
                "buffer_load_dwordx4 a[88:91], %[go], %[so], %[t0] offen\n\t"
                "s_waitcnt lgkmcnt(10)\n\t"
                "v_mfma_f32_32x32x16_bf16 %[a1], v[252:255], v[248:251], %[a1]\n\t"
                "ds_read_b64 v[220:221], %[kb] offset:3328\n\t"
                "ds_read_b64 v[222:223], %[kb] offset:3336\n\t"
                "s_waitcnt lgkmcnt(9)\n\t"
                "v_mfma_f32_32x32x16_bf16 %[a1], v[192:195], v[196:199], %[a1]\n\t"
                "ds_read_b64 v[224:225], %[kb] offset:1376\n\t"
                "ds_read_b64 v[226:227], %[kb] offset:1384\n\t"
                "ds_read_b128 v[228:231], %[ub] offset:3360\n\t"
                "s_waitcnt lgkmcnt(11)\n\t"
                "v_mfma_f32_32x32x16_bf16 %[a0], v[192:195], v[200:203], %[a0]\n\t"
                "ds_read_b128 v[232:235], %[ub] offset:800\n\t"
                "s_waitcnt lgkmcnt(10)\n\t"
                "v_mfma_f32_32x32x16_bf16 %[a1], v[204:207], v[200:203], %[a1]\n\t"
                "ds_read_b64 v[236:237], %[kb] offset:3424\n\t"
                "ds_read_b64 v[238:239], %[kb] offset:3432\n\t"
                "s_waitcnt lgkmcnt(9)\n\t"
                "v_mfma_f32_32x32x16_bf16 %[a1], v[208:211], v[212:215], %[a1]\n\t"
                "ds_read_b64 v[240:241], %[kb] offset:1344\n\t"
                "ds_read_b64 v[242:243], %[kb] offset:1352\n\t"
                "ds_read_b128 v[244:247], %[ub] offset:3392\n\t"
                "s_waitcnt lgkmcnt(11)\n\t"
                "v_mfma_f32_32x32x16_bf16 %[a0], v[208:211], v[216:219], %[a0]\n\t"
                "ds_read_b128 v[248:251], %[ub] offset:832\n\t"
                "s_add_u32 %[t0], %[t1], 114688\n\t"
                "buffer_load_dwordx4 a[92:95], %[go], %[so], %[t0] offen\n\t"
                "s_waitcnt lgkmcnt(10)\n\t"
                "v_mfma_f32_32x32x16_bf16 %[a1], v[220:223], v[216:219], %[a1]\n\t"
                "ds_read_b64 v[252:253], %[kb] offset:3392\n\t"
                "ds_read_b64 v[254:255], %[kb] offset:3400\n\t"
                "s_waitcnt lgkmcnt(9)\n\t"
                "v_mfma_f32_32x32x16_bf16 %[a1], v[224:227], v[228:231], %[a1]\n\t"
                "ds_read_b64 v[192:193], %[kb] offset:1440\n\t"
                "ds_read_b64 v[194:195], %[kb] offset:1448\n\t"
                "ds_read_b128 v[196:199], %[ub] offset:3280\n\t"
                "s_waitcnt lgkmcnt(11)\n\t"
                "v_mfma_f32_32x32x16_bf16 %[a0], v[224:227], v[232:235], %[a0]\n\t"
                "ds_read_b128 v[200:203], %[ub] offset:720\n\t"
                "s_add_u32 %[t1], %[ro], 1\n\t"
                "s_and_b32 %[t1], %[t1], 7\n\t"
                "s_lshl_b32 %[t1], %[t1], 18\n\t"
                "s_add_u32 %[t0], %[t1], 131072\n\t"
                "buffer_load_dwordx4 a[96:99], %[go], %[so], %[t0] offen\n\t"
                "s_waitcnt lgkmcnt(10)\n\t"
                "v_mfma_f32_32x32x16_bf16 %[a1], v[236:239], v[232:235], %[a1]\n\t"
                "ds_read_b64 v[204:205], %[kb] offset:3488\n\t"
                "ds_read_b64 v[206:207], %[kb] offset:3496\n\t"
                "s_waitcnt lgkmcnt(9)\n\t"
                "v_mfma_f32_32x32x16_bf16 %[a1], v[240:243], v[244:247], %[a1]\n\t"
                "ds_read_b64 v[208:209], %[kb] offset:1408\n\t"
                "ds_read_b64 v[210:211], %[kb] offset:1416\n\t"
                "ds_read_b128 v[212:215], %[ub] offset:3312\n\t"
                "s_waitcnt lgkmcnt(11)\n\t"
                "v_mfma_f32_32x32x16_bf16 %[a0], v[240:243], v[248:251], %[a0]\n\t"
                "ds_read_b128 v[216:219], %[ub] offset:752\n\t"
                "s_waitcnt lgkmcnt(10)\n\t"
                "v_mfma_f32_32x32x16_bf16 %[a1], v[252:255], v[248:251], %[a1]\n\t"
                "ds_read_b64 v[220:221], %[kb] offset:3456\n\t"
                "ds_read_b64 v[222:223], %[kb] offset:3464\n\t"
                "s_waitcnt lgkmcnt(9)\n\t"
                "v_mfma_f32_32x32x16_bf16 %[a1], v[192:195], v[196:199], %[a1]\n\t"
                "ds_read_b64 v[224:225], %[kb] offset:1504\n\t"
                "ds_read_b64 v[226:227], %[kb] offset:1512\n\t"
                "ds_read_b128 v[228:231], %[ub] offset:3200\n\t"
                "s_waitcnt lgkmcnt(11)\n\t"
                "v_mfma_f32_32x32x16_bf16 %[a0], v[192:195], v[200:203], %[a0]\n\t"
                "ds_read_b128 v[232:235], %[ub] offset:640\n\t"
                "s_add_u32 %[t0], %[t1], 147456\n\t"
                "buffer_load_dwordx4 a[100:103], %[go], %[so], %[t0] offen\n\t"
                "s_waitcnt lgkmcnt(10)\n\t"
                "v_mfma_f32_32x32x16_bf16 %[a1], v[204:207], v[200:203], %[a1]\n\t"
                "ds_read_b64 v[236:237], %[kb] offset:3552\n\t"
                "ds_read_b64 v[238:239], %[kb] offset:3560\n\t"
                "s_waitcnt lgkmcnt(9)\n\t"
                "v_mfma_f32_32x32x16_bf16 %[a1], v[208:211], v[212:215], %[a1]\n\t"
                "ds_read_b64 v[240:241], %[kb] offset:1472\n\t"
                "ds_read_b64 v[242:243], %[kb] offset:1480\n\t"
                "ds_read_b128 v[244:247], %[ub] offset:3232\n\t"
                "s_waitcnt lgkmcnt(11)\n\t"
                "v_mfma_f32_32x32x16_bf16 %[a0], v[208:211], v[216:219], %[a0]\n\t"
                "ds_read_b128 v[248:251], %[ub] offset:672\n\t"
                "s_waitcnt lgkmcnt(10)\n\t"
                "v_mfma_f32_32x32x16_bf16 %[a1], v[220:223], v[216:219], %[a1]\n\t"
                "ds_read_b64 v[252:253], %[kb] offset:3520\n\t"
                "ds_read_b64 v[254:255], %[kb] offset:3528\n\t"
                "s_waitcnt lgkmcnt(9)\n\t"
                "v_mfma_f32_32x32x16_bf16 %[a1], v[224:227], v[228:231], %[a1]\n\t"
                "ds_read_b64 v[192:193], %[kb] offset:1568\n\t"
                "ds_read_b64 v[194:195], %[kb] offset:1576\n\t"
                "ds_read_b128 v[196:199], %[ub] offset:3120\n\t"
                "s_waitcnt lgkmcnt(11)\n\t"
                "v_mfma_f32_32x32x16_bf16 %[a0], v[224:227], v[232:235], %[a0]\n\t"
                "ds_read_b128 v[200:203], %[ub] offset:560\n\t"
                "s_add_u32 %[t0], %[t1], 163840\n\t"
                "buffer_load_dwordx4 a[104:107], %[go], %[so], %[t0] offen\n\t"
                "s_waitcnt lgkmcnt(10)\n\t"
                "v_mfma_f32_32x32x16_bf16 %[a1], v[236:239], v[232:235], %[a1]\n\t"
                "ds_read_b64 v[204:205], %[kb] offset:3616\n\t"
                "ds_read_b64 v[206:207], %[kb] offset:3624\n\t"
                "s_waitcnt lgkmcnt(9)\n\t"
                "v_mfma_f32_32x32x16_bf16 %[a1], v[240:243], v[244:247], %[a1]\n\t"
                "ds_read_b64 v[208:209], %[kb] offset:1536\n\t"
                "ds_read_b64 v[210:211], %[kb] offset:1544\n\t"
                "ds_read_b128 v[212:215], %[ub] offset:3152\n\t"
                "s_waitcnt lgkmcnt(11)\n\t"
                "v_mfma_f32_32x32x16_bf16 %[a0], v[240:243], v[248:251], %[a0]\n\t"
                "ds_read_b128 v[216:219], %[ub] offset:592\n\t"
                "s_waitcnt lgkmcnt(10)\n\t"
                "v_mfma_f32_32x32x16_bf16 %[a1], v[252:255], v[248:251], %[a1]\n\t"
                "ds_read_b64 v[220:221], %[kb] offset:3584\n\t"
                "ds_read_b64 v[222:223], %[kb] offset:3592\n\t"
                "s_waitcnt lgkmcnt(9)\n\t"
                "v_mfma_f32_32x32x16_bf16 %[a1], v[192:195], v[196:199], %[a1]\n\t"
                "ds_read_b64 v[224:225], %[kb] offset:1632\n\t"
                "ds_read_b64 v[226:227], %[kb] offset:1640\n\t"
                "ds_read_b128 v[228:231], %[ub] offset:3040\n\t"
                "s_waitcnt lgkmcnt(11)\n\t"
                "v_mfma_f32_32x32x16_bf16 %[a0], v[192:195], v[200:203], %[a0]\n\t"
                "ds_read_b128 v[232:235], %[ub] offset:480\n\t"
                "s_add_u32 %[t0], %[t1], 180224\n\t"
                "buffer_load_dwordx4 a[108:111], %[go], %[so], %[t0] offen\n\t"
                "s_waitcnt lgkmcnt(10)\n\t"
                "v_mfma_f32_32x32x16_bf16 %[a1], v[204:207], v[200:203], %[a1]\n\t"
                "ds_read_b64 v[236:237], %[kb] offset:3680\n\t"
                "ds_read_b64 v[238:239], %[kb] offset:3688\n\t"
                "s_waitcnt lgkmcnt(9)\n\t"
                "v_mfma_f32_32x32x16_bf16 %[a1], v[208:211], v[212:215], %[a1]\n\t"
                "ds_read_b64 v[240:241], %[kb] offset:1600\n\t"
                "ds_read_b64 v[242:243], %[kb] offset:1608\n\t"
                "ds_read_b128 v[244:247], %[ub] offset:3072\n\t"
                "s_waitcnt lgkmcnt(11)\n\t"
                "v_mfma_f32_32x32x16_bf16 %[a0], v[208:211], v[216:219], %[a0]\n\t"
                "ds_read_b128 v[248:251], %[ub] offset:512\n\t"
                "s_add_u32 %[t0], %[t1], 196608\n\t"
                "buffer_load_dwordx4 a[112:115], %[go], %[so], %[t0] offen\n\t"
                "s_waitcnt lgkmcnt(10)\n\t"
                "v_mfma_f32_32x32x16_bf16 %[a1], v[220:223], v[216:219], %[a1]\n\t"
                "ds_read_b64 v[252:253], %[kb] offset:3648\n\t"
                "ds_read_b64 v[254:255], %[kb] offset:3656\n\t"
                "s_waitcnt lgkmcnt(9)\n\t"
                "v_mfma_f32_32x32x16_bf16 %[a1], v[224:227], v[228:231], %[a1]\n\t"
                "ds_read_b64 v[192:193], %[kb] offset:1696\n\t"
                "ds_read_b64 v[194:195], %[kb] offset:1704\n\t"
                "ds_read_b128 v[196:199], %[ub] offset:2960\n\t"
                "s_waitcnt lgkmcnt(11)\n\t"
                "v_mfma_f32_32x32x16_bf16 %[a0], v[224:227], v[232:235], %[a0]\n\t"
                "ds_read_b128 v[200:203], %[ub] offset:400\n\t"
                "s_waitcnt lgkmcnt(10)\n\t"
                "v_mfma_f32_32x32x16_bf16 %[a1], v[236:239], v[232:235], %[a1]\n\t"
                "ds_read_b64 v[204:205], %[kb] offset:3744\n\t"
                "ds_read_b64 v[206:207], %[kb] offset:3752\n\t"
                "s_waitcnt lgkmcnt(9)\n\t"
                "v_mfma_f32_32x32x16_bf16 %[a1], v[240:243], v[244:247], %[a1]\n\t"
                "ds_read_b64 v[208:209], %[kb] offset:1664\n\t"
                "ds_read_b64 v[210:211], %[kb] offset:1672\n\t"
                "ds_read_b128 v[212:215], %[ub] offset:2992\n\t"
                "s_waitcnt lgkmcnt(11)\n\t"
                "v_mfma_f32_32x32x16_bf16 %[a0], v[240:243], v[248:251], %[a0]\n\t"
                "ds_read_b128 v[216:219], %[ub] offset:432\n\t"
                "s_add_u32 %[t0], %[t1], 212992\n\t"
                "buffer_load_dwordx4 a[116:119], %[go], %[so], %[t0] offen\n\t"
                "s_waitcnt lgkmcnt(10)\n\t"
                "v_mfma_f32_32x32x16_bf16 %[a1], v[252:255], v[248:251], %[a1]\n\t"
                "ds_read_b64 v[220:221], %[kb] offset:3712\n\t"
                "ds_read_b64 v[222:223], %[kb] offset:3720\n\t"
                "s_waitcnt lgkmcnt(9)\n\t"
                "v_mfma_f32_32x32x16_bf16 %[a1], v[192:195], v[196:199], %[a1]\n\t"
                "ds_read_b64 v[224:225], %[kb] offset:1760\n\t"
                "ds_read_b64 v[226:227], %[kb] offset:1768\n\t"
                "ds_read_b128 v[228:231], %[ub] offset:2880\n\t"
                "s_waitcnt lgkmcnt(11)\n\t"
                "v_mfma_f32_32x32x16_bf16 %[a0], v[192:195], v[200:203], %[a0]\n\t"
                "ds_read_b128 v[232:235], %[ub] offset:320\n\t"
                "s_waitcnt lgkmcnt(10)\n\t"
                "v_mfma_f32_32x32x16_bf16 %[a1], v[204:207], v[200:203], %[a1]\n\t"
                "ds_read_b64 v[236:237], %[kb] offset:3808\n\t"
                "ds_read_b64 v[238:239], %[kb] offset:3816\n\t"
                "s_waitcnt lgkmcnt(9)\n\t"
                "v_mfma_f32_32x32x16_bf16 %[a1], v[208:211], v[212:215], %[a1]\n\t"
                "ds_read_b64 v[240:241], %[kb] offset:1728\n\t"
                "ds_read_b64 v[242:243], %[kb] offset:1736\n\t"
                "ds_read_b128 v[244:247], %[ub] offset:2912\n\t"
                "s_waitcnt lgkmcnt(11)\n\t"
                "v_mfma_f32_32x32x16_bf16 %[a0], v[208:211], v[216:219], %[a0]\n\t"
                "ds_read_b128 v[248:251], %[ub] offset:352\n\t"
                "s_add_u32 %[t0], %[t1], 229376\n\t"
                "buffer_load_dwordx4 a[120:123], %[go], %[so], %[t0] offen\n\t"
                "s_waitcnt lgkmcnt(10)\n\t"
                "v_mfma_f32_32x32x16_bf16 %[a1], v[220:223], v[216:219], %[a1]\n\t"
                "ds_read_b64 v[252:253], %[kb] offset:3776\n\t"
                "ds_read_b64 v[254:255], %[kb] offset:3784\n\t"
                "s_waitcnt lgkmcnt(9)\n\t"
                "v_mfma_f32_32x32x16_bf16 %[a1], v[224:227], v[228:231], %[a1]\n\t"
                "ds_read_b64 v[192:193], %[kb] offset:1824\n\t"
                "ds_read_b64 v[194:195], %[kb] offset:1832\n\t"
                "ds_read_b128 v[196:199], %[ub] offset:2800\n\t"
                "s_waitcnt lgkmcnt(11)\n\t"
                "v_mfma_f32_32x32x16_bf16 %[a0], v[224:227], v[232:235], %[a0]\n\t"
                "ds_read_b128 v[200:203], %[ub] offset:240\n\t"
                "s_waitcnt lgkmcnt(10)\n\t"
                "v_mfma_f32_32x32x16_bf16 %[a1], v[236:239], v[232:235], %[a1]\n\t"
                "ds_read_b64 v[204:205], %[kb] offset:3872\n\t"
                "ds_read_b64 v[206:207], %[kb] offset:3880\n\t"
                "s_waitcnt lgkmcnt(9)\n\t"
                "v_mfma_f32_32x32x16_bf16 %[a1], v[240:243], v[244:247], %[a1]\n\t"
                "ds_read_b64 v[208:209], %[kb] offset:1792\n\t"
                "ds_read_b64 v[210:211], %[kb] offset:1800\n\t"
                "ds_read_b128 v[212:215], %[ub] offset:2832\n\t"
                "s_waitcnt lgkmcnt(11)\n\t"
                "v_mfma_f32_32x32x16_bf16 %[a0], v[240:243], v[248:251], %[a0]\n\t"
                "ds_read_b128 v[216:219], %[ub] offset:272\n\t"
                "s_add_u32 %[t0], %[t1], 245760\n\t"
                "buffer_load_dwordx4 a[124:127], %[go], %[so], %[t0] offen\n\t"
                "s_waitcnt lgkmcnt(10)\n\t"
                "v_mfma_f32_32x32x16_bf16 %[a1], v[252:255], v[248:251], %[a1]\n\t"
                "ds_read_b64 v[220:221], %[kb] offset:3840\n\t"
                "ds_read_b64 v[222:223], %[kb] offset:3848\n\t"
                "s_waitcnt lgkmcnt(9)\n\t"
                "v_mfma_f32_32x32x16_bf16 %[a1], v[192:195], v[196:199], %[a1]\n\t"
                "ds_read_b64 v[224:225], %[kb] offset:1888\n\t"
                "ds_read_b64 v[226:227], %[kb] offset:1896\n\t"
                "ds_read_b128 v[228:231], %[ub] offset:2720\n\t"
                "s_waitcnt lgkmcnt(11)\n\t"
                "v_mfma_f32_32x32x16_bf16 %[a0], v[192:195], v[200:203], %[a0]\n\t"
                "ds_read_b128 v[232:235], %[ub] offset:160\n\t"
                "s_waitcnt lgkmcnt(10)\n\t"
                "v_mfma_f32_32x32x16_bf16 %[a1], v[204:207], v[200:203], %[a1]\n\t"
                "ds_read_b64 v[236:237], %[kb] offset:3936\n\t"
                "ds_read_b64 v[238:239], %[kb] offset:3944\n\t"
                "s_waitcnt lgkmcnt(9)\n\t"
                "v_mfma_f32_32x32x16_bf16 %[a1], v[208:211], v[212:215], %[a1]\n\t"
                "ds_read_b64 v[240:241], %[kb] offset:1856\n\t"
                "ds_read_b64 v[242:243], %[kb] offset:1864\n\t"
                "ds_read_b128 v[244:247], %[ub] offset:2752\n\t"
                "s_waitcnt lgkmcnt(11)\n\t"
                "v_mfma_f32_32x32x16_bf16 %[a0], v[208:211], v[216:219], %[a0]\n\t"
                "ds_read_b128 v[248:251], %[ub] offset:192\n\t"
                "s_waitcnt lgkmcnt(10)\n\t"
                "v_mfma_f32_32x32x16_bf16 %[a1], v[220:223], v[216:219], %[a1]\n\t"
                "ds_read_b64 v[252:253], %[kb] offset:3904\n\t"
                "ds_read_b64 v[254:255], %[kb] offset:3912\n\t"
                "s_waitcnt lgkmcnt(9)\n\t"
                "v_mfma_f32_32x32x16_bf16 %[a1], v[224:227], v[228:231], %[a1]\n\t"
                "ds_read_b64 v[192:193], %[kb] offset:1952\n\t"
                "ds_read_b64 v[194:195], %[kb] offset:1960\n\t"
                "ds_read_b128 v[196:199], %[ub] offset:2640\n\t"
                "s_waitcnt lgkmcnt(11)\n\t"
                "v_mfma_f32_32x32x16_bf16 %[a0], v[224:227], v[232:235], %[a0]\n\t"
                "ds_read_b128 v[200:203], %[ub] offset:80\n\t"
                "s_waitcnt lgkmcnt(10)\n\t"
                "v_mfma_f32_32x32x16_bf16 %[a1], v[236:239], v[232:235], %[a1]\n\t"
                "ds_read_b64 v[204:205], %[kb] offset:4000\n\t"
                "ds_read_b64 v[206:207], %[kb] offset:4008\n\t"
                "s_waitcnt lgkmcnt(9)\n\t"
                "v_mfma_f32_32x32x16_bf16 %[a1], v[240:243], v[244:247], %[a1]\n\t"
                "ds_read_b64 v[208:209], %[kb] offset:1920\n\t"
                "ds_read_b64 v[210:211], %[kb] offset:1928\n\t"
                "ds_read_b128 v[212:215], %[ub] offset:2672\n\t"
                "s_waitcnt lgkmcnt(11)\n\t"
                "v_mfma_f32_32x32x16_bf16 %[a0], v[240:243], v[248:251], %[a0]\n\t"
                "ds_read_b128 v[216:219], %[ub] offset:112\n\t"
                "s_waitcnt lgkmcnt(10)\n\t"
                "v_mfma_f32_32x32x16_bf16 %[a1], v[252:255], v[248:251], %[a1]\n\t"
                "ds_read_b64 v[220:221], %[kb] offset:3968\n\t"
                "ds_read_b64 v[222:223], %[kb] offset:3976\n\t"
                "s_waitcnt lgkmcnt(9)\n\t"
                "v_mfma_f32_32x32x16_bf16 %[a1], v[192:195], v[196:199], %[a1]\n\t"
                "ds_read_b64 v[224:225], %[kb] offset:2016\n\t"
                "ds_read_b64 v[226:227], %[kb] offset:2024\n\t"
                "ds_read_b128 v[228:231], %[ub] offset:2560\n\t"
                "s_waitcnt lgkmcnt(11)\n\t"
                "v_mfma_f32_32x32x16_bf16 %[a0], v[192:195], v[200:203], %[a0]\n\t"
                "ds_read_b128 v[232:235], %[ub] offset:0\n\t"
                "s_waitcnt lgkmcnt(10)\n\t"
                "v_mfma_f32_32x32x16_bf16 %[a1], v[204:207], v[200:203], %[a1]\n\t"
                "ds_read_b64 v[236:237], %[kb] offset:4064\n\t"
                "ds_read_b64 v[238:239], %[kb] offset:4072\n\t"
                "s_waitcnt lgkmcnt(9)\n\t"
                "v_mfma_f32_32x32x16_bf16 %[a1], v[208:211], v[212:215], %[a1]\n\t"
                "ds_read_b64 v[240:241], %[kb] offset:1984\n\t"
                "ds_read_b64 v[242:243], %[kb] offset:1992\n\t"
                "ds_read_b128 v[244:247], %[ub] offset:2592\n\t"
                "s_waitcnt lgkmcnt(11)\n\t"
                "v_mfma_f32_32x32x16_bf16 %[a0], v[208:211], v[216:219], %[a0]\n\t"
                "ds_read_b128 v[248:251], %[ub] offset:32\n\t"
                "s_waitcnt lgkmcnt(10)\n\t"
                "v_mfma_f32_32x32x16_bf16 %[a1], v[220:223], v[216:219], %[a1]\n\t"
                "ds_read_b64 v[252:253], %[kb] offset:4032\n\t"
                "ds_read_b64 v[254:255], %[kb] offset:4040\n\t"
                "s_waitcnt lgkmcnt(9)\n\t"
                "v_mfma_f32_32x32x16_bf16 %[a1], v[224:227], v[228:231], %[a1]\n\t"
                "s_waitcnt lgkmcnt(8)\n\t"
                "v_mfma_f32_32x32x16_bf16 %[a0], v[224:227], v[232:235], %[a0]\n\t"
                "s_waitcnt lgkmcnt(6)\n\t"
                "v_mfma_f32_32x32x16_bf16 %[a1], v[236:239], v[232:235], %[a1]\n\t"
                "s_waitcnt lgkmcnt(3)\n\t"
                "v_mfma_f32_32x32x16_bf16 %[a1], v[240:243], v[244:247], %[a1]\n\t"
                "s_waitcnt lgkmcnt(2)\n\t"
                "v_mfma_f32_32x32x16_bf16 %[a0], v[240:243], v[248:251], %[a0]\n\t"
                "s_waitcnt lgkmcnt(0)\n\t"
                "v_mfma_f32_32x32x16_bf16 %[a1], v[252:255], v[248:251], %[a1]\n\t"
                "s_waitcnt vmcnt(0)\n\t"
                "s_nop 7\n\t"
                "s_nop 3\n\t"
                : [a0] "=&v"(acc0), [a1] "=&v"(acc1), [t0] "=&s"(t0), [t1] "=&s"(t1)
                : [kb] "v"(kb), [ub] "v"(ub0 + c8 * CV_U), [go] "v"(go), [so] "s"(so), [ro] "s"(ro) : "memory", "scc", "v192", "v193", "v194", "v195", "v196", "v197", "v198", "v199", "v200", "v201", "v202", "v203", "v204", "v205", "v206", "v207", "v208", "v209", "v210", "v211", "v212", "v213", "v214", "v215", "v216", "v217", "v218", "v219", "v220", "v221", "v222", "v223", "v224", "v225", "v226", "v227", "v228", "v229", "v230", "v231", "v232", "v233", "v234", "v235", "v236", "v237", "v238", "v239", "v240", "v241", "v242", "v243", "v244", "v245", "v246", "v247", "v248", "v249", "v250", "v251", "v252", "v253", "v254", "v255", "a0", "a1", "a2", "a3", "a4", "a5", "a6", "a7", "a8", "a9", "a10", "a11", "a12", "a13", "a14", "a15", "a16", "a17", "a18", "a19", "a20", "a21", "a22", "a23", "a24", "a25", "a26", "a27", "a28", "a29", "a30", "a31", "a32", "a33", "a34", "a35", "a36", "a37", "a38", "a39", "a40", "a41", "a42", "a43", "a44", "a45", "a46", "a47", "a48", "a49", "a50", "a51", "a52", "a53", "a54", "a55", "a56", "a57", "a58", "a59", "a60", "a61", "a62", "a63", "a64", "a65", "a66", "a67", "a68", "a69", "a70", "a71", "a72", "a73", "a74", "a75", "a76", "a77", "a78", "a79", "a80", "a81", "a82", "a83", "a84", "a85", "a86", "a87", "a88", "a89", "a90", "a91", "a92", "a93", "a94", "a95", "a96", "a97", "a98", "a99", "a100", "a101", "a102", "a103", "a104", "a105", "a106", "a107", "a108", "a109", "a110", "a111", "a112", "a113", "a114", "a115", "a116", "a117", "a118", "a119", "a120", "a121", "a122", "a123", "a124", "a125", "a126", "a127", "a128", "a129", "a130", "a131", "a132", "a133", "a134", "a135", "a136", "a137", "a138", "a139", "a140", "a141", "a142", "a143", "a144", "a145", "a146", "a147", "a148", "a149", "a150", "a151", "a152", "a153", "a154", "a155", "a156", "a157", "a158", "a159", "a160", "a161", "a162", "a163", "a164", "a165", "a166", "a167", "a168", "a169", "a170", "a171", "a172", "a173", "a174", "a175", "a176", "a177", "a178", "a179", "a180", "a181", "a182", "a183", "a184", "a185", "a186", "a187", "a188", "a189", "a190", "a191", "a192", "a193", "a194", "a195", "a196", "a197", "a198", "a199", "a200", "a201", "a202", "a203", "a204", "a205", "a206", "a207", "a208", "a209", "a210", "a211", "a212", "a213", "a214", "a215", "a216", "a217", "a218", "a219", "a220", "a221", "a222", "a223", "a224", "a225", "a226", "a227", "a228", "a229", "a230", "a231", "a232", "a233", "a234", "a235", "a236", "a237", "a238", "a239", "a240", "a241", "a242", "a243", "a244", "a245", "a246", "a247", "a248", "a249", "a250", "a251", "a252", "a253", "a254", "a255");
            else asm volatile(
                "ds_read_b64 v[192:193], %[kb] offset:32\n\t"
                "ds_read_b64 v[194:195], %[kb] offset:40\n\t"
                "ds_read_b128 v[196:199], %[ub] offset:5040\n\t"
                "ds_read_b128 v[200:203], %[ub] offset:2480\n\t"
                "ds_read_b64 v[204:205], %[kb] offset:2080\n\t"
                "ds_read_b64 v[206:207], %[kb] offset:2088\n\t"
                "ds_read_b64 v[208:209], %[kb] offset:0\n\t"
                "ds_read_b64 v[210:211], %[kb] offset:8\n\t"
                "ds_read_b128 v[212:215], %[ub] offset:5072\n\t"
                "ds_read_b128 v[216:219], %[ub] offset:2512\n\t"
                "ds_read_b64 v[220:221], %[kb] offset:2048\n\t"
                "ds_read_b64 v[222:223], %[kb] offset:2056\n\t"
                "s_waitcnt lgkmcnt(9)\n\t"
                "v_mfma_f32_32x32x16_bf16 %[a1], v[192:195], v[196:199], 0\n\t"
                "ds_read_b64 v[224:225], %[kb] offset:96\n\t"
                "ds_read_b64 v[226:227], %[kb] offset:104\n\t"
                "ds_read_b128 v[228:231], %[ub] offset:4960\n\t"
                "s_waitcnt lgkmcnt(11)\n\t"
                "v_mfma_f32_32x32x16_bf16 %[a0], v[192:195], v[200:203], 0\n\t"
                "ds_read_b128 v[232:235], %[ub] offset:2400\n\t"
                "s_add_u32 %[t1], %[ro], 2\n\t"
                "s_and_b32 %[t1], %[t1], 7\n\t"
                "s_lshl_b32 %[t1], %[t1], 18\n\t"
                "s_add_u32 %[t0], %[t1], 0\n\t"
                "buffer_load_dwordx4 a[128:131], %[go], %[so], %[t0] offen\n\t"
                "s_waitcnt lgkmcnt(10)\n\t"
                "v_mfma_f32_32x32x16_bf16 %[a1], v[204:207], v[200:203], %[a1]\n\t"
                "ds_read_b64 v[236:237], %[kb] offset:2144\n\t"
                "ds_read_b64 v[238:239], %[kb] offset:2152\n\t"
                "s_waitcnt lgkmcnt(9)\n\t"
                "v_mfma_f32_32x32x16_bf16 %[a1], v[208:211], v[212:215], %[a1]\n\t"
                "ds_read_b64 v[240:241], %[kb] offset:64\n\t"
                "ds_read_b64 v[242:243], %[kb] offset:72\n\t"
                "ds_read_b128 v[244:247], %[ub] offset:4992\n\t"
                "s_waitcnt lgkmcnt(11)\n\t"
                "v_mfma_f32_32x32x16_bf16 %[a0], v[208:211], v[216:219], %[a0]\n\t"
                "ds_read_b128 v[248:251], %[ub] offset:2432\n\t"
                "s_waitcnt lgkmcnt(10)\n\t"
                "v_mfma_f32_32x32x16_bf16 %[a1], v[220:223], v[216:219], %[a1]\n\t"
                "ds_read_b64 v[252:253], %[kb] offset:2112\n\t"
                "ds_read_b64 v[254:255], %[kb] offset:2120\n\t"
                "s_waitcnt lgkmcnt(9)\n\t"
                "v_mfma_f32_32x32x16_bf16 %[a1], v[224:227], v[228:231], %[a1]\n\t"
                "ds_read_b64 v[192:193], %[kb] offset:160\n\t"
                "ds_read_b64 v[194:195], %[kb] offset:168\n\t"
                "ds_read_b128 v[196:199], %[ub] offset:4880\n\t"
                "s_waitcnt lgkmcnt(11)\n\t"
                "v_mfma_f32_32x32x16_bf16 %[a0], v[224:227], v[232:235], %[a0]\n\t"
                "ds_read_b128 v[200:203], %[ub] offset:2320\n\t"
                "s_add_u32 %[t0], %[t1], 16384\n\t"
                "buffer_load_dwordx4 a[132:135], %[go], %[so], %[t0] offen\n\t"
                "s_waitcnt lgkmcnt(10)\n\t"
                "v_mfma_f32_32x32x16_bf16 %[a1], v[236:239], v[232:235], %[a1]\n\t"
                "ds_read_b64 v[204:205], %[kb] offset:2208\n\t"
                "ds_read_b64 v[206:207], %[kb] offset:2216\n\t"
                "s_waitcnt lgkmcnt(9)\n\t"
                "v_mfma_f32_32x32x16_bf16 %[a1], v[240:243], v[244:247], %[a1]\n\t"
                "ds_read_b64 v[208:209], %[kb] offset:128\n\t"
                "ds_read_b64 v[210:211], %[kb] offset:136\n\t"
                "ds_read_b128 v[212:215], %[ub] offset:4912\n\t"
                "s_waitcnt lgkmcnt(11)\n\t"
                "v_mfma_f32_32x32x16_bf16 %[a0], v[240:243], v[248:251], %[a0]\n\t"
                "ds_read_b128 v[216:219], %[ub] offset:2352\n\t"
                "s_waitcnt lgkmcnt(10)\n\t"
                "v_mfma_f32_32x32x16_bf16 %[a1], v[252:255], v[248:251], %[a1]\n\t"
                "ds_read_b64 v[220:221], %[kb] offset:2176\n\t"
                "ds_read_b64 v[222:223], %[kb] offset:2184\n\t"
                "s_waitcnt lgkmcnt(9)\n\t"
                "v_mfma_f32_32x32x16_bf16 %[a1], v[192:195], v[196:199], %[a1]\n\t"
                "ds_read_b64 v[224:225], %[kb] offset:224\n\t"
                "ds_read_b64 v[226:227], %[kb] offset:232\n\t"
                "ds_read_b128 v[228:231], %[ub] offset:4800\n\t"
                "s_waitcnt lgkmcnt(11)\n\t"
                "v_mfma_f32_32x32x16_bf16 %[a0], v[192:195], v[200:203], %[a0]\n\t"
                "ds_read_b128 v[232:235], %[ub] offset:2240\n\t"
                "s_add_u32 %[t0], %[t1], 32768\n\t"
                "buffer_load_dwordx4 a[136:139], %[go], %[so], %[t0] offen\n\t"
                "s_waitcnt lgkmcnt(10)\n\t"
                "v_mfma_f32_32x32x16_bf16 %[a1], v[204:207], v[200:203], %[a1]\n\t"
                "ds_read_b64 v[236:237], %[kb] offset:2272\n\t"
                "ds_read_b64 v[238:239], %[kb] offset:2280\n\t"
                "s_waitcnt lgkmcnt(9)\n\t"
                "v_mfma_f32_32x32x16_bf16 %[a1], v[208:211], v[212:215], %[a1]\n\t"
                "ds_read_b64 v[240:241], %[kb] offset:192\n\t"
                "ds_read_b64 v[242:243], %[kb] offset:200\n\t"
                "ds_read_b128 v[244:247], %[ub] offset:4832\n\t"
                "s_waitcnt lgkmcnt(11)\n\t"
                "v_mfma_f32_32x32x16_bf16 %[a0], v[208:211], v[216:219], %[a0]\n\t"
                "ds_read_b128 v[248:251], %[ub] offset:2272\n\t"
                "s_waitcnt lgkmcnt(10)\n\t"
                "v_mfma_f32_32x32x16_bf16 %[a1], v[220:223], v[216:219], %[a1]\n\t"
                "ds_read_b64 v[252:253], %[kb] offset:2240\n\t"
                "ds_read_b64 v[254:255], %[kb] offset:2248\n\t"
                "s_waitcnt lgkmcnt(9)\n\t"
                "v_mfma_f32_32x32x16_bf16 %[a1], v[224:227], v[228:231], %[a1]\n\t"
                "ds_read_b64 v[192:193], %[kb] offset:288\n\t"
                "ds_read_b64 v[194:195], %[kb] offset:296\n\t"
                "ds_read_b128 v[196:199], %[ub] offset:4720\n\t"
                "s_waitcnt lgkmcnt(11)\n\t"
                "v_mfma_f32_32x32x16_bf16 %[a0], v[224:227], v[232:235], %[a0]\n\t"
                "ds_read_b128 v[200:203], %[ub] offset:2160\n\t"
                "s_add_u32 %[t0], %[t1], 49152\n\t"
                "buffer_load_dwordx4 a[140:143], %[go], %[so], %[t0] offen\n\t"
                "s_waitcnt lgkmcnt(10)\n\t"
                "v_mfma_f32_32x32x16_bf16 %[a1], v[236:239], v[232:235], %[a1]\n\t"
                "ds_read_b64 v[204:205], %[kb] offset:2336\n\t"
                "ds_read_b64 v[206:207], %[kb] offset:2344\n\t"
                "s_waitcnt lgkmcnt(9)\n\t"
                "v_mfma_f32_32x32x16_bf16 %[a1], v[240:243], v[244:247], %[a1]\n\t"
                "ds_read_b64 v[208:209], %[kb] offset:256\n\t"
                "ds_read_b64 v[210:211], %[kb] offset:264\n\t"
                "ds_read_b128 v[212:215], %[ub] offset:4752\n\t"
                "s_waitcnt lgkmcnt(11)\n\t"
                "v_mfma_f32_32x32x16_bf16 %[a0], v[240:243], v[248:251], %[a0]\n\t"
                "ds_read_b128 v[216:219], %[ub] offset:2192\n\t"
                "s_add_u32 %[t0], %[t1], 65536\n\t"
                "buffer_load_dwordx4 a[144:147], %[go], %[so], %[t0] offen\n\t"
                "s_waitcnt lgkmcnt(10)\n\t"
                "v_mfma_f32_32x32x16_bf16 %[a1], v[252:255], v[248:251], %[a1]\n\t"
                "ds_read_b64 v[220:221], %[kb] offset:2304\n\t"
                "ds_read_b64 v[222:223], %[kb] offset:2312\n\t"
                "s_waitcnt lgkmcnt(9)\n\t"
                "v_mfma_f32_32x32x16_bf16 %[a1], v[192:195], v[196:199], %[a1]\n\t"
                "ds_read_b64 v[224:225], %[kb] offset:352\n\t"
                "ds_read_b64 v[226:227], %[kb] offset:360\n\t"
                "ds_read_b128 v[228:231], %[ub] offset:4640\n\t"
                "s_waitcnt lgkmcnt(11)\n\t"
                "v_mfma_f32_32x32x16_bf16 %[a0], v[192:195], v[200:203], %[a0]\n\t"
                "ds_read_b128 v[232:235], %[ub] offset:2080\n\t"
                "s_waitcnt lgkmcnt(10)\n\t"
                "v_mfma_f32_32x32x16_bf16 %[a1], v[204:207], v[200:203], %[a1]\n\t"
                "ds_read_b64 v[236:237], %[kb] offset:2400\n\t"
                "ds_read_b64 v[238:239], %[kb] offset:2408\n\t"
                "s_waitcnt lgkmcnt(9)\n\t"
                "v_mfma_f32_32x32x16_bf16 %[a1], v[208:211], v[212:215], %[a1]\n\t"
                "ds_read_b64 v[240:241], %[kb] offset:320\n\t"
                "ds_read_b64 v[242:243], %[kb] offset:328\n\t"
                "ds_read_b128 v[244:247], %[ub] offset:4672\n\t"
                "s_waitcnt lgkmcnt(11)\n\t"
                "v_mfma_f32_32x32x16_bf16 %[a0], v[208:211], v[216:219], %[a0]\n\t"
                "ds_read_b128 v[248:251], %[ub] offset:2112\n\t"
                "s_add_u32 %[t0], %[t1], 81920\n\t"
                "buffer_load_dwordx4 a[148:151], %[go], %[so], %[t0] offen\n\t"
                "s_waitcnt lgkmcnt(10)\n\t"
                "v_mfma_f32_32x32x16_bf16 %[a1], v[220:223], v[216:219], %[a1]\n\t"
                "ds_read_b64 v[252:253], %[kb] offset:2368\n\t"
                "ds_read_b64 v[254:255], %[kb] offset:2376\n\t"
                "s_waitcnt lgkmcnt(9)\n\t"
                "v_mfma_f32_32x32x16_bf16 %[a1], v[224:227], v[228:231], %[a1]\n\t"
                "ds_read_b64 v[192:193], %[kb] offset:416\n\t"
                "ds_read_b64 v[194:195], %[kb] offset:424\n\t"
                "ds_read_b128 v[196:199], %[ub] offset:4560\n\t"
                "s_waitcnt lgkmcnt(11)\n\t"
                "v_mfma_f32_32x32x16_bf16 %[a0], v[224:227], v[232:235], %[a0]\n\t"
                "ds_read_b128 v[200:203], %[ub] offset:2000\n\t"
                "s_waitcnt lgkmcnt(10)\n\t"
                "v_mfma_f32_32x32x16_bf16 %[a1], v[236:239], v[232:235], %[a1]\n\t"
                "ds_read_b64 v[204:205], %[kb] offset:2464\n\t"
                "ds_read_b64 v[206:207], %[kb] offset:2472\n\t"
                "s_waitcnt lgkmcnt(9)\n\t"
                "v_mfma_f32_32x32x16_bf16 %[a1], v[240:243], v[244:247], %[a1]\n\t"
                "ds_read_b64 v[208:209], %[kb] offset:384\n\t"
                "ds_read_b64 v[210:211], %[kb] offset:392\n\t"
                "ds_read_b128 v[212:215], %[ub] offset:4592\n\t"
                "s_waitcnt lgkmcnt(11)\n\t"
                "v_mfma_f32_32x32x16_bf16 %[a0], v[240:243], v[248:251], %[a0]\n\t"
                "ds_read_b128 v[216:219], %[ub] offset:2032\n\t"
                "s_add_u32 %[t0], %[t1], 98304\n\t"
                "buffer_load_dwordx4 a[152:155], %[go], %[so], %[t0] offen\n\t"
                "s_waitcnt lgkmcnt(10)\n\t"
                "v_mfma_f32_32x32x16_bf16 %[a1], v[252:255], v[248:251], %[a1]\n\t"
                "ds_read_b64 v[220:221], %[kb] offset:2432\n\t"
                "ds_read_b64 v[222:223], %[kb] offset:2440\n\t"
                "s_waitcnt lgkmcnt(9)\n\t"
                "v_mfma_f32_32x32x16_bf16 %[a1], v[192:195], v[196:199], %[a1]\n\t"
                "ds_read_b64 v[224:225], %[kb] offset:480\n\t"
                "ds_read_b64 v[226:227], %[kb] offset:488\n\t"
                "ds_read_b128 v[228:231], %[ub] offset:4480\n\t"
                "s_waitcnt lgkmcnt(11)\n\t"
                "v_mfma_f32_32x32x16_bf16 %[a0], v[192:195], v[200:203], %[a0]\n\t"
                "ds_read_b128 v[232:235], %[ub] offset:1920\n\t"
                "s_waitcnt lgkmcnt(10)\n\t"
                "v_mfma_f32_32x32x16_bf16 %[a1], v[204:207], v[200:203], %[a1]\n\t"
                "ds_read_b64 v[236:237], %[kb] offset:2528\n\t"
                "ds_read_b64 v[238:239], %[kb] offset:2536\n\t"
                "s_waitcnt lgkmcnt(9)\n\t"
                "v_mfma_f32_32x32x16_bf16 %[a1], v[208:211], v[212:215], %[a1]\n\t"
                "ds_read_b64 v[240:241], %[kb] offset:448\n\t"
                "ds_read_b64 v[242:243], %[kb] offset:456\n\t"
                "ds_read_b128 v[244:247], %[ub] offset:4512\n\t"
                "s_waitcnt lgkmcnt(11)\n\t"
                "v_mfma_f32_32x32x16_bf16 %[a0], v[208:211], v[216:219], %[a0]\n\t"
                "ds_read_b128 v[248:251], %[ub] offset:1952\n\t"
                "s_add_u32 %[t0], %[t1], 114688\n\t"
                "buffer_load_dwordx4 a[156:159], %[go], %[so], %[t0] offen\n\t"
                "s_waitcnt lgkmcnt(10)\n\t"
                "v_mfma_f32_32x32x16_bf16 %[a1], v[220:223], v[216:219], %[a1]\n\t"
                "ds_read_b64 v[252:253], %[kb] offset:2496\n\t"
                "ds_read_b64 v[254:255], %[kb] offset:2504\n\t"
                "s_waitcnt lgkmcnt(9)\n\t"
                "v_mfma_f32_32x32x16_bf16 %[a1], v[224:227], v[228:231], %[a1]\n\t"
                "ds_read_b64 v[192:193], %[kb] offset:544\n\t"
                "ds_read_b64 v[194:195], %[kb] offset:552\n\t"
                "ds_read_b128 v[196:199], %[ub] offset:4400\n\t"
                "s_waitcnt lgkmcnt(11)\n\t"
                "v_mfma_f32_32x32x16_bf16 %[a0], v[224:227], v[232:235], %[a0]\n\t"
                "ds_read_b128 v[200:203], %[ub] offset:1840\n\t"
                "s_add_u32 %[t1], %[ro], 2\n\t"
                "s_and_b32 %[t1], %[t1], 7\n\t"
                "s_lshl_b32 %[t1], %[t1], 18\n\t"
                "s_add_u32 %[t0], %[t1], 131072\n\t"
                "buffer_load_dwordx4 a[160:163], %[go], %[so], %[t0] offen\n\t"
                "s_waitcnt lgkmcnt(10)\n\t"
                "v_mfma_f32_32x32x16_bf16 %[a1], v[236:239], v[232:235], %[a1]\n\t"
                "ds_read_b64 v[204:205], %[kb] offset:2592\n\t"
                "ds_read_b64 v[206:207], %[kb] offset:2600\n\t"
                "s_waitcnt lgkmcnt(9)\n\t"
                "v_mfma_f32_32x32x16_bf16 %[a1], v[240:243], v[244:247], %[a1]\n\t"
                "ds_read_b64 v[208:209], %[kb] offset:512\n\t"
                "ds_read_b64 v[210:211], %[kb] offset:520\n\t"
                "ds_read_b128 v[212:215], %[ub] offset:4432\n\t"
                "s_waitcnt lgkmcnt(11)\n\t"
                "v_mfma_f32_32x32x16_bf16 %[a0], v[240:243], v[248:251], %[a0]\n\t"
                "ds_read_b128 v[216:219], %[ub] offset:1872\n\t"
                "s_waitcnt lgkmcnt(10)\n\t"
                "v_mfma_f32_32x32x16_bf16 %[a1], v[252:255], v[248:251], %[a1]\n\t"
                "ds_read_b64 v[220:221], %[kb] offset:2560\n\t"
                "ds_read_b64 v[222:223], %[kb] offset:2568\n\t"
                "s_waitcnt lgkmcnt(9)\n\t"
                "v_mfma_f32_32x32x16_bf16 %[a1], v[192:195], v[196:199], %[a1]\n\t"
                "ds_read_b64 v[224:225], %[kb] offset:608\n\t"
                "ds_read_b64 v[226:227], %[kb] offset:616\n\t"
                "ds_read_b128 v[228:231], %[ub] offset:4320\n\t"
                "s_waitcnt lgkmcnt(11)\n\t"
                "v_mfma_f32_32x32x16_bf16 %[a0], v[192:195], v[200:203], %[a0]\n\t"
                "ds_read_b128 v[232:235], %[ub] offset:1760\n\t"
                "s_add_u32 %[t0], %[t1], 147456\n\t"
                "buffer_load_dwordx4 a[164:167], %[go], %[so], %[t0] offen\n\t"
                "s_waitcnt lgkmcnt(10)\n\t"
                "v_mfma_f32_32x32x16_bf16 %[a1], v[204:207], v[200:203], %[a1]\n\t"
                "ds_read_b64 v[236:237], %[kb] offset:2656\n\t"
                "ds_read_b64 v[238:239], %[kb] offset:2664\n\t"
                "s_waitcnt lgkmcnt(9)\n\t"
                "v_mfma_f32_32x32x16_bf16 %[a1], v[208:211], v[212:215], %[a1]\n\t"
                "ds_read_b64 v[240:241], %[kb] offset:576\n\t"
                "ds_read_b64 v[242:243], %[kb] offset:584\n\t"
                "ds_read_b128 v[244:247], %[ub] offset:4352\n\t"
                "s_waitcnt lgkmcnt(11)\n\t"
                "v_mfma_f32_32x32x16_bf16 %[a0], v[208:211], v[216:219], %[a0]\n\t"
                "ds_read_b128 v[248:251], %[ub] offset:1792\n\t"
                "s_waitcnt lgkmcnt(10)\n\t"
                "v_mfma_f32_32x32x16_bf16 %[a1], v[220:223], v[216:219], %[a1]\n\t"
                "ds_read_b64 v[252:253], %[kb] offset:2624\n\t"
                "ds_read_b64 v[254:255], %[kb] offset:2632\n\t"
                "s_waitcnt lgkmcnt(9)\n\t"
                "v_mfma_f32_32x32x16_bf16 %[a1], v[224:227], v[228:231], %[a1]\n\t"
                "ds_read_b64 v[192:193], %[kb] offset:672\n\t"
                "ds_read_b64 v[194:195], %[kb] offset:680\n\t"
                "ds_read_b128 v[196:199], %[ub] offset:4240\n\t"
                "s_waitcnt lgkmcnt(11)\n\t"
                "v_mfma_f32_32x32x16_bf16 %[a0], v[224:227], v[232:235], %[a0]\n\t"
                "ds_read_b128 v[200:203], %[ub] offset:1680\n\t"
                "s_add_u32 %[t0], %[t1], 163840\n\t"
                "buffer_load_dwordx4 a[168:171], %[go], %[so], %[t0] offen\n\t"
                "s_waitcnt lgkmcnt(10)\n\t"
                "v_mfma_f32_32x32x16_bf16 %[a1], v[236:239], v[232:235], %[a1]\n\t"
                "ds_read_b64 v[204:205], %[kb] offset:2720\n\t"
                "ds_read_b64 v[206:207], %[kb] offset:2728\n\t"
                "s_waitcnt lgkmcnt(9)\n\t"
                "v_mfma_f32_32x32x16_bf16 %[a1], v[240:243], v[244:247], %[a1]\n\t"
                "ds_read_b64 v[208:209], %[kb] offset:640\n\t"
                "ds_read_b64 v[210:211], %[kb] offset:648\n\t"
                "ds_read_b128 v[212:215], %[ub] offset:4272\n\t"
                "s_waitcnt lgkmcnt(11)\n\t"
                "v_mfma_f32_32x32x16_bf16 %[a0], v[240:243], v[248:251], %[a0]\n\t"
                "ds_read_b128 v[216:219], %[ub] offset:1712\n\t"
                "s_waitcnt lgkmcnt(10)\n\t"
                "v_mfma_f32_32x32x16_bf16 %[a1], v[252:255], v[248:251], %[a1]\n\t"
                "ds_read_b64 v[220:221], %[kb] offset:2688\n\t"
                "ds_read_b64 v[222:223], %[kb] offset:2696\n\t"
                "s_waitcnt lgkmcnt(9)\n\t"
                "v_mfma_f32_32x32x16_bf16 %[a1], v[192:195], v[196:199], %[a1]\n\t"
                "ds_read_b64 v[224:225], %[kb] offset:736\n\t"
                "ds_read_b64 v[226:227], %[kb] offset:744\n\t"
                "ds_read_b128 v[228:231], %[ub] offset:4160\n\t"
                "s_waitcnt lgkmcnt(11)\n\t"
                "v_mfma_f32_32x32x16_bf16 %[a0], v[192:195], v[200:203], %[a0]\n\t"
                "ds_read_b128 v[232:235], %[ub] offset:1600\n\t"
                "s_add_u32 %[t0], %[t1], 180224\n\t"
                "buffer_load_dwordx4 a[172:175], %[go], %[so], %[t0] offen\n\t"
                "s_waitcnt lgkmcnt(10)\n\t"
                "v_mfma_f32_32x32x16_bf16 %[a1], v[204:207], v[200:203], %[a1]\n\t"
                "ds_read_b64 v[236:237], %[kb] offset:2784\n\t"
                "ds_read_b64 v[238:239], %[kb] offset:2792\n\t"
                "s_waitcnt lgkmcnt(9)\n\t"
                "v_mfma_f32_32x32x16_bf16 %[a1], v[208:211], v[212:215], %[a1]\n\t"
                "ds_read_b64 v[240:241], %[kb] offset:704\n\t"
                "ds_read_b64 v[242:243], %[kb] offset:712\n\t"
                "ds_read_b128 v[244:247], %[ub] offset:4192\n\t"
                "s_waitcnt lgkmcnt(11)\n\t"
                "v_mfma_f32_32x32x16_bf16 %[a0], v[208:211], v[216:219], %[a0]\n\t"
                "ds_read_b128 v[248:251], %[ub] offset:1632\n\t"
                "s_add_u32 %[t0], %[t1], 196608\n\t"
                "buffer_load_dwordx4 a[176:179], %[go], %[so], %[t0] offen\n\t"
                "s_waitcnt lgkmcnt(10)\n\t"
                "v_mfma_f32_32x32x16_bf16 %[a1], v[220:223], v[216:219], %[a1]\n\t"
                "ds_read_b64 v[252:253], %[kb] offset:2752\n\t"
                "ds_read_b64 v[254:255], %[kb] offset:2760\n\t"
                "s_waitcnt lgkmcnt(9)\n\t"
                "v_mfma_f32_32x32x16_bf16 %[a1], v[224:227], v[228:231], %[a1]\n\t"
                "ds_read_b64 v[192:193], %[kb] offset:800\n\t"
                "ds_read_b64 v[194:195], %[kb] offset:808\n\t"
                "ds_read_b128 v[196:199], %[ub] offset:4080\n\t"
                "s_waitcnt lgkmcnt(11)\n\t"
                "v_mfma_f32_32x32x16_bf16 %[a0], v[224:227], v[232:235], %[a0]\n\t"
                "ds_read_b128 v[200:203], %[ub] offset:1520\n\t"
                "s_waitcnt lgkmcnt(10)\n\t"
                "v_mfma_f32_32x32x16_bf16 %[a1], v[236:239], v[232:235], %[a1]\n\t"
                "ds_read_b64 v[204:205], %[kb] offset:2848\n\t"
                "ds_read_b64 v[206:207], %[kb] offset:2856\n\t"
                "s_waitcnt lgkmcnt(9)\n\t"
                "v_mfma_f32_32x32x16_bf16 %[a1], v[240:243], v[244:247], %[a1]\n\t"
                "ds_read_b64 v[208:209], %[kb] offset:768\n\t"
                "ds_read_b64 v[210:211], %[kb] offset:776\n\t"
                "ds_read_b128 v[212:215], %[ub] offset:4112\n\t"
                "s_waitcnt lgkmcnt(11)\n\t"
                "v_mfma_f32_32x32x16_bf16 %[a0], v[240:243], v[248:251], %[a0]\n\t"
                "ds_read_b128 v[216:219], %[ub] offset:1552\n\t"
                "s_add_u32 %[t0], %[t1], 212992\n\t"
                "buffer_load_dwordx4 a[180:183], %[go], %[so], %[t0] offen\n\t"
                "s_waitcnt lgkmcnt(10)\n\t"
                "v_mfma_f32_32x32x16_bf16 %[a1], v[252:255], v[248:251], %[a1]\n\t"
                "ds_read_b64 v[220:221], %[kb] offset:2816\n\t"
                "ds_read_b64 v[222:223], %[kb] offset:2824\n\t"
                "s_waitcnt lgkmcnt(9)\n\t"
                "v_mfma_f32_32x32x16_bf16 %[a1], v[192:195], v[196:199], %[a1]\n\t"
                "ds_read_b64 v[224:225], %[kb] offset:864\n\t"
                "ds_read_b64 v[226:227], %[kb] offset:872\n\t"
                "ds_read_b128 v[228:231], %[ub] offset:4000\n\t"
                "s_waitcnt lgkmcnt(11)\n\t"
                "v_mfma_f32_32x32x16_bf16 %[a0], v[192:195], v[200:203], %[a0]\n\t"
                "ds_read_b128 v[232:235], %[ub] offset:1440\n\t"
                "s_waitcnt lgkmcnt(10)\n\t"
                "v_mfma_f32_32x32x16_bf16 %[a1], v[204:207], v[200:203], %[a1]\n\t"
                "ds_read_b64 v[236:237], %[kb] offset:2912\n\t"
                "ds_read_b64 v[238:239], %[kb] offset:2920\n\t"
                "s_waitcnt lgkmcnt(9)\n\t"
                "v_mfma_f32_32x32x16_bf16 %[a1], v[208:211], v[212:215], %[a1]\n\t"
                "ds_read_b64 v[240:241], %[kb] offset:832\n\t"
                "ds_read_b64 v[242:243], %[kb] offset:840\n\t"
                "ds_read_b128 v[244:247], %[ub] offset:4032\n\t"
                "s_waitcnt lgkmcnt(11)\n\t"
                "v_mfma_f32_32x32x16_bf16 %[a0], v[208:211], v[216:219], %[a0]\n\t"
                "ds_read_b128 v[248:251], %[ub] offset:1472\n\t"
                "s_add_u32 %[t0], %[t1], 229376\n\t"
                "buffer_load_dwordx4 a[184:187], %[go], %[so], %[t0] offen\n\t"
                "s_waitcnt lgkmcnt(10)\n\t"
                "v_mfma_f32_32x32x16_bf16 %[a1], v[220:223], v[216:219], %[a1]\n\t"
                "ds_read_b64 v[252:253], %[kb] offset:2880\n\t"
                "ds_read_b64 v[254:255], %[kb] offset:2888\n\t"
                "s_waitcnt lgkmcnt(9)\n\t"
                "v_mfma_f32_32x32x16_bf16 %[a1], v[224:227], v[228:231], %[a1]\n\t"
                "ds_read_b64 v[192:193], %[kb] offset:928\n\t"
                "ds_read_b64 v[194:195], %[kb] offset:936\n\t"
                "ds_read_b128 v[196:199], %[ub] offset:3920\n\t"
                "s_waitcnt lgkmcnt(11)\n\t"
                "v_mfma_f32_32x32x16_bf16 %[a0], v[224:227], v[232:235], %[a0]\n\t"
                "ds_read_b128 v[200:203], %[ub] offset:1360\n\t"
                "s_waitcnt lgkmcnt(10)\n\t"
                "v_mfma_f32_32x32x16_bf16 %[a1], v[236:239], v[232:235], %[a1]\n\t"
                "ds_read_b64 v[204:205], %[kb] offset:2976\n\t"
                "ds_read_b64 v[206:207], %[kb] offset:2984\n\t"
                "s_waitcnt lgkmcnt(9)\n\t"
                "v_mfma_f32_32x32x16_bf16 %[a1], v[240:243], v[244:247], %[a1]\n\t"
                "ds_read_b64 v[208:209], %[kb] offset:896\n\t"
                "ds_read_b64 v[210:211], %[kb] offset:904\n\t"
                "ds_read_b128 v[212:215], %[ub] offset:3952\n\t"
                "s_waitcnt lgkmcnt(11)\n\t"
                "v_mfma_f32_32x32x16_bf16 %[a0], v[240:243], v[248:251], %[a0]\n\t"
                "ds_read_b128 v[216:219], %[ub] offset:1392\n\t"
                "s_add_u32 %[t0], %[t1], 245760\n\t"
                "buffer_load_dwordx4 a[188:191], %[go], %[so], %[t0] offen\n\t"
                "s_waitcnt lgkmcnt(10)\n\t"
                "v_mfma_f32_32x32x16_bf16 %[a1], v[252:255], v[248:251], %[a1]\n\t"
                "ds_read_b64 v[220:221], %[kb] offset:2944\n\t"
                "ds_read_b64 v[222:223], %[kb] offset:2952\n\t"
                "s_waitcnt lgkmcnt(9)\n\t"
                "v_mfma_f32_32x32x16_bf16 %[a1], v[192:195], v[196:199], %[a1]\n\t"
                "ds_read_b64 v[224:225], %[kb] offset:992\n\t"
                "ds_read_b64 v[226:227], %[kb] offset:1000\n\t"
                "ds_read_b128 v[228:231], %[ub] offset:3840\n\t"
                "s_waitcnt lgkmcnt(11)\n\t"
                "v_mfma_f32_32x32x16_bf16 %[a0], v[192:195], v[200:203], %[a0]\n\t"
                "ds_read_b128 v[232:235], %[ub] offset:1280\n\t"
                "s_add_u32 %[t1], %[ro], 3\n\t"
                "s_and_b32 %[t1], %[t1], 7\n\t"
                "s_lshl_b32 %[t1], %[t1], 18\n\t"
                "s_add_u32 %[t0], %[t1], 0\n\t"
                "buffer_load_dwordx4 a[192:195], %[go], %[so], %[t0] offen\n\t"
                "s_waitcnt lgkmcnt(10)\n\t"
                "v_mfma_f32_32x32x16_bf16 %[a1], v[204:207], v[200:203], %[a1]\n\t"
                "ds_read_b64 v[236:237], %[kb] offset:3040\n\t"
                "ds_read_b64 v[238:239], %[kb] offset:3048\n\t"
                "s_waitcnt lgkmcnt(9)\n\t"
                "v_mfma_f32_32x32x16_bf16 %[a1], v[208:211], v[212:215], %[a1]\n\t"
                "ds_read_b64 v[240:241], %[kb] offset:960\n\t"
                "ds_read_b64 v[242:243], %[kb] offset:968\n\t"
                "ds_read_b128 v[244:247], %[ub] offset:3872\n\t"
                "s_waitcnt lgkmcnt(11)\n\t"
                "v_mfma_f32_32x32x16_bf16 %[a0], v[208:211], v[216:219], %[a0]\n\t"
                "ds_read_b128 v[248:251], %[ub] offset:1312\n\t"
                "s_waitcnt lgkmcnt(10)\n\t"
                "v_mfma_f32_32x32x16_bf16 %[a1], v[220:223], v[216:219], %[a1]\n\t"
                "ds_read_b64 v[252:253], %[kb] offset:3008\n\t"
                "ds_read_b64 v[254:255], %[kb] offset:3016\n\t"
                "s_waitcnt lgkmcnt(9)\n\t"
                "v_mfma_f32_32x32x16_bf16 %[a1], v[224:227], v[228:231], %[a1]\n\t"
                "ds_read_b64 v[192:193], %[kb] offset:1056\n\t"
                "ds_read_b64 v[194:195], %[kb] offset:1064\n\t"
                "ds_read_b128 v[196:199], %[ub] offset:3760\n\t"
                "s_waitcnt lgkmcnt(11)\n\t"
                "v_mfma_f32_32x32x16_bf16 %[a0], v[224:227], v[232:235], %[a0]\n\t"
                "ds_read_b128 v[200:203], %[ub] offset:1200\n\t"
                "s_add_u32 %[t0], %[t1], 16384\n\t"
                "buffer_load_dwordx4 a[196:199], %[go], %[so], %[t0] offen\n\t"
                "s_waitcnt lgkmcnt(10)\n\t"
                "v_mfma_f32_32x32x16_bf16 %[a1], v[236:239], v[232:235], %[a1]\n\t"
                "ds_read_b64 v[204:205], %[kb] offset:3104\n\t"
                "ds_read_b64 v[206:207], %[kb] offset:3112\n\t"
                "s_waitcnt lgkmcnt(9)\n\t"
                "v_mfma_f32_32x32x16_bf16 %[a1], v[240:243], v[244:247], %[a1]\n\t"
                "ds_read_b64 v[208:209], %[kb] offset:1024\n\t"
                "ds_read_b64 v[210:211], %[kb] offset:1032\n\t"
                "ds_read_b128 v[212:215], %[ub] offset:3792\n\t"
                "s_waitcnt lgkmcnt(11)\n\t"
                "v_mfma_f32_32x32x16_bf16 %[a0], v[240:243], v[248:251], %[a0]\n\t"
                "ds_read_b128 v[216:219], %[ub] offset:1232\n\t"
                "s_waitcnt lgkmcnt(10)\n\t"
                "v_mfma_f32_32x32x16_bf16 %[a1], v[252:255], v[248:251], %[a1]\n\t"
                "ds_read_b64 v[220:221], %[kb] offset:3072\n\t"
                "ds_read_b64 v[222:223], %[kb] offset:3080\n\t"
                "s_waitcnt lgkmcnt(9)\n\t"
                "v_mfma_f32_32x32x16_bf16 %[a1], v[192:195], v[196:199], %[a1]\n\t"
                "ds_read_b64 v[224:225], %[kb] offset:1120\n\t"
                "ds_read_b64 v[226:227], %[kb] offset:1128\n\t"
                "ds_read_b128 v[228:231], %[ub] offset:3680\n\t"
                "s_waitcnt lgkmcnt(11)\n\t"
                "v_mfma_f32_32x32x16_bf16 %[a0], v[192:195], v[200:203], %[a0]\n\t"
                "ds_read_b128 v[232:235], %[ub] offset:1120\n\t"
                "s_add_u32 %[t0], %[t1], 32768\n\t"
                "buffer_load_dwordx4 a[200:203], %[go], %[so], %[t0] offen\n\t"
                "s_waitcnt lgkmcnt(10)\n\t"
                "v_mfma_f32_32x32x16_bf16 %[a1], v[204:207], v[200:203], %[a1]\n\t"
                "ds_read_b64 v[236:237], %[kb] offset:3168\n\t"
                "ds_read_b64 v[238:239], %[kb] offset:3176\n\t"
                "s_waitcnt lgkmcnt(9)\n\t"
                "v_mfma_f32_32x32x16_bf16 %[a1], v[208:211], v[212:215], %[a1]\n\t"
                "ds_read_b64 v[240:241], %[kb] offset:1088\n\t"
                "ds_read_b64 v[242:243], %[kb] offset:1096\n\t"
                "ds_read_b128 v[244:247], %[ub] offset:3712\n\t"
                "s_waitcnt lgkmcnt(11)\n\t"
                "v_mfma_f32_32x32x16_bf16 %[a0], v[208:211], v[216:219], %[a0]\n\t"
                "ds_read_b128 v[248:251], %[ub] offset:1152\n\t"
                "s_waitcnt lgkmcnt(10)\n\t"
                "v_mfma_f32_32x32x16_bf16 %[a1], v[220:223], v[216:219], %[a1]\n\t"
                "ds_read_b64 v[252:253], %[kb] offset:3136\n\t"
                "ds_read_b64 v[254:255], %[kb] offset:3144\n\t"
                "s_waitcnt lgkmcnt(9)\n\t"
                "v_mfma_f32_32x32x16_bf16 %[a1], v[224:227], v[228:231], %[a1]\n\t"
                "ds_read_b64 v[192:193], %[kb] offset:1184\n\t"
                "ds_read_b64 v[194:195], %[kb] offset:1192\n\t"
                "ds_read_b128 v[196:199], %[ub] offset:3600\n\t"
                "s_waitcnt lgkmcnt(11)\n\t"
                "v_mfma_f32_32x32x16_bf16 %[a0], v[224:227], v[232:235], %[a0]\n\t"
                "ds_read_b128 v[200:203], %[ub] offset:1040\n\t"
                "s_add_u32 %[t0], %[t1], 49152\n\t"
                "buffer_load_dwordx4 a[204:207], %[go], %[so], %[t0] offen\n\t"
                "s_waitcnt lgkmcnt(10)\n\t"
                "v_mfma_f32_32x32x16_bf16 %[a1], v[236:239], v[232:235], %[a1]\n\t"
                "ds_read_b64 v[204:205], %[kb] offset:3232\n\t"
                "ds_read_b64 v[206:207], %[kb] offset:3240\n\t"
                "s_waitcnt lgkmcnt(9)\n\t"
                "v_mfma_f32_32x32x16_bf16 %[a1], v[240:243], v[244:247], %[a1]\n\t"
                "ds_read_b64 v[208:209], %[kb] offset:1152\n\t"
                "ds_read_b64 v[210:211], %[kb] offset:1160\n\t"
                "ds_read_b128 v[212:215], %[ub] offset:3632\n\t"
                "s_waitcnt lgkmcnt(11)\n\t"
                "v_mfma_f32_32x32x16_bf16 %[a0], v[240:243], v[248:251], %[a0]\n\t"
                "ds_read_b128 v[216:219], %[ub] offset:1072\n\t"
                "s_add_u32 %[t0], %[t1], 65536\n\t"
                "buffer_load_dwordx4 a[208:211], %[go], %[so], %[t0] offen\n\t"
                "s_waitcnt lgkmcnt(10)\n\t"
                "v_mfma_f32_32x32x16_bf16 %[a1], v[252:255], v[248:251], %[a1]\n\t"
                "ds_read_b64 v[220:221], %[kb] offset:3200\n\t"
                "ds_read_b64 v[222:223], %[kb] offset:3208\n\t"
                "s_waitcnt lgkmcnt(9)\n\t"
                "v_mfma_f32_32x32x16_bf16 %[a1], v[192:195], v[196:199], %[a1]\n\t"
                "ds_read_b64 v[224:225], %[kb] offset:1248\n\t"
                "ds_read_b64 v[226:227], %[kb] offset:1256\n\t"
                "ds_read_b128 v[228:231], %[ub] offset:3520\n\t"
                "s_waitcnt lgkmcnt(11)\n\t"
                "v_mfma_f32_32x32x16_bf16 %[a0], v[192:195], v[200:203], %[a0]\n\t"
                "ds_read_b128 v[232:235], %[ub] offset:960\n\t"
                "s_waitcnt lgkmcnt(10)\n\t"
                "v_mfma_f32_32x32x16_bf16 %[a1], v[204:207], v[200:203], %[a1]\n\t"
                "ds_read_b64 v[236:237], %[kb] offset:3296\n\t"
                "ds_read_b64 v[238:239], %[kb] offset:3304\n\t"
                "s_waitcnt lgkmcnt(9)\n\t"
                "v_mfma_f32_32x32x16_bf16 %[a1], v[208:211], v[212:215], %[a1]\n\t"
                "ds_read_b64 v[240:241], %[kb] offset:1216\n\t"
                "ds_read_b64 v[242:243], %[kb] offset:1224\n\t"
                "ds_read_b128 v[244:247], %[ub] offset:3552\n\t"
                "s_waitcnt lgkmcnt(11)\n\t"
                "v_mfma_f32_32x32x16_bf16 %[a0], v[208:211], v[216:219], %[a0]\n\t"
                "ds_read_b128 v[248:251], %[ub] offset:992\n\t"
                "s_add_u32 %[t0], %[t1], 81920\n\t"
                "buffer_load_dwordx4 a[212:215], %[go], %[so], %[t0] offen\n\t"
                "s_waitcnt lgkmcnt(10)\n\t"
                "v_mfma_f32_32x32x16_bf16 %[a1], v[220:223], v[216:219], %[a1]\n\t"
                "ds_read_b64 v[252:253], %[kb] offset:3264\n\t"
                "ds_read_b64 v[254:255], %[kb] offset:3272\n\t"
                "s_waitcnt lgkmcnt(9)\n\t"
                "v_mfma_f32_32x32x16_bf16 %[a1], v[224:227], v[228:231], %[a1]\n\t"
                "ds_read_b64 v[192:193], %[kb] offset:1312\n\t"
                "ds_read_b64 v[194:195], %[kb] offset:1320\n\t"
                "ds_read_b128 v[196:199], %[ub] offset:3440\n\t"
                "s_waitcnt lgkmcnt(11)\n\t"
                "v_mfma_f32_32x32x16_bf16 %[a0], v[224:227], v[232:235], %[a0]\n\t"
                "ds_read_b128 v[200:203], %[ub] offset:880\n\t"
                "s_waitcnt lgkmcnt(10)\n\t"
                "v_mfma_f32_32x32x16_bf16 %[a1], v[236:239], v[232:235], %[a1]\n\t"
                "ds_read_b64 v[204:205], %[kb] offset:3360\n\t"
                "ds_read_b64 v[206:207], %[kb] offset:3368\n\t"
                "s_waitcnt lgkmcnt(9)\n\t"
                "v_mfma_f32_32x32x16_bf16 %[a1], v[240:243], v[244:247], %[a1]\n\t"
                "ds_read_b64 v[208:209], %[kb] offset:1280\n\t"
                "ds_read_b64 v[210:211], %[kb] offset:1288\n\t"
                "ds_read_b128 v[212:215], %[ub] offset:3472\n\t"
                "s_waitcnt lgkmcnt(11)\n\t"
                "v_mfma_f32_32x32x16_bf16 %[a0], v[240:243], v[248:251], %[a0]\n\t"
                "ds_read_b128 v[216:219], %[ub] offset:912\n\t"
                "s_add_u32 %[t0], %[t1], 98304\n\t"
                "buffer_load_dwordx4 a[216:219], %[go], %[so], %[t0] offen\n\t"
                "s_waitcnt lgkmcnt(10)\n\t"
                "v_mfma_f32_32x32x16_bf16 %[a1], v[252:255], v[248:251], %[a1]\n\t"
                "ds_read_b64 v[220:221], %[kb] offset:3328\n\t"
                "ds_read_b64 v[222:223], %[kb] offset:3336\n\t"
                "s_waitcnt lgkmcnt(9)\n\t"
                "v_mfma_f32_32x32x16_bf16 %[a1], v[192:195], v[196:199], %[a1]\n\t"
                "ds_read_b64 v[224:225], %[kb] offset:1376\n\t"
                "ds_read_b64 v[226:227], %[kb] offset:1384\n\t"
                "ds_read_b128 v[228:231], %[ub] offset:3360\n\t"
                "s_waitcnt lgkmcnt(11)\n\t"
                "v_mfma_f32_32x32x16_bf16 %[a0], v[192:195], v[200:203], %[a0]\n\t"
                "ds_read_b128 v[232:235], %[ub] offset:800\n\t"
                "s_waitcnt lgkmcnt(10)\n\t"
                "v_mfma_f32_32x32x16_bf16 %[a1], v[204:207], v[200:203], %[a1]\n\t"
                "ds_read_b64 v[236:237], %[kb] offset:3424\n\t"
                "ds_read_b64 v[238:239], %[kb] offset:3432\n\t"
                "s_waitcnt lgkmcnt(9)\n\t"
                "v_mfma_f32_32x32x16_bf16 %[a1], v[208:211], v[212:215], %[a1]\n\t"
                "ds_read_b64 v[240:241], %[kb] offset:1344\n\t"
                "ds_read_b64 v[242:243], %[kb] offset:1352\n\t"
                "ds_read_b128 v[244:247], %[ub] offset:3392\n\t"
                "s_waitcnt lgkmcnt(11)\n\t"
                "v_mfma_f32_32x32x16_bf16 %[a0], v[208:211], v[216:219], %[a0]\n\t"
                "ds_read_b128 v[248:251], %[ub] offset:832\n\t"
                "s_add_u32 %[t0], %[t1], 114688\n\t"
                "buffer_load_dwordx4 a[220:223], %[go], %[so], %[t0] offen\n\t"
                "s_waitcnt lgkmcnt(10)\n\t"
                "v_mfma_f32_32x32x16_bf16 %[a1], v[220:223], v[216:219], %[a1]\n\t"
                "ds_read_b64 v[252:253], %[kb] offset:3392\n\t"
                "ds_read_b64 v[254:255], %[kb] offset:3400\n\t"
                "s_waitcnt lgkmcnt(9)\n\t"
                "v_mfma_f32_32x32x16_bf16 %[a1], v[224:227], v[228:231], %[a1]\n\t"
                "ds_read_b64 v[192:193], %[kb] offset:1440\n\t"
                "ds_read_b64 v[194:195], %[kb] offset:1448\n\t"
                "ds_read_b128 v[196:199], %[ub] offset:3280\n\t"
                "s_waitcnt lgkmcnt(11)\n\t"
                "v_mfma_f32_32x32x16_bf16 %[a0], v[224:227], v[232:235], %[a0]\n\t"
                "ds_read_b128 v[200:203], %[ub] offset:720\n\t"
                "s_add_u32 %[t1], %[ro], 3\n\t"
                "s_and_b32 %[t1], %[t1], 7\n\t"
                "s_lshl_b32 %[t1], %[t1], 18\n\t"
                "s_add_u32 %[t0], %[t1], 131072\n\t"
                "buffer_load_dwordx4 a[224:227], %[go], %[so], %[t0] offen\n\t"
                "s_waitcnt lgkmcnt(10)\n\t"
                "v_mfma_f32_32x32x16_bf16 %[a1], v[236:239], v[232:235], %[a1]\n\t"
                "ds_read_b64 v[204:205], %[kb] offset:3488\n\t"
                "ds_read_b64 v[206:207], %[kb] offset:3496\n\t"
                "s_waitcnt lgkmcnt(9)\n\t"
                "v_mfma_f32_32x32x16_bf16 %[a1], v[240:243], v[244:247], %[a1]\n\t"
                "ds_read_b64 v[208:209], %[kb] offset:1408\n\t"
                "ds_read_b64 v[210:211], %[kb] offset:1416\n\t"
                "ds_read_b128 v[212:215], %[ub] offset:3312\n\t"
                "s_waitcnt lgkmcnt(11)\n\t"
                "v_mfma_f32_32x32x16_bf16 %[a0], v[240:243], v[248:251], %[a0]\n\t"
                "ds_read_b128 v[216:219], %[ub] offset:752\n\t"
                "s_waitcnt lgkmcnt(10)\n\t"
                "v_mfma_f32_32x32x16_bf16 %[a1], v[252:255], v[248:251], %[a1]\n\t"
                "ds_read_b64 v[220:221], %[kb] offset:3456\n\t"
                "ds_read_b64 v[222:223], %[kb] offset:3464\n\t"
                "s_waitcnt lgkmcnt(9)\n\t"
                "v_mfma_f32_32x32x16_bf16 %[a1], v[192:195], v[196:199], %[a1]\n\t"
                "ds_read_b64 v[224:225], %[kb] offset:1504\n\t"
                "ds_read_b64 v[226:227], %[kb] offset:1512\n\t"
                "ds_read_b128 v[228:231], %[ub] offset:3200\n\t"
                "s_waitcnt lgkmcnt(11)\n\t"
                "v_mfma_f32_32x32x16_bf16 %[a0], v[192:195], v[200:203], %[a0]\n\t"
                "ds_read_b128 v[232:235], %[ub] offset:640\n\t"
                "s_add_u32 %[t0], %[t1], 147456\n\t"
                "buffer_load_dwordx4 a[228:231], %[go], %[so], %[t0] offen\n\t"
                "s_waitcnt lgkmcnt(10)\n\t"
                "v_mfma_f32_32x32x16_bf16 %[a1], v[204:207], v[200:203], %[a1]\n\t"
                "ds_read_b64 v[236:237], %[kb] offset:3552\n\t"
                "ds_read_b64 v[238:239], %[kb] offset:3560\n\t"
                "s_waitcnt lgkmcnt(9)\n\t"
                "v_mfma_f32_32x32x16_bf16 %[a1], v[208:211], v[212:215], %[a1]\n\t"
                "ds_read_b64 v[240:241], %[kb] offset:1472\n\t"
                "ds_read_b64 v[242:243], %[kb] offset:1480\n\t"
                "ds_read_b128 v[244:247], %[ub] offset:3232\n\t"
                "s_waitcnt lgkmcnt(11)\n\t"
                "v_mfma_f32_32x32x16_bf16 %[a0], v[208:211], v[216:219], %[a0]\n\t"
                "ds_read_b128 v[248:251], %[ub] offset:672\n\t"
                "s_waitcnt lgkmcnt(10)\n\t"
                "v_mfma_f32_32x32x16_bf16 %[a1], v[220:223], v[216:219], %[a1]\n\t"
                "ds_read_b64 v[252:253], %[kb] offset:3520\n\t"
                "ds_read_b64 v[254:255], %[kb] offset:3528\n\t"
                "s_waitcnt lgkmcnt(9)\n\t"
                "v_mfma_f32_32x32x16_bf16 %[a1], v[224:227], v[228:231], %[a1]\n\t"
                "ds_read_b64 v[192:193], %[kb] offset:1568\n\t"
                "ds_read_b64 v[194:195], %[kb] offset:1576\n\t"
                "ds_read_b128 v[196:199], %[ub] offset:3120\n\t"
                "s_waitcnt lgkmcnt(11)\n\t"
                "v_mfma_f32_32x32x16_bf16 %[a0], v[224:227], v[232:235], %[a0]\n\t"
                "ds_read_b128 v[200:203], %[ub] offset:560\n\t"
                "s_add_u32 %[t0], %[t1], 163840\n\t"
                "buffer_load_dwordx4 a[232:235], %[go], %[so], %[t0] offen\n\t"
                "s_waitcnt lgkmcnt(10)\n\t"
                "v_mfma_f32_32x32x16_bf16 %[a1], v[236:239], v[232:235], %[a1]\n\t"
                "ds_read_b64 v[204:205], %[kb] offset:3616\n\t"
                "ds_read_b64 v[206:207], %[kb] offset:3624\n\t"
                "s_waitcnt lgkmcnt(9)\n\t"
                "v_mfma_f32_32x32x16_bf16 %[a1], v[240:243], v[244:247], %[a1]\n\t"
                "ds_read_b64 v[208:209], %[kb] offset:1536\n\t"
                "ds_read_b64 v[210:211], %[kb] offset:1544\n\t"
                "ds_read_b128 v[212:215], %[ub] offset:3152\n\t"
                "s_waitcnt lgkmcnt(11)\n\t"
                "v_mfma_f32_32x32x16_bf16 %[a0], v[240:243], v[248:251], %[a0]\n\t"
                "ds_read_b128 v[216:219], %[ub] offset:592\n\t"
                "s_waitcnt lgkmcnt(10)\n\t"
                "v_mfma_f32_32x32x16_bf16 %[a1], v[252:255], v[248:251], %[a1]\n\t"
                "ds_read_b64 v[220:221], %[kb] offset:3584\n\t"
                "ds_read_b64 v[222:223], %[kb] offset:3592\n\t"
                "s_waitcnt lgkmcnt(9)\n\t"
                "v_mfma_f32_32x32x16_bf16 %[a1], v[192:195], v[196:199], %[a1]\n\t"
                "ds_read_b64 v[224:225], %[kb] offset:1632\n\t"
                "ds_read_b64 v[226:227], %[kb] offset:1640\n\t"
                "ds_read_b128 v[228:231], %[ub] offset:3040\n\t"
                "s_waitcnt lgkmcnt(11)\n\t"
                "v_mfma_f32_32x32x16_bf16 %[a0], v[192:195], v[200:203], %[a0]\n\t"
                "ds_read_b128 v[232:235], %[ub] offset:480\n\t"
                "s_add_u32 %[t0], %[t1], 180224\n\t"
                "buffer_load_dwordx4 a[236:239], %[go], %[so], %[t0] offen\n\t"
                "s_waitcnt lgkmcnt(10)\n\t"
                "v_mfma_f32_32x32x16_bf16 %[a1], v[204:207], v[200:203], %[a1]\n\t"
                "ds_read_b64 v[236:237], %[kb] offset:3680\n\t"
                "ds_read_b64 v[238:239], %[kb] offset:3688\n\t"
                "s_waitcnt lgkmcnt(9)\n\t"
                "v_mfma_f32_32x32x16_bf16 %[a1], v[208:211], v[212:215], %[a1]\n\t"
                "ds_read_b64 v[240:241], %[kb] offset:1600\n\t"
                "ds_read_b64 v[242:243], %[kb] offset:1608\n\t"
                "ds_read_b128 v[244:247], %[ub] offset:3072\n\t"
                "s_waitcnt lgkmcnt(11)\n\t"
                "v_mfma_f32_32x32x16_bf16 %[a0], v[208:211], v[216:219], %[a0]\n\t"
                "ds_read_b128 v[248:251], %[ub] offset:512\n\t"
                "s_add_u32 %[t0], %[t1], 196608\n\t"
                "buffer_load_dwordx4 a[240:243], %[go], %[so], %[t0] offen\n\t"
                "s_waitcnt lgkmcnt(10)\n\t"
                "v_mfma_f32_32x32x16_bf16 %[a1], v[220:223], v[216:219], %[a1]\n\t"
                "ds_read_b64 v[252:253], %[kb] offset:3648\n\t"
                "ds_read_b64 v[254:255], %[kb] offset:3656\n\t"
                "s_waitcnt lgkmcnt(9)\n\t"
                "v_mfma_f32_32x32x16_bf16 %[a1], v[224:227], v[228:231], %[a1]\n\t"
                "ds_read_b64 v[192:193], %[kb] offset:1696\n\t"
                "ds_read_b64 v[194:195], %[kb] offset:1704\n\t"
                "ds_read_b128 v[196:199], %[ub] offset:2960\n\t"
                "s_waitcnt lgkmcnt(11)\n\t"
                "v_mfma_f32_32x32x16_bf16 %[a0], v[224:227], v[232:235], %[a0]\n\t"
                "ds_read_b128 v[200:203], %[ub] offset:400\n\t"
                "s_waitcnt lgkmcnt(10)\n\t"
                "v_mfma_f32_32x32x16_bf16 %[a1], v[236:239], v[232:235], %[a1]\n\t"
                "ds_read_b64 v[204:205], %[kb] offset:3744\n\t"
                "ds_read_b64 v[206:207], %[kb] offset:3752\n\t"
                "s_waitcnt lgkmcnt(9)\n\t"
                "v_mfma_f32_32x32x16_bf16 %[a1], v[240:243], v[244:247], %[a1]\n\t"
                "ds_read_b64 v[208:209], %[kb] offset:1664\n\t"
                "ds_read_b64 v[210:211], %[kb] offset:1672\n\t"
                "ds_read_b128 v[212:215], %[ub] offset:2992\n\t"
                "s_waitcnt lgkmcnt(11)\n\t"
                "v_mfma_f32_32x32x16_bf16 %[a0], v[240:243], v[248:251], %[a0]\n\t"
                "ds_read_b128 v[216:219], %[ub] offset:432\n\t"
                "s_add_u32 %[t0], %[t1], 212992\n\t"
                "buffer_load_dwordx4 a[244:247], %[go], %[so], %[t0] offen\n\t"
                "s_waitcnt lgkmcnt(10)\n\t"
                "v_mfma_f32_32x32x16_bf16 %[a1], v[252:255], v[248:251], %[a1]\n\t"
                "ds_read_b64 v[220:221], %[kb] offset:3712\n\t"
                "ds_read_b64 v[222:223], %[kb] offset:3720\n\t"
                "s_waitcnt lgkmcnt(9)\n\t"
                "v_mfma_f32_32x32x16_bf16 %[a1], v[192:195], v[196:199], %[a1]\n\t"
                "ds_read_b64 v[224:225], %[kb] offset:1760\n\t"
                "ds_read_b64 v[226:227], %[kb] offset:1768\n\t"
                "ds_read_b128 v[228:231], %[ub] offset:2880\n\t"
                "s_waitcnt lgkmcnt(11)\n\t"
                "v_mfma_f32_32x32x16_bf16 %[a0], v[192:195], v[200:203], %[a0]\n\t"
                "ds_read_b128 v[232:235], %[ub] offset:320\n\t"
                "s_waitcnt lgkmcnt(10)\n\t"
                "v_mfma_f32_32x32x16_bf16 %[a1], v[204:207], v[200:203], %[a1]\n\t"
                "ds_read_b64 v[236:237], %[kb] offset:3808\n\t"
                "ds_read_b64 v[238:239], %[kb] offset:3816\n\t"
                "s_waitcnt lgkmcnt(9)\n\t"
                "v_mfma_f32_32x32x16_bf16 %[a1], v[208:211], v[212:215], %[a1]\n\t"
                "ds_read_b64 v[240:241], %[kb] offset:1728\n\t"
                "ds_read_b64 v[242:243], %[kb] offset:1736\n\t"
                "ds_read_b128 v[244:247], %[ub] offset:2912\n\t"
                "s_waitcnt lgkmcnt(11)\n\t"
                "v_mfma_f32_32x32x16_bf16 %[a0], v[208:211], v[216:219], %[a0]\n\t"
                "ds_read_b128 v[248:251], %[ub] offset:352\n\t"
                "s_add_u32 %[t0], %[t1], 229376\n\t"
                "buffer_load_dwordx4 a[248:251], %[go], %[so], %[t0] offen\n\t"
                "s_waitcnt lgkmcnt(10)\n\t"
                "v_mfma_f32_32x32x16_bf16 %[a1], v[220:223], v[216:219], %[a1]\n\t"
                "ds_read_b64 v[252:253], %[kb] offset:3776\n\t"
                "ds_read_b64 v[254:255], %[kb] offset:3784\n\t"
                "s_waitcnt lgkmcnt(9)\n\t"
                "v_mfma_f32_32x32x16_bf16 %[a1], v[224:227], v[228:231], %[a1]\n\t"
                "ds_read_b64 v[192:193], %[kb] offset:1824\n\t"
                "ds_read_b64 v[194:195], %[kb] offset:1832\n\t"
                "ds_read_b128 v[196:199], %[ub] offset:2800\n\t"
                "s_waitcnt lgkmcnt(11)\n\t"
                "v_mfma_f32_32x32x16_bf16 %[a0], v[224:227], v[232:235], %[a0]\n\t"
                "ds_read_b128 v[200:203], %[ub] offset:240\n\t"
                "s_waitcnt lgkmcnt(10)\n\t"
                "v_mfma_f32_32x32x16_bf16 %[a1], v[236:239], v[232:235], %[a1]\n\t"
                "ds_read_b64 v[204:205], %[kb] offset:3872\n\t"
                "ds_read_b64 v[206:207], %[kb] offset:3880\n\t"
                "s_waitcnt lgkmcnt(9)\n\t"
                "v_mfma_f32_32x32x16_bf16 %[a1], v[240:243], v[244:247], %[a1]\n\t"
                "ds_read_b64 v[208:209], %[kb] offset:1792\n\t"
                "ds_read_b64 v[210:211], %[kb] offset:1800\n\t"
                "ds_read_b128 v[212:215], %[ub] offset:2832\n\t"
                "s_waitcnt lgkmcnt(11)\n\t"
                "v_mfma_f32_32x32x16_bf16 %[a0], v[240:243], v[248:251], %[a0]\n\t"
                "ds_read_b128 v[216:219], %[ub] offset:272\n\t"
                "s_add_u32 %[t0], %[t1], 245760\n\t"
                "buffer_load_dwordx4 a[252:255], %[go], %[so], %[t0] offen\n\t"
                "s_waitcnt lgkmcnt(10)\n\t"
                "v_mfma_f32_32x32x16_bf16 %[a1], v[252:255], v[248:251], %[a1]\n\t"
                "ds_read_b64 v[220:221], %[kb] offset:3840\n\t"
                "ds_read_b64 v[222:223], %[kb] offset:3848\n\t"
                "s_waitcnt lgkmcnt(9)\n\t"
                "v_mfma_f32_32x32x16_bf16 %[a1], v[192:195], v[196:199], %[a1]\n\t"
                "ds_read_b64 v[224:225], %[kb] offset:1888\n\t"
                "ds_read_b64 v[226:227], %[kb] offset:1896\n\t"
                "ds_read_b128 v[228:231], %[ub] offset:2720\n\t"
                "s_waitcnt lgkmcnt(11)\n\t"
                "v_mfma_f32_32x32x16_bf16 %[a0], v[192:195], v[200:203], %[a0]\n\t"
                "ds_read_b128 v[232:235], %[ub] offset:160\n\t"
                "s_waitcnt lgkmcnt(10)\n\t"
                "v_mfma_f32_32x32x16_bf16 %[a1], v[204:207], v[200:203], %[a1]\n\t"
                "ds_read_b64 v[236:237], %[kb] offset:3936\n\t"
                "ds_read_b64 v[238:239], %[kb] offset:3944\n\t"
                "s_waitcnt lgkmcnt(9)\n\t"
                "v_mfma_f32_32x32x16_bf16 %[a1], v[208:211], v[212:215], %[a1]\n\t"
                "ds_read_b64 v[240:241], %[kb] offset:1856\n\t"
                "ds_read_b64 v[242:243], %[kb] offset:1864\n\t"
                "ds_read_b128 v[244:247], %[ub] offset:2752\n\t"
                "s_waitcnt lgkmcnt(11)\n\t"
                "v_mfma_f32_32x32x16_bf16 %[a0], v[208:211], v[216:219], %[a0]\n\t"
                "ds_read_b128 v[248:251], %[ub] offset:192\n\t"
                "s_waitcnt lgkmcnt(10)\n\t"
                "v_mfma_f32_32x32x16_bf16 %[a1], v[220:223], v[216:219], %[a1]\n\t"
                "ds_read_b64 v[252:253], %[kb] offset:3904\n\t"
                "ds_read_b64 v[254:255], %[kb] offset:3912\n\t"
                "s_waitcnt lgkmcnt(9)\n\t"
                "v_mfma_f32_32x32x16_bf16 %[a1], v[224:227], v[228:231], %[a1]\n\t"
                "ds_read_b64 v[192:193], %[kb] offset:1952\n\t"
                "ds_read_b64 v[194:195], %[kb] offset:1960\n\t"
                "ds_read_b128 v[196:199], %[ub] offset:2640\n\t"
                "s_waitcnt lgkmcnt(11)\n\t"
                "v_mfma_f32_32x32x16_bf16 %[a0], v[224:227], v[232:235], %[a0]\n\t"
                "ds_read_b128 v[200:203], %[ub] offset:80\n\t"
                "s_waitcnt lgkmcnt(10)\n\t"
                "v_mfma_f32_32x32x16_bf16 %[a1], v[236:239], v[232:235], %[a1]\n\t"
                "ds_read_b64 v[204:205], %[kb] offset:4000\n\t"
                "ds_read_b64 v[206:207], %[kb] offset:4008\n\t"
                "s_waitcnt lgkmcnt(9)\n\t"
                "v_mfma_f32_32x32x16_bf16 %[a1], v[240:243], v[244:247], %[a1]\n\t"
                "ds_read_b64 v[208:209], %[kb] offset:1920\n\t"
                "ds_read_b64 v[210:211], %[kb] offset:1928\n\t"
                "ds_read_b128 v[212:215], %[ub] offset:2672\n\t"
                "s_waitcnt lgkmcnt(11)\n\t"
                "v_mfma_f32_32x32x16_bf16 %[a0], v[240:243], v[248:251], %[a0]\n\t"
                "ds_read_b128 v[216:219], %[ub] offset:112\n\t"
                "s_waitcnt lgkmcnt(10)\n\t"
                "v_mfma_f32_32x32x16_bf16 %[a1], v[252:255], v[248:251], %[a1]\n\t"
                "ds_read_b64 v[220:221], %[kb] offset:3968\n\t"
                "ds_read_b64 v[222:223], %[kb] offset:3976\n\t"
                "s_waitcnt lgkmcnt(9)\n\t"
                "v_mfma_f32_32x32x16_bf16 %[a1], v[192:195], v[196:199], %[a1]\n\t"
                "ds_read_b64 v[224:225], %[kb] offset:2016\n\t"
                "ds_read_b64 v[226:227], %[kb] offset:2024\n\t"
                "ds_read_b128 v[228:231], %[ub] offset:2560\n\t"
                "s_waitcnt lgkmcnt(11)\n\t"
                "v_mfma_f32_32x32x16_bf16 %[a0], v[192:195], v[200:203], %[a0]\n\t"
                "ds_read_b128 v[232:235], %[ub] offset:0\n\t"
                "s_waitcnt lgkmcnt(10)\n\t"
                "v_mfma_f32_32x32x16_bf16 %[a1], v[204:207], v[200:203], %[a1]\n\t"
                "ds_read_b64 v[236:237], %[kb] offset:4064\n\t"
                "ds_read_b64 v[238:239], %[kb] offset:4072\n\t"
                "s_waitcnt lgkmcnt(9)\n\t"
                "v_mfma_f32_32x32x16_bf16 %[a1], v[208:211], v[212:215], %[a1]\n\t"
                "ds_read_b64 v[240:241], %[kb] offset:1984\n\t"
                "ds_read_b64 v[242:243], %[kb] offset:1992\n\t"
                "ds_read_b128 v[244:247], %[ub] offset:2592\n\t"
                "s_waitcnt lgkmcnt(11)\n\t"
                "v_mfma_f32_32x32x16_bf16 %[a0], v[208:211], v[216:219], %[a0]\n\t"
                "ds_read_b128 v[248:251], %[ub] offset:32\n\t"
                "s_waitcnt lgkmcnt(10)\n\t"
                "v_mfma_f32_32x32x16_bf16 %[a1], v[220:223], v[216:219], %[a1]\n\t"
                "ds_read_b64 v[252:253], %[kb] offset:4032\n\t"
                "ds_read_b64 v[254:255], %[kb] offset:4040\n\t"
                "s_waitcnt lgkmcnt(9)\n\t"
                "v_mfma_f32_32x32x16_bf16 %[a1], v[224:227], v[228:231], %[a1]\n\t"
                "s_waitcnt lgkmcnt(8)\n\t"
                "v_mfma_f32_32x32x16_bf16 %[a0], v[224:227], v[232:235], %[a0]\n\t"
                "s_waitcnt lgkmcnt(6)\n\t"
                "v_mfma_f32_32x32x16_bf16 %[a1], v[236:239], v[232:235], %[a1]\n\t"
                "s_waitcnt lgkmcnt(3)\n\t"
                "v_mfma_f32_32x32x16_bf16 %[a1], v[240:243], v[244:247], %[a1]\n\t"
                "s_waitcnt lgkmcnt(2)\n\t"
                "v_mfma_f32_32x32x16_bf16 %[a0], v[240:243], v[248:251], %[a0]\n\t"
                "s_waitcnt lgkmcnt(0)\n\t"
                "v_mfma_f32_32x32x16_bf16 %[a1], v[252:255], v[248:251], %[a1]\n\t"
                "s_waitcnt vmcnt(0)\n\t"
                "s_nop 7\n\t"
                "s_nop 3\n\t"
                : [a0] "=&v"(acc0), [a1] "=&v"(acc1), [t0] "=&s"(t0), [t1] "=&s"(t1)
                : [kb] "v"(kb), [ub] "v"(ub0 + c8 * CV_U), [go] "v"(go), [so] "s"(so), [ro] "s"(ro) : "memory", "scc", "v192", "v193", "v194", "v195", "v196", "v197", "v198", "v199", "v200", "v201", "v202", "v203", "v204", "v205", "v206", "v207", "v208", "v209", "v210", "v211", "v212", "v213", "v214", "v215", "v216", "v217", "v218", "v219", "v220", "v221", "v222", "v223", "v224", "v225", "v226", "v227", "v228", "v229", "v230", "v231", "v232", "v233", "v234", "v235", "v236", "v237", "v238", "v239", "v240", "v241", "v242", "v243", "v244", "v245", "v246", "v247", "v248", "v249", "v250", "v251", "v252", "v253", "v254", "v255", "a0", "a1", "a2", "a3", "a4", "a5", "a6", "a7", "a8", "a9", "a10", "a11", "a12", "a13", "a14", "a15", "a16", "a17", "a18", "a19", "a20", "a21", "a22", "a23", "a24", "a25", "a26", "a27", "a28", "a29", "a30", "a31", "a32", "a33", "a34", "a35", "a36", "a37", "a38", "a39", "a40", "a41", "a42", "a43", "a44", "a45", "a46", "a47", "a48", "a49", "a50", "a51", "a52", "a53", "a54", "a55", "a56", "a57", "a58", "a59", "a60", "a61", "a62", "a63", "a64", "a65", "a66", "a67", "a68", "a69", "a70", "a71", "a72", "a73", "a74", "a75", "a76", "a77", "a78", "a79", "a80", "a81", "a82", "a83", "a84", "a85", "a86", "a87", "a88", "a89", "a90", "a91", "a92", "a93", "a94", "a95", "a96", "a97", "a98", "a99", "a100", "a101", "a102", "a103", "a104", "a105", "a106", "a107", "a108", "a109", "a110", "a111", "a112", "a113", "a114", "a115", "a116", "a117", "a118", "a119", "a120", "a121", "a122", "a123", "a124", "a125", "a126", "a127", "a128", "a129", "a130", "a131", "a132", "a133", "a134", "a135", "a136", "a137", "a138", "a139", "a140", "a141", "a142", "a143", "a144", "a145", "a146", "a147", "a148", "a149", "a150", "a151", "a152", "a153", "a154", "a155", "a156", "a157", "a158", "a159", "a160", "a161", "a162", "a163", "a164", "a165", "a166", "a167", "a168", "a169", "a170", "a171", "a172", "a173", "a174", "a175", "a176", "a177", "a178", "a179", "a180", "a181", "a182", "a183", "a184", "a185", "a186", "a187", "a188", "a189", "a190", "a191", "a192", "a193", "a194", "a195", "a196", "a197", "a198", "a199", "a200", "a201", "a202", "a203", "a204", "a205", "a206", "a207", "a208", "a209", "a210", "a211", "a212", "a213", "a214", "a215", "a216", "a217", "a218", "a219", "a220", "a221", "a222", "a223", "a224", "a225", "a226", "a227", "a228", "a229", "a230", "a231", "a232", "a233", "a234", "a235", "a236", "a237", "a238", "a239", "a240", "a241", "a242", "a243", "a244", "a245", "a246", "a247", "a248", "a249", "a250", "a251", "a252", "a253", "a254", "a255");
            unsigned char* uc = lds + c8 * CV_U + (32 + b) * CV_RP + kg * 8;
#pragma unroll
            for (int q = 0; q < 4; ++q) {
                *(v2u*)(uc + q * 16) = (v2u){pack2(acc0[4 * q], acc0[4 * q + 1]), pack2(acc0[4 * q + 2], acc0[4 * q + 3])};
                *(v2u*)(uc + 32 * CV_RP + q * 16) = (v2u){pack2(acc1[4 * q], acc1[4 * q + 1]), pack2(acc1[4 * q + 2], acc1[4 * q + 3])}; }
            __builtin_amdgcn_wave_barrier();
        }
    __syncthreads();
#pragma unroll
    for (int G = 0; G < 4; ++G) { const v4u lo = *(const v4u*)(urow + G * 16), hh = *(const v4u*)(urow + CV_U + G * 16); v4u o[2];
#pragma unroll
        for (int i = 0; i < 8; ++i) { const unsigned u0 = ur[G][(7 - i) >> 1], u1 = ur[4 + G][(7 - i) >> 1], qq = qr[2 * G + (i >> 2)][i & 3];
            const unsigned ud = pack2(((i & 1) ? blo(u0) : bhi(u0)) * blo(dv), ((i & 1) ? blo(u1) : bhi(u1)) * bhi(dv));
            const unsigned y = pack2(((i & 1) ? bhi(lo[i >> 1]) : blo(lo[i >> 1])) + blo(ud), ((i & 1) ? bhi(hh[i >> 1]) : blo(hh[i >> 1])) + bhi(ud));
            o[i >> 2][i & 3] = pack2(blo(qq) * blo(y), bhi(qq) * bhi(y)); }
        st16(r, W_A + un * 32768 + 2 * G * 4096 + tid * 16, o[0]); st16(r, W_A + un * 32768 + (2 * G + 1) * 4096 + tid * 16, o[1]); }
    DRAIN(); __syncthreads();
    if (tid == 0) __hip_atomic_fetch_add((unsigned*)(p.ws + F_CVY + (un >> 3) * 128), 1u, __ATOMIC_RELAXED, __HIP_MEMORY_SCOPE_AGENT);
    return true;
}

__device__ __forceinline__ void proj_block(const Params& p, unsigned char* lds, const int wv, const int h, const int c) {
    const int w = wv, l = otid() & 63, n = l & 15, g = l >> 4, tm = w >> 1, tn = w & 1;
    const unsigned lb = __builtin_amdgcn_readfirstlane((unsigned)(size_t)lds);
    const v4u sx = mk_srd(p.Aq, S * D * 2), sq = mk_srd(c < 2 ? p.Wq : p.Wk, 1024 * 4096 * 2), sv = mk_srd(p.Wv, 1024 * 4096 * 2), sw = mk_srd(p.ws, W_TOTAL);
    const unsigned lx = lb + (32 * tm + n) * 128 + ((g ^ (n & 7)) << 4), lr = lb + 131072, rr = 8 * g + (n >> 2);
    const int cv = ((c & 1) ^ 1) * 2 + (c >> 1);
    const unsigned aq = (c < 2 ? W_QX : W_KX) + (tm * 32 + h) * 8192 + (2 * (c & 1) + tn) * 1024 + l * 16;
    unsigned t0, xo, so, cn;
    asm volatile(
        "s_nop 4\n\t"
        "s_mov_b32 %[xo], 0\n\t"
        "s_mov_b32 %[so], 0\n\t"
        "s_mov_b32 %[cn], 1\n\t"
        "s_add_u32 m0, %[ld], 0\n\t"
        "s_add_u32 %[t0], %[xo], 0\n\t"
        "buffer_load_dwordx4 v100, %[sx], %[t0] offen lds\n\t"
        "s_add_u32 m0, %[ld], 1024\n\t"
        "s_add_u32 %[t0], %[xo], 16384\n\t"
        "buffer_load_dwordx4 v100, %[sx], %[t0] offen lds\n\t"
        "s_add_u32 m0, %[ld], 8192\n\t"
        "s_add_u32 %[t0], %[xo], 128\n\t"
        "buffer_load_dwordx4 v100, %[sx], %[t0] offen lds\n\t"
        "s_add_u32 m0, %[ld], 9216\n\t"
        "s_add_u32 %[t0], %[xo], 16512\n\t"
        "buffer_load_dwordx4 v100, %[sx], %[t0] offen lds\n\t"
        "s_add_u32 m0, %[ld], 16384\n\t"
        "s_add_u32 %[t0], %[xo], 256\n\t"
        "buffer_load_dwordx4 v100, %[sx], %[t0] offen lds\n\t"
        "s_add_u32 m0, %[ld], 17408\n\t"
        "s_add_u32 %[t0], %[xo], 16640\n\t"
        "buffer_load_dwordx4 v100, %[sx], %[t0] offen lds\n\t"
        "s_add_u32 m0, %[ld], 24576\n\t"
        "s_add_u32 %[t0], %[xo], 384\n\t"
        "buffer_load_dwordx4 v100, %[sx], %[t0] offen lds\n\t"
        "s_add_u32 m0, %[ld], 25600\n\t"
        "s_add_u32 %[t0], %[xo], 16768\n\t"
        "buffer_load_dwordx4 v100, %[sx], %[t0] offen lds\n\t"
        "s_mov_b32 %[t0], 0\n\t"
        "buffer_load_dwordx4 a[192:195], v101, %[sq], %[t0] offen\n\t"
        "buffer_load_dwordx4 a[200:203], v112, %[sv], %[t0] offen\n\t"
        "s_mov_b32 %[t0], 262144\n\t"
        "buffer_load_dwordx4 a[196:199], v101, %[sq], %[t0] offen\n\t"
        "s_mov_b32 %[t0], 524288\n\t"
        "buffer_load_dwordx4 a[204:207], v101, %[sq], %[t0] offen\n\t"
        "buffer_load_dwordx4 a[212:215], v112, %[sv], %[t0] offen\n\t"
        "s_mov_b32 %[t0], 786432\n\t"
        "buffer_load_dwordx4 a[208:211], v101, %[sq], %[t0] offen\n\t"
        "s_mov_b32 %[t0], 1048576\n\t"
        "buffer_load_dwordx4 a[216:219], v101, %[sq], %[t0] offen\n\t"
        "buffer_load_dwordx4 a[224:227], v112, %[sv], %[t0] offen\n\t"
        "s_mov_b32 %[t0], 1310720\n\t"
        "buffer_load_dwordx4 a[220:223], v101, %[sq], %[t0] offen\n\t"
        "s_mov_b32 %[t0], 1572864\n\t"
        "buffer_load_dwordx4 a[228:231], v101, %[sq], %[t0] offen\n\t"
        "buffer_load_dwordx4 a[236:239], v112, %[sv], %[t0] offen\n\t"
        "s_mov_b32 %[t0], 1835008\n\t"
        "buffer_load_dwordx4 a[232:235], v101, %[sq], %[t0] offen\n\t"
        "s_waitcnt vmcnt(9)\n\t"
        "ds_write_b128 v102, a[192:195] offset:0\n\t"
        "ds_write_b128 v102, a[196:199] offset:4096\n\t"
        "ds_write_b128 v116, a[200:203] offset:8192\n\t"
        "s_waitcnt lgkmcnt(0)\n\t"
        "s_barrier\n\t"
        "ds_read_b64_tr_b16 a[0:1], v103 offset:0\n\t"
        "ds_read_b64_tr_b16 a[2:3], v103 offset:512\n\t"
        "ds_read_b64_tr_b16 a[4:5], v103 offset:8\n\t"
        "ds_read_b64_tr_b16 a[6:7], v103 offset:520\n\t"
        "ds_read_b64_tr_b16 a[8:9], v104 offset:0\n\t"
        "ds_read_b64_tr_b16 a[10:11], v104 offset:256\n\t"
        "s_waitcnt lgkmcnt(5)\n\t"
        "ds_read_b64_tr_b16 a[12:13], v103 offset:4096\n\t"
        "ds_read_b64_tr_b16 a[14:15], v103 offset:4608\n\t"
        "ds_read_b64_tr_b16 a[16:17], v103 offset:4104\n\t"
        "ds_read_b64_tr_b16 a[18:19], v103 offset:4616\n\t"
        "ds_read_b64_tr_b16 a[20:21], v104 offset:2048\n\t"
        "ds_read_b64_tr_b16 a[22:23], v104 offset:2304\n\t"
        "ds_read_b128 v[56:59], v105 offset:0\n\t"
        "ds_read_b128 v[60:63], v105 offset:2048\n\t"
        "ds_read_b128 v[64:67], v106 offset:0\n\t"
        "ds_read_b128 v[68:71], v106 offset:2048\n\t"
        "s_add_u32 m0, %[ld], 32768\n\t"
        "s_add_u32 %[t0], %[xo], 512\n\t"
        "buffer_load_dwordx4 v100, %[sx], %[t0] offen lds\n\t"
        "s_add_u32 m0, %[ld], 33792\n\t"
        "s_add_u32 %[t0], %[xo], 16896\n\t"
        "buffer_load_dwordx4 v100, %[sx], %[t0] offen lds\n\t"
        "s_add_u32 m0, %[ld], 40960\n\t"
        "s_add_u32 %[t0], %[xo], 640\n\t"
        "buffer_load_dwordx4 v100, %[sx], %[t0] offen lds\n\t"
        "s_add_u32 m0, %[ld], 41984\n\t"
        "s_add_u32 %[t0], %[xo], 17024\n\t"
        "buffer_load_dwordx4 v100, %[sx], %[t0] offen lds\n\t"
        "s_add_u32 m0, %[ld], 49152\n\t"
        "s_add_u32 %[t0], %[xo], 768\n\t"
        "buffer_load_dwordx4 v100, %[sx], %[t0] offen lds\n\t"
        "s_add_u32 m0, %[ld], 50176\n\t"
        "s_add_u32 %[t0], %[xo], 17152\n\t"
        "buffer_load_dwordx4 v100, %[sx], %[t0] offen lds\n\t"
        "s_add_u32 m0, %[ld], 57344\n\t"
        "s_add_u32 %[t0], %[xo], 896\n\t"
        "buffer_load_dwordx4 v100, %[sx], %[t0] offen lds\n\t"
        "s_add_u32 m0, %[ld], 58368\n\t"
        "s_add_u32 %[t0], %[xo], 17280\n\t"
        "buffer_load_dwordx4 v100, %[sx], %[t0] offen lds\n\t"
        "s_mov_b32 %[t0], 2097152\n\t"
        "buffer_load_dwordx4 a[240:243], v101, %[sq], %[t0] offen\n\t"
        "buffer_load_dwordx4 a[248:251], v112, %[sv], %[t0] offen\n\t"
        "s_mov_b32 %[t0], 2359296\n\t"
        "buffer_load_dwordx4 a[244:247], v101, %[sq], %[t0] offen\n\t"
        "s_waitcnt vmcnt(17)\n\t"
        "ds_write_b128 v102, a[204:207] offset:16384\n\t"
        "ds_write_b128 v102, a[208:211] offset:20480\n\t"
        "ds_write_b128 v116, a[212:215] offset:24576\n\t"
        "s_waitcnt lgkmcnt(0)\n\t"
        "s_barrier\n\t"
        "v_mfma_f32_16x16x32_bf16 v[8:11], a[0:3], v[56:59], 0\n\t"
        "ds_read_b64_tr_b16 a[24:25], v103 offset:16384\n\t"
        "ds_read_b64_tr_b16 a[26:27], v103 offset:16896\n\t"
        "v_mfma_f32_16x16x32_bf16 v[12:15], a[4:7], v[56:59], 0\n\t"
        "ds_read_b64_tr_b16 a[28:29], v103 offset:16392\n\t"
        "ds_read_b64_tr_b16 a[30:31], v103 offset:16904\n\t"
        "v_mfma_f32_16x16x32_bf16 v[16:19], v[56:59], a[8:11], 0\n\t"
        "ds_read_b64_tr_b16 a[32:33], v104 offset:16384\n\t"
        "ds_read_b64_tr_b16 a[34:35], v104 offset:16640\n\t"
        "v_mfma_f32_16x16x32_bf16 v[20:23], a[0:3], v[60:63], 0\n\t"
        "s_waitcnt lgkmcnt(5)\n\t"
        "ds_read_b64_tr_b16 a[36:37], v103 offset:20480\n\t"
        "v_mfma_f32_16x16x32_bf16 v[24:27], a[4:7], v[60:63], 0\n\t"
        "ds_read_b64_tr_b16 a[38:39], v103 offset:20992\n\t"
        "ds_read_b64_tr_b16 a[40:41], v103 offset:20488\n\t"
        "v_mfma_f32_16x16x32_bf16 v[28:31], v[60:63], a[8:11], 0\n\t"
        "ds_read_b64_tr_b16 a[42:43], v103 offset:21000\n\t"
        "ds_read_b64_tr_b16 a[44:45], v104 offset:18432\n\t"
        "v_mfma_f32_16x16x32_bf16 v[8:11], a[12:15], v[64:67], v[8:11]\n\t"
        "ds_read_b64_tr_b16 a[46:47], v104 offset:18688\n\t"
        "ds_read_b128 v[72:75], v105 offset:8192\n\t"
        "v_mfma_f32_16x16x32_bf16 v[12:15], a[16:19], v[64:67], v[12:15]\n\t"
        "ds_read_b128 v[76:79], v105 offset:10240\n\t"
        "ds_read_b128 v[80:83], v106 offset:8192\n\t"
        "v_mfma_f32_16x16x32_bf16 v[16:19], v[64:67], a[20:23], v[16:19]\n\t"
        "ds_read_b128 v[84:87], v106 offset:10240\n\t"
        "v_mfma_f32_16x16x32_bf16 v[20:23], a[12:15], v[68:71], v[20:23]\n\t"
        "v_mfma_f32_16x16x32_bf16 v[24:27], a[16:19], v[68:71], v[24:27]\n\t"
        "v_mfma_f32_16x16x32_bf16 v[28:31], v[68:71], a[20:23], v[28:31]\n\t"
        "s_mov_b32 %[t0], 2621440\n\t"
        "buffer_load_dwordx4 a[252:255], v101, %[sq], %[t0] offen\n\t"
        "buffer_load_dwordx4 v[132:135], v112, %[sv], %[t0] offen\n\t"
        "s_mov_b32 %[t0], 2883584\n\t"
        "buffer_load_dwordx4 v[128:131], v101, %[sq], %[t0] offen\n\t"
        "s_waitcnt vmcnt(17)\n\t"
        "ds_write_b128 v102, a[216:219] offset:0\n\t"
        "ds_write_b128 v102, a[220:223] offset:4096\n\t"
        "ds_write_b128 v116, a[224:227] offset:8192\n\t"
        "s_waitcnt lgkmcnt(0)\n\t"
        "s_barrier\n\t"
        "v_mfma_f32_16x16x32_bf16 v[8:11], a[24:27], v[72:75], v[8:11]\n\t"
        "ds_read_b64_tr_b16 a[48:49], v103 offset:0\n\t"
        "ds_read_b64_tr_b16 a[50:51], v103 offset:512\n\t"
        "v_mfma_f32_16x16x32_bf16 v[12:15], a[28:31], v[72:75], v[12:15]\n\t"
        "ds_read_b64_tr_b16 a[52:53], v103 offset:8\n\t"
        "ds_read_b64_tr_b16 a[54:55], v103 offset:520\n\t"
        "v_mfma_f32_16x16x32_bf16 v[16:19], v[72:75], a[32:35], v[16:19]\n\t"
        "ds_read_b64_tr_b16 a[56:57], v104 offset:0\n\t"
        "ds_read_b64_tr_b16 a[58:59], v104 offset:256\n\t"
        "v_mfma_f32_16x16x32_bf16 v[20:23], a[24:27], v[76:79], v[20:23]\n\t"
        "s_waitcnt lgkmcnt(5)\n\t"
        "ds_read_b64_tr_b16 a[60:61], v103 offset:4096\n\t"
        "v_mfma_f32_16x16x32_bf16 v[24:27], a[28:31], v[76:79], v[24:27]\n\t"
        "ds_read_b64_tr_b16 a[62:63], v103 offset:4608\n\t"
        "ds_read_b64_tr_b16 a[64:65], v103 offset:4104\n\t"
        "v_mfma_f32_16x16x32_bf16 v[28:31], v[76:79], a[32:35], v[28:31]\n\t"
        "ds_read_b64_tr_b16 a[66:67], v103 offset:4616\n\t"
        "ds_read_b64_tr_b16 a[68:69], v104 offset:2048\n\t"
        "v_mfma_f32_16x16x32_bf16 v[8:11], a[36:39], v[80:83], v[8:11]\n\t"
        "ds_read_b64_tr_b16 a[70:71], v104 offset:2304\n\t"
        "ds_read_b128 v[56:59], v105 offset:16384\n\t"
        "v_mfma_f32_16x16x32_bf16 v[12:15], a[40:43], v[80:83], v[12:15]\n\t"
        "ds_read_b128 v[60:63], v105 offset:18432\n\t"
        "ds_read_b128 v[64:67], v106 offset:16384\n\t"
        "v_mfma_f32_16x16x32_bf16 v[16:19], v[80:83], a[44:47], v[16:19]\n\t"
        "ds_read_b128 v[68:71], v106 offset:18432\n\t"
        "v_mfma_f32_16x16x32_bf16 v[20:23], a[36:39], v[84:87], v[20:23]\n\t"
        "v_mfma_f32_16x16x32_bf16 v[24:27], a[40:43], v[84:87], v[24:27]\n\t"
        "v_mfma_f32_16x16x32_bf16 v[28:31], v[84:87], a[44:47], v[28:31]\n\t"
        "s_mov_b32 %[t0], 3145728\n\t"
        "buffer_load_dwordx4 v[136:139], v101, %[sq], %[t0] offen\n\t"
        "buffer_load_dwordx4 v[144:147], v112, %[sv], %[t0] offen\n\t"
        "s_mov_b32 %[t0], 3407872\n\t"
        "buffer_load_dwordx4 v[140:143], v101, %[sq], %[t0] offen\n\t"
        "s_waitcnt vmcnt(17)\n\t"
        "ds_write_b128 v102, a[228:231] offset:16384\n\t"
        "ds_write_b128 v102, a[232:235] offset:20480\n\t"
        "ds_write_b128 v116, a[236:239] offset:24576\n\t"
        "s_waitcnt lgkmcnt(0)\n\t"
        "s_barrier\n\t"
        "v_mfma_f32_16x16x32_bf16 v[8:11], a[48:51], v[56:59], v[8:11]\n\t"
        "ds_read_b64_tr_b16 a[72:73], v103 offset:16384\n\t"
        "ds_read_b64_tr_b16 a[74:75], v103 offset:16896\n\t"
        "v_mfma_f32_16x16x32_bf16 v[12:15], a[52:55], v[56:59], v[12:15]\n\t"
        "ds_read_b64_tr_b16 a[76:77], v103 offset:16392\n\t"
        "ds_read_b64_tr_b16 a[78:79], v103 offset:16904\n\t"
        "v_mfma_f32_16x16x32_bf16 v[16:19], v[56:59], a[56:59], v[16:19]\n\t"
        "ds_read_b64_tr_b16 a[80:81], v104 offset:16384\n\t"
        "ds_read_b64_tr_b16 a[82:83], v104 offset:16640\n\t"
        "v_mfma_f32_16x16x32_bf16 v[20:23], a[48:51], v[60:63], v[20:23]\n\t"
        "s_waitcnt lgkmcnt(5)\n\t"
        "ds_read_b64_tr_b16 a[84:85], v103 offset:20480\n\t"
        "v_mfma_f32_16x16x32_bf16 v[24:27], a[52:55], v[60:63], v[24:27]\n\t"
        "ds_read_b64_tr_b16 a[86:87], v103 offset:20992\n\t"
        "ds_read_b64_tr_b16 a[88:89], v103 offset:20488\n\t"
        "v_mfma_f32_16x16x32_bf16 v[28:31], v[60:63], a[56:59], v[28:31]\n\t"
        "ds_read_b64_tr_b16 a[90:91], v103 offset:21000\n\t"
        "ds_read_b64_tr_b16 a[92:93], v104 offset:18432\n\t"
        "v_mfma_f32_16x16x32_bf16 v[8:11], a[60:63], v[64:67], v[8:11]\n\t"
        "ds_read_b64_tr_b16 a[94:95], v104 offset:18688\n\t"
        "ds_read_b128 v[72:75], v105 offset:24576\n\t"
        "v_mfma_f32_16x16x32_bf16 v[12:15], a[64:67], v[64:67], v[12:15]\n\t"
        "ds_read_b128 v[76:79], v105 offset:26624\n\t"
        "ds_read_b128 v[80:83], v106 offset:24576\n\t"
        "v_mfma_f32_16x16x32_bf16 v[16:19], v[64:67], a[68:71], v[16:19]\n\t"
        "ds_read_b128 v[84:87], v106 offset:26624\n\t"
        "v_mfma_f32_16x16x32_bf16 v[20:23], a[60:63], v[68:71], v[20:23]\n\t"
        "v_mfma_f32_16x16x32_bf16 v[24:27], a[64:67], v[68:71], v[24:27]\n\t"
        "v_mfma_f32_16x16x32_bf16 v[28:31], v[68:71], a[68:71], v[28:31]\n\t"
        "s_mov_b32 %[t0], 3670016\n\t"
        "buffer_load_dwordx4 v[148:151], v101, %[sq], %[t0] offen\n\t"
        "buffer_load_dwordx4 v[156:159], v112, %[sv], %[t0] offen\n\t"
        "s_mov_b32 %[t0], 3932160\n\t"
        "buffer_load_dwordx4 v[152:155], v101, %[sq], %[t0] offen\n\t"
        "s_waitcnt vmcnt(9)\n\t"
        "ds_write_b128 v102, a[240:243] offset:0\n\t"
        "ds_write_b128 v102, a[244:247] offset:4096\n\t"
        "ds_write_b128 v116, a[248:251] offset:8192\n\t"
        "s_waitcnt lgkmcnt(0)\n\t"
        "s_barrier\n\t"
        "v_mfma_f32_16x16x32_bf16 v[8:11], a[72:75], v[72:75], v[8:11]\n\t"
        "ds_read_b64_tr_b16 a[96:97], v103 offset:0\n\t"
        "ds_read_b64_tr_b16 a[98:99], v103 offset:512\n\t"
        "v_mfma_f32_16x16x32_bf16 v[12:15], a[76:79], v[72:75], v[12:15]\n\t"
        "ds_read_b64_tr_b16 a[100:101], v103 offset:8\n\t"
        "ds_read_b64_tr_b16 a[102:103], v103 offset:520\n\t"
        "v_mfma_f32_16x16x32_bf16 v[16:19], v[72:75], a[80:83], v[16:19]\n\t"
        "ds_read_b64_tr_b16 a[104:105], v104 offset:0\n\t"
        "ds_read_b64_tr_b16 a[106:107], v104 offset:256\n\t"
        "v_mfma_f32_16x16x32_bf16 v[20:23], a[72:75], v[76:79], v[20:23]\n\t"
        "s_waitcnt lgkmcnt(5)\n\t"
        "ds_read_b64_tr_b16 a[108:109], v103 offset:4096\n\t"
        "v_mfma_f32_16x16x32_bf16 v[24:27], a[76:79], v[76:79], v[24:27]\n\t"
        "ds_read_b64_tr_b16 a[110:111], v103 offset:4608\n\t"
        "ds_read_b64_tr_b16 a[112:113], v103 offset:4104\n\t"
        "v_mfma_f32_16x16x32_bf16 v[28:31], v[76:79], a[80:83], v[28:31]\n\t"
        "ds_read_b64_tr_b16 a[114:115], v103 offset:4616\n\t"
        "ds_read_b64_tr_b16 a[116:117], v104 offset:2048\n\t"
        "v_mfma_f32_16x16x32_bf16 v[8:11], a[84:87], v[80:83], v[8:11]\n\t"
        "ds_read_b64_tr_b16 a[118:119], v104 offset:2304\n\t"
        "ds_read_b128 v[56:59], v105 offset:32768\n\t"
        "v_mfma_f32_16x16x32_bf16 v[12:15], a[88:91], v[80:83], v[12:15]\n\t"
        "ds_read_b128 v[60:63], v105 offset:34816\n\t"
        "ds_read_b128 v[64:67], v106 offset:32768\n\t"
        "v_mfma_f32_16x16x32_bf16 v[16:19], v[80:83], a[92:95], v[16:19]\n\t"
        "ds_read_b128 v[68:71], v106 offset:34816\n\t"
        "v_mfma_f32_16x16x32_bf16 v[20:23], a[84:87], v[84:87], v[20:23]\n\t"
        "v_mfma_f32_16x16x32_bf16 v[24:27], a[88:91], v[84:87], v[24:27]\n\t"
        "v_mfma_f32_16x16x32_bf16 v[28:31], v[84:87], a[92:95], v[28:31]\n\t"
        "s_add_u32 m0, %[ld], 65536\n\t"
        "s_add_u32 %[t0], %[xo], 1024\n\t"
        "buffer_load_dwordx4 v100, %[sx], %[t0] offen lds\n\t"
        "s_add_u32 m0, %[ld], 66560\n\t"
        "s_add_u32 %[t0], %[xo], 17408\n\t"
        "buffer_load_dwordx4 v100, %[sx], %[t0] offen lds\n\t"
        "s_add_u32 m0, %[ld], 73728\n\t"
        "s_add_u32 %[t0], %[xo], 1152\n\t"
        "buffer_load_dwordx4 v100, %[sx], %[t0] offen lds\n\t"
        "s_add_u32 m0, %[ld], 74752\n\t"
        "s_add_u32 %[t0], %[xo], 17536\n\t"
        "buffer_load_dwordx4 v100, %[sx], %[t0] offen lds\n\t"
        "s_add_u32 m0, %[ld], 81920\n\t"
        "s_add_u32 %[t0], %[xo], 1280\n\t"
        "buffer_load_dwordx4 v100, %[sx], %[t0] offen lds\n\t"
        "s_add_u32 m0, %[ld], 82944\n\t"
        "s_add_u32 %[t0], %[xo], 17664\n\t"
        "buffer_load_dwordx4 v100, %[sx], %[t0] offen lds\n\t"
        "s_add_u32 m0, %[ld], 90112\n\t"
        "s_add_u32 %[t0], %[xo], 1408\n\t"
        "buffer_load_dwordx4 v100, %[sx], %[t0] offen lds\n\t"
        "s_add_u32 m0, %[ld], 91136\n\t"
        "s_add_u32 %[t0], %[xo], 17792\n\t"
        "buffer_load_dwordx4 v100, %[sx], %[t0] offen lds\n\t"
        "s_mov_b32 %[t0], 4194304\n\t"
        "buffer_load_dwordx4 v[160:163], v101, %[sq], %[t0] offen\n\t"
        "buffer_load_dwordx4 v[168:171], v112, %[sv], %[t0] offen\n\t"
        "s_mov_b32 %[t0], 4456448\n\t"
        "buffer_load_dwordx4 v[164:167], v101, %[sq], %[t0] offen\n\t"
        "s_waitcnt vmcnt(17)\n\t"
        "ds_write_b128 v102, a[252:255] offset:16384\n\t"
        "ds_write_b128 v102, v[128:131] offset:20480\n\t"
        "ds_write_b128 v116, v[132:135] offset:24576\n\t"
        "s_waitcnt lgkmcnt(0)\n\t"
        "s_barrier\n\t"
        "v_mfma_f32_16x16x32_bf16 v[8:11], a[96:99], v[56:59], v[8:11]\n\t"
        "ds_read_b64_tr_b16 a[120:121], v103 offset:16384\n\t"
        "ds_read_b64_tr_b16 a[122:123], v103 offset:16896\n\t"
        "v_mfma_f32_16x16x32_bf16 v[12:15], a[100:103], v[56:59], v[12:15]\n\t"
        "ds_read_b64_tr_b16 a[124:125], v103 offset:16392\n\t"
        "ds_read_b64_tr_b16 a[126:127], v103 offset:16904\n\t"
        "v_mfma_f32_16x16x32_bf16 v[16:19], v[56:59], a[104:107], v[16:19]\n\t"
        "ds_read_b64_tr_b16 a[128:129], v104 offset:16384\n\t"
        "ds_read_b64_tr_b16 a[130:131], v104 offset:16640\n\t"
        "v_mfma_f32_16x16x32_bf16 v[20:23], a[96:99], v[60:63], v[20:23]\n\t"
        "s_waitcnt lgkmcnt(5)\n\t"
        "ds_read_b64_tr_b16 a[132:133], v103 offset:20480\n\t"
        "v_mfma_f32_16x16x32_bf16 v[24:27], a[100:103], v[60:63], v[24:27]\n\t"
        "ds_read_b64_tr_b16 a[134:135], v103 offset:20992\n\t"
        "ds_read_b64_tr_b16 a[136:137], v103 offset:20488\n\t"
        "v_mfma_f32_16x16x32_bf16 v[28:31], v[60:63], a[104:107], v[28:31]\n\t"
        "ds_read_b64_tr_b16 a[138:139], v103 offset:21000\n\t"
        "ds_read_b64_tr_b16 a[140:141], v104 offset:18432\n\t"
        "v_mfma_f32_16x16x32_bf16 v[8:11], a[108:111], v[64:67], v[8:11]\n\t"
        "ds_read_b64_tr_b16 a[142:143], v104 offset:18688\n\t"
        "ds_read_b128 v[72:75], v105 offset:40960\n\t"
        "v_mfma_f32_16x16x32_bf16 v[12:15], a[112:115], v[64:67], v[12:15]\n\t"
        "ds_read_b128 v[76:79], v105 offset:43008\n\t"
        "ds_read_b128 v[80:83], v106 offset:40960\n\t"
        "v_mfma_f32_16x16x32_bf16 v[16:19], v[64:67], a[116:119], v[16:19]\n\t"
        "ds_read_b128 v[84:87], v106 offset:43008\n\t"
        "v_mfma_f32_16x16x32_bf16 v[20:23], a[108:111], v[68:71], v[20:23]\n\t"
        "v_mfma_f32_16x16x32_bf16 v[24:27], a[112:115], v[68:71], v[24:27]\n\t"
        "v_mfma_f32_16x16x32_bf16 v[28:31], v[68:71], a[116:119], v[28:31]\n\t"
        "s_mov_b32 %[t0], 4718592\n\t"
        "buffer_load_dwordx4 v[172:175], v101, %[sq], %[t0] offen\n\t"
        "buffer_load_dwordx4 v[180:183], v112, %[sv], %[t0] offen\n\t"
        "s_mov_b32 %[t0], 4980736\n\t"
        "buffer_load_dwordx4 v[176:179], v101, %[sq], %[t0] offen\n\t"
        "s_waitcnt vmcnt(17)\n\t"
        "ds_write_b128 v102, v[136:139] offset:0\n\t"
        "ds_write_b128 v102, v[140:143] offset:4096\n\t"
        "ds_write_b128 v116, v[144:147] offset:8192\n\t"
        "s_waitcnt lgkmcnt(0)\n\t"
        "s_barrier\n\t"
        "v_mfma_f32_16x16x32_bf16 v[8:11], a[120:123], v[72:75], v[8:11]\n\t"
        "ds_read_b64_tr_b16 a[144:145], v103 offset:0\n\t"
        "ds_read_b64_tr_b16 a[146:147], v103 offset:512\n\t"
        "v_mfma_f32_16x16x32_bf16 v[12:15], a[124:127], v[72:75], v[12:15]\n\t"
        "ds_read_b64_tr_b16 a[148:149], v103 offset:8\n\t"
        "ds_read_b64_tr_b16 a[150:151], v103 offset:520\n\t"
        "v_mfma_f32_16x16x32_bf16 v[16:19], v[72:75], a[128:131], v[16:19]\n\t"
        "ds_read_b64_tr_b16 a[152:153], v104 offset:0\n\t"
        "ds_read_b64_tr_b16 a[154:155], v104 offset:256\n\t"
        "v_mfma_f32_16x16x32_bf16 v[20:23], a[120:123], v[76:79], v[20:23]\n\t"
        "s_waitcnt lgkmcnt(5)\n\t"
        "ds_read_b64_tr_b16 a[156:157], v103 offset:4096\n\t"
        "v_mfma_f32_16x16x32_bf16 v[24:27], a[124:127], v[76:79], v[24:27]\n\t"
        "ds_read_b64_tr_b16 a[158:159], v103 offset:4608\n\t"
        "ds_read_b64_tr_b16 a[160:161], v103 offset:4104\n\t"
        "v_mfma_f32_16x16x32_bf16 v[28:31], v[76:79], a[128:131], v[28:31]\n\t"
        "ds_read_b64_tr_b16 a[162:163], v103 offset:4616\n\t"
        "ds_read_b64_tr_b16 a[164:165], v104 offset:2048\n\t"
        "v_mfma_f32_16x16x32_bf16 v[8:11], a[132:135], v[80:83], v[8:11]\n\t"
        "ds_read_b64_tr_b16 a[166:167], v104 offset:2304\n\t"
        "ds_read_b128 v[56:59], v105 offset:49152\n\t"
        "v_mfma_f32_16x16x32_bf16 v[12:15], a[136:139], v[80:83], v[12:15]\n\t"
        "ds_read_b128 v[60:63], v105 offset:51200\n\t"
        "ds_read_b128 v[64:67], v106 offset:49152\n\t"
        "v_mfma_f32_16x16x32_bf16 v[16:19], v[80:83], a[140:143], v[16:19]\n\t"
        "ds_read_b128 v[68:71], v106 offset:51200\n\t"
        "v_mfma_f32_16x16x32_bf16 v[20:23], a[132:135], v[84:87], v[20:23]\n\t"
        "v_mfma_f32_16x16x32_bf16 v[24:27], a[136:139], v[84:87], v[24:27]\n\t"
        "v_mfma_f32_16x16x32_bf16 v[28:31], v[84:87], a[140:143], v[28:31]\n\t"
        "s_mov_b32 %[t0], 5242880\n\t"
        "buffer_load_dwordx4 v[184:187], v101, %[sq], %[t0] offen\n\t"
        "buffer_load_dwordx4 v[192:195], v112, %[sv], %[t0] offen\n\t"
        "s_mov_b32 %[t0], 5505024\n\t"
        "buffer_load_dwordx4 v[188:191], v101, %[sq], %[t0] offen\n\t"
        "s_waitcnt vmcnt(17)\n\t"
        "ds_write_b128 v102, v[148:151] offset:16384\n\t"
        "ds_write_b128 v102, v[152:155] offset:20480\n\t"
        "ds_write_b128 v116, v[156:159] offset:24576\n\t"
        "s_waitcnt lgkmcnt(0)\n\t"
        "s_barrier\n\t"
        "v_mfma_f32_16x16x32_bf16 v[8:11], a[144:147], v[56:59], v[8:11]\n\t"
        "ds_read_b64_tr_b16 a[168:169], v103 offset:16384\n\t"
        "ds_read_b64_tr_b16 a[170:171], v103 offset:16896\n\t"
        "v_mfma_f32_16x16x32_bf16 v[12:15], a[148:151], v[56:59], v[12:15]\n\t"
        "ds_read_b64_tr_b16 a[172:173], v103 offset:16392\n\t"
        "ds_read_b64_tr_b16 a[174:175], v103 offset:16904\n\t"
        "v_mfma_f32_16x16x32_bf16 v[16:19], v[56:59], a[152:155], v[16:19]\n\t"
        "ds_read_b64_tr_b16 a[176:177], v104 offset:16384\n\t"
        "ds_read_b64_tr_b16 a[178:179], v104 offset:16640\n\t"
        "v_mfma_f32_16x16x32_bf16 v[20:23], a[144:147], v[60:63], v[20:23]\n\t"
        "s_waitcnt lgkmcnt(5)\n\t"
        "ds_read_b64_tr_b16 a[180:181], v103 offset:20480\n\t"
        "v_mfma_f32_16x16x32_bf16 v[24:27], a[148:151], v[60:63], v[24:27]\n\t"
        "ds_read_b64_tr_b16 a[182:183], v103 offset:20992\n\t"
        "ds_read_b64_tr_b16 a[184:185], v103 offset:20488\n\t"
        "v_mfma_f32_16x16x32_bf16 v[28:31], v[60:63], a[152:155], v[28:31]\n\t"
        "ds_read_b64_tr_b16 a[186:187], v103 offset:21000\n\t"
        "ds_read_b64_tr_b16 a[188:189], v104 offset:18432\n\t"
        "v_mfma_f32_16x16x32_bf16 v[8:11], a[156:159], v[64:67], v[8:11]\n\t"
        "ds_read_b64_tr_b16 a[190:191], v104 offset:18688\n\t"
        "ds_read_b128 v[72:75], v105 offset:57344\n\t"
        "v_mfma_f32_16x16x32_bf16 v[12:15], a[160:163], v[64:67], v[12:15]\n\t"
        "ds_read_b128 v[76:79], v105 offset:59392\n\t"
        "ds_read_b128 v[80:83], v106 offset:57344\n\t"
        "v_mfma_f32_16x16x32_bf16 v[16:19], v[64:67], a[164:167], v[16:19]\n\t"
        "ds_read_b128 v[84:87], v106 offset:59392\n\t"
        "v_mfma_f32_16x16x32_bf16 v[20:23], a[156:159], v[68:71], v[20:23]\n\t"
        "v_mfma_f32_16x16x32_bf16 v[24:27], a[160:163], v[68:71], v[24:27]\n\t"
        "v_mfma_f32_16x16x32_bf16 v[28:31], v[68:71], a[164:167], v[28:31]\n\t"
        "s_mov_b32 %[t0], 5767168\n\t"
        "buffer_load_dwordx4 v[196:199], v101, %[sq], %[t0] offen\n\t"
        "buffer_load_dwordx4 v[204:207], v112, %[sv], %[t0] offen\n\t"
        "s_mov_b32 %[t0], 6029312\n\t"
        "buffer_load_dwordx4 v[200:203], v101, %[sq], %[t0] offen\n\t"
        "s_waitcnt vmcnt(9)\n\t"
        "ds_write_b128 v102, v[160:163] offset:0\n\t"
        "ds_write_b128 v102, v[164:167] offset:4096\n\t"
        "ds_write_b128 v116, v[168:171] offset:8192\n\t"
        "s_waitcnt lgkmcnt(0)\n\t"
        "s_barrier\n\t"
        "v_mfma_f32_16x16x32_bf16 v[8:11], a[168:171], v[72:75], v[8:11]\n\t"
        "ds_read_b64_tr_b16 a[192:193], v103 offset:0\n\t"
        "ds_read_b64_tr_b16 a[194:195], v103 offset:512\n\t"
        "v_mfma_f32_16x16x32_bf16 v[12:15], a[172:175], v[72:75], v[12:15]\n\t"
        "ds_read_b64_tr_b16 a[196:197], v103 offset:8\n\t"
        "ds_read_b64_tr_b16 a[198:199], v103 offset:520\n\t"
        "v_mfma_f32_16x16x32_bf16 v[16:19], v[72:75], a[176:179], v[16:19]\n\t"
        "ds_read_b64_tr_b16 a[200:201], v104 offset:0\n\t"
        "ds_read_b64_tr_b16 a[202:203], v104 offset:256\n\t"
        "v_mfma_f32_16x16x32_bf16 v[20:23], a[168:171], v[76:79], v[20:23]\n\t"
        "s_waitcnt lgkmcnt(5)\n\t"
        "ds_read_b64_tr_b16 a[204:205], v103 offset:4096\n\t"
        "v_mfma_f32_16x16x32_bf16 v[24:27], a[172:175], v[76:79], v[24:27]\n\t"
        "ds_read_b64_tr_b16 a[206:207], v103 offset:4608\n\t"
        "ds_read_b64_tr_b16 a[208:209], v103 offset:4104\n\t"
        "v_mfma_f32_16x16x32_bf16 v[28:31], v[76:79], a[176:179], v[28:31]\n\t"
        "ds_read_b64_tr_b16 a[210:211], v103 offset:4616\n\t"
        "ds_read_b64_tr_b16 a[212:213], v104 offset:2048\n\t"
        "v_mfma_f32_16x16x32_bf16 v[8:11], a[180:183], v[80:83], v[8:11]\n\t"
        "ds_read_b64_tr_b16 a[214:215], v104 offset:2304\n\t"
        "ds_read_b128 v[56:59], v107 offset:0\n\t"
        "v_mfma_f32_16x16x32_bf16 v[12:15], a[184:187], v[80:83], v[12:15]\n\t"
        "ds_read_b128 v[60:63], v107 offset:2048\n\t"
        "ds_read_b128 v[64:67], v108 offset:0\n\t"
        "v_mfma_f32_16x16x32_bf16 v[16:19], v[80:83], a[188:191], v[16:19]\n\t"
        "ds_read_b128 v[68:71], v108 offset:2048\n\t"
        "v_mfma_f32_16x16x32_bf16 v[20:23], a[180:183], v[84:87], v[20:23]\n\t"
        "v_mfma_f32_16x16x32_bf16 v[24:27], a[184:187], v[84:87], v[24:27]\n\t"
        "v_mfma_f32_16x16x32_bf16 v[28:31], v[84:87], a[188:191], v[28:31]\n\t"
        "s_add_u32 m0, %[ld], 98304\n\t"
        "s_add_u32 %[t0], %[xo], 1536\n\t"
        "buffer_load_dwordx4 v100, %[sx], %[t0] offen lds\n\t"
        "s_add_u32 m0, %[ld], 99328\n\t"
        "s_add_u32 %[t0], %[xo], 17920\n\t"
        "buffer_load_dwordx4 v100, %[sx], %[t0] offen lds\n\t"
        "s_add_u32 m0, %[ld], 106496\n\t"
        "s_add_u32 %[t0], %[xo], 1664\n\t"
        "buffer_load_dwordx4 v100, %[sx], %[t0] offen lds\n\t"
        "s_add_u32 m0, %[ld], 107520\n\t"
        "s_add_u32 %[t0], %[xo], 18048\n\t"
        "buffer_load_dwordx4 v100, %[sx], %[t0] offen lds\n\t"
        "s_add_u32 m0, %[ld], 114688\n\t"
        "s_add_u32 %[t0], %[xo], 1792\n\t"
        "buffer_load_dwordx4 v100, %[sx], %[t0] offen lds\n\t"
        "s_add_u32 m0, %[ld], 115712\n\t"
        "s_add_u32 %[t0], %[xo], 18176\n\t"
        "buffer_load_dwordx4 v100, %[sx], %[t0] offen lds\n\t"
        "s_add_u32 m0, %[ld], 122880\n\t"
        "s_add_u32 %[t0], %[xo], 1920\n\t"
        "buffer_load_dwordx4 v100, %[sx], %[t0] offen lds\n\t"
        "s_add_u32 m0, %[ld], 123904\n\t"
        "s_add_u32 %[t0], %[xo], 18304\n\t"
        "buffer_load_dwordx4 v100, %[sx], %[t0] offen lds\n\t"
        "s_mov_b32 %[t0], 6291456\n\t"
        "buffer_load_dwordx4 v[208:211], v101, %[sq], %[t0] offen\n\t"
        "buffer_load_dwordx4 v[216:219], v112, %[sv], %[t0] offen\n\t"
        "s_mov_b32 %[t0], 6553600\n\t"
        "buffer_load_dwordx4 v[212:215], v101, %[sq], %[t0] offen\n\t"
        "s_waitcnt vmcnt(17)\n\t"
        "ds_write_b128 v102, v[172:175] offset:16384\n\t"
        "ds_write_b128 v102, v[176:179] offset:20480\n\t"
        "ds_write_b128 v116, v[180:183] offset:24576\n\t"
        "s_waitcnt lgkmcnt(0)\n\t"
        "s_barrier\n\t"
        "v_mfma_f32_16x16x32_bf16 v[8:11], a[192:195], v[56:59], v[8:11]\n\t"
        "ds_read_b64_tr_b16 a[216:217], v103 offset:16384\n\t"
        "ds_read_b64_tr_b16 a[218:219], v103 offset:16896\n\t"
        "v_mfma_f32_16x16x32_bf16 v[12:15], a[196:199], v[56:59], v[12:15]\n\t"
        "ds_read_b64_tr_b16 a[220:221], v103 offset:16392\n\t"
        "ds_read_b64_tr_b16 a[222:223], v103 offset:16904\n\t"
        "v_mfma_f32_16x16x32_bf16 v[16:19], v[56:59], a[200:203], v[16:19]\n\t"
        "ds_read_b64_tr_b16 a[224:225], v104 offset:16384\n\t"
        "ds_read_b64_tr_b16 a[226:227], v104 offset:16640\n\t"
        "v_mfma_f32_16x16x32_bf16 v[20:23], a[192:195], v[60:63], v[20:23]\n\t"
        "s_waitcnt lgkmcnt(5)\n\t"
        "ds_read_b64_tr_b16 a[228:229], v103 offset:20480\n\t"
        "v_mfma_f32_16x16x32_bf16 v[24:27], a[196:199], v[60:63], v[24:27]\n\t"
        "ds_read_b64_tr_b16 a[230:231], v103 offset:20992\n\t"
        "ds_read_b64_tr_b16 a[232:233], v103 offset:20488\n\t"
        "v_mfma_f32_16x16x32_bf16 v[28:31], v[60:63], a[200:203], v[28:31]\n\t"
        "ds_read_b64_tr_b16 a[234:235], v103 offset:21000\n\t"
        "ds_read_b64_tr_b16 a[236:237], v104 offset:18432\n\t"
        "v_mfma_f32_16x16x32_bf16 v[8:11], a[204:207], v[64:67], v[8:11]\n\t"
        "ds_read_b64_tr_b16 a[238:239], v104 offset:18688\n\t"
        "ds_read_b128 v[72:75], v107 offset:8192\n\t"
        "v_mfma_f32_16x16x32_bf16 v[12:15], a[208:211], v[64:67], v[12:15]\n\t"
        "ds_read_b128 v[76:79], v107 offset:10240\n\t"
        "ds_read_b128 v[80:83], v108 offset:8192\n\t"
        "v_mfma_f32_16x16x32_bf16 v[16:19], v[64:67], a[212:215], v[16:19]\n\t"
        "ds_read_b128 v[84:87], v108 offset:10240\n\t"
        "v_mfma_f32_16x16x32_bf16 v[20:23], a[204:207], v[68:71], v[20:23]\n\t"
        "v_mfma_f32_16x16x32_bf16 v[24:27], a[208:211], v[68:71], v[24:27]\n\t"
        "v_mfma_f32_16x16x32_bf16 v[28:31], v[68:71], a[212:215], v[28:31]\n\t"
        "s_mov_b32 %[t0], 6815744\n\t"
        "buffer_load_dwordx4 v[220:223], v101, %[sq], %[t0] offen\n\t"
        "buffer_load_dwordx4 v[228:231], v112, %[sv], %[t0] offen\n\t"
        "s_mov_b32 %[t0], 7077888\n\t"
        "buffer_load_dwordx4 v[224:227], v101, %[sq], %[t0] offen\n\t"
        "s_waitcnt vmcnt(17)\n\t"
        "ds_write_b128 v102, v[184:187] offset:0\n\t"
        "ds_write_b128 v102, v[188:191] offset:4096\n\t"
        "ds_write_b128 v116, v[192:195] offset:8192\n\t"
        "s_waitcnt lgkmcnt(0)\n\t"
        "s_barrier\n\t"
        "v_mfma_f32_16x16x32_bf16 v[8:11], a[216:219], v[72:75], v[8:11]\n\t"
        "ds_read_b64_tr_b16 a[240:241], v103 offset:0\n\t"
        "ds_read_b64_tr_b16 a[242:243], v103 offset:512\n\t"
        "v_mfma_f32_16x16x32_bf16 v[12:15], a[220:223], v[72:75], v[12:15]\n\t"
        "ds_read_b64_tr_b16 a[244:245], v103 offset:8\n\t"
        "ds_read_b64_tr_b16 a[246:247], v103 offset:520\n\t"
        "v_mfma_f32_16x16x32_bf16 v[16:19], v[72:75], a[224:227], v[16:19]\n\t"
        "ds_read_b64_tr_b16 a[248:249], v104 offset:0\n\t"
        "ds_read_b64_tr_b16 a[250:251], v104 offset:256\n\t"
        "v_mfma_f32_16x16x32_bf16 v[20:23], a[216:219], v[76:79], v[20:23]\n\t"
        "s_waitcnt lgkmcnt(5)\n\t"
        "ds_read_b64_tr_b16 a[252:253], v103 offset:4096\n\t"
        "v_mfma_f32_16x16x32_bf16 v[24:27], a[220:223], v[76:79], v[24:27]\n\t"
        "ds_read_b64_tr_b16 a[254:255], v103 offset:4608\n\t"
        "ds_read_b64_tr_b16 v[128:129], v103 offset:4104\n\t"
        "v_mfma_f32_16x16x32_bf16 v[28:31], v[76:79], a[224:227], v[28:31]\n\t"
        "ds_read_b64_tr_b16 v[130:131], v103 offset:4616\n\t"
        "ds_read_b64_tr_b16 v[132:133], v104 offset:2048\n\t"
        "v_mfma_f32_16x16x32_bf16 v[8:11], a[228:231], v[80:83], v[8:11]\n\t"
        "ds_read_b64_tr_b16 v[134:135], v104 offset:2304\n\t"
        "ds_read_b128 v[56:59], v107 offset:16384\n\t"
        "v_mfma_f32_16x16x32_bf16 v[12:15], a[232:235], v[80:83], v[12:15]\n\t"
        "ds_read_b128 v[60:63], v107 offset:18432\n\t"
        "ds_read_b128 v[64:67], v108 offset:16384\n\t"
        "v_mfma_f32_16x16x32_bf16 v[16:19], v[80:83], a[236:239], v[16:19]\n\t"
        "ds_read_b128 v[68:71], v108 offset:18432\n\t"
        "v_mfma_f32_16x16x32_bf16 v[20:23], a[228:231], v[84:87], v[20:23]\n\t"
        "v_mfma_f32_16x16x32_bf16 v[24:27], a[232:235], v[84:87], v[24:27]\n\t"
        "v_mfma_f32_16x16x32_bf16 v[28:31], v[84:87], a[236:239], v[28:31]\n\t"
        "s_mov_b32 %[t0], 7340032\n\t"
        "buffer_load_dwordx4 v[232:235], v101, %[sq], %[t0] offen\n\t"
        "buffer_load_dwordx4 v[240:243], v112, %[sv], %[t0] offen\n\t"
        "s_mov_b32 %[t0], 7602176\n\t"
        "buffer_load_dwordx4 v[236:239], v101, %[sq], %[t0] offen\n\t"
        "s_waitcnt vmcnt(17)\n\t"
        "ds_write_b128 v102, v[196:199] offset:16384\n\t"
        "ds_write_b128 v102, v[200:203] offset:20480\n\t"
        "ds_write_b128 v116, v[204:207] offset:24576\n\t"
        "s_waitcnt lgkmcnt(0)\n\t"
        "s_barrier\n\t"
        "v_mfma_f32_16x16x32_bf16 v[8:11], a[240:243], v[56:59], v[8:11]\n\t"
        "ds_read_b64_tr_b16 v[136:137], v103 offset:16384\n\t"
        "ds_read_b64_tr_b16 v[138:139], v103 offset:16896\n\t"
        "v_mfma_f32_16x16x32_bf16 v[12:15], a[244:247], v[56:59], v[12:15]\n\t"
        "ds_read_b64_tr_b16 v[140:141], v103 offset:16392\n\t"
        "ds_read_b64_tr_b16 v[142:143], v103 offset:16904\n\t"
        "v_mfma_f32_16x16x32_bf16 v[16:19], v[56:59], a[248:251], v[16:19]\n\t"
        "ds_read_b64_tr_b16 v[144:145], v104 offset:16384\n\t"
        "ds_read_b64_tr_b16 v[146:147], v104 offset:16640\n\t"
        "v_mfma_f32_16x16x32_bf16 v[20:23], a[240:243], v[60:63], v[20:23]\n\t"
        "s_waitcnt lgkmcnt(5)\n\t"
        "ds_read_b64_tr_b16 v[148:149], v103 offset:20480\n\t"
        "v_mfma_f32_16x16x32_bf16 v[24:27], a[244:247], v[60:63], v[24:27]\n\t"
        "ds_read_b64_tr_b16 v[150:151], v103 offset:20992\n\t"
        "ds_read_b64_tr_b16 v[152:153], v103 offset:20488\n\t"
        "v_mfma_f32_16x16x32_bf16 v[28:31], v[60:63], a[248:251], v[28:31]\n\t"
        "ds_read_b64_tr_b16 v[154:155], v103 offset:21000\n\t"
        "ds_read_b64_tr_b16 v[156:157], v104 offset:18432\n\t"
        "v_mfma_f32_16x16x32_bf16 v[8:11], a[252:255], v[64:67], v[8:11]\n\t"
        "ds_read_b64_tr_b16 v[158:159], v104 offset:18688\n\t"
        "ds_read_b128 v[72:75], v107 offset:24576\n\t"
        "v_mfma_f32_16x16x32_bf16 v[12:15], v[128:131], v[64:67], v[12:15]\n\t"
        "ds_read_b128 v[76:79], v107 offset:26624\n\t"
        "ds_read_b128 v[80:83], v108 offset:24576\n\t"
        "v_mfma_f32_16x16x32_bf16 v[16:19], v[64:67], v[132:135], v[16:19]\n\t"
        "ds_read_b128 v[84:87], v108 offset:26624\n\t"
        "v_mfma_f32_16x16x32_bf16 v[20:23], a[252:255], v[68:71], v[20:23]\n\t"
        "v_mfma_f32_16x16x32_bf16 v[24:27], v[128:131], v[68:71], v[24:27]\n\t"
        "v_mfma_f32_16x16x32_bf16 v[28:31], v[68:71], v[132:135], v[28:31]\n\t"
        "s_mov_b32 %[t0], 7864320\n\t"
        "buffer_load_dwordx4 v[244:247], v101, %[sq], %[t0] offen\n\t"
        "buffer_load_dwordx4 v[252:255], v112, %[sv], %[t0] offen\n\t"
        "s_mov_b32 %[t0], 8126464\n\t"
        "buffer_load_dwordx4 v[248:251], v101, %[sq], %[t0] offen\n\t"
        "s_waitcnt vmcnt(9)\n\t"
        "ds_write_b128 v102, v[208:211] offset:0\n\t"
        "ds_write_b128 v102, v[212:215] offset:4096\n\t"
        "ds_write_b128 v116, v[216:219] offset:8192\n\t"
        "s_waitcnt lgkmcnt(0)\n\t"
        "s_barrier\n\t"
        "v_mfma_f32_16x16x32_bf16 v[8:11], v[136:139], v[72:75], v[8:11]\n\t"
        "ds_read_b64_tr_b16 v[160:161], v103 offset:0\n\t"
        "ds_read_b64_tr_b16 v[162:163], v103 offset:512\n\t"
        "v_mfma_f32_16x16x32_bf16 v[12:15], v[140:143], v[72:75], v[12:15]\n\t"
        "ds_read_b64_tr_b16 v[164:165], v103 offset:8\n\t"
        "ds_read_b64_tr_b16 v[166:167], v103 offset:520\n\t"
        "v_mfma_f32_16x16x32_bf16 v[16:19], v[72:75], v[144:147], v[16:19]\n\t"
        "ds_read_b64_tr_b16 v[168:169], v104 offset:0\n\t"
        "ds_read_b64_tr_b16 v[170:171], v104 offset:256\n\t"
        "v_mfma_f32_16x16x32_bf16 v[20:23], v[136:139], v[76:79], v[20:23]\n\t"
        "s_waitcnt lgkmcnt(5)\n\t"
        "ds_read_b64_tr_b16 v[172:173], v103 offset:4096\n\t"
        "v_mfma_f32_16x16x32_bf16 v[24:27], v[140:143], v[76:79], v[24:27]\n\t"
        "ds_read_b64_tr_b16 v[174:175], v103 offset:4608\n\t"
        "ds_read_b64_tr_b16 v[176:177], v103 offset:4104\n\t"
        "v_mfma_f32_16x16x32_bf16 v[28:31], v[76:79], v[144:147], v[28:31]\n\t"
        "ds_read_b64_tr_b16 v[178:179], v103 offset:4616\n\t"
        "ds_read_b64_tr_b16 v[180:181], v104 offset:2048\n\t"
        "v_mfma_f32_16x16x32_bf16 v[8:11], v[148:151], v[80:83], v[8:11]\n\t"
        "ds_read_b64_tr_b16 v[182:183], v104 offset:2304\n\t"
        "ds_read_b128 v[56:59], v107 offset:32768\n\t"
        "v_mfma_f32_16x16x32_bf16 v[12:15], v[152:155], v[80:83], v[12:15]\n\t"
        "ds_read_b128 v[60:63], v107 offset:34816\n\t"
        "ds_read_b128 v[64:67], v108 offset:32768\n\t"
        "v_mfma_f32_16x16x32_bf16 v[16:19], v[80:83], v[156:159], v[16:19]\n\t"
        "ds_read_b128 v[68:71], v108 offset:34816\n\t"
        "v_mfma_f32_16x16x32_bf16 v[20:23], v[148:151], v[84:87], v[20:23]\n\t"
        "v_mfma_f32_16x16x32_bf16 v[24:27], v[152:155], v[84:87], v[24:27]\n\t"
        "v_mfma_f32_16x16x32_bf16 v[28:31], v[84:87], v[156:159], v[28:31]\n\t"
        "s_mov_b32 %[xo], 0x20000\n\t"
        "s_add_u32 m0, %[ld], 0\n\t"
        "s_add_u32 %[t0], %[xo], 0\n\t"
        "buffer_load_dwordx4 v100, %[sx], %[t0] offen lds\n\t"
        "s_add_u32 m0, %[ld], 1024\n\t"
        "s_add_u32 %[t0], %[xo], 16384\n\t"
        "buffer_load_dwordx4 v100, %[sx], %[t0] offen lds\n\t"
        "s_add_u32 m0, %[ld], 8192\n\t"
        "s_add_u32 %[t0], %[xo], 128\n\t"
        "buffer_load_dwordx4 v100, %[sx], %[t0] offen lds\n\t"
        "s_add_u32 m0, %[ld], 9216\n\t"
        "s_add_u32 %[t0], %[xo], 16512\n\t"
        "buffer_load_dwordx4 v100, %[sx], %[t0] offen lds\n\t"
        "s_add_u32 m0, %[ld], 16384\n\t"
        "s_add_u32 %[t0], %[xo], 256\n\t"
        "buffer_load_dwordx4 v100, %[sx], %[t0] offen lds\n\t"
        "s_add_u32 m0, %[ld], 17408\n\t"
        "s_add_u32 %[t0], %[xo], 16640\n\t"
        "buffer_load_dwordx4 v100, %[sx], %[t0] offen lds\n\t"
        "s_add_u32 m0, %[ld], 24576\n\t"
        "s_add_u32 %[t0], %[xo], 384\n\t"
        "buffer_load_dwordx4 v100, %[sx], %[t0] offen lds\n\t"
        "s_add_u32 m0, %[ld], 25600\n\t"
        "s_add_u32 %[t0], %[xo], 16768\n\t"
        "buffer_load_dwordx4 v100, %[sx], %[t0] offen lds\n\t"
        "s_mov_b32 %[xo], 0\n\t"
        "s_waitcnt vmcnt(14)\n\t"
        "ds_write_b128 v102, v[220:223] offset:16384\n\t"
        "ds_write_b128 v102, v[224:227] offset:20480\n\t"
        "ds_write_b128 v116, v[228:231] offset:24576\n\t"
        "s_waitcnt lgkmcnt(0)\n\t"
        "s_barrier\n\t"
        "v_mfma_f32_16x16x32_bf16 v[8:11], v[160:163], v[56:59], v[8:11]\n\t"
        "ds_read_b64_tr_b16 v[184:185], v103 offset:16384\n\t"
        "ds_read_b64_tr_b16 v[186:187], v103 offset:16896\n\t"
        "v_mfma_f32_16x16x32_bf16 v[12:15], v[164:167], v[56:59], v[12:15]\n\t"
        "ds_read_b64_tr_b16 v[188:189], v103 offset:16392\n\t"
        "ds_read_b64_tr_b16 v[190:191], v103 offset:16904\n\t"
        "v_mfma_f32_16x16x32_bf16 v[16:19], v[56:59], v[168:171], v[16:19]\n\t"
        "ds_read_b64_tr_b16 v[192:193], v104 offset:16384\n\t"
        "ds_read_b64_tr_b16 v[194:195], v104 offset:16640\n\t"
        "v_mfma_f32_16x16x32_bf16 v[20:23], v[160:163], v[60:63], v[20:23]\n\t"
        "s_waitcnt lgkmcnt(5)\n\t"
        "ds_read_b64_tr_b16 v[196:197], v103 offset:20480\n\t"
        "v_mfma_f32_16x16x32_bf16 v[24:27], v[164:167], v[60:63], v[24:27]\n\t"
        "ds_read_b64_tr_b16 v[198:199], v103 offset:20992\n\t"
        "ds_read_b64_tr_b16 v[200:201], v103 offset:20488\n\t"
        "v_mfma_f32_16x16x32_bf16 v[28:31], v[60:63], v[168:171], v[28:31]\n\t"
        "ds_read_b64_tr_b16 v[202:203], v103 offset:21000\n\t"
        "ds_read_b64_tr_b16 v[204:205], v104 offset:18432\n\t"
        "v_mfma_f32_16x16x32_bf16 v[8:11], v[172:175], v[64:67], v[8:11]\n\t"
        "ds_read_b64_tr_b16 v[206:207], v104 offset:18688\n\t"
        "ds_read_b128 v[72:75], v107 offset:40960\n\t"
        "v_mfma_f32_16x16x32_bf16 v[12:15], v[176:179], v[64:67], v[12:15]\n\t"
        "ds_read_b128 v[76:79], v107 offset:43008\n\t"
        "ds_read_b128 v[80:83], v108 offset:40960\n\t"
        "v_mfma_f32_16x16x32_bf16 v[16:19], v[64:67], v[180:183], v[16:19]\n\t"
        "ds_read_b128 v[84:87], v108 offset:43008\n\t"
        "v_mfma_f32_16x16x32_bf16 v[20:23], v[172:175], v[68:71], v[20:23]\n\t"
        "v_mfma_f32_16x16x32_bf16 v[24:27], v[176:179], v[68:71], v[24:27]\n\t"
        "v_mfma_f32_16x16x32_bf16 v[28:31], v[68:71], v[180:183], v[28:31]\n\t"
        "s_waitcnt vmcnt(11)\n\t"
        "ds_write_b128 v102, v[232:235] offset:0\n\t"
        "ds_write_b128 v102, v[236:239] offset:4096\n\t"
        "ds_write_b128 v116, v[240:243] offset:8192\n\t"
        "s_waitcnt lgkmcnt(0)\n\t"
        "s_barrier\n\t"
        "v_mfma_f32_16x16x32_bf16 v[8:11], v[184:187], v[72:75], v[8:11]\n\t"
        "ds_read_b64_tr_b16 v[208:209], v103 offset:0\n\t"
        "ds_read_b64_tr_b16 v[210:211], v103 offset:512\n\t"
        "v_mfma_f32_16x16x32_bf16 v[12:15], v[188:191], v[72:75], v[12:15]\n\t"
        "ds_read_b64_tr_b16 v[212:213], v103 offset:8\n\t"
        "ds_read_b64_tr_b16 v[214:215], v103 offset:520\n\t"
        "v_mfma_f32_16x16x32_bf16 v[16:19], v[72:75], v[192:195], v[16:19]\n\t"
        "ds_read_b64_tr_b16 v[216:217], v104 offset:0\n\t"
        "ds_read_b64_tr_b16 v[218:219], v104 offset:256\n\t"
        "v_mfma_f32_16x16x32_bf16 v[20:23], v[184:187], v[76:79], v[20:23]\n\t"
        "s_waitcnt lgkmcnt(5)\n\t"
        "ds_read_b64_tr_b16 v[220:221], v103 offset:4096\n\t"
        "v_mfma_f32_16x16x32_bf16 v[24:27], v[188:191], v[76:79], v[24:27]\n\t"
        "ds_read_b64_tr_b16 v[222:223], v103 offset:4608\n\t"
        "ds_read_b64_tr_b16 v[224:225], v103 offset:4104\n\t"
        "v_mfma_f32_16x16x32_bf16 v[28:31], v[76:79], v[192:195], v[28:31]\n\t"
        "ds_read_b64_tr_b16 v[226:227], v103 offset:4616\n\t"
        "ds_read_b64_tr_b16 v[228:229], v104 offset:2048\n\t"
        "v_mfma_f32_16x16x32_bf16 v[8:11], v[196:199], v[80:83], v[8:11]\n\t"
        "ds_read_b64_tr_b16 v[230:231], v104 offset:2304\n\t"
        "ds_read_b128 v[56:59], v107 offset:49152\n\t"
        "v_mfma_f32_16x16x32_bf16 v[12:15], v[200:203], v[80:83], v[12:15]\n\t"
        "ds_read_b128 v[60:63], v107 offset:51200\n\t"
        "ds_read_b128 v[64:67], v108 offset:49152\n\t"
        "v_mfma_f32_16x16x32_bf16 v[16:19], v[80:83], v[204:207], v[16:19]\n\t"
        "ds_read_b128 v[68:71], v108 offset:51200\n\t"
        "v_mfma_f32_16x16x32_bf16 v[20:23], v[196:199], v[84:87], v[20:23]\n\t"
        "v_mfma_f32_16x16x32_bf16 v[24:27], v[200:203], v[84:87], v[24:27]\n\t"
        "v_mfma_f32_16x16x32_bf16 v[28:31], v[84:87], v[204:207], v[28:31]\n\t"
        "s_waitcnt vmcnt(8)\n\t"
        "ds_write_b128 v102, v[244:247] offset:16384\n\t"
        "ds_write_b128 v102, v[248:251] offset:20480\n\t"
        "ds_write_b128 v116, v[252:255] offset:24576\n\t"
        "s_waitcnt lgkmcnt(0)\n\t"
        "s_barrier\n\t"
        "v_mfma_f32_16x16x32_bf16 v[8:11], v[208:211], v[56:59], v[8:11]\n\t"
        "ds_read_b64_tr_b16 v[232:233], v103 offset:16384\n\t"
        "ds_read_b64_tr_b16 v[234:235], v103 offset:16896\n\t"
        "v_mfma_f32_16x16x32_bf16 v[12:15], v[212:215], v[56:59], v[12:15]\n\t"
        "ds_read_b64_tr_b16 v[236:237], v103 offset:16392\n\t"
        "ds_read_b64_tr_b16 v[238:239], v103 offset:16904\n\t"
        "v_mfma_f32_16x16x32_bf16 v[16:19], v[56:59], v[216:219], v[16:19]\n\t"
        "ds_read_b64_tr_b16 v[240:241], v104 offset:16384\n\t"
        "ds_read_b64_tr_b16 v[242:243], v104 offset:16640\n\t"
        "v_mfma_f32_16x16x32_bf16 v[20:23], v[208:211], v[60:63], v[20:23]\n\t"
        "s_waitcnt lgkmcnt(5)\n\t"
        "ds_read_b64_tr_b16 v[244:245], v103 offset:20480\n\t"
        "v_mfma_f32_16x16x32_bf16 v[24:27], v[212:215], v[60:63], v[24:27]\n\t"
        "ds_read_b64_tr_b16 v[246:247], v103 offset:20992\n\t"
        "ds_read_b64_tr_b16 v[248:249], v103 offset:20488\n\t"
        "v_mfma_f32_16x16x32_bf16 v[28:31], v[60:63], v[216:219], v[28:31]\n\t"
        "ds_read_b64_tr_b16 v[250:251], v103 offset:21000\n\t"
        "ds_read_b64_tr_b16 v[252:253], v104 offset:18432\n\t"
        "v_mfma_f32_16x16x32_bf16 v[8:11], v[220:223], v[64:67], v[8:11]\n\t"
        "ds_read_b64_tr_b16 v[254:255], v104 offset:18688\n\t"
        "ds_read_b128 v[72:75], v107 offset:57344\n\t"
        "v_mfma_f32_16x16x32_bf16 v[12:15], v[224:227], v[64:67], v[12:15]\n\t"
        "ds_read_b128 v[76:79], v107 offset:59392\n\t"
        "ds_read_b128 v[80:83], v108 offset:57344\n\t"
        "v_mfma_f32_16x16x32_bf16 v[16:19], v[64:67], v[228:231], v[16:19]\n\t"
        "ds_read_b128 v[84:87], v108 offset:59392\n\t"
        "v_mfma_f32_16x16x32_bf16 v[20:23], v[220:223], v[68:71], v[20:23]\n\t"
        "v_mfma_f32_16x16x32_bf16 v[24:27], v[224:227], v[68:71], v[24:27]\n\t"
        "v_mfma_f32_16x16x32_bf16 v[28:31], v[68:71], v[228:231], v[28:31]\n\t"
        "s_waitcnt vmcnt(0) lgkmcnt(0)\n\t"
        "s_barrier\n\t"
        "v_mfma_f32_16x16x32_bf16 v[8:11], v[232:235], v[72:75], v[8:11]\n\t"
        "v_mfma_f32_16x16x32_bf16 v[12:15], v[236:239], v[72:75], v[12:15]\n\t"
        "v_mfma_f32_16x16x32_bf16 v[16:19], v[72:75], v[240:243], v[16:19]\n\t"
        "v_mfma_f32_16x16x32_bf16 v[20:23], v[232:235], v[76:79], v[20:23]\n\t"
        "v_mfma_f32_16x16x32_bf16 v[24:27], v[236:239], v[76:79], v[24:27]\n\t"
        "v_mfma_f32_16x16x32_bf16 v[28:31], v[76:79], v[240:243], v[28:31]\n\t"
        "v_mfma_f32_16x16x32_bf16 v[8:11], v[244:247], v[80:83], v[8:11]\n\t"
        "v_mfma_f32_16x16x32_bf16 v[12:15], v[248:251], v[80:83], v[12:15]\n\t"
        "v_mfma_f32_16x16x32_bf16 v[16:19], v[80:83], v[252:255], v[16:19]\n\t"
        "v_mfma_f32_16x16x32_bf16 v[20:23], v[244:247], v[84:87], v[20:23]\n\t"
        "v_mfma_f32_16x16x32_bf16 v[24:27], v[248:251], v[84:87], v[24:27]\n\t"
        "v_mfma_f32_16x16x32_bf16 v[28:31], v[84:87], v[252:255], v[28:31]\n\t"
        "s_mov_b32 %[xo], 0x20000\n\t"
        "ds_read_b128 v[56:59], v105 offset:0\n\t"
        "ds_read_b128 v[60:63], v105 offset:2048\n\t"
        "ds_read_b128 v[64:67], v106 offset:0\n\t"
        "ds_read_b128 v[68:71], v106 offset:2048\n\t"
        "ds_read_b128 v[72:75], v105 offset:8192\n\t"
        "ds_read_b128 v[76:79], v105 offset:10240\n\t"
        "ds_read_b128 v[80:83], v106 offset:8192\n\t"
        "v_cvt_pk_bf16_f32 v88, v8, v9\n\t"
        "v_cvt_pk_bf16_f32 v89, v10, v11\n\t"
        "v_cvt_pk_bf16_f32 v90, v12, v13\n\t"
        "v_cvt_pk_bf16_f32 v91, v14, v15\n\t"
        "buffer_store_dwordx4 v[88:91], v109, %[sw], %[so] offen sc1\n\t"
        "v_cvt_pk_bf16_f32 v92, v20, v21\n\t"
        "v_cvt_pk_bf16_f32 v93, v22, v23\n\t"
        "v_cvt_pk_bf16_f32 v94, v24, v25\n\t"
        "v_cvt_pk_bf16_f32 v95, v26, v27\n\t"
        "buffer_store_dwordx4 v[92:95], v110, %[sw], %[so] offen sc1\n\t"
        "v_cvt_pk_bf16_f32 v96, v16, v17\n\t"
        "v_cvt_pk_bf16_f32 v97, v18, v19\n\t"
        "v_cvt_pk_bf16_f32 v98, v28, v29\n\t"
        "v_cvt_pk_bf16_f32 v99, v30, v31\n\t"
        "buffer_store_dwordx4 v[96:99], v111, %[sw], %[so] offen sc1\n\t"
        "s_mov_b32 %[so], 0xf0000000\n\t"
        "s_mov_b32 %[xo], 33792\n\t"
        "s_mov_b32 %[cn], 0\n\t"
        "LG_cal%=:\n\t"
        "buffer_load_dword v118, off, %[sw], %[xo] sc1\n\t"
        "s_waitcnt vmcnt(0)\n\t"
        "v_readfirstlane_b32 %[t0], v118\n\t"
        "s_cmp_lg_u32 %[t0], 0\n\t"
        "s_cbranch_scc1 LG_calok%=\n\t"
        "s_sleep 4\n\t"
        "s_add_u32 %[cn], %[cn], 1\n\t"
        "s_cmp_lt_u32 %[cn], 0x100000\n\t"
        "s_cbranch_scc1 LG_cal%=\n\t"
        "s_endpgm\n\t"
        "LG_calok%=:\n\t"
        "s_mov_b32 %[xo], 0x20000\n\t"
        "s_mov_b32 %[cn], 1\n\t"
        "v_mov_b32_e32 v119, %[cn]\n\t"
        "buffer_store_dword v119, v113, %[sw], 0 offen sc1\n\t"
        "s_add_u32 m0, %[ld], 32768\n\t"
        "s_add_u32 %[t0], %[xo], 512\n\t"
        "buffer_load_dwordx4 v100, %[sx], %[t0] offen lds\n\t"
        "s_add_u32 m0, %[ld], 33792\n\t"
        "s_add_u32 %[t0], %[xo], 16896\n\t"
        "buffer_load_dwordx4 v100, %[sx], %[t0] offen lds\n\t"
        "s_add_u32 m0, %[ld], 40960\n\t"
        "s_add_u32 %[t0], %[xo], 640\n\t"
        "buffer_load_dwordx4 v100, %[sx], %[t0] offen lds\n\t"
        "s_add_u32 m0, %[ld], 41984\n\t"
        "s_add_u32 %[t0], %[xo], 17024\n\t"
        "buffer_load_dwordx4 v100, %[sx], %[t0] offen lds\n\t"
        "s_add_u32 m0, %[ld], 49152\n\t"
        "s_add_u32 %[t0], %[xo], 768\n\t"
        "buffer_load_dwordx4 v100, %[sx], %[t0] offen lds\n\t"
        "s_add_u32 m0, %[ld], 50176\n\t"
        "s_add_u32 %[t0], %[xo], 17152\n\t"
        "buffer_load_dwordx4 v100, %[sx], %[t0] offen lds\n\t"
        "s_add_u32 m0, %[ld], 57344\n\t"
        "s_add_u32 %[t0], %[xo], 896\n\t"
        "buffer_load_dwordx4 v100, %[sx], %[t0] offen lds\n\t"
        "s_add_u32 m0, %[ld], 58368\n\t"
        "s_add_u32 %[t0], %[xo], 17280\n\t"
        "buffer_load_dwordx4 v100, %[sx], %[t0] offen lds\n\t"
        "s_add_u32 m0, %[ld], 65536\n\t"
        "s_add_u32 %[t0], %[xo], 1024\n\t"
        "buffer_load_dwordx4 v100, %[sx], %[t0] offen lds\n\t"
        "s_add_u32 m0, %[ld], 66560\n\t"
        "s_add_u32 %[t0], %[xo], 17408\n\t"
        "buffer_load_dwordx4 v100, %[sx], %[t0] offen lds\n\t"
        "s_add_u32 m0, %[ld], 73728\n\t"
        "s_add_u32 %[t0], %[xo], 1152\n\t"
        "buffer_load_dwordx4 v100, %[sx], %[t0] offen lds\n\t"
        "s_add_u32 m0, %[ld], 74752\n\t"
        "s_add_u32 %[t0], %[xo], 17536\n\t"
        "buffer_load_dwordx4 v100, %[sx], %[t0] offen lds\n\t"
        "s_add_u32 m0, %[ld], 81920\n\t"
        "s_add_u32 %[t0], %[xo], 1280\n\t"
        "buffer_load_dwordx4 v100, %[sx], %[t0] offen lds\n\t"
        "s_add_u32 m0, %[ld], 82944\n\t"
        "s_add_u32 %[t0], %[xo], 17664\n\t"
        "buffer_load_dwordx4 v100, %[sx], %[t0] offen lds\n\t"
        "s_add_u32 m0, %[ld], 90112\n\t"
        "s_add_u32 %[t0], %[xo], 1408\n\t"
        "buffer_load_dwordx4 v100, %[sx], %[t0] offen lds\n\t"
        "s_add_u32 m0, %[ld], 91136\n\t"
        "s_add_u32 %[t0], %[xo], 17792\n\t"
        "buffer_load_dwordx4 v100, %[sx], %[t0] offen lds\n\t"
        "s_add_u32 m0, %[ld], 98304\n\t"
        "s_add_u32 %[t0], %[xo], 1536\n\t"
        "buffer_load_dwordx4 v100, %[sx], %[t0] offen lds\n\t"
        "s_add_u32 m0, %[ld], 99328\n\t"
        "s_add_u32 %[t0], %[xo], 17920\n\t"
        "buffer_load_dwordx4 v100, %[sx], %[t0] offen lds\n\t"
        "s_add_u32 m0, %[ld], 106496\n\t"
        "s_add_u32 %[t0], %[xo], 1664\n\t"
        "buffer_load_dwordx4 v100, %[sx], %[t0] offen lds\n\t"
        "s_branch LG_mid%=\n\t"
        "LG_loop%=:\n\t"
        "s_waitcnt lgkmcnt(6)\n\t"
        "v_mfma_f32_16x16x32_bf16 v[8:11], a[0:3], v[56:59], 0\n\t"
        "v_mfma_f32_16x16x32_bf16 v[12:15], a[4:7], v[56:59], 0\n\t"
        "ds_read_b128 v[84:87], v106 offset:10240\n\t"
        "v_mfma_f32_16x16x32_bf16 v[16:19], v[56:59], a[8:11], 0\n\t"
        "s_add_u32 m0, %[ld], 107520\n\t"
        "s_add_u32 %[t0], %[xo], 18048\n\t"
        "s_waitcnt lgkmcnt(6)\n\t"
        "v_mfma_f32_16x16x32_bf16 v[20:23], a[0:3], v[60:63], 0\n\t"
        "buffer_load_dwordx4 v100, %[sx], %[t0] offen lds\n\t"
        "v_mfma_f32_16x16x32_bf16 v[24:27], a[4:7], v[60:63], 0\n\t"
        "ds_read_b128 v[56:59], v105 offset:16384\n\t"
        "v_mfma_f32_16x16x32_bf16 v[28:31], v[60:63], a[8:11], 0\n\t"
        "s_waitcnt lgkmcnt(6)\n\t"
        "v_mfma_f32_16x16x32_bf16 v[8:11], a[12:15], v[64:67], v[8:11]\n\t"
        "v_cvt_pk_bf16_f32 v88, v32, v33\n\t"
        "v_cvt_pk_bf16_f32 v89, v34, v35\n\t"
        "v_mfma_f32_16x16x32_bf16 v[12:15], a[16:19], v[64:67], v[12:15]\n\t"
        "ds_read_b128 v[60:63], v105 offset:18432\n\t"
        "v_cvt_pk_bf16_f32 v90, v36, v37\n\t"
        "v_mfma_f32_16x16x32_bf16 v[16:19], v[64:67], a[20:23], v[16:19]\n\t"
        "s_add_u32 m0, %[ld], 114688\n\t"
        "s_add_u32 %[t0], %[xo], 1792\n\t"
        "s_waitcnt lgkmcnt(6)\n\t"
        "v_mfma_f32_16x16x32_bf16 v[20:23], a[12:15], v[68:71], v[20:23]\n\t"
        "buffer_load_dwordx4 v100, %[sx], %[t0] offen lds\n\t"
        "v_mfma_f32_16x16x32_bf16 v[24:27], a[16:19], v[68:71], v[24:27]\n\t"
        "ds_read_b128 v[64:67], v106 offset:16384\n\t"
        "v_cvt_pk_bf16_f32 v91, v38, v39\n\t"
        "v_mfma_f32_16x16x32_bf16 v[28:31], v[68:71], a[20:23], v[28:31]\n\t"
        "buffer_store_dwordx4 v[88:91], v109, %[sw], %[so] offen sc1\n\t"
        "s_waitcnt lgkmcnt(6)\n\t"
        "v_mfma_f32_16x16x32_bf16 v[8:11], a[24:27], v[72:75], v[8:11]\n\t"
        "v_cvt_pk_bf16_f32 v92, v44, v45\n\t"
        "v_cvt_pk_bf16_f32 v93, v46, v47\n\t"
        "v_mfma_f32_16x16x32_bf16 v[12:15], a[28:31], v[72:75], v[12:15]\n\t"
        "ds_read_b128 v[68:71], v106 offset:18432\n\t"
        "v_cvt_pk_bf16_f32 v94, v48, v49\n\t"
        "v_mfma_f32_16x16x32_bf16 v[16:19], v[72:75], a[32:35], v[16:19]\n\t"
        "s_add_u32 m0, %[ld], 115712\n\t"
        "s_add_u32 %[t0], %[xo], 18176\n\t"
        "s_waitcnt lgkmcnt(6)\n\t"
        "v_mfma_f32_16x16x32_bf16 v[20:23], a[24:27], v[76:79], v[20:23]\n\t"
        "buffer_load_dwordx4 v100, %[sx], %[t0] offen lds\n\t"
        "v_mfma_f32_16x16x32_bf16 v[24:27], a[28:31], v[76:79], v[24:27]\n\t"
        "ds_read_b128 v[72:75], v105 offset:24576\n\t"
        "v_cvt_pk_bf16_f32 v95, v50, v51\n\t"
        "v_mfma_f32_16x16x32_bf16 v[28:31], v[76:79], a[32:35], v[28:31]\n\t"
        "buffer_store_dwordx4 v[92:95], v110, %[sw], %[so] offen sc1\n\t"
        "s_waitcnt lgkmcnt(6)\n\t"
        "v_mfma_f32_16x16x32_bf16 v[8:11], a[36:39], v[80:83], v[8:11]\n\t"
        "v_cvt_pk_bf16_f32 v96, v40, v41\n\t"
        "v_cvt_pk_bf16_f32 v97, v42, v43\n\t"
        "v_mfma_f32_16x16x32_bf16 v[12:15], a[40:43], v[80:83], v[12:15]\n\t"
        "ds_read_b128 v[76:79], v105 offset:26624\n\t"
        "v_cvt_pk_bf16_f32 v98, v52, v53\n\t"
        "v_mfma_f32_16x16x32_bf16 v[16:19], v[80:83], a[44:47], v[16:19]\n\t"
        "s_add_u32 m0, %[ld], 122880\n\t"
        "s_add_u32 %[t0], %[xo], 1920\n\t"
        "s_waitcnt lgkmcnt(6)\n\t"
        "v_mfma_f32_16x16x32_bf16 v[20:23], a[36:39], v[84:87], v[20:23]\n\t"
        "buffer_load_dwordx4 v100, %[sx], %[t0] offen lds\n\t"
        "v_mfma_f32_16x16x32_bf16 v[24:27], a[40:43], v[84:87], v[24:27]\n\t"
        "ds_read_b128 v[80:83], v106 offset:24576\n\t"
        "v_cvt_pk_bf16_f32 v99, v54, v55\n\t"
        "v_mfma_f32_16x16x32_bf16 v[28:31], v[84:87], a[44:47], v[28:31]\n\t"
        "buffer_store_dwordx4 v[96:99], v111, %[sw], %[so] offen sc1\n\t"
        "s_waitcnt lgkmcnt(6)\n\t"
        "v_mfma_f32_16x16x32_bf16 v[8:11], a[48:51], v[56:59], v[8:11]\n\t"
        "s_lshl_b32 %[so], %[cn], 19\n\t"
        "s_cmp_eq_u32 %[cn], 2\n\t"
        "s_cbranch_scc1 LG_first%=\n\t"
        "LG_back%=:\n\t"
        "v_mfma_f32_16x16x32_bf16 v[12:15], a[52:55], v[56:59], v[12:15]\n\t"
        "ds_read_b128 v[84:87], v106 offset:26624\n\t"
        "v_mfma_f32_16x16x32_bf16 v[16:19], v[56:59], a[56:59], v[16:19]\n\t"
        "s_add_u32 m0, %[ld], 123904\n\t"
        "s_add_u32 %[t0], %[xo], 18304\n\t"
        "s_waitcnt lgkmcnt(6)\n\t"
        "v_mfma_f32_16x16x32_bf16 v[20:23], a[48:51], v[60:63], v[20:23]\n\t"
        "buffer_load_dwordx4 v100, %[sx], %[t0] offen lds\n\t"
        "v_mfma_f32_16x16x32_bf16 v[24:27], a[52:55], v[60:63], v[24:27]\n\t"
        "v_mfma_f32_16x16x32_bf16 v[28:31], v[60:63], a[56:59], v[28:31]\n\t"
        "s_waitcnt lgkmcnt(5)\n\t"
        "v_mfma_f32_16x16x32_bf16 v[8:11], a[60:63], v[64:67], v[8:11]\n\t"
        "s_waitcnt vmcnt(20) lgkmcnt(0)\n\t"
        "s_barrier\n\t"
        "s_add_u32 %[xo], %[xo], 0x20000\n\t"
        "v_mfma_f32_16x16x32_bf16 v[12:15], a[64:67], v[64:67], v[12:15]\n\t"
        "ds_read_b128 v[56:59], v105 offset:32768\n\t"
        "v_mfma_f32_16x16x32_bf16 v[16:19], v[64:67], a[68:71], v[16:19]\n\t"
        "ds_read_b128 v[60:63], v105 offset:34816\n\t"
        "s_add_u32 m0, %[ld], 0\n\t"
        "s_add_u32 %[t0], %[xo], 0\n\t"
        "v_mfma_f32_16x16x32_bf16 v[20:23], a[60:63], v[68:71], v[20:23]\n\t"
        "buffer_load_dwordx4 v100, %[sx], %[t0] offen lds\n\t"
        "v_mfma_f32_16x16x32_bf16 v[24:27], a[64:67], v[68:71], v[24:27]\n\t"
        "ds_read_b128 v[64:67], v106 offset:32768\n\t"
        "v_mfma_f32_16x16x32_bf16 v[28:31], v[68:71], a[68:71], v[28:31]\n\t"
        "v_mfma_f32_16x16x32_bf16 v[8:11], a[72:75], v[72:75], v[8:11]\n\t"
        "v_mfma_f32_16x16x32_bf16 v[12:15], a[76:79], v[72:75], v[12:15]\n\t"
        "ds_read_b128 v[68:71], v106 offset:34816\n\t"
        "v_mfma_f32_16x16x32_bf16 v[16:19], v[72:75], a[80:83], v[16:19]\n\t"
        "s_add_u32 m0, %[ld], 1024\n\t"
        "s_add_u32 %[t0], %[xo], 16384\n\t"
        "v_mfma_f32_16x16x32_bf16 v[20:23], a[72:75], v[76:79], v[20:23]\n\t"
        "buffer_load_dwordx4 v100, %[sx], %[t0] offen lds\n\t"
        "v_mfma_f32_16x16x32_bf16 v[24:27], a[76:79], v[76:79], v[24:27]\n\t"
        "ds_read_b128 v[72:75], v105 offset:40960\n\t"
        "v_mfma_f32_16x16x32_bf16 v[28:31], v[76:79], a[80:83], v[28:31]\n\t"
        "v_mfma_f32_16x16x32_bf16 v[8:11], a[84:87], v[80:83], v[8:11]\n\t"
        "v_mfma_f32_16x16x32_bf16 v[12:15], a[88:91], v[80:83], v[12:15]\n\t"
        "ds_read_b128 v[76:79], v105 offset:43008\n\t"
        "v_mfma_f32_16x16x32_bf16 v[16:19], v[80:83], a[92:95], v[16:19]\n\t"
        "s_add_u32 m0, %[ld], 8192\n\t"
        "s_add_u32 %[t0], %[xo], 128\n\t"
        "v_mfma_f32_16x16x32_bf16 v[20:23], a[84:87], v[84:87], v[20:23]\n\t"
        "buffer_load_dwordx4 v100, %[sx], %[t0] offen lds\n\t"
        "v_mfma_f32_16x16x32_bf16 v[24:27], a[88:91], v[84:87], v[24:27]\n\t"
        "ds_read_b128 v[80:83], v106 offset:40960\n\t"
        "v_mfma_f32_16x16x32_bf16 v[28:31], v[84:87], a[92:95], v[28:31]\n\t"
        "s_waitcnt lgkmcnt(6)\n\t"
        "v_mfma_f32_16x16x32_bf16 v[8:11], a[96:99], v[56:59], v[8:11]\n\t"
        "v_mfma_f32_16x16x32_bf16 v[12:15], a[100:103], v[56:59], v[12:15]\n\t"
        "ds_read_b128 v[84:87], v106 offset:43008\n\t"
        "v_mfma_f32_16x16x32_bf16 v[16:19], v[56:59], a[104:107], v[16:19]\n\t"
        "s_add_u32 m0, %[ld], 9216\n\t"
        "s_add_u32 %[t0], %[xo], 16512\n\t"
        "s_waitcnt lgkmcnt(6)\n\t"
        "v_mfma_f32_16x16x32_bf16 v[20:23], a[96:99], v[60:63], v[20:23]\n\t"
        "buffer_load_dwordx4 v100, %[sx], %[t0] offen lds\n\t"
        "v_mfma_f32_16x16x32_bf16 v[24:27], a[100:103], v[60:63], v[24:27]\n\t"
        "ds_read_b128 v[56:59], v105 offset:49152\n\t"
        "v_mfma_f32_16x16x32_bf16 v[28:31], v[60:63], a[104:107], v[28:31]\n\t"
        "s_waitcnt lgkmcnt(6)\n\t"
        "v_mfma_f32_16x16x32_bf16 v[8:11], a[108:111], v[64:67], v[8:11]\n\t"
        "v_mfma_f32_16x16x32_bf16 v[12:15], a[112:115], v[64:67], v[12:15]\n\t"
        "ds_read_b128 v[60:63], v105 offset:51200\n\t"
        "v_mfma_f32_16x16x32_bf16 v[16:19], v[64:67], a[116:119], v[16:19]\n\t"
        "s_add_u32 m0, %[ld], 16384\n\t"
        "s_add_u32 %[t0], %[xo], 256\n\t"
        "s_waitcnt lgkmcnt(6)\n\t"
        "v_mfma_f32_16x16x32_bf16 v[20:23], a[108:111], v[68:71], v[20:23]\n\t"
        "buffer_load_dwordx4 v100, %[sx], %[t0] offen lds\n\t"
        "v_mfma_f32_16x16x32_bf16 v[24:27], a[112:115], v[68:71], v[24:27]\n\t"
        "ds_read_b128 v[64:67], v106 offset:49152\n\t"
        "v_mfma_f32_16x16x32_bf16 v[28:31], v[68:71], a[116:119], v[28:31]\n\t"
        "s_waitcnt lgkmcnt(6)\n\t"
        "v_mfma_f32_16x16x32_bf16 v[8:11], a[120:123], v[72:75], v[8:11]\n\t"
        "v_mfma_f32_16x16x32_bf16 v[12:15], a[124:127], v[72:75], v[12:15]\n\t"
        "ds_read_b128 v[68:71], v106 offset:51200\n\t"
        "v_mfma_f32_16x16x32_bf16 v[16:19], v[72:75], a[128:131], v[16:19]\n\t"
        "s_add_u32 m0, %[ld], 17408\n\t"
        "s_add_u32 %[t0], %[xo], 16640\n\t"
        "s_waitcnt lgkmcnt(6)\n\t"
        "v_mfma_f32_16x16x32_bf16 v[20:23], a[120:123], v[76:79], v[20:23]\n\t"
        "buffer_load_dwordx4 v100, %[sx], %[t0] offen lds\n\t"
        "v_mfma_f32_16x16x32_bf16 v[24:27], a[124:127], v[76:79], v[24:27]\n\t"
        "ds_read_b128 v[72:75], v105 offset:57344\n\t"
        "v_mfma_f32_16x16x32_bf16 v[28:31], v[76:79], a[128:131], v[28:31]\n\t"
        "s_waitcnt lgkmcnt(6)\n\t"
        "v_mfma_f32_16x16x32_bf16 v[8:11], a[132:135], v[80:83], v[8:11]\n\t"
        "v_mfma_f32_16x16x32_bf16 v[12:15], a[136:139], v[80:83], v[12:15]\n\t"
        "ds_read_b128 v[76:79], v105 offset:59392\n\t"
        "v_mfma_f32_16x16x32_bf16 v[16:19], v[80:83], a[140:143], v[16:19]\n\t"
        "s_add_u32 m0, %[ld], 24576\n\t"
        "s_add_u32 %[t0], %[xo], 384\n\t"
        "s_waitcnt lgkmcnt(6)\n\t"
        "v_mfma_f32_16x16x32_bf16 v[20:23], a[132:135], v[84:87], v[20:23]\n\t"
        "buffer_load_dwordx4 v100, %[sx], %[t0] offen lds\n\t"
        "v_mfma_f32_16x16x32_bf16 v[24:27], a[136:139], v[84:87], v[24:27]\n\t"
        "ds_read_b128 v[80:83], v106 offset:57344\n\t"
        "v_mfma_f32_16x16x32_bf16 v[28:31], v[84:87], a[140:143], v[28:31]\n\t"
        "s_waitcnt lgkmcnt(6)\n\t"
        "v_mfma_f32_16x16x32_bf16 v[8:11], a[144:147], v[56:59], v[8:11]\n\t"
        "v_mfma_f32_16x16x32_bf16 v[12:15], a[148:151], v[56:59], v[12:15]\n\t"
        "ds_read_b128 v[84:87], v106 offset:59392\n\t"
        "v_mfma_f32_16x16x32_bf16 v[16:19], v[56:59], a[152:155], v[16:19]\n\t"
        "s_add_u32 m0, %[ld], 25600\n\t"
        "s_add_u32 %[t0], %[xo], 16768\n\t"
        "s_waitcnt lgkmcnt(6)\n\t"
        "v_mfma_f32_16x16x32_bf16 v[20:23], a[144:147], v[60:63], v[20:23]\n\t"
        "buffer_load_dwordx4 v100, %[sx], %[t0] offen lds\n\t"
        "v_mfma_f32_16x16x32_bf16 v[24:27], a[148:151], v[60:63], v[24:27]\n\t"
        "v_mfma_f32_16x16x32_bf16 v[28:31], v[60:63], a[152:155], v[28:31]\n\t"
        "s_waitcnt lgkmcnt(5)\n\t"
        "v_mfma_f32_16x16x32_bf16 v[8:11], a[156:159], v[64:67], v[8:11]\n\t"
        "s_waitcnt vmcnt(9) lgkmcnt(0)\n\t"
        "s_barrier\n\t"
        "v_mfma_f32_16x16x32_bf16 v[12:15], a[160:163], v[64:67], v[12:15]\n\t"
        "ds_read_b128 v[56:59], v107 offset:0\n\t"
        "v_mfma_f32_16x16x32_bf16 v[16:19], v[64:67], a[164:167], v[16:19]\n\t"
        "ds_read_b128 v[60:63], v107 offset:2048\n\t"
        "s_add_u32 m0, %[ld], 32768\n\t"
        "s_add_u32 %[t0], %[xo], 512\n\t"
        "v_mfma_f32_16x16x32_bf16 v[20:23], a[156:159], v[68:71], v[20:23]\n\t"
        "buffer_load_dwordx4 v100, %[sx], %[t0] offen lds\n\t"
        "v_mfma_f32_16x16x32_bf16 v[24:27], a[160:163], v[68:71], v[24:27]\n\t"
        "ds_read_b128 v[64:67], v108 offset:0\n\t"
        "v_mfma_f32_16x16x32_bf16 v[28:31], v[68:71], a[164:167], v[28:31]\n\t"
        "v_mfma_f32_16x16x32_bf16 v[8:11], a[168:171], v[72:75], v[8:11]\n\t"
        "v_mfma_f32_16x16x32_bf16 v[12:15], a[172:175], v[72:75], v[12:15]\n\t"
        "ds_read_b128 v[68:71], v108 offset:2048\n\t"
        "v_mov_b32_e32 v119, %[cn]\n\t"
        "buffer_store_dword v119, v113, %[sw], 0 offen sc1\n\t"
        "s_add_u32 %[cn], %[cn], 1\n\t"
        "v_mfma_f32_16x16x32_bf16 v[16:19], v[72:75], a[176:179], v[16:19]\n\t"
        "s_add_u32 m0, %[ld], 33792\n\t"
        "s_add_u32 %[t0], %[xo], 16896\n\t"
        "v_mfma_f32_16x16x32_bf16 v[20:23], a[168:171], v[76:79], v[20:23]\n\t"
        "buffer_load_dwordx4 v100, %[sx], %[t0] offen lds\n\t"
        "v_mfma_f32_16x16x32_bf16 v[24:27], a[172:175], v[76:79], v[24:27]\n\t"
        "ds_read_b128 v[72:75], v107 offset:8192\n\t"
        "v_mfma_f32_16x16x32_bf16 v[28:31], v[76:79], a[176:179], v[28:31]\n\t"
        "v_mfma_f32_16x16x32_bf16 v[8:11], a[180:183], v[80:83], v[8:11]\n\t"
        "v_mfma_f32_16x16x32_bf16 v[12:15], a[184:187], v[80:83], v[12:15]\n\t"
        "ds_read_b128 v[76:79], v107 offset:10240\n\t"
        "v_mfma_f32_16x16x32_bf16 v[16:19], v[80:83], a[188:191], v[16:19]\n\t"
        "s_add_u32 m0, %[ld], 40960\n\t"
        "s_add_u32 %[t0], %[xo], 640\n\t"
        "v_mfma_f32_16x16x32_bf16 v[20:23], a[180:183], v[84:87], v[20:23]\n\t"
        "buffer_load_dwordx4 v100, %[sx], %[t0] offen lds\n\t"
        "v_mfma_f32_16x16x32_bf16 v[24:27], a[184:187], v[84:87], v[24:27]\n\t"
        "ds_read_b128 v[80:83], v108 offset:8192\n\t"
        "v_mfma_f32_16x16x32_bf16 v[28:31], v[84:87], a[188:191], v[28:31]\n\t"
        "s_waitcnt lgkmcnt(6)\n\t"
        "v_mfma_f32_16x16x32_bf16 v[8:11], a[192:195], v[56:59], v[8:11]\n\t"
        "v_mfma_f32_16x16x32_bf16 v[12:15], a[196:199], v[56:59], v[12:15]\n\t"
        "ds_read_b128 v[84:87], v108 offset:10240\n\t"
        "v_mfma_f32_16x16x32_bf16 v[16:19], v[56:59], a[200:203], v[16:19]\n\t"
        "s_add_u32 m0, %[ld], 41984\n\t"
        "s_add_u32 %[t0], %[xo], 17024\n\t"
        "s_waitcnt lgkmcnt(6)\n\t"
        "v_mfma_f32_16x16x32_bf16 v[20:23], a[192:195], v[60:63], v[20:23]\n\t"
        "buffer_load_dwordx4 v100, %[sx], %[t0] offen lds\n\t"
        "v_mfma_f32_16x16x32_bf16 v[24:27], a[196:199], v[60:63], v[24:27]\n\t"
        "ds_read_b128 v[56:59], v107 offset:16384\n\t"
        "v_mfma_f32_16x16x32_bf16 v[28:31], v[60:63], a[200:203], v[28:31]\n\t"
        "s_waitcnt lgkmcnt(6)\n\t"
        "v_mfma_f32_16x16x32_bf16 v[8:11], a[204:207], v[64:67], v[8:11]\n\t"
        "v_mfma_f32_16x16x32_bf16 v[12:15], a[208:211], v[64:67], v[12:15]\n\t"
        "ds_read_b128 v[60:63], v107 offset:18432\n\t"
        "v_mfma_f32_16x16x32_bf16 v[16:19], v[64:67], a[212:215], v[16:19]\n\t"
        "s_add_u32 m0, %[ld], 49152\n\t"
        "s_add_u32 %[t0], %[xo], 768\n\t"
        "s_waitcnt lgkmcnt(6)\n\t"
        "v_mfma_f32_16x16x32_bf16 v[20:23], a[204:207], v[68:71], v[20:23]\n\t"
        "buffer_load_dwordx4 v100, %[sx], %[t0] offen lds\n\t"
        "v_mfma_f32_16x16x32_bf16 v[24:27], a[208:211], v[68:71], v[24:27]\n\t"
        "ds_read_b128 v[64:67], v108 offset:16384\n\t"
        "v_mfma_f32_16x16x32_bf16 v[28:31], v[68:71], a[212:215], v[28:31]\n\t"
        "s_waitcnt lgkmcnt(6)\n\t"
        "v_mfma_f32_16x16x32_bf16 v[8:11], a[216:219], v[72:75], v[8:11]\n\t"
        "v_mfma_f32_16x16x32_bf16 v[12:15], a[220:223], v[72:75], v[12:15]\n\t"
        "ds_read_b128 v[68:71], v108 offset:18432\n\t"
        "v_mfma_f32_16x16x32_bf16 v[16:19], v[72:75], a[224:227], v[16:19]\n\t"
        "s_add_u32 m0, %[ld], 50176\n\t"
        "s_add_u32 %[t0], %[xo], 17152\n\t"
        "s_waitcnt lgkmcnt(6)\n\t"
        "v_mfma_f32_16x16x32_bf16 v[20:23], a[216:219], v[76:79], v[20:23]\n\t"
        "buffer_load_dwordx4 v100, %[sx], %[t0] offen lds\n\t"
        "v_mfma_f32_16x16x32_bf16 v[24:27], a[220:223], v[76:79], v[24:27]\n\t"
        "ds_read_b128 v[72:75], v107 offset:24576\n\t"
        "v_mfma_f32_16x16x32_bf16 v[28:31], v[76:79], a[224:227], v[28:31]\n\t"
        "s_waitcnt lgkmcnt(6)\n\t"
        "v_mfma_f32_16x16x32_bf16 v[8:11], a[228:231], v[80:83], v[8:11]\n\t"
        "v_mfma_f32_16x16x32_bf16 v[12:15], a[232:235], v[80:83], v[12:15]\n\t"
        "ds_read_b128 v[76:79], v107 offset:26624\n\t"
        "v_mfma_f32_16x16x32_bf16 v[16:19], v[80:83], a[236:239], v[16:19]\n\t"
        "s_add_u32 m0, %[ld], 57344\n\t"
        "s_add_u32 %[t0], %[xo], 896\n\t"
        "s_waitcnt lgkmcnt(6)\n\t"
        "v_mfma_f32_16x16x32_bf16 v[20:23], a[228:231], v[84:87], v[20:23]\n\t"
        "buffer_load_dwordx4 v100, %[sx], %[t0] offen lds\n\t"
        "v_mfma_f32_16x16x32_bf16 v[24:27], a[232:235], v[84:87], v[24:27]\n\t"
        "ds_read_b128 v[80:83], v108 offset:24576\n\t"
        "v_mfma_f32_16x16x32_bf16 v[28:31], v[84:87], a[236:239], v[28:31]\n\t"
        "s_waitcnt lgkmcnt(6)\n\t"
        "v_mfma_f32_16x16x32_bf16 v[8:11], a[240:243], v[56:59], v[8:11]\n\t"
        "v_mfma_f32_16x16x32_bf16 v[12:15], a[244:247], v[56:59], v[12:15]\n\t"
        "ds_read_b128 v[84:87], v108 offset:26624\n\t"
        "v_mfma_f32_16x16x32_bf16 v[16:19], v[56:59], a[248:251], v[16:19]\n\t"
        "s_add_u32 m0, %[ld], 58368\n\t"
        "s_add_u32 %[t0], %[xo], 17280\n\t"
        "s_waitcnt lgkmcnt(6)\n\t"
        "v_mfma_f32_16x16x32_bf16 v[20:23], a[240:243], v[60:63], v[20:23]\n\t"
        "buffer_load_dwordx4 v100, %[sx], %[t0] offen lds\n\t"
        "v_mfma_f32_16x16x32_bf16 v[24:27], a[244:247], v[60:63], v[24:27]\n\t"
        "v_mfma_f32_16x16x32_bf16 v[28:31], v[60:63], a[248:251], v[28:31]\n\t"
        "s_waitcnt lgkmcnt(5)\n\t"
        "v_mfma_f32_16x16x32_bf16 v[8:11], a[252:255], v[64:67], v[8:11]\n\t"
        "s_waitcnt vmcnt(17) lgkmcnt(0)\n\t"
        "s_barrier\n\t"
        "v_mfma_f32_16x16x32_bf16 v[12:15], v[128:131], v[64:67], v[12:15]\n\t"
        "ds_read_b128 v[56:59], v107 offset:32768\n\t"
        "v_mfma_f32_16x16x32_bf16 v[16:19], v[64:67], v[132:135], v[16:19]\n\t"
        "ds_read_b128 v[60:63], v107 offset:34816\n\t"
        "s_add_u32 m0, %[ld], 65536\n\t"
        "s_add_u32 %[t0], %[xo], 1024\n\t"
        "v_mfma_f32_16x16x32_bf16 v[20:23], a[252:255], v[68:71], v[20:23]\n\t"
        "buffer_load_dwordx4 v100, %[sx], %[t0] offen lds\n\t"
        "v_mfma_f32_16x16x32_bf16 v[24:27], v[128:131], v[68:71], v[24:27]\n\t"
        "ds_read_b128 v[64:67], v108 offset:32768\n\t"
        "v_mfma_f32_16x16x32_bf16 v[28:31], v[68:71], v[132:135], v[28:31]\n\t"
        "v_mfma_f32_16x16x32_bf16 v[8:11], v[136:139], v[72:75], v[8:11]\n\t"
        "v_mfma_f32_16x16x32_bf16 v[12:15], v[140:143], v[72:75], v[12:15]\n\t"
        "ds_read_b128 v[68:71], v108 offset:34816\n\t"
        "v_mfma_f32_16x16x32_bf16 v[16:19], v[72:75], v[144:147], v[16:19]\n\t"
        "s_add_u32 m0, %[ld], 66560\n\t"
        "s_add_u32 %[t0], %[xo], 17408\n\t"
        "v_mfma_f32_16x16x32_bf16 v[20:23], v[136:139], v[76:79], v[20:23]\n\t"
        "buffer_load_dwordx4 v100, %[sx], %[t0] offen lds\n\t"
        "v_mfma_f32_16x16x32_bf16 v[24:27], v[140:143], v[76:79], v[24:27]\n\t"
        "ds_read_b128 v[72:75], v107 offset:40960\n\t"
        "v_mfma_f32_16x16x32_bf16 v[28:31], v[76:79], v[144:147], v[28:31]\n\t"
        "v_mfma_f32_16x16x32_bf16 v[8:11], v[148:151], v[80:83], v[8:11]\n\t"
        "v_mfma_f32_16x16x32_bf16 v[12:15], v[152:155], v[80:83], v[12:15]\n\t"
        "ds_read_b128 v[76:79], v107 offset:43008\n\t"
        "v_mfma_f32_16x16x32_bf16 v[16:19], v[80:83], v[156:159], v[16:19]\n\t"
        "s_add_u32 m0, %[ld], 73728\n\t"
        "s_add_u32 %[t0], %[xo], 1152\n\t"
        "v_mfma_f32_16x16x32_bf16 v[20:23], v[148:151], v[84:87], v[20:23]\n\t"
        "buffer_load_dwordx4 v100, %[sx], %[t0] offen lds\n\t"
        "v_mfma_f32_16x16x32_bf16 v[24:27], v[152:155], v[84:87], v[24:27]\n\t"
        "ds_read_b128 v[80:83], v108 offset:40960\n\t"
        "v_mfma_f32_16x16x32_bf16 v[28:31], v[84:87], v[156:159], v[28:31]\n\t"
        "s_waitcnt lgkmcnt(6)\n\t"
        "v_mfma_f32_16x16x32_bf16 v[8:11], v[160:163], v[56:59], v[8:11]\n\t"
        "v_mfma_f32_16x16x32_bf16 v[12:15], v[164:167], v[56:59], v[12:15]\n\t"
        "ds_read_b128 v[84:87], v108 offset:43008\n\t"
        "v_mfma_f32_16x16x32_bf16 v[16:19], v[56:59], v[168:171], v[16:19]\n\t"
        "s_add_u32 m0, %[ld], 74752\n\t"
        "s_add_u32 %[t0], %[xo], 17536\n\t"
        "s_waitcnt lgkmcnt(6)\n\t"
        "v_mfma_f32_16x16x32_bf16 v[20:23], v[160:163], v[60:63], v[20:23]\n\t"
        "buffer_load_dwordx4 v100, %[sx], %[t0] offen lds\n\t"
        "v_mfma_f32_16x16x32_bf16 v[24:27], v[164:167], v[60:63], v[24:27]\n\t"
        "ds_read_b128 v[56:59], v107 offset:49152\n\t"
        "v_mfma_f32_16x16x32_bf16 v[28:31], v[60:63], v[168:171], v[28:31]\n\t"
        "s_waitcnt lgkmcnt(6)\n\t"
        "v_mfma_f32_16x16x32_bf16 v[8:11], v[172:175], v[64:67], v[8:11]\n\t"
        "v_mfma_f32_16x16x32_bf16 v[12:15], v[176:179], v[64:67], v[12:15]\n\t"
        "ds_read_b128 v[60:63], v107 offset:51200\n\t"
        "v_mfma_f32_16x16x32_bf16 v[16:19], v[64:67], v[180:183], v[16:19]\n\t"
        "s_add_u32 m0, %[ld], 81920\n\t"
        "s_add_u32 %[t0], %[xo], 1280\n\t"
        "s_waitcnt lgkmcnt(6)\n\t"
        "v_mfma_f32_16x16x32_bf16 v[20:23], v[172:175], v[68:71], v[20:23]\n\t"
        "buffer_load_dwordx4 v100, %[sx], %[t0] offen lds\n\t"
        "v_mfma_f32_16x16x32_bf16 v[24:27], v[176:179], v[68:71], v[24:27]\n\t"
        "ds_read_b128 v[64:67], v108 offset:49152\n\t"
        "v_mfma_f32_16x16x32_bf16 v[28:31], v[68:71], v[180:183], v[28:31]\n\t"
        "s_waitcnt lgkmcnt(6)\n\t"
        "v_mfma_f32_16x16x32_bf16 v[8:11], v[184:187], v[72:75], v[8:11]\n\t"
        "v_mfma_f32_16x16x32_bf16 v[12:15], v[188:191], v[72:75], v[12:15]\n\t"
        "ds_read_b128 v[68:71], v108 offset:51200\n\t"
        "v_mfma_f32_16x16x32_bf16 v[16:19], v[72:75], v[192:195], v[16:19]\n\t"
        "s_add_u32 m0, %[ld], 82944\n\t"
        "s_add_u32 %[t0], %[xo], 17664\n\t"
        "s_waitcnt lgkmcnt(6)\n\t"
        "v_mfma_f32_16x16x32_bf16 v[20:23], v[184:187], v[76:79], v[20:23]\n\t"
        "buffer_load_dwordx4 v100, %[sx], %[t0] offen lds\n\t"
        "v_mfma_f32_16x16x32_bf16 v[24:27], v[188:191], v[76:79], v[24:27]\n\t"
        "ds_read_b128 v[72:75], v107 offset:57344\n\t"
        "v_mfma_f32_16x16x32_bf16 v[28:31], v[76:79], v[192:195], v[28:31]\n\t"
        "s_waitcnt lgkmcnt(6)\n\t"
        "v_mfma_f32_16x16x32_bf16 v[8:11], v[196:199], v[80:83], v[8:11]\n\t"
        "v_mfma_f32_16x16x32_bf16 v[12:15], v[200:203], v[80:83], v[12:15]\n\t"
        "ds_read_b128 v[76:79], v107 offset:59392\n\t"
        "v_mfma_f32_16x16x32_bf16 v[16:19], v[80:83], v[204:207], v[16:19]\n\t"
        "s_add_u32 m0, %[ld], 90112\n\t"
        "s_add_u32 %[t0], %[xo], 1408\n\t"
        "s_waitcnt lgkmcnt(6)\n\t"
        "v_mfma_f32_16x16x32_bf16 v[20:23], v[196:199], v[84:87], v[20:23]\n\t"
        "buffer_load_dwordx4 v100, %[sx], %[t0] offen lds\n\t"
        "v_mfma_f32_16x16x32_bf16 v[24:27], v[200:203], v[84:87], v[24:27]\n\t"
        "ds_read_b128 v[80:83], v108 offset:57344\n\t"
        "v_mfma_f32_16x16x32_bf16 v[28:31], v[84:87], v[204:207], v[28:31]\n\t"
        "s_waitcnt lgkmcnt(6)\n\t"
        "v_mfma_f32_16x16x32_bf16 v[8:11], v[208:211], v[56:59], v[8:11]\n\t"
        "v_mfma_f32_16x16x32_bf16 v[12:15], v[212:215], v[56:59], v[12:15]\n\t"
        "ds_read_b128 v[84:87], v108 offset:59392\n\t"
        "v_mfma_f32_16x16x32_bf16 v[16:19], v[56:59], v[216:219], v[16:19]\n\t"
        "s_add_u32 m0, %[ld], 91136\n\t"
        "s_add_u32 %[t0], %[xo], 17792\n\t"
        "s_waitcnt lgkmcnt(6)\n\t"
        "v_mfma_f32_16x16x32_bf16 v[20:23], v[208:211], v[60:63], v[20:23]\n\t"
        "buffer_load_dwordx4 v100, %[sx], %[t0] offen lds\n\t"
        "v_mfma_f32_16x16x32_bf16 v[24:27], v[212:215], v[60:63], v[24:27]\n\t"
        "v_mfma_f32_16x16x32_bf16 v[28:31], v[60:63], v[216:219], v[28:31]\n\t"
        "s_waitcnt lgkmcnt(5)\n\t"
        "v_mfma_f32_16x16x32_bf16 v[8:11], v[220:223], v[64:67], v[8:11]\n\t"
        "s_waitcnt vmcnt(17) lgkmcnt(0)\n\t"
        "s_barrier\n\t"
        "v_mfma_f32_16x16x32_bf16 v[12:15], v[224:227], v[64:67], v[12:15]\n\t"
        "ds_read_b128 v[56:59], v105 offset:0\n\t"
        "v_mfma_f32_16x16x32_bf16 v[16:19], v[64:67], v[228:231], v[16:19]\n\t"
        "ds_read_b128 v[60:63], v105 offset:2048\n\t"
        "s_add_u32 m0, %[ld], 98304\n\t"
        "s_add_u32 %[t0], %[xo], 1536\n\t"
        "v_mfma_f32_16x16x32_bf16 v[20:23], v[220:223], v[68:71], v[20:23]\n\t"
        "buffer_load_dwordx4 v100, %[sx], %[t0] offen lds\n\t"
        "v_mfma_f32_16x16x32_bf16 v[24:27], v[224:227], v[68:71], v[24:27]\n\t"
        "ds_read_b128 v[64:67], v106 offset:0\n\t"
        "v_mfma_f32_16x16x32_bf16 v[28:31], v[68:71], v[228:231], v[28:31]\n\t"
        "v_mfma_f32_16x16x32_bf16 v[8:11], v[232:235], v[72:75], v[8:11]\n\t"
        "v_mfma_f32_16x16x32_bf16 v[12:15], v[236:239], v[72:75], v[12:15]\n\t"
        "ds_read_b128 v[68:71], v106 offset:2048\n\t"
        "v_mfma_f32_16x16x32_bf16 v[16:19], v[72:75], v[240:243], v[16:19]\n\t"
        "s_add_u32 m0, %[ld], 99328\n\t"
        "s_add_u32 %[t0], %[xo], 17920\n\t"
        "v_mfma_f32_16x16x32_bf16 v[20:23], v[232:235], v[76:79], v[20:23]\n\t"
        "buffer_load_dwordx4 v100, %[sx], %[t0] offen lds\n\t"
        "v_mfma_f32_16x16x32_bf16 v[24:27], v[236:239], v[76:79], v[24:27]\n\t"
        "ds_read_b128 v[72:75], v105 offset:8192\n\t"
        "v_mfma_f32_16x16x32_bf16 v[28:31], v[76:79], v[240:243], v[28:31]\n\t"
        "v_mfma_f32_16x16x32_bf16 v[8:11], v[244:247], v[80:83], v[8:11]\n\t"
        "v_mfma_f32_16x16x32_bf16 v[12:15], v[248:251], v[80:83], v[12:15]\n\t"
        "ds_read_b128 v[76:79], v105 offset:10240\n\t"
        "v_mfma_f32_16x16x32_bf16 v[16:19], v[80:83], v[252:255], v[16:19]\n\t"
        "s_add_u32 m0, %[ld], 106496\n\t"
        "s_add_u32 %[t0], %[xo], 1664\n\t"
        "v_mfma_f32_16x16x32_bf16 v[20:23], v[244:247], v[84:87], v[20:23]\n\t"
        "buffer_load_dwordx4 v100, %[sx], %[t0] offen lds\n\t"
        "v_mfma_f32_16x16x32_bf16 v[24:27], v[248:251], v[84:87], v[24:27]\n\t"
        "ds_read_b128 v[80:83], v106 offset:8192\n\t"
        "v_mfma_f32_16x16x32_bf16 v[28:31], v[84:87], v[252:255], v[28:31]\n\t"
        "LG_mid%=:\n\t"
        "s_waitcnt lgkmcnt(6)\n\t"
        "v_mfma_f32_16x16x32_bf16 v[32:35], a[0:3], v[56:59], 0\n\t"
        "v_mfma_f32_16x16x32_bf16 v[36:39], a[4:7], v[56:59], 0\n\t"
        "ds_read_b128 v[84:87], v106 offset:10240\n\t"
        "v_mfma_f32_16x16x32_bf16 v[40:43], v[56:59], a[8:11], 0\n\t"
        "s_add_u32 m0, %[ld], 107520\n\t"
        "s_add_u32 %[t0], %[xo], 18048\n\t"
        "s_waitcnt lgkmcnt(6)\n\t"
        "v_mfma_f32_16x16x32_bf16 v[44:47], a[0:3], v[60:63], 0\n\t"
        "buffer_load_dwordx4 v100, %[sx], %[t0] offen lds\n\t"
        "v_mfma_f32_16x16x32_bf16 v[48:51], a[4:7], v[60:63], 0\n\t"
        "ds_read_b128 v[56:59], v105 offset:16384\n\t"
        "v_mfma_f32_16x16x32_bf16 v[52:55], v[60:63], a[8:11], 0\n\t"
        "s_waitcnt lgkmcnt(6)\n\t"
        "v_mfma_f32_16x16x32_bf16 v[32:35], a[12:15], v[64:67], v[32:35]\n\t"
        "v_cvt_pk_bf16_f32 v88, v8, v9\n\t"
        "v_cvt_pk_bf16_f32 v89, v10, v11\n\t"
        "v_mfma_f32_16x16x32_bf16 v[36:39], a[16:19], v[64:67], v[36:39]\n\t"
        "ds_read_b128 v[60:63], v105 offset:18432\n\t"
        "v_cvt_pk_bf16_f32 v90, v12, v13\n\t"
        "v_mfma_f32_16x16x32_bf16 v[40:43], v[64:67], a[20:23], v[40:43]\n\t"
        "s_add_u32 m0, %[ld], 114688\n\t"
        "s_add_u32 %[t0], %[xo], 1792\n\t"
        "s_waitcnt lgkmcnt(6)\n\t"
        "v_mfma_f32_16x16x32_bf16 v[44:47], a[12:15], v[68:71], v[44:47]\n\t"
        "buffer_load_dwordx4 v100, %[sx], %[t0] offen lds\n\t"
        "v_mfma_f32_16x16x32_bf16 v[48:51], a[16:19], v[68:71], v[48:51]\n\t"
        "ds_read_b128 v[64:67], v106 offset:16384\n\t"
        "v_cvt_pk_bf16_f32 v91, v14, v15\n\t"
        "v_mfma_f32_16x16x32_bf16 v[52:55], v[68:71], a[20:23], v[52:55]\n\t"
        "buffer_store_dwordx4 v[88:91], v109, %[sw], %[so] offen sc1\n\t"
        "s_waitcnt lgkmcnt(6)\n\t"
        "v_mfma_f32_16x16x32_bf16 v[32:35], a[24:27], v[72:75], v[32:35]\n\t"
        "v_cvt_pk_bf16_f32 v92, v20, v21\n\t"
        "v_cvt_pk_bf16_f32 v93, v22, v23\n\t"
        "v_mfma_f32_16x16x32_bf16 v[36:39], a[28:31], v[72:75], v[36:39]\n\t"
        "ds_read_b128 v[68:71], v106 offset:18432\n\t"
        "v_cvt_pk_bf16_f32 v94, v24, v25\n\t"
        "v_mfma_f32_16x16x32_bf16 v[40:43], v[72:75], a[32:35], v[40:43]\n\t"
        "s_add_u32 m0, %[ld], 115712\n\t"
        "s_add_u32 %[t0], %[xo], 18176\n\t"
        "s_waitcnt lgkmcnt(6)\n\t"
        "v_mfma_f32_16x16x32_bf16 v[44:47], a[24:27], v[76:79], v[44:47]\n\t"
        "buffer_load_dwordx4 v100, %[sx], %[t0] offen lds\n\t"
        "v_mfma_f32_16x16x32_bf16 v[48:51], a[28:31], v[76:79], v[48:51]\n\t"
        "ds_read_b128 v[72:75], v105 offset:24576\n\t"
        "v_cvt_pk_bf16_f32 v95, v26, v27\n\t"
        "v_mfma_f32_16x16x32_bf16 v[52:55], v[76:79], a[32:35], v[52:55]\n\t"
        "buffer_store_dwordx4 v[92:95], v110, %[sw], %[so] offen sc1\n\t"
        "s_waitcnt lgkmcnt(6)\n\t"
        "v_mfma_f32_16x16x32_bf16 v[32:35], a[36:39], v[80:83], v[32:35]\n\t"
        "v_cvt_pk_bf16_f32 v96, v16, v17\n\t"
        "v_cvt_pk_bf16_f32 v97, v18, v19\n\t"
        "v_mfma_f32_16x16x32_bf16 v[36:39], a[40:43], v[80:83], v[36:39]\n\t"
        "ds_read_b128 v[76:79], v105 offset:26624\n\t"
        "v_cvt_pk_bf16_f32 v98, v28, v29\n\t"
        "v_mfma_f32_16x16x32_bf16 v[40:43], v[80:83], a[44:47], v[40:43]\n\t"
        "s_add_u32 m0, %[ld], 122880\n\t"
        "s_add_u32 %[t0], %[xo], 1920\n\t"
        "s_waitcnt lgkmcnt(6)\n\t"
        "v_mfma_f32_16x16x32_bf16 v[44:47], a[36:39], v[84:87], v[44:47]\n\t"
        "buffer_load_dwordx4 v100, %[sx], %[t0] offen lds\n\t"
        "v_mfma_f32_16x16x32_bf16 v[48:51], a[40:43], v[84:87], v[48:51]\n\t"
        "ds_read_b128 v[80:83], v106 offset:24576\n\t"
        "v_cvt_pk_bf16_f32 v99, v30, v31\n\t"
        "v_mfma_f32_16x16x32_bf16 v[52:55], v[84:87], a[44:47], v[52:55]\n\t"
        "buffer_store_dwordx4 v[96:99], v111, %[sw], %[so] offen sc1\n\t"
        "s_waitcnt lgkmcnt(6)\n\t"
        "v_mfma_f32_16x16x32_bf16 v[32:35], a[48:51], v[56:59], v[32:35]\n\t"
        "s_lshl_b32 %[so], %[cn], 19\n\t"
        "v_mfma_f32_16x16x32_bf16 v[36:39], a[52:55], v[56:59], v[36:39]\n\t"
        "ds_read_b128 v[84:87], v106 offset:26624\n\t"
        "v_mfma_f32_16x16x32_bf16 v[40:43], v[56:59], a[56:59], v[40:43]\n\t"
        "s_add_u32 m0, %[ld], 123904\n\t"
        "s_add_u32 %[t0], %[xo], 18304\n\t"
        "s_waitcnt lgkmcnt(6)\n\t"
        "v_mfma_f32_16x16x32_bf16 v[44:47], a[48:51], v[60:63], v[44:47]\n\t"
        "buffer_load_dwordx4 v100, %[sx], %[t0] offen lds\n\t"
        "v_mfma_f32_16x16x32_bf16 v[48:51], a[52:55], v[60:63], v[48:51]\n\t"
        "v_mfma_f32_16x16x32_bf16 v[52:55], v[60:63], a[56:59], v[52:55]\n\t"
        "s_waitcnt lgkmcnt(5)\n\t"
        "v_mfma_f32_16x16x32_bf16 v[32:35], a[60:63], v[64:67], v[32:35]\n\t"
        "s_waitcnt vmcnt(19) lgkmcnt(0)\n\t"
        "s_barrier\n\t"
        "s_add_u32 %[xo], %[xo], 0x20000\n\t"
        "v_mfma_f32_16x16x32_bf16 v[36:39], a[64:67], v[64:67], v[36:39]\n\t"
        "ds_read_b128 v[56:59], v105 offset:32768\n\t"
        "v_mfma_f32_16x16x32_bf16 v[40:43], v[64:67], a[68:71], v[40:43]\n\t"
        "ds_read_b128 v[60:63], v105 offset:34816\n\t"
        "s_add_u32 m0, %[ld], 0\n\t"
        "s_add_u32 %[t0], %[xo], 0\n\t"
        "v_mfma_f32_16x16x32_bf16 v[44:47], a[60:63], v[68:71], v[44:47]\n\t"
        "buffer_load_dwordx4 v100, %[sx], %[t0] offen lds\n\t"
        "v_mfma_f32_16x16x32_bf16 v[48:51], a[64:67], v[68:71], v[48:51]\n\t"
        "ds_read_b128 v[64:67], v106 offset:32768\n\t"
        "v_mfma_f32_16x16x32_bf16 v[52:55], v[68:71], a[68:71], v[52:55]\n\t"
        "v_mfma_f32_16x16x32_bf16 v[32:35], a[72:75], v[72:75], v[32:35]\n\t"
        "v_mfma_f32_16x16x32_bf16 v[36:39], a[76:79], v[72:75], v[36:39]\n\t"
        "ds_read_b128 v[68:71], v106 offset:34816\n\t"
        "v_mfma_f32_16x16x32_bf16 v[40:43], v[72:75], a[80:83], v[40:43]\n\t"
        "s_add_u32 m0, %[ld], 1024\n\t"
        "s_add_u32 %[t0], %[xo], 16384\n\t"
        "v_mfma_f32_16x16x32_bf16 v[44:47], a[72:75], v[76:79], v[44:47]\n\t"
        "buffer_load_dwordx4 v100, %[sx], %[t0] offen lds\n\t"
        "v_mfma_f32_16x16x32_bf16 v[48:51], a[76:79], v[76:79], v[48:51]\n\t"
        "ds_read_b128 v[72:75], v105 offset:40960\n\t"
        "v_mfma_f32_16x16x32_bf16 v[52:55], v[76:79], a[80:83], v[52:55]\n\t"
        "v_mfma_f32_16x16x32_bf16 v[32:35], a[84:87], v[80:83], v[32:35]\n\t"
        "v_mfma_f32_16x16x32_bf16 v[36:39], a[88:91], v[80:83], v[36:39]\n\t"
        "ds_read_b128 v[76:79], v105 offset:43008\n\t"
        "v_mfma_f32_16x16x32_bf16 v[40:43], v[80:83], a[92:95], v[40:43]\n\t"
        "s_add_u32 m0, %[ld], 8192\n\t"
        "s_add_u32 %[t0], %[xo], 128\n\t"
        "v_mfma_f32_16x16x32_bf16 v[44:47], a[84:87], v[84:87], v[44:47]\n\t"
        "buffer_load_dwordx4 v100, %[sx], %[t0] offen lds\n\t"
        "v_mfma_f32_16x16x32_bf16 v[48:51], a[88:91], v[84:87], v[48:51]\n\t"
        "ds_read_b128 v[80:83], v106 offset:40960\n\t"
        "v_mfma_f32_16x16x32_bf16 v[52:55], v[84:87], a[92:95], v[52:55]\n\t"
        "s_waitcnt lgkmcnt(6)\n\t"
        "v_mfma_f32_16x16x32_bf16 v[32:35], a[96:99], v[56:59], v[32:35]\n\t"
        "v_mfma_f32_16x16x32_bf16 v[36:39], a[100:103], v[56:59], v[36:39]\n\t"
        "ds_read_b128 v[84:87], v106 offset:43008\n\t"
        "v_mfma_f32_16x16x32_bf16 v[40:43], v[56:59], a[104:107], v[40:43]\n\t"
        "s_add_u32 m0, %[ld], 9216\n\t"
        "s_add_u32 %[t0], %[xo], 16512\n\t"
        "s_waitcnt lgkmcnt(6)\n\t"
        "v_mfma_f32_16x16x32_bf16 v[44:47], a[96:99], v[60:63], v[44:47]\n\t"
        "buffer_load_dwordx4 v100, %[sx], %[t0] offen lds\n\t"
        "v_mfma_f32_16x16x32_bf16 v[48:51], a[100:103], v[60:63], v[48:51]\n\t"
        "ds_read_b128 v[56:59], v105 offset:49152\n\t"
        "v_mfma_f32_16x16x32_bf16 v[52:55], v[60:63], a[104:107], v[52:55]\n\t"
        "s_waitcnt lgkmcnt(6)\n\t"
        "v_mfma_f32_16x16x32_bf16 v[32:35], a[108:111], v[64:67], v[32:35]\n\t"
        "v_mfma_f32_16x16x32_bf16 v[36:39], a[112:115], v[64:67], v[36:39]\n\t"
        "ds_read_b128 v[60:63], v105 offset:51200\n\t"
        "v_mfma_f32_16x16x32_bf16 v[40:43], v[64:67], a[116:119], v[40:43]\n\t"
        "s_add_u32 m0, %[ld], 16384\n\t"
        "s_add_u32 %[t0], %[xo], 256\n\t"
        "s_waitcnt lgkmcnt(6)\n\t"
        "v_mfma_f32_16x16x32_bf16 v[44:47], a[108:111], v[68:71], v[44:47]\n\t"
        "buffer_load_dwordx4 v100, %[sx], %[t0] offen lds\n\t"
        "v_mfma_f32_16x16x32_bf16 v[48:51], a[112:115], v[68:71], v[48:51]\n\t"
        "ds_read_b128 v[64:67], v106 offset:49152\n\t"
        "v_mfma_f32_16x16x32_bf16 v[52:55], v[68:71], a[116:119], v[52:55]\n\t"
        "s_waitcnt lgkmcnt(6)\n\t"
        "v_mfma_f32_16x16x32_bf16 v[32:35], a[120:123], v[72:75], v[32:35]\n\t"
        "v_mfma_f32_16x16x32_bf16 v[36:39], a[124:127], v[72:75], v[36:39]\n\t"
        "ds_read_b128 v[68:71], v106 offset:51200\n\t"
        "v_mfma_f32_16x16x32_bf16 v[40:43], v[72:75], a[128:131], v[40:43]\n\t"
        "s_add_u32 m0, %[ld], 17408\n\t"
        "s_add_u32 %[t0], %[xo], 16640\n\t"
        "s_waitcnt lgkmcnt(6)\n\t"
        "v_mfma_f32_16x16x32_bf16 v[44:47], a[120:123], v[76:79], v[44:47]\n\t"
        "buffer_load_dwordx4 v100, %[sx], %[t0] offen lds\n\t"
        "v_mfma_f32_16x16x32_bf16 v[48:51], a[124:127], v[76:79], v[48:51]\n\t"
        "ds_read_b128 v[72:75], v105 offset:57344\n\t"
        "v_mfma_f32_16x16x32_bf16 v[52:55], v[76:79], a[128:131], v[52:55]\n\t"
        "s_waitcnt lgkmcnt(6)\n\t"
        "v_mfma_f32_16x16x32_bf16 v[32:35], a[132:135], v[80:83], v[32:35]\n\t"
        "v_mfma_f32_16x16x32_bf16 v[36:39], a[136:139], v[80:83], v[36:39]\n\t"
        "ds_read_b128 v[76:79], v105 offset:59392\n\t"
        "v_mfma_f32_16x16x32_bf16 v[40:43], v[80:83], a[140:143], v[40:43]\n\t"
        "s_add_u32 m0, %[ld], 24576\n\t"
        "s_add_u32 %[t0], %[xo], 384\n\t"
        "s_waitcnt lgkmcnt(6)\n\t"
        "v_mfma_f32_16x16x32_bf16 v[44:47], a[132:135], v[84:87], v[44:47]\n\t"
        "buffer_load_dwordx4 v100, %[sx], %[t0] offen lds\n\t"
        "v_mfma_f32_16x16x32_bf16 v[48:51], a[136:139], v[84:87], v[48:51]\n\t"
        "ds_read_b128 v[80:83], v106 offset:57344\n\t"
        "v_mfma_f32_16x16x32_bf16 v[52:55], v[84:87], a[140:143], v[52:55]\n\t"
        "s_waitcnt lgkmcnt(6)\n\t"
        "v_mfma_f32_16x16x32_bf16 v[32:35], a[144:147], v[56:59], v[32:35]\n\t"
        "v_mfma_f32_16x16x32_bf16 v[36:39], a[148:151], v[56:59], v[36:39]\n\t"
        "ds_read_b128 v[84:87], v106 offset:59392\n\t"
        "v_mfma_f32_16x16x32_bf16 v[40:43], v[56:59], a[152:155], v[40:43]\n\t"
        "s_add_u32 m0, %[ld], 25600\n\t"
        "s_add_u32 %[t0], %[xo], 16768\n\t"
        "s_waitcnt lgkmcnt(6)\n\t"
        "v_mfma_f32_16x16x32_bf16 v[44:47], a[144:147], v[60:63], v[44:47]\n\t"
        "buffer_load_dwordx4 v100, %[sx], %[t0] offen lds\n\t"
        "v_mfma_f32_16x16x32_bf16 v[48:51], a[148:151], v[60:63], v[48:51]\n\t"
        "v_mfma_f32_16x16x32_bf16 v[52:55], v[60:63], a[152:155], v[52:55]\n\t"
        "s_waitcnt lgkmcnt(5)\n\t"
        "v_mfma_f32_16x16x32_bf16 v[32:35], a[156:159], v[64:67], v[32:35]\n\t"
        "s_waitcnt vmcnt(9) lgkmcnt(0)\n\t"
        "s_barrier\n\t"
        "v_mfma_f32_16x16x32_bf16 v[36:39], a[160:163], v[64:67], v[36:39]\n\t"
        "ds_read_b128 v[56:59], v107 offset:0\n\t"
        "v_mfma_f32_16x16x32_bf16 v[40:43], v[64:67], a[164:167], v[40:43]\n\t"
        "ds_read_b128 v[60:63], v107 offset:2048\n\t"
        "s_add_u32 m0, %[ld], 32768\n\t"
        "s_add_u32 %[t0], %[xo], 512\n\t"
        "v_mfma_f32_16x16x32_bf16 v[44:47], a[156:159], v[68:71], v[44:47]\n\t"
        "buffer_load_dwordx4 v100, %[sx], %[t0] offen lds\n\t"
        "v_mfma_f32_16x16x32_bf16 v[48:51], a[160:163], v[68:71], v[48:51]\n\t"
        "ds_read_b128 v[64:67], v108 offset:0\n\t"
        "v_mfma_f32_16x16x32_bf16 v[52:55], v[68:71], a[164:167], v[52:55]\n\t"
        "v_mfma_f32_16x16x32_bf16 v[32:35], a[168:171], v[72:75], v[32:35]\n\t"
        "v_mfma_f32_16x16x32_bf16 v[36:39], a[172:175], v[72:75], v[36:39]\n\t"
        "ds_read_b128 v[68:71], v108 offset:2048\n\t"
        "v_mov_b32_e32 v119, %[cn]\n\t"
        "buffer_store_dword v119, v113, %[sw], 0 offen sc1\n\t"
        "s_add_u32 %[cn], %[cn], 1\n\t"
        "v_mfma_f32_16x16x32_bf16 v[40:43], v[72:75], a[176:179], v[40:43]\n\t"
        "s_add_u32 m0, %[ld], 33792\n\t"
        "s_add_u32 %[t0], %[xo], 16896\n\t"
        "v_mfma_f32_16x16x32_bf16 v[44:47], a[168:171], v[76:79], v[44:47]\n\t"
        "buffer_load_dwordx4 v100, %[sx], %[t0] offen lds\n\t"
        "v_mfma_f32_16x16x32_bf16 v[48:51], a[172:175], v[76:79], v[48:51]\n\t"
        "ds_read_b128 v[72:75], v107 offset:8192\n\t"
        "v_mfma_f32_16x16x32_bf16 v[52:55], v[76:79], a[176:179], v[52:55]\n\t"
        "v_mfma_f32_16x16x32_bf16 v[32:35], a[180:183], v[80:83], v[32:35]\n\t"
        "v_mfma_f32_16x16x32_bf16 v[36:39], a[184:187], v[80:83], v[36:39]\n\t"
        "ds_read_b128 v[76:79], v107 offset:10240\n\t"
        "v_mfma_f32_16x16x32_bf16 v[40:43], v[80:83], a[188:191], v[40:43]\n\t"
        "s_add_u32 m0, %[ld], 40960\n\t"
        "s_add_u32 %[t0], %[xo], 640\n\t"
        "v_mfma_f32_16x16x32_bf16 v[44:47], a[180:183], v[84:87], v[44:47]\n\t"
        "buffer_load_dwordx4 v100, %[sx], %[t0] offen lds\n\t"
        "v_mfma_f32_16x16x32_bf16 v[48:51], a[184:187], v[84:87], v[48:51]\n\t"
        "ds_read_b128 v[80:83], v108 offset:8192\n\t"
        "v_mfma_f32_16x16x32_bf16 v[52:55], v[84:87], a[188:191], v[52:55]\n\t"
        "s_waitcnt lgkmcnt(6)\n\t"
        "v_mfma_f32_16x16x32_bf16 v[32:35], a[192:195], v[56:59], v[32:35]\n\t"
        "v_mfma_f32_16x16x32_bf16 v[36:39], a[196:199], v[56:59], v[36:39]\n\t"
        "ds_read_b128 v[84:87], v108 offset:10240\n\t"
        "v_mfma_f32_16x16x32_bf16 v[40:43], v[56:59], a[200:203], v[40:43]\n\t"
        "s_add_u32 m0, %[ld], 41984\n\t"
        "s_add_u32 %[t0], %[xo], 17024\n\t"
        "s_waitcnt lgkmcnt(6)\n\t"
        "v_mfma_f32_16x16x32_bf16 v[44:47], a[192:195], v[60:63], v[44:47]\n\t"
        "buffer_load_dwordx4 v100, %[sx], %[t0] offen lds\n\t"
        "v_mfma_f32_16x16x32_bf16 v[48:51], a[196:199], v[60:63], v[48:51]\n\t"
        "ds_read_b128 v[56:59], v107 offset:16384\n\t"
        "v_mfma_f32_16x16x32_bf16 v[52:55], v[60:63], a[200:203], v[52:55]\n\t"
        "s_waitcnt lgkmcnt(6)\n\t"
        "v_mfma_f32_16x16x32_bf16 v[32:35], a[204:207], v[64:67], v[32:35]\n\t"
        "v_mfma_f32_16x16x32_bf16 v[36:39], a[208:211], v[64:67], v[36:39]\n\t"
        "ds_read_b128 v[60:63], v107 offset:18432\n\t"
        "v_mfma_f32_16x16x32_bf16 v[40:43], v[64:67], a[212:215], v[40:43]\n\t"
        "s_add_u32 m0, %[ld], 49152\n\t"
        "s_add_u32 %[t0], %[xo], 768\n\t"
        "s_waitcnt lgkmcnt(6)\n\t"
        "v_mfma_f32_16x16x32_bf16 v[44:47], a[204:207], v[68:71], v[44:47]\n\t"
        "buffer_load_dwordx4 v100, %[sx], %[t0] offen lds\n\t"
        "v_mfma_f32_16x16x32_bf16 v[48:51], a[208:211], v[68:71], v[48:51]\n\t"
        "ds_read_b128 v[64:67], v108 offset:16384\n\t"
        "v_mfma_f32_16x16x32_bf16 v[52:55], v[68:71], a[212:215], v[52:55]\n\t"
        "s_waitcnt lgkmcnt(6)\n\t"
        "v_mfma_f32_16x16x32_bf16 v[32:35], a[216:219], v[72:75], v[32:35]\n\t"
        "v_mfma_f32_16x16x32_bf16 v[36:39], a[220:223], v[72:75], v[36:39]\n\t"
        "ds_read_b128 v[68:71], v108 offset:18432\n\t"
        "v_mfma_f32_16x16x32_bf16 v[40:43], v[72:75], a[224:227], v[40:43]\n\t"
        "s_add_u32 m0, %[ld], 50176\n\t"
        "s_add_u32 %[t0], %[xo], 17152\n\t"
        "s_waitcnt lgkmcnt(6)\n\t"
        "v_mfma_f32_16x16x32_bf16 v[44:47], a[216:219], v[76:79], v[44:47]\n\t"
        "buffer_load_dwordx4 v100, %[sx], %[t0] offen lds\n\t"
        "v_mfma_f32_16x16x32_bf16 v[48:51], a[220:223], v[76:79], v[48:51]\n\t"
        "ds_read_b128 v[72:75], v107 offset:24576\n\t"
        "v_mfma_f32_16x16x32_bf16 v[52:55], v[76:79], a[224:227], v[52:55]\n\t"
        "s_waitcnt lgkmcnt(6)\n\t"
        "v_mfma_f32_16x16x32_bf16 v[32:35], a[228:231], v[80:83], v[32:35]\n\t"
        "v_mfma_f32_16x16x32_bf16 v[36:39], a[232:235], v[80:83], v[36:39]\n\t"
        "ds_read_b128 v[76:79], v107 offset:26624\n\t"
        "v_mfma_f32_16x16x32_bf16 v[40:43], v[80:83], a[236:239], v[40:43]\n\t"
        "s_add_u32 m0, %[ld], 57344\n\t"
        "s_add_u32 %[t0], %[xo], 896\n\t"
        "s_waitcnt lgkmcnt(6)\n\t"
        "v_mfma_f32_16x16x32_bf16 v[44:47], a[228:231], v[84:87], v[44:47]\n\t"
        "buffer_load_dwordx4 v100, %[sx], %[t0] offen lds\n\t"
        "v_mfma_f32_16x16x32_bf16 v[48:51], a[232:235], v[84:87], v[48:51]\n\t"
        "ds_read_b128 v[80:83], v108 offset:24576\n\t"
        "v_mfma_f32_16x16x32_bf16 v[52:55], v[84:87], a[236:239], v[52:55]\n\t"
        "s_waitcnt lgkmcnt(6)\n\t"
        "v_mfma_f32_16x16x32_bf16 v[32:35], a[240:243], v[56:59], v[32:35]\n\t"
        "v_mfma_f32_16x16x32_bf16 v[36:39], a[244:247], v[56:59], v[36:39]\n\t"
        "ds_read_b128 v[84:87], v108 offset:26624\n\t"
        "v_mfma_f32_16x16x32_bf16 v[40:43], v[56:59], a[248:251], v[40:43]\n\t"
        "s_add_u32 m0, %[ld], 58368\n\t"
        "s_add_u32 %[t0], %[xo], 17280\n\t"
        "s_waitcnt lgkmcnt(6)\n\t"
        "v_mfma_f32_16x16x32_bf16 v[44:47], a[240:243], v[60:63], v[44:47]\n\t"
        "buffer_load_dwordx4 v100, %[sx], %[t0] offen lds\n\t"
        "v_mfma_f32_16x16x32_bf16 v[48:51], a[244:247], v[60:63], v[48:51]\n\t"
        "v_mfma_f32_16x16x32_bf16 v[52:55], v[60:63], a[248:251], v[52:55]\n\t"
        "s_waitcnt lgkmcnt(5)\n\t"
        "v_mfma_f32_16x16x32_bf16 v[32:35], a[252:255], v[64:67], v[32:35]\n\t"
        "s_waitcnt vmcnt(17) lgkmcnt(0)\n\t"
        "s_barrier\n\t"
        "v_mfma_f32_16x16x32_bf16 v[36:39], v[128:131], v[64:67], v[36:39]\n\t"
        "ds_read_b128 v[56:59], v107 offset:32768\n\t"
        "s_cmp_eq_u32 %[cn], %[wv]\n\t"
        "s_cselect_b32 %[t0], 0, 0xf0000000\n\t"
        "buffer_load_dword v118, v117, %[sx], %[t0] offen\n\t"
        "v_mfma_f32_16x16x32_bf16 v[40:43], v[64:67], v[132:135], v[40:43]\n\t"
        "ds_read_b128 v[60:63], v107 offset:34816\n\t"
        "s_add_u32 m0, %[ld], 65536\n\t"
        "s_add_u32 %[t0], %[xo], 1024\n\t"
        "v_mfma_f32_16x16x32_bf16 v[44:47], a[252:255], v[68:71], v[44:47]\n\t"
        "buffer_load_dwordx4 v100, %[sx], %[t0] offen lds\n\t"
        "v_mfma_f32_16x16x32_bf16 v[48:51], v[128:131], v[68:71], v[48:51]\n\t"
        "ds_read_b128 v[64:67], v108 offset:32768\n\t"
        "v_mfma_f32_16x16x32_bf16 v[52:55], v[68:71], v[132:135], v[52:55]\n\t"
        "v_mfma_f32_16x16x32_bf16 v[32:35], v[136:139], v[72:75], v[32:35]\n\t"
        "v_mfma_f32_16x16x32_bf16 v[36:39], v[140:143], v[72:75], v[36:39]\n\t"
        "ds_read_b128 v[68:71], v108 offset:34816\n\t"
        "v_mfma_f32_16x16x32_bf16 v[40:43], v[72:75], v[144:147], v[40:43]\n\t"
        "s_add_u32 m0, %[ld], 66560\n\t"
        "s_add_u32 %[t0], %[xo], 17408\n\t"
        "v_mfma_f32_16x16x32_bf16 v[44:47], v[136:139], v[76:79], v[44:47]\n\t"
        "buffer_load_dwordx4 v100, %[sx], %[t0] offen lds\n\t"
        "v_mfma_f32_16x16x32_bf16 v[48:51], v[140:143], v[76:79], v[48:51]\n\t"
        "ds_read_b128 v[72:75], v107 offset:40960\n\t"
        "v_mfma_f32_16x16x32_bf16 v[52:55], v[76:79], v[144:147], v[52:55]\n\t"
        "v_mfma_f32_16x16x32_bf16 v[32:35], v[148:151], v[80:83], v[32:35]\n\t"
        "v_mfma_f32_16x16x32_bf16 v[36:39], v[152:155], v[80:83], v[36:39]\n\t"
        "ds_read_b128 v[76:79], v107 offset:43008\n\t"
        "v_mfma_f32_16x16x32_bf16 v[40:43], v[80:83], v[156:159], v[40:43]\n\t"
        "s_add_u32 m0, %[ld], 73728\n\t"
        "s_add_u32 %[t0], %[xo], 1152\n\t"
        "v_mfma_f32_16x16x32_bf16 v[44:47], v[148:151], v[84:87], v[44:47]\n\t"
        "buffer_load_dwordx4 v100, %[sx], %[t0] offen lds\n\t"
        "v_mfma_f32_16x16x32_bf16 v[48:51], v[152:155], v[84:87], v[48:51]\n\t"
        "ds_read_b128 v[80:83], v108 offset:40960\n\t"
        "v_mfma_f32_16x16x32_bf16 v[52:55], v[84:87], v[156:159], v[52:55]\n\t"
        "s_waitcnt lgkmcnt(6)\n\t"
        "v_mfma_f32_16x16x32_bf16 v[32:35], v[160:163], v[56:59], v[32:35]\n\t"
        "v_mfma_f32_16x16x32_bf16 v[36:39], v[164:167], v[56:59], v[36:39]\n\t"
        "ds_read_b128 v[84:87], v108 offset:43008\n\t"
        "v_mfma_f32_16x16x32_bf16 v[40:43], v[56:59], v[168:171], v[40:43]\n\t"
        "s_add_u32 m0, %[ld], 74752\n\t"
        "s_add_u32 %[t0], %[xo], 17536\n\t"
        "s_waitcnt lgkmcnt(6)\n\t"
        "v_mfma_f32_16x16x32_bf16 v[44:47], v[160:163], v[60:63], v[44:47]\n\t"
        "buffer_load_dwordx4 v100, %[sx], %[t0] offen lds\n\t"
        "v_mfma_f32_16x16x32_bf16 v[48:51], v[164:167], v[60:63], v[48:51]\n\t"
        "ds_read_b128 v[56:59], v107 offset:49152\n\t"
        "v_mfma_f32_16x16x32_bf16 v[52:55], v[60:63], v[168:171], v[52:55]\n\t"
        "s_waitcnt lgkmcnt(6)\n\t"
        "v_mfma_f32_16x16x32_bf16 v[32:35], v[172:175], v[64:67], v[32:35]\n\t"
        "v_mfma_f32_16x16x32_bf16 v[36:39], v[176:179], v[64:67], v[36:39]\n\t"
        "ds_read_b128 v[60:63], v107 offset:51200\n\t"
        "v_mfma_f32_16x16x32_bf16 v[40:43], v[64:67], v[180:183], v[40:43]\n\t"
        "s_add_u32 m0, %[ld], 81920\n\t"
        "s_add_u32 %[t0], %[xo], 1280\n\t"
        "s_waitcnt lgkmcnt(6)\n\t"
        "v_mfma_f32_16x16x32_bf16 v[44:47], v[172:175], v[68:71], v[44:47]\n\t"
        "buffer_load_dwordx4 v100, %[sx], %[t0] offen lds\n\t"
        "v_mfma_f32_16x16x32_bf16 v[48:51], v[176:179], v[68:71], v[48:51]\n\t"
        "ds_read_b128 v[64:67], v108 offset:49152\n\t"
        "v_mfma_f32_16x16x32_bf16 v[52:55], v[68:71], v[180:183], v[52:55]\n\t"
        "s_waitcnt lgkmcnt(6)\n\t"
        "v_mfma_f32_16x16x32_bf16 v[32:35], v[184:187], v[72:75], v[32:35]\n\t"
        "v_mfma_f32_16x16x32_bf16 v[36:39], v[188:191], v[72:75], v[36:39]\n\t"
        "ds_read_b128 v[68:71], v108 offset:51200\n\t"
        "v_mfma_f32_16x16x32_bf16 v[40:43], v[72:75], v[192:195], v[40:43]\n\t"
        "s_add_u32 m0, %[ld], 82944\n\t"
        "s_add_u32 %[t0], %[xo], 17664\n\t"
        "s_waitcnt lgkmcnt(6)\n\t"
        "v_mfma_f32_16x16x32_bf16 v[44:47], v[184:187], v[76:79], v[44:47]\n\t"
        "buffer_load_dwordx4 v100, %[sx], %[t0] offen lds\n\t"
        "v_mfma_f32_16x16x32_bf16 v[48:51], v[188:191], v[76:79], v[48:51]\n\t"
        "ds_read_b128 v[72:75], v107 offset:57344\n\t"
        "v_mfma_f32_16x16x32_bf16 v[52:55], v[76:79], v[192:195], v[52:55]\n\t"
        "s_waitcnt lgkmcnt(6)\n\t"
        "v_mfma_f32_16x16x32_bf16 v[32:35], v[196:199], v[80:83], v[32:35]\n\t"
        "v_mfma_f32_16x16x32_bf16 v[36:39], v[200:203], v[80:83], v[36:39]\n\t"
        "ds_read_b128 v[76:79], v107 offset:59392\n\t"
        "v_mfma_f32_16x16x32_bf16 v[40:43], v[80:83], v[204:207], v[40:43]\n\t"
        "s_add_u32 m0, %[ld], 90112\n\t"
        "s_add_u32 %[t0], %[xo], 1408\n\t"
        "s_waitcnt lgkmcnt(6)\n\t"
        "v_mfma_f32_16x16x32_bf16 v[44:47], v[196:199], v[84:87], v[44:47]\n\t"
        "buffer_load_dwordx4 v100, %[sx], %[t0] offen lds\n\t"
        "v_mfma_f32_16x16x32_bf16 v[48:51], v[200:203], v[84:87], v[48:51]\n\t"
        "ds_read_b128 v[80:83], v108 offset:57344\n\t"
        "v_mfma_f32_16x16x32_bf16 v[52:55], v[84:87], v[204:207], v[52:55]\n\t"
        "s_waitcnt lgkmcnt(6)\n\t"
        "v_mfma_f32_16x16x32_bf16 v[32:35], v[208:211], v[56:59], v[32:35]\n\t"
        "v_mfma_f32_16x16x32_bf16 v[36:39], v[212:215], v[56:59], v[36:39]\n\t"
        "ds_read_b128 v[84:87], v108 offset:59392\n\t"
        "v_mfma_f32_16x16x32_bf16 v[40:43], v[56:59], v[216:219], v[40:43]\n\t"
        "s_add_u32 m0, %[ld], 91136\n\t"
        "s_add_u32 %[t0], %[xo], 17792\n\t"
        "s_waitcnt lgkmcnt(6)\n\t"
        "v_mfma_f32_16x16x32_bf16 v[44:47], v[208:211], v[60:63], v[44:47]\n\t"
        "buffer_load_dwordx4 v100, %[sx], %[t0] offen lds\n\t"
        "v_mfma_f32_16x16x32_bf16 v[48:51], v[212:215], v[60:63], v[48:51]\n\t"
        "v_mfma_f32_16x16x32_bf16 v[52:55], v[60:63], v[216:219], v[52:55]\n\t"
        "s_waitcnt lgkmcnt(5)\n\t"
        "v_mfma_f32_16x16x32_bf16 v[32:35], v[220:223], v[64:67], v[32:35]\n\t"
        "s_waitcnt vmcnt(18) lgkmcnt(0)\n\t"
        "s_barrier\n\t"
        "v_mfma_f32_16x16x32_bf16 v[36:39], v[224:227], v[64:67], v[36:39]\n\t"
        "ds_read_b128 v[56:59], v105 offset:0\n\t"
        "v_mfma_f32_16x16x32_bf16 v[40:43], v[64:67], v[228:231], v[40:43]\n\t"
        "ds_read_b128 v[60:63], v105 offset:2048\n\t"
        "s_add_u32 m0, %[ld], 98304\n\t"
        "s_add_u32 %[t0], %[xo], 1536\n\t"
        "v_mfma_f32_16x16x32_bf16 v[44:47], v[220:223], v[68:71], v[44:47]\n\t"
        "buffer_load_dwordx4 v100, %[sx], %[t0] offen lds\n\t"
        "v_mfma_f32_16x16x32_bf16 v[48:51], v[224:227], v[68:71], v[48:51]\n\t"
        "ds_read_b128 v[64:67], v106 offset:0\n\t"
        "v_mfma_f32_16x16x32_bf16 v[52:55], v[68:71], v[228:231], v[52:55]\n\t"
        "v_mfma_f32_16x16x32_bf16 v[32:35], v[232:235], v[72:75], v[32:35]\n\t"
        "v_mfma_f32_16x16x32_bf16 v[36:39], v[236:239], v[72:75], v[36:39]\n\t"
        "ds_read_b128 v[68:71], v106 offset:2048\n\t"
        "v_mfma_f32_16x16x32_bf16 v[40:43], v[72:75], v[240:243], v[40:43]\n\t"
        "s_add_u32 m0, %[ld], 99328\n\t"
        "s_add_u32 %[t0], %[xo], 17920\n\t"
        "v_mfma_f32_16x16x32_bf16 v[44:47], v[232:235], v[76:79], v[44:47]\n\t"
        "buffer_load_dwordx4 v100, %[sx], %[t0] offen lds\n\t"
        "v_mfma_f32_16x16x32_bf16 v[48:51], v[236:239], v[76:79], v[48:51]\n\t"
        "ds_read_b128 v[72:75], v105 offset:8192\n\t"
        "v_mfma_f32_16x16x32_bf16 v[52:55], v[76:79], v[240:243], v[52:55]\n\t"
        "v_mfma_f32_16x16x32_bf16 v[32:35], v[244:247], v[80:83], v[32:35]\n\t"
        "v_mfma_f32_16x16x32_bf16 v[36:39], v[248:251], v[80:83], v[36:39]\n\t"
        "ds_read_b128 v[76:79], v105 offset:10240\n\t"
        "v_mfma_f32_16x16x32_bf16 v[40:43], v[80:83], v[252:255], v[40:43]\n\t"
        "s_add_u32 m0, %[ld], 106496\n\t"
        "s_add_u32 %[t0], %[xo], 1664\n\t"
        "v_mfma_f32_16x16x32_bf16 v[44:47], v[244:247], v[84:87], v[44:47]\n\t"
        "buffer_load_dwordx4 v100, %[sx], %[t0] offen lds\n\t"
        "v_mfma_f32_16x16x32_bf16 v[48:51], v[248:251], v[84:87], v[48:51]\n\t"
        "ds_read_b128 v[80:83], v106 offset:8192\n\t"
        "v_mfma_f32_16x16x32_bf16 v[52:55], v[84:87], v[252:255], v[52:55]\n\t"
        "s_cmp_lt_u32 %[cn], 32\n\t"
        "s_cbranch_scc1 LG_loop%=\n\t"
        "s_nop 7\n\t"
        "v_cvt_pk_bf16_f32 v88, v32, v33\n\t"
        "v_cvt_pk_bf16_f32 v89, v34, v35\n\t"
        "v_cvt_pk_bf16_f32 v90, v36, v37\n\t"
        "v_cvt_pk_bf16_f32 v91, v38, v39\n\t"
        "buffer_store_dwordx4 v[88:91], v109, %[sw], %[so] offen sc1\n\t"
        "v_cvt_pk_bf16_f32 v92, v44, v45\n\t"
        "v_cvt_pk_bf16_f32 v93, v46, v47\n\t"
        "v_cvt_pk_bf16_f32 v94, v48, v49\n\t"
        "v_cvt_pk_bf16_f32 v95, v50, v51\n\t"
        "buffer_store_dwordx4 v[92:95], v110, %[sw], %[so] offen sc1\n\t"
        "v_cvt_pk_bf16_f32 v96, v40, v41\n\t"
        "v_cvt_pk_bf16_f32 v97, v42, v43\n\t"
        "v_cvt_pk_bf16_f32 v98, v52, v53\n\t"
        "v_cvt_pk_bf16_f32 v99, v54, v55\n\t"
        "buffer_store_dwordx4 v[96:99], v111, %[sw], %[so] offen sc1\n\t"
        "s_waitcnt vmcnt(0) lgkmcnt(0)\n\t"
        "v_mov_b32_e32 v119, %[cn]\n\t"
        "buffer_store_dword v119, v113, %[sw], 0 offen sc1\n\t"
        "s_barrier\n\t"
        "s_branch LG_end%=\n\t"
        "LG_first%=:\n\t"
        "s_waitcnt vmcnt(0)\n\t"
        "v_mov_b32_e32 v119, %[cn]\n\t"
        "buffer_store_dword v119, v113, %[sw], 0 offen sc1\n\t"
        "s_branch LG_back%=\n\t"
        "LG_end%=:\n\t"
        ".Lk_end_G:\n\t"
        : [t0] "=&s"(t0), [xo] "=&s"(xo), [so] "=&s"(so), [cn] "=&s"(cn)
        : [sx] "s"(sx), [sq] "s"(sq), [sv] "s"(sv), [sw] "s"(sw), [ld] "s"(lb + w * 2048), [wv] "s"(2 * w + 2),
          "{v117}"((w * 8192 + (h * 4 + c) * 64 + l) * 128),
          "{v100}"((16 * w + (l >> 3)) * 2048 + (((l & 7) ^ (l >> 3)) << 4)), "{v101}"((8 * w + (l >> 3)) * 8192 + h * 256 + (c & 1) * 128 + (l & 7) * 16),
          "{v112}"((16 * w + (l >> 2)) * 8192 + h * 256 + cv * 64 + (l & 3) * 16), "{v102}"(lb + 131072 + w * 1024 + ((l * 16) ^ ((l & 16) << 2))), "{v116}"(lb + 131072 + w * 1024 + ((l * 16) ^ (l & 32))),
          "{v103}"(lr + rr * 128 + ((tn ^ (n >> 3)) * 64) + (n & 3) * 16), "{v104}"(lr + 8192 + rr * 64 + ((tn ^ (g & 1)) * 32) + (n & 3) * 8), "{v105}"(lx), "{v106}"(lx ^ 64u), "{v107}"(lx + 65536), "{v108}"((lx ^ 64u) + 65536),
          "{v109}"(aq), "{v110}"(aq + 4096), "{v111}"(W_VF + (tm * 32 + h) * 8192 + (2 * cv + tn) * 1024 + l * 16), "{v113}"(l == 0 ? F_KVQ + h * 1024 + (c * 4 + w) * 64 : OOB)
        : "memory", "scc", "m0", "v8", "v9", "v10", "v11", "v12", "v13", "v14", "v15", "v16", "v17", "v18", "v19", "v20", "v21", "v22", "v23", "v24", "v25", "v26", "v27", "v28", "v29", "v30", "v31", "v32", "v33", "v34", "v35", "v36", "v37", "v38", "v39", "v40", "v41", "v42", "v43", "v44", "v45", "v46", "v47", "v48", "v49", "v50", "v51", "v52", "v53", "v54", "v55", "v56", "v57", "v58", "v59", "v60", "v61", "v62", "v63", "v64", "v65", "v66", "v67", "v68", "v69", "v70", "v71", "v72", "v73", "v74", "v75", "v76", "v77", "v78", "v79", "v80", "v81", "v82", "v83", "v84", "v85", "v86", "v87", "v88", "v89", "v90", "v91", "v92", "v93", "v94", "v95", "v96", "v97", "v98", "v99", "v118", "v119", "v120", "v121", "v122", "v123", "v124", "v125", "v126", "v127", "v128", "v129", "v130", "v131", "v132", "v133", "v134", "v135", "v136", "v137", "v138", "v139", "v140", "v141", "v142", "v143", "v144", "v145", "v146", "v147", "v148", "v149", "v150", "v151", "v152", "v153", "v154", "v155", "v156", "v157", "v158", "v159", "v160", "v161", "v162", "v163", "v164", "v165", "v166", "v167", "v168", "v169", "v170", "v171", "v172", "v173", "v174", "v175", "v176", "v177", "v178", "v179", "v180", "v181", "v182", "v183", "v184", "v185", "v186", "v187", "v188", "v189", "v190", "v191", "v192", "v193", "v194", "v195", "v196", "v197", "v198", "v199", "v200", "v201", "v202", "v203", "v204", "v205", "v206", "v207", "v208", "v209", "v210", "v211", "v212", "v213", "v214", "v215", "v216", "v217", "v218", "v219", "v220", "v221", "v222", "v223", "v224", "v225", "v226", "v227", "v228", "v229", "v230", "v231", "v232", "v233", "v234", "v235", "v236", "v237", "v238", "v239", "v240", "v241", "v242", "v243", "v244", "v245", "v246", "v247", "v248", "v249", "v250", "v251", "v252", "v253", "v254", "v255", "a0", "a1", "a2", "a3", "a4", "a5", "a6", "a7", "a8", "a9", "a10", "a11", "a12", "a13", "a14", "a15", "a16", "a17", "a18", "a19", "a20", "a21", "a22", "a23", "a24", "a25", "a26", "a27", "a28", "a29", "a30", "a31", "a32", "a33", "a34", "a35", "a36", "a37", "a38", "a39", "a40", "a41", "a42", "a43", "a44", "a45", "a46", "a47", "a48", "a49", "a50", "a51", "a52", "a53", "a54", "a55", "a56", "a57", "a58", "a59", "a60", "a61", "a62", "a63", "a64", "a65", "a66", "a67", "a68", "a69", "a70", "a71", "a72", "a73", "a74", "a75", "a76", "a77", "a78", "a79", "a80", "a81", "a82", "a83", "a84", "a85", "a86", "a87", "a88", "a89", "a90", "a91", "a92", "a93", "a94", "a95", "a96", "a97", "a98", "a99", "a100", "a101", "a102", "a103", "a104", "a105", "a106", "a107", "a108", "a109", "a110", "a111", "a112", "a113", "a114", "a115", "a116", "a117", "a118", "a119", "a120", "a121", "a122", "a123", "a124", "a125", "a126", "a127", "a128", "a129", "a130", "a131", "a132", "a133", "a134", "a135", "a136", "a137", "a138", "a139", "a140", "a141", "a142", "a143", "a144", "a145", "a146", "a147", "a148", "a149", "a150", "a151", "a152", "a153", "a154", "a155", "a156", "a157", "a158", "a159", "a160", "a161", "a162", "a163", "a164", "a165", "a166", "a167", "a168", "a169", "a170", "a171", "a172", "a173", "a174", "a175", "a176", "a177", "a178", "a179", "a180", "a181", "a182", "a183", "a184", "a185", "a186", "a187", "a188", "a189", "a190", "a191", "a192", "a193", "a194", "a195", "a196", "a197", "a198", "a199", "a200", "a201", "a202", "a203", "a204", "a205", "a206", "a207", "a208", "a209", "a210", "a211", "a212", "a213", "a214", "a215", "a216", "a217", "a218", "a219", "a220", "a221", "a222", "a223", "a224", "a225", "a226", "a227", "a228", "a229", "a230", "a231", "a232", "a233", "a234", "a235", "a236", "a237", "a238", "a239", "a240", "a241", "a242", "a243", "a244", "a245", "a246", "a247", "a248", "a249", "a250", "a251", "a252", "a253", "a254", "a255");
}

__device__ __forceinline__ void out_block(const Params& p, unsigned char* lds, const int wv, const int rp, const int ro, const int nt) {
    const int w = wv, l = otid() & 63, n = l & 15, g = l >> 4;
    const unsigned lb = __builtin_amdgcn_readfirstlane((unsigned)(size_t)lds), stg = lb + 131072 + w * 8192;
    const v4u so = mk_srd(p.Wo, 1024 * 4096 * 2), sw = mk_srd(p.ws, W_TOTAL), su = mk_srd(p.out, S * D * 2);
    const unsigned lr = stg + (4 * g + (n >> 2)) * 128 + ((2 * (g & 1) + (n >> 3)) << 5) + (n & 3) * 8;
    unsigned t0, t1, tl = rp, pg, cu, nx, sp, re;
    asm volatile(
        "s_nop 4\n\t"
        "ds_write_b128 v111, a[0:3] offset:0\n\t"
        "ds_write_b128 v111, a[4:7] offset:1024\n\t"
        "ds_write_b128 v111, a[8:11] offset:2048\n\t"
        "ds_write_b128 v111, a[12:15] offset:3072\n\t"
        "ds_write_b128 v111, a[16:19] offset:4096\n\t"
        "ds_write_b128 v111, a[20:23] offset:5120\n\t"
        "ds_write_b128 v111, a[24:27] offset:6144\n\t"
        "ds_write_b128 v111, a[28:31] offset:7168\n\t"
        "s_add_u32 %[t1], %[ro], 7\n\t"
        "s_and_b32 %[t1], %[t1], 7\n\t"
        "s_lshl_b32 %[t1], %[t1], 18\n\t"
        "s_add_u32 %[t0], %[t1], 131072\n\t"
        "buffer_load_dwordx4 v[232:235], v110, %[so], %[t0] offen\n\t"
        "ds_read_b64_tr_b16 a[0:1], v112 offset:0\n\t"
        "ds_read_b64_tr_b16 a[2:3], v112 offset:2048\n\t"
        "ds_read_b64_tr_b16 a[12:13], v112 offset:4096\n\t"
        "ds_read_b64_tr_b16 a[14:15], v112 offset:6144\n\t"
        "s_add_u32 %[t0], %[t1], 147456\n\t"
        "buffer_load_dwordx4 v[236:239], v110, %[so], %[t0] offen\n\t"
        "s_waitcnt lgkmcnt(8)\n\t"
        "ds_read_b64_tr_b16 a[4:5], v113 offset:0\n\t"
        "ds_read_b64_tr_b16 a[6:7], v113 offset:2048\n\t"
        "ds_read_b64_tr_b16 a[16:17], v113 offset:4096\n\t"
        "ds_read_b64_tr_b16 a[18:19], v113 offset:6144\n\t"
        "s_add_u32 %[t0], %[t1], 163840\n\t"
        "buffer_load_dwordx4 v[240:243], v110, %[so], %[t0] offen\n\t"
        "s_waitcnt lgkmcnt(8)\n\t"
        "ds_read_b64_tr_b16 a[8:9], v114 offset:0\n\t"
        "ds_read_b64_tr_b16 a[10:11], v114 offset:2048\n\t"
        "ds_read_b64_tr_b16 a[20:21], v114 offset:4096\n\t"
        "ds_read_b64_tr_b16 a[22:23], v114 offset:6144\n\t"
        "s_add_u32 %[t0], %[t1], 180224\n\t"
        "buffer_load_dwordx4 v[244:247], v110, %[so], %[t0] offen\n\t"
        "s_waitcnt lgkmcnt(8)\n\t"
        "ds_read_b64_tr_b16 v[80:81], v115 offset:0\n\t"
        "ds_read_b64_tr_b16 v[82:83], v115 offset:2048\n\t"
        "ds_read_b64_tr_b16 v[84:85], v115 offset:4096\n\t"
        "ds_read_b64_tr_b16 v[86:87], v115 offset:6144\n\t"
        "s_add_u32 %[t0], %[t1], 196608\n\t"
        "buffer_load_dwordx4 v[248:251], v110, %[so], %[t0] offen\n\t"
        "s_waitcnt lgkmcnt(0)\n\t"
        "ds_write_b128 v116, v[80:83] offset:0\n\t"
        "ds_write_b128 v116, v[84:87] offset:1024\n\t"
        "ds_write_b128 v111, a[32:35] offset:0\n\t"
        "ds_write_b128 v111, a[36:39] offset:1024\n\t"
        "ds_write_b128 v111, a[40:43] offset:2048\n\t"
        "ds_write_b128 v111, a[44:47] offset:3072\n\t"
        "ds_write_b128 v111, a[48:51] offset:4096\n\t"
        "ds_write_b128 v111, a[52:55] offset:5120\n\t"
        "ds_write_b128 v111, a[56:59] offset:6144\n\t"
        "ds_write_b128 v111, a[60:63] offset:7168\n\t"
        "s_add_u32 %[t0], %[t1], 212992\n\t"
        "buffer_load_dwordx4 v[252:255], v110, %[so], %[t0] offen\n\t"
        "ds_read_b64_tr_b16 a[24:25], v112 offset:0\n\t"
        "ds_read_b64_tr_b16 a[26:27], v112 offset:2048\n\t"
        "ds_read_b64_tr_b16 a[36:37], v112 offset:4096\n\t"
        "ds_read_b64_tr_b16 a[38:39], v112 offset:6144\n\t"
        "s_add_u32 %[t0], %[t1], 229376\n\t"
        "buffer_load_dwordx4 v[24:27], v110, %[so], %[t0] offen\n\t"
        "s_waitcnt lgkmcnt(8)\n\t"
        "ds_read_b64_tr_b16 a[28:29], v113 offset:0\n\t"
        "ds_read_b64_tr_b16 a[30:31], v113 offset:2048\n\t"
        "ds_read_b64_tr_b16 a[40:41], v113 offset:4096\n\t"
        "ds_read_b64_tr_b16 a[42:43], v113 offset:6144\n\t"
        "s_add_u32 %[t0], %[t1], 245760\n\t"
        "buffer_load_dwordx4 v[28:31], v110, %[so], %[t0] offen\n\t"
        "s_waitcnt lgkmcnt(8)\n\t"
        "ds_read_b64_tr_b16 a[32:33], v114 offset:0\n\t"
        "ds_read_b64_tr_b16 a[34:35], v114 offset:2048\n\t"
        "ds_read_b64_tr_b16 a[44:45], v114 offset:4096\n\t"
        "ds_read_b64_tr_b16 a[46:47], v114 offset:6144\n\t"
        "s_add_u32 %[t1], %[ro], 7\n\t"
        "s_and_b32 %[t1], %[t1], 7\n\t"
        "s_lshl_b32 %[t1], %[t1], 18\n\t"
        "s_add_u32 %[t0], %[t1], 0\n\t"
        "buffer_load_dwordx4 v[208:211], v110, %[so], %[t0] offen\n\t"
        "s_waitcnt lgkmcnt(8)\n\t"
        "ds_read_b64_tr_b16 v[80:81], v115 offset:0\n\t"
        "ds_read_b64_tr_b16 v[82:83], v115 offset:2048\n\t"
        "ds_read_b64_tr_b16 v[84:85], v115 offset:4096\n\t"
        "ds_read_b64_tr_b16 v[86:87], v115 offset:6144\n\t"
        "s_add_u32 %[t0], %[t1], 16384\n\t"
        "buffer_load_dwordx4 v[212:215], v110, %[so], %[t0] offen\n\t"
        "s_waitcnt lgkmcnt(0)\n\t"
        "ds_write_b128 v116, v[80:83] offset:2048\n\t"
        "ds_write_b128 v116, v[84:87] offset:3072\n\t"
        "ds_write_b128 v111, a[64:67] offset:0\n\t"
        "ds_write_b128 v111, a[68:71] offset:1024\n\t"
        "ds_write_b128 v111, a[72:75] offset:2048\n\t"
        "ds_write_b128 v111, a[76:79] offset:3072\n\t"
        "ds_write_b128 v111, a[80:83] offset:4096\n\t"
        "ds_write_b128 v111, a[84:87] offset:5120\n\t"
        "ds_write_b128 v111, a[88:91] offset:6144\n\t"
        "ds_write_b128 v111, a[92:95] offset:7168\n\t"
        "s_add_u32 %[t0], %[t1], 32768\n\t"
        "buffer_load_dwordx4 v[216:219], v110, %[so], %[t0] offen\n\t"
        "ds_read_b64_tr_b16 a[48:49], v112 offset:0\n\t"
        "ds_read_b64_tr_b16 a[50:51], v112 offset:2048\n\t"
        "ds_read_b64_tr_b16 a[60:61], v112 offset:4096\n\t"
        "ds_read_b64_tr_b16 a[62:63], v112 offset:6144\n\t"
        "s_add_u32 %[t0], %[t1], 49152\n\t"
        "buffer_load_dwordx4 v[220:223], v110, %[so], %[t0] offen\n\t"
        "s_waitcnt lgkmcnt(8)\n\t"
        "ds_read_b64_tr_b16 a[52:53], v113 offset:0\n\t"
        "ds_read_b64_tr_b16 a[54:55], v113 offset:2048\n\t"
        "ds_read_b64_tr_b16 a[64:65], v113 offset:4096\n\t"
        "ds_read_b64_tr_b16 a[66:67], v113 offset:6144\n\t"
        "s_add_u32 %[t0], %[t1], 65536\n\t"
        "buffer_load_dwordx4 v[224:227], v110, %[so], %[t0] offen\n\t"
        "s_waitcnt lgkmcnt(8)\n\t"
        "ds_read_b64_tr_b16 a[56:57], v114 offset:0\n\t"
        "ds_read_b64_tr_b16 a[58:59], v114 offset:2048\n\t"
        "ds_read_b64_tr_b16 a[68:69], v114 offset:4096\n\t"
        "ds_read_b64_tr_b16 a[70:71], v114 offset:6144\n\t"
        "s_add_u32 %[t0], %[t1], 81920\n\t"
        "buffer_load_dwordx4 v[228:231], v110, %[so], %[t0] offen\n\t"
        "s_waitcnt lgkmcnt(8)\n\t"
        "ds_read_b64_tr_b16 v[80:81], v115 offset:0\n\t"
        "ds_read_b64_tr_b16 v[82:83], v115 offset:2048\n\t"
        "ds_read_b64_tr_b16 v[84:85], v115 offset:4096\n\t"
        "ds_read_b64_tr_b16 v[86:87], v115 offset:6144\n\t"
        "s_add_u32 %[t0], %[t1], 98304\n\t"
        "buffer_load_dwordx4 v[32:35], v110, %[so], %[t0] offen\n\t"
        "s_waitcnt lgkmcnt(0)\n\t"
        "ds_write_b128 v116, v[80:83] offset:4096\n\t"
        "ds_write_b128 v116, v[84:87] offset:5120\n\t"
        "ds_write_b128 v111, a[96:99] offset:0\n\t"
        "ds_write_b128 v111, a[100:103] offset:1024\n\t"
        "ds_write_b128 v111, a[104:107] offset:2048\n\t"
        "ds_write_b128 v111, a[108:111] offset:3072\n\t"
        "ds_write_b128 v111, a[112:115] offset:4096\n\t"
        "ds_write_b128 v111, a[116:119] offset:5120\n\t"
        "ds_write_b128 v111, a[120:123] offset:6144\n\t"
        "ds_write_b128 v111, a[124:127] offset:7168\n\t"
        "s_add_u32 %[t0], %[t1], 114688\n\t"
        "buffer_load_dwordx4 v[36:39], v110, %[so], %[t0] offen\n\t"
        "ds_read_b64_tr_b16 a[72:73], v112 offset:0\n\t"
        "ds_read_b64_tr_b16 a[74:75], v112 offset:2048\n\t"
        "ds_read_b64_tr_b16 a[84:85], v112 offset:4096\n\t"
        "ds_read_b64_tr_b16 a[86:87], v112 offset:6144\n\t"
        "s_add_u32 %[t1], %[ro], 6\n\t"
        "s_and_b32 %[t1], %[t1], 7\n\t"
        "s_lshl_b32 %[t1], %[t1], 18\n\t"
        "s_add_u32 %[t0], %[t1], 131072\n\t"
        "buffer_load_dwordx4 v[184:187], v110, %[so], %[t0] offen\n\t"
        "s_waitcnt lgkmcnt(8)\n\t"
        "ds_read_b64_tr_b16 a[76:77], v113 offset:0\n\t"
        "ds_read_b64_tr_b16 a[78:79], v113 offset:2048\n\t"
        "ds_read_b64_tr_b16 a[88:89], v113 offset:4096\n\t"
        "ds_read_b64_tr_b16 a[90:91], v113 offset:6144\n\t"
        "s_add_u32 %[t0], %[t1], 147456\n\t"
        "buffer_load_dwordx4 v[188:191], v110, %[so], %[t0] offen\n\t"
        "s_waitcnt lgkmcnt(8)\n\t"
        "ds_read_b64_tr_b16 a[80:81], v114 offset:0\n\t"
        "ds_read_b64_tr_b16 a[82:83], v114 offset:2048\n\t"
        "ds_read_b64_tr_b16 a[92:93], v114 offset:4096\n\t"
        "ds_read_b64_tr_b16 a[94:95], v114 offset:6144\n\t"
        "s_add_u32 %[t0], %[t1], 163840\n\t"
        "buffer_load_dwordx4 v[192:195], v110, %[so], %[t0] offen\n\t"
        "s_waitcnt lgkmcnt(8)\n\t"
        "ds_read_b64_tr_b16 v[80:81], v115 offset:0\n\t"
        "ds_read_b64_tr_b16 v[82:83], v115 offset:2048\n\t"
        "ds_read_b64_tr_b16 v[84:85], v115 offset:4096\n\t"
        "ds_read_b64_tr_b16 v[86:87], v115 offset:6144\n\t"
        "s_add_u32 %[t0], %[t1], 180224\n\t"
        "buffer_load_dwordx4 v[196:199], v110, %[so], %[t0] offen\n\t"
        "s_waitcnt lgkmcnt(0)\n\t"
        "ds_write_b128 v116, v[80:83] offset:6144\n\t"
        "ds_write_b128 v116, v[84:87] offset:7168\n\t"
        "ds_write_b128 v111, a[128:131] offset:0\n\t"
        "ds_write_b128 v111, a[132:135] offset:1024\n\t"
        "ds_write_b128 v111, a[136:139] offset:2048\n\t"
        "ds_write_b128 v111, a[140:143] offset:3072\n\t"
        "ds_write_b128 v111, a[144:147] offset:4096\n\t"
        "ds_write_b128 v111, a[148:151] offset:5120\n\t"
        "ds_write_b128 v111, a[152:155] offset:6144\n\t"
        "ds_write_b128 v111, a[156:159] offset:7168\n\t"
        "s_add_u32 %[t0], %[t1], 196608\n\t"
        "buffer_load_dwordx4 v[200:203], v110, %[so], %[t0] offen\n\t"
        "ds_read_b64_tr_b16 a[96:97], v112 offset:0\n\t"
        "ds_read_b64_tr_b16 a[98:99], v112 offset:2048\n\t"
        "ds_read_b64_tr_b16 a[108:109], v112 offset:4096\n\t"
        "ds_read_b64_tr_b16 a[110:111], v112 offset:6144\n\t"
        "s_add_u32 %[t0], %[t1], 212992\n\t"
        "buffer_load_dwordx4 v[204:207], v110, %[so], %[t0] offen\n\t"
        "s_waitcnt lgkmcnt(8)\n\t"
        "ds_read_b64_tr_b16 a[100:101], v113 offset:0\n\t"
        "ds_read_b64_tr_b16 a[102:103], v113 offset:2048\n\t"
        "ds_read_b64_tr_b16 a[112:113], v113 offset:4096\n\t"
        "ds_read_b64_tr_b16 a[114:115], v113 offset:6144\n\t"
        "s_add_u32 %[t0], %[t1], 229376\n\t"
        "buffer_load_dwordx4 v[40:43], v110, %[so], %[t0] offen\n\t"
        "s_waitcnt lgkmcnt(8)\n\t"
        "ds_read_b64_tr_b16 a[104:105], v114 offset:0\n\t"
        "ds_read_b64_tr_b16 a[106:107], v114 offset:2048\n\t"
        "ds_read_b64_tr_b16 a[116:117], v114 offset:4096\n\t"
        "ds_read_b64_tr_b16 a[118:119], v114 offset:6144\n\t"
        "s_add_u32 %[t0], %[t1], 245760\n\t"
        "buffer_load_dwordx4 v[44:47], v110, %[so], %[t0] offen\n\t"
        "s_waitcnt lgkmcnt(8)\n\t"
        "ds_read_b64_tr_b16 v[80:81], v115 offset:0\n\t"
        "ds_read_b64_tr_b16 v[82:83], v115 offset:2048\n\t"
        "ds_read_b64_tr_b16 v[84:85], v115 offset:4096\n\t"
        "ds_read_b64_tr_b16 v[86:87], v115 offset:6144\n\t"
        "s_add_u32 %[t1], %[ro], 6\n\t"
        "s_and_b32 %[t1], %[t1], 7\n\t"
        "s_lshl_b32 %[t1], %[t1], 18\n\t"
        "s_add_u32 %[t0], %[t1], 0\n\t"
        "buffer_load_dwordx4 v[160:163], v110, %[so], %[t0] offen\n\t"
        "s_waitcnt lgkmcnt(0)\n\t"
        "ds_write_b128 v116, v[80:83] offset:8192\n\t"
        "ds_write_b128 v116, v[84:87] offset:9216\n\t"
        "ds_write_b128 v111, a[160:163] offset:0\n\t"
        "ds_write_b128 v111, a[164:167] offset:1024\n\t"
        "ds_write_b128 v111, a[168:171] offset:2048\n\t"
        "ds_write_b128 v111, a[172:175] offset:3072\n\t"
        "ds_write_b128 v111, a[176:179] offset:4096\n\t"
        "ds_write_b128 v111, a[180:183] offset:5120\n\t"
        "ds_write_b128 v111, a[184:187] offset:6144\n\t"
        "ds_write_b128 v111, a[188:191] offset:7168\n\t"
        "s_add_u32 %[t0], %[t1], 16384\n\t"
        "buffer_load_dwordx4 v[164:167], v110, %[so], %[t0] offen\n\t"
        "ds_read_b64_tr_b16 a[120:121], v112 offset:0\n\t"
        "ds_read_b64_tr_b16 a[122:123], v112 offset:2048\n\t"
        "ds_read_b64_tr_b16 a[132:133], v112 offset:4096\n\t"
        "ds_read_b64_tr_b16 a[134:135], v112 offset:6144\n\t"
        "s_add_u32 %[t0], %[t1], 32768\n\t"
        "buffer_load_dwordx4 v[168:171], v110, %[so], %[t0] offen\n\t"
        "s_waitcnt lgkmcnt(8)\n\t"
        "ds_read_b64_tr_b16 a[124:125], v113 offset:0\n\t"
        "ds_read_b64_tr_b16 a[126:127], v113 offset:2048\n\t"
        "ds_read_b64_tr_b16 a[136:137], v113 offset:4096\n\t"
        "ds_read_b64_tr_b16 a[138:139], v113 offset:6144\n\t"
        "s_add_u32 %[t0], %[t1], 49152\n\t"
        "buffer_load_dwordx4 v[172:175], v110, %[so], %[t0] offen\n\t"
        "s_waitcnt lgkmcnt(8)\n\t"
        "ds_read_b64_tr_b16 a[128:129], v114 offset:0\n\t"
        "ds_read_b64_tr_b16 a[130:131], v114 offset:2048\n\t"
        "ds_read_b64_tr_b16 a[140:141], v114 offset:4096\n\t"
        "ds_read_b64_tr_b16 a[142:143], v114 offset:6144\n\t"
        "s_add_u32 %[t0], %[t1], 65536\n\t"
        "buffer_load_dwordx4 v[176:179], v110, %[so], %[t0] offen\n\t"
        "s_waitcnt lgkmcnt(8)\n\t"
        "ds_read_b64_tr_b16 v[80:81], v115 offset:0\n\t"
        "ds_read_b64_tr_b16 v[82:83], v115 offset:2048\n\t"
        "ds_read_b64_tr_b16 v[84:85], v115 offset:4096\n\t"
        "ds_read_b64_tr_b16 v[86:87], v115 offset:6144\n\t"
        "s_add_u32 %[t0], %[t1], 81920\n\t"
        "buffer_load_dwordx4 v[180:183], v110, %[so], %[t0] offen\n\t"
        "s_waitcnt lgkmcnt(0)\n\t"
        "ds_write_b128 v116, v[80:83] offset:10240\n\t"
        "ds_write_b128 v116, v[84:87] offset:11264\n\t"
        "ds_write_b128 v111, a[192:195] offset:0\n\t"
        "ds_write_b128 v111, a[196:199] offset:1024\n\t"
        "ds_write_b128 v111, a[200:203] offset:2048\n\t"
        "ds_write_b128 v111, a[204:207] offset:3072\n\t"
        "ds_write_b128 v111, a[208:211] offset:4096\n\t"
        "ds_write_b128 v111, a[212:215] offset:5120\n\t"
        "ds_write_b128 v111, a[216:219] offset:6144\n\t"
        "ds_write_b128 v111, a[220:223] offset:7168\n\t"
        "s_add_u32 %[t0], %[t1], 98304\n\t"
        "buffer_load_dwordx4 v[48:51], v110, %[so], %[t0] offen\n\t"
        "ds_read_b64_tr_b16 a[144:145], v112 offset:0\n\t"
        "ds_read_b64_tr_b16 a[146:147], v112 offset:2048\n\t"
        "ds_read_b64_tr_b16 a[156:157], v112 offset:4096\n\t"
        "ds_read_b64_tr_b16 a[158:159], v112 offset:6144\n\t"
        "s_add_u32 %[t0], %[t1], 114688\n\t"
        "buffer_load_dwordx4 v[52:55], v110, %[so], %[t0] offen\n\t"
        "s_waitcnt lgkmcnt(8)\n\t"
        "ds_read_b64_tr_b16 a[148:149], v113 offset:0\n\t"
        "ds_read_b64_tr_b16 a[150:151], v113 offset:2048\n\t"
        "ds_read_b64_tr_b16 a[160:161], v113 offset:4096\n\t"
        "ds_read_b64_tr_b16 a[162:163], v113 offset:6144\n\t"
        "s_add_u32 %[t1], %[ro], 5\n\t"
        "s_and_b32 %[t1], %[t1], 7\n\t"
        "s_lshl_b32 %[t1], %[t1], 18\n\t"
        "s_add_u32 %[t0], %[t1], 131072\n\t"
        "buffer_load_dwordx4 v[136:139], v110, %[so], %[t0] offen\n\t"
        "s_waitcnt lgkmcnt(8)\n\t"
        "ds_read_b64_tr_b16 a[152:153], v114 offset:0\n\t"
        "ds_read_b64_tr_b16 a[154:155], v114 offset:2048\n\t"
        "ds_read_b64_tr_b16 a[164:165], v114 offset:4096\n\t"
        "ds_read_b64_tr_b16 a[166:167], v114 offset:6144\n\t"
        "s_add_u32 %[t0], %[t1], 147456\n\t"
        "buffer_load_dwordx4 v[140:143], v110, %[so], %[t0] offen\n\t"
        "s_waitcnt lgkmcnt(8)\n\t"
        "ds_read_b64_tr_b16 v[80:81], v115 offset:0\n\t"
        "ds_read_b64_tr_b16 v[82:83], v115 offset:2048\n\t"
        "ds_read_b64_tr_b16 v[84:85], v115 offset:4096\n\t"
        "ds_read_b64_tr_b16 v[86:87], v115 offset:6144\n\t"
        "s_add_u32 %[t0], %[t1], 163840\n\t"
        "buffer_load_dwordx4 v[144:147], v110, %[so], %[t0] offen\n\t"
        "s_waitcnt lgkmcnt(0)\n\t"
        "ds_write_b128 v116, v[80:83] offset:12288\n\t"
        "ds_write_b128 v116, v[84:87] offset:13312\n\t"
        "ds_write_b128 v111, a[224:227] offset:0\n\t"
        "ds_write_b128 v111, a[228:231] offset:1024\n\t"
        "ds_write_b128 v111, a[232:235] offset:2048\n\t"
        "ds_write_b128 v111, a[236:239] offset:3072\n\t"
        "ds_write_b128 v111, a[240:243] offset:4096\n\t"
        "ds_write_b128 v111, a[244:247] offset:5120\n\t"
        "ds_write_b128 v111, a[248:251] offset:6144\n\t"
        "ds_write_b128 v111, a[252:255] offset:7168\n\t"
        "s_add_u32 %[t0], %[t1], 180224\n\t"
        "buffer_load_dwordx4 v[148:151], v110, %[so], %[t0] offen\n\t"
        "ds_read_b64_tr_b16 a[168:169], v112 offset:0\n\t"
        "ds_read_b64_tr_b16 a[170:171], v112 offset:2048\n\t"
        "ds_read_b64_tr_b16 a[180:181], v112 offset:4096\n\t"
        "ds_read_b64_tr_b16 a[182:183], v112 offset:6144\n\t"
        "s_add_u32 %[t0], %[t1], 196608\n\t"
        "buffer_load_dwordx4 v[152:155], v110, %[so], %[t0] offen\n\t"
        "s_waitcnt lgkmcnt(8)\n\t"
        "ds_read_b64_tr_b16 a[172:173], v113 offset:0\n\t"
        "ds_read_b64_tr_b16 a[174:175], v113 offset:2048\n\t"
        "ds_read_b64_tr_b16 a[184:185], v113 offset:4096\n\t"
        "ds_read_b64_tr_b16 a[186:187], v113 offset:6144\n\t"
        "s_add_u32 %[t0], %[t1], 212992\n\t"
        "buffer_load_dwordx4 v[156:159], v110, %[so], %[t0] offen\n\t"
        "s_waitcnt lgkmcnt(8)\n\t"
        "ds_read_b64_tr_b16 a[176:177], v114 offset:0\n\t"
        "ds_read_b64_tr_b16 a[178:179], v114 offset:2048\n\t"
        "ds_read_b64_tr_b16 a[188:189], v114 offset:4096\n\t"
        "ds_read_b64_tr_b16 a[190:191], v114 offset:6144\n\t"
        "s_add_u32 %[t0], %[t1], 229376\n\t"
        "buffer_load_dwordx4 v[56:59], v110, %[so], %[t0] offen\n\t"
        "s_waitcnt lgkmcnt(8)\n\t"
        "ds_read_b64_tr_b16 v[80:81], v115 offset:0\n\t"
        "ds_read_b64_tr_b16 v[82:83], v115 offset:2048\n\t"
        "ds_read_b64_tr_b16 v[84:85], v115 offset:4096\n\t"
        "ds_read_b64_tr_b16 v[86:87], v115 offset:6144\n\t"
        "s_add_u32 %[t0], %[t1], 245760\n\t"
        "buffer_load_dwordx4 v[60:63], v110, %[so], %[t0] offen\n\t"
        "s_waitcnt lgkmcnt(0)\n\t"
        "ds_write_b128 v116, v[80:83] offset:14336\n\t"
        "ds_write_b128 v116, v[84:87] offset:15360\n\t"
        "s_add_u32 %[t1], %[ro], 5\n\t"
        "s_and_b32 %[t1], %[t1], 7\n\t"
        "s_lshl_b32 %[t1], %[t1], 18\n\t"
        "s_add_u32 %[t0], %[t1], 0\n\t"
        "buffer_load_dwordx4 a[240:243], v110, %[so], %[t0] offen\n\t"
        "s_waitcnt vmcnt(33)\n\t"
        "ds_write_b128 v111, v[232:235] offset:0\n\t"
        "ds_write_b128 v111, v[236:239] offset:1024\n\t"
        "ds_write_b128 v111, v[240:243] offset:2048\n\t"
        "ds_write_b128 v111, v[244:247] offset:3072\n\t"
        "ds_write_b128 v111, v[248:251] offset:4096\n\t"
        "ds_write_b128 v111, v[252:255] offset:5120\n\t"
        "ds_write_b128 v111, v[24:27] offset:6144\n\t"
        "ds_write_b128 v111, v[28:31] offset:7168\n\t"
        "s_add_u32 %[t0], %[t1], 16384\n\t"
        "buffer_load_dwordx4 a[244:247], v110, %[so], %[t0] offen\n\t"
        "ds_read_b64_tr_b16 v[232:233], v112 offset:0\n\t"
        "ds_read_b64_tr_b16 v[234:235], v112 offset:2048\n\t"
        "ds_read_b64_tr_b16 v[244:245], v112 offset:4096\n\t"
        "ds_read_b64_tr_b16 v[246:247], v112 offset:6144\n\t"
        "s_add_u32 %[t0], %[t1], 32768\n\t"
        "buffer_load_dwordx4 a[248:251], v110, %[so], %[t0] offen\n\t"
        "s_waitcnt lgkmcnt(8)\n\t"
        "ds_read_b64_tr_b16 v[236:237], v113 offset:0\n\t"
        "ds_read_b64_tr_b16 v[238:239], v113 offset:2048\n\t"
        "ds_read_b64_tr_b16 v[248:249], v113 offset:4096\n\t"
        "ds_read_b64_tr_b16 v[250:251], v113 offset:6144\n\t"
        "s_add_u32 %[t0], %[t1], 49152\n\t"
        "buffer_load_dwordx4 a[252:255], v110, %[so], %[t0] offen\n\t"
        "s_waitcnt lgkmcnt(8)\n\t"
        "ds_read_b64_tr_b16 v[240:241], v114 offset:0\n\t"
        "ds_read_b64_tr_b16 v[242:243], v114 offset:2048\n\t"
        "ds_read_b64_tr_b16 v[252:253], v114 offset:4096\n\t"
        "ds_read_b64_tr_b16 v[254:255], v114 offset:6144\n\t"
        "s_add_u32 %[t0], %[t1], 65536\n\t"
        "buffer_load_dwordx4 v[128:131], v110, %[so], %[t0] offen\n\t"
        "s_waitcnt lgkmcnt(8)\n\t"
        "ds_read_b64_tr_b16 v[80:81], v115 offset:0\n\t"
        "ds_read_b64_tr_b16 v[82:83], v115 offset:2048\n\t"
        "ds_read_b64_tr_b16 v[84:85], v115 offset:4096\n\t"
        "ds_read_b64_tr_b16 v[86:87], v115 offset:6144\n\t"
        "s_add_u32 %[t0], %[t1], 81920\n\t"
        "buffer_load_dwordx4 v[132:135], v110, %[so], %[t0] offen\n\t"
        "s_waitcnt lgkmcnt(0)\n\t"
        "ds_write_b128 v116, v[80:83] offset:30720\n\t"
        "ds_write_b128 v116, v[84:87] offset:31744\n\t"
        "s_add_u32 %[t0], %[t1], 98304\n\t"
        "buffer_load_dwordx4 v[64:67], v110, %[so], %[t0] offen\n\t"
        "s_waitcnt vmcnt(31)\n\t"
        "ds_write_b128 v111, v[208:211] offset:0\n\t"
        "ds_write_b128 v111, v[212:215] offset:1024\n\t"
        "ds_write_b128 v111, v[216:219] offset:2048\n\t"
        "ds_write_b128 v111, v[220:223] offset:3072\n\t"
        "ds_write_b128 v111, v[224:227] offset:4096\n\t"
        "ds_write_b128 v111, v[228:231] offset:5120\n\t"
        "ds_write_b128 v111, v[32:35] offset:6144\n\t"
        "ds_write_b128 v111, v[36:39] offset:7168\n\t"
        "s_add_u32 %[t0], %[t1], 114688\n\t"
        "buffer_load_dwordx4 v[68:71], v110, %[so], %[t0] offen\n\t"
        "ds_read_b64_tr_b16 v[208:209], v112 offset:0\n\t"
        "ds_read_b64_tr_b16 v[210:211], v112 offset:2048\n\t"
        "ds_read_b64_tr_b16 v[220:221], v112 offset:4096\n\t"
        "ds_read_b64_tr_b16 v[222:223], v112 offset:6144\n\t"
        "s_add_u32 %[t1], %[ro], 4\n\t"
        "s_and_b32 %[t1], %[t1], 7\n\t"
        "s_lshl_b32 %[t1], %[t1], 18\n\t"
        "s_add_u32 %[t0], %[t1], 131072\n\t"
        "buffer_load_dwordx4 a[216:219], v110, %[so], %[t0] offen\n\t"
        "s_waitcnt lgkmcnt(8)\n\t"
        "ds_read_b64_tr_b16 v[212:213], v113 offset:0\n\t"
        "ds_read_b64_tr_b16 v[214:215], v113 offset:2048\n\t"
        "ds_read_b64_tr_b16 v[224:225], v113 offset:4096\n\t"
        "ds_read_b64_tr_b16 v[226:227], v113 offset:6144\n\t"
        "s_add_u32 %[t0], %[t1], 147456\n\t"
        "buffer_load_dwordx4 a[220:223], v110, %[so], %[t0] offen\n\t"
        "s_waitcnt lgkmcnt(8)\n\t"
        "ds_read_b64_tr_b16 v[216:217], v114 offset:0\n\t"
        "ds_read_b64_tr_b16 v[218:219], v114 offset:2048\n\t"
        "ds_read_b64_tr_b16 v[228:229], v114 offset:4096\n\t"
        "ds_read_b64_tr_b16 v[230:231], v114 offset:6144\n\t"
        "s_add_u32 %[t0], %[t1], 163840\n\t"
        "buffer_load_dwordx4 a[224:227], v110, %[so], %[t0] offen\n\t"
        "s_waitcnt lgkmcnt(8)\n\t"
        "ds_read_b64_tr_b16 v[80:81], v115 offset:0\n\t"
        "ds_read_b64_tr_b16 v[82:83], v115 offset:2048\n\t"
        "ds_read_b64_tr_b16 v[84:85], v115 offset:4096\n\t"
        "ds_read_b64_tr_b16 v[86:87], v115 offset:6144\n\t"
        "s_add_u32 %[t0], %[t1], 180224\n\t"
        "buffer_load_dwordx4 a[228:231], v110, %[so], %[t0] offen\n\t"
        "s_waitcnt lgkmcnt(0)\n\t"
        "ds_write_b128 v116, v[80:83] offset:28672\n\t"
        "ds_write_b128 v116, v[84:87] offset:29696\n\t"
        "s_add_u32 %[t0], %[t1], 196608\n\t"
        "buffer_load_dwordx4 a[232:235], v110, %[so], %[t0] offen\n\t"
        "s_waitcnt vmcnt(29)\n\t"
        "ds_write_b128 v111, v[184:187] offset:0\n\t"
        "ds_write_b128 v111, v[188:191] offset:1024\n\t"
        "ds_write_b128 v111, v[192:195] offset:2048\n\t"
        "ds_write_b128 v111, v[196:199] offset:3072\n\t"
        "ds_write_b128 v111, v[200:203] offset:4096\n\t"
        "ds_write_b128 v111, v[204:207] offset:5120\n\t"
        "ds_write_b128 v111, v[40:43] offset:6144\n\t"
        "ds_write_b128 v111, v[44:47] offset:7168\n\t"
        "s_add_u32 %[t0], %[t1], 212992\n\t"
        "buffer_load_dwordx4 a[236:239], v110, %[so], %[t0] offen\n\t"
        "ds_read_b64_tr_b16 v[184:185], v112 offset:0\n\t"
        "ds_read_b64_tr_b16 v[186:187], v112 offset:2048\n\t"
        "ds_read_b64_tr_b16 v[196:197], v112 offset:4096\n\t"
        "ds_read_b64_tr_b16 v[198:199], v112 offset:6144\n\t"
        "s_add_u32 %[t0], %[t1], 229376\n\t"
        "buffer_load_dwordx4 v[72:75], v110, %[so], %[t0] offen\n\t"
        "s_waitcnt lgkmcnt(8)\n\t"
        "ds_read_b64_tr_b16 v[188:189], v113 offset:0\n\t"
        "ds_read_b64_tr_b16 v[190:191], v113 offset:2048\n\t"
        "ds_read_b64_tr_b16 v[200:201], v113 offset:4096\n\t"
        "ds_read_b64_tr_b16 v[202:203], v113 offset:6144\n\t"
        "s_add_u32 %[t0], %[t1], 245760\n\t"
        "buffer_load_dwordx4 v[76:79], v110, %[so], %[t0] offen\n\t"
        "s_waitcnt lgkmcnt(8)\n\t"
        "ds_read_b64_tr_b16 v[192:193], v114 offset:0\n\t"
        "ds_read_b64_tr_b16 v[194:195], v114 offset:2048\n\t"
        "ds_read_b64_tr_b16 v[204:205], v114 offset:4096\n\t"
        "ds_read_b64_tr_b16 v[206:207], v114 offset:6144\n\t"
        "s_add_u32 %[t1], %[ro], 4\n\t"
        "s_and_b32 %[t1], %[t1], 7\n\t"
        "s_lshl_b32 %[t1], %[t1], 18\n\t"
        "s_add_u32 %[t0], %[t1], 0\n\t"
        "buffer_load_dwordx4 a[192:195], v110, %[so], %[t0] offen\n\t"
        "s_waitcnt lgkmcnt(8)\n\t"
        "ds_read_b64_tr_b16 v[80:81], v115 offset:0\n\t"
        "ds_read_b64_tr_b16 v[82:83], v115 offset:2048\n\t"
        "ds_read_b64_tr_b16 v[84:85], v115 offset:4096\n\t"
        "ds_read_b64_tr_b16 v[86:87], v115 offset:6144\n\t"
        "s_add_u32 %[t0], %[t1], 16384\n\t"
        "buffer_load_dwordx4 a[196:199], v110, %[so], %[t0] offen\n\t"
        "s_waitcnt lgkmcnt(0)\n\t"
        "ds_write_b128 v116, v[80:83] offset:26624\n\t"
        "ds_write_b128 v116, v[84:87] offset:27648\n\t"
        "s_add_u32 %[t0], %[t1], 32768\n\t"
        "buffer_load_dwordx4 a[200:203], v110, %[so], %[t0] offen\n\t"
        "s_waitcnt vmcnt(27)\n\t"
        "ds_write_b128 v111, v[160:163] offset:0\n\t"
        "ds_write_b128 v111, v[164:167] offset:1024\n\t"
        "ds_write_b128 v111, v[168:171] offset:2048\n\t"
        "ds_write_b128 v111, v[172:175] offset:3072\n\t"
        "ds_write_b128 v111, v[176:179] offset:4096\n\t"
        "ds_write_b128 v111, v[180:183] offset:5120\n\t"
        "ds_write_b128 v111, v[48:51] offset:6144\n\t"
        "ds_write_b128 v111, v[52:55] offset:7168\n\t"
        "s_add_u32 %[t0], %[t1], 49152\n\t"
        "buffer_load_dwordx4 a[204:207], v110, %[so], %[t0] offen\n\t"
        "ds_read_b64_tr_b16 v[160:161], v112 offset:0\n\t"
        "ds_read_b64_tr_b16 v[162:163], v112 offset:2048\n\t"
        "ds_read_b64_tr_b16 v[172:173], v112 offset:4096\n\t"
        "ds_read_b64_tr_b16 v[174:175], v112 offset:6144\n\t"
        "s_add_u32 %[t0], %[t1], 65536\n\t"
        "buffer_load_dwordx4 a[208:211], v110, %[so], %[t0] offen\n\t"
        "s_waitcnt lgkmcnt(8)\n\t"
        "ds_read_b64_tr_b16 v[164:165], v113 offset:0\n\t"
        "ds_read_b64_tr_b16 v[166:167], v113 offset:2048\n\t"
        "ds_read_b64_tr_b16 v[176:177], v113 offset:4096\n\t"
        "ds_read_b64_tr_b16 v[178:179], v113 offset:6144\n\t"
        "s_add_u32 %[t0], %[t1], 81920\n\t"
        "buffer_load_dwordx4 a[212:215], v110, %[so], %[t0] offen\n\t"
        "s_waitcnt lgkmcnt(8)\n\t"
        "ds_read_b64_tr_b16 v[168:169], v114 offset:0\n\t"
        "ds_read_b64_tr_b16 v[170:171], v114 offset:2048\n\t"
        "ds_read_b64_tr_b16 v[180:181], v114 offset:4096\n\t"
        "ds_read_b64_tr_b16 v[182:183], v114 offset:6144\n\t"
        "s_add_u32 %[t0], %[t1], 98304\n\t"
        "buffer_load_dwordx4 v[24:27], v110, %[so], %[t0] offen\n\t"
        "s_waitcnt lgkmcnt(8)\n\t"
        "ds_read_b64_tr_b16 v[80:81], v115 offset:0\n\t"
        "ds_read_b64_tr_b16 v[82:83], v115 offset:2048\n\t"
        "ds_read_b64_tr_b16 v[84:85], v115 offset:4096\n\t"
        "ds_read_b64_tr_b16 v[86:87], v115 offset:6144\n\t"
        "s_add_u32 %[t0], %[t1], 114688\n\t"
        "buffer_load_dwordx4 v[28:31], v110, %[so], %[t0] offen\n\t"
        "s_waitcnt lgkmcnt(0)\n\t"
        "ds_write_b128 v116, v[80:83] offset:24576\n\t"
        "ds_write_b128 v116, v[84:87] offset:25600\n\t"
        "s_waitcnt vmcnt(24)\n\t"
        "ds_write_b128 v111, v[136:139] offset:0\n\t"
        "ds_write_b128 v111, v[140:143] offset:1024\n\t"
        "ds_write_b128 v111, v[144:147] offset:2048\n\t"
        "ds_write_b128 v111, v[148:151] offset:3072\n\t"
        "ds_write_b128 v111, v[152:155] offset:4096\n\t"
        "ds_write_b128 v111, v[156:159] offset:5120\n\t"
        "ds_write_b128 v111, v[56:59] offset:6144\n\t"
        "ds_write_b128 v111, v[60:63] offset:7168\n\t"
        "ds_read_b64_tr_b16 v[136:137], v112 offset:0\n\t"
        "ds_read_b64_tr_b16 v[138:139], v112 offset:2048\n\t"
        "ds_read_b64_tr_b16 v[148:149], v112 offset:4096\n\t"
        "ds_read_b64_tr_b16 v[150:151], v112 offset:6144\n\t"
        "s_waitcnt lgkmcnt(8)\n\t"
        "ds_read_b64_tr_b16 v[140:141], v113 offset:0\n\t"
        "ds_read_b64_tr_b16 v[142:143], v113 offset:2048\n\t"
        "ds_read_b64_tr_b16 v[152:153], v113 offset:4096\n\t"
        "ds_read_b64_tr_b16 v[154:155], v113 offset:6144\n\t"
        "s_waitcnt lgkmcnt(8)\n\t"
        "ds_read_b64_tr_b16 v[144:145], v114 offset:0\n\t"
        "ds_read_b64_tr_b16 v[146:147], v114 offset:2048\n\t"
        "ds_read_b64_tr_b16 v[156:157], v114 offset:4096\n\t"
        "ds_read_b64_tr_b16 v[158:159], v114 offset:6144\n\t"
        "s_waitcnt lgkmcnt(8)\n\t"
        "ds_read_b64_tr_b16 v[80:81], v115 offset:0\n\t"
        "ds_read_b64_tr_b16 v[82:83], v115 offset:2048\n\t"
        "ds_read_b64_tr_b16 v[84:85], v115 offset:4096\n\t"
        "ds_read_b64_tr_b16 v[86:87], v115 offset:6144\n\t"
        "s_waitcnt lgkmcnt(0)\n\t"
        "ds_write_b128 v116, v[80:83] offset:22528\n\t"
        "ds_write_b128 v116, v[84:87] offset:23552\n\t"
        "s_waitcnt vmcnt(16)\n\t"
        "ds_write_b128 v111, a[240:243] offset:0\n\t"
        "ds_write_b128 v111, a[244:247] offset:1024\n\t"
        "ds_write_b128 v111, a[248:251] offset:2048\n\t"
        "ds_write_b128 v111, a[252:255] offset:3072\n\t"
        "ds_write_b128 v111, v[128:131] offset:4096\n\t"
        "ds_write_b128 v111, v[132:135] offset:5120\n\t"
        "ds_write_b128 v111, v[64:67] offset:6144\n\t"
        "ds_write_b128 v111, v[68:71] offset:7168\n\t"
        "ds_read_b64_tr_b16 a[240:241], v112 offset:0\n\t"
        "ds_read_b64_tr_b16 a[242:243], v112 offset:2048\n\t"
        "ds_read_b64_tr_b16 a[252:253], v112 offset:4096\n\t"
        "ds_read_b64_tr_b16 a[254:255], v112 offset:6144\n\t"
        "s_waitcnt lgkmcnt(8)\n\t"
        "ds_read_b64_tr_b16 a[244:245], v113 offset:0\n\t"
        "ds_read_b64_tr_b16 a[246:247], v113 offset:2048\n\t"
        "ds_read_b64_tr_b16 v[128:129], v113 offset:4096\n\t"
        "ds_read_b64_tr_b16 v[130:131], v113 offset:6144\n\t"
        "s_waitcnt lgkmcnt(8)\n\t"
        "ds_read_b64_tr_b16 a[248:249], v114 offset:0\n\t"
        "ds_read_b64_tr_b16 a[250:251], v114 offset:2048\n\t"
        "ds_read_b64_tr_b16 v[132:133], v114 offset:4096\n\t"
        "ds_read_b64_tr_b16 v[134:135], v114 offset:6144\n\t"
        "s_waitcnt lgkmcnt(8)\n\t"
        "ds_read_b64_tr_b16 v[80:81], v115 offset:0\n\t"
        "ds_read_b64_tr_b16 v[82:83], v115 offset:2048\n\t"
        "ds_read_b64_tr_b16 v[84:85], v115 offset:4096\n\t"
        "ds_read_b64_tr_b16 v[86:87], v115 offset:6144\n\t"
        "s_waitcnt lgkmcnt(0)\n\t"
        "ds_write_b128 v116, v[80:83] offset:20480\n\t"
        "ds_write_b128 v116, v[84:87] offset:21504\n\t"
        "s_waitcnt vmcnt(8)\n\t"
        "ds_write_b128 v111, a[216:219] offset:0\n\t"
        "ds_write_b128 v111, a[220:223] offset:1024\n\t"
        "ds_write_b128 v111, a[224:227] offset:2048\n\t"
        "ds_write_b128 v111, a[228:231] offset:3072\n\t"
        "ds_write_b128 v111, a[232:235] offset:4096\n\t"
        "ds_write_b128 v111, a[236:239] offset:5120\n\t"
        "ds_write_b128 v111, v[72:75] offset:6144\n\t"
        "ds_write_b128 v111, v[76:79] offset:7168\n\t"
        "ds_read_b64_tr_b16 a[216:217], v112 offset:0\n\t"
        "ds_read_b64_tr_b16 a[218:219], v112 offset:2048\n\t"
        "ds_read_b64_tr_b16 a[228:229], v112 offset:4096\n\t"
        "ds_read_b64_tr_b16 a[230:231], v112 offset:6144\n\t"
        "s_waitcnt lgkmcnt(8)\n\t"
        "ds_read_b64_tr_b16 a[220:221], v113 offset:0\n\t"
        "ds_read_b64_tr_b16 a[222:223], v113 offset:2048\n\t"
        "ds_read_b64_tr_b16 a[232:233], v113 offset:4096\n\t"
        "ds_read_b64_tr_b16 a[234:235], v113 offset:6144\n\t"
        "s_waitcnt lgkmcnt(8)\n\t"
        "ds_read_b64_tr_b16 a[224:225], v114 offset:0\n\t"
        "ds_read_b64_tr_b16 a[226:227], v114 offset:2048\n\t"
        "ds_read_b64_tr_b16 a[236:237], v114 offset:4096\n\t"
        "ds_read_b64_tr_b16 a[238:239], v114 offset:6144\n\t"
        "s_waitcnt lgkmcnt(8)\n\t"
        "ds_read_b64_tr_b16 v[80:81], v115 offset:0\n\t"
        "ds_read_b64_tr_b16 v[82:83], v115 offset:2048\n\t"
        "ds_read_b64_tr_b16 v[84:85], v115 offset:4096\n\t"
        "ds_read_b64_tr_b16 v[86:87], v115 offset:6144\n\t"
        "s_waitcnt lgkmcnt(0)\n\t"
        "ds_write_b128 v116, v[80:83] offset:18432\n\t"
        "ds_write_b128 v116, v[84:87] offset:19456\n\t"
        "s_waitcnt vmcnt(0)\n\t"
        "ds_write_b128 v111, a[192:195] offset:0\n\t"
        "ds_write_b128 v111, a[196:199] offset:1024\n\t"
        "ds_write_b128 v111, a[200:203] offset:2048\n\t"
        "ds_write_b128 v111, a[204:207] offset:3072\n\t"
        "ds_write_b128 v111, a[208:211] offset:4096\n\t"
        "ds_write_b128 v111, a[212:215] offset:5120\n\t"
        "ds_write_b128 v111, v[24:27] offset:6144\n\t"
        "ds_write_b128 v111, v[28:31] offset:7168\n\t"
        "ds_read_b64_tr_b16 a[192:193], v112 offset:0\n\t"
        "ds_read_b64_tr_b16 a[194:195], v112 offset:2048\n\t"
        "ds_read_b64_tr_b16 a[204:205], v112 offset:4096\n\t"
        "ds_read_b64_tr_b16 a[206:207], v112 offset:6144\n\t"
        "s_waitcnt lgkmcnt(8)\n\t"
        "ds_read_b64_tr_b16 a[196:197], v113 offset:0\n\t"
        "ds_read_b64_tr_b16 a[198:199], v113 offset:2048\n\t"
        "ds_read_b64_tr_b16 a[208:209], v113 offset:4096\n\t"
        "ds_read_b64_tr_b16 a[210:211], v113 offset:6144\n\t"
        "s_waitcnt lgkmcnt(8)\n\t"
        "ds_read_b64_tr_b16 a[200:201], v114 offset:0\n\t"
        "ds_read_b64_tr_b16 a[202:203], v114 offset:2048\n\t"
        "ds_read_b64_tr_b16 a[212:213], v114 offset:4096\n\t"
        "ds_read_b64_tr_b16 a[214:215], v114 offset:6144\n\t"
        "s_waitcnt lgkmcnt(8)\n\t"
        "ds_read_b64_tr_b16 v[80:81], v115 offset:0\n\t"
        "ds_read_b64_tr_b16 v[82:83], v115 offset:2048\n\t"
        "ds_read_b64_tr_b16 v[84:85], v115 offset:4096\n\t"
        "ds_read_b64_tr_b16 v[86:87], v115 offset:6144\n\t"
        "s_waitcnt lgkmcnt(0)\n\t"
        "ds_write_b128 v116, v[80:83] offset:16384\n\t"
        "ds_write_b128 v116, v[84:87] offset:17408\n\t"
        "ds_read_b128 v[88:91], v116 offset:0\n\t"
        "ds_read_b128 v[92:95], v116 offset:1024\n\t"
        "ds_read_b128 v[96:99], v116 offset:2048\n\t"
        "s_mov_b32 %[sp], 0\n\t"
        "LP_cv%=:\n\t"
        "buffer_load_dword v104, off, %[sw], %[cv] sc1\n\t"
        "s_waitcnt vmcnt(0) lgkmcnt(0)\n\t"
        "v_readfirstlane_b32 %[t0], v104\n\t"
        "s_cmp_ge_u32 %[t0], 8\n\t"
        "s_cbranch_scc1 LP_wait%=\n\t"
        "s_sleep 8\n\t"
        "s_add_u32 %[sp], %[sp], 1\n\t"
        "s_cmp_lt_u32 %[sp], 0x100000\n\t"
        "s_cbranch_scc1 LP_cv%=\n\t"
        "v_mov_b32_e32 v104, 256\n\t"
        "s_mov_b32 %[t0], 33024\n\t"
        "buffer_store_dword v104, off, %[sw], %[t0] sc1\n\t"
        "s_endpgm\n\t"
        "LP_wait%=:\n\t"
        "s_mov_b32 %[re], 0\n\t"
        "s_and_b32 %[sp], %[tl], 1\n\t"
        "s_lshl_b32 %[sp], %[sp], 14\n\t"
        "s_lshr_b32 %[t1], %[tl], 1\n\t"
        "s_lshl_b32 %[t1], %[t1], 6\n\t"
        "s_add_u32 %[t1], %[sp], %[t1]\n\t"
        "buffer_load_dword v100, v119, %[sw], %[t1] offen sc1\n\t"
        "s_add_u32 %[t1], %[t1], 32768\n\t"
        "buffer_load_dword v101, v119, %[sw], %[t1] offen sc1\n\t"
        "s_lshl_b32 %[t1], %[tl], 9\n\t"
        "buffer_load_dword v102, v120, %[sw], %[t1] offen sc1\n\t"
        "buffer_load_dword v103, v118, %[sw], 0 offen sc1\n\t"
        "LP_again%=:\n\t"
        "s_mov_b32 %[sp], 0\n\t"
        "LP_poll%=:\n\t"
        "buffer_load_dword v103, v118, %[sw], 0 offen sc1\n\t"
        "s_lshr_b32 %[t1], %[tl], 1\n\t"
        "s_waitcnt vmcnt(0)\n\t"
        "v_min_u32_dpp v103, v103, v103 quad_perm:[1,0,3,2] row_mask:0xf bank_mask:0xf\n\t"
        "s_nop 1\n\t"
        "v_min_u32_dpp v103, v103, v103 quad_perm:[2,3,0,1] row_mask:0xf bank_mask:0xf\n\t"
        "s_nop 1\n\t"
        "v_min_u32_dpp v103, v103, v103 row_ror:4 row_mask:0xf bank_mask:0xf\n\t"
        "s_nop 0\n\t"
        "v_readfirstlane_b32 %[pg], v103\n\t"
        "s_cmp_gt_u32 %[pg], %[t1]\n\t"
        "s_cbranch_scc1 LP_have%=\n\t"
        "s_add_u32 %[t0], %[pg], %[re]\n\t"
        "s_cmp_eq_u32 %[t0], 63\n\t"
        "s_cbranch_scc0 LP_no%=\n\t"
        "s_sleep 16\n\t"
        "s_branch LP_have%=\n\t"
        "LP_no%=:\n\t"
        "s_min_u32 %[t1], %[t1], 62\n\t"
        "s_cmp_ge_u32 %[pg], %[t1]\n\t"
        "s_cbranch_scc1 LP_near%=\n\t"
        "s_sleep 24\n\t"
        "LP_near%=:\n\t"
        "s_sleep 2\n\t"
        "s_add_u32 %[sp], %[sp], 1\n\t"
        "s_cmp_lt_u32 %[sp], 0x100000\n\t"
        "s_cbranch_scc1 LP_poll%=\n\t"
        "v_mov_b32_e32 v104, 128\n\t"
        "s_mov_b32 %[t0], 33024\n\t"
        "buffer_store_dword v104, off, %[sw], %[t0] sc1\n\t"
        "s_endpgm\n\t"
        "LP_have%=:\n\t"
        "s_lshl_b32 %[nx], %[tl], 17\n\t"
        "s_add_u32 %[t1], %[t1], 4\n\t"
        "s_cmp_gt_u32 %[pg], %[t1]\n\t"
        "s_cbranch_scc1 LP_run%=\n\t"
        "s_add_u32 %[t0], %[ro], 0\n\t"
        "s_and_b32 %[t0], %[t0], 7\n\t"
        "s_lshl_b32 %[t0], %[t0], 12\n\t"
        "s_add_u32 %[t0], %[t0], %[nx]\n\t"
        "buffer_load_dwordx4 v[24:27], v117, %[sw], %[t0] offen offset:0 sc1\n\t"
        "buffer_load_dwordx4 v[28:31], v117, %[sw], %[t0] offen offset:1024 sc1\n\t"
        "buffer_load_dwordx4 v[32:35], v117, %[sw], %[t0] offen offset:2048 sc1\n\t"
        "buffer_load_dwordx4 v[36:39], v117, %[sw], %[t0] offen offset:3072 sc1\n\t"
        "s_add_u32 %[t0], %[ro], 1\n\t"
        "s_and_b32 %[t0], %[t0], 7\n\t"
        "s_lshl_b32 %[t0], %[t0], 12\n\t"
        "s_add_u32 %[t0], %[t0], %[nx]\n\t"
        "buffer_load_dwordx4 v[40:43], v117, %[sw], %[t0] offen offset:0 sc1\n\t"
        "buffer_load_dwordx4 v[44:47], v117, %[sw], %[t0] offen offset:1024 sc1\n\t"
        "s_waitcnt vmcnt(5) lgkmcnt(2)\n\t"
        "v_mfma_f32_16x16x32_bf16 v[20:23], v[88:91], v[24:27], 0\n\t"
        "ds_read_b128 v[88:91], v116 offset:3072\n\t"
        "v_mfma_f32_16x16x32_bf16 v[8:11], a[0:3], v[24:27], 0\n\t"
        "v_max3_u32 v125, v24, v25, v26\n\t"
        "buffer_load_dwordx4 v[48:51], v117, %[sw], %[t0] offen offset:2048 sc1\n\t"
        "v_mfma_f32_16x16x32_bf16 v[12:15], a[4:7], v[24:27], 0\n\t"
        "v_max_u32_e32 v125, v27, v125\n\t"
        "v_mfma_f32_16x16x32_bf16 v[16:19], a[8:11], v[24:27], 0\n\t"
        "buffer_load_dwordx4 v[52:55], v117, %[sw], %[t0] offen offset:3072 sc1\n\t"
        "s_waitcnt vmcnt(6) lgkmcnt(2)\n\t"
        "v_mfma_f32_16x16x32_bf16 v[20:23], v[92:95], v[28:31], v[20:23]\n\t"
        "ds_read_b128 v[92:95], v116 offset:4096\n\t"
        "v_mfma_f32_16x16x32_bf16 v[8:11], a[12:15], v[28:31], v[8:11]\n\t"
        "v_max3_u32 v125, v28, v29, v125\n\t"
        "s_add_u32 %[t0], %[ro], 2\n\t"
        "s_and_b32 %[t0], %[t0], 7\n\t"
        "s_lshl_b32 %[t0], %[t0], 12\n\t"
        "s_add_u32 %[t0], %[t0], %[nx]\n\t"
        "buffer_load_dwordx4 v[56:59], v117, %[sw], %[t0] offen offset:0 sc1\n\t"
        "v_mfma_f32_16x16x32_bf16 v[12:15], a[16:19], v[28:31], v[12:15]\n\t"
        "v_max3_u32 v125, v30, v31, v125\n\t"
        "v_mfma_f32_16x16x32_bf16 v[16:19], a[20:23], v[28:31], v[16:19]\n\t"
        "buffer_load_dwordx4 v[60:63], v117, %[sw], %[t0] offen offset:1024 sc1\n\t"
        "s_waitcnt vmcnt(7) lgkmcnt(2)\n\t"
        "v_mfma_f32_16x16x32_bf16 v[20:23], v[96:99], v[32:35], v[20:23]\n\t"
        "ds_read_b128 v[96:99], v116 offset:5120\n\t"
        "v_mfma_f32_16x16x32_bf16 v[8:11], a[24:27], v[32:35], v[8:11]\n\t"
        "v_max3_u32 v125, v32, v33, v125\n\t"
        "buffer_load_dwordx4 v[64:67], v117, %[sw], %[t0] offen offset:2048 sc1\n\t"
        "v_mfma_f32_16x16x32_bf16 v[12:15], a[28:31], v[32:35], v[12:15]\n\t"
        "v_max3_u32 v125, v34, v35, v125\n\t"
        "v_mfma_f32_16x16x32_bf16 v[16:19], a[32:35], v[32:35], v[16:19]\n\t"
        "buffer_load_dwordx4 v[68:71], v117, %[sw], %[t0] offen offset:3072 sc1\n\t"
        "s_waitcnt vmcnt(8) lgkmcnt(2)\n\t"
        "v_mfma_f32_16x16x32_bf16 v[20:23], v[88:91], v[36:39], v[20:23]\n\t"
        "ds_read_b128 v[88:91], v116 offset:6144\n\t"
        "v_mfma_f32_16x16x32_bf16 v[8:11], a[36:39], v[36:39], v[8:11]\n\t"
        "v_max3_u32 v125, v36, v37, v125\n\t"
        "s_add_u32 %[t0], %[ro], 3\n\t"
        "s_and_b32 %[t0], %[t0], 7\n\t"
        "s_lshl_b32 %[t0], %[t0], 12\n\t"
        "s_add_u32 %[t0], %[t0], %[nx]\n\t"
        "buffer_load_dwordx4 v[72:75], v117, %[sw], %[t0] offen offset:0 sc1\n\t"
        "v_mfma_f32_16x16x32_bf16 v[12:15], a[40:43], v[36:39], v[12:15]\n\t"
        "v_max3_u32 v125, v38, v39, v125\n\t"
        "v_mfma_f32_16x16x32_bf16 v[16:19], a[44:47], v[36:39], v[16:19]\n\t"
        "buffer_load_dwordx4 v[76:79], v117, %[sw], %[t0] offen offset:1024 sc1\n\t"
        "s_waitcnt vmcnt(9) lgkmcnt(2)\n\t"
        "v_mfma_f32_16x16x32_bf16 v[20:23], v[92:95], v[40:43], v[20:23]\n\t"
        "ds_read_b128 v[92:95], v116 offset:7168\n\t"
        "v_mfma_f32_16x16x32_bf16 v[8:11], a[48:51], v[40:43], v[8:11]\n\t"
        "v_max3_u32 v125, v40, v41, v125\n\t"
        "buffer_load_dwordx4 v[80:83], v117, %[sw], %[t0] offen offset:2048 sc1\n\t"
        "v_mfma_f32_16x16x32_bf16 v[12:15], a[52:55], v[40:43], v[12:15]\n\t"
        "v_max3_u32 v125, v42, v43, v125\n\t"
        "v_mfma_f32_16x16x32_bf16 v[16:19], a[56:59], v[40:43], v[16:19]\n\t"
        "buffer_load_dwordx4 v[84:87], v117, %[sw], %[t0] offen offset:3072 sc1\n\t"
        "s_waitcnt vmcnt(10) lgkmcnt(2)\n\t"
        "v_mfma_f32_16x16x32_bf16 v[20:23], v[96:99], v[44:47], v[20:23]\n\t"
        "ds_read_b128 v[96:99], v116 offset:8192\n\t"
        "v_mfma_f32_16x16x32_bf16 v[8:11], a[60:63], v[44:47], v[8:11]\n\t"
        "v_max3_u32 v125, v44, v45, v125\n\t"
        "s_add_u32 %[t0], %[ro], 4\n\t"
        "s_and_b32 %[t0], %[t0], 7\n\t"
        "s_lshl_b32 %[t0], %[t0], 12\n\t"
        "s_add_u32 %[t0], %[t0], %[nx]\n\t"
        "buffer_load_dwordx4 v[24:27], v117, %[sw], %[t0] offen offset:0 sc1\n\t"
        "v_mfma_f32_16x16x32_bf16 v[12:15], a[64:67], v[44:47], v[12:15]\n\t"
        "v_max3_u32 v125, v46, v47, v125\n\t"
        "v_mfma_f32_16x16x32_bf16 v[16:19], a[68:71], v[44:47], v[16:19]\n\t"
        "buffer_load_dwordx4 v[28:31], v117, %[sw], %[t0] offen offset:1024 sc1\n\t"
        "s_waitcnt vmcnt(11) lgkmcnt(2)\n\t"
        "v_mfma_f32_16x16x32_bf16 v[20:23], v[88:91], v[48:51], v[20:23]\n\t"
        "ds_read_b128 v[88:91], v116 offset:9216\n\t"
        "v_mfma_f32_16x16x32_bf16 v[8:11], a[72:75], v[48:51], v[8:11]\n\t"
        "v_max3_u32 v125, v48, v49, v125\n\t"
        "v_mfma_f32_16x16x32_bf16 v[12:15], a[76:79], v[48:51], v[12:15]\n\t"
        "v_max3_u32 v125, v50, v51, v125\n\t"
        "v_mfma_f32_16x16x32_bf16 v[16:19], a[80:83], v[48:51], v[16:19]\n\t"
        "buffer_load_dwordx4 v[32:35], v117, %[sw], %[t0] offen offset:2048 sc1\n\t"
        "s_waitcnt vmcnt(11) lgkmcnt(2)\n\t"
        "v_mfma_f32_16x16x32_bf16 v[20:23], v[92:95], v[52:55], v[20:23]\n\t"
        "ds_read_b128 v[92:95], v116 offset:10240\n\t"
        "v_mfma_f32_16x16x32_bf16 v[8:11], a[84:87], v[52:55], v[8:11]\n\t"
        "v_max3_u32 v125, v52, v53, v125\n\t"
        "v_mfma_f32_16x16x32_bf16 v[12:15], a[88:91], v[52:55], v[12:15]\n\t"
        "v_max3_u32 v125, v54, v55, v125\n\t"
        "v_mfma_f32_16x16x32_bf16 v[16:19], a[92:95], v[52:55], v[16:19]\n\t"
        "buffer_load_dwordx4 v[36:39], v117, %[sw], %[t0] offen offset:3072 sc1\n\t"
        "s_waitcnt vmcnt(11) lgkmcnt(2)\n\t"
        "s_cmp_lg_u32 %[re], 0\n\t"
        "s_cbranch_scc1 LP_nb%=\n\t"
        "s_barrier\n\t"
        "LP_nb%=:\n\t"
        "v_mfma_f32_16x16x32_bf16 v[20:23], v[96:99], v[56:59], v[20:23]\n\t"
        "ds_read_b128 v[96:99], v116 offset:11264\n\t"
        "v_mfma_f32_16x16x32_bf16 v[8:11], a[96:99], v[56:59], v[8:11]\n\t"
        "v_max3_u32 v125, v56, v57, v125\n\t"
        "v_mfma_f32_16x16x32_bf16 v[12:15], a[100:103], v[56:59], v[12:15]\n\t"
        "v_max3_u32 v125, v58, v59, v125\n\t"
        "v_mfma_f32_16x16x32_bf16 v[16:19], a[104:107], v[56:59], v[16:19]\n\t"
        "s_add_u32 %[t0], %[ro], 5\n\t"
        "s_and_b32 %[t0], %[t0], 7\n\t"
        "s_lshl_b32 %[t0], %[t0], 12\n\t"
        "s_add_u32 %[t0], %[t0], %[nx]\n\t"
        "buffer_load_dwordx4 v[40:43], v117, %[sw], %[t0] offen offset:0 sc1\n\t"
        "s_waitcnt vmcnt(11) lgkmcnt(2)\n\t"
        "v_mfma_f32_16x16x32_bf16 v[20:23], v[88:91], v[60:63], v[20:23]\n\t"
        "ds_read_b128 v[88:91], v116 offset:12288\n\t"
        "v_mfma_f32_16x16x32_bf16 v[8:11], a[108:111], v[60:63], v[8:11]\n\t"
        "v_max3_u32 v125, v60, v61, v125\n\t"
        "v_mfma_f32_16x16x32_bf16 v[12:15], a[112:115], v[60:63], v[12:15]\n\t"
        "v_max3_u32 v125, v62, v63, v125\n\t"
        "v_mfma_f32_16x16x32_bf16 v[16:19], a[116:119], v[60:63], v[16:19]\n\t"
        "buffer_load_dwordx4 v[44:47], v117, %[sw], %[t0] offen offset:1024 sc1\n\t"
        "s_waitcnt vmcnt(11) lgkmcnt(2)\n\t"
        "v_mfma_f32_16x16x32_bf16 v[20:23], v[92:95], v[64:67], v[20:23]\n\t"
        "ds_read_b128 v[92:95], v116 offset:13312\n\t"
        "v_mfma_f32_16x16x32_bf16 v[8:11], a[120:123], v[64:67], v[8:11]\n\t"
        "v_max3_u32 v125, v64, v65, v125\n\t"
        "v_mfma_f32_16x16x32_bf16 v[12:15], a[124:127], v[64:67], v[12:15]\n\t"
        "v_max3_u32 v125, v66, v67, v125\n\t"
        "v_mfma_f32_16x16x32_bf16 v[16:19], a[128:131], v[64:67], v[16:19]\n\t"
        "buffer_load_dwordx4 v[48:51], v117, %[sw], %[t0] offen offset:2048 sc1\n\t"
        "s_waitcnt vmcnt(11) lgkmcnt(2)\n\t"
        "v_mfma_f32_16x16x32_bf16 v[20:23], v[96:99], v[68:71], v[20:23]\n\t"
        "ds_read_b128 v[96:99], v116 offset:14336\n\t"
        "v_mfma_f32_16x16x32_bf16 v[8:11], a[132:135], v[68:71], v[8:11]\n\t"
        "v_max3_u32 v125, v68, v69, v125\n\t"
        "v_mfma_f32_16x16x32_bf16 v[12:15], a[136:139], v[68:71], v[12:15]\n\t"
        "v_max3_u32 v125, v70, v71, v125\n\t"
        "v_mfma_f32_16x16x32_bf16 v[16:19], a[140:143], v[68:71], v[16:19]\n\t"
        "buffer_load_dwordx4 v[52:55], v117, %[sw], %[t0] offen offset:3072 sc1\n\t"
        "s_waitcnt vmcnt(11) lgkmcnt(2)\n\t"
        "v_mfma_f32_16x16x32_bf16 v[20:23], v[88:91], v[72:75], v[20:23]\n\t"
        "ds_read_b128 v[88:91], v116 offset:15360\n\t"
        "v_mfma_f32_16x16x32_bf16 v[8:11], a[144:147], v[72:75], v[8:11]\n\t"
        "v_max3_u32 v125, v72, v73, v125\n\t"
        "v_mfma_f32_16x16x32_bf16 v[12:15], a[148:151], v[72:75], v[12:15]\n\t"
        "v_max3_u32 v125, v74, v75, v125\n\t"
        "v_mfma_f32_16x16x32_bf16 v[16:19], a[152:155], v[72:75], v[16:19]\n\t"
        "s_add_u32 %[t0], %[ro], 6\n\t"
        "s_and_b32 %[t0], %[t0], 7\n\t"
        "s_lshl_b32 %[t0], %[t0], 12\n\t"
        "s_add_u32 %[t0], %[t0], %[nx]\n\t"
        "buffer_load_dwordx4 v[56:59], v117, %[sw], %[t0] offen offset:0 sc1\n\t"
        "s_waitcnt vmcnt(11) lgkmcnt(2)\n\t"
        "v_mfma_f32_16x16x32_bf16 v[20:23], v[92:95], v[76:79], v[20:23]\n\t"
        "ds_read_b128 v[92:95], v116 offset:16384\n\t"
        "v_mfma_f32_16x16x32_bf16 v[8:11], a[156:159], v[76:79], v[8:11]\n\t"
        "v_max3_u32 v125, v76, v77, v125\n\t"
        "v_mfma_f32_16x16x32_bf16 v[12:15], a[160:163], v[76:79], v[12:15]\n\t"
        "v_max3_u32 v125, v78, v79, v125\n\t"
        "v_mfma_f32_16x16x32_bf16 v[16:19], a[164:167], v[76:79], v[16:19]\n\t"
        "buffer_load_dwordx4 v[60:63], v117, %[sw], %[t0] offen offset:1024 sc1\n\t"
        "s_waitcnt vmcnt(11) lgkmcnt(2)\n\t"
        "v_mfma_f32_16x16x32_bf16 v[20:23], v[96:99], v[80:83], v[20:23]\n\t"
        "ds_read_b128 v[96:99], v116 offset:17408\n\t"
        "v_mfma_f32_16x16x32_bf16 v[8:11], a[168:171], v[80:83], v[8:11]\n\t"
        "v_max3_u32 v125, v80, v81, v125\n\t"
        "v_mfma_f32_16x16x32_bf16 v[12:15], a[172:175], v[80:83], v[12:15]\n\t"
        "v_max3_u32 v125, v82, v83, v125\n\t"
        "v_mfma_f32_16x16x32_bf16 v[16:19], a[176:179], v[80:83], v[16:19]\n\t"
        "buffer_load_dwordx4 v[64:67], v117, %[sw], %[t0] offen offset:2048 sc1\n\t"
        "s_waitcnt vmcnt(11) lgkmcnt(2)\n\t"
        "v_mfma_f32_16x16x32_bf16 v[20:23], v[88:91], v[84:87], v[20:23]\n\t"
        "ds_read_b128 v[88:91], v116 offset:18432\n\t"
        "v_mfma_f32_16x16x32_bf16 v[8:11], a[180:183], v[84:87], v[8:11]\n\t"
        "v_max3_u32 v125, v84, v85, v125\n\t"
        "v_mfma_f32_16x16x32_bf16 v[12:15], a[184:187], v[84:87], v[12:15]\n\t"
        "v_max3_u32 v125, v86, v87, v125\n\t"
        "v_mfma_f32_16x16x32_bf16 v[16:19], a[188:191], v[84:87], v[16:19]\n\t"
        "buffer_load_dwordx4 v[68:71], v117, %[sw], %[t0] offen offset:3072 sc1\n\t"
        "s_waitcnt vmcnt(11) lgkmcnt(2)\n\t"
        "v_mfma_f32_16x16x32_bf16 v[20:23], v[92:95], v[24:27], v[20:23]\n\t"
        "ds_read_b128 v[92:95], v116 offset:19456\n\t"
        "v_mfma_f32_16x16x32_bf16 v[8:11], a[192:195], v[24:27], v[8:11]\n\t"
        "v_max3_u32 v125, v24, v25, v125\n\t"
        "v_mfma_f32_16x16x32_bf16 v[12:15], a[196:199], v[24:27], v[12:15]\n\t"
        "v_max3_u32 v125, v26, v27, v125\n\t"
        "v_mfma_f32_16x16x32_bf16 v[16:19], a[200:203], v[24:27], v[16:19]\n\t"
        "s_add_u32 %[t0], %[ro], 7\n\t"
        "s_and_b32 %[t0], %[t0], 7\n\t"
        "s_lshl_b32 %[t0], %[t0], 12\n\t"
        "s_add_u32 %[t0], %[t0], %[nx]\n\t"
        "buffer_load_dwordx4 v[72:75], v117, %[sw], %[t0] offen offset:0 sc1\n\t"
        "s_waitcnt vmcnt(11) lgkmcnt(2)\n\t"
        "v_mfma_f32_16x16x32_bf16 v[20:23], v[96:99], v[28:31], v[20:23]\n\t"
        "ds_read_b128 v[96:99], v116 offset:20480\n\t"
        "v_mfma_f32_16x16x32_bf16 v[8:11], a[204:207], v[28:31], v[8:11]\n\t"
        "v_max3_u32 v125, v28, v29, v125\n\t"
        "v_mfma_f32_16x16x32_bf16 v[12:15], a[208:211], v[28:31], v[12:15]\n\t"
        "v_max3_u32 v125, v30, v31, v125\n\t"
        "v_sub_f32_e32 v104, 1.0, v102\n\t"
        "v_mfma_f32_16x16x32_bf16 v[16:19], a[212:215], v[28:31], v[16:19]\n\t"
        "buffer_load_dwordx4 v[76:79], v117, %[sw], %[t0] offen offset:1024 sc1\n\t"
        "s_waitcnt vmcnt(11) lgkmcnt(2)\n\t"
        "v_mfma_f32_16x16x32_bf16 v[20:23], v[88:91], v[32:35], v[20:23]\n\t"
        "ds_read_b128 v[88:91], v116 offset:21504\n\t"
        "v_mfma_f32_16x16x32_bf16 v[8:11], a[216:219], v[32:35], v[8:11]\n\t"
        "v_max3_u32 v125, v32, v33, v125\n\t"
        "v_mfma_f32_16x16x32_bf16 v[12:15], a[220:223], v[32:35], v[12:15]\n\t"
        "v_max3_u32 v125, v34, v35, v125\n\t"
        "v_cvt_pk_bf16_f32 v104, 0, v104\n\t"
        "v_mfma_f32_16x16x32_bf16 v[16:19], a[224:227], v[32:35], v[16:19]\n\t"
        "buffer_load_dwordx4 v[80:83], v117, %[sw], %[t0] offen offset:2048 sc1\n\t"
        "s_waitcnt vmcnt(11) lgkmcnt(2)\n\t"
        "v_mfma_f32_16x16x32_bf16 v[20:23], v[92:95], v[36:39], v[20:23]\n\t"
        "ds_read_b128 v[92:95], v116 offset:22528\n\t"
        "v_mfma_f32_16x16x32_bf16 v[8:11], a[228:231], v[36:39], v[8:11]\n\t"
        "v_max3_u32 v125, v36, v37, v125\n\t"
        "v_mfma_f32_16x16x32_bf16 v[12:15], a[232:235], v[36:39], v[12:15]\n\t"
        "v_max3_u32 v125, v38, v39, v125\n\t"
        "v_lshlrev_b32_e32 v106, 16, v100\n\t"
        "v_mfma_f32_16x16x32_bf16 v[16:19], a[236:239], v[36:39], v[16:19]\n\t"
        "buffer_load_dwordx4 v[84:87], v117, %[sw], %[t0] offen offset:3072 sc1\n\t"
        "s_waitcnt vmcnt(11) lgkmcnt(2)\n\t"
        "v_mfma_f32_16x16x32_bf16 v[20:23], v[96:99], v[40:43], v[20:23]\n\t"
        "ds_read_b128 v[96:99], v116 offset:23552\n\t"
        "v_mfma_f32_16x16x32_bf16 v[8:11], a[240:243], v[40:43], v[8:11]\n\t"
        "v_max3_u32 v125, v40, v41, v125\n\t"
        "v_mfma_f32_16x16x32_bf16 v[12:15], a[244:247], v[40:43], v[12:15]\n\t"
        "v_max3_u32 v125, v42, v43, v125\n\t"
        "v_mul_f32_e32 v106, v102, v106\n\t"
        "v_mfma_f32_16x16x32_bf16 v[16:19], a[248:251], v[40:43], v[16:19]\n\t"
        "s_waitcnt vmcnt(10) lgkmcnt(2)\n\t"
        "v_mfma_f32_16x16x32_bf16 v[20:23], v[88:91], v[44:47], v[20:23]\n\t"
        "ds_read_b128 v[88:91], v116 offset:24576\n\t"
        "v_mfma_f32_16x16x32_bf16 v[8:11], a[252:255], v[44:47], v[8:11]\n\t"
        "v_max3_u32 v125, v44, v45, v125\n\t"
        "v_mfma_f32_16x16x32_bf16 v[12:15], v[128:131], v[44:47], v[12:15]\n\t"
        "v_max3_u32 v125, v46, v47, v125\n\t"
        "v_cvt_pk_bf16_f32 v106, 0, v106\n\t"
        "v_mfma_f32_16x16x32_bf16 v[16:19], v[132:135], v[44:47], v[16:19]\n\t"
        "s_waitcnt vmcnt(9) lgkmcnt(2)\n\t"
        "v_mfma_f32_16x16x32_bf16 v[20:23], v[92:95], v[48:51], v[20:23]\n\t"
        "ds_read_b128 v[92:95], v116 offset:25600\n\t"
        "v_mfma_f32_16x16x32_bf16 v[8:11], v[136:139], v[48:51], v[8:11]\n\t"
        "v_max3_u32 v125, v48, v49, v125\n\t"
        "v_mfma_f32_16x16x32_bf16 v[12:15], v[140:143], v[48:51], v[12:15]\n\t"
        "v_max3_u32 v125, v50, v51, v125\n\t"
        "v_and_b32_e32 v107, 0xffff0000, v100\n\t"
        "v_mfma_f32_16x16x32_bf16 v[16:19], v[144:147], v[48:51], v[16:19]\n\t"
        "s_waitcnt vmcnt(8) lgkmcnt(2)\n\t"
        "v_mfma_f32_16x16x32_bf16 v[20:23], v[96:99], v[52:55], v[20:23]\n\t"
        "ds_read_b128 v[96:99], v116 offset:26624\n\t"
        "v_mfma_f32_16x16x32_bf16 v[8:11], v[148:151], v[52:55], v[8:11]\n\t"
        "v_max3_u32 v125, v52, v53, v125\n\t"
        "v_mfma_f32_16x16x32_bf16 v[12:15], v[152:155], v[52:55], v[12:15]\n\t"
        "v_max3_u32 v125, v54, v55, v125\n\t"
        "v_mul_f32_e32 v107, v102, v107\n\t"
        "v_mfma_f32_16x16x32_bf16 v[16:19], v[156:159], v[52:55], v[16:19]\n\t"
        "s_waitcnt vmcnt(7) lgkmcnt(2)\n\t"
        "v_mfma_f32_16x16x32_bf16 v[20:23], v[88:91], v[56:59], v[20:23]\n\t"
        "ds_read_b128 v[88:91], v116 offset:27648\n\t"
        "v_mfma_f32_16x16x32_bf16 v[8:11], v[160:163], v[56:59], v[8:11]\n\t"
        "v_max3_u32 v125, v56, v57, v125\n\t"
        "v_mfma_f32_16x16x32_bf16 v[12:15], v[164:167], v[56:59], v[12:15]\n\t"
        "v_max3_u32 v125, v58, v59, v125\n\t"
        "v_cvt_pk_bf16_f32 v107, 0, v107\n\t"
        "v_mfma_f32_16x16x32_bf16 v[16:19], v[168:171], v[56:59], v[16:19]\n\t"
        "s_waitcnt vmcnt(6) lgkmcnt(2)\n\t"
        "v_mfma_f32_16x16x32_bf16 v[20:23], v[92:95], v[60:63], v[20:23]\n\t"
        "ds_read_b128 v[92:95], v116 offset:28672\n\t"
        "v_mfma_f32_16x16x32_bf16 v[8:11], v[172:175], v[60:63], v[8:11]\n\t"
        "v_max3_u32 v125, v60, v61, v125\n\t"
        "v_mfma_f32_16x16x32_bf16 v[12:15], v[176:179], v[60:63], v[12:15]\n\t"
        "v_max3_u32 v125, v62, v63, v125\n\t"
        "v_lshlrev_b32_e32 v108, 16, v101\n\t"
        "v_mfma_f32_16x16x32_bf16 v[16:19], v[180:183], v[60:63], v[16:19]\n\t"
        "s_waitcnt vmcnt(5) lgkmcnt(2)\n\t"
        "v_mfma_f32_16x16x32_bf16 v[20:23], v[96:99], v[64:67], v[20:23]\n\t"
        "ds_read_b128 v[96:99], v116 offset:29696\n\t"
        "v_mfma_f32_16x16x32_bf16 v[8:11], v[184:187], v[64:67], v[8:11]\n\t"
        "v_max3_u32 v125, v64, v65, v125\n\t"
        "v_mfma_f32_16x16x32_bf16 v[12:15], v[188:191], v[64:67], v[12:15]\n\t"
        "v_max3_u32 v125, v66, v67, v125\n\t"
        "v_mul_f32_e32 v108, v102, v108\n\t"
        "v_mfma_f32_16x16x32_bf16 v[16:19], v[192:195], v[64:67], v[16:19]\n\t"
        "s_waitcnt vmcnt(4) lgkmcnt(2)\n\t"
        "v_mfma_f32_16x16x32_bf16 v[20:23], v[88:91], v[68:71], v[20:23]\n\t"
        "ds_read_b128 v[88:91], v116 offset:30720\n\t"
        "v_mfma_f32_16x16x32_bf16 v[8:11], v[196:199], v[68:71], v[8:11]\n\t"
        "v_max3_u32 v125, v68, v69, v125\n\t"
        "v_mfma_f32_16x16x32_bf16 v[12:15], v[200:203], v[68:71], v[12:15]\n\t"
        "v_max3_u32 v125, v70, v71, v125\n\t"
        "v_cvt_pk_bf16_f32 v108, 0, v108\n\t"
        "v_mfma_f32_16x16x32_bf16 v[16:19], v[204:207], v[68:71], v[16:19]\n\t"
        "s_waitcnt vmcnt(3) lgkmcnt(2)\n\t"
        "v_mfma_f32_16x16x32_bf16 v[20:23], v[92:95], v[72:75], v[20:23]\n\t"
        "ds_read_b128 v[92:95], v116 offset:31744\n\t"
        "v_mfma_f32_16x16x32_bf16 v[8:11], v[208:211], v[72:75], v[8:11]\n\t"
        "v_max3_u32 v125, v72, v73, v125\n\t"
        "v_mfma_f32_16x16x32_bf16 v[12:15], v[212:215], v[72:75], v[12:15]\n\t"
        "v_max3_u32 v125, v74, v75, v125\n\t"
        "v_and_b32_e32 v109, 0xffff0000, v101\n\t"
        "v_mfma_f32_16x16x32_bf16 v[16:19], v[216:219], v[72:75], v[16:19]\n\t"
        "s_waitcnt vmcnt(2) lgkmcnt(2)\n\t"
        "v_mfma_f32_16x16x32_bf16 v[20:23], v[96:99], v[76:79], v[20:23]\n\t"
        "v_mfma_f32_16x16x32_bf16 v[8:11], v[220:223], v[76:79], v[8:11]\n\t"
        "v_max3_u32 v125, v76, v77, v125\n\t"
        "v_mfma_f32_16x16x32_bf16 v[12:15], v[224:227], v[76:79], v[12:15]\n\t"
        "v_max3_u32 v125, v78, v79, v125\n\t"
        "v_mul_f32_e32 v109, v102, v109\n\t"
        "v_mfma_f32_16x16x32_bf16 v[16:19], v[228:231], v[76:79], v[16:19]\n\t"
        "s_waitcnt vmcnt(1) lgkmcnt(1)\n\t"
        "v_mfma_f32_16x16x32_bf16 v[20:23], v[88:91], v[80:83], v[20:23]\n\t"
        "ds_read_b128 v[88:91], v116 offset:0\n\t"
        "v_mfma_f32_16x16x32_bf16 v[8:11], v[232:235], v[80:83], v[8:11]\n\t"
        "v_max3_u32 v125, v80, v81, v125\n\t"
        "v_mfma_f32_16x16x32_bf16 v[12:15], v[236:239], v[80:83], v[12:15]\n\t"
        "v_max3_u32 v125, v82, v83, v125\n\t"
        "v_cvt_pk_bf16_f32 v109, 0, v109\n\t"
        "v_mfma_f32_16x16x32_bf16 v[16:19], v[240:243], v[80:83], v[16:19]\n\t"
        "s_waitcnt vmcnt(0) lgkmcnt(1)\n\t"
        "v_mfma_f32_16x16x32_bf16 v[20:23], v[92:95], v[84:87], v[20:23]\n\t"
        "ds_read_b128 v[92:95], v116 offset:1024\n\t"
        "v_mfma_f32_16x16x32_bf16 v[8:11], v[244:247], v[84:87], v[8:11]\n\t"
        "ds_read_b128 v[96:99], v116 offset:2048\n\t"
        "v_max3_u32 v125, v84, v85, v125\n\t"
        "v_mfma_f32_16x16x32_bf16 v[12:15], v[248:251], v[84:87], v[12:15]\n\t"
        "v_max3_u32 v125, v86, v87, v125\n\t"
        "v_mfma_f32_16x16x32_bf16 v[16:19], v[252:255], v[84:87], v[16:19]\n\t"
        "v_cmp_eq_u32_e32 vcc, -1, v125\n\t"
        "s_mov_b32 %[nx], 0xf0000000\n\t"
        "s_mov_b32 %[re], 64\n\t"
        "s_cbranch_vccz LP_epi%=\n\t"
        "s_waitcnt lgkmcnt(0)\n\t"
        "s_branch LP_again%=\n\t"
        "LP_run%=:\n\t"
        "s_add_u32 %[t0], %[ro], 0\n\t"
        "s_and_b32 %[t0], %[t0], 7\n\t"
        "s_lshl_b32 %[t0], %[t0], 12\n\t"
        "s_add_u32 %[t0], %[t0], %[nx]\n\t"
        "buffer_load_dwordx4 v[24:27], v117, %[sw], %[t0] offen offset:0 sc1\n\t"
        "buffer_load_dwordx4 v[28:31], v117, %[sw], %[t0] offen offset:1024 sc1\n\t"
        "buffer_load_dwordx4 v[32:35], v117, %[sw], %[t0] offen offset:2048 sc1\n\t"
        "buffer_load_dwordx4 v[36:39], v117, %[sw], %[t0] offen offset:3072 sc1\n\t"
        "s_add_u32 %[t0], %[ro], 1\n\t"
        "s_and_b32 %[t0], %[t0], 7\n\t"
        "s_lshl_b32 %[t0], %[t0], 12\n\t"
        "s_add_u32 %[t0], %[t0], %[nx]\n\t"
        "buffer_load_dwordx4 v[40:43], v117, %[sw], %[t0] offen offset:0 sc1\n\t"
        "buffer_load_dwordx4 v[44:47], v117, %[sw], %[t0] offen offset:1024 sc1\n\t"
        "buffer_load_dwordx4 v[48:51], v117, %[sw], %[t0] offen offset:2048 sc1\n\t"
        "buffer_load_dwordx4 v[52:55], v117, %[sw], %[t0] offen offset:3072 sc1\n\t"
        "s_add_u32 %[t0], %[ro], 2\n\t"
        "s_and_b32 %[t0], %[t0], 7\n\t"
        "s_lshl_b32 %[t0], %[t0], 12\n\t"
        "s_add_u32 %[t0], %[t0], %[nx]\n\t"
        "buffer_load_dwordx4 v[56:59], v117, %[sw], %[t0] offen offset:0 sc1\n\t"
        "buffer_load_dwordx4 v[60:63], v117, %[sw], %[t0] offen offset:1024 sc1\n\t"
        "buffer_load_dwordx4 v[64:67], v117, %[sw], %[t0] offen offset:2048 sc1\n\t"
        "buffer_load_dwordx4 v[68:71], v117, %[sw], %[t0] offen offset:3072 sc1\n\t"
        "s_add_u32 %[t0], %[ro], 3\n\t"
        "s_and_b32 %[t0], %[t0], 7\n\t"
        "s_lshl_b32 %[t0], %[t0], 12\n\t"
        "s_add_u32 %[t0], %[t0], %[nx]\n\t"
        "buffer_load_dwordx4 v[72:75], v117, %[sw], %[t0] offen offset:0 sc1\n\t"
        "buffer_load_dwordx4 v[76:79], v117, %[sw], %[t0] offen offset:1024 sc1\n\t"
        "buffer_load_dwordx4 v[80:83], v117, %[sw], %[t0] offen offset:2048 sc1\n\t"
        "buffer_load_dwordx4 v[84:87], v117, %[sw], %[t0] offen offset:3072 sc1\n\t"
        "buffer_load_dword v103, v118, %[sw], 0 offen sc1\n\t"
        "LP_tile%=:\n\t"
        "s_and_b32 %[sp], %[tl], 1\n\t"
        "s_lshl_b32 %[sp], %[sp], 14\n\t"
        "s_lshr_b32 %[t1], %[tl], 1\n\t"
        "s_lshl_b32 %[t1], %[t1], 6\n\t"
        "s_add_u32 %[t1], %[sp], %[t1]\n\t"
        "buffer_load_dword v100, v119, %[sw], %[t1] offen sc1\n\t"
        "s_add_u32 %[t1], %[t1], 32768\n\t"
        "buffer_load_dword v101, v119, %[sw], %[t1] offen sc1\n\t"
        "s_lshl_b32 %[t1], %[tl], 9\n\t"
        "buffer_load_dword v102, v120, %[sw], %[t1] offen sc1\n\t"
        "buffer_load_dword v103, v118, %[sw], 0 offen sc1\n\t"
        "s_mov_b32 %[cu], %[nx]\n\t"
        "s_waitcnt vmcnt(20) lgkmcnt(2)\n\t"
        "v_mfma_f32_16x16x32_bf16 v[20:23], v[88:91], v[24:27], 0\n\t"
        "ds_read_b128 v[88:91], v116 offset:3072\n\t"
        "v_mfma_f32_16x16x32_bf16 v[8:11], a[0:3], v[24:27], 0\n\t"
        "v_mfma_f32_16x16x32_bf16 v[12:15], a[4:7], v[24:27], 0\n\t"
        "v_mfma_f32_16x16x32_bf16 v[16:19], a[8:11], v[24:27], 0\n\t"
        "s_add_u32 %[t0], %[ro], 4\n\t"
        "s_and_b32 %[t0], %[t0], 7\n\t"
        "s_lshl_b32 %[t0], %[t0], 12\n\t"
        "s_add_u32 %[t0], %[t0], %[cu]\n\t"
        "buffer_load_dwordx4 v[24:27], v117, %[sw], %[t0] offen offset:0 sc1\n\t"
        "s_waitcnt vmcnt(20) lgkmcnt(2)\n\t"
        "v_mfma_f32_16x16x32_bf16 v[20:23], v[92:95], v[28:31], v[20:23]\n\t"
        "ds_read_b128 v[92:95], v116 offset:4096\n\t"
        "v_mfma_f32_16x16x32_bf16 v[8:11], a[12:15], v[28:31], v[8:11]\n\t"
        "v_mfma_f32_16x16x32_bf16 v[12:15], a[16:19], v[28:31], v[12:15]\n\t"
        "v_mfma_f32_16x16x32_bf16 v[16:19], a[20:23], v[28:31], v[16:19]\n\t"
        "buffer_load_dwordx4 v[28:31], v117, %[sw], %[t0] offen offset:1024 sc1\n\t"
        "s_waitcnt vmcnt(20) lgkmcnt(2)\n\t"
        "v_mfma_f32_16x16x32_bf16 v[20:23], v[96:99], v[32:35], v[20:23]\n\t"
        "ds_read_b128 v[96:99], v116 offset:5120\n\t"
        "v_mfma_f32_16x16x32_bf16 v[8:11], a[24:27], v[32:35], v[8:11]\n\t"
        "v_mfma_f32_16x16x32_bf16 v[12:15], a[28:31], v[32:35], v[12:15]\n\t"
        "v_mfma_f32_16x16x32_bf16 v[16:19], a[32:35], v[32:35], v[16:19]\n\t"
        "buffer_load_dwordx4 v[32:35], v117, %[sw], %[t0] offen offset:2048 sc1\n\t"
        "s_waitcnt vmcnt(20) lgkmcnt(2)\n\t"
        "v_mfma_f32_16x16x32_bf16 v[20:23], v[88:91], v[36:39], v[20:23]\n\t"
        "ds_read_b128 v[88:91], v116 offset:6144\n\t"
        "v_mfma_f32_16x16x32_bf16 v[8:11], a[36:39], v[36:39], v[8:11]\n\t"
        "v_mfma_f32_16x16x32_bf16 v[12:15], a[40:43], v[36:39], v[12:15]\n\t"
        "v_mfma_f32_16x16x32_bf16 v[16:19], a[44:47], v[36:39], v[16:19]\n\t"
        "buffer_load_dwordx4 v[36:39], v117, %[sw], %[t0] offen offset:3072 sc1\n\t"
        "s_waitcnt vmcnt(20) lgkmcnt(2)\n\t"
        "v_mfma_f32_16x16x32_bf16 v[20:23], v[92:95], v[40:43], v[20:23]\n\t"
        "ds_read_b128 v[92:95], v116 offset:7168\n\t"
        "v_mfma_f32_16x16x32_bf16 v[8:11], a[48:51], v[40:43], v[8:11]\n\t"
        "v_mfma_f32_16x16x32_bf16 v[12:15], a[52:55], v[40:43], v[12:15]\n\t"
        "v_mfma_f32_16x16x32_bf16 v[16:19], a[56:59], v[40:43], v[16:19]\n\t"
        "s_add_u32 %[t0], %[ro], 5\n\t"
        "s_and_b32 %[t0], %[t0], 7\n\t"
        "s_lshl_b32 %[t0], %[t0], 12\n\t"
        "s_add_u32 %[t0], %[t0], %[cu]\n\t"
        "buffer_load_dwordx4 v[40:43], v117, %[sw], %[t0] offen offset:0 sc1\n\t"
        "s_waitcnt vmcnt(20) lgkmcnt(2)\n\t"
        "v_mfma_f32_16x16x32_bf16 v[20:23], v[96:99], v[44:47], v[20:23]\n\t"
        "ds_read_b128 v[96:99], v116 offset:8192\n\t"
        "v_mfma_f32_16x16x32_bf16 v[8:11], a[60:63], v[44:47], v[8:11]\n\t"
        "v_mfma_f32_16x16x32_bf16 v[12:15], a[64:67], v[44:47], v[12:15]\n\t"
        "v_mfma_f32_16x16x32_bf16 v[16:19], a[68:71], v[44:47], v[16:19]\n\t"
        "buffer_load_dwordx4 v[44:47], v117, %[sw], %[t0] offen offset:1024 sc1\n\t"
        "s_waitcnt vmcnt(20) lgkmcnt(2)\n\t"
        "v_mfma_f32_16x16x32_bf16 v[20:23], v[88:91], v[48:51], v[20:23]\n\t"
        "ds_read_b128 v[88:91], v116 offset:9216\n\t"
        "v_mfma_f32_16x16x32_bf16 v[8:11], a[72:75], v[48:51], v[8:11]\n\t"
        "v_mfma_f32_16x16x32_bf16 v[12:15], a[76:79], v[48:51], v[12:15]\n\t"
        "v_mfma_f32_16x16x32_bf16 v[16:19], a[80:83], v[48:51], v[16:19]\n\t"
        "buffer_load_dwordx4 v[48:51], v117, %[sw], %[t0] offen offset:2048 sc1\n\t"
        "s_waitcnt vmcnt(20) lgkmcnt(2)\n\t"
        "v_mfma_f32_16x16x32_bf16 v[20:23], v[92:95], v[52:55], v[20:23]\n\t"
        "ds_read_b128 v[92:95], v116 offset:10240\n\t"
        "v_mfma_f32_16x16x32_bf16 v[8:11], a[84:87], v[52:55], v[8:11]\n\t"
        "v_mfma_f32_16x16x32_bf16 v[12:15], a[88:91], v[52:55], v[12:15]\n\t"
        "v_mfma_f32_16x16x32_bf16 v[16:19], a[92:95], v[52:55], v[16:19]\n\t"
        "buffer_load_dwordx4 v[52:55], v117, %[sw], %[t0] offen offset:3072 sc1\n\t"
        "s_waitcnt vmcnt(20) lgkmcnt(2)\n\t"
        "s_barrier\n\t"
        "v_mfma_f32_16x16x32_bf16 v[20:23], v[96:99], v[56:59], v[20:23]\n\t"
        "ds_read_b128 v[96:99], v116 offset:11264\n\t"
        "v_mfma_f32_16x16x32_bf16 v[8:11], a[96:99], v[56:59], v[8:11]\n\t"
        "v_mfma_f32_16x16x32_bf16 v[12:15], a[100:103], v[56:59], v[12:15]\n\t"
        "v_mfma_f32_16x16x32_bf16 v[16:19], a[104:107], v[56:59], v[16:19]\n\t"
        "s_add_u32 %[t0], %[ro], 6\n\t"
        "s_and_b32 %[t0], %[t0], 7\n\t"
        "s_lshl_b32 %[t0], %[t0], 12\n\t"
        "s_add_u32 %[t0], %[t0], %[cu]\n\t"
        "buffer_load_dwordx4 v[56:59], v117, %[sw], %[t0] offen offset:0 sc1\n\t"
        "s_waitcnt vmcnt(20) lgkmcnt(2)\n\t"
        "v_mfma_f32_16x16x32_bf16 v[20:23], v[88:91], v[60:63], v[20:23]\n\t"
        "ds_read_b128 v[88:91], v116 offset:12288\n\t"
        "v_mfma_f32_16x16x32_bf16 v[8:11], a[108:111], v[60:63], v[8:11]\n\t"
        "v_mfma_f32_16x16x32_bf16 v[12:15], a[112:115], v[60:63], v[12:15]\n\t"
        "v_mfma_f32_16x16x32_bf16 v[16:19], a[116:119], v[60:63], v[16:19]\n\t"
        "buffer_load_dwordx4 v[60:63], v117, %[sw], %[t0] offen offset:1024 sc1\n\t"
        "s_waitcnt vmcnt(20) lgkmcnt(2)\n\t"
        "v_mfma_f32_16x16x32_bf16 v[20:23], v[92:95], v[64:67], v[20:23]\n\t"
        "ds_read_b128 v[92:95], v116 offset:13312\n\t"
        "v_mfma_f32_16x16x32_bf16 v[8:11], a[120:123], v[64:67], v[8:11]\n\t"
        "v_mfma_f32_16x16x32_bf16 v[12:15], a[124:127], v[64:67], v[12:15]\n\t"
        "v_mfma_f32_16x16x32_bf16 v[16:19], a[128:131], v[64:67], v[16:19]\n\t"
        "buffer_load_dwordx4 v[64:67], v117, %[sw], %[t0] offen offset:2048 sc1\n\t"
        "s_waitcnt vmcnt(20) lgkmcnt(2)\n\t"
        "v_mfma_f32_16x16x32_bf16 v[20:23], v[96:99], v[68:71], v[20:23]\n\t"
        "ds_read_b128 v[96:99], v116 offset:14336\n\t"
        "v_mfma_f32_16x16x32_bf16 v[8:11], a[132:135], v[68:71], v[8:11]\n\t"
        "v_mfma_f32_16x16x32_bf16 v[12:15], a[136:139], v[68:71], v[12:15]\n\t"
        "v_mfma_f32_16x16x32_bf16 v[16:19], a[140:143], v[68:71], v[16:19]\n\t"
        "buffer_load_dwordx4 v[68:71], v117, %[sw], %[t0] offen offset:3072 sc1\n\t"
        "s_waitcnt vmcnt(20) lgkmcnt(2)\n\t"
        "v_mfma_f32_16x16x32_bf16 v[20:23], v[88:91], v[72:75], v[20:23]\n\t"
        "ds_read_b128 v[88:91], v116 offset:15360\n\t"
        "v_mfma_f32_16x16x32_bf16 v[8:11], a[144:147], v[72:75], v[8:11]\n\t"
        "v_mfma_f32_16x16x32_bf16 v[12:15], a[148:151], v[72:75], v[12:15]\n\t"
        "v_mfma_f32_16x16x32_bf16 v[16:19], a[152:155], v[72:75], v[16:19]\n\t"
        "s_add_u32 %[t0], %[ro], 7\n\t"
        "s_and_b32 %[t0], %[t0], 7\n\t"
        "s_lshl_b32 %[t0], %[t0], 12\n\t"
        "s_add_u32 %[t0], %[t0], %[cu]\n\t"
        "buffer_load_dwordx4 v[72:75], v117, %[sw], %[t0] offen offset:0 sc1\n\t"
        "s_waitcnt vmcnt(13) lgkmcnt(2)\n\t"
        "v_mfma_f32_16x16x32_bf16 v[20:23], v[92:95], v[76:79], v[20:23]\n\t"
        "ds_read_b128 v[92:95], v116 offset:16384\n\t"
        "v_min_u32_dpp v103, v103, v103 quad_perm:[1,0,3,2] row_mask:0xf bank_mask:0xf\n\t"
        "s_nop 0\n\t"
        "v_mfma_f32_16x16x32_bf16 v[8:11], a[156:159], v[76:79], v[8:11]\n\t"
        "v_min_u32_dpp v103, v103, v103 quad_perm:[2,3,0,1] row_mask:0xf bank_mask:0xf\n\t"
        "s_nop 0\n\t"
        "v_mfma_f32_16x16x32_bf16 v[12:15], a[160:163], v[76:79], v[12:15]\n\t"
        "v_min_u32_dpp v103, v103, v103 row_ror:4 row_mask:0xf bank_mask:0xf\n\t"
        "s_nop 0\n\t"
        "v_mfma_f32_16x16x32_bf16 v[16:19], a[164:167], v[76:79], v[16:19]\n\t"
        "buffer_load_dwordx4 v[76:79], v117, %[sw], %[t0] offen offset:1024 sc1\n\t"
        "s_waitcnt vmcnt(20) lgkmcnt(2)\n\t"
        "v_mfma_f32_16x16x32_bf16 v[20:23], v[96:99], v[80:83], v[20:23]\n\t"
        "ds_read_b128 v[96:99], v116 offset:17408\n\t"
        "v_readfirstlane_b32 %[pg], v103\n\t"
        "s_add_u32 %[t1], %[tl], 8\n\t"
        "s_lshl_b32 %[nx], %[t1], 17\n\t"
        "s_lshr_b32 %[t1], %[t1], 1\n\t"
        "v_mfma_f32_16x16x32_bf16 v[8:11], a[168:171], v[80:83], v[8:11]\n\t"
        "s_cmp_gt_u32 %[pg], %[t1]\n\t"
        "s_cselect_b32 %[nx], %[nx], 0xf0000000\n\t"
        "v_mfma_f32_16x16x32_bf16 v[12:15], a[172:175], v[80:83], v[12:15]\n\t"
        "v_mfma_f32_16x16x32_bf16 v[16:19], a[176:179], v[80:83], v[16:19]\n\t"
        "buffer_load_dwordx4 v[80:83], v117, %[sw], %[t0] offen offset:2048 sc1\n\t"
        "s_waitcnt vmcnt(20) lgkmcnt(2)\n\t"
        "v_mfma_f32_16x16x32_bf16 v[20:23], v[88:91], v[84:87], v[20:23]\n\t"
        "ds_read_b128 v[88:91], v116 offset:18432\n\t"
        "v_mfma_f32_16x16x32_bf16 v[8:11], a[180:183], v[84:87], v[8:11]\n\t"
        "v_mfma_f32_16x16x32_bf16 v[12:15], a[184:187], v[84:87], v[12:15]\n\t"
        "v_mfma_f32_16x16x32_bf16 v[16:19], a[188:191], v[84:87], v[16:19]\n\t"
        "buffer_load_dwordx4 v[84:87], v117, %[sw], %[t0] offen offset:3072 sc1\n\t"
        "s_waitcnt vmcnt(15) lgkmcnt(2)\n\t"
        "v_mfma_f32_16x16x32_bf16 v[20:23], v[92:95], v[24:27], v[20:23]\n\t"
        "ds_read_b128 v[92:95], v116 offset:19456\n\t"
        "v_mfma_f32_16x16x32_bf16 v[8:11], a[192:195], v[24:27], v[8:11]\n\t"
        "v_mfma_f32_16x16x32_bf16 v[12:15], a[196:199], v[24:27], v[12:15]\n\t"
        "v_mfma_f32_16x16x32_bf16 v[16:19], a[200:203], v[24:27], v[16:19]\n\t"
        "s_add_u32 %[t0], %[ro], 0\n\t"
        "s_and_b32 %[t0], %[t0], 7\n\t"
        "s_lshl_b32 %[t0], %[t0], 12\n\t"
        "s_add_u32 %[t0], %[t0], %[nx]\n\t"
        "buffer_load_dwordx4 v[24:27], v117, %[sw], %[t0] offen offset:0 sc1\n\t"
        "s_waitcnt vmcnt(15) lgkmcnt(2)\n\t"
        "v_mfma_f32_16x16x32_bf16 v[20:23], v[96:99], v[28:31], v[20:23]\n\t"
        "ds_read_b128 v[96:99], v116 offset:20480\n\t"
        "v_mfma_f32_16x16x32_bf16 v[8:11], a[204:207], v[28:31], v[8:11]\n\t"
        "v_mfma_f32_16x16x32_bf16 v[12:15], a[208:211], v[28:31], v[12:15]\n\t"
        "v_sub_f32_e32 v104, 1.0, v102\n\t"
        "v_mfma_f32_16x16x32_bf16 v[16:19], a[212:215], v[28:31], v[16:19]\n\t"
        "buffer_load_dwordx4 v[28:31], v117, %[sw], %[t0] offen offset:1024 sc1\n\t"
        "s_waitcnt vmcnt(15) lgkmcnt(2)\n\t"
        "v_mfma_f32_16x16x32_bf16 v[20:23], v[88:91], v[32:35], v[20:23]\n\t"
        "ds_read_b128 v[88:91], v116 offset:21504\n\t"
        "v_mfma_f32_16x16x32_bf16 v[8:11], a[216:219], v[32:35], v[8:11]\n\t"
        "v_mfma_f32_16x16x32_bf16 v[12:15], a[220:223], v[32:35], v[12:15]\n\t"
        "v_cvt_pk_bf16_f32 v104, 0, v104\n\t"
        "v_mfma_f32_16x16x32_bf16 v[16:19], a[224:227], v[32:35], v[16:19]\n\t"
        "buffer_load_dwordx4 v[32:35], v117, %[sw], %[t0] offen offset:2048 sc1\n\t"
        "s_waitcnt vmcnt(15) lgkmcnt(2)\n\t"
        "v_mfma_f32_16x16x32_bf16 v[20:23], v[92:95], v[36:39], v[20:23]\n\t"
        "ds_read_b128 v[92:95], v116 offset:22528\n\t"
        "v_mfma_f32_16x16x32_bf16 v[8:11], a[228:231], v[36:39], v[8:11]\n\t"
        "v_mfma_f32_16x16x32_bf16 v[12:15], a[232:235], v[36:39], v[12:15]\n\t"
        "v_lshlrev_b32_e32 v106, 16, v100\n\t"
        "v_mfma_f32_16x16x32_bf16 v[16:19], a[236:239], v[36:39], v[16:19]\n\t"
        "buffer_load_dwordx4 v[36:39], v117, %[sw], %[t0] offen offset:3072 sc1\n\t"
        "s_waitcnt vmcnt(15) lgkmcnt(2)\n\t"
        "v_mfma_f32_16x16x32_bf16 v[20:23], v[96:99], v[40:43], v[20:23]\n\t"
        "ds_read_b128 v[96:99], v116 offset:23552\n\t"
        "v_mfma_f32_16x16x32_bf16 v[8:11], a[240:243], v[40:43], v[8:11]\n\t"
        "v_mfma_f32_16x16x32_bf16 v[12:15], a[244:247], v[40:43], v[12:15]\n\t"
        "v_mul_f32_e32 v106, v102, v106\n\t"
        "v_mfma_f32_16x16x32_bf16 v[16:19], a[248:251], v[40:43], v[16:19]\n\t"
        "s_add_u32 %[t0], %[ro], 1\n\t"
        "s_and_b32 %[t0], %[t0], 7\n\t"
        "s_lshl_b32 %[t0], %[t0], 12\n\t"
        "s_add_u32 %[t0], %[t0], %[nx]\n\t"
        "buffer_load_dwordx4 v[40:43], v117, %[sw], %[t0] offen offset:0 sc1\n\t"
        "s_waitcnt vmcnt(15) lgkmcnt(2)\n\t"
        "v_mfma_f32_16x16x32_bf16 v[20:23], v[88:91], v[44:47], v[20:23]\n\t"
        "ds_read_b128 v[88:91], v116 offset:24576\n\t"
        "v_mfma_f32_16x16x32_bf16 v[8:11], a[252:255], v[44:47], v[8:11]\n\t"
        "v_mfma_f32_16x16x32_bf16 v[12:15], v[128:131], v[44:47], v[12:15]\n\t"
        "v_cvt_pk_bf16_f32 v106, 0, v106\n\t"
        "v_mfma_f32_16x16x32_bf16 v[16:19], v[132:135], v[44:47], v[16:19]\n\t"
        "buffer_load_dwordx4 v[44:47], v117, %[sw], %[t0] offen offset:1024 sc1\n\t"
        "s_waitcnt vmcnt(15) lgkmcnt(2)\n\t"
        "v_mfma_f32_16x16x32_bf16 v[20:23], v[92:95], v[48:51], v[20:23]\n\t"
        "ds_read_b128 v[92:95], v116 offset:25600\n\t"
        "v_mfma_f32_16x16x32_bf16 v[8:11], v[136:139], v[48:51], v[8:11]\n\t"
        "v_mfma_f32_16x16x32_bf16 v[12:15], v[140:143], v[48:51], v[12:15]\n\t"
        "v_and_b32_e32 v107, 0xffff0000, v100\n\t"
        "v_mfma_f32_16x16x32_bf16 v[16:19], v[144:147], v[48:51], v[16:19]\n\t"
        "buffer_load_dwordx4 v[48:51], v117, %[sw], %[t0] offen offset:2048 sc1\n\t"
        "s_waitcnt vmcnt(15) lgkmcnt(2)\n\t"
        "v_mfma_f32_16x16x32_bf16 v[20:23], v[96:99], v[52:55], v[20:23]\n\t"
        "ds_read_b128 v[96:99], v116 offset:26624\n\t"
        "v_mfma_f32_16x16x32_bf16 v[8:11], v[148:151], v[52:55], v[8:11]\n\t"
        "v_mfma_f32_16x16x32_bf16 v[12:15], v[152:155], v[52:55], v[12:15]\n\t"
        "v_mul_f32_e32 v107, v102, v107\n\t"
        "v_mfma_f32_16x16x32_bf16 v[16:19], v[156:159], v[52:55], v[16:19]\n\t"
        "buffer_load_dwordx4 v[52:55], v117, %[sw], %[t0] offen offset:3072 sc1\n\t"
        "s_waitcnt vmcnt(15) lgkmcnt(2)\n\t"
        "v_mfma_f32_16x16x32_bf16 v[20:23], v[88:91], v[56:59], v[20:23]\n\t"
        "ds_read_b128 v[88:91], v116 offset:27648\n\t"
        "v_mfma_f32_16x16x32_bf16 v[8:11], v[160:163], v[56:59], v[8:11]\n\t"
        "v_mfma_f32_16x16x32_bf16 v[12:15], v[164:167], v[56:59], v[12:15]\n\t"
        "v_cvt_pk_bf16_f32 v107, 0, v107\n\t"
        "v_mfma_f32_16x16x32_bf16 v[16:19], v[168:171], v[56:59], v[16:19]\n\t"
        "s_add_u32 %[t0], %[ro], 2\n\t"
        "s_and_b32 %[t0], %[t0], 7\n\t"
        "s_lshl_b32 %[t0], %[t0], 12\n\t"
        "s_add_u32 %[t0], %[t0], %[nx]\n\t"
        "buffer_load_dwordx4 v[56:59], v117, %[sw], %[t0] offen offset:0 sc1\n\t"
        "s_waitcnt vmcnt(15) lgkmcnt(2)\n\t"
        "v_mfma_f32_16x16x32_bf16 v[20:23], v[92:95], v[60:63], v[20:23]\n\t"
        "ds_read_b128 v[92:95], v116 offset:28672\n\t"
        "v_mfma_f32_16x16x32_bf16 v[8:11], v[172:175], v[60:63], v[8:11]\n\t"
        "v_mfma_f32_16x16x32_bf16 v[12:15], v[176:179], v[60:63], v[12:15]\n\t"
        "v_lshlrev_b32_e32 v108, 16, v101\n\t"
        "v_mfma_f32_16x16x32_bf16 v[16:19], v[180:183], v[60:63], v[16:19]\n\t"
        "buffer_load_dwordx4 v[60:63], v117, %[sw], %[t0] offen offset:1024 sc1\n\t"
        "s_waitcnt vmcnt(15) lgkmcnt(2)\n\t"
        "v_mfma_f32_16x16x32_bf16 v[20:23], v[96:99], v[64:67], v[20:23]\n\t"
        "ds_read_b128 v[96:99], v116 offset:29696\n\t"
        "v_mfma_f32_16x16x32_bf16 v[8:11], v[184:187], v[64:67], v[8:11]\n\t"
        "v_mfma_f32_16x16x32_bf16 v[12:15], v[188:191], v[64:67], v[12:15]\n\t"
        "v_mul_f32_e32 v108, v102, v108\n\t"
        "v_mfma_f32_16x16x32_bf16 v[16:19], v[192:195], v[64:67], v[16:19]\n\t"
        "buffer_load_dwordx4 v[64:67], v117, %[sw], %[t0] offen offset:2048 sc1\n\t"
        "s_waitcnt vmcnt(15) lgkmcnt(2)\n\t"
        "v_mfma_f32_16x16x32_bf16 v[20:23], v[88:91], v[68:71], v[20:23]\n\t"
        "ds_read_b128 v[88:91], v116 offset:30720\n\t"
        "v_mfma_f32_16x16x32_bf16 v[8:11], v[196:199], v[68:71], v[8:11]\n\t"
        "v_mfma_f32_16x16x32_bf16 v[12:15], v[200:203], v[68:71], v[12:15]\n\t"
        "v_cvt_pk_bf16_f32 v108, 0, v108\n\t"
        "v_mfma_f32_16x16x32_bf16 v[16:19], v[204:207], v[68:71], v[16:19]\n\t"
        "buffer_load_dwordx4 v[68:71], v117, %[sw], %[t0] offen offset:3072 sc1\n\t"
        "s_waitcnt vmcnt(15) lgkmcnt(2)\n\t"
        "v_mfma_f32_16x16x32_bf16 v[20:23], v[92:95], v[72:75], v[20:23]\n\t"
        "ds_read_b128 v[92:95], v116 offset:31744\n\t"
        "v_mfma_f32_16x16x32_bf16 v[8:11], v[208:211], v[72:75], v[8:11]\n\t"
        "v_mfma_f32_16x16x32_bf16 v[12:15], v[212:215], v[72:75], v[12:15]\n\t"
        "v_and_b32_e32 v109, 0xffff0000, v101\n\t"
        "v_mfma_f32_16x16x32_bf16 v[16:19], v[216:219], v[72:75], v[16:19]\n\t"
        "s_add_u32 %[t0], %[ro], 3\n\t"
        "s_and_b32 %[t0], %[t0], 7\n\t"
        "s_lshl_b32 %[t0], %[t0], 12\n\t"
        "s_add_u32 %[t0], %[t0], %[nx]\n\t"
        "buffer_load_dwordx4 v[72:75], v117, %[sw], %[t0] offen offset:0 sc1\n\t"
        "s_waitcnt vmcnt(15) lgkmcnt(2)\n\t"
        "v_mfma_f32_16x16x32_bf16 v[20:23], v[96:99], v[76:79], v[20:23]\n\t"
        "v_mfma_f32_16x16x32_bf16 v[8:11], v[220:223], v[76:79], v[8:11]\n\t"
        "v_mfma_f32_16x16x32_bf16 v[12:15], v[224:227], v[76:79], v[12:15]\n\t"
        "v_mul_f32_e32 v109, v102, v109\n\t"
        "v_mfma_f32_16x16x32_bf16 v[16:19], v[228:231], v[76:79], v[16:19]\n\t"
        "buffer_load_dwordx4 v[76:79], v117, %[sw], %[t0] offen offset:1024 sc1\n\t"
        "s_waitcnt vmcnt(15) lgkmcnt(1)\n\t"
        "v_mfma_f32_16x16x32_bf16 v[20:23], v[88:91], v[80:83], v[20:23]\n\t"
        "ds_read_b128 v[88:91], v116 offset:0\n\t"
        "v_mfma_f32_16x16x32_bf16 v[8:11], v[232:235], v[80:83], v[8:11]\n\t"
        "v_mfma_f32_16x16x32_bf16 v[12:15], v[236:239], v[80:83], v[12:15]\n\t"
        "v_cvt_pk_bf16_f32 v109, 0, v109\n\t"
        "v_mfma_f32_16x16x32_bf16 v[16:19], v[240:243], v[80:83], v[16:19]\n\t"
        "buffer_load_dwordx4 v[80:83], v117, %[sw], %[t0] offen offset:2048 sc1\n\t"
        "s_waitcnt vmcnt(15) lgkmcnt(1)\n\t"
        "v_mfma_f32_16x16x32_bf16 v[20:23], v[92:95], v[84:87], v[20:23]\n\t"
        "ds_read_b128 v[92:95], v116 offset:1024\n\t"
        "v_mfma_f32_16x16x32_bf16 v[8:11], v[244:247], v[84:87], v[8:11]\n\t"
        "ds_read_b128 v[96:99], v116 offset:2048\n\t"
        "v_mfma_f32_16x16x32_bf16 v[12:15], v[248:251], v[84:87], v[12:15]\n\t"
        "v_mfma_f32_16x16x32_bf16 v[16:19], v[252:255], v[84:87], v[16:19]\n\t"
        "buffer_load_dwordx4 v[84:87], v117, %[sw], %[t0] offen offset:3072 sc1\n\t"
        "LP_epi%=:\n\t"
        "s_lshl_b32 %[t0], %[tl], 15\n\t"
        "s_nop 0\n\t"
        "ds_write_b128 v122, v[20:23] offset:192\n\t"
        "s_nop 0\n\t"
        "ds_write_b128 v122, v[8:11] offset:0\n\t"
        "s_nop 0\n\t"
        "ds_write_b128 v122, v[12:15] offset:64\n\t"
        "ds_write_b128 v122, v[16:19] offset:128\n\t"
        "s_waitcnt lgkmcnt(0)\n\t"
        "s_barrier\n\t"
        "ds_read_b128 v[8:11], v123 offset:0\n\t"
        "ds_read_b128 v[12:15], v123 offset:4352\n\t"
        "ds_read_b128 v[16:19], v123 offset:8704\n\t"
        "ds_read_b128 v[20:23], v123 offset:13056\n\t"
        "s_waitcnt lgkmcnt(2)\n\t"
        "v_pk_add_f32 v[8:9], v[8:9], v[12:13]\n\t"
        "v_pk_add_f32 v[10:11], v[10:11], v[14:15]\n\t"
        "s_waitcnt lgkmcnt(0)\n\t"
        "v_pk_add_f32 v[16:17], v[16:17], v[20:21]\n\t"
        "v_pk_add_f32 v[18:19], v[18:19], v[22:23]\n\t"
        "v_pk_add_f32 v[8:9], v[8:9], v[16:17]\n\t"
        "v_pk_add_f32 v[10:11], v[10:11], v[18:19]\n\t"
        "v_cvt_pk_bf16_f32 v8, 0, v8\n\t"
        "v_cvt_pk_bf16_f32 v9, 0, v9\n\t"
        "v_cvt_pk_bf16_f32 v10, 0, v10\n\t"
        "v_cvt_pk_bf16_f32 v11, 0, v11\n\t"
        "v_mul_f32_e32 v8, v104, v8\n\t"
        "v_mul_f32_e32 v9, v104, v9\n\t"
        "v_mul_f32_e32 v10, v104, v10\n\t"
        "v_mul_f32_e32 v11, v104, v11\n\t"
        "v_cvt_pk_bf16_f32 v8, 0, v8\n\t"
        "v_cvt_pk_bf16_f32 v9, 0, v9\n\t"
        "v_cvt_pk_bf16_f32 v10, 0, v10\n\t"
        "v_cvt_pk_bf16_f32 v11, 0, v11\n\t"
        "v_add_f32_e32 v8, v106, v8\n\t"
        "v_add_f32_e32 v9, v107, v9\n\t"
        "v_add_f32_e32 v10, v108, v10\n\t"
        "v_add_f32_e32 v11, v109, v11\n\t"
        "v_cvt_pk_bf16_f32 v104, v8, v9\n\t"
        "v_cvt_pk_bf16_f32 v105, v10, v11\n\t"
        "buffer_store_dwordx2 v[104:105], v121, %[su], %[t0] offen sc1\n\t"
        "s_add_u32 %[tl], %[tl], 8\n\t"
        "s_cmp_ge_u32 %[tl], 128\n\t"
        "s_cbranch_scc1 LP_end%=\n\t"
        "s_cmp_lg_u32 %[nx], 0xf0000000\n\t"
        "s_cbranch_scc1 LP_tile%=\n\t"
        "s_branch LP_wait%=\n\t"
        "LP_end%=:\n\t"
        "s_waitcnt vmcnt(0) lgkmcnt(0)\n\t"
        ".Lk_end_P:\n\t"
        : [t0] "=&s"(t0), [t1] "=&s"(t1), [tl] "+s"(tl), [pg] "=&s"(pg), [cu] "=&s"(cu), [nx] "=&s"(nx), [sp] "=&s"(sp), [re] "=&s"(re)
        : [so] "s"(so), [sw] "s"(sw), [su] "s"(su), [cv] "s"(F_CVY + nt * 128), [ro] "s"(ro),
          "{v110}"((w * 1024 + (l >> 3)) * 2048 + nt * 128 + (l & 7) * 16), "{v111}"(stg + ((l * 16) ^ ((l & 48) << 1))), "{v112}"(lr), "{v113}"(lr ^ 32u), "{v114}"(lr ^ 64u), "{v115}"(lr ^ 96u),
          "{v116}"(lb + w * 32768 + l * 16), "{v117}"(W_OX + w * 32768 + l * 16), "{v118}"(F_PROG + (w * 8 + (l & 7)) * 128),
          "{v119}"(W_A + ((nt * 2 + (n >> 3)) * 4 + 2 * (n & 1)) * 32768 + w * 4096 + ((n & 7) >> 1) * 16 + g * 4), "{v120}"(W_GATE + w * 128 + g * 4), "{v121}"((4 * w + g) * 2048 + nt * 128 + n * 8),
          "{v122}"(lb + 131072 + w * 4352 + n * 272 + g * 16), "{v123}"(lb + 131072 + (4 * w + g) * 272 + n * 16)
        : "memory", "scc", "vcc", "v8", "v9", "v10", "v11", "v12", "v13", "v14", "v15", "v16", "v17", "v18", "v19", "v20", "v21", "v22", "v23", "v24", "v25", "v26", "v27", "v28", "v29", "v30", "v31", "v32", "v33", "v34", "v35", "v36", "v37", "v38", "v39", "v40", "v41", "v42", "v43", "v44", "v45", "v46", "v47", "v48", "v49", "v50", "v51", "v52", "v53", "v54", "v55", "v56", "v57", "v58", "v59", "v60", "v61", "v62", "v63", "v64", "v65", "v66", "v67", "v68", "v69", "v70", "v71", "v72", "v73", "v74", "v75", "v76", "v77", "v78", "v79", "v80", "v81", "v82", "v83", "v84", "v85", "v86", "v87", "v88", "v89", "v90", "v91", "v92", "v93", "v94", "v95", "v96", "v97", "v98", "v99", "v100", "v101", "v102", "v103", "v104", "v105", "v106", "v107", "v108", "v109", "v124", "v125", "v126", "v127", "v128", "v129", "v130", "v131", "v132", "v133", "v134", "v135", "v136", "v137", "v138", "v139", "v140", "v141", "v142", "v143", "v144", "v145", "v146", "v147", "v148", "v149", "v150", "v151", "v152", "v153", "v154", "v155", "v156", "v157", "v158", "v159", "v160", "v161", "v162", "v163", "v164", "v165", "v166", "v167", "v168", "v169", "v170", "v171", "v172", "v173", "v174", "v175", "v176", "v177", "v178", "v179", "v180", "v181", "v182", "v183", "v184", "v185", "v186", "v187", "v188", "v189", "v190", "v191", "v192", "v193", "v194", "v195", "v196", "v197", "v198", "v199", "v200", "v201", "v202", "v203", "v204", "v205", "v206", "v207", "v208", "v209", "v210", "v211", "v212", "v213", "v214", "v215", "v216", "v217", "v218", "v219", "v220", "v221", "v222", "v223", "v224", "v225", "v226", "v227", "v228", "v229", "v230", "v231", "v232", "v233", "v234", "v235", "v236", "v237", "v238", "v239", "v240", "v241", "v242", "v243", "v244", "v245", "v246", "v247", "v248", "v249", "v250", "v251", "v252", "v253", "v254", "v255", "a0", "a1", "a2", "a3", "a4", "a5", "a6", "a7", "a8", "a9", "a10", "a11", "a12", "a13", "a14", "a15", "a16", "a17", "a18", "a19", "a20", "a21", "a22", "a23", "a24", "a25", "a26", "a27", "a28", "a29", "a30", "a31", "a32", "a33", "a34", "a35", "a36", "a37", "a38", "a39", "a40", "a41", "a42", "a43", "a44", "a45", "a46", "a47", "a48", "a49", "a50", "a51", "a52", "a53", "a54", "a55", "a56", "a57", "a58", "a59", "a60", "a61", "a62", "a63", "a64", "a65", "a66", "a67", "a68", "a69", "a70", "a71", "a72", "a73", "a74", "a75", "a76", "a77", "a78", "a79", "a80", "a81", "a82", "a83", "a84", "a85", "a86", "a87", "a88", "a89", "a90", "a91", "a92", "a93", "a94", "a95", "a96", "a97", "a98", "a99", "a100", "a101", "a102", "a103", "a104", "a105", "a106", "a107", "a108", "a109", "a110", "a111", "a112", "a113", "a114", "a115", "a116", "a117", "a118", "a119", "a120", "a121", "a122", "a123", "a124", "a125", "a126", "a127", "a128", "a129", "a130", "a131", "a132", "a133", "a134", "a135", "a136", "a137", "a138", "a139", "a140", "a141", "a142", "a143", "a144", "a145", "a146", "a147", "a148", "a149", "a150", "a151", "a152", "a153", "a154", "a155", "a156", "a157", "a158", "a159", "a160", "a161", "a162", "a163", "a164", "a165", "a166", "a167", "a168", "a169", "a170", "a171", "a172", "a173", "a174", "a175", "a176", "a177", "a178", "a179", "a180", "a181", "a182", "a183", "a184", "a185", "a186", "a187", "a188", "a189", "a190", "a191", "a192", "a193", "a194", "a195", "a196", "a197", "a198", "a199", "a200", "a201", "a202", "a203", "a204", "a205", "a206", "a207", "a208", "a209", "a210", "a211", "a212", "a213", "a214", "a215", "a216", "a217", "a218", "a219", "a220", "a221", "a222", "a223", "a224", "a225", "a226", "a227", "a228", "a229", "a230", "a231", "a232", "a233", "a234", "a235", "a236", "a237", "a238", "a239", "a240", "a241", "a242", "a243", "a244", "a245", "a246", "a247", "a248", "a249", "a250", "a251", "a252", "a253", "a254", "a255");
}
__device__ __forceinline__ void bulk_block(const Params& p, rsrc_t r, unsigned char* lds, const int wv, const int j) {
    proj_block(p, lds, wv, (j & 6) * 4 + (j >> 4), ((j >> 2) & 2) | (j & 1));
    const int un = ((j & 7) >> 1) * 32 + (j >> 3) * 2 + (j & 1);
    conv_prep(p, r, wv, un >> 2, un & 3, j);
    const int rp = ((j & 7) >> 1) + (j & 1) * 4, nt = (j >> 3) ^ 2, ro = (nt + 2 * rp) & 7;
    if (!conv_core(p, r, lds, wv, un, nt, ro)) return;
    if (!wait_word(r, F_CONV, 128u, 4096u)) return;
    out_block(p, lds, wv, rp, ro, nt);
}

__device__ __forceinline__ void rec_block(const Params& p, rsrc_t r, unsigned char* lds, const int wv, const int b) {
    const int xq = (b & 7) >> 1, t = (b >> 3) * 2 + (b & 1);
    const unsigned lb = __builtin_amdgcn_readfirstlane((unsigned)(size_t)lds);
    if (t < 8) rec_a(p, lb, wv, xq * 8 + t, xq);
    else if (t < 24) rec_b(p, lb, wv, (xq & 1) * 16 + t - 8, xq >> 1, xq);
    else {
        const int hi = xq * 8 + t - 24;
        rec_helper(p, r, lb, wv, hi);
    }
}

__global__ void __launch_bounds__(256, 1) k_main(Params p) {
    extern __shared__ __attribute__((aligned(16))) unsigned char lds[];
    rsrc_t r = __builtin_amdgcn_make_buffer_rsrc(p.ws, 0, W_TOTAL, 0x00020000);
    const int wv = __builtin_amdgcn_readfirstlane(threadIdx.x >> 6);
    {
        unsigned long long pc; int ea, eb, eg;
        asm volatile("s_getpc_b64 %0\n.Lk_begin:\n\ts_mov_b32 %1, .Lk_end_A-.Lk_begin\n\ts_mov_b32 %2, .Lk_end_B-.Lk_begin\n\ts_mov_b32 %3, .Lk_end_P-.Lk_begin" : "=s"(pc), "=s"(ea), "=s"(eb), "=s"(eg));
        const int o = ((blockIdx.x >> 3) * 64 + (threadIdx.x >> 2)) * 128;
        if ((threadIdx.x & 3) == 0 && o < max(ea, max(eb, eg)) - 4) touch_l2((const unsigned char*)pc + o, __builtin_amdgcn_readfirstlane((unsigned)(size_t)lds) + 1024);
    }
    if (wv == 3) {
        unsigned xc; asm volatile("s_getreg_b32 %0, hwreg(HW_REG_XCC_ID)" : "=s"(xc));
        if (xc & 4u) touch_l2(p.Aq + ((blockIdx.x >> 3) * 64 + (threadIdx.x & 63)) * 64, __builtin_amdgcn_readfirstlane((unsigned)(size_t)lds) + 2048);
    }
    if (blockIdx.x == 0 && wv == 1) {
        const int l = threadIdx.x & 63; const v4u z = {0u, 0u, 0u, 0u};
#pragma unroll
        for (int i = 0; i < 6; ++i) st16(r, F_CONV + i * 1024 + l * 16, z);
        DRAIN();
        for (unsigned spin = 0; spin < (1u << 20); ++spin) {
            if (l < 8) __hip_atomic_fetch_add((unsigned*)(p.ws + F_BEAT + l * 128), 1u, __ATOMIC_RELAXED, __HIP_MEMORY_SCOPE_AGENT);
            unsigned c = ld4(r, l < 8 ? F_TKT + l * 64 : OOB);
            c += __shfl_xor(c, 1); c += __shfl_xor(c, 2); c += __shfl_xor(c, 4);
            if (__builtin_amdgcn_readfirstlane(c) >= 256u) break; }
    }
    if (threadIdx.x == 0) {
        const unsigned bo = F_BEAT + (blockIdx.x & 7) * 128, b0 = ld4(r, bo);
        for (unsigned spin = 0; spin < (1u << 22); ++spin) { MEMBAR(); if (ld4(r, bo) != b0) break; }
        MEMBAR();
        unsigned xcc; asm volatile("s_getreg_b32 %0, hwreg(HW_REG_XCC_ID)" : "=s"(xcc)); xcc &= 7u;
        unsigned* tk = (unsigned*)(p.ws + F_TKT);
        unsigned t = __hip_atomic_fetch_add(tk + xcc * 16, 1u, __ATOMIC_RELAXED, __HIP_MEMORY_SCOPE_AGENT);
        if (t >= 32u) {
            unsigned o = __hip_atomic_fetch_add(tk + 128, 1u, __ATOMIC_RELAXED, __HIP_MEMORY_SCOPE_AGENT);
            for (unsigned spin = 0; spin < (1u << 22); ++spin) { unsigned tot = 0u;
                for (int x = 0; x < 8; ++x) tot += __hip_atomic_load(tk + x * 16, __ATOMIC_RELAXED, __HIP_MEMORY_SCOPE_AGENT);
                if (tot >= 256u) break; }
            for (int x = 0; x < 8; ++x) { const unsigned cnt = __hip_atomic_load(tk + x * 16, __ATOMIC_RELAXED, __HIP_MEMORY_SCOPE_AGENT), fr = cnt < 32u ? 32u - cnt : 0u;
                if (o < fr) { xcc = (unsigned)x; t = cnt + o; break; }
                o -= fr; }
        }
        *(volatile int*)lds = (int)((xcc >> 2) * 128u + (t >> 1) * 8u + (xcc & 3u) * 2u + (t & 1u));
    }
    DRAIN();
    __syncthreads();
    const int role = __builtin_amdgcn_readfirstlane(*(volatile int*)lds) & 255;
    __syncthreads();
    if (role >= 128) bulk_block(p, r, lds, wv, role - 128); else rec_block(p, r, lds, wv, role);
}

extern "C" void kernel_launch(void* const* d_in, const int* in_sizes, int n_in, void* d_out, int out_size, void* d_ws, size_t ws_size, hipStream_t stream) {
    Params p;
    p.Aq = (const bf16_t*)d_in[0]; p.Ak = (const bf16_t*)d_in[1]; p.Av = (const bf16_t*)d_in[2];
    p.shift_k = (const bf16_t*)d_in[3]; p.ssm_k = (const bf16_t*)d_in[4]; p.ssm_D = (const bf16_t*)d_in[5];
    p.mem0 = (const bf16_t*)d_in[7]; p.Wq = (const bf16_t*)d_in[8]; p.Wk = (const bf16_t*)d_in[9];
    p.Wv = (const bf16_t*)d_in[10]; p.Wo = (const bf16_t*)d_in[11]; p.gate_w = (const bf16_t*)d_in[13];
    p.out = (bf16_t*)d_out; p.ws = (unsigned char*)d_ws;
    (void)hipFuncSetAttribute((const void*)k_main, hipFuncAttributeMaxDynamicSharedMemorySize, L_TOTAL);
    k_main<<<256, 256, L_TOTAL, stream>>>(p);
}
```

```cpp
#include <hip/hip_runtime.h>
#include <stdint.h>
#include <type_traits>
typedef unsigned short bf16_t;
using bf16x8 = __attribute__((ext_vector_type(8))) short;
using f32x4 = __attribute__((ext_vector_type(4))) float;
typedef unsigned v4u __attribute__((ext_vector_type(4)));
typedef unsigned v2u __attribute__((ext_vector_type(2)));
typedef __amdgpu_buffer_rsrc_t rsrc_t;

__device__ __forceinline__ float bf2f(bf16_t b) { return __uint_as_float(((unsigned)b) << 16); }
typedef __bf16 hbf2 __attribute__((ext_vector_type(2)));
typedef float f32x2 __attribute__((ext_vector_type(2)));
__device__ __forceinline__ unsigned pack2(float a, float b) { f32x2 v = {a, b}; return __builtin_bit_cast(unsigned, __builtin_convertvector(v, hbf2)); }
__device__ __forceinline__ float rbfh(float f) { return __uint_as_float(pack2(f, 0.f) << 16); }
__device__ __forceinline__ unsigned nosent(unsigned x) {
    typedef unsigned short us2 __attribute__((ext_vector_type(2)));
    const us2 a = __builtin_bit_cast(us2, x), c = {(unsigned short)0xFFFE, (unsigned short)0xFFFE}; return __builtin_bit_cast(unsigned, __builtin_elementwise_min(a, c)); }

constexpr int S = 2048, D = 1024, H = 32, DH = 128, M = 8, SEG = 32, NSEG = 64;

constexpr unsigned W_FLG = 0;
constexpr unsigned F_KVQ = W_FLG;
constexpr unsigned F_CONV = W_FLG + 32768;
constexpr unsigned F_ERR = W_FLG + 32768 + 256;
constexpr unsigned F_PREP = W_FLG + 32768 + 512;
constexpr unsigned F_CAL = W_FLG + 33792;
constexpr unsigned F_TKT = W_FLG + 34816;
constexpr unsigned F_BEAT = W_FLG + 40960;
constexpr unsigned F_PROG = W_FLG + 49152;
constexpr unsigned F_CVU = W_FLG + 53248, F_CVY = W_FLG + 57344;
constexpr unsigned F_OP = W_FLG + 65536;
constexpr unsigned F_ATT = W_FLG + 131072;
constexpr unsigned F_RDY = W_FLG + 147456;
constexpr unsigned FLG_BYTES = 180224;
constexpr unsigned HO_KVM = W_FLG + FLG_BYTES;
constexpr unsigned W_A = HO_KVM + 64 * 32 * 4096;
constexpr unsigned W_GATE = W_A + S * D * 2;
constexpr unsigned W_QX = W_GATE + 8192;
constexpr unsigned W_KX = W_QX + S * 4096 * 2;
constexpr unsigned W_VF = W_KX + S * 4096 * 2;
constexpr unsigned W_OX = W_VF + 64 * 32 * 8192;
constexpr unsigned W_PART = W_OX + S * 4096 * 2;
constexpr unsigned W_Y = W_PART;
constexpr unsigned W_DBG = W_PART + 32 * 4 * 64 * 1024 * 4;
constexpr unsigned W_U = W_DBG + 256 * 64 * 16 * 8;
constexpr unsigned W_TOTAL = W_U + S * D * 2;

__device__ __forceinline__ v4u ld16(rsrc_t r, unsigned off) { return __builtin_amdgcn_raw_buffer_load_b128(r, off, 0, 16); }
__device__ __forceinline__ v2u ld8(rsrc_t r, unsigned off) { return __builtin_amdgcn_raw_buffer_load_b64(r, off, 0, 16); }
__device__ __forceinline__ unsigned ld4(rsrc_t r, unsigned off) { return __builtin_amdgcn_raw_buffer_load_b32(r, off, 0, 16); }
__device__ __forceinline__ void st16(rsrc_t r, unsigned off, v4u v) { __builtin_amdgcn_raw_buffer_store_b128(v, r, off, 0, 16); }
__device__ __forceinline__ void st8(rsrc_t r, unsigned off, v2u v) { __builtin_amdgcn_raw_buffer_store_b64(v, r, off, 0, 16); }
__device__ __forceinline__ void st4(rsrc_t r, unsigned off, unsigned v) { __builtin_amdgcn_raw_buffer_store_b32(v, r, off, 0, 16); }
__device__ __forceinline__ bool okdw(unsigned x) { return ((x & 0xFFFFu) != 0xFFFFu) & ((x >> 16) != 0xFFFFu); }
typedef unsigned short us2_t __attribute__((ext_vector_type(2)));
__device__ __forceinline__ unsigned pkmax(unsigned a, unsigned b) { return __builtin_bit_cast(unsigned, __builtin_elementwise_max(__builtin_bit_cast(us2_t, a), __builtin_bit_cast(us2_t, b))); }
__device__ __forceinline__ unsigned pkmax4(v4u v) { return pkmax(pkmax(v.x, v.y), pkmax(v.z, v.w)); }
__device__ __forceinline__ bool ok16(v4u v) { return okdw(pkmax4(v)); }
__device__ __forceinline__ bool ok8(v2u v) { return okdw(v.x) & okdw(v.y); }
__device__ __forceinline__ bf16x8 as_frag(v4u v) { return __builtin_bit_cast(bf16x8, v); }
#define MFMA(a, b, c) __builtin_amdgcn_mfma_f32_16x16x32_bf16((a), (b), (c), 0, 0, 0)
#define D1 128
#define D2 77
#define D3 76
#define LT_A 1
#define LB0 12
#define LC0 20
#define LA0 24
#define TR_UP 4
#define TR_DN 2
#define L_DEC 3
#define WARM1 0
#define KROT 0
#define STAG 1
#define UP_SH 2
#define EPI_SLP 0
#define SITA 1
#define PAIRX 4
#define DUE_P 1
#define LATE 1
#define EARLY_FROM 56
#define SKEW 0
#define OUT_WARM 1
#define ADAPT 1
#define RPL 0
#define CV_POL 2
#define POLL_SLP 4
#define TOUCH 1
#define ROT 0
#define CODE_T 1
#define CAL_T 450u
#define GTOUCH 0
#define XTOUCH 32
#define ST_P 32
#define CV_NAP 4000u
#define XSLP 3
#define XDLY 0
#define GDLY 0
#define YDLY 0
#define PROT 3
#define XSTG 0
#define XG 1
#define MS_BYTES 65536
#define BEAT_SLP 0
#define T2U 2
#define T2A 0
#define T2F 0
#define TPRE 48
#define XWAIT 1
#define PEN 48
#define MEMBAR() asm volatile("" ::: "memory")
#define FT(i) do { } while (0)
#define ENDPGM() asm volatile("s_endpgm" ::: "memory")
#define DRAIN() asm volatile("s_waitcnt vmcnt(0)" ::: "memory")

__device__ __forceinline__ float xor16_max(float x) { auto r = __builtin_amdgcn_permlane16_swap(__float_as_uint(x), __float_as_uint(x), false, false); return fmaxf(__uint_as_float(r[0]), __uint_as_float(r[1])); }
__device__ __forceinline__ float xor32_max(float x) { auto r = __builtin_amdgcn_permlane32_swap(__float_as_uint(x), __float_as_uint(x), false, false); return fmaxf(__uint_as_float(r[0]), __uint_as_float(r[1])); }
__device__ __forceinline__ float xor16_add(float x) { auto r = __builtin_amdgcn_permlane16_swap(__float_as_uint(x), __float_as_uint(x), false, false); return __uint_as_float(r[0]) + __uint_as_float(r[1]); }
__device__ __forceinline__ float xor32_add(float x) { auto r = __builtin_amdgcn_permlane32_swap(__float_as_uint(x), __float_as_uint(x), false, false); return __uint_as_float(r[0]) + __uint_as_float(r[1]); }
__device__ __forceinline__ v2u tr_read(unsigned lds_addr) { v2u r; asm volatile("ds_read_b64_tr_b16 %0, %1\n\ts_waitcnt lgkmcnt(0)" : "=&v"(r) : "v"(lds_addr) : "memory"); return r; }

typedef short v4i16_t __attribute__((ext_vector_type(4)));
__device__ __forceinline__ v2u trb(unsigned lds_addr) { return __builtin_bit_cast(v2u, __builtin_amdgcn_ds_read_tr16_b64_v4i16((__attribute__((address_space(3))) v4i16_t*)(size_t)lds_addr)); }
__device__ __forceinline__ int otid_(int wv) { int t; asm volatile("v_mbcnt_lo_u32_b32 %0, -1, 0\n\tv_mbcnt_hi_u32_b32 %0, -1, %0" : "=v"(t)); return wv * 64 + t; }
#define otid() otid_(wv)
__device__ __forceinline__ bool wait_word(rsrc_t r, unsigned off, unsigned want, unsigned code) {
    for (unsigned spin = 0;; ++spin) { MEMBAR();
        const unsigned v = __builtin_amdgcn_readfirstlane(ld4(r, off));
        if (v >= want) return true;
        if (spin > (1u << 20)) { st4(r, F_ERR, code); return false; }
        __builtin_amdgcn_s_sleep(8); }
}

__device__ __forceinline__ unsigned rtnow() { return (unsigned)__builtin_amdgcn_s_memrealtime(); }
__device__ __forceinline__ void wait_until(unsigned target) { for (int i = 0; i < 4096; ++i) { if ((int)(rtnow() - target) >= 0) break; } }
__device__ __forceinline__ void wait_long(unsigned target) { for (int i = 0; i < 4096; ++i) { if ((int)(rtnow() - target) >= 0) break; __builtin_amdgcn_s_sleep(8); } }
constexpr int RED_G = 320;
constexpr int RED_T = 4 * RED_G;
constexpr int L_RED = 0;
constexpr int L_KX = 65536;
constexpr int KXP = 272;
constexpr int L_VF = L_KX + 2 * 32 * KXP;
constexpr int L_QKV = L_VF + 16384;
constexpr int L_OFF = L_QKV + 6144;
constexpr int L_STMP = L_OFF + 256;
constexpr int L_TOTAL = 163840;
constexpr unsigned U_A = 0, U_S = 32, U_B = 36, U_INIT = 113, U_C = 128, U_END = 512;
#define PLACE 1
#define CAL_N 5
#define STEP_MIN 190u

struct Params {
    const bf16_t *Aq, *Ak, *Av, *shift_k, *ssm_k, *ssm_D, *mem0, *Wq, *Wk, *Wv, *Wo, *gate_w;
    bf16_t* out; unsigned char* ws;
};
__host__ __device__ __forceinline__ unsigned hx_base(const void* ws) { const size_t a = (size_t)ws; return (unsigned)((((a + W_TOTAL + 0x7FFFFFu) >> 23) << 23) - a); }

#define STAMP(i) do { } while (0)
#define BSTAMP(i) do { } while (0)
#define EV(code) do { } while (0)
#define SPINS(i) do { } while (0)
typedef short v4s __attribute__((ext_vector_type(4)));
typedef unsigned v32u __attribute__((ext_vector_type(32)));
typedef unsigned v16u __attribute__((ext_vector_type(16)));
static_assert(L_VF - L_KX == 16384 + 1024, "fragment read offsets");
#define MFMA16(a, b, c) __builtin_amdgcn_mfma_f32_16x16x16bf16_1k(__builtin_bit_cast(v4s, (a)), __builtin_bit_cast(v4s, (b)), (c), 0, 0, 0)
__device__ __forceinline__ unsigned ror8(unsigned x) { return (unsigned)__builtin_amdgcn_update_dpp(0, (int)x, 0x128, 0xF, 0xF, false); }
__device__ __forceinline__ unsigned qx1(unsigned x) { return (unsigned)__builtin_amdgcn_update_dpp(0, (int)x, 0xB1, 0xF, 0xF, false); }
__device__ __forceinline__ unsigned wave_pkmax(unsigned d) {
    d = pkmax(d, (unsigned)__builtin_amdgcn_update_dpp((int)d, (int)d, 0xB1, 0xF, 0xF, false)); d = pkmax(d, (unsigned)__builtin_amdgcn_update_dpp((int)d, (int)d, 0x4E, 0xF, 0xF, false));
    d = pkmax(d, (unsigned)__builtin_amdgcn_update_dpp((int)d, (int)d, 0x124, 0xF, 0xF, false)); d = pkmax(d, (unsigned)__builtin_amdgcn_update_dpp((int)d, (int)d, 0x128, 0xF, 0xF, false));
    { auto q = __builtin_amdgcn_permlane16_swap(d, d, false, false); d = pkmax(q[0], q[1]); }
    { auto q = __builtin_amdgcn_permlane32_swap(d, d, false, false); d = pkmax(q[0], q[1]); }
    return d; }
__device__ __forceinline__ int wave_max(int d) {
    d = max(d, __builtin_amdgcn_update_dpp(d, d, 0xB1, 0xF, 0xF, false)); d = max(d, __builtin_amdgcn_update_dpp(d, d, 0x4E, 0xF, 0xF, false));
    d = max(d, __builtin_amdgcn_update_dpp(d, d, 0x124, 0xF, 0xF, false)); d = max(d, __builtin_amdgcn_update_dpp(d, d, 0x128, 0xF, 0xF, false));
    { auto q = __builtin_amdgcn_permlane16_swap((unsigned)d, (unsigned)d, false, false); d = max((int)q[0], (int)q[1]); }
    { auto q = __builtin_amdgcn_permlane32_swap((unsigned)d, (unsigned)d, false, false); d = max((int)q[0], (int)q[1]); }
    return d; }

__device__ __forceinline__ v4u mk_srd(const void* p, unsigned bytes) { const size_t a = (size_t)p; v4u d;
    d.x = __builtin_amdgcn_readfirstlane((unsigned)a); d.y = __builtin_amdgcn_readfirstlane((unsigned)(a >> 32) & 0xFFFFu); d.z = __builtin_amdgcn_readfirstlane(bytes); d.w = 0x00020000u; return d; }
__device__ __forceinline__ bf16x8 ldsa(unsigned addr) { return *(const __attribute__((address_space(3))) bf16x8*)(size_t)addr; }
constexpr unsigned HX_BYTES = 2097152, OOB = 0xF0000000u;
constexpr unsigned F_RDY0 = W_FLG + 34304;
constexpr unsigned F_GO = W_FLG + 36864;
constexpr int L_KV = 90112, L_MS = 139264, L_IMG = 155648, IMG_PS = 1056, IMG_MS = 2112, LB_R = 8192, LB_STG = 16384, LB_STGP = 8704;
static_assert(F_KVQ == 0 && F_ERR == 33024 && F_CONV == 32768 && F_CAL == 33792 && F_RDY0 == 34304 && F_GO == 36864 && F_PROG == 49152 && W_QX == 12771328 && W_KX == 29548544 && W_OX == 63102976 && W_VF == 46325760 && W_DBG == 113434624 && L_KV == 90112 && L_MS == 139264 && L_IMG == 155648 && IMG_PS == 1056 && IMG_MS == 2112 && LB_R == 8192 && LB_STG == 16384 && LB_STGP == 8704, "constants of the generated code");
#define REC_S_IN "{s[8:11]}"(sh), "{s[12:15]}"(sw), "{s[16:19]}"(s1), "{s[20:23]}"(s2), "{s29}"(D1), "{s30}"(D2), "{s43}"(w), "{s44}"(id), "{s49}"(rb), "{s50}"(PEN), "{s51}"(LT_A), "{s45}"(F_GO + (cp * 4 + w) * 128)
__device__ __forceinline__ void rec_a(const Params& p, unsigned lb, const int wv, const int id, const int cp) {
    const int w = wv, l = otid() & 63, n = l & 15, g = l >> 4, h = id, rb = id;
    const v4u sh = mk_srd(p.ws + hx_base(p.ws), HX_BYTES - 8192), sw = mk_srd(p.ws, W_TOTAL), s1 = mk_srd(w < 2 ? p.Wq : p.Wk, 1024 * 4096 * 2), s2 = s1;
    const int r8 = l >> 3, pc = l & 7, pp = n >> 3, bb = (n >> 2) & 1, f = (g & 1) | (pp << 1);
    const unsigned stg = lb + 131072 + w * 8192;
    const unsigned g_w = r8 * 8192 + h * 256 + (w & 1) * 128 + pc * 16;
    const unsigned wb0 = stg + r8 * 128 + (((pc >> 1) ^ (r8 >> 2)) << 5) + (pc & 1) * 16, wb1 = wb0 ^ 64u;
    const unsigned rb0 = stg + (16 * (g >> 1) + 8 * pp + 4 * (g & 1) + bb) * 128 + (f << 5) + (n & 3) * 8;
    const unsigned a_mem = cp * 32768 + (w >> 1) * 16384 + (w & 1) * 4096 + l * 16;
    const unsigned l_ms = lb + L_MS + l * 16;
    const unsigned l_iw = lb + L_IMG + (w >> 1) * IMG_MS + (g >> 1) * IMG_PS + (64 * (w & 1) + n) * 8 + (g & 1) * 4;
    const unsigned l_iq = lb + L_IMG + (n & 1) * IMG_PS + (8 * g + (n >> 2)) * 8;
    const unsigned a_vm = (16 + (h >> 2)) * 16384 + (h & 3) * 2048 + w * 512 + (l & 31) * 16;
    const unsigned a_o = n < 8 ? (8 + (n >> 2) * 4 + (h >> 3)) * 16384 + (h & 7) * 1024 + w * 256 + (n & 3) * 64 + g * 16 : OOB;
    const unsigned a_ox = (((w & 1) * 32 + h) * 4 + w) * 1024 + l * 16;
    asm volatile(
        "s_lshl_b32 s34, s43, 2\n\t"
        "s_add_u32 s34, s34, 0\n\t"
        "s_and_b32 s34, s34, 15\n\t"
        "s_lshl_b32 s34, s34, 19\n\t"
        "buffer_load_dwordx4 v[224:227], v96, s[16:19], s34 offen nt\n\t"
        "s_add_u32 s34, s34, 0x10000\n\t"
        "buffer_load_dwordx4 v[228:231], v96, s[16:19], s34 offen nt\n\t"
        "s_add_u32 s34, s34, 0x10000\n\t"
        "buffer_load_dwordx4 v[232:235], v96, s[16:19], s34 offen nt\n\t"
        "s_add_u32 s34, s34, 0x10000\n\t"
        "buffer_load_dwordx4 v[236:239], v96, s[16:19], s34 offen nt\n\t"
        "s_add_u32 s34, s34, 0x10000\n\t"
        "buffer_load_dwordx4 v[240:243], v96, s[16:19], s34 offen nt\n\t"
        "s_add_u32 s34, s34, 0x10000\n\t"
        "buffer_load_dwordx4 v[244:247], v96, s[16:19], s34 offen nt\n\t"
        "s_add_u32 s34, s34, 0x10000\n\t"
        "buffer_load_dwordx4 v[248:251], v96, s[16:19], s34 offen nt\n\t"
        "s_add_u32 s34, s34, 0x10000\n\t"
        "buffer_load_dwordx4 v[252:255], v96, s[16:19], s34 offen nt\n\t"
        "s_lshl_b32 s34, s43, 2\n\t"
        "s_add_u32 s34, s34, 1\n\t"
        "s_and_b32 s34, s34, 15\n\t"
        "s_lshl_b32 s34, s34, 19\n\t"
        "buffer_load_dwordx4 v[0:3], v96, s[16:19], s34 offen nt\n\t"
        "s_add_u32 s34, s34, 0x10000\n\t"
        "buffer_load_dwordx4 v[4:7], v96, s[16:19], s34 offen nt\n\t"
        "s_add_u32 s34, s34, 0x10000\n\t"
        "buffer_load_dwordx4 v[8:11], v96, s[16:19], s34 offen nt\n\t"
        "s_add_u32 s34, s34, 0x10000\n\t"
        "buffer_load_dwordx4 v[12:15], v96, s[16:19], s34 offen nt\n\t"
        "s_add_u32 s34, s34, 0x10000\n\t"
        "buffer_load_dwordx4 v[16:19], v96, s[16:19], s34 offen nt\n\t"
        "s_add_u32 s34, s34, 0x10000\n\t"
        "buffer_load_dwordx4 v[20:23], v96, s[16:19], s34 offen nt\n\t"
        "s_add_u32 s34, s34, 0x10000\n\t"
        "buffer_load_dwordx4 v[24:27], v96, s[16:19], s34 offen nt\n\t"
        "s_add_u32 s34, s34, 0x10000\n\t"
        "buffer_load_dwordx4 v[28:31], v96, s[16:19], s34 offen nt\n\t"
        "s_lshl_b32 s34, s43, 2\n\t"
        "s_add_u32 s34, s34, 2\n\t"
        "s_and_b32 s34, s34, 15\n\t"
        "s_lshl_b32 s34, s34, 19\n\t"
        "buffer_load_dwordx4 v[32:35], v96, s[16:19], s34 offen nt\n\t"
        "s_add_u32 s34, s34, 0x10000\n\t"
        "buffer_load_dwordx4 v[36:39], v96, s[16:19], s34 offen nt\n\t"
        "s_add_u32 s34, s34, 0x10000\n\t"
        "buffer_load_dwordx4 v[40:43], v96, s[16:19], s34 offen nt\n\t"
        "s_add_u32 s34, s34, 0x10000\n\t"
        "buffer_load_dwordx4 v[44:47], v96, s[16:19], s34 offen nt\n\t"
        "s_add_u32 s34, s34, 0x10000\n\t"
        "buffer_load_dwordx4 v[48:51], v96, s[16:19], s34 offen nt\n\t"
        "s_add_u32 s34, s34, 0x10000\n\t"
        "buffer_load_dwordx4 v[52:55], v96, s[16:19], s34 offen nt\n\t"
        "s_add_u32 s34, s34, 0x10000\n\t"
        "buffer_load_dwordx4 v[56:59], v96, s[16:19], s34 offen nt\n\t"
        "s_add_u32 s34, s34, 0x10000\n\t"
        "buffer_load_dwordx4 v[60:63], v96, s[16:19], s34 offen nt\n\t"
        "v_cmp_le_i32_e64 s[60:61], 0, v126\n\t"
        "v_cmp_le_i32_e64 s[62:63], 1, v126\n\t"
        "v_cmp_le_i32_e64 s[64:65], 2, v126\n\t"
        "v_cmp_le_i32_e64 s[66:67], 3, v126\n\t"
        "v_cmp_le_i32_e64 s[68:69], 16, v126\n\t"
        "v_cmp_le_i32_e64 s[70:71], 17, v126\n\t"
        "v_cmp_le_i32_e64 s[72:73], 18, v126\n\t"
        "v_cmp_le_i32_e64 s[74:75], 19, v126\n\t"
        "s_mul_i32 s35, s43, 10240\n\t"
        "v_add_u32_e32 v126, s35, v124\n\t"
        "s_waitcnt vmcnt(16)\n\t"
        "ds_write_b128 v97, v[224:227] offset:0\n\t"
        "ds_write_b128 v98, v[228:231] offset:1024\n\t"
        "ds_write_b128 v97, v[232:235] offset:2048\n\t"
        "ds_write_b128 v98, v[236:239] offset:3072\n\t"
        "ds_write_b128 v97, v[240:243] offset:4096\n\t"
        "ds_write_b128 v98, v[244:247] offset:5120\n\t"
        "ds_write_b128 v97, v[248:251] offset:6144\n\t"
        "ds_write_b128 v98, v[252:255] offset:7168\n\t"
        "ds_read_b64_tr_b16 a[0:1], v99 offset:0\n\t"
        "ds_read_b64_tr_b16 a[2:3], v99 offset:256\n\t"
        "ds_read_b64_tr_b16 a[4:5], v99 offset:4096\n\t"
        "ds_read_b64_tr_b16 a[6:7], v99 offset:4352\n\t"
        "s_waitcnt lgkmcnt(8)\n\t"
        "ds_read_b64_tr_b16 a[8:9], v100 offset:0\n\t"
        "ds_read_b64_tr_b16 a[10:11], v100 offset:256\n\t"
        "ds_read_b64_tr_b16 a[12:13], v100 offset:4096\n\t"
        "ds_read_b64_tr_b16 a[14:15], v100 offset:4352\n\t"
        "s_waitcnt lgkmcnt(8)\n\t"
        "ds_read_b64_tr_b16 a[16:17], v101 offset:0\n\t"
        "ds_read_b64_tr_b16 a[18:19], v101 offset:256\n\t"
        "ds_read_b64_tr_b16 a[20:21], v101 offset:4096\n\t"
        "ds_read_b64_tr_b16 a[22:23], v101 offset:4352\n\t"
        "s_waitcnt lgkmcnt(8)\n\t"
        "ds_read_b64_tr_b16 v[104:105], v102 offset:0\n\t"
        "ds_read_b64_tr_b16 v[106:107], v102 offset:256\n\t"
        "ds_read_b64_tr_b16 v[108:109], v102 offset:4096\n\t"
        "ds_read_b64_tr_b16 v[110:111], v102 offset:4352\n\t"
        "s_lshl_b32 s34, s43, 2\n\t"
        "s_add_u32 s34, s34, 3\n\t"
        "s_and_b32 s34, s34, 15\n\t"
        "s_lshl_b32 s34, s34, 19\n\t"
        "buffer_load_dwordx4 v[64:67], v96, s[16:19], s34 offen nt\n\t"
        "s_add_u32 s34, s34, 0x10000\n\t"
        "buffer_load_dwordx4 v[68:71], v96, s[16:19], s34 offen nt\n\t"
        "s_add_u32 s34, s34, 0x10000\n\t"
        "buffer_load_dwordx4 v[72:75], v96, s[16:19], s34 offen nt\n\t"
        "s_add_u32 s34, s34, 0x10000\n\t"
        "buffer_load_dwordx4 v[76:79], v96, s[16:19], s34 offen nt\n\t"
        "s_add_u32 s34, s34, 0x10000\n\t"
        "buffer_load_dwordx4 v[80:83], v96, s[16:19], s34 offen nt\n\t"
        "s_add_u32 s34, s34, 0x10000\n\t"
        "buffer_load_dwordx4 v[84:87], v96, s[16:19], s34 offen nt\n\t"
        "s_add_u32 s34, s34, 0x10000\n\t"
        "buffer_load_dwordx4 v[88:91], v96, s[16:19], s34 offen nt\n\t"
        "s_add_u32 s34, s34, 0x10000\n\t"
        "buffer_load_dwordx4 v[92:95], v96, s[16:19], s34 offen nt\n\t"
        "s_waitcnt lgkmcnt(0)\n\t"
        "ds_write_b128 v117, v[104:107] offset:0\n\t"
        "ds_write_b128 v117, v[108:111] offset:1024\n\t"
        "s_waitcnt vmcnt(16)\n\t"
        "ds_write_b128 v97, v[0:3] offset:0\n\t"
        "ds_write_b128 v98, v[4:7] offset:1024\n\t"
        "ds_write_b128 v97, v[8:11] offset:2048\n\t"
        "ds_write_b128 v98, v[12:15] offset:3072\n\t"
        "ds_write_b128 v97, v[16:19] offset:4096\n\t"
        "ds_write_b128 v98, v[20:23] offset:5120\n\t"
        "ds_write_b128 v97, v[24:27] offset:6144\n\t"
        "ds_write_b128 v98, v[28:31] offset:7168\n\t"
        "ds_read_b64_tr_b16 a[24:25], v99 offset:0\n\t"
        "ds_read_b64_tr_b16 a[26:27], v99 offset:256\n\t"
        "ds_read_b64_tr_b16 a[28:29], v99 offset:4096\n\t"
        "ds_read_b64_tr_b16 a[30:31], v99 offset:4352\n\t"
        "s_waitcnt lgkmcnt(8)\n\t"
        "ds_read_b64_tr_b16 a[32:33], v100 offset:0\n\t"
        "ds_read_b64_tr_b16 a[34:35], v100 offset:256\n\t"
        "ds_read_b64_tr_b16 a[36:37], v100 offset:4096\n\t"
        "ds_read_b64_tr_b16 a[38:39], v100 offset:4352\n\t"
        "s_waitcnt lgkmcnt(8)\n\t"
        "ds_read_b64_tr_b16 a[40:41], v101 offset:0\n\t"
        "ds_read_b64_tr_b16 a[42:43], v101 offset:256\n\t"
        "ds_read_b64_tr_b16 a[44:45], v101 offset:4096\n\t"
        "ds_read_b64_tr_b16 a[46:47], v101 offset:4352\n\t"
        "s_waitcnt lgkmcnt(8)\n\t"
        "ds_read_b64_tr_b16 v[104:105], v102 offset:0\n\t"
        "ds_read_b64_tr_b16 v[106:107], v102 offset:256\n\t"
        "ds_read_b64_tr_b16 v[108:109], v102 offset:4096\n\t"
        "ds_read_b64_tr_b16 v[110:111], v102 offset:4352\n\t"
        "s_lshl_b32 s34, s43, 2\n\t"
        "s_add_u32 s34, s34, 4\n\t"
        "s_and_b32 s34, s34, 15\n\t"
        "s_lshl_b32 s34, s34, 19\n\t"
        "buffer_load_dwordx4 a[96:99], v96, s[16:19], s34 offen nt\n\t"
        "s_add_u32 s34, s34, 0x10000\n\t"
        "buffer_load_dwordx4 a[100:103], v96, s[16:19], s34 offen nt\n\t"
        "s_add_u32 s34, s34, 0x10000\n\t"
        "buffer_load_dwordx4 a[104:107], v96, s[16:19], s34 offen nt\n\t"
        "s_add_u32 s34, s34, 0x10000\n\t"
        "buffer_load_dwordx4 a[108:111], v96, s[16:19], s34 offen nt\n\t"
        "s_add_u32 s34, s34, 0x10000\n\t"
        "buffer_load_dwordx4 a[112:115], v96, s[16:19], s34 offen nt\n\t"
        "s_add_u32 s34, s34, 0x10000\n\t"
        "buffer_load_dwordx4 a[116:119], v96, s[16:19], s34 offen nt\n\t"
        "s_add_u32 s34, s34, 0x10000\n\t"
        "buffer_load_dwordx4 a[120:123], v96, s[16:19], s34 offen nt\n\t"
        "s_add_u32 s34, s34, 0x10000\n\t"
        "buffer_load_dwordx4 a[124:127], v96, s[16:19], s34 offen nt\n\t"
        "s_waitcnt lgkmcnt(0)\n\t"
        "ds_write_b128 v117, v[104:107] offset:2048\n\t"
        "ds_write_b128 v117, v[108:111] offset:3072\n\t"
        "s_waitcnt vmcnt(16)\n\t"
        "ds_write_b128 v97, v[32:35] offset:0\n\t"
        "ds_write_b128 v98, v[36:39] offset:1024\n\t"
        "ds_write_b128 v97, v[40:43] offset:2048\n\t"
        "ds_write_b128 v98, v[44:47] offset:3072\n\t"
        "ds_write_b128 v97, v[48:51] offset:4096\n\t"
        "ds_write_b128 v98, v[52:55] offset:5120\n\t"
        "ds_write_b128 v97, v[56:59] offset:6144\n\t"
        "ds_write_b128 v98, v[60:63] offset:7168\n\t"
        "ds_read_b64_tr_b16 a[48:49], v99 offset:0\n\t"
        "ds_read_b64_tr_b16 a[50:51], v99 offset:256\n\t"
        "ds_read_b64_tr_b16 a[52:53], v99 offset:4096\n\t"
        "ds_read_b64_tr_b16 a[54:55], v99 offset:4352\n\t"
        "s_waitcnt lgkmcnt(8)\n\t"
        "ds_read_b64_tr_b16 a[56:57], v100 offset:0\n\t"
        "ds_read_b64_tr_b16 a[58:59], v100 offset:256\n\t"
        "ds_read_b64_tr_b16 a[60:61], v100 offset:4096\n\t"
        "ds_read_b64_tr_b16 a[62:63], v100 offset:4352\n\t"
        "s_waitcnt lgkmcnt(8)\n\t"
        "ds_read_b64_tr_b16 a[64:65], v101 offset:0\n\t"
        "ds_read_b64_tr_b16 a[66:67], v101 offset:256\n\t"
        "ds_read_b64_tr_b16 a[68:69], v101 offset:4096\n\t"
        "ds_read_b64_tr_b16 a[70:71], v101 offset:4352\n\t"
        "s_waitcnt lgkmcnt(8)\n\t"
        "ds_read_b64_tr_b16 v[104:105], v102 offset:0\n\t"
        "ds_read_b64_tr_b16 v[106:107], v102 offset:256\n\t"
        "ds_read_b64_tr_b16 v[108:109], v102 offset:4096\n\t"
        "ds_read_b64_tr_b16 v[110:111], v102 offset:4352\n\t"
        "s_lshl_b32 s34, s43, 2\n\t"
        "s_add_u32 s34, s34, 5\n\t"
        "s_and_b32 s34, s34, 15\n\t"
        "s_lshl_b32 s34, s34, 19\n\t"
        "buffer_load_dwordx4 a[128:131], v96, s[16:19], s34 offen nt\n\t"
        "s_add_u32 s34, s34, 0x10000\n\t"
        "buffer_load_dwordx4 a[132:135], v96, s[16:19], s34 offen nt\n\t"
        "s_add_u32 s34, s34, 0x10000\n\t"
        "buffer_load_dwordx4 a[136:139], v96, s[16:19], s34 offen nt\n\t"
        "s_add_u32 s34, s34, 0x10000\n\t"
        "buffer_load_dwordx4 a[140:143], v96, s[16:19], s34 offen nt\n\t"
        "s_add_u32 s34, s34, 0x10000\n\t"
        "buffer_load_dwordx4 a[144:147], v96, s[16:19], s34 offen nt\n\t"
        "s_add_u32 s34, s34, 0x10000\n\t"
        "buffer_load_dwordx4 a[148:151], v96, s[16:19], s34 offen nt\n\t"
        "s_add_u32 s34, s34, 0x10000\n\t"
        "buffer_load_dwordx4 a[152:155], v96, s[16:19], s34 offen nt\n\t"
        "s_add_u32 s34, s34, 0x10000\n\t"
        "buffer_load_dwordx4 a[156:159], v96, s[16:19], s34 offen nt\n\t"
        "s_waitcnt lgkmcnt(0)\n\t"
        "ds_write_b128 v117, v[104:107] offset:4096\n\t"
        "ds_write_b128 v117, v[108:111] offset:5120\n\t"
        "s_waitcnt vmcnt(16)\n\t"
        "ds_write_b128 v97, v[64:67] offset:0\n\t"
        "ds_write_b128 v98, v[68:71] offset:1024\n\t"
        "ds_write_b128 v97, v[72:75] offset:2048\n\t"
        "ds_write_b128 v98, v[76:79] offset:3072\n\t"
        "ds_write_b128 v97, v[80:83] offset:4096\n\t"
        "ds_write_b128 v98, v[84:87] offset:5120\n\t"
        "ds_write_b128 v97, v[88:91] offset:6144\n\t"
        "ds_write_b128 v98, v[92:95] offset:7168\n\t"
        "ds_read_b64_tr_b16 a[72:73], v99 offset:0\n\t"
        "ds_read_b64_tr_b16 a[74:75], v99 offset:256\n\t"
        "ds_read_b64_tr_b16 a[76:77], v99 offset:4096\n\t"
        "ds_read_b64_tr_b16 a[78:79], v99 offset:4352\n\t"
        "s_waitcnt lgkmcnt(8)\n\t"
        "ds_read_b64_tr_b16 a[80:81], v100 offset:0\n\t"
        "ds_read_b64_tr_b16 a[82:83], v100 offset:256\n\t"
        "ds_read_b64_tr_b16 a[84:85], v100 offset:4096\n\t"
        "ds_read_b64_tr_b16 a[86:87], v100 offset:4352\n\t"
        "s_waitcnt lgkmcnt(8)\n\t"
        "ds_read_b64_tr_b16 a[88:89], v101 offset:0\n\t"
        "ds_read_b64_tr_b16 a[90:91], v101 offset:256\n\t"
        "ds_read_b64_tr_b16 a[92:93], v101 offset:4096\n\t"
        "ds_read_b64_tr_b16 a[94:95], v101 offset:4352\n\t"
        "s_waitcnt lgkmcnt(8)\n\t"
        "ds_read_b64_tr_b16 v[104:105], v102 offset:0\n\t"
        "ds_read_b64_tr_b16 v[106:107], v102 offset:256\n\t"
        "ds_read_b64_tr_b16 v[108:109], v102 offset:4096\n\t"
        "ds_read_b64_tr_b16 v[110:111], v102 offset:4352\n\t"
        "s_lshl_b32 s34, s43, 2\n\t"
        "s_add_u32 s34, s34, 6\n\t"
        "s_and_b32 s34, s34, 15\n\t"
        "s_lshl_b32 s34, s34, 19\n\t"
        "buffer_load_dwordx4 a[160:163], v96, s[16:19], s34 offen nt\n\t"
        "s_add_u32 s34, s34, 0x10000\n\t"
        "buffer_load_dwordx4 a[164:167], v96, s[16:19], s34 offen nt\n\t"
        "s_add_u32 s34, s34, 0x10000\n\t"
        "buffer_load_dwordx4 a[168:171], v96, s[16:19], s34 offen nt\n\t"
        "s_add_u32 s34, s34, 0x10000\n\t"
        "buffer_load_dwordx4 a[172:175], v96, s[16:19], s34 offen nt\n\t"
        "s_add_u32 s34, s34, 0x10000\n\t"
        "buffer_load_dwordx4 a[176:179], v96, s[16:19], s34 offen nt\n\t"
        "s_add_u32 s34, s34, 0x10000\n\t"
        "buffer_load_dwordx4 a[180:183], v96, s[16:19], s34 offen nt\n\t"
        "s_add_u32 s34, s34, 0x10000\n\t"
        "buffer_load_dwordx4 a[184:187], v96, s[16:19], s34 offen nt\n\t"
        "s_add_u32 s34, s34, 0x10000\n\t"
        "buffer_load_dwordx4 a[188:191], v96, s[16:19], s34 offen nt\n\t"
        "s_waitcnt lgkmcnt(0)\n\t"
        "ds_write_b128 v117, v[104:107] offset:6144\n\t"
        "ds_write_b128 v117, v[108:111] offset:7168\n\t"
        "s_waitcnt vmcnt(16)\n\t"
        "ds_write_b128 v97, a[96:99] offset:0\n\t"
        "ds_write_b128 v98, a[100:103] offset:1024\n\t"
        "ds_write_b128 v97, a[104:107] offset:2048\n\t"
        "ds_write_b128 v98, a[108:111] offset:3072\n\t"
        "ds_write_b128 v97, a[112:115] offset:4096\n\t"
        "ds_write_b128 v98, a[116:119] offset:5120\n\t"
        "ds_write_b128 v97, a[120:123] offset:6144\n\t"
        "ds_write_b128 v98, a[124:127] offset:7168\n\t"
        "ds_read_b64_tr_b16 a[96:97], v99 offset:0\n\t"
        "ds_read_b64_tr_b16 a[98:99], v99 offset:256\n\t"
        "ds_read_b64_tr_b16 a[100:101], v99 offset:4096\n\t"
        "ds_read_b64_tr_b16 a[102:103], v99 offset:4352\n\t"
        "s_waitcnt lgkmcnt(8)\n\t"
        "ds_read_b64_tr_b16 a[104:105], v100 offset:0\n\t"
        "ds_read_b64_tr_b16 a[106:107], v100 offset:256\n\t"
        "ds_read_b64_tr_b16 a[108:109], v100 offset:4096\n\t"
        "ds_read_b64_tr_b16 a[110:111], v100 offset:4352\n\t"
        "s_waitcnt lgkmcnt(8)\n\t"
        "ds_read_b64_tr_b16 a[112:113], v101 offset:0\n\t"
        "ds_read_b64_tr_b16 a[114:115], v101 offset:256\n\t"
        "ds_read_b64_tr_b16 a[116:117], v101 offset:4096\n\t"
        "ds_read_b64_tr_b16 a[118:119], v101 offset:4352\n\t"
        "s_waitcnt lgkmcnt(8)\n\t"
        "ds_read_b64_tr_b16 v[104:105], v102 offset:0\n\t"
        "ds_read_b64_tr_b16 v[106:107], v102 offset:256\n\t"
        "ds_read_b64_tr_b16 v[108:109], v102 offset:4096\n\t"
        "ds_read_b64_tr_b16 v[110:111], v102 offset:4352\n\t"
        "s_lshl_b32 s34, s43, 2\n\t"
        "s_add_u32 s34, s34, 7\n\t"
        "s_and_b32 s34, s34, 15\n\t"
        "s_lshl_b32 s34, s34, 19\n\t"
        "buffer_load_dwordx4 a[192:195], v96, s[16:19], s34 offen nt\n\t"
        "s_add_u32 s34, s34, 0x10000\n\t"
        "buffer_load_dwordx4 a[196:199], v96, s[16:19], s34 offen nt\n\t"
        "s_add_u32 s34, s34, 0x10000\n\t"
        "buffer_load_dwordx4 a[200:203], v96, s[16:19], s34 offen nt\n\t"
        "s_add_u32 s34, s34, 0x10000\n\t"
        "buffer_load_dwordx4 a[204:207], v96, s[16:19], s34 offen nt\n\t"
        "s_add_u32 s34, s34, 0x10000\n\t"
        "buffer_load_dwordx4 a[208:211], v96, s[16:19], s34 offen nt\n\t"
        "s_add_u32 s34, s34, 0x10000\n\t"
        "buffer_load_dwordx4 a[212:215], v96, s[16:19], s34 offen nt\n\t"
        "s_add_u32 s34, s34, 0x10000\n\t"
        "buffer_load_dwordx4 a[216:219], v96, s[16:19], s34 offen nt\n\t"
        "s_add_u32 s34, s34, 0x10000\n\t"
        "buffer_load_dwordx4 a[220:223], v96, s[16:19], s34 offen nt\n\t"
        "s_waitcnt lgkmcnt(0)\n\t"
        "ds_write_b128 v126, v[104:107] offset:0\n\t"
        "ds_write_b128 v126, v[108:111] offset:1024\n\t"
        "s_waitcnt vmcnt(16)\n\t"
        "ds_write_b128 v97, a[128:131] offset:0\n\t"
        "ds_write_b128 v98, a[132:135] offset:1024\n\t"
        "ds_write_b128 v97, a[136:139] offset:2048\n\t"
        "ds_write_b128 v98, a[140:143] offset:3072\n\t"
        "ds_write_b128 v97, a[144:147] offset:4096\n\t"
        "ds_write_b128 v98, a[148:151] offset:5120\n\t"
        "ds_write_b128 v97, a[152:155] offset:6144\n\t"
        "ds_write_b128 v98, a[156:159] offset:7168\n\t"
        "ds_read_b64_tr_b16 a[120:121], v99 offset:0\n\t"
        "ds_read_b64_tr_b16 a[122:123], v99 offset:256\n\t"
        "ds_read_b64_tr_b16 a[124:125], v99 offset:4096\n\t"
        "ds_read_b64_tr_b16 a[126:127], v99 offset:4352\n\t"
        "s_waitcnt lgkmcnt(8)\n\t"
        "ds_read_b64_tr_b16 a[128:129], v100 offset:0\n\t"
        "ds_read_b64_tr_b16 a[130:131], v100 offset:256\n\t"
        "ds_read_b64_tr_b16 a[132:133], v100 offset:4096\n\t"
        "ds_read_b64_tr_b16 a[134:135], v100 offset:4352\n\t"
        "s_waitcnt lgkmcnt(8)\n\t"
        "ds_read_b64_tr_b16 a[136:137], v101 offset:0\n\t"
        "ds_read_b64_tr_b16 a[138:139], v101 offset:256\n\t"
        "ds_read_b64_tr_b16 a[140:141], v101 offset:4096\n\t"
        "ds_read_b64_tr_b16 a[142:143], v101 offset:4352\n\t"
        "s_waitcnt lgkmcnt(8)\n\t"
        "ds_read_b64_tr_b16 v[104:105], v102 offset:0\n\t"
        "ds_read_b64_tr_b16 v[106:107], v102 offset:256\n\t"
        "ds_read_b64_tr_b16 v[108:109], v102 offset:4096\n\t"
        "ds_read_b64_tr_b16 v[110:111], v102 offset:4352\n\t"
        "s_lshl_b32 s34, s43, 2\n\t"
        "s_add_u32 s34, s34, 8\n\t"
        "s_and_b32 s34, s34, 15\n\t"
        "s_lshl_b32 s34, s34, 19\n\t"
        "buffer_load_dwordx4 a[224:227], v96, s[16:19], s34 offen nt\n\t"
        "s_add_u32 s34, s34, 0x10000\n\t"
        "buffer_load_dwordx4 a[228:231], v96, s[16:19], s34 offen nt\n\t"
        "s_add_u32 s34, s34, 0x10000\n\t"
        "buffer_load_dwordx4 a[232:235], v96, s[16:19], s34 offen nt\n\t"
        "s_add_u32 s34, s34, 0x10000\n\t"
        "buffer_load_dwordx4 a[236:239], v96, s[16:19], s34 offen nt\n\t"
        "s_add_u32 s34, s34, 0x10000\n\t"
        "buffer_load_dwordx4 a[240:243], v96, s[16:19], s34 offen nt\n\t"
        "s_add_u32 s34, s34, 0x10000\n\t"
        "buffer_load_dwordx4 a[244:247], v96, s[16:19], s34 offen nt\n\t"
        "s_add_u32 s34, s34, 0x10000\n\t"
        "buffer_load_dwordx4 a[248:251], v96, s[16:19], s34 offen nt\n\t"
        "s_add_u32 s34, s34, 0x10000\n\t"
        "buffer_load_dwordx4 a[252:255], v96, s[16:19], s34 offen nt\n\t"
        "s_waitcnt lgkmcnt(0)\n\t"
        "ds_write_b128 v126, v[104:107] offset:2048\n\t"
        "ds_write_b128 v126, v[108:111] offset:3072\n\t"
        "s_waitcnt vmcnt(16)\n\t"
        "ds_write_b128 v97, a[160:163] offset:0\n\t"
        "ds_write_b128 v98, a[164:167] offset:1024\n\t"
        "ds_write_b128 v97, a[168:171] offset:2048\n\t"
        "ds_write_b128 v98, a[172:175] offset:3072\n\t"
        "ds_write_b128 v97, a[176:179] offset:4096\n\t"
        "ds_write_b128 v98, a[180:183] offset:5120\n\t"
        "ds_write_b128 v97, a[184:187] offset:6144\n\t"
        "ds_write_b128 v98, a[188:191] offset:7168\n\t"
        "ds_read_b64_tr_b16 a[144:145], v99 offset:0\n\t"
        "ds_read_b64_tr_b16 a[146:147], v99 offset:256\n\t"
        "ds_read_b64_tr_b16 a[148:149], v99 offset:4096\n\t"
        "ds_read_b64_tr_b16 a[150:151], v99 offset:4352\n\t"
        "s_waitcnt lgkmcnt(8)\n\t"
        "ds_read_b64_tr_b16 a[152:153], v100 offset:0\n\t"
        "ds_read_b64_tr_b16 a[154:155], v100 offset:256\n\t"
        "ds_read_b64_tr_b16 a[156:157], v100 offset:4096\n\t"
        "ds_read_b64_tr_b16 a[158:159], v100 offset:4352\n\t"
        "s_waitcnt lgkmcnt(8)\n\t"
        "ds_read_b64_tr_b16 a[160:161], v101 offset:0\n\t"
        "ds_read_b64_tr_b16 a[162:163], v101 offset:256\n\t"
        "ds_read_b64_tr_b16 a[164:165], v101 offset:4096\n\t"
        "ds_read_b64_tr_b16 a[166:167], v101 offset:4352\n\t"
        "s_waitcnt lgkmcnt(8)\n\t"
        "ds_read_b64_tr_b16 v[104:105], v102 offset:0\n\t"
        "ds_read_b64_tr_b16 v[106:107], v102 offset:256\n\t"
        "ds_read_b64_tr_b16 v[108:109], v102 offset:4096\n\t"
        "ds_read_b64_tr_b16 v[110:111], v102 offset:4352\n\t"
        "s_lshl_b32 s34, s43, 2\n\t"
        "s_add_u32 s34, s34, 9\n\t"
        "s_and_b32 s34, s34, 15\n\t"
        "s_lshl_b32 s34, s34, 19\n\t"
        "buffer_load_dwordx4 v[128:131], v96, s[16:19], s34 offen nt\n\t"
        "s_add_u32 s34, s34, 0x10000\n\t"
        "buffer_load_dwordx4 v[132:135], v96, s[16:19], s34 offen nt\n\t"
        "s_add_u32 s34, s34, 0x10000\n\t"
        "buffer_load_dwordx4 v[136:139], v96, s[16:19], s34 offen nt\n\t"
        "s_add_u32 s34, s34, 0x10000\n\t"
        "buffer_load_dwordx4 v[140:143], v96, s[16:19], s34 offen nt\n\t"
        "s_add_u32 s34, s34, 0x10000\n\t"
        "buffer_load_dwordx4 v[144:147], v96, s[16:19], s34 offen nt\n\t"
        "s_add_u32 s34, s34, 0x10000\n\t"
        "buffer_load_dwordx4 v[148:151], v96, s[16:19], s34 offen nt\n\t"
        "s_add_u32 s34, s34, 0x10000\n\t"
        "buffer_load_dwordx4 v[152:155], v96, s[16:19], s34 offen nt\n\t"
        "s_add_u32 s34, s34, 0x10000\n\t"
        "buffer_load_dwordx4 v[156:159], v96, s[16:19], s34 offen nt\n\t"
        "s_waitcnt lgkmcnt(0)\n\t"
        "ds_write_b128 v126, v[104:107] offset:4096\n\t"
        "ds_write_b128 v126, v[108:111] offset:5120\n\t"
        "s_waitcnt vmcnt(16)\n\t"
        "ds_write_b128 v97, a[192:195] offset:0\n\t"
        "ds_write_b128 v98, a[196:199] offset:1024\n\t"
        "ds_write_b128 v97, a[200:203] offset:2048\n\t"
        "ds_write_b128 v98, a[204:207] offset:3072\n\t"
        "ds_write_b128 v97, a[208:211] offset:4096\n\t"
        "ds_write_b128 v98, a[212:215] offset:5120\n\t"
        "ds_write_b128 v97, a[216:219] offset:6144\n\t"
        "ds_write_b128 v98, a[220:223] offset:7168\n\t"
        "ds_read_b64_tr_b16 a[168:169], v99 offset:0\n\t"
        "ds_read_b64_tr_b16 a[170:171], v99 offset:256\n\t"
        "ds_read_b64_tr_b16 a[172:173], v99 offset:4096\n\t"
        "ds_read_b64_tr_b16 a[174:175], v99 offset:4352\n\t"
        "s_waitcnt lgkmcnt(8)\n\t"
        "ds_read_b64_tr_b16 a[176:177], v100 offset:0\n\t"
        "ds_read_b64_tr_b16 a[178:179], v100 offset:256\n\t"
        "ds_read_b64_tr_b16 a[180:181], v100 offset:4096\n\t"
        "ds_read_b64_tr_b16 a[182:183], v100 offset:4352\n\t"
        "s_waitcnt lgkmcnt(8)\n\t"
        "ds_read_b64_tr_b16 a[184:185], v101 offset:0\n\t"
        "ds_read_b64_tr_b16 a[186:187], v101 offset:256\n\t"
        "ds_read_b64_tr_b16 a[188:189], v101 offset:4096\n\t"
        "ds_read_b64_tr_b16 a[190:191], v101 offset:4352\n\t"
        "s_waitcnt lgkmcnt(8)\n\t"
        "ds_read_b64_tr_b16 v[104:105], v102 offset:0\n\t"
        "ds_read_b64_tr_b16 v[106:107], v102 offset:256\n\t"
        "ds_read_b64_tr_b16 v[108:109], v102 offset:4096\n\t"
        "ds_read_b64_tr_b16 v[110:111], v102 offset:4352\n\t"
        "s_lshl_b32 s34, s43, 2\n\t"
        "s_add_u32 s34, s34, 10\n\t"
        "s_and_b32 s34, s34, 15\n\t"
        "s_lshl_b32 s34, s34, 19\n\t"
        "buffer_load_dwordx4 v[160:163], v96, s[16:19], s34 offen nt\n\t"
        "s_add_u32 s34, s34, 0x10000\n\t"
        "buffer_load_dwordx4 v[164:167], v96, s[16:19], s34 offen nt\n\t"
        "s_add_u32 s34, s34, 0x10000\n\t"
        "buffer_load_dwordx4 v[168:171], v96, s[16:19], s34 offen nt\n\t"
        "s_add_u32 s34, s34, 0x10000\n\t"
        "buffer_load_dwordx4 v[172:175], v96, s[16:19], s34 offen nt\n\t"
        "s_add_u32 s34, s34, 0x10000\n\t"
        "buffer_load_dwordx4 v[176:179], v96, s[16:19], s34 offen nt\n\t"
        "s_add_u32 s34, s34, 0x10000\n\t"
        "buffer_load_dwordx4 v[180:183], v96, s[16:19], s34 offen nt\n\t"
        "s_add_u32 s34, s34, 0x10000\n\t"
        "buffer_load_dwordx4 v[184:187], v96, s[16:19], s34 offen nt\n\t"
        "s_add_u32 s34, s34, 0x10000\n\t"
        "buffer_load_dwordx4 v[188:191], v96, s[16:19], s34 offen nt\n\t"
        "s_waitcnt lgkmcnt(0)\n\t"
        "ds_write_b128 v126, v[104:107] offset:6144\n\t"
        "ds_write_b128 v126, v[108:111] offset:7168\n\t"
        "s_waitcnt vmcnt(16)\n\t"
        "ds_write_b128 v97, a[224:227] offset:0\n\t"
        "ds_write_b128 v98, a[228:231] offset:1024\n\t"
        "ds_write_b128 v97, a[232:235] offset:2048\n\t"
        "ds_write_b128 v98, a[236:239] offset:3072\n\t"
        "ds_write_b128 v97, a[240:243] offset:4096\n\t"
        "ds_write_b128 v98, a[244:247] offset:5120\n\t"
        "ds_write_b128 v97, a[248:251] offset:6144\n\t"
        "ds_write_b128 v98, a[252:255] offset:7168\n\t"
        "ds_read_b64_tr_b16 a[192:193], v99 offset:0\n\t"
        "ds_read_b64_tr_b16 a[194:195], v99 offset:256\n\t"
        "ds_read_b64_tr_b16 a[196:197], v99 offset:4096\n\t"
        "ds_read_b64_tr_b16 a[198:199], v99 offset:4352\n\t"
        "s_waitcnt lgkmcnt(8)\n\t"
        "ds_read_b64_tr_b16 a[200:201], v100 offset:0\n\t"
        "ds_read_b64_tr_b16 a[202:203], v100 offset:256\n\t"
        "ds_read_b64_tr_b16 a[204:205], v100 offset:4096\n\t"
        "ds_read_b64_tr_b16 a[206:207], v100 offset:4352\n\t"
        "s_waitcnt lgkmcnt(8)\n\t"
        "ds_read_b64_tr_b16 a[208:209], v101 offset:0\n\t"
        "ds_read_b64_tr_b16 a[210:211], v101 offset:256\n\t"
        "ds_read_b64_tr_b16 a[212:213], v101 offset:4096\n\t"
        "ds_read_b64_tr_b16 a[214:215], v101 offset:4352\n\t"
        "s_waitcnt lgkmcnt(8)\n\t"
        "ds_read_b64_tr_b16 v[104:105], v102 offset:0\n\t"
        "ds_read_b64_tr_b16 v[106:107], v102 offset:256\n\t"
        "ds_read_b64_tr_b16 v[108:109], v102 offset:4096\n\t"
        "ds_read_b64_tr_b16 v[110:111], v102 offset:4352\n\t"
        "s_lshl_b32 s34, s43, 2\n\t"
        "s_add_u32 s34, s34, 11\n\t"
        "s_and_b32 s34, s34, 15\n\t"
        "s_lshl_b32 s34, s34, 19\n\t"
        "buffer_load_dwordx4 v[192:195], v96, s[16:19], s34 offen nt\n\t"
        "s_add_u32 s34, s34, 0x10000\n\t"
        "buffer_load_dwordx4 v[196:199], v96, s[16:19], s34 offen nt\n\t"
        "s_add_u32 s34, s34, 0x10000\n\t"
        "buffer_load_dwordx4 v[200:203], v96, s[16:19], s34 offen nt\n\t"
        "s_add_u32 s34, s34, 0x10000\n\t"
        "buffer_load_dwordx4 v[204:207], v96, s[16:19], s34 offen nt\n\t"
        "s_add_u32 s34, s34, 0x10000\n\t"
        "buffer_load_dwordx4 v[208:211], v96, s[16:19], s34 offen nt\n\t"
        "s_add_u32 s34, s34, 0x10000\n\t"
        "buffer_load_dwordx4 v[212:215], v96, s[16:19], s34 offen nt\n\t"
        "s_add_u32 s34, s34, 0x10000\n\t"
        "buffer_load_dwordx4 v[216:219], v96, s[16:19], s34 offen nt\n\t"
        "s_add_u32 s34, s34, 0x10000\n\t"
        "buffer_load_dwordx4 v[220:223], v96, s[16:19], s34 offen nt\n\t"
        "s_waitcnt lgkmcnt(0)\n\t"
        "ds_write_b128 v117, v[104:107] offset:8192\n\t"
        "ds_write_b128 v117, v[108:111] offset:9216\n\t"
        "s_waitcnt vmcnt(16)\n\t"
        "ds_write_b128 v97, v[128:131] offset:0\n\t"
        "ds_write_b128 v98, v[132:135] offset:1024\n\t"
        "ds_write_b128 v97, v[136:139] offset:2048\n\t"
        "ds_write_b128 v98, v[140:143] offset:3072\n\t"
        "ds_write_b128 v97, v[144:147] offset:4096\n\t"
        "ds_write_b128 v98, v[148:151] offset:5120\n\t"
        "ds_write_b128 v97, v[152:155] offset:6144\n\t"
        "ds_write_b128 v98, v[156:159] offset:7168\n\t"
        "ds_read_b64_tr_b16 a[216:217], v99 offset:0\n\t"
        "ds_read_b64_tr_b16 a[218:219], v99 offset:256\n\t"
        "ds_read_b64_tr_b16 a[220:221], v99 offset:4096\n\t"
        "ds_read_b64_tr_b16 a[222:223], v99 offset:4352\n\t"
        "s_waitcnt lgkmcnt(8)\n\t"
        "ds_read_b64_tr_b16 a[224:225], v100 offset:0\n\t"
        "ds_read_b64_tr_b16 a[226:227], v100 offset:256\n\t"
        "ds_read_b64_tr_b16 a[228:229], v100 offset:4096\n\t"
        "ds_read_b64_tr_b16 a[230:231], v100 offset:4352\n\t"
        "s_waitcnt lgkmcnt(8)\n\t"
        "ds_read_b64_tr_b16 a[232:233], v101 offset:0\n\t"
        "ds_read_b64_tr_b16 a[234:235], v101 offset:256\n\t"
        "ds_read_b64_tr_b16 a[236:237], v101 offset:4096\n\t"
        "ds_read_b64_tr_b16 a[238:239], v101 offset:4352\n\t"
        "s_waitcnt lgkmcnt(8)\n\t"
        "ds_read_b64_tr_b16 v[104:105], v102 offset:0\n\t"
        "ds_read_b64_tr_b16 v[106:107], v102 offset:256\n\t"
        "ds_read_b64_tr_b16 v[108:109], v102 offset:4096\n\t"
        "ds_read_b64_tr_b16 v[110:111], v102 offset:4352\n\t"
        "s_lshl_b32 s34, s43, 2\n\t"
        "s_add_u32 s34, s34, 12\n\t"
        "s_and_b32 s34, s34, 15\n\t"
        "s_lshl_b32 s34, s34, 19\n\t"
        "buffer_load_dwordx4 v[224:227], v96, s[16:19], s34 offen nt\n\t"
        "s_add_u32 s34, s34, 0x10000\n\t"
        "buffer_load_dwordx4 v[228:231], v96, s[16:19], s34 offen nt\n\t"
        "s_add_u32 s34, s34, 0x10000\n\t"
        "buffer_load_dwordx4 v[232:235], v96, s[16:19], s34 offen nt\n\t"
        "s_add_u32 s34, s34, 0x10000\n\t"
        "buffer_load_dwordx4 v[236:239], v96, s[16:19], s34 offen nt\n\t"
        "s_add_u32 s34, s34, 0x10000\n\t"
        "buffer_load_dwordx4 v[240:243], v96, s[16:19], s34 offen nt\n\t"
        "s_add_u32 s34, s34, 0x10000\n\t"
        "buffer_load_dwordx4 v[244:247], v96, s[16:19], s34 offen nt\n\t"
        "s_add_u32 s34, s34, 0x10000\n\t"
        "buffer_load_dwordx4 v[248:251], v96, s[16:19], s34 offen nt\n\t"
        "s_add_u32 s34, s34, 0x10000\n\t"
        "buffer_load_dwordx4 v[252:255], v96, s[16:19], s34 offen nt\n\t"
        "s_waitcnt lgkmcnt(0)\n\t"
        "ds_write_b128 v117, v[104:107] offset:10240\n\t"
        "ds_write_b128 v117, v[108:111] offset:11264\n\t"
        "s_waitcnt vmcnt(16)\n\t"
        "ds_write_b128 v97, v[160:163] offset:0\n\t"
        "ds_write_b128 v98, v[164:167] offset:1024\n\t"
        "ds_write_b128 v97, v[168:171] offset:2048\n\t"
        "ds_write_b128 v98, v[172:175] offset:3072\n\t"
        "ds_write_b128 v97, v[176:179] offset:4096\n\t"
        "ds_write_b128 v98, v[180:183] offset:5120\n\t"
        "ds_write_b128 v97, v[184:187] offset:6144\n\t"
        "ds_write_b128 v98, v[188:191] offset:7168\n\t"
        "ds_read_b64_tr_b16 a[240:241], v99 offset:0\n\t"
        "ds_read_b64_tr_b16 a[242:243], v99 offset:256\n\t"
        "ds_read_b64_tr_b16 a[244:245], v99 offset:4096\n\t"
        "ds_read_b64_tr_b16 a[246:247], v99 offset:4352\n\t"
        "s_waitcnt lgkmcnt(8)\n\t"
        "ds_read_b64_tr_b16 a[248:249], v100 offset:0\n\t"
        "ds_read_b64_tr_b16 a[250:251], v100 offset:256\n\t"
        "ds_read_b64_tr_b16 a[252:253], v100 offset:4096\n\t"
        "ds_read_b64_tr_b16 a[254:255], v100 offset:4352\n\t"
        "s_waitcnt lgkmcnt(8)\n\t"
        "ds_read_b64_tr_b16 v[128:129], v101 offset:0\n\t"
        "ds_read_b64_tr_b16 v[130:131], v101 offset:256\n\t"
        "ds_read_b64_tr_b16 v[132:133], v101 offset:4096\n\t"
        "ds_read_b64_tr_b16 v[134:135], v101 offset:4352\n\t"
        "s_waitcnt lgkmcnt(8)\n\t"
        "ds_read_b64_tr_b16 v[104:105], v102 offset:0\n\t"
        "ds_read_b64_tr_b16 v[106:107], v102 offset:256\n\t"
        "ds_read_b64_tr_b16 v[108:109], v102 offset:4096\n\t"
        "ds_read_b64_tr_b16 v[110:111], v102 offset:4352\n\t"
        "s_lshl_b32 s34, s43, 2\n\t"
        "s_add_u32 s34, s34, 13\n\t"
        "s_and_b32 s34, s34, 15\n\t"
        "s_lshl_b32 s34, s34, 19\n\t"
        "buffer_load_dwordx4 v[0:3], v96, s[16:19], s34 offen nt\n\t"
        "s_add_u32 s34, s34, 0x10000\n\t"
        "buffer_load_dwordx4 v[4:7], v96, s[16:19], s34 offen nt\n\t"
        "s_add_u32 s34, s34, 0x10000\n\t"
        "buffer_load_dwordx4 v[8:11], v96, s[16:19], s34 offen nt\n\t"
        "s_add_u32 s34, s34, 0x10000\n\t"
        "buffer_load_dwordx4 v[12:15], v96, s[16:19], s34 offen nt\n\t"
        "s_add_u32 s34, s34, 0x10000\n\t"
        "buffer_load_dwordx4 v[16:19], v96, s[16:19], s34 offen nt\n\t"
        "s_add_u32 s34, s34, 0x10000\n\t"
        "buffer_load_dwordx4 v[20:23], v96, s[16:19], s34 offen nt\n\t"
        "s_add_u32 s34, s34, 0x10000\n\t"
        "buffer_load_dwordx4 v[24:27], v96, s[16:19], s34 offen nt\n\t"
        "s_add_u32 s34, s34, 0x10000\n\t"
        "buffer_load_dwordx4 v[28:31], v96, s[16:19], s34 offen nt\n\t"
        "s_waitcnt lgkmcnt(0)\n\t"
        "ds_write_b128 v117, v[104:107] offset:12288\n\t"
        "ds_write_b128 v117, v[108:111] offset:13312\n\t"
        "s_waitcnt vmcnt(16)\n\t"
        "ds_write_b128 v97, v[192:195] offset:0\n\t"
        "ds_write_b128 v98, v[196:199] offset:1024\n\t"
        "ds_write_b128 v97, v[200:203] offset:2048\n\t"
        "ds_write_b128 v98, v[204:207] offset:3072\n\t"
        "ds_write_b128 v97, v[208:211] offset:4096\n\t"
        "ds_write_b128 v98, v[212:215] offset:5120\n\t"
        "ds_write_b128 v97, v[216:219] offset:6144\n\t"
        "ds_write_b128 v98, v[220:223] offset:7168\n\t"
        "ds_read_b64_tr_b16 v[136:137], v99 offset:0\n\t"
        "ds_read_b64_tr_b16 v[138:139], v99 offset:256\n\t"
        "ds_read_b64_tr_b16 v[140:141], v99 offset:4096\n\t"
        "ds_read_b64_tr_b16 v[142:143], v99 offset:4352\n\t"
        "s_waitcnt lgkmcnt(8)\n\t"
        "ds_read_b64_tr_b16 v[144:145], v100 offset:0\n\t"
        "ds_read_b64_tr_b16 v[146:147], v100 offset:256\n\t"
        "ds_read_b64_tr_b16 v[148:149], v100 offset:4096\n\t"
        "ds_read_b64_tr_b16 v[150:151], v100 offset:4352\n\t"
        "s_waitcnt lgkmcnt(8)\n\t"
        "ds_read_b64_tr_b16 v[152:153], v101 offset:0\n\t"
        "ds_read_b64_tr_b16 v[154:155], v101 offset:256\n\t"
        "ds_read_b64_tr_b16 v[156:157], v101 offset:4096\n\t"
        "ds_read_b64_tr_b16 v[158:159], v101 offset:4352\n\t"
        "s_waitcnt lgkmcnt(8)\n\t"
        "ds_read_b64_tr_b16 v[104:105], v102 offset:0\n\t"
        "ds_read_b64_tr_b16 v[106:107], v102 offset:256\n\t"
        "ds_read_b64_tr_b16 v[108:109], v102 offset:4096\n\t"
        "ds_read_b64_tr_b16 v[110:111], v102 offset:4352\n\t"
        "s_lshl_b32 s34, s43, 2\n\t"
        "s_add_u32 s34, s34, 14\n\t"
        "s_and_b32 s34, s34, 15\n\t"
        "s_lshl_b32 s34, s34, 19\n\t"
        "buffer_load_dwordx4 v[32:35], v96, s[16:19], s34 offen nt\n\t"
        "s_add_u32 s34, s34, 0x10000\n\t"
        "buffer_load_dwordx4 v[36:39], v96, s[16:19], s34 offen nt\n\t"
        "s_add_u32 s34, s34, 0x10000\n\t"
        "buffer_load_dwordx4 v[40:43], v96, s[16:19], s34 offen nt\n\t"
        "s_add_u32 s34, s34, 0x10000\n\t"
        "buffer_load_dwordx4 v[44:47], v96, s[16:19], s34 offen nt\n\t"
        "s_add_u32 s34, s34, 0x10000\n\t"
        "buffer_load_dwordx4 v[48:51], v96, s[16:19], s34 offen nt\n\t"
        "s_add_u32 s34, s34, 0x10000\n\t"
        "buffer_load_dwordx4 v[52:55], v96, s[16:19], s34 offen nt\n\t"
        "s_add_u32 s34, s34, 0x10000\n\t"
        "buffer_load_dwordx4 v[56:59], v96, s[16:19], s34 offen nt\n\t"
        "s_add_u32 s34, s34, 0x10000\n\t"
        "buffer_load_dwordx4 v[60:63], v96, s[16:19], s34 offen nt\n\t"
        "s_waitcnt lgkmcnt(0)\n\t"
        "ds_write_b128 v117, v[104:107] offset:14336\n\t"
        "ds_write_b128 v117, v[108:111] offset:15360\n\t"
        "s_waitcnt vmcnt(16)\n\t"
        "ds_write_b128 v97, v[224:227] offset:0\n\t"
        "ds_write_b128 v98, v[228:231] offset:1024\n\t"
        "ds_write_b128 v97, v[232:235] offset:2048\n\t"
        "ds_write_b128 v98, v[236:239] offset:3072\n\t"
        "ds_write_b128 v97, v[240:243] offset:4096\n\t"
        "ds_write_b128 v98, v[244:247] offset:5120\n\t"
        "ds_write_b128 v97, v[248:251] offset:6144\n\t"
        "ds_write_b128 v98, v[252:255] offset:7168\n\t"
        "ds_read_b64_tr_b16 v[160:161], v99 offset:0\n\t"
        "ds_read_b64_tr_b16 v[162:163], v99 offset:256\n\t"
        "ds_read_b64_tr_b16 v[164:165], v99 offset:4096\n\t"
        "ds_read_b64_tr_b16 v[166:167], v99 offset:4352\n\t"
        "s_waitcnt lgkmcnt(8)\n\t"
        "ds_read_b64_tr_b16 v[168:169], v100 offset:0\n\t"
        "ds_read_b64_tr_b16 v[170:171], v100 offset:256\n\t"
        "ds_read_b64_tr_b16 v[172:173], v100 offset:4096\n\t"
        "ds_read_b64_tr_b16 v[174:175], v100 offset:4352\n\t"
        "s_waitcnt lgkmcnt(8)\n\t"
        "ds_read_b64_tr_b16 v[176:177], v101 offset:0\n\t"
        "ds_read_b64_tr_b16 v[178:179], v101 offset:256\n\t"
        "ds_read_b64_tr_b16 v[180:181], v101 offset:4096\n\t"
        "ds_read_b64_tr_b16 v[182:183], v101 offset:4352\n\t"
        "s_waitcnt lgkmcnt(8)\n\t"
        "ds_read_b64_tr_b16 v[104:105], v102 offset:0\n\t"
        "ds_read_b64_tr_b16 v[106:107], v102 offset:256\n\t"
        "ds_read_b64_tr_b16 v[108:109], v102 offset:4096\n\t"
        "ds_read_b64_tr_b16 v[110:111], v102 offset:4352\n\t"
        "s_lshl_b32 s34, s43, 2\n\t"
        "s_add_u32 s34, s34, 15\n\t"
        "s_and_b32 s34, s34, 15\n\t"
        "s_lshl_b32 s34, s34, 19\n\t"
        "buffer_load_dwordx4 v[64:67], v96, s[16:19], s34 offen nt\n\t"
        "s_add_u32 s34, s34, 0x10000\n\t"
        "buffer_load_dwordx4 v[68:71], v96, s[16:19], s34 offen nt\n\t"
        "s_add_u32 s34, s34, 0x10000\n\t"
        "buffer_load_dwordx4 v[72:75], v96, s[16:19], s34 offen nt\n\t"
        "s_add_u32 s34, s34, 0x10000\n\t"
        "buffer_load_dwordx4 v[76:79], v96, s[16:19], s34 offen nt\n\t"
        "s_add_u32 s34, s34, 0x10000\n\t"
        "buffer_load_dwordx4 v[80:83], v96, s[16:19], s34 offen nt\n\t"
        "s_add_u32 s34, s34, 0x10000\n\t"
        "buffer_load_dwordx4 v[84:87], v96, s[16:19], s34 offen nt\n\t"
        "s_add_u32 s34, s34, 0x10000\n\t"
        "buffer_load_dwordx4 v[88:91], v96, s[16:19], s34 offen nt\n\t"
        "s_add_u32 s34, s34, 0x10000\n\t"
        "buffer_load_dwordx4 v[92:95], v96, s[16:19], s34 offen nt\n\t"
        "s_waitcnt lgkmcnt(0)\n\t"
        "ds_write_b128 v117, v[104:107] offset:16384\n\t"
        "ds_write_b128 v117, v[108:111] offset:17408\n\t"
        "s_waitcnt vmcnt(16)\n\t"
        "ds_write_b128 v97, v[0:3] offset:0\n\t"
        "ds_write_b128 v98, v[4:7] offset:1024\n\t"
        "ds_write_b128 v97, v[8:11] offset:2048\n\t"
        "ds_write_b128 v98, v[12:15] offset:3072\n\t"
        "ds_write_b128 v97, v[16:19] offset:4096\n\t"
        "ds_write_b128 v98, v[20:23] offset:5120\n\t"
        "ds_write_b128 v97, v[24:27] offset:6144\n\t"
        "ds_write_b128 v98, v[28:31] offset:7168\n\t"
        "ds_read_b64_tr_b16 v[184:185], v99 offset:0\n\t"
        "ds_read_b64_tr_b16 v[186:187], v99 offset:256\n\t"
        "ds_read_b64_tr_b16 v[188:189], v99 offset:4096\n\t"
        "ds_read_b64_tr_b16 v[190:191], v99 offset:4352\n\t"
        "s_waitcnt lgkmcnt(8)\n\t"
        "ds_read_b64_tr_b16 v[192:193], v100 offset:0\n\t"
        "ds_read_b64_tr_b16 v[194:195], v100 offset:256\n\t"
        "ds_read_b64_tr_b16 v[196:197], v100 offset:4096\n\t"
        "ds_read_b64_tr_b16 v[198:199], v100 offset:4352\n\t"
        "s_waitcnt lgkmcnt(8)\n\t"
        "ds_read_b64_tr_b16 v[200:201], v101 offset:0\n\t"
        "ds_read_b64_tr_b16 v[202:203], v101 offset:256\n\t"
        "ds_read_b64_tr_b16 v[204:205], v101 offset:4096\n\t"
        "ds_read_b64_tr_b16 v[206:207], v101 offset:4352\n\t"
        "s_waitcnt lgkmcnt(8)\n\t"
        "ds_read_b64_tr_b16 v[104:105], v102 offset:0\n\t"
        "ds_read_b64_tr_b16 v[106:107], v102 offset:256\n\t"
        "ds_read_b64_tr_b16 v[108:109], v102 offset:4096\n\t"
        "ds_read_b64_tr_b16 v[110:111], v102 offset:4352\n\t"
        "s_waitcnt lgkmcnt(0)\n\t"
        "ds_write_b128 v117, v[104:107] offset:18432\n\t"
        "ds_write_b128 v117, v[108:111] offset:19456\n\t"
        "s_waitcnt vmcnt(8)\n\t"
        "ds_write_b128 v97, v[32:35] offset:0\n\t"
        "ds_write_b128 v98, v[36:39] offset:1024\n\t"
        "ds_write_b128 v97, v[40:43] offset:2048\n\t"
        "ds_write_b128 v98, v[44:47] offset:3072\n\t"
        "ds_write_b128 v97, v[48:51] offset:4096\n\t"
        "ds_write_b128 v98, v[52:55] offset:5120\n\t"
        "ds_write_b128 v97, v[56:59] offset:6144\n\t"
        "ds_write_b128 v98, v[60:63] offset:7168\n\t"
        "ds_read_b64_tr_b16 v[208:209], v99 offset:0\n\t"
        "ds_read_b64_tr_b16 v[210:211], v99 offset:256\n\t"
        "ds_read_b64_tr_b16 v[212:213], v99 offset:4096\n\t"
        "ds_read_b64_tr_b16 v[214:215], v99 offset:4352\n\t"
        "s_waitcnt lgkmcnt(8)\n\t"
        "ds_read_b64_tr_b16 v[216:217], v100 offset:0\n\t"
        "ds_read_b64_tr_b16 v[218:219], v100 offset:256\n\t"
        "ds_read_b64_tr_b16 v[220:221], v100 offset:4096\n\t"
        "ds_read_b64_tr_b16 v[222:223], v100 offset:4352\n\t"
        "s_waitcnt lgkmcnt(8)\n\t"
        "ds_read_b64_tr_b16 v[224:225], v101 offset:0\n\t"
        "ds_read_b64_tr_b16 v[226:227], v101 offset:256\n\t"
        "ds_read_b64_tr_b16 v[228:229], v101 offset:4096\n\t"
        "ds_read_b64_tr_b16 v[230:231], v101 offset:4352\n\t"
        "s_waitcnt lgkmcnt(8)\n\t"
        "ds_read_b64_tr_b16 v[104:105], v102 offset:0\n\t"
        "ds_read_b64_tr_b16 v[106:107], v102 offset:256\n\t"
        "ds_read_b64_tr_b16 v[108:109], v102 offset:4096\n\t"
        "ds_read_b64_tr_b16 v[110:111], v102 offset:4352\n\t"
        "s_mov_b32 s35, 33792\n\t"
        "buffer_load_dword v0, off, s[12:15], s35 sc1\n\t"
        "s_waitcnt lgkmcnt(0)\n\t"
        "ds_write_b128 v117, v[104:107] offset:20480\n\t"
        "ds_write_b128 v117, v[108:111] offset:21504\n\t"
        "s_waitcnt vmcnt(0)\n\t"
        "ds_write_b128 v97, v[64:67] offset:0\n\t"
        "ds_write_b128 v98, v[68:71] offset:1024\n\t"
        "ds_write_b128 v97, v[72:75] offset:2048\n\t"
        "ds_write_b128 v98, v[76:79] offset:3072\n\t"
        "ds_write_b128 v97, v[80:83] offset:4096\n\t"
        "ds_write_b128 v98, v[84:87] offset:5120\n\t"
        "ds_write_b128 v97, v[88:91] offset:6144\n\t"
        "ds_write_b128 v98, v[92:95] offset:7168\n\t"
        "ds_read_b64_tr_b16 v[232:233], v99 offset:0\n\t"
        "ds_read_b64_tr_b16 v[234:235], v99 offset:256\n\t"
        "ds_read_b64_tr_b16 v[236:237], v99 offset:4096\n\t"
        "ds_read_b64_tr_b16 v[238:239], v99 offset:4352\n\t"
        "s_waitcnt lgkmcnt(8)\n\t"
        "ds_read_b64_tr_b16 v[240:241], v100 offset:0\n\t"
        "ds_read_b64_tr_b16 v[242:243], v100 offset:256\n\t"
        "ds_read_b64_tr_b16 v[244:245], v100 offset:4096\n\t"
        "ds_read_b64_tr_b16 v[246:247], v100 offset:4352\n\t"
        "s_waitcnt lgkmcnt(8)\n\t"
        "ds_read_b64_tr_b16 v[248:249], v101 offset:0\n\t"
        "ds_read_b64_tr_b16 v[250:251], v101 offset:256\n\t"
        "ds_read_b64_tr_b16 v[252:253], v101 offset:4096\n\t"
        "ds_read_b64_tr_b16 v[254:255], v101 offset:4352\n\t"
        "s_waitcnt lgkmcnt(8)\n\t"
        "ds_read_b64_tr_b16 v[104:105], v102 offset:0\n\t"
        "ds_read_b64_tr_b16 v[106:107], v102 offset:256\n\t"
        "ds_read_b64_tr_b16 v[108:109], v102 offset:4096\n\t"
        "ds_read_b64_tr_b16 v[110:111], v102 offset:4352\n\t"
        "s_mov_b32 s42, 0\n\t"
        "LA_cal%=:\n\t"
        "v_readfirstlane_b32 s34, v0\n\t"
        "s_nop 0\n\t"
        "s_cmp_lg_u32 s34, 0\n\t"
        "s_cbranch_scc1 LA_calok%=\n\t"
        "s_sleep 4\n\t"
        "s_mov_b32 s35, 33792\n\t"
        "buffer_load_dword v0, off, s[12:15], s35 sc1\n\t"
        "s_waitcnt vmcnt(0)\n\t"
        "s_add_u32 s42, s42, 1\n\t"
        "s_cmp_lt_u32 s42, 0x100000\n\t"
        "s_cbranch_scc1 LA_cal%=\n\t"
        "s_movk_i32 s34, 8\n\t"
        "s_branch LA_die%=\n\t"
        "LA_calok%=:\n\t"
        "s_lshl_b32 s35, s44, 10\n\t"
        "v_lshlrev_b32_e32 v1, 2, v122\n\t"
        "v_and_b32_e32 v1, 0x3c0, v1\n\t"
        "buffer_load_dword v1, v1, s[12:15], s35 offen offset:0 sc1\n\t"
        "s_waitcnt lgkmcnt(0)\n\t"
        "ds_write_b128 v126, v[104:107] offset:8192\n\t"
        "ds_write_b128 v126, v[108:111] offset:9216\n\t"
        "s_waitcnt lgkmcnt(0)\n\t"
        "v_mov_b32_e32 v110, v103\n\t"
        "ds_read_b128 v[64:67], v126 offset:0\n\t"
        "ds_read_b128 v[68:71], v126 offset:1024\n\t"
        "ds_read_b128 v[72:75], v126 offset:2048\n\t"
        "ds_read_b128 v[76:79], v126 offset:3072\n\t"
        "ds_read_b128 v[80:83], v126 offset:4096\n\t"
        "ds_read_b128 v[84:87], v126 offset:5120\n\t"
        "ds_read_b128 v[88:91], v126 offset:6144\n\t"
        "ds_read_b128 v[92:95], v126 offset:7168\n\t"
        "ds_read_b128 v[96:99], v126 offset:8192\n\t"
        "ds_read_b128 v[100:103], v126 offset:9216\n\t"
        "s_waitcnt lgkmcnt(0)\n\t"
        "v_mov_b32_e32 v126, 0\n\t"
        "s_barrier\n\t"
        "s_mov_b32 s48, 0\n\t"
        "s_mov_b32 s54, 0\n\t"
        "s_mov_b32 s0, 0x3f803f80\n\t"
        "s_mov_b32 s1, s0\n\t"
        "s_movk_i32 s56, 24576\n\t"
        "s_and_b32 s58, s43, 1\n\t"
        "s_lshl_b32 s58, s58, 12\n\t"
        "s_lshl_b32 s57, s44, 5\n\t"
        "s_add_u32 s57, s57, s43\n\t"
        "s_lshl_b32 s57, s57, 2\n\t"
        "s_add_u32 s57, s57, 458752\n\t"
        "s_waitcnt vmcnt(0)\n\t"
        "v_mov_b32_e32 v111, v1\n\t"
        "v_readfirstlane_b32 s34, v0\n\t"
        "s_nop 0\n\t"
        "s_and_b32 s34, s34, 8192\n\t"
        "s_add_u32 s8, s8, s34\n\t"
        "s_addc_u32 s9, s9, 0\n\t"
        "s_barrier\n\t"
        "s_cmp_lg_u32 s43, 0\n\t"
        "s_cbranch_scc1 LA_st%=\n\t"
        "v_mov_b32_e32 v2, 1\n\t"
        "s_mov_b32 s35, 34304\n\t"
        "s_mov_b64 exec, 1\n\t"
        "buffer_atomic_add v2, off, s[12:15], s35 sc0\n\t"
        "s_mov_b64 exec, -1\n\t"
        "s_waitcnt vmcnt(0)\n\t"
        "v_readfirstlane_b32 s34, v2\n\t"
        "s_nop 0\n\t"
        "s_cmp_lg_u32 s34, 95\n\t"
        "s_cbranch_scc1 LA_st%=\n\t"
        "s_memrealtime s[32:33]\n\t"
        "v_mbcnt_lo_u32_b32 v3, -1, 0\n\t"
        "v_lshlrev_b32_e32 v3, 7, v3\n\t"
        "s_waitcnt lgkmcnt(0)\n\t"
        "s_add_u32 s34, s32, 50\n\t"
        "s_or_b32 s34, s34, 1\n\t"
        "v_mov_b32_e32 v2, s34\n\t"
        "s_mov_b32 s35, 36864\n\t"
        "s_mov_b64 exec, 0xffff\n\t"
        "buffer_store_dword v2, v3, s[12:15], s35 offen sc1\n\t"
        "s_mov_b64 exec, -1\n\t"
        "LA_st%=:\n\t"
        "s_mov_b32 s24, 0\n\t"
        "s_mov_b32 s26, 0\n\t"
        "s_mov_b32 s27, 0x80000\n\t"
        "s_mov_b32 s28, 0x180000\n\t"
        "s_mov_b32 s31, 0\n\t"
        "s_mov_b32 s47, 0\n\t"
        "s_mov_b32 s38, 0xff00ff00\n\t"
        "s_mov_b32 s39, 0xff00ff00\n\t"
        "s_mov_b32 s40, 0\n\t"
        "s_mov_b32 s41, -1\n\t"
        "ds_read_b128 v[32:35], v117 offset:0\n\t"
        "ds_read_b128 v[36:39], v117 offset:1024\n\t"
        "ds_read_b128 v[40:43], v117 offset:2048\n\t"
        "ds_read_b128 v[44:47], v117 offset:3072\n\t"
        "ds_read_b128 v[48:51], v117 offset:4096\n\t"
        "ds_read_b128 v[52:55], v117 offset:5120\n\t"
        "ds_read_b128 v[56:59], v117 offset:6144\n\t"
        "ds_read_b128 v[60:63], v117 offset:7168\n\t"
        "v_mov_b64_e32 v[0:1], 0\n\t"
        "v_mov_b64_e32 v[2:3], 0\n\t"
        "v_mov_b64_e32 v[4:5], 0\n\t"
        "v_mov_b64_e32 v[6:7], 0\n\t"
        "v_mov_b64_e32 v[8:9], 0\n\t"
        "v_mov_b64_e32 v[10:11], 0\n\t"
        "v_mov_b64_e32 v[12:13], 0\n\t"
        "v_mov_b64_e32 v[14:15], 0\n\t"
        "s_lshr_b32 s59, s48, 1\n\t"
        "s_cmp_gt_u32 s54, s59\n\t"
        "s_cbranch_scc1 LA_kv0ok%=\n\t"
        "s_mov_b32 s42, 0\n\t"
        "s_waitcnt vmcnt(0)\n\t"
        "s_branch LA_kv0w%=\n\t"
        "LA_kv0%=:\n\t"
        "s_waitcnt vmcnt(0)\n\t"
        "LA_kv0w%=:\n\t"
        "v_min_u32_dpp v111, v111, v111 quad_perm:[1,0,3,2] row_mask:0xf bank_mask:0xf\n\t"
        "s_nop 1\n\t"
        "v_min_u32_dpp v111, v111, v111 quad_perm:[2,3,0,1] row_mask:0xf bank_mask:0xf\n\t"
        "s_nop 1\n\t"
        "v_min_u32_dpp v111, v111, v111 row_ror:4 row_mask:0xf bank_mask:0xf\n\t"
        "s_nop 1\n\t"
        "v_min_u32_dpp v111, v111, v111 row_ror:8 row_mask:0xf bank_mask:0xf\n\t"
        "s_nop 0\n\t"
        "v_readfirstlane_b32 s54, v111\n\t"
        "s_nop 0\n\t"
        "s_cmp_gt_u32 s54, s59\n\t"
        "s_cbranch_scc1 LA_kv0ok%=\n\t"
        "s_sleep 4\n\t"
        "s_lshl_b32 s35, s44, 10\n\t"
        "v_lshlrev_b32_e32 v111, 2, v122\n\t"
        "v_and_b32_e32 v111, 0x3c0, v111\n\t"
        "buffer_load_dword v111, v111, s[12:15], s35 offen offset:0 sc1\n\t"
        "s_add_u32 s42, s42, 1\n\t"
        "s_cmp_lt_u32 s42, 0x100000\n\t"
        "s_cbranch_scc1 LA_kv0%=\n\t"
        "s_movk_i32 s34, 16\n\t"
        "s_branch LA_die%=\n\t"
        "LA_kv0ok%=:\n\t"
        "s_and_b32 s36, s48, 1\n\t"
        "s_mul_i32 s36, s36, 24576\n\t"
        "s_add_u32 s36, s36, s55\n\t"
        "s_lshl_b32 s37, s48, 18\n\t"
        "s_lshl_b32 s34, s43, 11\n\t"
        "s_add_u32 s37, s37, s34\n\t"
        "s_add_u32 m0, s36, 0\n\t"
        "s_add_u32 s34, s37, 29548544\n\t"
        "buffer_load_dwordx4 v122, s[12:15], s34 offen sc1 lds\n\t"
        "s_add_u32 m0, s36, 1024\n\t"
        "s_add_u32 s34, s37, 29549568\n\t"
        "buffer_load_dwordx4 v122, s[12:15], s34 offen sc1 lds\n\t"
        "s_add_u32 m0, s36, 8192\n\t"
        "s_add_u32 s34, s37, 12771328\n\t"
        "buffer_load_dwordx4 v122, s[12:15], s34 offen sc1 lds\n\t"
        "s_add_u32 m0, s36, 9216\n\t"
        "s_add_u32 s34, s37, 12772352\n\t"
        "buffer_load_dwordx4 v122, s[12:15], s34 offen sc1 lds\n\t"
        "s_add_u32 m0, s36, 16384\n\t"
        "s_add_u32 s34, s37, 46325760\n\t"
        "buffer_load_dwordx4 v122, s[12:15], s34 offen sc1 lds\n\t"
        "s_add_u32 m0, s36, 17408\n\t"
        "s_add_u32 s34, s37, 46326784\n\t"
        "buffer_load_dwordx4 v122, s[12:15], s34 offen sc1 lds\n\t"
        "s_mov_b32 s42, 0\n\t"
        "LA_gw%=:\n\t"
        "buffer_load_dword v16, off, s[12:15], s45 sc1\n\t"
        "s_waitcnt vmcnt(0)\n\t"
        "v_readfirstlane_b32 s25, v16\n\t"
        "s_nop 0\n\t"
        "s_cmp_lg_u32 s25, 0\n\t"
        "s_cbranch_scc1 LA_loop%=\n\t"
        "s_sleep 2\n\t"
        "s_add_u32 s42, s42, 1\n\t"
        "s_cmp_lt_u32 s42, 0x100000\n\t"
        "s_cbranch_scc1 LA_gw%=\n\t"
        "s_movk_i32 s34, 9\n\t"
        "s_branch LA_die%=\n\t"
        ".p2align 8\n\t"
        "LA_loop%=:\n\t"
        "s_mov_b32 s42, 0\n\t"
        "s_sub_u32 s35, s25, 1\n\t"
        "LA_wa%=:\n\t"
        "s_memrealtime s[32:33]\n\t"
        "s_waitcnt lgkmcnt(0)\n\t"
        "s_sub_u32 s34, s35, s32\n\t"
        "s_cmpk_lt_u32 s34, 0x2000\n\t"
        "s_cbranch_scc1 LA_wa%=\n\t"
        "LA_rd%=:\n\t"
        "buffer_load_dwordx4 v[16:19], v112, s[8:11], s26 offen offset:0 sc1\n\t"
        "s_nop 12\n\t"
        "buffer_load_dwordx4 v[20:23], v112, s[8:11], s26 offen offset:1024 sc1\n\t"
        "s_nop 12\n\t"
        "buffer_load_dwordx4 v[24:27], v112, s[8:11], s26 offen offset:2048 sc1\n\t"
        "s_nop 12\n\t"
        "buffer_load_dwordx4 v[28:31], v112, s[8:11], s26 offen offset:3072 sc1\n\t"
        "LA_go%=:\n\t"
        "s_waitcnt vmcnt(3)\n\t"
        "v_smfmac_f32_16x16x64_bf16 v[0:3], v[16:19], a[0:7], v110\n\t"
        "ds_write_b128 v113, v[16:19] offset:0\n\t"
        "v_smfmac_f32_16x16x64_bf16 v[4:7], v[16:19], a[8:15], v110\n\t"
        "v_max3_u32 v111, v16, v17, v18\n\t"
        "v_max_u32_e32 v111, v19, v111\n\t"
        "v_smfmac_f32_16x16x64_bf16 v[8:11], v[16:19], a[16:23], v110\n\t"
        "v_smfmac_f32_16x16x64_bf16 v[12:15], v[16:19], v[32:39], v110\n\t"
        "s_waitcnt vmcnt(2)\n\t"
        "v_smfmac_f32_16x16x64_bf16 v[0:3], v[20:23], a[24:31], v110\n\t"
        "ds_write_b128 v113, v[20:23] offset:1024\n\t"
        "v_smfmac_f32_16x16x64_bf16 v[4:7], v[20:23], a[32:39], v110\n\t"
        "v_max3_u32 v111, v20, v21, v111\n\t"
        "v_max3_u32 v111, v22, v23, v111\n\t"
        "v_smfmac_f32_16x16x64_bf16 v[8:11], v[20:23], a[40:47], v110\n\t"
        "s_waitcnt vmcnt(1)\n\t"
        "ds_write_b128 v113, v[24:27] offset:2048\n\t"
        "v_smfmac_f32_16x16x64_bf16 v[12:15], v[20:23], v[40:47], v110\n\t"
        "v_max3_u32 v111, v24, v25, v111\n\t"
        "v_max3_u32 v111, v26, v27, v111\n\t"
        "v_smfmac_f32_16x16x64_bf16 v[0:3], v[24:27], a[48:55], v110\n\t"
        "v_smfmac_f32_16x16x64_bf16 v[4:7], v[24:27], a[56:63], v110\n\t"
        "s_waitcnt vmcnt(0)\n\t"
        "ds_write_b128 v113, v[28:31] offset:3072\n\t"
        "v_max3_u32 v111, v28, v29, v111\n\t"
        "v_max3_u32 v111, v30, v31, v111\n\t"
        "v_smfmac_f32_16x16x64_bf16 v[8:11], v[24:27], a[64:71], v110\n\t"
        "v_cmp_eq_u32_e32 vcc, -1, v111\n\t"
        "v_smfmac_f32_16x16x64_bf16 v[12:15], v[24:27], v[48:55], v110\n\t"
        "s_waitcnt lgkmcnt(0)\n\t"
        "s_cbranch_vccnz LA_rdfail%=\n\t"
        "s_barrier\n\t"
        "ds_read_b128 v[16:19], v114 offset:0\n\t"
        "ds_read_b128 v[20:23], v114 offset:1024\n\t"
        "ds_read_b128 v[24:27], v114 offset:2048\n\t"
        "v_smfmac_f32_16x16x64_bf16 v[0:3], v[28:31], a[72:79], v110\n\t"
        "v_smfmac_f32_16x16x64_bf16 v[4:7], v[28:31], a[80:87], v110\n\t"
        "v_smfmac_f32_16x16x64_bf16 v[8:11], v[28:31], a[88:95], v110\n\t"
        "v_smfmac_f32_16x16x64_bf16 v[12:15], v[28:31], v[56:63], v110\n\t"
        "ds_read_b128 v[28:31], v114 offset:3072\n\t"
        "s_waitcnt lgkmcnt(3)\n\t"
        "v_smfmac_f32_16x16x64_bf16 v[12:15], v[16:19], v[64:71], v110\n\t"
        "ds_read_b128 v[32:35], v117 offset:8192\n\t"
        "v_smfmac_f32_16x16x64_bf16 v[0:3], v[16:19], a[96:103], v110\n\t"
        "ds_read_b128 v[36:39], v117 offset:9216\n\t"
        "v_smfmac_f32_16x16x64_bf16 v[4:7], v[16:19], a[104:111], v110\n\t"
        "s_waitcnt lgkmcnt(4)\n\t"
        "v_smfmac_f32_16x16x64_bf16 v[8:11], v[16:19], a[112:119], v110\n\t"
        "ds_read_b128 v[16:19], v115 offset:0\n\t"
        "v_smfmac_f32_16x16x64_bf16 v[12:15], v[20:23], v[72:79], v110\n\t"
        "ds_read_b128 v[40:43], v117 offset:10240\n\t"
        "v_smfmac_f32_16x16x64_bf16 v[0:3], v[20:23], a[120:127], v110\n\t"
        "ds_read_b128 v[44:47], v117 offset:11264\n\t"
        "v_smfmac_f32_16x16x64_bf16 v[4:7], v[20:23], a[128:135], v110\n\t"
        "s_waitcnt lgkmcnt(6)\n\t"
        "v_smfmac_f32_16x16x64_bf16 v[8:11], v[20:23], a[136:143], v110\n\t"
        "ds_read_b128 v[20:23], v115 offset:1024\n\t"
        "v_smfmac_f32_16x16x64_bf16 v[12:15], v[24:27], v[80:87], v110\n\t"
        "ds_read_b128 v[48:51], v117 offset:12288\n\t"
        "v_smfmac_f32_16x16x64_bf16 v[0:3], v[24:27], a[144:151], v110\n\t"
        "ds_read_b128 v[52:55], v117 offset:13312\n\t"
        "v_smfmac_f32_16x16x64_bf16 v[4:7], v[24:27], a[152:159], v110\n\t"
        "s_waitcnt lgkmcnt(8)\n\t"
        "v_smfmac_f32_16x16x64_bf16 v[8:11], v[24:27], a[160:167], v110\n\t"
        "ds_read_b128 v[24:27], v115 offset:2048\n\t"
        "v_smfmac_f32_16x16x64_bf16 v[12:15], v[28:31], v[88:95], v110\n\t"
        "ds_read_b128 v[56:59], v117 offset:14336\n\t"
        "v_smfmac_f32_16x16x64_bf16 v[0:3], v[28:31], a[168:175], v110\n\t"
        "ds_read_b128 v[60:63], v117 offset:15360\n\t"
        "v_smfmac_f32_16x16x64_bf16 v[4:7], v[28:31], a[176:183], v110\n\t"
        "s_waitcnt lgkmcnt(8)\n\t"
        "v_smfmac_f32_16x16x64_bf16 v[8:11], v[28:31], a[184:191], v110\n\t"
        "ds_read_b128 v[28:31], v115 offset:3072\n\t"
        "v_smfmac_f32_16x16x64_bf16 v[12:15], v[16:19], v[32:39], v110\n\t"
        "ds_read_b128 v[32:35], v117 offset:16384\n\t"
        "v_smfmac_f32_16x16x64_bf16 v[0:3], v[16:19], a[192:199], v110\n\t"
        "ds_read_b128 v[36:39], v117 offset:17408\n\t"
        "v_smfmac_f32_16x16x64_bf16 v[4:7], v[16:19], a[200:207], v110\n\t"
        "s_waitcnt lgkmcnt(8)\n\t"
        "v_smfmac_f32_16x16x64_bf16 v[8:11], v[16:19], a[208:215], v110\n\t"
        "ds_read_b128 v[16:19], v116 offset:0\n\t"
        "v_smfmac_f32_16x16x64_bf16 v[12:15], v[20:23], v[40:47], v110\n\t"
        "ds_read_b128 v[40:43], v117 offset:18432\n\t"
        "v_smfmac_f32_16x16x64_bf16 v[0:3], v[20:23], a[216:223], v110\n\t"
        "ds_read_b128 v[44:47], v117 offset:19456\n\t"
        "v_smfmac_f32_16x16x64_bf16 v[4:7], v[20:23], a[224:231], v110\n\t"
        "s_waitcnt lgkmcnt(8)\n\t"
        "v_smfmac_f32_16x16x64_bf16 v[8:11], v[20:23], a[232:239], v110\n\t"
        "ds_read_b128 v[20:23], v116 offset:1024\n\t"
        "v_smfmac_f32_16x16x64_bf16 v[12:15], v[24:27], v[48:55], v110\n\t"
        "ds_read_b128 v[48:51], v117 offset:20480\n\t"
        "v_smfmac_f32_16x16x64_bf16 v[0:3], v[24:27], a[240:247], v110\n\t"
        "ds_read_b128 v[52:55], v117 offset:21504\n\t"
        "v_smfmac_f32_16x16x64_bf16 v[4:7], v[24:27], a[248:255], v110\n\t"
        "s_waitcnt lgkmcnt(8)\n\t"
        "v_smfmac_f32_16x16x64_bf16 v[8:11], v[24:27], v[128:135], v110\n\t"
        "ds_read_b128 v[24:27], v116 offset:2048\n\t"
        "v_smfmac_f32_16x16x64_bf16 v[12:15], v[28:31], v[56:63], v110\n\t"
        "ds_read_b128 v[56:59], v124 offset:3072\n\t"
        "v_smfmac_f32_16x16x64_bf16 v[0:3], v[28:31], v[136:143], v110\n\t"
        "ds_read_b128 v[60:63], v124 offset:7168\n\t"
        "v_smfmac_f32_16x16x64_bf16 v[4:7], v[28:31], v[144:151], v110\n\t"
        "s_waitcnt lgkmcnt(8)\n\t"
        "v_smfmac_f32_16x16x64_bf16 v[8:11], v[28:31], v[152:159], v110\n\t"
        "ds_read_b128 v[28:31], v116 offset:3072\n\t"
        "v_smfmac_f32_16x16x64_bf16 v[12:15], v[16:19], v[32:39], v110\n\t"
        "ds_read_b128 v[32:35], v124 offset:0\n\t"
        "v_smfmac_f32_16x16x64_bf16 v[0:3], v[16:19], v[160:167], v110\n\t"
        "ds_read_b128 v[36:39], v124 offset:4096\n\t"
        "v_smfmac_f32_16x16x64_bf16 v[4:7], v[16:19], v[168:175], v110\n\t"
        "s_waitcnt lgkmcnt(8)\n\t"
        "v_smfmac_f32_16x16x64_bf16 v[8:11], v[16:19], v[176:183], v110\n\t"
        "v_smfmac_f32_16x16x64_bf16 v[12:15], v[20:23], v[40:47], v110\n\t"
        "ds_read_b128 v[40:43], v124 offset:1024\n\t"
        "v_smfmac_f32_16x16x64_bf16 v[8:11], v[20:23], v[200:207], v110\n\t"
        "ds_read_b128 v[44:47], v124 offset:5120\n\t"
        "s_waitcnt lgkmcnt(7)\n\t"
        "v_smfmac_f32_16x16x64_bf16 v[12:15], v[24:27], v[48:55], v110\n\t"
        "ds_read_b128 v[48:51], v124 offset:2048\n\t"
        "v_smfmac_f32_16x16x64_bf16 v[8:11], v[24:27], v[224:231], v110\n\t"
        "ds_read_b128 v[52:55], v124 offset:6144\n\t"
        "s_waitcnt lgkmcnt(6)\n\t"
        "v_smfmac_f32_16x16x64_bf16 v[12:15], v[28:31], v[96:103], v110\n\t"
        "v_smfmac_f32_16x16x64_bf16 v[8:11], v[28:31], v[248:255], v110\n\t"
        "v_smfmac_f32_16x16x64_bf16 v[0:3], v[20:23], v[184:191], v110\n\t"
        "s_lshl_b32 s34, s43, 11\n\t"
        "v_add_u32_e32 v108, s34, v124\n\t"
        "v_smfmac_f32_16x16x64_bf16 v[0:3], v[24:27], v[208:215], v110\n\t"
        "v_add_u32_e32 v109, s58, v124\n\t"
        "v_smfmac_f32_16x16x64_bf16 v[4:7], v[20:23], v[192:199], v110\n\t"
        "v_smfmac_f32_16x16x64_bf16 v[0:3], v[28:31], v[232:239], v110\n\t"
        "v_smfmac_f32_16x16x64_bf16 v[4:7], v[24:27], v[216:223], v110\n\t"
        "buffer_load_dwordx4 v[104:107], v120, s[8:11], s26 offen sc1\n\t"
        "v_smfmac_f32_16x16x64_bf16 v[4:7], v[28:31], v[240:247], v110\n\t"
        "v_add_f32_e32 v12, v12, v13\n\t"
        "v_add_f32_e32 v14, v14, v15\n\t"
        "v_cvt_pk_bf16_f32 v12, v12, v14\n\t"
        "v_add_f32_e32 v8, v8, v9\n\t"
        "v_add_f32_e32 v10, v10, v11\n\t"
        "v_cvt_pk_bf16_f32 v8, v8, v10\n\t"
        "ds_write2_b32 v118, v8, v12 offset0:64 offset1:96\n\t"
        "v_add_f32_e32 v0, v0, v1\n\t"
        "v_add_f32_e32 v2, v2, v3\n\t"
        "v_cvt_pk_bf16_f32 v0, v0, v2\n\t"
        "v_add_f32_e32 v4, v4, v5\n\t"
        "v_add_f32_e32 v6, v6, v7\n\t"
        "v_cvt_pk_bf16_f32 v4, v4, v6\n\t"
        "ds_write2_b32 v118, v0, v4 offset0:0 offset1:32\n\t"
        "s_cmp_eq_u32 s24, 63\n\t"
        "s_waitcnt lgkmcnt(0)\n\t"
        "s_barrier\n\t"
        "s_cbranch_scc1 LA_skip%=\n\t"
        "ds_read_b64_tr_b16 v[16:17], v119 offset:0\n\t"
        "ds_read_b64_tr_b16 v[18:19], v119 offset:32\n\t"
        "ds_read_b64_tr_b16 v[20:21], v119 offset:256\n\t"
        "ds_read_b64_tr_b16 v[22:23], v119 offset:288\n\t"
        "ds_read_b64_tr_b16 v[24:25], v119 offset:512\n\t"
        "ds_read_b64_tr_b16 v[26:27], v119 offset:544\n\t"
        "ds_read_b64_tr_b16 v[28:29], v119 offset:768\n\t"
        "ds_read_b64_tr_b16 v[30:31], v119 offset:800\n\t"
        "buffer_store_dword v126, v127, s[12:15], 0 offen sc1\n\t"
        "s_waitcnt lgkmcnt(6)\n\t"
        "v_mfma_f32_16x16x32_bf16 v[0:3], v[32:35], v[16:19], 0\n\t"
        "ds_read_b64_tr_b16 v[32:33], v119 offset:2112\n\t"
        "ds_read_b64_tr_b16 v[34:35], v119 offset:2144\n\t"
        "v_mfma_f32_16x16x32_bf16 v[4:7], v[36:39], v[16:19], 0\n\t"
        "ds_read_b64_tr_b16 v[36:37], v119 offset:2368\n\t"
        "ds_read_b64_tr_b16 v[38:39], v119 offset:2400\n\t"
        "s_waitcnt lgkmcnt(8)\n\t"
        "v_mfma_f32_16x16x32_bf16 v[0:3], v[40:43], v[20:23], v[0:3]\n\t"
        "ds_read_b64_tr_b16 v[40:41], v119 offset:2624\n\t"
        "ds_read_b64_tr_b16 v[42:43], v119 offset:2656\n\t"
        "v_mfma_f32_16x16x32_bf16 v[4:7], v[44:47], v[20:23], v[4:7]\n\t"
        "ds_read_b64_tr_b16 v[44:45], v119 offset:2880\n\t"
        "ds_read_b64_tr_b16 v[46:47], v119 offset:2912\n\t"
        "s_waitcnt lgkmcnt(10)\n\t"
        "v_mfma_f32_16x16x32_bf16 v[0:3], v[48:51], v[24:27], v[0:3]\n\t"
        "ds_read_b128 v[48:51], v108 offset:16384\n\t"
        "v_mfma_f32_16x16x32_bf16 v[4:7], v[52:55], v[24:27], v[4:7]\n\t"
        "ds_read_b128 v[52:55], v108 offset:17408\n\t"
        "s_waitcnt lgkmcnt(10)\n\t"
        "v_mfma_f32_16x16x32_bf16 v[0:3], v[56:59], v[28:31], v[0:3]\n\t"
        "v_mfma_f32_16x16x32_bf16 v[4:7], v[60:63], v[28:31], v[4:7]\n\t"
        "s_waitcnt lgkmcnt(2)\n\t"
        "v_mfma_f32_16x16x32_bf16 v[8:11], v[32:35], v[16:19], 0\n\t"
        "s_nop 1\n\t"
        "v_mfma_f32_16x16x32_bf16 v[8:11], v[36:39], v[20:23], v[8:11]\n\t"
        "v_mov_b64_e32 v[16:17], s[0:1]\n\t"
        "v_mov_b64_e32 v[18:19], s[0:1]\n\t"
        "v_mfma_f32_16x16x32_bf16 v[8:11], v[40:43], v[24:27], v[8:11]\n\t"
        "v_max3_f32 v59, v0, v1, v2\n\t"
        "v_max3_f32 v59, v59, v3, v4\n\t"
        "v_mfma_f32_16x16x32_bf16 v[8:11], v[44:47], v[28:31], v[8:11]\n\t"
        "v_max3_f32 v59, v59, v5, v6\n\t"
        "v_max_f32_e32 v60, v59, v7\n\t"
        "v_max_f32_e32 v59, v59, v7\n\t"
        "v_cndmask_b32_e64 v0, v0, v4, s[38:39]\n\t"
        "v_cndmask_b32_e64 v1, v1, v5, s[38:39]\n\t"
        "v_permlane32_swap_b32_e32 v59, v60\n\t"
        "v_cndmask_b32_e64 v2, v2, v6, s[38:39]\n\t"
        "v_cndmask_b32_e64 v3, v3, v7, s[38:39]\n\t"
        "v_max3_f32 v59, v59, v60, v8\n\t"
        "v_max3_f32 v59, v59, v9, v10\n\t"
        "v_max_f32_e32 v60, v59, v11\n\t"
        "v_max_f32_e32 v59, v59, v11\n\t"
        "v_cndmask_b32_e64 v8, v8, v9, s[38:39]\n\t"
        "v_cndmask_b32_e64 v10, v10, v11, s[38:39]\n\t"
        "v_permlane16_swap_b32_e32 v59, v60\n\t"
        "v_max_f32_e32 v59, v59, v60\n\t"
        "v_mul_f32_e32 v59, 0xbe02940c, v59\n\t"
        "v_cndmask_b32_e64 v58, v8, v10, s[40:41]\n\t"
        "v_fmamk_f32 v0, v0, 0x3e02940c, v59\n\t"
        "v_exp_f32_e32 v0, v0\n\t"
        "v_fmamk_f32 v1, v1, 0x3e02940c, v59\n\t"
        "v_exp_f32_e32 v1, v1\n\t"
        "v_fmamk_f32 v2, v2, 0x3e02940c, v59\n\t"
        "v_exp_f32_e32 v2, v2\n\t"
        "v_fmamk_f32 v3, v3, 0x3e02940c, v59\n\t"
        "v_exp_f32_e32 v3, v3\n\t"
        "v_fmamk_f32 v58, v58, 0x3e02940c, v59\n\t"
        "v_cvt_pk_bf16_f32 v12, v0, v1\n\t"
        "v_cvt_pk_bf16_f32 v13, v2, v3\n\t"
        "v_exp_f32_e32 v58, v58\n\t"
        "s_waitcnt lgkmcnt(0)\n\t"
        "v_mov_b32_dpp v14, v12 row_ror:8 row_mask:0xf bank_mask:0xf\n\t"
        "v_mov_b32_dpp v15, v13 row_ror:8 row_mask:0xf bank_mask:0xf\n\t"
        "v_mov_b32_dpp v59, v58 row_ror:8 row_mask:0xf bank_mask:0xf\n\t"
        "v_cvt_pk_bf16_f32 v59, v58, v59\n\t"
        "v_mfma_f32_16x16x32_bf16 v[20:23], v[16:19], v[12:15], 0\n\t"
        "v_cndmask_b32_e64 v56, v59, 0, s[40:41]\n\t"
        "v_cndmask_b32_e64 v57, 0, v59, s[40:41]\n\t"
        "v_mfma_f32_16x16x32_bf16 v[4:7], v[48:51], v[12:15], 0\n\t"
        "s_waitcnt vmcnt(1)\n\t"
        "v_max3_u32 v111, v104, v105, v106\n\t"
        "v_mfma_f32_16x16x32_bf16 v[8:11], v[52:55], v[12:15], 0\n\t"
        "v_max_u32_e32 v111, v111, v107\n\t"
        "v_mfma_f32_16x16x16_bf16 v[20:23], v[16:17], v[56:57], v[20:23]\n\t"
        "v_cmp_eq_u32_e32 vcc, -1, v111\n\t"
        "v_mfma_f32_16x16x16_bf16 v[4:7], v[104:105], v[56:57], v[4:7]\n\t"
        "s_nop 1\n\t"
        "v_mfma_f32_16x16x16_bf16 v[8:11], v[106:107], v[56:57], v[8:11]\n\t"
        "s_cbranch_vccnz LA_vmfail%=\n\t"
        "LA_vmok%=:\n\t"
        "s_nop 1\n\t"
        "v_rcp_f32_e32 v59, v20\n\t"
        "s_nop 0\n\t"
        "v_pk_mul_f32 v[4:5], v[4:5], v[58:59] op_sel:[0,1] op_sel_hi:[1,1]\n\t"
        "v_cvt_pk_bf16_f32 v0, v4, v5\n\t"
        "v_pk_mul_f32 v[6:7], v[6:7], v[58:59] op_sel:[0,1] op_sel_hi:[1,1]\n\t"
        "v_cvt_pk_bf16_f32 v1, v6, v7\n\t"
        "v_pk_mul_f32 v[8:9], v[8:9], v[58:59] op_sel:[0,1] op_sel_hi:[1,1]\n\t"
        "v_cvt_pk_bf16_f32 v2, v8, v9\n\t"
        "v_pk_mul_f32 v[10:11], v[10:11], v[58:59] op_sel:[0,1] op_sel_hi:[1,1]\n\t"
        "v_cvt_pk_bf16_f32 v3, v10, v11\n\t"
        "buffer_store_dwordx4 v[0:3], v121, s[8:11], s26 offen sc1\n\t"
        "LA_x%=:\n\t"
        "s_memrealtime s[52:53]\n\t"
        "ds_read_b128 v[16:19], v109 offset:8192\n\t"
        "ds_read_b128 v[20:23], v109 offset:9216\n\t"
        "ds_read_b128 v[24:27], v109 offset:10240\n\t"
        "ds_read_b128 v[28:31], v109 offset:11264\n\t"
        "ds_read_b128 v[56:59], v124 offset:0\n\t"
        "ds_read_b128 v[60:63], v124 offset:4096\n\t"
        "ds_read_b128 v[12:15], v124 offset:1024\n\t"
        "v_xor_b32_e32 v126, 0x800, v108\n\t"
        "s_add_u32 s48, s24, 1\n\t"
        "s_min_u32 s48, s48, 63\n\t"
        "buffer_load_dwordx2 v[108:109], v125, s[8:11], s26 offen sc1\n\t"
        "s_lshl_b32 s35, s44, 10\n\t"
        "s_lshr_b32 s34, s48, 1\n\t"
        "s_add_u32 s34, s34, 1\n\t"
        "s_min_u32 s34, s34, 31\n\t"
        "s_cmp_gt_u32 s54, s34\n\t"
        "s_cselect_b32 s35, 0xf0000000, s35\n\t"
        "v_lshlrev_b32_e32 v111, 2, v122\n\t"
        "v_and_b32_e32 v111, 0x3c0, v111\n\t"
        "buffer_load_dword v111, v111, s[12:15], s35 offen offset:0 sc1\n\t"
        "s_waitcnt lgkmcnt(6)\n\t"
        "v_mfma_f32_16x16x32_bf16 v[8:11], v[32:35], v[16:19], 0\n\t"
        "ds_read_b128 v[32:35], v124 offset:5120\n\t"
        "s_waitcnt lgkmcnt(6)\n\t"
        "v_mfma_f32_16x16x32_bf16 v[8:11], v[36:39], v[20:23], v[8:11]\n\t"
        "ds_read_b128 v[36:39], v124 offset:2048\n\t"
        "s_waitcnt lgkmcnt(6)\n\t"
        "v_mfma_f32_16x16x32_bf16 v[8:11], v[40:43], v[24:27], v[8:11]\n\t"
        "ds_read_b128 v[40:43], v124 offset:6144\n\t"
        "s_waitcnt lgkmcnt(6)\n\t"
        "v_mfma_f32_16x16x32_bf16 v[8:11], v[44:47], v[28:31], v[8:11]\n\t"
        "ds_read_b128 v[44:47], v124 offset:3072\n\t"
        "s_add_u32 s35, s24, 1\n\t"
        "s_waitcnt lgkmcnt(6)\n\t"
        "v_mfma_f32_16x16x32_bf16 v[0:3], v[56:59], v[16:19], 0\n\t"
        "ds_read_b128 v[56:59], v124 offset:7168\n\t"
        "s_waitcnt lgkmcnt(6)\n\t"
        "v_mfma_f32_16x16x32_bf16 v[4:7], v[60:63], v[16:19], 0\n\t"
        "s_and_b32 s36, s35, 1\n\t"
        "s_mul_i32 s36, s36, 24576\n\t"
        "s_waitcnt lgkmcnt(5)\n\t"
        "v_mfma_f32_16x16x32_bf16 v[0:3], v[12:15], v[20:23], v[0:3]\n\t"
        "s_add_u32 s36, s36, s55\n\t"
        "s_lshl_b32 s37, s48, 18\n\t"
        "s_waitcnt lgkmcnt(4)\n\t"
        "v_mfma_f32_16x16x32_bf16 v[4:7], v[32:35], v[20:23], v[4:7]\n\t"
        "v_xor_b32_e32 v32, 0x200, v120\n\t"
        "buffer_load_dwordx4 v[12:15], v32, s[8:11], s26 offen sc1\n\t"
        "s_waitcnt lgkmcnt(3)\n\t"
        "v_mfma_f32_16x16x32_bf16 v[0:3], v[36:39], v[24:27], v[0:3]\n\t"
        "s_lshl_b32 s34, s43, 11\n\t"
        "s_add_u32 s37, s37, s34\n\t"
        "s_waitcnt lgkmcnt(2)\n\t"
        "v_mfma_f32_16x16x32_bf16 v[4:7], v[40:43], v[24:27], v[4:7]\n\t"
        "v_cvt_pk_bf16_f32 v8, 0, v8\n\t"
        "v_cvt_pk_bf16_f32 v9, 0, v9\n\t"
        "v_cvt_pk_bf16_f32 v10, 0, v10\n\t"
        "v_cvt_pk_bf16_f32 v11, 0, v11\n\t"
        "s_waitcnt lgkmcnt(1)\n\t"
        "v_mfma_f32_16x16x32_bf16 v[0:3], v[44:47], v[28:31], v[0:3]\n\t"
        "v_mul_f32_e32 v8, 0x3db504f3, v8\n\t"
        "v_mul_f32_e32 v9, 0x3db504f3, v9\n\t"
        "v_mul_f32_e32 v10, 0x3db504f3, v10\n\t"
        "v_mul_f32_e32 v11, 0x3db504f3, v11\n\t"
        "s_waitcnt lgkmcnt(0)\n\t"
        "v_mfma_f32_16x16x32_bf16 v[4:7], v[56:59], v[28:31], v[4:7]\n\t"
        "v_cvt_pk_bf16_f32 v8, 0, v8\n\t"
        "v_cvt_pk_bf16_f32 v9, 0, v9\n\t"
        "v_cvt_pk_bf16_f32 v10, 0, v10\n\t"
        "v_cvt_pk_bf16_f32 v11, 0, v11\n\t"
        "ds_read_b128 v[56:59], v126 offset:16384\n\t"
        "ds_read_b128 v[60:63], v126 offset:17408\n\t"
        "v_mov_b64_e32 v[36:37], -1\n\t"
        "v_mov_b64_e32 v[38:39], -1\n\t"
        "v_mov_b32_e32 v40, 0xce6e6b28\n\t"
        "v_mov_b32_e32 v46, 0x3db504f3\n\t"
        "v_mov_b32_e32 v47, 0x3fb8aa3b\n\t"
        "v_cvt_pk_bf16_f32 v0, 0, v0\n\t"
        "v_cvt_pk_bf16_f32 v1, 0, v1\n\t"
        "v_cvt_pk_bf16_f32 v2, 0, v2\n\t"
        "v_cvt_pk_bf16_f32 v3, 0, v3\n\t"
        "v_cvt_pk_bf16_f32 v4, 0, v4\n\t"
        "v_cvt_pk_bf16_f32 v5, 0, v5\n\t"
        "v_cvt_pk_bf16_f32 v6, 0, v6\n\t"
        "v_cvt_pk_bf16_f32 v7, 0, v7\n\t"
        "v_pk_mul_f32 v[0:1], v[0:1], v[46:47] op_sel_hi:[1,0]\n\t"
        "v_pk_mul_f32 v[2:3], v[2:3], v[46:47] op_sel_hi:[1,0]\n\t"
        "v_pk_mul_f32 v[4:5], v[4:5], v[46:47] op_sel_hi:[1,0]\n\t"
        "v_pk_mul_f32 v[6:7], v[6:7], v[46:47] op_sel_hi:[1,0]\n\t"
        "s_lshr_b32 s59, s48, 1\n\t"
        "s_cmp_gt_u32 s54, s59\n\t"
        "s_cbranch_scc1 LA_kvok%=\n\t"
        "s_mov_b32 s42, 0\n\t"
        "s_waitcnt vmcnt(1)\n\t"
        "s_branch LA_kvw%=\n\t"
        "LA_kv%=:\n\t"
        "s_waitcnt vmcnt(0)\n\t"
        "LA_kvw%=:\n\t"
        "v_min_u32_dpp v111, v111, v111 quad_perm:[1,0,3,2] row_mask:0xf bank_mask:0xf\n\t"
        "s_nop 1\n\t"
        "v_min_u32_dpp v111, v111, v111 quad_perm:[2,3,0,1] row_mask:0xf bank_mask:0xf\n\t"
        "s_nop 1\n\t"
        "v_min_u32_dpp v111, v111, v111 row_ror:4 row_mask:0xf bank_mask:0xf\n\t"
        "s_nop 1\n\t"
        "v_min_u32_dpp v111, v111, v111 row_ror:8 row_mask:0xf bank_mask:0xf\n\t"
        "s_nop 0\n\t"
        "v_readfirstlane_b32 s54, v111\n\t"
        "s_nop 0\n\t"
        "s_cmp_gt_u32 s54, s59\n\t"
        "s_cbranch_scc1 LA_kvok%=\n\t"
        "s_sleep 4\n\t"
        "s_lshl_b32 s35, s44, 10\n\t"
        "v_lshlrev_b32_e32 v111, 2, v122\n\t"
        "v_and_b32_e32 v111, 0x3c0, v111\n\t"
        "buffer_load_dword v111, v111, s[12:15], s35 offen offset:0 sc1\n\t"
        "s_add_u32 s42, s42, 1\n\t"
        "s_cmp_lt_u32 s42, 0x100000\n\t"
        "s_cbranch_scc1 LA_kv%=\n\t"
        "s_movk_i32 s34, 16\n\t"
        "s_branch LA_die%=\n\t"
        "LA_kvok%=:\n\t"
        "s_add_u32 m0, s36, 0\n\t"
        "s_add_u32 s34, s37, 29548544\n\t"
        "buffer_load_dwordx4 v122, s[12:15], s34 offen sc1 lds\n\t"
        "v_cvt_pk_bf16_f32 v0, 0, v0\n\t"
        "v_cvt_pk_bf16_f32 v1, 0, v1\n\t"
        "v_cvt_pk_bf16_f32 v2, 0, v2\n\t"
        "v_cvt_pk_bf16_f32 v3, 0, v3\n\t"
        "v_cvt_pk_bf16_f32 v4, 0, v4\n\t"
        "v_cvt_pk_bf16_f32 v5, 0, v5\n\t"
        "v_cvt_pk_bf16_f32 v6, 0, v6\n\t"
        "v_cvt_pk_bf16_f32 v7, 0, v7\n\t"
        "s_add_u32 m0, s36, 1024\n\t"
        "s_add_u32 s34, s37, 29549568\n\t"
        "buffer_load_dwordx4 v122, s[12:15], s34 offen sc1 lds\n\t"
        "v_cndmask_b32_e64 v0, v40, v0, s[60:61]\n\t"
        "v_cndmask_b32_e64 v1, v40, v1, s[62:63]\n\t"
        "v_cndmask_b32_e64 v2, v40, v2, s[64:65]\n\t"
        "v_cndmask_b32_e64 v3, v40, v3, s[66:67]\n\t"
        "v_cndmask_b32_e64 v4, v40, v4, s[68:69]\n\t"
        "v_cndmask_b32_e64 v5, v40, v5, s[70:71]\n\t"
        "v_cndmask_b32_e64 v6, v40, v6, s[72:73]\n\t"
        "v_cndmask_b32_e64 v7, v40, v7, s[74:75]\n\t"
        "s_add_u32 m0, s36, 8192\n\t"
        "s_add_u32 s34, s37, 12771328\n\t"
        "buffer_load_dwordx4 v122, s[12:15], s34 offen sc1 lds\n\t"
        "v_max3_f32 v42, v0, v1, v2\n\t"
        "v_max3_f32 v42, v42, v3, v4\n\t"
        "v_max3_f32 v42, v42, v5, v6\n\t"
        "v_max3_f32 v42, v42, v7, v8\n\t"
        "v_max3_f32 v42, v42, v9, v10\n\t"
        "v_mov_b32_e32 v43, v11\n\t"
        "v_max_f32_e32 v44, v42, v43\n\t"
        "v_max_f32_e32 v42, v42, v43\n\t"
        "s_pack_ll_b32_b16 s34, s52, s24\n\t"
        "v_mov_b32_e32 v40, s34\n\t"
        "s_add_u32 s35, s26, s57\n\t"
        "v_permlane16_swap_b32_e32 v42, v44\n\t"
        "v_max_f32_e32 v45, v42, v44\n\t"
        "v_max_f32_e32 v42, v42, v44\n\t"
        "s_mov_b64 exec, 1\n\t"
        "buffer_store_dword v40, off, s[8:11], s35 sc1\n\t"
        "s_mov_b64 exec, -1\n\t"
        "v_permlane32_swap_b32_e32 v42, v45\n\t"
        "v_max_f32_e32 v42, v42, v45\n\t"
        "v_mul_f32_e32 v42, 0xbfb8aa3b, v42\n\t"
        "v_pk_fma_f32 v[0:1], v[0:1], v[46:47], v[42:43] op_sel:[0,1,0] op_sel_hi:[1,1,0]\n\t"
        "s_add_u32 m0, s36, 9216\n\t"
        "s_add_u32 s34, s37, 12772352\n\t"
        "buffer_load_dwordx4 v122, s[12:15], s34 offen sc1 lds\n\t"
        "v_exp_f32_e32 v0, v0\n\t"
        "v_exp_f32_e32 v1, v1\n\t"
        "v_pk_fma_f32 v[2:3], v[2:3], v[46:47], v[42:43] op_sel:[0,1,0] op_sel_hi:[1,1,0]\n\t"
        "v_exp_f32_e32 v2, v2\n\t"
        "v_exp_f32_e32 v3, v3\n\t"
        "v_pk_fma_f32 v[4:5], v[4:5], v[46:47], v[42:43] op_sel:[0,1,0] op_sel_hi:[1,1,0]\n\t"
        "v_exp_f32_e32 v4, v4\n\t"
        "s_add_u32 m0, s36, 16384\n\t"
        "s_add_u32 s34, s37, 46325760\n\t"
        "buffer_load_dwordx4 v122, s[12:15], s34 offen sc1 lds\n\t"
        "v_exp_f32_e32 v5, v5\n\t"
        "v_pk_fma_f32 v[6:7], v[6:7], v[46:47], v[42:43] op_sel:[0,1,0] op_sel_hi:[1,1,0]\n\t"
        "v_exp_f32_e32 v6, v6\n\t"
        "v_exp_f32_e32 v7, v7\n\t"
        "v_pk_fma_f32 v[8:9], v[8:9], v[46:47], v[42:43] op_sel:[0,1,0] op_sel_hi:[1,1,0]\n\t"
        "v_exp_f32_e32 v8, v8\n\t"
        "v_exp_f32_e32 v9, v9\n\t"
        "s_add_u32 m0, s36, 17408\n\t"
        "s_add_u32 s34, s37, 46326784\n\t"
        "buffer_load_dwordx4 v122, s[12:15], s34 offen sc1 lds\n\t"
        "v_pk_fma_f32 v[10:11], v[10:11], v[46:47], v[42:43] op_sel:[0,1,0] op_sel_hi:[1,1,0]\n\t"
        "v_exp_f32_e32 v10, v10\n\t"
        "v_exp_f32_e32 v11, v11\n\t"
        "v_pk_add_f32 v[42:43], v[0:1], v[2:3]\n\t"
        "v_cndmask_b32_e64 v8, v8, 0, s[40:41]\n\t"
        "v_cndmask_b32_e64 v9, v9, 0, s[40:41]\n\t"
        "v_cndmask_b32_e64 v10, v10, 0, s[40:41]\n\t"
        "buffer_store_dwordx4 v[36:39], v121, s[8:11], s28 offen sc1\n\t"
        "v_cndmask_b32_e64 v11, v11, 0, s[40:41]\n\t"
        "v_pk_add_f32 v[42:43], v[42:43], v[4:5]\n\t"
        "v_pk_add_f32 v[42:43], v[42:43], v[6:7]\n\t"
        "v_pk_add_f32 v[42:43], v[42:43], v[8:9]\n\t"
        "v_pk_add_f32 v[42:43], v[42:43], v[10:11]\n\t"
        "v_add_f32_e32 v44, v42, v43\n\t"
        "v_add_f32_e32 v42, v42, v43\n\t"
        "s_lshl_b32 s34, s24, 18\n\t"
        "s_add_u32 s34, s34, 63102976\n\t"
        "v_permlane16_swap_b32_e32 v42, v44\n\t"
        "v_add_f32_e32 v45, v42, v44\n\t"
        "v_add_f32_e32 v42, v42, v44\n\t"
        "s_add_u32 s35, s24, 1\n\t"
        "v_mov_b32_e32 v126, s35\n\t"
        "v_permlane32_swap_b32_e32 v42, v45\n\t"
        "v_add_f32_e32 v42, v42, v45\n\t"
        "v_rcp_f32_e32 v42, v42\n\t"
        "s_waitcnt vmcnt(8) lgkmcnt(0)\n\t"
        "v_max3_u32 v45, v12, v13, v14\n\t"
        "v_max3_u32 v45, v45, v15, v104\n\t"
        "v_max3_u32 v45, v45, v105, v106\n\t"
        "v_max_u32_e32 v45, v45, v107\n\t"
        "v_cmp_eq_u32_e32 vcc, -1, v45\n\t"
        "s_nop 0\n\t"
        "s_cbranch_vccnz LA_v2fail%=\n\t"
        "LA_v2ok%=:\n\t"
        "v_and_b32_e32 v45, 0xff, v109\n\t"
        "v_cmp_ne_u32_e32 vcc, s24, v45\n\t"
        "s_nop 0\n\t"
        "s_cbranch_vccnz LA_stre%=\n\t"
        "LA_stok%=:\n\t"
        "v_pk_mul_f32 v[0:1], v[0:1], v[42:43] op_sel_hi:[1,0]\n\t"
        "v_subrev_u32_e32 v108, s25, v108\n\t"
        "v_min_u32_dpp v111, v111, v111 quad_perm:[1,0,3,2] row_mask:0xf bank_mask:0xf\n\t"
        "v_cvt_pk_bf16_f32 v32, v0, v1\n\t"
        "v_pk_mul_f32 v[2:3], v[2:3], v[42:43] op_sel_hi:[1,0]\n\t"
        "v_cvt_pk_bf16_f32 v33, v2, v3\n\t"
        "v_max_i32_dpp v108, v108, v108 quad_perm:[1,0,3,2] row_mask:0xf bank_mask:0xf\n\t"
        "v_min_u32_dpp v111, v111, v111 quad_perm:[2,3,0,1] row_mask:0xf bank_mask:0xf\n\t"
        "v_pk_mul_f32 v[4:5], v[4:5], v[42:43] op_sel_hi:[1,0]\n\t"
        "v_cvt_pk_bf16_f32 v34, v4, v5\n\t"
        "v_pk_mul_f32 v[6:7], v[6:7], v[42:43] op_sel_hi:[1,0]\n\t"
        "v_max_i32_dpp v108, v108, v108 quad_perm:[2,3,0,1] row_mask:0xf bank_mask:0xf\n\t"
        "v_min_u32_dpp v111, v111, v111 row_ror:4 row_mask:0xf bank_mask:0xf\n\t"
        "v_cvt_pk_bf16_f32 v35, v6, v7\n\t"
        "v_pk_mul_f32 v[8:9], v[8:9], v[42:43] op_sel_hi:[1,0]\n\t"
        "v_cvt_pk_bf16_f32 v36, v8, v9\n\t"
        "v_max_i32_dpp v108, v108, v108 row_ror:4 row_mask:0xf bank_mask:0xf\n\t"
        "v_min_u32_dpp v111, v111, v111 row_ror:8 row_mask:0xf bank_mask:0xf\n\t"
        "v_pk_mul_f32 v[10:11], v[10:11], v[42:43] op_sel_hi:[1,0]\n\t"
        "v_cvt_pk_bf16_f32 v37, v10, v11\n\t"
        "v_mfma_f32_16x16x32_bf16 v[16:19], v[48:51], v[32:35], 0\n\t"
        "v_max_i32_dpp v108, v108, v108 row_ror:8 row_mask:0xf bank_mask:0xf\n\t"
        "v_mfma_f32_16x16x32_bf16 v[20:23], v[52:55], v[32:35], 0\n\t"
        "v_mfma_f32_16x16x32_bf16 v[24:27], v[56:59], v[32:35], 0\n\t"
        "v_mfma_f32_16x16x32_bf16 v[28:31], v[60:63], v[32:35], 0\n\t"
        "v_max_i32_dpp v108, v108, v108 row_bcast:15 row_mask:0xa bank_mask:0xf\n\t"
        "v_xor_b32_e32 v45, 0x400, v123\n\t"
        "v_add_u32_e32 v124, s56, v124\n\t"
        "v_mfma_f32_16x16x16_bf16 v[16:19], v[104:105], v[36:37], v[16:19]\n\t"
        "v_max_i32_dpp v108, v108, v108 row_bcast:31 row_mask:0xc bank_mask:0xf\n\t"
        "v_mfma_f32_16x16x16_bf16 v[20:23], v[106:107], v[36:37], v[20:23]\n\t"
        "v_mfma_f32_16x16x16_bf16 v[24:27], v[12:13], v[36:37], v[24:27]\n\t"
        "v_mfma_f32_16x16x16_bf16 v[28:31], v[14:15], v[36:37], v[28:31]\n\t"
        "ds_read_b128 v[48:51], v117 offset:4096\n\t"
        "ds_read_b128 v[52:55], v117 offset:5120\n\t"
        "ds_read_b128 v[56:59], v117 offset:6144\n\t"
        "ds_read_b128 v[60:63], v117 offset:7168\n\t"
        "s_sub_u32 s56, 0, s56\n\t"
        "v_cvt_pk_bf16_f32 v0, v16, v17\n\t"
        "v_cvt_pk_bf16_f32 v1, v18, v19\n\t"
        "v_cvt_pk_bf16_f32 v2, v20, v21\n\t"
        "v_cvt_pk_bf16_f32 v3, v22, v23\n\t"
        "buffer_store_dwordx4 v[0:3], v123, s[12:15], s34 offen sc1\n\t"
        "v_cvt_pk_bf16_f32 v4, v24, v25\n\t"
        "v_cvt_pk_bf16_f32 v5, v26, v27\n\t"
        "v_cvt_pk_bf16_f32 v6, v28, v29\n\t"
        "v_cvt_pk_bf16_f32 v7, v30, v31\n\t"
        "buffer_store_dwordx4 v[4:7], v45, s[12:15], s34 offen sc1\n\t"
        "s_cmp_eq_u32 s24, 63\n\t"
        "s_cbranch_scc1 LA_last%=\n\t"
        "ds_read_b128 v[32:35], v117 offset:0\n\t"
        "ds_read_b128 v[36:39], v117 offset:1024\n\t"
        "ds_read_b128 v[40:43], v117 offset:2048\n\t"
        "ds_read_b128 v[44:47], v117 offset:3072\n\t"
        "v_mov_b64_e32 v[14:15], 0\n\t"
        "v_mov_b64_e32 v[12:13], 0\n\t"
        "v_mov_b64_e32 v[10:11], 0\n\t"
        "v_mov_b64_e32 v[8:9], 0\n\t"
        "v_mov_b64_e32 v[6:7], 0\n\t"
        "v_mov_b64_e32 v[4:5], 0\n\t"
        "v_mov_b64_e32 v[2:3], 0\n\t"
        "v_mov_b64_e32 v[0:1], 0\n\t"
        "v_readfirstlane_b32 s34, v111\n\t"
        "s_nop 0\n\t"
        "s_max_u32 s54, s54, s34\n\t"
        "v_readfirstlane_b32 s35, v109\n\t"
        "v_readlane_b32 s36, v108, 63\n\t"
        "s_bfe_u32 s29, s35, 0xc0008\n\t"
        "s_lshr_b32 s30, s35, 20\n\t"
        "s_add_u32 s35, s31, s50\n\t"
        "s_mul_i32 s37, s50, 3\n\t"
        "s_min_u32 s35, s35, s37\n\t"
        "s_add_i32 s34, s36, s51\n\t"
        "s_cmp_gt_i32 s34, 0\n\t"
        "s_cselect_b32 s31, s35, 0\n\t"
        "s_cselect_b32 s34, s34, 0\n\t"
        "s_add_u32 s34, s34, s31\n\t"
        "s_add_u32 s37, s25, s34\n\t"
        "s_add_u32 s45, s37, s29\n\t"
        "s_add_u32 s25, s45, s30\n\t"
        "s_add_u32 s24, s24, 1\n\t"
        "s_mov_b32 s28, s26\n\t"
        "s_mov_b32 s26, s27\n\t"
        "s_add_u32 s27, s27, 0x80000\n\t"
        "s_and_b32 s27, s27, 0x1fffff\n\t"
        "s_branch LA_loop%=\n\t"
        "LA_last%=:\n\t"
        "s_waitcnt vmcnt(0)\n\t"
        "s_barrier\n\t"
        "buffer_store_dword v126, v127, s[12:15], 0 offen sc1\n\t"
        "s_endpgm\n\t"
        "LA_skip%=:\n\t"
        "buffer_store_dword v126, v127, s[12:15], 0 offen sc1\n\t"
        "ds_read_b64_tr_b16 v[32:33], v119 offset:2112\n\t"
        "ds_read_b64_tr_b16 v[34:35], v119 offset:2144\n\t"
        "ds_read_b64_tr_b16 v[36:37], v119 offset:2368\n\t"
        "ds_read_b64_tr_b16 v[38:39], v119 offset:2400\n\t"
        "ds_read_b64_tr_b16 v[40:41], v119 offset:2624\n\t"
        "ds_read_b64_tr_b16 v[42:43], v119 offset:2656\n\t"
        "ds_read_b64_tr_b16 v[44:45], v119 offset:2880\n\t"
        "ds_read_b64_tr_b16 v[46:47], v119 offset:2912\n\t"
        "ds_read_b128 v[48:51], v108 offset:16384\n\t"
        "ds_read_b128 v[52:55], v108 offset:17408\n\t"
        "s_waitcnt lgkmcnt(0)\n\t"
        "s_branch LA_x%=\n\t"
        "LA_stre%=:\n\t"
        "s_mov_b32 s42, 0\n\t"
        "LA_str2%=:\n\t"
        "buffer_load_dwordx2 v[108:109], v125, s[8:11], s26 offen sc1\n\t"
        "s_waitcnt vmcnt(0)\n\t"
        "v_and_b32_e32 v45, 0xff, v109\n\t"
        "v_cmp_ne_u32_e32 vcc, s24, v45\n\t"
        "s_nop 0\n\t"
        "s_cbranch_vccz LA_stok%=\n\t"
        "s_add_u32 s42, s42, 1\n\t"
        "s_cmp_lt_u32 s42, 0x100000\n\t"
        "s_cbranch_scc1 LA_str2%=\n\t"
        "s_movk_i32 s34, 5\n\t"
        "s_branch LA_die%=\n\t"
        "LA_v2fail%=:\n\t"
        "s_mov_b32 s42, 0\n\t"
        "LA_v2re%=:\n\t"
        "v_xor_b32_e32 v45, 0x200, v120\n\t"
        "buffer_load_dwordx4 v[12:15], v45, s[8:11], s26 offen sc1\n\t"
        "buffer_load_dwordx4 v[104:107], v120, s[8:11], s26 offen sc1\n\t"
        "s_waitcnt vmcnt(0)\n\t"
        "v_max3_u32 v45, v12, v13, v14\n\t"
        "v_max3_u32 v45, v45, v15, v104\n\t"
        "v_max3_u32 v45, v45, v105, v106\n\t"
        "v_max_u32_e32 v45, v45, v107\n\t"
        "v_cmp_eq_u32_e32 vcc, -1, v45\n\t"
        "s_nop 0\n\t"
        "s_cbranch_vccz LA_v2ok%=\n\t"
        "s_add_u32 s42, s42, 1\n\t"
        "s_cmp_lt_u32 s42, 0x100000\n\t"
        "s_cbranch_scc1 LA_v2re%=\n\t"
        "s_movk_i32 s34, 6\n\t"
        "s_branch LA_die%=\n\t"
        "LA_rdfail%=:\n\t"
        "s_or_b32 s47, s47, 1\n\t"
        "buffer_load_dwordx4 v[16:19], v112, s[8:11], s26 offen offset:0 sc1\n\t"
        "buffer_load_dwordx4 v[20:23], v112, s[8:11], s26 offen offset:1024 sc1\n\t"
        "buffer_load_dwordx4 v[24:27], v112, s[8:11], s26 offen offset:2048 sc1\n\t"
        "buffer_load_dwordx4 v[28:31], v112, s[8:11], s26 offen offset:3072 sc1\n\t"
        "s_nop 7\n\t"
        "v_mov_b64_e32 v[0:1], 0\n\t"
        "v_mov_b64_e32 v[2:3], 0\n\t"
        "v_mov_b64_e32 v[4:5], 0\n\t"
        "v_mov_b64_e32 v[6:7], 0\n\t"
        "v_mov_b64_e32 v[8:9], 0\n\t"
        "v_mov_b64_e32 v[10:11], 0\n\t"
        "v_mov_b64_e32 v[12:13], 0\n\t"
        "v_mov_b64_e32 v[14:15], 0\n\t"
        "s_branch LA_rdre%=w\n\t"
        "LA_rdre%=:\n\t"
        "buffer_load_dwordx4 v[16:19], v112, s[8:11], s26 offen offset:0 sc1\n\t"
        "buffer_load_dwordx4 v[20:23], v112, s[8:11], s26 offen offset:1024 sc1\n\t"
        "buffer_load_dwordx4 v[24:27], v112, s[8:11], s26 offen offset:2048 sc1\n\t"
        "buffer_load_dwordx4 v[28:31], v112, s[8:11], s26 offen offset:3072 sc1\n\t"
        "LA_rdre%=w:\n\t"
        "s_waitcnt vmcnt(0) lgkmcnt(0)\n\t"
        "v_max3_u32 v111, v16, v17, v18\n\t"
        "v_max3_u32 v111, v111, v19, v20\n\t"
        "v_max3_u32 v111, v111, v21, v22\n\t"
        "v_max3_u32 v111, v111, v23, v24\n\t"
        "v_max3_u32 v111, v111, v25, v26\n\t"
        "v_max3_u32 v111, v111, v27, v28\n\t"
        "v_max3_u32 v111, v111, v29, v30\n\t"
        "v_max_u32_e32 v111, v111, v31\n\t"
        "v_cmp_eq_u32_e32 vcc, -1, v111\n\t"
        "s_nop 0\n\t"
        "s_cbranch_vccz LA_go%=\n\t"
        "s_add_u32 s42, s42, 1\n\t"
        "s_cmp_lt_u32 s42, 0x100000\n\t"
        "s_cbranch_scc1 LA_rdre%=\n\t"
        "s_movk_i32 s34, 1\n\t"
        "s_branch LA_die%=\n\t"
        "LA_f2%=:\n\t"
        "s_or_b32 s47, s47, 1\n\t"
        "buffer_load_dwordx4 v[24:27], v112, s[8:11], s26 offen offset:2048 sc1\n\t"
        "s_nop 7\n\t"
        "s_branch LA_r2%=w\n\t"
        "LA_r2%=:\n\t"
        "buffer_load_dwordx4 v[24:27], v112, s[8:11], s26 offen offset:2048 sc1\n\t"
        "LA_r2%=w:\n\t"
        "s_waitcnt vmcnt(0) lgkmcnt(0)\n\t"
        "v_max3_u32 v111, v24, v25, v26\n\t"
        "v_max_u32_e32 v111, v111, v27\n\t"
        "v_cmp_eq_u32_e32 vcc, -1, v111\n\t"
        "s_nop 0\n\t"
        "s_cbranch_vccz LA_go%=\n\t"
        "s_add_u32 s42, s42, 1\n\t"
        "s_cmp_lt_u32 s42, 0x100000\n\t"
        "s_cbranch_scc1 LA_r2%=\n\t"
        "s_movk_i32 s34, 1\n\t"
        "s_branch LA_die%=\n\t"
        "LA_f3%=:\n\t"
        "s_or_b32 s47, s47, 1\n\t"
        "buffer_load_dwordx4 v[28:31], v112, s[8:11], s26 offen offset:3072 sc1\n\t"
        "s_nop 7\n\t"
        "s_branch LA_r3%=w\n\t"
        "LA_r3%=:\n\t"
        "buffer_load_dwordx4 v[28:31], v112, s[8:11], s26 offen offset:3072 sc1\n\t"
        "LA_r3%=w:\n\t"
        "s_waitcnt vmcnt(0) lgkmcnt(0)\n\t"
        "v_max3_u32 v111, v28, v29, v30\n\t"
        "v_max_u32_e32 v111, v111, v31\n\t"
        "v_cmp_eq_u32_e32 vcc, -1, v111\n\t"
        "s_nop 0\n\t"
        "s_cbranch_vccz LA_go%=\n\t"
        "s_add_u32 s42, s42, 1\n\t"
        "s_cmp_lt_u32 s42, 0x100000\n\t"
        "s_cbranch_scc1 LA_r3%=\n\t"
        "s_movk_i32 s34, 1\n\t"
        "s_branch LA_die%=\n\t"
        "LA_vmfail%=:\n\t"
        "s_or_b32 s47, s47, 2\n\t"
        "s_mov_b32 s42, 0\n\t"
        "LA_vmre%=:\n\t"
        "buffer_load_dwordx4 v[104:107], v120, s[8:11], s26 offen sc1\n\t"
        "v_mfma_f32_16x16x32_bf16 v[4:7], v[48:51], v[12:15], 0\n\t"
        "v_mfma_f32_16x16x32_bf16 v[8:11], v[52:55], v[12:15], 0\n\t"
        "s_nop 7\n\t"
        "s_waitcnt vmcnt(0)\n\t"
        "v_mfma_f32_16x16x16_bf16 v[4:7], v[104:105], v[56:57], v[4:7]\n\t"
        "v_mfma_f32_16x16x16_bf16 v[8:11], v[106:107], v[56:57], v[8:11]\n\t"
        "v_max3_u32 v111, v104, v105, v106\n\t"
        "v_max_u32_e32 v111, v111, v107\n\t"
        "v_cmp_eq_u32_e32 vcc, -1, v111\n\t"
        "s_nop 7\n\t"
        "s_cbranch_vccz LA_vmok%=\n\t"
        "s_add_u32 s42, s42, 1\n\t"
        "s_cmp_lt_u32 s42, 0x100000\n\t"
        "s_cbranch_scc1 LA_vmre%=\n\t"
        "s_movk_i32 s34, 2\n\t"
        "s_branch LA_die%=\n\t"
        "LA_die%=:\n\t"
        "v_mov_b32_e32 v30, s34\n\t"
        "s_mov_b32 s35, 33024\n\t"
        "s_mov_b64 exec, 1\n\t"
        "buffer_store_dword v30, off, s[12:15], s35 sc1\n\t"
        "s_endpgm\n\t"
        ".Lk_end_A:\n\t"
        :: REC_S_IN, "{v96}"(g_w), "{v97}"(wb0), "{v98}"(wb1), "{v99}"(rb0), "{v100}"(rb0 ^ 32u), "{v101}"(rb0 ^ 64u), "{v102}"(rb0 ^ 96u),
           "{v112}"(a_mem), "{v113}"(l_ms + w * 4096), "{v114}"(l_ms + ((w + 1) & 3) * 4096), "{v115}"(l_ms + ((w + 2) & 3) * 4096), "{v116}"(l_ms + ((w + 3) & 3) * 4096),
           "{v117}"(lb + w * 22528 + l * 16), "{v118}"(l_iw), "{v119}"(l_iq), "{v120}"(a_vm), "{v121}"(a_o), "{v122}"(h * 8192 + l * 16), "{v123}"(a_ox), "{v124}"(lb + L_KV + l * 16), "{s55}"(lb + L_KV + w * 2048),
           "{v125}"((24 + cp) * 16384 + l * ST_P), "{v126}"(16 * (w & 1) + n - 4 * g), "{v103}"((l & 1) ? 0xEEEEEEEEu : 0x44444444u),
           "{v127}"((l == 0 && w == 0) ? F_PROG + h * 128 : OOB)
        : "memory", "vcc", "scc", "v0", "v255", "a0", "a255", "s101");
    __builtin_unreachable();
}
__device__ __forceinline__ void rec_b(const Params& p, unsigned lb, const int wv, const int id, const int rg, const int cp, const int rot, const int bq) {
    const int w = wv, l = otid() & 63, n = l & 15, g = l >> 4, cs = id, rb = 32 + id * 2 + rg;
    const v4u sh = mk_srd(p.ws + hx_base(p.ws), HX_BYTES - 8192), sw = mk_srd(p.ws, W_TOTAL), s1 = mk_srd(p.Wv, 1024 * 4096 * 2), s2 = mk_srd(p.Wo, 1024 * 4096 * 2);
    const int r8 = l >> 3, pc = l & 7, pp = n >> 3, bb = (n >> 2) & 1, f = (g & 1) | (pp << 1);
    const unsigned stg = lb + LB_STG + w * LB_STGP;
    const unsigned g_v = r8 * 8192 + cs * 256 + rg * 128 + pc * 16, g_o = (l >> 2) * 2048 + cs * 64 + (l & 3) * 16;
    const unsigned wb0 = stg + r8 * 128 + (((pc >> 1) ^ (r8 >> 2)) << 5) + (pc & 1) * 16, wb1 = wb0 ^ 64u;
    const unsigned rb0 = stg + (16 * (g >> 1) + 8 * pp + 4 * (g & 1) + bb) * 128 + (f << 5) + (n & 3) * 8;
    const unsigned o_r = stg + (g >> 1) * (2048 + 128) + (g & 1) * (1024 + 32) + (bb + 4 * pp) * 64 + (n & 3) * 8;
    const unsigned a_mem = cp * 32768 + (w >> 1) * 16384 + (w & 1) * 4096 + l * 16;
    const unsigned a_vmw = w < 2 ? (16 + (cs >> 2)) * 16384 + (cs & 3) * 2048 + (2 * rg + w) * 512 + (l & 31) * 16 + (g >> 1) * 8 : OOB;
    const unsigned a_or = (8 + rg * 4 + w) * 16384 + l * 16;
    const unsigned a_mw = (RPL != 0 && RPL != 4 && w == 3) ? OOB : w * 32768 + (cs >> 4) * 16384 + ((cs >> 1) & 7) * 1024 + (cs & 1) * 512 + (l >> 5) * 256 + rg * 128 + (l & 31) * 4;
    const unsigned a_sw = (w == 0 && l < 4) ? (24 + l) * 16384 + (id * 2 + rg) * ST_P : OOB;
    const bf16_t* m0p = p.mem0 + (4 * rg + ((l >> 3) & 3)) * 1024 + cs * 32 + (l >> 5) * 16 + (l & 7) * 2;
    asm volatile(
        "global_load_dword v109, v[108:109], off\n\t"
        "s_add_u32 s35, s68, 0\n\t"
        "s_and_b32 s35, s35, 7\n\t"
        "s_lshl_b32 s69, s35, 10\n\t"
        "s_add_u32 s35, s68, 2\n\t"
        "s_and_b32 s35, s35, 7\n\t"
        "s_lshl_b32 s70, s35, 10\n\t"
        "s_add_u32 s35, s68, 4\n\t"
        "s_and_b32 s35, s35, 7\n\t"
        "s_lshl_b32 s71, s35, 10\n\t"
        "s_add_u32 s35, s68, 6\n\t"
        "s_and_b32 s35, s35, 7\n\t"
        "s_lshl_b32 s72, s35, 10\n\t"
        "s_lshl_b32 s34, s43, 21\n\t"
        "s_add_u32 s34, s34, 0\n\t"
        "buffer_load_dwordx4 v[128:131], v96, s[16:19], s34 offen nt\n\t"
        "s_add_u32 s34, s34, 0x10000\n\t"
        "buffer_load_dwordx4 v[132:135], v96, s[16:19], s34 offen nt\n\t"
        "s_add_u32 s34, s34, 0x10000\n\t"
        "buffer_load_dwordx4 v[136:139], v96, s[16:19], s34 offen nt\n\t"
        "s_add_u32 s34, s34, 0x10000\n\t"
        "buffer_load_dwordx4 v[140:143], v96, s[16:19], s34 offen nt\n\t"
        "s_add_u32 s34, s34, 0x10000\n\t"
        "buffer_load_dwordx4 v[144:147], v96, s[16:19], s34 offen nt\n\t"
        "s_add_u32 s34, s34, 0x10000\n\t"
        "buffer_load_dwordx4 v[148:151], v96, s[16:19], s34 offen nt\n\t"
        "s_add_u32 s34, s34, 0x10000\n\t"
        "buffer_load_dwordx4 v[152:155], v96, s[16:19], s34 offen nt\n\t"
        "s_add_u32 s34, s34, 0x10000\n\t"
        "buffer_load_dwordx4 v[156:159], v96, s[16:19], s34 offen nt\n\t"
        "s_lshl_b32 s34, s43, 21\n\t"
        "s_add_u32 s34, s34, 524288\n\t"
        "buffer_load_dwordx4 v[160:163], v96, s[16:19], s34 offen nt\n\t"
        "s_add_u32 s34, s34, 0x10000\n\t"
        "buffer_load_dwordx4 v[164:167], v96, s[16:19], s34 offen nt\n\t"
        "s_add_u32 s34, s34, 0x10000\n\t"
        "buffer_load_dwordx4 v[168:171], v96, s[16:19], s34 offen nt\n\t"
        "s_add_u32 s34, s34, 0x10000\n\t"
        "buffer_load_dwordx4 v[172:175], v96, s[16:19], s34 offen nt\n\t"
        "s_add_u32 s34, s34, 0x10000\n\t"
        "buffer_load_dwordx4 v[176:179], v96, s[16:19], s34 offen nt\n\t"
        "s_add_u32 s34, s34, 0x10000\n\t"
        "buffer_load_dwordx4 v[180:183], v96, s[16:19], s34 offen nt\n\t"
        "s_add_u32 s34, s34, 0x10000\n\t"
        "buffer_load_dwordx4 v[184:187], v96, s[16:19], s34 offen nt\n\t"
        "s_add_u32 s34, s34, 0x10000\n\t"
        "buffer_load_dwordx4 v[188:191], v96, s[16:19], s34 offen nt\n\t"
        "s_lshl_b32 s34, s43, 21\n\t"
        "s_add_u32 s34, s34, 1048576\n\t"
        "buffer_load_dwordx4 v[192:195], v96, s[16:19], s34 offen nt\n\t"
        "s_add_u32 s34, s34, 0x10000\n\t"
        "buffer_load_dwordx4 v[196:199], v96, s[16:19], s34 offen nt\n\t"
        "s_add_u32 s34, s34, 0x10000\n\t"
        "buffer_load_dwordx4 v[200:203], v96, s[16:19], s34 offen nt\n\t"
        "s_add_u32 s34, s34, 0x10000\n\t"
        "buffer_load_dwordx4 v[204:207], v96, s[16:19], s34 offen nt\n\t"
        "s_add_u32 s34, s34, 0x10000\n\t"
        "buffer_load_dwordx4 v[208:211], v96, s[16:19], s34 offen nt\n\t"
        "s_add_u32 s34, s34, 0x10000\n\t"
        "buffer_load_dwordx4 v[212:215], v96, s[16:19], s34 offen nt\n\t"
        "s_add_u32 s34, s34, 0x10000\n\t"
        "buffer_load_dwordx4 v[216:219], v96, s[16:19], s34 offen nt\n\t"
        "s_add_u32 s34, s34, 0x10000\n\t"
        "buffer_load_dwordx4 v[220:223], v96, s[16:19], s34 offen nt\n\t"
        "s_waitcnt vmcnt(16)\n\t"
        "ds_write_b128 v98, v[128:131] offset:0\n\t"
        "ds_write_b128 v99, v[132:135] offset:1024\n\t"
        "ds_write_b128 v98, v[136:139] offset:2048\n\t"
        "ds_write_b128 v99, v[140:143] offset:3072\n\t"
        "ds_write_b128 v98, v[144:147] offset:4096\n\t"
        "ds_write_b128 v99, v[148:151] offset:5120\n\t"
        "ds_write_b128 v98, v[152:155] offset:6144\n\t"
        "ds_write_b128 v99, v[156:159] offset:7168\n\t"
        "ds_read_b64_tr_b16 v[128:129], v100 offset:0\n\t"
        "ds_read_b64_tr_b16 v[130:131], v100 offset:256\n\t"
        "ds_read_b64_tr_b16 v[132:133], v100 offset:4096\n\t"
        "ds_read_b64_tr_b16 v[134:135], v100 offset:4352\n\t"
        "s_waitcnt lgkmcnt(8)\n\t"
        "ds_read_b64_tr_b16 v[136:137], v101 offset:0\n\t"
        "ds_read_b64_tr_b16 v[138:139], v101 offset:256\n\t"
        "ds_read_b64_tr_b16 v[140:141], v101 offset:4096\n\t"
        "ds_read_b64_tr_b16 v[142:143], v101 offset:4352\n\t"
        "s_waitcnt lgkmcnt(8)\n\t"
        "ds_read_b64_tr_b16 v[144:145], v102 offset:0\n\t"
        "ds_read_b64_tr_b16 v[146:147], v102 offset:256\n\t"
        "ds_read_b64_tr_b16 v[148:149], v102 offset:4096\n\t"
        "ds_read_b64_tr_b16 v[150:151], v102 offset:4352\n\t"
        "s_waitcnt lgkmcnt(8)\n\t"
        "ds_read_b64_tr_b16 v[152:153], v103 offset:0\n\t"
        "ds_read_b64_tr_b16 v[154:155], v103 offset:256\n\t"
        "ds_read_b64_tr_b16 v[156:157], v103 offset:4096\n\t"
        "ds_read_b64_tr_b16 v[158:159], v103 offset:4352\n\t"
        "s_lshl_b32 s34, s43, 21\n\t"
        "s_add_u32 s34, s34, 1572864\n\t"
        "buffer_load_dwordx4 v[224:227], v96, s[16:19], s34 offen nt\n\t"
        "s_add_u32 s34, s34, 0x10000\n\t"
        "buffer_load_dwordx4 v[228:231], v96, s[16:19], s34 offen nt\n\t"
        "s_add_u32 s34, s34, 0x10000\n\t"
        "buffer_load_dwordx4 v[232:235], v96, s[16:19], s34 offen nt\n\t"
        "s_add_u32 s34, s34, 0x10000\n\t"
        "buffer_load_dwordx4 v[236:239], v96, s[16:19], s34 offen nt\n\t"
        "s_add_u32 s34, s34, 0x10000\n\t"
        "buffer_load_dwordx4 v[240:243], v96, s[16:19], s34 offen nt\n\t"
        "s_add_u32 s34, s34, 0x10000\n\t"
        "buffer_load_dwordx4 v[244:247], v96, s[16:19], s34 offen nt\n\t"
        "s_add_u32 s34, s34, 0x10000\n\t"
        "buffer_load_dwordx4 v[248:251], v96, s[16:19], s34 offen nt\n\t"
        "s_add_u32 s34, s34, 0x10000\n\t"
        "buffer_load_dwordx4 v[252:255], v96, s[16:19], s34 offen nt\n\t"
        "s_waitcnt lgkmcnt(0)\n\t"
        "s_waitcnt vmcnt(16)\n\t"
        "ds_write_b128 v98, v[160:163] offset:0\n\t"
        "ds_write_b128 v99, v[164:167] offset:1024\n\t"
        "ds_write_b128 v98, v[168:171] offset:2048\n\t"
        "ds_write_b128 v99, v[172:175] offset:3072\n\t"
        "ds_write_b128 v98, v[176:179] offset:4096\n\t"
        "ds_write_b128 v99, v[180:183] offset:5120\n\t"
        "ds_write_b128 v98, v[184:187] offset:6144\n\t"
        "ds_write_b128 v99, v[188:191] offset:7168\n\t"
        "ds_read_b64_tr_b16 v[160:161], v100 offset:0\n\t"
        "ds_read_b64_tr_b16 v[162:163], v100 offset:256\n\t"
        "ds_read_b64_tr_b16 v[164:165], v100 offset:4096\n\t"
        "ds_read_b64_tr_b16 v[166:167], v100 offset:4352\n\t"
        "s_waitcnt lgkmcnt(8)\n\t"
        "ds_read_b64_tr_b16 v[168:169], v101 offset:0\n\t"
        "ds_read_b64_tr_b16 v[170:171], v101 offset:256\n\t"
        "ds_read_b64_tr_b16 v[172:173], v101 offset:4096\n\t"
        "ds_read_b64_tr_b16 v[174:175], v101 offset:4352\n\t"
        "s_waitcnt lgkmcnt(8)\n\t"
        "ds_read_b64_tr_b16 v[176:177], v102 offset:0\n\t"
        "ds_read_b64_tr_b16 v[178:179], v102 offset:256\n\t"
        "ds_read_b64_tr_b16 v[180:181], v102 offset:4096\n\t"
        "ds_read_b64_tr_b16 v[182:183], v102 offset:4352\n\t"
        "s_waitcnt lgkmcnt(8)\n\t"
        "ds_read_b64_tr_b16 v[184:185], v103 offset:0\n\t"
        "ds_read_b64_tr_b16 v[186:187], v103 offset:256\n\t"
        "ds_read_b64_tr_b16 v[188:189], v103 offset:4096\n\t"
        "ds_read_b64_tr_b16 v[190:191], v103 offset:4352\n\t"
        "s_add_u32 s35, s68, 0\n\t"
        "s_and_b32 s35, s35, 7\n\t"
        "s_lshl_b32 s35, s35, 18\n\t"
        "s_lshl_b32 s34, s43, 21\n\t"
        "s_add_u32 s34, s34, s35\n\t"
        "buffer_load_dwordx4 a[0:3], v97, s[20:23], s34 offen\n\t"
        "s_add_u32 s34, s34, 0x8000\n\t"
        "buffer_load_dwordx4 a[4:7], v97, s[20:23], s34 offen\n\t"
        "s_add_u32 s34, s34, 0x8000\n\t"
        "buffer_load_dwordx4 a[8:11], v97, s[20:23], s34 offen\n\t"
        "s_add_u32 s34, s34, 0x8000\n\t"
        "buffer_load_dwordx4 a[12:15], v97, s[20:23], s34 offen\n\t"
        "s_add_u32 s34, s34, 0x8000\n\t"
        "buffer_load_dwordx4 a[16:19], v97, s[20:23], s34 offen\n\t"
        "s_add_u32 s34, s34, 0x8000\n\t"
        "buffer_load_dwordx4 a[20:23], v97, s[20:23], s34 offen\n\t"
        "s_add_u32 s34, s34, 0x8000\n\t"
        "buffer_load_dwordx4 a[24:27], v97, s[20:23], s34 offen\n\t"
        "s_add_u32 s34, s34, 0x8000\n\t"
        "buffer_load_dwordx4 a[28:31], v97, s[20:23], s34 offen\n\t"
        "s_waitcnt lgkmcnt(0)\n\t"
        "s_waitcnt vmcnt(16)\n\t"
        "ds_write_b128 v98, v[192:195] offset:0\n\t"
        "ds_write_b128 v99, v[196:199] offset:1024\n\t"
        "ds_write_b128 v98, v[200:203] offset:2048\n\t"
        "ds_write_b128 v99, v[204:207] offset:3072\n\t"
        "ds_write_b128 v98, v[208:211] offset:4096\n\t"
        "ds_write_b128 v99, v[212:215] offset:5120\n\t"
        "ds_write_b128 v98, v[216:219] offset:6144\n\t"
        "ds_write_b128 v99, v[220:223] offset:7168\n\t"
        "ds_read_b64_tr_b16 v[192:193], v100 offset:0\n\t"
        "ds_read_b64_tr_b16 v[194:195], v100 offset:256\n\t"
        "ds_read_b64_tr_b16 v[196:197], v100 offset:4096\n\t"
        "ds_read_b64_tr_b16 v[198:199], v100 offset:4352\n\t"
        "s_waitcnt lgkmcnt(8)\n\t"
        "ds_read_b64_tr_b16 v[200:201], v101 offset:0\n\t"
        "ds_read_b64_tr_b16 v[202:203], v101 offset:256\n\t"
        "ds_read_b64_tr_b16 v[204:205], v101 offset:4096\n\t"
        "ds_read_b64_tr_b16 v[206:207], v101 offset:4352\n\t"
        "s_waitcnt lgkmcnt(8)\n\t"
        "ds_read_b64_tr_b16 v[208:209], v102 offset:0\n\t"
        "ds_read_b64_tr_b16 v[210:211], v102 offset:256\n\t"
        "ds_read_b64_tr_b16 v[212:213], v102 offset:4096\n\t"
        "ds_read_b64_tr_b16 v[214:215], v102 offset:4352\n\t"
        "s_waitcnt lgkmcnt(8)\n\t"
        "ds_read_b64_tr_b16 v[216:217], v103 offset:0\n\t"
        "ds_read_b64_tr_b16 v[218:219], v103 offset:256\n\t"
        "ds_read_b64_tr_b16 v[220:221], v103 offset:4096\n\t"
        "ds_read_b64_tr_b16 v[222:223], v103 offset:4352\n\t"
        "s_add_u32 s35, s68, 1\n\t"
        "s_and_b32 s35, s35, 7\n\t"
        "s_lshl_b32 s35, s35, 18\n\t"
        "s_lshl_b32 s34, s43, 21\n\t"
        "s_add_u32 s34, s34, s35\n\t"
        "buffer_load_dwordx4 a[32:35], v97, s[20:23], s34 offen\n\t"
        "s_add_u32 s34, s34, 0x8000\n\t"
        "buffer_load_dwordx4 a[36:39], v97, s[20:23], s34 offen\n\t"
        "s_add_u32 s34, s34, 0x8000\n\t"
        "buffer_load_dwordx4 a[40:43], v97, s[20:23], s34 offen\n\t"
        "s_add_u32 s34, s34, 0x8000\n\t"
        "buffer_load_dwordx4 a[44:47], v97, s[20:23], s34 offen\n\t"
        "s_add_u32 s34, s34, 0x8000\n\t"
        "buffer_load_dwordx4 a[48:51], v97, s[20:23], s34 offen\n\t"
        "s_add_u32 s34, s34, 0x8000\n\t"
        "buffer_load_dwordx4 a[52:55], v97, s[20:23], s34 offen\n\t"
        "s_add_u32 s34, s34, 0x8000\n\t"
        "buffer_load_dwordx4 a[56:59], v97, s[20:23], s34 offen\n\t"
        "s_add_u32 s34, s34, 0x8000\n\t"
        "buffer_load_dwordx4 a[60:63], v97, s[20:23], s34 offen\n\t"
        "s_waitcnt lgkmcnt(0)\n\t"
        "s_waitcnt vmcnt(16)\n\t"
        "ds_write_b128 v98, v[224:227] offset:0\n\t"
        "ds_write_b128 v99, v[228:231] offset:1024\n\t"
        "ds_write_b128 v98, v[232:235] offset:2048\n\t"
        "ds_write_b128 v99, v[236:239] offset:3072\n\t"
        "ds_write_b128 v98, v[240:243] offset:4096\n\t"
        "ds_write_b128 v99, v[244:247] offset:5120\n\t"
        "ds_write_b128 v98, v[248:251] offset:6144\n\t"
        "ds_write_b128 v99, v[252:255] offset:7168\n\t"
        "ds_read_b64_tr_b16 v[224:225], v100 offset:0\n\t"
        "ds_read_b64_tr_b16 v[226:227], v100 offset:256\n\t"
        "ds_read_b64_tr_b16 v[228:229], v100 offset:4096\n\t"
        "ds_read_b64_tr_b16 v[230:231], v100 offset:4352\n\t"
        "s_waitcnt lgkmcnt(8)\n\t"
        "ds_read_b64_tr_b16 v[232:233], v101 offset:0\n\t"
        "ds_read_b64_tr_b16 v[234:235], v101 offset:256\n\t"
        "ds_read_b64_tr_b16 v[236:237], v101 offset:4096\n\t"
        "ds_read_b64_tr_b16 v[238:239], v101 offset:4352\n\t"
        "s_waitcnt lgkmcnt(8)\n\t"
        "ds_read_b64_tr_b16 v[240:241], v102 offset:0\n\t"
        "ds_read_b64_tr_b16 v[242:243], v102 offset:256\n\t"
        "ds_read_b64_tr_b16 v[244:245], v102 offset:4096\n\t"
        "ds_read_b64_tr_b16 v[246:247], v102 offset:4352\n\t"
        "s_waitcnt lgkmcnt(8)\n\t"
        "ds_read_b64_tr_b16 v[248:249], v103 offset:0\n\t"
        "ds_read_b64_tr_b16 v[250:251], v103 offset:256\n\t"
        "ds_read_b64_tr_b16 v[252:253], v103 offset:4096\n\t"
        "ds_read_b64_tr_b16 v[254:255], v103 offset:4352\n\t"
        "s_add_u32 s35, s68, 2\n\t"
        "s_and_b32 s35, s35, 7\n\t"
        "s_lshl_b32 s35, s35, 18\n\t"
        "s_lshl_b32 s34, s43, 21\n\t"
        "s_add_u32 s34, s34, s35\n\t"
        "buffer_load_dwordx4 a[64:67], v97, s[20:23], s34 offen\n\t"
        "s_add_u32 s34, s34, 0x8000\n\t"
        "buffer_load_dwordx4 a[68:71], v97, s[20:23], s34 offen\n\t"
        "s_add_u32 s34, s34, 0x8000\n\t"
        "buffer_load_dwordx4 a[72:75], v97, s[20:23], s34 offen\n\t"
        "s_add_u32 s34, s34, 0x8000\n\t"
        "buffer_load_dwordx4 a[76:79], v97, s[20:23], s34 offen\n\t"
        "s_add_u32 s34, s34, 0x8000\n\t"
        "buffer_load_dwordx4 a[80:83], v97, s[20:23], s34 offen\n\t"
        "s_add_u32 s34, s34, 0x8000\n\t"
        "buffer_load_dwordx4 a[84:87], v97, s[20:23], s34 offen\n\t"
        "s_add_u32 s34, s34, 0x8000\n\t"
        "buffer_load_dwordx4 a[88:91], v97, s[20:23], s34 offen\n\t"
        "s_add_u32 s34, s34, 0x8000\n\t"
        "buffer_load_dwordx4 a[92:95], v97, s[20:23], s34 offen\n\t"
        "s_waitcnt lgkmcnt(0)\n\t"
        "s_waitcnt vmcnt(16)\n\t"
        "ds_write_b128 v122, a[0:3] offset:0\n\t"
        "ds_write_b128 v122, a[4:7] offset:1056\n\t"
        "ds_write_b128 v122, a[8:11] offset:2176\n\t"
        "ds_write_b128 v122, a[12:15] offset:3232\n\t"
        "ds_write_b128 v122, a[16:19] offset:4352\n\t"
        "ds_write_b128 v122, a[20:23] offset:5408\n\t"
        "ds_write_b128 v122, a[24:27] offset:6528\n\t"
        "ds_write_b128 v122, a[28:31] offset:7584\n\t"
        "ds_read_b64_tr_b16 a[0:1], v123 offset:0\n\t"
        "ds_read_b64_tr_b16 a[2:3], v123 offset:128\n\t"
        "ds_read_b64_tr_b16 a[4:5], v123 offset:4352\n\t"
        "ds_read_b64_tr_b16 a[6:7], v123 offset:4480\n\t"
        "s_waitcnt lgkmcnt(8)\n\t"
        "ds_read_b64_tr_b16 a[8:9], v123 offset:32\n\t"
        "ds_read_b64_tr_b16 a[10:11], v123 offset:160\n\t"
        "ds_read_b64_tr_b16 a[12:13], v123 offset:4384\n\t"
        "ds_read_b64_tr_b16 a[14:15], v123 offset:4512\n\t"
        "s_waitcnt lgkmcnt(8)\n\t"
        "ds_read_b64_tr_b16 a[16:17], v123 offset:512\n\t"
        "ds_read_b64_tr_b16 a[18:19], v123 offset:640\n\t"
        "ds_read_b64_tr_b16 a[20:21], v123 offset:4864\n\t"
        "ds_read_b64_tr_b16 a[22:23], v123 offset:4992\n\t"
        "s_waitcnt lgkmcnt(8)\n\t"
        "ds_read_b64_tr_b16 a[24:25], v123 offset:544\n\t"
        "ds_read_b64_tr_b16 a[26:27], v123 offset:672\n\t"
        "ds_read_b64_tr_b16 a[28:29], v123 offset:4896\n\t"
        "ds_read_b64_tr_b16 a[30:31], v123 offset:5024\n\t"
        "s_waitcnt lgkmcnt(8)\n\t"
        "s_add_u32 s35, s68, 3\n\t"
        "s_and_b32 s35, s35, 7\n\t"
        "s_lshl_b32 s35, s35, 18\n\t"
        "s_lshl_b32 s34, s43, 21\n\t"
        "s_add_u32 s34, s34, s35\n\t"
        "buffer_load_dwordx4 a[96:99], v97, s[20:23], s34 offen\n\t"
        "s_add_u32 s34, s34, 0x8000\n\t"
        "buffer_load_dwordx4 a[100:103], v97, s[20:23], s34 offen\n\t"
        "s_add_u32 s34, s34, 0x8000\n\t"
        "buffer_load_dwordx4 a[104:107], v97, s[20:23], s34 offen\n\t"
        "s_add_u32 s34, s34, 0x8000\n\t"
        "buffer_load_dwordx4 a[108:111], v97, s[20:23], s34 offen\n\t"
        "s_add_u32 s34, s34, 0x8000\n\t"
        "buffer_load_dwordx4 a[112:115], v97, s[20:23], s34 offen\n\t"
        "s_add_u32 s34, s34, 0x8000\n\t"
        "buffer_load_dwordx4 a[116:119], v97, s[20:23], s34 offen\n\t"
        "s_add_u32 s34, s34, 0x8000\n\t"
        "buffer_load_dwordx4 a[120:123], v97, s[20:23], s34 offen\n\t"
        "s_add_u32 s34, s34, 0x8000\n\t"
        "buffer_load_dwordx4 a[124:127], v97, s[20:23], s34 offen\n\t"
        "s_waitcnt lgkmcnt(0)\n\t"
        "s_waitcnt vmcnt(16)\n\t"
        "ds_write_b128 v122, a[32:35] offset:0\n\t"
        "ds_write_b128 v122, a[36:39] offset:1056\n\t"
        "ds_write_b128 v122, a[40:43] offset:2176\n\t"
        "ds_write_b128 v122, a[44:47] offset:3232\n\t"
        "ds_write_b128 v122, a[48:51] offset:4352\n\t"
        "ds_write_b128 v122, a[52:55] offset:5408\n\t"
        "ds_write_b128 v122, a[56:59] offset:6528\n\t"
        "ds_write_b128 v122, a[60:63] offset:7584\n\t"
        "ds_read_b64_tr_b16 a[32:33], v123 offset:0\n\t"
        "ds_read_b64_tr_b16 a[34:35], v123 offset:128\n\t"
        "ds_read_b64_tr_b16 a[36:37], v123 offset:4352\n\t"
        "ds_read_b64_tr_b16 a[38:39], v123 offset:4480\n\t"
        "s_waitcnt lgkmcnt(8)\n\t"
        "ds_read_b64_tr_b16 a[40:41], v123 offset:32\n\t"
        "ds_read_b64_tr_b16 a[42:43], v123 offset:160\n\t"
        "ds_read_b64_tr_b16 a[44:45], v123 offset:4384\n\t"
        "ds_read_b64_tr_b16 a[46:47], v123 offset:4512\n\t"
        "s_waitcnt lgkmcnt(8)\n\t"
        "ds_read_b64_tr_b16 a[48:49], v123 offset:512\n\t"
        "ds_read_b64_tr_b16 a[50:51], v123 offset:640\n\t"
        "ds_read_b64_tr_b16 a[52:53], v123 offset:4864\n\t"
        "ds_read_b64_tr_b16 a[54:55], v123 offset:4992\n\t"
        "s_waitcnt lgkmcnt(8)\n\t"
        "ds_read_b64_tr_b16 a[56:57], v123 offset:544\n\t"
        "ds_read_b64_tr_b16 a[58:59], v123 offset:672\n\t"
        "ds_read_b64_tr_b16 a[60:61], v123 offset:4896\n\t"
        "ds_read_b64_tr_b16 a[62:63], v123 offset:5024\n\t"
        "s_waitcnt lgkmcnt(8)\n\t"
        "s_add_u32 s35, s68, 4\n\t"
        "s_and_b32 s35, s35, 7\n\t"
        "s_lshl_b32 s35, s35, 18\n\t"
        "s_lshl_b32 s34, s43, 21\n\t"
        "s_add_u32 s34, s34, s35\n\t"
        "buffer_load_dwordx4 a[128:131], v97, s[20:23], s34 offen\n\t"
        "s_add_u32 s34, s34, 0x8000\n\t"
        "buffer_load_dwordx4 a[132:135], v97, s[20:23], s34 offen\n\t"
        "s_add_u32 s34, s34, 0x8000\n\t"
        "buffer_load_dwordx4 a[136:139], v97, s[20:23], s34 offen\n\t"
        "s_add_u32 s34, s34, 0x8000\n\t"
        "buffer_load_dwordx4 a[140:143], v97, s[20:23], s34 offen\n\t"
        "s_add_u32 s34, s34, 0x8000\n\t"
        "buffer_load_dwordx4 a[144:147], v97, s[20:23], s34 offen\n\t"
        "s_add_u32 s34, s34, 0x8000\n\t"
        "buffer_load_dwordx4 a[148:151], v97, s[20:23], s34 offen\n\t"
        "s_add_u32 s34, s34, 0x8000\n\t"
        "buffer_load_dwordx4 a[152:155], v97, s[20:23], s34 offen\n\t"
        "s_add_u32 s34, s34, 0x8000\n\t"
        "buffer_load_dwordx4 a[156:159], v97, s[20:23], s34 offen\n\t"
        "s_waitcnt lgkmcnt(0)\n\t"
        "s_waitcnt vmcnt(16)\n\t"
        "ds_write_b128 v122, a[64:67] offset:0\n\t"
        "ds_write_b128 v122, a[68:71] offset:1056\n\t"
        "ds_write_b128 v122, a[72:75] offset:2176\n\t"
        "ds_write_b128 v122, a[76:79] offset:3232\n\t"
        "ds_write_b128 v122, a[80:83] offset:4352\n\t"
        "ds_write_b128 v122, a[84:87] offset:5408\n\t"
        "ds_write_b128 v122, a[88:91] offset:6528\n\t"
        "ds_write_b128 v122, a[92:95] offset:7584\n\t"
        "ds_read_b64_tr_b16 a[64:65], v123 offset:0\n\t"
        "ds_read_b64_tr_b16 a[66:67], v123 offset:128\n\t"
        "ds_read_b64_tr_b16 a[68:69], v123 offset:4352\n\t"
        "ds_read_b64_tr_b16 a[70:71], v123 offset:4480\n\t"
        "s_waitcnt lgkmcnt(8)\n\t"
        "ds_read_b64_tr_b16 a[72:73], v123 offset:32\n\t"
        "ds_read_b64_tr_b16 a[74:75], v123 offset:160\n\t"
        "ds_read_b64_tr_b16 a[76:77], v123 offset:4384\n\t"
        "ds_read_b64_tr_b16 a[78:79], v123 offset:4512\n\t"
        "s_waitcnt lgkmcnt(8)\n\t"
        "ds_read_b64_tr_b16 a[80:81], v123 offset:512\n\t"
        "ds_read_b64_tr_b16 a[82:83], v123 offset:640\n\t"
        "ds_read_b64_tr_b16 a[84:85], v123 offset:4864\n\t"
        "ds_read_b64_tr_b16 a[86:87], v123 offset:4992\n\t"
        "s_waitcnt lgkmcnt(8)\n\t"
        "ds_read_b64_tr_b16 a[88:89], v123 offset:544\n\t"
        "ds_read_b64_tr_b16 a[90:91], v123 offset:672\n\t"
        "ds_read_b64_tr_b16 a[92:93], v123 offset:4896\n\t"
        "ds_read_b64_tr_b16 a[94:95], v123 offset:5024\n\t"
        "s_waitcnt lgkmcnt(8)\n\t"
        "s_add_u32 s35, s68, 5\n\t"
        "s_and_b32 s35, s35, 7\n\t"
        "s_lshl_b32 s35, s35, 18\n\t"
        "s_lshl_b32 s34, s43, 21\n\t"
        "s_add_u32 s34, s34, s35\n\t"
        "buffer_load_dwordx4 a[160:163], v97, s[20:23], s34 offen\n\t"
        "s_add_u32 s34, s34, 0x8000\n\t"
        "buffer_load_dwordx4 a[164:167], v97, s[20:23], s34 offen\n\t"
        "s_add_u32 s34, s34, 0x8000\n\t"
        "buffer_load_dwordx4 a[168:171], v97, s[20:23], s34 offen\n\t"
        "s_add_u32 s34, s34, 0x8000\n\t"
        "buffer_load_dwordx4 a[172:175], v97, s[20:23], s34 offen\n\t"
        "s_add_u32 s34, s34, 0x8000\n\t"
        "buffer_load_dwordx4 a[176:179], v97, s[20:23], s34 offen\n\t"
        "s_add_u32 s34, s34, 0x8000\n\t"
        "buffer_load_dwordx4 a[180:183], v97, s[20:23], s34 offen\n\t"
        "s_add_u32 s34, s34, 0x8000\n\t"
        "buffer_load_dwordx4 a[184:187], v97, s[20:23], s34 offen\n\t"
        "s_add_u32 s34, s34, 0x8000\n\t"
        "buffer_load_dwordx4 a[188:191], v97, s[20:23], s34 offen\n\t"
        "s_waitcnt lgkmcnt(0)\n\t"
        "s_waitcnt vmcnt(16)\n\t"
        "ds_write_b128 v122, a[96:99] offset:0\n\t"
        "ds_write_b128 v122, a[100:103] offset:1056\n\t"
        "ds_write_b128 v122, a[104:107] offset:2176\n\t"
        "ds_write_b128 v122, a[108:111] offset:3232\n\t"
        "ds_write_b128 v122, a[112:115] offset:4352\n\t"
        "ds_write_b128 v122, a[116:119] offset:5408\n\t"
        "ds_write_b128 v122, a[120:123] offset:6528\n\t"
        "ds_write_b128 v122, a[124:127] offset:7584\n\t"
        "ds_read_b64_tr_b16 a[96:97], v123 offset:0\n\t"
        "ds_read_b64_tr_b16 a[98:99], v123 offset:128\n\t"
        "ds_read_b64_tr_b16 a[100:101], v123 offset:4352\n\t"
        "ds_read_b64_tr_b16 a[102:103], v123 offset:4480\n\t"
        "s_waitcnt lgkmcnt(8)\n\t"
        "ds_read_b64_tr_b16 a[104:105], v123 offset:32\n\t"
        "ds_read_b64_tr_b16 a[106:107], v123 offset:160\n\t"
        "ds_read_b64_tr_b16 a[108:109], v123 offset:4384\n\t"
        "ds_read_b64_tr_b16 a[110:111], v123 offset:4512\n\t"
        "s_waitcnt lgkmcnt(8)\n\t"
        "ds_read_b64_tr_b16 a[112:113], v123 offset:512\n\t"
        "ds_read_b64_tr_b16 a[114:115], v123 offset:640\n\t"
        "ds_read_b64_tr_b16 a[116:117], v123 offset:4864\n\t"
        "ds_read_b64_tr_b16 a[118:119], v123 offset:4992\n\t"
        "s_waitcnt lgkmcnt(8)\n\t"
        "ds_read_b64_tr_b16 a[120:121], v123 offset:544\n\t"
        "ds_read_b64_tr_b16 a[122:123], v123 offset:672\n\t"
        "ds_read_b64_tr_b16 a[124:125], v123 offset:4896\n\t"
        "ds_read_b64_tr_b16 a[126:127], v123 offset:5024\n\t"
        "s_waitcnt lgkmcnt(8)\n\t"
        "s_add_u32 s35, s68, 6\n\t"
        "s_and_b32 s35, s35, 7\n\t"
        "s_lshl_b32 s35, s35, 18\n\t"
        "s_lshl_b32 s34, s43, 21\n\t"
        "s_add_u32 s34, s34, s35\n\t"
        "buffer_load_dwordx4 a[192:195], v97, s[20:23], s34 offen\n\t"
        "s_add_u32 s34, s34, 0x8000\n\t"
        "buffer_load_dwordx4 a[196:199], v97, s[20:23], s34 offen\n\t"
        "s_add_u32 s34, s34, 0x8000\n\t"
        "buffer_load_dwordx4 a[200:203], v97, s[20:23], s34 offen\n\t"
        "s_add_u32 s34, s34, 0x8000\n\t"
        "buffer_load_dwordx4 a[204:207], v97, s[20:23], s34 offen\n\t"
        "s_add_u32 s34, s34, 0x8000\n\t"
        "buffer_load_dwordx4 a[208:211], v97, s[20:23], s34 offen\n\t"
        "s_add_u32 s34, s34, 0x8000\n\t"
        "buffer_load_dwordx4 a[212:215], v97, s[20:23], s34 offen\n\t"
        "s_add_u32 s34, s34, 0x8000\n\t"
        "buffer_load_dwordx4 a[216:219], v97, s[20:23], s34 offen\n\t"
        "s_add_u32 s34, s34, 0x8000\n\t"
        "buffer_load_dwordx4 a[220:223], v97, s[20:23], s34 offen\n\t"
        "s_waitcnt lgkmcnt(0)\n\t"
        "s_waitcnt vmcnt(16)\n\t"
        "ds_write_b128 v122, a[128:131] offset:0\n\t"
        "ds_write_b128 v122, a[132:135] offset:1056\n\t"
        "ds_write_b128 v122, a[136:139] offset:2176\n\t"
        "ds_write_b128 v122, a[140:143] offset:3232\n\t"
        "ds_write_b128 v122, a[144:147] offset:4352\n\t"
        "ds_write_b128 v122, a[148:151] offset:5408\n\t"
        "ds_write_b128 v122, a[152:155] offset:6528\n\t"
        "ds_write_b128 v122, a[156:159] offset:7584\n\t"
        "ds_read_b64_tr_b16 a[128:129], v123 offset:0\n\t"
        "ds_read_b64_tr_b16 a[130:131], v123 offset:128\n\t"
        "ds_read_b64_tr_b16 a[132:133], v123 offset:4352\n\t"
        "ds_read_b64_tr_b16 a[134:135], v123 offset:4480\n\t"
        "s_waitcnt lgkmcnt(8)\n\t"
        "ds_read_b64_tr_b16 a[136:137], v123 offset:32\n\t"
        "ds_read_b64_tr_b16 a[138:139], v123 offset:160\n\t"
        "ds_read_b64_tr_b16 a[140:141], v123 offset:4384\n\t"
        "ds_read_b64_tr_b16 a[142:143], v123 offset:4512\n\t"
        "s_waitcnt lgkmcnt(8)\n\t"
        "ds_read_b64_tr_b16 a[144:145], v123 offset:512\n\t"
        "ds_read_b64_tr_b16 a[146:147], v123 offset:640\n\t"
        "ds_read_b64_tr_b16 a[148:149], v123 offset:4864\n\t"
        "ds_read_b64_tr_b16 a[150:151], v123 offset:4992\n\t"
        "s_waitcnt lgkmcnt(8)\n\t"
        "ds_read_b64_tr_b16 a[152:153], v123 offset:544\n\t"
        "ds_read_b64_tr_b16 a[154:155], v123 offset:672\n\t"
        "ds_read_b64_tr_b16 a[156:157], v123 offset:4896\n\t"
        "ds_read_b64_tr_b16 a[158:159], v123 offset:5024\n\t"
        "s_waitcnt lgkmcnt(8)\n\t"
        "s_add_u32 s35, s68, 7\n\t"
        "s_and_b32 s35, s35, 7\n\t"
        "s_lshl_b32 s35, s35, 18\n\t"
        "s_lshl_b32 s34, s43, 21\n\t"
        "s_add_u32 s34, s34, s35\n\t"
        "buffer_load_dwordx4 a[224:227], v97, s[20:23], s34 offen\n\t"
        "s_add_u32 s34, s34, 0x8000\n\t"
        "buffer_load_dwordx4 a[228:231], v97, s[20:23], s34 offen\n\t"
        "s_add_u32 s34, s34, 0x8000\n\t"
        "buffer_load_dwordx4 a[232:235], v97, s[20:23], s34 offen\n\t"
        "s_add_u32 s34, s34, 0x8000\n\t"
        "buffer_load_dwordx4 a[236:239], v97, s[20:23], s34 offen\n\t"
        "s_add_u32 s34, s34, 0x8000\n\t"
        "buffer_load_dwordx4 a[240:243], v97, s[20:23], s34 offen\n\t"
        "s_add_u32 s34, s34, 0x8000\n\t"
        "buffer_load_dwordx4 a[244:247], v97, s[20:23], s34 offen\n\t"
        "s_add_u32 s34, s34, 0x8000\n\t"
        "buffer_load_dwordx4 a[248:251], v97, s[20:23], s34 offen\n\t"
        "s_add_u32 s34, s34, 0x8000\n\t"
        "buffer_load_dwordx4 a[252:255], v97, s[20:23], s34 offen\n\t"
        "s_waitcnt lgkmcnt(0)\n\t"
        "s_waitcnt vmcnt(16)\n\t"
        "ds_write_b128 v122, a[160:163] offset:0\n\t"
        "ds_write_b128 v122, a[164:167] offset:1056\n\t"
        "ds_write_b128 v122, a[168:171] offset:2176\n\t"
        "ds_write_b128 v122, a[172:175] offset:3232\n\t"
        "ds_write_b128 v122, a[176:179] offset:4352\n\t"
        "ds_write_b128 v122, a[180:183] offset:5408\n\t"
        "ds_write_b128 v122, a[184:187] offset:6528\n\t"
        "ds_write_b128 v122, a[188:191] offset:7584\n\t"
        "ds_read_b64_tr_b16 a[160:161], v123 offset:0\n\t"
        "ds_read_b64_tr_b16 a[162:163], v123 offset:128\n\t"
        "ds_read_b64_tr_b16 a[164:165], v123 offset:4352\n\t"
        "ds_read_b64_tr_b16 a[166:167], v123 offset:4480\n\t"
        "s_waitcnt lgkmcnt(8)\n\t"
        "ds_read_b64_tr_b16 a[168:169], v123 offset:32\n\t"
        "ds_read_b64_tr_b16 a[170:171], v123 offset:160\n\t"
        "ds_read_b64_tr_b16 a[172:173], v123 offset:4384\n\t"
        "ds_read_b64_tr_b16 a[174:175], v123 offset:4512\n\t"
        "s_waitcnt lgkmcnt(8)\n\t"
        "ds_read_b64_tr_b16 a[176:177], v123 offset:512\n\t"
        "ds_read_b64_tr_b16 a[178:179], v123 offset:640\n\t"
        "ds_read_b64_tr_b16 a[180:181], v123 offset:4864\n\t"
        "ds_read_b64_tr_b16 a[182:183], v123 offset:4992\n\t"
        "s_waitcnt lgkmcnt(8)\n\t"
        "ds_read_b64_tr_b16 a[184:185], v123 offset:544\n\t"
        "ds_read_b64_tr_b16 a[186:187], v123 offset:672\n\t"
        "ds_read_b64_tr_b16 a[188:189], v123 offset:4896\n\t"
        "ds_read_b64_tr_b16 a[190:191], v123 offset:5024\n\t"
        "s_waitcnt lgkmcnt(8)\n\t"
        "s_waitcnt lgkmcnt(0)\n\t"
        "s_waitcnt vmcnt(8)\n\t"
        "ds_write_b128 v122, a[192:195] offset:0\n\t"
        "ds_write_b128 v122, a[196:199] offset:1056\n\t"
        "ds_write_b128 v122, a[200:203] offset:2176\n\t"
        "ds_write_b128 v122, a[204:207] offset:3232\n\t"
        "ds_write_b128 v122, a[208:211] offset:4352\n\t"
        "ds_write_b128 v122, a[212:215] offset:5408\n\t"
        "ds_write_b128 v122, a[216:219] offset:6528\n\t"
        "ds_write_b128 v122, a[220:223] offset:7584\n\t"
        "ds_read_b64_tr_b16 a[192:193], v123 offset:0\n\t"
        "ds_read_b64_tr_b16 a[194:195], v123 offset:128\n\t"
        "ds_read_b64_tr_b16 a[196:197], v123 offset:4352\n\t"
        "ds_read_b64_tr_b16 a[198:199], v123 offset:4480\n\t"
        "s_waitcnt lgkmcnt(8)\n\t"
        "ds_read_b64_tr_b16 a[200:201], v123 offset:32\n\t"
        "ds_read_b64_tr_b16 a[202:203], v123 offset:160\n\t"
        "ds_read_b64_tr_b16 a[204:205], v123 offset:4384\n\t"
        "ds_read_b64_tr_b16 a[206:207], v123 offset:4512\n\t"
        "s_waitcnt lgkmcnt(8)\n\t"
        "ds_read_b64_tr_b16 a[208:209], v123 offset:512\n\t"
        "ds_read_b64_tr_b16 a[210:211], v123 offset:640\n\t"
        "ds_read_b64_tr_b16 a[212:213], v123 offset:4864\n\t"
        "ds_read_b64_tr_b16 a[214:215], v123 offset:4992\n\t"
        "s_waitcnt lgkmcnt(8)\n\t"
        "ds_read_b64_tr_b16 a[216:217], v123 offset:544\n\t"
        "ds_read_b64_tr_b16 a[218:219], v123 offset:672\n\t"
        "ds_read_b64_tr_b16 a[220:221], v123 offset:4896\n\t"
        "ds_read_b64_tr_b16 a[222:223], v123 offset:5024\n\t"
        "s_waitcnt lgkmcnt(8)\n\t"
        "s_waitcnt lgkmcnt(0)\n\t"
        "s_waitcnt vmcnt(0)\n\t"
        "ds_write_b128 v122, a[224:227] offset:0\n\t"
        "ds_write_b128 v122, a[228:231] offset:1056\n\t"
        "ds_write_b128 v122, a[232:235] offset:2176\n\t"
        "ds_write_b128 v122, a[236:239] offset:3232\n\t"
        "ds_write_b128 v122, a[240:243] offset:4352\n\t"
        "ds_write_b128 v122, a[244:247] offset:5408\n\t"
        "ds_write_b128 v122, a[248:251] offset:6528\n\t"
        "ds_write_b128 v122, a[252:255] offset:7584\n\t"
        "ds_read_b64_tr_b16 a[224:225], v123 offset:0\n\t"
        "ds_read_b64_tr_b16 a[226:227], v123 offset:128\n\t"
        "ds_read_b64_tr_b16 a[228:229], v123 offset:4352\n\t"
        "ds_read_b64_tr_b16 a[230:231], v123 offset:4480\n\t"
        "s_waitcnt lgkmcnt(8)\n\t"
        "ds_read_b64_tr_b16 a[232:233], v123 offset:32\n\t"
        "ds_read_b64_tr_b16 a[234:235], v123 offset:160\n\t"
        "ds_read_b64_tr_b16 a[236:237], v123 offset:4384\n\t"
        "ds_read_b64_tr_b16 a[238:239], v123 offset:4512\n\t"
        "s_waitcnt lgkmcnt(8)\n\t"
        "ds_read_b64_tr_b16 a[240:241], v123 offset:512\n\t"
        "ds_read_b64_tr_b16 a[242:243], v123 offset:640\n\t"
        "ds_read_b64_tr_b16 a[244:245], v123 offset:4864\n\t"
        "ds_read_b64_tr_b16 a[246:247], v123 offset:4992\n\t"
        "s_waitcnt lgkmcnt(8)\n\t"
        "ds_read_b64_tr_b16 a[248:249], v123 offset:544\n\t"
        "ds_read_b64_tr_b16 a[250:251], v123 offset:672\n\t"
        "ds_read_b64_tr_b16 a[252:253], v123 offset:4896\n\t"
        "ds_read_b64_tr_b16 a[254:255], v123 offset:5024\n\t"
        "s_waitcnt lgkmcnt(8)\n\t"
        "s_mov_b32 s35, 33792\n\t"
        "buffer_load_dword v0, off, s[12:15], s35 sc1\n\t"
        "s_waitcnt lgkmcnt(0)\n\t"
        "s_waitcnt vmcnt(0)\n\t"
        "s_mov_b32 s42, 0\n\t"
        "LB_cal%=:\n\t"
        "v_readfirstlane_b32 s34, v0\n\t"
        "s_nop 0\n\t"
        "s_cmp_lg_u32 s34, 0\n\t"
        "s_cbranch_scc1 LB_calok%=\n\t"
        "s_sleep 4\n\t"
        "s_mov_b32 s35, 33792\n\t"
        "buffer_load_dword v0, off, s[12:15], s35 sc1\n\t"
        "s_waitcnt vmcnt(0)\n\t"
        "s_add_u32 s42, s42, 1\n\t"
        "s_cmp_lt_u32 s42, 0x100000\n\t"
        "s_cbranch_scc1 LB_cal%=\n\t"
        "s_movk_i32 s34, 8\n\t"
        "s_branch LB_die%=\n\t"
        "LB_calok%=:\n\t"
        "v_readfirstlane_b32 s34, v0\n\t"
        "s_nop 0\n\t"
        "s_and_b32 s34, s34, 8192\n\t"
        "s_add_u32 s8, s8, s34\n\t"
        "s_addc_u32 s9, s9, 0\n\t"
        "v_min_u32_e32 v109, -2, v109\n\t"
        "buffer_store_dword v109, v116, s[8:11], 0 offen sc1\n\t"
        "s_memrealtime s[32:33]\n\t"
        "s_waitcnt lgkmcnt(0)\n\t"
        "v_mov_b32_e32 v110, s32\n\t"
        "s_lshl_b32 s34, s29, 8\n\t"
        "s_lshl_b32 s35, s30, 20\n\t"
        "s_or_b32 s54, s34, s35\n\t"
        "s_add_u32 s34, s54, 12589056\n\t"
        "v_mov_b32_e32 v111, s34\n\t"
        "s_barrier\n\t"
        "s_cmp_lg_u32 s43, 0\n\t"
        "s_cbranch_scc1 LB_st%=\n\t"
        "v_mov_b32_e32 v2, 1\n\t"
        "s_mov_b32 s35, 34304\n\t"
        "s_mov_b64 exec, 1\n\t"
        "buffer_atomic_add v2, off, s[12:15], s35 sc0\n\t"
        "s_mov_b64 exec, -1\n\t"
        "s_waitcnt vmcnt(0)\n\t"
        "v_readfirstlane_b32 s34, v2\n\t"
        "s_nop 0\n\t"
        "s_cmp_lg_u32 s34, 95\n\t"
        "s_cbranch_scc1 LB_st%=\n\t"
        "s_memrealtime s[32:33]\n\t"
        "v_mbcnt_lo_u32_b32 v3, -1, 0\n\t"
        "v_lshlrev_b32_e32 v3, 7, v3\n\t"
        "s_waitcnt lgkmcnt(0)\n\t"
        "s_add_u32 s34, s32, 50\n\t"
        "s_or_b32 s34, s34, 1\n\t"
        "v_mov_b32_e32 v2, s34\n\t"
        "s_mov_b32 s35, 36864\n\t"
        "s_mov_b64 exec, 0xffff\n\t"
        "buffer_store_dword v2, v3, s[12:15], s35 offen sc1\n\t"
        "s_mov_b64 exec, -1\n\t"
        "LB_st%=:\n\t"
        "s_mov_b32 s24, 0\n\t"
        "s_mov_b32 s26, 0\n\t"
        "s_mov_b32 s27, 0x80000\n\t"
        "s_mov_b32 s28, 0x180000\n\t"
        "s_mov_b32 s31, 0\n\t"
        "s_mov_b32 s47, 0\n\t"
        "s_mov_b32 s38, 0xff00ff00\n\t"
        "s_mov_b32 s39, 0xff00ff00\n\t"
        "s_mov_b32 s40, 0\n\t"
        "s_mov_b32 s41, -1\n\t"
        "v_mov_b64_e32 v[0:1], 0\n\t"
        "v_mov_b64_e32 v[2:3], 0\n\t"
        "v_mov_b64_e32 v[4:5], 0\n\t"
        "v_mov_b64_e32 v[6:7], 0\n\t"
        "v_mov_b64_e32 v[8:9], 0\n\t"
        "v_mov_b64_e32 v[10:11], 0\n\t"
        "v_mov_b64_e32 v[12:13], 0\n\t"
        "v_mov_b64_e32 v[14:15], 0\n\t"
        "s_mov_b32 s42, 0\n\t"
        "LB_gw%=:\n\t"
        "buffer_load_dword v16, off, s[12:15], s45 sc1\n\t"
        "s_waitcnt vmcnt(0)\n\t"
        "v_readfirstlane_b32 s25, v16\n\t"
        "s_nop 0\n\t"
        "s_cmp_lg_u32 s25, 0\n\t"
        "s_cbranch_scc1 LB_loop%=\n\t"
        "s_sleep 2\n\t"
        "s_add_u32 s42, s42, 1\n\t"
        "s_cmp_lt_u32 s42, 0x100000\n\t"
        "s_cbranch_scc1 LB_gw%=\n\t"
        "s_movk_i32 s34, 9\n\t"
        "s_branch LB_die%=\n\t"
        ".p2align 8\n\t"
        "LB_loop%=:\n\t"
        "s_mov_b32 s42, 0\n\t"
        "s_sub_u32 s35, s25, 1\n\t"
        "LB_wa%=:\n\t"
        "s_memrealtime s[32:33]\n\t"
        "s_waitcnt lgkmcnt(0)\n\t"
        "s_sub_u32 s34, s35, s32\n\t"
        "s_cmpk_lt_u32 s34, 0x2000\n\t"
        "s_cbranch_scc1 LB_wa%=\n\t"
        "LB_rd%=:\n\t"
        "buffer_load_dwordx4 v[16:19], v112, s[8:11], s26 offen offset:0 sc1\n\t"
        "s_nop 9\n\t"
        "buffer_load_dwordx4 v[20:23], v112, s[8:11], s26 offen offset:1024 sc1\n\t"
        "s_nop 9\n\t"
        "buffer_load_dwordx4 v[24:27], v112, s[8:11], s26 offen offset:2048 sc1\n\t"
        "s_nop 9\n\t"
        "buffer_load_dwordx4 v[28:31], v112, s[8:11], s26 offen offset:3072 sc1\n\t"
        "LB_go%=:\n\t"
        "s_waitcnt vmcnt(3)\n\t"
        "v_smfmac_f32_16x16x64_bf16 v[0:3], v[16:19], v[128:135], v106\n\t"
        "v_max3_u32 v108, v16, v17, v18\n\t"
        "v_max_u32_e32 v108, v19, v108\n\t"
        "v_smfmac_f32_16x16x64_bf16 v[4:7], v[16:19], v[136:143], v106\n\t"
        "v_smfmac_f32_16x16x64_bf16 v[8:11], v[16:19], v[144:151], v106\n\t"
        "v_smfmac_f32_16x16x64_bf16 v[12:15], v[16:19], v[152:159], v106\n\t"
        "s_waitcnt vmcnt(2)\n\t"
        "v_smfmac_f32_16x16x64_bf16 v[0:3], v[20:23], v[160:167], v106\n\t"
        "v_max3_u32 v108, v20, v21, v108\n\t"
        "v_max3_u32 v108, v22, v23, v108\n\t"
        "v_smfmac_f32_16x16x64_bf16 v[4:7], v[20:23], v[168:175], v106\n\t"
        "v_smfmac_f32_16x16x64_bf16 v[8:11], v[20:23], v[176:183], v106\n\t"
        "v_smfmac_f32_16x16x64_bf16 v[12:15], v[20:23], v[184:191], v106\n\t"
        "s_waitcnt vmcnt(1)\n\t"
        "v_smfmac_f32_16x16x64_bf16 v[0:3], v[24:27], v[192:199], v106\n\t"
        "v_max3_u32 v108, v24, v25, v108\n\t"
        "v_max3_u32 v108, v26, v27, v108\n\t"
        "v_smfmac_f32_16x16x64_bf16 v[4:7], v[24:27], v[200:207], v106\n\t"
        "v_smfmac_f32_16x16x64_bf16 v[8:11], v[24:27], v[208:215], v106\n\t"
        "v_smfmac_f32_16x16x64_bf16 v[12:15], v[24:27], v[216:223], v106\n\t"
        "s_waitcnt vmcnt(0)\n\t"
        "v_smfmac_f32_16x16x64_bf16 v[0:3], v[28:31], v[224:231], v106\n\t"
        "v_max3_u32 v108, v28, v29, v108\n\t"
        "v_max3_u32 v108, v30, v31, v108\n\t"
        "v_smfmac_f32_16x16x64_bf16 v[4:7], v[28:31], v[232:239], v106\n\t"
        "v_cmp_eq_u32_e32 vcc, -1, v108\n\t"
        "v_smfmac_f32_16x16x64_bf16 v[8:11], v[28:31], v[240:247], v106\n\t"
        "v_smfmac_f32_16x16x64_bf16 v[12:15], v[28:31], v[248:255], v106\n\t"
        "s_cbranch_vccnz LB_rdfail%=\n\t"
        "s_nop 0\n\t"
        "v_add_f32_e32 v0, v0, v1\n\t"
        "v_add_f32_e32 v1, v2, v3\n\t"
        "ds_write_b64 v118, v[0:1] offset:0\n\t"
        "v_add_f32_e32 v4, v4, v5\n\t"
        "v_add_f32_e32 v5, v6, v7\n\t"
        "ds_write_b64 v118, v[4:5] offset:512\n\t"
        "v_add_f32_e32 v8, v8, v9\n\t"
        "v_add_f32_e32 v9, v10, v11\n\t"
        "ds_write_b64 v118, v[8:9] offset:1024\n\t"
        "v_add_f32_e32 v12, v12, v13\n\t"
        "v_add_f32_e32 v13, v14, v15\n\t"
        "ds_write_b64 v118, v[12:13] offset:1536\n\t"
        "buffer_store_dwordx2 v[110:111], v117, s[8:11], s26 offen sc1\n\t"
        "s_waitcnt lgkmcnt(0)\n\t"
        "s_barrier\n\t"
        "ds_read_b128 v[16:19], v119 offset:0\n\t"
        "ds_read_b128 v[20:23], v119 offset:2048\n\t"
        "ds_read_b128 v[24:27], v119 offset:4096\n\t"
        "ds_read_b128 v[28:31], v119 offset:6144\n\t"
        "buffer_load_dwordx4 v[64:67], v124, s[8:11], s28 offen sc1\n\t"
        "s_waitcnt lgkmcnt(2)\n\t"
        "v_pk_add_f32 v[16:17], v[16:17], v[20:21]\n\t"
        "v_pk_add_f32 v[18:19], v[18:19], v[22:23]\n\t"
        "s_waitcnt lgkmcnt(0)\n\t"
        "v_pk_add_f32 v[24:25], v[24:25], v[28:29]\n\t"
        "v_pk_add_f32 v[26:27], v[26:27], v[30:31]\n\t"
        "v_pk_add_f32 v[16:17], v[16:17], v[24:25]\n\t"
        "v_pk_add_f32 v[18:19], v[18:19], v[26:27]\n\t"
        "v_cvt_pk_bf16_f32 v20, v16, v17\n\t"
        "v_cvt_pk_bf16_f32 v21, v18, v19\n\t"
        "buffer_store_dwordx2 v[20:21], v114, s[8:11], s26 offen sc1\n\t"
        "s_cmp_eq_u32 s24, 63\n\t"
        "s_cbranch_scc1 LB_end%=\n\t"
        "s_memrealtime s[52:53]\n\t"
        "v_mov_b64_e32 v[24:25], -1\n\t"
        "v_mov_b64_e32 v[32:33], 0\n\t"
        "v_mov_b64_e32 v[34:35], 0\n\t"
        "v_mov_b64_e32 v[36:37], 0\n\t"
        "v_mov_b64_e32 v[38:39], 0\n\t"
        "v_mov_b64_e32 v[40:41], 0\n\t"
        "v_mov_b64_e32 v[42:43], 0\n\t"
        "v_mov_b64_e32 v[44:45], 0\n\t"
        "v_mov_b64_e32 v[46:47], 0\n\t"
        "v_mov_b64_e32 v[0:1], 0\n\t"
        "v_mov_b64_e32 v[2:3], 0\n\t"
        "v_mov_b64_e32 v[4:5], 0\n\t"
        "v_mov_b64_e32 v[6:7], 0\n\t"
        "v_mov_b64_e32 v[8:9], 0\n\t"
        "v_mov_b64_e32 v[10:11], 0\n\t"
        "v_mov_b64_e32 v[12:13], 0\n\t"
        "v_mov_b64_e32 v[14:15], 0\n\t"
        "s_add_u32 s46, s26, s69\n\t"
        "s_add_u32 s48, s26, s70\n\t"
        "s_add_u32 s73, s26, s71\n\t"
        "s_add_u32 s74, s26, s72\n\t"
        "s_sleep 1\n\t"
        "buffer_load_dwordx2 v[104:105], v113, s[8:11], s26 offen sc1\n\t"
        "buffer_store_dword v24, v116, s[8:11], s28 offen sc1\n\t"
        "s_xor_b32 s34, s26, 0x100000\n\t"
        "buffer_store_dwordx2 v[24:25], v114, s[8:11], s34 offen sc1\n\t"
        "s_add_u32 s66, s49, s43\n\t"
        "s_cmp_lg_u32 s66, 32\n\t"
        "s_cbranch_scc1 LB_norep%=\n\t"
        "s_waitcnt vmcnt(4)\n\t"
        "s_sub_u32 s66, s24, 1\n\t"
        "v_lshrrev_b32_e32 v68, 16, v64\n\t"
        "v_cmp_eq_u32_e32 vcc, s66, v68\n\t"
        "v_subrev_u32_e32 v64, s59, v64\n\t"
        "v_bfe_i32 v64, v64, 0, 16\n\t"
        "v_cndmask_b32_e32 v64, 0, v64, vcc\n\t"
        "v_lshrrev_b32_e32 v68, 16, v65\n\t"
        "v_cmp_eq_u32_e32 vcc, s66, v68\n\t"
        "v_subrev_u32_e32 v65, s59, v65\n\t"
        "v_bfe_i32 v65, v65, 0, 16\n\t"
        "v_cndmask_b32_e32 v65, 0, v65, vcc\n\t"
        "v_lshrrev_b32_e32 v68, 16, v66\n\t"
        "v_cmp_eq_u32_e32 vcc, s66, v68\n\t"
        "v_subrev_u32_e32 v66, s59, v66\n\t"
        "v_bfe_i32 v66, v66, 0, 16\n\t"
        "v_cndmask_b32_e32 v66, 0, v66, vcc\n\t"
        "v_lshrrev_b32_e32 v68, 16, v67\n\t"
        "v_cmp_eq_u32_e32 vcc, s66, v68\n\t"
        "v_subrev_u32_e32 v67, s59, v67\n\t"
        "v_bfe_i32 v67, v67, 0, 16\n\t"
        "v_cndmask_b32_e32 v67, 0, v67, vcc\n\t"
        "v_max3_i32 v64, v64, v65, v66\n\t"
        "v_max_i32_e32 v64, v64, v67\n\t"
        "s_nop 1\n\t"
        "v_max_i32_dpp v64, v64, v64 quad_perm:[1,0,3,2] row_mask:0xf bank_mask:0xf\n\t"
        "s_nop 1\n\t"
        "v_max_i32_dpp v64, v64, v64 quad_perm:[2,3,0,1] row_mask:0xf bank_mask:0xf\n\t"
        "s_nop 1\n\t"
        "v_max_i32_dpp v64, v64, v64 row_ror:4 row_mask:0xf bank_mask:0xf\n\t"
        "s_nop 1\n\t"
        "v_max_i32_dpp v64, v64, v64 row_ror:8 row_mask:0xf bank_mask:0xf\n\t"
        "v_mov_b32_e32 v65, v64\n\t"
        "s_nop 1\n\t"
        "v_permlane16_swap_b32_e32 v64, v65\n\t"
        "v_max_i32_e32 v64, v64, v65\n\t"
        "v_mov_b32_e32 v65, v64\n\t"
        "s_nop 1\n\t"
        "v_permlane32_swap_b32_e32 v64, v65\n\t"
        "v_max_i32_e32 v64, v64, v65\n\t"
        "s_nop 0\n\t"
        "v_readfirstlane_b32 s64, v64\n\t"
        "LB_norep%=:\n\t"
        "s_waitcnt vmcnt(2) lgkmcnt(0)\n\t"
        "s_mov_b32 s61, s29\n\t"
        "s_mov_b32 s62, s30\n\t"
        "s_mov_b32 s63, s31\n\t"
        "v_and_b32_e32 v30, 0xff, v105\n\t"
        "v_cmp_ne_u32_e32 vcc, s24, v30\n\t"
        "s_nop 0\n\t"
        "s_cbranch_vccnz LB_stre%=\n\t"
        "LB_stok%=:\n\t"
        "v_subrev_u32_e32 v104, s25, v104\n\t"
        "s_nop 1\n\t"
        "v_max_i32_dpp v104, v104, v104 quad_perm:[1,0,3,2] row_mask:0xf bank_mask:0xf\n\t"
        "s_nop 1\n\t"
        "v_max_i32_dpp v104, v104, v104 quad_perm:[2,3,0,1] row_mask:0xf bank_mask:0xf\n\t"
        "s_nop 1\n\t"
        "v_max_i32_dpp v104, v104, v104 row_ror:4 row_mask:0xf bank_mask:0xf\n\t"
        "s_nop 1\n\t"
        "v_max_i32_dpp v104, v104, v104 row_ror:8 row_mask:0xf bank_mask:0xf\n\t"
        "s_nop 1\n\t"
        "v_max_i32_dpp v104, v104, v104 row_bcast:15 row_mask:0xa bank_mask:0xf\n\t"
        "s_nop 1\n\t"
        "v_max_i32_dpp v104, v104, v104 row_bcast:31 row_mask:0xc bank_mask:0xf\n\t"
        "v_readfirstlane_b32 s35, v105\n\t"
        "v_readlane_b32 s36, v104, 63\n\t"
        "s_bfe_u32 s29, s35, 0xc0008\n\t"
        "s_lshr_b32 s30, s35, 20\n\t"
        "s_add_u32 s35, s31, s50\n\t"
        "s_mul_i32 s37, s50, 3\n\t"
        "s_min_u32 s35, s35, s37\n\t"
        "s_add_i32 s34, s36, s51\n\t"
        "s_cmp_gt_i32 s34, 0\n\t"
        "s_cselect_b32 s31, s35, 0\n\t"
        "s_cselect_b32 s34, s34, 0\n\t"
        "s_add_u32 s34, s34, s31\n\t"
        "s_add_u32 s37, s25, s34\n\t"
        "s_add_u32 s45, s37, s29\n\t"
        "s_add_u32 s25, s45, s30\n\t"
        "s_add_u32 s66, s49, s43\n\t"
        "s_cmp_lg_u32 s66, 32\n\t"
        "s_cbranch_scc1 LB_noctl%=\n\t"
        "s_cmp_eq_u32 s24, 0\n\t"
        "s_cbranch_scc1 LB_ctl0%=\n\t"
        "s_cmp_lt_u32 s24, 6\n\t"
        "s_cselect_b32 s68, 32, 8\n\t"
        "s_add_i32 s65, s36, s62\n\t"
        "s_cmp_eq_u32 s24, 1\n\t"
        "s_cselect_b32 s34, 6, 0\n\t"
        "s_cselect_b32 s35, 3, 0\n\t"
        "s_sub_u32 s64, s64, s34\n\t"
        "s_sub_u32 s65, s65, s35\n\t"
        "s_lshl_b32 s66, s65, 1\n\t"
        "s_sub_i32 s66, s66, s56\n\t"
        "s_cmp_gt_i32 s66, 56\n\t"
        "s_cbranch_scc1 LB_c1%=\n\t"
        "s_lshl_b32 s66, s65, 1\n\t"
        "s_add_u32 s34, s56, s68\n\t"
        "s_min_i32 s34, s34, s66\n\t"
        "s_sub_u32 s35, s56, s66\n\t"
        "s_ashr_i32 s35, s35, 1\n\t"
        "s_max_i32 s35, s35, 1\n\t"
        "s_sub_u32 s35, s56, s35\n\t"
        "s_max_i32 s56, s34, s35\n\t"
        "s_branch LB_c2%=\n\t"
        "LB_c1%=:\n\t"
        "s_add_u32 s56, s56, 2\n\t"
        "s_add_i32 s66, s64, s57\n\t"
        "s_cmp_gt_i32 s66, s61\n\t"
        "s_cbranch_scc1 LB_c2%=\n\t"
        "s_add_u32 s57, s57, 4\n\t"
        "s_min_u32 s57, s57, 24\n\t"
        "LB_c2%=:\n\t"
        "s_cmp_lg_u32 s63, 0\n\t"
        "s_cbranch_scc1 LB_c4%=\n\t"
        "s_lshl_b32 s66, s64, 1\n\t"
        "s_sub_i32 s66, s66, s55\n\t"
        "s_cmp_gt_i32 s66, 56\n\t"
        "s_cbranch_scc1 LB_c3%=\n\t"
        "s_lshl_b32 s66, s64, 1\n\t"
        "s_add_u32 s34, s55, s68\n\t"
        "s_min_i32 s34, s34, s66\n\t"
        "s_sub_u32 s35, s55, s66\n\t"
        "s_ashr_i32 s35, s35, 1\n\t"
        "s_max_i32 s35, s35, 1\n\t"
        "s_sub_u32 s35, s55, s35\n\t"
        "s_max_i32 s55, s34, s35\n\t"
        "s_branch LB_c4%=\n\t"
        "LB_c3%=:\n\t"
        "s_add_u32 s55, s55, 2\n\t"
        "s_cmp_lt_i32 s60, s58\n\t"
        "s_cbranch_scc1 LB_c4%=\n\t"
        "s_add_u32 s66, s58, 8\n\t"
        "s_cmp_gt_i32 s60, s66\n\t"
        "s_cbranch_scc1 LB_c4%=\n\t"
        "s_add_u32 s58, s58, 4\n\t"
        "s_min_u32 s58, s58, 20\n\t"
        "LB_c4%=:\n\t"
        "s_and_b32 s66, s24, 3\n\t"
        "s_cmp_eq_u32 s66, 0\n\t"
        "s_cselect_b32 s66, 1, 0\n\t"
        "s_cmp_lt_u32 s24, 30\n\t"
        "s_cselect_b32 s34, 8, 8\n\t"
        "s_cselect_b32 s35, 4, 4\n\t"
        "s_sub_u32 s57, s57, s66\n\t"
        "s_max_i32 s57, s57, s34\n\t"
        "s_sub_u32 s58, s58, s66\n\t"
        "s_max_i32 s58, s58, s35\n\t"
        "s_add_u32 s34, s55, 1\n\t"
        "s_lshr_b32 s34, s34, 1\n\t"
        "s_add_u32 s34, s34, s57\n\t"
        "s_min_u32 s34, s34, 1000\n\t"
        "s_lshl_b32 s34, s34, 8\n\t"
        "s_add_u32 s35, s56, 1\n\t"
        "s_lshr_b32 s35, s35, 1\n\t"
        "s_add_u32 s35, s35, s58\n\t"
        "s_min_u32 s35, s35, 1000\n\t"
        "s_lshl_b32 s35, s35, 20\n\t"
        "s_or_b32 s54, s34, s35\n\t"
        "s_branch LB_ctl1%=\n\t"
        "LB_ctl0%=:\n\t"
        "s_movk_i32 s57, 8\n\t"
        "s_movk_i32 s58, 4\n\t"
        "s_sub_u32 s55, s29, 32\n\t"
        "s_lshl_b32 s55, s55, 1\n\t"
        "s_sub_u32 s56, s30, 16\n\t"
        "s_lshl_b32 s56, s56, 1\n\t"
        "LB_ctl1%=:\n\t"
        "s_mov_b32 s59, s37\n\t"
        "s_sub_u32 s60, 0, s36\n\t"
        "LB_noctl%=:\n\t"
        "s_cmp_eq_u32 s24, 0\n\t"
        "s_cselect_b32 s34, 1537, 1\n\t"
        "s_add_u32 s34, s34, s24\n\t"
        "s_add_u32 s34, s34, s54\n\t"
        "v_mov_b32_e32 v111, s34\n\t"
        "s_mov_b32 s42, 0\n\t"
        "s_sub_u32 s35, s45, 1\n\t"
        "LB_wo%=:\n\t"
        "s_memrealtime s[32:33]\n\t"
        "s_waitcnt lgkmcnt(0)\n\t"
        "s_sub_u32 s34, s35, s32\n\t"
        "s_cmpk_lt_u32 s34, 0x2000\n\t"
        "s_cbranch_scc1 LB_wo%=\n\t"
        "LB_ro%=:\n\t"
        "buffer_load_dwordx4 v[64:67], v115, s[8:11], s46 offen offset:0 sc1\n\t"
        "s_nop 12\n\t"
        "buffer_load_dwordx4 v[68:71], v115, s[8:11], s46 offen offset:1024 sc1\n\t"
        "s_nop 12\n\t"
        "buffer_load_dwordx4 v[72:75], v115, s[8:11], s48 offen offset:0 sc1\n\t"
        "s_nop 12\n\t"
        "buffer_load_dwordx4 v[76:79], v115, s[8:11], s48 offen offset:1024 sc1\n\t"
        "s_nop 12\n\t"
        "buffer_load_dwordx4 v[80:83], v115, s[8:11], s73 offen offset:0 sc1\n\t"
        "s_nop 12\n\t"
        "buffer_load_dwordx4 v[84:87], v115, s[8:11], s73 offen offset:1024 sc1\n\t"
        "s_nop 12\n\t"
        "buffer_load_dwordx4 v[88:91], v115, s[8:11], s74 offen offset:0 sc1\n\t"
        "s_nop 12\n\t"
        "buffer_load_dwordx4 v[92:95], v115, s[8:11], s74 offen offset:1024 sc1\n\t"
        "LB_go2%=:\n\t"
        "s_waitcnt vmcnt(7)\n\t"
        "v_smfmac_f32_16x16x64_bf16 v[32:35], v[64:67], a[0:7], v106\n\t"
        "v_max3_u32 v108, v64, v65, v66\n\t"
        "v_max_u32_e32 v108, v67, v108\n\t"
        "v_smfmac_f32_16x16x64_bf16 v[40:43], v[64:67], a[16:23], v106\n\t"
        "v_smfmac_f32_16x16x64_bf16 v[36:39], v[64:67], a[8:15], v106\n\t"
        "v_smfmac_f32_16x16x64_bf16 v[44:47], v[64:67], a[24:31], v106\n\t"
        "s_waitcnt vmcnt(6)\n\t"
        "v_smfmac_f32_16x16x64_bf16 v[32:35], v[68:71], a[32:39], v106\n\t"
        "v_max3_u32 v108, v68, v69, v108\n\t"
        "v_max3_u32 v108, v70, v71, v108\n\t"
        "v_smfmac_f32_16x16x64_bf16 v[40:43], v[68:71], a[48:55], v106\n\t"
        "v_smfmac_f32_16x16x64_bf16 v[36:39], v[68:71], a[40:47], v106\n\t"
        "v_smfmac_f32_16x16x64_bf16 v[44:47], v[68:71], a[56:63], v106\n\t"
        "s_waitcnt vmcnt(5)\n\t"
        "v_smfmac_f32_16x16x64_bf16 v[32:35], v[72:75], a[64:71], v106\n\t"
        "v_max3_u32 v108, v72, v73, v108\n\t"
        "v_max3_u32 v108, v74, v75, v108\n\t"
        "v_smfmac_f32_16x16x64_bf16 v[40:43], v[72:75], a[80:87], v106\n\t"
        "v_smfmac_f32_16x16x64_bf16 v[36:39], v[72:75], a[72:79], v106\n\t"
        "v_smfmac_f32_16x16x64_bf16 v[44:47], v[72:75], a[88:95], v106\n\t"
        "s_waitcnt vmcnt(4)\n\t"
        "v_smfmac_f32_16x16x64_bf16 v[32:35], v[76:79], a[96:103], v106\n\t"
        "v_max3_u32 v108, v76, v77, v108\n\t"
        "v_max3_u32 v108, v78, v79, v108\n\t"
        "v_smfmac_f32_16x16x64_bf16 v[40:43], v[76:79], a[112:119], v106\n\t"
        "v_smfmac_f32_16x16x64_bf16 v[36:39], v[76:79], a[104:111], v106\n\t"
        "v_smfmac_f32_16x16x64_bf16 v[44:47], v[76:79], a[120:127], v106\n\t"
        "s_waitcnt vmcnt(3)\n\t"
        "v_smfmac_f32_16x16x64_bf16 v[32:35], v[80:83], a[128:135], v106\n\t"
        "v_max3_u32 v108, v80, v81, v108\n\t"
        "v_max3_u32 v108, v82, v83, v108\n\t"
        "v_smfmac_f32_16x16x64_bf16 v[40:43], v[80:83], a[144:151], v106\n\t"
        "v_smfmac_f32_16x16x64_bf16 v[36:39], v[80:83], a[136:143], v106\n\t"
        "v_smfmac_f32_16x16x64_bf16 v[44:47], v[80:83], a[152:159], v106\n\t"
        "s_waitcnt vmcnt(2)\n\t"
        "v_smfmac_f32_16x16x64_bf16 v[32:35], v[84:87], a[160:167], v106\n\t"
        "v_max3_u32 v108, v84, v85, v108\n\t"
        "v_max3_u32 v108, v86, v87, v108\n\t"
        "v_smfmac_f32_16x16x64_bf16 v[40:43], v[84:87], a[176:183], v106\n\t"
        "v_smfmac_f32_16x16x64_bf16 v[36:39], v[84:87], a[168:175], v106\n\t"
        "v_smfmac_f32_16x16x64_bf16 v[44:47], v[84:87], a[184:191], v106\n\t"
        "s_waitcnt vmcnt(1)\n\t"
        "v_smfmac_f32_16x16x64_bf16 v[32:35], v[88:91], a[192:199], v106\n\t"
        "v_max3_u32 v108, v88, v89, v108\n\t"
        "v_max3_u32 v108, v90, v91, v108\n\t"
        "v_smfmac_f32_16x16x64_bf16 v[40:43], v[88:91], a[208:215], v106\n\t"
        "v_smfmac_f32_16x16x64_bf16 v[36:39], v[88:91], a[200:207], v106\n\t"
        "v_smfmac_f32_16x16x64_bf16 v[44:47], v[88:91], a[216:223], v106\n\t"
        "s_waitcnt vmcnt(0)\n\t"
        "v_smfmac_f32_16x16x64_bf16 v[32:35], v[92:95], a[224:231], v106\n\t"
        "v_max3_u32 v108, v92, v93, v108\n\t"
        "v_max3_u32 v108, v94, v95, v108\n\t"
        "v_smfmac_f32_16x16x64_bf16 v[40:43], v[92:95], a[240:247], v106\n\t"
        "v_cmp_eq_u32_e32 vcc, -1, v108\n\t"
        "v_smfmac_f32_16x16x64_bf16 v[36:39], v[92:95], a[232:239], v106\n\t"
        "v_smfmac_f32_16x16x64_bf16 v[44:47], v[92:95], a[248:255], v106\n\t"
        "s_cbranch_vccnz LB_rofail%=\n\t"
        "s_nop 0\n\t"
        "v_add_f32_e32 v32, v32, v33\n\t"
        "s_nop 1\n\t"
        "v_add_f32_e32 v40, v42, v43\n\t"
        "v_add_f32_e32 v32, v32, v40\n\t"
        "v_add_f32_e32 v36, v36, v37\n\t"
        "v_add_f32_e32 v44, v46, v47\n\t"
        "v_add_f32_e32 v36, v36, v44\n\t"
        "ds_write2_b32 v120, v32, v36 offset0:0 offset1:64\n\t"
        "s_waitcnt lgkmcnt(0)\n\t"
        "s_barrier\n\t"
        "ds_read_b64 v[64:65], v121 offset:0\n\t"
        "ds_read_b64 v[66:67], v121 offset:512\n\t"
        "ds_read_b64 v[68:69], v121 offset:1024\n\t"
        "ds_read_b64 v[70:71], v121 offset:1536\n\t"
        "s_waitcnt lgkmcnt(2)\n\t"
        "v_pk_add_f32 v[64:65], v[64:65], v[66:67]\n\t"
        "s_waitcnt lgkmcnt(0)\n\t"
        "v_pk_add_f32 v[68:69], v[68:69], v[70:71]\n\t"
        "v_pk_add_f32 v[64:65], v[64:65], v[68:69]\n\t"
        "v_cvt_pk_bf16_f32 v64, v64, v65\n\t"
        "buffer_store_dword v64, v116, s[8:11], s27 offen sc1\n\t"
        "s_memrealtime s[32:33]\n\t"
        "s_waitcnt lgkmcnt(0)\n\t"
        "v_mov_b32_e32 v110, s32\n\t"
        "s_add_u32 s24, s24, 1\n\t"
        "s_mov_b32 s28, s26\n\t"
        "s_mov_b32 s26, s27\n\t"
        "s_add_u32 s27, s27, 0x80000\n\t"
        "s_and_b32 s27, s27, 0x1fffff\n\t"
        "s_branch LB_loop%=\n\t"
        "LB_end%=:\n\t"
        "s_endpgm\n\t"
        "LB_stre%=:\n\t"
        "s_mov_b32 s42, 0\n\t"
        "LB_str2%=:\n\t"
        "buffer_load_dwordx2 v[104:105], v113, s[8:11], s26 offen sc1\n\t"
        "s_waitcnt vmcnt(0)\n\t"
        "v_and_b32_e32 v30, 0xff, v105\n\t"
        "v_cmp_ne_u32_e32 vcc, s24, v30\n\t"
        "s_nop 0\n\t"
        "s_cbranch_vccz LB_stok%=\n\t"
        "s_add_u32 s42, s42, 1\n\t"
        "s_cmp_lt_u32 s42, 0x100000\n\t"
        "s_cbranch_scc1 LB_str2%=\n\t"
        "s_movk_i32 s34, 5\n\t"
        "s_branch LB_die%=\n\t"
        "LB_rdfail%=:\n\t"
        "s_or_b32 s47, s47, 1\n\t"
        "buffer_load_dwordx4 v[16:19], v112, s[8:11], s26 offen offset:0 sc1\n\t"
        "buffer_load_dwordx4 v[20:23], v112, s[8:11], s26 offen offset:1024 sc1\n\t"
        "buffer_load_dwordx4 v[24:27], v112, s[8:11], s26 offen offset:2048 sc1\n\t"
        "buffer_load_dwordx4 v[28:31], v112, s[8:11], s26 offen offset:3072 sc1\n\t"
        "s_nop 7\n\t"
        "v_mov_b64_e32 v[0:1], 0\n\t"
        "v_mov_b64_e32 v[2:3], 0\n\t"
        "v_mov_b64_e32 v[4:5], 0\n\t"
        "v_mov_b64_e32 v[6:7], 0\n\t"
        "v_mov_b64_e32 v[8:9], 0\n\t"
        "v_mov_b64_e32 v[10:11], 0\n\t"
        "v_mov_b64_e32 v[12:13], 0\n\t"
        "v_mov_b64_e32 v[14:15], 0\n\t"
        "s_branch LB_rdre%=w\n\t"
        "LB_rdre%=:\n\t"
        "buffer_load_dwordx4 v[16:19], v112, s[8:11], s26 offen offset:0 sc1\n\t"
        "buffer_load_dwordx4 v[20:23], v112, s[8:11], s26 offen offset:1024 sc1\n\t"
        "buffer_load_dwordx4 v[24:27], v112, s[8:11], s26 offen offset:2048 sc1\n\t"
        "buffer_load_dwordx4 v[28:31], v112, s[8:11], s26 offen offset:3072 sc1\n\t"
        "LB_rdre%=w:\n\t"
        "s_waitcnt vmcnt(0) lgkmcnt(0)\n\t"
        "v_max3_u32 v108, v16, v17, v18\n\t"
        "v_max3_u32 v108, v108, v19, v20\n\t"
        "v_max3_u32 v108, v108, v21, v22\n\t"
        "v_max3_u32 v108, v108, v23, v24\n\t"
        "v_max3_u32 v108, v108, v25, v26\n\t"
        "v_max3_u32 v108, v108, v27, v28\n\t"
        "v_max3_u32 v108, v108, v29, v30\n\t"
        "v_max_u32_e32 v108, v108, v31\n\t"
        "v_cmp_eq_u32_e32 vcc, -1, v108\n\t"
        "s_nop 0\n\t"
        "s_cbranch_vccz LB_go%=\n\t"
        "s_add_u32 s42, s42, 1\n\t"
        "s_cmp_lt_u32 s42, 0x100000\n\t"
        "s_cbranch_scc1 LB_rdre%=\n\t"
        "s_movk_i32 s34, 3\n\t"
        "s_branch LB_die%=\n\t"
        "LB_rofail%=:\n\t"
        "s_or_b32 s47, s47, 4\n\t"
        "buffer_load_dwordx4 v[64:67], v115, s[8:11], s46 offen offset:0 sc1\n\t"
        "buffer_load_dwordx4 v[68:71], v115, s[8:11], s46 offen offset:1024 sc1\n\t"
        "buffer_load_dwordx4 v[72:75], v115, s[8:11], s48 offen offset:0 sc1\n\t"
        "buffer_load_dwordx4 v[76:79], v115, s[8:11], s48 offen offset:1024 sc1\n\t"
        "buffer_load_dwordx4 v[80:83], v115, s[8:11], s73 offen offset:0 sc1\n\t"
        "buffer_load_dwordx4 v[84:87], v115, s[8:11], s73 offen offset:1024 sc1\n\t"
        "buffer_load_dwordx4 v[88:91], v115, s[8:11], s74 offen offset:0 sc1\n\t"
        "buffer_load_dwordx4 v[92:95], v115, s[8:11], s74 offen offset:1024 sc1\n\t"
        "s_nop 7\n\t"
        "v_mov_b64_e32 v[32:33], 0\n\t"
        "v_mov_b64_e32 v[34:35], 0\n\t"
        "v_mov_b64_e32 v[36:37], 0\n\t"
        "v_mov_b64_e32 v[38:39], 0\n\t"
        "v_mov_b64_e32 v[40:41], 0\n\t"
        "v_mov_b64_e32 v[42:43], 0\n\t"
        "v_mov_b64_e32 v[44:45], 0\n\t"
        "v_mov_b64_e32 v[46:47], 0\n\t"
        "s_branch LB_rore%=w\n\t"
        "LB_rore%=:\n\t"
        "buffer_load_dwordx4 v[64:67], v115, s[8:11], s46 offen offset:0 sc1\n\t"
        "buffer_load_dwordx4 v[68:71], v115, s[8:11], s46 offen offset:1024 sc1\n\t"
        "buffer_load_dwordx4 v[72:75], v115, s[8:11], s48 offen offset:0 sc1\n\t"
        "buffer_load_dwordx4 v[76:79], v115, s[8:11], s48 offen offset:1024 sc1\n\t"
        "buffer_load_dwordx4 v[80:83], v115, s[8:11], s73 offen offset:0 sc1\n\t"
        "buffer_load_dwordx4 v[84:87], v115, s[8:11], s73 offen offset:1024 sc1\n\t"
        "buffer_load_dwordx4 v[88:91], v115, s[8:11], s74 offen offset:0 sc1\n\t"
        "buffer_load_dwordx4 v[92:95], v115, s[8:11], s74 offen offset:1024 sc1\n\t"
        "LB_rore%=w:\n\t"
        "s_waitcnt vmcnt(0) lgkmcnt(0)\n\t"
        "v_max3_u32 v108, v64, v65, v66\n\t"
        "v_max3_u32 v108, v108, v67, v68\n\t"
        "v_max3_u32 v108, v108, v69, v70\n\t"
        "v_max3_u32 v108, v108, v71, v72\n\t"
        "v_max3_u32 v108, v108, v73, v74\n\t"
        "v_max3_u32 v108, v108, v75, v76\n\t"
        "v_max3_u32 v108, v108, v77, v78\n\t"
        "v_max3_u32 v108, v108, v79, v80\n\t"
        "v_max3_u32 v108, v108, v81, v82\n\t"
        "v_max3_u32 v108, v108, v83, v84\n\t"
        "v_max3_u32 v108, v108, v85, v86\n\t"
        "v_max3_u32 v108, v108, v87, v88\n\t"
        "v_max3_u32 v108, v108, v89, v90\n\t"
        "v_max3_u32 v108, v108, v91, v92\n\t"
        "v_max3_u32 v108, v108, v93, v94\n\t"
        "v_max_u32_e32 v108, v108, v95\n\t"
        "v_cmp_eq_u32_e32 vcc, -1, v108\n\t"
        "s_nop 0\n\t"
        "s_cbranch_vccz LB_go2%=\n\t"
        "s_add_u32 s42, s42, 1\n\t"
        "s_cmp_lt_u32 s42, 0x100000\n\t"
        "s_cbranch_scc1 LB_rore%=\n\t"
        "s_movk_i32 s34, 4\n\t"
        "s_branch LB_die%=\n\t"
        "LB_die%=:\n\t"
        "v_mov_b32_e32 v30, s34\n\t"
        "s_mov_b32 s35, 33024\n\t"
        "s_mov_b64 exec, 1\n\t"
        "buffer_store_dword v30, off, s[12:15], s35 sc1\n\t"
        "s_endpgm\n\t"
        ".Lk_end_B:\n\t"
        :: REC_S_IN, "{v96}"(g_v), "{v97}"(g_o), "{v98}"(wb0), "{v99}"(wb1), "{v100}"(rb0), "{v101}"(rb0 ^ 32u), "{v102}"(rb0 ^ 64u), "{v103}"(rb0 ^ 96u),
           "{v106}"((l & 1) ? 0xEEEEEEEEu : 0x44444444u), "{v[108:109]}"(m0p), "{s68}"(rot),
           "{v112}"(a_mem), "{v113}"((24 + cp) * 16384 + l * ST_P), "{v114}"(a_vmw), "{v115}"(a_or), "{v116}"(a_mw), "{v117}"(a_sw),
           "{v118}"(lb + w * 2048 + (g >> 1) * 256 + n * 16 + (g & 1) * 8), "{v119}"(lb + (w & 1) * 1024 + l * 16), "{v120}"(lb + LB_R + w * 512 + l * 4),
           "{v121}"(lb + LB_R + l * 8), "{v122}"(stg + l * 16), "{v123}"(o_r), "{v124}"(rb + w == 32 ? 28 * 16384 + (l & 31) * 128 : OOB)
        : "memory", "vcc", "scc", "v0", "v255", "a0", "a255", "s101");
    __builtin_unreachable();
}
__device__ __forceinline__ void rec_helper(const Params& p, rsrc_t r, const int wv, const int hi) {
    const int tid = otid(), l = tid & 63, xq = hi >> 3;
    const rsrc_t rh = __builtin_amdgcn_make_buffer_rsrc(p.ws + hx_base(p.ws), 0, HX_BYTES, 0x00020000);
    const v4u ones = {0xFFFFFFFFu, 0xFFFFFFFFu, 0xFFFFFFFFu, 0xFFFFFFFFu};
    const bool meas = (hi & 7) < 2;
    if (meas) {
        const unsigned ca = l >= 8 ? OOB : (31 + wv * 32) * 16384 + hi * 256 + l * 16, tend = rtnow() + CAL_T;
        unsigned best0 = ~0u, best1 = ~0u;
#pragma unroll 1
        for (int i = 0; i < 64 && (i < 3 || (int)(rtnow() - tend) < 0); ++i) {
            const unsigned t0 = (unsigned)__builtin_amdgcn_s_memtime(); __builtin_amdgcn_s_waitcnt(0);
            st16(rh, ca, ones); DRAIN();
            const unsigned t1 = (unsigned)__builtin_amdgcn_s_memtime(); __builtin_amdgcn_s_waitcnt(0);
            st16(rh, ca + 8192, ones); DRAIN();
            const unsigned t2 = (unsigned)__builtin_amdgcn_s_memtime(); __builtin_amdgcn_s_waitcnt(0);
            best0 = min(best0, t1 - t0); best1 = min(best1, t2 - t1); }
        if (l < 2) __hip_atomic_fetch_max((unsigned*)(p.ws + F_CAL + 128) + l, 65535u - min(l ? best1 : best0, 65535u), __ATOMIC_RELAXED, __HIP_MEMORY_SCOPE_AGENT);
    }
    unsigned d = 0u;
    auto touch = [&](int z0, int z1) {
#pragma unroll 1
        for (int z = z0 + ((hi & 7) - 2) * 4 + wv; z < z1; z += 24) { const bf16_t* q;
            if (z < 48) q = ((z & 1) ? p.Wk : p.Wq) + (size_t)(((((z & 1) * 2 + 1) * 4 + 4 + (z >> 2)) & 15) * 64 + l) * 4096 + (xq * 8 + 1 + (z & 2) * 2) * 128 + 64;
            else if (z < 112) { const int y = z - 48; q = p.Wo + (size_t)(((y & 3) * 8 + (y >> 3)) * 128 + ((y >> 2) & 1) * 64 + l) * 1024 + (xq & 1) * 512 + 192; }
            else { const int s7 = (z - 112) % 7, y = (z - 112) / 7; q = p.Wo + (size_t)(((y & 3) * 8 + 8 - T2U + (y >> 3)) * 128 + ((y >> 2) & 1) * 64 + l) * 1024 + (xq & 1) * 512 + (s7 + (s7 >= 3)) * 64; }
            asm volatile("global_load_dword %0, %1, off" : "+v"(d) : "v"(q) : "memory"); } };
    if (!meas) touch(0, TPRE);
    st16(r, tid < 64 ? F_KVQ + hi * 1024 + tid * 16 : tid < 72 ? F_PROG + hi * 128 + (tid - 64) * 16 : tid < 80 ? F_CVY + hi * 128 + (tid - 72) * 16 : OOB, (v4u){0u, 0u, 0u, 0u});
#pragma unroll
    for (int i = 0; i < 2; ++i) st16(r, W_OX + 63 * 262144 + hi * 8192 + i * 4096 + tid * 16, ones);
#pragma unroll
    for (int i = 0; i < 16; ++i) st16(rh, hi * 65536 + i * 4096 + tid * 16, ones);
    asm volatile("s_waitcnt vmcnt(0)" : "+v"(d) :: "memory");
    __syncthreads();
    if (tid == 0 && __hip_atomic_fetch_add((unsigned*)(p.ws + F_CAL + 256), 1u, __ATOMIC_RELAXED, __HIP_MEMORY_SCOPE_AGENT) == 31u) {
        const v2u c = ld8(r, F_CAL + 128); st4(r, F_CAL, c.x < c.y ? 8193u : 1u); }
    if (!meas) touch(TPRE, 112 + 56 * T2U);
    asm volatile("s_waitcnt vmcnt(0)" : "+v"(d) :: "memory");
}
using f32x16 = __attribute__((ext_vector_type(16))) float;
constexpr int CV_RP = 80;
constexpr int CV_U = 96 * CV_RP;
constexpr int CV_KP = 4416;
constexpr int CV_K0 = 8 * CV_U;
__device__ __forceinline__ float blo(unsigned x) { return __uint_as_float(x << 16); }
__device__ __forceinline__ float bhi(unsigned x) { return __uint_as_float(x & 0xFFFF0000u); }
#define NAP() do { __builtin_amdgcn_sched_barrier(0); __builtin_amdgcn_s_sleep(XSLP); __builtin_amdgcn_sched_barrier(0); } while (0)
template <int NT>
__device__ __forceinline__ void gate_block(const Params& p, rsrc_t r, const int hi, const int wv) {
    const int l = otid() & 63, t0 = (hi * 4 + wv) * NT;
    v4u xg[NT][2], gw[2];
#pragma unroll
    for (int q = 0; q < 2; ++q) gw[q] = *(const v4u*)(p.gate_w + q * 512 + l * 8);
#pragma unroll
    for (int i = 0; i < NT; ++i)
#pragma unroll
        for (int q = 0; q < 2; ++q) { xg[i][q] = *(const v4u*)(p.Aq + (size_t)(t0 + i) * D + q * 512 + l * 8); NAP(); }
    float mine = 0.f;
#pragma unroll
    for (int i = 0; i < NT; ++i) { float acc = 0.f;
#pragma unroll
        for (int q = 0; q < 2; ++q)
#pragma unroll
            for (int e = 0; e < 4; ++e) { const unsigned pr = pack2(blo(xg[i][q][e]) * blo(gw[q][e]), bhi(xg[i][q][e]) * bhi(gw[q][e])); acc += blo(pr); acc += bhi(pr); }
        for (int o = 32; o > 0; o >>= 1) acc += __shfl_xor(acc, o);
        if (l == i) mine = acc; }
    if (l < NT) { const float z = rbfh(mine); st4(r, W_GATE + (t0 + l) * 4, __float_as_uint(rbfh(1.f / (1.f + expf(-z))))); }
}
__device__ __forceinline__ void conv_prep(const Params& p, rsrc_t r, const int wv, const int hi, const int G0, const int G1) {
    const int tid = otid(), Q = tid >> 2, cg = tid & 3, ch0 = hi * 32 + cg * 8;
    v4u sk[4]; float sv[8][4];
#pragma unroll
    for (int q = 0; q < 4; ++q) sk[q] = *(const v4u*)(p.shift_k + ch0 * 4 + q * 8);
#pragma unroll
    for (int c8 = 0; c8 < 8; ++c8)
#pragma unroll
        for (int tp = 0; tp < 4; ++tp) { const unsigned sw = sk[c8 >> 1][(c8 & 1) * 2 + (tp >> 1)]; sv[c8][tp] = (tp & 1) ? bhi(sw) : blo(sw); }
    const rsrc_t rk = __builtin_amdgcn_make_buffer_rsrc((void*)p.Ak, 0, S * D * 2, 0x00020000), rv = __builtin_amdgcn_make_buffer_rsrc((void*)p.Av, 0, S * D * 2, 0x00020000),
                 rq = __builtin_amdgcn_make_buffer_rsrc((void*)p.Aq, 0, S * D * 2, 0x00020000);
#pragma unroll 1
    for (int G = G0; G < G1; ++G) {
        const unsigned ro = (unsigned)((Q * 32 + G * 8) * D + ch0) * 2u, uo = hi * 131072 + tid * 16;
        v4u kr[11], u[8], qv[8];
#pragma unroll
        for (int i = 0; i < 11; ++i) { kr[i] = __builtin_amdgcn_raw_buffer_load_b128(rk, (i < 3 && Q + G == 0) ? OOB : ro + (i - 3) * 2048, 0, 0); NAP(); }
#pragma unroll
        for (int i = 0; i < 8; ++i) { u[i] = __builtin_amdgcn_raw_buffer_load_b128(rv, ro + i * 2048, 0, 0); NAP(); }
#pragma unroll
        for (int i = 0; i < 8; ++i) { qv[i] = __builtin_amdgcn_raw_buffer_load_b128(rq, ro + i * 2048, 0, 0); NAP(); }
#pragma unroll
        for (int q = 0; q < 4; ++q) {
#pragma unroll
            for (int i = 0; i < 8; ++i) {
                unsigned ks = pack2(blo(kr[i][q]) * sv[2 * q][3], bhi(kr[i][q]) * sv[2 * q + 1][3]), pr;
#pragma unroll
                for (int d = 1; d < 4; ++d) { pr = pack2(blo(kr[i + d][q]) * sv[2 * q][3 - d], bhi(kr[i + d][q]) * sv[2 * q + 1][3 - d]); ks = pack2(blo(ks) + blo(pr), bhi(ks) + bhi(pr)); }
                u[i][q] = pack2(blo(ks) * blo(u[i][q]), bhi(ks) * bhi(u[i][q])); }
            v4u lo, hh;
#pragma unroll
            for (int z = 0; z < 4; ++z) { const unsigned x = u[7 - 2 * z][q], y = u[6 - 2 * z][q]; lo[z] = __builtin_amdgcn_perm(y, x, 0x05040100u); hh[z] = __builtin_amdgcn_perm(y, x, 0x07060302u); }
            st16(r, W_U + uo + q * 32768 + G * 4096, lo); NAP(); st16(r, W_U + uo + q * 32768 + 16384 + G * 4096, hh); NAP();
#pragma unroll
            for (int z = 0; z < 2; ++z) { st16(r, W_Y + uo + q * 32768 + (2 * G + z) * 4096, (v4u){qv[4 * z][q], qv[4 * z + 1][q], qv[4 * z + 2][q], qv[4 * z + 3][q]}); NAP(); }
        }
    }
    DRAIN(); __syncthreads();
    if (tid == 0) __hip_atomic_fetch_add((unsigned*)(p.ws + F_CONV), 1u, __ATOMIC_RELAXED, __HIP_MEMORY_SCOPE_AGENT);
    if (XG && tid == 0) __hip_atomic_fetch_add((unsigned*)(p.ws + F_CVY + hi * 128 + 64), 1u, __ATOMIC_RELAXED, __HIP_MEMORY_SCOPE_AGENT);
}
__device__ __forceinline__ bool conv_core(const Params& p, rsrc_t r, unsigned char* lds, const int wv, const int un) {
    const int tid = otid(), w = wv, l = tid & 63, Q = tid >> 2, cg = tid & 3;
    const rsrc_t rs = __builtin_amdgcn_make_buffer_rsrc((void*)p.ssm_k, 0, S * D * 2, 0x00020000);
    v4u kraw[4]; v2u kext[4];
    auto kload = [&](int n) { const unsigned o = (unsigned)(((un >> 2) * 32 + w * 8 + (un & 3) * 2 + n) * S + l * 8) * 2u;
#pragma unroll
        for (int i = 0; i < 4; ++i) { kraw[i] = __builtin_amdgcn_raw_buffer_load_b128(rs, o + i * 1024, 0, CV_POL);
            kext[i] = __builtin_amdgcn_raw_buffer_load_b64(rs, (i == 3 && l == 63) ? OOB : o + i * 1024 + 16, 0, 0); } };
    kload(0);
    if (!wait_word(r, XG ? F_CVY + (un >> 2) * 128 + 64 : F_CONV, XG ? 4u : 32u, 2048u)) return false;
    v4u ur[8], qr[8];
#pragma unroll
    for (int i = 0; i < 8; ++i) ur[i] = ld16(r, W_U + un * 32768 + i * 4096 + tid * 16);
#pragma unroll
    for (int i = 0; i < 8; ++i) qr[i] = ld16(r, W_Y + un * 32768 + i * 4096 + tid * 16);
    const unsigned dv = *(const unsigned*)(p.ssm_D + (un >> 2) * 32 + cg * 8 + (un & 3) * 2);
    for (int i = tid; i < 8 * 160; i += 256) *(v4u*)(lds + (i / 160) * CV_U + (i % 160) * 16) = (v4u){0u, 0u, 0u, 0u};
    unsigned char* kc = lds + CV_K0 + w * 4 * CV_KP;
    if (l < 32) {
#pragma unroll
        for (int q = 0; q < 4; ++q) *(unsigned*)(kc + q * CV_KP + l * 4) = 0u; }
    const int b = l & 31, kg = l >> 5;
    const unsigned kb = (unsigned)(size_t)kc + (b & 3) * CV_KP + ((b & 28) - 8 * kg + 56 - 16) * 2, ub0 = (unsigned)(size_t)lds + (1 + b) * CV_RP + kg * 16;
    unsigned char* const urow = lds + 2 * cg * CV_U + (32 + Q) * CV_RP;
#pragma unroll
    for (int i = 0; i < 8; ++i) *(v4u*)(urow + (i >> 2) * CV_U + (i & 3) * 16) = ur[i];
    __syncthreads();
#pragma unroll 1
        for (int ci = 0; ci < 2; ++ci) {
            const int c8 = 2 * w + ci;
#pragma unroll
            for (int i = 0; i < 4; ++i) { const v4u e = kraw[i]; const v2u f = kext[i]; unsigned char* q = kc + 128 + (i * 64 + l) * 16;
                const unsigned s0 = __builtin_amdgcn_alignbit(e.y, e.x, 16), s1 = __builtin_amdgcn_alignbit(e.z, e.y, 16), s2 = __builtin_amdgcn_alignbit(e.w, e.z, 16),
                               s3 = __builtin_amdgcn_alignbit(f.x, e.w, 16), s4 = __builtin_amdgcn_alignbit(f.y, f.x, 16);
                *(v4u*)q = (v4u){s0, s1, s2, s3}; *(v4u*)(q + CV_KP) = (v4u){e.y, e.z, e.w, f.x}; *(v4u*)(q + 2 * CV_KP) = (v4u){s1, s2, s3, s4}; *(v4u*)(q + 3 * CV_KP) = (v4u){e.z, e.w, f.x, f.y};
                if (i == 0 && l == 0) {
                    *(v2u*)(q - 8) = (v2u){0u, e.x << 16}; *(v2u*)(q + CV_KP - 8) = (v2u){0u, e.x}; *(v2u*)(q + 2 * CV_KP - 8) = (v2u){e.x << 16, s0}; *(v2u*)(q + 3 * CV_KP - 8) = (v2u){e.x, e.y}; } }
            if (ci == 0) kload(1);
            __builtin_amdgcn_wave_barrier();
            f32x16 acc0, acc1;
            asm volatile(
                "ds_read_b64 v[192:193], %[kb] offset:32\n\t"
                "ds_read_b64 v[194:195], %[kb] offset:40\n\t"
                "ds_read_b128 v[196:199], %[ub] offset:5040\n\t"
                "ds_read_b128 v[200:203], %[ub] offset:2480\n\t"
                "ds_read_b64 v[204:205], %[kb] offset:2080\n\t"
                "ds_read_b64 v[206:207], %[kb] offset:2088\n\t"
                "ds_read_b64 v[208:209], %[kb] offset:0\n\t"
                "ds_read_b64 v[210:211], %[kb] offset:8\n\t"
                "ds_read_b128 v[212:215], %[ub] offset:5072\n\t"
                "ds_read_b128 v[216:219], %[ub] offset:2512\n\t"
                "ds_read_b64 v[220:221], %[kb] offset:2048\n\t"
                "ds_read_b64 v[222:223], %[kb] offset:2056\n\t"
                "s_waitcnt lgkmcnt(9)\n\t"
                "v_mfma_f32_32x32x16_bf16 %[a1], v[192:195], v[196:199], 0\n\t"
                "ds_read_b64 v[224:225], %[kb] offset:96\n\t"
                "ds_read_b64 v[226:227], %[kb] offset:104\n\t"
                "ds_read_b128 v[228:231], %[ub] offset:4960\n\t"
                "s_waitcnt lgkmcnt(11)\n\t"
                "v_mfma_f32_32x32x16_bf16 %[a0], v[192:195], v[200:203], 0\n\t"
                "ds_read_b128 v[232:235], %[ub] offset:2400\n\t"
                "s_waitcnt lgkmcnt(10)\n\t"
                "v_mfma_f32_32x32x16_bf16 %[a1], v[204:207], v[200:203], %[a1]\n\t"
                "ds_read_b64 v[236:237], %[kb] offset:2144\n\t"
                "ds_read_b64 v[238:239], %[kb] offset:2152\n\t"
                "s_waitcnt lgkmcnt(9)\n\t"
                "v_mfma_f32_32x32x16_bf16 %[a1], v[208:211], v[212:215], %[a1]\n\t"
                "ds_read_b64 v[240:241], %[kb] offset:64\n\t"
                "ds_read_b64 v[242:243], %[kb] offset:72\n\t"
                "ds_read_b128 v[244:247], %[ub] offset:4992\n\t"
                "s_waitcnt lgkmcnt(11)\n\t"
                "v_mfma_f32_32x32x16_bf16 %[a0], v[208:211], v[216:219], %[a0]\n\t"
                "ds_read_b128 v[248:251], %[ub] offset:2432\n\t"
                "s_waitcnt lgkmcnt(10)\n\t"
                "v_mfma_f32_32x32x16_bf16 %[a1], v[220:223], v[216:219], %[a1]\n\t"
                "ds_read_b64 v[252:253], %[kb] offset:2112\n\t"
                "ds_read_b64 v[254:255], %[kb] offset:2120\n\t"
                "s_waitcnt lgkmcnt(9)\n\t"
                "v_mfma_f32_32x32x16_bf16 %[a1], v[224:227], v[228:231], %[a1]\n\t"
                "ds_read_b64 v[192:193], %[kb] offset:160\n\t"
                "ds_read_b64 v[194:195], %[kb] offset:168\n\t"
                "ds_read_b128 v[196:199], %[ub] offset:4880\n\t"
                "s_waitcnt lgkmcnt(11)\n\t"
                "v_mfma_f32_32x32x16_bf16 %[a0], v[224:227], v[232:235], %[a0]\n\t"
                "ds_read_b128 v[200:203], %[ub] offset:2320\n\t"
                "s_waitcnt lgkmcnt(10)\n\t"
                "v_mfma_f32_32x32x16_bf16 %[a1], v[236:239], v[232:235], %[a1]\n\t"
                "ds_read_b64 v[204:205], %[kb] offset:2208\n\t"
                "ds_read_b64 v[206:207], %[kb] offset:2216\n\t"
                "s_waitcnt lgkmcnt(9)\n\t"
                "v_mfma_f32_32x32x16_bf16 %[a1], v[240:243], v[244:247], %[a1]\n\t"
                "ds_read_b64 v[208:209], %[kb] offset:128\n\t"
                "ds_read_b64 v[210:211], %[kb] offset:136\n\t"
                "ds_read_b128 v[212:215], %[ub] offset:4912\n\t"
                "s_waitcnt lgkmcnt(11)\n\t"
                "v_mfma_f32_32x32x16_bf16 %[a0], v[240:243], v[248:251], %[a0]\n\t"
                "ds_read_b128 v[216:219], %[ub] offset:2352\n\t"
                "s_waitcnt lgkmcnt(10)\n\t"
                "v_mfma_f32_32x32x16_bf16 %[a1], v[252:255], v[248:251], %[a1]\n\t"
                "ds_read_b64 v[220:221], %[kb] offset:2176\n\t"
                "ds_read_b64 v[222:223], %[kb] offset:2184\n\t"
                "s_waitcnt lgkmcnt(9)\n\t"
                "v_mfma_f32_32x32x16_bf16 %[a1], v[192:195], v[196:199], %[a1]\n\t"
                "ds_read_b64 v[224:225], %[kb] offset:224\n\t"
                "ds_read_b64 v[226:227], %[kb] offset:232\n\t"
                "ds_read_b128 v[228:231], %[ub] offset:4800\n\t"
                "s_waitcnt lgkmcnt(11)\n\t"
                "v_mfma_f32_32x32x16_bf16 %[a0], v[192:195], v[200:203], %[a0]\n\t"
                "ds_read_b128 v[232:235], %[ub] offset:2240\n\t"
                "s_waitcnt lgkmcnt(10)\n\t"
                "v_mfma_f32_32x32x16_bf16 %[a1], v[204:207], v[200:203], %[a1]\n\t"
                "ds_read_b64 v[236:237], %[kb] offset:2272\n\t"
                "ds_read_b64 v[238:239], %[kb] offset:2280\n\t"
                "s_waitcnt lgkmcnt(9)\n\t"
                "v_mfma_f32_32x32x16_bf16 %[a1], v[208:211], v[212:215], %[a1]\n\t"
                "ds_read_b64 v[240:241], %[kb] offset:192\n\t"
                "ds_read_b64 v[242:243], %[kb] offset:200\n\t"
                "ds_read_b128 v[244:247], %[ub] offset:4832\n\t"
                "s_waitcnt lgkmcnt(11)\n\t"
                "v_mfma_f32_32x32x16_bf16 %[a0], v[208:211], v[216:219], %[a0]\n\t"
                "ds_read_b128 v[248:251], %[ub] offset:2272\n\t"
                "s_waitcnt lgkmcnt(10)\n\t"
                "v_mfma_f32_32x32x16_bf16 %[a1], v[220:223], v[216:219], %[a1]\n\t"
                "ds_read_b64 v[252:253], %[kb] offset:2240\n\t"
                "ds_read_b64 v[254:255], %[kb] offset:2248\n\t"
                "s_waitcnt lgkmcnt(9)\n\t"
                "v_mfma_f32_32x32x16_bf16 %[a1], v[224:227], v[228:231], %[a1]\n\t"
                "ds_read_b64 v[192:193], %[kb] offset:288\n\t"
                "ds_read_b64 v[194:195], %[kb] offset:296\n\t"
                "ds_read_b128 v[196:199], %[ub] offset:4720\n\t"
                "s_waitcnt lgkmcnt(11)\n\t"
                "v_mfma_f32_32x32x16_bf16 %[a0], v[224:227], v[232:235], %[a0]\n\t"
                "ds_read_b128 v[200:203], %[ub] offset:2160\n\t"
                "s_waitcnt lgkmcnt(10)\n\t"
                "v_mfma_f32_32x32x16_bf16 %[a1], v[236:239], v[232:235], %[a1]\n\t"
                "ds_read_b64 v[204:205], %[kb] offset:2336\n\t"
                "ds_read_b64 v[206:207], %[kb] offset:2344\n\t"
                "s_waitcnt lgkmcnt(9)\n\t"
                "v_mfma_f32_32x32x16_bf16 %[a1], v[240:243], v[244:247], %[a1]\n\t"
                "ds_read_b64 v[208:209], %[kb] offset:256\n\t"
                "ds_read_b64 v[210:211], %[kb] offset:264\n\t"
                "ds_read_b128 v[212:215], %[ub] offset:4752\n\t"
                "s_waitcnt lgkmcnt(11)\n\t"
                "v_mfma_f32_32x32x16_bf16 %[a0], v[240:243], v[248:251], %[a0]\n\t"
                "ds_read_b128 v[216:219], %[ub] offset:2192\n\t"
                "s_waitcnt lgkmcnt(10)\n\t"
                "v_mfma_f32_32x32x16_bf16 %[a1], v[252:255], v[248:251], %[a1]\n\t"
                "ds_read_b64 v[220:221], %[kb] offset:2304\n\t"
                "ds_read_b64 v[222:223], %[kb] offset:2312\n\t"
                "s_waitcnt lgkmcnt(9)\n\t"
                "v_mfma_f32_32x32x16_bf16 %[a1], v[192:195], v[196:199], %[a1]\n\t"
                "ds_read_b64 v[224:225], %[kb] offset:352\n\t"
                "ds_read_b64 v[226:227], %[kb] offset:360\n\t"
                "ds_read_b128 v[228:231], %[ub] offset:4640\n\t"
                "s_waitcnt lgkmcnt(11)\n\t"
                "v_mfma_f32_32x32x16_bf16 %[a0], v[192:195], v[200:203], %[a0]\n\t"
                "ds_read_b128 v[232:235], %[ub] offset:2080\n\t"
                "s_waitcnt lgkmcnt(10)\n\t"
                "v_mfma_f32_32x32x16_bf16 %[a1], v[204:207], v[200:203], %[a1]\n\t"
                "ds_read_b64 v[236:237], %[kb] offset:2400\n\t"
                "ds_read_b64 v[238:239], %[kb] offset:2408\n\t"
                "s_waitcnt lgkmcnt(9)\n\t"
                "v_mfma_f32_32x32x16_bf16 %[a1], v[208:211], v[212:215], %[a1]\n\t"
                "ds_read_b64 v[240:241], %[kb] offset:320\n\t"
                "ds_read_b64 v[242:243], %[kb] offset:328\n\t"
                "ds_read_b128 v[244:247], %[ub] offset:4672\n\t"
                "s_waitcnt lgkmcnt(11)\n\t"
                "v_mfma_f32_32x32x16_bf16 %[a0], v[208:211], v[216:219], %[a0]\n\t"
                "ds_read_b128 v[248:251], %[ub] offset:2112\n\t"
                "s_waitcnt lgkmcnt(10)\n\t"
                "v_mfma_f32_32x32x16_bf16 %[a1], v[220:223], v[216:219], %[a1]\n\t"
                "ds_read_b64 v[252:253], %[kb] offset:2368\n\t"
                "ds_read_b64 v[254:255], %[kb] offset:2376\n\t"
                "s_waitcnt lgkmcnt(9)\n\t"
                "v_mfma_f32_32x32x16_bf16 %[a1], v[224:227], v[228:231], %[a1]\n\t"
                "ds_read_b64 v[192:193], %[kb] offset:416\n\t"
                "ds_read_b64 v[194:195], %[kb] offset:424\n\t"
                "ds_read_b128 v[196:199], %[ub] offset:4560\n\t"
                "s_waitcnt lgkmcnt(11)\n\t"
                "v_mfma_f32_32x32x16_bf16 %[a0], v[224:227], v[232:235], %[a0]\n\t"
                "ds_read_b128 v[200:203], %[ub] offset:2000\n\t"
                "s_waitcnt lgkmcnt(10)\n\t"
                "v_mfma_f32_32x32x16_bf16 %[a1], v[236:239], v[232:235], %[a1]\n\t"
                "ds_read_b64 v[204:205], %[kb] offset:2464\n\t"
                "ds_read_b64 v[206:207], %[kb] offset:2472\n\t"
                "s_waitcnt lgkmcnt(9)\n\t"
                "v_mfma_f32_32x32x16_bf16 %[a1], v[240:243], v[244:247], %[a1]\n\t"
                "ds_read_b64 v[208:209], %[kb] offset:384\n\t"
                "ds_read_b64 v[210:211], %[kb] offset:392\n\t"
                "ds_read_b128 v[212:215], %[ub] offset:4592\n\t"
                "s_waitcnt lgkmcnt(11)\n\t"
                "v_mfma_f32_32x32x16_bf16 %[a0], v[240:243], v[248:251], %[a0]\n\t"
                "ds_read_b128 v[216:219], %[ub] offset:2032\n\t"
                "s_waitcnt lgkmcnt(10)\n\t"
                "v_mfma_f32_32x32x16_bf16 %[a1], v[252:255], v[248:251], %[a1]\n\t"
                "ds_read_b64 v[220:221], %[kb] offset:2432\n\t"
                "ds_read_b64 v[222:223], %[kb] offset:2440\n\t"
                "s_waitcnt lgkmcnt(9)\n\t"
                "v_mfma_f32_32x32x16_bf16 %[a1], v[192:195], v[196:199], %[a1]\n\t"
                "ds_read_b64 v[224:225], %[kb] offset:480\n\t"
                "ds_read_b64 v[226:227], %[kb] offset:488\n\t"
                "ds_read_b128 v[228:231], %[ub] offset:4480\n\t"
                "s_waitcnt lgkmcnt(11)\n\t"
                "v_mfma_f32_32x32x16_bf16 %[a0], v[192:195], v[200:203], %[a0]\n\t"
                "ds_read_b128 v[232:235], %[ub] offset:1920\n\t"
                "s_waitcnt lgkmcnt(10)\n\t"
                "v_mfma_f32_32x32x16_bf16 %[a1], v[204:207], v[200:203], %[a1]\n\t"
                "ds_read_b64 v[236:237], %[kb] offset:2528\n\t"
                "ds_read_b64 v[238:239], %[kb] offset:2536\n\t"
                "s_waitcnt lgkmcnt(9)\n\t"
                "v_mfma_f32_32x32x16_bf16 %[a1], v[208:211], v[212:215], %[a1]\n\t"
                "ds_read_b64 v[240:241], %[kb] offset:448\n\t"
                "ds_read_b64 v[242:243], %[kb] offset:456\n\t"
                "ds_read_b128 v[244:247], %[ub] offset:4512\n\t"
                "s_waitcnt lgkmcnt(11)\n\t"
                "v_mfma_f32_32x32x16_bf16 %[a0], v[208:211], v[216:219], %[a0]\n\t"
                "ds_read_b128 v[248:251], %[ub] offset:1952\n\t"
                "s_waitcnt lgkmcnt(10)\n\t"
                "v_mfma_f32_32x32x16_bf16 %[a1], v[220:223], v[216:219], %[a1]\n\t"
                "ds_read_b64 v[252:253], %[kb] offset:2496\n\t"
                "ds_read_b64 v[254:255], %[kb] offset:2504\n\t"
                "s_waitcnt lgkmcnt(9)\n\t"
                "v_mfma_f32_32x32x16_bf16 %[a1], v[224:227], v[228:231], %[a1]\n\t"
                "ds_read_b64 v[192:193], %[kb] offset:544\n\t"
                "ds_read_b64 v[194:195], %[kb] offset:552\n\t"
                "ds_read_b128 v[196:199], %[ub] offset:4400\n\t"
                "s_waitcnt lgkmcnt(11)\n\t"
                "v_mfma_f32_32x32x16_bf16 %[a0], v[224:227], v[232:235], %[a0]\n\t"
                "ds_read_b128 v[200:203], %[ub] offset:1840\n\t"
                "s_waitcnt lgkmcnt(10)\n\t"
                "v_mfma_f32_32x32x16_bf16 %[a1], v[236:239], v[232:235], %[a1]\n\t"
                "ds_read_b64 v[204:205], %[kb] offset:2592\n\t"
                "ds_read_b64 v[206:207], %[kb] offset:2600\n\t"
                "s_waitcnt lgkmcnt(9)\n\t"
                "v_mfma_f32_32x32x16_bf16 %[a1], v[240:243], v[244:247], %[a1]\n\t"
                "ds_read_b64 v[208:209], %[kb] offset:512\n\t"
                "ds_read_b64 v[210:211], %[kb] offset:520\n\t"
                "ds_read_b128 v[212:215], %[ub] offset:4432\n\t"
                "s_waitcnt lgkmcnt(11)\n\t"
                "v_mfma_f32_32x32x16_bf16 %[a0], v[240:243], v[248:251], %[a0]\n\t"
                "ds_read_b128 v[216:219], %[ub] offset:1872\n\t"
                "s_waitcnt lgkmcnt(10)\n\t"
                "v_mfma_f32_32x32x16_bf16 %[a1], v[252:255], v[248:251], %[a1]\n\t"
                "ds_read_b64 v[220:221], %[kb] offset:2560\n\t"
                "ds_read_b64 v[222:223], %[kb] offset:2568\n\t"
                "s_waitcnt lgkmcnt(9)\n\t"
                "v_mfma_f32_32x32x16_bf16 %[a1], v[192:195], v[196:199], %[a1]\n\t"
                "ds_read_b64 v[224:225], %[kb] offset:608\n\t"
                "ds_read_b64 v[226:227], %[kb] offset:616\n\t"
                "ds_read_b128 v[228:231], %[ub] offset:4320\n\t"
                "s_waitcnt lgkmcnt(11)\n\t"
                "v_mfma_f32_32x32x16_bf16 %[a0], v[192:195], v[200:203], %[a0]\n\t"
                "ds_read_b128 v[232:235], %[ub] offset:1760\n\t"
                "s_waitcnt lgkmcnt(10)\n\t"
                "v_mfma_f32_32x32x16_bf16 %[a1], v[204:207], v[200:203], %[a1]\n\t"
                "ds_read_b64 v[236:237], %[kb] offset:2656\n\t"
                "ds_read_b64 v[238:239], %[kb] offset:2664\n\t"
                "s_waitcnt lgkmcnt(9)\n\t"
                "v_mfma_f32_32x32x16_bf16 %[a1], v[208:211], v[212:215], %[a1]\n\t"
                "ds_read_b64 v[240:241], %[kb] offset:576\n\t"
                "ds_read_b64 v[242:243], %[kb] offset:584\n\t"
                "ds_read_b128 v[244:247], %[ub] offset:4352\n\t"
                "s_waitcnt lgkmcnt(11)\n\t"
                "v_mfma_f32_32x32x16_bf16 %[a0], v[208:211], v[216:219], %[a0]\n\t"
                "ds_read_b128 v[248:251], %[ub] offset:1792\n\t"
                "s_waitcnt lgkmcnt(10)\n\t"
                "v_mfma_f32_32x32x16_bf16 %[a1], v[220:223], v[216:219], %[a1]\n\t"
                "ds_read_b64 v[252:253], %[kb] offset:2624\n\t"
                "ds_read_b64 v[254:255], %[kb] offset:2632\n\t"
                "s_waitcnt lgkmcnt(9)\n\t"
                "v_mfma_f32_32x32x16_bf16 %[a1], v[224:227], v[228:231], %[a1]\n\t"
                "ds_read_b64 v[192:193], %[kb] offset:672\n\t"
                "ds_read_b64 v[194:195], %[kb] offset:680\n\t"
                "ds_read_b128 v[196:199], %[ub] offset:4240\n\t"
                "s_waitcnt lgkmcnt(11)\n\t"
                "v_mfma_f32_32x32x16_bf16 %[a0], v[224:227], v[232:235], %[a0]\n\t"
                "ds_read_b128 v[200:203], %[ub] offset:1680\n\t"
                "s_waitcnt lgkmcnt(10)\n\t"
                "v_mfma_f32_32x32x16_bf16 %[a1], v[236:239], v[232:235], %[a1]\n\t"
                "ds_read_b64 v[204:205], %[kb] offset:2720\n\t"
                "ds_read_b64 v[206:207], %[kb] offset:2728\n\t"
                "s_waitcnt lgkmcnt(9)\n\t"
                "v_mfma_f32_32x32x16_bf16 %[a1], v[240:243], v[244:247], %[a1]\n\t"
                "ds_read_b64 v[208:209], %[kb] offset:640\n\t"
                "ds_read_b64 v[210:211], %[kb] offset:648\n\t"
                "ds_read_b128 v[212:215], %[ub] offset:4272\n\t"
                "s_waitcnt lgkmcnt(11)\n\t"
                "v_mfma_f32_32x32x16_bf16 %[a0], v[240:243], v[248:251], %[a0]\n\t"
                "ds_read_b128 v[216:219], %[ub] offset:1712\n\t"
                "s_waitcnt lgkmcnt(10)\n\t"
                "v_mfma_f32_32x32x16_bf16 %[a1], v[252:255], v[248:251], %[a1]\n\t"
                "ds_read_b64 v[220:221], %[kb] offset:2688\n\t"
                "ds_read_b64 v[222:223], %[kb] offset:2696\n\t"
                "s_waitcnt lgkmcnt(9)\n\t"
                "v_mfma_f32_32x32x16_bf16 %[a1], v[192:195], v[196:199], %[a1]\n\t"
                "ds_read_b64 v[224:225], %[kb] offset:736\n\t"
                "ds_read_b64 v[226:227], %[kb] offset:744\n\t"
                "ds_read_b128 v[228:231], %[ub] offset:4160\n\t"
                "s_waitcnt lgkmcnt(11)\n\t"
                "v_mfma_f32_32x32x16_bf16 %[a0], v[192:195], v[200:203], %[a0]\n\t"
                "ds_read_b128 v[232:235], %[ub] offset:1600\n\t"
                "s_waitcnt lgkmcnt(10)\n\t"
                "v_mfma_f32_32x32x16_bf16 %[a1], v[204:207], v[200:203], %[a1]\n\t"
                "ds_read_b64 v[236:237], %[kb] offset:2784\n\t"
                "ds_read_b64 v[238:239], %[kb] offset:2792\n\t"
                "s_waitcnt lgkmcnt(9)\n\t"
                "v_mfma_f32_32x32x16_bf16 %[a1], v[208:211], v[212:215], %[a1]\n\t"
                "ds_read_b64 v[240:241], %[kb] offset:704\n\t"
                "ds_read_b64 v[242:243], %[kb] offset:712\n\t"
                "ds_read_b128 v[244:247], %[ub] offset:4192\n\t"
                "s_waitcnt lgkmcnt(11)\n\t"
                "v_mfma_f32_32x32x16_bf16 %[a0], v[208:211], v[216:219], %[a0]\n\t"
                "ds_read_b128 v[248:251], %[ub] offset:1632\n\t"
                "s_waitcnt lgkmcnt(10)\n\t"
                "v_mfma_f32_32x32x16_bf16 %[a1], v[220:223], v[216:219], %[a1]\n\t"
                "ds_read_b64 v[252:253], %[kb] offset:2752\n\t"
                "ds_read_b64 v[254:255], %[kb] offset:2760\n\t"
                "s_waitcnt lgkmcnt(9)\n\t"
                "v_mfma_f32_32x32x16_bf16 %[a1], v[224:227], v[228:231], %[a1]\n\t"
                "ds_read_b64 v[192:193], %[kb] offset:800\n\t"
                "ds_read_b64 v[194:195], %[kb] offset:808\n\t"
                "ds_read_b128 v[196:199], %[ub] offset:4080\n\t"
                "s_waitcnt lgkmcnt(11)\n\t"
                "v_mfma_f32_32x32x16_bf16 %[a0], v[224:227], v[232:235], %[a0]\n\t"
                "ds_read_b128 v[200:203], %[ub] offset:1520\n\t"
                "s_waitcnt lgkmcnt(10)\n\t"
                "v_mfma_f32_32x32x16_bf16 %[a1], v[236:239], v[232:235], %[a1]\n\t"
                "ds_read_b64 v[204:205], %[kb] offset:2848\n\t"
                "ds_read_b64 v[206:207], %[kb] offset:2856\n\t"
                "s_waitcnt lgkmcnt(9)\n\t"
                "v_mfma_f32_32x32x16_bf16 %[a1], v[240:243], v[244:247], %[a1]\n\t"
                "ds_read_b64 v[208:209], %[kb] offset:768\n\t"
                "ds_read_b64 v[210:211], %[kb] offset:776\n\t"
                "ds_read_b128 v[212:215], %[ub] offset:4112\n\t"
                "s_waitcnt lgkmcnt(11)\n\t"
                "v_mfma_f32_32x32x16_bf16 %[a0], v[240:243], v[248:251], %[a0]\n\t"
                "ds_read_b128 v[216:219], %[ub] offset:1552\n\t"
                "s_waitcnt lgkmcnt(10)\n\t"
                "v_mfma_f32_32x32x16_bf16 %[a1], v[252:255], v[248:251], %[a1]\n\t"
                "ds_read_b64 v[220:221], %[kb] offset:2816\n\t"
                "ds_read_b64 v[222:223], %[kb] offset:2824\n\t"
                "s_waitcnt lgkmcnt(9)\n\t"
                "v_mfma_f32_32x32x16_bf16 %[a1], v[192:195], v[196:199], %[a1]\n\t"
                "ds_read_b64 v[224:225], %[kb] offset:864\n\t"
                "ds_read_b64 v[226:227], %[kb] offset:872\n\t"
                "ds_read_b128 v[228:231], %[ub] offset:4000\n\t"
                "s_waitcnt lgkmcnt(11)\n\t"
                "v_mfma_f32_32x32x16_bf16 %[a0], v[192:195], v[200:203], %[a0]\n\t"
                "ds_read_b128 v[232:235], %[ub] offset:1440\n\t"
                "s_waitcnt lgkmcnt(10)\n\t"
                "v_mfma_f32_32x32x16_bf16 %[a1], v[204:207], v[200:203], %[a1]\n\t"
                "ds_read_b64 v[236:237], %[kb] offset:2912\n\t"
                "ds_read_b64 v[238:239], %[kb] offset:2920\n\t"
                "s_waitcnt lgkmcnt(9)\n\t"
                "v_mfma_f32_32x32x16_bf16 %[a1], v[208:211], v[212:215], %[a1]\n\t"
                "ds_read_b64 v[240:241], %[kb] offset:832\n\t"
                "ds_read_b64 v[242:243], %[kb] offset:840\n\t"
                "ds_read_b128 v[244:247], %[ub] offset:4032\n\t"
                "s_waitcnt lgkmcnt(11)\n\t"
                "v_mfma_f32_32x32x16_bf16 %[a0], v[208:211], v[216:219], %[a0]\n\t"
                "ds_read_b128 v[248:251], %[ub] offset:1472\n\t"
                "s_waitcnt lgkmcnt(10)\n\t"
                "v_mfma_f32_32x32x16_bf16 %[a1], v[220:223], v[216:219], %[a1]\n\t"
                "ds_read_b64 v[252:253], %[kb] offset:2880\n\t"
                "ds_read_b64 v[254:255], %[kb] offset:2888\n\t"
                "s_waitcnt lgkmcnt(9)\n\t"
                "v_mfma_f32_32x32x16_bf16 %[a1], v[224:227], v[228:231], %[a1]\n\t"
                "ds_read_b64 v[192:193], %[kb] offset:928\n\t"
                "ds_read_b64 v[194:195], %[kb] offset:936\n\t"
                "ds_read_b128 v[196:199], %[ub] offset:3920\n\t"
                "s_waitcnt lgkmcnt(11)\n\t"
                "v_mfma_f32_32x32x16_bf16 %[a0], v[224:227], v[232:235], %[a0]\n\t"
                "ds_read_b128 v[200:203], %[ub] offset:1360\n\t"
                "s_waitcnt lgkmcnt(10)\n\t"
                "v_mfma_f32_32x32x16_bf16 %[a1], v[236:239], v[232:235], %[a1]\n\t"
                "ds_read_b64 v[204:205], %[kb] offset:2976\n\t"
                "ds_read_b64 v[206:207], %[kb] offset:2984\n\t"
                "s_waitcnt lgkmcnt(9)\n\t"
                "v_mfma_f32_32x32x16_bf16 %[a1], v[240:243], v[244:247], %[a1]\n\t"
                "ds_read_b64 v[208:209], %[kb] offset:896\n\t"
                "ds_read_b64 v[210:211], %[kb] offset:904\n\t"
                "ds_read_b128 v[212:215], %[ub] offset:3952\n\t"
                "s_waitcnt lgkmcnt(11)\n\t"
                "v_mfma_f32_32x32x16_bf16 %[a0], v[240:243], v[248:251], %[a0]\n\t"
                "ds_read_b128 v[216:219], %[ub] offset:1392\n\t"
                "s_waitcnt lgkmcnt(10)\n\t"
                "v_mfma_f32_32x32x16_bf16 %[a1], v[252:255], v[248:251], %[a1]\n\t"
                "ds_read_b64 v[220:221], %[kb] offset:2944\n\t"
                "ds_read_b64 v[222:223], %[kb] offset:2952\n\t"
                "s_waitcnt lgkmcnt(9)\n\t"
                "v_mfma_f32_32x32x16_bf16 %[a1], v[192:195], v[196:199], %[a1]\n\t"
                "ds_read_b64 v[224:225], %[kb] offset:992\n\t"
                "ds_read_b64 v[226:227], %[kb] offset:1000\n\t"
                "ds_read_b128 v[228:231], %[ub] offset:3840\n\t"
                "s_waitcnt lgkmcnt(11)\n\t"
                "v_mfma_f32_32x32x16_bf16 %[a0], v[192:195], v[200:203], %[a0]\n\t"
                "ds_read_b128 v[232:235], %[ub] offset:1280\n\t"
                "s_waitcnt lgkmcnt(10)\n\t"
                "v_mfma_f32_32x32x16_bf16 %[a1], v[204:207], v[200:203], %[a1]\n\t"
                "ds_read_b64 v[236:237], %[kb] offset:3040\n\t"
                "ds_read_b64 v[238:239], %[kb] offset:3048\n\t"
                "s_waitcnt lgkmcnt(9)\n\t"
                "v_mfma_f32_32x32x16_bf16 %[a1], v[208:211], v[212:215], %[a1]\n\t"
                "ds_read_b64 v[240:241], %[kb] offset:960\n\t"
                "ds_read_b64 v[242:243], %[kb] offset:968\n\t"
                "ds_read_b128 v[244:247], %[ub] offset:3872\n\t"
                "s_waitcnt lgkmcnt(11)\n\t"
                "v_mfma_f32_32x32x16_bf16 %[a0], v[208:211], v[216:219], %[a0]\n\t"
                "ds_read_b128 v[248:251], %[ub] offset:1312\n\t"
                "s_waitcnt lgkmcnt(10)\n\t"
                "v_mfma_f32_32x32x16_bf16 %[a1], v[220:223], v[216:219], %[a1]\n\t"
                "ds_read_b64 v[252:253], %[kb] offset:3008\n\t"
                "ds_read_b64 v[254:255], %[kb] offset:3016\n\t"
                "s_waitcnt lgkmcnt(9)\n\t"
                "v_mfma_f32_32x32x16_bf16 %[a1], v[224:227], v[228:231], %[a1]\n\t"
                "ds_read_b64 v[192:193], %[kb] offset:1056\n\t"
                "ds_read_b64 v[194:195], %[kb] offset:1064\n\t"
                "ds_read_b128 v[196:199], %[ub] offset:3760\n\t"
                "s_waitcnt lgkmcnt(11)\n\t"
                "v_mfma_f32_32x32x16_bf16 %[a0], v[224:227], v[232:235], %[a0]\n\t"
                "ds_read_b128 v[200:203], %[ub] offset:1200\n\t"
                "s_waitcnt lgkmcnt(10)\n\t"
                "v_mfma_f32_32x32x16_bf16 %[a1], v[236:239], v[232:235], %[a1]\n\t"
                "ds_read_b64 v[204:205], %[kb] offset:3104\n\t"
                "ds_read_b64 v[206:207], %[kb] offset:3112\n\t"
                "s_waitcnt lgkmcnt(9)\n\t"
                "v_mfma_f32_32x32x16_bf16 %[a1], v[240:243], v[244:247], %[a1]\n\t"
                "ds_read_b64 v[208:209], %[kb] offset:1024\n\t"
                "ds_read_b64 v[210:211], %[kb] offset:1032\n\t"
                "ds_read_b128 v[212:215], %[ub] offset:3792\n\t"
                "s_waitcnt lgkmcnt(11)\n\t"
                "v_mfma_f32_32x32x16_bf16 %[a0], v[240:243], v[248:251], %[a0]\n\t"
                "ds_read_b128 v[216:219], %[ub] offset:1232\n\t"
                "s_waitcnt lgkmcnt(10)\n\t"
                "v_mfma_f32_32x32x16_bf16 %[a1], v[252:255], v[248:251], %[a1]\n\t"
                "ds_read_b64 v[220:221], %[kb] offset:3072\n\t"
                "ds_read_b64 v[222:223], %[kb] offset:3080\n\t"
                "s_waitcnt lgkmcnt(9)\n\t"
                "v_mfma_f32_32x32x16_bf16 %[a1], v[192:195], v[196:199], %[a1]\n\t"
                "ds_read_b64 v[224:225], %[kb] offset:1120\n\t"
                "ds_read_b64 v[226:227], %[kb] offset:1128\n\t"
                "ds_read_b128 v[228:231], %[ub] offset:3680\n\t"
                "s_waitcnt lgkmcnt(11)\n\t"
                "v_mfma_f32_32x32x16_bf16 %[a0], v[192:195], v[200:203], %[a0]\n\t"
                "ds_read_b128 v[232:235], %[ub] offset:1120\n\t"
                "s_waitcnt lgkmcnt(10)\n\t"
                "v_mfma_f32_32x32x16_bf16 %[a1], v[204:207], v[200:203], %[a1]\n\t"
                "ds_read_b64 v[236:237], %[kb] offset:3168\n\t"
                "ds_read_b64 v[238:239], %[kb] offset:3176\n\t"
                "s_waitcnt lgkmcnt(9)\n\t"
                "v_mfma_f32_32x32x16_bf16 %[a1], v[208:211], v[212:215], %[a1]\n\t"
                "ds_read_b64 v[240:241], %[kb] offset:1088\n\t"
                "ds_read_b64 v[242:243], %[kb] offset:1096\n\t"
                "ds_read_b128 v[244:247], %[ub] offset:3712\n\t"
                "s_waitcnt lgkmcnt(11)\n\t"
                "v_mfma_f32_32x32x16_bf16 %[a0], v[208:211], v[216:219], %[a0]\n\t"
                "ds_read_b128 v[248:251], %[ub] offset:1152\n\t"
                "s_waitcnt lgkmcnt(10)\n\t"
                "v_mfma_f32_32x32x16_bf16 %[a1], v[220:223], v[216:219], %[a1]\n\t"
                "ds_read_b64 v[252:253], %[kb] offset:3136\n\t"
                "ds_read_b64 v[254:255], %[kb] offset:3144\n\t"
                "s_waitcnt lgkmcnt(9)\n\t"
                "v_mfma_f32_32x32x16_bf16 %[a1], v[224:227], v[228:231], %[a1]\n\t"
                "ds_read_b64 v[192:193], %[kb] offset:1184\n\t"
                "ds_read_b64 v[194:195], %[kb] offset:1192\n\t"
                "ds_read_b128 v[196:199], %[ub] offset:3600\n\t"
                "s_waitcnt lgkmcnt(11)\n\t"
                "v_mfma_f32_32x32x16_bf16 %[a0], v[224:227], v[232:235], %[a0]\n\t"
                "ds_read_b128 v[200:203], %[ub] offset:1040\n\t"
                "s_waitcnt lgkmcnt(10)\n\t"
                "v_mfma_f32_32x32x16_bf16 %[a1], v[236:239], v[232:235], %[a1]\n\t"
                "ds_read_b64 v[204:205], %[kb] offset:3232\n\t"
                "ds_read_b64 v[206:207], %[kb] offset:3240\n\t"
                "s_waitcnt lgkmcnt(9)\n\t"
                "v_mfma_f32_32x32x16_bf16 %[a1], v[240:243], v[244:247], %[a1]\n\t"
                "ds_read_b64 v[208:209], %[kb] offset:1152\n\t"
                "ds_read_b64 v[210:211], %[kb] offset:1160\n\t"
                "ds_read_b128 v[212:215], %[ub] offset:3632\n\t"
                "s_waitcnt lgkmcnt(11)\n\t"
                "v_mfma_f32_32x32x16_bf16 %[a0], v[240:243], v[248:251], %[a0]\n\t"
                "ds_read_b128 v[216:219], %[ub] offset:1072\n\t"
                "s_waitcnt lgkmcnt(10)\n\t"
                "v_mfma_f32_32x32x16_bf16 %[a1], v[252:255], v[248:251], %[a1]\n\t"
                "ds_read_b64 v[220:221], %[kb] offset:3200\n\t"
                "ds_read_b64 v[222:223], %[kb] offset:3208\n\t"
                "s_waitcnt lgkmcnt(9)\n\t"
                "v_mfma_f32_32x32x16_bf16 %[a1], v[192:195], v[196:199], %[a1]\n\t"
                "ds_read_b64 v[224:225], %[kb] offset:1248\n\t"
                "ds_read_b64 v[226:227], %[kb] offset:1256\n\t"
                "ds_read_b128 v[228:231], %[ub] offset:3520\n\t"
                "s_waitcnt lgkmcnt(11)\n\t"
                "v_mfma_f32_32x32x16_bf16 %[a0], v[192:195], v[200:203], %[a0]\n\t"
                "ds_read_b128 v[232:235], %[ub] offset:960\n\t"
                "s_waitcnt lgkmcnt(10)\n\t"
                "v_mfma_f32_32x32x16_bf16 %[a1], v[204:207], v[200:203], %[a1]\n\t"
                "ds_read_b64 v[236:237], %[kb] offset:3296\n\t"
                "ds_read_b64 v[238:239], %[kb] offset:3304\n\t"
                "s_waitcnt lgkmcnt(9)\n\t"
                "v_mfma_f32_32x32x16_bf16 %[a1], v[208:211], v[212:215], %[a1]\n\t"
                "ds_read_b64 v[240:241], %[kb] offset:1216\n\t"
                "ds_read_b64 v[242:243], %[kb] offset:1224\n\t"
                "ds_read_b128 v[244:247], %[ub] offset:3552\n\t"
                "s_waitcnt lgkmcnt(11)\n\t"
                "v_mfma_f32_32x32x16_bf16 %[a0], v[208:211], v[216:219], %[a0]\n\t"
                "ds_read_b128 v[248:251], %[ub] offset:992\n\t"
                "s_waitcnt lgkmcnt(10)\n\t"
                "v_mfma_f32_32x32x16_bf16 %[a1], v[220:223], v[216:219], %[a1]\n\t"
                "ds_read_b64 v[252:253], %[kb] offset:3264\n\t"
                "ds_read_b64 v[254:255], %[kb] offset:3272\n\t"
                "s_waitcnt lgkmcnt(9)\n\t"
                "v_mfma_f32_32x32x16_bf16 %[a1], v[224:227], v[228:231], %[a1]\n\t"
                "ds_read_b64 v[192:193], %[kb] offset:1312\n\t"
                "ds_read_b64 v[194:195], %[kb] offset:1320\n\t"
                "ds_read_b128 v[196:199], %[ub] offset:3440\n\t"
                "s_waitcnt lgkmcnt(11)\n\t"
                "v_mfma_f32_32x32x16_bf16 %[a0], v[224:227], v[232:235], %[a0]\n\t"
                "ds_read_b128 v[200:203], %[ub] offset:880\n\t"
                "s_waitcnt lgkmcnt(10)\n\t"
                "v_mfma_f32_32x32x16_bf16 %[a1], v[236:239], v[232:235], %[a1]\n\t"
                "ds_read_b64 v[204:205], %[kb] offset:3360\n\t"
                "ds_read_b64 v[206:207], %[kb] offset:3368\n\t"
                "s_waitcnt lgkmcnt(9)\n\t"
                "v_mfma_f32_32x32x16_bf16 %[a1], v[240:243], v[244:247], %[a1]\n\t"
                "ds_read_b64 v[208:209], %[kb] offset:1280\n\t"
                "ds_read_b64 v[210:211], %[kb] offset:1288\n\t"
                "ds_read_b128 v[212:215], %[ub] offset:3472\n\t"
                "s_waitcnt lgkmcnt(11)\n\t"
                "v_mfma_f32_32x32x16_bf16 %[a0], v[240:243], v[248:251], %[a0]\n\t"
                "ds_read_b128 v[216:219], %[ub] offset:912\n\t"
                "s_waitcnt lgkmcnt(10)\n\t"
                "v_mfma_f32_32x32x16_bf16 %[a1], v[252:255], v[248:251], %[a1]\n\t"
                "ds_read_b64 v[220:221], %[kb] offset:3328\n\t"
                "ds_read_b64 v[222:223], %[kb] offset:3336\n\t"
                "s_waitcnt lgkmcnt(9)\n\t"
                "v_mfma_f32_32x32x16_bf16 %[a1], v[192:195], v[196:199], %[a1]\n\t"
                "ds_read_b64 v[224:225], %[kb] offset:1376\n\t"
                "ds_read_b64 v[226:227], %[kb] offset:1384\n\t"
                "ds_read_b128 v[228:231], %[ub] offset:3360\n\t"
                "s_waitcnt lgkmcnt(11)\n\t"
                "v_mfma_f32_32x32x16_bf16 %[a0], v[192:195], v[200:203], %[a0]\n\t"
                "ds_read_b128 v[232:235], %[ub] offset:800\n\t"
                "s_waitcnt lgkmcnt(10)\n\t"
                "v_mfma_f32_32x32x16_bf16 %[a1], v[204:207], v[200:203], %[a1]\n\t"
                "ds_read_b64 v[236:237], %[kb] offset:3424\n\t"
                "ds_read_b64 v[238:239], %[kb] offset:3432\n\t"
                "s_waitcnt lgkmcnt(9)\n\t"
                "v_mfma_f32_32x32x16_bf16 %[a1], v[208:211], v[212:215], %[a1]\n\t"
                "ds_read_b64 v[240:241], %[kb] offset:1344\n\t"
                "ds_read_b64 v[242:243], %[kb] offset:1352\n\t"
                "ds_read_b128 v[244:247], %[ub] offset:3392\n\t"
                "s_waitcnt lgkmcnt(11)\n\t"
                "v_mfma_f32_32x32x16_bf16 %[a0], v[208:211], v[216:219], %[a0]\n\t"
                "ds_read_b128 v[248:251], %[ub] offset:832\n\t"
                "s_waitcnt lgkmcnt(10)\n\t"
                "v_mfma_f32_32x32x16_bf16 %[a1], v[220:223], v[216:219], %[a1]\n\t"
                "ds_read_b64 v[252:253], %[kb] offset:3392\n\t"
                "ds_read_b64 v[254:255], %[kb] offset:3400\n\t"
                "s_waitcnt lgkmcnt(9)\n\t"
                "v_mfma_f32_32x32x16_bf16 %[a1], v[224:227], v[228:231], %[a1]\n\t"
                "ds_read_b64 v[192:193], %[kb] offset:1440\n\t"
                "ds_read_b64 v[194:195], %[kb] offset:1448\n\t"
                "ds_read_b128 v[196:199], %[ub] offset:3280\n\t"
                "s_waitcnt lgkmcnt(11)\n\t"
                "v_mfma_f32_32x32x16_bf16 %[a0], v[224:227], v[232:235], %[a0]\n\t"
                "ds_read_b128 v[200:203], %[ub] offset:720\n\t"
                "s_waitcnt lgkmcnt(10)\n\t"
                "v_mfma_f32_32x32x16_bf16 %[a1], v[236:239], v[232:235], %[a1]\n\t"
                "ds_read_b64 v[204:205], %[kb] offset:3488\n\t"
                "ds_read_b64 v[206:207], %[kb] offset:3496\n\t"
                "s_waitcnt lgkmcnt(9)\n\t"
                "v_mfma_f32_32x32x16_bf16 %[a1], v[240:243], v[244:247], %[a1]\n\t"
                "ds_read_b64 v[208:209], %[kb] offset:1408\n\t"
                "ds_read_b64 v[210:211], %[kb] offset:1416\n\t"
                "ds_read_b128 v[212:215], %[ub] offset:3312\n\t"
                "s_waitcnt lgkmcnt(11)\n\t"
                "v_mfma_f32_32x32x16_bf16 %[a0], v[240:243], v[248:251], %[a0]\n\t"
                "ds_read_b128 v[216:219], %[ub] offset:752\n\t"
                "s_waitcnt lgkmcnt(10)\n\t"
                "v_mfma_f32_32x32x16_bf16 %[a1], v[252:255], v[248:251], %[a1]\n\t"
                "ds_read_b64 v[220:221], %[kb] offset:3456\n\t"
                "ds_read_b64 v[222:223], %[kb] offset:3464\n\t"
                "s_waitcnt lgkmcnt(9)\n\t"
                "v_mfma_f32_32x32x16_bf16 %[a1], v[192:195], v[196:199], %[a1]\n\t"
                "ds_read_b64 v[224:225], %[kb] offset:1504\n\t"
                "ds_read_b64 v[226:227], %[kb] offset:1512\n\t"
                "ds_read_b128 v[228:231], %[ub] offset:3200\n\t"
                "s_waitcnt lgkmcnt(11)\n\t"
                "v_mfma_f32_32x32x16_bf16 %[a0], v[192:195], v[200:203], %[a0]\n\t"
                "ds_read_b128 v[232:235], %[ub] offset:640\n\t"
                "s_waitcnt lgkmcnt(10)\n\t"
                "v_mfma_f32_32x32x16_bf16 %[a1], v[204:207], v[200:203], %[a1]\n\t"
                "ds_read_b64 v[236:237], %[kb] offset:3552\n\t"
                "ds_read_b64 v[238:239], %[kb] offset:3560\n\t"
                "s_waitcnt lgkmcnt(9)\n\t"
                "v_mfma_f32_32x32x16_bf16 %[a1], v[208:211], v[212:215], %[a1]\n\t"
                "ds_read_b64 v[240:241], %[kb] offset:1472\n\t"
                "ds_read_b64 v[242:243], %[kb] offset:1480\n\t"
                "ds_read_b128 v[244:247], %[ub] offset:3232\n\t"
                "s_waitcnt lgkmcnt(11)\n\t"
                "v_mfma_f32_32x32x16_bf16 %[a0], v[208:211], v[216:219], %[a0]\n\t"
                "ds_read_b128 v[248:251], %[ub] offset:672\n\t"
                "s_waitcnt lgkmcnt(10)\n\t"
                "v_mfma_f32_32x32x16_bf16 %[a1], v[220:223], v[216:219], %[a1]\n\t"
                "ds_read_b64 v[252:253], %[kb] offset:3520\n\t"
                "ds_read_b64 v[254:255], %[kb] offset:3528\n\t"
                "s_waitcnt lgkmcnt(9)\n\t"
                "v_mfma_f32_32x32x16_bf16 %[a1], v[224:227], v[228:231], %[a1]\n\t"
                "ds_read_b64 v[192:193], %[kb] offset:1568\n\t"
                "ds_read_b64 v[194:195], %[kb] offset:1576\n\t"
                "ds_read_b128 v[196:199], %[ub] offset:3120\n\t"
                "s_waitcnt lgkmcnt(11)\n\t"
                "v_mfma_f32_32x32x16_bf16 %[a0], v[224:227], v[232:235], %[a0]\n\t"
                "ds_read_b128 v[200:203], %[ub] offset:560\n\t"
                "s_waitcnt lgkmcnt(10)\n\t"
                "v_mfma_f32_32x32x16_bf16 %[a1], v[236:239], v[232:235], %[a1]\n\t"
                "ds_read_b64 v[204:205], %[kb] offset:3616\n\t"
                "ds_read_b64 v[206:207], %[kb] offset:3624\n\t"
                "s_waitcnt lgkmcnt(9)\n\t"
                "v_mfma_f32_32x32x16_bf16 %[a1], v[240:243], v[244:247], %[a1]\n\t"
                "ds_read_b64 v[208:209], %[kb] offset:1536\n\t"
                "ds_read_b64 v[210:211], %[kb] offset:1544\n\t"
                "ds_read_b128 v[212:215], %[ub] offset:3152\n\t"
                "s_waitcnt lgkmcnt(11)\n\t"
                "v_mfma_f32_32x32x16_bf16 %[a0], v[240:243], v[248:251], %[a0]\n\t"
                "ds_read_b128 v[216:219], %[ub] offset:592\n\t"
                "s_waitcnt lgkmcnt(10)\n\t"
                "v_mfma_f32_32x32x16_bf16 %[a1], v[252:255], v[248:251], %[a1]\n\t"
                "ds_read_b64 v[220:221], %[kb] offset:3584\n\t"
                "ds_read_b64 v[222:223], %[kb] offset:3592\n\t"
                "s_waitcnt lgkmcnt(9)\n\t"
                "v_mfma_f32_32x32x16_bf16 %[a1], v[192:195], v[196:199], %[a1]\n\t"
                "ds_read_b64 v[224:225], %[kb] offset:1632\n\t"
                "ds_read_b64 v[226:227], %[kb] offset:1640\n\t"
                "ds_read_b128 v[228:231], %[ub] offset:3040\n\t"
                "s_waitcnt lgkmcnt(11)\n\t"
                "v_mfma_f32_32x32x16_bf16 %[a0], v[192:195], v[200:203], %[a0]\n\t"
                "ds_read_b128 v[232:235], %[ub] offset:480\n\t"
                "s_waitcnt lgkmcnt(10)\n\t"
                "v_mfma_f32_32x32x16_bf16 %[a1], v[204:207], v[200:203], %[a1]\n\t"
                "ds_read_b64 v[236:237], %[kb] offset:3680\n\t"
                "ds_read_b64 v[238:239], %[kb] offset:3688\n\t"
                "s_waitcnt lgkmcnt(9)\n\t"
                "v_mfma_f32_32x32x16_bf16 %[a1], v[208:211], v[212:215], %[a1]\n\t"
                "ds_read_b64 v[240:241], %[kb] offset:1600\n\t"
                "ds_read_b64 v[242:243], %[kb] offset:1608\n\t"
                "ds_read_b128 v[244:247], %[ub] offset:3072\n\t"
                "s_waitcnt lgkmcnt(11)\n\t"
                "v_mfma_f32_32x32x16_bf16 %[a0], v[208:211], v[216:219], %[a0]\n\t"
                "ds_read_b128 v[248:251], %[ub] offset:512\n\t"
                "s_waitcnt lgkmcnt(10)\n\t"
                "v_mfma_f32_32x32x16_bf16 %[a1], v[220:223], v[216:219], %[a1]\n\t"
                "ds_read_b64 v[252:253], %[kb] offset:3648\n\t"
                "ds_read_b64 v[254:255], %[kb] offset:3656\n\t"
                "s_waitcnt lgkmcnt(9)\n\t"
                "v_mfma_f32_32x32x16_bf16 %[a1], v[224:227], v[228:231], %[a1]\n\t"
                "ds_read_b64 v[192:193], %[kb] offset:1696\n\t"
                "ds_read_b64 v[194:195], %[kb] offset:1704\n\t"
                "ds_read_b128 v[196:199], %[ub] offset:2960\n\t"
                "s_waitcnt lgkmcnt(11)\n\t"
                "v_mfma_f32_32x32x16_bf16 %[a0], v[224:227], v[232:235], %[a0]\n\t"
                "ds_read_b128 v[200:203], %[ub] offset:400\n\t"
                "s_waitcnt lgkmcnt(10)\n\t"
                "v_mfma_f32_32x32x16_bf16 %[a1], v[236:239], v[232:235], %[a1]\n\t"
                "ds_read_b64 v[204:205], %[kb] offset:3744\n\t"
                "ds_read_b64 v[206:207], %[kb] offset:3752\n\t"
                "s_waitcnt lgkmcnt(9)\n\t"
                "v_mfma_f32_32x32x16_bf16 %[a1], v[240:243], v[244:247], %[a1]\n\t"
                "ds_read_b64 v[208:209], %[kb] offset:1664\n\t"
                "ds_read_b64 v[210:211], %[kb] offset:1672\n\t"
                "ds_read_b128 v[212:215], %[ub] offset:2992\n\t"
                "s_waitcnt lgkmcnt(11)\n\t"
                "v_mfma_f32_32x32x16_bf16 %[a0], v[240:243], v[248:251], %[a0]\n\t"
                "ds_read_b128 v[216:219], %[ub] offset:432\n\t"
                "s_waitcnt lgkmcnt(10)\n\t"
                "v_mfma_f32_32x32x16_bf16 %[a1], v[252:255], v[248:251], %[a1]\n\t"
                "ds_read_b64 v[220:221], %[kb] offset:3712\n\t"
                "ds_read_b64 v[222:223], %[kb] offset:3720\n\t"
                "s_waitcnt lgkmcnt(9)\n\t"
                "v_mfma_f32_32x32x16_bf16 %[a1], v[192:195], v[196:199], %[a1]\n\t"
                "ds_read_b64 v[224:225], %[kb] offset:1760\n\t"
                "ds_read_b64 v[226:227], %[kb] offset:1768\n\t"
                "ds_read_b128 v[228:231], %[ub] offset:2880\n\t"
                "s_waitcnt lgkmcnt(11)\n\t"
                "v_mfma_f32_32x32x16_bf16 %[a0], v[192:195], v[200:203], %[a0]\n\t"
                "ds_read_b128 v[232:235], %[ub] offset:320\n\t"
                "s_waitcnt lgkmcnt(10)\n\t"
                "v_mfma_f32_32x32x16_bf16 %[a1], v[204:207], v[200:203], %[a1]\n\t"
                "ds_read_b64 v[236:237], %[kb] offset:3808\n\t"
                "ds_read_b64 v[238:239], %[kb] offset:3816\n\t"
                "s_waitcnt lgkmcnt(9)\n\t"
                "v_mfma_f32_32x32x16_bf16 %[a1], v[208:211], v[212:215], %[a1]\n\t"
                "ds_read_b64 v[240:241], %[kb] offset:1728\n\t"
                "ds_read_b64 v[242:243], %[kb] offset:1736\n\t"
                "ds_read_b128 v[244:247], %[ub] offset:2912\n\t"
                "s_waitcnt lgkmcnt(11)\n\t"
                "v_mfma_f32_32x32x16_bf16 %[a0], v[208:211], v[216:219], %[a0]\n\t"
                "ds_read_b128 v[248:251], %[ub] offset:352\n\t"
                "s_waitcnt lgkmcnt(10)\n\t"
                "v_mfma_f32_32x32x16_bf16 %[a1], v[220:223], v[216:219], %[a1]\n\t"
                "ds_read_b64 v[252:253], %[kb] offset:3776\n\t"
                "ds_read_b64 v[254:255], %[kb] offset:3784\n\t"
                "s_waitcnt lgkmcnt(9)\n\t"
                "v_mfma_f32_32x32x16_bf16 %[a1], v[224:227], v[228:231], %[a1]\n\t"
                "ds_read_b64 v[192:193], %[kb] offset:1824\n\t"
                "ds_read_b64 v[194:195], %[kb] offset:1832\n\t"
                "ds_read_b128 v[196:199], %[ub] offset:2800\n\t"
                "s_waitcnt lgkmcnt(11)\n\t"
                "v_mfma_f32_32x32x16_bf16 %[a0], v[224:227], v[232:235], %[a0]\n\t"
                "ds_read_b128 v[200:203], %[ub] offset:240\n\t"
                "s_waitcnt lgkmcnt(10)\n\t"
                "v_mfma_f32_32x32x16_bf16 %[a1], v[236:239], v[232:235], %[a1]\n\t"
                "ds_read_b64 v[204:205], %[kb] offset:3872\n\t"
                "ds_read_b64 v[206:207], %[kb] offset:3880\n\t"
                "s_waitcnt lgkmcnt(9)\n\t"
                "v_mfma_f32_32x32x16_bf16 %[a1], v[240:243], v[244:247], %[a1]\n\t"
                "ds_read_b64 v[208:209], %[kb] offset:1792\n\t"
                "ds_read_b64 v[210:211], %[kb] offset:1800\n\t"
                "ds_read_b128 v[212:215], %[ub] offset:2832\n\t"
                "s_waitcnt lgkmcnt(11)\n\t"
                "v_mfma_f32_32x32x16_bf16 %[a0], v[240:243], v[248:251], %[a0]\n\t"
                "ds_read_b128 v[216:219], %[ub] offset:272\n\t"
                "s_waitcnt lgkmcnt(10)\n\t"
                "v_mfma_f32_32x32x16_bf16 %[a1], v[252:255], v[248:251], %[a1]\n\t"
                "ds_read_b64 v[220:221], %[kb] offset:3840\n\t"
                "ds_read_b64 v[222:223], %[kb] offset:3848\n\t"
                "s_waitcnt lgkmcnt(9)\n\t"
                "v_mfma_f32_32x32x16_bf16 %[a1], v[192:195], v[196:199], %[a1]\n\t"
                "ds_read_b64 v[224:225], %[kb] offset:1888\n\t"
                "ds_read_b64 v[226:227], %[kb] offset:1896\n\t"
                "ds_read_b128 v[228:231], %[ub] offset:2720\n\t"
                "s_waitcnt lgkmcnt(11)\n\t"
                "v_mfma_f32_32x32x16_bf16 %[a0], v[192:195], v[200:203], %[a0]\n\t"
                "ds_read_b128 v[232:235], %[ub] offset:160\n\t"
                "s_waitcnt lgkmcnt(10)\n\t"
                "v_mfma_f32_32x32x16_bf16 %[a1], v[204:207], v[200:203], %[a1]\n\t"
                "ds_read_b64 v[236:237], %[kb] offset:3936\n\t"
                "ds_read_b64 v[238:239], %[kb] offset:3944\n\t"
                "s_waitcnt lgkmcnt(9)\n\t"
                "v_mfma_f32_32x32x16_bf16 %[a1], v[208:211], v[212:215], %[a1]\n\t"
                "ds_read_b64 v[240:241], %[kb] offset:1856\n\t"
                "ds_read_b64 v[242:243], %[kb] offset:1864\n\t"
                "ds_read_b128 v[244:247], %[ub] offset:2752\n\t"
                "s_waitcnt lgkmcnt(11)\n\t"
                "v_mfma_f32_32x32x16_bf16 %[a0], v[208:211], v[216:219], %[a0]\n\t"
                "ds_read_b128 v[248:251], %[ub] offset:192\n\t"
                "s_waitcnt lgkmcnt(10)\n\t"
                "v_mfma_f32_32x32x16_bf16 %[a1], v[220:223], v[216:219], %[a1]\n\t"
                "ds_read_b64 v[252:253], %[kb] offset:3904\n\t"
                "ds_read_b64 v[254:255], %[kb] offset:3912\n\t"
                "s_waitcnt lgkmcnt(9)\n\t"
                "v_mfma_f32_32x32x16_bf16 %[a1], v[224:227], v[228:231], %[a1]\n\t"
                "ds_read_b64 v[192:193], %[kb] offset:1952\n\t"
                "ds_read_b64 v[194:195], %[kb] offset:1960\n\t"
                "ds_read_b128 v[196:199], %[ub] offset:2640\n\t"
                "s_waitcnt lgkmcnt(11)\n\t"
                "v_mfma_f32_32x32x16_bf16 %[a0], v[224:227], v[232:235], %[a0]\n\t"
                "ds_read_b128 v[200:203], %[ub] offset:80\n\t"
                "s_waitcnt lgkmcnt(10)\n\t"
                "v_mfma_f32_32x32x16_bf16 %[a1], v[236:239], v[232:235], %[a1]\n\t"
                "ds_read_b64 v[204:205], %[kb] offset:4000\n\t"
                "ds_read_b64 v[206:207], %[kb] offset:4008\n\t"
                "s_waitcnt lgkmcnt(9)\n\t"
                "v_mfma_f32_32x32x16_bf16 %[a1], v[240:243], v[244:247], %[a1]\n\t"
                "ds_read_b64 v[208:209], %[kb] offset:1920\n\t"
                "ds_read_b64 v[210:211], %[kb] offset:1928\n\t"
                "ds_read_b128 v[212:215], %[ub] offset:2672\n\t"
                "s_waitcnt lgkmcnt(11)\n\t"
                "v_mfma_f32_32x32x16_bf16 %[a0], v[240:243], v[248:251], %[a0]\n\t"
                "ds_read_b128 v[216:219], %[ub] offset:112\n\t"
                "s_waitcnt lgkmcnt(10)\n\t"
                "v_mfma_f32_32x32x16_bf16 %[a1], v[252:255], v[248:251], %[a1]\n\t"
                "ds_read_b64 v[220:221], %[kb] offset:3968\n\t"
                "ds_read_b64 v[222:223], %[kb] offset:3976\n\t"
                "s_waitcnt lgkmcnt(9)\n\t"
                "v_mfma_f32_32x32x16_bf16 %[a1], v[192:195], v[196:199], %[a1]\n\t"
                "ds_read_b64 v[224:225], %[kb] offset:2016\n\t"
                "ds_read_b64 v[226:227], %[kb] offset:2024\n\t"
                "ds_read_b128 v[228:231], %[ub] offset:2560\n\t"
                "s_waitcnt lgkmcnt(11)\n\t"
                "v_mfma_f32_32x32x16_bf16 %[a0], v[192:195], v[200:203], %[a0]\n\t"
                "ds_read_b128 v[232:235], %[ub] offset:0\n\t"
                "s_waitcnt lgkmcnt(10)\n\t"
                "v_mfma_f32_32x32x16_bf16 %[a1], v[204:207], v[200:203], %[a1]\n\t"
                "ds_read_b64 v[236:237], %[kb] offset:4064\n\t"
                "ds_read_b64 v[238:239], %[kb] offset:4072\n\t"
                "s_waitcnt lgkmcnt(9)\n\t"
                "v_mfma_f32_32x32x16_bf16 %[a1], v[208:211], v[212:215], %[a1]\n\t"
                "ds_read_b64 v[240:241], %[kb] offset:1984\n\t"
                "ds_read_b64 v[242:243], %[kb] offset:1992\n\t"
                "ds_read_b128 v[244:247], %[ub] offset:2592\n\t"
                "s_waitcnt lgkmcnt(11)\n\t"
                "v_mfma_f32_32x32x16_bf16 %[a0], v[208:211], v[216:219], %[a0]\n\t"
                "ds_read_b128 v[248:251], %[ub] offset:32\n\t"
                "s_waitcnt lgkmcnt(10)\n\t"
                "v_mfma_f32_32x32x16_bf16 %[a1], v[220:223], v[216:219], %[a1]\n\t"
                "ds_read_b64 v[252:253], %[kb] offset:4032\n\t"
                "ds_read_b64 v[254:255], %[kb] offset:4040\n\t"
                "s_waitcnt lgkmcnt(9)\n\t"
                "v_mfma_f32_32x32x16_bf16 %[a1], v[224:227], v[228:231], %[a1]\n\t"
                "s_waitcnt lgkmcnt(8)\n\t"
                "v_mfma_f32_32x32x16_bf16 %[a0], v[224:227], v[232:235], %[a0]\n\t"
                "s_waitcnt lgkmcnt(6)\n\t"
                "v_mfma_f32_32x32x16_bf16 %[a1], v[236:239], v[232:235], %[a1]\n\t"
                "s_waitcnt lgkmcnt(3)\n\t"
                "v_mfma_f32_32x32x16_bf16 %[a1], v[240:243], v[244:247], %[a1]\n\t"
                "s_waitcnt lgkmcnt(2)\n\t"
                "v_mfma_f32_32x32x16_bf16 %[a0], v[240:243], v[248:251], %[a0]\n\t"
                "s_waitcnt lgkmcnt(0)\n\t"
                "v_mfma_f32_32x32x16_bf16 %[a1], v[252:255], v[248:251], %[a1]\n\t"
                "s_nop 7\n\t"
                "s_nop 3\n\t"
                : [a0] "=&v"(acc0), [a1] "=&v"(acc1) : [kb] "v"(kb), [ub] "v"(ub0 + c8 * CV_U) : "memory", "v192", "v193", "v194", "v195", "v196", "v197", "v198", "v199", "v200", "v201", "v202", "v203", "v204", "v205", "v206", "v207", "v208", "v209", "v210", "v211", "v212", "v213", "v214", "v215", "v216", "v217", "v218", "v219", "v220", "v221", "v222", "v223", "v224", "v225", "v226", "v227", "v228", "v229", "v230", "v231", "v232", "v233", "v234", "v235", "v236", "v237", "v238", "v239", "v240", "v241", "v242", "v243", "v244", "v245", "v246", "v247", "v248", "v249", "v250", "v251", "v252", "v253", "v254", "v255");
            unsigned char* uc = lds + c8 * CV_U + (32 + b) * CV_RP + kg * 8;
#pragma unroll
            for (int q = 0; q < 4; ++q) {
                *(v2u*)(uc + q * 16) = (v2u){pack2(acc0[4 * q], acc0[4 * q + 1]), pack2(acc0[4 * q + 2], acc0[4 * q + 3])};
                *(v2u*)(uc + 32 * CV_RP + q * 16) = (v2u){pack2(acc1[4 * q], acc1[4 * q + 1]), pack2(acc1[4 * q + 2], acc1[4 * q + 3])}; }
            __builtin_amdgcn_wave_barrier();
        }
    __syncthreads();
#pragma unroll
    for (int G = 0; G < 4; ++G) { const v4u lo = *(const v4u*)(urow + G * 16), hh = *(const v4u*)(urow + CV_U + G * 16); v4u o[2];
#pragma unroll
        for (int i = 0; i < 8; ++i) { const unsigned u0 = ur[G][(7 - i) >> 1], u1 = ur[4 + G][(7 - i) >> 1], qq = qr[2 * G + (i >> 2)][i & 3];
            const unsigned ud = pack2(((i & 1) ? blo(u0) : bhi(u0)) * blo(dv), ((i & 1) ? blo(u1) : bhi(u1)) * bhi(dv));
            const unsigned y = pack2(((i & 1) ? bhi(lo[i >> 1]) : blo(lo[i >> 1])) + blo(ud), ((i & 1) ? bhi(hh[i >> 1]) : blo(hh[i >> 1])) + bhi(ud));
            o[i >> 2][i & 3] = pack2(blo(qq) * blo(y), bhi(qq) * bhi(y)); }
        st16(r, W_A + un * 32768 + 2 * G * 4096 + tid * 16, o[0]); st16(r, W_A + un * 32768 + (2 * G + 1) * 4096 + tid * 16, o[1]); }
    DRAIN(); __syncthreads();
    if (tid == 0) __hip_atomic_fetch_add((unsigned*)(p.ws + F_CVY + (un >> 3) * 128), 1u, __ATOMIC_RELAXED, __HIP_MEMORY_SCOPE_AGENT);
    return true;
}

__device__ __forceinline__ void proj_block(const Params& p, unsigned char* lds, const int wv, const int h, const int c, const int j) {
    const int w = wv, l = otid() & 63, n = l & 15, g = l >> 4, tm = w >> 1, tn = w & 1;
    const unsigned lb = __builtin_amdgcn_readfirstlane((unsigned)(size_t)lds);
    const v4u sx = mk_srd(p.Aq, S * D * 2), sq = mk_srd(c < 2 ? p.Wq : p.Wk, 1024 * 4096 * 2), sv = mk_srd(p.Wv, 1024 * 4096 * 2), sw = mk_srd(p.ws, W_TOTAL);
    const unsigned lx = lb + (32 * tm + n) * 128 + ((g ^ (n & 7)) << 4), lr = lb + 131072, rr = 8 * g + (n >> 2);
    const int cv = ((c & 1) ^ 1) * 2 + (c >> 1);
    const unsigned aq = (c < 2 ? W_QX : W_KX) + (tm * 32 + h) * 8192 + (2 * (c & 1) + tn) * 1024 + l * 16;
    const int hq = h & 7, tk = ((hq - (hq > 1) - (hq > 5)) * 4 + c) * 4 + w, ts6 = tk % 6, tch = 15 - tk / 6, tx = tk - 6 * GTOUCH;
    const bf16_t* tp = (h & 3) == 1 ? p.gate_w : tx < 0 ? (ts6 % 3 == 0 ? p.Wq : ts6 % 3 == 1 ? p.Wk : p.Wv) + (size_t)(tch * 64 + l) * 4096 + ((h & 24) + 1 + (ts6 / 3) * 4) * 128 + 64
                     : tx < XTOUCH ? p.Aq + (tx * 64 + l) * 64 : p.gate_w;
    unsigned t0, xo, so, cn; unsigned long long ts, ta, tb, tc, td;
    asm volatile(
        "s_mov_b32 %[xo], 0\n\t"
        "s_mov_b32 %[so], 0\n\t"
        "s_mov_b32 %[cn], 1\n\t"
        "global_load_dword v88, v[114:115], off\n\t"
        "s_add_u32 m0, %[ld], 0\n\t"
        "s_add_u32 %[t0], %[xo], 0\n\t"
        "buffer_load_dwordx4 v100, %[sx], %[t0] offen lds\n\t"
        "s_add_u32 m0, %[ld], 1024\n\t"
        "s_add_u32 %[t0], %[xo], 16384\n\t"
        "buffer_load_dwordx4 v100, %[sx], %[t0] offen lds\n\t"
        "s_add_u32 m0, %[ld], 8192\n\t"
        "s_add_u32 %[t0], %[xo], 128\n\t"
        "buffer_load_dwordx4 v100, %[sx], %[t0] offen lds\n\t"
        "s_add_u32 m0, %[ld], 9216\n\t"
        "s_add_u32 %[t0], %[xo], 16512\n\t"
        "buffer_load_dwordx4 v100, %[sx], %[t0] offen lds\n\t"
        "s_add_u32 m0, %[ld], 16384\n\t"
        "s_add_u32 %[t0], %[xo], 256\n\t"
        "buffer_load_dwordx4 v100, %[sx], %[t0] offen lds\n\t"
        "s_add_u32 m0, %[ld], 17408\n\t"
        "s_add_u32 %[t0], %[xo], 16640\n\t"
        "buffer_load_dwordx4 v100, %[sx], %[t0] offen lds\n\t"
        "s_add_u32 m0, %[ld], 24576\n\t"
        "s_add_u32 %[t0], %[xo], 384\n\t"
        "buffer_load_dwordx4 v100, %[sx], %[t0] offen lds\n\t"
        "s_add_u32 m0, %[ld], 25600\n\t"
        "s_add_u32 %[t0], %[xo], 16768\n\t"
        "buffer_load_dwordx4 v100, %[sx], %[t0] offen lds\n\t"
        "s_mov_b32 %[t0], 0\n\t"
        "buffer_load_dwordx4 a[192:195], v101, %[sq], %[t0] offen\n\t"
        "buffer_load_dwordx4 a[200:203], v112, %[sv], %[t0] offen\n\t"
        "s_mov_b32 %[t0], 262144\n\t"
        "buffer_load_dwordx4 a[196:199], v101, %[sq], %[t0] offen\n\t"
        "s_mov_b32 %[t0], 524288\n\t"
        "buffer_load_dwordx4 a[204:207], v101, %[sq], %[t0] offen\n\t"
        "buffer_load_dwordx4 a[212:215], v112, %[sv], %[t0] offen\n\t"
        "s_mov_b32 %[t0], 786432\n\t"
        "buffer_load_dwordx4 a[208:211], v101, %[sq], %[t0] offen\n\t"
        "s_mov_b32 %[t0], 1048576\n\t"
        "buffer_load_dwordx4 a[216:219], v101, %[sq], %[t0] offen\n\t"
        "buffer_load_dwordx4 a[224:227], v112, %[sv], %[t0] offen\n\t"
        "s_mov_b32 %[t0], 1310720\n\t"
        "buffer_load_dwordx4 a[220:223], v101, %[sq], %[t0] offen\n\t"
        "s_mov_b32 %[t0], 1572864\n\t"
        "buffer_load_dwordx4 a[228:231], v101, %[sq], %[t0] offen\n\t"
        "buffer_load_dwordx4 a[236:239], v112, %[sv], %[t0] offen\n\t"
        "s_mov_b32 %[t0], 1835008\n\t"
        "buffer_load_dwordx4 a[232:235], v101, %[sq], %[t0] offen\n\t"
        "s_waitcnt vmcnt(9)\n\t"
        "ds_write_b128 v102, a[192:195] offset:0\n\t"
        "ds_write_b128 v102, a[196:199] offset:4096\n\t"
        "ds_write_b128 v116, a[200:203] offset:8192\n\t"
        "s_waitcnt lgkmcnt(0)\n\t"
        "s_barrier\n\t"
        "ds_read_b64_tr_b16 a[0:1], v103 offset:0\n\t"
        "ds_read_b64_tr_b16 a[2:3], v103 offset:512\n\t"
        "ds_read_b64_tr_b16 a[4:5], v103 offset:8\n\t"
        "ds_read_b64_tr_b16 a[6:7], v103 offset:520\n\t"
        "ds_read_b64_tr_b16 a[8:9], v104 offset:0\n\t"
        "ds_read_b64_tr_b16 a[10:11], v104 offset:256\n\t"
        "s_waitcnt lgkmcnt(5)\n\t"
        "ds_read_b64_tr_b16 a[12:13], v103 offset:4096\n\t"
        "ds_read_b64_tr_b16 a[14:15], v103 offset:4608\n\t"
        "ds_read_b64_tr_b16 a[16:17], v103 offset:4104\n\t"
        "ds_read_b64_tr_b16 a[18:19], v103 offset:4616\n\t"
        "ds_read_b64_tr_b16 a[20:21], v104 offset:2048\n\t"
        "ds_read_b64_tr_b16 a[22:23], v104 offset:2304\n\t"
        "ds_read_b128 v[56:59], v105 offset:0\n\t"
        "ds_read_b128 v[60:63], v105 offset:2048\n\t"
        "ds_read_b128 v[64:67], v106 offset:0\n\t"
        "ds_read_b128 v[68:71], v106 offset:2048\n\t"
        "s_add_u32 m0, %[ld], 32768\n\t"
        "s_add_u32 %[t0], %[xo], 512\n\t"
        "buffer_load_dwordx4 v100, %[sx], %[t0] offen lds\n\t"
        "s_add_u32 m0, %[ld], 33792\n\t"
        "s_add_u32 %[t0], %[xo], 16896\n\t"
        "buffer_load_dwordx4 v100, %[sx], %[t0] offen lds\n\t"
        "s_add_u32 m0, %[ld], 40960\n\t"
        "s_add_u32 %[t0], %[xo], 640\n\t"
        "buffer_load_dwordx4 v100, %[sx], %[t0] offen lds\n\t"
        "s_add_u32 m0, %[ld], 41984\n\t"
        "s_add_u32 %[t0], %[xo], 17024\n\t"
        "buffer_load_dwordx4 v100, %[sx], %[t0] offen lds\n\t"
        "s_add_u32 m0, %[ld], 49152\n\t"
        "s_add_u32 %[t0], %[xo], 768\n\t"
        "buffer_load_dwordx4 v100, %[sx], %[t0] offen lds\n\t"
        "s_add_u32 m0, %[ld], 50176\n\t"
        "s_add_u32 %[t0], %[xo], 17152\n\t"
        "buffer_load_dwordx4 v100, %[sx], %[t0] offen lds\n\t"
        "s_add_u32 m0, %[ld], 57344\n\t"
        "s_add_u32 %[t0], %[xo], 896\n\t"
        "buffer_load_dwordx4 v100, %[sx], %[t0] offen lds\n\t"
        "s_add_u32 m0, %[ld], 58368\n\t"
        "s_add_u32 %[t0], %[xo], 17280\n\t"
        "buffer_load_dwordx4 v100, %[sx], %[t0] offen lds\n\t"
        "s_mov_b32 %[t0], 2097152\n\t"
        "buffer_load_dwordx4 a[240:243], v101, %[sq], %[t0] offen\n\t"
        "buffer_load_dwordx4 a[248:251], v112, %[sv], %[t0] offen\n\t"
        "s_mov_b32 %[t0], 2359296\n\t"
        "buffer_load_dwordx4 a[244:247], v101, %[sq], %[t0] offen\n\t"
        "s_waitcnt vmcnt(17)\n\t"
        "ds_write_b128 v102, a[204:207] offset:16384\n\t"
        "ds_write_b128 v102, a[208:211] offset:20480\n\t"
        "ds_write_b128 v116, a[212:215] offset:24576\n\t"
        "s_waitcnt lgkmcnt(0)\n\t"
        "s_barrier\n\t"
        "v_mfma_f32_16x16x32_bf16 v[8:11], a[0:3], v[56:59], 0\n\t"
        "ds_read_b64_tr_b16 a[24:25], v103 offset:16384\n\t"
        "ds_read_b64_tr_b16 a[26:27], v103 offset:16896\n\t"
        "v_mfma_f32_16x16x32_bf16 v[12:15], a[4:7], v[56:59], 0\n\t"
        "ds_read_b64_tr_b16 a[28:29], v103 offset:16392\n\t"
        "ds_read_b64_tr_b16 a[30:31], v103 offset:16904\n\t"
        "v_mfma_f32_16x16x32_bf16 v[16:19], v[56:59], a[8:11], 0\n\t"
        "ds_read_b64_tr_b16 a[32:33], v104 offset:16384\n\t"
        "ds_read_b64_tr_b16 a[34:35], v104 offset:16640\n\t"
        "v_mfma_f32_16x16x32_bf16 v[20:23], a[0:3], v[60:63], 0\n\t"
        "s_waitcnt lgkmcnt(5)\n\t"
        "ds_read_b64_tr_b16 a[36:37], v103 offset:20480\n\t"
        "v_mfma_f32_16x16x32_bf16 v[24:27], a[4:7], v[60:63], 0\n\t"
        "ds_read_b64_tr_b16 a[38:39], v103 offset:20992\n\t"
        "ds_read_b64_tr_b16 a[40:41], v103 offset:20488\n\t"
        "v_mfma_f32_16x16x32_bf16 v[28:31], v[60:63], a[8:11], 0\n\t"
        "ds_read_b64_tr_b16 a[42:43], v103 offset:21000\n\t"
        "ds_read_b64_tr_b16 a[44:45], v104 offset:18432\n\t"
        "v_mfma_f32_16x16x32_bf16 v[8:11], a[12:15], v[64:67], v[8:11]\n\t"
        "ds_read_b64_tr_b16 a[46:47], v104 offset:18688\n\t"
        "ds_read_b128 v[72:75], v105 offset:8192\n\t"
        "v_mfma_f32_16x16x32_bf16 v[12:15], a[16:19], v[64:67], v[12:15]\n\t"
        "ds_read_b128 v[76:79], v105 offset:10240\n\t"
        "ds_read_b128 v[80:83], v106 offset:8192\n\t"
        "v_mfma_f32_16x16x32_bf16 v[16:19], v[64:67], a[20:23], v[16:19]\n\t"
        "ds_read_b128 v[84:87], v106 offset:10240\n\t"
        "v_mfma_f32_16x16x32_bf16 v[20:23], a[12:15], v[68:71], v[20:23]\n\t"
        "v_mfma_f32_16x16x32_bf16 v[24:27], a[16:19], v[68:71], v[24:27]\n\t"
        "v_mfma_f32_16x16x32_bf16 v[28:31], v[68:71], a[20:23], v[28:31]\n\t"
        "s_mov_b32 %[t0], 2621440\n\t"
        "buffer_load_dwordx4 a[252:255], v101, %[sq], %[t0] offen\n\t"
        "buffer_load_dwordx4 v[132:135], v112, %[sv], %[t0] offen\n\t"
        "s_mov_b32 %[t0], 2883584\n\t"
        "buffer_load_dwordx4 v[128:131], v101, %[sq], %[t0] offen\n\t"
        "s_waitcnt vmcnt(17)\n\t"
        "ds_write_b128 v102, a[216:219] offset:0\n\t"
        "ds_write_b128 v102, a[220:223] offset:4096\n\t"
        "ds_write_b128 v116, a[224:227] offset:8192\n\t"
        "s_waitcnt lgkmcnt(0)\n\t"
        "s_barrier\n\t"
        "v_mfma_f32_16x16x32_bf16 v[8:11], a[24:27], v[72:75], v[8:11]\n\t"
        "ds_read_b64_tr_b16 a[48:49], v103 offset:0\n\t"
        "ds_read_b64_tr_b16 a[50:51], v103 offset:512\n\t"
        "v_mfma_f32_16x16x32_bf16 v[12:15], a[28:31], v[72:75], v[12:15]\n\t"
        "ds_read_b64_tr_b16 a[52:53], v103 offset:8\n\t"
        "ds_read_b64_tr_b16 a[54:55], v103 offset:520\n\t"
        "v_mfma_f32_16x16x32_bf16 v[16:19], v[72:75], a[32:35], v[16:19]\n\t"
        "ds_read_b64_tr_b16 a[56:57], v104 offset:0\n\t"
        "ds_read_b64_tr_b16 a[58:59], v104 offset:256\n\t"
        "v_mfma_f32_16x16x32_bf16 v[20:23], a[24:27], v[76:79], v[20:23]\n\t"
        "s_waitcnt lgkmcnt(5)\n\t"
        "ds_read_b64_tr_b16 a[60:61], v103 offset:4096\n\t"
        "v_mfma_f32_16x16x32_bf16 v[24:27], a[28:31], v[76:79], v[24:27]\n\t"
        "ds_read_b64_tr_b16 a[62:63], v103 offset:4608\n\t"
        "ds_read_b64_tr_b16 a[64:65], v103 offset:4104\n\t"
        "v_mfma_f32_16x16x32_bf16 v[28:31], v[76:79], a[32:35], v[28:31]\n\t"
        "ds_read_b64_tr_b16 a[66:67], v103 offset:4616\n\t"
        "ds_read_b64_tr_b16 a[68:69], v104 offset:2048\n\t"
        "v_mfma_f32_16x16x32_bf16 v[8:11], a[36:39], v[80:83], v[8:11]\n\t"
        "ds_read_b64_tr_b16 a[70:71], v104 offset:2304\n\t"
        "ds_read_b128 v[56:59], v105 offset:16384\n\t"
        "v_mfma_f32_16x16x32_bf16 v[12:15], a[40:43], v[80:83], v[12:15]\n\t"
        "ds_read_b128 v[60:63], v105 offset:18432\n\t"
        "ds_read_b128 v[64:67], v106 offset:16384\n\t"
        "v_mfma_f32_16x16x32_bf16 v[16:19], v[80:83], a[44:47], v[16:19]\n\t"
        "ds_read_b128 v[68:71], v106 offset:18432\n\t"
        "v_mfma_f32_16x16x32_bf16 v[20:23], a[36:39], v[84:87], v[20:23]\n\t"
        "v_mfma_f32_16x16x32_bf16 v[24:27], a[40:43], v[84:87], v[24:27]\n\t"
        "v_mfma_f32_16x16x32_bf16 v[28:31], v[84:87], a[44:47], v[28:31]\n\t"
        "s_mov_b32 %[t0], 3145728\n\t"
        "buffer_load_dwordx4 v[136:139], v101, %[sq], %[t0] offen\n\t"
        "buffer_load_dwordx4 v[144:147], v112, %[sv], %[t0] offen\n\t"
        "s_mov_b32 %[t0], 3407872\n\t"
        "buffer_load_dwordx4 v[140:143], v101, %[sq], %[t0] offen\n\t"
        "s_waitcnt vmcnt(17)\n\t"
        "ds_write_b128 v102, a[228:231] offset:16384\n\t"
        "ds_write_b128 v102, a[232:235] offset:20480\n\t"
        "ds_write_b128 v116, a[236:239] offset:24576\n\t"
        "s_waitcnt lgkmcnt(0)\n\t"
        "s_barrier\n\t"
        "v_mfma_f32_16x16x32_bf16 v[8:11], a[48:51], v[56:59], v[8:11]\n\t"
        "ds_read_b64_tr_b16 a[72:73], v103 offset:16384\n\t"
        "ds_read_b64_tr_b16 a[74:75], v103 offset:16896\n\t"
        "v_mfma_f32_16x16x32_bf16 v[12:15], a[52:55], v[56:59], v[12:15]\n\t"
        "ds_read_b64_tr_b16 a[76:77], v103 offset:16392\n\t"
        "ds_read_b64_tr_b16 a[78:79], v103 offset:16904\n\t"
        "v_mfma_f32_16x16x32_bf16 v[16:19], v[56:59], a[56:59], v[16:19]\n\t"
        "ds_read_b64_tr_b16 a[80:81], v104 offset:16384\n\t"
        "ds_read_b64_tr_b16 a[82:83], v104 offset:16640\n\t"
        "v_mfma_f32_16x16x32_bf16 v[20:23], a[48:51], v[60:63], v[20:23]\n\t"
        "s_waitcnt lgkmcnt(5)\n\t"
        "ds_read_b64_tr_b16 a[84:85], v103 offset:20480\n\t"
        "v_mfma_f32_16x16x32_bf16 v[24:27], a[52:55], v[60:63], v[24:27]\n\t"
        "ds_read_b64_tr_b16 a[86:87], v103 offset:20992\n\t"
        "ds_read_b64_tr_b16 a[88:89], v103 offset:20488\n\t"
        "v_mfma_f32_16x16x32_bf16 v[28:31], v[60:63], a[56:59], v[28:31]\n\t"
        "ds_read_b64_tr_b16 a[90:91], v103 offset:21000\n\t"
        "ds_read_b64_tr_b16 a[92:93], v104 offset:18432\n\t"
        "v_mfma_f32_16x16x32_bf16 v[8:11], a[60:63], v[64:67], v[8:11]\n\t"
        "ds_read_b64_tr_b16 a[94:95], v104 offset:18688\n\t"
        "ds_read_b128 v[72:75], v105 offset:24576\n\t"
        "v_mfma_f32_16x16x32_bf16 v[12:15], a[64:67], v[64:67], v[12:15]\n\t"
        "ds_read_b128 v[76:79], v105 offset:26624\n\t"
        "ds_read_b128 v[80:83], v106 offset:24576\n\t"
        "v_mfma_f32_16x16x32_bf16 v[16:19], v[64:67], a[68:71], v[16:19]\n\t"
        "ds_read_b128 v[84:87], v106 offset:26624\n\t"
        "v_mfma_f32_16x16x32_bf16 v[20:23], a[60:63], v[68:71], v[20:23]\n\t"
        "v_mfma_f32_16x16x32_bf16 v[24:27], a[64:67], v[68:71], v[24:27]\n\t"
        "v_mfma_f32_16x16x32_bf16 v[28:31], v[68:71], a[68:71], v[28:31]\n\t"
        "s_mov_b32 %[t0], 3670016\n\t"
        "buffer_load_dwordx4 v[148:151], v101, %[sq], %[t0] offen\n\t"
        "buffer_load_dwordx4 v[156:159], v112, %[sv], %[t0] offen\n\t"
        "s_mov_b32 %[t0], 3932160\n\t"
        "buffer_load_dwordx4 v[152:155], v101, %[sq], %[t0] offen\n\t"
        "s_waitcnt vmcnt(9)\n\t"
        "ds_write_b128 v102, a[240:243] offset:0\n\t"
        "ds_write_b128 v102, a[244:247] offset:4096\n\t"
        "ds_write_b128 v116, a[248:251] offset:8192\n\t"
        "s_waitcnt lgkmcnt(0)\n\t"
        "s_barrier\n\t"
        "v_mfma_f32_16x16x32_bf16 v[8:11], a[72:75], v[72:75], v[8:11]\n\t"
        "ds_read_b64_tr_b16 a[96:97], v103 offset:0\n\t"
        "ds_read_b64_tr_b16 a[98:99], v103 offset:512\n\t"
        "v_mfma_f32_16x16x32_bf16 v[12:15], a[76:79], v[72:75], v[12:15]\n\t"
        "ds_read_b64_tr_b16 a[100:101], v103 offset:8\n\t"
        "ds_read_b64_tr_b16 a[102:103], v103 offset:520\n\t"
        "v_mfma_f32_16x16x32_bf16 v[16:19], v[72:75], a[80:83], v[16:19]\n\t"
        "ds_read_b64_tr_b16 a[104:105], v104 offset:0\n\t"
        "ds_read_b64_tr_b16 a[106:107], v104 offset:256\n\t"
        "v_mfma_f32_16x16x32_bf16 v[20:23], a[72:75], v[76:79], v[20:23]\n\t"
        "s_waitcnt lgkmcnt(5)\n\t"
        "ds_read_b64_tr_b16 a[108:109], v103 offset:4096\n\t"
        "v_mfma_f32_16x16x32_bf16 v[24:27], a[76:79], v[76:79], v[24:27]\n\t"
        "ds_read_b64_tr_b16 a[110:111], v103 offset:4608\n\t"
        "ds_read_b64_tr_b16 a[112:113], v103 offset:4104\n\t"
        "v_mfma_f32_16x16x32_bf16 v[28:31], v[76:79], a[80:83], v[28:31]\n\t"
        "ds_read_b64_tr_b16 a[114:115], v103 offset:4616\n\t"
        "ds_read_b64_tr_b16 a[116:117], v104 offset:2048\n\t"
        "v_mfma_f32_16x16x32_bf16 v[8:11], a[84:87], v[80:83], v[8:11]\n\t"
        "ds_read_b64_tr_b16 a[118:119], v104 offset:2304\n\t"
        "ds_read_b128 v[56:59], v105 offset:32768\n\t"
        "v_mfma_f32_16x16x32_bf16 v[12:15], a[88:91], v[80:83], v[12:15]\n\t"
        "ds_read_b128 v[60:63], v105 offset:34816\n\t"
        "ds_read_b128 v[64:67], v106 offset:32768\n\t"
        "v_mfma_f32_16x16x32_bf16 v[16:19], v[80:83], a[92:95], v[16:19]\n\t"
        "ds_read_b128 v[68:71], v106 offset:34816\n\t"
        "v_mfma_f32_16x16x32_bf16 v[20:23], a[84:87], v[84:87], v[20:23]\n\t"
        "v_mfma_f32_16x16x32_bf16 v[24:27], a[88:91], v[84:87], v[24:27]\n\t"
        "v_mfma_f32_16x16x32_bf16 v[28:31], v[84:87], a[92:95], v[28:31]\n\t"
        "s_add_u32 m0, %[ld], 65536\n\t"
        "s_add_u32 %[t0], %[xo], 1024\n\t"
        "buffer_load_dwordx4 v100, %[sx], %[t0] offen lds\n\t"
        "s_add_u32 m0, %[ld], 66560\n\t"
        "s_add_u32 %[t0], %[xo], 17408\n\t"
        "buffer_load_dwordx4 v100, %[sx], %[t0] offen lds\n\t"
        "s_add_u32 m0, %[ld], 73728\n\t"
        "s_add_u32 %[t0], %[xo], 1152\n\t"
        "buffer_load_dwordx4 v100, %[sx], %[t0] offen lds\n\t"
        "s_add_u32 m0, %[ld], 74752\n\t"
        "s_add_u32 %[t0], %[xo], 17536\n\t"
        "buffer_load_dwordx4 v100, %[sx], %[t0] offen lds\n\t"
        "s_add_u32 m0, %[ld], 81920\n\t"
        "s_add_u32 %[t0], %[xo], 1280\n\t"
        "buffer_load_dwordx4 v100, %[sx], %[t0] offen lds\n\t"
        "s_add_u32 m0, %[ld], 82944\n\t"
        "s_add_u32 %[t0], %[xo], 17664\n\t"
        "buffer_load_dwordx4 v100, %[sx], %[t0] offen lds\n\t"
        "s_add_u32 m0, %[ld], 90112\n\t"
        "s_add_u32 %[t0], %[xo], 1408\n\t"
        "buffer_load_dwordx4 v100, %[sx], %[t0] offen lds\n\t"
        "s_add_u32 m0, %[ld], 91136\n\t"
        "s_add_u32 %[t0], %[xo], 17792\n\t"
        "buffer_load_dwordx4 v100, %[sx], %[t0] offen lds\n\t"
        "s_mov_b32 %[t0], 4194304\n\t"
        "buffer_load_dwordx4 v[160:163], v101, %[sq], %[t0] offen\n\t"
        "buffer_load_dwordx4 v[168:171], v112, %[sv], %[t0] offen\n\t"
        "s_mov_b32 %[t0], 4456448\n\t"
        "buffer_load_dwordx4 v[164:167], v101, %[sq], %[t0] offen\n\t"
        "s_waitcnt vmcnt(17)\n\t"
        "ds_write_b128 v102, a[252:255] offset:16384\n\t"
        "ds_write_b128 v102, v[128:131] offset:20480\n\t"
        "ds_write_b128 v116, v[132:135] offset:24576\n\t"
        "s_waitcnt lgkmcnt(0)\n\t"
        "s_barrier\n\t"
        "v_mfma_f32_16x16x32_bf16 v[8:11], a[96:99], v[56:59], v[8:11]\n\t"
        "ds_read_b64_tr_b16 a[120:121], v103 offset:16384\n\t"
        "ds_read_b64_tr_b16 a[122:123], v103 offset:16896\n\t"
        "v_mfma_f32_16x16x32_bf16 v[12:15], a[100:103], v[56:59], v[12:15]\n\t"
        "ds_read_b64_tr_b16 a[124:125], v103 offset:16392\n\t"
        "ds_read_b64_tr_b16 a[126:127], v103 offset:16904\n\t"
        "v_mfma_f32_16x16x32_bf16 v[16:19], v[56:59], a[104:107], v[16:19]\n\t"
        "ds_read_b64_tr_b16 a[128:129], v104 offset:16384\n\t"
        "ds_read_b64_tr_b16 a[130:131], v104 offset:16640\n\t"
        "v_mfma_f32_16x16x32_bf16 v[20:23], a[96:99], v[60:63], v[20:23]\n\t"
        "s_waitcnt lgkmcnt(5)\n\t"
        "ds_read_b64_tr_b16 a[132:133], v103 offset:20480\n\t"
        "v_mfma_f32_16x16x32_bf16 v[24:27], a[100:103], v[60:63], v[24:27]\n\t"
        "ds_read_b64_tr_b16 a[134:135], v103 offset:20992\n\t"
        "ds_read_b64_tr_b16 a[136:137], v103 offset:20488\n\t"
        "v_mfma_f32_16x16x32_bf16 v[28:31], v[60:63], a[104:107], v[28:31]\n\t"
        "ds_read_b64_tr_b16 a[138:139], v103 offset:21000\n\t"
        "ds_read_b64_tr_b16 a[140:141], v104 offset:18432\n\t"
        "v_mfma_f32_16x16x32_bf16 v[8:11], a[108:111], v[64:67], v[8:11]\n\t"
        "ds_read_b64_tr_b16 a[142:143], v104 offset:18688\n\t"
        "ds_read_b128 v[72:75], v105 offset:40960\n\t"
        "v_mfma_f32_16x16x32_bf16 v[12:15], a[112:115], v[64:67], v[12:15]\n\t"
        "ds_read_b128 v[76:79], v105 offset:43008\n\t"
        "ds_read_b128 v[80:83], v106 offset:40960\n\t"
        "v_mfma_f32_16x16x32_bf16 v[16:19], v[64:67], a[116:119], v[16:19]\n\t"
        "ds_read_b128 v[84:87], v106 offset:43008\n\t"
        "v_mfma_f32_16x16x32_bf16 v[20:23], a[108:111], v[68:71], v[20:23]\n\t"
        "v_mfma_f32_16x16x32_bf16 v[24:27], a[112:115], v[68:71], v[24:27]\n\t"
        "v_mfma_f32_16x16x32_bf16 v[28:31], v[68:71], a[116:119], v[28:31]\n\t"
        "s_mov_b32 %[t0], 4718592\n\t"
        "buffer_load_dwordx4 v[172:175], v101, %[sq], %[t0] offen\n\t"
        "buffer_load_dwordx4 v[180:183], v112, %[sv], %[t0] offen\n\t"
        "s_mov_b32 %[t0], 4980736\n\t"
        "buffer_load_dwordx4 v[176:179], v101, %[sq], %[t0] offen\n\t"
        "s_waitcnt vmcnt(17)\n\t"
        "ds_write_b128 v102, v[136:139] offset:0\n\t"
        "ds_write_b128 v102, v[140:143] offset:4096\n\t"
        "ds_write_b128 v116, v[144:147] offset:8192\n\t"
        "s_waitcnt lgkmcnt(0)\n\t"
        "s_barrier\n\t"
        "v_mfma_f32_16x16x32_bf16 v[8:11], a[120:123], v[72:75], v[8:11]\n\t"
        "ds_read_b64_tr_b16 a[144:145], v103 offset:0\n\t"
        "ds_read_b64_tr_b16 a[146:147], v103 offset:512\n\t"
        "v_mfma_f32_16x16x32_bf16 v[12:15], a[124:127], v[72:75], v[12:15]\n\t"
        "ds_read_b64_tr_b16 a[148:149], v103 offset:8\n\t"
        "ds_read_b64_tr_b16 a[150:151], v103 offset:520\n\t"
        "v_mfma_f32_16x16x32_bf16 v[16:19], v[72:75], a[128:131], v[16:19]\n\t"
        "ds_read_b64_tr_b16 a[152:153], v104 offset:0\n\t"
        "ds_read_b64_tr_b16 a[154:155], v104 offset:256\n\t"
        "v_mfma_f32_16x16x32_bf16 v[20:23], a[120:123], v[76:79], v[20:23]\n\t"
        "s_waitcnt lgkmcnt(5)\n\t"
        "ds_read_b64_tr_b16 a[156:157], v103 offset:4096\n\t"
        "v_mfma_f32_16x16x32_bf16 v[24:27], a[124:127], v[76:79], v[24:27]\n\t"
        "ds_read_b64_tr_b16 a[158:159], v103 offset:4608\n\t"
        "ds_read_b64_tr_b16 a[160:161], v103 offset:4104\n\t"
        "v_mfma_f32_16x16x32_bf16 v[28:31], v[76:79], a[128:131], v[28:31]\n\t"
        "ds_read_b64_tr_b16 a[162:163], v103 offset:4616\n\t"
        "ds_read_b64_tr_b16 a[164:165], v104 offset:2048\n\t"
        "v_mfma_f32_16x16x32_bf16 v[8:11], a[132:135], v[80:83], v[8:11]\n\t"
        "ds_read_b64_tr_b16 a[166:167], v104 offset:2304\n\t"
        "ds_read_b128 v[56:59], v105 offset:49152\n\t"
        "v_mfma_f32_16x16x32_bf16 v[12:15], a[136:139], v[80:83], v[12:15]\n\t"
        "ds_read_b128 v[60:63], v105 offset:51200\n\t"
        "ds_read_b128 v[64:67], v106 offset:49152\n\t"
        "v_mfma_f32_16x16x32_bf16 v[16:19], v[80:83], a[140:143], v[16:19]\n\t"
        "ds_read_b128 v[68:71], v106 offset:51200\n\t"
        "v_mfma_f32_16x16x32_bf16 v[20:23], a[132:135], v[84:87], v[20:23]\n\t"
        "v_mfma_f32_16x16x32_bf16 v[24:27], a[136:139], v[84:87], v[24:27]\n\t"
        "v_mfma_f32_16x16x32_bf16 v[28:31], v[84:87], a[140:143], v[28:31]\n\t"
        "s_mov_b32 %[t0], 5242880\n\t"
        "buffer_load_dwordx4 v[184:187], v101, %[sq], %[t0] offen\n\t"
        "buffer_load_dwordx4 v[192:195], v112, %[sv], %[t0] offen\n\t"
        "s_mov_b32 %[t0], 5505024\n\t"
        "buffer_load_dwordx4 v[188:191], v101, %[sq], %[t0] offen\n\t"
        "s_waitcnt vmcnt(17)\n\t"
        "ds_write_b128 v102, v[148:151] offset:16384\n\t"
        "ds_write_b128 v102, v[152:155] offset:20480\n\t"
        "ds_write_b128 v116, v[156:159] offset:24576\n\t"
        "s_waitcnt lgkmcnt(0)\n\t"
        "s_barrier\n\t"
        "v_mfma_f32_16x16x32_bf16 v[8:11], a[144:147], v[56:59], v[8:11]\n\t"
        "ds_read_b64_tr_b16 a[168:169], v103 offset:16384\n\t"
        "ds_read_b64_tr_b16 a[170:171], v103 offset:16896\n\t"
        "v_mfma_f32_16x16x32_bf16 v[12:15], a[148:151], v[56:59], v[12:15]\n\t"
        "ds_read_b64_tr_b16 a[172:173], v103 offset:16392\n\t"
        "ds_read_b64_tr_b16 a[174:175], v103 offset:16904\n\t"
        "v_mfma_f32_16x16x32_bf16 v[16:19], v[56:59], a[152:155], v[16:19]\n\t"
        "ds_read_b64_tr_b16 a[176:177], v104 offset:16384\n\t"
        "ds_read_b64_tr_b16 a[178:179], v104 offset:16640\n\t"
        "v_mfma_f32_16x16x32_bf16 v[20:23], a[144:147], v[60:63], v[20:23]\n\t"
        "s_waitcnt lgkmcnt(5)\n\t"
        "ds_read_b64_tr_b16 a[180:181], v103 offset:20480\n\t"
        "v_mfma_f32_16x16x32_bf16 v[24:27], a[148:151], v[60:63], v[24:27]\n\t"
        "ds_read_b64_tr_b16 a[182:183], v103 offset:20992\n\t"
        "ds_read_b64_tr_b16 a[184:185], v103 offset:20488\n\t"
        "v_mfma_f32_16x16x32_bf16 v[28:31], v[60:63], a[152:155], v[28:31]\n\t"
        "ds_read_b64_tr_b16 a[186:187], v103 offset:21000\n\t"
        "ds_read_b64_tr_b16 a[188:189], v104 offset:18432\n\t"
        "v_mfma_f32_16x16x32_bf16 v[8:11], a[156:159], v[64:67], v[8:11]\n\t"
        "ds_read_b64_tr_b16 a[190:191], v104 offset:18688\n\t"
        "ds_read_b128 v[72:75], v105 offset:57344\n\t"
        "v_mfma_f32_16x16x32_bf16 v[12:15], a[160:163], v[64:67], v[12:15]\n\t"
        "ds_read_b128 v[76:79], v105 offset:59392\n\t"
        "ds_read_b128 v[80:83], v106 offset:57344\n\t"
        "v_mfma_f32_16x16x32_bf16 v[16:19], v[64:67], a[164:167], v[16:19]\n\t"
        "ds_read_b128 v[84:87], v106 offset:59392\n\t"
        "v_mfma_f32_16x16x32_bf16 v[20:23], a[156:159], v[68:71], v[20:23]\n\t"
        "v_mfma_f32_16x16x32_bf16 v[24:27], a[160:163], v[68:71], v[24:27]\n\t"
        "v_mfma_f32_16x16x32_bf16 v[28:31], v[68:71], a[164:167], v[28:31]\n\t"
        "s_mov_b32 %[t0], 5767168\n\t"
        "buffer_load_dwordx4 v[196:199], v101, %[sq], %[t0] offen\n\t"
        "buffer_load_dwordx4 v[204:207], v112, %[sv], %[t0] offen\n\t"
        "s_mov_b32 %[t0], 6029312\n\t"
        "buffer_load_dwordx4 v[200:203], v101, %[sq], %[t0] offen\n\t"
        "s_waitcnt vmcnt(9)\n\t"
        "ds_write_b128 v102, v[160:163] offset:0\n\t"
        "ds_write_b128 v102, v[164:167] offset:4096\n\t"
        "ds_write_b128 v116, v[168:171] offset:8192\n\t"
        "s_waitcnt lgkmcnt(0)\n\t"
        "s_barrier\n\t"
        "v_mfma_f32_16x16x32_bf16 v[8:11], a[168:171], v[72:75], v[8:11]\n\t"
        "ds_read_b64_tr_b16 a[192:193], v103 offset:0\n\t"
        "ds_read_b64_tr_b16 a[194:195], v103 offset:512\n\t"
        "v_mfma_f32_16x16x32_bf16 v[12:15], a[172:175], v[72:75], v[12:15]\n\t"
        "ds_read_b64_tr_b16 a[196:197], v103 offset:8\n\t"
        "ds_read_b64_tr_b16 a[198:199], v103 offset:520\n\t"
        "v_mfma_f32_16x16x32_bf16 v[16:19], v[72:75], a[176:179], v[16:19]\n\t"
        "ds_read_b64_tr_b16 a[200:201], v104 offset:0\n\t"
        "ds_read_b64_tr_b16 a[202:203], v104 offset:256\n\t"
        "v_mfma_f32_16x16x32_bf16 v[20:23], a[168:171], v[76:79], v[20:23]\n\t"
        "s_waitcnt lgkmcnt(5)\n\t"
        "ds_read_b64_tr_b16 a[204:205], v103 offset:4096\n\t"
        "v_mfma_f32_16x16x32_bf16 v[24:27], a[172:175], v[76:79], v[24:27]\n\t"
        "ds_read_b64_tr_b16 a[206:207], v103 offset:4608\n\t"
        "ds_read_b64_tr_b16 a[208:209], v103 offset:4104\n\t"
        "v_mfma_f32_16x16x32_bf16 v[28:31], v[76:79], a[176:179], v[28:31]\n\t"
        "ds_read_b64_tr_b16 a[210:211], v103 offset:4616\n\t"
        "ds_read_b64_tr_b16 a[212:213], v104 offset:2048\n\t"
        "v_mfma_f32_16x16x32_bf16 v[8:11], a[180:183], v[80:83], v[8:11]\n\t"
        "ds_read_b64_tr_b16 a[214:215], v104 offset:2304\n\t"
        "ds_read_b128 v[56:59], v107 offset:0\n\t"
        "v_mfma_f32_16x16x32_bf16 v[12:15], a[184:187], v[80:83], v[12:15]\n\t"
        "ds_read_b128 v[60:63], v107 offset:2048\n\t"
        "ds_read_b128 v[64:67], v108 offset:0\n\t"
        "v_mfma_f32_16x16x32_bf16 v[16:19], v[80:83], a[188:191], v[16:19]\n\t"
        "ds_read_b128 v[68:71], v108 offset:2048\n\t"
        "v_mfma_f32_16x16x32_bf16 v[20:23], a[180:183], v[84:87], v[20:23]\n\t"
        "v_mfma_f32_16x16x32_bf16 v[24:27], a[184:187], v[84:87], v[24:27]\n\t"
        "v_mfma_f32_16x16x32_bf16 v[28:31], v[84:87], a[188:191], v[28:31]\n\t"
        "s_add_u32 m0, %[ld], 98304\n\t"
        "s_add_u32 %[t0], %[xo], 1536\n\t"
        "buffer_load_dwordx4 v100, %[sx], %[t0] offen lds\n\t"
        "s_add_u32 m0, %[ld], 99328\n\t"
        "s_add_u32 %[t0], %[xo], 17920\n\t"
        "buffer_load_dwordx4 v100, %[sx], %[t0] offen lds\n\t"
        "s_add_u32 m0, %[ld], 106496\n\t"
        "s_add_u32 %[t0], %[xo], 1664\n\t"
        "buffer_load_dwordx4 v100, %[sx], %[t0] offen lds\n\t"
        "s_add_u32 m0, %[ld], 107520\n\t"
        "s_add_u32 %[t0], %[xo], 18048\n\t"
        "buffer_load_dwordx4 v100, %[sx], %[t0] offen lds\n\t"
        "s_add_u32 m0, %[ld], 114688\n\t"
        "s_add_u32 %[t0], %[xo], 1792\n\t"
        "buffer_load_dwordx4 v100, %[sx], %[t0] offen lds\n\t"
        "s_add_u32 m0, %[ld], 115712\n\t"
        "s_add_u32 %[t0], %[xo], 18176\n\t"
        "buffer_load_dwordx4 v100, %[sx], %[t0] offen lds\n\t"
        "s_add_u32 m0, %[ld], 122880\n\t"
        "s_add_u32 %[t0], %[xo], 1920\n\t"
        "buffer_load_dwordx4 v100, %[sx], %[t0] offen lds\n\t"
        "s_add_u32 m0, %[ld], 123904\n\t"
        "s_add_u32 %[t0], %[xo], 18304\n\t"
        "buffer_load_dwordx4 v100, %[sx], %[t0] offen lds\n\t"
        "s_mov_b32 %[t0], 6291456\n\t"
        "buffer_load_dwordx4 v[208:211], v101, %[sq], %[t0] offen\n\t"
        "buffer_load_dwordx4 v[216:219], v112, %[sv], %[t0] offen\n\t"
        "s_mov_b32 %[t0], 6553600\n\t"
        "buffer_load_dwordx4 v[212:215], v101, %[sq], %[t0] offen\n\t"
        "s_waitcnt vmcnt(17)\n\t"
        "ds_write_b128 v102, v[172:175] offset:16384\n\t"
        "ds_write_b128 v102, v[176:179] offset:20480\n\t"
        "ds_write_b128 v116, v[180:183] offset:24576\n\t"
        "s_waitcnt lgkmcnt(0)\n\t"
        "s_barrier\n\t"
        "v_mfma_f32_16x16x32_bf16 v[8:11], a[192:195], v[56:59], v[8:11]\n\t"
        "ds_read_b64_tr_b16 a[216:217], v103 offset:16384\n\t"
        "ds_read_b64_tr_b16 a[218:219], v103 offset:16896\n\t"
        "v_mfma_f32_16x16x32_bf16 v[12:15], a[196:199], v[56:59], v[12:15]\n\t"
        "ds_read_b64_tr_b16 a[220:221], v103 offset:16392\n\t"
        "ds_read_b64_tr_b16 a[222:223], v103 offset:16904\n\t"
        "v_mfma_f32_16x16x32_bf16 v[16:19], v[56:59], a[200:203], v[16:19]\n\t"
        "ds_read_b64_tr_b16 a[224:225], v104 offset:16384\n\t"
        "ds_read_b64_tr_b16 a[226:227], v104 offset:16640\n\t"
        "v_mfma_f32_16x16x32_bf16 v[20:23], a[192:195], v[60:63], v[20:23]\n\t"
        "s_waitcnt lgkmcnt(5)\n\t"
        "ds_read_b64_tr_b16 a[228:229], v103 offset:20480\n\t"
        "v_mfma_f32_16x16x32_bf16 v[24:27], a[196:199], v[60:63], v[24:27]\n\t"
        "ds_read_b64_tr_b16 a[230:231], v103 offset:20992\n\t"
        "ds_read_b64_tr_b16 a[232:233], v103 offset:20488\n\t"
        "v_mfma_f32_16x16x32_bf16 v[28:31], v[60:63], a[200:203], v[28:31]\n\t"
        "ds_read_b64_tr_b16 a[234:235], v103 offset:21000\n\t"
        "ds_read_b64_tr_b16 a[236:237], v104 offset:18432\n\t"
        "v_mfma_f32_16x16x32_bf16 v[8:11], a[204:207], v[64:67], v[8:11]\n\t"
        "ds_read_b64_tr_b16 a[238:239], v104 offset:18688\n\t"
        "ds_read_b128 v[72:75], v107 offset:8192\n\t"
        "v_mfma_f32_16x16x32_bf16 v[12:15], a[208:211], v[64:67], v[12:15]\n\t"
        "ds_read_b128 v[76:79], v107 offset:10240\n\t"
        "ds_read_b128 v[80:83], v108 offset:8192\n\t"
        "v_mfma_f32_16x16x32_bf16 v[16:19], v[64:67], a[212:215], v[16:19]\n\t"
        "ds_read_b128 v[84:87], v108 offset:10240\n\t"
        "v_mfma_f32_16x16x32_bf16 v[20:23], a[204:207], v[68:71], v[20:23]\n\t"
        "v_mfma_f32_16x16x32_bf16 v[24:27], a[208:211], v[68:71], v[24:27]\n\t"
        "v_mfma_f32_16x16x32_bf16 v[28:31], v[68:71], a[212:215], v[28:31]\n\t"
        "s_mov_b32 %[t0], 6815744\n\t"
        "buffer_load_dwordx4 v[220:223], v101, %[sq], %[t0] offen\n\t"
        "buffer_load_dwordx4 v[228:231], v112, %[sv], %[t0] offen\n\t"
        "s_mov_b32 %[t0], 7077888\n\t"
        "buffer_load_dwordx4 v[224:227], v101, %[sq], %[t0] offen\n\t"
        "s_waitcnt vmcnt(17)\n\t"
        "ds_write_b128 v102, v[184:187] offset:0\n\t"
        "ds_write_b128 v102, v[188:191] offset:4096\n\t"
        "ds_write_b128 v116, v[192:195] offset:8192\n\t"
        "s_waitcnt lgkmcnt(0)\n\t"
        "s_barrier\n\t"
        "v_mfma_f32_16x16x32_bf16 v[8:11], a[216:219], v[72:75], v[8:11]\n\t"
        "ds_read_b64_tr_b16 a[240:241], v103 offset:0\n\t"
        "ds_read_b64_tr_b16 a[242:243], v103 offset:512\n\t"
        "v_mfma_f32_16x16x32_bf16 v[12:15], a[220:223], v[72:75], v[12:15]\n\t"
        "ds_read_b64_tr_b16 a[244:245], v103 offset:8\n\t"
        "ds_read_b64_tr_b16 a[246:247], v103 offset:520\n\t"
        "v_mfma_f32_16x16x32_bf16 v[16:19], v[72:75], a[224:227], v[16:19]\n\t"
        "ds_read_b64_tr_b16 a[248:249], v104 offset:0\n\t"
        "ds_read_b64_tr_b16 a[250:251], v104 offset:256\n\t"
        "v_mfma_f32_16x16x32_bf16 v[20:23], a[216:219], v[76:79], v[20:23]\n\t"
        "s_waitcnt lgkmcnt(5)\n\t"
        "ds_read_b64_tr_b16 a[252:253], v103 offset:4096\n\t"
        "v_mfma_f32_16x16x32_bf16 v[24:27], a[220:223], v[76:79], v[24:27]\n\t"
        "ds_read_b64_tr_b16 a[254:255], v103 offset:4608\n\t"
        "ds_read_b64_tr_b16 v[128:129], v103 offset:4104\n\t"
        "v_mfma_f32_16x16x32_bf16 v[28:31], v[76:79], a[224:227], v[28:31]\n\t"
        "ds_read_b64_tr_b16 v[130:131], v103 offset:4616\n\t"
        "ds_read_b64_tr_b16 v[132:133], v104 offset:2048\n\t"
        "v_mfma_f32_16x16x32_bf16 v[8:11], a[228:231], v[80:83], v[8:11]\n\t"
        "ds_read_b64_tr_b16 v[134:135], v104 offset:2304\n\t"
        "ds_read_b128 v[56:59], v107 offset:16384\n\t"
        "v_mfma_f32_16x16x32_bf16 v[12:15], a[232:235], v[80:83], v[12:15]\n\t"
        "ds_read_b128 v[60:63], v107 offset:18432\n\t"
        "ds_read_b128 v[64:67], v108 offset:16384\n\t"
        "v_mfma_f32_16x16x32_bf16 v[16:19], v[80:83], a[236:239], v[16:19]\n\t"
        "ds_read_b128 v[68:71], v108 offset:18432\n\t"
        "v_mfma_f32_16x16x32_bf16 v[20:23], a[228:231], v[84:87], v[20:23]\n\t"
        "v_mfma_f32_16x16x32_bf16 v[24:27], a[232:235], v[84:87], v[24:27]\n\t"
        "v_mfma_f32_16x16x32_bf16 v[28:31], v[84:87], a[236:239], v[28:31]\n\t"
        "s_mov_b32 %[t0], 7340032\n\t"
        "buffer_load_dwordx4 v[232:235], v101, %[sq], %[t0] offen\n\t"
        "buffer_load_dwordx4 v[240:243], v112, %[sv], %[t0] offen\n\t"
        "s_mov_b32 %[t0], 7602176\n\t"
        "buffer_load_dwordx4 v[236:239], v101, %[sq], %[t0] offen\n\t"
        "s_waitcnt vmcnt(17)\n\t"
        "ds_write_b128 v102, v[196:199] offset:16384\n\t"
        "ds_write_b128 v102, v[200:203] offset:20480\n\t"
        "ds_write_b128 v116, v[204:207] offset:24576\n\t"
        "s_waitcnt lgkmcnt(0)\n\t"
        "s_barrier\n\t"
        "v_mfma_f32_16x16x32_bf16 v[8:11], a[240:243], v[56:59], v[8:11]\n\t"
        "ds_read_b64_tr_b16 v[136:137], v103 offset:16384\n\t"
        "ds_read_b64_tr_b16 v[138:139], v103 offset:16896\n\t"
        "v_mfma_f32_16x16x32_bf16 v[12:15], a[244:247], v[56:59], v[12:15]\n\t"
        "ds_read_b64_tr_b16 v[140:141], v103 offset:16392\n\t"
        "ds_read_b64_tr_b16 v[142:143], v103 offset:16904\n\t"
        "v_mfma_f32_16x16x32_bf16 v[16:19], v[56:59], a[248:251], v[16:19]\n\t"
        "ds_read_b64_tr_b16 v[144:145], v104 offset:16384\n\t"
        "ds_read_b64_tr_b16 v[146:147], v104 offset:16640\n\t"
        "v_mfma_f32_16x16x32_bf16 v[20:23], a[240:243], v[60:63], v[20:23]\n\t"
        "s_waitcnt lgkmcnt(5)\n\t"
        "ds_read_b64_tr_b16 v[148:149], v103 offset:20480\n\t"
        "v_mfma_f32_16x16x32_bf16 v[24:27], a[244:247], v[60:63], v[24:27]\n\t"
        "ds_read_b64_tr_b16 v[150:151], v103 offset:20992\n\t"
        "ds_read_b64_tr_b16 v[152:153], v103 offset:20488\n\t"
        "v_mfma_f32_16x16x32_bf16 v[28:31], v[60:63], a[248:251], v[28:31]\n\t"
        "ds_read_b64_tr_b16 v[154:155], v103 offset:21000\n\t"
        "ds_read_b64_tr_b16 v[156:157], v104 offset:18432\n\t"
        "v_mfma_f32_16x16x32_bf16 v[8:11], a[252:255], v[64:67], v[8:11]\n\t"
        "ds_read_b64_tr_b16 v[158:159], v104 offset:18688\n\t"
        "ds_read_b128 v[72:75], v107 offset:24576\n\t"
        "v_mfma_f32_16x16x32_bf16 v[12:15], v[128:131], v[64:67], v[12:15]\n\t"
        "ds_read_b128 v[76:79], v107 offset:26624\n\t"
        "ds_read_b128 v[80:83], v108 offset:24576\n\t"
        "v_mfma_f32_16x16x32_bf16 v[16:19], v[64:67], v[132:135], v[16:19]\n\t"
        "ds_read_b128 v[84:87], v108 offset:26624\n\t"
        "v_mfma_f32_16x16x32_bf16 v[20:23], a[252:255], v[68:71], v[20:23]\n\t"
        "v_mfma_f32_16x16x32_bf16 v[24:27], v[128:131], v[68:71], v[24:27]\n\t"
        "v_mfma_f32_16x16x32_bf16 v[28:31], v[68:71], v[132:135], v[28:31]\n\t"
        "s_mov_b32 %[t0], 7864320\n\t"
        "buffer_load_dwordx4 v[244:247], v101, %[sq], %[t0] offen\n\t"
        "buffer_load_dwordx4 v[252:255], v112, %[sv], %[t0] offen\n\t"
        "s_mov_b32 %[t0], 8126464\n\t"
        "buffer_load_dwordx4 v[248:251], v101, %[sq], %[t0] offen\n\t"
        "s_waitcnt vmcnt(9)\n\t"
        "ds_write_b128 v102, v[208:211] offset:0\n\t"
        "ds_write_b128 v102, v[212:215] offset:4096\n\t"
        "ds_write_b128 v116, v[216:219] offset:8192\n\t"
        "s_waitcnt lgkmcnt(0)\n\t"
        "s_barrier\n\t"
        "v_mfma_f32_16x16x32_bf16 v[8:11], v[136:139], v[72:75], v[8:11]\n\t"
        "ds_read_b64_tr_b16 v[160:161], v103 offset:0\n\t"
        "ds_read_b64_tr_b16 v[162:163], v103 offset:512\n\t"
        "v_mfma_f32_16x16x32_bf16 v[12:15], v[140:143], v[72:75], v[12:15]\n\t"
        "ds_read_b64_tr_b16 v[164:165], v103 offset:8\n\t"
        "ds_read_b64_tr_b16 v[166:167], v103 offset:520\n\t"
        "v_mfma_f32_16x16x32_bf16 v[16:19], v[72:75], v[144:147], v[16:19]\n\t"
        "ds_read_b64_tr_b16 v[168:169], v104 offset:0\n\t"
        "ds_read_b64_tr_b16 v[170:171], v104 offset:256\n\t"
        "v_mfma_f32_16x16x32_bf16 v[20:23], v[136:139], v[76:79], v[20:23]\n\t"
        "s_waitcnt lgkmcnt(5)\n\t"
        "ds_read_b64_tr_b16 v[172:173], v103 offset:4096\n\t"
        "v_mfma_f32_16x16x32_bf16 v[24:27], v[140:143], v[76:79], v[24:27]\n\t"
        "ds_read_b64_tr_b16 v[174:175], v103 offset:4608\n\t"
        "ds_read_b64_tr_b16 v[176:177], v103 offset:4104\n\t"
        "v_mfma_f32_16x16x32_bf16 v[28:31], v[76:79], v[144:147], v[28:31]\n\t"
        "ds_read_b64_tr_b16 v[178:179], v103 offset:4616\n\t"
        "ds_read_b64_tr_b16 v[180:181], v104 offset:2048\n\t"
        "v_mfma_f32_16x16x32_bf16 v[8:11], v[148:151], v[80:83], v[8:11]\n\t"
        "ds_read_b64_tr_b16 v[182:183], v104 offset:2304\n\t"
        "ds_read_b128 v[56:59], v107 offset:32768\n\t"
        "v_mfma_f32_16x16x32_bf16 v[12:15], v[152:155], v[80:83], v[12:15]\n\t"
        "ds_read_b128 v[60:63], v107 offset:34816\n\t"
        "ds_read_b128 v[64:67], v108 offset:32768\n\t"
        "v_mfma_f32_16x16x32_bf16 v[16:19], v[80:83], v[156:159], v[16:19]\n\t"
        "ds_read_b128 v[68:71], v108 offset:34816\n\t"
        "v_mfma_f32_16x16x32_bf16 v[20:23], v[148:151], v[84:87], v[20:23]\n\t"
        "v_mfma_f32_16x16x32_bf16 v[24:27], v[152:155], v[84:87], v[24:27]\n\t"
        "v_mfma_f32_16x16x32_bf16 v[28:31], v[84:87], v[156:159], v[28:31]\n\t"
        "s_mov_b32 %[xo], 0x20000\n\t"
        "s_add_u32 m0, %[ld], 0\n\t"
        "s_add_u32 %[t0], %[xo], 0\n\t"
        "buffer_load_dwordx4 v100, %[sx], %[t0] offen lds\n\t"
        "s_add_u32 m0, %[ld], 1024\n\t"
        "s_add_u32 %[t0], %[xo], 16384\n\t"
        "buffer_load_dwordx4 v100, %[sx], %[t0] offen lds\n\t"
        "s_add_u32 m0, %[ld], 8192\n\t"
        "s_add_u32 %[t0], %[xo], 128\n\t"
        "buffer_load_dwordx4 v100, %[sx], %[t0] offen lds\n\t"
        "s_add_u32 m0, %[ld], 9216\n\t"
        "s_add_u32 %[t0], %[xo], 16512\n\t"
        "buffer_load_dwordx4 v100, %[sx], %[t0] offen lds\n\t"
        "s_add_u32 m0, %[ld], 16384\n\t"
        "s_add_u32 %[t0], %[xo], 256\n\t"
        "buffer_load_dwordx4 v100, %[sx], %[t0] offen lds\n\t"
        "s_add_u32 m0, %[ld], 17408\n\t"
        "s_add_u32 %[t0], %[xo], 16640\n\t"
        "buffer_load_dwordx4 v100, %[sx], %[t0] offen lds\n\t"
        "s_add_u32 m0, %[ld], 24576\n\t"
        "s_add_u32 %[t0], %[xo], 384\n\t"
        "buffer_load_dwordx4 v100, %[sx], %[t0] offen lds\n\t"
        "s_add_u32 m0, %[ld], 25600\n\t"
        "s_add_u32 %[t0], %[xo], 16768\n\t"
        "buffer_load_dwordx4 v100, %[sx], %[t0] offen lds\n\t"
        "s_mov_b32 %[xo], 0\n\t"
        "s_waitcnt vmcnt(14)\n\t"
        "ds_write_b128 v102, v[220:223] offset:16384\n\t"
        "ds_write_b128 v102, v[224:227] offset:20480\n\t"
        "ds_write_b128 v116, v[228:231] offset:24576\n\t"
        "s_waitcnt lgkmcnt(0)\n\t"
        "s_barrier\n\t"
        "v_mfma_f32_16x16x32_bf16 v[8:11], v[160:163], v[56:59], v[8:11]\n\t"
        "ds_read_b64_tr_b16 v[184:185], v103 offset:16384\n\t"
        "ds_read_b64_tr_b16 v[186:187], v103 offset:16896\n\t"
        "v_mfma_f32_16x16x32_bf16 v[12:15], v[164:167], v[56:59], v[12:15]\n\t"
        "ds_read_b64_tr_b16 v[188:189], v103 offset:16392\n\t"
        "ds_read_b64_tr_b16 v[190:191], v103 offset:16904\n\t"
        "v_mfma_f32_16x16x32_bf16 v[16:19], v[56:59], v[168:171], v[16:19]\n\t"
        "ds_read_b64_tr_b16 v[192:193], v104 offset:16384\n\t"
        "ds_read_b64_tr_b16 v[194:195], v104 offset:16640\n\t"
        "v_mfma_f32_16x16x32_bf16 v[20:23], v[160:163], v[60:63], v[20:23]\n\t"
        "s_waitcnt lgkmcnt(5)\n\t"
        "ds_read_b64_tr_b16 v[196:197], v103 offset:20480\n\t"
        "v_mfma_f32_16x16x32_bf16 v[24:27], v[164:167], v[60:63], v[24:27]\n\t"
        "ds_read_b64_tr_b16 v[198:199], v103 offset:20992\n\t"
        "ds_read_b64_tr_b16 v[200:201], v103 offset:20488\n\t"
        "v_mfma_f32_16x16x32_bf16 v[28:31], v[60:63], v[168:171], v[28:31]\n\t"
        "ds_read_b64_tr_b16 v[202:203], v103 offset:21000\n\t"
        "ds_read_b64_tr_b16 v[204:205], v104 offset:18432\n\t"
        "v_mfma_f32_16x16x32_bf16 v[8:11], v[172:175], v[64:67], v[8:11]\n\t"
        "ds_read_b64_tr_b16 v[206:207], v104 offset:18688\n\t"
        "ds_read_b128 v[72:75], v107 offset:40960\n\t"
        "v_mfma_f32_16x16x32_bf16 v[12:15], v[176:179], v[64:67], v[12:15]\n\t"
        "ds_read_b128 v[76:79], v107 offset:43008\n\t"
        "ds_read_b128 v[80:83], v108 offset:40960\n\t"
        "v_mfma_f32_16x16x32_bf16 v[16:19], v[64:67], v[180:183], v[16:19]\n\t"
        "ds_read_b128 v[84:87], v108 offset:43008\n\t"
        "v_mfma_f32_16x16x32_bf16 v[20:23], v[172:175], v[68:71], v[20:23]\n\t"
        "v_mfma_f32_16x16x32_bf16 v[24:27], v[176:179], v[68:71], v[24:27]\n\t"
        "v_mfma_f32_16x16x32_bf16 v[28:31], v[68:71], v[180:183], v[28:31]\n\t"
        "s_waitcnt vmcnt(11)\n\t"
        "ds_write_b128 v102, v[232:235] offset:0\n\t"
        "ds_write_b128 v102, v[236:239] offset:4096\n\t"
        "ds_write_b128 v116, v[240:243] offset:8192\n\t"
        "s_waitcnt lgkmcnt(0)\n\t"
        "s_barrier\n\t"
        "v_mfma_f32_16x16x32_bf16 v[8:11], v[184:187], v[72:75], v[8:11]\n\t"
        "ds_read_b64_tr_b16 v[208:209], v103 offset:0\n\t"
        "ds_read_b64_tr_b16 v[210:211], v103 offset:512\n\t"
        "v_mfma_f32_16x16x32_bf16 v[12:15], v[188:191], v[72:75], v[12:15]\n\t"
        "ds_read_b64_tr_b16 v[212:213], v103 offset:8\n\t"
        "ds_read_b64_tr_b16 v[214:215], v103 offset:520\n\t"
        "v_mfma_f32_16x16x32_bf16 v[16:19], v[72:75], v[192:195], v[16:19]\n\t"
        "ds_read_b64_tr_b16 v[216:217], v104 offset:0\n\t"
        "ds_read_b64_tr_b16 v[218:219], v104 offset:256\n\t"
        "v_mfma_f32_16x16x32_bf16 v[20:23], v[184:187], v[76:79], v[20:23]\n\t"
        "s_waitcnt lgkmcnt(5)\n\t"
        "ds_read_b64_tr_b16 v[220:221], v103 offset:4096\n\t"
        "v_mfma_f32_16x16x32_bf16 v[24:27], v[188:191], v[76:79], v[24:27]\n\t"
        "ds_read_b64_tr_b16 v[222:223], v103 offset:4608\n\t"
        "ds_read_b64_tr_b16 v[224:225], v103 offset:4104\n\t"
        "v_mfma_f32_16x16x32_bf16 v[28:31], v[76:79], v[192:195], v[28:31]\n\t"
        "ds_read_b64_tr_b16 v[226:227], v103 offset:4616\n\t"
        "ds_read_b64_tr_b16 v[228:229], v104 offset:2048\n\t"
        "v_mfma_f32_16x16x32_bf16 v[8:11], v[196:199], v[80:83], v[8:11]\n\t"
        "ds_read_b64_tr_b16 v[230:231], v104 offset:2304\n\t"
        "ds_read_b128 v[56:59], v107 offset:49152\n\t"
        "v_mfma_f32_16x16x32_bf16 v[12:15], v[200:203], v[80:83], v[12:15]\n\t"
        "ds_read_b128 v[60:63], v107 offset:51200\n\t"
        "ds_read_b128 v[64:67], v108 offset:49152\n\t"
        "v_mfma_f32_16x16x32_bf16 v[16:19], v[80:83], v[204:207], v[16:19]\n\t"
        "ds_read_b128 v[68:71], v108 offset:51200\n\t"
        "v_mfma_f32_16x16x32_bf16 v[20:23], v[196:199], v[84:87], v[20:23]\n\t"
        "v_mfma_f32_16x16x32_bf16 v[24:27], v[200:203], v[84:87], v[24:27]\n\t"
        "v_mfma_f32_16x16x32_bf16 v[28:31], v[84:87], v[204:207], v[28:31]\n\t"
        "s_waitcnt vmcnt(8)\n\t"
        "ds_write_b128 v102, v[244:247] offset:16384\n\t"
        "ds_write_b128 v102, v[248:251] offset:20480\n\t"
        "ds_write_b128 v116, v[252:255] offset:24576\n\t"
        "s_waitcnt lgkmcnt(0)\n\t"
        "s_barrier\n\t"
        "v_mfma_f32_16x16x32_bf16 v[8:11], v[208:211], v[56:59], v[8:11]\n\t"
        "ds_read_b64_tr_b16 v[232:233], v103 offset:16384\n\t"
        "ds_read_b64_tr_b16 v[234:235], v103 offset:16896\n\t"
        "v_mfma_f32_16x16x32_bf16 v[12:15], v[212:215], v[56:59], v[12:15]\n\t"
        "ds_read_b64_tr_b16 v[236:237], v103 offset:16392\n\t"
        "ds_read_b64_tr_b16 v[238:239], v103 offset:16904\n\t"
        "v_mfma_f32_16x16x32_bf16 v[16:19], v[56:59], v[216:219], v[16:19]\n\t"
        "ds_read_b64_tr_b16 v[240:241], v104 offset:16384\n\t"
        "ds_read_b64_tr_b16 v[242:243], v104 offset:16640\n\t"
        "v_mfma_f32_16x16x32_bf16 v[20:23], v[208:211], v[60:63], v[20:23]\n\t"
        "s_waitcnt lgkmcnt(5)\n\t"
        "ds_read_b64_tr_b16 v[244:245], v103 offset:20480\n\t"
        "v_mfma_f32_16x16x32_bf16 v[24:27], v[212:215], v[60:63], v[24:27]\n\t"
        "ds_read_b64_tr_b16 v[246:247], v103 offset:20992\n\t"
        "ds_read_b64_tr_b16 v[248:249], v103 offset:20488\n\t"
        "v_mfma_f32_16x16x32_bf16 v[28:31], v[60:63], v[216:219], v[28:31]\n\t"
        "ds_read_b64_tr_b16 v[250:251], v103 offset:21000\n\t"
        "ds_read_b64_tr_b16 v[252:253], v104 offset:18432\n\t"
        "v_mfma_f32_16x16x32_bf16 v[8:11], v[220:223], v[64:67], v[8:11]\n\t"
        "ds_read_b64_tr_b16 v[254:255], v104 offset:18688\n\t"
        "ds_read_b128 v[72:75], v107 offset:57344\n\t"
        "v_mfma_f32_16x16x32_bf16 v[12:15], v[224:227], v[64:67], v[12:15]\n\t"
        "ds_read_b128 v[76:79], v107 offset:59392\n\t"
        "ds_read_b128 v[80:83], v108 offset:57344\n\t"
        "v_mfma_f32_16x16x32_bf16 v[16:19], v[64:67], v[228:231], v[16:19]\n\t"
        "ds_read_b128 v[84:87], v108 offset:59392\n\t"
        "v_mfma_f32_16x16x32_bf16 v[20:23], v[220:223], v[68:71], v[20:23]\n\t"
        "v_mfma_f32_16x16x32_bf16 v[24:27], v[224:227], v[68:71], v[24:27]\n\t"
        "v_mfma_f32_16x16x32_bf16 v[28:31], v[68:71], v[228:231], v[28:31]\n\t"
        "s_waitcnt vmcnt(0) lgkmcnt(0)\n\t"
        "s_barrier\n\t"
        "v_mfma_f32_16x16x32_bf16 v[8:11], v[232:235], v[72:75], v[8:11]\n\t"
        "v_mfma_f32_16x16x32_bf16 v[12:15], v[236:239], v[72:75], v[12:15]\n\t"
        "v_mfma_f32_16x16x32_bf16 v[16:19], v[72:75], v[240:243], v[16:19]\n\t"
        "v_mfma_f32_16x16x32_bf16 v[20:23], v[232:235], v[76:79], v[20:23]\n\t"
        "v_mfma_f32_16x16x32_bf16 v[24:27], v[236:239], v[76:79], v[24:27]\n\t"
        "v_mfma_f32_16x16x32_bf16 v[28:31], v[76:79], v[240:243], v[28:31]\n\t"
        "v_mfma_f32_16x16x32_bf16 v[8:11], v[244:247], v[80:83], v[8:11]\n\t"
        "v_mfma_f32_16x16x32_bf16 v[12:15], v[248:251], v[80:83], v[12:15]\n\t"
        "v_mfma_f32_16x16x32_bf16 v[16:19], v[80:83], v[252:255], v[16:19]\n\t"
        "v_mfma_f32_16x16x32_bf16 v[20:23], v[244:247], v[84:87], v[20:23]\n\t"
        "v_mfma_f32_16x16x32_bf16 v[24:27], v[248:251], v[84:87], v[24:27]\n\t"
        "v_mfma_f32_16x16x32_bf16 v[28:31], v[84:87], v[252:255], v[28:31]\n\t"
        "s_mov_b32 %[xo], 0x20000\n\t"
        "ds_read_b128 v[56:59], v105 offset:0\n\t"
        "ds_read_b128 v[60:63], v105 offset:2048\n\t"
        "ds_read_b128 v[64:67], v106 offset:0\n\t"
        "ds_read_b128 v[68:71], v106 offset:2048\n\t"
        "ds_read_b128 v[72:75], v105 offset:8192\n\t"
        "ds_read_b128 v[76:79], v105 offset:10240\n\t"
        "ds_read_b128 v[80:83], v106 offset:8192\n\t"
        "v_cvt_pk_bf16_f32 v88, v8, v9\n\t"
        "v_cvt_pk_bf16_f32 v89, v10, v11\n\t"
        "v_cvt_pk_bf16_f32 v90, v12, v13\n\t"
        "v_cvt_pk_bf16_f32 v91, v14, v15\n\t"
        "buffer_store_dwordx4 v[88:91], v109, %[sw], %[so] offen sc1\n\t"
        "v_cvt_pk_bf16_f32 v92, v20, v21\n\t"
        "v_cvt_pk_bf16_f32 v93, v22, v23\n\t"
        "v_cvt_pk_bf16_f32 v94, v24, v25\n\t"
        "v_cvt_pk_bf16_f32 v95, v26, v27\n\t"
        "buffer_store_dwordx4 v[92:95], v110, %[sw], %[so] offen sc1\n\t"
        "v_cvt_pk_bf16_f32 v96, v16, v17\n\t"
        "v_cvt_pk_bf16_f32 v97, v18, v19\n\t"
        "v_cvt_pk_bf16_f32 v98, v28, v29\n\t"
        "v_cvt_pk_bf16_f32 v99, v30, v31\n\t"
        "buffer_store_dwordx4 v[96:99], v111, %[sw], %[so] offen sc1\n\t"
        "s_mov_b32 %[so], 0xf0000000\n\t"
        "s_mov_b32 %[xo], 33792\n\t"
        "s_mov_b32 %[cn], 0\n\t"
        "LG_cal%=:\n\t"
        "buffer_load_dword v118, off, %[sw], %[xo] sc1\n\t"
        "s_waitcnt vmcnt(0)\n\t"
        "v_readfirstlane_b32 %[t0], v118\n\t"
        "s_cmp_lg_u32 %[t0], 0\n\t"
        "s_cbranch_scc1 LG_calok%=\n\t"
        "s_sleep 4\n\t"
        "s_add_u32 %[cn], %[cn], 1\n\t"
        "s_cmp_lt_u32 %[cn], 0x100000\n\t"
        "s_cbranch_scc1 LG_cal%=\n\t"
        "s_endpgm\n\t"
        "LG_calok%=:\n\t"
        "s_mov_b32 %[xo], 0x20000\n\t"
        "s_mov_b32 %[cn], 1\n\t"
        "v_mov_b32_e32 v117, %[cn]\n\t"
        "buffer_store_dword v117, v113, %[sw], 0 offen sc1\n\t"
        "s_add_u32 m0, %[ld], 32768\n\t"
        "s_add_u32 %[t0], %[xo], 512\n\t"
        "buffer_load_dwordx4 v100, %[sx], %[t0] offen lds\n\t"
        "s_add_u32 m0, %[ld], 33792\n\t"
        "s_add_u32 %[t0], %[xo], 16896\n\t"
        "buffer_load_dwordx4 v100, %[sx], %[t0] offen lds\n\t"
        "s_add_u32 m0, %[ld], 40960\n\t"
        "s_add_u32 %[t0], %[xo], 640\n\t"
        "buffer_load_dwordx4 v100, %[sx], %[t0] offen lds\n\t"
        "s_add_u32 m0, %[ld], 41984\n\t"
        "s_add_u32 %[t0], %[xo], 17024\n\t"
        "buffer_load_dwordx4 v100, %[sx], %[t0] offen lds\n\t"
        "s_add_u32 m0, %[ld], 49152\n\t"
        "s_add_u32 %[t0], %[xo], 768\n\t"
        "buffer_load_dwordx4 v100, %[sx], %[t0] offen lds\n\t"
        "s_add_u32 m0, %[ld], 50176\n\t"
        "s_add_u32 %[t0], %[xo], 17152\n\t"
        "buffer_load_dwordx4 v100, %[sx], %[t0] offen lds\n\t"
        "s_add_u32 m0, %[ld], 57344\n\t"
        "s_add_u32 %[t0], %[xo], 896\n\t"
        "buffer_load_dwordx4 v100, %[sx], %[t0] offen lds\n\t"
        "s_add_u32 m0, %[ld], 58368\n\t"
        "s_add_u32 %[t0], %[xo], 17280\n\t"
        "buffer_load_dwordx4 v100, %[sx], %[t0] offen lds\n\t"
        "s_add_u32 m0, %[ld], 65536\n\t"
        "s_add_u32 %[t0], %[xo], 1024\n\t"
        "buffer_load_dwordx4 v100, %[sx], %[t0] offen lds\n\t"
        "s_add_u32 m0, %[ld], 66560\n\t"
        "s_add_u32 %[t0], %[xo], 17408\n\t"
        "buffer_load_dwordx4 v100, %[sx], %[t0] offen lds\n\t"
        "s_add_u32 m0, %[ld], 73728\n\t"
        "s_add_u32 %[t0], %[xo], 1152\n\t"
        "buffer_load_dwordx4 v100, %[sx], %[t0] offen lds\n\t"
        "s_add_u32 m0, %[ld], 74752\n\t"
        "s_add_u32 %[t0], %[xo], 17536\n\t"
        "buffer_load_dwordx4 v100, %[sx], %[t0] offen lds\n\t"
        "s_add_u32 m0, %[ld], 81920\n\t"
        "s_add_u32 %[t0], %[xo], 1280\n\t"
        "buffer_load_dwordx4 v100, %[sx], %[t0] offen lds\n\t"
        "s_add_u32 m0, %[ld], 82944\n\t"
        "s_add_u32 %[t0], %[xo], 17664\n\t"
        "buffer_load_dwordx4 v100, %[sx], %[t0] offen lds\n\t"
        "s_add_u32 m0, %[ld], 90112\n\t"
        "s_add_u32 %[t0], %[xo], 1408\n\t"
        "buffer_load_dwordx4 v100, %[sx], %[t0] offen lds\n\t"
        "s_add_u32 m0, %[ld], 91136\n\t"
        "s_add_u32 %[t0], %[xo], 17792\n\t"
        "buffer_load_dwordx4 v100, %[sx], %[t0] offen lds\n\t"
        "s_add_u32 m0, %[ld], 98304\n\t"
        "s_add_u32 %[t0], %[xo], 1536\n\t"
        "buffer_load_dwordx4 v100, %[sx], %[t0] offen lds\n\t"
        "s_add_u32 m0, %[ld], 99328\n\t"
        "s_add_u32 %[t0], %[xo], 17920\n\t"
        "buffer_load_dwordx4 v100, %[sx], %[t0] offen lds\n\t"
        "s_add_u32 m0, %[ld], 106496\n\t"
        "s_add_u32 %[t0], %[xo], 1664\n\t"
        "buffer_load_dwordx4 v100, %[sx], %[t0] offen lds\n\t"
        "s_branch LG_mid%=\n\t"
        "LG_loop%=:\n\t"
        "s_waitcnt lgkmcnt(6)\n\t"
        "v_mfma_f32_16x16x32_bf16 v[8:11], a[0:3], v[56:59], 0\n\t"
        "v_mfma_f32_16x16x32_bf16 v[12:15], a[4:7], v[56:59], 0\n\t"
        "ds_read_b128 v[84:87], v106 offset:10240\n\t"
        "v_mfma_f32_16x16x32_bf16 v[16:19], v[56:59], a[8:11], 0\n\t"
        "s_add_u32 m0, %[ld], 107520\n\t"
        "s_add_u32 %[t0], %[xo], 18048\n\t"
        "s_waitcnt lgkmcnt(6)\n\t"
        "v_mfma_f32_16x16x32_bf16 v[20:23], a[0:3], v[60:63], 0\n\t"
        "buffer_load_dwordx4 v100, %[sx], %[t0] offen lds\n\t"
        "v_mfma_f32_16x16x32_bf16 v[24:27], a[4:7], v[60:63], 0\n\t"
        "ds_read_b128 v[56:59], v105 offset:16384\n\t"
        "v_mfma_f32_16x16x32_bf16 v[28:31], v[60:63], a[8:11], 0\n\t"
        "s_waitcnt lgkmcnt(6)\n\t"
        "v_mfma_f32_16x16x32_bf16 v[8:11], a[12:15], v[64:67], v[8:11]\n\t"
        "v_cvt_pk_bf16_f32 v88, v32, v33\n\t"
        "v_cvt_pk_bf16_f32 v89, v34, v35\n\t"
        "v_mfma_f32_16x16x32_bf16 v[12:15], a[16:19], v[64:67], v[12:15]\n\t"
        "ds_read_b128 v[60:63], v105 offset:18432\n\t"
        "v_cvt_pk_bf16_f32 v90, v36, v37\n\t"
        "v_mfma_f32_16x16x32_bf16 v[16:19], v[64:67], a[20:23], v[16:19]\n\t"
        "s_add_u32 m0, %[ld], 114688\n\t"
        "s_add_u32 %[t0], %[xo], 1792\n\t"
        "s_waitcnt lgkmcnt(6)\n\t"
        "v_mfma_f32_16x16x32_bf16 v[20:23], a[12:15], v[68:71], v[20:23]\n\t"
        "buffer_load_dwordx4 v100, %[sx], %[t0] offen lds\n\t"
        "v_mfma_f32_16x16x32_bf16 v[24:27], a[16:19], v[68:71], v[24:27]\n\t"
        "ds_read_b128 v[64:67], v106 offset:16384\n\t"
        "v_cvt_pk_bf16_f32 v91, v38, v39\n\t"
        "v_mfma_f32_16x16x32_bf16 v[28:31], v[68:71], a[20:23], v[28:31]\n\t"
        "buffer_store_dwordx4 v[88:91], v109, %[sw], %[so] offen sc1\n\t"
        "s_waitcnt lgkmcnt(6)\n\t"
        "v_mfma_f32_16x16x32_bf16 v[8:11], a[24:27], v[72:75], v[8:11]\n\t"
        "v_cvt_pk_bf16_f32 v92, v44, v45\n\t"
        "v_cvt_pk_bf16_f32 v93, v46, v47\n\t"
        "v_mfma_f32_16x16x32_bf16 v[12:15], a[28:31], v[72:75], v[12:15]\n\t"
        "ds_read_b128 v[68:71], v106 offset:18432\n\t"
        "v_cvt_pk_bf16_f32 v94, v48, v49\n\t"
        "v_mfma_f32_16x16x32_bf16 v[16:19], v[72:75], a[32:35], v[16:19]\n\t"
        "s_add_u32 m0, %[ld], 115712\n\t"
        "s_add_u32 %[t0], %[xo], 18176\n\t"
        "s_waitcnt lgkmcnt(6)\n\t"
        "v_mfma_f32_16x16x32_bf16 v[20:23], a[24:27], v[76:79], v[20:23]\n\t"
        "buffer_load_dwordx4 v100, %[sx], %[t0] offen lds\n\t"
        "v_mfma_f32_16x16x32_bf16 v[24:27], a[28:31], v[76:79], v[24:27]\n\t"
        "ds_read_b128 v[72:75], v105 offset:24576\n\t"
        "v_cvt_pk_bf16_f32 v95, v50, v51\n\t"
        "v_mfma_f32_16x16x32_bf16 v[28:31], v[76:79], a[32:35], v[28:31]\n\t"
        "buffer_store_dwordx4 v[92:95], v110, %[sw], %[so] offen sc1\n\t"
        "s_waitcnt lgkmcnt(6)\n\t"
        "v_mfma_f32_16x16x32_bf16 v[8:11], a[36:39], v[80:83], v[8:11]\n\t"
        "v_cvt_pk_bf16_f32 v96, v40, v41\n\t"
        "v_cvt_pk_bf16_f32 v97, v42, v43\n\t"
        "v_mfma_f32_16x16x32_bf16 v[12:15], a[40:43], v[80:83], v[12:15]\n\t"
        "ds_read_b128 v[76:79], v105 offset:26624\n\t"
        "v_cvt_pk_bf16_f32 v98, v52, v53\n\t"
        "v_mfma_f32_16x16x32_bf16 v[16:19], v[80:83], a[44:47], v[16:19]\n\t"
        "s_add_u32 m0, %[ld], 122880\n\t"
        "s_add_u32 %[t0], %[xo], 1920\n\t"
        "s_waitcnt lgkmcnt(6)\n\t"
        "v_mfma_f32_16x16x32_bf16 v[20:23], a[36:39], v[84:87], v[20:23]\n\t"
        "buffer_load_dwordx4 v100, %[sx], %[t0] offen lds\n\t"
        "v_mfma_f32_16x16x32_bf16 v[24:27], a[40:43], v[84:87], v[24:27]\n\t"
        "ds_read_b128 v[80:83], v106 offset:24576\n\t"
        "v_cvt_pk_bf16_f32 v99, v54, v55\n\t"
        "v_mfma_f32_16x16x32_bf16 v[28:31], v[84:87], a[44:47], v[28:31]\n\t"
        "buffer_store_dwordx4 v[96:99], v111, %[sw], %[so] offen sc1\n\t"
        "s_waitcnt lgkmcnt(6)\n\t"
        "v_mfma_f32_16x16x32_bf16 v[8:11], a[48:51], v[56:59], v[8:11]\n\t"
        "s_lshl_b32 %[so], %[cn], 19\n\t"
        "s_cmp_eq_u32 %[cn], 2\n\t"
        "s_cbranch_scc1 LG_first%=\n\t"
        "LG_back%=:\n\t"
        "v_mfma_f32_16x16x32_bf16 v[12:15], a[52:55], v[56:59], v[12:15]\n\t"
        "ds_read_b128 v[84:87], v106 offset:26624\n\t"
        "v_mfma_f32_16x16x32_bf16 v[16:19], v[56:59], a[56:59], v[16:19]\n\t"
        "s_add_u32 m0, %[ld], 123904\n\t"
        "s_add_u32 %[t0], %[xo], 18304\n\t"
        "s_waitcnt lgkmcnt(6)\n\t"
        "v_mfma_f32_16x16x32_bf16 v[20:23], a[48:51], v[60:63], v[20:23]\n\t"
        "buffer_load_dwordx4 v100, %[sx], %[t0] offen lds\n\t"
        "v_mfma_f32_16x16x32_bf16 v[24:27], a[52:55], v[60:63], v[24:27]\n\t"
        "v_mfma_f32_16x16x32_bf16 v[28:31], v[60:63], a[56:59], v[28:31]\n\t"
        "s_waitcnt lgkmcnt(5)\n\t"
        "v_mfma_f32_16x16x32_bf16 v[8:11], a[60:63], v[64:67], v[8:11]\n\t"
        "s_waitcnt vmcnt(19) lgkmcnt(0)\n\t"
        "s_barrier\n\t"
        "s_add_u32 %[xo], %[xo], 0x20000\n\t"
        "v_mfma_f32_16x16x32_bf16 v[12:15], a[64:67], v[64:67], v[12:15]\n\t"
        "ds_read_b128 v[56:59], v105 offset:32768\n\t"
        "v_mfma_f32_16x16x32_bf16 v[16:19], v[64:67], a[68:71], v[16:19]\n\t"
        "ds_read_b128 v[60:63], v105 offset:34816\n\t"
        "s_add_u32 m0, %[ld], 0\n\t"
        "s_add_u32 %[t0], %[xo], 0\n\t"
        "v_mfma_f32_16x16x32_bf16 v[20:23], a[60:63], v[68:71], v[20:23]\n\t"
        "buffer_load_dwordx4 v100, %[sx], %[t0] offen lds\n\t"
        "v_mfma_f32_16x16x32_bf16 v[24:27], a[64:67], v[68:71], v[24:27]\n\t"
        "ds_read_b128 v[64:67], v106 offset:32768\n\t"
        "v_mfma_f32_16x16x32_bf16 v[28:31], v[68:71], a[68:71], v[28:31]\n\t"
        "v_mfma_f32_16x16x32_bf16 v[8:11], a[72:75], v[72:75], v[8:11]\n\t"
        "v_mfma_f32_16x16x32_bf16 v[12:15], a[76:79], v[72:75], v[12:15]\n\t"
        "ds_read_b128 v[68:71], v106 offset:34816\n\t"
        "v_mfma_f32_16x16x32_bf16 v[16:19], v[72:75], a[80:83], v[16:19]\n\t"
        "s_add_u32 m0, %[ld], 1024\n\t"
        "s_add_u32 %[t0], %[xo], 16384\n\t"
        "v_mfma_f32_16x16x32_bf16 v[20:23], a[72:75], v[76:79], v[20:23]\n\t"
        "buffer_load_dwordx4 v100, %[sx], %[t0] offen lds\n\t"
        "v_mfma_f32_16x16x32_bf16 v[24:27], a[76:79], v[76:79], v[24:27]\n\t"
        "ds_read_b128 v[72:75], v105 offset:40960\n\t"
        "v_mfma_f32_16x16x32_bf16 v[28:31], v[76:79], a[80:83], v[28:31]\n\t"
        "v_mfma_f32_16x16x32_bf16 v[8:11], a[84:87], v[80:83], v[8:11]\n\t"
        "v_mfma_f32_16x16x32_bf16 v[12:15], a[88:91], v[80:83], v[12:15]\n\t"
        "ds_read_b128 v[76:79], v105 offset:43008\n\t"
        "v_mfma_f32_16x16x32_bf16 v[16:19], v[80:83], a[92:95], v[16:19]\n\t"
        "s_add_u32 m0, %[ld], 8192\n\t"
        "s_add_u32 %[t0], %[xo], 128\n\t"
        "v_mfma_f32_16x16x32_bf16 v[20:23], a[84:87], v[84:87], v[20:23]\n\t"
        "buffer_load_dwordx4 v100, %[sx], %[t0] offen lds\n\t"
        "v_mfma_f32_16x16x32_bf16 v[24:27], a[88:91], v[84:87], v[24:27]\n\t"
        "ds_read_b128 v[80:83], v106 offset:40960\n\t"
        "v_mfma_f32_16x16x32_bf16 v[28:31], v[84:87], a[92:95], v[28:31]\n\t"
        "s_waitcnt lgkmcnt(6)\n\t"
        "v_mfma_f32_16x16x32_bf16 v[8:11], a[96:99], v[56:59], v[8:11]\n\t"
        "v_mfma_f32_16x16x32_bf16 v[12:15], a[100:103], v[56:59], v[12:15]\n\t"
        "ds_read_b128 v[84:87], v106 offset:43008\n\t"
        "v_mfma_f32_16x16x32_bf16 v[16:19], v[56:59], a[104:107], v[16:19]\n\t"
        "s_add_u32 m0, %[ld], 9216\n\t"
        "s_add_u32 %[t0], %[xo], 16512\n\t"
        "s_waitcnt lgkmcnt(6)\n\t"
        "v_mfma_f32_16x16x32_bf16 v[20:23], a[96:99], v[60:63], v[20:23]\n\t"
        "buffer_load_dwordx4 v100, %[sx], %[t0] offen lds\n\t"
        "v_mfma_f32_16x16x32_bf16 v[24:27], a[100:103], v[60:63], v[24:27]\n\t"
        "ds_read_b128 v[56:59], v105 offset:49152\n\t"
        "v_mfma_f32_16x16x32_bf16 v[28:31], v[60:63], a[104:107], v[28:31]\n\t"
        "s_waitcnt lgkmcnt(6)\n\t"
        "v_mfma_f32_16x16x32_bf16 v[8:11], a[108:111], v[64:67], v[8:11]\n\t"
        "v_mfma_f32_16x16x32_bf16 v[12:15], a[112:115], v[64:67], v[12:15]\n\t"
        "ds_read_b128 v[60:63], v105 offset:51200\n\t"
        "v_mfma_f32_16x16x32_bf16 v[16:19], v[64:67], a[116:119], v[16:19]\n\t"
        "s_add_u32 m0, %[ld], 16384\n\t"
        "s_add_u32 %[t0], %[xo], 256\n\t"
        "s_waitcnt lgkmcnt(6)\n\t"
        "v_mfma_f32_16x16x32_bf16 v[20:23], a[108:111], v[68:71], v[20:23]\n\t"
        "buffer_load_dwordx4 v100, %[sx], %[t0] offen lds\n\t"
        "v_mfma_f32_16x16x32_bf16 v[24:27], a[112:115], v[68:71], v[24:27]\n\t"
        "ds_read_b128 v[64:67], v106 offset:49152\n\t"
        "v_mfma_f32_16x16x32_bf16 v[28:31], v[68:71], a[116:119], v[28:31]\n\t"
        "s_waitcnt lgkmcnt(6)\n\t"
        "v_mfma_f32_16x16x32_bf16 v[8:11], a[120:123], v[72:75], v[8:11]\n\t"
        "v_mfma_f32_16x16x32_bf16 v[12:15], a[124:127], v[72:75], v[12:15]\n\t"
        "ds_read_b128 v[68:71], v106 offset:51200\n\t"
        "v_mfma_f32_16x16x32_bf16 v[16:19], v[72:75], a[128:131], v[16:19]\n\t"
        "s_add_u32 m0, %[ld], 17408\n\t"
        "s_add_u32 %[t0], %[xo], 16640\n\t"
        "s_waitcnt lgkmcnt(6)\n\t"
        "v_mfma_f32_16x16x32_bf16 v[20:23], a[120:123], v[76:79], v[20:23]\n\t"
        "buffer_load_dwordx4 v100, %[sx], %[t0] offen lds\n\t"
        "v_mfma_f32_16x16x32_bf16 v[24:27], a[124:127], v[76:79], v[24:27]\n\t"
        "ds_read_b128 v[72:75], v105 offset:57344\n\t"
        "v_mfma_f32_16x16x32_bf16 v[28:31], v[76:79], a[128:131], v[28:31]\n\t"
        "s_waitcnt lgkmcnt(6)\n\t"
        "v_mfma_f32_16x16x32_bf16 v[8:11], a[132:135], v[80:83], v[8:11]\n\t"
        "v_mfma_f32_16x16x32_bf16 v[12:15], a[136:139], v[80:83], v[12:15]\n\t"
        "ds_read_b128 v[76:79], v105 offset:59392\n\t"
        "v_mfma_f32_16x16x32_bf16 v[16:19], v[80:83], a[140:143], v[16:19]\n\t"
        "s_add_u32 m0, %[ld], 24576\n\t"
        "s_add_u32 %[t0], %[xo], 384\n\t"
        "s_waitcnt lgkmcnt(6)\n\t"
        "v_mfma_f32_16x16x32_bf16 v[20:23], a[132:135], v[84:87], v[20:23]\n\t"
        "buffer_load_dwordx4 v100, %[sx], %[t0] offen lds\n\t"
        "v_mfma_f32_16x16x32_bf16 v[24:27], a[136:139], v[84:87], v[24:27]\n\t"
        "ds_read_b128 v[80:83], v106 offset:57344\n\t"
        "v_mfma_f32_16x16x32_bf16 v[28:31], v[84:87], a[140:143], v[28:31]\n\t"
        "s_waitcnt lgkmcnt(6)\n\t"
        "v_mfma_f32_16x16x32_bf16 v[8:11], a[144:147], v[56:59], v[8:11]\n\t"
        "v_mfma_f32_16x16x32_bf16 v[12:15], a[148:151], v[56:59], v[12:15]\n\t"
        "ds_read_b128 v[84:87], v106 offset:59392\n\t"
        "v_mfma_f32_16x16x32_bf16 v[16:19], v[56:59], a[152:155], v[16:19]\n\t"
        "s_add_u32 m0, %[ld], 25600\n\t"
        "s_add_u32 %[t0], %[xo], 16768\n\t"
        "s_waitcnt lgkmcnt(6)\n\t"
        "v_mfma_f32_16x16x32_bf16 v[20:23], a[144:147], v[60:63], v[20:23]\n\t"
        "buffer_load_dwordx4 v100, %[sx], %[t0] offen lds\n\t"
        "v_mfma_f32_16x16x32_bf16 v[24:27], a[148:151], v[60:63], v[24:27]\n\t"
        "v_mfma_f32_16x16x32_bf16 v[28:31], v[60:63], a[152:155], v[28:31]\n\t"
        "s_waitcnt lgkmcnt(5)\n\t"
        "v_mfma_f32_16x16x32_bf16 v[8:11], a[156:159], v[64:67], v[8:11]\n\t"
        "s_waitcnt vmcnt(9) lgkmcnt(0)\n\t"
        "s_barrier\n\t"
        "v_mfma_f32_16x16x32_bf16 v[12:15], a[160:163], v[64:67], v[12:15]\n\t"
        "ds_read_b128 v[56:59], v107 offset:0\n\t"
        "v_mfma_f32_16x16x32_bf16 v[16:19], v[64:67], a[164:167], v[16:19]\n\t"
        "ds_read_b128 v[60:63], v107 offset:2048\n\t"
        "s_add_u32 m0, %[ld], 32768\n\t"
        "s_add_u32 %[t0], %[xo], 512\n\t"
        "v_mfma_f32_16x16x32_bf16 v[20:23], a[156:159], v[68:71], v[20:23]\n\t"
        "buffer_load_dwordx4 v100, %[sx], %[t0] offen lds\n\t"
        "v_mfma_f32_16x16x32_bf16 v[24:27], a[160:163], v[68:71], v[24:27]\n\t"
        "ds_read_b128 v[64:67], v108 offset:0\n\t"
        "v_mfma_f32_16x16x32_bf16 v[28:31], v[68:71], a[164:167], v[28:31]\n\t"
        "v_mfma_f32_16x16x32_bf16 v[8:11], a[168:171], v[72:75], v[8:11]\n\t"
        "v_mfma_f32_16x16x32_bf16 v[12:15], a[172:175], v[72:75], v[12:15]\n\t"
        "ds_read_b128 v[68:71], v108 offset:2048\n\t"
        "v_mov_b32_e32 v117, %[cn]\n\t"
        "buffer_store_dword v117, v113, %[sw], 0 offen sc1\n\t"
        "s_add_u32 %[cn], %[cn], 1\n\t"
        "v_mfma_f32_16x16x32_bf16 v[16:19], v[72:75], a[176:179], v[16:19]\n\t"
        "s_add_u32 m0, %[ld], 33792\n\t"
        "s_add_u32 %[t0], %[xo], 16896\n\t"
        "v_mfma_f32_16x16x32_bf16 v[20:23], a[168:171], v[76:79], v[20:23]\n\t"
        "buffer_load_dwordx4 v100, %[sx], %[t0] offen lds\n\t"
        "v_mfma_f32_16x16x32_bf16 v[24:27], a[172:175], v[76:79], v[24:27]\n\t"
        "ds_read_b128 v[72:75], v107 offset:8192\n\t"
        "v_mfma_f32_16x16x32_bf16 v[28:31], v[76:79], a[176:179], v[28:31]\n\t"
        "v_mfma_f32_16x16x32_bf16 v[8:11], a[180:183], v[80:83], v[8:11]\n\t"
        "v_mfma_f32_16x16x32_bf16 v[12:15], a[184:187], v[80:83], v[12:15]\n\t"
        "ds_read_b128 v[76:79], v107 offset:10240\n\t"
        "v_mfma_f32_16x16x32_bf16 v[16:19], v[80:83], a[188:191], v[16:19]\n\t"
        "s_add_u32 m0, %[ld], 40960\n\t"
        "s_add_u32 %[t0], %[xo], 640\n\t"
        "v_mfma_f32_16x16x32_bf16 v[20:23], a[180:183], v[84:87], v[20:23]\n\t"
        "buffer_load_dwordx4 v100, %[sx], %[t0] offen lds\n\t"
        "v_mfma_f32_16x16x32_bf16 v[24:27], a[184:187], v[84:87], v[24:27]\n\t"
        "ds_read_b128 v[80:83], v108 offset:8192\n\t"
        "v_mfma_f32_16x16x32_bf16 v[28:31], v[84:87], a[188:191], v[28:31]\n\t"
        "s_waitcnt lgkmcnt(6)\n\t"
        "v_mfma_f32_16x16x32_bf16 v[8:11], a[192:195], v[56:59], v[8:11]\n\t"
        "v_mfma_f32_16x16x32_bf16 v[12:15], a[196:199], v[56:59], v[12:15]\n\t"
        "ds_read_b128 v[84:87], v108 offset:10240\n\t"
        "v_mfma_f32_16x16x32_bf16 v[16:19], v[56:59], a[200:203], v[16:19]\n\t"
        "s_add_u32 m0, %[ld], 41984\n\t"
        "s_add_u32 %[t0], %[xo], 17024\n\t"
        "s_waitcnt lgkmcnt(6)\n\t"
        "v_mfma_f32_16x16x32_bf16 v[20:23], a[192:195], v[60:63], v[20:23]\n\t"
        "buffer_load_dwordx4 v100, %[sx], %[t0] offen lds\n\t"
        "v_mfma_f32_16x16x32_bf16 v[24:27], a[196:199], v[60:63], v[24:27]\n\t"
        "ds_read_b128 v[56:59], v107 offset:16384\n\t"
        "v_mfma_f32_16x16x32_bf16 v[28:31], v[60:63], a[200:203], v[28:31]\n\t"
        "s_waitcnt lgkmcnt(6)\n\t"
        "v_mfma_f32_16x16x32_bf16 v[8:11], a[204:207], v[64:67], v[8:11]\n\t"
        "v_mfma_f32_16x16x32_bf16 v[12:15], a[208:211], v[64:67], v[12:15]\n\t"
        "ds_read_b128 v[60:63], v107 offset:18432\n\t"
        "v_mfma_f32_16x16x32_bf16 v[16:19], v[64:67], a[212:215], v[16:19]\n\t"
        "s_add_u32 m0, %[ld], 49152\n\t"
        "s_add_u32 %[t0], %[xo], 768\n\t"
        "s_waitcnt lgkmcnt(6)\n\t"
        "v_mfma_f32_16x16x32_bf16 v[20:23], a[204:207], v[68:71], v[20:23]\n\t"
        "buffer_load_dwordx4 v100, %[sx], %[t0] offen lds\n\t"
        "v_mfma_f32_16x16x32_bf16 v[24:27], a[208:211], v[68:71], v[24:27]\n\t"
        "ds_read_b128 v[64:67], v108 offset:16384\n\t"
        "v_mfma_f32_16x16x32_bf16 v[28:31], v[68:71], a[212:215], v[28:31]\n\t"
        "s_waitcnt lgkmcnt(6)\n\t"
        "v_mfma_f32_16x16x32_bf16 v[8:11], a[216:219], v[72:75], v[8:11]\n\t"
        "v_mfma_f32_16x16x32_bf16 v[12:15], a[220:223], v[72:75], v[12:15]\n\t"
        "ds_read_b128 v[68:71], v108 offset:18432\n\t"
        "v_mfma_f32_16x16x32_bf16 v[16:19], v[72:75], a[224:227], v[16:19]\n\t"
        "s_add_u32 m0, %[ld], 50176\n\t"
        "s_add_u32 %[t0], %[xo], 17152\n\t"
        "s_waitcnt lgkmcnt(6)\n\t"
        "v_mfma_f32_16x16x32_bf16 v[20:23], a[216:219], v[76:79], v[20:23]\n\t"
        "buffer_load_dwordx4 v100, %[sx], %[t0] offen lds\n\t"
        "v_mfma_f32_16x16x32_bf16 v[24:27], a[220:223], v[76:79], v[24:27]\n\t"
        "ds_read_b128 v[72:75], v107 offset:24576\n\t"
        "v_mfma_f32_16x16x32_bf16 v[28:31], v[76:79], a[224:227], v[28:31]\n\t"
        "s_waitcnt lgkmcnt(6)\n\t"
        "v_mfma_f32_16x16x32_bf16 v[8:11], a[228:231], v[80:83], v[8:11]\n\t"
        "v_mfma_f32_16x16x32_bf16 v[12:15], a[232:235], v[80:83], v[12:15]\n\t"
        "ds_read_b128 v[76:79], v107 offset:26624\n\t"
        "v_mfma_f32_16x16x32_bf16 v[16:19], v[80:83], a[236:239], v[16:19]\n\t"
        "s_add_u32 m0, %[ld], 57344\n\t"
        "s_add_u32 %[t0], %[xo], 896\n\t"
        "s_waitcnt lgkmcnt(6)\n\t"
        "v_mfma_f32_16x16x32_bf16 v[20:23], a[228:231], v[84:87], v[20:23]\n\t"
        "buffer_load_dwordx4 v100, %[sx], %[t0] offen lds\n\t"
        "v_mfma_f32_16x16x32_bf16 v[24:27], a[232:235], v[84:87], v[24:27]\n\t"
        "ds_read_b128 v[80:83], v108 offset:24576\n\t"
        "v_mfma_f32_16x16x32_bf16 v[28:31], v[84:87], a[236:239], v[28:31]\n\t"
        "s_waitcnt lgkmcnt(6)\n\t"
        "v_mfma_f32_16x16x32_bf16 v[8:11], a[240:243], v[56:59], v[8:11]\n\t"
        "v_mfma_f32_16x16x32_bf16 v[12:15], a[244:247], v[56:59], v[12:15]\n\t"
        "ds_read_b128 v[84:87], v108 offset:26624\n\t"
        "v_mfma_f32_16x16x32_bf16 v[16:19], v[56:59], a[248:251], v[16:19]\n\t"
        "s_add_u32 m0, %[ld], 58368\n\t"
        "s_add_u32 %[t0], %[xo], 17280\n\t"
        "s_waitcnt lgkmcnt(6)\n\t"
        "v_mfma_f32_16x16x32_bf16 v[20:23], a[240:243], v[60:63], v[20:23]\n\t"
        "buffer_load_dwordx4 v100, %[sx], %[t0] offen lds\n\t"
        "v_mfma_f32_16x16x32_bf16 v[24:27], a[244:247], v[60:63], v[24:27]\n\t"
        "v_mfma_f32_16x16x32_bf16 v[28:31], v[60:63], a[248:251], v[28:31]\n\t"
        "s_waitcnt lgkmcnt(5)\n\t"
        "v_mfma_f32_16x16x32_bf16 v[8:11], a[252:255], v[64:67], v[8:11]\n\t"
        "s_waitcnt vmcnt(17) lgkmcnt(0)\n\t"
        "s_barrier\n\t"
        "v_mfma_f32_16x16x32_bf16 v[12:15], v[128:131], v[64:67], v[12:15]\n\t"
        "ds_read_b128 v[56:59], v107 offset:32768\n\t"
        "v_mfma_f32_16x16x32_bf16 v[16:19], v[64:67], v[132:135], v[16:19]\n\t"
        "ds_read_b128 v[60:63], v107 offset:34816\n\t"
        "s_add_u32 m0, %[ld], 65536\n\t"
        "s_add_u32 %[t0], %[xo], 1024\n\t"
        "v_mfma_f32_16x16x32_bf16 v[20:23], a[252:255], v[68:71], v[20:23]\n\t"
        "buffer_load_dwordx4 v100, %[sx], %[t0] offen lds\n\t"
        "v_mfma_f32_16x16x32_bf16 v[24:27], v[128:131], v[68:71], v[24:27]\n\t"
        "ds_read_b128 v[64:67], v108 offset:32768\n\t"
        "v_mfma_f32_16x16x32_bf16 v[28:31], v[68:71], v[132:135], v[28:31]\n\t"
        "v_mfma_f32_16x16x32_bf16 v[8:11], v[136:139], v[72:75], v[8:11]\n\t"
        "v_mfma_f32_16x16x32_bf16 v[12:15], v[140:143], v[72:75], v[12:15]\n\t"
        "ds_read_b128 v[68:71], v108 offset:34816\n\t"
        "v_mfma_f32_16x16x32_bf16 v[16:19], v[72:75], v[144:147], v[16:19]\n\t"
        "s_add_u32 m0, %[ld], 66560\n\t"
        "s_add_u32 %[t0], %[xo], 17408\n\t"
        "v_mfma_f32_16x16x32_bf16 v[20:23], v[136:139], v[76:79], v[20:23]\n\t"
        "buffer_load_dwordx4 v100, %[sx], %[t0] offen lds\n\t"
        "v_mfma_f32_16x16x32_bf16 v[24:27], v[140:143], v[76:79], v[24:27]\n\t"
        "ds_read_b128 v[72:75], v107 offset:40960\n\t"
        "v_mfma_f32_16x16x32_bf16 v[28:31], v[76:79], v[144:147], v[28:31]\n\t"
        "v_mfma_f32_16x16x32_bf16 v[8:11], v[148:151], v[80:83], v[8:11]\n\t"
        "v_mfma_f32_16x16x32_bf16 v[12:15], v[152:155], v[80:83], v[12:15]\n\t"
        "ds_read_b128 v[76:79], v107 offset:43008\n\t"
        "v_mfma_f32_16x16x32_bf16 v[16:19], v[80:83], v[156:159], v[16:19]\n\t"
        "s_add_u32 m0, %[ld], 73728\n\t"
        "s_add_u32 %[t0], %[xo], 1152\n\t"
        "v_mfma_f32_16x16x32_bf16 v[20:23], v[148:151], v[84:87], v[20:23]\n\t"
        "buffer_load_dwordx4 v100, %[sx], %[t0] offen lds\n\t"
        "v_mfma_f32_16x16x32_bf16 v[24:27], v[152:155], v[84:87], v[24:27]\n\t"
        "ds_read_b128 v[80:83], v108 offset:40960\n\t"
        "v_mfma_f32_16x16x32_bf16 v[28:31], v[84:87], v[156:159], v[28:31]\n\t"
        "s_waitcnt lgkmcnt(6)\n\t"
        "v_mfma_f32_16x16x32_bf16 v[8:11], v[160:163], v[56:59], v[8:11]\n\t"
        "v_mfma_f32_16x16x32_bf16 v[12:15], v[164:167], v[56:59], v[12:15]\n\t"
        "ds_read_b128 v[84:87], v108 offset:43008\n\t"
        "v_mfma_f32_16x16x32_bf16 v[16:19], v[56:59], v[168:171], v[16:19]\n\t"
        "s_add_u32 m0, %[ld], 74752\n\t"
        "s_add_u32 %[t0], %[xo], 17536\n\t"
        "s_waitcnt lgkmcnt(6)\n\t"
        "v_mfma_f32_16x16x32_bf16 v[20:23], v[160:163], v[60:63], v[20:23]\n\t"
        "buffer_load_dwordx4 v100, %[sx], %[t0] offen lds\n\t"
        "v_mfma_f32_16x16x32_bf16 v[24:27], v[164:167], v[60:63], v[24:27]\n\t"
        "ds_read_b128 v[56:59], v107 offset:49152\n\t"
        "v_mfma_f32_16x16x32_bf16 v[28:31], v[60:63], v[168:171], v[28:31]\n\t"
        "s_waitcnt lgkmcnt(6)\n\t"
        "v_mfma_f32_16x16x32_bf16 v[8:11], v[172:175], v[64:67], v[8:11]\n\t"
        "v_mfma_f32_16x16x32_bf16 v[12:15], v[176:179], v[64:67], v[12:15]\n\t"
        "ds_read_b128 v[60:63], v107 offset:51200\n\t"
        "v_mfma_f32_16x16x32_bf16 v[16:19], v[64:67], v[180:183], v[16:19]\n\t"
        "s_add_u32 m0, %[ld], 81920\n\t"
        "s_add_u32 %[t0], %[xo], 1280\n\t"
        "s_waitcnt lgkmcnt(6)\n\t"
        "v_mfma_f32_16x16x32_bf16 v[20:23], v[172:175], v[68:71], v[20:23]\n\t"
        "buffer_load_dwordx4 v100, %[sx], %[t0] offen lds\n\t"
        "v_mfma_f32_16x16x32_bf16 v[24:27], v[176:179], v[68:71], v[24:27]\n\t"
        "ds_read_b128 v[64:67], v108 offset:49152\n\t"
        "v_mfma_f32_16x16x32_bf16 v[28:31], v[68:71], v[180:183], v[28:31]\n\t"
        "s_waitcnt lgkmcnt(6)\n\t"
        "v_mfma_f32_16x16x32_bf16 v[8:11], v[184:187], v[72:75], v[8:11]\n\t"
        "v_mfma_f32_16x16x32_bf16 v[12:15], v[188:191], v[72:75], v[12:15]\n\t"
        "ds_read_b128 v[68:71], v108 offset:51200\n\t"
        "v_mfma_f32_16x16x32_bf16 v[16:19], v[72:75], v[192:195], v[16:19]\n\t"
        "s_add_u32 m0, %[ld], 82944\n\t"
        "s_add_u32 %[t0], %[xo], 17664\n\t"
        "s_waitcnt lgkmcnt(6)\n\t"
        "v_mfma_f32_16x16x32_bf16 v[20:23], v[184:187], v[76:79], v[20:23]\n\t"
        "buffer_load_dwordx4 v100, %[sx], %[t0] offen lds\n\t"
        "v_mfma_f32_16x16x32_bf16 v[24:27], v[188:191], v[76:79], v[24:27]\n\t"
        "ds_read_b128 v[72:75], v107 offset:57344\n\t"
        "v_mfma_f32_16x16x32_bf16 v[28:31], v[76:79], v[192:195], v[28:31]\n\t"
        "s_waitcnt lgkmcnt(6)\n\t"
        "v_mfma_f32_16x16x32_bf16 v[8:11], v[196:199], v[80:83], v[8:11]\n\t"
        "v_mfma_f32_16x16x32_bf16 v[12:15], v[200:203], v[80:83], v[12:15]\n\t"
        "ds_read_b128 v[76:79], v107 offset:59392\n\t"
        "v_mfma_f32_16x16x32_bf16 v[16:19], v[80:83], v[204:207], v[16:19]\n\t"
        "s_add_u32 m0, %[ld], 90112\n\t"
        "s_add_u32 %[t0], %[xo], 1408\n\t"
        "s_waitcnt lgkmcnt(6)\n\t"
        "v_mfma_f32_16x16x32_bf16 v[20:23], v[196:199], v[84:87], v[20:23]\n\t"
        "buffer_load_dwordx4 v100, %[sx], %[t0] offen lds\n\t"
        "v_mfma_f32_16x16x32_bf16 v[24:27], v[200:203], v[84:87], v[24:27]\n\t"
        "ds_read_b128 v[80:83], v108 offset:57344\n\t"
        "v_mfma_f32_16x16x32_bf16 v[28:31], v[84:87], v[204:207], v[28:31]\n\t"
        "s_waitcnt lgkmcnt(6)\n\t"
        "v_mfma_f32_16x16x32_bf16 v[8:11], v[208:211], v[56:59], v[8:11]\n\t"
        "v_mfma_f32_16x16x32_bf16 v[12:15], v[212:215], v[56:59], v[12:15]\n\t"
        "ds_read_b128 v[84:87], v108 offset:59392\n\t"
        "v_mfma_f32_16x16x32_bf16 v[16:19], v[56:59], v[216:219], v[16:19]\n\t"
        "s_add_u32 m0, %[ld], 91136\n\t"
        "s_add_u32 %[t0], %[xo], 17792\n\t"
        "s_waitcnt lgkmcnt(6)\n\t"
        "v_mfma_f32_16x16x32_bf16 v[20:23], v[208:211], v[60:63], v[20:23]\n\t"
        "buffer_load_dwordx4 v100, %[sx], %[t0] offen lds\n\t"
        "v_mfma_f32_16x16x32_bf16 v[24:27], v[212:215], v[60:63], v[24:27]\n\t"
        "v_mfma_f32_16x16x32_bf16 v[28:31], v[60:63], v[216:219], v[28:31]\n\t"
        "s_waitcnt lgkmcnt(5)\n\t"
        "v_mfma_f32_16x16x32_bf16 v[8:11], v[220:223], v[64:67], v[8:11]\n\t"
        "s_waitcnt vmcnt(17) lgkmcnt(0)\n\t"
        "s_barrier\n\t"
        "v_mfma_f32_16x16x32_bf16 v[12:15], v[224:227], v[64:67], v[12:15]\n\t"
        "ds_read_b128 v[56:59], v105 offset:0\n\t"
        "v_mfma_f32_16x16x32_bf16 v[16:19], v[64:67], v[228:231], v[16:19]\n\t"
        "ds_read_b128 v[60:63], v105 offset:2048\n\t"
        "s_add_u32 m0, %[ld], 98304\n\t"
        "s_add_u32 %[t0], %[xo], 1536\n\t"
        "v_mfma_f32_16x16x32_bf16 v[20:23], v[220:223], v[68:71], v[20:23]\n\t"
        "buffer_load_dwordx4 v100, %[sx], %[t0] offen lds\n\t"
        "v_mfma_f32_16x16x32_bf16 v[24:27], v[224:227], v[68:71], v[24:27]\n\t"
        "ds_read_b128 v[64:67], v106 offset:0\n\t"
        "v_mfma_f32_16x16x32_bf16 v[28:31], v[68:71], v[228:231], v[28:31]\n\t"
        "v_mfma_f32_16x16x32_bf16 v[8:11], v[232:235], v[72:75], v[8:11]\n\t"
        "v_mfma_f32_16x16x32_bf16 v[12:15], v[236:239], v[72:75], v[12:15]\n\t"
        "ds_read_b128 v[68:71], v106 offset:2048\n\t"
        "v_mfma_f32_16x16x32_bf16 v[16:19], v[72:75], v[240:243], v[16:19]\n\t"
        "s_add_u32 m0, %[ld], 99328\n\t"
        "s_add_u32 %[t0], %[xo], 17920\n\t"
        "v_mfma_f32_16x16x32_bf16 v[20:23], v[232:235], v[76:79], v[20:23]\n\t"
        "buffer_load_dwordx4 v100, %[sx], %[t0] offen lds\n\t"
        "v_mfma_f32_16x16x32_bf16 v[24:27], v[236:239], v[76:79], v[24:27]\n\t"
        "ds_read_b128 v[72:75], v105 offset:8192\n\t"
        "v_mfma_f32_16x16x32_bf16 v[28:31], v[76:79], v[240:243], v[28:31]\n\t"
        "v_mfma_f32_16x16x32_bf16 v[8:11], v[244:247], v[80:83], v[8:11]\n\t"
        "v_mfma_f32_16x16x32_bf16 v[12:15], v[248:251], v[80:83], v[12:15]\n\t"
        "ds_read_b128 v[76:79], v105 offset:10240\n\t"
        "v_mfma_f32_16x16x32_bf16 v[16:19], v[80:83], v[252:255], v[16:19]\n\t"
        "s_add_u32 m0, %[ld], 106496\n\t"
        "s_add_u32 %[t0], %[xo], 1664\n\t"
        "v_mfma_f32_16x16x32_bf16 v[20:23], v[244:247], v[84:87], v[20:23]\n\t"
        "buffer_load_dwordx4 v100, %[sx], %[t0] offen lds\n\t"
        "v_mfma_f32_16x16x32_bf16 v[24:27], v[248:251], v[84:87], v[24:27]\n\t"
        "ds_read_b128 v[80:83], v106 offset:8192\n\t"
        "v_mfma_f32_16x16x32_bf16 v[28:31], v[84:87], v[252:255], v[28:31]\n\t"
        "LG_mid%=:\n\t"
        "s_waitcnt lgkmcnt(6)\n\t"
        "v_mfma_f32_16x16x32_bf16 v[32:35], a[0:3], v[56:59], 0\n\t"
        "v_mfma_f32_16x16x32_bf16 v[36:39], a[4:7], v[56:59], 0\n\t"
        "ds_read_b128 v[84:87], v106 offset:10240\n\t"
        "v_mfma_f32_16x16x32_bf16 v[40:43], v[56:59], a[8:11], 0\n\t"
        "s_add_u32 m0, %[ld], 107520\n\t"
        "s_add_u32 %[t0], %[xo], 18048\n\t"
        "s_waitcnt lgkmcnt(6)\n\t"
        "v_mfma_f32_16x16x32_bf16 v[44:47], a[0:3], v[60:63], 0\n\t"
        "buffer_load_dwordx4 v100, %[sx], %[t0] offen lds\n\t"
        "v_mfma_f32_16x16x32_bf16 v[48:51], a[4:7], v[60:63], 0\n\t"
        "ds_read_b128 v[56:59], v105 offset:16384\n\t"
        "v_mfma_f32_16x16x32_bf16 v[52:55], v[60:63], a[8:11], 0\n\t"
        "s_waitcnt lgkmcnt(6)\n\t"
        "v_mfma_f32_16x16x32_bf16 v[32:35], a[12:15], v[64:67], v[32:35]\n\t"
        "v_cvt_pk_bf16_f32 v88, v8, v9\n\t"
        "v_cvt_pk_bf16_f32 v89, v10, v11\n\t"
        "v_mfma_f32_16x16x32_bf16 v[36:39], a[16:19], v[64:67], v[36:39]\n\t"
        "ds_read_b128 v[60:63], v105 offset:18432\n\t"
        "v_cvt_pk_bf16_f32 v90, v12, v13\n\t"
        "v_mfma_f32_16x16x32_bf16 v[40:43], v[64:67], a[20:23], v[40:43]\n\t"
        "s_add_u32 m0, %[ld], 114688\n\t"
        "s_add_u32 %[t0], %[xo], 1792\n\t"
        "s_waitcnt lgkmcnt(6)\n\t"
        "v_mfma_f32_16x16x32_bf16 v[44:47], a[12:15], v[68:71], v[44:47]\n\t"
        "buffer_load_dwordx4 v100, %[sx], %[t0] offen lds\n\t"
        "v_mfma_f32_16x16x32_bf16 v[48:51], a[16:19], v[68:71], v[48:51]\n\t"
        "ds_read_b128 v[64:67], v106 offset:16384\n\t"
        "v_cvt_pk_bf16_f32 v91, v14, v15\n\t"
        "v_mfma_f32_16x16x32_bf16 v[52:55], v[68:71], a[20:23], v[52:55]\n\t"
        "buffer_store_dwordx4 v[88:91], v109, %[sw], %[so] offen sc1\n\t"
        "s_waitcnt lgkmcnt(6)\n\t"
        "v_mfma_f32_16x16x32_bf16 v[32:35], a[24:27], v[72:75], v[32:35]\n\t"
        "v_cvt_pk_bf16_f32 v92, v20, v21\n\t"
        "v_cvt_pk_bf16_f32 v93, v22, v23\n\t"
        "v_mfma_f32_16x16x32_bf16 v[36:39], a[28:31], v[72:75], v[36:39]\n\t"
        "ds_read_b128 v[68:71], v106 offset:18432\n\t"
        "v_cvt_pk_bf16_f32 v94, v24, v25\n\t"
        "v_mfma_f32_16x16x32_bf16 v[40:43], v[72:75], a[32:35], v[40:43]\n\t"
        "s_add_u32 m0, %[ld], 115712\n\t"
        "s_add_u32 %[t0], %[xo], 18176\n\t"
        "s_waitcnt lgkmcnt(6)\n\t"
        "v_mfma_f32_16x16x32_bf16 v[44:47], a[24:27], v[76:79], v[44:47]\n\t"
        "buffer_load_dwordx4 v100, %[sx], %[t0] offen lds\n\t"
        "v_mfma_f32_16x16x32_bf16 v[48:51], a[28:31], v[76:79], v[48:51]\n\t"
        "ds_read_b128 v[72:75], v105 offset:24576\n\t"
        "v_cvt_pk_bf16_f32 v95, v26, v27\n\t"
        "v_mfma_f32_16x16x32_bf16 v[52:55], v[76:79], a[32:35], v[52:55]\n\t"
        "buffer_store_dwordx4 v[92:95], v110, %[sw], %[so] offen sc1\n\t"
        "s_waitcnt lgkmcnt(6)\n\t"
        "v_mfma_f32_16x16x32_bf16 v[32:35], a[36:39], v[80:83], v[32:35]\n\t"
        "v_cvt_pk_bf16_f32 v96, v16, v17\n\t"
        "v_cvt_pk_bf16_f32 v97, v18, v19\n\t"
        "v_mfma_f32_16x16x32_bf16 v[36:39], a[40:43], v[80:83], v[36:39]\n\t"
        "ds_read_b128 v[76:79], v105 offset:26624\n\t"
        "v_cvt_pk_bf16_f32 v98, v28, v29\n\t"
        "v_mfma_f32_16x16x32_bf16 v[40:43], v[80:83], a[44:47], v[40:43]\n\t"
        "s_add_u32 m0, %[ld], 122880\n\t"
        "s_add_u32 %[t0], %[xo], 1920\n\t"
        "s_waitcnt lgkmcnt(6)\n\t"
        "v_mfma_f32_16x16x32_bf16 v[44:47], a[36:39], v[84:87], v[44:47]\n\t"
        "buffer_load_dwordx4 v100, %[sx], %[t0] offen lds\n\t"
        "v_mfma_f32_16x16x32_bf16 v[48:51], a[40:43], v[84:87], v[48:51]\n\t"
        "ds_read_b128 v[80:83], v106 offset:24576\n\t"
        "v_cvt_pk_bf16_f32 v99, v30, v31\n\t"
        "v_mfma_f32_16x16x32_bf16 v[52:55], v[84:87], a[44:47], v[52:55]\n\t"
        "buffer_store_dwordx4 v[96:99], v111, %[sw], %[so] offen sc1\n\t"
        "s_waitcnt lgkmcnt(6)\n\t"
        "v_mfma_f32_16x16x32_bf16 v[32:35], a[48:51], v[56:59], v[32:35]\n\t"
        "s_lshl_b32 %[so], %[cn], 19\n\t"
        "v_mfma_f32_16x16x32_bf16 v[36:39], a[52:55], v[56:59], v[36:39]\n\t"
        "ds_read_b128 v[84:87], v106 offset:26624\n\t"
        "v_mfma_f32_16x16x32_bf16 v[40:43], v[56:59], a[56:59], v[40:43]\n\t"
        "s_add_u32 m0, %[ld], 123904\n\t"
        "s_add_u32 %[t0], %[xo], 18304\n\t"
        "s_waitcnt lgkmcnt(6)\n\t"
        "v_mfma_f32_16x16x32_bf16 v[44:47], a[48:51], v[60:63], v[44:47]\n\t"
        "buffer_load_dwordx4 v100, %[sx], %[t0] offen lds\n\t"
        "v_mfma_f32_16x16x32_bf16 v[48:51], a[52:55], v[60:63], v[48:51]\n\t"
        "v_mfma_f32_16x16x32_bf16 v[52:55], v[60:63], a[56:59], v[52:55]\n\t"
        "s_waitcnt lgkmcnt(5)\n\t"
        "v_mfma_f32_16x16x32_bf16 v[32:35], a[60:63], v[64:67], v[32:35]\n\t"
        "s_waitcnt vmcnt(19) lgkmcnt(0)\n\t"
        "s_barrier\n\t"
        "s_add_u32 %[xo], %[xo], 0x20000\n\t"
        "v_mfma_f32_16x16x32_bf16 v[36:39], a[64:67], v[64:67], v[36:39]\n\t"
        "ds_read_b128 v[56:59], v105 offset:32768\n\t"
        "v_mfma_f32_16x16x32_bf16 v[40:43], v[64:67], a[68:71], v[40:43]\n\t"
        "ds_read_b128 v[60:63], v105 offset:34816\n\t"
        "s_add_u32 m0, %[ld], 0\n\t"
        "s_add_u32 %[t0], %[xo], 0\n\t"
        "v_mfma_f32_16x16x32_bf16 v[44:47], a[60:63], v[68:71], v[44:47]\n\t"
        "buffer_load_dwordx4 v100, %[sx], %[t0] offen lds\n\t"
        "v_mfma_f32_16x16x32_bf16 v[48:51], a[64:67], v[68:71], v[48:51]\n\t"
        "ds_read_b128 v[64:67], v106 offset:32768\n\t"
        "v_mfma_f32_16x16x32_bf16 v[52:55], v[68:71], a[68:71], v[52:55]\n\t"
        "v_mfma_f32_16x16x32_bf16 v[32:35], a[72:75], v[72:75], v[32:35]\n\t"
        "v_mfma_f32_16x16x32_bf16 v[36:39], a[76:79], v[72:75], v[36:39]\n\t"
        "ds_read_b128 v[68:71], v106 offset:34816\n\t"
        "v_mfma_f32_16x16x32_bf16 v[40:43], v[72:75], a[80:83], v[40:43]\n\t"
        "s_add_u32 m0, %[ld], 1024\n\t"
        "s_add_u32 %[t0], %[xo], 16384\n\t"
        "v_mfma_f32_16x16x32_bf16 v[44:47], a[72:75], v[76:79], v[44:47]\n\t"
        "buffer_load_dwordx4 v100, %[sx], %[t0] offen lds\n\t"
        "v_mfma_f32_16x16x32_bf16 v[48:51], a[76:79], v[76:79], v[48:51]\n\t"
        "ds_read_b128 v[72:75], v105 offset:40960\n\t"
        "v_mfma_f32_16x16x32_bf16 v[52:55], v[76:79], a[80:83], v[52:55]\n\t"
        "v_mfma_f32_16x16x32_bf16 v[32:35], a[84:87], v[80:83], v[32:35]\n\t"
        "v_mfma_f32_16x16x32_bf16 v[36:39], a[88:91], v[80:83], v[36:39]\n\t"
        "ds_read_b128 v[76:79], v105 offset:43008\n\t"
        "v_mfma_f32_16x16x32_bf16 v[40:43], v[80:83], a[92:95], v[40:43]\n\t"
        "s_add_u32 m0, %[ld], 8192\n\t"
        "s_add_u32 %[t0], %[xo], 128\n\t"
        "v_mfma_f32_16x16x32_bf16 v[44:47], a[84:87], v[84:87], v[44:47]\n\t"
        "buffer_load_dwordx4 v100, %[sx], %[t0] offen lds\n\t"
        "v_mfma_f32_16x16x32_bf16 v[48:51], a[88:91], v[84:87], v[48:51]\n\t"
        "ds_read_b128 v[80:83], v106 offset:40960\n\t"
        "v_mfma_f32_16x16x32_bf16 v[52:55], v[84:87], a[92:95], v[52:55]\n\t"
        "s_waitcnt lgkmcnt(6)\n\t"
        "v_mfma_f32_16x16x32_bf16 v[32:35], a[96:99], v[56:59], v[32:35]\n\t"
        "v_mfma_f32_16x16x32_bf16 v[36:39], a[100:103], v[56:59], v[36:39]\n\t"
        "ds_read_b128 v[84:87], v106 offset:43008\n\t"
        "v_mfma_f32_16x16x32_bf16 v[40:43], v[56:59], a[104:107], v[40:43]\n\t"
        "s_add_u32 m0, %[ld], 9216\n\t"
        "s_add_u32 %[t0], %[xo], 16512\n\t"
        "s_waitcnt lgkmcnt(6)\n\t"
        "v_mfma_f32_16x16x32_bf16 v[44:47], a[96:99], v[60:63], v[44:47]\n\t"
        "buffer_load_dwordx4 v100, %[sx], %[t0] offen lds\n\t"
        "v_mfma_f32_16x16x32_bf16 v[48:51], a[100:103], v[60:63], v[48:51]\n\t"
        "ds_read_b128 v[56:59], v105 offset:49152\n\t"
        "v_mfma_f32_16x16x32_bf16 v[52:55], v[60:63], a[104:107], v[52:55]\n\t"
        "s_waitcnt lgkmcnt(6)\n\t"
        "v_mfma_f32_16x16x32_bf16 v[32:35], a[108:111], v[64:67], v[32:35]\n\t"
        "v_mfma_f32_16x16x32_bf16 v[36:39], a[112:115], v[64:67], v[36:39]\n\t"
        "ds_read_b128 v[60:63], v105 offset:51200\n\t"
        "v_mfma_f32_16x16x32_bf16 v[40:43], v[64:67], a[116:119], v[40:43]\n\t"
        "s_add_u32 m0, %[ld], 16384\n\t"
        "s_add_u32 %[t0], %[xo], 256\n\t"
        "s_waitcnt lgkmcnt(6)\n\t"
        "v_mfma_f32_16x16x32_bf16 v[44:47], a[108:111], v[68:71], v[44:47]\n\t"
        "buffer_load_dwordx4 v100, %[sx], %[t0] offen lds\n\t"
        "v_mfma_f32_16x16x32_bf16 v[48:51], a[112:115], v[68:71], v[48:51]\n\t"
        "ds_read_b128 v[64:67], v106 offset:49152\n\t"
        "v_mfma_f32_16x16x32_bf16 v[52:55], v[68:71], a[116:119], v[52:55]\n\t"
        "s_waitcnt lgkmcnt(6)\n\t"
        "v_mfma_f32_16x16x32_bf16 v[32:35], a[120:123], v[72:75], v[32:35]\n\t"
        "v_mfma_f32_16x16x32_bf16 v[36:39], a[124:127], v[72:75], v[36:39]\n\t"
        "ds_read_b128 v[68:71], v106 offset:51200\n\t"
        "v_mfma_f32_16x16x32_bf16 v[40:43], v[72:75], a[128:131], v[40:43]\n\t"
        "s_add_u32 m0, %[ld], 17408\n\t"
        "s_add_u32 %[t0], %[xo], 16640\n\t"
        "s_waitcnt lgkmcnt(6)\n\t"
        "v_mfma_f32_16x16x32_bf16 v[44:47], a[120:123], v[76:79], v[44:47]\n\t"
        "buffer_load_dwordx4 v100, %[sx], %[t0] offen lds\n\t"
        "v_mfma_f32_16x16x32_bf16 v[48:51], a[124:127], v[76:79], v[48:51]\n\t"
        "ds_read_b128 v[72:75], v105 offset:57344\n\t"
        "v_mfma_f32_16x16x32_bf16 v[52:55], v[76:79], a[128:131], v[52:55]\n\t"
        "s_waitcnt lgkmcnt(6)\n\t"
        "v_mfma_f32_16x16x32_bf16 v[32:35], a[132:135], v[80:83], v[32:35]\n\t"
        "v_mfma_f32_16x16x32_bf16 v[36:39], a[136:139], v[80:83], v[36:39]\n\t"
        "ds_read_b128 v[76:79], v105 offset:59392\n\t"
        "v_mfma_f32_16x16x32_bf16 v[40:43], v[80:83], a[140:143], v[40:43]\n\t"
        "s_add_u32 m0, %[ld], 24576\n\t"
        "s_add_u32 %[t0], %[xo], 384\n\t"
        "s_waitcnt lgkmcnt(6)\n\t"
        "v_mfma_f32_16x16x32_bf16 v[44:47], a[132:135], v[84:87], v[44:47]\n\t"
        "buffer_load_dwordx4 v100, %[sx], %[t0] offen lds\n\t"
        "v_mfma_f32_16x16x32_bf16 v[48:51], a[136:139], v[84:87], v[48:51]\n\t"
        "ds_read_b128 v[80:83], v106 offset:57344\n\t"
        "v_mfma_f32_16x16x32_bf16 v[52:55], v[84:87], a[140:143], v[52:55]\n\t"
        "s_waitcnt lgkmcnt(6)\n\t"
        "v_mfma_f32_16x16x32_bf16 v[32:35], a[144:147], v[56:59], v[32:35]\n\t"
        "v_mfma_f32_16x16x32_bf16 v[36:39], a[148:151], v[56:59], v[36:39]\n\t"
        "ds_read_b128 v[84:87], v106 offset:59392\n\t"
        "v_mfma_f32_16x16x32_bf16 v[40:43], v[56:59], a[152:155], v[40:43]\n\t"
        "s_add_u32 m0, %[ld], 25600\n\t"
        "s_add_u32 %[t0], %[xo], 16768\n\t"
        "s_waitcnt lgkmcnt(6)\n\t"
        "v_mfma_f32_16x16x32_bf16 v[44:47], a[144:147], v[60:63], v[44:47]\n\t"
        "buffer_load_dwordx4 v100, %[sx], %[t0] offen lds\n\t"
        "v_mfma_f32_16x16x32_bf16 v[48:51], a[148:151], v[60:63], v[48:51]\n\t"
        "v_mfma_f32_16x16x32_bf16 v[52:55], v[60:63], a[152:155], v[52:55]\n\t"
        "s_waitcnt lgkmcnt(5)\n\t"
        "v_mfma_f32_16x16x32_bf16 v[32:35], a[156:159], v[64:67], v[32:35]\n\t"
        "s_waitcnt vmcnt(9) lgkmcnt(0)\n\t"
        "s_barrier\n\t"
        "v_mfma_f32_16x16x32_bf16 v[36:39], a[160:163], v[64:67], v[36:39]\n\t"
        "ds_read_b128 v[56:59], v107 offset:0\n\t"
        "v_mfma_f32_16x16x32_bf16 v[40:43], v[64:67], a[164:167], v[40:43]\n\t"
        "ds_read_b128 v[60:63], v107 offset:2048\n\t"
        "s_add_u32 m0, %[ld], 32768\n\t"
        "s_add_u32 %[t0], %[xo], 512\n\t"
        "v_mfma_f32_16x16x32_bf16 v[44:47], a[156:159], v[68:71], v[44:47]\n\t"
        "buffer_load_dwordx4 v100, %[sx], %[t0] offen lds\n\t"
        "v_mfma_f32_16x16x32_bf16 v[48:51], a[160:163], v[68:71], v[48:51]\n\t"
        "ds_read_b128 v[64:67], v108 offset:0\n\t"
        "v_mfma_f32_16x16x32_bf16 v[52:55], v[68:71], a[164:167], v[52:55]\n\t"
        "v_mfma_f32_16x16x32_bf16 v[32:35], a[168:171], v[72:75], v[32:35]\n\t"
        "v_mfma_f32_16x16x32_bf16 v[36:39], a[172:175], v[72:75], v[36:39]\n\t"
        "ds_read_b128 v[68:71], v108 offset:2048\n\t"
        "v_mov_b32_e32 v117, %[cn]\n\t"
        "buffer_store_dword v117, v113, %[sw], 0 offen sc1\n\t"
        "s_add_u32 %[cn], %[cn], 1\n\t"
        "v_mfma_f32_16x16x32_bf16 v[40:43], v[72:75], a[176:179], v[40:43]\n\t"
        "s_add_u32 m0, %[ld], 33792\n\t"
        "s_add_u32 %[t0], %[xo], 16896\n\t"
        "v_mfma_f32_16x16x32_bf16 v[44:47], a[168:171], v[76:79], v[44:47]\n\t"
        "buffer_load_dwordx4 v100, %[sx], %[t0] offen lds\n\t"
        "v_mfma_f32_16x16x32_bf16 v[48:51], a[172:175], v[76:79], v[48:51]\n\t"
        "ds_read_b128 v[72:75], v107 offset:8192\n\t"
        "v_mfma_f32_16x16x32_bf16 v[52:55], v[76:79], a[176:179], v[52:55]\n\t"
        "v_mfma_f32_16x16x32_bf16 v[32:35], a[180:183], v[80:83], v[32:35]\n\t"
        "v_mfma_f32_16x16x32_bf16 v[36:39], a[184:187], v[80:83], v[36:39]\n\t"
        "ds_read_b128 v[76:79], v107 offset:10240\n\t"
        "v_mfma_f32_16x16x32_bf16 v[40:43], v[80:83], a[188:191], v[40:43]\n\t"
        "s_add_u32 m0, %[ld], 40960\n\t"
        "s_add_u32 %[t0], %[xo], 640\n\t"
        "v_mfma_f32_16x16x32_bf16 v[44:47], a[180:183], v[84:87], v[44:47]\n\t"
        "buffer_load_dwordx4 v100, %[sx], %[t0] offen lds\n\t"
        "v_mfma_f32_16x16x32_bf16 v[48:51], a[184:187], v[84:87], v[48:51]\n\t"
        "ds_read_b128 v[80:83], v108 offset:8192\n\t"
        "v_mfma_f32_16x16x32_bf16 v[52:55], v[84:87], a[188:191], v[52:55]\n\t"
        "s_waitcnt lgkmcnt(6)\n\t"
        "v_mfma_f32_16x16x32_bf16 v[32:35], a[192:195], v[56:59], v[32:35]\n\t"
        "v_mfma_f32_16x16x32_bf16 v[36:39], a[196:199], v[56:59], v[36:39]\n\t"
        "ds_read_b128 v[84:87], v108 offset:10240\n\t"
        "v_mfma_f32_16x16x32_bf16 v[40:43], v[56:59], a[200:203], v[40:43]\n\t"
        "s_add_u32 m0, %[ld], 41984\n\t"
        "s_add_u32 %[t0], %[xo], 17024\n\t"
        "s_waitcnt lgkmcnt(6)\n\t"
        "v_mfma_f32_16x16x32_bf16 v[44:47], a[192:195], v[60:63], v[44:47]\n\t"
        "buffer_load_dwordx4 v100, %[sx], %[t0] offen lds\n\t"
        "v_mfma_f32_16x16x32_bf16 v[48:51], a[196:199], v[60:63], v[48:51]\n\t"
        "ds_read_b128 v[56:59], v107 offset:16384\n\t"
        "v_mfma_f32_16x16x32_bf16 v[52:55], v[60:63], a[200:203], v[52:55]\n\t"
        "s_waitcnt lgkmcnt(6)\n\t"
        "v_mfma_f32_16x16x32_bf16 v[32:35], a[204:207], v[64:67], v[32:35]\n\t"
        "v_mfma_f32_16x16x32_bf16 v[36:39], a[208:211], v[64:67], v[36:39]\n\t"
        "ds_read_b128 v[60:63], v107 offset:18432\n\t"
        "v_mfma_f32_16x16x32_bf16 v[40:43], v[64:67], a[212:215], v[40:43]\n\t"
        "s_add_u32 m0, %[ld], 49152\n\t"
        "s_add_u32 %[t0], %[xo], 768\n\t"
        "s_waitcnt lgkmcnt(6)\n\t"
        "v_mfma_f32_16x16x32_bf16 v[44:47], a[204:207], v[68:71], v[44:47]\n\t"
        "buffer_load_dwordx4 v100, %[sx], %[t0] offen lds\n\t"
        "v_mfma_f32_16x16x32_bf16 v[48:51], a[208:211], v[68:71], v[48:51]\n\t"
        "ds_read_b128 v[64:67], v108 offset:16384\n\t"
        "v_mfma_f32_16x16x32_bf16 v[52:55], v[68:71], a[212:215], v[52:55]\n\t"
        "s_waitcnt lgkmcnt(6)\n\t"
        "v_mfma_f32_16x16x32_bf16 v[32:35], a[216:219], v[72:75], v[32:35]\n\t"
        "v_mfma_f32_16x16x32_bf16 v[36:39], a[220:223], v[72:75], v[36:39]\n\t"
        "ds_read_b128 v[68:71], v108 offset:18432\n\t"
        "v_mfma_f32_16x16x32_bf16 v[40:43], v[72:75], a[224:227], v[40:43]\n\t"
        "s_add_u32 m0, %[ld], 50176\n\t"
        "s_add_u32 %[t0], %[xo], 17152\n\t"
        "s_waitcnt lgkmcnt(6)\n\t"
        "v_mfma_f32_16x16x32_bf16 v[44:47], a[216:219], v[76:79], v[44:47]\n\t"
        "buffer_load_dwordx4 v100, %[sx], %[t0] offen lds\n\t"
        "v_mfma_f32_16x16x32_bf16 v[48:51], a[220:223], v[76:79], v[48:51]\n\t"
        "ds_read_b128 v[72:75], v107 offset:24576\n\t"
        "v_mfma_f32_16x16x32_bf16 v[52:55], v[76:79], a[224:227], v[52:55]\n\t"
        "s_waitcnt lgkmcnt(6)\n\t"
        "v_mfma_f32_16x16x32_bf16 v[32:35], a[228:231], v[80:83], v[32:35]\n\t"
        "v_mfma_f32_16x16x32_bf16 v[36:39], a[232:235], v[80:83], v[36:39]\n\t"
        "ds_read_b128 v[76:79], v107 offset:26624\n\t"
        "v_mfma_f32_16x16x32_bf16 v[40:43], v[80:83], a[236:239], v[40:43]\n\t"
        "s_add_u32 m0, %[ld], 57344\n\t"
        "s_add_u32 %[t0], %[xo], 896\n\t"
        "s_waitcnt lgkmcnt(6)\n\t"
        "v_mfma_f32_16x16x32_bf16 v[44:47], a[228:231], v[84:87], v[44:47]\n\t"
        "buffer_load_dwordx4 v100, %[sx], %[t0] offen lds\n\t"
        "v_mfma_f32_16x16x32_bf16 v[48:51], a[232:235], v[84:87], v[48:51]\n\t"
        "ds_read_b128 v[80:83], v108 offset:24576\n\t"
        "v_mfma_f32_16x16x32_bf16 v[52:55], v[84:87], a[236:239], v[52:55]\n\t"
        "s_waitcnt lgkmcnt(6)\n\t"
        "v_mfma_f32_16x16x32_bf16 v[32:35], a[240:243], v[56:59], v[32:35]\n\t"
        "v_mfma_f32_16x16x32_bf16 v[36:39], a[244:247], v[56:59], v[36:39]\n\t"
        "ds_read_b128 v[84:87], v108 offset:26624\n\t"
        "v_mfma_f32_16x16x32_bf16 v[40:43], v[56:59], a[248:251], v[40:43]\n\t"
        "s_add_u32 m0, %[ld], 58368\n\t"
        "s_add_u32 %[t0], %[xo], 17280\n\t"
        "s_waitcnt lgkmcnt(6)\n\t"
        "v_mfma_f32_16x16x32_bf16 v[44:47], a[240:243], v[60:63], v[44:47]\n\t"
        "buffer_load_dwordx4 v100, %[sx], %[t0] offen lds\n\t"
        "v_mfma_f32_16x16x32_bf16 v[48:51], a[244:247], v[60:63], v[48:51]\n\t"
        "v_mfma_f32_16x16x32_bf16 v[52:55], v[60:63], a[248:251], v[52:55]\n\t"
        "s_waitcnt lgkmcnt(5)\n\t"
        "v_mfma_f32_16x16x32_bf16 v[32:35], a[252:255], v[64:67], v[32:35]\n\t"
        "s_waitcnt vmcnt(17) lgkmcnt(0)\n\t"
        "s_barrier\n\t"
        "v_mfma_f32_16x16x32_bf16 v[36:39], v[128:131], v[64:67], v[36:39]\n\t"
        "ds_read_b128 v[56:59], v107 offset:32768\n\t"
        "v_mfma_f32_16x16x32_bf16 v[40:43], v[64:67], v[132:135], v[40:43]\n\t"
        "ds_read_b128 v[60:63], v107 offset:34816\n\t"
        "s_add_u32 m0, %[ld], 65536\n\t"
        "s_add_u32 %[t0], %[xo], 1024\n\t"
        "v_mfma_f32_16x16x32_bf16 v[44:47], a[252:255], v[68:71], v[44:47]\n\t"
        "buffer_load_dwordx4 v100, %[sx], %[t0] offen lds\n\t"
        "v_mfma_f32_16x16x32_bf16 v[48:51], v[128:131], v[68:71], v[48:51]\n\t"
        "ds_read_b128 v[64:67], v108 offset:32768\n\t"
        "v_mfma_f32_16x16x32_bf16 v[52:55], v[68:71], v[132:135], v[52:55]\n\t"
        "v_mfma_f32_16x16x32_bf16 v[32:35], v[136:139], v[72:75], v[32:35]\n\t"
        "v_mfma_f32_16x16x32_bf16 v[36:39], v[140:143], v[72:75], v[36:39]\n\t"
        "ds_read_b128 v[68:71], v108 offset:34816\n\t"
        "v_mfma_f32_16x16x32_bf16 v[40:43], v[72:75], v[144:147], v[40:43]\n\t"
        "s_add_u32 m0, %[ld], 66560\n\t"
        "s_add_u32 %[t0], %[xo], 17408\n\t"
        "v_mfma_f32_16x16x32_bf16 v[44:47], v[136:139], v[76:79], v[44:47]\n\t"
        "buffer_load_dwordx4 v100, %[sx], %[t0] offen lds\n\t"
        "v_mfma_f32_16x16x32_bf16 v[48:51], v[140:143], v[76:79], v[48:51]\n\t"
        "ds_read_b128 v[72:75], v107 offset:40960\n\t"
        "v_mfma_f32_16x16x32_bf16 v[52:55], v[76:79], v[144:147], v[52:55]\n\t"
        "v_mfma_f32_16x16x32_bf16 v[32:35], v[148:151], v[80:83], v[32:35]\n\t"
        "v_mfma_f32_16x16x32_bf16 v[36:39], v[152:155], v[80:83], v[36:39]\n\t"
        "ds_read_b128 v[76:79], v107 offset:43008\n\t"
        "v_mfma_f32_16x16x32_bf16 v[40:43], v[80:83], v[156:159], v[40:43]\n\t"
        "s_add_u32 m0, %[ld], 73728\n\t"
        "s_add_u32 %[t0], %[xo], 1152\n\t"
        "v_mfma_f32_16x16x32_bf16 v[44:47], v[148:151], v[84:87], v[44:47]\n\t"
        "buffer_load_dwordx4 v100, %[sx], %[t0] offen lds\n\t"
        "v_mfma_f32_16x16x32_bf16 v[48:51], v[152:155], v[84:87], v[48:51]\n\t"
        "ds_read_b128 v[80:83], v108 offset:40960\n\t"
        "v_mfma_f32_16x16x32_bf16 v[52:55], v[84:87], v[156:159], v[52:55]\n\t"
        "s_waitcnt lgkmcnt(6)\n\t"
        "v_mfma_f32_16x16x32_bf16 v[32:35], v[160:163], v[56:59], v[32:35]\n\t"
        "v_mfma_f32_16x16x32_bf16 v[36:39], v[164:167], v[56:59], v[36:39]\n\t"
        "ds_read_b128 v[84:87], v108 offset:43008\n\t"
        "v_mfma_f32_16x16x32_bf16 v[40:43], v[56:59], v[168:171], v[40:43]\n\t"
        "s_add_u32 m0, %[ld], 74752\n\t"
        "s_add_u32 %[t0], %[xo], 17536\n\t"
        "s_waitcnt lgkmcnt(6)\n\t"
        "v_mfma_f32_16x16x32_bf16 v[44:47], v[160:163], v[60:63], v[44:47]\n\t"
        "buffer_load_dwordx4 v100, %[sx], %[t0] offen lds\n\t"
        "v_mfma_f32_16x16x32_bf16 v[48:51], v[164:167], v[60:63], v[48:51]\n\t"
        "ds_read_b128 v[56:59], v107 offset:49152\n\t"
        "v_mfma_f32_16x16x32_bf16 v[52:55], v[60:63], v[168:171], v[52:55]\n\t"
        "s_waitcnt lgkmcnt(6)\n\t"
        "v_mfma_f32_16x16x32_bf16 v[32:35], v[172:175], v[64:67], v[32:35]\n\t"
        "v_mfma_f32_16x16x32_bf16 v[36:39], v[176:179], v[64:67], v[36:39]\n\t"
        "ds_read_b128 v[60:63], v107 offset:51200\n\t"
        "v_mfma_f32_16x16x32_bf16 v[40:43], v[64:67], v[180:183], v[40:43]\n\t"
        "s_add_u32 m0, %[ld], 81920\n\t"
        "s_add_u32 %[t0], %[xo], 1280\n\t"
        "s_waitcnt lgkmcnt(6)\n\t"
        "v_mfma_f32_16x16x32_bf16 v[44:47], v[172:175], v[68:71], v[44:47]\n\t"
        "buffer_load_dwordx4 v100, %[sx], %[t0] offen lds\n\t"
        "v_mfma_f32_16x16x32_bf16 v[48:51], v[176:179], v[68:71], v[48:51]\n\t"
        "ds_read_b128 v[64:67], v108 offset:49152\n\t"
        "v_mfma_f32_16x16x32_bf16 v[52:55], v[68:71], v[180:183], v[52:55]\n\t"
        "s_waitcnt lgkmcnt(6)\n\t"
        "v_mfma_f32_16x16x32_bf16 v[32:35], v[184:187], v[72:75], v[32:35]\n\t"
        "v_mfma_f32_16x16x32_bf16 v[36:39], v[188:191], v[72:75], v[36:39]\n\t"
        "ds_read_b128 v[68:71], v108 offset:51200\n\t"
        "v_mfma_f32_16x16x32_bf16 v[40:43], v[72:75], v[192:195], v[40:43]\n\t"
        "s_add_u32 m0, %[ld], 82944\n\t"
        "s_add_u32 %[t0], %[xo], 17664\n\t"
        "s_waitcnt lgkmcnt(6)\n\t"
        "v_mfma_f32_16x16x32_bf16 v[44:47], v[184:187], v[76:79], v[44:47]\n\t"
        "buffer_load_dwordx4 v100, %[sx], %[t0] offen lds\n\t"
        "v_mfma_f32_16x16x32_bf16 v[48:51], v[188:191], v[76:79], v[48:51]\n\t"
        "ds_read_b128 v[72:75], v107 offset:57344\n\t"
        "v_mfma_f32_16x16x32_bf16 v[52:55], v[76:79], v[192:195], v[52:55]\n\t"
        "s_waitcnt lgkmcnt(6)\n\t"
        "v_mfma_f32_16x16x32_bf16 v[32:35], v[196:199], v[80:83], v[32:35]\n\t"
        "v_mfma_f32_16x16x32_bf16 v[36:39], v[200:203], v[80:83], v[36:39]\n\t"
        "ds_read_b128 v[76:79], v107 offset:59392\n\t"
        "v_mfma_f32_16x16x32_bf16 v[40:43], v[80:83], v[204:207], v[40:43]\n\t"
        "s_add_u32 m0, %[ld], 90112\n\t"
        "s_add_u32 %[t0], %[xo], 1408\n\t"
        "s_waitcnt lgkmcnt(6)\n\t"
        "v_mfma_f32_16x16x32_bf16 v[44:47], v[196:199], v[84:87], v[44:47]\n\t"
        "buffer_load_dwordx4 v100, %[sx], %[t0] offen lds\n\t"
        "v_mfma_f32_16x16x32_bf16 v[48:51], v[200:203], v[84:87], v[48:51]\n\t"
        "ds_read_b128 v[80:83], v108 offset:57344\n\t"
        "v_mfma_f32_16x16x32_bf16 v[52:55], v[84:87], v[204:207], v[52:55]\n\t"
        "s_waitcnt lgkmcnt(6)\n\t"
        "v_mfma_f32_16x16x32_bf16 v[32:35], v[208:211], v[56:59], v[32:35]\n\t"
        "v_mfma_f32_16x16x32_bf16 v[36:39], v[212:215], v[56:59], v[36:39]\n\t"
        "ds_read_b128 v[84:87], v108 offset:59392\n\t"
        "v_mfma_f32_16x16x32_bf16 v[40:43], v[56:59], v[216:219], v[40:43]\n\t"
        "s_add_u32 m0, %[ld], 91136\n\t"
        "s_add_u32 %[t0], %[xo], 17792\n\t"
        "s_waitcnt lgkmcnt(6)\n\t"
        "v_mfma_f32_16x16x32_bf16 v[44:47], v[208:211], v[60:63], v[44:47]\n\t"
        "buffer_load_dwordx4 v100, %[sx], %[t0] offen lds\n\t"
        "v_mfma_f32_16x16x32_bf16 v[48:51], v[212:215], v[60:63], v[48:51]\n\t"
        "v_mfma_f32_16x16x32_bf16 v[52:55], v[60:63], v[216:219], v[52:55]\n\t"
        "s_waitcnt lgkmcnt(5)\n\t"
        "v_mfma_f32_16x16x32_bf16 v[32:35], v[220:223], v[64:67], v[32:35]\n\t"
        "s_waitcnt vmcnt(17) lgkmcnt(0)\n\t"
        "s_barrier\n\t"
        "v_mfma_f32_16x16x32_bf16 v[36:39], v[224:227], v[64:67], v[36:39]\n\t"
        "ds_read_b128 v[56:59], v105 offset:0\n\t"
        "v_mfma_f32_16x16x32_bf16 v[40:43], v[64:67], v[228:231], v[40:43]\n\t"
        "ds_read_b128 v[60:63], v105 offset:2048\n\t"
        "s_add_u32 m0, %[ld], 98304\n\t"
        "s_add_u32 %[t0], %[xo], 1536\n\t"
        "v_mfma_f32_16x16x32_bf16 v[44:47], v[220:223], v[68:71], v[44:47]\n\t"
        "buffer_load_dwordx4 v100, %[sx], %[t0] offen lds\n\t"
        "v_mfma_f32_16x16x32_bf16 v[48:51], v[224:227], v[68:71], v[48:51]\n\t"
        "ds_read_b128 v[64:67], v106 offset:0\n\t"
        "v_mfma_f32_16x16x32_bf16 v[52:55], v[68:71], v[228:231], v[52:55]\n\t"
        "v_mfma_f32_16x16x32_bf16 v[32:35], v[232:235], v[72:75], v[32:35]\n\t"
        "v_mfma_f32_16x16x32_bf16 v[36:39], v[236:239], v[72:75], v[36:39]\n\t"
        "ds_read_b128 v[68:71], v106 offset:2048\n\t"
        "v_mfma_f32_16x16x32_bf16 v[40:43], v[72:75], v[240:243], v[40:43]\n\t"
        "s_add_u32 m0, %[ld], 99328\n\t"
        "s_add_u32 %[t0], %[xo], 17920\n\t"
        "v_mfma_f32_16x16x32_bf16 v[44:47], v[232:235], v[76:79], v[44:47]\n\t"
        "buffer_load_dwordx4 v100, %[sx], %[t0] offen lds\n\t"
        "v_mfma_f32_16x16x32_bf16 v[48:51], v[236:239], v[76:79], v[48:51]\n\t"
        "ds_read_b128 v[72:75], v105 offset:8192\n\t"
        "v_mfma_f32_16x16x32_bf16 v[52:55], v[76:79], v[240:243], v[52:55]\n\t"
        "v_mfma_f32_16x16x32_bf16 v[32:35], v[244:247], v[80:83], v[32:35]\n\t"
        "v_mfma_f32_16x16x32_bf16 v[36:39], v[248:251], v[80:83], v[36:39]\n\t"
        "ds_read_b128 v[76:79], v105 offset:10240\n\t"
        "v_mfma_f32_16x16x32_bf16 v[40:43], v[80:83], v[252:255], v[40:43]\n\t"
        "s_add_u32 m0, %[ld], 106496\n\t"
        "s_add_u32 %[t0], %[xo], 1664\n\t"
        "v_mfma_f32_16x16x32_bf16 v[44:47], v[244:247], v[84:87], v[44:47]\n\t"
        "buffer_load_dwordx4 v100, %[sx], %[t0] offen lds\n\t"
        "v_mfma_f32_16x16x32_bf16 v[48:51], v[248:251], v[84:87], v[48:51]\n\t"
        "ds_read_b128 v[80:83], v106 offset:8192\n\t"
        "v_mfma_f32_16x16x32_bf16 v[52:55], v[84:87], v[252:255], v[52:55]\n\t"
        "s_cmp_lt_u32 %[cn], 32\n\t"
        "s_cbranch_scc1 LG_loop%=\n\t"
        "s_nop 7\n\t"
        "v_cvt_pk_bf16_f32 v88, v32, v33\n\t"
        "v_cvt_pk_bf16_f32 v89, v34, v35\n\t"
        "v_cvt_pk_bf16_f32 v90, v36, v37\n\t"
        "v_cvt_pk_bf16_f32 v91, v38, v39\n\t"
        "buffer_store_dwordx4 v[88:91], v109, %[sw], %[so] offen sc1\n\t"
        "v_cvt_pk_bf16_f32 v92, v44, v45\n\t"
        "v_cvt_pk_bf16_f32 v93, v46, v47\n\t"
        "v_cvt_pk_bf16_f32 v94, v48, v49\n\t"
        "v_cvt_pk_bf16_f32 v95, v50, v51\n\t"
        "buffer_store_dwordx4 v[92:95], v110, %[sw], %[so] offen sc1\n\t"
        "v_cvt_pk_bf16_f32 v96, v40, v41\n\t"
        "v_cvt_pk_bf16_f32 v97, v42, v43\n\t"
        "v_cvt_pk_bf16_f32 v98, v52, v53\n\t"
        "v_cvt_pk_bf16_f32 v99, v54, v55\n\t"
        "buffer_store_dwordx4 v[96:99], v111, %[sw], %[so] offen sc1\n\t"
        "s_waitcnt vmcnt(0) lgkmcnt(0)\n\t"
        "v_mov_b32_e32 v117, %[cn]\n\t"
        "buffer_store_dword v117, v113, %[sw], 0 offen sc1\n\t"
        "s_barrier\n\t"
        "s_branch LG_end%=\n\t"
        "LG_first%=:\n\t"
        "s_waitcnt vmcnt(0)\n\t"
        "v_mov_b32_e32 v117, %[cn]\n\t"
        "buffer_store_dword v117, v113, %[sw], 0 offen sc1\n\t"
        "s_branch LG_back%=\n\t"
        "LG_end%=:\n\t"
        ".Lk_end_G:\n\t"
        : [t0] "=&s"(t0), [xo] "=&s"(xo), [so] "=&s"(so), [cn] "=&s"(cn), [ts] "=&s"(ts), [ta] "=&s"(ta), [tb] "=&s"(tb), [tc] "=&s"(tc), [td] "=&s"(td)
        : [sx] "s"(sx), [sq] "s"(sq), [sv] "s"(sv), [sw] "s"(sw), [ld] "s"(lb + w * 2048),
          "{v100}"((16 * w + (l >> 3)) * 2048 + (((l & 7) ^ (l >> 3)) << 4)), "{v101}"((8 * w + (l >> 3)) * 8192 + h * 256 + (c & 1) * 128 + (l & 7) * 16),
          "{v112}"((16 * w + (l >> 2)) * 8192 + h * 256 + cv * 64 + (l & 3) * 16), "{v102}"(lb + 131072 + w * 1024 + ((l * 16) ^ ((l & 16) << 2))), "{v116}"(lb + 131072 + w * 1024 + ((l * 16) ^ (l & 32))),
          "{v103}"(lr + rr * 128 + ((tn ^ (n >> 3)) * 64) + (n & 3) * 16), "{v104}"(lr + 8192 + rr * 64 + ((tn ^ (g & 1)) * 32) + (n & 3) * 8), "{v105}"(lx), "{v106}"(lx ^ 64u), "{v107}"(lx + 65536), "{v108}"((lx ^ 64u) + 65536),
          "{v109}"(aq), "{v110}"(aq + 4096), "{v111}"(W_VF + (tm * 32 + h) * 8192 + (2 * cv + tn) * 1024 + l * 16), "{v113}"(l == 0 ? F_KVQ + h * 1024 + (c * 4 + w) * 64 : OOB), "{v[114:115]}"(tp)
        : "memory", "scc", "m0", "v8", "v9", "v10", "v11", "v12", "v13", "v14", "v15", "v16", "v17", "v18", "v19", "v20", "v21", "v22", "v23", "v24", "v25", "v26", "v27", "v28", "v29", "v30", "v31", "v32", "v33", "v34", "v35", "v36", "v37", "v38", "v39", "v40", "v41", "v42", "v43", "v44", "v45", "v46", "v47", "v48", "v49", "v50", "v51", "v52", "v53", "v54", "v55", "v56", "v57", "v58", "v59", "v60", "v61", "v62", "v63", "v64", "v65", "v66", "v67", "v68", "v69", "v70", "v71", "v72", "v73", "v74", "v75", "v76", "v77", "v78", "v79", "v80", "v81", "v82", "v83", "v84", "v85", "v86", "v87", "v88", "v89", "v90", "v91", "v92", "v93", "v94", "v95", "v96", "v97", "v98", "v99", "v117", "v118", "v119", "v120", "v121", "v122", "v123", "v124", "v125", "v126", "v127", "v128", "v129", "v130", "v131", "v132", "v133", "v134", "v135", "v136", "v137", "v138", "v139", "v140", "v141", "v142", "v143", "v144", "v145", "v146", "v147", "v148", "v149", "v150", "v151", "v152", "v153", "v154", "v155", "v156", "v157", "v158", "v159", "v160", "v161", "v162", "v163", "v164", "v165", "v166", "v167", "v168", "v169", "v170", "v171", "v172", "v173", "v174", "v175", "v176", "v177", "v178", "v179", "v180", "v181", "v182", "v183", "v184", "v185", "v186", "v187", "v188", "v189", "v190", "v191", "v192", "v193", "v194", "v195", "v196", "v197", "v198", "v199", "v200", "v201", "v202", "v203", "v204", "v205", "v206", "v207", "v208", "v209", "v210", "v211", "v212", "v213", "v214", "v215", "v216", "v217", "v218", "v219", "v220", "v221", "v222", "v223", "v224", "v225", "v226", "v227", "v228", "v229", "v230", "v231", "v232", "v233", "v234", "v235", "v236", "v237", "v238", "v239", "v240", "v241", "v242", "v243", "v244", "v245", "v246", "v247", "v248", "v249", "v250", "v251", "v252", "v253", "v254", "v255", "a0", "a1", "a2", "a3", "a4", "a5", "a6", "a7", "a8", "a9", "a10", "a11", "a12", "a13", "a14", "a15", "a16", "a17", "a18", "a19", "a20", "a21", "a22", "a23", "a24", "a25", "a26", "a27", "a28", "a29", "a30", "a31", "a32", "a33", "a34", "a35", "a36", "a37", "a38", "a39", "a40", "a41", "a42", "a43", "a44", "a45", "a46", "a47", "a48", "a49", "a50", "a51", "a52", "a53", "a54", "a55", "a56", "a57", "a58", "a59", "a60", "a61", "a62", "a63", "a64", "a65", "a66", "a67", "a68", "a69", "a70", "a71", "a72", "a73", "a74", "a75", "a76", "a77", "a78", "a79", "a80", "a81", "a82", "a83", "a84", "a85", "a86", "a87", "a88", "a89", "a90", "a91", "a92", "a93", "a94", "a95", "a96", "a97", "a98", "a99", "a100", "a101", "a102", "a103", "a104", "a105", "a106", "a107", "a108", "a109", "a110", "a111", "a112", "a113", "a114", "a115", "a116", "a117", "a118", "a119", "a120", "a121", "a122", "a123", "a124", "a125", "a126", "a127", "a128", "a129", "a130", "a131", "a132", "a133", "a134", "a135", "a136", "a137", "a138", "a139", "a140", "a141", "a142", "a143", "a144", "a145", "a146", "a147", "a148", "a149", "a150", "a151", "a152", "a153", "a154", "a155", "a156", "a157", "a158", "a159", "a160", "a161", "a162", "a163", "a164", "a165", "a166", "a167", "a168", "a169", "a170", "a171", "a172", "a173", "a174", "a175", "a176", "a177", "a178", "a179", "a180", "a181", "a182", "a183", "a184", "a185", "a186", "a187", "a188", "a189", "a190", "a191", "a192", "a193", "a194", "a195", "a196", "a197", "a198", "a199", "a200", "a201", "a202", "a203", "a204", "a205", "a206", "a207", "a208", "a209", "a210", "a211", "a212", "a213", "a214", "a215", "a216", "a217", "a218", "a219", "a220", "a221", "a222", "a223", "a224", "a225", "a226", "a227", "a228", "a229", "a230", "a231", "a232", "a233", "a234", "a235", "a236", "a237", "a238", "a239", "a240", "a241", "a242", "a243", "a244", "a245", "a246", "a247", "a248", "a249", "a250", "a251", "a252", "a253", "a254", "a255");
}

__device__ __forceinline__ void out_block(const Params& p, unsigned char* lds, const int wv, const int rp, const int nt, const int j) {
    const int w = wv, l = otid() & 63, n = l & 15, g = l >> 4;
    const unsigned lb = __builtin_amdgcn_readfirstlane((unsigned)(size_t)lds), stg = lb + 131072 + w * 8192;
    const v4u so = mk_srd(p.Wo, 1024 * 4096 * 2), sw = mk_srd(p.ws, W_TOTAL), su = mk_srd(p.out, S * D * 2);
    const unsigned lr = stg + (4 * g + (n >> 2)) * 128 + ((2 * (g & 1) + (n >> 3)) << 5) + (n & 3) * 8;
    unsigned t0, t1, tl = rp, pg, cu, nx, sp, re; unsigned long long tu;
    asm volatile(
        "s_add_u32 %[t1], %[ro], 0\n\t"
        "s_and_b32 %[t1], %[t1], 7\n\t"
        "s_lshl_b32 %[t1], %[t1], 18\n\t"
        "s_add_u32 %[t0], %[t1], 0\n\t"
        "buffer_load_dwordx4 a[0:3], v110, %[so], %[t0] offen\n\t"
        "s_sleep 2\n\t"
        "s_add_u32 %[t0], %[t1], 16384\n\t"
        "buffer_load_dwordx4 a[4:7], v110, %[so], %[t0] offen\n\t"
        "s_sleep 2\n\t"
        "s_add_u32 %[t0], %[t1], 32768\n\t"
        "buffer_load_dwordx4 a[8:11], v110, %[so], %[t0] offen\n\t"
        "s_sleep 2\n\t"
        "s_add_u32 %[t0], %[t1], 49152\n\t"
        "buffer_load_dwordx4 a[12:15], v110, %[so], %[t0] offen\n\t"
        "s_sleep 2\n\t"
        "s_add_u32 %[t0], %[t1], 65536\n\t"
        "buffer_load_dwordx4 a[16:19], v110, %[so], %[t0] offen\n\t"
        "s_sleep 2\n\t"
        "s_add_u32 %[t0], %[t1], 81920\n\t"
        "buffer_load_dwordx4 a[20:23], v110, %[so], %[t0] offen\n\t"
        "s_sleep 2\n\t"
        "s_add_u32 %[t0], %[t1], 98304\n\t"
        "buffer_load_dwordx4 v[24:27], v110, %[so], %[t0] offen\n\t"
        "s_sleep 2\n\t"
        "s_add_u32 %[t0], %[t1], 114688\n\t"
        "buffer_load_dwordx4 v[28:31], v110, %[so], %[t0] offen\n\t"
        "s_sleep 2\n\t"
        "s_add_u32 %[t1], %[ro], 0\n\t"
        "s_and_b32 %[t1], %[t1], 7\n\t"
        "s_lshl_b32 %[t1], %[t1], 18\n\t"
        "s_add_u32 %[t0], %[t1], 131072\n\t"
        "buffer_load_dwordx4 a[24:27], v110, %[so], %[t0] offen\n\t"
        "s_sleep 2\n\t"
        "s_add_u32 %[t0], %[t1], 147456\n\t"
        "buffer_load_dwordx4 a[28:31], v110, %[so], %[t0] offen\n\t"
        "s_sleep 2\n\t"
        "s_add_u32 %[t0], %[t1], 163840\n\t"
        "buffer_load_dwordx4 a[32:35], v110, %[so], %[t0] offen\n\t"
        "s_sleep 2\n\t"
        "s_add_u32 %[t0], %[t1], 180224\n\t"
        "buffer_load_dwordx4 a[36:39], v110, %[so], %[t0] offen\n\t"
        "s_sleep 2\n\t"
        "s_add_u32 %[t0], %[t1], 196608\n\t"
        "buffer_load_dwordx4 a[40:43], v110, %[so], %[t0] offen\n\t"
        "s_sleep 2\n\t"
        "s_add_u32 %[t0], %[t1], 212992\n\t"
        "buffer_load_dwordx4 a[44:47], v110, %[so], %[t0] offen\n\t"
        "s_sleep 2\n\t"
        "s_add_u32 %[t0], %[t1], 229376\n\t"
        "buffer_load_dwordx4 v[32:35], v110, %[so], %[t0] offen\n\t"
        "s_sleep 2\n\t"
        "s_add_u32 %[t0], %[t1], 245760\n\t"
        "buffer_load_dwordx4 v[36:39], v110, %[so], %[t0] offen\n\t"
        "s_sleep 2\n\t"
        "s_waitcnt vmcnt(8)\n\t"
        "ds_write_b128 v111, a[0:3] offset:0\n\t"
        "ds_write_b128 v111, a[4:7] offset:1024\n\t"
        "ds_write_b128 v111, a[8:11] offset:2048\n\t"
        "ds_write_b128 v111, a[12:15] offset:3072\n\t"
        "ds_write_b128 v111, a[16:19] offset:4096\n\t"
        "ds_write_b128 v111, a[20:23] offset:5120\n\t"
        "ds_write_b128 v111, v[24:27] offset:6144\n\t"
        "ds_write_b128 v111, v[28:31] offset:7168\n\t"
        "ds_read_b64_tr_b16 a[0:1], v112 offset:0\n\t"
        "ds_read_b64_tr_b16 a[2:3], v112 offset:2048\n\t"
        "ds_read_b64_tr_b16 a[12:13], v112 offset:4096\n\t"
        "ds_read_b64_tr_b16 a[14:15], v112 offset:6144\n\t"
        "s_waitcnt lgkmcnt(8)\n\t"
        "ds_read_b64_tr_b16 a[4:5], v113 offset:0\n\t"
        "ds_read_b64_tr_b16 a[6:7], v113 offset:2048\n\t"
        "ds_read_b64_tr_b16 a[16:17], v113 offset:4096\n\t"
        "ds_read_b64_tr_b16 a[18:19], v113 offset:6144\n\t"
        "s_waitcnt lgkmcnt(8)\n\t"
        "ds_read_b64_tr_b16 a[8:9], v114 offset:0\n\t"
        "ds_read_b64_tr_b16 a[10:11], v114 offset:2048\n\t"
        "ds_read_b64_tr_b16 a[20:21], v114 offset:4096\n\t"
        "ds_read_b64_tr_b16 a[22:23], v114 offset:6144\n\t"
        "s_waitcnt lgkmcnt(8)\n\t"
        "ds_read_b64_tr_b16 v[56:57], v115 offset:0\n\t"
        "ds_read_b64_tr_b16 v[58:59], v115 offset:2048\n\t"
        "ds_read_b64_tr_b16 v[60:61], v115 offset:4096\n\t"
        "ds_read_b64_tr_b16 v[62:63], v115 offset:6144\n\t"
        "s_add_u32 %[t1], %[ro], 1\n\t"
        "s_and_b32 %[t1], %[t1], 7\n\t"
        "s_lshl_b32 %[t1], %[t1], 18\n\t"
        "s_add_u32 %[t0], %[t1], 0\n\t"
        "buffer_load_dwordx4 a[48:51], v110, %[so], %[t0] offen\n\t"
        "s_sleep 2\n\t"
        "s_add_u32 %[t0], %[t1], 16384\n\t"
        "buffer_load_dwordx4 a[52:55], v110, %[so], %[t0] offen\n\t"
        "s_sleep 2\n\t"
        "s_add_u32 %[t0], %[t1], 32768\n\t"
        "buffer_load_dwordx4 a[56:59], v110, %[so], %[t0] offen\n\t"
        "s_sleep 2\n\t"
        "s_add_u32 %[t0], %[t1], 49152\n\t"
        "buffer_load_dwordx4 a[60:63], v110, %[so], %[t0] offen\n\t"
        "s_sleep 2\n\t"
        "s_add_u32 %[t0], %[t1], 65536\n\t"
        "buffer_load_dwordx4 a[64:67], v110, %[so], %[t0] offen\n\t"
        "s_sleep 2\n\t"
        "s_add_u32 %[t0], %[t1], 81920\n\t"
        "buffer_load_dwordx4 a[68:71], v110, %[so], %[t0] offen\n\t"
        "s_sleep 2\n\t"
        "s_add_u32 %[t0], %[t1], 98304\n\t"
        "buffer_load_dwordx4 v[40:43], v110, %[so], %[t0] offen\n\t"
        "s_sleep 2\n\t"
        "s_add_u32 %[t0], %[t1], 114688\n\t"
        "buffer_load_dwordx4 v[44:47], v110, %[so], %[t0] offen\n\t"
        "s_sleep 2\n\t"
        "s_waitcnt lgkmcnt(0)\n\t"
        "ds_write_b128 v116, v[56:59] offset:0\n\t"
        "ds_write_b128 v116, v[60:63] offset:1024\n\t"
        "s_waitcnt vmcnt(8)\n\t"
        "ds_write_b128 v111, a[24:27] offset:0\n\t"
        "ds_write_b128 v111, a[28:31] offset:1024\n\t"
        "ds_write_b128 v111, a[32:35] offset:2048\n\t"
        "ds_write_b128 v111, a[36:39] offset:3072\n\t"
        "ds_write_b128 v111, a[40:43] offset:4096\n\t"
        "ds_write_b128 v111, a[44:47] offset:5120\n\t"
        "ds_write_b128 v111, v[32:35] offset:6144\n\t"
        "ds_write_b128 v111, v[36:39] offset:7168\n\t"
        "ds_read_b64_tr_b16 a[24:25], v112 offset:0\n\t"
        "ds_read_b64_tr_b16 a[26:27], v112 offset:2048\n\t"
        "ds_read_b64_tr_b16 a[36:37], v112 offset:4096\n\t"
        "ds_read_b64_tr_b16 a[38:39], v112 offset:6144\n\t"
        "s_waitcnt lgkmcnt(8)\n\t"
        "ds_read_b64_tr_b16 a[28:29], v113 offset:0\n\t"
        "ds_read_b64_tr_b16 a[30:31], v113 offset:2048\n\t"
        "ds_read_b64_tr_b16 a[40:41], v113 offset:4096\n\t"
        "ds_read_b64_tr_b16 a[42:43], v113 offset:6144\n\t"
        "s_waitcnt lgkmcnt(8)\n\t"
        "ds_read_b64_tr_b16 a[32:33], v114 offset:0\n\t"
        "ds_read_b64_tr_b16 a[34:35], v114 offset:2048\n\t"
        "ds_read_b64_tr_b16 a[44:45], v114 offset:4096\n\t"
        "ds_read_b64_tr_b16 a[46:47], v114 offset:6144\n\t"
        "s_waitcnt lgkmcnt(8)\n\t"
        "ds_read_b64_tr_b16 v[56:57], v115 offset:0\n\t"
        "ds_read_b64_tr_b16 v[58:59], v115 offset:2048\n\t"
        "ds_read_b64_tr_b16 v[60:61], v115 offset:4096\n\t"
        "ds_read_b64_tr_b16 v[62:63], v115 offset:6144\n\t"
        "s_add_u32 %[t1], %[ro], 1\n\t"
        "s_and_b32 %[t1], %[t1], 7\n\t"
        "s_lshl_b32 %[t1], %[t1], 18\n\t"
        "s_add_u32 %[t0], %[t1], 131072\n\t"
        "buffer_load_dwordx4 a[72:75], v110, %[so], %[t0] offen\n\t"
        "s_sleep 2\n\t"
        "s_add_u32 %[t0], %[t1], 147456\n\t"
        "buffer_load_dwordx4 a[76:79], v110, %[so], %[t0] offen\n\t"
        "s_sleep 2\n\t"
        "s_add_u32 %[t0], %[t1], 163840\n\t"
        "buffer_load_dwordx4 a[80:83], v110, %[so], %[t0] offen\n\t"
        "s_sleep 2\n\t"
        "s_add_u32 %[t0], %[t1], 180224\n\t"
        "buffer_load_dwordx4 a[84:87], v110, %[so], %[t0] offen\n\t"
        "s_sleep 2\n\t"
        "s_add_u32 %[t0], %[t1], 196608\n\t"
        "buffer_load_dwordx4 a[88:91], v110, %[so], %[t0] offen\n\t"
        "s_sleep 2\n\t"
        "s_add_u32 %[t0], %[t1], 212992\n\t"
        "buffer_load_dwordx4 a[92:95], v110, %[so], %[t0] offen\n\t"
        "s_sleep 2\n\t"
        "s_add_u32 %[t0], %[t1], 229376\n\t"
        "buffer_load_dwordx4 v[48:51], v110, %[so], %[t0] offen\n\t"
        "s_sleep 2\n\t"
        "s_add_u32 %[t0], %[t1], 245760\n\t"
        "buffer_load_dwordx4 v[52:55], v110, %[so], %[t0] offen\n\t"
        "s_sleep 2\n\t"
        "s_waitcnt lgkmcnt(0)\n\t"
        "ds_write_b128 v116, v[56:59] offset:2048\n\t"
        "ds_write_b128 v116, v[60:63] offset:3072\n\t"
        "s_waitcnt vmcnt(8)\n\t"
        "ds_write_b128 v111, a[48:51] offset:0\n\t"
        "ds_write_b128 v111, a[52:55] offset:1024\n\t"
        "ds_write_b128 v111, a[56:59] offset:2048\n\t"
        "ds_write_b128 v111, a[60:63] offset:3072\n\t"
        "ds_write_b128 v111, a[64:67] offset:4096\n\t"
        "ds_write_b128 v111, a[68:71] offset:5120\n\t"
        "ds_write_b128 v111, v[40:43] offset:6144\n\t"
        "ds_write_b128 v111, v[44:47] offset:7168\n\t"
        "ds_read_b64_tr_b16 a[48:49], v112 offset:0\n\t"
        "ds_read_b64_tr_b16 a[50:51], v112 offset:2048\n\t"
        "ds_read_b64_tr_b16 a[60:61], v112 offset:4096\n\t"
        "ds_read_b64_tr_b16 a[62:63], v112 offset:6144\n\t"
        "s_waitcnt lgkmcnt(8)\n\t"
        "ds_read_b64_tr_b16 a[52:53], v113 offset:0\n\t"
        "ds_read_b64_tr_b16 a[54:55], v113 offset:2048\n\t"
        "ds_read_b64_tr_b16 a[64:65], v113 offset:4096\n\t"
        "ds_read_b64_tr_b16 a[66:67], v113 offset:6144\n\t"
        "s_waitcnt lgkmcnt(8)\n\t"
        "ds_read_b64_tr_b16 a[56:57], v114 offset:0\n\t"
        "ds_read_b64_tr_b16 a[58:59], v114 offset:2048\n\t"
        "ds_read_b64_tr_b16 a[68:69], v114 offset:4096\n\t"
        "ds_read_b64_tr_b16 a[70:71], v114 offset:6144\n\t"
        "s_waitcnt lgkmcnt(8)\n\t"
        "ds_read_b64_tr_b16 v[56:57], v115 offset:0\n\t"
        "ds_read_b64_tr_b16 v[58:59], v115 offset:2048\n\t"
        "ds_read_b64_tr_b16 v[60:61], v115 offset:4096\n\t"
        "ds_read_b64_tr_b16 v[62:63], v115 offset:6144\n\t"
        "s_add_u32 %[t1], %[ro], 2\n\t"
        "s_and_b32 %[t1], %[t1], 7\n\t"
        "s_lshl_b32 %[t1], %[t1], 18\n\t"
        "s_add_u32 %[t0], %[t1], 0\n\t"
        "buffer_load_dwordx4 a[96:99], v110, %[so], %[t0] offen\n\t"
        "s_sleep 2\n\t"
        "s_add_u32 %[t0], %[t1], 16384\n\t"
        "buffer_load_dwordx4 a[100:103], v110, %[so], %[t0] offen\n\t"
        "s_sleep 2\n\t"
        "s_add_u32 %[t0], %[t1], 32768\n\t"
        "buffer_load_dwordx4 a[104:107], v110, %[so], %[t0] offen\n\t"
        "s_sleep 2\n\t"
        "s_add_u32 %[t0], %[t1], 49152\n\t"
        "buffer_load_dwordx4 a[108:111], v110, %[so], %[t0] offen\n\t"
        "s_sleep 2\n\t"
        "s_add_u32 %[t0], %[t1], 65536\n\t"
        "buffer_load_dwordx4 a[112:115], v110, %[so], %[t0] offen\n\t"
        "s_sleep 2\n\t"
        "s_add_u32 %[t0], %[t1], 81920\n\t"
        "buffer_load_dwordx4 a[116:119], v110, %[so], %[t0] offen\n\t"
        "s_sleep 2\n\t"
        "s_add_u32 %[t0], %[t1], 98304\n\t"
        "buffer_load_dwordx4 v[24:27], v110, %[so], %[t0] offen\n\t"
        "s_sleep 2\n\t"
        "s_add_u32 %[t0], %[t1], 114688\n\t"
        "buffer_load_dwordx4 v[28:31], v110, %[so], %[t0] offen\n\t"
        "s_sleep 2\n\t"
        "s_waitcnt lgkmcnt(0)\n\t"
        "ds_write_b128 v116, v[56:59] offset:4096\n\t"
        "ds_write_b128 v116, v[60:63] offset:5120\n\t"
        "s_waitcnt vmcnt(8)\n\t"
        "ds_write_b128 v111, a[72:75] offset:0\n\t"
        "ds_write_b128 v111, a[76:79] offset:1024\n\t"
        "ds_write_b128 v111, a[80:83] offset:2048\n\t"
        "ds_write_b128 v111, a[84:87] offset:3072\n\t"
        "ds_write_b128 v111, a[88:91] offset:4096\n\t"
        "ds_write_b128 v111, a[92:95] offset:5120\n\t"
        "ds_write_b128 v111, v[48:51] offset:6144\n\t"
        "ds_write_b128 v111, v[52:55] offset:7168\n\t"
        "ds_read_b64_tr_b16 a[72:73], v112 offset:0\n\t"
        "ds_read_b64_tr_b16 a[74:75], v112 offset:2048\n\t"
        "ds_read_b64_tr_b16 a[84:85], v112 offset:4096\n\t"
        "ds_read_b64_tr_b16 a[86:87], v112 offset:6144\n\t"
        "s_waitcnt lgkmcnt(8)\n\t"
        "ds_read_b64_tr_b16 a[76:77], v113 offset:0\n\t"
        "ds_read_b64_tr_b16 a[78:79], v113 offset:2048\n\t"
        "ds_read_b64_tr_b16 a[88:89], v113 offset:4096\n\t"
        "ds_read_b64_tr_b16 a[90:91], v113 offset:6144\n\t"
        "s_waitcnt lgkmcnt(8)\n\t"
        "ds_read_b64_tr_b16 a[80:81], v114 offset:0\n\t"
        "ds_read_b64_tr_b16 a[82:83], v114 offset:2048\n\t"
        "ds_read_b64_tr_b16 a[92:93], v114 offset:4096\n\t"
        "ds_read_b64_tr_b16 a[94:95], v114 offset:6144\n\t"
        "s_waitcnt lgkmcnt(8)\n\t"
        "ds_read_b64_tr_b16 v[56:57], v115 offset:0\n\t"
        "ds_read_b64_tr_b16 v[58:59], v115 offset:2048\n\t"
        "ds_read_b64_tr_b16 v[60:61], v115 offset:4096\n\t"
        "ds_read_b64_tr_b16 v[62:63], v115 offset:6144\n\t"
        "s_add_u32 %[t1], %[ro], 2\n\t"
        "s_and_b32 %[t1], %[t1], 7\n\t"
        "s_lshl_b32 %[t1], %[t1], 18\n\t"
        "s_add_u32 %[t0], %[t1], 131072\n\t"
        "buffer_load_dwordx4 a[120:123], v110, %[so], %[t0] offen\n\t"
        "s_sleep 2\n\t"
        "s_add_u32 %[t0], %[t1], 147456\n\t"
        "buffer_load_dwordx4 a[124:127], v110, %[so], %[t0] offen\n\t"
        "s_sleep 2\n\t"
        "s_add_u32 %[t0], %[t1], 163840\n\t"
        "buffer_load_dwordx4 a[128:131], v110, %[so], %[t0] offen\n\t"
        "s_sleep 2\n\t"
        "s_add_u32 %[t0], %[t1], 180224\n\t"
        "buffer_load_dwordx4 a[132:135], v110, %[so], %[t0] offen\n\t"
        "s_sleep 2\n\t"
        "s_add_u32 %[t0], %[t1], 196608\n\t"
        "buffer_load_dwordx4 a[136:139], v110, %[so], %[t0] offen\n\t"
        "s_sleep 2\n\t"
        "s_add_u32 %[t0], %[t1], 212992\n\t"
        "buffer_load_dwordx4 a[140:143], v110, %[so], %[t0] offen\n\t"
        "s_sleep 2\n\t"
        "s_add_u32 %[t0], %[t1], 229376\n\t"
        "buffer_load_dwordx4 v[32:35], v110, %[so], %[t0] offen\n\t"
        "s_sleep 2\n\t"
        "s_add_u32 %[t0], %[t1], 245760\n\t"
        "buffer_load_dwordx4 v[36:39], v110, %[so], %[t0] offen\n\t"
        "s_sleep 2\n\t"
        "s_waitcnt lgkmcnt(0)\n\t"
        "ds_write_b128 v116, v[56:59] offset:6144\n\t"
        "ds_write_b128 v116, v[60:63] offset:7168\n\t"
        "s_waitcnt vmcnt(8)\n\t"
        "ds_write_b128 v111, a[96:99] offset:0\n\t"
        "ds_write_b128 v111, a[100:103] offset:1024\n\t"
        "ds_write_b128 v111, a[104:107] offset:2048\n\t"
        "ds_write_b128 v111, a[108:111] offset:3072\n\t"
        "ds_write_b128 v111, a[112:115] offset:4096\n\t"
        "ds_write_b128 v111, a[116:119] offset:5120\n\t"
        "ds_write_b128 v111, v[24:27] offset:6144\n\t"
        "ds_write_b128 v111, v[28:31] offset:7168\n\t"
        "ds_read_b64_tr_b16 a[96:97], v112 offset:0\n\t"
        "ds_read_b64_tr_b16 a[98:99], v112 offset:2048\n\t"
        "ds_read_b64_tr_b16 a[108:109], v112 offset:4096\n\t"
        "ds_read_b64_tr_b16 a[110:111], v112 offset:6144\n\t"
        "s_waitcnt lgkmcnt(8)\n\t"
        "ds_read_b64_tr_b16 a[100:101], v113 offset:0\n\t"
        "ds_read_b64_tr_b16 a[102:103], v113 offset:2048\n\t"
        "ds_read_b64_tr_b16 a[112:113], v113 offset:4096\n\t"
        "ds_read_b64_tr_b16 a[114:115], v113 offset:6144\n\t"
        "s_waitcnt lgkmcnt(8)\n\t"
        "ds_read_b64_tr_b16 a[104:105], v114 offset:0\n\t"
        "ds_read_b64_tr_b16 a[106:107], v114 offset:2048\n\t"
        "ds_read_b64_tr_b16 a[116:117], v114 offset:4096\n\t"
        "ds_read_b64_tr_b16 a[118:119], v114 offset:6144\n\t"
        "s_waitcnt lgkmcnt(8)\n\t"
        "ds_read_b64_tr_b16 v[56:57], v115 offset:0\n\t"
        "ds_read_b64_tr_b16 v[58:59], v115 offset:2048\n\t"
        "ds_read_b64_tr_b16 v[60:61], v115 offset:4096\n\t"
        "ds_read_b64_tr_b16 v[62:63], v115 offset:6144\n\t"
        "s_add_u32 %[t1], %[ro], 3\n\t"
        "s_and_b32 %[t1], %[t1], 7\n\t"
        "s_lshl_b32 %[t1], %[t1], 18\n\t"
        "s_add_u32 %[t0], %[t1], 0\n\t"
        "buffer_load_dwordx4 a[144:147], v110, %[so], %[t0] offen\n\t"
        "s_sleep 2\n\t"
        "s_add_u32 %[t0], %[t1], 16384\n\t"
        "buffer_load_dwordx4 a[148:151], v110, %[so], %[t0] offen\n\t"
        "s_sleep 2\n\t"
        "s_add_u32 %[t0], %[t1], 32768\n\t"
        "buffer_load_dwordx4 a[152:155], v110, %[so], %[t0] offen\n\t"
        "s_sleep 2\n\t"
        "s_add_u32 %[t0], %[t1], 49152\n\t"
        "buffer_load_dwordx4 a[156:159], v110, %[so], %[t0] offen\n\t"
        "s_sleep 2\n\t"
        "s_add_u32 %[t0], %[t1], 65536\n\t"
        "buffer_load_dwordx4 a[160:163], v110, %[so], %[t0] offen\n\t"
        "s_sleep 2\n\t"
        "s_add_u32 %[t0], %[t1], 81920\n\t"
        "buffer_load_dwordx4 a[164:167], v110, %[so], %[t0] offen\n\t"
        "s_sleep 2\n\t"
        "s_add_u32 %[t0], %[t1], 98304\n\t"
        "buffer_load_dwordx4 v[40:43], v110, %[so], %[t0] offen\n\t"
        "s_sleep 2\n\t"
        "s_add_u32 %[t0], %[t1], 114688\n\t"
        "buffer_load_dwordx4 v[44:47], v110, %[so], %[t0] offen\n\t"
        "s_sleep 2\n\t"
        "s_waitcnt lgkmcnt(0)\n\t"
        "ds_write_b128 v116, v[56:59] offset:8192\n\t"
        "ds_write_b128 v116, v[60:63] offset:9216\n\t"
        "s_waitcnt vmcnt(8)\n\t"
        "ds_write_b128 v111, a[120:123] offset:0\n\t"
        "ds_write_b128 v111, a[124:127] offset:1024\n\t"
        "ds_write_b128 v111, a[128:131] offset:2048\n\t"
        "ds_write_b128 v111, a[132:135] offset:3072\n\t"
        "ds_write_b128 v111, a[136:139] offset:4096\n\t"
        "ds_write_b128 v111, a[140:143] offset:5120\n\t"
        "ds_write_b128 v111, v[32:35] offset:6144\n\t"
        "ds_write_b128 v111, v[36:39] offset:7168\n\t"
        "ds_read_b64_tr_b16 a[120:121], v112 offset:0\n\t"
        "ds_read_b64_tr_b16 a[122:123], v112 offset:2048\n\t"
        "ds_read_b64_tr_b16 a[132:133], v112 offset:4096\n\t"
        "ds_read_b64_tr_b16 a[134:135], v112 offset:6144\n\t"
        "s_waitcnt lgkmcnt(8)\n\t"
        "ds_read_b64_tr_b16 a[124:125], v113 offset:0\n\t"
        "ds_read_b64_tr_b16 a[126:127], v113 offset:2048\n\t"
        "ds_read_b64_tr_b16 a[136:137], v113 offset:4096\n\t"
        "ds_read_b64_tr_b16 a[138:139], v113 offset:6144\n\t"
        "s_waitcnt lgkmcnt(8)\n\t"
        "ds_read_b64_tr_b16 a[128:129], v114 offset:0\n\t"
        "ds_read_b64_tr_b16 a[130:131], v114 offset:2048\n\t"
        "ds_read_b64_tr_b16 a[140:141], v114 offset:4096\n\t"
        "ds_read_b64_tr_b16 a[142:143], v114 offset:6144\n\t"
        "s_waitcnt lgkmcnt(8)\n\t"
        "ds_read_b64_tr_b16 v[56:57], v115 offset:0\n\t"
        "ds_read_b64_tr_b16 v[58:59], v115 offset:2048\n\t"
        "ds_read_b64_tr_b16 v[60:61], v115 offset:4096\n\t"
        "ds_read_b64_tr_b16 v[62:63], v115 offset:6144\n\t"
        "s_add_u32 %[t1], %[ro], 3\n\t"
        "s_and_b32 %[t1], %[t1], 7\n\t"
        "s_lshl_b32 %[t1], %[t1], 18\n\t"
        "s_add_u32 %[t0], %[t1], 131072\n\t"
        "buffer_load_dwordx4 a[168:171], v110, %[so], %[t0] offen\n\t"
        "s_sleep 2\n\t"
        "s_add_u32 %[t0], %[t1], 147456\n\t"
        "buffer_load_dwordx4 a[172:175], v110, %[so], %[t0] offen\n\t"
        "s_sleep 2\n\t"
        "s_add_u32 %[t0], %[t1], 163840\n\t"
        "buffer_load_dwordx4 a[176:179], v110, %[so], %[t0] offen\n\t"
        "s_sleep 2\n\t"
        "s_add_u32 %[t0], %[t1], 180224\n\t"
        "buffer_load_dwordx4 a[180:183], v110, %[so], %[t0] offen\n\t"
        "s_sleep 2\n\t"
        "s_add_u32 %[t0], %[t1], 196608\n\t"
        "buffer_load_dwordx4 a[184:187], v110, %[so], %[t0] offen\n\t"
        "s_sleep 2\n\t"
        "s_add_u32 %[t0], %[t1], 212992\n\t"
        "buffer_load_dwordx4 a[188:191], v110, %[so], %[t0] offen\n\t"
        "s_sleep 2\n\t"
        "s_add_u32 %[t0], %[t1], 229376\n\t"
        "buffer_load_dwordx4 v[48:51], v110, %[so], %[t0] offen\n\t"
        "s_sleep 2\n\t"
        "s_add_u32 %[t0], %[t1], 245760\n\t"
        "buffer_load_dwordx4 v[52:55], v110, %[so], %[t0] offen\n\t"
        "s_sleep 2\n\t"
        "s_waitcnt lgkmcnt(0)\n\t"
        "ds_write_b128 v116, v[56:59] offset:10240\n\t"
        "ds_write_b128 v116, v[60:63] offset:11264\n\t"
        "s_waitcnt vmcnt(8)\n\t"
        "ds_write_b128 v111, a[144:147] offset:0\n\t"
        "ds_write_b128 v111, a[148:151] offset:1024\n\t"
        "ds_write_b128 v111, a[152:155] offset:2048\n\t"
        "ds_write_b128 v111, a[156:159] offset:3072\n\t"
        "ds_write_b128 v111, a[160:163] offset:4096\n\t"
        "ds_write_b128 v111, a[164:167] offset:5120\n\t"
        "ds_write_b128 v111, v[40:43] offset:6144\n\t"
        "ds_write_b128 v111, v[44:47] offset:7168\n\t"
        "ds_read_b64_tr_b16 a[144:145], v112 offset:0\n\t"
        "ds_read_b64_tr_b16 a[146:147], v112 offset:2048\n\t"
        "ds_read_b64_tr_b16 a[156:157], v112 offset:4096\n\t"
        "ds_read_b64_tr_b16 a[158:159], v112 offset:6144\n\t"
        "s_waitcnt lgkmcnt(8)\n\t"
        "ds_read_b64_tr_b16 a[148:149], v113 offset:0\n\t"
        "ds_read_b64_tr_b16 a[150:151], v113 offset:2048\n\t"
        "ds_read_b64_tr_b16 a[160:161], v113 offset:4096\n\t"
        "ds_read_b64_tr_b16 a[162:163], v113 offset:6144\n\t"
        "s_waitcnt lgkmcnt(8)\n\t"
        "ds_read_b64_tr_b16 a[152:153], v114 offset:0\n\t"
        "ds_read_b64_tr_b16 a[154:155], v114 offset:2048\n\t"
        "ds_read_b64_tr_b16 a[164:165], v114 offset:4096\n\t"
        "ds_read_b64_tr_b16 a[166:167], v114 offset:6144\n\t"
        "s_waitcnt lgkmcnt(8)\n\t"
        "ds_read_b64_tr_b16 v[56:57], v115 offset:0\n\t"
        "ds_read_b64_tr_b16 v[58:59], v115 offset:2048\n\t"
        "ds_read_b64_tr_b16 v[60:61], v115 offset:4096\n\t"
        "ds_read_b64_tr_b16 v[62:63], v115 offset:6144\n\t"
        "s_add_u32 %[t1], %[ro], 4\n\t"
        "s_and_b32 %[t1], %[t1], 7\n\t"
        "s_lshl_b32 %[t1], %[t1], 18\n\t"
        "s_add_u32 %[t0], %[t1], 0\n\t"
        "buffer_load_dwordx4 a[192:195], v110, %[so], %[t0] offen\n\t"
        "s_sleep 2\n\t"
        "s_add_u32 %[t0], %[t1], 16384\n\t"
        "buffer_load_dwordx4 a[196:199], v110, %[so], %[t0] offen\n\t"
        "s_sleep 2\n\t"
        "s_add_u32 %[t0], %[t1], 32768\n\t"
        "buffer_load_dwordx4 a[200:203], v110, %[so], %[t0] offen\n\t"
        "s_sleep 2\n\t"
        "s_add_u32 %[t0], %[t1], 49152\n\t"
        "buffer_load_dwordx4 a[204:207], v110, %[so], %[t0] offen\n\t"
        "s_sleep 2\n\t"
        "s_add_u32 %[t0], %[t1], 65536\n\t"
        "buffer_load_dwordx4 a[208:211], v110, %[so], %[t0] offen\n\t"
        "s_sleep 2\n\t"
        "s_add_u32 %[t0], %[t1], 81920\n\t"
        "buffer_load_dwordx4 a[212:215], v110, %[so], %[t0] offen\n\t"
        "s_sleep 2\n\t"
        "s_add_u32 %[t0], %[t1], 98304\n\t"
        "buffer_load_dwordx4 v[24:27], v110, %[so], %[t0] offen\n\t"
        "s_sleep 2\n\t"
        "s_add_u32 %[t0], %[t1], 114688\n\t"
        "buffer_load_dwordx4 v[28:31], v110, %[so], %[t0] offen\n\t"
        "s_sleep 2\n\t"
        "s_waitcnt lgkmcnt(0)\n\t"
        "ds_write_b128 v116, v[56:59] offset:12288\n\t"
        "ds_write_b128 v116, v[60:63] offset:13312\n\t"
        "s_waitcnt vmcnt(8)\n\t"
        "ds_write_b128 v111, a[168:171] offset:0\n\t"
        "ds_write_b128 v111, a[172:175] offset:1024\n\t"
        "ds_write_b128 v111, a[176:179] offset:2048\n\t"
        "ds_write_b128 v111, a[180:183] offset:3072\n\t"
        "ds_write_b128 v111, a[184:187] offset:4096\n\t"
        "ds_write_b128 v111, a[188:191] offset:5120\n\t"
        "ds_write_b128 v111, v[48:51] offset:6144\n\t"
        "ds_write_b128 v111, v[52:55] offset:7168\n\t"
        "ds_read_b64_tr_b16 a[168:169], v112 offset:0\n\t"
        "ds_read_b64_tr_b16 a[170:171], v112 offset:2048\n\t"
        "ds_read_b64_tr_b16 a[180:181], v112 offset:4096\n\t"
        "ds_read_b64_tr_b16 a[182:183], v112 offset:6144\n\t"
        "s_waitcnt lgkmcnt(8)\n\t"
        "ds_read_b64_tr_b16 a[172:173], v113 offset:0\n\t"
        "ds_read_b64_tr_b16 a[174:175], v113 offset:2048\n\t"
        "ds_read_b64_tr_b16 a[184:185], v113 offset:4096\n\t"
        "ds_read_b64_tr_b16 a[186:187], v113 offset:6144\n\t"
        "s_waitcnt lgkmcnt(8)\n\t"
        "ds_read_b64_tr_b16 a[176:177], v114 offset:0\n\t"
        "ds_read_b64_tr_b16 a[178:179], v114 offset:2048\n\t"
        "ds_read_b64_tr_b16 a[188:189], v114 offset:4096\n\t"
        "ds_read_b64_tr_b16 a[190:191], v114 offset:6144\n\t"
        "s_waitcnt lgkmcnt(8)\n\t"
        "ds_read_b64_tr_b16 v[56:57], v115 offset:0\n\t"
        "ds_read_b64_tr_b16 v[58:59], v115 offset:2048\n\t"
        "ds_read_b64_tr_b16 v[60:61], v115 offset:4096\n\t"
        "ds_read_b64_tr_b16 v[62:63], v115 offset:6144\n\t"
        "s_add_u32 %[t1], %[ro], 4\n\t"
        "s_and_b32 %[t1], %[t1], 7\n\t"
        "s_lshl_b32 %[t1], %[t1], 18\n\t"
        "s_add_u32 %[t0], %[t1], 131072\n\t"
        "buffer_load_dwordx4 a[216:219], v110, %[so], %[t0] offen\n\t"
        "s_sleep 2\n\t"
        "s_add_u32 %[t0], %[t1], 147456\n\t"
        "buffer_load_dwordx4 a[220:223], v110, %[so], %[t0] offen\n\t"
        "s_sleep 2\n\t"
        "s_add_u32 %[t0], %[t1], 163840\n\t"
        "buffer_load_dwordx4 a[224:227], v110, %[so], %[t0] offen\n\t"
        "s_sleep 2\n\t"
        "s_add_u32 %[t0], %[t1], 180224\n\t"
        "buffer_load_dwordx4 a[228:231], v110, %[so], %[t0] offen\n\t"
        "s_sleep 2\n\t"
        "s_add_u32 %[t0], %[t1], 196608\n\t"
        "buffer_load_dwordx4 a[232:235], v110, %[so], %[t0] offen\n\t"
        "s_sleep 2\n\t"
        "s_add_u32 %[t0], %[t1], 212992\n\t"
        "buffer_load_dwordx4 a[236:239], v110, %[so], %[t0] offen\n\t"
        "s_sleep 2\n\t"
        "s_add_u32 %[t0], %[t1], 229376\n\t"
        "buffer_load_dwordx4 v[32:35], v110, %[so], %[t0] offen\n\t"
        "s_sleep 2\n\t"
        "s_add_u32 %[t0], %[t1], 245760\n\t"
        "buffer_load_dwordx4 v[36:39], v110, %[so], %[t0] offen\n\t"
        "s_sleep 2\n\t"
        "s_waitcnt lgkmcnt(0)\n\t"
        "ds_write_b128 v116, v[56:59] offset:14336\n\t"
        "ds_write_b128 v116, v[60:63] offset:15360\n\t"
        "s_waitcnt vmcnt(8)\n\t"
        "ds_write_b128 v111, a[192:195] offset:0\n\t"
        "ds_write_b128 v111, a[196:199] offset:1024\n\t"
        "ds_write_b128 v111, a[200:203] offset:2048\n\t"
        "ds_write_b128 v111, a[204:207] offset:3072\n\t"
        "ds_write_b128 v111, a[208:211] offset:4096\n\t"
        "ds_write_b128 v111, a[212:215] offset:5120\n\t"
        "ds_write_b128 v111, v[24:27] offset:6144\n\t"
        "ds_write_b128 v111, v[28:31] offset:7168\n\t"
        "ds_read_b64_tr_b16 a[192:193], v112 offset:0\n\t"
        "ds_read_b64_tr_b16 a[194:195], v112 offset:2048\n\t"
        "ds_read_b64_tr_b16 a[204:205], v112 offset:4096\n\t"
        "ds_read_b64_tr_b16 a[206:207], v112 offset:6144\n\t"
        "s_waitcnt lgkmcnt(8)\n\t"
        "ds_read_b64_tr_b16 a[196:197], v113 offset:0\n\t"
        "ds_read_b64_tr_b16 a[198:199], v113 offset:2048\n\t"
        "ds_read_b64_tr_b16 a[208:209], v113 offset:4096\n\t"
        "ds_read_b64_tr_b16 a[210:211], v113 offset:6144\n\t"
        "s_waitcnt lgkmcnt(8)\n\t"
        "ds_read_b64_tr_b16 a[200:201], v114 offset:0\n\t"
        "ds_read_b64_tr_b16 a[202:203], v114 offset:2048\n\t"
        "ds_read_b64_tr_b16 a[212:213], v114 offset:4096\n\t"
        "ds_read_b64_tr_b16 a[214:215], v114 offset:6144\n\t"
        "s_waitcnt lgkmcnt(8)\n\t"
        "ds_read_b64_tr_b16 v[56:57], v115 offset:0\n\t"
        "ds_read_b64_tr_b16 v[58:59], v115 offset:2048\n\t"
        "ds_read_b64_tr_b16 v[60:61], v115 offset:4096\n\t"
        "ds_read_b64_tr_b16 v[62:63], v115 offset:6144\n\t"
        "s_add_u32 %[t1], %[ro], 5\n\t"
        "s_and_b32 %[t1], %[t1], 7\n\t"
        "s_lshl_b32 %[t1], %[t1], 18\n\t"
        "s_add_u32 %[t0], %[t1], 0\n\t"
        "buffer_load_dwordx4 a[240:243], v110, %[so], %[t0] offen\n\t"
        "s_sleep 2\n\t"
        "s_add_u32 %[t0], %[t1], 16384\n\t"
        "buffer_load_dwordx4 a[244:247], v110, %[so], %[t0] offen\n\t"
        "s_sleep 2\n\t"
        "s_add_u32 %[t0], %[t1], 32768\n\t"
        "buffer_load_dwordx4 a[248:251], v110, %[so], %[t0] offen\n\t"
        "s_sleep 2\n\t"
        "s_add_u32 %[t0], %[t1], 49152\n\t"
        "buffer_load_dwordx4 a[252:255], v110, %[so], %[t0] offen\n\t"
        "s_sleep 2\n\t"
        "s_add_u32 %[t0], %[t1], 65536\n\t"
        "buffer_load_dwordx4 v[128:131], v110, %[so], %[t0] offen\n\t"
        "s_sleep 2\n\t"
        "s_add_u32 %[t0], %[t1], 81920\n\t"
        "buffer_load_dwordx4 v[132:135], v110, %[so], %[t0] offen\n\t"
        "s_sleep 2\n\t"
        "s_add_u32 %[t0], %[t1], 98304\n\t"
        "buffer_load_dwordx4 v[40:43], v110, %[so], %[t0] offen\n\t"
        "s_sleep 2\n\t"
        "s_add_u32 %[t0], %[t1], 114688\n\t"
        "buffer_load_dwordx4 v[44:47], v110, %[so], %[t0] offen\n\t"
        "s_sleep 2\n\t"
        "s_waitcnt lgkmcnt(0)\n\t"
        "ds_write_b128 v116, v[56:59] offset:16384\n\t"
        "ds_write_b128 v116, v[60:63] offset:17408\n\t"
        "s_waitcnt vmcnt(8)\n\t"
        "ds_write_b128 v111, a[216:219] offset:0\n\t"
        "ds_write_b128 v111, a[220:223] offset:1024\n\t"
        "ds_write_b128 v111, a[224:227] offset:2048\n\t"
        "ds_write_b128 v111, a[228:231] offset:3072\n\t"
        "ds_write_b128 v111, a[232:235] offset:4096\n\t"
        "ds_write_b128 v111, a[236:239] offset:5120\n\t"
        "ds_write_b128 v111, v[32:35] offset:6144\n\t"
        "ds_write_b128 v111, v[36:39] offset:7168\n\t"
        "ds_read_b64_tr_b16 a[216:217], v112 offset:0\n\t"
        "ds_read_b64_tr_b16 a[218:219], v112 offset:2048\n\t"
        "ds_read_b64_tr_b16 a[228:229], v112 offset:4096\n\t"
        "ds_read_b64_tr_b16 a[230:231], v112 offset:6144\n\t"
        "s_waitcnt lgkmcnt(8)\n\t"
        "ds_read_b64_tr_b16 a[220:221], v113 offset:0\n\t"
        "ds_read_b64_tr_b16 a[222:223], v113 offset:2048\n\t"
        "ds_read_b64_tr_b16 a[232:233], v113 offset:4096\n\t"
        "ds_read_b64_tr_b16 a[234:235], v113 offset:6144\n\t"
        "s_waitcnt lgkmcnt(8)\n\t"
        "ds_read_b64_tr_b16 a[224:225], v114 offset:0\n\t"
        "ds_read_b64_tr_b16 a[226:227], v114 offset:2048\n\t"
        "ds_read_b64_tr_b16 a[236:237], v114 offset:4096\n\t"
        "ds_read_b64_tr_b16 a[238:239], v114 offset:6144\n\t"
        "s_waitcnt lgkmcnt(8)\n\t"
        "ds_read_b64_tr_b16 v[56:57], v115 offset:0\n\t"
        "ds_read_b64_tr_b16 v[58:59], v115 offset:2048\n\t"
        "ds_read_b64_tr_b16 v[60:61], v115 offset:4096\n\t"
        "ds_read_b64_tr_b16 v[62:63], v115 offset:6144\n\t"
        "s_add_u32 %[t1], %[ro], 5\n\t"
        "s_and_b32 %[t1], %[t1], 7\n\t"
        "s_lshl_b32 %[t1], %[t1], 18\n\t"
        "s_add_u32 %[t0], %[t1], 131072\n\t"
        "buffer_load_dwordx4 v[136:139], v110, %[so], %[t0] offen\n\t"
        "s_sleep 2\n\t"
        "s_add_u32 %[t0], %[t1], 147456\n\t"
        "buffer_load_dwordx4 v[140:143], v110, %[so], %[t0] offen\n\t"
        "s_sleep 2\n\t"
        "s_add_u32 %[t0], %[t1], 163840\n\t"
        "buffer_load_dwordx4 v[144:147], v110, %[so], %[t0] offen\n\t"
        "s_sleep 2\n\t"
        "s_add_u32 %[t0], %[t1], 180224\n\t"
        "buffer_load_dwordx4 v[148:151], v110, %[so], %[t0] offen\n\t"
        "s_sleep 2\n\t"
        "s_add_u32 %[t0], %[t1], 196608\n\t"
        "buffer_load_dwordx4 v[152:155], v110, %[so], %[t0] offen\n\t"
        "s_sleep 2\n\t"
        "s_add_u32 %[t0], %[t1], 212992\n\t"
        "buffer_load_dwordx4 v[156:159], v110, %[so], %[t0] offen\n\t"
        "s_sleep 2\n\t"
        "s_add_u32 %[t0], %[t1], 229376\n\t"
        "buffer_load_dwordx4 v[48:51], v110, %[so], %[t0] offen\n\t"
        "s_sleep 2\n\t"
        "s_add_u32 %[t0], %[t1], 245760\n\t"
        "buffer_load_dwordx4 v[52:55], v110, %[so], %[t0] offen\n\t"
        "s_sleep 2\n\t"
        "s_waitcnt lgkmcnt(0)\n\t"
        "ds_write_b128 v116, v[56:59] offset:18432\n\t"
        "ds_write_b128 v116, v[60:63] offset:19456\n\t"
        "s_waitcnt vmcnt(8)\n\t"
        "ds_write_b128 v111, a[240:243] offset:0\n\t"
        "ds_write_b128 v111, a[244:247] offset:1024\n\t"
        "ds_write_b128 v111, a[248:251] offset:2048\n\t"
        "ds_write_b128 v111, a[252:255] offset:3072\n\t"
        "ds_write_b128 v111, v[128:131] offset:4096\n\t"
        "ds_write_b128 v111, v[132:135] offset:5120\n\t"
        "ds_write_b128 v111, v[40:43] offset:6144\n\t"
        "ds_write_b128 v111, v[44:47] offset:7168\n\t"
        "ds_read_b64_tr_b16 a[240:241], v112 offset:0\n\t"
        "ds_read_b64_tr_b16 a[242:243], v112 offset:2048\n\t"
        "ds_read_b64_tr_b16 a[252:253], v112 offset:4096\n\t"
        "ds_read_b64_tr_b16 a[254:255], v112 offset:6144\n\t"
        "s_waitcnt lgkmcnt(8)\n\t"
        "ds_read_b64_tr_b16 a[244:245], v113 offset:0\n\t"
        "ds_read_b64_tr_b16 a[246:247], v113 offset:2048\n\t"
        "ds_read_b64_tr_b16 v[128:129], v113 offset:4096\n\t"
        "ds_read_b64_tr_b16 v[130:131], v113 offset:6144\n\t"
        "s_waitcnt lgkmcnt(8)\n\t"
        "ds_read_b64_tr_b16 a[248:249], v114 offset:0\n\t"
        "ds_read_b64_tr_b16 a[250:251], v114 offset:2048\n\t"
        "ds_read_b64_tr_b16 v[132:133], v114 offset:4096\n\t"
        "ds_read_b64_tr_b16 v[134:135], v114 offset:6144\n\t"
        "s_waitcnt lgkmcnt(8)\n\t"
        "ds_read_b64_tr_b16 v[56:57], v115 offset:0\n\t"
        "ds_read_b64_tr_b16 v[58:59], v115 offset:2048\n\t"
        "ds_read_b64_tr_b16 v[60:61], v115 offset:4096\n\t"
        "ds_read_b64_tr_b16 v[62:63], v115 offset:6144\n\t"
        "s_add_u32 %[t1], %[ro], 6\n\t"
        "s_and_b32 %[t1], %[t1], 7\n\t"
        "s_lshl_b32 %[t1], %[t1], 18\n\t"
        "s_add_u32 %[t0], %[t1], 0\n\t"
        "buffer_load_dwordx4 v[160:163], v110, %[so], %[t0] offen\n\t"
        "s_sleep 2\n\t"
        "s_add_u32 %[t0], %[t1], 16384\n\t"
        "buffer_load_dwordx4 v[164:167], v110, %[so], %[t0] offen\n\t"
        "s_sleep 2\n\t"
        "s_add_u32 %[t0], %[t1], 32768\n\t"
        "buffer_load_dwordx4 v[168:171], v110, %[so], %[t0] offen\n\t"
        "s_sleep 2\n\t"
        "s_add_u32 %[t0], %[t1], 49152\n\t"
        "buffer_load_dwordx4 v[172:175], v110, %[so], %[t0] offen\n\t"
        "s_sleep 2\n\t"
        "s_add_u32 %[t0], %[t1], 65536\n\t"
        "buffer_load_dwordx4 v[176:179], v110, %[so], %[t0] offen\n\t"
        "s_sleep 2\n\t"
        "s_add_u32 %[t0], %[t1], 81920\n\t"
        "buffer_load_dwordx4 v[180:183], v110, %[so], %[t0] offen\n\t"
        "s_sleep 2\n\t"
        "s_add_u32 %[t0], %[t1], 98304\n\t"
        "buffer_load_dwordx4 v[24:27], v110, %[so], %[t0] offen\n\t"
        "s_sleep 2\n\t"
        "s_add_u32 %[t0], %[t1], 114688\n\t"
        "buffer_load_dwordx4 v[28:31], v110, %[so], %[t0] offen\n\t"
        "s_sleep 2\n\t"
        "s_waitcnt lgkmcnt(0)\n\t"
        "ds_write_b128 v116, v[56:59] offset:20480\n\t"
        "ds_write_b128 v116, v[60:63] offset:21504\n\t"
        "s_waitcnt vmcnt(8)\n\t"
        "ds_write_b128 v111, v[136:139] offset:0\n\t"
        "ds_write_b128 v111, v[140:143] offset:1024\n\t"
        "ds_write_b128 v111, v[144:147] offset:2048\n\t"
        "ds_write_b128 v111, v[148:151] offset:3072\n\t"
        "ds_write_b128 v111, v[152:155] offset:4096\n\t"
        "ds_write_b128 v111, v[156:159] offset:5120\n\t"
        "ds_write_b128 v111, v[48:51] offset:6144\n\t"
        "ds_write_b128 v111, v[52:55] offset:7168\n\t"
        "ds_read_b64_tr_b16 v[136:137], v112 offset:0\n\t"
        "ds_read_b64_tr_b16 v[138:139], v112 offset:2048\n\t"
        "ds_read_b64_tr_b16 v[148:149], v112 offset:4096\n\t"
        "ds_read_b64_tr_b16 v[150:151], v112 offset:6144\n\t"
        "s_waitcnt lgkmcnt(8)\n\t"
        "ds_read_b64_tr_b16 v[140:141], v113 offset:0\n\t"
        "ds_read_b64_tr_b16 v[142:143], v113 offset:2048\n\t"
        "ds_read_b64_tr_b16 v[152:153], v113 offset:4096\n\t"
        "ds_read_b64_tr_b16 v[154:155], v113 offset:6144\n\t"
        "s_waitcnt lgkmcnt(8)\n\t"
        "ds_read_b64_tr_b16 v[144:145], v114 offset:0\n\t"
        "ds_read_b64_tr_b16 v[146:147], v114 offset:2048\n\t"
        "ds_read_b64_tr_b16 v[156:157], v114 offset:4096\n\t"
        "ds_read_b64_tr_b16 v[158:159], v114 offset:6144\n\t"
        "s_waitcnt lgkmcnt(8)\n\t"
        "ds_read_b64_tr_b16 v[56:57], v115 offset:0\n\t"
        "ds_read_b64_tr_b16 v[58:59], v115 offset:2048\n\t"
        "ds_read_b64_tr_b16 v[60:61], v115 offset:4096\n\t"
        "ds_read_b64_tr_b16 v[62:63], v115 offset:6144\n\t"
        "s_add_u32 %[t1], %[ro], 6\n\t"
        "s_and_b32 %[t1], %[t1], 7\n\t"
        "s_lshl_b32 %[t1], %[t1], 18\n\t"
        "s_add_u32 %[t0], %[t1], 131072\n\t"
        "buffer_load_dwordx4 v[184:187], v110, %[so], %[t0] offen\n\t"
        "s_sleep 2\n\t"
        "s_add_u32 %[t0], %[t1], 147456\n\t"
        "buffer_load_dwordx4 v[188:191], v110, %[so], %[t0] offen\n\t"
        "s_sleep 2\n\t"
        "s_add_u32 %[t0], %[t1], 163840\n\t"
        "buffer_load_dwordx4 v[192:195], v110, %[so], %[t0] offen\n\t"
        "s_sleep 2\n\t"
        "s_add_u32 %[t0], %[t1], 180224\n\t"
        "buffer_load_dwordx4 v[196:199], v110, %[so], %[t0] offen\n\t"
        "s_sleep 2\n\t"
        "s_add_u32 %[t0], %[t1], 196608\n\t"
        "buffer_load_dwordx4 v[200:203], v110, %[so], %[t0] offen\n\t"
        "s_sleep 2\n\t"
        "s_add_u32 %[t0], %[t1], 212992\n\t"
        "buffer_load_dwordx4 v[204:207], v110, %[so], %[t0] offen\n\t"
        "s_sleep 2\n\t"
        "s_add_u32 %[t0], %[t1], 229376\n\t"
        "buffer_load_dwordx4 v[32:35], v110, %[so], %[t0] offen\n\t"
        "s_sleep 2\n\t"
        "s_add_u32 %[t0], %[t1], 245760\n\t"
        "buffer_load_dwordx4 v[36:39], v110, %[so], %[t0] offen\n\t"
        "s_sleep 2\n\t"
        "s_waitcnt lgkmcnt(0)\n\t"
        "ds_write_b128 v116, v[56:59] offset:22528\n\t"
        "ds_write_b128 v116, v[60:63] offset:23552\n\t"
        "s_waitcnt vmcnt(8)\n\t"
        "ds_write_b128 v111, v[160:163] offset:0\n\t"
        "ds_write_b128 v111, v[164:167] offset:1024\n\t"
        "ds_write_b128 v111, v[168:171] offset:2048\n\t"
        "ds_write_b128 v111, v[172:175] offset:3072\n\t"
        "ds_write_b128 v111, v[176:179] offset:4096\n\t"
        "ds_write_b128 v111, v[180:183] offset:5120\n\t"
        "ds_write_b128 v111, v[24:27] offset:6144\n\t"
        "ds_write_b128 v111, v[28:31] offset:7168\n\t"
        "ds_read_b64_tr_b16 v[160:161], v112 offset:0\n\t"
        "ds_read_b64_tr_b16 v[162:163], v112 offset:2048\n\t"
        "ds_read_b64_tr_b16 v[172:173], v112 offset:4096\n\t"
        "ds_read_b64_tr_b16 v[174:175], v112 offset:6144\n\t"
        "s_waitcnt lgkmcnt(8)\n\t"
        "ds_read_b64_tr_b16 v[164:165], v113 offset:0\n\t"
        "ds_read_b64_tr_b16 v[166:167], v113 offset:2048\n\t"
        "ds_read_b64_tr_b16 v[176:177], v113 offset:4096\n\t"
        "ds_read_b64_tr_b16 v[178:179], v113 offset:6144\n\t"
        "s_waitcnt lgkmcnt(8)\n\t"
        "ds_read_b64_tr_b16 v[168:169], v114 offset:0\n\t"
        "ds_read_b64_tr_b16 v[170:171], v114 offset:2048\n\t"
        "ds_read_b64_tr_b16 v[180:181], v114 offset:4096\n\t"
        "ds_read_b64_tr_b16 v[182:183], v114 offset:6144\n\t"
        "s_waitcnt lgkmcnt(8)\n\t"
        "ds_read_b64_tr_b16 v[56:57], v115 offset:0\n\t"
        "ds_read_b64_tr_b16 v[58:59], v115 offset:2048\n\t"
        "ds_read_b64_tr_b16 v[60:61], v115 offset:4096\n\t"
        "ds_read_b64_tr_b16 v[62:63], v115 offset:6144\n\t"
        "s_add_u32 %[t1], %[ro], 7\n\t"
        "s_and_b32 %[t1], %[t1], 7\n\t"
        "s_lshl_b32 %[t1], %[t1], 18\n\t"
        "s_add_u32 %[t0], %[t1], 0\n\t"
        "buffer_load_dwordx4 v[208:211], v110, %[so], %[t0] offen\n\t"
        "s_sleep 2\n\t"
        "s_add_u32 %[t0], %[t1], 16384\n\t"
        "buffer_load_dwordx4 v[212:215], v110, %[so], %[t0] offen\n\t"
        "s_sleep 2\n\t"
        "s_add_u32 %[t0], %[t1], 32768\n\t"
        "buffer_load_dwordx4 v[216:219], v110, %[so], %[t0] offen\n\t"
        "s_sleep 2\n\t"
        "s_add_u32 %[t0], %[t1], 49152\n\t"
        "buffer_load_dwordx4 v[220:223], v110, %[so], %[t0] offen\n\t"
        "s_sleep 2\n\t"
        "s_add_u32 %[t0], %[t1], 65536\n\t"
        "buffer_load_dwordx4 v[224:227], v110, %[so], %[t0] offen\n\t"
        "s_sleep 2\n\t"
        "s_add_u32 %[t0], %[t1], 81920\n\t"
        "buffer_load_dwordx4 v[228:231], v110, %[so], %[t0] offen\n\t"
        "s_sleep 2\n\t"
        "s_add_u32 %[t0], %[t1], 98304\n\t"
        "buffer_load_dwordx4 v[40:43], v110, %[so], %[t0] offen\n\t"
        "s_sleep 2\n\t"
        "s_add_u32 %[t0], %[t1], 114688\n\t"
        "buffer_load_dwordx4 v[44:47], v110, %[so], %[t0] offen\n\t"
        "s_sleep 2\n\t"
        "s_waitcnt lgkmcnt(0)\n\t"
        "ds_write_b128 v116, v[56:59] offset:24576\n\t"
        "ds_write_b128 v116, v[60:63] offset:25600\n\t"
        "s_waitcnt vmcnt(8)\n\t"
        "ds_write_b128 v111, v[184:187] offset:0\n\t"
        "ds_write_b128 v111, v[188:191] offset:1024\n\t"
        "ds_write_b128 v111, v[192:195] offset:2048\n\t"
        "ds_write_b128 v111, v[196:199] offset:3072\n\t"
        "ds_write_b128 v111, v[200:203] offset:4096\n\t"
        "ds_write_b128 v111, v[204:207] offset:5120\n\t"
        "ds_write_b128 v111, v[32:35] offset:6144\n\t"
        "ds_write_b128 v111, v[36:39] offset:7168\n\t"
        "ds_read_b64_tr_b16 v[184:185], v112 offset:0\n\t"
        "ds_read_b64_tr_b16 v[186:187], v112 offset:2048\n\t"
        "ds_read_b64_tr_b16 v[196:197], v112 offset:4096\n\t"
        "ds_read_b64_tr_b16 v[198:199], v112 offset:6144\n\t"
        "s_waitcnt lgkmcnt(8)\n\t"
        "ds_read_b64_tr_b16 v[188:189], v113 offset:0\n\t"
        "ds_read_b64_tr_b16 v[190:191], v113 offset:2048\n\t"
        "ds_read_b64_tr_b16 v[200:201], v113 offset:4096\n\t"
        "ds_read_b64_tr_b16 v[202:203], v113 offset:6144\n\t"
        "s_waitcnt lgkmcnt(8)\n\t"
        "ds_read_b64_tr_b16 v[192:193], v114 offset:0\n\t"
        "ds_read_b64_tr_b16 v[194:195], v114 offset:2048\n\t"
        "ds_read_b64_tr_b16 v[204:205], v114 offset:4096\n\t"
        "ds_read_b64_tr_b16 v[206:207], v114 offset:6144\n\t"
        "s_waitcnt lgkmcnt(8)\n\t"
        "ds_read_b64_tr_b16 v[56:57], v115 offset:0\n\t"
        "ds_read_b64_tr_b16 v[58:59], v115 offset:2048\n\t"
        "ds_read_b64_tr_b16 v[60:61], v115 offset:4096\n\t"
        "ds_read_b64_tr_b16 v[62:63], v115 offset:6144\n\t"
        "s_add_u32 %[t1], %[ro], 7\n\t"
        "s_and_b32 %[t1], %[t1], 7\n\t"
        "s_lshl_b32 %[t1], %[t1], 18\n\t"
        "s_add_u32 %[t0], %[t1], 131072\n\t"
        "buffer_load_dwordx4 v[232:235], v110, %[so], %[t0] offen\n\t"
        "s_sleep 2\n\t"
        "s_add_u32 %[t0], %[t1], 147456\n\t"
        "buffer_load_dwordx4 v[236:239], v110, %[so], %[t0] offen\n\t"
        "s_sleep 2\n\t"
        "s_add_u32 %[t0], %[t1], 163840\n\t"
        "buffer_load_dwordx4 v[240:243], v110, %[so], %[t0] offen\n\t"
        "s_sleep 2\n\t"
        "s_add_u32 %[t0], %[t1], 180224\n\t"
        "buffer_load_dwordx4 v[244:247], v110, %[so], %[t0] offen\n\t"
        "s_sleep 2\n\t"
        "s_add_u32 %[t0], %[t1], 196608\n\t"
        "buffer_load_dwordx4 v[248:251], v110, %[so], %[t0] offen\n\t"
        "s_sleep 2\n\t"
        "s_add_u32 %[t0], %[t1], 212992\n\t"
        "buffer_load_dwordx4 v[252:255], v110, %[so], %[t0] offen\n\t"
        "s_sleep 2\n\t"
        "s_add_u32 %[t0], %[t1], 229376\n\t"
        "buffer_load_dwordx4 v[48:51], v110, %[so], %[t0] offen\n\t"
        "s_sleep 2\n\t"
        "s_add_u32 %[t0], %[t1], 245760\n\t"
        "buffer_load_dwordx4 v[52:55], v110, %[so], %[t0] offen\n\t"
        "s_sleep 2\n\t"
        "s_waitcnt lgkmcnt(0)\n\t"
        "ds_write_b128 v116, v[56:59] offset:26624\n\t"
        "ds_write_b128 v116, v[60:63] offset:27648\n\t"
        "s_waitcnt vmcnt(8)\n\t"
        "ds_write_b128 v111, v[208:211] offset:0\n\t"
        "ds_write_b128 v111, v[212:215] offset:1024\n\t"
        "ds_write_b128 v111, v[216:219] offset:2048\n\t"
        "ds_write_b128 v111, v[220:223] offset:3072\n\t"
        "ds_write_b128 v111, v[224:227] offset:4096\n\t"
        "ds_write_b128 v111, v[228:231] offset:5120\n\t"
        "ds_write_b128 v111, v[40:43] offset:6144\n\t"
        "ds_write_b128 v111, v[44:47] offset:7168\n\t"
        "ds_read_b64_tr_b16 v[208:209], v112 offset:0\n\t"
        "ds_read_b64_tr_b16 v[210:211], v112 offset:2048\n\t"
        "ds_read_b64_tr_b16 v[220:221], v112 offset:4096\n\t"
        "ds_read_b64_tr_b16 v[222:223], v112 offset:6144\n\t"
        "s_waitcnt lgkmcnt(8)\n\t"
        "ds_read_b64_tr_b16 v[212:213], v113 offset:0\n\t"
        "ds_read_b64_tr_b16 v[214:215], v113 offset:2048\n\t"
        "ds_read_b64_tr_b16 v[224:225], v113 offset:4096\n\t"
        "ds_read_b64_tr_b16 v[226:227], v113 offset:6144\n\t"
        "s_waitcnt lgkmcnt(8)\n\t"
        "ds_read_b64_tr_b16 v[216:217], v114 offset:0\n\t"
        "ds_read_b64_tr_b16 v[218:219], v114 offset:2048\n\t"
        "ds_read_b64_tr_b16 v[228:229], v114 offset:4096\n\t"
        "ds_read_b64_tr_b16 v[230:231], v114 offset:6144\n\t"
        "s_waitcnt lgkmcnt(8)\n\t"
        "ds_read_b64_tr_b16 v[56:57], v115 offset:0\n\t"
        "ds_read_b64_tr_b16 v[58:59], v115 offset:2048\n\t"
        "ds_read_b64_tr_b16 v[60:61], v115 offset:4096\n\t"
        "ds_read_b64_tr_b16 v[62:63], v115 offset:6144\n\t"
        "s_waitcnt lgkmcnt(0)\n\t"
        "ds_write_b128 v116, v[56:59] offset:28672\n\t"
        "ds_write_b128 v116, v[60:63] offset:29696\n\t"
        "s_waitcnt vmcnt(0)\n\t"
        "ds_write_b128 v111, v[232:235] offset:0\n\t"
        "ds_write_b128 v111, v[236:239] offset:1024\n\t"
        "ds_write_b128 v111, v[240:243] offset:2048\n\t"
        "ds_write_b128 v111, v[244:247] offset:3072\n\t"
        "ds_write_b128 v111, v[248:251] offset:4096\n\t"
        "ds_write_b128 v111, v[252:255] offset:5120\n\t"
        "ds_write_b128 v111, v[48:51] offset:6144\n\t"
        "ds_write_b128 v111, v[52:55] offset:7168\n\t"
        "ds_read_b64_tr_b16 v[232:233], v112 offset:0\n\t"
        "ds_read_b64_tr_b16 v[234:235], v112 offset:2048\n\t"
        "ds_read_b64_tr_b16 v[244:245], v112 offset:4096\n\t"
        "ds_read_b64_tr_b16 v[246:247], v112 offset:6144\n\t"
        "s_waitcnt lgkmcnt(8)\n\t"
        "ds_read_b64_tr_b16 v[236:237], v113 offset:0\n\t"
        "ds_read_b64_tr_b16 v[238:239], v113 offset:2048\n\t"
        "ds_read_b64_tr_b16 v[248:249], v113 offset:4096\n\t"
        "ds_read_b64_tr_b16 v[250:251], v113 offset:6144\n\t"
        "s_waitcnt lgkmcnt(8)\n\t"
        "ds_read_b64_tr_b16 v[240:241], v114 offset:0\n\t"
        "ds_read_b64_tr_b16 v[242:243], v114 offset:2048\n\t"
        "ds_read_b64_tr_b16 v[252:253], v114 offset:4096\n\t"
        "ds_read_b64_tr_b16 v[254:255], v114 offset:6144\n\t"
        "s_waitcnt lgkmcnt(8)\n\t"
        "ds_read_b64_tr_b16 v[56:57], v115 offset:0\n\t"
        "ds_read_b64_tr_b16 v[58:59], v115 offset:2048\n\t"
        "ds_read_b64_tr_b16 v[60:61], v115 offset:4096\n\t"
        "ds_read_b64_tr_b16 v[62:63], v115 offset:6144\n\t"
        "s_waitcnt lgkmcnt(0)\n\t"
        "ds_write_b128 v116, v[56:59] offset:30720\n\t"
        "ds_write_b128 v116, v[60:63] offset:31744\n\t"
        "ds_read_b128 v[88:91], v116 offset:0\n\t"
        "ds_read_b128 v[92:95], v116 offset:1024\n\t"
        "ds_read_b128 v[96:99], v116 offset:2048\n\t"
        "s_mov_b32 %[sp], 0\n\t"
        "LP_cv%=:\n\t"
        "buffer_load_dword v104, off, %[sw], %[cv] sc1\n\t"
        "s_waitcnt vmcnt(0) lgkmcnt(0)\n\t"
        "v_readfirstlane_b32 %[t0], v104\n\t"
        "s_cmp_ge_u32 %[t0], 8\n\t"
        "s_cbranch_scc1 LP_wait%=\n\t"
        "s_sleep 8\n\t"
        "s_add_u32 %[sp], %[sp], 1\n\t"
        "s_cmp_lt_u32 %[sp], 0x100000\n\t"
        "s_cbranch_scc1 LP_cv%=\n\t"
        "v_mov_b32_e32 v104, 256\n\t"
        "s_mov_b32 %[t0], 33024\n\t"
        "buffer_store_dword v104, off, %[sw], %[t0] sc1\n\t"
        "s_endpgm\n\t"
        "LP_wait%=:\n\t"
        "s_mov_b32 %[re], 0\n\t"
        "LP_again%=:\n\t"
        "s_mov_b32 %[sp], 0\n\t"
        "LP_poll%=:\n\t"
        "buffer_load_dword v103, v118, %[sw], 0 offen sc1\n\t"
        "s_lshr_b32 %[t1], %[tl], 1\n\t"
        "s_waitcnt vmcnt(0)\n\t"
        "v_min_u32_dpp v103, v103, v103 quad_perm:[1,0,3,2] row_mask:0xf bank_mask:0xf\n\t"
        "s_nop 1\n\t"
        "v_min_u32_dpp v103, v103, v103 quad_perm:[2,3,0,1] row_mask:0xf bank_mask:0xf\n\t"
        "s_nop 1\n\t"
        "v_min_u32_dpp v103, v103, v103 row_ror:4 row_mask:0xf bank_mask:0xf\n\t"
        "s_nop 0\n\t"
        "v_readfirstlane_b32 %[pg], v103\n\t"
        "s_cmp_gt_u32 %[pg], %[t1]\n\t"
        "s_cbranch_scc1 LP_have%=\n\t"
        "s_add_u32 %[t0], %[pg], %[re]\n\t"
        "s_cmp_eq_u32 %[t0], 63\n\t"
        "s_cbranch_scc0 LP_no%=\n\t"
        "s_sleep 16\n\t"
        "s_branch LP_have%=\n\t"
        "LP_no%=:\n\t"
        "s_min_u32 %[t1], %[t1], 62\n\t"
        "s_cmp_ge_u32 %[pg], %[t1]\n\t"
        "s_cbranch_scc1 LP_near%=\n\t"
        "s_sleep 24\n\t"
        "LP_near%=:\n\t"
        "s_sleep 2\n\t"
        "s_add_u32 %[sp], %[sp], 1\n\t"
        "s_cmp_lt_u32 %[sp], 0x100000\n\t"
        "s_cbranch_scc1 LP_poll%=\n\t"
        "v_mov_b32_e32 v104, 128\n\t"
        "s_mov_b32 %[t0], 33024\n\t"
        "buffer_store_dword v104, off, %[sw], %[t0] sc1\n\t"
        "s_endpgm\n\t"
        "LP_have%=:\n\t"
        "s_lshl_b32 %[nx], %[tl], 17\n\t"
        "s_add_u32 %[t1], %[t1], 4\n\t"
        "s_cmp_gt_u32 %[pg], %[t1]\n\t"
        "s_cbranch_scc1 LP_run%=\n\t"
        "s_add_u32 %[t0], %[ro], 0\n\t"
        "s_and_b32 %[t0], %[t0], 7\n\t"
        "s_lshl_b32 %[t0], %[t0], 12\n\t"
        "s_add_u32 %[t0], %[t0], %[nx]\n\t"
        "buffer_load_dwordx4 v[24:27], v117, %[sw], %[t0] offen offset:0 sc1\n\t"
        "buffer_load_dwordx4 v[28:31], v117, %[sw], %[t0] offen offset:1024 sc1\n\t"
        "buffer_load_dwordx4 v[32:35], v117, %[sw], %[t0] offen offset:2048 sc1\n\t"
        "buffer_load_dwordx4 v[36:39], v117, %[sw], %[t0] offen offset:3072 sc1\n\t"
        "s_add_u32 %[t0], %[ro], 1\n\t"
        "s_and_b32 %[t0], %[t0], 7\n\t"
        "s_lshl_b32 %[t0], %[t0], 12\n\t"
        "s_add_u32 %[t0], %[t0], %[nx]\n\t"
        "buffer_load_dwordx4 v[40:43], v117, %[sw], %[t0] offen offset:0 sc1\n\t"
        "buffer_load_dwordx4 v[44:47], v117, %[sw], %[t0] offen offset:1024 sc1\n\t"
        "s_waitcnt vmcnt(5) lgkmcnt(2)\n\t"
        "v_mfma_f32_16x16x32_bf16 v[20:23], v[88:91], v[24:27], 0\n\t"
        "ds_read_b128 v[88:91], v116 offset:3072\n\t"
        "v_mfma_f32_16x16x32_bf16 v[8:11], a[0:3], v[24:27], 0\n\t"
        "v_max3_u32 v125, v24, v25, v26\n\t"
        "buffer_load_dwordx4 v[48:51], v117, %[sw], %[t0] offen offset:2048 sc1\n\t"
        "v_mfma_f32_16x16x32_bf16 v[12:15], a[4:7], v[24:27], 0\n\t"
        "v_max_u32_e32 v125, v27, v125\n\t"
        "v_mfma_f32_16x16x32_bf16 v[16:19], a[8:11], v[24:27], 0\n\t"
        "buffer_load_dwordx4 v[52:55], v117, %[sw], %[t0] offen offset:3072 sc1\n\t"
        "s_waitcnt vmcnt(6) lgkmcnt(2)\n\t"
        "v_mfma_f32_16x16x32_bf16 v[20:23], v[92:95], v[28:31], v[20:23]\n\t"
        "ds_read_b128 v[92:95], v116 offset:4096\n\t"
        "v_mfma_f32_16x16x32_bf16 v[8:11], a[12:15], v[28:31], v[8:11]\n\t"
        "v_max3_u32 v125, v28, v29, v125\n\t"
        "s_add_u32 %[t0], %[ro], 2\n\t"
        "s_and_b32 %[t0], %[t0], 7\n\t"
        "s_lshl_b32 %[t0], %[t0], 12\n\t"
        "s_add_u32 %[t0], %[t0], %[nx]\n\t"
        "buffer_load_dwordx4 v[56:59], v117, %[sw], %[t0] offen offset:0 sc1\n\t"
        "v_mfma_f32_16x16x32_bf16 v[12:15], a[16:19], v[28:31], v[12:15]\n\t"
        "v_max3_u32 v125, v30, v31, v125\n\t"
        "v_mfma_f32_16x16x32_bf16 v[16:19], a[20:23], v[28:31], v[16:19]\n\t"
        "buffer_load_dwordx4 v[60:63], v117, %[sw], %[t0] offen offset:1024 sc1\n\t"
        "s_and_b32 %[sp], %[tl], 1\n\t"
        "s_lshl_b32 %[sp], %[sp], 14\n\t"
        "s_lshr_b32 %[t1], %[tl], 1\n\t"
        "s_lshl_b32 %[t1], %[t1], 6\n\t"
        "s_add_u32 %[t1], %[sp], %[t1]\n\t"
        "buffer_load_dword v100, v119, %[sw], %[t1] offen sc1\n\t"
        "s_add_u32 %[t1], %[t1], 32768\n\t"
        "buffer_load_dword v101, v119, %[sw], %[t1] offen sc1\n\t"
        "s_lshl_b32 %[t1], %[tl], 6\n\t"
        "buffer_load_dword v102, v120, %[sw], %[t1] offen sc1\n\t"
        "buffer_load_dword v103, v118, %[sw], 0 offen sc1\n\t"
        "s_waitcnt vmcnt(11) lgkmcnt(2)\n\t"
        "v_mfma_f32_16x16x32_bf16 v[20:23], v[96:99], v[32:35], v[20:23]\n\t"
        "ds_read_b128 v[96:99], v116 offset:5120\n\t"
        "v_mfma_f32_16x16x32_bf16 v[8:11], a[24:27], v[32:35], v[8:11]\n\t"
        "v_max3_u32 v125, v32, v33, v125\n\t"
        "buffer_load_dwordx4 v[64:67], v117, %[sw], %[t0] offen offset:2048 sc1\n\t"
        "v_mfma_f32_16x16x32_bf16 v[12:15], a[28:31], v[32:35], v[12:15]\n\t"
        "v_max3_u32 v125, v34, v35, v125\n\t"
        "v_mfma_f32_16x16x32_bf16 v[16:19], a[32:35], v[32:35], v[16:19]\n\t"
        "buffer_load_dwordx4 v[68:71], v117, %[sw], %[t0] offen offset:3072 sc1\n\t"
        "s_waitcnt vmcnt(12) lgkmcnt(2)\n\t"
        "v_mfma_f32_16x16x32_bf16 v[20:23], v[88:91], v[36:39], v[20:23]\n\t"
        "ds_read_b128 v[88:91], v116 offset:6144\n\t"
        "v_mfma_f32_16x16x32_bf16 v[8:11], a[36:39], v[36:39], v[8:11]\n\t"
        "v_max3_u32 v125, v36, v37, v125\n\t"
        "s_add_u32 %[t0], %[ro], 3\n\t"
        "s_and_b32 %[t0], %[t0], 7\n\t"
        "s_lshl_b32 %[t0], %[t0], 12\n\t"
        "s_add_u32 %[t0], %[t0], %[nx]\n\t"
        "buffer_load_dwordx4 v[72:75], v117, %[sw], %[t0] offen offset:0 sc1\n\t"
        "v_mfma_f32_16x16x32_bf16 v[12:15], a[40:43], v[36:39], v[12:15]\n\t"
        "v_max3_u32 v125, v38, v39, v125\n\t"
        "v_mfma_f32_16x16x32_bf16 v[16:19], a[44:47], v[36:39], v[16:19]\n\t"
        "buffer_load_dwordx4 v[76:79], v117, %[sw], %[t0] offen offset:1024 sc1\n\t"
        "s_waitcnt vmcnt(13) lgkmcnt(2)\n\t"
        "v_mfma_f32_16x16x32_bf16 v[20:23], v[92:95], v[40:43], v[20:23]\n\t"
        "ds_read_b128 v[92:95], v116 offset:7168\n\t"
        "v_mfma_f32_16x16x32_bf16 v[8:11], a[48:51], v[40:43], v[8:11]\n\t"
        "v_max3_u32 v125, v40, v41, v125\n\t"
        "buffer_load_dwordx4 v[80:83], v117, %[sw], %[t0] offen offset:2048 sc1\n\t"
        "v_mfma_f32_16x16x32_bf16 v[12:15], a[52:55], v[40:43], v[12:15]\n\t"
        "v_max3_u32 v125, v42, v43, v125\n\t"
        "v_mfma_f32_16x16x32_bf16 v[16:19], a[56:59], v[40:43], v[16:19]\n\t"
        "buffer_load_dwordx4 v[84:87], v117, %[sw], %[t0] offen offset:3072 sc1\n\t"
        "s_waitcnt vmcnt(14) lgkmcnt(2)\n\t"
        "v_mfma_f32_16x16x32_bf16 v[20:23], v[96:99], v[44:47], v[20:23]\n\t"
        "ds_read_b128 v[96:99], v116 offset:8192\n\t"
        "v_mfma_f32_16x16x32_bf16 v[8:11], a[60:63], v[44:47], v[8:11]\n\t"
        "v_max3_u32 v125, v44, v45, v125\n\t"
        "s_add_u32 %[t0], %[ro], 4\n\t"
        "s_and_b32 %[t0], %[t0], 7\n\t"
        "s_lshl_b32 %[t0], %[t0], 12\n\t"
        "s_add_u32 %[t0], %[t0], %[nx]\n\t"
        "buffer_load_dwordx4 v[24:27], v117, %[sw], %[t0] offen offset:0 sc1\n\t"
        "v_mfma_f32_16x16x32_bf16 v[12:15], a[64:67], v[44:47], v[12:15]\n\t"
        "v_max3_u32 v125, v46, v47, v125\n\t"
        "v_mfma_f32_16x16x32_bf16 v[16:19], a[68:71], v[44:47], v[16:19]\n\t"
        "buffer_load_dwordx4 v[28:31], v117, %[sw], %[t0] offen offset:1024 sc1\n\t"
        "s_waitcnt vmcnt(15) lgkmcnt(2)\n\t"
        "v_mfma_f32_16x16x32_bf16 v[20:23], v[88:91], v[48:51], v[20:23]\n\t"
        "ds_read_b128 v[88:91], v116 offset:9216\n\t"
        "v_mfma_f32_16x16x32_bf16 v[8:11], a[72:75], v[48:51], v[8:11]\n\t"
        "v_max3_u32 v125, v48, v49, v125\n\t"
        "v_mfma_f32_16x16x32_bf16 v[12:15], a[76:79], v[48:51], v[12:15]\n\t"
        "v_max3_u32 v125, v50, v51, v125\n\t"
        "v_mfma_f32_16x16x32_bf16 v[16:19], a[80:83], v[48:51], v[16:19]\n\t"
        "buffer_load_dwordx4 v[32:35], v117, %[sw], %[t0] offen offset:2048 sc1\n\t"
        "s_waitcnt vmcnt(15) lgkmcnt(2)\n\t"
        "v_mfma_f32_16x16x32_bf16 v[20:23], v[92:95], v[52:55], v[20:23]\n\t"
        "ds_read_b128 v[92:95], v116 offset:10240\n\t"
        "v_mfma_f32_16x16x32_bf16 v[8:11], a[84:87], v[52:55], v[8:11]\n\t"
        "v_max3_u32 v125, v52, v53, v125\n\t"
        "v_mfma_f32_16x16x32_bf16 v[12:15], a[88:91], v[52:55], v[12:15]\n\t"
        "v_max3_u32 v125, v54, v55, v125\n\t"
        "v_mfma_f32_16x16x32_bf16 v[16:19], a[92:95], v[52:55], v[16:19]\n\t"
        "buffer_load_dwordx4 v[36:39], v117, %[sw], %[t0] offen offset:3072 sc1\n\t"
        "s_waitcnt vmcnt(15) lgkmcnt(2)\n\t"
        "s_cmp_lg_u32 %[re], 0\n\t"
        "s_cbranch_scc1 LP_nb%=\n\t"
        "s_barrier\n\t"
        "LP_nb%=:\n\t"
        "v_mfma_f32_16x16x32_bf16 v[20:23], v[96:99], v[56:59], v[20:23]\n\t"
        "ds_read_b128 v[96:99], v116 offset:11264\n\t"
        "v_mfma_f32_16x16x32_bf16 v[8:11], a[96:99], v[56:59], v[8:11]\n\t"
        "v_max3_u32 v125, v56, v57, v125\n\t"
        "v_mfma_f32_16x16x32_bf16 v[12:15], a[100:103], v[56:59], v[12:15]\n\t"
        "v_max3_u32 v125, v58, v59, v125\n\t"
        "v_mfma_f32_16x16x32_bf16 v[16:19], a[104:107], v[56:59], v[16:19]\n\t"
        "s_add_u32 %[t0], %[ro], 5\n\t"
        "s_and_b32 %[t0], %[t0], 7\n\t"
        "s_lshl_b32 %[t0], %[t0], 12\n\t"
        "s_add_u32 %[t0], %[t0], %[nx]\n\t"
        "buffer_load_dwordx4 v[40:43], v117, %[sw], %[t0] offen offset:0 sc1\n\t"
        "s_waitcnt vmcnt(15) lgkmcnt(2)\n\t"
        "v_mfma_f32_16x16x32_bf16 v[20:23], v[88:91], v[60:63], v[20:23]\n\t"
        "ds_read_b128 v[88:91], v116 offset:12288\n\t"
        "v_mfma_f32_16x16x32_bf16 v[8:11], a[108:111], v[60:63], v[8:11]\n\t"
        "v_max3_u32 v125, v60, v61, v125\n\t"
        "v_mfma_f32_16x16x32_bf16 v[12:15], a[112:115], v[60:63], v[12:15]\n\t"
        "v_max3_u32 v125, v62, v63, v125\n\t"
        "v_mfma_f32_16x16x32_bf16 v[16:19], a[116:119], v[60:63], v[16:19]\n\t"
        "buffer_load_dwordx4 v[44:47], v117, %[sw], %[t0] offen offset:1024 sc1\n\t"
        "s_waitcnt vmcnt(11) lgkmcnt(2)\n\t"
        "v_mfma_f32_16x16x32_bf16 v[20:23], v[92:95], v[64:67], v[20:23]\n\t"
        "ds_read_b128 v[92:95], v116 offset:13312\n\t"
        "v_mfma_f32_16x16x32_bf16 v[8:11], a[120:123], v[64:67], v[8:11]\n\t"
        "v_max3_u32 v125, v64, v65, v125\n\t"
        "v_mfma_f32_16x16x32_bf16 v[12:15], a[124:127], v[64:67], v[12:15]\n\t"
        "v_max3_u32 v125, v66, v67, v125\n\t"
        "v_mfma_f32_16x16x32_bf16 v[16:19], a[128:131], v[64:67], v[16:19]\n\t"
        "buffer_load_dwordx4 v[48:51], v117, %[sw], %[t0] offen offset:2048 sc1\n\t"
        "s_waitcnt vmcnt(11) lgkmcnt(2)\n\t"
        "v_mfma_f32_16x16x32_bf16 v[20:23], v[96:99], v[68:71], v[20:23]\n\t"
        "ds_read_b128 v[96:99], v116 offset:14336\n\t"
        "v_mfma_f32_16x16x32_bf16 v[8:11], a[132:135], v[68:71], v[8:11]\n\t"
        "v_max3_u32 v125, v68, v69, v125\n\t"
        "v_mfma_f32_16x16x32_bf16 v[12:15], a[136:139], v[68:71], v[12:15]\n\t"
        "v_max3_u32 v125, v70, v71, v125\n\t"
        "v_mfma_f32_16x16x32_bf16 v[16:19], a[140:143], v[68:71], v[16:19]\n\t"
        "buffer_load_dwordx4 v[52:55], v117, %[sw], %[t0] offen offset:3072 sc1\n\t"
        "s_waitcnt vmcnt(11) lgkmcnt(2)\n\t"
        "v_mfma_f32_16x16x32_bf16 v[20:23], v[88:91], v[72:75], v[20:23]\n\t"
        "ds_read_b128 v[88:91], v116 offset:15360\n\t"
        "v_mfma_f32_16x16x32_bf16 v[8:11], a[144:147], v[72:75], v[8:11]\n\t"
        "v_max3_u32 v125, v72, v73, v125\n\t"
        "v_mfma_f32_16x16x32_bf16 v[12:15], a[148:151], v[72:75], v[12:15]\n\t"
        "v_max3_u32 v125, v74, v75, v125\n\t"
        "v_mfma_f32_16x16x32_bf16 v[16:19], a[152:155], v[72:75], v[16:19]\n\t"
        "s_add_u32 %[t0], %[ro], 6\n\t"
        "s_and_b32 %[t0], %[t0], 7\n\t"
        "s_lshl_b32 %[t0], %[t0], 12\n\t"
        "s_add_u32 %[t0], %[t0], %[nx]\n\t"
        "buffer_load_dwordx4 v[56:59], v117, %[sw], %[t0] offen offset:0 sc1\n\t"
        "s_waitcnt vmcnt(11) lgkmcnt(2)\n\t"
        "v_mfma_f32_16x16x32_bf16 v[20:23], v[92:95], v[76:79], v[20:23]\n\t"
        "ds_read_b128 v[92:95], v116 offset:16384\n\t"
        "v_mfma_f32_16x16x32_bf16 v[8:11], a[156:159], v[76:79], v[8:11]\n\t"
        "v_max3_u32 v125, v76, v77, v125\n\t"
        "v_mfma_f32_16x16x32_bf16 v[12:15], a[160:163], v[76:79], v[12:15]\n\t"
        "v_max3_u32 v125, v78, v79, v125\n\t"
        "v_mfma_f32_16x16x32_bf16 v[16:19], a[164:167], v[76:79], v[16:19]\n\t"
        "buffer_load_dwordx4 v[60:63], v117, %[sw], %[t0] offen offset:1024 sc1\n\t"
        "s_waitcnt vmcnt(11) lgkmcnt(2)\n\t"
        "v_mfma_f32_16x16x32_bf16 v[20:23], v[96:99], v[80:83], v[20:23]\n\t"
        "ds_read_b128 v[96:99], v116 offset:17408\n\t"
        "v_mfma_f32_16x16x32_bf16 v[8:11], a[168:171], v[80:83], v[8:11]\n\t"
        "v_max3_u32 v125, v80, v81, v125\n\t"
        "v_mfma_f32_16x16x32_bf16 v[12:15], a[172:175], v[80:83], v[12:15]\n\t"
        "v_max3_u32 v125, v82, v83, v125\n\t"
        "v_mfma_f32_16x16x32_bf16 v[16:19], a[176:179], v[80:83], v[16:19]\n\t"
        "buffer_load_dwordx4 v[64:67], v117, %[sw], %[t0] offen offset:2048 sc1\n\t"
        "s_waitcnt vmcnt(11) lgkmcnt(2)\n\t"
        "v_mfma_f32_16x16x32_bf16 v[20:23], v[88:91], v[84:87], v[20:23]\n\t"
        "ds_read_b128 v[88:91], v116 offset:18432\n\t"
        "v_mfma_f32_16x16x32_bf16 v[8:11], a[180:183], v[84:87], v[8:11]\n\t"
        "v_max3_u32 v125, v84, v85, v125\n\t"
        "v_mfma_f32_16x16x32_bf16 v[12:15], a[184:187], v[84:87], v[12:15]\n\t"
        "v_max3_u32 v125, v86, v87, v125\n\t"
        "v_mfma_f32_16x16x32_bf16 v[16:19], a[188:191], v[84:87], v[16:19]\n\t"
        "buffer_load_dwordx4 v[68:71], v117, %[sw], %[t0] offen offset:3072 sc1\n\t"
        "s_waitcnt vmcnt(11) lgkmcnt(2)\n\t"
        "v_mfma_f32_16x16x32_bf16 v[20:23], v[92:95], v[24:27], v[20:23]\n\t"
        "ds_read_b128 v[92:95], v116 offset:19456\n\t"
        "v_mfma_f32_16x16x32_bf16 v[8:11], a[192:195], v[24:27], v[8:11]\n\t"
        "v_max3_u32 v125, v24, v25, v125\n\t"
        "v_mfma_f32_16x16x32_bf16 v[12:15], a[196:199], v[24:27], v[12:15]\n\t"
        "v_max3_u32 v125, v26, v27, v125\n\t"
        "v_mfma_f32_16x16x32_bf16 v[16:19], a[200:203], v[24:27], v[16:19]\n\t"
        "s_add_u32 %[t0], %[ro], 7\n\t"
        "s_and_b32 %[t0], %[t0], 7\n\t"
        "s_lshl_b32 %[t0], %[t0], 12\n\t"
        "s_add_u32 %[t0], %[t0], %[nx]\n\t"
        "buffer_load_dwordx4 v[72:75], v117, %[sw], %[t0] offen offset:0 sc1\n\t"
        "s_waitcnt vmcnt(11) lgkmcnt(2)\n\t"
        "v_mfma_f32_16x16x32_bf16 v[20:23], v[96:99], v[28:31], v[20:23]\n\t"
        "ds_read_b128 v[96:99], v116 offset:20480\n\t"
        "v_mfma_f32_16x16x32_bf16 v[8:11], a[204:207], v[28:31], v[8:11]\n\t"
        "v_max3_u32 v125, v28, v29, v125\n\t"
        "v_mfma_f32_16x16x32_bf16 v[12:15], a[208:211], v[28:31], v[12:15]\n\t"
        "v_max3_u32 v125, v30, v31, v125\n\t"
        "v_sub_f32_e32 v104, 1.0, v102\n\t"
        "v_mfma_f32_16x16x32_bf16 v[16:19], a[212:215], v[28:31], v[16:19]\n\t"
        "buffer_load_dwordx4 v[76:79], v117, %[sw], %[t0] offen offset:1024 sc1\n\t"
        "s_waitcnt vmcnt(11) lgkmcnt(2)\n\t"
        "v_mfma_f32_16x16x32_bf16 v[20:23], v[88:91], v[32:35], v[20:23]\n\t"
        "ds_read_b128 v[88:91], v116 offset:21504\n\t"
        "v_mfma_f32_16x16x32_bf16 v[8:11], a[216:219], v[32:35], v[8:11]\n\t"
        "v_max3_u32 v125, v32, v33, v125\n\t"
        "v_mfma_f32_16x16x32_bf16 v[12:15], a[220:223], v[32:35], v[12:15]\n\t"
        "v_max3_u32 v125, v34, v35, v125\n\t"
        "v_cvt_pk_bf16_f32 v104, 0, v104\n\t"
        "v_mfma_f32_16x16x32_bf16 v[16:19], a[224:227], v[32:35], v[16:19]\n\t"
        "buffer_load_dwordx4 v[80:83], v117, %[sw], %[t0] offen offset:2048 sc1\n\t"
        "s_waitcnt vmcnt(11) lgkmcnt(2)\n\t"
        "v_mfma_f32_16x16x32_bf16 v[20:23], v[92:95], v[36:39], v[20:23]\n\t"
        "ds_read_b128 v[92:95], v116 offset:22528\n\t"
        "v_mfma_f32_16x16x32_bf16 v[8:11], a[228:231], v[36:39], v[8:11]\n\t"
        "v_max3_u32 v125, v36, v37, v125\n\t"
        "v_mfma_f32_16x16x32_bf16 v[12:15], a[232:235], v[36:39], v[12:15]\n\t"
        "v_max3_u32 v125, v38, v39, v125\n\t"
        "v_lshlrev_b32_e32 v106, 16, v100\n\t"
        "v_mfma_f32_16x16x32_bf16 v[16:19], a[236:239], v[36:39], v[16:19]\n\t"
        "buffer_load_dwordx4 v[84:87], v117, %[sw], %[t0] offen offset:3072 sc1\n\t"
        "s_waitcnt vmcnt(11) lgkmcnt(2)\n\t"
        "v_mfma_f32_16x16x32_bf16 v[20:23], v[96:99], v[40:43], v[20:23]\n\t"
        "ds_read_b128 v[96:99], v116 offset:23552\n\t"
        "v_mfma_f32_16x16x32_bf16 v[8:11], a[240:243], v[40:43], v[8:11]\n\t"
        "v_max3_u32 v125, v40, v41, v125\n\t"
        "v_mfma_f32_16x16x32_bf16 v[12:15], a[244:247], v[40:43], v[12:15]\n\t"
        "v_max3_u32 v125, v42, v43, v125\n\t"
        "v_mul_f32_e32 v106, v102, v106\n\t"
        "v_mfma_f32_16x16x32_bf16 v[16:19], a[248:251], v[40:43], v[16:19]\n\t"
        "s_waitcnt vmcnt(10) lgkmcnt(2)\n\t"
        "v_mfma_f32_16x16x32_bf16 v[20:23], v[88:91], v[44:47], v[20:23]\n\t"
        "ds_read_b128 v[88:91], v116 offset:24576\n\t"
        "v_mfma_f32_16x16x32_bf16 v[8:11], a[252:255], v[44:47], v[8:11]\n\t"
        "v_max3_u32 v125, v44, v45, v125\n\t"
        "v_mfma_f32_16x16x32_bf16 v[12:15], v[128:131], v[44:47], v[12:15]\n\t"
        "v_max3_u32 v125, v46, v47, v125\n\t"
        "v_cvt_pk_bf16_f32 v106, 0, v106\n\t"
        "v_mfma_f32_16x16x32_bf16 v[16:19], v[132:135], v[44:47], v[16:19]\n\t"
        "s_waitcnt vmcnt(9) lgkmcnt(2)\n\t"
        "v_mfma_f32_16x16x32_bf16 v[20:23], v[92:95], v[48:51], v[20:23]\n\t"
        "ds_read_b128 v[92:95], v116 offset:25600\n\t"
        "v_mfma_f32_16x16x32_bf16 v[8:11], v[136:139], v[48:51], v[8:11]\n\t"
        "v_max3_u32 v125, v48, v49, v125\n\t"
        "v_mfma_f32_16x16x32_bf16 v[12:15], v[140:143], v[48:51], v[12:15]\n\t"
        "v_max3_u32 v125, v50, v51, v125\n\t"
        "v_and_b32_e32 v107, 0xffff0000, v100\n\t"
        "v_mfma_f32_16x16x32_bf16 v[16:19], v[144:147], v[48:51], v[16:19]\n\t"
        "s_waitcnt vmcnt(8) lgkmcnt(2)\n\t"
        "v_mfma_f32_16x16x32_bf16 v[20:23], v[96:99], v[52:55], v[20:23]\n\t"
        "ds_read_b128 v[96:99], v116 offset:26624\n\t"
        "v_mfma_f32_16x16x32_bf16 v[8:11], v[148:151], v[52:55], v[8:11]\n\t"
        "v_max3_u32 v125, v52, v53, v125\n\t"
        "v_mfma_f32_16x16x32_bf16 v[12:15], v[152:155], v[52:55], v[12:15]\n\t"
        "v_max3_u32 v125, v54, v55, v125\n\t"
        "v_mul_f32_e32 v107, v102, v107\n\t"
        "v_mfma_f32_16x16x32_bf16 v[16:19], v[156:159], v[52:55], v[16:19]\n\t"
        "s_waitcnt vmcnt(7) lgkmcnt(2)\n\t"
        "v_mfma_f32_16x16x32_bf16 v[20:23], v[88:91], v[56:59], v[20:23]\n\t"
        "ds_read_b128 v[88:91], v116 offset:27648\n\t"
        "v_mfma_f32_16x16x32_bf16 v[8:11], v[160:163], v[56:59], v[8:11]\n\t"
        "v_max3_u32 v125, v56, v57, v125\n\t"
        "v_mfma_f32_16x16x32_bf16 v[12:15], v[164:167], v[56:59], v[12:15]\n\t"
        "v_max3_u32 v125, v58, v59, v125\n\t"
        "v_cvt_pk_bf16_f32 v107, 0, v107\n\t"
        "v_mfma_f32_16x16x32_bf16 v[16:19], v[168:171], v[56:59], v[16:19]\n\t"
        "s_waitcnt vmcnt(6) lgkmcnt(2)\n\t"
        "v_mfma_f32_16x16x32_bf16 v[20:23], v[92:95], v[60:63], v[20:23]\n\t"
        "ds_read_b128 v[92:95], v116 offset:28672\n\t"
        "v_mfma_f32_16x16x32_bf16 v[8:11], v[172:175], v[60:63], v[8:11]\n\t"
        "v_max3_u32 v125, v60, v61, v125\n\t"
        "v_mfma_f32_16x16x32_bf16 v[12:15], v[176:179], v[60:63], v[12:15]\n\t"
        "v_max3_u32 v125, v62, v63, v125\n\t"
        "v_lshlrev_b32_e32 v108, 16, v101\n\t"
        "v_mfma_f32_16x16x32_bf16 v[16:19], v[180:183], v[60:63], v[16:19]\n\t"
        "s_waitcnt vmcnt(5) lgkmcnt(2)\n\t"
        "v_mfma_f32_16x16x32_bf16 v[20:23], v[96:99], v[64:67], v[20:23]\n\t"
        "ds_read_b128 v[96:99], v116 offset:29696\n\t"
        "v_mfma_f32_16x16x32_bf16 v[8:11], v[184:187], v[64:67], v[8:11]\n\t"
        "v_max3_u32 v125, v64, v65, v125\n\t"
        "v_mfma_f32_16x16x32_bf16 v[12:15], v[188:191], v[64:67], v[12:15]\n\t"
        "v_max3_u32 v125, v66, v67, v125\n\t"
        "v_mul_f32_e32 v108, v102, v108\n\t"
        "v_mfma_f32_16x16x32_bf16 v[16:19], v[192:195], v[64:67], v[16:19]\n\t"
        "s_waitcnt vmcnt(4) lgkmcnt(2)\n\t"
        "v_mfma_f32_16x16x32_bf16 v[20:23], v[88:91], v[68:71], v[20:23]\n\t"
        "ds_read_b128 v[88:91], v116 offset:30720\n\t"
        "v_mfma_f32_16x16x32_bf16 v[8:11], v[196:199], v[68:71], v[8:11]\n\t"
        "v_max3_u32 v125, v68, v69, v125\n\t"
        "v_mfma_f32_16x16x32_bf16 v[12:15], v[200:203], v[68:71], v[12:15]\n\t"
        "v_max3_u32 v125, v70, v71, v125\n\t"
        "v_cvt_pk_bf16_f32 v108, 0, v108\n\t"
        "v_mfma_f32_16x16x32_bf16 v[16:19], v[204:207], v[68:71], v[16:19]\n\t"
        "s_waitcnt vmcnt(3) lgkmcnt(2)\n\t"
        "v_mfma_f32_16x16x32_bf16 v[20:23], v[92:95], v[72:75], v[20:23]\n\t"
        "ds_read_b128 v[92:95], v116 offset:31744\n\t"
        "v_mfma_f32_16x16x32_bf16 v[8:11], v[208:211], v[72:75], v[8:11]\n\t"
        "v_max3_u32 v125, v72, v73, v125\n\t"
        "v_mfma_f32_16x16x32_bf16 v[12:15], v[212:215], v[72:75], v[12:15]\n\t"
        "v_max3_u32 v125, v74, v75, v125\n\t"
        "v_and_b32_e32 v109, 0xffff0000, v101\n\t"
        "v_mfma_f32_16x16x32_bf16 v[16:19], v[216:219], v[72:75], v[16:19]\n\t"
        "s_waitcnt vmcnt(2) lgkmcnt(2)\n\t"
        "v_mfma_f32_16x16x32_bf16 v[20:23], v[96:99], v[76:79], v[20:23]\n\t"
        "v_mfma_f32_16x16x32_bf16 v[8:11], v[220:223], v[76:79], v[8:11]\n\t"
        "v_max3_u32 v125, v76, v77, v125\n\t"
        "v_mfma_f32_16x16x32_bf16 v[12:15], v[224:227], v[76:79], v[12:15]\n\t"
        "v_max3_u32 v125, v78, v79, v125\n\t"
        "v_mul_f32_e32 v109, v102, v109\n\t"
        "v_mfma_f32_16x16x32_bf16 v[16:19], v[228:231], v[76:79], v[16:19]\n\t"
        "s_waitcnt vmcnt(1) lgkmcnt(1)\n\t"
        "v_mfma_f32_16x16x32_bf16 v[20:23], v[88:91], v[80:83], v[20:23]\n\t"
        "ds_read_b128 v[88:91], v116 offset:0\n\t"
        "v_mfma_f32_16x16x32_bf16 v[8:11], v[232:235], v[80:83], v[8:11]\n\t"
        "v_max3_u32 v125, v80, v81, v125\n\t"
        "v_mfma_f32_16x16x32_bf16 v[12:15], v[236:239], v[80:83], v[12:15]\n\t"
        "v_max3_u32 v125, v82, v83, v125\n\t"
        "v_cvt_pk_bf16_f32 v109, 0, v109\n\t"
        "v_mfma_f32_16x16x32_bf16 v[16:19], v[240:243], v[80:83], v[16:19]\n\t"
        "s_waitcnt vmcnt(0) lgkmcnt(1)\n\t"
        "v_mfma_f32_16x16x32_bf16 v[20:23], v[92:95], v[84:87], v[20:23]\n\t"
        "ds_read_b128 v[92:95], v116 offset:1024\n\t"
        "v_mfma_f32_16x16x32_bf16 v[8:11], v[244:247], v[84:87], v[8:11]\n\t"
        "ds_read_b128 v[96:99], v116 offset:2048\n\t"
        "v_max3_u32 v125, v84, v85, v125\n\t"
        "v_mfma_f32_16x16x32_bf16 v[12:15], v[248:251], v[84:87], v[12:15]\n\t"
        "v_max3_u32 v125, v86, v87, v125\n\t"
        "v_mfma_f32_16x16x32_bf16 v[16:19], v[252:255], v[84:87], v[16:19]\n\t"
        "v_cmp_eq_u32_e32 vcc, -1, v125\n\t"
        "s_mov_b32 %[nx], 0xf0000000\n\t"
        "s_mov_b32 %[re], 64\n\t"
        "s_cbranch_vccz LP_epi%=\n\t"
        "s_waitcnt lgkmcnt(0)\n\t"
        "s_branch LP_again%=\n\t"
        "LP_run%=:\n\t"
        "s_add_u32 %[t0], %[ro], 0\n\t"
        "s_and_b32 %[t0], %[t0], 7\n\t"
        "s_lshl_b32 %[t0], %[t0], 12\n\t"
        "s_add_u32 %[t0], %[t0], %[nx]\n\t"
        "buffer_load_dwordx4 v[24:27], v117, %[sw], %[t0] offen offset:0 sc1\n\t"
        "buffer_load_dwordx4 v[28:31], v117, %[sw], %[t0] offen offset:1024 sc1\n\t"
        "buffer_load_dwordx4 v[32:35], v117, %[sw], %[t0] offen offset:2048 sc1\n\t"
        "buffer_load_dwordx4 v[36:39], v117, %[sw], %[t0] offen offset:3072 sc1\n\t"
        "s_add_u32 %[t0], %[ro], 1\n\t"
        "s_and_b32 %[t0], %[t0], 7\n\t"
        "s_lshl_b32 %[t0], %[t0], 12\n\t"
        "s_add_u32 %[t0], %[t0], %[nx]\n\t"
        "buffer_load_dwordx4 v[40:43], v117, %[sw], %[t0] offen offset:0 sc1\n\t"
        "buffer_load_dwordx4 v[44:47], v117, %[sw], %[t0] offen offset:1024 sc1\n\t"
        "buffer_load_dwordx4 v[48:51], v117, %[sw], %[t0] offen offset:2048 sc1\n\t"
        "buffer_load_dwordx4 v[52:55], v117, %[sw], %[t0] offen offset:3072 sc1\n\t"
        "s_add_u32 %[t0], %[ro], 2\n\t"
        "s_and_b32 %[t0], %[t0], 7\n\t"
        "s_lshl_b32 %[t0], %[t0], 12\n\t"
        "s_add_u32 %[t0], %[t0], %[nx]\n\t"
        "buffer_load_dwordx4 v[56:59], v117, %[sw], %[t0] offen offset:0 sc1\n\t"
        "buffer_load_dwordx4 v[60:63], v117, %[sw], %[t0] offen offset:1024 sc1\n\t"
        "buffer_load_dwordx4 v[64:67], v117, %[sw], %[t0] offen offset:2048 sc1\n\t"
        "buffer_load_dwordx4 v[68:71], v117, %[sw], %[t0] offen offset:3072 sc1\n\t"
        "s_add_u32 %[t0], %[ro], 3\n\t"
        "s_and_b32 %[t0], %[t0], 7\n\t"
        "s_lshl_b32 %[t0], %[t0], 12\n\t"
        "s_add_u32 %[t0], %[t0], %[nx]\n\t"
        "buffer_load_dwordx4 v[72:75], v117, %[sw], %[t0] offen offset:0 sc1\n\t"
        "buffer_load_dwordx4 v[76:79], v117, %[sw], %[t0] offen offset:1024 sc1\n\t"
        "buffer_load_dwordx4 v[80:83], v117, %[sw], %[t0] offen offset:2048 sc1\n\t"
        "buffer_load_dwordx4 v[84:87], v117, %[sw], %[t0] offen offset:3072 sc1\n\t"
        "buffer_load_dword v103, v118, %[sw], 0 offen sc1\n\t"
        "LP_tile%=:\n\t"
        "s_and_b32 %[sp], %[tl], 1\n\t"
        "s_lshl_b32 %[sp], %[sp], 14\n\t"
        "s_lshr_b32 %[t1], %[tl], 1\n\t"
        "s_lshl_b32 %[t1], %[t1], 6\n\t"
        "s_add_u32 %[t1], %[sp], %[t1]\n\t"
        "buffer_load_dword v100, v119, %[sw], %[t1] offen sc1\n\t"
        "s_add_u32 %[t1], %[t1], 32768\n\t"
        "buffer_load_dword v101, v119, %[sw], %[t1] offen sc1\n\t"
        "s_lshl_b32 %[t1], %[tl], 6\n\t"
        "buffer_load_dword v102, v120, %[sw], %[t1] offen sc1\n\t"
        "buffer_load_dword v103, v118, %[sw], 0 offen sc1\n\t"
        "s_mov_b32 %[cu], %[nx]\n\t"
        "s_waitcnt vmcnt(20) lgkmcnt(2)\n\t"
        "v_mfma_f32_16x16x32_bf16 v[20:23], v[88:91], v[24:27], 0\n\t"
        "ds_read_b128 v[88:91], v116 offset:3072\n\t"
        "v_mfma_f32_16x16x32_bf16 v[8:11], a[0:3], v[24:27], 0\n\t"
        "v_mfma_f32_16x16x32_bf16 v[12:15], a[4:7], v[24:27], 0\n\t"
        "v_mfma_f32_16x16x32_bf16 v[16:19], a[8:11], v[24:27], 0\n\t"
        "s_add_u32 %[t0], %[ro], 4\n\t"
        "s_and_b32 %[t0], %[t0], 7\n\t"
        "s_lshl_b32 %[t0], %[t0], 12\n\t"
        "s_add_u32 %[t0], %[t0], %[cu]\n\t"
        "buffer_load_dwordx4 v[24:27], v117, %[sw], %[t0] offen offset:0 sc1\n\t"
        "s_waitcnt vmcnt(20) lgkmcnt(2)\n\t"
        "v_mfma_f32_16x16x32_bf16 v[20:23], v[92:95], v[28:31], v[20:23]\n\t"
        "ds_read_b128 v[92:95], v116 offset:4096\n\t"
        "v_mfma_f32_16x16x32_bf16 v[8:11], a[12:15], v[28:31], v[8:11]\n\t"
        "v_mfma_f32_16x16x32_bf16 v[12:15], a[16:19], v[28:31], v[12:15]\n\t"
        "v_mfma_f32_16x16x32_bf16 v[16:19], a[20:23], v[28:31], v[16:19]\n\t"
        "buffer_load_dwordx4 v[28:31], v117, %[sw], %[t0] offen offset:1024 sc1\n\t"
        "s_waitcnt vmcnt(20) lgkmcnt(2)\n\t"
        "v_mfma_f32_16x16x32_bf16 v[20:23], v[96:99], v[32:35], v[20:23]\n\t"
        "ds_read_b128 v[96:99], v116 offset:5120\n\t"
        "v_mfma_f32_16x16x32_bf16 v[8:11], a[24:27], v[32:35], v[8:11]\n\t"
        "v_mfma_f32_16x16x32_bf16 v[12:15], a[28:31], v[32:35], v[12:15]\n\t"
        "v_mfma_f32_16x16x32_bf16 v[16:19], a[32:35], v[32:35], v[16:19]\n\t"
        "buffer_load_dwordx4 v[32:35], v117, %[sw], %[t0] offen offset:2048 sc1\n\t"
        "s_waitcnt vmcnt(20) lgkmcnt(2)\n\t"
        "v_mfma_f32_16x16x32_bf16 v[20:23], v[88:91], v[36:39], v[20:23]\n\t"
        "ds_read_b128 v[88:91], v116 offset:6144\n\t"
        "v_mfma_f32_16x16x32_bf16 v[8:11], a[36:39], v[36:39], v[8:11]\n\t"
        "v_mfma_f32_16x16x32_bf16 v[12:15], a[40:43], v[36:39], v[12:15]\n\t"
        "v_mfma_f32_16x16x32_bf16 v[16:19], a[44:47], v[36:39], v[16:19]\n\t"
        "buffer_load_dwordx4 v[36:39], v117, %[sw], %[t0] offen offset:3072 sc1\n\t"
        "s_waitcnt vmcnt(20) lgkmcnt(2)\n\t"
        "v_mfma_f32_16x16x32_bf16 v[20:23], v[92:95], v[40:43], v[20:23]\n\t"
        "ds_read_b128 v[92:95], v116 offset:7168\n\t"
        "v_mfma_f32_16x16x32_bf16 v[8:11], a[48:51], v[40:43], v[8:11]\n\t"
        "v_mfma_f32_16x16x32_bf16 v[12:15], a[52:55], v[40:43], v[12:15]\n\t"
        "v_mfma_f32_16x16x32_bf16 v[16:19], a[56:59], v[40:43], v[16:19]\n\t"
        "s_add_u32 %[t0], %[ro], 5\n\t"
        "s_and_b32 %[t0], %[t0], 7\n\t"
        "s_lshl_b32 %[t0], %[t0], 12\n\t"
        "s_add_u32 %[t0], %[t0], %[cu]\n\t"
        "buffer_load_dwordx4 v[40:43], v117, %[sw], %[t0] offen offset:0 sc1\n\t"
        "s_waitcnt vmcnt(20) lgkmcnt(2)\n\t"
        "v_mfma_f32_16x16x32_bf16 v[20:23], v[96:99], v[44:47], v[20:23]\n\t"
        "ds_read_b128 v[96:99], v116 offset:8192\n\t"
        "v_mfma_f32_16x16x32_bf16 v[8:11], a[60:63], v[44:47], v[8:11]\n\t"
        "v_mfma_f32_16x16x32_bf16 v[12:15], a[64:67], v[44:47], v[12:15]\n\t"
        "v_mfma_f32_16x16x32_bf16 v[16:19], a[68:71], v[44:47], v[16:19]\n\t"
        "buffer_load_dwordx4 v[44:47], v117, %[sw], %[t0] offen offset:1024 sc1\n\t"
        "s_waitcnt vmcnt(20) lgkmcnt(2)\n\t"
        "v_mfma_f32_16x16x32_bf16 v[20:23], v[88:91], v[48:51], v[20:23]\n\t"
        "ds_read_b128 v[88:91], v116 offset:9216\n\t"
        "v_mfma_f32_16x16x32_bf16 v[8:11], a[72:75], v[48:51], v[8:11]\n\t"
        "v_mfma_f32_16x16x32_bf16 v[12:15], a[76:79], v[48:51], v[12:15]\n\t"
        "v_mfma_f32_16x16x32_bf16 v[16:19], a[80:83], v[48:51], v[16:19]\n\t"
        "buffer_load_dwordx4 v[48:51], v117, %[sw], %[t0] offen offset:2048 sc1\n\t"
        "s_waitcnt vmcnt(20) lgkmcnt(2)\n\t"
        "v_mfma_f32_16x16x32_bf16 v[20:23], v[92:95], v[52:55], v[20:23]\n\t"
        "ds_read_b128 v[92:95], v116 offset:10240\n\t"
        "v_mfma_f32_16x16x32_bf16 v[8:11], a[84:87], v[52:55], v[8:11]\n\t"
        "v_mfma_f32_16x16x32_bf16 v[12:15], a[88:91], v[52:55], v[12:15]\n\t"
        "v_mfma_f32_16x16x32_bf16 v[16:19], a[92:95], v[52:55], v[16:19]\n\t"
        "buffer_load_dwordx4 v[52:55], v117, %[sw], %[t0] offen offset:3072 sc1\n\t"
        "s_waitcnt vmcnt(20) lgkmcnt(2)\n\t"
        "s_barrier\n\t"
        "v_mfma_f32_16x16x32_bf16 v[20:23], v[96:99], v[56:59], v[20:23]\n\t"
        "ds_read_b128 v[96:99], v116 offset:11264\n\t"
        "v_mfma_f32_16x16x32_bf16 v[8:11], a[96:99], v[56:59], v[8:11]\n\t"
        "v_mfma_f32_16x16x32_bf16 v[12:15], a[100:103], v[56:59], v[12:15]\n\t"
        "v_mfma_f32_16x16x32_bf16 v[16:19], a[104:107], v[56:59], v[16:19]\n\t"
        "s_add_u32 %[t0], %[ro], 6\n\t"
        "s_and_b32 %[t0], %[t0], 7\n\t"
        "s_lshl_b32 %[t0], %[t0], 12\n\t"
        "s_add_u32 %[t0], %[t0], %[cu]\n\t"
        "buffer_load_dwordx4 v[56:59], v117, %[sw], %[t0] offen offset:0 sc1\n\t"
        "s_waitcnt vmcnt(20) lgkmcnt(2)\n\t"
        "v_mfma_f32_16x16x32_bf16 v[20:23], v[88:91], v[60:63], v[20:23]\n\t"
        "ds_read_b128 v[88:91], v116 offset:12288\n\t"
        "v_mfma_f32_16x16x32_bf16 v[8:11], a[108:111], v[60:63], v[8:11]\n\t"
        "v_mfma_f32_16x16x32_bf16 v[12:15], a[112:115], v[60:63], v[12:15]\n\t"
        "v_mfma_f32_16x16x32_bf16 v[16:19], a[116:119], v[60:63], v[16:19]\n\t"
        "buffer_load_dwordx4 v[60:63], v117, %[sw], %[t0] offen offset:1024 sc1\n\t"
        "s_waitcnt vmcnt(20) lgkmcnt(2)\n\t"
        "v_mfma_f32_16x16x32_bf16 v[20:23], v[92:95], v[64:67], v[20:23]\n\t"
        "ds_read_b128 v[92:95], v116 offset:13312\n\t"
        "v_mfma_f32_16x16x32_bf16 v[8:11], a[120:123], v[64:67], v[8:11]\n\t"
        "v_mfma_f32_16x16x32_bf16 v[12:15], a[124:127], v[64:67], v[12:15]\n\t"
        "v_mfma_f32_16x16x32_bf16 v[16:19], a[128:131], v[64:67], v[16:19]\n\t"
        "buffer_load_dwordx4 v[64:67], v117, %[sw], %[t0] offen offset:2048 sc1\n\t"
        "s_waitcnt vmcnt(20) lgkmcnt(2)\n\t"
        "v_mfma_f32_16x16x32_bf16 v[20:23], v[96:99], v[68:71], v[20:23]\n\t"
        "ds_read_b128 v[96:99], v116 offset:14336\n\t"
        "v_mfma_f32_16x16x32_bf16 v[8:11], a[132:135], v[68:71], v[8:11]\n\t"
        "v_mfma_f32_16x16x32_bf16 v[12:15], a[136:139], v[68:71], v[12:15]\n\t"
        "v_mfma_f32_16x16x32_bf16 v[16:19], a[140:143], v[68:71], v[16:19]\n\t"
        "buffer_load_dwordx4 v[68:71], v117, %[sw], %[t0] offen offset:3072 sc1\n\t"
        "s_waitcnt vmcnt(20) lgkmcnt(2)\n\t"
        "v_mfma_f32_16x16x32_bf16 v[20:23], v[88:91], v[72:75], v[20:23]\n\t"
        "ds_read_b128 v[88:91], v116 offset:15360\n\t"
        "v_mfma_f32_16x16x32_bf16 v[8:11], a[144:147], v[72:75], v[8:11]\n\t"
        "v_mfma_f32_16x16x32_bf16 v[12:15], a[148:151], v[72:75], v[12:15]\n\t"
        "v_mfma_f32_16x16x32_bf16 v[16:19], a[152:155], v[72:75], v[16:19]\n\t"
        "s_add_u32 %[t0], %[ro], 7\n\t"
        "s_and_b32 %[t0], %[t0], 7\n\t"
        "s_lshl_b32 %[t0], %[t0], 12\n\t"
        "s_add_u32 %[t0], %[t0], %[cu]\n\t"
        "buffer_load_dwordx4 v[72:75], v117, %[sw], %[t0] offen offset:0 sc1\n\t"
        "s_waitcnt vmcnt(13) lgkmcnt(2)\n\t"
        "v_mfma_f32_16x16x32_bf16 v[20:23], v[92:95], v[76:79], v[20:23]\n\t"
        "ds_read_b128 v[92:95], v116 offset:16384\n\t"
        "v_min_u32_dpp v103, v103, v103 quad_perm:[1,0,3,2] row_mask:0xf bank_mask:0xf\n\t"
        "s_nop 0\n\t"
        "v_mfma_f32_16x16x32_bf16 v[8:11], a[156:159], v[76:79], v[8:11]\n\t"
        "v_min_u32_dpp v103, v103, v103 quad_perm:[2,3,0,1] row_mask:0xf bank_mask:0xf\n\t"
        "s_nop 0\n\t"
        "v_mfma_f32_16x16x32_bf16 v[12:15], a[160:163], v[76:79], v[12:15]\n\t"
        "v_min_u32_dpp v103, v103, v103 row_ror:4 row_mask:0xf bank_mask:0xf\n\t"
        "s_nop 0\n\t"
        "v_mfma_f32_16x16x32_bf16 v[16:19], a[164:167], v[76:79], v[16:19]\n\t"
        "buffer_load_dwordx4 v[76:79], v117, %[sw], %[t0] offen offset:1024 sc1\n\t"
        "s_waitcnt vmcnt(20) lgkmcnt(2)\n\t"
        "v_mfma_f32_16x16x32_bf16 v[20:23], v[96:99], v[80:83], v[20:23]\n\t"
        "ds_read_b128 v[96:99], v116 offset:17408\n\t"
        "v_readfirstlane_b32 %[pg], v103\n\t"
        "s_add_u32 %[t1], %[tl], 8\n\t"
        "s_lshl_b32 %[nx], %[t1], 17\n\t"
        "s_lshr_b32 %[t1], %[t1], 1\n\t"
        "v_mfma_f32_16x16x32_bf16 v[8:11], a[168:171], v[80:83], v[8:11]\n\t"
        "s_cmp_gt_u32 %[pg], %[t1]\n\t"
        "s_cselect_b32 %[nx], %[nx], 0xf0000000\n\t"
        "v_mfma_f32_16x16x32_bf16 v[12:15], a[172:175], v[80:83], v[12:15]\n\t"
        "v_mfma_f32_16x16x32_bf16 v[16:19], a[176:179], v[80:83], v[16:19]\n\t"
        "buffer_load_dwordx4 v[80:83], v117, %[sw], %[t0] offen offset:2048 sc1\n\t"
        "s_waitcnt vmcnt(20) lgkmcnt(2)\n\t"
        "v_mfma_f32_16x16x32_bf16 v[20:23], v[88:91], v[84:87], v[20:23]\n\t"
        "ds_read_b128 v[88:91], v116 offset:18432\n\t"
        "v_mfma_f32_16x16x32_bf16 v[8:11], a[180:183], v[84:87], v[8:11]\n\t"
        "v_mfma_f32_16x16x32_bf16 v[12:15], a[184:187], v[84:87], v[12:15]\n\t"
        "v_mfma_f32_16x16x32_bf16 v[16:19], a[188:191], v[84:87], v[16:19]\n\t"
        "buffer_load_dwordx4 v[84:87], v117, %[sw], %[t0] offen offset:3072 sc1\n\t"
        "s_waitcnt vmcnt(15) lgkmcnt(2)\n\t"
        "v_mfma_f32_16x16x32_bf16 v[20:23], v[92:95], v[24:27], v[20:23]\n\t"
        "ds_read_b128 v[92:95], v116 offset:19456\n\t"
        "v_mfma_f32_16x16x32_bf16 v[8:11], a[192:195], v[24:27], v[8:11]\n\t"
        "v_mfma_f32_16x16x32_bf16 v[12:15], a[196:199], v[24:27], v[12:15]\n\t"
        "v_mfma_f32_16x16x32_bf16 v[16:19], a[200:203], v[24:27], v[16:19]\n\t"
        "s_add_u32 %[t0], %[ro], 0\n\t"
        "s_and_b32 %[t0], %[t0], 7\n\t"
        "s_lshl_b32 %[t0], %[t0], 12\n\t"
        "s_add_u32 %[t0], %[t0], %[nx]\n\t"
        "buffer_load_dwordx4 v[24:27], v117, %[sw], %[t0] offen offset:0 sc1\n\t"
        "s_waitcnt vmcnt(15) lgkmcnt(2)\n\t"
        "v_mfma_f32_16x16x32_bf16 v[20:23], v[96:99], v[28:31], v[20:23]\n\t"
        "ds_read_b128 v[96:99], v116 offset:20480\n\t"
        "v_mfma_f32_16x16x32_bf16 v[8:11], a[204:207], v[28:31], v[8:11]\n\t"
        "v_mfma_f32_16x16x32_bf16 v[12:15], a[208:211], v[28:31], v[12:15]\n\t"
        "v_sub_f32_e32 v104, 1.0, v102\n\t"
        "v_mfma_f32_16x16x32_bf16 v[16:19], a[212:215], v[28:31], v[16:19]\n\t"
        "buffer_load_dwordx4 v[28:31], v117, %[sw], %[t0] offen offset:1024 sc1\n\t"
        "s_waitcnt vmcnt(15) lgkmcnt(2)\n\t"
        "v_mfma_f32_16x16x32_bf16 v[20:23], v[88:91], v[32:35], v[20:23]\n\t"
        "ds_read_b128 v[88:91], v116 offset:21504\n\t"
        "v_mfma_f32_16x16x32_bf16 v[8:11], a[216:219], v[32:35], v[8:11]\n\t"
        "v_mfma_f32_16x16x32_bf16 v[12:15], a[220:223], v[32:35], v[12:15]\n\t"
        "v_cvt_pk_bf16_f32 v104, 0, v104\n\t"
        "v_mfma_f32_16x16x32_bf16 v[16:19], a[224:227], v[32:35], v[16:19]\n\t"
        "buffer_load_dwordx4 v[32:35], v117, %[sw], %[t0] offen offset:2048 sc1\n\t"
        "s_waitcnt vmcnt(15) lgkmcnt(2)\n\t"
        "v_mfma_f32_16x16x32_bf16 v[20:23], v[92:95], v[36:39], v[20:23]\n\t"
        "ds_read_b128 v[92:95], v116 offset:22528\n\t"
        "v_mfma_f32_16x16x32_bf16 v[8:11], a[228:231], v[36:39], v[8:11]\n\t"
        "v_mfma_f32_16x16x32_bf16 v[12:15], a[232:235], v[36:39], v[12:15]\n\t"
        "v_lshlrev_b32_e32 v106, 16, v100\n\t"
        "v_mfma_f32_16x16x32_bf16 v[16:19], a[236:239], v[36:39], v[16:19]\n\t"
        "buffer_load_dwordx4 v[36:39], v117, %[sw], %[t0] offen offset:3072 sc1\n\t"
        "s_waitcnt vmcnt(15) lgkmcnt(2)\n\t"
        "v_mfma_f32_16x16x32_bf16 v[20:23], v[96:99], v[40:43], v[20:23]\n\t"
        "ds_read_b128 v[96:99], v116 offset:23552\n\t"
        "v_mfma_f32_16x16x32_bf16 v[8:11], a[240:243], v[40:43], v[8:11]\n\t"
        "v_mfma_f32_16x16x32_bf16 v[12:15], a[244:247], v[40:43], v[12:15]\n\t"
        "v_mul_f32_e32 v106, v102, v106\n\t"
        "v_mfma_f32_16x16x32_bf16 v[16:19], a[248:251], v[40:43], v[16:19]\n\t"
        "s_add_u32 %[t0], %[ro], 1\n\t"
        "s_and_b32 %[t0], %[t0], 7\n\t"
        "s_lshl_b32 %[t0], %[t0], 12\n\t"
        "s_add_u32 %[t0], %[t0], %[nx]\n\t"
        "buffer_load_dwordx4 v[40:43], v117, %[sw], %[t0] offen offset:0 sc1\n\t"
        "s_waitcnt vmcnt(15) lgkmcnt(2)\n\t"
        "v_mfma_f32_16x16x32_bf16 v[20:23], v[88:91], v[44:47], v[20:23]\n\t"
        "ds_read_b128 v[88:91], v116 offset:24576\n\t"
        "v_mfma_f32_16x16x32_bf16 v[8:11], a[252:255], v[44:47], v[8:11]\n\t"
        "v_mfma_f32_16x16x32_bf16 v[12:15], v[128:131], v[44:47], v[12:15]\n\t"
        "v_cvt_pk_bf16_f32 v106, 0, v106\n\t"
        "v_mfma_f32_16x16x32_bf16 v[16:19], v[132:135], v[44:47], v[16:19]\n\t"
        "buffer_load_dwordx4 v[44:47], v117, %[sw], %[t0] offen offset:1024 sc1\n\t"
        "s_waitcnt vmcnt(15) lgkmcnt(2)\n\t"
        "v_mfma_f32_16x16x32_bf16 v[20:23], v[92:95], v[48:51], v[20:23]\n\t"
        "ds_read_b128 v[92:95], v116 offset:25600\n\t"
        "v_mfma_f32_16x16x32_bf16 v[8:11], v[136:139], v[48:51], v[8:11]\n\t"
        "v_mfma_f32_16x16x32_bf16 v[12:15], v[140:143], v[48:51], v[12:15]\n\t"
        "v_and_b32_e32 v107, 0xffff0000, v100\n\t"
        "v_mfma_f32_16x16x32_bf16 v[16:19], v[144:147], v[48:51], v[16:19]\n\t"
        "buffer_load_dwordx4 v[48:51], v117, %[sw], %[t0] offen offset:2048 sc1\n\t"
        "s_waitcnt vmcnt(15) lgkmcnt(2)\n\t"
        "v_mfma_f32_16x16x32_bf16 v[20:23], v[96:99], v[52:55], v[20:23]\n\t"
        "ds_read_b128 v[96:99], v116 offset:26624\n\t"
        "v_mfma_f32_16x16x32_bf16 v[8:11], v[148:151], v[52:55], v[8:11]\n\t"
        "v_mfma_f32_16x16x32_bf16 v[12:15], v[152:155], v[52:55], v[12:15]\n\t"
        "v_mul_f32_e32 v107, v102, v107\n\t"
        "v_mfma_f32_16x16x32_bf16 v[16:19], v[156:159], v[52:55], v[16:19]\n\t"
        "buffer_load_dwordx4 v[52:55], v117, %[sw], %[t0] offen offset:3072 sc1\n\t"
        "s_waitcnt vmcnt(15) lgkmcnt(2)\n\t"
        "v_mfma_f32_16x16x32_bf16 v[20:23], v[88:91], v[56:59], v[20:23]\n\t"
        "ds_read_b128 v[88:91], v116 offset:27648\n\t"
        "v_mfma_f32_16x16x32_bf16 v[8:11], v[160:163], v[56:59], v[8:11]\n\t"
        "v_mfma_f32_16x16x32_bf16 v[12:15], v[164:167], v[56:59], v[12:15]\n\t"
        "v_cvt_pk_bf16_f32 v107, 0, v107\n\t"
        "v_mfma_f32_16x16x32_bf16 v[16:19], v[168:171], v[56:59], v[16:19]\n\t"
        "s_add_u32 %[t0], %[ro], 2\n\t"
        "s_and_b32 %[t0], %[t0], 7\n\t"
        "s_lshl_b32 %[t0], %[t0], 12\n\t"
        "s_add_u32 %[t0], %[t0], %[nx]\n\t"
        "buffer_load_dwordx4 v[56:59], v117, %[sw], %[t0] offen offset:0 sc1\n\t"
        "s_waitcnt vmcnt(15) lgkmcnt(2)\n\t"
        "v_mfma_f32_16x16x32_bf16 v[20:23], v[92:95], v[60:63], v[20:23]\n\t"
        "ds_read_b128 v[92:95], v116 offset:28672\n\t"
        "v_mfma_f32_16x16x32_bf16 v[8:11], v[172:175], v[60:63], v[8:11]\n\t"
        "v_mfma_f32_16x16x32_bf16 v[12:15], v[176:179], v[60:63], v[12:15]\n\t"
        "v_lshlrev_b32_e32 v108, 16, v101\n\t"
        "v_mfma_f32_16x16x32_bf16 v[16:19], v[180:183], v[60:63], v[16:19]\n\t"
        "buffer_load_dwordx4 v[60:63], v117, %[sw], %[t0] offen offset:1024 sc1\n\t"
        "s_waitcnt vmcnt(15) lgkmcnt(2)\n\t"
        "v_mfma_f32_16x16x32_bf16 v[20:23], v[96:99], v[64:67], v[20:23]\n\t"
        "ds_read_b128 v[96:99], v116 offset:29696\n\t"
        "v_mfma_f32_16x16x32_bf16 v[8:11], v[184:187], v[64:67], v[8:11]\n\t"
        "v_mfma_f32_16x16x32_bf16 v[12:15], v[188:191], v[64:67], v[12:15]\n\t"
        "v_mul_f32_e32 v108, v102, v108\n\t"
        "v_mfma_f32_16x16x32_bf16 v[16:19], v[192:195], v[64:67], v[16:19]\n\t"
        "buffer_load_dwordx4 v[64:67], v117, %[sw], %[t0] offen offset:2048 sc1\n\t"
        "s_waitcnt vmcnt(15) lgkmcnt(2)\n\t"
        "v_mfma_f32_16x16x32_bf16 v[20:23], v[88:91], v[68:71], v[20:23]\n\t"
        "ds_read_b128 v[88:91], v116 offset:30720\n\t"
        "v_mfma_f32_16x16x32_bf16 v[8:11], v[196:199], v[68:71], v[8:11]\n\t"
        "v_mfma_f32_16x16x32_bf16 v[12:15], v[200:203], v[68:71], v[12:15]\n\t"
        "v_cvt_pk_bf16_f32 v108, 0, v108\n\t"
        "v_mfma_f32_16x16x32_bf16 v[16:19], v[204:207], v[68:71], v[16:19]\n\t"
        "buffer_load_dwordx4 v[68:71], v117, %[sw], %[t0] offen offset:3072 sc1\n\t"
        "s_waitcnt vmcnt(15) lgkmcnt(2)\n\t"
        "v_mfma_f32_16x16x32_bf16 v[20:23], v[92:95], v[72:75], v[20:23]\n\t"
        "ds_read_b128 v[92:95], v116 offset:31744\n\t"
        "v_mfma_f32_16x16x32_bf16 v[8:11], v[208:211], v[72:75], v[8:11]\n\t"
        "v_mfma_f32_16x16x32_bf16 v[12:15], v[212:215], v[72:75], v[12:15]\n\t"
        "v_and_b32_e32 v109, 0xffff0000, v101\n\t"
        "v_mfma_f32_16x16x32_bf16 v[16:19], v[216:219], v[72:75], v[16:19]\n\t"
        "s_add_u32 %[t0], %[ro], 3\n\t"
        "s_and_b32 %[t0], %[t0], 7\n\t"
        "s_lshl_b32 %[t0], %[t0], 12\n\t"
        "s_add_u32 %[t0], %[t0], %[nx]\n\t"
        "buffer_load_dwordx4 v[72:75], v117, %[sw], %[t0] offen offset:0 sc1\n\t"
        "s_waitcnt vmcnt(15) lgkmcnt(2)\n\t"
        "v_mfma_f32_16x16x32_bf16 v[20:23], v[96:99], v[76:79], v[20:23]\n\t"
        "v_mfma_f32_16x16x32_bf16 v[8:11], v[220:223], v[76:79], v[8:11]\n\t"
        "v_mfma_f32_16x16x32_bf16 v[12:15], v[224:227], v[76:79], v[12:15]\n\t"
        "v_mul_f32_e32 v109, v102, v109\n\t"
        "v_mfma_f32_16x16x32_bf16 v[16:19], v[228:231], v[76:79], v[16:19]\n\t"
        "buffer_load_dwordx4 v[76:79], v117, %[sw], %[t0] offen offset:1024 sc1\n\t"
        "s_waitcnt vmcnt(15) lgkmcnt(1)\n\t"
        "v_mfma_f32_16x16x32_bf16 v[20:23], v[88:91], v[80:83], v[20:23]\n\t"
        "ds_read_b128 v[88:91], v116 offset:0\n\t"
        "v_mfma_f32_16x16x32_bf16 v[8:11], v[232:235], v[80:83], v[8:11]\n\t"
        "v_mfma_f32_16x16x32_bf16 v[12:15], v[236:239], v[80:83], v[12:15]\n\t"
        "v_cvt_pk_bf16_f32 v109, 0, v109\n\t"
        "v_mfma_f32_16x16x32_bf16 v[16:19], v[240:243], v[80:83], v[16:19]\n\t"
        "buffer_load_dwordx4 v[80:83], v117, %[sw], %[t0] offen offset:2048 sc1\n\t"
        "s_waitcnt vmcnt(15) lgkmcnt(1)\n\t"
        "v_mfma_f32_16x16x32_bf16 v[20:23], v[92:95], v[84:87], v[20:23]\n\t"
        "ds_read_b128 v[92:95], v116 offset:1024\n\t"
        "v_mfma_f32_16x16x32_bf16 v[8:11], v[244:247], v[84:87], v[8:11]\n\t"
        "ds_read_b128 v[96:99], v116 offset:2048\n\t"
        "v_mfma_f32_16x16x32_bf16 v[12:15], v[248:251], v[84:87], v[12:15]\n\t"
        "v_mfma_f32_16x16x32_bf16 v[16:19], v[252:255], v[84:87], v[16:19]\n\t"
        "buffer_load_dwordx4 v[84:87], v117, %[sw], %[t0] offen offset:3072 sc1\n\t"
        "LP_epi%=:\n\t"
        "s_lshl_b32 %[t0], %[tl], 15\n\t"
        "s_nop 0\n\t"
        "ds_write_b128 v122, v[20:23] offset:192\n\t"
        "s_nop 0\n\t"
        "ds_write_b128 v122, v[8:11] offset:0\n\t"
        "s_nop 0\n\t"
        "ds_write_b128 v122, v[12:15] offset:64\n\t"
        "ds_write_b128 v122, v[16:19] offset:128\n\t"
        "s_waitcnt lgkmcnt(0)\n\t"
        "s_barrier\n\t"
        "ds_read_b128 v[8:11], v123 offset:0\n\t"
        "ds_read_b128 v[12:15], v123 offset:4352\n\t"
        "ds_read_b128 v[16:19], v123 offset:8704\n\t"
        "ds_read_b128 v[20:23], v123 offset:13056\n\t"
        "s_waitcnt lgkmcnt(2)\n\t"
        "v_pk_add_f32 v[8:9], v[8:9], v[12:13]\n\t"
        "v_pk_add_f32 v[10:11], v[10:11], v[14:15]\n\t"
        "s_waitcnt lgkmcnt(0)\n\t"
        "v_pk_add_f32 v[16:17], v[16:17], v[20:21]\n\t"
        "v_pk_add_f32 v[18:19], v[18:19], v[22:23]\n\t"
        "v_pk_add_f32 v[8:9], v[8:9], v[16:17]\n\t"
        "v_pk_add_f32 v[10:11], v[10:11], v[18:19]\n\t"
        "v_cvt_pk_bf16_f32 v8, 0, v8\n\t"
        "v_cvt_pk_bf16_f32 v9, 0, v9\n\t"
        "v_cvt_pk_bf16_f32 v10, 0, v10\n\t"
        "v_cvt_pk_bf16_f32 v11, 0, v11\n\t"
        "v_mul_f32_e32 v8, v104, v8\n\t"
        "v_mul_f32_e32 v9, v104, v9\n\t"
        "v_mul_f32_e32 v10, v104, v10\n\t"
        "v_mul_f32_e32 v11, v104, v11\n\t"
        "v_cvt_pk_bf16_f32 v8, 0, v8\n\t"
        "v_cvt_pk_bf16_f32 v9, 0, v9\n\t"
        "v_cvt_pk_bf16_f32 v10, 0, v10\n\t"
        "v_cvt_pk_bf16_f32 v11, 0, v11\n\t"
        "v_add_f32_e32 v8, v106, v8\n\t"
        "v_add_f32_e32 v9, v107, v9\n\t"
        "v_add_f32_e32 v10, v108, v10\n\t"
        "v_add_f32_e32 v11, v109, v11\n\t"
        "v_cvt_pk_bf16_f32 v104, v8, v9\n\t"
        "v_cvt_pk_bf16_f32 v105, v10, v11\n\t"
        "buffer_store_dwordx2 v[104:105], v121, %[su], %[t0] offen sc1\n\t"
        "s_add_u32 %[tl], %[tl], 8\n\t"
        "s_cmp_ge_u32 %[tl], 128\n\t"
        "s_cbranch_scc1 LP_end%=\n\t"
        "s_cmp_lg_u32 %[nx], 0xf0000000\n\t"
        "s_cbranch_scc1 LP_tile%=\n\t"
        "s_branch LP_wait%=\n\t"
        "LP_end%=:\n\t"
        "s_waitcnt vmcnt(0) lgkmcnt(0)\n\t"
        ".Lk_end_P:\n\t"
        : [t0] "=&s"(t0), [t1] "=&s"(t1), [tl] "+s"(tl), [pg] "=&s"(pg), [cu] "=&s"(cu), [nx] "=&s"(nx), [sp] "=&s"(sp), [re] "=&s"(re), [tu] "=&s"(tu)
        : [so] "s"(so), [sw] "s"(sw), [su] "s"(su), [cv] "s"(F_CVY + nt * 128), [ro] "s"(PROT ? (nt + 2 * rp) & 7 : 0), [db] "s"(W_DBG + (128 + j) * 8192 + 512),
          "{v110}"((w * 1024 + (l >> 3)) * 2048 + nt * 128 + (l & 7) * 16), "{v111}"(stg + ((l * 16) ^ ((l & 48) << 1))), "{v112}"(lr), "{v113}"(lr ^ 32u), "{v114}"(lr ^ 64u), "{v115}"(lr ^ 96u),
          "{v116}"(lb + w * 32768 + l * 16), "{v117}"(W_OX + w * 32768 + l * 16), "{v118}"(F_PROG + (w * 8 + (l & 7)) * 128),
          "{v119}"(W_A + ((nt * 2 + (n >> 3)) * 4 + 2 * (n & 1)) * 32768 + w * 4096 + ((n & 7) >> 1) * 16 + g * 4), "{v120}"(W_GATE + (4 * w + g) * 4), "{v121}"((4 * w + g) * 2048 + nt * 128 + n * 8),
          "{v122}"(lb + 131072 + w * 4352 + n * 272 + g * 16), "{v123}"(lb + 131072 + (4 * w + g) * 272 + n * 16)
        : "memory", "scc", "vcc", "v8", "v9", "v10", "v11", "v12", "v13", "v14", "v15", "v16", "v17", "v18", "v19", "v20", "v21", "v22", "v23", "v24", "v25", "v26", "v27", "v28", "v29", "v30", "v31", "v32", "v33", "v34", "v35", "v36", "v37", "v38", "v39", "v40", "v41", "v42", "v43", "v44", "v45", "v46", "v47", "v48", "v49", "v50", "v51", "v52", "v53", "v54", "v55", "v56", "v57", "v58", "v59", "v60", "v61", "v62", "v63", "v64", "v65", "v66", "v67", "v68", "v69", "v70", "v71", "v72", "v73", "v74", "v75", "v76", "v77", "v78", "v79", "v80", "v81", "v82", "v83", "v84", "v85", "v86", "v87", "v88", "v89", "v90", "v91", "v92", "v93", "v94", "v95", "v96", "v97", "v98", "v99", "v100", "v101", "v102", "v103", "v104", "v105", "v106", "v107", "v108", "v109", "v124", "v125", "v126", "v127", "v128", "v129", "v130", "v131", "v132", "v133", "v134", "v135", "v136", "v137", "v138", "v139", "v140", "v141", "v142", "v143", "v144", "v145", "v146", "v147", "v148", "v149", "v150", "v151", "v152", "v153", "v154", "v155", "v156", "v157", "v158", "v159", "v160", "v161", "v162", "v163", "v164", "v165", "v166", "v167", "v168", "v169", "v170", "v171", "v172", "v173", "v174", "v175", "v176", "v177", "v178", "v179", "v180", "v181", "v182", "v183", "v184", "v185", "v186", "v187", "v188", "v189", "v190", "v191", "v192", "v193", "v194", "v195", "v196", "v197", "v198", "v199", "v200", "v201", "v202", "v203", "v204", "v205", "v206", "v207", "v208", "v209", "v210", "v211", "v212", "v213", "v214", "v215", "v216", "v217", "v218", "v219", "v220", "v221", "v222", "v223", "v224", "v225", "v226", "v227", "v228", "v229", "v230", "v231", "v232", "v233", "v234", "v235", "v236", "v237", "v238", "v239", "v240", "v241", "v242", "v243", "v244", "v245", "v246", "v247", "v248", "v249", "v250", "v251", "v252", "v253", "v254", "v255", "a0", "a1", "a2", "a3", "a4", "a5", "a6", "a7", "a8", "a9", "a10", "a11", "a12", "a13", "a14", "a15", "a16", "a17", "a18", "a19", "a20", "a21", "a22", "a23", "a24", "a25", "a26", "a27", "a28", "a29", "a30", "a31", "a32", "a33", "a34", "a35", "a36", "a37", "a38", "a39", "a40", "a41", "a42", "a43", "a44", "a45", "a46", "a47", "a48", "a49", "a50", "a51", "a52", "a53", "a54", "a55", "a56", "a57", "a58", "a59", "a60", "a61", "a62", "a63", "a64", "a65", "a66", "a67", "a68", "a69", "a70", "a71", "a72", "a73", "a74", "a75", "a76", "a77", "a78", "a79", "a80", "a81", "a82", "a83", "a84", "a85", "a86", "a87", "a88", "a89", "a90", "a91", "a92", "a93", "a94", "a95", "a96", "a97", "a98", "a99", "a100", "a101", "a102", "a103", "a104", "a105", "a106", "a107", "a108", "a109", "a110", "a111", "a112", "a113", "a114", "a115", "a116", "a117", "a118", "a119", "a120", "a121", "a122", "a123", "a124", "a125", "a126", "a127", "a128", "a129", "a130", "a131", "a132", "a133", "a134", "a135", "a136", "a137", "a138", "a139", "a140", "a141", "a142", "a143", "a144", "a145", "a146", "a147", "a148", "a149", "a150", "a151", "a152", "a153", "a154", "a155", "a156", "a157", "a158", "a159", "a160", "a161", "a162", "a163", "a164", "a165", "a166", "a167", "a168", "a169", "a170", "a171", "a172", "a173", "a174", "a175", "a176", "a177", "a178", "a179", "a180", "a181", "a182", "a183", "a184", "a185", "a186", "a187", "a188", "a189", "a190", "a191", "a192", "a193", "a194", "a195", "a196", "a197", "a198", "a199", "a200", "a201", "a202", "a203", "a204", "a205", "a206", "a207", "a208", "a209", "a210", "a211", "a212", "a213", "a214", "a215", "a216", "a217", "a218", "a219", "a220", "a221", "a222", "a223", "a224", "a225", "a226", "a227", "a228", "a229", "a230", "a231", "a232", "a233", "a234", "a235", "a236", "a237", "a238", "a239", "a240", "a241", "a242", "a243", "a244", "a245", "a246", "a247", "a248", "a249", "a250", "a251", "a252", "a253", "a254", "a255");
}
__device__ __forceinline__ void bulk_block(const Params& p, rsrc_t r, unsigned char* lds, const int wv, const int j, const unsigned te) {
    BSTAMP(0);
    proj_block(p, lds, wv, (j & 6) * 4 + (j >> 4), ((j >> 2) & 2) | (j & 1), j);
    BSTAMP(2);
    const int un = ((j & 7) >> 1) * 32 + (j >> 3) * 2 + (j & 1);
    if (XG) { gate_block<4>(p, r, j, wv); conv_prep(p, r, wv, un >> 2, un & 3, (un & 3) + 1); }
    BSTAMP(5);
    if (!conv_core(p, r, lds, wv, un)) return;
    if (XG) { if (!wait_word(r, F_CONV, 128u, 4096u)) return; }
    BSTAMP(3);
    if (GDLY) wait_long(rtnow() + GDLY);
    out_block(p, lds, wv, ((j & 7) >> 1) + (j & 1) * 4, j >> 3, j);
    BSTAMP(7);
}

__device__ __forceinline__ void rec_block(const Params& p, rsrc_t r, unsigned char* lds, const int wv, const int b) {
    const int xq = (b & 7) >> 1, t = (b >> 3) * 2 + (b & 1);
    const unsigned lb = __builtin_amdgcn_readfirstlane((unsigned)(size_t)lds);
    int kind, id, rg = 0, rot = 0, bq = 99;
    if (RPL == 0) { kind = t < 8 ? 0 : t < 24 ? 1 : 2; id = t < 8 ? xq * 8 + t : t < 24 ? (xq & 1) * 16 + t - 8 : xq * 8 + t - 24; rg = xq >> 1; }
    else if (RPL == 4) { kind = t < 8 ? 0 : t < 24 ? 1 : 2; id = t < 8 ? xq * 8 + t : t < 24 ? xq * 8 + ((t - 8) >> 1) : xq * 8 + t - 24; rg = t & 1; }
    else if (RPL == 1) { kind = xq == 0 ? 0 : xq < 3 ? 1 : 2; id = t; rg = xq - 1; }
    else if (RPL == 2) { const int na = xq == 2 ? 10 : 11, q = xq * 21 + t - na; kind = xq == 3 ? 2 : t < na ? 0 : 1; id = xq == 3 ? t : t < na ? xq * 11 + t : q >> 1; rg = q & 1; }
    else { const int na = xq == 2 ? 12 : 10, q = t - na; kind = xq == 3 ? 2 : t < na ? 0 : 1; rg = xq < 2 ? xq : q & 1; bq = q;
        const int c2 = q >> 1, cs = xq < 2 ? (q < 6 ? q : q < 20 ? q + 2 : q + 4) : (c2 < 2 ? 6 + c2 : c2 < 4 ? 20 + c2 : 22 + c2);
        id = xq == 3 ? t : t < na ? xq * 10 + t : cs; rot = !ROT ? 0 : xq < 2 ? (cs & 1) * 4 : ((cs & 1) * 2 + rg) * 2; }
    if (kind == 0) rec_a(p, lb, wv, id, xq);
    else if (kind == 1) rec_b(p, lb, wv, id, rg, xq, rot, bq);
    else {
        const int hi = id, j = hi - 32; (void)j;
        BSTAMP(0);
        rec_helper(p, r, wv, hi);
        BSTAMP(6);
        if (XG) return;
        if (XWAIT == 1) { if (!wait_word(r, F_GO + (hi >> 1) * 128, 1u, 1024u)) return; }
        if (XSTG) wait_long(rtnow() + (hi & 7) * XSTG);
        gate_block<16>(p, r, hi, wv);
        BSTAMP(2);
        if (XWAIT == 2) { if (!wait_word(r, F_GO + (hi >> 1) * 128, 1u, 1024u)) return; }
        conv_prep(p, r, wv, hi, 0, 4);
        BSTAMP(3);
    }
}

__global__ void __launch_bounds__(256, 1) k_main(Params p) {
    extern __shared__ __attribute__((aligned(16))) unsigned char lds[];
    rsrc_t r = __builtin_amdgcn_make_buffer_rsrc(p.ws, 0, W_TOTAL, 0x00020000);
    const int wv = __builtin_amdgcn_readfirstlane(threadIdx.x >> 6);
    int role = blockIdx.x;
    unsigned ctouch = 0u;
    const unsigned te = 0u;
    if (CODE_T) {
        unsigned long long pc; int ea, eb, eg;
        asm volatile("s_getpc_b64 %0\n.Lk_begin:\n\ts_mov_b32 %1, .Lk_end_A-.Lk_begin\n\ts_mov_b32 %2, .Lk_end_B-.Lk_begin\n\ts_mov_b32 %3, .Lk_end_P-.Lk_begin" : "=s"(pc), "=s"(ea), "=s"(eb), "=s"(eg));
        const int o = ((blockIdx.x >> 3) * 64 + (threadIdx.x >> 2)) * 128;
        if ((threadIdx.x & 3) == 0 && o < max(ea, max(eb, eg)) - 4) asm volatile("global_load_dword %0, %1, off" : "+v"(ctouch) : "v"((const unsigned char*)pc + o) : "memory");
    }
    if (blockIdx.x == 0 && wv == 1) {
        const int l = threadIdx.x & 63; const v4u z = {0u, 0u, 0u, 0u};
#pragma unroll
        for (int i = 0; i < 6; ++i) st16(r, F_CONV + i * 1024 + l * 16, z);
        DRAIN();
        for (unsigned spin = 0; spin < (1u << 20); ++spin) {
            if (l < 8) __hip_atomic_fetch_add((unsigned*)(p.ws + F_BEAT + l * 128), 1u, __ATOMIC_RELAXED, __HIP_MEMORY_SCOPE_AGENT);
            __builtin_amdgcn_s_sleep(BEAT_SLP);
            unsigned c = ld4(r, l < 8 ? F_TKT + l * 64 : OOB);
            c += __shfl_xor(c, 1); c += __shfl_xor(c, 2); c += __shfl_xor(c, 4);
            if (__builtin_amdgcn_readfirstlane(c) >= 256u) break; }
    }
    if (threadIdx.x == 0) {
        const unsigned bo = F_BEAT + (blockIdx.x & 7) * 128, b0 = ld4(r, bo);
        for (unsigned spin = 0; spin < (1u << 22); ++spin) { MEMBAR(); if (ld4(r, bo) != b0) break; }
        MEMBAR();
        unsigned xcc; asm volatile("s_getreg_b32 %0, hwreg(HW_REG_XCC_ID)" : "=s"(xcc)); xcc &= 7u;
        unsigned* tk = (unsigned*)(p.ws + F_TKT);
        unsigned t = __hip_atomic_fetch_add(tk + xcc * 16, 1u, __ATOMIC_RELAXED, __HIP_MEMORY_SCOPE_AGENT);
        if (t >= 32u) {
            unsigned o = __hip_atomic_fetch_add(tk + 128, 1u, __ATOMIC_RELAXED, __HIP_MEMORY_SCOPE_AGENT);
            for (unsigned spin = 0; spin < (1u << 22); ++spin) { unsigned tot = 0u;
                for (int x = 0; x < 8; ++x) tot += __hip_atomic_load(tk + x * 16, __ATOMIC_RELAXED, __HIP_MEMORY_SCOPE_AGENT);
                if (tot >= 256u) break; }
            for (int x = 0; x < 8; ++x) { const unsigned cnt = __hip_atomic_load(tk + x * 16, __ATOMIC_RELAXED, __HIP_MEMORY_SCOPE_AGENT), fr = cnt < 32u ? 32u - cnt : 0u;
                if (o < fr) { xcc = (unsigned)x; t = cnt + o; break; }
                o -= fr; }
        }
        *(volatile int*)lds = (int)((xcc >> 2) * 128u + (t >> 1) * 8u + (xcc & 3u) * 2u + (t & 1u));
    }
    asm volatile("s_waitcnt vmcnt(0)" : "+v"(ctouch) :: "memory");
    __syncthreads();
    role = __builtin_amdgcn_readfirstlane(*(volatile int*)lds) & 255;
    __syncthreads();
    if (role >= 128) bulk_block(p, r, lds, wv, role - 128, te); else rec_block(p, r, lds, wv, role);
}

extern "C" void kernel_launch(void* const* d_in, const int* in_sizes, int n_in, void* d_out, int out_size, void* d_ws, size_t ws_size, hipStream_t stream) {
    Params p;
    p.Aq = (const bf16_t*)d_in[0]; p.Ak = (const bf16_t*)d_in[1]; p.Av = (const bf16_t*)d_in[2];
    p.shift_k = (const bf16_t*)d_in[3]; p.ssm_k = (const bf16_t*)d_in[4]; p.ssm_D = (const bf16_t*)d_in[5];
    p.mem0 = (const bf16_t*)d_in[7]; p.Wq = (const bf16_t*)d_in[8]; p.Wk = (const bf16_t*)d_in[9];
    p.Wv = (const bf16_t*)d_in[10]; p.Wo = (const bf16_t*)d_in[11]; p.gate_w = (const bf16_t*)d_in[13];
    p.out = (bf16_t*)d_out; p.ws = (unsigned char*)d_ws;
    (void)hipFuncSetAttribute((const void*)k_main, hipFuncAttributeMaxDynamicSharedMemorySize, L_TOTAL);
    k_main<<<256, 256, L_TOTAL, stream>>>(p);
}
```

```cpp
#include <hip/hip_runtime.h>
#include <stdint.h>
typedef unsigned short bf16_t;
using f32x16 = __attribute__((ext_vector_type(16))) float;
typedef unsigned v4u __attribute__((ext_vector_type(4)));
typedef unsigned v2u __attribute__((ext_vector_type(2)));
typedef __amdgpu_buffer_rsrc_t rsrc_t;
typedef __bf16 hbf2 __attribute__((ext_vector_type(2)));
typedef float f32x2 __attribute__((ext_vector_type(2)));
__device__ __forceinline__ unsigned pack2(float a, float b) { f32x2 v = {a, b}; return __builtin_bit_cast(unsigned, __builtin_convertvector(v, hbf2)); }
__device__ __forceinline__ float rbfh(float f) { return __uint_as_float(pack2(f, 0.f) << 16); }
__device__ __forceinline__ float blo(unsigned x) { return __uint_as_float(x << 16); }
__device__ __forceinline__ float bhi(unsigned x) { return __uint_as_float(x & 0xFFFF0000u); }

constexpr int S = 2048, D = 1024;

constexpr unsigned F_KVQ = 0;
constexpr unsigned F_CONV = 32768;
constexpr unsigned F_ERR = 33024;
constexpr unsigned F_CAL = 33792;
constexpr unsigned F_RDY0 = 34304;
constexpr unsigned F_TKT = 34816;
constexpr unsigned F_GO = 36864;
constexpr unsigned F_BEAT = 40960;
constexpr unsigned F_PROG = 49152;
constexpr unsigned F_CVY = 57344;
constexpr unsigned W_A = 8568832;
constexpr unsigned W_GATE = 65536;
constexpr unsigned W_QX = W_A + S * D * 2 + 8192;
constexpr unsigned W_KX = W_QX + S * 4096 * 2;
constexpr unsigned W_VF = W_KX + S * 4096 * 2;
constexpr unsigned W_OX = W_VF + S * 4096 * 2;
constexpr unsigned W_Y = W_OX + S * 4096 * 2;
constexpr unsigned W_U = 115531776;
constexpr unsigned W_TOTAL = W_U + S * D * 2;
constexpr unsigned OOB = 0xF0000000u;
constexpr int L_TOTAL = 163840;

__device__ __forceinline__ v4u ld16(rsrc_t r, unsigned off) { return __builtin_amdgcn_raw_buffer_load_b128(r, off, 0, 16); }
__device__ __forceinline__ v2u ld8(rsrc_t r, unsigned off) { return __builtin_amdgcn_raw_buffer_load_b64(r, off, 0, 16); }
__device__ __forceinline__ unsigned ld4(rsrc_t r, unsigned off) { return __builtin_amdgcn_raw_buffer_load_b32(r, off, 0, 16); }
__device__ __forceinline__ void st16(rsrc_t r, unsigned off, v4u v) { __builtin_amdgcn_raw_buffer_store_b128(v, r, off, 0, 16); }
__device__ __forceinline__ void st4(rsrc_t r, unsigned off, unsigned v) { __builtin_amdgcn_raw_buffer_store_b32(v, r, off, 0, 16); }
#define D1 128
#define D2 77
#define LT_A 1
#define PEN 48
#define CAL_T 450u
#define XSLP 1
#define T2U 2
#define TPRE 48
#define MEMBAR() asm volatile("" ::: "memory")
#define DRAIN() asm volatile("s_waitcnt vmcnt(0)" ::: "memory")
__device__ __forceinline__ int otid_(int wv) { int t; asm volatile("v_mbcnt_lo_u32_b32 %0, -1, 0\n\tv_mbcnt_hi_u32_b32 %0, -1, %0" : "=v"(t)); return wv * 64 + t; }
#define otid() otid_(wv)
__device__ __forceinline__ bool wait_word(rsrc_t r, unsigned off, unsigned want, unsigned code) {
    for (unsigned spin = 0;; ++spin) { MEMBAR();
        const unsigned v = __builtin_amdgcn_readfirstlane(ld4(r, off));
        if (v >= want) { MEMBAR(); return true; }
        if (spin > (1u << 20)) { st4(r, F_ERR, code); return false; }
        __builtin_amdgcn_s_sleep(8); }
}
__device__ __forceinline__ void touch_l2(const void* q, unsigned ldsdst) { asm volatile("s_mov_b32 m0, %1\n\ts_nop 0\n\tglobal_load_lds_dword %0, off" :: "v"(q), "s"(ldsdst) : "m0", "memory"); }
__device__ __forceinline__ unsigned rtnow() { return (unsigned)__builtin_amdgcn_s_memrealtime(); }

struct Params {
    const bf16_t *Aq, *Ak, *Av, *shift_k, *ssm_k, *ssm_D, *mem0, *Wq, *Wk, *Wv, *Wo, *gate_w;
    bf16_t* out; unsigned char* ws;
};
__device__ __forceinline__ unsigned ho_base(const void* ws) { const size_t a = (size_t)ws; return (unsigned)((((a + W_TOTAL + 0x7FFFFFu) >> 23) << 23) - a); }
__device__ __forceinline__ v4u mk_srd(const void* p, unsigned bytes) { const size_t a = (size_t)p; v4u d;
    d.x = __builtin_amdgcn_readfirstlane((unsigned)a); d.y = __builtin_amdgcn_readfirstlane((unsigned)(a >> 32) & 0xFFFFu); d.z = __builtin_amdgcn_readfirstlane(bytes); d.w = 0x00020000u; return d; }
constexpr unsigned HO_BYTES = 2097152;
constexpr int L_KV = 90112, L_MS = 139264, L_IMG = 155648, IMG_PS = 1056, IMG_MS = 2112, LB_R = 8192, LB_STG = 16384, LB_STGP = 8704;
static_assert(F_KVQ == 0 && F_ERR == 33024 && F_CONV == 32768 && F_CAL == 33792 && F_RDY0 == 34304 && F_GO == 36864 && F_PROG == 49152 && W_QX == 12771328 && W_KX == 29548544 && W_OX == 63102976 && W_VF == 46325760 && L_KV == 90112 && L_MS == 139264 && L_IMG == 155648 && IMG_PS == 1056 && IMG_MS == 2112 && LB_R == 8192 && LB_STG == 16384 && LB_STGP == 8704, "constants of the generated code");
#define REC_S_IN "{s[8:11]}"(sh), "{s[12:15]}"(sw), "{s[16:19]}"(s1), "{s[20:23]}"(s2), "{s29}"(D1), "{s30}"(D2), "{s43}"(w), "{s44}"(id), "{s49}"(rb), "{s50}"(PEN), "{s51}"(LT_A), "{s45}"(F_GO + (cp * 4 + w) * 128)
__device__ __forceinline__ void rec_a(const Params& p, unsigned lb, const int wv, const int id, const int cp) {
    const int w = wv, l = otid() & 63, n = l & 15, g = l >> 4, h = id, rb = id;
    const v4u sh = mk_srd(p.ws + ho_base(p.ws), HO_BYTES - 8192), sw = mk_srd(p.ws, W_TOTAL), s1 = mk_srd(w < 2 ? p.Wq : p.Wk, 1024 * 4096 * 2), s2 = s1;
    const int r8 = l >> 3, pc = l & 7, pp = n >> 3, bb = (n >> 2) & 1, f = (g & 1) | (pp << 1);
    const unsigned stg = lb + 131072 + w * 8192;
    const unsigned g_w = r8 * 8192 + h * 256 + (w & 1) * 128 + pc * 16;
    const unsigned wb0 = stg + r8 * 128 + (((pc >> 1) ^ (r8 >> 2)) << 5) + (pc & 1) * 16, wb1 = wb0 ^ 64u;
    const unsigned rb0 = stg + (16 * (g >> 1) + 8 * pp + 4 * (g & 1) + bb) * 128 + (f << 5) + (n & 3) * 8;
    const unsigned a_mem = cp * 32768 + (w >> 1) * 16384 + (w & 1) * 4096 + l * 16;
    const unsigned l_ms = lb + L_MS + l * 16;
    const unsigned l_iw = lb + L_IMG + (w >> 1) * IMG_MS + (g >> 1) * IMG_PS + (64 * (w & 1) + n) * 8 + (g & 1) * 4;
    const unsigned l_iq = lb + L_IMG + (n & 1) * IMG_PS + (8 * g + (n >> 2)) * 8;
    const unsigned a_vm = (16 + (h >> 2)) * 16384 + (h & 3) * 2048 + w * 512 + (l & 31) * 16;
    const unsigned a_o = n < 8 ? (8 + (n >> 2) * 4 + (h >> 3)) * 16384 + (h & 7) * 1024 + w * 256 + (n & 3) * 64 + g * 16 : OOB;
    const unsigned a_ox = (((w & 1) * 32 + h) * 4 + w) * 1024 + l * 16;
    asm volatile(
        "s_nop 4\n\t"
        "s_lshl_b32 s34, s43, 2\n\t"
        "s_add_u32 s34, s34, 0\n\t"
        "s_and_b32 s34, s34, 15\n\t"
        "s_lshl_b32 s34, s34, 19\n\t"
        "buffer_load_dwordx4 v[224:227], v96, s[16:19], s34 offen nt\n\t"
        "s_add_u32 s34, s34, 0x10000\n\t"
        "buffer_load_dwordx4 v[228:231], v96, s[16:19], s34 offen nt\n\t"
        "s_add_u32 s34, s34, 0x10000\n\t"
        "buffer_load_dwordx4 v[232:235], v96, s[16:19], s34 offen nt\n\t"
        "s_add_u32 s34, s34, 0x10000\n\t"
        "buffer_load_dwordx4 v[236:239], v96, s[16:19], s34 offen nt\n\t"
        "s_add_u32 s34, s34, 0x10000\n\t"
        "buffer_load_dwordx4 v[240:243], v96, s[16:19], s34 offen nt\n\t"
        "s_add_u32 s34, s34, 0x10000\n\t"
        "buffer_load_dwordx4 v[244:247], v96, s[16:19], s34 offen nt\n\t"
        "s_add_u32 s34, s34, 0x10000\n\t"
        "buffer_load_dwordx4 v[248:251], v96, s[16:19], s34 offen nt\n\t"
        "s_add_u32 s34, s34, 0x10000\n\t"
        "buffer_load_dwordx4 v[252:255], v96, s[16:19], s34 offen nt\n\t"
        "s_lshl_b32 s34, s43, 2\n\t"
        "s_add_u32 s34, s34, 1\n\t"
        "s_and_b32 s34, s34, 15\n\t"
        "s_lshl_b32 s34, s34, 19\n\t"
        "buffer_load_dwordx4 v[0:3], v96, s[16:19], s34 offen nt\n\t"
        "s_add_u32 s34, s34, 0x10000\n\t"
        "buffer_load_dwordx4 v[4:7], v96, s[16:19], s34 offen nt\n\t"
        "s_add_u32 s34, s34, 0x10000\n\t"
        "buffer_load_dwordx4 v[8:11], v96, s[16:19], s34 offen nt\n\t"
        "s_add_u32 s34, s34, 0x10000\n\t"
        "buffer_load_dwordx4 v[12:15], v96, s[16:19], s34 offen nt\n\t"
        "s_add_u32 s34, s34, 0x10000\n\t"
        "buffer_load_dwordx4 v[16:19], v96, s[16:19], s34 offen nt\n\t"
        "s_add_u32 s34, s34, 0x10000\n\t"
        "buffer_load_dwordx4 v[20:23], v96, s[16:19], s34 offen nt\n\t"
        "s_add_u32 s34, s34, 0x10000\n\t"
        "buffer_load_dwordx4 v[24:27], v96, s[16:19], s34 offen nt\n\t"
        "s_add_u32 s34, s34, 0x10000\n\t"
        "buffer_load_dwordx4 v[28:31], v96, s[16:19], s34 offen nt\n\t"
        "s_lshl_b32 s34, s43, 2\n\t"
        "s_add_u32 s34, s34, 2\n\t"
        "s_and_b32 s34, s34, 15\n\t"
        "s_lshl_b32 s34, s34, 19\n\t"
        "buffer_load_dwordx4 v[32:35], v96, s[16:19], s34 offen nt\n\t"
        "s_add_u32 s34, s34, 0x10000\n\t"
        "buffer_load_dwordx4 v[36:39], v96, s[16:19], s34 offen nt\n\t"
        "s_add_u32 s34, s34, 0x10000\n\t"
        "buffer_load_dwordx4 v[40:43], v96, s[16:19], s34 offen nt\n\t"
        "s_add_u32 s34, s34, 0x10000\n\t"
        "buffer_load_dwordx4 v[44:47], v96, s[16:19], s34 offen nt\n\t"
        "s_add_u32 s34, s34, 0x10000\n\t"
        "buffer_load_dwordx4 v[48:51], v96, s[16:19], s34 offen nt\n\t"
        "s_add_u32 s34, s34, 0x10000\n\t"
        "buffer_load_dwordx4 v[52:55], v96, s[16:19], s34 offen nt\n\t"
        "s_add_u32 s34, s34, 0x10000\n\t"
        "buffer_load_dwordx4 v[56:59], v96, s[16:19], s34 offen nt\n\t"
        "s_add_u32 s34, s34, 0x10000\n\t"
        "buffer_load_dwordx4 v[60:63], v96, s[16:19], s34 offen nt\n\t"
        "v_cmp_le_i32_e64 s[60:61], 0, v126\n\t"
        "v_cmp_le_i32_e64 s[62:63], 1, v126\n\t"
        "v_cmp_le_i32_e64 s[64:65], 2, v126\n\t"
        "v_cmp_le_i32_e64 s[66:67], 3, v126\n\t"
        "v_cmp_le_i32_e64 s[68:69], 16, v126\n\t"
        "v_cmp_le_i32_e64 s[70:71], 17, v126\n\t"
        "v_cmp_le_i32_e64 s[72:73], 18, v126\n\t"
        "v_cmp_le_i32_e64 s[74:75], 19, v126\n\t"
        "s_mul_i32 s35, s43, 10240\n\t"
        "v_add_u32_e32 v126, s35, v124\n\t"
        "s_waitcnt vmcnt(16)\n\t"
        "ds_write_b128 v97, v[224:227] offset:0\n\t"
        "ds_write_b128 v98, v[228:231] offset:1024\n\t"
        "ds_write_b128 v97, v[232:235] offset:2048\n\t"
        "ds_write_b128 v98, v[236:239] offset:3072\n\t"
        "ds_write_b128 v97, v[240:243] offset:4096\n\t"
        "ds_write_b128 v98, v[244:247] offset:5120\n\t"
        "ds_write_b128 v97, v[248:251] offset:6144\n\t"
        "ds_write_b128 v98, v[252:255] offset:7168\n\t"
        "ds_read_b64_tr_b16 a[0:1], v99 offset:0\n\t"
        "ds_read_b64_tr_b16 a[2:3], v99 offset:256\n\t"
        "ds_read_b64_tr_b16 a[4:5], v99 offset:4096\n\t"
        "ds_read_b64_tr_b16 a[6:7], v99 offset:4352\n\t"
        "s_waitcnt lgkmcnt(8)\n\t"
        "ds_read_b64_tr_b16 a[8:9], v100 offset:0\n\t"
        "ds_read_b64_tr_b16 a[10:11], v100 offset:256\n\t"
        "ds_read_b64_tr_b16 a[12:13], v100 offset:4096\n\t"
        "ds_read_b64_tr_b16 a[14:15], v100 offset:4352\n\t"
        "s_waitcnt lgkmcnt(8)\n\t"
        "ds_read_b64_tr_b16 a[16:17], v101 offset:0\n\t"
        "ds_read_b64_tr_b16 a[18:19], v101 offset:256\n\t"
        "ds_read_b64_tr_b16 a[20:21], v101 offset:4096\n\t"
        "ds_read_b64_tr_b16 a[22:23], v101 offset:4352\n\t"
        "s_waitcnt lgkmcnt(8)\n\t"
        "ds_read_b64_tr_b16 v[104:105], v102 offset:0\n\t"
        "ds_read_b64_tr_b16 v[106:107], v102 offset:256\n\t"
        "ds_read_b64_tr_b16 v[108:109], v102 offset:4096\n\t"
        "ds_read_b64_tr_b16 v[110:111], v102 offset:4352\n\t"
        "s_lshl_b32 s34, s43, 2\n\t"
        "s_add_u32 s34, s34, 3\n\t"
        "s_and_b32 s34, s34, 15\n\t"
        "s_lshl_b32 s34, s34, 19\n\t"
        "buffer_load_dwordx4 v[64:67], v96, s[16:19], s34 offen nt\n\t"
        "s_add_u32 s34, s34, 0x10000\n\t"
        "buffer_load_dwordx4 v[68:71], v96, s[16:19], s34 offen nt\n\t"
        "s_add_u32 s34, s34, 0x10000\n\t"
        "buffer_load_dwordx4 v[72:75], v96, s[16:19], s34 offen nt\n\t"
        "s_add_u32 s34, s34, 0x10000\n\t"
        "buffer_load_dwordx4 v[76:79], v96, s[16:19], s34 offen nt\n\t"
        "s_add_u32 s34, s34, 0x10000\n\t"
        "buffer_load_dwordx4 v[80:83], v96, s[16:19], s34 offen nt\n\t"
        "s_add_u32 s34, s34, 0x10000\n\t"
        "buffer_load_dwordx4 v[84:87], v96, s[16:19], s34 offen nt\n\t"
        "s_add_u32 s34, s34, 0x10000\n\t"
        "buffer_load_dwordx4 v[88:91], v96, s[16:19], s34 offen nt\n\t"
        "s_add_u32 s34, s34, 0x10000\n\t"
        "buffer_load_dwordx4 v[92:95], v96, s[16:19], s34 offen nt\n\t"
        "s_waitcnt lgkmcnt(0)\n\t"
        "ds_write_b128 v117, v[104:107] offset:0\n\t"
        "ds_write_b128 v117, v[108:111] offset:1024\n\t"
        "s_waitcnt vmcnt(16)\n\t"
        "ds_write_b128 v97, v[0:3] offset:0\n\t"
        "ds_write_b128 v98, v[4:7] offset:1024\n\t"
        "ds_write_b128 v97, v[8:11] offset:2048\n\t"
        "ds_write_b128 v98, v[12:15] offset:3072\n\t"
        "ds_write_b128 v97, v[16:19] offset:4096\n\t"
        "ds_write_b128 v98, v[20:23] offset:5120\n\t"
        "ds_write_b128 v97, v[24:27] offset:6144\n\t"
        "ds_write_b128 v98, v[28:31] offset:7168\n\t"
        "ds_read_b64_tr_b16 a[24:25], v99 offset:0\n\t"
        "ds_read_b64_tr_b16 a[26:27], v99 offset:256\n\t"
        "ds_read_b64_tr_b16 a[28:29], v99 offset:4096\n\t"
        "ds_read_b64_tr_b16 a[30:31], v99 offset:4352\n\t"
        "s_waitcnt lgkmcnt(8)\n\t"
        "ds_read_b64_tr_b16 a[32:33], v100 offset:0\n\t"
        "ds_read_b64_tr_b16 a[34:35], v100 offset:256\n\t"
        "ds_read_b64_tr_b16 a[36:37], v100 offset:4096\n\t"
        "ds_read_b64_tr_b16 a[38:39], v100 offset:4352\n\t"
        "s_waitcnt lgkmcnt(8)\n\t"
        "ds_read_b64_tr_b16 a[40:41], v101 offset:0\n\t"
        "ds_read_b64_tr_b16 a[42:43], v101 offset:256\n\t"
        "ds_read_b64_tr_b16 a[44:45], v101 offset:4096\n\t"
        "ds_read_b64_tr_b16 a[46:47], v101 offset:4352\n\t"
        "s_waitcnt lgkmcnt(8)\n\t"
        "ds_read_b64_tr_b16 v[104:105], v102 offset:0\n\t"
        "ds_read_b64_tr_b16 v[106:107], v102 offset:256\n\t"
        "ds_read_b64_tr_b16 v[108:109], v102 offset:4096\n\t"
        "ds_read_b64_tr_b16 v[110:111], v102 offset:4352\n\t"
        "s_lshl_b32 s34, s43, 2\n\t"
        "s_add_u32 s34, s34, 4\n\t"
        "s_and_b32 s34, s34, 15\n\t"
        "s_lshl_b32 s34, s34, 19\n\t"
        "buffer_load_dwordx4 a[96:99], v96, s[16:19], s34 offen nt\n\t"
        "s_add_u32 s34, s34, 0x10000\n\t"
        "buffer_load_dwordx4 a[100:103], v96, s[16:19], s34 offen nt\n\t"
        "s_add_u32 s34, s34, 0x10000\n\t"
        "buffer_load_dwordx4 a[104:107], v96, s[16:19], s34 offen nt\n\t"
        "s_add_u32 s34, s34, 0x10000\n\t"
        "buffer_load_dwordx4 a[108:111], v96, s[16:19], s34 offen nt\n\t"
        "s_add_u32 s34, s34, 0x10000\n\t"
        "buffer_load_dwordx4 a[112:115], v96, s[16:19], s34 offen nt\n\t"
        "s_add_u32 s34, s34, 0x10000\n\t"
        "buffer_load_dwordx4 a[116:119], v96, s[16:19], s34 offen nt\n\t"
        "s_add_u32 s34, s34, 0x10000\n\t"
        "buffer_load_dwordx4 a[120:123], v96, s[16:19], s34 offen nt\n\t"
        "s_add_u32 s34, s34, 0x10000\n\t"
        "buffer_load_dwordx4 a[124:127], v96, s[16:19], s34 offen nt\n\t"
        "s_waitcnt lgkmcnt(0)\n\t"
        "ds_write_b128 v117, v[104:107] offset:2048\n\t"
        "ds_write_b128 v117, v[108:111] offset:3072\n\t"
        "s_waitcnt vmcnt(16)\n\t"
        "ds_write_b128 v97, v[32:35] offset:0\n\t"
        "ds_write_b128 v98, v[36:39] offset:1024\n\t"
        "ds_write_b128 v97, v[40:43] offset:2048\n\t"
        "ds_write_b128 v98, v[44:47] offset:3072\n\t"
        "ds_write_b128 v97, v[48:51] offset:4096\n\t"
        "ds_write_b128 v98, v[52:55] offset:5120\n\t"
        "ds_write_b128 v97, v[56:59] offset:6144\n\t"
        "ds_write_b128 v98, v[60:63] offset:7168\n\t"
        "ds_read_b64_tr_b16 a[48:49], v99 offset:0\n\t"
        "ds_read_b64_tr_b16 a[50:51], v99 offset:256\n\t"
        "ds_read_b64_tr_b16 a[52:53], v99 offset:4096\n\t"
        "ds_read_b64_tr_b16 a[54:55], v99 offset:4352\n\t"
        "s_waitcnt lgkmcnt(8)\n\t"
        "ds_read_b64_tr_b16 a[56:57], v100 offset:0\n\t"
        "ds_read_b64_tr_b16 a[58:59], v100 offset:256\n\t"
        "ds_read_b64_tr_b16 a[60:61], v100 offset:4096\n\t"
        "ds_read_b64_tr_b16 a[62:63], v100 offset:4352\n\t"
        "s_waitcnt lgkmcnt(8)\n\t"
        "ds_read_b64_tr_b16 a[64:65], v101 offset:0\n\t"
        "ds_read_b64_tr_b16 a[66:67], v101 offset:256\n\t"
        "ds_read_b64_tr_b16 a[68:69], v101 offset:4096\n\t"
        "ds_read_b64_tr_b16 a[70:71], v101 offset:4352\n\t"
        "s_waitcnt lgkmcnt(8)\n\t"
        "ds_read_b64_tr_b16 v[104:105], v102 offset:0\n\t"
        "ds_read_b64_tr_b16 v[106:107], v102 offset:256\n\t"
        "ds_read_b64_tr_b16 v[108:109], v102 offset:4096\n\t"
        "ds_read_b64_tr_b16 v[110:111], v102 offset:4352\n\t"
        "s_lshl_b32 s34, s43, 2\n\t"
        "s_add_u32 s34, s34, 5\n\t"
        "s_and_b32 s34, s34, 15\n\t"
        "s_lshl_b32 s34, s34, 19\n\t"
        "buffer_load_dwordx4 a[128:131], v96, s[16:19], s34 offen nt\n\t"
        "s_add_u32 s34, s34, 0x10000\n\t"
        "buffer_load_dwordx4 a[132:135], v96, s[16:19], s34 offen nt\n\t"
        "s_add_u32 s34, s34, 0x10000\n\t"
        "buffer_load_dwordx4 a[136:139], v96, s[16:19], s34 offen nt\n\t"
        "s_add_u32 s34, s34, 0x10000\n\t"
        "buffer_load_dwordx4 a[140:143], v96, s[16:19], s34 offen nt\n\t"
        "s_add_u32 s34, s34, 0x10000\n\t"
        "buffer_load_dwordx4 a[144:147], v96, s[16:19], s34 offen nt\n\t"
        "s_add_u32 s34, s34, 0x10000\n\t"
        "buffer_load_dwordx4 a[148:151], v96, s[16:19], s34 offen nt\n\t"
        "s_add_u32 s34, s34, 0x10000\n\t"
        "buffer_load_dwordx4 a[152:155], v96, s[16:19], s34 offen nt\n\t"
        "s_add_u32 s34, s34, 0x10000\n\t"
        "buffer_load_dwordx4 a[156:159], v96, s[16:19], s34 offen nt\n\t"
        "s_waitcnt lgkmcnt(0)\n\t"
        "ds_write_b128 v117, v[104:107] offset:4096\n\t"
        "ds_write_b128 v117, v[108:111] offset:5120\n\t"
        "s_waitcnt vmcnt(16)\n\t"
        "ds_write_b128 v97, v[64:67] offset:0\n\t"
        "ds_write_b128 v98, v[68:71] offset:1024\n\t"
        "ds_write_b128 v97, v[72:75] offset:2048\n\t"
        "ds_write_b128 v98, v[76:79] offset:3072\n\t"
        "ds_write_b128 v97, v[80:83] offset:4096\n\t"
        "ds_write_b128 v98, v[84:87] offset:5120\n\t"
        "ds_write_b128 v97, v[88:91] offset:6144\n\t"
        "ds_write_b128 v98, v[92:95] offset:7168\n\t"
        "ds_read_b64_tr_b16 a[72:73], v99 offset:0\n\t"
        "ds_read_b64_tr_b16 a[74:75], v99 offset:256\n\t"
        "ds_read_b64_tr_b16 a[76:77], v99 offset:4096\n\t"
        "ds_read_b64_tr_b16 a[78:79], v99 offset:4352\n\t"
        "s_waitcnt lgkmcnt(8)\n\t"
        "ds_read_b64_tr_b16 a[80:81], v100 offset:0\n\t"
        "ds_read_b64_tr_b16 a[82:83], v100 offset:256\n\t"
        "ds_read_b64_tr_b16 a[84:85], v100 offset:4096\n\t"
        "ds_read_b64_tr_b16 a[86:87], v100 offset:4352\n\t"
        "s_waitcnt lgkmcnt(8)\n\t"
        "ds_read_b64_tr_b16 a[88:89], v101 offset:0\n\t"
        "ds_read_b64_tr_b16 a[90:91], v101 offset:256\n\t"
        "ds_read_b64_tr_b16 a[92:93], v101 offset:4096\n\t"
        "ds_read_b64_tr_b16 a[94:95], v101 offset:4352\n\t"
        "s_waitcnt lgkmcnt(8)\n\t"
        "ds_read_b64_tr_b16 v[104:105], v102 offset:0\n\t"
        "ds_read_b64_tr_b16 v[106:107], v102 offset:256\n\t"
        "ds_read_b64_tr_b16 v[108:109], v102 offset:4096\n\t"
        "ds_read_b64_tr_b16 v[110:111], v102 offset:4352\n\t"
        "s_lshl_b32 s34, s43, 2\n\t"
        "s_add_u32 s34, s34, 6\n\t"
        "s_and_b32 s34, s34, 15\n\t"
        "s_lshl_b32 s34, s34, 19\n\t"
        "buffer_load_dwordx4 a[160:163], v96, s[16:19], s34 offen nt\n\t"
        "s_add_u32 s34, s34, 0x10000\n\t"
        "buffer_load_dwordx4 a[164:167], v96, s[16:19], s34 offen nt\n\t"
        "s_add_u32 s34, s34, 0x10000\n\t"
        "buffer_load_dwordx4 a[168:171], v96, s[16:19], s34 offen nt\n\t"
        "s_add_u32 s34, s34, 0x10000\n\t"
        "buffer_load_dwordx4 a[172:175], v96, s[16:19], s34 offen nt\n\t"
        "s_add_u32 s34, s34, 0x10000\n\t"
        "buffer_load_dwordx4 a[176:179], v96, s[16:19], s34 offen nt\n\t"
        "s_add_u32 s34, s34, 0x10000\n\t"
        "buffer_load_dwordx4 a[180:183], v96, s[16:19], s34 offen nt\n\t"
        "s_add_u32 s34, s34, 0x10000\n\t"
        "buffer_load_dwordx4 a[184:187], v96, s[16:19], s34 offen nt\n\t"
        "s_add_u32 s34, s34, 0x10000\n\t"
        "buffer_load_dwordx4 a[188:191], v96, s[16:19], s34 offen nt\n\t"
        "s_waitcnt lgkmcnt(0)\n\t"
        "ds_write_b128 v117, v[104:107] offset:6144\n\t"
        "ds_write_b128 v117, v[108:111] offset:7168\n\t"
        "s_waitcnt vmcnt(16)\n\t"
        "ds_write_b128 v97, a[96:99] offset:0\n\t"
        "ds_write_b128 v98, a[100:103] offset:1024\n\t"
        "ds_write_b128 v97, a[104:107] offset:2048\n\t"
        "ds_write_b128 v98, a[108:111] offset:3072\n\t"
        "ds_write_b128 v97, a[112:115] offset:4096\n\t"
        "ds_write_b128 v98, a[116:119] offset:5120\n\t"
        "ds_write_b128 v97, a[120:123] offset:6144\n\t"
        "ds_write_b128 v98, a[124:127] offset:7168\n\t"
        "ds_read_b64_tr_b16 a[96:97], v99 offset:0\n\t"
        "ds_read_b64_tr_b16 a[98:99], v99 offset:256\n\t"
        "ds_read_b64_tr_b16 a[100:101], v99 offset:4096\n\t"
        "ds_read_b64_tr_b16 a[102:103], v99 offset:4352\n\t"
        "s_waitcnt lgkmcnt(8)\n\t"
        "ds_read_b64_tr_b16 a[104:105], v100 offset:0\n\t"
        "ds_read_b64_tr_b16 a[106:107], v100 offset:256\n\t"
        "ds_read_b64_tr_b16 a[108:109], v100 offset:4096\n\t"
        "ds_read_b64_tr_b16 a[110:111], v100 offset:4352\n\t"
        "s_waitcnt lgkmcnt(8)\n\t"
        "ds_read_b64_tr_b16 a[112:113], v101 offset:0\n\t"
        "ds_read_b64_tr_b16 a[114:115], v101 offset:256\n\t"
        "ds_read_b64_tr_b16 a[116:117], v101 offset:4096\n\t"
        "ds_read_b64_tr_b16 a[118:119], v101 offset:4352\n\t"
        "s_waitcnt lgkmcnt(8)\n\t"
        "ds_read_b64_tr_b16 v[104:105], v102 offset:0\n\t"
        "ds_read_b64_tr_b16 v[106:107], v102 offset:256\n\t"
        "ds_read_b64_tr_b16 v[108:109], v102 offset:4096\n\t"
        "ds_read_b64_tr_b16 v[110:111], v102 offset:4352\n\t"
        "s_lshl_b32 s34, s43, 2\n\t"
        "s_add_u32 s34, s34, 7\n\t"
        "s_and_b32 s34, s34, 15\n\t"
        "s_lshl_b32 s34, s34, 19\n\t"
        "buffer_load_dwordx4 a[192:195], v96, s[16:19], s34 offen nt\n\t"
        "s_add_u32 s34, s34, 0x10000\n\t"
        "buffer_load_dwordx4 a[196:199], v96, s[16:19], s34 offen nt\n\t"
        "s_add_u32 s34, s34, 0x10000\n\t"
        "buffer_load_dwordx4 a[200:203], v96, s[16:19], s34 offen nt\n\t"
        "s_add_u32 s34, s34, 0x10000\n\t"
        "buffer_load_dwordx4 a[204:207], v96, s[16:19], s34 offen nt\n\t"
        "s_add_u32 s34, s34, 0x10000\n\t"
        "buffer_load_dwordx4 a[208:211], v96, s[16:19], s34 offen nt\n\t"
        "s_add_u32 s34, s34, 0x10000\n\t"
        "buffer_load_dwordx4 a[212:215], v96, s[16:19], s34 offen nt\n\t"
        "s_add_u32 s34, s34, 0x10000\n\t"
        "buffer_load_dwordx4 a[216:219], v96, s[16:19], s34 offen nt\n\t"
        "s_add_u32 s34, s34, 0x10000\n\t"
        "buffer_load_dwordx4 a[220:223], v96, s[16:19], s34 offen nt\n\t"
        "s_waitcnt lgkmcnt(0)\n\t"
        "ds_write_b128 v126, v[104:107] offset:0\n\t"
        "ds_write_b128 v126, v[108:111] offset:1024\n\t"
        "s_waitcnt vmcnt(16)\n\t"
        "ds_write_b128 v97, a[128:131] offset:0\n\t"
        "ds_write_b128 v98, a[132:135] offset:1024\n\t"
        "ds_write_b128 v97, a[136:139] offset:2048\n\t"
        "ds_write_b128 v98, a[140:143] offset:3072\n\t"
        "ds_write_b128 v97, a[144:147] offset:4096\n\t"
        "ds_write_b128 v98, a[148:151] offset:5120\n\t"
        "ds_write_b128 v97, a[152:155] offset:6144\n\t"
        "ds_write_b128 v98, a[156:159] offset:7168\n\t"
        "ds_read_b64_tr_b16 a[120:121], v99 offset:0\n\t"
        "ds_read_b64_tr_b16 a[122:123], v99 offset:256\n\t"
        "ds_read_b64_tr_b16 a[124:125], v99 offset:4096\n\t"
        "ds_read_b64_tr_b16 a[126:127], v99 offset:4352\n\t"
        "s_waitcnt lgkmcnt(8)\n\t"
        "ds_read_b64_tr_b16 a[128:129], v100 offset:0\n\t"
        "ds_read_b64_tr_b16 a[130:131], v100 offset:256\n\t"
        "ds_read_b64_tr_b16 a[132:133], v100 offset:4096\n\t"
        "ds_read_b64_tr_b16 a[134:135], v100 offset:4352\n\t"
        "s_waitcnt lgkmcnt(8)\n\t"
        "ds_read_b64_tr_b16 a[136:137], v101 offset:0\n\t"
        "ds_read_b64_tr_b16 a[138:139], v101 offset:256\n\t"
        "ds_read_b64_tr_b16 a[140:141], v101 offset:4096\n\t"
        "ds_read_b64_tr_b16 a[142:143], v101 offset:4352\n\t"
        "s_waitcnt lgkmcnt(8)\n\t"
        "ds_read_b64_tr_b16 v[104:105], v102 offset:0\n\t"
        "ds_read_b64_tr_b16 v[106:107], v102 offset:256\n\t"
        "ds_read_b64_tr_b16 v[108:109], v102 offset:4096\n\t"
        "ds_read_b64_tr_b16 v[110:111], v102 offset:4352\n\t"
        "s_lshl_b32 s34, s43, 2\n\t"
        "s_add_u32 s34, s34, 8\n\t"
        "s_and_b32 s34, s34, 15\n\t"
        "s_lshl_b32 s34, s34, 19\n\t"
        "buffer_load_dwordx4 a[224:227], v96, s[16:19], s34 offen nt\n\t"
        "s_add_u32 s34, s34, 0x10000\n\t"
        "buffer_load_dwordx4 a[228:231], v96, s[16:19], s34 offen nt\n\t"
        "s_add_u32 s34, s34, 0x10000\n\t"
        "buffer_load_dwordx4 a[232:235], v96, s[16:19], s34 offen nt\n\t"
        "s_add_u32 s34, s34, 0x10000\n\t"
        "buffer_load_dwordx4 a[236:239], v96, s[16:19], s34 offen nt\n\t"
        "s_add_u32 s34, s34, 0x10000\n\t"
        "buffer_load_dwordx4 a[240:243], v96, s[16:19], s34 offen nt\n\t"
        "s_add_u32 s34, s34, 0x10000\n\t"
        "buffer_load_dwordx4 a[244:247], v96, s[16:19], s34 offen nt\n\t"
        "s_add_u32 s34, s34, 0x10000\n\t"
        "buffer_load_dwordx4 a[248:251], v96, s[16:19], s34 offen nt\n\t"
        "s_add_u32 s34, s34, 0x10000\n\t"
        "buffer_load_dwordx4 a[252:255], v96, s[16:19], s34 offen nt\n\t"
        "s_waitcnt lgkmcnt(0)\n\t"
        "ds_write_b128 v126, v[104:107] offset:2048\n\t"
        "ds_write_b128 v126, v[108:111] offset:3072\n\t"
        "s_waitcnt vmcnt(16)\n\t"
        "ds_write_b128 v97, a[160:163] offset:0\n\t"
        "ds_write_b128 v98, a[164:167] offset:1024\n\t"
        "ds_write_b128 v97, a[168:171] offset:2048\n\t"
        "ds_write_b128 v98, a[172:175] offset:3072\n\t"
        "ds_write_b128 v97, a[176:179] offset:4096\n\t"
        "ds_write_b128 v98, a[180:183] offset:5120\n\t"
        "ds_write_b128 v97, a[184:187] offset:6144\n\t"
        "ds_write_b128 v98, a[188:191] offset:7168\n\t"
        "ds_read_b64_tr_b16 a[144:145], v99 offset:0\n\t"
        "ds_read_b64_tr_b16 a[146:147], v99 offset:256\n\t"
        "ds_read_b64_tr_b16 a[148:149], v99 offset:4096\n\t"
        "ds_read_b64_tr_b16 a[150:151], v99 offset:4352\n\t"
        "s_waitcnt lgkmcnt(8)\n\t"
        "ds_read_b64_tr_b16 a[152:153], v100 offset:0\n\t"
        "ds_read_b64_tr_b16 a[154:155], v100 offset:256\n\t"
        "ds_read_b64_tr_b16 a[156:157], v100 offset:4096\n\t"
        "ds_read_b64_tr_b16 a[158:159], v100 offset:4352\n\t"
        "s_waitcnt lgkmcnt(8)\n\t"
        "ds_read_b64_tr_b16 a[160:161], v101 offset:0\n\t"
        "ds_read_b64_tr_b16 a[162:163], v101 offset:256\n\t"
        "ds_read_b64_tr_b16 a[164:165], v101 offset:4096\n\t"
        "ds_read_b64_tr_b16 a[166:167], v101 offset:4352\n\t"
        "s_waitcnt lgkmcnt(8)\n\t"
        "ds_read_b64_tr_b16 v[104:105], v102 offset:0\n\t"
        "ds_read_b64_tr_b16 v[106:107], v102 offset:256\n\t"
        "ds_read_b64_tr_b16 v[108:109], v102 offset:4096\n\t"
        "ds_read_b64_tr_b16 v[110:111], v102 offset:4352\n\t"
        "s_lshl_b32 s34, s43, 2\n\t"
        "s_add_u32 s34, s34, 9\n\t"
        "s_and_b32 s34, s34, 15\n\t"
        "s_lshl_b32 s34, s34, 19\n\t"
        "buffer_load_dwordx4 v[128:131], v96, s[16:19], s34 offen nt\n\t"
        "s_add_u32 s34, s34, 0x10000\n\t"
        "buffer_load_dwordx4 v[132:135], v96, s[16:19], s34 offen nt\n\t"
        "s_add_u32 s34, s34, 0x10000\n\t"
        "buffer_load_dwordx4 v[136:139], v96, s[16:19], s34 offen nt\n\t"
        "s_add_u32 s34, s34, 0x10000\n\t"
        "buffer_load_dwordx4 v[140:143], v96, s[16:19], s34 offen nt\n\t"
        "s_add_u32 s34, s34, 0x10000\n\t"
        "buffer_load_dwordx4 v[144:147], v96, s[16:19], s34 offen nt\n\t"
        "s_add_u32 s34, s34, 0x10000\n\t"
        "buffer_load_dwordx4 v[148:151], v96, s[16:19], s34 offen nt\n\t"
        "s_add_u32 s34, s34, 0x10000\n\t"
        "buffer_load_dwordx4 v[152:155], v96, s[16:19], s34 offen nt\n\t"
        "s_add_u32 s34, s34, 0x10000\n\t"
        "buffer_load_dwordx4 v[156:159], v96, s[16:19], s34 offen nt\n\t"
        "s_waitcnt lgkmcnt(0)\n\t"
        "ds_write_b128 v126, v[104:107] offset:4096\n\t"
        "ds_write_b128 v126, v[108:111] offset:5120\n\t"
        "s_waitcnt vmcnt(16)\n\t"
        "ds_write_b128 v97, a[192:195] offset:0\n\t"
        "ds_write_b128 v98, a[196:199] offset:1024\n\t"
        "ds_write_b128 v97, a[200:203] offset:2048\n\t"
        "ds_write_b128 v98, a[204:207] offset:3072\n\t"
        "ds_write_b128 v97, a[208:211] offset:4096\n\t"
        "ds_write_b128 v98, a[212:215] offset:5120\n\t"
        "ds_write_b128 v97, a[216:219] offset:6144\n\t"
        "ds_write_b128 v98, a[220:223] offset:7168\n\t"
        "ds_read_b64_tr_b16 a[168:169], v99 offset:0\n\t"
        "ds_read_b64_tr_b16 a[170:171], v99 offset:256\n\t"
        "ds_read_b64_tr_b16 a[172:173], v99 offset:4096\n\t"
        "ds_read_b64_tr_b16 a[174:175], v99 offset:4352\n\t"
        "s_waitcnt lgkmcnt(8)\n\t"
        "ds_read_b64_tr_b16 a[176:177], v100 offset:0\n\t"
        "ds_read_b64_tr_b16 a[178:179], v100 offset:256\n\t"
        "ds_read_b64_tr_b16 a[180:181], v100 offset:4096\n\t"
        "ds_read_b64_tr_b16 a[182:183], v100 offset:4352\n\t"
        "s_waitcnt lgkmcnt(8)\n\t"
        "ds_read_b64_tr_b16 a[184:185], v101 offset:0\n\t"
        "ds_read_b64_tr_b16 a[186:187], v101 offset:256\n\t"
        "ds_read_b64_tr_b16 a[188:189], v101 offset:4096\n\t"
        "ds_read_b64_tr_b16 a[190:191], v101 offset:4352\n\t"
        "s_waitcnt lgkmcnt(8)\n\t"
        "ds_read_b64_tr_b16 v[104:105], v102 offset:0\n\t"
        "ds_read_b64_tr_b16 v[106:107], v102 offset:256\n\t"
        "ds_read_b64_tr_b16 v[108:109], v102 offset:4096\n\t"
        "ds_read_b64_tr_b16 v[110:111], v102 offset:4352\n\t"
        "s_lshl_b32 s34, s43, 2\n\t"
        "s_add_u32 s34, s34, 10\n\t"
        "s_and_b32 s34, s34, 15\n\t"
        "s_lshl_b32 s34, s34, 19\n\t"
        "buffer_load_dwordx4 v[160:163], v96, s[16:19], s34 offen nt\n\t"
        "s_add_u32 s34, s34, 0x10000\n\t"
        "buffer_load_dwordx4 v[164:167], v96, s[16:19], s34 offen nt\n\t"
        "s_add_u32 s34, s34, 0x10000\n\t"
        "buffer_load_dwordx4 v[168:171], v96, s[16:19], s34 offen nt\n\t"
        "s_add_u32 s34, s34, 0x10000\n\t"
        "buffer_load_dwordx4 v[172:175], v96, s[16:19], s34 offen nt\n\t"
        "s_add_u32 s34, s34, 0x10000\n\t"
        "buffer_load_dwordx4 v[176:179], v96, s[16:19], s34 offen nt\n\t"
        "s_add_u32 s34, s34, 0x10000\n\t"
        "buffer_load_dwordx4 v[180:183], v96, s[16:19], s34 offen nt\n\t"
        "s_add_u32 s34, s34, 0x10000\n\t"
        "buffer_load_dwordx4 v[184:187], v96, s[16:19], s34 offen nt\n\t"
        "s_add_u32 s34, s34, 0x10000\n\t"
        "buffer_load_dwordx4 v[188:191], v96, s[16:19], s34 offen nt\n\t"
        "s_waitcnt lgkmcnt(0)\n\t"
        "ds_write_b128 v126, v[104:107] offset:6144\n\t"
        "ds_write_b128 v126, v[108:111] offset:7168\n\t"
        "s_waitcnt vmcnt(16)\n\t"
        "ds_write_b128 v97, a[224:227] offset:0\n\t"
        "ds_write_b128 v98, a[228:231] offset:1024\n\t"
        "ds_write_b128 v97, a[232:235] offset:2048\n\t"
        "ds_write_b128 v98, a[236:239] offset:3072\n\t"
        "ds_write_b128 v97, a[240:243] offset:4096\n\t"
        "ds_write_b128 v98, a[244:247] offset:5120\n\t"
        "ds_write_b128 v97, a[248:251] offset:6144\n\t"
        "ds_write_b128 v98, a[252:255] offset:7168\n\t"
        "ds_read_b64_tr_b16 a[192:193], v99 offset:0\n\t"
        "ds_read_b64_tr_b16 a[194:195], v99 offset:256\n\t"
        "ds_read_b64_tr_b16 a[196:197], v99 offset:4096\n\t"
        "ds_read_b64_tr_b16 a[198:199], v99 offset:4352\n\t"
        "s_waitcnt lgkmcnt(8)\n\t"
        "ds_read_b64_tr_b16 a[200:201], v100 offset:0\n\t"
        "ds_read_b64_tr_b16 a[202:203], v100 offset:256\n\t"
        "ds_read_b64_tr_b16 a[204:205], v100 offset:4096\n\t"
        "ds_read_b64_tr_b16 a[206:207], v100 offset:4352\n\t"
        "s_waitcnt lgkmcnt(8)\n\t"
        "ds_read_b64_tr_b16 a[208:209], v101 offset:0\n\t"
        "ds_read_b64_tr_b16 a[210:211], v101 offset:256\n\t"
        "ds_read_b64_tr_b16 a[212:213], v101 offset:4096\n\t"
        "ds_read_b64_tr_b16 a[214:215], v101 offset:4352\n\t"
        "s_waitcnt lgkmcnt(8)\n\t"
        "ds_read_b64_tr_b16 v[104:105], v102 offset:0\n\t"
        "ds_read_b64_tr_b16 v[106:107], v102 offset:256\n\t"
        "ds_read_b64_tr_b16 v[108:109], v102 offset:4096\n\t"
        "ds_read_b64_tr_b16 v[110:111], v102 offset:4352\n\t"
        "s_lshl_b32 s34, s43, 2\n\t"
        "s_add_u32 s34, s34, 11\n\t"
        "s_and_b32 s34, s34, 15\n\t"
        "s_lshl_b32 s34, s34, 19\n\t"
        "buffer_load_dwordx4 v[192:195], v96, s[16:19], s34 offen nt\n\t"
        "s_add_u32 s34, s34, 0x10000\n\t"
        "buffer_load_dwordx4 v[196:199], v96, s[16:19], s34 offen nt\n\t"
        "s_add_u32 s34, s34, 0x10000\n\t"
        "buffer_load_dwordx4 v[200:203], v96, s[16:19], s34 offen nt\n\t"
        "s_add_u32 s34, s34, 0x10000\n\t"
        "buffer_load_dwordx4 v[204:207], v96, s[16:19], s34 offen nt\n\t"
        "s_add_u32 s34, s34, 0x10000\n\t"
        "buffer_load_dwordx4 v[208:211], v96, s[16:19], s34 offen nt\n\t"
        "s_add_u32 s34, s34, 0x10000\n\t"
        "buffer_load_dwordx4 v[212:215], v96, s[16:19], s34 offen nt\n\t"
        "s_add_u32 s34, s34, 0x10000\n\t"
        "buffer_load_dwordx4 v[216:219], v96, s[16:19], s34 offen nt\n\t"
        "s_add_u32 s34, s34, 0x10000\n\t"
        "buffer_load_dwordx4 v[220:223], v96, s[16:19], s34 offen nt\n\t"
        "s_waitcnt lgkmcnt(0)\n\t"
        "ds_write_b128 v117, v[104:107] offset:8192\n\t"
        "ds_write_b128 v117, v[108:111] offset:9216\n\t"
        "s_waitcnt vmcnt(16)\n\t"
        "ds_write_b128 v97, v[128:131] offset:0\n\t"
        "ds_write_b128 v98, v[132:135] offset:1024\n\t"
        "ds_write_b128 v97, v[136:139] offset:2048\n\t"
        "ds_write_b128 v98, v[140:143] offset:3072\n\t"
        "ds_write_b128 v97, v[144:147] offset:4096\n\t"
        "ds_write_b128 v98, v[148:151] offset:5120\n\t"
        "ds_write_b128 v97, v[152:155] offset:6144\n\t"
        "ds_write_b128 v98, v[156:159] offset:7168\n\t"
        "ds_read_b64_tr_b16 a[216:217], v99 offset:0\n\t"
        "ds_read_b64_tr_b16 a[218:219], v99 offset:256\n\t"
        "ds_read_b64_tr_b16 a[220:221], v99 offset:4096\n\t"
        "ds_read_b64_tr_b16 a[222:223], v99 offset:4352\n\t"
        "s_waitcnt lgkmcnt(8)\n\t"
        "ds_read_b64_tr_b16 a[224:225], v100 offset:0\n\t"
        "ds_read_b64_tr_b16 a[226:227], v100 offset:256\n\t"
        "ds_read_b64_tr_b16 a[228:229], v100 offset:4096\n\t"
        "ds_read_b64_tr_b16 a[230:231], v100 offset:4352\n\t"
        "s_waitcnt lgkmcnt(8)\n\t"
        "ds_read_b64_tr_b16 a[232:233], v101 offset:0\n\t"
        "ds_read_b64_tr_b16 a[234:235], v101 offset:256\n\t"
        "ds_read_b64_tr_b16 a[236:237], v101 offset:4096\n\t"
        "ds_read_b64_tr_b16 a[238:239], v101 offset:4352\n\t"
        "s_waitcnt lgkmcnt(8)\n\t"
        "ds_read_b64_tr_b16 v[104:105], v102 offset:0\n\t"
        "ds_read_b64_tr_b16 v[106:107], v102 offset:256\n\t"
        "ds_read_b64_tr_b16 v[108:109], v102 offset:4096\n\t"
        "ds_read_b64_tr_b16 v[110:111], v102 offset:4352\n\t"
        "s_lshl_b32 s34, s43, 2\n\t"
        "s_add_u32 s34, s34, 12\n\t"
        "s_and_b32 s34, s34, 15\n\t"
        "s_lshl_b32 s34, s34, 19\n\t"
        "buffer_load_dwordx4 v[224:227], v96, s[16:19], s34 offen nt\n\t"
        "s_add_u32 s34, s34, 0x10000\n\t"
        "buffer_load_dwordx4 v[228:231], v96, s[16:19], s34 offen nt\n\t"
        "s_add_u32 s34, s34, 0x10000\n\t"
        "buffer_load_dwordx4 v[232:235], v96, s[16:19], s34 offen nt\n\t"
        "s_add_u32 s34, s34, 0x10000\n\t"
        "buffer_load_dwordx4 v[236:239], v96, s[16:19], s34 offen nt\n\t"
        "s_add_u32 s34, s34, 0x10000\n\t"
        "buffer_load_dwordx4 v[240:243], v96, s[16:19], s34 offen nt\n\t"
        "s_add_u32 s34, s34, 0x10000\n\t"
        "buffer_load_dwordx4 v[244:247], v96, s[16:19], s34 offen nt\n\t"
        "s_add_u32 s34, s34, 0x10000\n\t"
        "buffer_load_dwordx4 v[248:251], v96, s[16:19], s34 offen nt\n\t"
        "s_add_u32 s34, s34, 0x10000\n\t"
        "buffer_load_dwordx4 v[252:255], v96, s[16:19], s34 offen nt\n\t"
        "s_waitcnt lgkmcnt(0)\n\t"
        "ds_write_b128 v117, v[104:107] offset:10240\n\t"
        "ds_write_b128 v117, v[108:111] offset:11264\n\t"
        "s_waitcnt vmcnt(16)\n\t"
        "ds_write_b128 v97, v[160:163] offset:0\n\t"
        "ds_write_b128 v98, v[164:167] offset:1024\n\t"
        "ds_write_b128 v97, v[168:171] offset:2048\n\t"
        "ds_write_b128 v98, v[172:175] offset:3072\n\t"
        "ds_write_b128 v97, v[176:179] offset:4096\n\t"
        "ds_write_b128 v98, v[180:183] offset:5120\n\t"
        "ds_write_b128 v97, v[184:187] offset:6144\n\t"
        "ds_write_b128 v98, v[188:191] offset:7168\n\t"
        "ds_read_b64_tr_b16 a[240:241], v99 offset:0\n\t"
        "ds_read_b64_tr_b16 a[242:243], v99 offset:256\n\t"
        "ds_read_b64_tr_b16 a[244:245], v99 offset:4096\n\t"
        "ds_read_b64_tr_b16 a[246:247], v99 offset:4352\n\t"
        "s_waitcnt lgkmcnt(8)\n\t"
        "ds_read_b64_tr_b16 a[248:249], v100 offset:0\n\t"
        "ds_read_b64_tr_b16 a[250:251], v100 offset:256\n\t"
        "ds_read_b64_tr_b16 a[252:253], v100 offset:4096\n\t"
        "ds_read_b64_tr_b16 a[254:255], v100 offset:4352\n\t"
        "s_waitcnt lgkmcnt(8)\n\t"
        "ds_read_b64_tr_b16 v[128:129], v101 offset:0\n\t"
        "ds_read_b64_tr_b16 v[130:131], v101 offset:256\n\t"
        "ds_read_b64_tr_b16 v[132:133], v101 offset:4096\n\t"
        "ds_read_b64_tr_b16 v[134:135], v101 offset:4352\n\t"
        "s_waitcnt lgkmcnt(8)\n\t"
        "ds_read_b64_tr_b16 v[104:105], v102 offset:0\n\t"
        "ds_read_b64_tr_b16 v[106:107], v102 offset:256\n\t"
        "ds_read_b64_tr_b16 v[108:109], v102 offset:4096\n\t"
        "ds_read_b64_tr_b16 v[110:111], v102 offset:4352\n\t"
        "s_lshl_b32 s34, s43, 2\n\t"
        "s_add_u32 s34, s34, 13\n\t"
        "s_and_b32 s34, s34, 15\n\t"
        "s_lshl_b32 s34, s34, 19\n\t"
        "buffer_load_dwordx4 v[0:3], v96, s[16:19], s34 offen nt\n\t"
        "s_add_u32 s34, s34, 0x10000\n\t"
        "buffer_load_dwordx4 v[4:7], v96, s[16:19], s34 offen nt\n\t"
        "s_add_u32 s34, s34, 0x10000\n\t"
        "buffer_load_dwordx4 v[8:11], v96, s[16:19], s34 offen nt\n\t"
        "s_add_u32 s34, s34, 0x10000\n\t"
        "buffer_load_dwordx4 v[12:15], v96, s[16:19], s34 offen nt\n\t"
        "s_add_u32 s34, s34, 0x10000\n\t"
        "buffer_load_dwordx4 v[16:19], v96, s[16:19], s34 offen nt\n\t"
        "s_add_u32 s34, s34, 0x10000\n\t"
        "buffer_load_dwordx4 v[20:23], v96, s[16:19], s34 offen nt\n\t"
        "s_add_u32 s34, s34, 0x10000\n\t"
        "buffer_load_dwordx4 v[24:27], v96, s[16:19], s34 offen nt\n\t"
        "s_add_u32 s34, s34, 0x10000\n\t"
        "buffer_load_dwordx4 v[28:31], v96, s[16:19], s34 offen nt\n\t"
        "s_waitcnt lgkmcnt(0)\n\t"
        "ds_write_b128 v117, v[104:107] offset:12288\n\t"
        "ds_write_b128 v117, v[108:111] offset:13312\n\t"
        "s_waitcnt vmcnt(16)\n\t"
        "ds_write_b128 v97, v[192:195] offset:0\n\t"
        "ds_write_b128 v98, v[196:199] offset:1024\n\t"
        "ds_write_b128 v97, v[200:203] offset:2048\n\t"
        "ds_write_b128 v98, v[204:207] offset:3072\n\t"
        "ds_write_b128 v97, v[208:211] offset:4096\n\t"
        "ds_write_b128 v98, v[212:215] offset:5120\n\t"
        "ds_write_b128 v97, v[216:219] offset:6144\n\t"
        "ds_write_b128 v98, v[220:223] offset:7168\n\t"
        "ds_read_b64_tr_b16 v[136:137], v99 offset:0\n\t"
        "ds_read_b64_tr_b16 v[138:139], v99 offset:256\n\t"
        "ds_read_b64_tr_b16 v[140:141], v99 offset:4096\n\t"
        "ds_read_b64_tr_b16 v[142:143], v99 offset:4352\n\t"
        "s_waitcnt lgkmcnt(8)\n\t"
        "ds_read_b64_tr_b16 v[144:145], v100 offset:0\n\t"
        "ds_read_b64_tr_b16 v[146:147], v100 offset:256\n\t"
        "ds_read_b64_tr_b16 v[148:149], v100 offset:4096\n\t"
        "ds_read_b64_tr_b16 v[150:151], v100 offset:4352\n\t"
        "s_waitcnt lgkmcnt(8)\n\t"
        "ds_read_b64_tr_b16 v[152:153], v101 offset:0\n\t"
        "ds_read_b64_tr_b16 v[154:155], v101 offset:256\n\t"
        "ds_read_b64_tr_b16 v[156:157], v101 offset:4096\n\t"
        "ds_read_b64_tr_b16 v[158:159], v101 offset:4352\n\t"
        "s_waitcnt lgkmcnt(8)\n\t"
        "ds_read_b64_tr_b16 v[104:105], v102 offset:0\n\t"
        "ds_read_b64_tr_b16 v[106:107], v102 offset:256\n\t"
        "ds_read_b64_tr_b16 v[108:109], v102 offset:4096\n\t"
        "ds_read_b64_tr_b16 v[110:111], v102 offset:4352\n\t"
        "s_lshl_b32 s34, s43, 2\n\t"
        "s_add_u32 s34, s34, 14\n\t"
        "s_and_b32 s34, s34, 15\n\t"
        "s_lshl_b32 s34, s34, 19\n\t"
        "buffer_load_dwordx4 v[32:35], v96, s[16:19], s34 offen nt\n\t"
        "s_add_u32 s34, s34, 0x10000\n\t"
        "buffer_load_dwordx4 v[36:39], v96, s[16:19], s34 offen nt\n\t"
        "s_add_u32 s34, s34, 0x10000\n\t"
        "buffer_load_dwordx4 v[40:43], v96, s[16:19], s34 offen nt\n\t"
        "s_add_u32 s34, s34, 0x10000\n\t"
        "buffer_load_dwordx4 v[44:47], v96, s[16:19], s34 offen nt\n\t"
        "s_add_u32 s34, s34, 0x10000\n\t"
        "buffer_load_dwordx4 v[48:51], v96, s[16:19], s34 offen nt\n\t"
        "s_add_u32 s34, s34, 0x10000\n\t"
        "buffer_load_dwordx4 v[52:55], v96, s[16:19], s34 offen nt\n\t"
        "s_add_u32 s34, s34, 0x10000\n\t"
        "buffer_load_dwordx4 v[56:59], v96, s[16:19], s34 offen nt\n\t"
        "s_add_u32 s34, s34, 0x10000\n\t"
        "buffer_load_dwordx4 v[60:63], v96, s[16:19], s34 offen nt\n\t"
        "s_waitcnt lgkmcnt(0)\n\t"
        "ds_write_b128 v117, v[104:107] offset:14336\n\t"
        "ds_write_b128 v117, v[108:111] offset:15360\n\t"
        "s_waitcnt vmcnt(16)\n\t"
        "ds_write_b128 v97, v[224:227] offset:0\n\t"
        "ds_write_b128 v98, v[228:231] offset:1024\n\t"
        "ds_write_b128 v97, v[232:235] offset:2048\n\t"
        "ds_write_b128 v98, v[236:239] offset:3072\n\t"
        "ds_write_b128 v97, v[240:243] offset:4096\n\t"
        "ds_write_b128 v98, v[244:247] offset:5120\n\t"
        "ds_write_b128 v97, v[248:251] offset:6144\n\t"
        "ds_write_b128 v98, v[252:255] offset:7168\n\t"
        "ds_read_b64_tr_b16 v[160:161], v99 offset:0\n\t"
        "ds_read_b64_tr_b16 v[162:163], v99 offset:256\n\t"
        "ds_read_b64_tr_b16 v[164:165], v99 offset:4096\n\t"
        "ds_read_b64_tr_b16 v[166:167], v99 offset:4352\n\t"
        "s_waitcnt lgkmcnt(8)\n\t"
        "ds_read_b64_tr_b16 v[168:169], v100 offset:0\n\t"
        "ds_read_b64_tr_b16 v[170:171], v100 offset:256\n\t"
        "ds_read_b64_tr_b16 v[172:173], v100 offset:4096\n\t"
        "ds_read_b64_tr_b16 v[174:175], v100 offset:4352\n\t"
        "s_waitcnt lgkmcnt(8)\n\t"
        "ds_read_b64_tr_b16 v[176:177], v101 offset:0\n\t"
        "ds_read_b64_tr_b16 v[178:179], v101 offset:256\n\t"
        "ds_read_b64_tr_b16 v[180:181], v101 offset:4096\n\t"
        "ds_read_b64_tr_b16 v[182:183], v101 offset:4352\n\t"
        "s_waitcnt lgkmcnt(8)\n\t"
        "ds_read_b64_tr_b16 v[104:105], v102 offset:0\n\t"
        "ds_read_b64_tr_b16 v[106:107], v102 offset:256\n\t"
        "ds_read_b64_tr_b16 v[108:109], v102 offset:4096\n\t"
        "ds_read_b64_tr_b16 v[110:111], v102 offset:4352\n\t"
        "s_lshl_b32 s34, s43, 2\n\t"
        "s_add_u32 s34, s34, 15\n\t"
        "s_and_b32 s34, s34, 15\n\t"
        "s_lshl_b32 s34, s34, 19\n\t"
        "buffer_load_dwordx4 v[64:67], v96, s[16:19], s34 offen nt\n\t"
        "s_add_u32 s34, s34, 0x10000\n\t"
        "buffer_load_dwordx4 v[68:71], v96, s[16:19], s34 offen nt\n\t"
        "s_add_u32 s34, s34, 0x10000\n\t"
        "buffer_load_dwordx4 v[72:75], v96, s[16:19], s34 offen nt\n\t"
        "s_add_u32 s34, s34, 0x10000\n\t"
        "buffer_load_dwordx4 v[76:79], v96, s[16:19], s34 offen nt\n\t"
        "s_add_u32 s34, s34, 0x10000\n\t"
        "buffer_load_dwordx4 v[80:83], v96, s[16:19], s34 offen nt\n\t"
        "s_add_u32 s34, s34, 0x10000\n\t"
        "buffer_load_dwordx4 v[84:87], v96, s[16:19], s34 offen nt\n\t"
        "s_add_u32 s34, s34, 0x10000\n\t"
        "buffer_load_dwordx4 v[88:91], v96, s[16:19], s34 offen nt\n\t"
        "s_add_u32 s34, s34, 0x10000\n\t"
        "buffer_load_dwordx4 v[92:95], v96, s[16:19], s34 offen nt\n\t"
        "s_waitcnt lgkmcnt(0)\n\t"
        "ds_write_b128 v117, v[104:107] offset:16384\n\t"
        "ds_write_b128 v117, v[108:111] offset:17408\n\t"
        "s_waitcnt vmcnt(16)\n\t"
        "ds_write_b128 v97, v[0:3] offset:0\n\t"
        "ds_write_b128 v98, v[4:7] offset:1024\n\t"
        "ds_write_b128 v97, v[8:11] offset:2048\n\t"
        "ds_write_b128 v98, v[12:15] offset:3072\n\t"
        "ds_write_b128 v97, v[16:19] offset:4096\n\t"
        "ds_write_b128 v98, v[20:23] offset:5120\n\t"
        "ds_write_b128 v97, v[24:27] offset:6144\n\t"
        "ds_write_b128 v98, v[28:31] offset:7168\n\t"
        "ds_read_b64_tr_b16 v[184:185], v99 offset:0\n\t"
        "ds_read_b64_tr_b16 v[186:187], v99 offset:256\n\t"
        "ds_read_b64_tr_b16 v[188:189], v99 offset:4096\n\t"
        "ds_read_b64_tr_b16 v[190:191], v99 offset:4352\n\t"
        "s_waitcnt lgkmcnt(8)\n\t"
        "ds_read_b64_tr_b16 v[192:193], v100 offset:0\n\t"
        "ds_read_b64_tr_b16 v[194:195], v100 offset:256\n\t"
        "ds_read_b64_tr_b16 v[196:197], v100 offset:4096\n\t"
        "ds_read_b64_tr_b16 v[198:199], v100 offset:4352\n\t"
        "s_waitcnt lgkmcnt(8)\n\t"
        "ds_read_b64_tr_b16 v[200:201], v101 offset:0\n\t"
        "ds_read_b64_tr_b16 v[202:203], v101 offset:256\n\t"
        "ds_read_b64_tr_b16 v[204:205], v101 offset:4096\n\t"
        "ds_read_b64_tr_b16 v[206:207], v101 offset:4352\n\t"
        "s_waitcnt lgkmcnt(8)\n\t"
        "ds_read_b64_tr_b16 v[104:105], v102 offset:0\n\t"
        "ds_read_b64_tr_b16 v[106:107], v102 offset:256\n\t"
        "ds_read_b64_tr_b16 v[108:109], v102 offset:4096\n\t"
        "ds_read_b64_tr_b16 v[110:111], v102 offset:4352\n\t"
        "s_waitcnt lgkmcnt(0)\n\t"
        "ds_write_b128 v117, v[104:107] offset:18432\n\t"
        "ds_write_b128 v117, v[108:111] offset:19456\n\t"
        "s_waitcnt vmcnt(8)\n\t"
        "ds_write_b128 v97, v[32:35] offset:0\n\t"
        "ds_write_b128 v98, v[36:39] offset:1024\n\t"
        "ds_write_b128 v97, v[40:43] offset:2048\n\t"
        "ds_write_b128 v98, v[44:47] offset:3072\n\t"
        "ds_write_b128 v97, v[48:51] offset:4096\n\t"
        "ds_write_b128 v98, v[52:55] offset:5120\n\t"
        "ds_write_b128 v97, v[56:59] offset:6144\n\t"
        "ds_write_b128 v98, v[60:63] offset:7168\n\t"
        "ds_read_b64_tr_b16 v[208:209], v99 offset:0\n\t"
        "ds_read_b64_tr_b16 v[210:211], v99 offset:256\n\t"
        "ds_read_b64_tr_b16 v[212:213], v99 offset:4096\n\t"
        "ds_read_b64_tr_b16 v[214:215], v99 offset:4352\n\t"
        "s_waitcnt lgkmcnt(8)\n\t"
        "ds_read_b64_tr_b16 v[216:217], v100 offset:0\n\t"
        "ds_read_b64_tr_b16 v[218:219], v100 offset:256\n\t"
        "ds_read_b64_tr_b16 v[220:221], v100 offset:4096\n\t"
        "ds_read_b64_tr_b16 v[222:223], v100 offset:4352\n\t"
        "s_waitcnt lgkmcnt(8)\n\t"
        "ds_read_b64_tr_b16 v[224:225], v101 offset:0\n\t"
        "ds_read_b64_tr_b16 v[226:227], v101 offset:256\n\t"
        "ds_read_b64_tr_b16 v[228:229], v101 offset:4096\n\t"
        "ds_read_b64_tr_b16 v[230:231], v101 offset:4352\n\t"
        "s_waitcnt lgkmcnt(8)\n\t"
        "ds_read_b64_tr_b16 v[104:105], v102 offset:0\n\t"
        "ds_read_b64_tr_b16 v[106:107], v102 offset:256\n\t"
        "ds_read_b64_tr_b16 v[108:109], v102 offset:4096\n\t"
        "ds_read_b64_tr_b16 v[110:111], v102 offset:4352\n\t"
        "s_mov_b32 s35, 33792\n\t"
        "buffer_load_dword v0, off, s[12:15], s35 sc1\n\t"
        "s_waitcnt lgkmcnt(0)\n\t"
        "ds_write_b128 v117, v[104:107] offset:20480\n\t"
        "ds_write_b128 v117, v[108:111] offset:21504\n\t"
        "s_waitcnt vmcnt(0)\n\t"
        "ds_write_b128 v97, v[64:67] offset:0\n\t"
        "ds_write_b128 v98, v[68:71] offset:1024\n\t"
        "ds_write_b128 v97, v[72:75] offset:2048\n\t"
        "ds_write_b128 v98, v[76:79] offset:3072\n\t"
        "ds_write_b128 v97, v[80:83] offset:4096\n\t"
        "ds_write_b128 v98, v[84:87] offset:5120\n\t"
        "ds_write_b128 v97, v[88:91] offset:6144\n\t"
        "ds_write_b128 v98, v[92:95] offset:7168\n\t"
        "ds_read_b64_tr_b16 v[232:233], v99 offset:0\n\t"
        "ds_read_b64_tr_b16 v[234:235], v99 offset:256\n\t"
        "ds_read_b64_tr_b16 v[236:237], v99 offset:4096\n\t"
        "ds_read_b64_tr_b16 v[238:239], v99 offset:4352\n\t"
        "s_waitcnt lgkmcnt(8)\n\t"
        "ds_read_b64_tr_b16 v[240:241], v100 offset:0\n\t"
        "ds_read_b64_tr_b16 v[242:243], v100 offset:256\n\t"
        "ds_read_b64_tr_b16 v[244:245], v100 offset:4096\n\t"
        "ds_read_b64_tr_b16 v[246:247], v100 offset:4352\n\t"
        "s_waitcnt lgkmcnt(8)\n\t"
        "ds_read_b64_tr_b16 v[248:249], v101 offset:0\n\t"
        "ds_read_b64_tr_b16 v[250:251], v101 offset:256\n\t"
        "ds_read_b64_tr_b16 v[252:253], v101 offset:4096\n\t"
        "ds_read_b64_tr_b16 v[254:255], v101 offset:4352\n\t"
        "s_waitcnt lgkmcnt(8)\n\t"
        "ds_read_b64_tr_b16 v[104:105], v102 offset:0\n\t"
        "ds_read_b64_tr_b16 v[106:107], v102 offset:256\n\t"
        "ds_read_b64_tr_b16 v[108:109], v102 offset:4096\n\t"
        "ds_read_b64_tr_b16 v[110:111], v102 offset:4352\n\t"
        "s_mov_b32 s42, 0\n\t"
        "LA_cal%=:\n\t"
        "v_readfirstlane_b32 s34, v0\n\t"
        "s_nop 0\n\t"
        "s_cmp_lg_u32 s34, 0\n\t"
        "s_cbranch_scc1 LA_calok%=\n\t"
        "s_sleep 4\n\t"
        "s_mov_b32 s35, 33792\n\t"
        "buffer_load_dword v0, off, s[12:15], s35 sc1\n\t"
        "s_waitcnt vmcnt(0)\n\t"
        "s_add_u32 s42, s42, 1\n\t"
        "s_cmp_lt_u32 s42, 0x100000\n\t"
        "s_cbranch_scc1 LA_cal%=\n\t"
        "s_movk_i32 s34, 8\n\t"
        "s_branch LA_die%=\n\t"
        "LA_calok%=:\n\t"
        "s_lshl_b32 s35, s44, 10\n\t"
        "v_lshlrev_b32_e32 v1, 2, v122\n\t"
        "v_and_b32_e32 v1, 0x3c0, v1\n\t"
        "buffer_load_dword v1, v1, s[12:15], s35 offen offset:0 sc1\n\t"
        "s_waitcnt lgkmcnt(0)\n\t"
        "ds_write_b128 v126, v[104:107] offset:8192\n\t"
        "ds_write_b128 v126, v[108:111] offset:9216\n\t"
        "s_waitcnt lgkmcnt(0)\n\t"
        "v_mov_b32_e32 v110, v103\n\t"
        "ds_read_b128 v[64:67], v126 offset:0\n\t"
        "ds_read_b128 v[68:71], v126 offset:1024\n\t"
        "ds_read_b128 v[72:75], v126 offset:2048\n\t"
        "ds_read_b128 v[76:79], v126 offset:3072\n\t"
        "ds_read_b128 v[80:83], v126 offset:4096\n\t"
        "ds_read_b128 v[84:87], v126 offset:5120\n\t"
        "ds_read_b128 v[88:91], v126 offset:6144\n\t"
        "ds_read_b128 v[92:95], v126 offset:7168\n\t"
        "ds_read_b128 v[96:99], v126 offset:8192\n\t"
        "ds_read_b128 v[100:103], v126 offset:9216\n\t"
        "s_waitcnt lgkmcnt(0)\n\t"
        "v_mov_b32_e32 v126, 0\n\t"
        "s_barrier\n\t"
        "s_mov_b32 s48, 0\n\t"
        "s_mov_b32 s54, 0\n\t"
        "s_mov_b32 s0, 0x3f803f80\n\t"
        "s_mov_b32 s1, s0\n\t"
        "s_movk_i32 s56, 24576\n\t"
        "s_and_b32 s58, s43, 1\n\t"
        "s_lshl_b32 s58, s58, 12\n\t"
        "s_lshl_b32 s57, s44, 5\n\t"
        "s_add_u32 s57, s57, s43\n\t"
        "s_lshl_b32 s57, s57, 2\n\t"
        "s_add_u32 s57, s57, 458752\n\t"
        "s_waitcnt vmcnt(0)\n\t"
        "v_mov_b32_e32 v111, v1\n\t"
        "v_readfirstlane_b32 s34, v0\n\t"
        "s_nop 0\n\t"
        "s_and_b32 s34, s34, 8192\n\t"
        "s_add_u32 s8, s8, s34\n\t"
        "s_addc_u32 s9, s9, 0\n\t"
        "s_barrier\n\t"
        "s_cmp_lg_u32 s43, 0\n\t"
        "s_cbranch_scc1 LA_st%=\n\t"
        "v_mov_b32_e32 v2, 1\n\t"
        "s_mov_b32 s35, 34304\n\t"
        "s_mov_b64 exec, 1\n\t"
        "buffer_atomic_add v2, off, s[12:15], s35 sc0\n\t"
        "s_mov_b64 exec, -1\n\t"
        "s_waitcnt vmcnt(0)\n\t"
        "v_readfirstlane_b32 s34, v2\n\t"
        "s_nop 0\n\t"
        "s_cmp_lg_u32 s34, 95\n\t"
        "s_cbranch_scc1 LA_st%=\n\t"
        "s_memrealtime s[32:33]\n\t"
        "v_mbcnt_lo_u32_b32 v3, -1, 0\n\t"
        "v_lshlrev_b32_e32 v3, 7, v3\n\t"
        "s_waitcnt lgkmcnt(0)\n\t"
        "s_add_u32 s34, s32, 50\n\t"
        "s_or_b32 s34, s34, 1\n\t"
        "v_mov_b32_e32 v2, s34\n\t"
        "s_mov_b32 s35, 36864\n\t"
        "s_mov_b64 exec, 0xffff\n\t"
        "buffer_store_dword v2, v3, s[12:15], s35 offen sc1\n\t"
        "s_mov_b64 exec, -1\n\t"
        "LA_st%=:\n\t"
        "s_mov_b32 s24, 0\n\t"
        "s_mov_b32 s26, 0\n\t"
        "s_mov_b32 s27, 0x80000\n\t"
        "s_mov_b32 s28, 0x180000\n\t"
        "s_mov_b32 s31, 0\n\t"
        "s_mov_b32 s47, 0\n\t"
        "s_mov_b32 s38, 0xff00ff00\n\t"
        "s_mov_b32 s39, 0xff00ff00\n\t"
        "s_mov_b32 s40, 0\n\t"
        "s_mov_b32 s41, -1\n\t"
        "ds_read_b128 v[32:35], v117 offset:0\n\t"
        "ds_read_b128 v[36:39], v117 offset:1024\n\t"
        "ds_read_b128 v[40:43], v117 offset:2048\n\t"
        "ds_read_b128 v[44:47], v117 offset:3072\n\t"
        "ds_read_b128 v[48:51], v117 offset:4096\n\t"
        "ds_read_b128 v[52:55], v117 offset:5120\n\t"
        "ds_read_b128 v[56:59], v117 offset:6144\n\t"
        "ds_read_b128 v[60:63], v117 offset:7168\n\t"
        "v_mov_b64_e32 v[0:1], 0\n\t"
        "v_mov_b64_e32 v[2:3], 0\n\t"
        "v_mov_b64_e32 v[4:5], 0\n\t"
        "v_mov_b64_e32 v[6:7], 0\n\t"
        "v_mov_b64_e32 v[8:9], 0\n\t"
        "v_mov_b64_e32 v[10:11], 0\n\t"
        "v_mov_b64_e32 v[12:13], 0\n\t"
        "v_mov_b64_e32 v[14:15], 0\n\t"
        "s_lshr_b32 s59, s48, 1\n\t"
        "s_cmp_gt_u32 s54, s59\n\t"
        "s_cbranch_scc1 LA_kv0ok%=\n\t"
        "s_mov_b32 s42, 0\n\t"
        "s_waitcnt vmcnt(0)\n\t"
        "s_branch LA_kv0w%=\n\t"
        "LA_kv0%=:\n\t"
        "s_waitcnt vmcnt(0)\n\t"
        "LA_kv0w%=:\n\t"
        "v_min_u32_dpp v111, v111, v111 quad_perm:[1,0,3,2] row_mask:0xf bank_mask:0xf\n\t"
        "s_nop 1\n\t"
        "v_min_u32_dpp v111, v111, v111 quad_perm:[2,3,0,1] row_mask:0xf bank_mask:0xf\n\t"
        "s_nop 1\n\t"
        "v_min_u32_dpp v111, v111, v111 row_ror:4 row_mask:0xf bank_mask:0xf\n\t"
        "s_nop 1\n\t"
        "v_min_u32_dpp v111, v111, v111 row_ror:8 row_mask:0xf bank_mask:0xf\n\t"
        "s_nop 0\n\t"
        "v_readfirstlane_b32 s54, v111\n\t"
        "s_nop 0\n\t"
        "s_cmp_gt_u32 s54, s59\n\t"
        "s_cbranch_scc1 LA_kv0ok%=\n\t"
        "s_sleep 4\n\t"
        "s_lshl_b32 s35, s44, 10\n\t"
        "v_lshlrev_b32_e32 v111, 2, v122\n\t"
        "v_and_b32_e32 v111, 0x3c0, v111\n\t"
        "buffer_load_dword v111, v111, s[12:15], s35 offen offset:0 sc1\n\t"
        "s_add_u32 s42, s42, 1\n\t"
        "s_cmp_lt_u32 s42, 0x100000\n\t"
        "s_cbranch_scc1 LA_kv0%=\n\t"
        "s_movk_i32 s34, 16\n\t"
        "s_branch LA_die%=\n\t"
        "LA_kv0ok%=:\n\t"
        "s_and_b32 s36, s48, 1\n\t"
        "s_mul_i32 s36, s36, 24576\n\t"
        "s_add_u32 s36, s36, s55\n\t"
        "s_lshl_b32 s37, s48, 18\n\t"
        "s_lshl_b32 s34, s43, 11\n\t"
        "s_add_u32 s37, s37, s34\n\t"
        "s_add_u32 m0, s36, 0\n\t"
        "s_add_u32 s34, s37, 29548544\n\t"
        "buffer_load_dwordx4 v122, s[12:15], s34 offen sc1 lds\n\t"
        "s_add_u32 m0, s36, 1024\n\t"
        "s_add_u32 s34, s37, 29549568\n\t"
        "buffer_load_dwordx4 v122, s[12:15], s34 offen sc1 lds\n\t"
        "s_add_u32 m0, s36, 8192\n\t"
        "s_add_u32 s34, s37, 12771328\n\t"
        "buffer_load_dwordx4 v122, s[12:15], s34 offen sc1 lds\n\t"
        "s_add_u32 m0, s36, 9216\n\t"
        "s_add_u32 s34, s37, 12772352\n\t"
        "buffer_load_dwordx4 v122, s[12:15], s34 offen sc1 lds\n\t"
        "s_add_u32 m0, s36, 16384\n\t"
        "s_add_u32 s34, s37, 46325760\n\t"
        "buffer_load_dwordx4 v122, s[12:15], s34 offen sc1 lds\n\t"
        "s_add_u32 m0, s36, 17408\n\t"
        "s_add_u32 s34, s37, 46326784\n\t"
        "buffer_load_dwordx4 v122, s[12:15], s34 offen sc1 lds\n\t"
        "s_mov_b32 s42, 0\n\t"
        "LA_gw%=:\n\t"
        "buffer_load_dword v16, off, s[12:15], s45 sc1\n\t"
        "s_waitcnt vmcnt(0)\n\t"
        "v_readfirstlane_b32 s25, v16\n\t"
        "s_nop 0\n\t"
        "s_cmp_lg_u32 s25, 0\n\t"
        "s_cbranch_scc1 LA_loop%=\n\t"
        "s_sleep 2\n\t"
        "s_add_u32 s42, s42, 1\n\t"
        "s_cmp_lt_u32 s42, 0x100000\n\t"
        "s_cbranch_scc1 LA_gw%=\n\t"
        "s_movk_i32 s34, 9\n\t"
        "s_branch LA_die%=\n\t"
        ".p2align 8\n\t"
        "LA_loop%=:\n\t"
        "s_mov_b32 s42, 0\n\t"
        "s_sub_u32 s35, s25, 8\n\t"
        "s_waitcnt lgkmcnt(0)\n\t"
        "s_memrealtime s[2:3]\n\t"
        "s_memrealtime s[4:5]\n\t"
        "s_memrealtime s[6:7]\n\t"
        "LA_wa%=:\n\t"
        "s_memrealtime s[32:33]\n\t"
        "s_waitcnt lgkmcnt(6)\n\t"
        "s_sub_u32 s34, s35, s2\n\t"
        "s_cmpk_lt_u32 s34, 0x2000\n\t"
        "s_cbranch_scc0 LA_wa%=x\n\t"
        "s_memrealtime s[2:3]\n\t"
        "s_waitcnt lgkmcnt(6)\n\t"
        "s_sub_u32 s34, s35, s4\n\t"
        "s_cmpk_lt_u32 s34, 0x2000\n\t"
        "s_cbranch_scc0 LA_wa%=x\n\t"
        "s_memrealtime s[4:5]\n\t"
        "s_waitcnt lgkmcnt(6)\n\t"
        "s_sub_u32 s34, s35, s6\n\t"
        "s_cmpk_lt_u32 s34, 0x2000\n\t"
        "s_cbranch_scc0 LA_wa%=x\n\t"
        "s_memrealtime s[6:7]\n\t"
        "s_waitcnt lgkmcnt(6)\n\t"
        "s_sub_u32 s34, s35, s32\n\t"
        "s_cmpk_lt_u32 s34, 0x2000\n\t"
        "s_cbranch_scc1 LA_wa%=\n\t"
        "s_nop 3\n\t"
        "LA_wa%=x:\n\t"
        "s_add_i32 s34, s34, 4\n\t"
        "s_max_i32 s34, s34, 0\n\t"
        "s_bitcmp1_b32 s34, 1\n\t"
        "s_cbranch_scc0 LA_wa%=y\n\t"
        "s_nop 15\n\t"
        "LA_wa%=y:\n\t"
        "s_bitcmp1_b32 s34, 0\n\t"
        "s_cbranch_scc0 LA_wa%=z\n\t"
        "s_nop 9\n\t"
        "LA_wa%=z:\n\t"
        "LA_rd%=:\n\t"
        "buffer_load_dwordx4 v[16:19], v112, s[8:11], s26 offen offset:0 sc1\n\t"
        "s_nop 12\n\t"
        "buffer_load_dwordx4 v[20:23], v112, s[8:11], s26 offen offset:1024 sc1\n\t"
        "s_nop 12\n\t"
        "buffer_load_dwordx4 v[24:27], v112, s[8:11], s26 offen offset:2048 sc1\n\t"
        "s_nop 12\n\t"
        "buffer_load_dwordx4 v[28:31], v112, s[8:11], s26 offen offset:3072 sc1\n\t"
        "LA_go%=:\n\t"
        "s_waitcnt vmcnt(3)\n\t"
        "v_smfmac_f32_16x16x64_bf16 v[0:3], v[16:19], a[0:7], v110\n\t"
        "ds_write_b128 v113, v[16:19] offset:0\n\t"
        "v_smfmac_f32_16x16x64_bf16 v[4:7], v[16:19], a[8:15], v110\n\t"
        "v_max3_u32 v111, v16, v17, v18\n\t"
        "v_max_u32_e32 v111, v19, v111\n\t"
        "v_smfmac_f32_16x16x64_bf16 v[8:11], v[16:19], a[16:23], v110\n\t"
        "v_smfmac_f32_16x16x64_bf16 v[12:15], v[16:19], v[32:39], v110\n\t"
        "s_waitcnt vmcnt(2)\n\t"
        "v_smfmac_f32_16x16x64_bf16 v[0:3], v[20:23], a[24:31], v110\n\t"
        "ds_write_b128 v113, v[20:23] offset:1024\n\t"
        "v_smfmac_f32_16x16x64_bf16 v[4:7], v[20:23], a[32:39], v110\n\t"
        "v_max3_u32 v111, v20, v21, v111\n\t"
        "v_max3_u32 v111, v22, v23, v111\n\t"
        "v_smfmac_f32_16x16x64_bf16 v[8:11], v[20:23], a[40:47], v110\n\t"
        "s_waitcnt vmcnt(1)\n\t"
        "ds_write_b128 v113, v[24:27] offset:2048\n\t"
        "v_smfmac_f32_16x16x64_bf16 v[12:15], v[20:23], v[40:47], v110\n\t"
        "v_max3_u32 v111, v24, v25, v111\n\t"
        "v_max3_u32 v111, v26, v27, v111\n\t"
        "v_smfmac_f32_16x16x64_bf16 v[0:3], v[24:27], a[48:55], v110\n\t"
        "v_smfmac_f32_16x16x64_bf16 v[4:7], v[24:27], a[56:63], v110\n\t"
        "s_waitcnt vmcnt(0)\n\t"
        "ds_write_b128 v113, v[28:31] offset:3072\n\t"
        "v_max3_u32 v111, v28, v29, v111\n\t"
        "v_max3_u32 v111, v30, v31, v111\n\t"
        "v_smfmac_f32_16x16x64_bf16 v[8:11], v[24:27], a[64:71], v110\n\t"
        "v_cmp_eq_u32_e32 vcc, -1, v111\n\t"
        "v_smfmac_f32_16x16x64_bf16 v[12:15], v[24:27], v[48:55], v110\n\t"
        "s_waitcnt lgkmcnt(0)\n\t"
        "s_cbranch_vccnz LA_rdfail%=\n\t"
        "s_barrier\n\t"
        "ds_read_b128 v[16:19], v114 offset:0\n\t"
        "ds_read_b128 v[20:23], v114 offset:1024\n\t"
        "ds_read_b128 v[24:27], v114 offset:2048\n\t"
        "v_smfmac_f32_16x16x64_bf16 v[0:3], v[28:31], a[72:79], v110\n\t"
        "v_smfmac_f32_16x16x64_bf16 v[4:7], v[28:31], a[80:87], v110\n\t"
        "v_smfmac_f32_16x16x64_bf16 v[8:11], v[28:31], a[88:95], v110\n\t"
        "v_smfmac_f32_16x16x64_bf16 v[12:15], v[28:31], v[56:63], v110\n\t"
        "ds_read_b128 v[28:31], v114 offset:3072\n\t"
        "s_waitcnt lgkmcnt(3)\n\t"
        "v_smfmac_f32_16x16x64_bf16 v[12:15], v[16:19], v[64:71], v110\n\t"
        "ds_read_b128 v[32:35], v117 offset:8192\n\t"
        "v_smfmac_f32_16x16x64_bf16 v[0:3], v[16:19], a[96:103], v110\n\t"
        "ds_read_b128 v[36:39], v117 offset:9216\n\t"
        "v_smfmac_f32_16x16x64_bf16 v[4:7], v[16:19], a[104:111], v110\n\t"
        "s_waitcnt lgkmcnt(4)\n\t"
        "v_smfmac_f32_16x16x64_bf16 v[8:11], v[16:19], a[112:119], v110\n\t"
        "ds_read_b128 v[16:19], v115 offset:0\n\t"
        "v_smfmac_f32_16x16x64_bf16 v[12:15], v[20:23], v[72:79], v110\n\t"
        "ds_read_b128 v[40:43], v117 offset:10240\n\t"
        "v_smfmac_f32_16x16x64_bf16 v[0:3], v[20:23], a[120:127], v110\n\t"
        "ds_read_b128 v[44:47], v117 offset:11264\n\t"
        "v_smfmac_f32_16x16x64_bf16 v[4:7], v[20:23], a[128:135], v110\n\t"
        "s_waitcnt lgkmcnt(6)\n\t"
        "v_smfmac_f32_16x16x64_bf16 v[8:11], v[20:23], a[136:143], v110\n\t"
        "ds_read_b128 v[20:23], v115 offset:1024\n\t"
        "v_smfmac_f32_16x16x64_bf16 v[12:15], v[24:27], v[80:87], v110\n\t"
        "ds_read_b128 v[48:51], v117 offset:12288\n\t"
        "v_smfmac_f32_16x16x64_bf16 v[0:3], v[24:27], a[144:151], v110\n\t"
        "ds_read_b128 v[52:55], v117 offset:13312\n\t"
        "v_smfmac_f32_16x16x64_bf16 v[4:7], v[24:27], a[152:159], v110\n\t"
        "s_waitcnt lgkmcnt(8)\n\t"
        "v_smfmac_f32_16x16x64_bf16 v[8:11], v[24:27], a[160:167], v110\n\t"
        "ds_read_b128 v[24:27], v115 offset:2048\n\t"
        "v_smfmac_f32_16x16x64_bf16 v[12:15], v[28:31], v[88:95], v110\n\t"
        "ds_read_b128 v[56:59], v117 offset:14336\n\t"
        "v_smfmac_f32_16x16x64_bf16 v[0:3], v[28:31], a[168:175], v110\n\t"
        "ds_read_b128 v[60:63], v117 offset:15360\n\t"
        "v_smfmac_f32_16x16x64_bf16 v[4:7], v[28:31], a[176:183], v110\n\t"
        "s_waitcnt lgkmcnt(8)\n\t"
        "v_smfmac_f32_16x16x64_bf16 v[8:11], v[28:31], a[184:191], v110\n\t"
        "ds_read_b128 v[28:31], v115 offset:3072\n\t"
        "v_smfmac_f32_16x16x64_bf16 v[12:15], v[16:19], v[32:39], v110\n\t"
        "ds_read_b128 v[32:35], v117 offset:16384\n\t"
        "v_smfmac_f32_16x16x64_bf16 v[0:3], v[16:19], a[192:199], v110\n\t"
        "ds_read_b128 v[36:39], v117 offset:17408\n\t"
        "v_smfmac_f32_16x16x64_bf16 v[4:7], v[16:19], a[200:207], v110\n\t"
        "s_waitcnt lgkmcnt(8)\n\t"
        "v_smfmac_f32_16x16x64_bf16 v[8:11], v[16:19], a[208:215], v110\n\t"
        "ds_read_b128 v[16:19], v116 offset:0\n\t"
        "v_smfmac_f32_16x16x64_bf16 v[12:15], v[20:23], v[40:47], v110\n\t"
        "ds_read_b128 v[40:43], v117 offset:18432\n\t"
        "v_smfmac_f32_16x16x64_bf16 v[0:3], v[20:23], a[216:223], v110\n\t"
        "ds_read_b128 v[44:47], v117 offset:19456\n\t"
        "v_smfmac_f32_16x16x64_bf16 v[4:7], v[20:23], a[224:231], v110\n\t"
        "s_waitcnt lgkmcnt(8)\n\t"
        "v_smfmac_f32_16x16x64_bf16 v[8:11], v[20:23], a[232:239], v110\n\t"
        "ds_read_b128 v[20:23], v116 offset:1024\n\t"
        "v_smfmac_f32_16x16x64_bf16 v[12:15], v[24:27], v[48:55], v110\n\t"
        "ds_read_b128 v[48:51], v117 offset:20480\n\t"
        "v_smfmac_f32_16x16x64_bf16 v[0:3], v[24:27], a[240:247], v110\n\t"
        "ds_read_b128 v[52:55], v117 offset:21504\n\t"
        "v_smfmac_f32_16x16x64_bf16 v[4:7], v[24:27], a[248:255], v110\n\t"
        "s_waitcnt lgkmcnt(8)\n\t"
        "v_smfmac_f32_16x16x64_bf16 v[8:11], v[24:27], v[128:135], v110\n\t"
        "ds_read_b128 v[24:27], v116 offset:2048\n\t"
        "v_smfmac_f32_16x16x64_bf16 v[12:15], v[28:31], v[56:63], v110\n\t"
        "ds_read_b128 v[56:59], v124 offset:3072\n\t"
        "v_smfmac_f32_16x16x64_bf16 v[0:3], v[28:31], v[136:143], v110\n\t"
        "ds_read_b128 v[60:63], v124 offset:7168\n\t"
        "v_smfmac_f32_16x16x64_bf16 v[4:7], v[28:31], v[144:151], v110\n\t"
        "s_waitcnt lgkmcnt(8)\n\t"
        "v_smfmac_f32_16x16x64_bf16 v[8:11], v[28:31], v[152:159], v110\n\t"
        "ds_read_b128 v[28:31], v116 offset:3072\n\t"
        "v_smfmac_f32_16x16x64_bf16 v[12:15], v[16:19], v[32:39], v110\n\t"
        "ds_read_b128 v[32:35], v124 offset:0\n\t"
        "v_smfmac_f32_16x16x64_bf16 v[0:3], v[16:19], v[160:167], v110\n\t"
        "ds_read_b128 v[36:39], v124 offset:4096\n\t"
        "v_smfmac_f32_16x16x64_bf16 v[4:7], v[16:19], v[168:175], v110\n\t"
        "s_waitcnt lgkmcnt(8)\n\t"
        "v_smfmac_f32_16x16x64_bf16 v[8:11], v[16:19], v[176:183], v110\n\t"
        "v_smfmac_f32_16x16x64_bf16 v[12:15], v[20:23], v[40:47], v110\n\t"
        "ds_read_b128 v[40:43], v124 offset:1024\n\t"
        "v_smfmac_f32_16x16x64_bf16 v[8:11], v[20:23], v[200:207], v110\n\t"
        "ds_read_b128 v[44:47], v124 offset:5120\n\t"
        "s_waitcnt lgkmcnt(7)\n\t"
        "v_smfmac_f32_16x16x64_bf16 v[12:15], v[24:27], v[48:55], v110\n\t"
        "ds_read_b128 v[48:51], v124 offset:2048\n\t"
        "v_smfmac_f32_16x16x64_bf16 v[8:11], v[24:27], v[224:231], v110\n\t"
        "ds_read_b128 v[52:55], v124 offset:6144\n\t"
        "s_waitcnt lgkmcnt(6)\n\t"
        "v_smfmac_f32_16x16x64_bf16 v[12:15], v[28:31], v[96:103], v110\n\t"
        "v_smfmac_f32_16x16x64_bf16 v[8:11], v[28:31], v[248:255], v110\n\t"
        "v_smfmac_f32_16x16x64_bf16 v[0:3], v[20:23], v[184:191], v110\n\t"
        "s_lshl_b32 s34, s43, 11\n\t"
        "v_add_u32_e32 v108, s34, v124\n\t"
        "v_smfmac_f32_16x16x64_bf16 v[0:3], v[24:27], v[208:215], v110\n\t"
        "v_add_u32_e32 v109, s58, v124\n\t"
        "v_smfmac_f32_16x16x64_bf16 v[4:7], v[20:23], v[192:199], v110\n\t"
        "v_smfmac_f32_16x16x64_bf16 v[0:3], v[28:31], v[232:239], v110\n\t"
        "v_smfmac_f32_16x16x64_bf16 v[4:7], v[24:27], v[216:223], v110\n\t"
        "buffer_load_dwordx4 v[104:107], v120, s[8:11], s26 offen sc1\n\t"
        "v_smfmac_f32_16x16x64_bf16 v[4:7], v[28:31], v[240:247], v110\n\t"
        "v_add_f32_e32 v12, v12, v13\n\t"
        "v_add_f32_e32 v14, v14, v15\n\t"
        "v_cvt_pk_bf16_f32 v12, v12, v14\n\t"
        "v_add_f32_e32 v8, v8, v9\n\t"
        "v_add_f32_e32 v10, v10, v11\n\t"
        "v_cvt_pk_bf16_f32 v8, v8, v10\n\t"
        "ds_write2_b32 v118, v8, v12 offset0:64 offset1:96\n\t"
        "v_add_f32_e32 v0, v0, v1\n\t"
        "v_add_f32_e32 v2, v2, v3\n\t"
        "v_cvt_pk_bf16_f32 v0, v0, v2\n\t"
        "v_add_f32_e32 v4, v4, v5\n\t"
        "v_add_f32_e32 v6, v6, v7\n\t"
        "v_cvt_pk_bf16_f32 v4, v4, v6\n\t"
        "ds_write2_b32 v118, v0, v4 offset0:0 offset1:32\n\t"
        "s_cmp_eq_u32 s24, 63\n\t"
        "s_waitcnt lgkmcnt(0)\n\t"
        "s_barrier\n\t"
        "s_cbranch_scc1 LA_skip%=\n\t"
        "ds_read_b64_tr_b16 v[16:17], v119 offset:0\n\t"
        "ds_read_b64_tr_b16 v[18:19], v119 offset:32\n\t"
        "ds_read_b64_tr_b16 v[20:21], v119 offset:256\n\t"
        "ds_read_b64_tr_b16 v[22:23], v119 offset:288\n\t"
        "ds_read_b64_tr_b16 v[24:25], v119 offset:512\n\t"
        "ds_read_b64_tr_b16 v[26:27], v119 offset:544\n\t"
        "ds_read_b64_tr_b16 v[28:29], v119 offset:768\n\t"
        "ds_read_b64_tr_b16 v[30:31], v119 offset:800\n\t"
        "buffer_store_dword v126, v127, s[12:15], 0 offen sc1\n\t"
        "s_waitcnt lgkmcnt(6)\n\t"
        "v_mfma_f32_16x16x32_bf16 v[0:3], v[32:35], v[16:19], 0\n\t"
        "ds_read_b64_tr_b16 v[32:33], v119 offset:2112\n\t"
        "ds_read_b64_tr_b16 v[34:35], v119 offset:2144\n\t"
        "v_mfma_f32_16x16x32_bf16 v[4:7], v[36:39], v[16:19], 0\n\t"
        "ds_read_b64_tr_b16 v[36:37], v119 offset:2368\n\t"
        "ds_read_b64_tr_b16 v[38:39], v119 offset:2400\n\t"
        "s_waitcnt lgkmcnt(8)\n\t"
        "v_mfma_f32_16x16x32_bf16 v[0:3], v[40:43], v[20:23], v[0:3]\n\t"
        "ds_read_b64_tr_b16 v[40:41], v119 offset:2624\n\t"
        "ds_read_b64_tr_b16 v[42:43], v119 offset:2656\n\t"
        "v_mfma_f32_16x16x32_bf16 v[4:7], v[44:47], v[20:23], v[4:7]\n\t"
        "ds_read_b64_tr_b16 v[44:45], v119 offset:2880\n\t"
        "ds_read_b64_tr_b16 v[46:47], v119 offset:2912\n\t"
        "s_waitcnt lgkmcnt(10)\n\t"
        "v_mfma_f32_16x16x32_bf16 v[0:3], v[48:51], v[24:27], v[0:3]\n\t"
        "ds_read_b128 v[48:51], v108 offset:16384\n\t"
        "v_mfma_f32_16x16x32_bf16 v[4:7], v[52:55], v[24:27], v[4:7]\n\t"
        "ds_read_b128 v[52:55], v108 offset:17408\n\t"
        "s_waitcnt lgkmcnt(10)\n\t"
        "v_mfma_f32_16x16x32_bf16 v[0:3], v[56:59], v[28:31], v[0:3]\n\t"
        "v_mfma_f32_16x16x32_bf16 v[4:7], v[60:63], v[28:31], v[4:7]\n\t"
        "s_waitcnt lgkmcnt(2)\n\t"
        "v_mfma_f32_16x16x32_bf16 v[8:11], v[32:35], v[16:19], 0\n\t"
        "s_nop 1\n\t"
        "v_mfma_f32_16x16x32_bf16 v[8:11], v[36:39], v[20:23], v[8:11]\n\t"
        "v_mov_b64_e32 v[16:17], s[0:1]\n\t"
        "v_mov_b64_e32 v[18:19], s[0:1]\n\t"
        "v_mfma_f32_16x16x32_bf16 v[8:11], v[40:43], v[24:27], v[8:11]\n\t"
        "v_max3_f32 v59, v0, v1, v2\n\t"
        "v_max3_f32 v59, v59, v3, v4\n\t"
        "v_mfma_f32_16x16x32_bf16 v[8:11], v[44:47], v[28:31], v[8:11]\n\t"
        "v_max3_f32 v59, v59, v5, v6\n\t"
        "v_max_f32_e32 v60, v59, v7\n\t"
        "v_max_f32_e32 v59, v59, v7\n\t"
        "v_cndmask_b32_e64 v0, v0, v4, s[38:39]\n\t"
        "v_cndmask_b32_e64 v1, v1, v5, s[38:39]\n\t"
        "v_permlane32_swap_b32_e32 v59, v60\n\t"
        "v_cndmask_b32_e64 v2, v2, v6, s[38:39]\n\t"
        "v_cndmask_b32_e64 v3, v3, v7, s[38:39]\n\t"
        "v_max3_f32 v59, v59, v60, v8\n\t"
        "v_max3_f32 v59, v59, v9, v10\n\t"
        "v_max_f32_e32 v60, v59, v11\n\t"
        "v_max_f32_e32 v59, v59, v11\n\t"
        "v_cndmask_b32_e64 v8, v8, v9, s[38:39]\n\t"
        "v_cndmask_b32_e64 v10, v10, v11, s[38:39]\n\t"
        "v_permlane16_swap_b32_e32 v59, v60\n\t"
        "v_max_f32_e32 v59, v59, v60\n\t"
        "v_mul_f32_e32 v59, 0xbe02940c, v59\n\t"
        "v_cndmask_b32_e64 v58, v8, v10, s[40:41]\n\t"
        "v_fmamk_f32 v0, v0, 0x3e02940c, v59\n\t"
        "v_exp_f32_e32 v0, v0\n\t"
        "v_fmamk_f32 v1, v1, 0x3e02940c, v59\n\t"
        "v_exp_f32_e32 v1, v1\n\t"
        "v_fmamk_f32 v2, v2, 0x3e02940c, v59\n\t"
        "v_exp_f32_e32 v2, v2\n\t"
        "v_fmamk_f32 v3, v3, 0x3e02940c, v59\n\t"
        "v_exp_f32_e32 v3, v3\n\t"
        "v_fmamk_f32 v58, v58, 0x3e02940c, v59\n\t"
        "v_cvt_pk_bf16_f32 v12, v0, v1\n\t"
        "v_cvt_pk_bf16_f32 v13, v2, v3\n\t"
        "v_exp_f32_e32 v58, v58\n\t"
        "s_waitcnt lgkmcnt(0)\n\t"
        "v_mov_b32_dpp v14, v12 row_ror:8 row_mask:0xf bank_mask:0xf\n\t"
        "v_mov_b32_dpp v15, v13 row_ror:8 row_mask:0xf bank_mask:0xf\n\t"
        "v_mov_b32_dpp v59, v58 row_ror:8 row_mask:0xf bank_mask:0xf\n\t"
        "v_cvt_pk_bf16_f32 v59, v58, v59\n\t"
        "v_mfma_f32_16x16x32_bf16 v[20:23], v[16:19], v[12:15], 0\n\t"
        "v_cndmask_b32_e64 v56, v59, 0, s[40:41]\n\t"
        "v_cndmask_b32_e64 v57, 0, v59, s[40:41]\n\t"
        "v_mfma_f32_16x16x32_bf16 v[4:7], v[48:51], v[12:15], 0\n\t"
        "s_waitcnt vmcnt(1)\n\t"
        "v_max3_u32 v111, v104, v105, v106\n\t"
        "v_mfma_f32_16x16x32_bf16 v[8:11], v[52:55], v[12:15], 0\n\t"
        "v_max_u32_e32 v111, v111, v107\n\t"
        "v_mfma_f32_16x16x16_bf16 v[20:23], v[16:17], v[56:57], v[20:23]\n\t"
        "v_cmp_eq_u32_e32 vcc, -1, v111\n\t"
        "v_mfma_f32_16x16x16_bf16 v[4:7], v[104:105], v[56:57], v[4:7]\n\t"
        "s_nop 1\n\t"
        "v_mfma_f32_16x16x16_bf16 v[8:11], v[106:107], v[56:57], v[8:11]\n\t"
        "s_cbranch_vccnz LA_vmfail%=\n\t"
        "LA_vmok%=:\n\t"
        "s_nop 1\n\t"
        "v_rcp_f32_e32 v59, v20\n\t"
        "s_nop 0\n\t"
        "v_pk_mul_f32 v[4:5], v[4:5], v[58:59] op_sel:[0,1] op_sel_hi:[1,1]\n\t"
        "v_cvt_pk_bf16_f32 v0, v4, v5\n\t"
        "v_pk_mul_f32 v[6:7], v[6:7], v[58:59] op_sel:[0,1] op_sel_hi:[1,1]\n\t"
        "v_cvt_pk_bf16_f32 v1, v6, v7\n\t"
        "v_pk_mul_f32 v[8:9], v[8:9], v[58:59] op_sel:[0,1] op_sel_hi:[1,1]\n\t"
        "v_cvt_pk_bf16_f32 v2, v8, v9\n\t"
        "v_pk_mul_f32 v[10:11], v[10:11], v[58:59] op_sel:[0,1] op_sel_hi:[1,1]\n\t"
        "v_cvt_pk_bf16_f32 v3, v10, v11\n\t"
        "buffer_store_dwordx4 v[0:3], v121, s[8:11], s26 offen sc1\n\t"
        "LA_x%=:\n\t"
        "s_memrealtime s[52:53]\n\t"
        "ds_read_b128 v[16:19], v109 offset:8192\n\t"
        "ds_read_b128 v[20:23], v109 offset:9216\n\t"
        "ds_read_b128 v[24:27], v109 offset:10240\n\t"
        "ds_read_b128 v[28:31], v109 offset:11264\n\t"
        "ds_read_b128 v[56:59], v124 offset:0\n\t"
        "ds_read_b128 v[60:63], v124 offset:4096\n\t"
        "ds_read_b128 v[12:15], v124 offset:1024\n\t"
        "v_xor_b32_e32 v126, 0x800, v108\n\t"
        "s_add_u32 s48, s24, 1\n\t"
        "s_min_u32 s48, s48, 63\n\t"
        "buffer_load_dwordx2 v[108:109], v125, s[8:11], s26 offen sc1\n\t"
        "s_lshl_b32 s35, s44, 10\n\t"
        "s_lshr_b32 s34, s48, 1\n\t"
        "s_add_u32 s34, s34, 1\n\t"
        "s_min_u32 s34, s34, 31\n\t"
        "s_cmp_gt_u32 s54, s34\n\t"
        "s_cselect_b32 s35, 0xf0000000, s35\n\t"
        "v_lshlrev_b32_e32 v111, 2, v122\n\t"
        "v_and_b32_e32 v111, 0x3c0, v111\n\t"
        "buffer_load_dword v111, v111, s[12:15], s35 offen offset:0 sc1\n\t"
        "s_waitcnt lgkmcnt(6)\n\t"
        "v_mfma_f32_16x16x32_bf16 v[8:11], v[32:35], v[16:19], 0\n\t"
        "ds_read_b128 v[32:35], v124 offset:5120\n\t"
        "s_waitcnt lgkmcnt(6)\n\t"
        "v_mfma_f32_16x16x32_bf16 v[8:11], v[36:39], v[20:23], v[8:11]\n\t"
        "ds_read_b128 v[36:39], v124 offset:2048\n\t"
        "s_waitcnt lgkmcnt(6)\n\t"
        "v_mfma_f32_16x16x32_bf16 v[8:11], v[40:43], v[24:27], v[8:11]\n\t"
        "ds_read_b128 v[40:43], v124 offset:6144\n\t"
        "s_waitcnt lgkmcnt(6)\n\t"
        "v_mfma_f32_16x16x32_bf16 v[8:11], v[44:47], v[28:31], v[8:11]\n\t"
        "ds_read_b128 v[44:47], v124 offset:3072\n\t"
        "s_add_u32 s35, s24, 1\n\t"
        "s_waitcnt lgkmcnt(6)\n\t"
        "v_mfma_f32_16x16x32_bf16 v[0:3], v[56:59], v[16:19], 0\n\t"
        "ds_read_b128 v[56:59], v124 offset:7168\n\t"
        "s_waitcnt lgkmcnt(6)\n\t"
        "v_mfma_f32_16x16x32_bf16 v[4:7], v[60:63], v[16:19], 0\n\t"
        "s_and_b32 s36, s35, 1\n\t"
        "s_mul_i32 s36, s36, 24576\n\t"
        "s_waitcnt lgkmcnt(5)\n\t"
        "v_mfma_f32_16x16x32_bf16 v[0:3], v[12:15], v[20:23], v[0:3]\n\t"
        "s_add_u32 s36, s36, s55\n\t"
        "s_lshl_b32 s37, s48, 18\n\t"
        "s_waitcnt lgkmcnt(4)\n\t"
        "v_mfma_f32_16x16x32_bf16 v[4:7], v[32:35], v[20:23], v[4:7]\n\t"
        "v_xor_b32_e32 v32, 0x200, v120\n\t"
        "buffer_load_dwordx4 v[12:15], v32, s[8:11], s26 offen sc1\n\t"
        "s_waitcnt lgkmcnt(3)\n\t"
        "v_mfma_f32_16x16x32_bf16 v[0:3], v[36:39], v[24:27], v[0:3]\n\t"
        "s_lshl_b32 s34, s43, 11\n\t"
        "s_add_u32 s37, s37, s34\n\t"
        "s_waitcnt lgkmcnt(2)\n\t"
        "v_mfma_f32_16x16x32_bf16 v[4:7], v[40:43], v[24:27], v[4:7]\n\t"
        "v_cvt_pk_bf16_f32 v8, 0, v8\n\t"
        "v_cvt_pk_bf16_f32 v9, 0, v9\n\t"
        "v_cvt_pk_bf16_f32 v10, 0, v10\n\t"
        "v_cvt_pk_bf16_f32 v11, 0, v11\n\t"
        "s_waitcnt lgkmcnt(1)\n\t"
        "v_mfma_f32_16x16x32_bf16 v[0:3], v[44:47], v[28:31], v[0:3]\n\t"
        "v_mul_f32_e32 v8, 0x3db504f3, v8\n\t"
        "v_mul_f32_e32 v9, 0x3db504f3, v9\n\t"
        "v_mul_f32_e32 v10, 0x3db504f3, v10\n\t"
        "v_mul_f32_e32 v11, 0x3db504f3, v11\n\t"
        "s_waitcnt lgkmcnt(0)\n\t"
        "v_mfma_f32_16x16x32_bf16 v[4:7], v[56:59], v[28:31], v[4:7]\n\t"
        "v_cvt_pk_bf16_f32 v8, 0, v8\n\t"
        "v_cvt_pk_bf16_f32 v9, 0, v9\n\t"
        "v_cvt_pk_bf16_f32 v10, 0, v10\n\t"
        "v_cvt_pk_bf16_f32 v11, 0, v11\n\t"
        "ds_read_b128 v[56:59], v126 offset:16384\n\t"
        "ds_read_b128 v[60:63], v126 offset:17408\n\t"
        "v_mov_b64_e32 v[36:37], -1\n\t"
        "v_mov_b64_e32 v[38:39], -1\n\t"
        "v_mov_b32_e32 v40, 0xce6e6b28\n\t"
        "v_mov_b32_e32 v46, 0x3db504f3\n\t"
        "v_mov_b32_e32 v47, 0x3fb8aa3b\n\t"
        "v_cvt_pk_bf16_f32 v0, 0, v0\n\t"
        "v_cvt_pk_bf16_f32 v1, 0, v1\n\t"
        "v_cvt_pk_bf16_f32 v2, 0, v2\n\t"
        "v_cvt_pk_bf16_f32 v3, 0, v3\n\t"
        "v_cvt_pk_bf16_f32 v4, 0, v4\n\t"
        "v_cvt_pk_bf16_f32 v5, 0, v5\n\t"
        "v_cvt_pk_bf16_f32 v6, 0, v6\n\t"
        "v_cvt_pk_bf16_f32 v7, 0, v7\n\t"
        "v_pk_mul_f32 v[0:1], v[0:1], v[46:47] op_sel_hi:[1,0]\n\t"
        "v_pk_mul_f32 v[2:3], v[2:3], v[46:47] op_sel_hi:[1,0]\n\t"
        "v_pk_mul_f32 v[4:5], v[4:5], v[46:47] op_sel_hi:[1,0]\n\t"
        "v_pk_mul_f32 v[6:7], v[6:7], v[46:47] op_sel_hi:[1,0]\n\t"
        "s_lshr_b32 s59, s48, 1\n\t"
        "s_cmp_gt_u32 s54, s59\n\t"
        "s_cbranch_scc1 LA_kvok%=\n\t"
        "s_mov_b32 s42, 0\n\t"
        "s_waitcnt vmcnt(1)\n\t"
        "s_branch LA_kvw%=\n\t"
        "LA_kv%=:\n\t"
        "s_waitcnt vmcnt(0)\n\t"
        "LA_kvw%=:\n\t"
        "v_min_u32_dpp v111, v111, v111 quad_perm:[1,0,3,2] row_mask:0xf bank_mask:0xf\n\t"
        "s_nop 1\n\t"
        "v_min_u32_dpp v111, v111, v111 quad_perm:[2,3,0,1] row_mask:0xf bank_mask:0xf\n\t"
        "s_nop 1\n\t"
        "v_min_u32_dpp v111, v111, v111 row_ror:4 row_mask:0xf bank_mask:0xf\n\t"
        "s_nop 1\n\t"
        "v_min_u32_dpp v111, v111, v111 row_ror:8 row_mask:0xf bank_mask:0xf\n\t"
        "s_nop 0\n\t"
        "v_readfirstlane_b32 s54, v111\n\t"
        "s_nop 0\n\t"
        "s_cmp_gt_u32 s54, s59\n\t"
        "s_cbranch_scc1 LA_kvok%=\n\t"
        "s_sleep 4\n\t"
        "s_lshl_b32 s35, s44, 10\n\t"
        "v_lshlrev_b32_e32 v111, 2, v122\n\t"
        "v_and_b32_e32 v111, 0x3c0, v111\n\t"
        "buffer_load_dword v111, v111, s[12:15], s35 offen offset:0 sc1\n\t"
        "s_add_u32 s42, s42, 1\n\t"
        "s_cmp_lt_u32 s42, 0x100000\n\t"
        "s_cbranch_scc1 LA_kv%=\n\t"
        "s_movk_i32 s34, 16\n\t"
        "s_branch LA_die%=\n\t"
        "LA_kvok%=:\n\t"
        "v_cvt_pk_bf16_f32 v0, 0, v0\n\t"
        "v_cvt_pk_bf16_f32 v1, 0, v1\n\t"
        "v_cvt_pk_bf16_f32 v2, 0, v2\n\t"
        "v_cvt_pk_bf16_f32 v3, 0, v3\n\t"
        "v_cvt_pk_bf16_f32 v4, 0, v4\n\t"
        "v_cvt_pk_bf16_f32 v5, 0, v5\n\t"
        "v_cvt_pk_bf16_f32 v6, 0, v6\n\t"
        "v_cvt_pk_bf16_f32 v7, 0, v7\n\t"
        "v_cndmask_b32_e64 v0, v40, v0, s[60:61]\n\t"
        "v_cndmask_b32_e64 v1, v40, v1, s[62:63]\n\t"
        "v_cndmask_b32_e64 v2, v40, v2, s[64:65]\n\t"
        "v_cndmask_b32_e64 v3, v40, v3, s[66:67]\n\t"
        "v_cndmask_b32_e64 v4, v40, v4, s[68:69]\n\t"
        "v_cndmask_b32_e64 v5, v40, v5, s[70:71]\n\t"
        "v_cndmask_b32_e64 v6, v40, v6, s[72:73]\n\t"
        "v_cndmask_b32_e64 v7, v40, v7, s[74:75]\n\t"
        "v_max3_f32 v42, v0, v1, v2\n\t"
        "v_max3_f32 v42, v42, v3, v4\n\t"
        "v_max3_f32 v42, v42, v5, v6\n\t"
        "v_max3_f32 v42, v42, v7, v8\n\t"
        "v_max3_f32 v42, v42, v9, v10\n\t"
        "v_mov_b32_e32 v43, v11\n\t"
        "v_max_f32_e32 v44, v42, v43\n\t"
        "v_max_f32_e32 v42, v42, v43\n\t"
        "s_pack_ll_b32_b16 s34, s52, s24\n\t"
        "v_mov_b32_e32 v40, s34\n\t"
        "s_add_u32 s35, s26, s57\n\t"
        "v_permlane16_swap_b32_e32 v42, v44\n\t"
        "v_max_f32_e32 v45, v42, v44\n\t"
        "v_max_f32_e32 v42, v42, v44\n\t"
        "s_mov_b64 exec, 1\n\t"
        "buffer_store_dword v40, off, s[8:11], s35 sc1\n\t"
        "s_mov_b64 exec, -1\n\t"
        "v_permlane32_swap_b32_e32 v42, v45\n\t"
        "v_max_f32_e32 v42, v42, v45\n\t"
        "v_mul_f32_e32 v42, 0xbfb8aa3b, v42\n\t"
        "v_pk_fma_f32 v[0:1], v[0:1], v[46:47], v[42:43] op_sel:[0,1,0] op_sel_hi:[1,1,0]\n\t"
        "s_add_u32 m0, s36, 0\n\t"
        "s_add_u32 s34, s37, 29548544\n\t"
        "buffer_load_dwordx4 v122, s[12:15], s34 offen sc1 lds\n\t"
        "v_exp_f32_e32 v0, v0\n\t"
        "v_exp_f32_e32 v1, v1\n\t"
        "v_pk_fma_f32 v[2:3], v[2:3], v[46:47], v[42:43] op_sel:[0,1,0] op_sel_hi:[1,1,0]\n\t"
        "v_exp_f32_e32 v2, v2\n\t"
        "v_exp_f32_e32 v3, v3\n\t"
        "v_pk_fma_f32 v[4:5], v[4:5], v[46:47], v[42:43] op_sel:[0,1,0] op_sel_hi:[1,1,0]\n\t"
        "v_exp_f32_e32 v4, v4\n\t"
        "s_add_u32 m0, s36, 1024\n\t"
        "s_add_u32 s34, s37, 29549568\n\t"
        "buffer_load_dwordx4 v122, s[12:15], s34 offen sc1 lds\n\t"
        "v_exp_f32_e32 v5, v5\n\t"
        "v_pk_fma_f32 v[6:7], v[6:7], v[46:47], v[42:43] op_sel:[0,1,0] op_sel_hi:[1,1,0]\n\t"
        "v_exp_f32_e32 v6, v6\n\t"
        "v_exp_f32_e32 v7, v7\n\t"
        "v_pk_fma_f32 v[8:9], v[8:9], v[46:47], v[42:43] op_sel:[0,1,0] op_sel_hi:[1,1,0]\n\t"
        "v_exp_f32_e32 v8, v8\n\t"
        "v_exp_f32_e32 v9, v9\n\t"
        "s_add_u32 m0, s36, 8192\n\t"
        "s_add_u32 s34, s37, 12771328\n\t"
        "buffer_load_dwordx4 v122, s[12:15], s34 offen sc1 lds\n\t"
        "v_pk_fma_f32 v[10:11], v[10:11], v[46:47], v[42:43] op_sel:[0,1,0] op_sel_hi:[1,1,0]\n\t"
        "v_exp_f32_e32 v10, v10\n\t"
        "v_exp_f32_e32 v11, v11\n\t"
        "v_pk_add_f32 v[42:43], v[0:1], v[2:3]\n\t"
        "v_cndmask_b32_e64 v8, v8, 0, s[40:41]\n\t"
        "v_cndmask_b32_e64 v9, v9, 0, s[40:41]\n\t"
        "v_cndmask_b32_e64 v10, v10, 0, s[40:41]\n\t"
        "buffer_store_dwordx4 v[36:39], v121, s[8:11], s28 offen sc1\n\t"
        "v_cndmask_b32_e64 v11, v11, 0, s[40:41]\n\t"
        "v_pk_add_f32 v[42:43], v[42:43], v[4:5]\n\t"
        "v_pk_add_f32 v[42:43], v[42:43], v[6:7]\n\t"
        "v_pk_add_f32 v[42:43], v[42:43], v[8:9]\n\t"
        "v_pk_add_f32 v[42:43], v[42:43], v[10:11]\n\t"
        "v_add_f32_e32 v44, v42, v43\n\t"
        "v_add_f32_e32 v42, v42, v43\n\t"
        "s_lshl_b32 s34, s24, 18\n\t"
        "s_add_u32 s34, s34, 63102976\n\t"
        "v_permlane16_swap_b32_e32 v42, v44\n\t"
        "v_add_f32_e32 v45, v42, v44\n\t"
        "v_add_f32_e32 v42, v42, v44\n\t"
        "s_add_u32 s35, s24, 1\n\t"
        "v_mov_b32_e32 v126, s35\n\t"
        "v_permlane32_swap_b32_e32 v42, v45\n\t"
        "v_add_f32_e32 v42, v42, v45\n\t"
        "v_rcp_f32_e32 v42, v42\n\t"
        "s_waitcnt vmcnt(5) lgkmcnt(0)\n\t"
        "v_max3_u32 v45, v12, v13, v14\n\t"
        "v_max3_u32 v45, v45, v15, v104\n\t"
        "v_max3_u32 v45, v45, v105, v106\n\t"
        "v_max_u32_e32 v45, v45, v107\n\t"
        "v_cmp_eq_u32_e32 vcc, -1, v45\n\t"
        "s_nop 0\n\t"
        "s_cbranch_vccnz LA_v2fail%=\n\t"
        "LA_v2ok%=:\n\t"
        "v_and_b32_e32 v45, 0xff, v109\n\t"
        "v_cmp_ne_u32_e32 vcc, s24, v45\n\t"
        "s_nop 0\n\t"
        "s_cbranch_vccnz LA_stre%=\n\t"
        "LA_stok%=:\n\t"
        "v_pk_mul_f32 v[0:1], v[0:1], v[42:43] op_sel_hi:[1,0]\n\t"
        "v_subrev_u32_e32 v108, s25, v108\n\t"
        "v_min_u32_dpp v111, v111, v111 quad_perm:[1,0,3,2] row_mask:0xf bank_mask:0xf\n\t"
        "s_add_u32 m0, s36, 9216\n\t"
        "s_add_u32 s22, s37, 12772352\n\t"
        "buffer_load_dwordx4 v122, s[12:15], s22 offen sc1 lds\n\t"
        "v_cvt_pk_bf16_f32 v32, v0, v1\n\t"
        "v_pk_mul_f32 v[2:3], v[2:3], v[42:43] op_sel_hi:[1,0]\n\t"
        "v_cvt_pk_bf16_f32 v33, v2, v3\n\t"
        "v_max_i32_dpp v108, v108, v108 quad_perm:[1,0,3,2] row_mask:0xf bank_mask:0xf\n\t"
        "v_min_u32_dpp v111, v111, v111 quad_perm:[2,3,0,1] row_mask:0xf bank_mask:0xf\n\t"
        "s_add_u32 m0, s36, 16384\n\t"
        "s_add_u32 s22, s37, 46325760\n\t"
        "buffer_load_dwordx4 v122, s[12:15], s22 offen sc1 lds\n\t"
        "v_pk_mul_f32 v[4:5], v[4:5], v[42:43] op_sel_hi:[1,0]\n\t"
        "v_cvt_pk_bf16_f32 v34, v4, v5\n\t"
        "v_pk_mul_f32 v[6:7], v[6:7], v[42:43] op_sel_hi:[1,0]\n\t"
        "v_max_i32_dpp v108, v108, v108 quad_perm:[2,3,0,1] row_mask:0xf bank_mask:0xf\n\t"
        "v_min_u32_dpp v111, v111, v111 row_ror:4 row_mask:0xf bank_mask:0xf\n\t"
        "s_add_u32 m0, s36, 17408\n\t"
        "s_add_u32 s22, s37, 46326784\n\t"
        "buffer_load_dwordx4 v122, s[12:15], s22 offen sc1 lds\n\t"
        "v_cvt_pk_bf16_f32 v35, v6, v7\n\t"
        "v_pk_mul_f32 v[8:9], v[8:9], v[42:43] op_sel_hi:[1,0]\n\t"
        "v_cvt_pk_bf16_f32 v36, v8, v9\n\t"
        "v_max_i32_dpp v108, v108, v108 row_ror:4 row_mask:0xf bank_mask:0xf\n\t"
        "v_min_u32_dpp v111, v111, v111 row_ror:8 row_mask:0xf bank_mask:0xf\n\t"
        "v_pk_mul_f32 v[10:11], v[10:11], v[42:43] op_sel_hi:[1,0]\n\t"
        "v_cvt_pk_bf16_f32 v37, v10, v11\n\t"
        "v_mfma_f32_16x16x32_bf16 v[16:19], v[48:51], v[32:35], 0\n\t"
        "v_max_i32_dpp v108, v108, v108 row_ror:8 row_mask:0xf bank_mask:0xf\n\t"
        "v_mfma_f32_16x16x32_bf16 v[20:23], v[52:55], v[32:35], 0\n\t"
        "v_mfma_f32_16x16x32_bf16 v[24:27], v[56:59], v[32:35], 0\n\t"
        "v_mfma_f32_16x16x32_bf16 v[28:31], v[60:63], v[32:35], 0\n\t"
        "v_max_i32_dpp v108, v108, v108 row_bcast:15 row_mask:0xa bank_mask:0xf\n\t"
        "v_xor_b32_e32 v45, 0x400, v123\n\t"
        "v_add_u32_e32 v124, s56, v124\n\t"
        "v_mfma_f32_16x16x16_bf16 v[16:19], v[104:105], v[36:37], v[16:19]\n\t"
        "v_max_i32_dpp v108, v108, v108 row_bcast:31 row_mask:0xc bank_mask:0xf\n\t"
        "v_mfma_f32_16x16x16_bf16 v[20:23], v[106:107], v[36:37], v[20:23]\n\t"
        "v_mfma_f32_16x16x16_bf16 v[24:27], v[12:13], v[36:37], v[24:27]\n\t"
        "v_mfma_f32_16x16x16_bf16 v[28:31], v[14:15], v[36:37], v[28:31]\n\t"
        "ds_read_b128 v[48:51], v117 offset:4096\n\t"
        "ds_read_b128 v[52:55], v117 offset:5120\n\t"
        "ds_read_b128 v[56:59], v117 offset:6144\n\t"
        "ds_read_b128 v[60:63], v117 offset:7168\n\t"
        "s_sub_u32 s56, 0, s56\n\t"
        "v_cvt_pk_bf16_f32 v0, v16, v17\n\t"
        "v_cvt_pk_bf16_f32 v1, v18, v19\n\t"
        "v_cvt_pk_bf16_f32 v2, v20, v21\n\t"
        "v_cvt_pk_bf16_f32 v3, v22, v23\n\t"
        "buffer_store_dwordx4 v[0:3], v123, s[12:15], s34 offen sc1\n\t"
        "v_cvt_pk_bf16_f32 v4, v24, v25\n\t"
        "v_cvt_pk_bf16_f32 v5, v26, v27\n\t"
        "v_cvt_pk_bf16_f32 v6, v28, v29\n\t"
        "v_cvt_pk_bf16_f32 v7, v30, v31\n\t"
        "buffer_store_dwordx4 v[4:7], v45, s[12:15], s34 offen sc1\n\t"
        "s_cmp_eq_u32 s24, 63\n\t"
        "s_cbranch_scc1 LA_last%=\n\t"
        "ds_read_b128 v[32:35], v117 offset:0\n\t"
        "ds_read_b128 v[36:39], v117 offset:1024\n\t"
        "ds_read_b128 v[40:43], v117 offset:2048\n\t"
        "ds_read_b128 v[44:47], v117 offset:3072\n\t"
        "v_mov_b64_e32 v[14:15], 0\n\t"
        "v_mov_b64_e32 v[12:13], 0\n\t"
        "v_mov_b64_e32 v[10:11], 0\n\t"
        "v_mov_b64_e32 v[8:9], 0\n\t"
        "v_mov_b64_e32 v[6:7], 0\n\t"
        "v_mov_b64_e32 v[4:5], 0\n\t"
        "v_mov_b64_e32 v[2:3], 0\n\t"
        "v_mov_b64_e32 v[0:1], 0\n\t"
        "v_readfirstlane_b32 s34, v111\n\t"
        "s_nop 0\n\t"
        "s_max_u32 s54, s54, s34\n\t"
        "v_readfirstlane_b32 s35, v109\n\t"
        "v_readlane_b32 s36, v108, 63\n\t"
        "s_bfe_u32 s29, s35, 0xc0008\n\t"
        "s_lshr_b32 s30, s35, 20\n\t"
        "s_add_u32 s35, s31, s50\n\t"
        "s_mul_i32 s37, s50, 3\n\t"
        "s_min_u32 s35, s35, s37\n\t"
        "s_add_i32 s34, s36, s51\n\t"
        "s_cmp_gt_i32 s34, 0\n\t"
        "s_cselect_b32 s31, s35, 0\n\t"
        "s_cselect_b32 s34, s34, 0\n\t"
        "s_add_u32 s34, s34, s31\n\t"
        "s_add_u32 s37, s25, s34\n\t"
        "s_add_u32 s45, s37, s29\n\t"
        "s_add_u32 s25, s45, s30\n\t"
        "s_add_u32 s24, s24, 1\n\t"
        "s_mov_b32 s28, s26\n\t"
        "s_mov_b32 s26, s27\n\t"
        "s_add_u32 s27, s27, 0x80000\n\t"
        "s_and_b32 s27, s27, 0x1fffff\n\t"
        "s_branch LA_loop%=\n\t"
        "LA_last%=:\n\t"
        "s_waitcnt vmcnt(0)\n\t"
        "s_barrier\n\t"
        "buffer_store_dword v126, v127, s[12:15], 0 offen sc1\n\t"
        "s_endpgm\n\t"
        "LA_skip%=:\n\t"
        "buffer_store_dword v126, v127, s[12:15], 0 offen sc1\n\t"
        "ds_read_b64_tr_b16 v[32:33], v119 offset:2112\n\t"
        "ds_read_b64_tr_b16 v[34:35], v119 offset:2144\n\t"
        "ds_read_b64_tr_b16 v[36:37], v119 offset:2368\n\t"
        "ds_read_b64_tr_b16 v[38:39], v119 offset:2400\n\t"
        "ds_read_b64_tr_b16 v[40:41], v119 offset:2624\n\t"
        "ds_read_b64_tr_b16 v[42:43], v119 offset:2656\n\t"
        "ds_read_b64_tr_b16 v[44:45], v119 offset:2880\n\t"
        "ds_read_b64_tr_b16 v[46:47], v119 offset:2912\n\t"
        "ds_read_b128 v[48:51], v108 offset:16384\n\t"
        "ds_read_b128 v[52:55], v108 offset:17408\n\t"
        "s_waitcnt lgkmcnt(0)\n\t"
        "s_branch LA_x%=\n\t"
        "LA_stre%=:\n\t"
        "s_mov_b32 s42, 0\n\t"
        "LA_str2%=:\n\t"
        "buffer_load_dwordx2 v[108:109], v125, s[8:11], s26 offen sc1\n\t"
        "s_waitcnt vmcnt(0)\n\t"
        "v_and_b32_e32 v45, 0xff, v109\n\t"
        "v_cmp_ne_u32_e32 vcc, s24, v45\n\t"
        "s_nop 0\n\t"
        "s_cbranch_vccz LA_stok%=\n\t"
        "s_add_u32 s42, s42, 1\n\t"
        "s_cmp_lt_u32 s42, 0x100000\n\t"
        "s_cbranch_scc1 LA_str2%=\n\t"
        "s_movk_i32 s34, 5\n\t"
        "s_branch LA_die%=\n\t"
        "LA_v2fail%=:\n\t"
        "s_mov_b32 s42, 0\n\t"
        "LA_v2re%=:\n\t"
        "v_xor_b32_e32 v45, 0x200, v120\n\t"
        "buffer_load_dwordx4 v[12:15], v45, s[8:11], s26 offen sc1\n\t"
        "buffer_load_dwordx4 v[104:107], v120, s[8:11], s26 offen sc1\n\t"
        "s_waitcnt vmcnt(0)\n\t"
        "v_max3_u32 v45, v12, v13, v14\n\t"
        "v_max3_u32 v45, v45, v15, v104\n\t"
        "v_max3_u32 v45, v45, v105, v106\n\t"
        "v_max_u32_e32 v45, v45, v107\n\t"
        "v_cmp_eq_u32_e32 vcc, -1, v45\n\t"
        "s_nop 0\n\t"
        "s_cbranch_vccz LA_v2ok%=\n\t"
        "s_add_u32 s42, s42, 1\n\t"
        "s_cmp_lt_u32 s42, 0x100000\n\t"
        "s_cbranch_scc1 LA_v2re%=\n\t"
        "s_movk_i32 s34, 6\n\t"
        "s_branch LA_die%=\n\t"
        "LA_rdfail%=:\n\t"
        "s_or_b32 s47, s47, 1\n\t"
        "buffer_load_dwordx4 v[16:19], v112, s[8:11], s26 offen offset:0 sc1\n\t"
        "buffer_load_dwordx4 v[20:23], v112, s[8:11], s26 offen offset:1024 sc1\n\t"
        "buffer_load_dwordx4 v[24:27], v112, s[8:11], s26 offen offset:2048 sc1\n\t"
        "buffer_load_dwordx4 v[28:31], v112, s[8:11], s26 offen offset:3072 sc1\n\t"
        "s_nop 7\n\t"
        "v_mov_b64_e32 v[0:1], 0\n\t"
        "v_mov_b64_e32 v[2:3], 0\n\t"
        "v_mov_b64_e32 v[4:5], 0\n\t"
        "v_mov_b64_e32 v[6:7], 0\n\t"
        "v_mov_b64_e32 v[8:9], 0\n\t"
        "v_mov_b64_e32 v[10:11], 0\n\t"
        "v_mov_b64_e32 v[12:13], 0\n\t"
        "v_mov_b64_e32 v[14:15], 0\n\t"
        "s_branch LA_rdre%=w\n\t"
        "LA_rdre%=:\n\t"
        "buffer_load_dwordx4 v[16:19], v112, s[8:11], s26 offen offset:0 sc1\n\t"
        "buffer_load_dwordx4 v[20:23], v112, s[8:11], s26 offen offset:1024 sc1\n\t"
        "buffer_load_dwordx4 v[24:27], v112, s[8:11], s26 offen offset:2048 sc1\n\t"
        "buffer_load_dwordx4 v[28:31], v112, s[8:11], s26 offen offset:3072 sc1\n\t"
        "LA_rdre%=w:\n\t"
        "s_waitcnt vmcnt(0) lgkmcnt(0)\n\t"
        "v_max3_u32 v111, v16, v17, v18\n\t"
        "v_max3_u32 v111, v111, v19, v20\n\t"
        "v_max3_u32 v111, v111, v21, v22\n\t"
        "v_max3_u32 v111, v111, v23, v24\n\t"
        "v_max3_u32 v111, v111, v25, v26\n\t"
        "v_max3_u32 v111, v111, v27, v28\n\t"
        "v_max3_u32 v111, v111, v29, v30\n\t"
        "v_max_u32_e32 v111, v111, v31\n\t"
        "v_cmp_eq_u32_e32 vcc, -1, v111\n\t"
        "s_nop 0\n\t"
        "s_cbranch_vccz LA_go%=\n\t"
        "s_add_u32 s42, s42, 1\n\t"
        "s_cmp_lt_u32 s42, 0x100000\n\t"
        "s_cbranch_scc1 LA_rdre%=\n\t"
        "s_movk_i32 s34, 1\n\t"
        "s_branch LA_die%=\n\t"
        "LA_vmfail%=:\n\t"
        "s_or_b32 s47, s47, 2\n\t"
        "s_mov_b32 s42, 0\n\t"
        "LA_vmre%=:\n\t"
        "buffer_load_dwordx4 v[104:107], v120, s[8:11], s26 offen sc1\n\t"
        "v_mfma_f32_16x16x32_bf16 v[4:7], v[48:51], v[12:15], 0\n\t"
        "v_mfma_f32_16x16x32_bf16 v[8:11], v[52:55], v[12:15], 0\n\t"
        "s_nop 7\n\t"
        "s_waitcnt vmcnt(0)\n\t"
        "v_mfma_f32_16x16x16_bf16 v[4:7], v[104:105], v[56:57], v[4:7]\n\t"
        "v_mfma_f32_16x16x16_bf16 v[8:11], v[106:107], v[56:57], v[8:11]\n\t"
        "v_max3_u32 v111, v104, v105, v106\n\t"
        "v_max_u32_e32 v111, v111, v107\n\t"
        "v_cmp_eq_u32_e32 vcc, -1, v111\n\t"
        "s_nop 7\n\t"
        "s_cbranch_vccz LA_vmok%=\n\t"
        "s_add_u32 s42, s42, 1\n\t"
        "s_cmp_lt_u32 s42, 0x100000\n\t"
        "s_cbranch_scc1 LA_vmre%=\n\t"
        "s_movk_i32 s34, 2\n\t"
        "s_branch LA_die%=\n\t"
        "LA_die%=:\n\t"
        "v_mov_b32_e32 v30, s34\n\t"
        "s_mov_b32 s35, 33024\n\t"
        "s_mov_b64 exec, 1\n\t"
        "buffer_store_dword v30, off, s[12:15], s35 sc1\n\t"
        "s_endpgm\n\t"
        ".Lk_end_A:\n\t"
        :: REC_S_IN, "{v96}"(g_w), "{v97}"(wb0), "{v98}"(wb1), "{v99}"(rb0), "{v100}"(rb0 ^ 32u), "{v101}"(rb0 ^ 64u), "{v102}"(rb0 ^ 96u),
           "{v112}"(a_mem), "{v113}"(l_ms + w * 4096), "{v114}"(l_ms + ((w + 1) & 3) * 4096), "{v115}"(l_ms + ((w + 2) & 3) * 4096), "{v116}"(l_ms + ((w + 3) & 3) * 4096),
           "{v117}"(lb + w * 22528 + l * 16), "{v118}"(l_iw), "{v119}"(l_iq), "{v120}"(a_vm), "{v121}"(a_o), "{v122}"(h * 8192 + l * 16), "{v123}"(a_ox), "{v124}"(lb + L_KV + l * 16), "{s55}"(lb + L_KV + w * 2048),
           "{v125}"((24 + cp) * 16384 + l * 32), "{v126}"(16 * (w & 1) + n - 4 * g), "{v103}"((l & 1) ? 0xEEEEEEEEu : 0x44444444u),
           "{v127}"((l == 0 && w == 0) ? F_PROG + h * 128 : OOB)
        : "memory", "vcc", "scc", "v0", "v255", "a0", "a255", "s101");
    __builtin_unreachable();
}
__device__ __forceinline__ void rec_b(const Params& p, unsigned lb, const int wv, const int id, const int rg, const int cp) {
    const int w = wv, l = otid() & 63, n = l & 15, g = l >> 4, cs = id, rb = 32 + id * 2 + rg;
    const v4u sh = mk_srd(p.ws + ho_base(p.ws), HO_BYTES - 8192), sw = mk_srd(p.ws, W_TOTAL), s1 = mk_srd(p.Wv, 1024 * 4096 * 2), s2 = mk_srd(p.Wo, 1024 * 4096 * 2);
    const int r8 = l >> 3, pc = l & 7, pp = n >> 3, bb = (n >> 2) & 1, f = (g & 1) | (pp << 1);
    const unsigned stg = lb + LB_STG + w * LB_STGP;
    const unsigned g_v = r8 * 8192 + cs * 256 + rg * 128 + pc * 16, g_o = (l >> 2) * 2048 + cs * 64 + (l & 3) * 16;
    const unsigned wb0 = stg + r8 * 128 + (((pc >> 1) ^ (r8 >> 2)) << 5) + (pc & 1) * 16, wb1 = wb0 ^ 64u;
    const unsigned rb0 = stg + (16 * (g >> 1) + 8 * pp + 4 * (g & 1) + bb) * 128 + (f << 5) + (n & 3) * 8;
    const unsigned o_r = stg + (g >> 1) * (2048 + 128) + (g & 1) * (1024 + 32) + (bb + 4 * pp) * 64 + (n & 3) * 8;
    const unsigned a_mem = cp * 32768 + (w >> 1) * 16384 + (w & 1) * 4096 + l * 16;
    const unsigned a_vmw = w < 2 ? (16 + (cs >> 2)) * 16384 + (cs & 3) * 2048 + (2 * rg + w) * 512 + (l & 31) * 16 + (g >> 1) * 8 : OOB;
    const unsigned a_or = (8 + rg * 4 + w) * 16384 + l * 16;
    const unsigned a_mw = w * 32768 + (cs >> 4) * 16384 + ((cs >> 1) & 7) * 1024 + (cs & 1) * 512 + (l >> 5) * 256 + rg * 128 + (l & 31) * 4;
    const unsigned a_sw = (w == 0 && l < 4) ? (24 + l) * 16384 + (id * 2 + rg) * 32 : OOB;
    const bf16_t* m0p = p.mem0 + (4 * rg + ((l >> 3) & 3)) * 1024 + cs * 32 + (l >> 5) * 16 + (l & 7) * 2;
    asm volatile(
        "s_nop 4\n\t"
        "s_memtime s[70:71]\n\t"
        "s_memrealtime s[72:73]\n\t"
        "global_load_dword v109, v[108:109], off\n\t"
        "s_lshl_b32 s34, s43, 21\n\t"
        "s_add_u32 s34, s34, 0\n\t"
        "buffer_load_dwordx4 v[128:131], v96, s[16:19], s34 offen nt\n\t"
        "s_add_u32 s34, s34, 0x10000\n\t"
        "buffer_load_dwordx4 v[132:135], v96, s[16:19], s34 offen nt\n\t"
        "s_add_u32 s34, s34, 0x10000\n\t"
        "buffer_load_dwordx4 v[136:139], v96, s[16:19], s34 offen nt\n\t"
        "s_add_u32 s34, s34, 0x10000\n\t"
        "buffer_load_dwordx4 v[140:143], v96, s[16:19], s34 offen nt\n\t"
        "s_add_u32 s34, s34, 0x10000\n\t"
        "buffer_load_dwordx4 v[144:147], v96, s[16:19], s34 offen nt\n\t"
        "s_add_u32 s34, s34, 0x10000\n\t"
        "buffer_load_dwordx4 v[148:151], v96, s[16:19], s34 offen nt\n\t"
        "s_add_u32 s34, s34, 0x10000\n\t"
        "buffer_load_dwordx4 v[152:155], v96, s[16:19], s34 offen nt\n\t"
        "s_add_u32 s34, s34, 0x10000\n\t"
        "buffer_load_dwordx4 v[156:159], v96, s[16:19], s34 offen nt\n\t"
        "s_lshl_b32 s34, s43, 21\n\t"
        "s_add_u32 s34, s34, 524288\n\t"
        "buffer_load_dwordx4 v[160:163], v96, s[16:19], s34 offen nt\n\t"
        "s_add_u32 s34, s34, 0x10000\n\t"
        "buffer_load_dwordx4 v[164:167], v96, s[16:19], s34 offen nt\n\t"
        "s_add_u32 s34, s34, 0x10000\n\t"
        "buffer_load_dwordx4 v[168:171], v96, s[16:19], s34 offen nt\n\t"
        "s_add_u32 s34, s34, 0x10000\n\t"
        "buffer_load_dwordx4 v[172:175], v96, s[16:19], s34 offen nt\n\t"
        "s_add_u32 s34, s34, 0x10000\n\t"
        "buffer_load_dwordx4 v[176:179], v96, s[16:19], s34 offen nt\n\t"
        "s_add_u32 s34, s34, 0x10000\n\t"
        "buffer_load_dwordx4 v[180:183], v96, s[16:19], s34 offen nt\n\t"
        "s_add_u32 s34, s34, 0x10000\n\t"
        "buffer_load_dwordx4 v[184:187], v96, s[16:19], s34 offen nt\n\t"
        "s_add_u32 s34, s34, 0x10000\n\t"
        "buffer_load_dwordx4 v[188:191], v96, s[16:19], s34 offen nt\n\t"
        "s_lshl_b32 s34, s43, 21\n\t"
        "s_add_u32 s34, s34, 1048576\n\t"
        "buffer_load_dwordx4 v[192:195], v96, s[16:19], s34 offen nt\n\t"
        "s_add_u32 s34, s34, 0x10000\n\t"
        "buffer_load_dwordx4 v[196:199], v96, s[16:19], s34 offen nt\n\t"
        "s_add_u32 s34, s34, 0x10000\n\t"
        "buffer_load_dwordx4 v[200:203], v96, s[16:19], s34 offen nt\n\t"
        "s_add_u32 s34, s34, 0x10000\n\t"
        "buffer_load_dwordx4 v[204:207], v96, s[16:19], s34 offen nt\n\t"
        "s_add_u32 s34, s34, 0x10000\n\t"
        "buffer_load_dwordx4 v[208:211], v96, s[16:19], s34 offen nt\n\t"
        "s_add_u32 s34, s34, 0x10000\n\t"
        "buffer_load_dwordx4 v[212:215], v96, s[16:19], s34 offen nt\n\t"
        "s_add_u32 s34, s34, 0x10000\n\t"
        "buffer_load_dwordx4 v[216:219], v96, s[16:19], s34 offen nt\n\t"
        "s_add_u32 s34, s34, 0x10000\n\t"
        "buffer_load_dwordx4 v[220:223], v96, s[16:19], s34 offen nt\n\t"
        "s_waitcnt vmcnt(16)\n\t"
        "ds_write_b128 v98, v[128:131] offset:0\n\t"
        "ds_write_b128 v99, v[132:135] offset:1024\n\t"
        "ds_write_b128 v98, v[136:139] offset:2048\n\t"
        "ds_write_b128 v99, v[140:143] offset:3072\n\t"
        "ds_write_b128 v98, v[144:147] offset:4096\n\t"
        "ds_write_b128 v99, v[148:151] offset:5120\n\t"
        "ds_write_b128 v98, v[152:155] offset:6144\n\t"
        "ds_write_b128 v99, v[156:159] offset:7168\n\t"
        "ds_read_b64_tr_b16 v[128:129], v100 offset:0\n\t"
        "ds_read_b64_tr_b16 v[130:131], v100 offset:256\n\t"
        "ds_read_b64_tr_b16 v[132:133], v100 offset:4096\n\t"
        "ds_read_b64_tr_b16 v[134:135], v100 offset:4352\n\t"
        "s_waitcnt lgkmcnt(8)\n\t"
        "ds_read_b64_tr_b16 v[136:137], v101 offset:0\n\t"
        "ds_read_b64_tr_b16 v[138:139], v101 offset:256\n\t"
        "ds_read_b64_tr_b16 v[140:141], v101 offset:4096\n\t"
        "ds_read_b64_tr_b16 v[142:143], v101 offset:4352\n\t"
        "s_waitcnt lgkmcnt(8)\n\t"
        "ds_read_b64_tr_b16 v[144:145], v102 offset:0\n\t"
        "ds_read_b64_tr_b16 v[146:147], v102 offset:256\n\t"
        "ds_read_b64_tr_b16 v[148:149], v102 offset:4096\n\t"
        "ds_read_b64_tr_b16 v[150:151], v102 offset:4352\n\t"
        "s_waitcnt lgkmcnt(8)\n\t"
        "ds_read_b64_tr_b16 v[152:153], v103 offset:0\n\t"
        "ds_read_b64_tr_b16 v[154:155], v103 offset:256\n\t"
        "ds_read_b64_tr_b16 v[156:157], v103 offset:4096\n\t"
        "ds_read_b64_tr_b16 v[158:159], v103 offset:4352\n\t"
        "s_lshl_b32 s34, s43, 21\n\t"
        "s_add_u32 s34, s34, 1572864\n\t"
        "buffer_load_dwordx4 v[224:227], v96, s[16:19], s34 offen nt\n\t"
        "s_add_u32 s34, s34, 0x10000\n\t"
        "buffer_load_dwordx4 v[228:231], v96, s[16:19], s34 offen nt\n\t"
        "s_add_u32 s34, s34, 0x10000\n\t"
        "buffer_load_dwordx4 v[232:235], v96, s[16:19], s34 offen nt\n\t"
        "s_add_u32 s34, s34, 0x10000\n\t"
        "buffer_load_dwordx4 v[236:239], v96, s[16:19], s34 offen nt\n\t"
        "s_add_u32 s34, s34, 0x10000\n\t"
        "buffer_load_dwordx4 v[240:243], v96, s[16:19], s34 offen nt\n\t"
        "s_add_u32 s34, s34, 0x10000\n\t"
        "buffer_load_dwordx4 v[244:247], v96, s[16:19], s34 offen nt\n\t"
        "s_add_u32 s34, s34, 0x10000\n\t"
        "buffer_load_dwordx4 v[248:251], v96, s[16:19], s34 offen nt\n\t"
        "s_add_u32 s34, s34, 0x10000\n\t"
        "buffer_load_dwordx4 v[252:255], v96, s[16:19], s34 offen nt\n\t"
        "s_waitcnt lgkmcnt(0)\n\t"
        "s_waitcnt vmcnt(16)\n\t"
        "ds_write_b128 v98, v[160:163] offset:0\n\t"
        "ds_write_b128 v99, v[164:167] offset:1024\n\t"
        "ds_write_b128 v98, v[168:171] offset:2048\n\t"
        "ds_write_b128 v99, v[172:175] offset:3072\n\t"
        "ds_write_b128 v98, v[176:179] offset:4096\n\t"
        "ds_write_b128 v99, v[180:183] offset:5120\n\t"
        "ds_write_b128 v98, v[184:187] offset:6144\n\t"
        "ds_write_b128 v99, v[188:191] offset:7168\n\t"
        "ds_read_b64_tr_b16 v[160:161], v100 offset:0\n\t"
        "ds_read_b64_tr_b16 v[162:163], v100 offset:256\n\t"
        "ds_read_b64_tr_b16 v[164:165], v100 offset:4096\n\t"
        "ds_read_b64_tr_b16 v[166:167], v100 offset:4352\n\t"
        "s_waitcnt lgkmcnt(8)\n\t"
        "ds_read_b64_tr_b16 v[168:169], v101 offset:0\n\t"
        "ds_read_b64_tr_b16 v[170:171], v101 offset:256\n\t"
        "ds_read_b64_tr_b16 v[172:173], v101 offset:4096\n\t"
        "ds_read_b64_tr_b16 v[174:175], v101 offset:4352\n\t"
        "s_waitcnt lgkmcnt(8)\n\t"
        "ds_read_b64_tr_b16 v[176:177], v102 offset:0\n\t"
        "ds_read_b64_tr_b16 v[178:179], v102 offset:256\n\t"
        "ds_read_b64_tr_b16 v[180:181], v102 offset:4096\n\t"
        "ds_read_b64_tr_b16 v[182:183], v102 offset:4352\n\t"
        "s_waitcnt lgkmcnt(8)\n\t"
        "ds_read_b64_tr_b16 v[184:185], v103 offset:0\n\t"
        "ds_read_b64_tr_b16 v[186:187], v103 offset:256\n\t"
        "ds_read_b64_tr_b16 v[188:189], v103 offset:4096\n\t"
        "ds_read_b64_tr_b16 v[190:191], v103 offset:4352\n\t"
        "s_lshl_b32 s34, s43, 21\n\t"
        "s_add_u32 s34, s34, 0\n\t"
        "buffer_load_dwordx4 a[0:3], v97, s[20:23], s34 offen\n\t"
        "s_add_u32 s34, s34, 0x8000\n\t"
        "buffer_load_dwordx4 a[4:7], v97, s[20:23], s34 offen\n\t"
        "s_add_u32 s34, s34, 0x8000\n\t"
        "buffer_load_dwordx4 a[8:11], v97, s[20:23], s34 offen\n\t"
        "s_add_u32 s34, s34, 0x8000\n\t"
        "buffer_load_dwordx4 a[12:15], v97, s[20:23], s34 offen\n\t"
        "s_add_u32 s34, s34, 0x8000\n\t"
        "buffer_load_dwordx4 a[16:19], v97, s[20:23], s34 offen\n\t"
        "s_add_u32 s34, s34, 0x8000\n\t"
        "buffer_load_dwordx4 a[20:23], v97, s[20:23], s34 offen\n\t"
        "s_add_u32 s34, s34, 0x8000\n\t"
        "buffer_load_dwordx4 a[24:27], v97, s[20:23], s34 offen\n\t"
        "s_add_u32 s34, s34, 0x8000\n\t"
        "buffer_load_dwordx4 a[28:31], v97, s[20:23], s34 offen\n\t"
        "s_waitcnt lgkmcnt(0)\n\t"
        "s_waitcnt vmcnt(16)\n\t"
        "ds_write_b128 v98, v[192:195] offset:0\n\t"
        "ds_write_b128 v99, v[196:199] offset:1024\n\t"
        "ds_write_b128 v98, v[200:203] offset:2048\n\t"
        "ds_write_b128 v99, v[204:207] offset:3072\n\t"
        "ds_write_b128 v98, v[208:211] offset:4096\n\t"
        "ds_write_b128 v99, v[212:215] offset:5120\n\t"
        "ds_write_b128 v98, v[216:219] offset:6144\n\t"
        "ds_write_b128 v99, v[220:223] offset:7168\n\t"
        "ds_read_b64_tr_b16 v[192:193], v100 offset:0\n\t"
        "ds_read_b64_tr_b16 v[194:195], v100 offset:256\n\t"
        "ds_read_b64_tr_b16 v[196:197], v100 offset:4096\n\t"
        "ds_read_b64_tr_b16 v[198:199], v100 offset:4352\n\t"
        "s_waitcnt lgkmcnt(8)\n\t"
        "ds_read_b64_tr_b16 v[200:201], v101 offset:0\n\t"
        "ds_read_b64_tr_b16 v[202:203], v101 offset:256\n\t"
        "ds_read_b64_tr_b16 v[204:205], v101 offset:4096\n\t"
        "ds_read_b64_tr_b16 v[206:207], v101 offset:4352\n\t"
        "s_waitcnt lgkmcnt(8)\n\t"
        "ds_read_b64_tr_b16 v[208:209], v102 offset:0\n\t"
        "ds_read_b64_tr_b16 v[210:211], v102 offset:256\n\t"
        "ds_read_b64_tr_b16 v[212:213], v102 offset:4096\n\t"
        "ds_read_b64_tr_b16 v[214:215], v102 offset:4352\n\t"
        "s_waitcnt lgkmcnt(8)\n\t"
        "ds_read_b64_tr_b16 v[216:217], v103 offset:0\n\t"
        "ds_read_b64_tr_b16 v[218:219], v103 offset:256\n\t"
        "ds_read_b64_tr_b16 v[220:221], v103 offset:4096\n\t"
        "ds_read_b64_tr_b16 v[222:223], v103 offset:4352\n\t"
        "s_lshl_b32 s34, s43, 21\n\t"
        "s_add_u32 s34, s34, 262144\n\t"
        "buffer_load_dwordx4 a[32:35], v97, s[20:23], s34 offen\n\t"
        "s_add_u32 s34, s34, 0x8000\n\t"
        "buffer_load_dwordx4 a[36:39], v97, s[20:23], s34 offen\n\t"
        "s_add_u32 s34, s34, 0x8000\n\t"
        "buffer_load_dwordx4 a[40:43], v97, s[20:23], s34 offen\n\t"
        "s_add_u32 s34, s34, 0x8000\n\t"
        "buffer_load_dwordx4 a[44:47], v97, s[20:23], s34 offen\n\t"
        "s_add_u32 s34, s34, 0x8000\n\t"
        "buffer_load_dwordx4 a[48:51], v97, s[20:23], s34 offen\n\t"
        "s_add_u32 s34, s34, 0x8000\n\t"
        "buffer_load_dwordx4 a[52:55], v97, s[20:23], s34 offen\n\t"
        "s_add_u32 s34, s34, 0x8000\n\t"
        "buffer_load_dwordx4 a[56:59], v97, s[20:23], s34 offen\n\t"
        "s_add_u32 s34, s34, 0x8000\n\t"
        "buffer_load_dwordx4 a[60:63], v97, s[20:23], s34 offen\n\t"
        "s_waitcnt lgkmcnt(0)\n\t"
        "s_waitcnt vmcnt(16)\n\t"
        "ds_write_b128 v98, v[224:227] offset:0\n\t"
        "ds_write_b128 v99, v[228:231] offset:1024\n\t"
        "ds_write_b128 v98, v[232:235] offset:2048\n\t"
        "ds_write_b128 v99, v[236:239] offset:3072\n\t"
        "ds_write_b128 v98, v[240:243] offset:4096\n\t"
        "ds_write_b128 v99, v[244:247] offset:5120\n\t"
        "ds_write_b128 v98, v[248:251] offset:6144\n\t"
        "ds_write_b128 v99, v[252:255] offset:7168\n\t"
        "ds_read_b64_tr_b16 v[224:225], v100 offset:0\n\t"
        "ds_read_b64_tr_b16 v[226:227], v100 offset:256\n\t"
        "ds_read_b64_tr_b16 v[228:229], v100 offset:4096\n\t"
        "ds_read_b64_tr_b16 v[230:231], v100 offset:4352\n\t"
        "s_waitcnt lgkmcnt(8)\n\t"
        "ds_read_b64_tr_b16 v[232:233], v101 offset:0\n\t"
        "ds_read_b64_tr_b16 v[234:235], v101 offset:256\n\t"
        "ds_read_b64_tr_b16 v[236:237], v101 offset:4096\n\t"
        "ds_read_b64_tr_b16 v[238:239], v101 offset:4352\n\t"
        "s_waitcnt lgkmcnt(8)\n\t"
        "ds_read_b64_tr_b16 v[240:241], v102 offset:0\n\t"
        "ds_read_b64_tr_b16 v[242:243], v102 offset:256\n\t"
        "ds_read_b64_tr_b16 v[244:245], v102 offset:4096\n\t"
        "ds_read_b64_tr_b16 v[246:247], v102 offset:4352\n\t"
        "s_waitcnt lgkmcnt(8)\n\t"
        "ds_read_b64_tr_b16 v[248:249], v103 offset:0\n\t"
        "ds_read_b64_tr_b16 v[250:251], v103 offset:256\n\t"
        "ds_read_b64_tr_b16 v[252:253], v103 offset:4096\n\t"
        "ds_read_b64_tr_b16 v[254:255], v103 offset:4352\n\t"
        "s_lshl_b32 s34, s43, 21\n\t"
        "s_add_u32 s34, s34, 524288\n\t"
        "buffer_load_dwordx4 a[64:67], v97, s[20:23], s34 offen\n\t"
        "s_add_u32 s34, s34, 0x8000\n\t"
        "buffer_load_dwordx4 a[68:71], v97, s[20:23], s34 offen\n\t"
        "s_add_u32 s34, s34, 0x8000\n\t"
        "buffer_load_dwordx4 a[72:75], v97, s[20:23], s34 offen\n\t"
        "s_add_u32 s34, s34, 0x8000\n\t"
        "buffer_load_dwordx4 a[76:79], v97, s[20:23], s34 offen\n\t"
        "s_add_u32 s34, s34, 0x8000\n\t"
        "buffer_load_dwordx4 a[80:83], v97, s[20:23], s34 offen\n\t"
        "s_add_u32 s34, s34, 0x8000\n\t"
        "buffer_load_dwordx4 a[84:87], v97, s[20:23], s34 offen\n\t"
        "s_add_u32 s34, s34, 0x8000\n\t"
        "buffer_load_dwordx4 a[88:91], v97, s[20:23], s34 offen\n\t"
        "s_add_u32 s34, s34, 0x8000\n\t"
        "buffer_load_dwordx4 a[92:95], v97, s[20:23], s34 offen\n\t"
        "s_waitcnt lgkmcnt(0)\n\t"
        "s_waitcnt vmcnt(16)\n\t"
        "ds_write_b128 v122, a[0:3] offset:0\n\t"
        "ds_write_b128 v122, a[4:7] offset:1056\n\t"
        "ds_write_b128 v122, a[8:11] offset:2176\n\t"
        "ds_write_b128 v122, a[12:15] offset:3232\n\t"
        "ds_write_b128 v122, a[16:19] offset:4352\n\t"
        "ds_write_b128 v122, a[20:23] offset:5408\n\t"
        "ds_write_b128 v122, a[24:27] offset:6528\n\t"
        "ds_write_b128 v122, a[28:31] offset:7584\n\t"
        "ds_read_b64_tr_b16 a[0:1], v123 offset:0\n\t"
        "ds_read_b64_tr_b16 a[2:3], v123 offset:128\n\t"
        "ds_read_b64_tr_b16 a[4:5], v123 offset:4352\n\t"
        "ds_read_b64_tr_b16 a[6:7], v123 offset:4480\n\t"
        "s_waitcnt lgkmcnt(8)\n\t"
        "ds_read_b64_tr_b16 a[8:9], v123 offset:32\n\t"
        "ds_read_b64_tr_b16 a[10:11], v123 offset:160\n\t"
        "ds_read_b64_tr_b16 a[12:13], v123 offset:4384\n\t"
        "ds_read_b64_tr_b16 a[14:15], v123 offset:4512\n\t"
        "s_waitcnt lgkmcnt(8)\n\t"
        "ds_read_b64_tr_b16 a[16:17], v123 offset:512\n\t"
        "ds_read_b64_tr_b16 a[18:19], v123 offset:640\n\t"
        "ds_read_b64_tr_b16 a[20:21], v123 offset:4864\n\t"
        "ds_read_b64_tr_b16 a[22:23], v123 offset:4992\n\t"
        "s_waitcnt lgkmcnt(8)\n\t"
        "ds_read_b64_tr_b16 a[24:25], v123 offset:544\n\t"
        "ds_read_b64_tr_b16 a[26:27], v123 offset:672\n\t"
        "ds_read_b64_tr_b16 a[28:29], v123 offset:4896\n\t"
        "ds_read_b64_tr_b16 a[30:31], v123 offset:5024\n\t"
        "s_waitcnt lgkmcnt(8)\n\t"
        "s_lshl_b32 s34, s43, 21\n\t"
        "s_add_u32 s34, s34, 786432\n\t"
        "buffer_load_dwordx4 a[96:99], v97, s[20:23], s34 offen\n\t"
        "s_add_u32 s34, s34, 0x8000\n\t"
        "buffer_load_dwordx4 a[100:103], v97, s[20:23], s34 offen\n\t"
        "s_add_u32 s34, s34, 0x8000\n\t"
        "buffer_load_dwordx4 a[104:107], v97, s[20:23], s34 offen\n\t"
        "s_add_u32 s34, s34, 0x8000\n\t"
        "buffer_load_dwordx4 a[108:111], v97, s[20:23], s34 offen\n\t"
        "s_add_u32 s34, s34, 0x8000\n\t"
        "buffer_load_dwordx4 a[112:115], v97, s[20:23], s34 offen\n\t"
        "s_add_u32 s34, s34, 0x8000\n\t"
        "buffer_load_dwordx4 a[116:119], v97, s[20:23], s34 offen\n\t"
        "s_add_u32 s34, s34, 0x8000\n\t"
        "buffer_load_dwordx4 a[120:123], v97, s[20:23], s34 offen\n\t"
        "s_add_u32 s34, s34, 0x8000\n\t"
        "buffer_load_dwordx4 a[124:127], v97, s[20:23], s34 offen\n\t"
        "s_waitcnt lgkmcnt(0)\n\t"
        "s_waitcnt vmcnt(16)\n\t"
        "ds_write_b128 v122, a[32:35] offset:0\n\t"
        "ds_write_b128 v122, a[36:39] offset:1056\n\t"
        "ds_write_b128 v122, a[40:43] offset:2176\n\t"
        "ds_write_b128 v122, a[44:47] offset:3232\n\t"
        "ds_write_b128 v122, a[48:51] offset:4352\n\t"
        "ds_write_b128 v122, a[52:55] offset:5408\n\t"
        "ds_write_b128 v122, a[56:59] offset:6528\n\t"
        "ds_write_b128 v122, a[60:63] offset:7584\n\t"
        "ds_read_b64_tr_b16 a[32:33], v123 offset:0\n\t"
        "ds_read_b64_tr_b16 a[34:35], v123 offset:128\n\t"
        "ds_read_b64_tr_b16 a[36:37], v123 offset:4352\n\t"
        "ds_read_b64_tr_b16 a[38:39], v123 offset:4480\n\t"
        "s_waitcnt lgkmcnt(8)\n\t"
        "ds_read_b64_tr_b16 a[40:41], v123 offset:32\n\t"
        "ds_read_b64_tr_b16 a[42:43], v123 offset:160\n\t"
        "ds_read_b64_tr_b16 a[44:45], v123 offset:4384\n\t"
        "ds_read_b64_tr_b16 a[46:47], v123 offset:4512\n\t"
        "s_waitcnt lgkmcnt(8)\n\t"
        "ds_read_b64_tr_b16 a[48:49], v123 offset:512\n\t"
        "ds_read_b64_tr_b16 a[50:51], v123 offset:640\n\t"
        "ds_read_b64_tr_b16 a[52:53], v123 offset:4864\n\t"
        "ds_read_b64_tr_b16 a[54:55], v123 offset:4992\n\t"
        "s_waitcnt lgkmcnt(8)\n\t"
        "ds_read_b64_tr_b16 a[56:57], v123 offset:544\n\t"
        "ds_read_b64_tr_b16 a[58:59], v123 offset:672\n\t"
        "ds_read_b64_tr_b16 a[60:61], v123 offset:4896\n\t"
        "ds_read_b64_tr_b16 a[62:63], v123 offset:5024\n\t"
        "s_waitcnt lgkmcnt(8)\n\t"
        "s_lshl_b32 s34, s43, 21\n\t"
        "s_add_u32 s34, s34, 1048576\n\t"
        "buffer_load_dwordx4 a[128:131], v97, s[20:23], s34 offen\n\t"
        "s_add_u32 s34, s34, 0x8000\n\t"
        "buffer_load_dwordx4 a[132:135], v97, s[20:23], s34 offen\n\t"
        "s_add_u32 s34, s34, 0x8000\n\t"
        "buffer_load_dwordx4 a[136:139], v97, s[20:23], s34 offen\n\t"
        "s_add_u32 s34, s34, 0x8000\n\t"
        "buffer_load_dwordx4 a[140:143], v97, s[20:23], s34 offen\n\t"
        "s_add_u32 s34, s34, 0x8000\n\t"
        "buffer_load_dwordx4 a[144:147], v97, s[20:23], s34 offen\n\t"
        "s_add_u32 s34, s34, 0x8000\n\t"
        "buffer_load_dwordx4 a[148:151], v97, s[20:23], s34 offen\n\t"
        "s_add_u32 s34, s34, 0x8000\n\t"
        "buffer_load_dwordx4 a[152:155], v97, s[20:23], s34 offen\n\t"
        "s_add_u32 s34, s34, 0x8000\n\t"
        "buffer_load_dwordx4 a[156:159], v97, s[20:23], s34 offen\n\t"
        "s_waitcnt lgkmcnt(0)\n\t"
        "s_waitcnt vmcnt(16)\n\t"
        "ds_write_b128 v122, a[64:67] offset:0\n\t"
        "ds_write_b128 v122, a[68:71] offset:1056\n\t"
        "ds_write_b128 v122, a[72:75] offset:2176\n\t"
        "ds_write_b128 v122, a[76:79] offset:3232\n\t"
        "ds_write_b128 v122, a[80:83] offset:4352\n\t"
        "ds_write_b128 v122, a[84:87] offset:5408\n\t"
        "ds_write_b128 v122, a[88:91] offset:6528\n\t"
        "ds_write_b128 v122, a[92:95] offset:7584\n\t"
        "ds_read_b64_tr_b16 a[64:65], v123 offset:0\n\t"
        "ds_read_b64_tr_b16 a[66:67], v123 offset:128\n\t"
        "ds_read_b64_tr_b16 a[68:69], v123 offset:4352\n\t"
        "ds_read_b64_tr_b16 a[70:71], v123 offset:4480\n\t"
        "s_waitcnt lgkmcnt(8)\n\t"
        "ds_read_b64_tr_b16 a[72:73], v123 offset:32\n\t"
        "ds_read_b64_tr_b16 a[74:75], v123 offset:160\n\t"
        "ds_read_b64_tr_b16 a[76:77], v123 offset:4384\n\t"
        "ds_read_b64_tr_b16 a[78:79], v123 offset:4512\n\t"
        "s_waitcnt lgkmcnt(8)\n\t"
        "ds_read_b64_tr_b16 a[80:81], v123 offset:512\n\t"
        "ds_read_b64_tr_b16 a[82:83], v123 offset:640\n\t"
        "ds_read_b64_tr_b16 a[84:85], v123 offset:4864\n\t"
        "ds_read_b64_tr_b16 a[86:87], v123 offset:4992\n\t"
        "s_waitcnt lgkmcnt(8)\n\t"
        "ds_read_b64_tr_b16 a[88:89], v123 offset:544\n\t"
        "ds_read_b64_tr_b16 a[90:91], v123 offset:672\n\t"
        "ds_read_b64_tr_b16 a[92:93], v123 offset:4896\n\t"
        "ds_read_b64_tr_b16 a[94:95], v123 offset:5024\n\t"
        "s_waitcnt lgkmcnt(8)\n\t"
        "s_lshl_b32 s34, s43, 21\n\t"
        "s_add_u32 s34, s34, 1310720\n\t"
        "buffer_load_dwordx4 a[160:163], v97, s[20:23], s34 offen\n\t"
        "s_add_u32 s34, s34, 0x8000\n\t"
        "buffer_load_dwordx4 a[164:167], v97, s[20:23], s34 offen\n\t"
        "s_add_u32 s34, s34, 0x8000\n\t"
        "buffer_load_dwordx4 a[168:171], v97, s[20:23], s34 offen\n\t"
        "s_add_u32 s34, s34, 0x8000\n\t"
        "buffer_load_dwordx4 a[172:175], v97, s[20:23], s34 offen\n\t"
        "s_add_u32 s34, s34, 0x8000\n\t"
        "buffer_load_dwordx4 a[176:179], v97, s[20:23], s34 offen\n\t"
        "s_add_u32 s34, s34, 0x8000\n\t"
        "buffer_load_dwordx4 a[180:183], v97, s[20:23], s34 offen\n\t"
        "s_add_u32 s34, s34, 0x8000\n\t"
        "buffer_load_dwordx4 a[184:187], v97, s[20:23], s34 offen\n\t"
        "s_add_u32 s34, s34, 0x8000\n\t"
        "buffer_load_dwordx4 a[188:191], v97, s[20:23], s34 offen\n\t"
        "s_waitcnt lgkmcnt(0)\n\t"
        "s_waitcnt vmcnt(16)\n\t"
        "ds_write_b128 v122, a[96:99] offset:0\n\t"
        "ds_write_b128 v122, a[100:103] offset:1056\n\t"
        "ds_write_b128 v122, a[104:107] offset:2176\n\t"
        "ds_write_b128 v122, a[108:111] offset:3232\n\t"
        "ds_write_b128 v122, a[112:115] offset:4352\n\t"
        "ds_write_b128 v122, a[116:119] offset:5408\n\t"
        "ds_write_b128 v122, a[120:123] offset:6528\n\t"
        "ds_write_b128 v122, a[124:127] offset:7584\n\t"
        "ds_read_b64_tr_b16 a[96:97], v123 offset:0\n\t"
        "ds_read_b64_tr_b16 a[98:99], v123 offset:128\n\t"
        "ds_read_b64_tr_b16 a[100:101], v123 offset:4352\n\t"
        "ds_read_b64_tr_b16 a[102:103], v123 offset:4480\n\t"
        "s_waitcnt lgkmcnt(8)\n\t"
        "ds_read_b64_tr_b16 a[104:105], v123 offset:32\n\t"
        "ds_read_b64_tr_b16 a[106:107], v123 offset:160\n\t"
        "ds_read_b64_tr_b16 a[108:109], v123 offset:4384\n\t"
        "ds_read_b64_tr_b16 a[110:111], v123 offset:4512\n\t"
        "s_waitcnt lgkmcnt(8)\n\t"
        "ds_read_b64_tr_b16 a[112:113], v123 offset:512\n\t"
        "ds_read_b64_tr_b16 a[114:115], v123 offset:640\n\t"
        "ds_read_b64_tr_b16 a[116:117], v123 offset:4864\n\t"
        "ds_read_b64_tr_b16 a[118:119], v123 offset:4992\n\t"
        "s_waitcnt lgkmcnt(8)\n\t"
        "ds_read_b64_tr_b16 a[120:121], v123 offset:544\n\t"
        "ds_read_b64_tr_b16 a[122:123], v123 offset:672\n\t"
        "ds_read_b64_tr_b16 a[124:125], v123 offset:4896\n\t"
        "ds_read_b64_tr_b16 a[126:127], v123 offset:5024\n\t"
        "s_waitcnt lgkmcnt(8)\n\t"
        "s_lshl_b32 s34, s43, 21\n\t"
        "s_add_u32 s34, s34, 1572864\n\t"
        "buffer_load_dwordx4 a[192:195], v97, s[20:23], s34 offen\n\t"
        "s_add_u32 s34, s34, 0x8000\n\t"
        "buffer_load_dwordx4 a[196:199], v97, s[20:23], s34 offen\n\t"
        "s_add_u32 s34, s34, 0x8000\n\t"
        "buffer_load_dwordx4 a[200:203], v97, s[20:23], s34 offen\n\t"
        "s_add_u32 s34, s34, 0x8000\n\t"
        "buffer_load_dwordx4 a[204:207], v97, s[20:23], s34 offen\n\t"
        "s_add_u32 s34, s34, 0x8000\n\t"
        "buffer_load_dwordx4 a[208:211], v97, s[20:23], s34 offen\n\t"
        "s_add_u32 s34, s34, 0x8000\n\t"
        "buffer_load_dwordx4 a[212:215], v97, s[20:23], s34 offen\n\t"
        "s_add_u32 s34, s34, 0x8000\n\t"
        "buffer_load_dwordx4 a[216:219], v97, s[20:23], s34 offen\n\t"
        "s_add_u32 s34, s34, 0x8000\n\t"
        "buffer_load_dwordx4 a[220:223], v97, s[20:23], s34 offen\n\t"
        "s_waitcnt lgkmcnt(0)\n\t"
        "s_waitcnt vmcnt(16)\n\t"
        "ds_write_b128 v122, a[128:131] offset:0\n\t"
        "ds_write_b128 v122, a[132:135] offset:1056\n\t"
        "ds_write_b128 v122, a[136:139] offset:2176\n\t"
        "ds_write_b128 v122, a[140:143] offset:3232\n\t"
        "ds_write_b128 v122, a[144:147] offset:4352\n\t"
        "ds_write_b128 v122, a[148:151] offset:5408\n\t"
        "ds_write_b128 v122, a[152:155] offset:6528\n\t"
        "ds_write_b128 v122, a[156:159] offset:7584\n\t"
        "ds_read_b64_tr_b16 a[128:129], v123 offset:0\n\t"
        "ds_read_b64_tr_b16 a[130:131], v123 offset:128\n\t"
        "ds_read_b64_tr_b16 a[132:133], v123 offset:4352\n\t"
        "ds_read_b64_tr_b16 a[134:135], v123 offset:4480\n\t"
        "s_waitcnt lgkmcnt(8)\n\t"
        "ds_read_b64_tr_b16 a[136:137], v123 offset:32\n\t"
        "ds_read_b64_tr_b16 a[138:139], v123 offset:160\n\t"
        "ds_read_b64_tr_b16 a[140:141], v123 offset:4384\n\t"
        "ds_read_b64_tr_b16 a[142:143], v123 offset:4512\n\t"
        "s_waitcnt lgkmcnt(8)\n\t"
        "ds_read_b64_tr_b16 a[144:145], v123 offset:512\n\t"
        "ds_read_b64_tr_b16 a[146:147], v123 offset:640\n\t"
        "ds_read_b64_tr_b16 a[148:149], v123 offset:4864\n\t"
        "ds_read_b64_tr_b16 a[150:151], v123 offset:4992\n\t"
        "s_waitcnt lgkmcnt(8)\n\t"
        "ds_read_b64_tr_b16 a[152:153], v123 offset:544\n\t"
        "ds_read_b64_tr_b16 a[154:155], v123 offset:672\n\t"
        "ds_read_b64_tr_b16 a[156:157], v123 offset:4896\n\t"
        "ds_read_b64_tr_b16 a[158:159], v123 offset:5024\n\t"
        "s_waitcnt lgkmcnt(8)\n\t"
        "s_lshl_b32 s34, s43, 21\n\t"
        "s_add_u32 s34, s34, 1835008\n\t"
        "buffer_load_dwordx4 a[224:227], v97, s[20:23], s34 offen\n\t"
        "s_add_u32 s34, s34, 0x8000\n\t"
        "buffer_load_dwordx4 a[228:231], v97, s[20:23], s34 offen\n\t"
        "s_add_u32 s34, s34, 0x8000\n\t"
        "buffer_load_dwordx4 a[232:235], v97, s[20:23], s34 offen\n\t"
        "s_add_u32 s34, s34, 0x8000\n\t"
        "buffer_load_dwordx4 a[236:239], v97, s[20:23], s34 offen\n\t"
        "s_add_u32 s34, s34, 0x8000\n\t"
        "buffer_load_dwordx4 a[240:243], v97, s[20:23], s34 offen\n\t"
        "s_add_u32 s34, s34, 0x8000\n\t"
        "buffer_load_dwordx4 a[244:247], v97, s[20:23], s34 offen\n\t"
        "s_add_u32 s34, s34, 0x8000\n\t"
        "buffer_load_dwordx4 a[248:251], v97, s[20:23], s34 offen\n\t"
        "s_add_u32 s34, s34, 0x8000\n\t"
        "buffer_load_dwordx4 a[252:255], v97, s[20:23], s34 offen\n\t"
        "s_waitcnt lgkmcnt(0)\n\t"
        "s_waitcnt vmcnt(16)\n\t"
        "ds_write_b128 v122, a[160:163] offset:0\n\t"
        "ds_write_b128 v122, a[164:167] offset:1056\n\t"
        "ds_write_b128 v122, a[168:171] offset:2176\n\t"
        "ds_write_b128 v122, a[172:175] offset:3232\n\t"
        "ds_write_b128 v122, a[176:179] offset:4352\n\t"
        "ds_write_b128 v122, a[180:183] offset:5408\n\t"
        "ds_write_b128 v122, a[184:187] offset:6528\n\t"
        "ds_write_b128 v122, a[188:191] offset:7584\n\t"
        "ds_read_b64_tr_b16 a[160:161], v123 offset:0\n\t"
        "ds_read_b64_tr_b16 a[162:163], v123 offset:128\n\t"
        "ds_read_b64_tr_b16 a[164:165], v123 offset:4352\n\t"
        "ds_read_b64_tr_b16 a[166:167], v123 offset:4480\n\t"
        "s_waitcnt lgkmcnt(8)\n\t"
        "ds_read_b64_tr_b16 a[168:169], v123 offset:32\n\t"
        "ds_read_b64_tr_b16 a[170:171], v123 offset:160\n\t"
        "ds_read_b64_tr_b16 a[172:173], v123 offset:4384\n\t"
        "ds_read_b64_tr_b16 a[174:175], v123 offset:4512\n\t"
        "s_waitcnt lgkmcnt(8)\n\t"
        "ds_read_b64_tr_b16 a[176:177], v123 offset:512\n\t"
        "ds_read_b64_tr_b16 a[178:179], v123 offset:640\n\t"
        "ds_read_b64_tr_b16 a[180:181], v123 offset:4864\n\t"
        "ds_read_b64_tr_b16 a[182:183], v123 offset:4992\n\t"
        "s_waitcnt lgkmcnt(8)\n\t"
        "ds_read_b64_tr_b16 a[184:185], v123 offset:544\n\t"
        "ds_read_b64_tr_b16 a[186:187], v123 offset:672\n\t"
        "ds_read_b64_tr_b16 a[188:189], v123 offset:4896\n\t"
        "ds_read_b64_tr_b16 a[190:191], v123 offset:5024\n\t"
        "s_waitcnt lgkmcnt(8)\n\t"
        "s_waitcnt lgkmcnt(0)\n\t"
        "s_waitcnt vmcnt(8)\n\t"
        "ds_write_b128 v122, a[192:195] offset:0\n\t"
        "ds_write_b128 v122, a[196:199] offset:1056\n\t"
        "ds_write_b128 v122, a[200:203] offset:2176\n\t"
        "ds_write_b128 v122, a[204:207] offset:3232\n\t"
        "ds_write_b128 v122, a[208:211] offset:4352\n\t"
        "ds_write_b128 v122, a[212:215] offset:5408\n\t"
        "ds_write_b128 v122, a[216:219] offset:6528\n\t"
        "ds_write_b128 v122, a[220:223] offset:7584\n\t"
        "ds_read_b64_tr_b16 a[192:193], v123 offset:0\n\t"
        "ds_read_b64_tr_b16 a[194:195], v123 offset:128\n\t"
        "ds_read_b64_tr_b16 a[196:197], v123 offset:4352\n\t"
        "ds_read_b64_tr_b16 a[198:199], v123 offset:4480\n\t"
        "s_waitcnt lgkmcnt(8)\n\t"
        "ds_read_b64_tr_b16 a[200:201], v123 offset:32\n\t"
        "ds_read_b64_tr_b16 a[202:203], v123 offset:160\n\t"
        "ds_read_b64_tr_b16 a[204:205], v123 offset:4384\n\t"
        "ds_read_b64_tr_b16 a[206:207], v123 offset:4512\n\t"
        "s_waitcnt lgkmcnt(8)\n\t"
        "ds_read_b64_tr_b16 a[208:209], v123 offset:512\n\t"
        "ds_read_b64_tr_b16 a[210:211], v123 offset:640\n\t"
        "ds_read_b64_tr_b16 a[212:213], v123 offset:4864\n\t"
        "ds_read_b64_tr_b16 a[214:215], v123 offset:4992\n\t"
        "s_waitcnt lgkmcnt(8)\n\t"
        "ds_read_b64_tr_b16 a[216:217], v123 offset:544\n\t"
        "ds_read_b64_tr_b16 a[218:219], v123 offset:672\n\t"
        "ds_read_b64_tr_b16 a[220:221], v123 offset:4896\n\t"
        "ds_read_b64_tr_b16 a[222:223], v123 offset:5024\n\t"
        "s_waitcnt lgkmcnt(8)\n\t"
        "s_waitcnt lgkmcnt(0)\n\t"
        "s_waitcnt vmcnt(0)\n\t"
        "ds_write_b128 v122, a[224:227] offset:0\n\t"
        "ds_write_b128 v122, a[228:231] offset:1056\n\t"
        "ds_write_b128 v122, a[232:235] offset:2176\n\t"
        "ds_write_b128 v122, a[236:239] offset:3232\n\t"
        "ds_write_b128 v122, a[240:243] offset:4352\n\t"
        "ds_write_b128 v122, a[244:247] offset:5408\n\t"
        "ds_write_b128 v122, a[248:251] offset:6528\n\t"
        "ds_write_b128 v122, a[252:255] offset:7584\n\t"
        "ds_read_b64_tr_b16 a[224:225], v123 offset:0\n\t"
        "ds_read_b64_tr_b16 a[226:227], v123 offset:128\n\t"
        "ds_read_b64_tr_b16 a[228:229], v123 offset:4352\n\t"
        "ds_read_b64_tr_b16 a[230:231], v123 offset:4480\n\t"
        "s_waitcnt lgkmcnt(8)\n\t"
        "ds_read_b64_tr_b16 a[232:233], v123 offset:32\n\t"
        "ds_read_b64_tr_b16 a[234:235], v123 offset:160\n\t"
        "ds_read_b64_tr_b16 a[236:237], v123 offset:4384\n\t"
        "ds_read_b64_tr_b16 a[238:239], v123 offset:4512\n\t"
        "s_waitcnt lgkmcnt(8)\n\t"
        "ds_read_b64_tr_b16 a[240:241], v123 offset:512\n\t"
        "ds_read_b64_tr_b16 a[242:243], v123 offset:640\n\t"
        "ds_read_b64_tr_b16 a[244:245], v123 offset:4864\n\t"
        "ds_read_b64_tr_b16 a[246:247], v123 offset:4992\n\t"
        "s_waitcnt lgkmcnt(8)\n\t"
        "ds_read_b64_tr_b16 a[248:249], v123 offset:544\n\t"
        "ds_read_b64_tr_b16 a[250:251], v123 offset:672\n\t"
        "ds_read_b64_tr_b16 a[252:253], v123 offset:4896\n\t"
        "ds_read_b64_tr_b16 a[254:255], v123 offset:5024\n\t"
        "s_waitcnt lgkmcnt(8)\n\t"
        "s_mov_b32 s35, 33792\n\t"
        "buffer_load_dword v0, off, s[12:15], s35 sc1\n\t"
        "s_waitcnt lgkmcnt(0)\n\t"
        "s_waitcnt vmcnt(0)\n\t"
        "s_mov_b32 s42, 0\n\t"
        "LB_cal%=:\n\t"
        "v_readfirstlane_b32 s34, v0\n\t"
        "s_nop 0\n\t"
        "s_cmp_lg_u32 s34, 0\n\t"
        "s_cbranch_scc1 LB_calok%=\n\t"
        "s_sleep 4\n\t"
        "s_mov_b32 s35, 33792\n\t"
        "buffer_load_dword v0, off, s[12:15], s35 sc1\n\t"
        "s_waitcnt vmcnt(0)\n\t"
        "s_add_u32 s42, s42, 1\n\t"
        "s_cmp_lt_u32 s42, 0x100000\n\t"
        "s_cbranch_scc1 LB_cal%=\n\t"
        "s_movk_i32 s34, 8\n\t"
        "s_branch LB_die%=\n\t"
        "LB_calok%=:\n\t"
        "v_readfirstlane_b32 s34, v0\n\t"
        "s_nop 0\n\t"
        "s_and_b32 s34, s34, 8192\n\t"
        "s_add_u32 s8, s8, s34\n\t"
        "s_addc_u32 s9, s9, 0\n\t"
        "v_min_u32_e32 v109, -2, v109\n\t"
        "buffer_store_dword v109, v116, s[8:11], 0 offen sc1\n\t"
        "s_memtime s[74:75]\n\t"
        "s_memrealtime s[32:33]\n\t"
        "s_waitcnt lgkmcnt(0)\n\t"
        "v_mov_b32_e32 v110, s32\n\t"
        "s_sub_u32 s34, s74, s70\n\t"
        "s_sub_u32 s35, s32, s72\n\t"
        "v_cvt_f32_u32_e32 v2, s34\n\t"
        "v_cvt_f32_u32_e32 v3, s35\n\t"
        "v_rcp_f32_e32 v2, v2\n\t"
        "s_nop 0\n\t"
        "v_mul_f32_e32 v2, v2, v3\n\t"
        "v_mul_f32_e32 v3, 0x45354000, v2\n\t"
        "v_cvt_u32_f32_e32 v3, v3\n\t"
        "s_nop 0\n\t"
        "v_readfirstlane_b32 s34, v3\n\t"
        "s_add_u32 s35, s29, 40\n\t"
        "s_min_u32 s34, s34, s35\n\t"
        "s_sub_u32 s35, s29, 10\n\t"
        "s_max_u32 s29, s34, s35\n\t"
        "v_mul_f32_e32 v3, 0x44d70000, v2\n\t"
        "v_cvt_u32_f32_e32 v3, v3\n\t"
        "s_nop 0\n\t"
        "v_readfirstlane_b32 s34, v3\n\t"
        "s_add_u32 s35, s30, 40\n\t"
        "s_min_u32 s34, s34, s35\n\t"
        "s_sub_u32 s35, s30, 10\n\t"
        "s_max_u32 s30, s34, s35\n\t"
        "s_lshl_b32 s34, s29, 8\n\t"
        "s_lshl_b32 s35, s30, 20\n\t"
        "s_or_b32 s54, s34, s35\n\t"
        "s_add_u32 s34, s54, 8400128\n\t"
        "v_mov_b32_e32 v111, s34\n\t"
        "s_barrier\n\t"
        "s_cmp_lg_u32 s43, 0\n\t"
        "s_cbranch_scc1 LB_st%=\n\t"
        "v_mov_b32_e32 v2, 1\n\t"
        "s_mov_b32 s35, 34304\n\t"
        "s_mov_b64 exec, 1\n\t"
        "buffer_atomic_add v2, off, s[12:15], s35 sc0\n\t"
        "s_mov_b64 exec, -1\n\t"
        "s_waitcnt vmcnt(0)\n\t"
        "v_readfirstlane_b32 s34, v2\n\t"
        "s_nop 0\n\t"
        "s_cmp_lg_u32 s34, 95\n\t"
        "s_cbranch_scc1 LB_st%=\n\t"
        "s_memrealtime s[32:33]\n\t"
        "v_mbcnt_lo_u32_b32 v3, -1, 0\n\t"
        "v_lshlrev_b32_e32 v3, 7, v3\n\t"
        "s_waitcnt lgkmcnt(0)\n\t"
        "s_add_u32 s34, s32, 50\n\t"
        "s_or_b32 s34, s34, 1\n\t"
        "v_mov_b32_e32 v2, s34\n\t"
        "s_mov_b32 s35, 36864\n\t"
        "s_mov_b64 exec, 0xffff\n\t"
        "buffer_store_dword v2, v3, s[12:15], s35 offen sc1\n\t"
        "s_mov_b64 exec, -1\n\t"
        "LB_st%=:\n\t"
        "s_mov_b32 s24, 0\n\t"
        "s_mov_b32 s26, 0\n\t"
        "s_mov_b32 s27, 0x80000\n\t"
        "s_mov_b32 s28, 0x180000\n\t"
        "s_mov_b32 s31, 0\n\t"
        "s_mov_b32 s47, 0\n\t"
        "s_mov_b32 s38, 0xff00ff00\n\t"
        "s_mov_b32 s39, 0xff00ff00\n\t"
        "s_mov_b32 s40, 0\n\t"
        "s_mov_b32 s41, -1\n\t"
        "v_mov_b64_e32 v[0:1], 0\n\t"
        "v_mov_b64_e32 v[2:3], 0\n\t"
        "v_mov_b64_e32 v[4:5], 0\n\t"
        "v_mov_b64_e32 v[6:7], 0\n\t"
        "v_mov_b64_e32 v[8:9], 0\n\t"
        "v_mov_b64_e32 v[10:11], 0\n\t"
        "v_mov_b64_e32 v[12:13], 0\n\t"
        "v_mov_b64_e32 v[14:15], 0\n\t"
        "s_mov_b32 s42, 0\n\t"
        "LB_gw%=:\n\t"
        "buffer_load_dword v16, off, s[12:15], s45 sc1\n\t"
        "s_waitcnt vmcnt(0)\n\t"
        "v_readfirstlane_b32 s25, v16\n\t"
        "s_nop 0\n\t"
        "s_cmp_lg_u32 s25, 0\n\t"
        "s_cbranch_scc1 LB_loop%=\n\t"
        "s_sleep 2\n\t"
        "s_add_u32 s42, s42, 1\n\t"
        "s_cmp_lt_u32 s42, 0x100000\n\t"
        "s_cbranch_scc1 LB_gw%=\n\t"
        "s_movk_i32 s34, 9\n\t"
        "s_branch LB_die%=\n\t"
        ".p2align 8\n\t"
        "LB_loop%=:\n\t"
        "s_mov_b32 s42, 0\n\t"
        "s_sub_u32 s35, s25, -5\n\t"
        "s_waitcnt lgkmcnt(0)\n\t"
        "s_memrealtime s[2:3]\n\t"
        "s_memrealtime s[4:5]\n\t"
        "s_memrealtime s[6:7]\n\t"
        "LB_wa%=:\n\t"
        "s_memrealtime s[32:33]\n\t"
        "s_waitcnt lgkmcnt(6)\n\t"
        "s_sub_u32 s34, s35, s2\n\t"
        "s_cmpk_lt_u32 s34, 0x2000\n\t"
        "s_cbranch_scc0 LB_wa%=x\n\t"
        "s_memrealtime s[2:3]\n\t"
        "s_waitcnt lgkmcnt(6)\n\t"
        "s_sub_u32 s34, s35, s4\n\t"
        "s_cmpk_lt_u32 s34, 0x2000\n\t"
        "s_cbranch_scc0 LB_wa%=x\n\t"
        "s_memrealtime s[4:5]\n\t"
        "s_waitcnt lgkmcnt(6)\n\t"
        "s_sub_u32 s34, s35, s6\n\t"
        "s_cmpk_lt_u32 s34, 0x2000\n\t"
        "s_cbranch_scc0 LB_wa%=x\n\t"
        "s_memrealtime s[6:7]\n\t"
        "s_waitcnt lgkmcnt(6)\n\t"
        "s_sub_u32 s34, s35, s32\n\t"
        "s_cmpk_lt_u32 s34, 0x2000\n\t"
        "s_cbranch_scc1 LB_wa%=\n\t"
        "s_nop 3\n\t"
        "LB_wa%=x:\n\t"
        "s_add_i32 s34, s34, 4\n\t"
        "s_max_i32 s34, s34, 0\n\t"
        "s_bitcmp1_b32 s34, 1\n\t"
        "s_cbranch_scc0 LB_wa%=y\n\t"
        "s_nop 15\n\t"
        "LB_wa%=y:\n\t"
        "s_bitcmp1_b32 s34, 0\n\t"
        "s_cbranch_scc0 LB_wa%=z\n\t"
        "s_nop 9\n\t"
        "LB_wa%=z:\n\t"
        "LB_rd%=:\n\t"
        "buffer_load_dwordx4 v[16:19], v112, s[8:11], s26 offen offset:0 sc1\n\t"
        "s_nop 9\n\t"
        "buffer_load_dwordx4 v[20:23], v112, s[8:11], s26 offen offset:1024 sc1\n\t"
        "s_nop 9\n\t"
        "buffer_load_dwordx4 v[24:27], v112, s[8:11], s26 offen offset:2048 sc1\n\t"
        "s_nop 9\n\t"
        "buffer_load_dwordx4 v[28:31], v112, s[8:11], s26 offen offset:3072 sc1\n\t"
        "LB_go%=:\n\t"
        "s_waitcnt vmcnt(3)\n\t"
        "v_smfmac_f32_16x16x64_bf16 v[0:3], v[16:19], v[128:135], v106\n\t"
        "v_max3_u32 v108, v16, v17, v18\n\t"
        "v_max_u32_e32 v108, v19, v108\n\t"
        "v_smfmac_f32_16x16x64_bf16 v[4:7], v[16:19], v[136:143], v106\n\t"
        "v_smfmac_f32_16x16x64_bf16 v[8:11], v[16:19], v[144:151], v106\n\t"
        "v_smfmac_f32_16x16x64_bf16 v[12:15], v[16:19], v[152:159], v106\n\t"
        "s_waitcnt vmcnt(2)\n\t"
        "v_smfmac_f32_16x16x64_bf16 v[0:3], v[20:23], v[160:167], v106\n\t"
        "v_max3_u32 v108, v20, v21, v108\n\t"
        "v_max3_u32 v108, v22, v23, v108\n\t"
        "v_smfmac_f32_16x16x64_bf16 v[4:7], v[20:23], v[168:175], v106\n\t"
        "v_smfmac_f32_16x16x64_bf16 v[8:11], v[20:23], v[176:183], v106\n\t"
        "v_smfmac_f32_16x16x64_bf16 v[12:15], v[20:23], v[184:191], v106\n\t"
        "s_waitcnt vmcnt(1)\n\t"
        "v_smfmac_f32_16x16x64_bf16 v[0:3], v[24:27], v[192:199], v106\n\t"
        "v_max3_u32 v108, v24, v25, v108\n\t"
        "v_max3_u32 v108, v26, v27, v108\n\t"
        "v_smfmac_f32_16x16x64_bf16 v[4:7], v[24:27], v[200:207], v106\n\t"
        "v_smfmac_f32_16x16x64_bf16 v[8:11], v[24:27], v[208:215], v106\n\t"
        "v_smfmac_f32_16x16x64_bf16 v[12:15], v[24:27], v[216:223], v106\n\t"
        "s_waitcnt vmcnt(0)\n\t"
        "v_smfmac_f32_16x16x64_bf16 v[0:3], v[28:31], v[224:231], v106\n\t"
        "v_max3_u32 v108, v28, v29, v108\n\t"
        "v_max3_u32 v108, v30, v31, v108\n\t"
        "v_smfmac_f32_16x16x64_bf16 v[4:7], v[28:31], v[232:239], v106\n\t"
        "v_cmp_eq_u32_e32 vcc, -1, v108\n\t"
        "v_smfmac_f32_16x16x64_bf16 v[8:11], v[28:31], v[240:247], v106\n\t"
        "v_smfmac_f32_16x16x64_bf16 v[12:15], v[28:31], v[248:255], v106\n\t"
        "s_cbranch_vccnz LB_rdfail%=\n\t"
        "s_nop 0\n\t"
        "v_add_f32_e32 v0, v0, v1\n\t"
        "v_add_f32_e32 v1, v2, v3\n\t"
        "ds_write_b64 v118, v[0:1] offset:0\n\t"
        "v_add_f32_e32 v4, v4, v5\n\t"
        "v_add_f32_e32 v5, v6, v7\n\t"
        "ds_write_b64 v118, v[4:5] offset:512\n\t"
        "v_add_f32_e32 v8, v8, v9\n\t"
        "v_add_f32_e32 v9, v10, v11\n\t"
        "ds_write_b64 v118, v[8:9] offset:1024\n\t"
        "v_add_f32_e32 v12, v12, v13\n\t"
        "v_add_f32_e32 v13, v14, v15\n\t"
        "ds_write_b64 v118, v[12:13] offset:1536\n\t"
        "buffer_store_dwordx2 v[110:111], v117, s[8:11], s26 offen sc1\n\t"
        "s_waitcnt lgkmcnt(0)\n\t"
        "s_barrier\n\t"
        "ds_read_b128 v[16:19], v119 offset:0\n\t"
        "ds_read_b128 v[20:23], v119 offset:2048\n\t"
        "ds_read_b128 v[24:27], v119 offset:4096\n\t"
        "ds_read_b128 v[28:31], v119 offset:6144\n\t"
        "buffer_load_dwordx4 v[64:67], v124, s[8:11], s28 offen sc1\n\t"
        "s_waitcnt lgkmcnt(2)\n\t"
        "v_pk_add_f32 v[16:17], v[16:17], v[20:21]\n\t"
        "v_pk_add_f32 v[18:19], v[18:19], v[22:23]\n\t"
        "s_waitcnt lgkmcnt(0)\n\t"
        "v_pk_add_f32 v[24:25], v[24:25], v[28:29]\n\t"
        "v_pk_add_f32 v[26:27], v[26:27], v[30:31]\n\t"
        "v_pk_add_f32 v[16:17], v[16:17], v[24:25]\n\t"
        "v_pk_add_f32 v[18:19], v[18:19], v[26:27]\n\t"
        "v_cvt_pk_bf16_f32 v20, v16, v17\n\t"
        "v_cvt_pk_bf16_f32 v21, v18, v19\n\t"
        "buffer_store_dwordx2 v[20:21], v114, s[8:11], s26 offen sc1\n\t"
        "s_cmp_eq_u32 s24, 63\n\t"
        "s_cbranch_scc1 LB_end%=\n\t"
        "s_memrealtime s[52:53]\n\t"
        "v_mov_b64_e32 v[24:25], -1\n\t"
        "v_mov_b64_e32 v[32:33], 0\n\t"
        "v_mov_b64_e32 v[34:35], 0\n\t"
        "v_mov_b64_e32 v[36:37], 0\n\t"
        "v_mov_b64_e32 v[38:39], 0\n\t"
        "v_mov_b64_e32 v[40:41], 0\n\t"
        "v_mov_b64_e32 v[42:43], 0\n\t"
        "v_mov_b64_e32 v[44:45], 0\n\t"
        "v_mov_b64_e32 v[46:47], 0\n\t"
        "v_mov_b64_e32 v[0:1], 0\n\t"
        "v_mov_b64_e32 v[2:3], 0\n\t"
        "v_mov_b64_e32 v[4:5], 0\n\t"
        "v_mov_b64_e32 v[6:7], 0\n\t"
        "v_mov_b64_e32 v[8:9], 0\n\t"
        "v_mov_b64_e32 v[10:11], 0\n\t"
        "v_mov_b64_e32 v[12:13], 0\n\t"
        "v_mov_b64_e32 v[14:15], 0\n\t"
        "s_add_u32 s46, s26, 4096\n\t"
        "s_sleep 1\n\t"
        "buffer_load_dwordx2 v[104:105], v113, s[8:11], s26 offen sc1\n\t"
        "buffer_store_dword v24, v116, s[8:11], s28 offen sc1\n\t"
        "s_xor_b32 s34, s26, 0x100000\n\t"
        "buffer_store_dwordx2 v[24:25], v114, s[8:11], s34 offen sc1\n\t"
        "s_add_u32 s66, s49, s43\n\t"
        "s_cmp_lg_u32 s66, 32\n\t"
        "s_cbranch_scc1 LB_norep%=\n\t"
        "s_waitcnt vmcnt(4)\n\t"
        "s_sub_u32 s66, s24, 1\n\t"
        "v_lshrrev_b32_e32 v68, 16, v64\n\t"
        "v_cmp_eq_u32_e32 vcc, s66, v68\n\t"
        "v_subrev_u32_e32 v64, s59, v64\n\t"
        "v_bfe_i32 v64, v64, 0, 16\n\t"
        "v_cndmask_b32_e32 v64, 0, v64, vcc\n\t"
        "v_lshrrev_b32_e32 v68, 16, v65\n\t"
        "v_cmp_eq_u32_e32 vcc, s66, v68\n\t"
        "v_subrev_u32_e32 v65, s59, v65\n\t"
        "v_bfe_i32 v65, v65, 0, 16\n\t"
        "v_cndmask_b32_e32 v65, 0, v65, vcc\n\t"
        "v_lshrrev_b32_e32 v68, 16, v66\n\t"
        "v_cmp_eq_u32_e32 vcc, s66, v68\n\t"
        "v_subrev_u32_e32 v66, s59, v66\n\t"
        "v_bfe_i32 v66, v66, 0, 16\n\t"
        "v_cndmask_b32_e32 v66, 0, v66, vcc\n\t"
        "v_lshrrev_b32_e32 v68, 16, v67\n\t"
        "v_cmp_eq_u32_e32 vcc, s66, v68\n\t"
        "v_subrev_u32_e32 v67, s59, v67\n\t"
        "v_bfe_i32 v67, v67, 0, 16\n\t"
        "v_cndmask_b32_e32 v67, 0, v67, vcc\n\t"
        "v_max3_i32 v64, v64, v65, v66\n\t"
        "v_max_i32_e32 v64, v64, v67\n\t"
        "s_nop 1\n\t"
        "v_max_i32_dpp v64, v64, v64 quad_perm:[1,0,3,2] row_mask:0xf bank_mask:0xf\n\t"
        "s_nop 1\n\t"
        "v_max_i32_dpp v64, v64, v64 quad_perm:[2,3,0,1] row_mask:0xf bank_mask:0xf\n\t"
        "s_nop 1\n\t"
        "v_max_i32_dpp v64, v64, v64 row_ror:4 row_mask:0xf bank_mask:0xf\n\t"
        "s_nop 1\n\t"
        "v_max_i32_dpp v64, v64, v64 row_ror:8 row_mask:0xf bank_mask:0xf\n\t"
        "v_mov_b32_e32 v65, v64\n\t"
        "s_nop 1\n\t"
        "v_permlane16_swap_b32_e32 v64, v65\n\t"
        "v_max_i32_e32 v64, v64, v65\n\t"
        "v_mov_b32_e32 v65, v64\n\t"
        "s_nop 1\n\t"
        "v_permlane32_swap_b32_e32 v64, v65\n\t"
        "v_max_i32_e32 v64, v64, v65\n\t"
        "s_nop 0\n\t"
        "v_readfirstlane_b32 s64, v64\n\t"
        "LB_norep%=:\n\t"
        "s_waitcnt vmcnt(2) lgkmcnt(0)\n\t"
        "s_mov_b32 s61, s29\n\t"
        "s_mov_b32 s62, s30\n\t"
        "s_cmp_eq_u32 s24, 1\n\t"
        "s_cselect_b32 s63, 1, s31\n\t"
        "v_and_b32_e32 v30, 0xff, v105\n\t"
        "v_cmp_ne_u32_e32 vcc, s24, v30\n\t"
        "s_nop 0\n\t"
        "s_cbranch_vccnz LB_stre%=\n\t"
        "LB_stok%=:\n\t"
        "v_subrev_u32_e32 v104, s25, v104\n\t"
        "s_nop 1\n\t"
        "v_max_i32_dpp v104, v104, v104 quad_perm:[1,0,3,2] row_mask:0xf bank_mask:0xf\n\t"
        "s_nop 1\n\t"
        "v_max_i32_dpp v104, v104, v104 quad_perm:[2,3,0,1] row_mask:0xf bank_mask:0xf\n\t"
        "s_nop 1\n\t"
        "v_max_i32_dpp v104, v104, v104 row_ror:4 row_mask:0xf bank_mask:0xf\n\t"
        "s_nop 1\n\t"
        "v_max_i32_dpp v104, v104, v104 row_ror:8 row_mask:0xf bank_mask:0xf\n\t"
        "s_nop 1\n\t"
        "v_max_i32_dpp v104, v104, v104 row_bcast:15 row_mask:0xa bank_mask:0xf\n\t"
        "s_nop 1\n\t"
        "v_max_i32_dpp v104, v104, v104 row_bcast:31 row_mask:0xc bank_mask:0xf\n\t"
        "v_readfirstlane_b32 s35, v105\n\t"
        "v_readlane_b32 s36, v104, 63\n\t"
        "s_bfe_u32 s29, s35, 0xc0008\n\t"
        "s_lshr_b32 s30, s35, 20\n\t"
        "s_add_u32 s35, s31, s50\n\t"
        "s_mul_i32 s37, s50, 3\n\t"
        "s_min_u32 s35, s35, s37\n\t"
        "s_add_i32 s34, s36, s51\n\t"
        "s_cmp_gt_i32 s34, 0\n\t"
        "s_cselect_b32 s31, s35, 0\n\t"
        "s_cselect_b32 s34, s34, 0\n\t"
        "s_add_u32 s34, s34, s31\n\t"
        "s_add_u32 s37, s25, s34\n\t"
        "s_add_u32 s45, s37, s29\n\t"
        "s_add_u32 s25, s45, s30\n\t"
        "s_add_u32 s66, s49, s43\n\t"
        "s_cmp_lg_u32 s66, 32\n\t"
        "s_cbranch_scc1 LB_noctl%=\n\t"
        "s_cmp_eq_u32 s24, 0\n\t"
        "s_cbranch_scc1 LB_ctl0%=\n\t"
        "s_cmp_lt_u32 s24, 6\n\t"
        "s_cselect_b32 s68, 32, 8\n\t"
        "s_add_i32 s65, s36, s62\n\t"
        "s_cmp_eq_u32 s24, 1\n\t"
        "s_cselect_b32 s34, 6, 0\n\t"
        "s_cselect_b32 s35, 3, 0\n\t"
        "s_sub_u32 s64, s64, s34\n\t"
        "s_sub_u32 s65, s65, s35\n\t"
        "s_lshl_b32 s66, s65, 1\n\t"
        "s_sub_i32 s66, s66, s56\n\t"
        "s_cmp_gt_i32 s66, 56\n\t"
        "s_cbranch_scc1 LB_c1%=\n\t"
        "s_lshl_b32 s66, s65, 1\n\t"
        "s_add_u32 s34, s56, s68\n\t"
        "s_min_i32 s34, s34, s66\n\t"
        "s_sub_u32 s35, s56, s66\n\t"
        "s_ashr_i32 s35, s35, 1\n\t"
        "s_max_i32 s35, s35, 1\n\t"
        "s_sub_u32 s35, s56, s35\n\t"
        "s_max_i32 s56, s34, s35\n\t"
        "s_branch LB_c2%=\n\t"
        "LB_c1%=:\n\t"
        "s_add_u32 s56, s56, 2\n\t"
        "s_add_i32 s66, s64, s57\n\t"
        "s_cmp_gt_i32 s66, s61\n\t"
        "s_cbranch_scc1 LB_c2%=\n\t"
        "s_add_u32 s57, s57, 4\n\t"
        "s_min_u32 s57, s57, 24\n\t"
        "LB_c2%=:\n\t"
        "s_cmp_lg_u32 s63, 0\n\t"
        "s_cbranch_scc1 LB_c4%=\n\t"
        "s_lshl_b32 s66, s64, 1\n\t"
        "s_sub_i32 s66, s66, s55\n\t"
        "s_cmp_gt_i32 s66, 56\n\t"
        "s_cbranch_scc1 LB_c3%=\n\t"
        "s_lshl_b32 s66, s64, 1\n\t"
        "s_add_u32 s34, s55, s68\n\t"
        "s_min_i32 s34, s34, s66\n\t"
        "s_sub_u32 s35, s55, s66\n\t"
        "s_ashr_i32 s35, s35, 1\n\t"
        "s_max_i32 s35, s35, 1\n\t"
        "s_sub_u32 s35, s55, s35\n\t"
        "s_max_i32 s55, s34, s35\n\t"
        "s_branch LB_c4%=\n\t"
        "LB_c3%=:\n\t"
        "s_add_u32 s55, s55, 2\n\t"
        "s_cmp_lt_i32 s60, s58\n\t"
        "s_cbranch_scc1 LB_c4%=\n\t"
        "s_add_u32 s66, s58, 8\n\t"
        "s_cmp_gt_i32 s60, s66\n\t"
        "s_cbranch_scc1 LB_c4%=\n\t"
        "s_add_u32 s58, s58, 4\n\t"
        "s_min_u32 s58, s58, 22\n\t"
        "LB_c4%=:\n\t"
        "s_and_b32 s66, s24, 3\n\t"
        "s_cmp_eq_u32 s66, 0\n\t"
        "s_cselect_b32 s66, 1, 0\n\t"
        "s_cmp_lt_u32 s24, 30\n\t"
        "s_cselect_b32 s34, 8, 8\n\t"
        "s_cselect_b32 s35, 6, 6\n\t"
        "s_sub_u32 s57, s57, s66\n\t"
        "s_max_i32 s57, s57, s34\n\t"
        "s_sub_u32 s58, s58, s66\n\t"
        "s_max_i32 s58, s58, s35\n\t"
        "s_add_u32 s34, s55, 1\n\t"
        "s_lshr_b32 s34, s34, 1\n\t"
        "s_add_u32 s34, s34, s57\n\t"
        "s_min_u32 s34, s34, 1000\n\t"
        "s_lshl_b32 s34, s34, 8\n\t"
        "s_add_u32 s35, s56, 1\n\t"
        "s_lshr_b32 s35, s35, 1\n\t"
        "s_add_u32 s35, s35, s58\n\t"
        "s_min_u32 s35, s35, 1000\n\t"
        "s_lshl_b32 s35, s35, 20\n\t"
        "s_or_b32 s54, s34, s35\n\t"
        "s_branch LB_ctl1%=\n\t"
        "LB_ctl0%=:\n\t"
        "s_movk_i32 s57, 8\n\t"
        "s_movk_i32 s58, 6\n\t"
        "s_sub_u32 s55, s29, 53\n\t"
        "s_lshl_b32 s55, s55, 1\n\t"
        "s_sub_u32 s56, s30, 14\n\t"
        "s_lshl_b32 s56, s56, 1\n\t"
        "LB_ctl1%=:\n\t"
        "s_mov_b32 s59, s37\n\t"
        "s_sub_u32 s60, 0, s36\n\t"
        "LB_noctl%=:\n\t"
        "s_cmp_eq_u32 s24, 0\n\t"
        "s_cselect_b32 s34, 2561, 1\n\t"
        "s_add_u32 s34, s34, s24\n\t"
        "s_add_u32 s34, s34, s54\n\t"
        "v_mov_b32_e32 v111, s34\n\t"
        "s_mov_b32 s42, 0\n\t"
        "s_sub_u32 s35, s45, 8\n\t"
        "s_waitcnt lgkmcnt(0)\n\t"
        "s_memrealtime s[2:3]\n\t"
        "s_memrealtime s[4:5]\n\t"
        "s_memrealtime s[6:7]\n\t"
        "LB_wo%=:\n\t"
        "s_memrealtime s[32:33]\n\t"
        "s_waitcnt lgkmcnt(6)\n\t"
        "s_sub_u32 s34, s35, s2\n\t"
        "s_cmpk_lt_u32 s34, 0x2000\n\t"
        "s_cbranch_scc0 LB_wo%=x\n\t"
        "s_memrealtime s[2:3]\n\t"
        "s_waitcnt lgkmcnt(6)\n\t"
        "s_sub_u32 s34, s35, s4\n\t"
        "s_cmpk_lt_u32 s34, 0x2000\n\t"
        "s_cbranch_scc0 LB_wo%=x\n\t"
        "s_memrealtime s[4:5]\n\t"
        "s_waitcnt lgkmcnt(6)\n\t"
        "s_sub_u32 s34, s35, s6\n\t"
        "s_cmpk_lt_u32 s34, 0x2000\n\t"
        "s_cbranch_scc0 LB_wo%=x\n\t"
        "s_memrealtime s[6:7]\n\t"
        "s_waitcnt lgkmcnt(6)\n\t"
        "s_sub_u32 s34, s35, s32\n\t"
        "s_cmpk_lt_u32 s34, 0x2000\n\t"
        "s_cbranch_scc1 LB_wo%=\n\t"
        "s_nop 3\n\t"
        "LB_wo%=x:\n\t"
        "s_add_i32 s34, s34, 4\n\t"
        "s_max_i32 s34, s34, 0\n\t"
        "s_bitcmp1_b32 s34, 1\n\t"
        "s_cbranch_scc0 LB_wo%=y\n\t"
        "s_nop 15\n\t"
        "LB_wo%=y:\n\t"
        "s_bitcmp1_b32 s34, 0\n\t"
        "s_cbranch_scc0 LB_wo%=z\n\t"
        "s_nop 9\n\t"
        "LB_wo%=z:\n\t"
        "LB_ro%=:\n\t"
        "buffer_load_dwordx4 v[64:67], v115, s[8:11], s26 offen offset:0 sc1\n\t"
        "s_nop 12\n\t"
        "buffer_load_dwordx4 v[68:71], v115, s[8:11], s26 offen offset:1024 sc1\n\t"
        "s_nop 12\n\t"
        "buffer_load_dwordx4 v[72:75], v115, s[8:11], s26 offen offset:2048 sc1\n\t"
        "s_nop 12\n\t"
        "buffer_load_dwordx4 v[76:79], v115, s[8:11], s26 offen offset:3072 sc1\n\t"
        "s_nop 12\n\t"
        "buffer_load_dwordx4 v[80:83], v115, s[8:11], s46 offen offset:0 sc1\n\t"
        "s_nop 12\n\t"
        "buffer_load_dwordx4 v[84:87], v115, s[8:11], s46 offen offset:1024 sc1\n\t"
        "s_nop 12\n\t"
        "buffer_load_dwordx4 v[88:91], v115, s[8:11], s46 offen offset:2048 sc1\n\t"
        "s_nop 12\n\t"
        "buffer_load_dwordx4 v[92:95], v115, s[8:11], s46 offen offset:3072 sc1\n\t"
        "LB_go2%=:\n\t"
        "s_waitcnt vmcnt(7)\n\t"
        "v_smfmac_f32_16x16x64_bf16 v[32:35], v[64:67], a[0:7], v106\n\t"
        "v_max3_u32 v108, v64, v65, v66\n\t"
        "v_max_u32_e32 v108, v67, v108\n\t"
        "v_smfmac_f32_16x16x64_bf16 v[40:43], v[64:67], a[16:23], v106\n\t"
        "v_smfmac_f32_16x16x64_bf16 v[36:39], v[64:67], a[8:15], v106\n\t"
        "v_smfmac_f32_16x16x64_bf16 v[44:47], v[64:67], a[24:31], v106\n\t"
        "s_waitcnt vmcnt(6)\n\t"
        "v_smfmac_f32_16x16x64_bf16 v[32:35], v[68:71], a[32:39], v106\n\t"
        "v_max3_u32 v108, v68, v69, v108\n\t"
        "v_max3_u32 v108, v70, v71, v108\n\t"
        "v_smfmac_f32_16x16x64_bf16 v[40:43], v[68:71], a[48:55], v106\n\t"
        "v_smfmac_f32_16x16x64_bf16 v[36:39], v[68:71], a[40:47], v106\n\t"
        "v_smfmac_f32_16x16x64_bf16 v[44:47], v[68:71], a[56:63], v106\n\t"
        "s_waitcnt vmcnt(5)\n\t"
        "v_smfmac_f32_16x16x64_bf16 v[32:35], v[72:75], a[64:71], v106\n\t"
        "v_max3_u32 v108, v72, v73, v108\n\t"
        "v_max3_u32 v108, v74, v75, v108\n\t"
        "v_smfmac_f32_16x16x64_bf16 v[40:43], v[72:75], a[80:87], v106\n\t"
        "v_smfmac_f32_16x16x64_bf16 v[36:39], v[72:75], a[72:79], v106\n\t"
        "v_smfmac_f32_16x16x64_bf16 v[44:47], v[72:75], a[88:95], v106\n\t"
        "s_waitcnt vmcnt(4)\n\t"
        "v_smfmac_f32_16x16x64_bf16 v[32:35], v[76:79], a[96:103], v106\n\t"
        "v_max3_u32 v108, v76, v77, v108\n\t"
        "v_max3_u32 v108, v78, v79, v108\n\t"
        "v_smfmac_f32_16x16x64_bf16 v[40:43], v[76:79], a[112:119], v106\n\t"
        "v_smfmac_f32_16x16x64_bf16 v[36:39], v[76:79], a[104:111], v106\n\t"
        "v_smfmac_f32_16x16x64_bf16 v[44:47], v[76:79], a[120:127], v106\n\t"
        "s_waitcnt vmcnt(3)\n\t"
        "v_smfmac_f32_16x16x64_bf16 v[32:35], v[80:83], a[128:135], v106\n\t"
        "v_max3_u32 v108, v80, v81, v108\n\t"
        "v_max3_u32 v108, v82, v83, v108\n\t"
        "v_smfmac_f32_16x16x64_bf16 v[40:43], v[80:83], a[144:151], v106\n\t"
        "v_smfmac_f32_16x16x64_bf16 v[36:39], v[80:83], a[136:143], v106\n\t"
        "v_smfmac_f32_16x16x64_bf16 v[44:47], v[80:83], a[152:159], v106\n\t"
        "s_waitcnt vmcnt(2)\n\t"
        "v_smfmac_f32_16x16x64_bf16 v[32:35], v[84:87], a[160:167], v106\n\t"
        "v_max3_u32 v108, v84, v85, v108\n\t"
        "v_max3_u32 v108, v86, v87, v108\n\t"
        "v_smfmac_f32_16x16x64_bf16 v[40:43], v[84:87], a[176:183], v106\n\t"
        "v_smfmac_f32_16x16x64_bf16 v[36:39], v[84:87], a[168:175], v106\n\t"
        "v_smfmac_f32_16x16x64_bf16 v[44:47], v[84:87], a[184:191], v106\n\t"
        "s_waitcnt vmcnt(1)\n\t"
        "v_smfmac_f32_16x16x64_bf16 v[32:35], v[88:91], a[192:199], v106\n\t"
        "v_max3_u32 v108, v88, v89, v108\n\t"
        "v_max3_u32 v108, v90, v91, v108\n\t"
        "v_smfmac_f32_16x16x64_bf16 v[40:43], v[88:91], a[208:215], v106\n\t"
        "v_smfmac_f32_16x16x64_bf16 v[36:39], v[88:91], a[200:207], v106\n\t"
        "v_smfmac_f32_16x16x64_bf16 v[44:47], v[88:91], a[216:223], v106\n\t"
        "s_waitcnt vmcnt(0)\n\t"
        "v_smfmac_f32_16x16x64_bf16 v[32:35], v[92:95], a[224:231], v106\n\t"
        "v_max3_u32 v108, v92, v93, v108\n\t"
        "v_max3_u32 v108, v94, v95, v108\n\t"
        "v_smfmac_f32_16x16x64_bf16 v[40:43], v[92:95], a[240:247], v106\n\t"
        "v_cmp_eq_u32_e32 vcc, -1, v108\n\t"
        "v_smfmac_f32_16x16x64_bf16 v[36:39], v[92:95], a[232:239], v106\n\t"
        "v_smfmac_f32_16x16x64_bf16 v[44:47], v[92:95], a[248:255], v106\n\t"
        "s_cbranch_vccnz LB_rofail%=\n\t"
        "s_nop 0\n\t"
        "v_add_f32_e32 v32, v32, v33\n\t"
        "s_nop 1\n\t"
        "v_add_f32_e32 v40, v42, v43\n\t"
        "v_add_f32_e32 v32, v32, v40\n\t"
        "v_add_f32_e32 v36, v36, v37\n\t"
        "v_add_f32_e32 v44, v46, v47\n\t"
        "v_add_f32_e32 v36, v36, v44\n\t"
        "ds_write2_b32 v120, v32, v36 offset0:0 offset1:64\n\t"
        "s_waitcnt lgkmcnt(0)\n\t"
        "s_barrier\n\t"
        "ds_read_b64 v[64:65], v121 offset:0\n\t"
        "ds_read_b64 v[66:67], v121 offset:512\n\t"
        "ds_read_b64 v[68:69], v121 offset:1024\n\t"
        "ds_read_b64 v[70:71], v121 offset:1536\n\t"
        "s_waitcnt lgkmcnt(2)\n\t"
        "v_pk_add_f32 v[64:65], v[64:65], v[66:67]\n\t"
        "s_waitcnt lgkmcnt(0)\n\t"
        "v_pk_add_f32 v[68:69], v[68:69], v[70:71]\n\t"
        "v_pk_add_f32 v[64:65], v[64:65], v[68:69]\n\t"
        "v_cvt_pk_bf16_f32 v64, v64, v65\n\t"
        "buffer_store_dword v64, v116, s[8:11], s27 offen sc1\n\t"
        "s_memrealtime s[32:33]\n\t"
        "s_waitcnt lgkmcnt(0)\n\t"
        "v_mov_b32_e32 v110, s32\n\t"
        "s_add_u32 s24, s24, 1\n\t"
        "s_mov_b32 s28, s26\n\t"
        "s_mov_b32 s26, s27\n\t"
        "s_add_u32 s27, s27, 0x80000\n\t"
        "s_and_b32 s27, s27, 0x1fffff\n\t"
        "s_branch LB_loop%=\n\t"
        "LB_end%=:\n\t"
        "s_endpgm\n\t"
        "LB_stre%=:\n\t"
        "s_mov_b32 s42, 0\n\t"
        "LB_str2%=:\n\t"
        "buffer_load_dwordx2 v[104:105], v113, s[8:11], s26 offen sc1\n\t"
        "s_waitcnt vmcnt(0)\n\t"
        "v_and_b32_e32 v30, 0xff, v105\n\t"
        "v_cmp_ne_u32_e32 vcc, s24, v30\n\t"
        "s_nop 0\n\t"
        "s_cbranch_vccz LB_stok%=\n\t"
        "s_add_u32 s42, s42, 1\n\t"
        "s_cmp_lt_u32 s42, 0x100000\n\t"
        "s_cbranch_scc1 LB_str2%=\n\t"
        "s_movk_i32 s34, 5\n\t"
        "s_branch LB_die%=\n\t"
        "LB_rdfail%=:\n\t"
        "s_or_b32 s47, s47, 1\n\t"
        "buffer_load_dwordx4 v[16:19], v112, s[8:11], s26 offen offset:0 sc1\n\t"
        "buffer_load_dwordx4 v[20:23], v112, s[8:11], s26 offen offset:1024 sc1\n\t"
        "buffer_load_dwordx4 v[24:27], v112, s[8:11], s26 offen offset:2048 sc1\n\t"
        "buffer_load_dwordx4 v[28:31], v112, s[8:11], s26 offen offset:3072 sc1\n\t"
        "s_nop 7\n\t"
        "v_mov_b64_e32 v[0:1], 0\n\t"
        "v_mov_b64_e32 v[2:3], 0\n\t"
        "v_mov_b64_e32 v[4:5], 0\n\t"
        "v_mov_b64_e32 v[6:7], 0\n\t"
        "v_mov_b64_e32 v[8:9], 0\n\t"
        "v_mov_b64_e32 v[10:11], 0\n\t"
        "v_mov_b64_e32 v[12:13], 0\n\t"
        "v_mov_b64_e32 v[14:15], 0\n\t"
        "s_branch LB_rdre%=w\n\t"
        "LB_rdre%=:\n\t"
        "buffer_load_dwordx4 v[16:19], v112, s[8:11], s26 offen offset:0 sc1\n\t"
        "buffer_load_dwordx4 v[20:23], v112, s[8:11], s26 offen offset:1024 sc1\n\t"
        "buffer_load_dwordx4 v[24:27], v112, s[8:11], s26 offen offset:2048 sc1\n\t"
        "buffer_load_dwordx4 v[28:31], v112, s[8:11], s26 offen offset:3072 sc1\n\t"
        "LB_rdre%=w:\n\t"
        "s_waitcnt vmcnt(0) lgkmcnt(0)\n\t"
        "v_max3_u32 v108, v16, v17, v18\n\t"
        "v_max3_u32 v108, v108, v19, v20\n\t"
        "v_max3_u32 v108, v108, v21, v22\n\t"
        "v_max3_u32 v108, v108, v23, v24\n\t"
        "v_max3_u32 v108, v108, v25, v26\n\t"
        "v_max3_u32 v108, v108, v27, v28\n\t"
        "v_max3_u32 v108, v108, v29, v30\n\t"
        "v_max_u32_e32 v108, v108, v31\n\t"
        "v_cmp_eq_u32_e32 vcc, -1, v108\n\t"
        "s_nop 0\n\t"
        "s_cbranch_vccz LB_go%=\n\t"
        "s_add_u32 s42, s42, 1\n\t"
        "s_cmp_lt_u32 s42, 0x100000\n\t"
        "s_cbranch_scc1 LB_rdre%=\n\t"
        "s_movk_i32 s34, 3\n\t"
        "s_branch LB_die%=\n\t"
        "LB_rofail%=:\n\t"
        "s_or_b32 s47, s47, 4\n\t"
        "buffer_load_dwordx4 v[64:67], v115, s[8:11], s26 offen offset:0 sc1\n\t"
        "buffer_load_dwordx4 v[68:71], v115, s[8:11], s26 offen offset:1024 sc1\n\t"
        "buffer_load_dwordx4 v[72:75], v115, s[8:11], s26 offen offset:2048 sc1\n\t"
        "buffer_load_dwordx4 v[76:79], v115, s[8:11], s26 offen offset:3072 sc1\n\t"
        "buffer_load_dwordx4 v[80:83], v115, s[8:11], s46 offen offset:0 sc1\n\t"
        "buffer_load_dwordx4 v[84:87], v115, s[8:11], s46 offen offset:1024 sc1\n\t"
        "buffer_load_dwordx4 v[88:91], v115, s[8:11], s46 offen offset:2048 sc1\n\t"
        "buffer_load_dwordx4 v[92:95], v115, s[8:11], s46 offen offset:3072 sc1\n\t"
        "s_nop 7\n\t"
        "v_mov_b64_e32 v[32:33], 0\n\t"
        "v_mov_b64_e32 v[34:35], 0\n\t"
        "v_mov_b64_e32 v[36:37], 0\n\t"
        "v_mov_b64_e32 v[38:39], 0\n\t"
        "v_mov_b64_e32 v[40:41], 0\n\t"
        "v_mov_b64_e32 v[42:43], 0\n\t"
        "v_mov_b64_e32 v[44:45], 0\n\t"
        "v_mov_b64_e32 v[46:47], 0\n\t"
        "s_branch LB_rore%=w\n\t"
        "LB_rore%=:\n\t"
        "buffer_load_dwordx4 v[64:67], v115, s[8:11], s26 offen offset:0 sc1\n\t"
        "buffer_load_dwordx4 v[68:71], v115, s[8:11], s26 offen offset:1024 sc1\n\t"
        "buffer_load_dwordx4 v[72:75], v115, s[8:11], s26 offen offset:2048 sc1\n\t"
        "buffer_load_dwordx4 v[76:79], v115, s[8:11], s26 offen offset:3072 sc1\n\t"
        "buffer_load_dwordx4 v[80:83], v115, s[8:11], s46 offen offset:0 sc1\n\t"
        "buffer_load_dwordx4 v[84:87], v115, s[8:11], s46 offen offset:1024 sc1\n\t"
        "buffer_load_dwordx4 v[88:91], v115, s[8:11], s46 offen offset:2048 sc1\n\t"
        "buffer_load_dwordx4 v[92:95], v115, s[8:11], s46 offen offset:3072 sc1\n\t"
        "LB_rore%=w:\n\t"
        "s_waitcnt vmcnt(0) lgkmcnt(0)\n\t"
        "v_max3_u32 v108, v64, v65, v66\n\t"
        "v_max3_u32 v108, v108, v67, v68\n\t"
        "v_max3_u32 v108, v108, v69, v70\n\t"
        "v_max3_u32 v108, v108, v71, v72\n\t"
        "v_max3_u32 v108, v108, v73, v74\n\t"
        "v_max3_u32 v108, v108, v75, v76\n\t"
        "v_max3_u32 v108, v108, v77, v78\n\t"
        "v_max3_u32 v108, v108, v79, v80\n\t"
        "v_max3_u32 v108, v108, v81, v82\n\t"
        "v_max3_u32 v108, v108, v83, v84\n\t"
        "v_max3_u32 v108, v108, v85, v86\n\t"
        "v_max3_u32 v108, v108, v87, v88\n\t"
        "v_max3_u32 v108, v108, v89, v90\n\t"
        "v_max3_u32 v108, v108, v91, v92\n\t"
        "v_max3_u32 v108, v108, v93, v94\n\t"
        "v_max_u32_e32 v108, v108, v95\n\t"
        "v_cmp_eq_u32_e32 vcc, -1, v108\n\t"
        "s_nop 0\n\t"
        "s_cbranch_vccz LB_go2%=\n\t"
        "s_add_u32 s42, s42, 1\n\t"
        "s_cmp_lt_u32 s42, 0x100000\n\t"
        "s_cbranch_scc1 LB_rore%=\n\t"
        "s_movk_i32 s34, 4\n\t"
        "s_branch LB_die%=\n\t"
        "LB_die%=:\n\t"
        "v_mov_b32_e32 v30, s34\n\t"
        "s_mov_b32 s35, 33024\n\t"
        "s_mov_b64 exec, 1\n\t"
        "buffer_store_dword v30, off, s[12:15], s35 sc1\n\t"
        "s_endpgm\n\t"
        ".Lk_end_B:\n\t"
        :: REC_S_IN, "{v96}"(g_v), "{v97}"(g_o), "{v98}"(wb0), "{v99}"(wb1), "{v100}"(rb0), "{v101}"(rb0 ^ 32u), "{v102}"(rb0 ^ 64u), "{v103}"(rb0 ^ 96u),
           "{v106}"((l & 1) ? 0xEEEEEEEEu : 0x44444444u), "{v[108:109]}"(m0p),
           "{v112}"(a_mem), "{v113}"((24 + cp) * 16384 + l * 32), "{v114}"(a_vmw), "{v115}"(a_or), "{v116}"(a_mw), "{v117}"(a_sw),
           "{v118}"(lb + w * 2048 + (g >> 1) * 256 + n * 16 + (g & 1) * 8), "{v119}"(lb + (w & 1) * 1024 + l * 16), "{v120}"(lb + LB_R + w * 512 + l * 4),
           "{v121}"(lb + LB_R + l * 8), "{v122}"(stg + l * 16), "{v123}"(o_r), "{v124}"(rb + w == 32 ? 28 * 16384 + (l & 31) * 128 : OOB)
        : "memory", "vcc", "scc", "v0", "v255", "a0", "a255", "s101");
    __builtin_unreachable();
}
__device__ __forceinline__ void rec_helper(const Params& p, rsrc_t r, unsigned lb, const int wv, const int hi) {
    const int tid = otid(), l = tid & 63, xq = hi >> 3;
    const rsrc_t rh = __builtin_amdgcn_make_buffer_rsrc(p.ws + ho_base(p.ws), 0, HO_BYTES, 0x00020000);
    const v4u ones = {0xFFFFFFFFu, 0xFFFFFFFFu, 0xFFFFFFFFu, 0xFFFFFFFFu};
    const bool meas = (hi & 7) < 2;
    if (meas) {
        const unsigned ca = l >= 8 ? OOB : (31 + wv * 32) * 16384 + hi * 256 + l * 16, tend = rtnow() + CAL_T;
        unsigned best0 = ~0u, best1 = ~0u;
#pragma unroll 1
        for (int i = 0; i < 64 && (i < 3 || (int)(rtnow() - tend) < 0); ++i) {
            const unsigned t0 = (unsigned)__builtin_amdgcn_s_memtime(); __builtin_amdgcn_s_waitcnt(0);
            st16(rh, ca, ones); DRAIN();
            const unsigned t1 = (unsigned)__builtin_amdgcn_s_memtime(); __builtin_amdgcn_s_waitcnt(0);
            st16(rh, ca + 8192, ones); DRAIN();
            const unsigned t2 = (unsigned)__builtin_amdgcn_s_memtime(); __builtin_amdgcn_s_waitcnt(0);
            best0 = min(best0, t1 - t0); best1 = min(best1, t2 - t1); }
        if (l < 2) __hip_atomic_fetch_max((unsigned*)(p.ws + F_CAL + 128) + l, 65535u - min(l ? best1 : best0, 65535u), __ATOMIC_RELAXED, __HIP_MEMORY_SCOPE_AGENT);
    }
    auto touch = [&](int z0, int z1) {
#pragma unroll 1
        for (int z = z0 + ((hi & 7) - 2) * 4 + wv; z < z1; z += 24) { const bf16_t* q;
            if (z < 48) q = ((z & 1) ? p.Wk : p.Wq) + (size_t)(((((z & 1) * 2 + 1) * 4 + 4 + (z >> 2)) & 15) * 64 + l) * 4096 + (xq * 8 + 1 + (z & 2) * 2) * 128 + 64;
            else if (z < 112) { const int y = z - 48; q = p.Wo + (size_t)(((y & 3) * 8 + (y >> 3)) * 128 + ((y >> 2) & 1) * 64 + l) * 1024 + (xq & 1) * 512 + 192; }
            else { const int s7 = (z - 112) % 7, y = (z - 112) / 7; q = p.Wo + (size_t)(((y & 3) * 8 + 8 - T2U + (y >> 3)) * 128 + ((y >> 2) & 1) * 64 + l) * 1024 + (xq & 1) * 512 + (s7 + (s7 >= 3)) * 64; }
            touch_l2(q, lb + 1024); } };
    if (!meas) touch(0, TPRE);
    st16(r, tid < 64 ? F_KVQ + hi * 1024 + tid * 16 : tid < 72 ? F_PROG + hi * 128 + (tid - 64) * 16 : tid < 80 ? F_CVY + hi * 128 + (tid - 72) * 16 : OOB, (v4u){0u, 0u, 0u, 0u});
#pragma unroll
    for (int i = 0; i < 2; ++i) st16(r, W_OX + 63 * 262144 + hi * 8192 + i * 4096 + tid * 16, ones);
#pragma unroll
    for (int i = 0; i < 16; ++i) st16(rh, hi * 65536 + i * 4096 + tid * 16, ones);
    DRAIN();
    __syncthreads();
    if (tid == 0 && __hip_atomic_fetch_add((unsigned*)(p.ws + F_CAL + 256), 1u, __ATOMIC_RELAXED, __HIP_MEMORY_SCOPE_AGENT) == 31u) {
        const v2u c = ld8(r, F_CAL + 128); st4(r, F_CAL, c.x < c.y ? 8193u : 1u); }
    if (!meas) touch(TPRE, 112 + 56 * T2U);
    DRAIN();
}
constexpr int CV_RP = 80;
constexpr int CV_U = 96 * CV_RP;
constexpr int CV_KP = 4416;
constexpr int CV_K0 = 8 * CV_U;
#define NAP() do { __builtin_amdgcn_sched_barrier(0); __builtin_amdgcn_s_sleep(XSLP); __builtin_amdgcn_sched_barrier(0); } while (0)
__device__ __forceinline__ void gate_begin(const Params& p, const int jb, const int wv, v4u (&xg)[4][2], v4u (&gw)[2]) {
    const int l = otid() & 63, t0 = (jb * 4 + wv) * 4;
#pragma unroll
    for (int q = 0; q < 2; ++q) gw[q] = *(const v4u*)(p.gate_w + q * 512 + l * 8);
#pragma unroll
    for (int i = 0; i < 4; ++i)
#pragma unroll
        for (int q = 0; q < 2; ++q) { xg[i][q] = *(const v4u*)(p.Aq + (size_t)(t0 + i) * D + q * 512 + l * 8); NAP(); }
}
__device__ __forceinline__ void gate_end(rsrc_t r, const int jb, const int wv, const v4u (&xg)[4][2], const v4u (&gw)[2]) {
    const int l = otid() & 63;
    float mine = 0.f;
#pragma unroll
    for (int i = 0; i < 4; ++i) { float acc = 0.f;
#pragma unroll
        for (int q = 0; q < 2; ++q)
#pragma unroll
            for (int e = 0; e < 4; ++e) { const unsigned pr = pack2(blo(xg[i][q][e]) * blo(gw[q][e]), bhi(xg[i][q][e]) * bhi(gw[q][e])); acc += blo(pr); acc += bhi(pr); }
        for (int o = 32; o > 0; o >>= 1) acc += __shfl_xor(acc, o);
        if ((l & 3) == i) mine = acc; }
    if (l < 32) { const float z = rbfh(mine); st4(r, W_GATE + (jb * 4 + wv) * 128 + l * 4, __float_as_uint(rbfh(1.f / (1.f + expf(-z))))); }
}
__device__ __forceinline__ void conv_prep(const Params& p, rsrc_t r, const int wv, const int hi, const int G, const int jb) {
    const int tid = otid(), Q = tid >> 2, cg = tid & 3, ch0 = hi * 32 + cg * 8;
    v4u sk[4]; float sv[8][4];
#pragma unroll
    for (int q = 0; q < 4; ++q) sk[q] = *(const v4u*)(p.shift_k + ch0 * 4 + q * 8);
#pragma unroll
    for (int c8 = 0; c8 < 8; ++c8)
#pragma unroll
        for (int tp = 0; tp < 4; ++tp) { const unsigned sw = sk[c8 >> 1][(c8 & 1) * 2 + (tp >> 1)]; sv[c8][tp] = (tp & 1) ? bhi(sw) : blo(sw); }
    const rsrc_t rk = __builtin_amdgcn_make_buffer_rsrc((void*)p.Ak, 0, S * D * 2, 0x00020000), rv = __builtin_amdgcn_make_buffer_rsrc((void*)p.Av, 0, S * D * 2, 0x00020000),
                 rq = __builtin_amdgcn_make_buffer_rsrc((void*)p.Aq, 0, S * D * 2, 0x00020000);
    {
        const unsigned ro = (unsigned)((Q * 32 + G * 8) * D + ch0) * 2u, uo = hi * 131072 + tid * 16;
        v4u kr[11], u[8], qv[8], xg[4][2], gw[2];
        gate_begin(p, jb, wv, xg, gw);
#pragma unroll
        for (int i = 0; i < 11; ++i) { kr[i] = __builtin_amdgcn_raw_buffer_load_b128(rk, (i < 3 && Q + G == 0) ? OOB : ro + (i - 3) * 2048, 0, 2); NAP(); }
#pragma unroll
        for (int i = 0; i < 8; ++i) { u[i] = __builtin_amdgcn_raw_buffer_load_b128(rv, ro + i * 2048, 0, 2); NAP(); }
#pragma unroll
        for (int i = 0; i < 8; ++i) { qv[i] = __builtin_amdgcn_raw_buffer_load_b128(rq, ro + i * 2048, 0, 0); NAP(); }
        gate_end(r, jb, wv, xg, gw);
        __builtin_amdgcn_sched_barrier(0);
#pragma unroll
        for (int q = 0; q < 4; ++q) {
#pragma unroll
            for (int i = 0; i < 8; ++i) {
                unsigned ks = pack2(blo(kr[i][q]) * sv[2 * q][3], bhi(kr[i][q]) * sv[2 * q + 1][3]), pr;
#pragma unroll
                for (int d = 1; d < 4; ++d) { pr = pack2(blo(kr[i + d][q]) * sv[2 * q][3 - d], bhi(kr[i + d][q]) * sv[2 * q + 1][3 - d]); ks = pack2(blo(ks) + blo(pr), bhi(ks) + bhi(pr)); }
                u[i][q] = pack2(blo(ks) * blo(u[i][q]), bhi(ks) * bhi(u[i][q])); }
            v4u lo, hh;
#pragma unroll
            for (int z = 0; z < 4; ++z) { const unsigned x = u[7 - 2 * z][q], y = u[6 - 2 * z][q]; lo[z] = __builtin_amdgcn_perm(y, x, 0x05040100u); hh[z] = __builtin_amdgcn_perm(y, x, 0x07060302u); }
            st16(r, W_U + uo + q * 32768 + G * 4096, lo); NAP(); st16(r, W_U + uo + q * 32768 + 16384 + G * 4096, hh); NAP();
#pragma unroll
            for (int z = 0; z < 2; ++z) { st16(r, W_Y + uo + q * 32768 + (2 * G + z) * 4096, (v4u){qv[4 * z][q], qv[4 * z + 1][q], qv[4 * z + 2][q], qv[4 * z + 3][q]}); NAP(); }
        }
    }
    DRAIN(); __syncthreads();
    if (tid == 0) __hip_atomic_fetch_add((unsigned*)(p.ws + F_CONV), 1u, __ATOMIC_RELAXED, __HIP_MEMORY_SCOPE_AGENT);
    if (tid == 0) __hip_atomic_fetch_add((unsigned*)(p.ws + F_CVY + hi * 128 + 64), 1u, __ATOMIC_RELAXED, __HIP_MEMORY_SCOPE_AGENT);
}
__device__ __forceinline__ bool conv_core(const Params& p, rsrc_t r, unsigned char* lds, const int wv, const int un, const int nt, const int ro) {
    const int tid = otid(), w = wv, l = tid & 63, Q = tid >> 2, cg = tid & 3;
    const rsrc_t rs = __builtin_amdgcn_make_buffer_rsrc((void*)p.ssm_k, 0, S * D * 2, 0x00020000);
    v4u kraw[4]; v2u kext[4];
    auto kload = [&](int n) { const unsigned o = (unsigned)(((un >> 2) * 32 + w * 8 + (un & 3) * 2 + n) * S + l * 8) * 2u;
#pragma unroll
        for (int i = 0; i < 4; ++i) { kraw[i] = __builtin_amdgcn_raw_buffer_load_b128(rs, o + i * 1024, 0, 2);
            kext[i] = __builtin_amdgcn_raw_buffer_load_b64(rs, (i == 3 && l == 63) ? OOB : o + i * 1024 + 16, 0, 2); } };
    kload(0);
    for (int i = tid; i < 8 * 160; i += 256) *(v4u*)(lds + (i / 160) * CV_U + (i % 160) * 16) = (v4u){0u, 0u, 0u, 0u};
    unsigned char* kc = lds + CV_K0 + w * 4 * CV_KP;
    if (l < 32) {
#pragma unroll
        for (int q = 0; q < 4; ++q) *(unsigned*)(kc + q * CV_KP + l * 4) = 0u; }
    if (!wait_word(r, F_CVY + (un >> 2) * 128 + 64, 4u, 2048u)) return false;
    v4u ur[8], qr[8];
#pragma unroll
    for (int i = 0; i < 8; ++i) ur[i] = ld16(r, W_U + un * 32768 + i * 4096 + tid * 16);
#pragma unroll
    for (int i = 0; i < 8; ++i) qr[i] = ld16(r, W_Y + un * 32768 + i * 4096 + tid * 16);
    const unsigned dv = *(const unsigned*)(p.ssm_D + (un >> 2) * 32 + cg * 8 + (un & 3) * 2);
    const int b = l & 31, kg = l >> 5;
    const unsigned kb = (unsigned)(size_t)kc + (b & 3) * CV_KP + ((b & 28) - 8 * kg + 56 - 16) * 2, ub0 = (unsigned)(size_t)lds + (1 + b) * CV_RP + kg * 16;
    unsigned char* const urow = lds + 2 * cg * CV_U + (32 + Q) * CV_RP;
#pragma unroll
    for (int i = 0; i < 8; ++i) *(v4u*)(urow + (i >> 2) * CV_U + (i & 3) * 16) = ur[i];
    __syncthreads();
    const v4u so = mk_srd(p.Wo, 1024 * 4096 * 2); const unsigned go = (w * 1024 + (l >> 3)) * 2048 + nt * 128 + (l & 7) * 16; unsigned t0, t1;
#pragma unroll
        for (int ci = 0; ci < 2; ++ci) {
            const int c8 = 2 * w + ci;
#pragma unroll
            for (int i = 0; i < 4; ++i) { const v4u e = kraw[i]; const v2u f = kext[i]; unsigned char* q = kc + 128 + (i * 64 + l) * 16;
                const unsigned s0 = __builtin_amdgcn_alignbit(e.y, e.x, 16), s1 = __builtin_amdgcn_alignbit(e.z, e.y, 16), s2 = __builtin_amdgcn_alignbit(e.w, e.z, 16),
                               s3 = __builtin_amdgcn_alignbit(f.x, e.w, 16), s4 = __builtin_amdgcn_alignbit(f.y, f.x, 16);
                *(v4u*)q = (v4u){s0, s1, s2, s3}; *(v4u*)(q + CV_KP) = (v4u){e.y, e.z, e.w, f.x}; *(v4u*)(q + 2 * CV_KP) = (v4u){s1, s2, s3, s4}; *(v4u*)(q + 3 * CV_KP) = (v4u){e.z, e.w, f.x, f.y};
                if (i == 0 && l == 0) {
                    *(v2u*)(q - 8) = (v2u){0u, e.x << 16}; *(v2u*)(q + CV_KP - 8) = (v2u){0u, e.x}; *(v2u*)(q + 2 * CV_KP - 8) = (v2u){e.x << 16, s0}; *(v2u*)(q + 3 * CV_KP - 8) = (v2u){e.x, e.y}; } }
            if (ci == 0) kload(1);
            __builtin_amdgcn_wave_barrier();
            f32x16 acc0, acc1;
            if (ci == 0) asm volatile(
                "ds_read_b64 v[192:193], %[kb] offset:32\n\t"
                "ds_read_b64 v[194:195], %[kb] offset:40\n\t"
                "ds_read_b128 v[196:199], %[ub] offset:5040\n\t"
                "ds_read_b128 v[200:203], %[ub] offset:2480\n\t"
                "ds_read_b64 v[204:205], %[kb] offset:2080\n\t"
                "ds_read_b64 v[206:207], %[kb] offset:2088\n\t"
                "ds_read_b64 v[208:209], %[kb] offset:0\n\t"
                "ds_read_b64 v[210:211], %[kb] offset:8\n\t"
                "ds_read_b128 v[212:215], %[ub] offset:5072\n\t"
                "ds_read_b128 v[216:219], %[ub] offset:2512\n\t"
                "ds_read_b64 v[220:221], %[kb] offset:2048\n\t"
                "ds_read_b64 v[222:223], %[kb] offset:2056\n\t"
                "s_waitcnt lgkmcnt(9)\n\t"
                "v_mfma_f32_32x32x16_bf16 %[a1], v[192:195], v[196:199], 0\n\t"
                "ds_read_b64 v[224:225], %[kb] offset:96\n\t"
                "ds_read_b64 v[226:227], %[kb] offset:104\n\t"
                "ds_read_b128 v[228:231], %[ub] offset:4960\n\t"
                "s_waitcnt lgkmcnt(11)\n\t"
                "v_mfma_f32_32x32x16_bf16 %[a0], v[192:195], v[200:203], 0\n\t"
                "ds_read_b128 v[232:235], %[ub] offset:2400\n\t"
                "s_add_u32 %[t1], %[ro], 0\n\t"
                "s_and_b32 %[t1], %[t1], 7\n\t"
                "s_lshl_b32 %[t1], %[t1], 18\n\t"
                "s_add_u32 %[t0], %[t1], 0\n\t"
                "buffer_load_dwordx4 a[0:3], %[go], %[so], %[t0] offen\n\t"
                "s_waitcnt lgkmcnt(10)\n\t"
                "v_mfma_f32_32x32x16_bf16 %[a1], v[204:207], v[200:203], %[a1]\n\t"
                "ds_read_b64 v[236:237], %[kb] offset:2144\n\t"
                "ds_read_b64 v[238:239], %[kb] offset:2152\n\t"
                "s_waitcnt lgkmcnt(9)\n\t"
                "v_mfma_f32_32x32x16_bf16 %[a1], v[208:211], v[212:215], %[a1]\n\t"
                "ds_read_b64 v[240:241], %[kb] offset:64\n\t"
                "ds_read_b64 v[242:243], %[kb] offset:72\n\t"
                "ds_read_b128 v[244:247], %[ub] offset:4992\n\t"
                "s_waitcnt lgkmcnt(11)\n\t"
                "v_mfma_f32_32x32x16_bf16 %[a0], v[208:211], v[216:219], %[a0]\n\t"
                "ds_read_b128 v[248:251], %[ub] offset:2432\n\t"
                "s_waitcnt lgkmcnt(10)\n\t"
                "v_mfma_f32_32x32x16_bf16 %[a1], v[220:223], v[216:219], %[a1]\n\t"
                "ds_read_b64 v[252:253], %[kb] offset:2112\n\t"
                "ds_read_b64 v[254:255], %[kb] offset:2120\n\t"
                "s_waitcnt lgkmcnt(9)\n\t"
                "v_mfma_f32_32x32x16_bf16 %[a1], v[224:227], v[228:231], %[a1]\n\t"
                "ds_read_b64 v[192:193], %[kb] offset:160\n\t"
                "ds_read_b64 v[194:195], %[kb] offset:168\n\t"
                "ds_read_b128 v[196:199], %[ub] offset:4880\n\t"
                "s_waitcnt lgkmcnt(11)\n\t"
                "v_mfma_f32_32x32x16_bf16 %[a0], v[224:227], v[232:235], %[a0]\n\t"
                "ds_read_b128 v[200:203], %[ub] offset:2320\n\t"
                "s_add_u32 %[t0], %[t1], 16384\n\t"
                "buffer_load_dwordx4 a[4:7], %[go], %[so], %[t0] offen\n\t"
                "s_waitcnt lgkmcnt(10)\n\t"
                "v_mfma_f32_32x32x16_bf16 %[a1], v[236:239], v[232:235], %[a1]\n\t"
                "ds_read_b64 v[204:205], %[kb] offset:2208\n\t"
                "ds_read_b64 v[206:207], %[kb] offset:2216\n\t"
                "s_waitcnt lgkmcnt(9)\n\t"
                "v_mfma_f32_32x32x16_bf16 %[a1], v[240:243], v[244:247], %[a1]\n\t"
                "ds_read_b64 v[208:209], %[kb] offset:128\n\t"
                "ds_read_b64 v[210:211], %[kb] offset:136\n\t"
                "ds_read_b128 v[212:215], %[ub] offset:4912\n\t"
                "s_waitcnt lgkmcnt(11)\n\t"
                "v_mfma_f32_32x32x16_bf16 %[a0], v[240:243], v[248:251], %[a0]\n\t"
                "ds_read_b128 v[216:219], %[ub] offset:2352\n\t"
                "s_waitcnt lgkmcnt(10)\n\t"
                "v_mfma_f32_32x32x16_bf16 %[a1], v[252:255], v[248:251], %[a1]\n\t"
                "ds_read_b64 v[220:221], %[kb] offset:2176\n\t"
                "ds_read_b64 v[222:223], %[kb] offset:2184\n\t"
                "s_waitcnt lgkmcnt(9)\n\t"
                "v_mfma_f32_32x32x16_bf16 %[a1], v[192:195], v[196:199], %[a1]\n\t"
                "ds_read_b64 v[224:225], %[kb] offset:224\n\t"
                "ds_read_b64 v[226:227], %[kb] offset:232\n\t"
                "ds_read_b128 v[228:231], %[ub] offset:4800\n\t"
                "s_waitcnt lgkmcnt(11)\n\t"
                "v_mfma_f32_32x32x16_bf16 %[a0], v[192:195], v[200:203], %[a0]\n\t"
                "ds_read_b128 v[232:235], %[ub] offset:2240\n\t"
                "s_add_u32 %[t0], %[t1], 32768\n\t"
                "buffer_load_dwordx4 a[8:11], %[go], %[so], %[t0] offen\n\t"
                "s_waitcnt lgkmcnt(10)\n\t"
                "v_mfma_f32_32x32x16_bf16 %[a1], v[204:207], v[200:203], %[a1]\n\t"
                "ds_read_b64 v[236:237], %[kb] offset:2272\n\t"
                "ds_read_b64 v[238:239], %[kb] offset:2280\n\t"
                "s_waitcnt lgkmcnt(9)\n\t"
                "v_mfma_f32_32x32x16_bf16 %[a1], v[208:211], v[212:215], %[a1]\n\t"
                "ds_read_b64 v[240:241], %[kb] offset:192\n\t"
                "ds_read_b64 v[242:243], %[kb] offset:200\n\t"
                "ds_read_b128 v[244:247], %[ub] offset:4832\n\t"
                "s_waitcnt lgkmcnt(11)\n\t"
                "v_mfma_f32_32x32x16_bf16 %[a0], v[208:211], v[216:219], %[a0]\n\t"
                "ds_read_b128 v[248:251], %[ub] offset:2272\n\t"
                "s_waitcnt lgkmcnt(10)\n\t"
                "v_mfma_f32_32x32x16_bf16 %[a1], v[220:223], v[216:219], %[a1]\n\t"
                "ds_read_b64 v[252:253], %[kb] offset:2240\n\t"
                "ds_read_b64 v[254:255], %[kb] offset:2248\n\t"
                "s_waitcnt lgkmcnt(9)\n\t"
                "v_mfma_f32_32x32x16_bf16 %[a1], v[224:227], v[228:231], %[a1]\n\t"
                "ds_read_b64 v[192:193], %[kb] offset:288\n\t"
                "ds_read_b64 v[194:195], %[kb] offset:296\n\t"
                "ds_read_b128 v[196:199], %[ub] offset:4720\n\t"
                "s_waitcnt lgkmcnt(11)\n\t"
                "v_mfma_f32_32x32x16_bf16 %[a0], v[224:227], v[232:235], %[a0]\n\t"
                "ds_read_b128 v[200:203], %[ub] offset:2160\n\t"
                "s_add_u32 %[t0], %[t1], 49152\n\t"
                "buffer_load_dwordx4 a[12:15], %[go], %[so], %[t0] offen\n\t"
                "s_waitcnt lgkmcnt(10)\n\t"
                "v_mfma_f32_32x32x16_bf16 %[a1], v[236:239], v[232:235], %[a1]\n\t"
                "ds_read_b64 v[204:205], %[kb] offset:2336\n\t"
                "ds_read_b64 v[206:207], %[kb] offset:2344\n\t"
                "s_waitcnt lgkmcnt(9)\n\t"
                "v_mfma_f32_32x32x16_bf16 %[a1], v[240:243], v[244:247], %[a1]\n\t"
                "ds_read_b64 v[208:209], %[kb] offset:256\n\t"
                "ds_read_b64 v[210:211], %[kb] offset:264\n\t"
                "ds_read_b128 v[212:215], %[ub] offset:4752\n\t"
                "s_waitcnt lgkmcnt(11)\n\t"
                "v_mfma_f32_32x32x16_bf16 %[a0], v[240:243], v[248:251], %[a0]\n\t"
                "ds_read_b128 v[216:219], %[ub] offset:2192\n\t"
                "s_add_u32 %[t0], %[t1], 65536\n\t"
                "buffer_load_dwordx4 a[16:19], %[go], %[so], %[t0] offen\n\t"
                "s_waitcnt lgkmcnt(10)\n\t"
                "v_mfma_f32_32x32x16_bf16 %[a1], v[252:255], v[248:251], %[a1]\n\t"
                "ds_read_b64 v[220:221], %[kb] offset:2304\n\t"
                "ds_read_b64 v[222:223], %[kb] offset:2312\n\t"
                "s_waitcnt lgkmcnt(9)\n\t"
                "v_mfma_f32_32x32x16_bf16 %[a1], v[192:195], v[196:199], %[a1]\n\t"
                "ds_read_b64 v[224:225], %[kb] offset:352\n\t"
                "ds_read_b64 v[226:227], %[kb] offset:360\n\t"
                "ds_read_b128 v[228:231], %[ub] offset:4640\n\t"
                "s_waitcnt lgkmcnt(11)\n\t"
                "v_mfma_f32_32x32x16_bf16 %[a0], v[192:195], v[200:203], %[a0]\n\t"
                "ds_read_b128 v[232:235], %[ub] offset:2080\n\t"
                "s_waitcnt lgkmcnt(10)\n\t"
                "v_mfma_f32_32x32x16_bf16 %[a1], v[204:207], v[200:203], %[a1]\n\t"
                "ds_read_b64 v[236:237], %[kb] offset:2400\n\t"
                "ds_read_b64 v[238:239], %[kb] offset:2408\n\t"
                "s_waitcnt lgkmcnt(9)\n\t"
                "v_mfma_f32_32x32x16_bf16 %[a1], v[208:211], v[212:215], %[a1]\n\t"
                "ds_read_b64 v[240:241], %[kb] offset:320\n\t"
                "ds_read_b64 v[242:243], %[kb] offset:328\n\t"
                "ds_read_b128 v[244:247], %[ub] offset:4672\n\t"
                "s_waitcnt lgkmcnt(11)\n\t"
                "v_mfma_f32_32x32x16_bf16 %[a0], v[208:211], v[216:219], %[a0]\n\t"
                "ds_read_b128 v[248:251], %[ub] offset:2112\n\t"
                "s_add_u32 %[t0], %[t1], 81920\n\t"
                "buffer_load_dwordx4 a[20:23], %[go], %[so], %[t0] offen\n\t"
                "s_waitcnt lgkmcnt(10)\n\t"
                "v_mfma_f32_32x32x16_bf16 %[a1], v[220:223], v[216:219], %[a1]\n\t"
                "ds_read_b64 v[252:253], %[kb] offset:2368\n\t"
                "ds_read_b64 v[254:255], %[kb] offset:2376\n\t"
                "s_waitcnt lgkmcnt(9)\n\t"
                "v_mfma_f32_32x32x16_bf16 %[a1], v[224:227], v[228:231], %[a1]\n\t"
                "ds_read_b64 v[192:193], %[kb] offset:416\n\t"
                "ds_read_b64 v[194:195], %[kb] offset:424\n\t"
                "ds_read_b128 v[196:199], %[ub] offset:4560\n\t"
                "s_waitcnt lgkmcnt(11)\n\t"
                "v_mfma_f32_32x32x16_bf16 %[a0], v[224:227], v[232:235], %[a0]\n\t"
                "ds_read_b128 v[200:203], %[ub] offset:2000\n\t"
                "s_waitcnt lgkmcnt(10)\n\t"
                "v_mfma_f32_32x32x16_bf16 %[a1], v[236:239], v[232:235], %[a1]\n\t"
                "ds_read_b64 v[204:205], %[kb] offset:2464\n\t"
                "ds_read_b64 v[206:207], %[kb] offset:2472\n\t"
                "s_waitcnt lgkmcnt(9)\n\t"
                "v_mfma_f32_32x32x16_bf16 %[a1], v[240:243], v[244:247], %[a1]\n\t"
                "ds_read_b64 v[208:209], %[kb] offset:384\n\t"
                "ds_read_b64 v[210:211], %[kb] offset:392\n\t"
                "ds_read_b128 v[212:215], %[ub] offset:4592\n\t"
                "s_waitcnt lgkmcnt(11)\n\t"
                "v_mfma_f32_32x32x16_bf16 %[a0], v[240:243], v[248:251], %[a0]\n\t"
                "ds_read_b128 v[216:219], %[ub] offset:2032\n\t"
                "s_add_u32 %[t0], %[t1], 98304\n\t"
                "buffer_load_dwordx4 a[24:27], %[go], %[so], %[t0] offen\n\t"
                "s_waitcnt lgkmcnt(10)\n\t"
                "v_mfma_f32_32x32x16_bf16 %[a1], v[252:255], v[248:251], %[a1]\n\t"
                "ds_read_b64 v[220:221], %[kb] offset:2432\n\t"
                "ds_read_b64 v[222:223], %[kb] offset:2440\n\t"
                "s_waitcnt lgkmcnt(9)\n\t"
                "v_mfma_f32_32x32x16_bf16 %[a1], v[192:195], v[196:199], %[a1]\n\t"
                "ds_read_b64 v[224:225], %[kb] offset:480\n\t"
                "ds_read_b64 v[226:227], %[kb] offset:488\n\t"
                "ds_read_b128 v[228:231], %[ub] offset:4480\n\t"
                "s_waitcnt lgkmcnt(11)\n\t"
                "v_mfma_f32_32x32x16_bf16 %[a0], v[192:195], v[200:203], %[a0]\n\t"
                "ds_read_b128 v[232:235], %[ub] offset:1920\n\t"
                "s_waitcnt lgkmcnt(10)\n\t"
                "v_mfma_f32_32x32x16_bf16 %[a1], v[204:207], v[200:203], %[a1]\n\t"
                "ds_read_b64 v[236:237], %[kb] offset:2528\n\t"
                "ds_read_b64 v[238:239], %[kb] offset:2536\n\t"
                "s_waitcnt lgkmcnt(9)\n\t"
                "v_mfma_f32_32x32x16_bf16 %[a1], v[208:211], v[212:215], %[a1]\n\t"
                "ds_read_b64 v[240:241], %[kb] offset:448\n\t"
                "ds_read_b64 v[242:243], %[kb] offset:456\n\t"
                "ds_read_b128 v[244:247], %[ub] offset:4512\n\t"
                "s_waitcnt lgkmcnt(11)\n\t"
                "v_mfma_f32_32x32x16_bf16 %[a0], v[208:211], v[216:219], %[a0]\n\t"
                "ds_read_b128 v[248:251], %[ub] offset:1952\n\t"
                "s_add_u32 %[t0], %[t1], 114688\n\t"
                "buffer_load_dwordx4 a[28:31], %[go], %[so], %[t0] offen\n\t"
                "s_waitcnt lgkmcnt(10)\n\t"
                "v_mfma_f32_32x32x16_bf16 %[a1], v[220:223], v[216:219], %[a1]\n\t"
                "ds_read_b64 v[252:253], %[kb] offset:2496\n\t"
                "ds_read_b64 v[254:255], %[kb] offset:2504\n\t"
                "s_waitcnt lgkmcnt(9)\n\t"
                "v_mfma_f32_32x32x16_bf16 %[a1], v[224:227], v[228:231], %[a1]\n\t"
                "ds_read_b64 v[192:193], %[kb] offset:544\n\t"
                "ds_read_b64 v[194:195], %[kb] offset:552\n\t"
                "ds_read_b128 v[196:199], %[ub] offset:4400\n\t"
                "s_waitcnt lgkmcnt(11)\n\t"
                "v_mfma_f32_32x32x16_bf16 %[a0], v[224:227], v[232:235], %[a0]\n\t"
                "ds_read_b128 v[200:203], %[ub] offset:1840\n\t"
                "s_add_u32 %[t1], %[ro], 0\n\t"
                "s_and_b32 %[t1], %[t1], 7\n\t"
                "s_lshl_b32 %[t1], %[t1], 18\n\t"
                "s_add_u32 %[t0], %[t1], 131072\n\t"
                "buffer_load_dwordx4 a[32:35], %[go], %[so], %[t0] offen\n\t"
                "s_waitcnt lgkmcnt(10)\n\t"
                "v_mfma_f32_32x32x16_bf16 %[a1], v[236:239], v[232:235], %[a1]\n\t"
                "ds_read_b64 v[204:205], %[kb] offset:2592\n\t"
                "ds_read_b64 v[206:207], %[kb] offset:2600\n\t"
                "s_waitcnt lgkmcnt(9)\n\t"
                "v_mfma_f32_32x32x16_bf16 %[a1], v[240:243], v[244:247], %[a1]\n\t"
                "ds_read_b64 v[208:209], %[kb] offset:512\n\t"
                "ds_read_b64 v[210:211], %[kb] offset:520\n\t"
                "ds_read_b128 v[212:215], %[ub] offset:4432\n\t"
                "s_waitcnt lgkmcnt(11)\n\t"
                "v_mfma_f32_32x32x16_bf16 %[a0], v[240:243], v[248:251], %[a0]\n\t"
                "ds_read_b128 v[216:219], %[ub] offset:1872\n\t"
                "s_waitcnt lgkmcnt(10)\n\t"
                "v_mfma_f32_32x32x16_bf16 %[a1], v[252:255], v[248:251], %[a1]\n\t"
                "ds_read_b64 v[220:221], %[kb] offset:2560\n\t"
                "ds_read_b64 v[222:223], %[kb] offset:2568\n\t"
                "s_waitcnt lgkmcnt(9)\n\t"
                "v_mfma_f32_32x32x16_bf16 %[a1], v[192:195], v[196:199], %[a1]\n\t"
                "ds_read_b64 v[224:225], %[kb] offset:608\n\t"
                "ds_read_b64 v[226:227], %[kb] offset:616\n\t"
                "ds_read_b128 v[228:231], %[ub] offset:4320\n\t"
                "s_waitcnt lgkmcnt(11)\n\t"
                "v_mfma_f32_32x32x16_bf16 %[a0], v[192:195], v[200:203], %[a0]\n\t"
                "ds_read_b128 v[232:235], %[ub] offset:1760\n\t"
                "s_add_u32 %[t0], %[t1], 147456\n\t"
                "buffer_load_dwordx4 a[36:39], %[go], %[so], %[t0] offen\n\t"
                "s_waitcnt lgkmcnt(10)\n\t"
                "v_mfma_f32_32x32x16_bf16 %[a1], v[204:207], v[200:203], %[a1]\n\t"
                "ds_read_b64 v[236:237], %[kb] offset:2656\n\t"
                "ds_read_b64 v[238:239], %[kb] offset:2664\n\t"
                "s_waitcnt lgkmcnt(9)\n\t"
                "v_mfma_f32_32x32x16_bf16 %[a1], v[208:211], v[212:215], %[a1]\n\t"
                "ds_read_b64 v[240:241], %[kb] offset:576\n\t"
                "ds_read_b64 v[242:243], %[kb] offset:584\n\t"
                "ds_read_b128 v[244:247], %[ub] offset:4352\n\t"
                "s_waitcnt lgkmcnt(11)\n\t"
                "v_mfma_f32_32x32x16_bf16 %[a0], v[208:211], v[216:219], %[a0]\n\t"
                "ds_read_b128 v[248:251], %[ub] offset:1792\n\t"
                "s_waitcnt lgkmcnt(10)\n\t"
                "v_mfma_f32_32x32x16_bf16 %[a1], v[220:223], v[216:219], %[a1]\n\t"
                "ds_read_b64 v[252:253], %[kb] offset:2624\n\t"
                "ds_read_b64 v[254:255], %[kb] offset:2632\n\t"
                "s_waitcnt lgkmcnt(9)\n\t"
                "v_mfma_f32_32x32x16_bf16 %[a1], v[224:227], v[228:231], %[a1]\n\t"
                "ds_read_b64 v[192:193], %[kb] offset:672\n\t"
                "ds_read_b64 v[194:195], %[kb] offset:680\n\t"
                "ds_read_b128 v[196:199], %[ub] offset:4240\n\t"
                "s_waitcnt lgkmcnt(11)\n\t"
                "v_mfma_f32_32x32x16_bf16 %[a0], v[224:227], v[232:235], %[a0]\n\t"
                "ds_read_b128 v[200:203], %[ub] offset:1680\n\t"
                "s_add_u32 %[t0], %[t1], 163840\n\t"
                "buffer_load_dwordx4 a[40:43], %[go], %[so], %[t0] offen\n\t"
                "s_waitcnt lgkmcnt(10)\n\t"
                "v_mfma_f32_32x32x16_bf16 %[a1], v[236:239], v[232:235], %[a1]\n\t"
                "ds_read_b64 v[204:205], %[kb] offset:2720\n\t"
                "ds_read_b64 v[206:207], %[kb] offset:2728\n\t"
                "s_waitcnt lgkmcnt(9)\n\t"
                "v_mfma_f32_32x32x16_bf16 %[a1], v[240:243], v[244:247], %[a1]\n\t"
                "ds_read_b64 v[208:209], %[kb] offset:640\n\t"
                "ds_read_b64 v[210:211], %[kb] offset:648\n\t"
                "ds_read_b128 v[212:215], %[ub] offset:4272\n\t"
                "s_waitcnt lgkmcnt(11)\n\t"
                "v_mfma_f32_32x32x16_bf16 %[a0], v[240:243], v[248:251], %[a0]\n\t"
                "ds_read_b128 v[216:219], %[ub] offset:1712\n\t"
                "s_waitcnt lgkmcnt(10)\n\t"
                "v_mfma_f32_32x32x16_bf16 %[a1], v[252:255], v[248:251], %[a1]\n\t"
                "ds_read_b64 v[220:221], %[kb] offset:2688\n\t"
                "ds_read_b64 v[222:223], %[kb] offset:2696\n\t"
                "s_waitcnt lgkmcnt(9)\n\t"
                "v_mfma_f32_32x32x16_bf16 %[a1], v[192:195], v[196:199], %[a1]\n\t"
                "ds_read_b64 v[224:225], %[kb] offset:736\n\t"
                "ds_read_b64 v[226:227], %[kb] offset:744\n\t"
                "ds_read_b128 v[228:231], %[ub] offset:4160\n\t"
                "s_waitcnt lgkmcnt(11)\n\t"
                "v_mfma_f32_32x32x16_bf16 %[a0], v[192:195], v[200:203], %[a0]\n\t"
                "ds_read_b128 v[232:235], %[ub] offset:1600\n\t"
                "s_add_u32 %[t0], %[t1], 180224\n\t"
                "buffer_load_dwordx4 a[44:47], %[go], %[so], %[t0] offen\n\t"
                "s_waitcnt lgkmcnt(10)\n\t"
                "v_mfma_f32_32x32x16_bf16 %[a1], v[204:207], v[200:203], %[a1]\n\t"
                "ds_read_b64 v[236:237], %[kb] offset:2784\n\t"
                "ds_read_b64 v[238:239], %[kb] offset:2792\n\t"
                "s_waitcnt lgkmcnt(9)\n\t"
                "v_mfma_f32_32x32x16_bf16 %[a1], v[208:211], v[212:215], %[a1]\n\t"
                "ds_read_b64 v[240:241], %[kb] offset:704\n\t"
                "ds_read_b64 v[242:243], %[kb] offset:712\n\t"
                "ds_read_b128 v[244:247], %[ub] offset:4192\n\t"
                "s_waitcnt lgkmcnt(11)\n\t"
                "v_mfma_f32_32x32x16_bf16 %[a0], v[208:211], v[216:219], %[a0]\n\t"
                "ds_read_b128 v[248:251], %[ub] offset:1632\n\t"
                "s_add_u32 %[t0], %[t1], 196608\n\t"
                "buffer_load_dwordx4 a[48:51], %[go], %[so], %[t0] offen\n\t"
                "s_waitcnt lgkmcnt(10)\n\t"
                "v_mfma_f32_32x32x16_bf16 %[a1], v[220:223], v[216:219], %[a1]\n\t"
                "ds_read_b64 v[252:253], %[kb] offset:2752\n\t"
                "ds_read_b64 v[254:255], %[kb] offset:2760\n\t"
                "s_waitcnt lgkmcnt(9)\n\t"
                "v_mfma_f32_32x32x16_bf16 %[a1], v[224:227], v[228:231], %[a1]\n\t"
                "ds_read_b64 v[192:193], %[kb] offset:800\n\t"
                "ds_read_b64 v[194:195], %[kb] offset:808\n\t"
                "ds_read_b128 v[196:199], %[ub] offset:4080\n\t"
                "s_waitcnt lgkmcnt(11)\n\t"
                "v_mfma_f32_32x32x16_bf16 %[a0], v[224:227], v[232:235], %[a0]\n\t"
                "ds_read_b128 v[200:203], %[ub] offset:1520\n\t"
                "s_waitcnt lgkmcnt(10)\n\t"
                "v_mfma_f32_32x32x16_bf16 %[a1], v[236:239], v[232:235], %[a1]\n\t"
                "ds_read_b64 v[204:205], %[kb] offset:2848\n\t"
                "ds_read_b64 v[206:207], %[kb] offset:2856\n\t"
                "s_waitcnt lgkmcnt(9)\n\t"
                "v_mfma_f32_32x32x16_bf16 %[a1], v[240:243], v[244:247], %[a1]\n\t"
                "ds_read_b64 v[208:209], %[kb] offset:768\n\t"
                "ds_read_b64 v[210:211], %[kb] offset:776\n\t"
                "ds_read_b128 v[212:215], %[ub] offset:4112\n\t"
                "s_waitcnt lgkmcnt(11)\n\t"
                "v_mfma_f32_32x32x16_bf16 %[a0], v[240:243], v[248:251], %[a0]\n\t"
                "ds_read_b128 v[216:219], %[ub] offset:1552\n\t"
                "s_add_u32 %[t0], %[t1], 212992\n\t"
                "buffer_load_dwordx4 a[52:55], %[go], %[so], %[t0] offen\n\t"
                "s_waitcnt lgkmcnt(10)\n\t"
                "v_mfma_f32_32x32x16_bf16 %[a1], v[252:255], v[248:251], %[a1]\n\t"
                "ds_read_b64 v[220:221], %[kb] offset:2816\n\t"
                "ds_read_b64 v[222:223], %[kb] offset:2824\n\t"
                "s_waitcnt lgkmcnt(9)\n\t"
                "v_mfma_f32_32x32x16_bf16 %[a1], v[192:195], v[196:199], %[a1]\n\t"
                "ds_read_b64 v[224:225], %[kb] offset:864\n\t"
                "ds_read_b64 v[226:227], %[kb] offset:872\n\t"
                "ds_read_b128 v[228:231], %[ub] offset:4000\n\t"
                "s_waitcnt lgkmcnt(11)\n\t"
                "v_mfma_f32_32x32x16_bf16 %[a0], v[192:195], v[200:203], %[a0]\n\t"
                "ds_read_b128 v[232:235], %[ub] offset:1440\n\t"
                "s_waitcnt lgkmcnt(10)\n\t"
                "v_mfma_f32_32x32x16_bf16 %[a1], v[204:207], v[200:203], %[a1]\n\t"
                "ds_read_b64 v[236:237], %[kb] offset:2912\n\t"
                "ds_read_b64 v[238:239], %[kb] offset:2920\n\t"
                "s_waitcnt lgkmcnt(9)\n\t"
                "v_mfma_f32_32x32x16_bf16 %[a1], v[208:211], v[212:215], %[a1]\n\t"
                "ds_read_b64 v[240:241], %[kb] offset:832\n\t"
                "ds_read_b64 v[242:243], %[kb] offset:840\n\t"
                "ds_read_b128 v[244:247], %[ub] offset:4032\n\t"
                "s_waitcnt lgkmcnt(11)\n\t"
                "v_mfma_f32_32x32x16_bf16 %[a0], v[208:211], v[216:219], %[a0]\n\t"
                "ds_read_b128 v[248:251], %[ub] offset:1472\n\t"
                "s_add_u32 %[t0], %[t1], 229376\n\t"
                "buffer_load_dwordx4 a[56:59], %[go], %[so], %[t0] offen\n\t"
                "s_waitcnt lgkmcnt(10)\n\t"
                "v_mfma_f32_32x32x16_bf16 %[a1], v[220:223], v[216:219], %[a1]\n\t"
                "ds_read_b64 v[252:253], %[kb] offset:2880\n\t"
                "ds_read_b64 v[254:255], %[kb] offset:2888\n\t"
                "s_waitcnt lgkmcnt(9)\n\t"
                "v_mfma_f32_32x32x16_bf16 %[a1], v[224:227], v[228:231], %[a1]\n\t"
                "ds_read_b64 v[192:193], %[kb] offset:928\n\t"
                "ds_read_b64 v[194:195], %[kb] offset:936\n\t"
                "ds_read_b128 v[196:199], %[ub] offset:3920\n\t"
                "s_waitcnt lgkmcnt(11)\n\t"
                "v_mfma_f32_32x32x16_bf16 %[a0], v[224:227], v[232:235], %[a0]\n\t"
                "ds_read_b128 v[200:203], %[ub] offset:1360\n\t"
                "s_waitcnt lgkmcnt(10)\n\t"
                "v_mfma_f32_32x32x16_bf16 %[a1], v[236:239], v[232:235], %[a1]\n\t"
                "ds_read_b64 v[204:205], %[kb] offset:2976\n\t"
                "ds_read_b64 v[206:207], %[kb] offset:2984\n\t"
                "s_waitcnt lgkmcnt(9)\n\t"
                "v_mfma_f32_32x32x16_bf16 %[a1], v[240:243], v[244:247], %[a1]\n\t"
                "ds_read_b64 v[208:209], %[kb] offset:896\n\t"
                "ds_read_b64 v[210:211], %[kb] offset:904\n\t"
                "ds_read_b128 v[212:215], %[ub] offset:3952\n\t"
                "s_waitcnt lgkmcnt(11)\n\t"
                "v_mfma_f32_32x32x16_bf16 %[a0], v[240:243], v[248:251], %[a0]\n\t"
                "ds_read_b128 v[216:219], %[ub] offset:1392\n\t"
                "s_add_u32 %[t0], %[t1], 245760\n\t"
                "buffer_load_dwordx4 a[60:63], %[go], %[so], %[t0] offen\n\t"
                "s_waitcnt lgkmcnt(10)\n\t"
                "v_mfma_f32_32x32x16_bf16 %[a1], v[252:255], v[248:251], %[a1]\n\t"
                "ds_read_b64 v[220:221], %[kb] offset:2944\n\t"
                "ds_read_b64 v[222:223], %[kb] offset:2952\n\t"
                "s_waitcnt lgkmcnt(9)\n\t"
                "v_mfma_f32_32x32x16_bf16 %[a1], v[192:195], v[196:199], %[a1]\n\t"
                "ds_read_b64 v[224:225], %[kb] offset:992\n\t"
                "ds_read_b64 v[226:227], %[kb] offset:1000\n\t"
                "ds_read_b128 v[228:231], %[ub] offset:3840\n\t"
                "s_waitcnt lgkmcnt(11)\n\t"
                "v_mfma_f32_32x32x16_bf16 %[a0], v[192:195], v[200:203], %[a0]\n\t"
                "ds_read_b128 v[232:235], %[ub] offset:1280\n\t"
                "s_add_u32 %[t1], %[ro], 1\n\t"
                "s_and_b32 %[t1], %[t1], 7\n\t"
                "s_lshl_b32 %[t1], %[t1], 18\n\t"
                "s_add_u32 %[t0], %[t1], 0\n\t"
                "buffer_load_dwordx4 a[64:67], %[go], %[so], %[t0] offen\n\t"
                "s_waitcnt lgkmcnt(10)\n\t"
                "v_mfma_f32_32x32x16_bf16 %[a1], v[204:207], v[200:203], %[a1]\n\t"
                "ds_read_b64 v[236:237], %[kb] offset:3040\n\t"
                "ds_read_b64 v[238:239], %[kb] offset:3048\n\t"
                "s_waitcnt lgkmcnt(9)\n\t"
                "v_mfma_f32_32x32x16_bf16 %[a1], v[208:211], v[212:215], %[a1]\n\t"
                "ds_read_b64 v[240:241], %[kb] offset:960\n\t"
                "ds_read_b64 v[242:243], %[kb] offset:968\n\t"
                "ds_read_b128 v[244:247], %[ub] offset:3872\n\t"
                "s_waitcnt lgkmcnt(11)\n\t"
                "v_mfma_f32_32x32x16_bf16 %[a0], v[208:211], v[216:219], %[a0]\n\t"
                "ds_read_b128 v[248:251], %[ub] offset:1312\n\t"
                "s_waitcnt lgkmcnt(10)\n\t"
                "v_mfma_f32_32x32x16_bf16 %[a1], v[220:223], v[216:219], %[a1]\n\t"
                "ds_read_b64 v[252:253], %[kb] offset:3008\n\t"
                "ds_read_b64 v[254:255], %[kb] offset:3016\n\t"
                "s_waitcnt lgkmcnt(9)\n\t"
                "v_mfma_f32_32x32x16_bf16 %[a1], v[224:227], v[228:231], %[a1]\n\t"
                "ds_read_b64 v[192:193], %[kb] offset:1056\n\t"
                "ds_read_b64 v[194:195], %[kb] offset:1064\n\t"
                "ds_read_b128 v[196:199], %[ub] offset:3760\n\t"
                "s_waitcnt lgkmcnt(11)\n\t"
                "v_mfma_f32_32x32x16_bf16 %[a0], v[224:227], v[232:235], %[a0]\n\t"
                "ds_read_b128 v[200:203], %[ub] offset:1200\n\t"
                "s_add_u32 %[t0], %[t1], 16384\n\t"
                "buffer_load_dwordx4 a[68:71], %[go], %[so], %[t0] offen\n\t"
                "s_waitcnt lgkmcnt(10)\n\t"
                "v_mfma_f32_32x32x16_bf16 %[a1], v[236:239], v[232:235], %[a1]\n\t"
                "ds_read_b64 v[204:205], %[kb] offset:3104\n\t"
                "ds_read_b64 v[206:207], %[kb] offset:3112\n\t"
                "s_waitcnt lgkmcnt(9)\n\t"
                "v_mfma_f32_32x32x16_bf16 %[a1], v[240:243], v[244:247], %[a1]\n\t"
                "ds_read_b64 v[208:209], %[kb] offset:1024\n\t"
                "ds_read_b64 v[210:211], %[kb] offset:1032\n\t"
                "ds_read_b128 v[212:215], %[ub] offset:3792\n\t"
                "s_waitcnt lgkmcnt(11)\n\t"
                "v_mfma_f32_32x32x16_bf16 %[a0], v[240:243], v[248:251], %[a0]\n\t"
                "ds_read_b128 v[216:219], %[ub] offset:1232\n\t"
                "s_waitcnt lgkmcnt(10)\n\t"
                "v_mfma_f32_32x32x16_bf16 %[a1], v[252:255], v[248:251], %[a1]\n\t"
                "ds_read_b64 v[220:221], %[kb] offset:3072\n\t"
                "ds_read_b64 v[222:223], %[kb] offset:3080\n\t"
                "s_waitcnt lgkmcnt(9)\n\t"
                "v_mfma_f32_32x32x16_bf16 %[a1], v[192:195], v[196:199], %[a1]\n\t"
                "ds_read_b64 v[224:225], %[kb] offset:1120\n\t"
                "ds_read_b64 v[226:227], %[kb] offset:1128\n\t"
                "ds_read_b128 v[228:231], %[ub] offset:3680\n\t"
                "s_waitcnt lgkmcnt(11)\n\t"
                "v_mfma_f32_32x32x16_bf16 %[a0], v[192:195], v[200:203], %[a0]\n\t"
                "ds_read_b128 v[232:235], %[ub] offset:1120\n\t"
                "s_add_u32 %[t0], %[t1], 32768\n\t"
                "buffer_load_dwordx4 a[72:75], %[go], %[so], %[t0] offen\n\t"
                "s_waitcnt lgkmcnt(10)\n\t"
                "v_mfma_f32_32x32x16_bf16 %[a1], v[204:207], v[200:203], %[a1]\n\t"
                "ds_read_b64 v[236:237], %[kb] offset:3168\n\t"
                "ds_read_b64 v[238:239], %[kb] offset:3176\n\t"
                "s_waitcnt lgkmcnt(9)\n\t"
                "v_mfma_f32_32x32x16_bf16 %[a1], v[208:211], v[212:215], %[a1]\n\t"
                "ds_read_b64 v[240:241], %[kb] offset:1088\n\t"
                "ds_read_b64 v[242:243], %[kb] offset:1096\n\t"
                "ds_read_b128 v[244:247], %[ub] offset:3712\n\t"
                "s_waitcnt lgkmcnt(11)\n\t"
                "v_mfma_f32_32x32x16_bf16 %[a0], v[208:211], v[216:219], %[a0]\n\t"
                "ds_read_b128 v[248:251], %[ub] offset:1152\n\t"
                "s_waitcnt lgkmcnt(10)\n\t"
                "v_mfma_f32_32x32x16_bf16 %[a1], v[220:223], v[216:219], %[a1]\n\t"
                "ds_read_b64 v[252:253], %[kb] offset:3136\n\t"
                "ds_read_b64 v[254:255], %[kb] offset:3144\n\t"
                "s_waitcnt lgkmcnt(9)\n\t"
                "v_mfma_f32_32x32x16_bf16 %[a1], v[224:227], v[228:231], %[a1]\n\t"
                "ds_read_b64 v[192:193], %[kb] offset:1184\n\t"
                "ds_read_b64 v[194:195], %[kb] offset:1192\n\t"
                "ds_read_b128 v[196:199], %[ub] offset:3600\n\t"
                "s_waitcnt lgkmcnt(11)\n\t"
                "v_mfma_f32_32x32x16_bf16 %[a0], v[224:227], v[232:235], %[a0]\n\t"
                "ds_read_b128 v[200:203], %[ub] offset:1040\n\t"
                "s_add_u32 %[t0], %[t1], 49152\n\t"
                "buffer_load_dwordx4 a[76:79], %[go], %[so], %[t0] offen\n\t"
                "s_waitcnt lgkmcnt(10)\n\t"
                "v_mfma_f32_32x32x16_bf16 %[a1], v[236:239], v[232:235], %[a1]\n\t"
                "ds_read_b64 v[204:205], %[kb] offset:3232\n\t"
                "ds_read_b64 v[206:207], %[kb] offset:3240\n\t"
                "s_waitcnt lgkmcnt(9)\n\t"
                "v_mfma_f32_32x32x16_bf16 %[a1], v[240:243], v[244:247], %[a1]\n\t"
                "ds_read_b64 v[208:209], %[kb] offset:1152\n\t"
                "ds_read_b64 v[210:211], %[kb] offset:1160\n\t"
                "ds_read_b128 v[212:215], %[ub] offset:3632\n\t"
                "s_waitcnt lgkmcnt(11)\n\t"
                "v_mfma_f32_32x32x16_bf16 %[a0], v[240:243], v[248:251], %[a0]\n\t"
                "ds_read_b128 v[216:219], %[ub] offset:1072\n\t"
                "s_add_u32 %[t0], %[t1], 65536\n\t"
                "buffer_load_dwordx4 a[80:83], %[go], %[so], %[t0] offen\n\t"
                "s_waitcnt lgkmcnt(10)\n\t"
                "v_mfma_f32_32x32x16_bf16 %[a1], v[252:255], v[248:251], %[a1]\n\t"
                "ds_read_b64 v[220:221], %[kb] offset:3200\n\t"
                "ds_read_b64 v[222:223], %[kb] offset:3208\n\t"
                "s_waitcnt lgkmcnt(9)\n\t"
                "v_mfma_f32_32x32x16_bf16 %[a1], v[192:195], v[196:199], %[a1]\n\t"
                "ds_read_b64 v[224:225], %[kb] offset:1248\n\t"
                "ds_read_b64 v[226:227], %[kb] offset:1256\n\t"
                "ds_read_b128 v[228:231], %[ub] offset:3520\n\t"
                "s_waitcnt lgkmcnt(11)\n\t"
                "v_mfma_f32_32x32x16_bf16 %[a0], v[192:195], v[200:203], %[a0]\n\t"
                "ds_read_b128 v[232:235], %[ub] offset:960\n\t"
                "s_waitcnt lgkmcnt(10)\n\t"
                "v_mfma_f32_32x32x16_bf16 %[a1], v[204:207], v[200:203], %[a1]\n\t"
                "ds_read_b64 v[236:237], %[kb] offset:3296\n\t"
                "ds_read_b64 v[238:239], %[kb] offset:3304\n\t"
                "s_waitcnt lgkmcnt(9)\n\t"
                "v_mfma_f32_32x32x16_bf16 %[a1], v[208:211], v[212:215], %[a1]\n\t"
                "ds_read_b64 v[240:241], %[kb] offset:1216\n\t"
                "ds_read_b64 v[242:243], %[kb] offset:1224\n\t"
                "ds_read_b128 v[244:247], %[ub] offset:3552\n\t"
                "s_waitcnt lgkmcnt(11)\n\t"
                "v_mfma_f32_32x32x16_bf16 %[a0], v[208:211], v[216:219], %[a0]\n\t"
                "ds_read_b128 v[248:251], %[ub] offset:992\n\t"
                "s_add_u32 %[t0], %[t1], 81920\n\t"
                "buffer_load_dwordx4 a[84:87], %[go], %[so], %[t0] offen\n\t"
                "s_waitcnt lgkmcnt(10)\n\t"
                "v_mfma_f32_32x32x16_bf16 %[a1], v[220:223], v[216:219], %[a1]\n\t"
                "ds_read_b64 v[252:253], %[kb] offset:3264\n\t"
                "ds_read_b64 v[254:255], %[kb] offset:3272\n\t"
                "s_waitcnt lgkmcnt(9)\n\t"
                "v_mfma_f32_32x32x16_bf16 %[a1], v[224:227], v[228:231], %[a1]\n\t"
                "ds_read_b64 v[192:193], %[kb] offset:1312\n\t"
                "ds_read_b64 v[194:195], %[kb] offset:1320\n\t"
                "ds_read_b128 v[196:199], %[ub] offset:3440\n\t"
                "s_waitcnt lgkmcnt(11)\n\t"
                "v_mfma_f32_32x32x16_bf16 %[a0], v[224:227], v[232:235], %[a0]\n\t"
                "ds_read_b128 v[200:203], %[ub] offset:880\n\t"
                "s_waitcnt lgkmcnt(10)\n\t"
                "v_mfma_f32_32x32x16_bf16 %[a1], v[236:239], v[232:235], %[a1]\n\t"
                "ds_read_b64 v[204:205], %[kb] offset:3360\n\t"
                "ds_read_b64 v[206:207], %[kb] offset:3368\n\t"
                "s_waitcnt lgkmcnt(9)\n\t"
                "v_mfma_f32_32x32x16_bf16 %[a1], v[240:243], v[244:247], %[a1]\n\t"
                "ds_read_b64 v[208:209], %[kb] offset:1280\n\t"
                "ds_read_b64 v[210:211], %[kb] offset:1288\n\t"
                "ds_read_b128 v[212:215], %[ub] offset:3472\n\t"
                "s_waitcnt lgkmcnt(11)\n\t"
                "v_mfma_f32_32x32x16_bf16 %[a0], v[240:243], v[248:251], %[a0]\n\t"
                "ds_read_b128 v[216:219], %[ub] offset:912\n\t"
                "s_add_u32 %[t0], %[t1], 98304\n\t"
                "buffer_load_dwordx4 a[88:91], %[go], %[so], %[t0] offen\n\t"
                "s_waitcnt lgkmcnt(10)\n\t"
                "v_mfma_f32_32x32x16_bf16 %[a1], v[252:255], v[248:251], %[a1]\n\t"
                "ds_read_b64 v[220:221], %[kb] offset:3328\n\t"
                "ds_read_b64 v[222:223], %[kb] offset:3336\n\t"
                "s_waitcnt lgkmcnt(9)\n\t"
                "v_mfma_f32_32x32x16_bf16 %[a1], v[192:195], v[196:199], %[a1]\n\t"
                "ds_read_b64 v[224:225], %[kb] offset:1376\n\t"
                "ds_read_b64 v[226:227], %[kb] offset:1384\n\t"
                "ds_read_b128 v[228:231], %[ub] offset:3360\n\t"
                "s_waitcnt lgkmcnt(11)\n\t"
                "v_mfma_f32_32x32x16_bf16 %[a0], v[192:195], v[200:203], %[a0]\n\t"
                "ds_read_b128 v[232:235], %[ub] offset:800\n\t"
                "s_waitcnt lgkmcnt(10)\n\t"
                "v_mfma_f32_32x32x16_bf16 %[a1], v[204:207], v[200:203], %[a1]\n\t"
                "ds_read_b64 v[236:237], %[kb] offset:3424\n\t"
                "ds_read_b64 v[238:239], %[kb] offset:3432\n\t"
                "s_waitcnt lgkmcnt(9)\n\t"
                "v_mfma_f32_32x32x16_bf16 %[a1], v[208:211], v[212:215], %[a1]\n\t"
                "ds_read_b64 v[240:241], %[kb] offset:1344\n\t"
                "ds_read_b64 v[242:243], %[kb] offset:1352\n\t"
                "ds_read_b128 v[244:247], %[ub] offset:3392\n\t"
                "s_waitcnt lgkmcnt(11)\n\t"
                "v_mfma_f32_32x32x16_bf16 %[a0], v[208:211], v[216:219], %[a0]\n\t"
                "ds_read_b128 v[248:251], %[ub] offset:832\n\t"
                "s_add_u32 %[t0], %[t1], 114688\n\t"
                "buffer_load_dwordx4 a[92:95], %[go], %[so], %[t0] offen\n\t"
                "s_waitcnt lgkmcnt(10)\n\t"
                "v_mfma_f32_32x32x16_bf16 %[a1], v[220:223], v[216:219], %[a1]\n\t"
                "ds_read_b64 v[252:253], %[kb] offset:3392\n\t"
                "ds_read_b64 v[254:255], %[kb] offset:3400\n\t"
                "s_waitcnt lgkmcnt(9)\n\t"
                "v_mfma_f32_32x32x16_bf16 %[a1], v[224:227], v[228:231], %[a1]\n\t"
                "ds_read_b64 v[192:193], %[kb] offset:1440\n\t"
                "ds_read_b64 v[194:195], %[kb] offset:1448\n\t"
                "ds_read_b128 v[196:199], %[ub] offset:3280\n\t"
                "s_waitcnt lgkmcnt(11)\n\t"
                "v_mfma_f32_32x32x16_bf16 %[a0], v[224:227], v[232:235], %[a0]\n\t"
                "ds_read_b128 v[200:203], %[ub] offset:720\n\t"
                "s_add_u32 %[t1], %[ro], 1\n\t"
                "s_and_b32 %[t1], %[t1], 7\n\t"
                "s_lshl_b32 %[t1], %[t1], 18\n\t"
                "s_add_u32 %[t0], %[t1], 131072\n\t"
                "buffer_load_dwordx4 a[96:99], %[go], %[so], %[t0] offen\n\t"
                "s_waitcnt lgkmcnt(10)\n\t"
                "v_mfma_f32_32x32x16_bf16 %[a1], v[236:239], v[232:235], %[a1]\n\t"
                "ds_read_b64 v[204:205], %[kb] offset:3488\n\t"
                "ds_read_b64 v[206:207], %[kb] offset:3496\n\t"
                "s_waitcnt lgkmcnt(9)\n\t"
                "v_mfma_f32_32x32x16_bf16 %[a1], v[240:243], v[244:247], %[a1]\n\t"
                "ds_read_b64 v[208:209], %[kb] offset:1408\n\t"
                "ds_read_b64 v[210:211], %[kb] offset:1416\n\t"
                "ds_read_b128 v[212:215], %[ub] offset:3312\n\t"
                "s_waitcnt lgkmcnt(11)\n\t"
                "v_mfma_f32_32x32x16_bf16 %[a0], v[240:243], v[248:251], %[a0]\n\t"
                "ds_read_b128 v[216:219], %[ub] offset:752\n\t"
                "s_waitcnt lgkmcnt(10)\n\t"
                "v_mfma_f32_32x32x16_bf16 %[a1], v[252:255], v[248:251], %[a1]\n\t"
                "ds_read_b64 v[220:221], %[kb] offset:3456\n\t"
                "ds_read_b64 v[222:223], %[kb] offset:3464\n\t"
                "s_waitcnt lgkmcnt(9)\n\t"
                "v_mfma_f32_32x32x16_bf16 %[a1], v[192:195], v[196:199], %[a1]\n\t"
                "ds_read_b64 v[224:225], %[kb] offset:1504\n\t"
                "ds_read_b64 v[226:227], %[kb] offset:1512\n\t"
                "ds_read_b128 v[228:231], %[ub] offset:3200\n\t"
                "s_waitcnt lgkmcnt(11)\n\t"
                "v_mfma_f32_32x32x16_bf16 %[a0], v[192:195], v[200:203], %[a0]\n\t"
                "ds_read_b128 v[232:235], %[ub] offset:640\n\t"
                "s_add_u32 %[t0], %[t1], 147456\n\t"
                "buffer_load_dwordx4 a[100:103], %[go], %[so], %[t0] offen\n\t"
                "s_waitcnt lgkmcnt(10)\n\t"
                "v_mfma_f32_32x32x16_bf16 %[a1], v[204:207], v[200:203], %[a1]\n\t"
                "ds_read_b64 v[236:237], %[kb] offset:3552\n\t"
                "ds_read_b64 v[238:239], %[kb] offset:3560\n\t"
                "s_waitcnt lgkmcnt(9)\n\t"
                "v_mfma_f32_32x32x16_bf16 %[a1], v[208:211], v[212:215], %[a1]\n\t"
                "ds_read_b64 v[240:241], %[kb] offset:1472\n\t"
                "ds_read_b64 v[242:243], %[kb] offset:1480\n\t"
                "ds_read_b128 v[244:247], %[ub] offset:3232\n\t"
                "s_waitcnt lgkmcnt(11)\n\t"
                "v_mfma_f32_32x32x16_bf16 %[a0], v[208:211], v[216:219], %[a0]\n\t"
                "ds_read_b128 v[248:251], %[ub] offset:672\n\t"
                "s_waitcnt lgkmcnt(10)\n\t"
                "v_mfma_f32_32x32x16_bf16 %[a1], v[220:223], v[216:219], %[a1]\n\t"
                "ds_read_b64 v[252:253], %[kb] offset:3520\n\t"
                "ds_read_b64 v[254:255], %[kb] offset:3528\n\t"
                "s_waitcnt lgkmcnt(9)\n\t"
                "v_mfma_f32_32x32x16_bf16 %[a1], v[224:227], v[228:231], %[a1]\n\t"
                "ds_read_b64 v[192:193], %[kb] offset:1568\n\t"
                "ds_read_b64 v[194:195], %[kb] offset:1576\n\t"
                "ds_read_b128 v[196:199], %[ub] offset:3120\n\t"
                "s_waitcnt lgkmcnt(11)\n\t"
                "v_mfma_f32_32x32x16_bf16 %[a0], v[224:227], v[232:235], %[a0]\n\t"
                "ds_read_b128 v[200:203], %[ub] offset:560\n\t"
                "s_add_u32 %[t0], %[t1], 163840\n\t"
                "buffer_load_dwordx4 a[104:107], %[go], %[so], %[t0] offen\n\t"
                "s_waitcnt lgkmcnt(10)\n\t"
                "v_mfma_f32_32x32x16_bf16 %[a1], v[236:239], v[232:235], %[a1]\n\t"
                "ds_read_b64 v[204:205], %[kb] offset:3616\n\t"
                "ds_read_b64 v[206:207], %[kb] offset:3624\n\t"
                "s_waitcnt lgkmcnt(9)\n\t"
                "v_mfma_f32_32x32x16_bf16 %[a1], v[240:243], v[244:247], %[a1]\n\t"
                "ds_read_b64 v[208:209], %[kb] offset:1536\n\t"
                "ds_read_b64 v[210:211], %[kb] offset:1544\n\t"
                "ds_read_b128 v[212:215], %[ub] offset:3152\n\t"
                "s_waitcnt lgkmcnt(11)\n\t"
                "v_mfma_f32_32x32x16_bf16 %[a0], v[240:243], v[248:251], %[a0]\n\t"
                "ds_read_b128 v[216:219], %[ub] offset:592\n\t"
                "s_waitcnt lgkmcnt(10)\n\t"
                "v_mfma_f32_32x32x16_bf16 %[a1], v[252:255], v[248:251], %[a1]\n\t"
                "ds_read_b64 v[220:221], %[kb] offset:3584\n\t"
                "ds_read_b64 v[222:223], %[kb] offset:3592\n\t"
                "s_waitcnt lgkmcnt(9)\n\t"
                "v_mfma_f32_32x32x16_bf16 %[a1], v[192:195], v[196:199], %[a1]\n\t"
                "ds_read_b64 v[224:225], %[kb] offset:1632\n\t"
                "ds_read_b64 v[226:227], %[kb] offset:1640\n\t"
                "ds_read_b128 v[228:231], %[ub] offset:3040\n\t"
                "s_waitcnt lgkmcnt(11)\n\t"
                "v_mfma_f32_32x32x16_bf16 %[a0], v[192:195], v[200:203], %[a0]\n\t"
                "ds_read_b128 v[232:235], %[ub] offset:480\n\t"
                "s_add_u32 %[t0], %[t1], 180224\n\t"
                "buffer_load_dwordx4 a[108:111], %[go], %[so], %[t0] offen\n\t"
                "s_waitcnt lgkmcnt(10)\n\t"
                "v_mfma_f32_32x32x16_bf16 %[a1], v[204:207], v[200:203], %[a1]\n\t"
                "ds_read_b64 v[236:237], %[kb] offset:3680\n\t"
                "ds_read_b64 v[238:239], %[kb] offset:3688\n\t"
                "s_waitcnt lgkmcnt(9)\n\t"
                "v_mfma_f32_32x32x16_bf16 %[a1], v[208:211], v[212:215], %[a1]\n\t"
                "ds_read_b64 v[240:241], %[kb] offset:1600\n\t"
                "ds_read_b64 v[242:243], %[kb] offset:1608\n\t"
                "ds_read_b128 v[244:247], %[ub] offset:3072\n\t"
                "s_waitcnt lgkmcnt(11)\n\t"
                "v_mfma_f32_32x32x16_bf16 %[a0], v[208:211], v[216:219], %[a0]\n\t"
                "ds_read_b128 v[248:251], %[ub] offset:512\n\t"
                "s_add_u32 %[t0], %[t1], 196608\n\t"
                "buffer_load_dwordx4 a[112:115], %[go], %[so], %[t0] offen\n\t"
                "s_waitcnt lgkmcnt(10)\n\t"
                "v_mfma_f32_32x32x16_bf16 %[a1], v[220:223], v[216:219], %[a1]\n\t"
                "ds_read_b64 v[252:253], %[kb] offset:3648\n\t"
                "ds_read_b64 v[254:255], %[kb] offset:3656\n\t"
                "s_waitcnt lgkmcnt(9)\n\t"
                "v_mfma_f32_32x32x16_bf16 %[a1], v[224:227], v[228:231], %[a1]\n\t"
                "ds_read_b64 v[192:193], %[kb] offset:1696\n\t"
                "ds_read_b64 v[194:195], %[kb] offset:1704\n\t"
                "ds_read_b128 v[196:199], %[ub] offset:2960\n\t"
                "s_waitcnt lgkmcnt(11)\n\t"
                "v_mfma_f32_32x32x16_bf16 %[a0], v[224:227], v[232:235], %[a0]\n\t"
                "ds_read_b128 v[200:203], %[ub] offset:400\n\t"
                "s_waitcnt lgkmcnt(10)\n\t"
                "v_mfma_f32_32x32x16_bf16 %[a1], v[236:239], v[232:235], %[a1]\n\t"
                "ds_read_b64 v[204:205], %[kb] offset:3744\n\t"
                "ds_read_b64 v[206:207], %[kb] offset:3752\n\t"
                "s_waitcnt lgkmcnt(9)\n\t"
                "v_mfma_f32_32x32x16_bf16 %[a1], v[240:243], v[244:247], %[a1]\n\t"
                "ds_read_b64 v[208:209], %[kb] offset:1664\n\t"
                "ds_read_b64 v[210:211], %[kb] offset:1672\n\t"
                "ds_read_b128 v[212:215], %[ub] offset:2992\n\t"
                "s_waitcnt lgkmcnt(11)\n\t"
                "v_mfma_f32_32x32x16_bf16 %[a0], v[240:243], v[248:251], %[a0]\n\t"
                "ds_read_b128 v[216:219], %[ub] offset:432\n\t"
                "s_add_u32 %[t0], %[t1], 212992\n\t"
                "buffer_load_dwordx4 a[116:119], %[go], %[so], %[t0] offen\n\t"
                "s_waitcnt lgkmcnt(10)\n\t"
                "v_mfma_f32_32x32x16_bf16 %[a1], v[252:255], v[248:251], %[a1]\n\t"
                "ds_read_b64 v[220:221], %[kb] offset:3712\n\t"
                "ds_read_b64 v[222:223], %[kb] offset:3720\n\t"
                "s_waitcnt lgkmcnt(9)\n\t"
                "v_mfma_f32_32x32x16_bf16 %[a1], v[192:195], v[196:199], %[a1]\n\t"
                "ds_read_b64 v[224:225], %[kb] offset:1760\n\t"
                "ds_read_b64 v[226:227], %[kb] offset:1768\n\t"
                "ds_read_b128 v[228:231], %[ub] offset:2880\n\t"
                "s_waitcnt lgkmcnt(11)\n\t"
                "v_mfma_f32_32x32x16_bf16 %[a0], v[192:195], v[200:203], %[a0]\n\t"
                "ds_read_b128 v[232:235], %[ub] offset:320\n\t"
                "s_waitcnt lgkmcnt(10)\n\t"
                "v_mfma_f32_32x32x16_bf16 %[a1], v[204:207], v[200:203], %[a1]\n\t"
                "ds_read_b64 v[236:237], %[kb] offset:3808\n\t"
                "ds_read_b64 v[238:239], %[kb] offset:3816\n\t"
                "s_waitcnt lgkmcnt(9)\n\t"
                "v_mfma_f32_32x32x16_bf16 %[a1], v[208:211], v[212:215], %[a1]\n\t"
                "ds_read_b64 v[240:241], %[kb] offset:1728\n\t"
                "ds_read_b64 v[242:243], %[kb] offset:1736\n\t"
                "ds_read_b128 v[244:247], %[ub] offset:2912\n\t"
                "s_waitcnt lgkmcnt(11)\n\t"
                "v_mfma_f32_32x32x16_bf16 %[a0], v[208:211], v[216:219], %[a0]\n\t"
                "ds_read_b128 v[248:251], %[ub] offset:352\n\t"
                "s_add_u32 %[t0], %[t1], 229376\n\t"
                "buffer_load_dwordx4 a[120:123], %[go], %[so], %[t0] offen\n\t"
                "s_waitcnt lgkmcnt(10)\n\t"
                "v_mfma_f32_32x32x16_bf16 %[a1], v[220:223], v[216:219], %[a1]\n\t"
                "ds_read_b64 v[252:253], %[kb] offset:3776\n\t"
                "ds_read_b64 v[254:255], %[kb] offset:3784\n\t"
                "s_waitcnt lgkmcnt(9)\n\t"
                "v_mfma_f32_32x32x16_bf16 %[a1], v[224:227], v[228:231], %[a1]\n\t"
                "ds_read_b64 v[192:193], %[kb] offset:1824\n\t"
                "ds_read_b64 v[194:195], %[kb] offset:1832\n\t"
                "ds_read_b128 v[196:199], %[ub] offset:2800\n\t"
                "s_waitcnt lgkmcnt(11)\n\t"
                "v_mfma_f32_32x32x16_bf16 %[a0], v[224:227], v[232:235], %[a0]\n\t"
                "ds_read_b128 v[200:203], %[ub] offset:240\n\t"
                "s_waitcnt lgkmcnt(10)\n\t"
                "v_mfma_f32_32x32x16_bf16 %[a1], v[236:239], v[232:235], %[a1]\n\t"
                "ds_read_b64 v[204:205], %[kb] offset:3872\n\t"
                "ds_read_b64 v[206:207], %[kb] offset:3880\n\t"
                "s_waitcnt lgkmcnt(9)\n\t"
                "v_mfma_f32_32x32x16_bf16 %[a1], v[240:243], v[244:247], %[a1]\n\t"
                "ds_read_b64 v[208:209], %[kb] offset:1792\n\t"
                "ds_read_b64 v[210:211], %[kb] offset:1800\n\t"
                "ds_read_b128 v[212:215], %[ub] offset:2832\n\t"
                "s_waitcnt lgkmcnt(11)\n\t"
                "v_mfma_f32_32x32x16_bf16 %[a0], v[240:243], v[248:251], %[a0]\n\t"
                "ds_read_b128 v[216:219], %[ub] offset:272\n\t"
                "s_add_u32 %[t0], %[t1], 245760\n\t"
                "buffer_load_dwordx4 a[124:127], %[go], %[so], %[t0] offen\n\t"
                "s_waitcnt lgkmcnt(10)\n\t"
                "v_mfma_f32_32x32x16_bf16 %[a1], v[252:255], v[248:251], %[a1]\n\t"
                "ds_read_b64 v[220:221], %[kb] offset:3840\n\t"
                "ds_read_b64 v[222:223], %[kb] offset:3848\n\t"
                "s_waitcnt lgkmcnt(9)\n\t"
                "v_mfma_f32_32x32x16_bf16 %[a1], v[192:195], v[196:199], %[a1]\n\t"
                "ds_read_b64 v[224:225], %[kb] offset:1888\n\t"
                "ds_read_b64 v[226:227], %[kb] offset:1896\n\t"
                "ds_read_b128 v[228:231], %[ub] offset:2720\n\t"
                "s_waitcnt lgkmcnt(11)\n\t"
                "v_mfma_f32_32x32x16_bf16 %[a0], v[192:195], v[200:203], %[a0]\n\t"
                "ds_read_b128 v[232:235], %[ub] offset:160\n\t"
                "s_waitcnt lgkmcnt(10)\n\t"
                "v_mfma_f32_32x32x16_bf16 %[a1], v[204:207], v[200:203], %[a1]\n\t"
                "ds_read_b64 v[236:237], %[kb] offset:3936\n\t"
                "ds_read_b64 v[238:239], %[kb] offset:3944\n\t"
                "s_waitcnt lgkmcnt(9)\n\t"
                "v_mfma_f32_32x32x16_bf16 %[a1], v[208:211], v[212:215], %[a1]\n\t"
                "ds_read_b64 v[240:241], %[kb] offset:1856\n\t"
                "ds_read_b64 v[242:243], %[kb] offset:1864\n\t"
                "ds_read_b128 v[244:247], %[ub] offset:2752\n\t"
                "s_waitcnt lgkmcnt(11)\n\t"
                "v_mfma_f32_32x32x16_bf16 %[a0], v[208:211], v[216:219], %[a0]\n\t"
                "ds_read_b128 v[248:251], %[ub] offset:192\n\t"
                "s_waitcnt lgkmcnt(10)\n\t"
                "v_mfma_f32_32x32x16_bf16 %[a1], v[220:223], v[216:219], %[a1]\n\t"
                "ds_read_b64 v[252:253], %[kb] offset:3904\n\t"
                "ds_read_b64 v[254:255], %[kb] offset:3912\n\t"
                "s_waitcnt lgkmcnt(9)\n\t"
                "v_mfma_f32_32x32x16_bf16 %[a1], v[224:227], v[228:231], %[a1]\n\t"
                "ds_read_b64 v[192:193], %[kb] offset:1952\n\t"
                "ds_read_b64 v[194:195], %[kb] offset:1960\n\t"
                "ds_read_b128 v[196:199], %[ub] offset:2640\n\t"
                "s_waitcnt lgkmcnt(11)\n\t"
                "v_mfma_f32_32x32x16_bf16 %[a0], v[224:227], v[232:235], %[a0]\n\t"
                "ds_read_b128 v[200:203], %[ub] offset:80\n\t"
                "s_waitcnt lgkmcnt(10)\n\t"
                "v_mfma_f32_32x32x16_bf16 %[a1], v[236:239], v[232:235], %[a1]\n\t"
                "ds_read_b64 v[204:205], %[kb] offset:4000\n\t"
                "ds_read_b64 v[206:207], %[kb] offset:4008\n\t"
                "s_waitcnt lgkmcnt(9)\n\t"
                "v_mfma_f32_32x32x16_bf16 %[a1], v[240:243], v[244:247], %[a1]\n\t"
                "ds_read_b64 v[208:209], %[kb] offset:1920\n\t"
                "ds_read_b64 v[210:211], %[kb] offset:1928\n\t"
                "ds_read_b128 v[212:215], %[ub] offset:2672\n\t"
                "s_waitcnt lgkmcnt(11)\n\t"
                "v_mfma_f32_32x32x16_bf16 %[a0], v[240:243], v[248:251], %[a0]\n\t"
                "ds_read_b128 v[216:219], %[ub] offset:112\n\t"
                "s_waitcnt lgkmcnt(10)\n\t"
                "v_mfma_f32_32x32x16_bf16 %[a1], v[252:255], v[248:251], %[a1]\n\t"
                "ds_read_b64 v[220:221], %[kb] offset:3968\n\t"
                "ds_read_b64 v[222:223], %[kb] offset:3976\n\t"
                "s_waitcnt lgkmcnt(9)\n\t"
                "v_mfma_f32_32x32x16_bf16 %[a1], v[192:195], v[196:199], %[a1]\n\t"
                "ds_read_b64 v[224:225], %[kb] offset:2016\n\t"
                "ds_read_b64 v[226:227], %[kb] offset:2024\n\t"
                "ds_read_b128 v[228:231], %[ub] offset:2560\n\t"
                "s_waitcnt lgkmcnt(11)\n\t"
                "v_mfma_f32_32x32x16_bf16 %[a0], v[192:195], v[200:203], %[a0]\n\t"
                "ds_read_b128 v[232:235], %[ub] offset:0\n\t"
                "s_waitcnt lgkmcnt(10)\n\t"
                "v_mfma_f32_32x32x16_bf16 %[a1], v[204:207], v[200:203], %[a1]\n\t"
                "ds_read_b64 v[236:237], %[kb] offset:4064\n\t"
                "ds_read_b64 v[238:239], %[kb] offset:4072\n\t"
                "s_waitcnt lgkmcnt(9)\n\t"
                "v_mfma_f32_32x32x16_bf16 %[a1], v[208:211], v[212:215], %[a1]\n\t"
                "ds_read_b64 v[240:241], %[kb] offset:1984\n\t"
                "ds_read_b64 v[242:243], %[kb] offset:1992\n\t"
                "ds_read_b128 v[244:247], %[ub] offset:2592\n\t"
                "s_waitcnt lgkmcnt(11)\n\t"
                "v_mfma_f32_32x32x16_bf16 %[a0], v[208:211], v[216:219], %[a0]\n\t"
                "ds_read_b128 v[248:251], %[ub] offset:32\n\t"
                "s_waitcnt lgkmcnt(10)\n\t"
                "v_mfma_f32_32x32x16_bf16 %[a1], v[220:223], v[216:219], %[a1]\n\t"
                "ds_read_b64 v[252:253], %[kb] offset:4032\n\t"
                "ds_read_b64 v[254:255], %[kb] offset:4040\n\t"
                "s_waitcnt lgkmcnt(9)\n\t"
                "v_mfma_f32_32x32x16_bf16 %[a1], v[224:227], v[228:231], %[a1]\n\t"
                "s_waitcnt lgkmcnt(8)\n\t"
                "v_mfma_f32_32x32x16_bf16 %[a0], v[224:227], v[232:235], %[a0]\n\t"
                "s_waitcnt lgkmcnt(6)\n\t"
                "v_mfma_f32_32x32x16_bf16 %[a1], v[236:239], v[232:235], %[a1]\n\t"
                "s_waitcnt lgkmcnt(3)\n\t"
                "v_mfma_f32_32x32x16_bf16 %[a1], v[240:243], v[244:247], %[a1]\n\t"
                "s_waitcnt lgkmcnt(2)\n\t"
                "v_mfma_f32_32x32x16_bf16 %[a0], v[240:243], v[248:251], %[a0]\n\t"
                "s_waitcnt lgkmcnt(0)\n\t"
                "v_mfma_f32_32x32x16_bf16 %[a1], v[252:255], v[248:251], %[a1]\n\t"
                "s_waitcnt vmcnt(0)\n\t"
                "s_nop 7\n\t"
                "s_nop 3\n\t"
                : [a0] "=&v"(acc0), [a1] "=&v"(acc1), [t0] "=&s"(t0), [t1] "=&s"(t1)
                : [kb] "v"(kb), [ub] "v"(ub0 + c8 * CV_U), [go] "v"(go), [so] "s"(so), [ro] "s"(ro) : "memory", "scc", "v192", "v193", "v194", "v195", "v196", "v197", "v198", "v199", "v200", "v201", "v202", "v203", "v204", "v205", "v206", "v207", "v208", "v209", "v210", "v211", "v212", "v213", "v214", "v215", "v216", "v217", "v218", "v219", "v220", "v221", "v222", "v223", "v224", "v225", "v226", "v227", "v228", "v229", "v230", "v231", "v232", "v233", "v234", "v235", "v236", "v237", "v238", "v239", "v240", "v241", "v242", "v243", "v244", "v245", "v246", "v247", "v248", "v249", "v250", "v251", "v252", "v253", "v254", "v255", "a0", "a1", "a2", "a3", "a4", "a5", "a6", "a7", "a8", "a9", "a10", "a11", "a12", "a13", "a14", "a15", "a16", "a17", "a18", "a19", "a20", "a21", "a22", "a23", "a24", "a25", "a26", "a27", "a28", "a29", "a30", "a31", "a32", "a33", "a34", "a35", "a36", "a37", "a38", "a39", "a40", "a41", "a42", "a43", "a44", "a45", "a46", "a47", "a48", "a49", "a50", "a51", "a52", "a53", "a54", "a55", "a56", "a57", "a58", "a59", "a60", "a61", "a62", "a63", "a64", "a65", "a66", "a67", "a68", "a69", "a70", "a71", "a72", "a73", "a74", "a75", "a76", "a77", "a78", "a79", "a80", "a81", "a82", "a83", "a84", "a85", "a86", "a87", "a88", "a89", "a90", "a91", "a92", "a93", "a94", "a95", "a96", "a97", "a98", "a99", "a100", "a101", "a102", "a103", "a104", "a105", "a106", "a107", "a108", "a109", "a110", "a111", "a112", "a113", "a114", "a115", "a116", "a117", "a118", "a119", "a120", "a121", "a122", "a123", "a124", "a125", "a126", "a127", "a128", "a129", "a130", "a131", "a132", "a133", "a134", "a135", "a136", "a137", "a138", "a139", "a140", "a141", "a142", "a143", "a144", "a145", "a146", "a147", "a148", "a149", "a150", "a151", "a152", "a153", "a154", "a155", "a156", "a157", "a158", "a159", "a160", "a161", "a162", "a163", "a164", "a165", "a166", "a167", "a168", "a169", "a170", "a171", "a172", "a173", "a174", "a175", "a176", "a177", "a178", "a179", "a180", "a181", "a182", "a183", "a184", "a185", "a186", "a187", "a188", "a189", "a190", "a191", "a192", "a193", "a194", "a195", "a196", "a197", "a198", "a199", "a200", "a201", "a202", "a203", "a204", "a205", "a206", "a207", "a208", "a209", "a210", "a211", "a212", "a213", "a214", "a215", "a216", "a217", "a218", "a219", "a220", "a221", "a222", "a223", "a224", "a225", "a226", "a227", "a228", "a229", "a230", "a231", "a232", "a233", "a234", "a235", "a236", "a237", "a238", "a239", "a240", "a241", "a242", "a243", "a244", "a245", "a246", "a247", "a248", "a249", "a250", "a251", "a252", "a253", "a254", "a255");
            else asm volatile(
                "ds_read_b64 v[192:193], %[kb] offset:32\n\t"
                "ds_read_b64 v[194:195], %[kb] offset:40\n\t"
                "ds_read_b128 v[196:199], %[ub] offset:5040\n\t"
                "ds_read_b128 v[200:203], %[ub] offset:2480\n\t"
                "ds_read_b64 v[204:205], %[kb] offset:2080\n\t"
                "ds_read_b64 v[206:207], %[kb] offset:2088\n\t"
                "ds_read_b64 v[208:209], %[kb] offset:0\n\t"
                "ds_read_b64 v[210:211], %[kb] offset:8\n\t"
                "ds_read_b128 v[212:215], %[ub] offset:5072\n\t"
                "ds_read_b128 v[216:219], %[ub] offset:2512\n\t"
                "ds_read_b64 v[220:221], %[kb] offset:2048\n\t"
                "ds_read_b64 v[222:223], %[kb] offset:2056\n\t"
                "s_waitcnt lgkmcnt(9)\n\t"
                "v_mfma_f32_32x32x16_bf16 %[a1], v[192:195], v[196:199], 0\n\t"
                "ds_read_b64 v[224:225], %[kb] offset:96\n\t"
                "ds_read_b64 v[226:227], %[kb] offset:104\n\t"
                "ds_read_b128 v[228:231], %[ub] offset:4960\n\t"
                "s_waitcnt lgkmcnt(11)\n\t"
                "v_mfma_f32_32x32x16_bf16 %[a0], v[192:195], v[200:203], 0\n\t"
                "ds_read_b128 v[232:235], %[ub] offset:2400\n\t"
                "s_add_u32 %[t1], %[ro], 2\n\t"
                "s_and_b32 %[t1], %[t1], 7\n\t"
                "s_lshl_b32 %[t1], %[t1], 18\n\t"
                "s_add_u32 %[t0], %[t1], 0\n\t"
                "buffer_load_dwordx4 a[128:131], %[go], %[so], %[t0] offen\n\t"
                "s_waitcnt lgkmcnt(10)\n\t"
                "v_mfma_f32_32x32x16_bf16 %[a1], v[204:207], v[200:203], %[a1]\n\t"
                "ds_read_b64 v[236:237], %[kb] offset:2144\n\t"
                "ds_read_b64 v[238:239], %[kb] offset:2152\n\t"
                "s_waitcnt lgkmcnt(9)\n\t"
                "v_mfma_f32_32x32x16_bf16 %[a1], v[208:211], v[212:215], %[a1]\n\t"
                "ds_read_b64 v[240:241], %[kb] offset:64\n\t"
                "ds_read_b64 v[242:243], %[kb] offset:72\n\t"
                "ds_read_b128 v[244:247], %[ub] offset:4992\n\t"
                "s_waitcnt lgkmcnt(11)\n\t"
                "v_mfma_f32_32x32x16_bf16 %[a0], v[208:211], v[216:219], %[a0]\n\t"
                "ds_read_b128 v[248:251], %[ub] offset:2432\n\t"
                "s_waitcnt lgkmcnt(10)\n\t"
                "v_mfma_f32_32x32x16_bf16 %[a1], v[220:223], v[216:219], %[a1]\n\t"
                "ds_read_b64 v[252:253], %[kb] offset:2112\n\t"
                "ds_read_b64 v[254:255], %[kb] offset:2120\n\t"
                "s_waitcnt lgkmcnt(9)\n\t"
                "v_mfma_f32_32x32x16_bf16 %[a1], v[224:227], v[228:231], %[a1]\n\t"
                "ds_read_b64 v[192:193], %[kb] offset:160\n\t"
                "ds_read_b64 v[194:195], %[kb] offset:168\n\t"
                "ds_read_b128 v[196:199], %[ub] offset:4880\n\t"
                "s_waitcnt lgkmcnt(11)\n\t"
                "v_mfma_f32_32x32x16_bf16 %[a0], v[224:227], v[232:235], %[a0]\n\t"
                "ds_read_b128 v[200:203], %[ub] offset:2320\n\t"
                "s_add_u32 %[t0], %[t1], 16384\n\t"
                "buffer_load_dwordx4 a[132:135], %[go], %[so], %[t0] offen\n\t"
                "s_waitcnt lgkmcnt(10)\n\t"
                "v_mfma_f32_32x32x16_bf16 %[a1], v[236:239], v[232:235], %[a1]\n\t"
                "ds_read_b64 v[204:205], %[kb] offset:2208\n\t"
                "ds_read_b64 v[206:207], %[kb] offset:2216\n\t"
                "s_waitcnt lgkmcnt(9)\n\t"
                "v_mfma_f32_32x32x16_bf16 %[a1], v[240:243], v[244:247], %[a1]\n\t"
                "ds_read_b64 v[208:209], %[kb] offset:128\n\t"
                "ds_read_b64 v[210:211], %[kb] offset:136\n\t"
                "ds_read_b128 v[212:215], %[ub] offset:4912\n\t"
                "s_waitcnt lgkmcnt(11)\n\t"
                "v_mfma_f32_32x32x16_bf16 %[a0], v[240:243], v[248:251], %[a0]\n\t"
                "ds_read_b128 v[216:219], %[ub] offset:2352\n\t"
                "s_waitcnt lgkmcnt(10)\n\t"
                "v_mfma_f32_32x32x16_bf16 %[a1], v[252:255], v[248:251], %[a1]\n\t"
                "ds_read_b64 v[220:221], %[kb] offset:2176\n\t"
                "ds_read_b64 v[222:223], %[kb] offset:2184\n\t"
                "s_waitcnt lgkmcnt(9)\n\t"
                "v_mfma_f32_32x32x16_bf16 %[a1], v[192:195], v[196:199], %[a1]\n\t"
                "ds_read_b64 v[224:225], %[kb] offset:224\n\t"
                "ds_read_b64 v[226:227], %[kb] offset:232\n\t"
                "ds_read_b128 v[228:231], %[ub] offset:4800\n\t"
                "s_waitcnt lgkmcnt(11)\n\t"
                "v_mfma_f32_32x32x16_bf16 %[a0], v[192:195], v[200:203], %[a0]\n\t"
                "ds_read_b128 v[232:235], %[ub] offset:2240\n\t"
                "s_add_u32 %[t0], %[t1], 32768\n\t"
                "buffer_load_dwordx4 a[136:139], %[go], %[so], %[t0] offen\n\t"
                "s_waitcnt lgkmcnt(10)\n\t"
                "v_mfma_f32_32x32x16_bf16 %[a1], v[204:207], v[200:203], %[a1]\n\t"
                "ds_read_b64 v[236:237], %[kb] offset:2272\n\t"
                "ds_read_b64 v[238:239], %[kb] offset:2280\n\t"
                "s_waitcnt lgkmcnt(9)\n\t"
                "v_mfma_f32_32x32x16_bf16 %[a1], v[208:211], v[212:215], %[a1]\n\t"
                "ds_read_b64 v[240:241], %[kb] offset:192\n\t"
                "ds_read_b64 v[242:243], %[kb] offset:200\n\t"
                "ds_read_b128 v[244:247], %[ub] offset:4832\n\t"
                "s_waitcnt lgkmcnt(11)\n\t"
                "v_mfma_f32_32x32x16_bf16 %[a0], v[208:211], v[216:219], %[a0]\n\t"
                "ds_read_b128 v[248:251], %[ub] offset:2272\n\t"
                "s_waitcnt lgkmcnt(10)\n\t"
                "v_mfma_f32_32x32x16_bf16 %[a1], v[220:223], v[216:219], %[a1]\n\t"
                "ds_read_b64 v[252:253], %[kb] offset:2240\n\t"
                "ds_read_b64 v[254:255], %[kb] offset:2248\n\t"
                "s_waitcnt lgkmcnt(9)\n\t"
                "v_mfma_f32_32x32x16_bf16 %[a1], v[224:227], v[228:231], %[a1]\n\t"
                "ds_read_b64 v[192:193], %[kb] offset:288\n\t"
                "ds_read_b64 v[194:195], %[kb] offset:296\n\t"
                "ds_read_b128 v[196:199], %[ub] offset:4720\n\t"
                "s_waitcnt lgkmcnt(11)\n\t"
                "v_mfma_f32_32x32x16_bf16 %[a0], v[224:227], v[232:235], %[a0]\n\t"
                "ds_read_b128 v[200:203], %[ub] offset:2160\n\t"
                "s_add_u32 %[t0], %[t1], 49152\n\t"
                "buffer_load_dwordx4 a[140:143], %[go], %[so], %[t0] offen\n\t"
                "s_waitcnt lgkmcnt(10)\n\t"
                "v_mfma_f32_32x32x16_bf16 %[a1], v[236:239], v[232:235], %[a1]\n\t"
                "ds_read_b64 v[204:205], %[kb] offset:2336\n\t"
                "ds_read_b64 v[206:207], %[kb] offset:2344\n\t"
                "s_waitcnt lgkmcnt(9)\n\t"
                "v_mfma_f32_32x32x16_bf16 %[a1], v[240:243], v[244:247], %[a1]\n\t"
                "ds_read_b64 v[208:209], %[kb] offset:256\n\t"
                "ds_read_b64 v[210:211], %[kb] offset:264\n\t"
                "ds_read_b128 v[212:215], %[ub] offset:4752\n\t"
                "s_waitcnt lgkmcnt(11)\n\t"
                "v_mfma_f32_32x32x16_bf16 %[a0], v[240:243], v[248:251], %[a0]\n\t"
                "ds_read_b128 v[216:219], %[ub] offset:2192\n\t"
                "s_add_u32 %[t0], %[t1], 65536\n\t"
                "buffer_load_dwordx4 a[144:147], %[go], %[so], %[t0] offen\n\t"
                "s_waitcnt lgkmcnt(10)\n\t"
                "v_mfma_f32_32x32x16_bf16 %[a1], v[252:255], v[248:251], %[a1]\n\t"
                "ds_read_b64 v[220:221], %[kb] offset:2304\n\t"
                "ds_read_b64 v[222:223], %[kb] offset:2312\n\t"
                "s_waitcnt lgkmcnt(9)\n\t"
                "v_mfma_f32_32x32x16_bf16 %[a1], v[192:195], v[196:199], %[a1]\n\t"
                "ds_read_b64 v[224:225], %[kb] offset:352\n\t"
                "ds_read_b64 v[226:227], %[kb] offset:360\n\t"
                "ds_read_b128 v[228:231], %[ub] offset:4640\n\t"
                "s_waitcnt lgkmcnt(11)\n\t"
                "v_mfma_f32_32x32x16_bf16 %[a0], v[192:195], v[200:203], %[a0]\n\t"
                "ds_read_b128 v[232:235], %[ub] offset:2080\n\t"
                "s_waitcnt lgkmcnt(10)\n\t"
                "v_mfma_f32_32x32x16_bf16 %[a1], v[204:207], v[200:203], %[a1]\n\t"
                "ds_read_b64 v[236:237], %[kb] offset:2400\n\t"
                "ds_read_b64 v[238:239], %[kb] offset:2408\n\t"
                "s_waitcnt lgkmcnt(9)\n\t"
                "v_mfma_f32_32x32x16_bf16 %[a1], v[208:211], v[212:215], %[a1]\n\t"
                "ds_read_b64 v[240:241], %[kb] offset:320\n\t"
                "ds_read_b64 v[242:243], %[kb] offset:328\n\t"
                "ds_read_b128 v[244:247], %[ub] offset:4672\n\t"
                "s_waitcnt lgkmcnt(11)\n\t"
                "v_mfma_f32_32x32x16_bf16 %[a0], v[208:211], v[216:219], %[a0]\n\t"
                "ds_read_b128 v[248:251], %[ub] offset:2112\n\t"
                "s_add_u32 %[t0], %[t1], 81920\n\t"
                "buffer_load_dwordx4 a[148:151], %[go], %[so], %[t0] offen\n\t"
                "s_waitcnt lgkmcnt(10)\n\t"
                "v_mfma_f32_32x32x16_bf16 %[a1], v[220:223], v[216:219], %[a1]\n\t"
                "ds_read_b64 v[252:253], %[kb] offset:2368\n\t"
                "ds_read_b64 v[254:255], %[kb] offset:2376\n\t"
                "s_waitcnt lgkmcnt(9)\n\t"
                "v_mfma_f32_32x32x16_bf16 %[a1], v[224:227], v[228:231], %[a1]\n\t"
                "ds_read_b64 v[192:193], %[kb] offset:416\n\t"
                "ds_read_b64 v[194:195], %[kb] offset:424\n\t"
                "ds_read_b128 v[196:199], %[ub] offset:4560\n\t"
                "s_waitcnt lgkmcnt(11)\n\t"
                "v_mfma_f32_32x32x16_bf16 %[a0], v[224:227], v[232:235], %[a0]\n\t"
                "ds_read_b128 v[200:203], %[ub] offset:2000\n\t"
                "s_waitcnt lgkmcnt(10)\n\t"
                "v_mfma_f32_32x32x16_bf16 %[a1], v[236:239], v[232:235], %[a1]\n\t"
                "ds_read_b64 v[204:205], %[kb] offset:2464\n\t"
                "ds_read_b64 v[206:207], %[kb] offset:2472\n\t"
                "s_waitcnt lgkmcnt(9)\n\t"
                "v_mfma_f32_32x32x16_bf16 %[a1], v[240:243], v[244:247], %[a1]\n\t"
                "ds_read_b64 v[208:209], %[kb] offset:384\n\t"
                "ds_read_b64 v[210:211], %[kb] offset:392\n\t"
                "ds_read_b128 v[212:215], %[ub] offset:4592\n\t"
                "s_waitcnt lgkmcnt(11)\n\t"
                "v_mfma_f32_32x32x16_bf16 %[a0], v[240:243], v[248:251], %[a0]\n\t"
                "ds_read_b128 v[216:219], %[ub] offset:2032\n\t"
                "s_add_u32 %[t0], %[t1], 98304\n\t"
                "buffer_load_dwordx4 a[152:155], %[go], %[so], %[t0] offen\n\t"
                "s_waitcnt lgkmcnt(10)\n\t"
                "v_mfma_f32_32x32x16_bf16 %[a1], v[252:255], v[248:251], %[a1]\n\t"
                "ds_read_b64 v[220:221], %[kb] offset:2432\n\t"
                "ds_read_b64 v[222:223], %[kb] offset:2440\n\t"
                "s_waitcnt lgkmcnt(9)\n\t"
                "v_mfma_f32_32x32x16_bf16 %[a1], v[192:195], v[196:199], %[a1]\n\t"
                "ds_read_b64 v[224:225], %[kb] offset:480\n\t"
                "ds_read_b64 v[226:227], %[kb] offset:488\n\t"
                "ds_read_b128 v[228:231], %[ub] offset:4480\n\t"
                "s_waitcnt lgkmcnt(11)\n\t"
                "v_mfma_f32_32x32x16_bf16 %[a0], v[192:195], v[200:203], %[a0]\n\t"
                "ds_read_b128 v[232:235], %[ub] offset:1920\n\t"
                "s_waitcnt lgkmcnt(10)\n\t"
                "v_mfma_f32_32x32x16_bf16 %[a1], v[204:207], v[200:203], %[a1]\n\t"
                "ds_read_b64 v[236:237], %[kb] offset:2528\n\t"
                "ds_read_b64 v[238:239], %[kb] offset:2536\n\t"
                "s_waitcnt lgkmcnt(9)\n\t"
                "v_mfma_f32_32x32x16_bf16 %[a1], v[208:211], v[212:215], %[a1]\n\t"
                "ds_read_b64 v[240:241], %[kb] offset:448\n\t"
                "ds_read_b64 v[242:243], %[kb] offset:456\n\t"
                "ds_read_b128 v[244:247], %[ub] offset:4512\n\t"
                "s_waitcnt lgkmcnt(11)\n\t"
                "v_mfma_f32_32x32x16_bf16 %[a0], v[208:211], v[216:219], %[a0]\n\t"
                "ds_read_b128 v[248:251], %[ub] offset:1952\n\t"
                "s_add_u32 %[t0], %[t1], 114688\n\t"
                "buffer_load_dwordx4 a[156:159], %[go], %[so], %[t0] offen\n\t"
                "s_waitcnt lgkmcnt(10)\n\t"
                "v_mfma_f32_32x32x16_bf16 %[a1], v[220:223], v[216:219], %[a1]\n\t"
                "ds_read_b64 v[252:253], %[kb] offset:2496\n\t"
                "ds_read_b64 v[254:255], %[kb] offset:2504\n\t"
                "s_waitcnt lgkmcnt(9)\n\t"
                "v_mfma_f32_32x32x16_bf16 %[a1], v[224:227], v[228:231], %[a1]\n\t"
                "ds_read_b64 v[192:193], %[kb] offset:544\n\t"
                "ds_read_b64 v[194:195], %[kb] offset:552\n\t"
                "ds_read_b128 v[196:199], %[ub] offset:4400\n\t"
                "s_waitcnt lgkmcnt(11)\n\t"
                "v_mfma_f32_32x32x16_bf16 %[a0], v[224:227], v[232:235], %[a0]\n\t"
                "ds_read_b128 v[200:203], %[ub] offset:1840\n\t"
                "s_add_u32 %[t1], %[ro], 2\n\t"
                "s_and_b32 %[t1], %[t1], 7\n\t"
                "s_lshl_b32 %[t1], %[t1], 18\n\t"
                "s_add_u32 %[t0], %[t1], 131072\n\t"
                "buffer_load_dwordx4 a[160:163], %[go], %[so], %[t0] offen\n\t"
                "s_waitcnt lgkmcnt(10)\n\t"
                "v_mfma_f32_32x32x16_bf16 %[a1], v[236:239], v[232:235], %[a1]\n\t"
                "ds_read_b64 v[204:205], %[kb] offset:2592\n\t"
                "ds_read_b64 v[206:207], %[kb] offset:2600\n\t"
                "s_waitcnt lgkmcnt(9)\n\t"
                "v_mfma_f32_32x32x16_bf16 %[a1], v[240:243], v[244:247], %[a1]\n\t"
                "ds_read_b64 v[208:209], %[kb] offset:512\n\t"
                "ds_read_b64 v[210:211], %[kb] offset:520\n\t"
                "ds_read_b128 v[212:215], %[ub] offset:4432\n\t"
                "s_waitcnt lgkmcnt(11)\n\t"
                "v_mfma_f32_32x32x16_bf16 %[a0], v[240:243], v[248:251], %[a0]\n\t"
                "ds_read_b128 v[216:219], %[ub] offset:1872\n\t"
                "s_waitcnt lgkmcnt(10)\n\t"
                "v_mfma_f32_32x32x16_bf16 %[a1], v[252:255], v[248:251], %[a1]\n\t"
                "ds_read_b64 v[220:221], %[kb] offset:2560\n\t"
                "ds_read_b64 v[222:223], %[kb] offset:2568\n\t"
                "s_waitcnt lgkmcnt(9)\n\t"
                "v_mfma_f32_32x32x16_bf16 %[a1], v[192:195], v[196:199], %[a1]\n\t"
                "ds_read_b64 v[224:225], %[kb] offset:608\n\t"
                "ds_read_b64 v[226:227], %[kb] offset:616\n\t"
                "ds_read_b128 v[228:231], %[ub] offset:4320\n\t"
                "s_waitcnt lgkmcnt(11)\n\t"
                "v_mfma_f32_32x32x16_bf16 %[a0], v[192:195], v[200:203], %[a0]\n\t"
                "ds_read_b128 v[232:235], %[ub] offset:1760\n\t"
                "s_add_u32 %[t0], %[t1], 147456\n\t"
                "buffer_load_dwordx4 a[164:167], %[go], %[so], %[t0] offen\n\t"
                "s_waitcnt lgkmcnt(10)\n\t"
                "v_mfma_f32_32x32x16_bf16 %[a1], v[204:207], v[200:203], %[a1]\n\t"
                "ds_read_b64 v[236:237], %[kb] offset:2656\n\t"
                "ds_read_b64 v[238:239], %[kb] offset:2664\n\t"
                "s_waitcnt lgkmcnt(9)\n\t"
                "v_mfma_f32_32x32x16_bf16 %[a1], v[208:211], v[212:215], %[a1]\n\t"
                "ds_read_b64 v[240:241], %[kb] offset:576\n\t"
                "ds_read_b64 v[242:243], %[kb] offset:584\n\t"
                "ds_read_b128 v[244:247], %[ub] offset:4352\n\t"
                "s_waitcnt lgkmcnt(11)\n\t"
                "v_mfma_f32_32x32x16_bf16 %[a0], v[208:211], v[216:219], %[a0]\n\t"
                "ds_read_b128 v[248:251], %[ub] offset:1792\n\t"
                "s_waitcnt lgkmcnt(10)\n\t"
                "v_mfma_f32_32x32x16_bf16 %[a1], v[220:223], v[216:219], %[a1]\n\t"
                "ds_read_b64 v[252:253], %[kb] offset:2624\n\t"
                "ds_read_b64 v[254:255], %[kb] offset:2632\n\t"
                "s_waitcnt lgkmcnt(9)\n\t"
                "v_mfma_f32_32x32x16_bf16 %[a1], v[224:227], v[228:231], %[a1]\n\t"
                "ds_read_b64 v[192:193], %[kb] offset:672\n\t"
                "ds_read_b64 v[194:195], %[kb] offset:680\n\t"
                "ds_read_b128 v[196:199], %[ub] offset:4240\n\t"
                "s_waitcnt lgkmcnt(11)\n\t"
                "v_mfma_f32_32x32x16_bf16 %[a0], v[224:227], v[232:235], %[a0]\n\t"
                "ds_read_b128 v[200:203], %[ub] offset:1680\n\t"
                "s_add_u32 %[t0], %[t1], 163840\n\t"
                "buffer_load_dwordx4 a[168:171], %[go], %[so], %[t0] offen\n\t"
                "s_waitcnt lgkmcnt(10)\n\t"
                "v_mfma_f32_32x32x16_bf16 %[a1], v[236:239], v[232:235], %[a1]\n\t"
                "ds_read_b64 v[204:205], %[kb] offset:2720\n\t"
                "ds_read_b64 v[206:207], %[kb] offset:2728\n\t"
                "s_waitcnt lgkmcnt(9)\n\t"
                "v_mfma_f32_32x32x16_bf16 %[a1], v[240:243], v[244:247], %[a1]\n\t"
                "ds_read_b64 v[208:209], %[kb] offset:640\n\t"
                "ds_read_b64 v[210:211], %[kb] offset:648\n\t"
                "ds_read_b128 v[212:215], %[ub] offset:4272\n\t"
                "s_waitcnt lgkmcnt(11)\n\t"
                "v_mfma_f32_32x32x16_bf16 %[a0], v[240:243], v[248:251], %[a0]\n\t"
                "ds_read_b128 v[216:219], %[ub] offset:1712\n\t"
                "s_waitcnt lgkmcnt(10)\n\t"
                "v_mfma_f32_32x32x16_bf16 %[a1], v[252:255], v[248:251], %[a1]\n\t"
                "ds_read_b64 v[220:221], %[kb] offset:2688\n\t"
                "ds_read_b64 v[222:223], %[kb] offset:2696\n\t"
                "s_waitcnt lgkmcnt(9)\n\t"
                "v_mfma_f32_32x32x16_bf16 %[a1], v[192:195], v[196:199], %[a1]\n\t"
                "ds_read_b64 v[224:225], %[kb] offset:736\n\t"
                "ds_read_b64 v[226:227], %[kb] offset:744\n\t"
                "ds_read_b128 v[228:231], %[ub] offset:4160\n\t"
                "s_waitcnt lgkmcnt(11)\n\t"
                "v_mfma_f32_32x32x16_bf16 %[a0], v[192:195], v[200:203], %[a0]\n\t"
                "ds_read_b128 v[232:235], %[ub] offset:1600\n\t"
                "s_add_u32 %[t0], %[t1], 180224\n\t"
                "buffer_load_dwordx4 a[172:175], %[go], %[so], %[t0] offen\n\t"
                "s_waitcnt lgkmcnt(10)\n\t"
                "v_mfma_f32_32x32x16_bf16 %[a1], v[204:207], v[200:203], %[a1]\n\t"
                "ds_read_b64 v[236:237], %[kb] offset:2784\n\t"
                "ds_read_b64 v[238:239], %[kb] offset:2792\n\t"
                "s_waitcnt lgkmcnt(9)\n\t"
                "v_mfma_f32_32x32x16_bf16 %[a1], v[208:211], v[212:215], %[a1]\n\t"
                "ds_read_b64 v[240:241], %[kb] offset:704\n\t"
                "ds_read_b64 v[242:243], %[kb] offset:712\n\t"
                "ds_read_b128 v[244:247], %[ub] offset:4192\n\t"
                "s_waitcnt lgkmcnt(11)\n\t"
                "v_mfma_f32_32x32x16_bf16 %[a0], v[208:211], v[216:219], %[a0]\n\t"
                "ds_read_b128 v[248:251], %[ub] offset:1632\n\t"
                "s_add_u32 %[t0], %[t1], 196608\n\t"
                "buffer_load_dwordx4 a[176:179], %[go], %[so], %[t0] offen\n\t"
                "s_waitcnt lgkmcnt(10)\n\t"
                "v_mfma_f32_32x32x16_bf16 %[a1], v[220:223], v[216:219], %[a1]\n\t"
                "ds_read_b64 v[252:253], %[kb] offset:2752\n\t"
                "ds_read_b64 v[254:255], %[kb] offset:2760\n\t"
                "s_waitcnt lgkmcnt(9)\n\t"
                "v_mfma_f32_32x32x16_bf16 %[a1], v[224:227], v[228:231], %[a1]\n\t"
                "ds_read_b64 v[192:193], %[kb] offset:800\n\t"
                "ds_read_b64 v[194:195], %[kb] offset:808\n\t"
                "ds_read_b128 v[196:199], %[ub] offset:4080\n\t"
                "s_waitcnt lgkmcnt(11)\n\t"
                "v_mfma_f32_32x32x16_bf16 %[a0], v[224:227], v[232:235], %[a0]\n\t"
                "ds_read_b128 v[200:203], %[ub] offset:1520\n\t"
                "s_waitcnt lgkmcnt(10)\n\t"
                "v_mfma_f32_32x32x16_bf16 %[a1], v[236:239], v[232:235], %[a1]\n\t"
                "ds_read_b64 v[204:205], %[kb] offset:2848\n\t"
                "ds_read_b64 v[206:207], %[kb] offset:2856\n\t"
                "s_waitcnt lgkmcnt(9)\n\t"
                "v_mfma_f32_32x32x16_bf16 %[a1], v[240:243], v[244:247], %[a1]\n\t"
                "ds_read_b64 v[208:209], %[kb] offset:768\n\t"
                "ds_read_b64 v[210:211], %[kb] offset:776\n\t"
                "ds_read_b128 v[212:215], %[ub] offset:4112\n\t"
                "s_waitcnt lgkmcnt(11)\n\t"
                "v_mfma_f32_32x32x16_bf16 %[a0], v[240:243], v[248:251], %[a0]\n\t"
                "ds_read_b128 v[216:219], %[ub] offset:1552\n\t"
                "s_add_u32 %[t0], %[t1], 212992\n\t"
                "buffer_load_dwordx4 a[180:183], %[go], %[so], %[t0] offen\n\t"
                "s_waitcnt lgkmcnt(10)\n\t"
                "v_mfma_f32_32x32x16_bf16 %[a1], v[252:255], v[248:251], %[a1]\n\t"
                "ds_read_b64 v[220:221], %[kb] offset:2816\n\t"
                "ds_read_b64 v[222:223], %[kb] offset:2824\n\t"
                "s_waitcnt lgkmcnt(9)\n\t"
                "v_mfma_f32_32x32x16_bf16 %[a1], v[192:195], v[196:199], %[a1]\n\t"
                "ds_read_b64 v[224:225], %[kb] offset:864\n\t"
                "ds_read_b64 v[226:227], %[kb] offset:872\n\t"
                "ds_read_b128 v[228:231], %[ub] offset:4000\n\t"
                "s_waitcnt lgkmcnt(11)\n\t"
                "v_mfma_f32_32x32x16_bf16 %[a0], v[192:195], v[200:203], %[a0]\n\t"
                "ds_read_b128 v[232:235], %[ub] offset:1440\n\t"
                "s_waitcnt lgkmcnt(10)\n\t"
                "v_mfma_f32_32x32x16_bf16 %[a1], v[204:207], v[200:203], %[a1]\n\t"
                "ds_read_b64 v[236:237], %[kb] offset:2912\n\t"
                "ds_read_b64 v[238:239], %[kb] offset:2920\n\t"
                "s_waitcnt lgkmcnt(9)\n\t"
                "v_mfma_f32_32x32x16_bf16 %[a1], v[208:211], v[212:215], %[a1]\n\t"
                "ds_read_b64 v[240:241], %[kb] offset:832\n\t"
                "ds_read_b64 v[242:243], %[kb] offset:840\n\t"
                "ds_read_b128 v[244:247], %[ub] offset:4032\n\t"
                "s_waitcnt lgkmcnt(11)\n\t"
                "v_mfma_f32_32x32x16_bf16 %[a0], v[208:211], v[216:219], %[a0]\n\t"
                "ds_read_b128 v[248:251], %[ub] offset:1472\n\t"
                "s_add_u32 %[t0], %[t1], 229376\n\t"
                "buffer_load_dwordx4 a[184:187], %[go], %[so], %[t0] offen\n\t"
                "s_waitcnt lgkmcnt(10)\n\t"
                "v_mfma_f32_32x32x16_bf16 %[a1], v[220:223], v[216:219], %[a1]\n\t"
                "ds_read_b64 v[252:253], %[kb] offset:2880\n\t"
                "ds_read_b64 v[254:255], %[kb] offset:2888\n\t"
                "s_waitcnt lgkmcnt(9)\n\t"
                "v_mfma_f32_32x32x16_bf16 %[a1], v[224:227], v[228:231], %[a1]\n\t"
                "ds_read_b64 v[192:193], %[kb] offset:928\n\t"
                "ds_read_b64 v[194:195], %[kb] offset:936\n\t"
                "ds_read_b128 v[196:199], %[ub] offset:3920\n\t"
                "s_waitcnt lgkmcnt(11)\n\t"
                "v_mfma_f32_32x32x16_bf16 %[a0], v[224:227], v[232:235], %[a0]\n\t"
                "ds_read_b128 v[200:203], %[ub] offset:1360\n\t"
                "s_waitcnt lgkmcnt(10)\n\t"
                "v_mfma_f32_32x32x16_bf16 %[a1], v[236:239], v[232:235], %[a1]\n\t"
                "ds_read_b64 v[204:205], %[kb] offset:2976\n\t"
                "ds_read_b64 v[206:207], %[kb] offset:2984\n\t"
                "s_waitcnt lgkmcnt(9)\n\t"
                "v_mfma_f32_32x32x16_bf16 %[a1], v[240:243], v[244:247], %[a1]\n\t"
                "ds_read_b64 v[208:209], %[kb] offset:896\n\t"
                "ds_read_b64 v[210:211], %[kb] offset:904\n\t"
                "ds_read_b128 v[212:215], %[ub] offset:3952\n\t"
                "s_waitcnt lgkmcnt(11)\n\t"
                "v_mfma_f32_32x32x16_bf16 %[a0], v[240:243], v[248:251], %[a0]\n\t"
                "ds_read_b128 v[216:219], %[ub] offset:1392\n\t"
                "s_add_u32 %[t0], %[t1], 245760\n\t"
                "buffer_load_dwordx4 a[188:191], %[go], %[so], %[t0] offen\n\t"
                "s_waitcnt lgkmcnt(10)\n\t"
                "v_mfma_f32_32x32x16_bf16 %[a1], v[252:255], v[248:251], %[a1]\n\t"
                "ds_read_b64 v[220:221], %[kb] offset:2944\n\t"
                "ds_read_b64 v[222:223], %[kb] offset:2952\n\t"
                "s_waitcnt lgkmcnt(9)\n\t"
                "v_mfma_f32_32x32x16_bf16 %[a1], v[192:195], v[196:199], %[a1]\n\t"
                "ds_read_b64 v[224:225], %[kb] offset:992\n\t"
                "ds_read_b64 v[226:227], %[kb] offset:1000\n\t"
                "ds_read_b128 v[228:231], %[ub] offset:3840\n\t"
                "s_waitcnt lgkmcnt(11)\n\t"
                "v_mfma_f32_32x32x16_bf16 %[a0], v[192:195], v[200:203], %[a0]\n\t"
                "ds_read_b128 v[232:235], %[ub] offset:1280\n\t"
                "s_add_u32 %[t1], %[ro], 3\n\t"
                "s_and_b32 %[t1], %[t1], 7\n\t"
                "s_lshl_b32 %[t1], %[t1], 18\n\t"
                "s_add_u32 %[t0], %[t1], 0\n\t"
                "buffer_load_dwordx4 a[192:195], %[go], %[so], %[t0] offen\n\t"
                "s_waitcnt lgkmcnt(10)\n\t"
                "v_mfma_f32_32x32x16_bf16 %[a1], v[204:207], v[200:203], %[a1]\n\t"
                "ds_read_b64 v[236:237], %[kb] offset:3040\n\t"
                "ds_read_b64 v[238:239], %[kb] offset:3048\n\t"
                "s_waitcnt lgkmcnt(9)\n\t"
                "v_mfma_f32_32x32x16_bf16 %[a1], v[208:211], v[212:215], %[a1]\n\t"
                "ds_read_b64 v[240:241], %[kb] offset:960\n\t"
                "ds_read_b64 v[242:243], %[kb] offset:968\n\t"
                "ds_read_b128 v[244:247], %[ub] offset:3872\n\t"
                "s_waitcnt lgkmcnt(11)\n\t"
                "v_mfma_f32_32x32x16_bf16 %[a0], v[208:211], v[216:219], %[a0]\n\t"
                "ds_read_b128 v[248:251], %[ub] offset:1312\n\t"
                "s_waitcnt lgkmcnt(10)\n\t"
                "v_mfma_f32_32x32x16_bf16 %[a1], v[220:223], v[216:219], %[a1]\n\t"
                "ds_read_b64 v[252:253], %[kb] offset:3008\n\t"
                "ds_read_b64 v[254:255], %[kb] offset:3016\n\t"
                "s_waitcnt lgkmcnt(9)\n\t"
                "v_mfma_f32_32x32x16_bf16 %[a1], v[224:227], v[228:231], %[a1]\n\t"
                "ds_read_b64 v[192:193], %[kb] offset:1056\n\t"
                "ds_read_b64 v[194:195], %[kb] offset:1064\n\t"
                "ds_read_b128 v[196:199], %[ub] offset:3760\n\t"
                "s_waitcnt lgkmcnt(11)\n\t"
                "v_mfma_f32_32x32x16_bf16 %[a0], v[224:227], v[232:235], %[a0]\n\t"
                "ds_read_b128 v[200:203], %[ub] offset:1200\n\t"
                "s_add_u32 %[t0], %[t1], 16384\n\t"
                "buffer_load_dwordx4 a[196:199], %[go], %[so], %[t0] offen\n\t"
                "s_waitcnt lgkmcnt(10)\n\t"
                "v_mfma_f32_32x32x16_bf16 %[a1], v[236:239], v[232:235], %[a1]\n\t"
                "ds_read_b64 v[204:205], %[kb] offset:3104\n\t"
                "ds_read_b64 v[206:207], %[kb] offset:3112\n\t"
                "s_waitcnt lgkmcnt(9)\n\t"
                "v_mfma_f32_32x32x16_bf16 %[a1], v[240:243], v[244:247], %[a1]\n\t"
                "ds_read_b64 v[208:209], %[kb] offset:1024\n\t"
                "ds_read_b64 v[210:211], %[kb] offset:1032\n\t"
                "ds_read_b128 v[212:215], %[ub] offset:3792\n\t"
                "s_waitcnt lgkmcnt(11)\n\t"
                "v_mfma_f32_32x32x16_bf16 %[a0], v[240:243], v[248:251], %[a0]\n\t"
                "ds_read_b128 v[216:219], %[ub] offset:1232\n\t"
                "s_waitcnt lgkmcnt(10)\n\t"
                "v_mfma_f32_32x32x16_bf16 %[a1], v[252:255], v[248:251], %[a1]\n\t"
                "ds_read_b64 v[220:221], %[kb] offset:3072\n\t"
                "ds_read_b64 v[222:223], %[kb] offset:3080\n\t"
                "s_waitcnt lgkmcnt(9)\n\t"
                "v_mfma_f32_32x32x16_bf16 %[a1], v[192:195], v[196:199], %[a1]\n\t"
                "ds_read_b64 v[224:225], %[kb] offset:1120\n\t"
                "ds_read_b64 v[226:227], %[kb] offset:1128\n\t"
                "ds_read_b128 v[228:231], %[ub] offset:3680\n\t"
                "s_waitcnt lgkmcnt(11)\n\t"
                "v_mfma_f32_32x32x16_bf16 %[a0], v[192:195], v[200:203], %[a0]\n\t"
                "ds_read_b128 v[232:235], %[ub] offset:1120\n\t"
                "s_add_u32 %[t0], %[t1], 32768\n\t"
                "buffer_load_dwordx4 a[200:203], %[go], %[so], %[t0] offen\n\t"
                "s_waitcnt lgkmcnt(10)\n\t"
                "v_mfma_f32_32x32x16_bf16 %[a1], v[204:207], v[200:203], %[a1]\n\t"
                "ds_read_b64 v[236:237], %[kb] offset:3168\n\t"
                "ds_read_b64 v[238:239], %[kb] offset:3176\n\t"
                "s_waitcnt lgkmcnt(9)\n\t"
                "v_mfma_f32_32x32x16_bf16 %[a1], v[208:211], v[212:215], %[a1]\n\t"
                "ds_read_b64 v[240:241], %[kb] offset:1088\n\t"
                "ds_read_b64 v[242:243], %[kb] offset:1096\n\t"
                "ds_read_b128 v[244:247], %[ub] offset:3712\n\t"
                "s_waitcnt lgkmcnt(11)\n\t"
                "v_mfma_f32_32x32x16_bf16 %[a0], v[208:211], v[216:219], %[a0]\n\t"
                "ds_read_b128 v[248:251], %[ub] offset:1152\n\t"
                "s_waitcnt lgkmcnt(10)\n\t"
                "v_mfma_f32_32x32x16_bf16 %[a1], v[220:223], v[216:219], %[a1]\n\t"
                "ds_read_b64 v[252:253], %[kb] offset:3136\n\t"
                "ds_read_b64 v[254:255], %[kb] offset:3144\n\t"
                "s_waitcnt lgkmcnt(9)\n\t"
                "v_mfma_f32_32x32x16_bf16 %[a1], v[224:227], v[228:231], %[a1]\n\t"
                "ds_read_b64 v[192:193], %[kb] offset:1184\n\t"
                "ds_read_b64 v[194:195], %[kb] offset:1192\n\t"
                "ds_read_b128 v[196:199], %[ub] offset:3600\n\t"
                "s_waitcnt lgkmcnt(11)\n\t"
                "v_mfma_f32_32x32x16_bf16 %[a0], v[224:227], v[232:235], %[a0]\n\t"
                "ds_read_b128 v[200:203], %[ub] offset:1040\n\t"
                "s_add_u32 %[t0], %[t1], 49152\n\t"
                "buffer_load_dwordx4 a[204:207], %[go], %[so], %[t0] offen\n\t"
                "s_waitcnt lgkmcnt(10)\n\t"
                "v_mfma_f32_32x32x16_bf16 %[a1], v[236:239], v[232:235], %[a1]\n\t"
                "ds_read_b64 v[204:205], %[kb] offset:3232\n\t"
                "ds_read_b64 v[206:207], %[kb] offset:3240\n\t"
                "s_waitcnt lgkmcnt(9)\n\t"
                "v_mfma_f32_32x32x16_bf16 %[a1], v[240:243], v[244:247], %[a1]\n\t"
                "ds_read_b64 v[208:209], %[kb] offset:1152\n\t"
                "ds_read_b64 v[210:211], %[kb] offset:1160\n\t"
                "ds_read_b128 v[212:215], %[ub] offset:3632\n\t"
                "s_waitcnt lgkmcnt(11)\n\t"
                "v_mfma_f32_32x32x16_bf16 %[a0], v[240:243], v[248:251], %[a0]\n\t"
                "ds_read_b128 v[216:219], %[ub] offset:1072\n\t"
                "s_add_u32 %[t0], %[t1], 65536\n\t"
                "buffer_load_dwordx4 a[208:211], %[go], %[so], %[t0] offen\n\t"
                "s_waitcnt lgkmcnt(10)\n\t"
                "v_mfma_f32_32x32x16_bf16 %[a1], v[252:255], v[248:251], %[a1]\n\t"
                "ds_read_b64 v[220:221], %[kb] offset:3200\n\t"
                "ds_read_b64 v[222:223], %[kb] offset:3208\n\t"
                "s_waitcnt lgkmcnt(9)\n\t"
                "v_mfma_f32_32x32x16_bf16 %[a1], v[192:195], v[196:199], %[a1]\n\t"
                "ds_read_b64 v[224:225], %[kb] offset:1248\n\t"
                "ds_read_b64 v[226:227], %[kb] offset:1256\n\t"
                "ds_read_b128 v[228:231], %[ub] offset:3520\n\t"
                "s_waitcnt lgkmcnt(11)\n\t"
                "v_mfma_f32_32x32x16_bf16 %[a0], v[192:195], v[200:203], %[a0]\n\t"
                "ds_read_b128 v[232:235], %[ub] offset:960\n\t"
                "s_waitcnt lgkmcnt(10)\n\t"
                "v_mfma_f32_32x32x16_bf16 %[a1], v[204:207], v[200:203], %[a1]\n\t"
                "ds_read_b64 v[236:237], %[kb] offset:3296\n\t"
                "ds_read_b64 v[238:239], %[kb] offset:3304\n\t"
                "s_waitcnt lgkmcnt(9)\n\t"
                "v_mfma_f32_32x32x16_bf16 %[a1], v[208:211], v[212:215], %[a1]\n\t"
                "ds_read_b64 v[240:241], %[kb] offset:1216\n\t"
                "ds_read_b64 v[242:243], %[kb] offset:1224\n\t"
                "ds_read_b128 v[244:247], %[ub] offset:3552\n\t"
                "s_waitcnt lgkmcnt(11)\n\t"
                "v_mfma_f32_32x32x16_bf16 %[a0], v[208:211], v[216:219], %[a0]\n\t"
                "ds_read_b128 v[248:251], %[ub] offset:992\n\t"
                "s_add_u32 %[t0], %[t1], 81920\n\t"
                "buffer_load_dwordx4 a[212:215], %[go], %[so], %[t0] offen\n\t"
                "s_waitcnt lgkmcnt(10)\n\t"
                "v_mfma_f32_32x32x16_bf16 %[a1], v[220:223], v[216:219], %[a1]\n\t"
                "ds_read_b64 v[252:253], %[kb] offset:3264\n\t"
                "ds_read_b64 v[254:255], %[kb] offset:3272\n\t"
                "s_waitcnt lgkmcnt(9)\n\t"
                "v_mfma_f32_32x32x16_bf16 %[a1], v[224:227], v[228:231], %[a1]\n\t"
                "ds_read_b64 v[192:193], %[kb] offset:1312\n\t"
                "ds_read_b64 v[194:195], %[kb] offset:1320\n\t"
                "ds_read_b128 v[196:199], %[ub] offset:3440\n\t"
                "s_waitcnt lgkmcnt(11)\n\t"
                "v_mfma_f32_32x32x16_bf16 %[a0], v[224:227], v[232:235], %[a0]\n\t"
                "ds_read_b128 v[200:203], %[ub] offset:880\n\t"
                "s_waitcnt lgkmcnt(10)\n\t"
                "v_mfma_f32_32x32x16_bf16 %[a1], v[236:239], v[232:235], %[a1]\n\t"
                "ds_read_b64 v[204:205], %[kb] offset:3360\n\t"
                "ds_read_b64 v[206:207], %[kb] offset:3368\n\t"
                "s_waitcnt lgkmcnt(9)\n\t"
                "v_mfma_f32_32x32x16_bf16 %[a1], v[240:243], v[244:247], %[a1]\n\t"
                "ds_read_b64 v[208:209], %[kb] offset:1280\n\t"
                "ds_read_b64 v[210:211], %[kb] offset:1288\n\t"
                "ds_read_b128 v[212:215], %[ub] offset:3472\n\t"
                "s_waitcnt lgkmcnt(11)\n\t"
                "v_mfma_f32_32x32x16_bf16 %[a0], v[240:243], v[248:251], %[a0]\n\t"
                "ds_read_b128 v[216:219], %[ub] offset:912\n\t"
                "s_add_u32 %[t0], %[t1], 98304\n\t"
                "buffer_load_dwordx4 a[216:219], %[go], %[so], %[t0] offen\n\t"
                "s_waitcnt lgkmcnt(10)\n\t"
                "v_mfma_f32_32x32x16_bf16 %[a1], v[252:255], v[248:251], %[a1]\n\t"
                "ds_read_b64 v[220:221], %[kb] offset:3328\n\t"
                "ds_read_b64 v[222:223], %[kb] offset:3336\n\t"
                "s_waitcnt lgkmcnt(9)\n\t"
                "v_mfma_f32_32x32x16_bf16 %[a1], v[192:195], v[196:199], %[a1]\n\t"
                "ds_read_b64 v[224:225], %[kb] offset:1376\n\t"
                "ds_read_b64 v[226:227], %[kb] offset:1384\n\t"
                "ds_read_b128 v[228:231], %[ub] offset:3360\n\t"
                "s_waitcnt lgkmcnt(11)\n\t"
                "v_mfma_f32_32x32x16_bf16 %[a0], v[192:195], v[200:203], %[a0]\n\t"
                "ds_read_b128 v[232:235], %[ub] offset:800\n\t"
                "s_waitcnt lgkmcnt(10)\n\t"
                "v_mfma_f32_32x32x16_bf16 %[a1], v[204:207], v[200:203], %[a1]\n\t"
                "ds_read_b64 v[236:237], %[kb] offset:3424\n\t"
                "ds_read_b64 v[238:239], %[kb] offset:3432\n\t"
                "s_waitcnt lgkmcnt(9)\n\t"
                "v_mfma_f32_32x32x16_bf16 %[a1], v[208:211], v[212:215], %[a1]\n\t"
                "ds_read_b64 v[240:241], %[kb] offset:1344\n\t"
                "ds_read_b64 v[242:243], %[kb] offset:1352\n\t"
                "ds_read_b128 v[244:247], %[ub] offset:3392\n\t"
                "s_waitcnt lgkmcnt(11)\n\t"
                "v_mfma_f32_32x32x16_bf16 %[a0], v[208:211], v[216:219], %[a0]\n\t"
                "ds_read_b128 v[248:251], %[ub] offset:832\n\t"
                "s_add_u32 %[t0], %[t1], 114688\n\t"
                "buffer_load_dwordx4 a[220:223], %[go], %[so], %[t0] offen\n\t"
                "s_waitcnt lgkmcnt(10)\n\t"
                "v_mfma_f32_32x32x16_bf16 %[a1], v[220:223], v[216:219], %[a1]\n\t"
                "ds_read_b64 v[252:253], %[kb] offset:3392\n\t"
                "ds_read_b64 v[254:255], %[kb] offset:3400\n\t"
                "s_waitcnt lgkmcnt(9)\n\t"
                "v_mfma_f32_32x32x16_bf16 %[a1], v[224:227], v[228:231], %[a1]\n\t"
                "ds_read_b64 v[192:193], %[kb] offset:1440\n\t"
                "ds_read_b64 v[194:195], %[kb] offset:1448\n\t"
                "ds_read_b128 v[196:199], %[ub] offset:3280\n\t"
                "s_waitcnt lgkmcnt(11)\n\t"
                "v_mfma_f32_32x32x16_bf16 %[a0], v[224:227], v[232:235], %[a0]\n\t"
                "ds_read_b128 v[200:203], %[ub] offset:720\n\t"
                "s_add_u32 %[t1], %[ro], 3\n\t"
                "s_and_b32 %[t1], %[t1], 7\n\t"
                "s_lshl_b32 %[t1], %[t1], 18\n\t"
                "s_add_u32 %[t0], %[t1], 131072\n\t"
                "buffer_load_dwordx4 a[224:227], %[go], %[so], %[t0] offen\n\t"
                "s_waitcnt lgkmcnt(10)\n\t"
                "v_mfma_f32_32x32x16_bf16 %[a1], v[236:239], v[232:235], %[a1]\n\t"
                "ds_read_b64 v[204:205], %[kb] offset:3488\n\t"
                "ds_read_b64 v[206:207], %[kb] offset:3496\n\t"
                "s_waitcnt lgkmcnt(9)\n\t"
                "v_mfma_f32_32x32x16_bf16 %[a1], v[240:243], v[244:247], %[a1]\n\t"
                "ds_read_b64 v[208:209], %[kb] offset:1408\n\t"
                "ds_read_b64 v[210:211], %[kb] offset:1416\n\t"
                "ds_read_b128 v[212:215], %[ub] offset:3312\n\t"
                "s_waitcnt lgkmcnt(11)\n\t"
                "v_mfma_f32_32x32x16_bf16 %[a0], v[240:243], v[248:251], %[a0]\n\t"
                "ds_read_b128 v[216:219], %[ub] offset:752\n\t"
                "s_waitcnt lgkmcnt(10)\n\t"
                "v_mfma_f32_32x32x16_bf16 %[a1], v[252:255], v[248:251], %[a1]\n\t"
                "ds_read_b64 v[220:221], %[kb] offset:3456\n\t"
                "ds_read_b64 v[222:223], %[kb] offset:3464\n\t"
                "s_waitcnt lgkmcnt(9)\n\t"
                "v_mfma_f32_32x32x16_bf16 %[a1], v[192:195], v[196:199], %[a1]\n\t"
                "ds_read_b64 v[224:225], %[kb] offset:1504\n\t"
                "ds_read_b64 v[226:227], %[kb] offset:1512\n\t"
                "ds_read_b128 v[228:231], %[ub] offset:3200\n\t"
                "s_waitcnt lgkmcnt(11)\n\t"
                "v_mfma_f32_32x32x16_bf16 %[a0], v[192:195], v[200:203], %[a0]\n\t"
                "ds_read_b128 v[232:235], %[ub] offset:640\n\t"
                "s_add_u32 %[t0], %[t1], 147456\n\t"
                "buffer_load_dwordx4 a[228:231], %[go], %[so], %[t0] offen\n\t"
                "s_waitcnt lgkmcnt(10)\n\t"
                "v_mfma_f32_32x32x16_bf16 %[a1], v[204:207], v[200:203], %[a1]\n\t"
                "ds_read_b64 v[236:237], %[kb] offset:3552\n\t"
                "ds_read_b64 v[238:239], %[kb] offset:3560\n\t"
                "s_waitcnt lgkmcnt(9)\n\t"
                "v_mfma_f32_32x32x16_bf16 %[a1], v[208:211], v[212:215], %[a1]\n\t"
                "ds_read_b64 v[240:241], %[kb] offset:1472\n\t"
                "ds_read_b64 v[242:243], %[kb] offset:1480\n\t"
                "ds_read_b128 v[244:247], %[ub] offset:3232\n\t"
                "s_waitcnt lgkmcnt(11)\n\t"
                "v_mfma_f32_32x32x16_bf16 %[a0], v[208:211], v[216:219], %[a0]\n\t"
                "ds_read_b128 v[248:251], %[ub] offset:672\n\t"
                "s_waitcnt lgkmcnt(10)\n\t"
                "v_mfma_f32_32x32x16_bf16 %[a1], v[220:223], v[216:219], %[a1]\n\t"
                "ds_read_b64 v[252:253], %[kb] offset:3520\n\t"
                "ds_read_b64 v[254:255], %[kb] offset:3528\n\t"
                "s_waitcnt lgkmcnt(9)\n\t"
                "v_mfma_f32_32x32x16_bf16 %[a1], v[224:227], v[228:231], %[a1]\n\t"
                "ds_read_b64 v[192:193], %[kb] offset:1568\n\t"
                "ds_read_b64 v[194:195], %[kb] offset:1576\n\t"
                "ds_read_b128 v[196:199], %[ub] offset:3120\n\t"
                "s_waitcnt lgkmcnt(11)\n\t"
                "v_mfma_f32_32x32x16_bf16 %[a0], v[224:227], v[232:235], %[a0]\n\t"
                "ds_read_b128 v[200:203], %[ub] offset:560\n\t"
                "s_add_u32 %[t0], %[t1], 163840\n\t"
                "buffer_load_dwordx4 a[232:235], %[go], %[so], %[t0] offen\n\t"
                "s_waitcnt lgkmcnt(10)\n\t"
                "v_mfma_f32_32x32x16_bf16 %[a1], v[236:239], v[232:235], %[a1]\n\t"
                "ds_read_b64 v[204:205], %[kb] offset:3616\n\t"
                "ds_read_b64 v[206:207], %[kb] offset:3624\n\t"
                "s_waitcnt lgkmcnt(9)\n\t"
                "v_mfma_f32_32x32x16_bf16 %[a1], v[240:243], v[244:247], %[a1]\n\t"
                "ds_read_b64 v[208:209], %[kb] offset:1536\n\t"
                "ds_read_b64 v[210:211], %[kb] offset:1544\n\t"
                "ds_read_b128 v[212:215], %[ub] offset:3152\n\t"
                "s_waitcnt lgkmcnt(11)\n\t"
                "v_mfma_f32_32x32x16_bf16 %[a0], v[240:243], v[248:251], %[a0]\n\t"
                "ds_read_b128 v[216:219], %[ub] offset:592\n\t"
                "s_waitcnt lgkmcnt(10)\n\t"
                "v_mfma_f32_32x32x16_bf16 %[a1], v[252:255], v[248:251], %[a1]\n\t"
                "ds_read_b64 v[220:221], %[kb] offset:3584\n\t"
                "ds_read_b64 v[222:223], %[kb] offset:3592\n\t"
                "s_waitcnt lgkmcnt(9)\n\t"
                "v_mfma_f32_32x32x16_bf16 %[a1], v[192:195], v[196:199], %[a1]\n\t"
                "ds_read_b64 v[224:225], %[kb] offset:1632\n\t"
                "ds_read_b64 v[226:227], %[kb] offset:1640\n\t"
                "ds_read_b128 v[228:231], %[ub] offset:3040\n\t"
                "s_waitcnt lgkmcnt(11)\n\t"
                "v_mfma_f32_32x32x16_bf16 %[a0], v[192:195], v[200:203], %[a0]\n\t"
                "ds_read_b128 v[232:235], %[ub] offset:480\n\t"
                "s_add_u32 %[t0], %[t1], 180224\n\t"
                "buffer_load_dwordx4 a[236:239], %[go], %[so], %[t0] offen\n\t"
                "s_waitcnt lgkmcnt(10)\n\t"
                "v_mfma_f32_32x32x16_bf16 %[a1], v[204:207], v[200:203], %[a1]\n\t"
                "ds_read_b64 v[236:237], %[kb] offset:3680\n\t"
                "ds_read_b64 v[238:239], %[kb] offset:3688\n\t"
                "s_waitcnt lgkmcnt(9)\n\t"
                "v_mfma_f32_32x32x16_bf16 %[a1], v[208:211], v[212:215], %[a1]\n\t"
                "ds_read_b64 v[240:241], %[kb] offset:1600\n\t"
                "ds_read_b64 v[242:243], %[kb] offset:1608\n\t"
                "ds_read_b128 v[244:247], %[ub] offset:3072\n\t"
                "s_waitcnt lgkmcnt(11)\n\t"
                "v_mfma_f32_32x32x16_bf16 %[a0], v[208:211], v[216:219], %[a0]\n\t"
                "ds_read_b128 v[248:251], %[ub] offset:512\n\t"
                "s_add_u32 %[t0], %[t1], 196608\n\t"
                "buffer_load_dwordx4 a[240:243], %[go], %[so], %[t0] offen\n\t"
                "s_waitcnt lgkmcnt(10)\n\t"
                "v_mfma_f32_32x32x16_bf16 %[a1], v[220:223], v[216:219], %[a1]\n\t"
                "ds_read_b64 v[252:253], %[kb] offset:3648\n\t"
                "ds_read_b64 v[254:255], %[kb] offset:3656\n\t"
                "s_waitcnt lgkmcnt(9)\n\t"
                "v_mfma_f32_32x32x16_bf16 %[a1], v[224:227], v[228:231], %[a1]\n\t"
                "ds_read_b64 v[192:193], %[kb] offset:1696\n\t"
                "ds_read_b64 v[194:195], %[kb] offset:1704\n\t"
                "ds_read_b128 v[196:199], %[ub] offset:2960\n\t"
                "s_waitcnt lgkmcnt(11)\n\t"
                "v_mfma_f32_32x32x16_bf16 %[a0], v[224:227], v[232:235], %[a0]\n\t"
                "ds_read_b128 v[200:203], %[ub] offset:400\n\t"
                "s_waitcnt lgkmcnt(10)\n\t"
                "v_mfma_f32_32x32x16_bf16 %[a1], v[236:239], v[232:235], %[a1]\n\t"
                "ds_read_b64 v[204:205], %[kb] offset:3744\n\t"
                "ds_read_b64 v[206:207], %[kb] offset:3752\n\t"
                "s_waitcnt lgkmcnt(9)\n\t"
                "v_mfma_f32_32x32x16_bf16 %[a1], v[240:243], v[244:247], %[a1]\n\t"
                "ds_read_b64 v[208:209], %[kb] offset:1664\n\t"
                "ds_read_b64 v[210:211], %[kb] offset:1672\n\t"
                "ds_read_b128 v[212:215], %[ub] offset:2992\n\t"
                "s_waitcnt lgkmcnt(11)\n\t"
                "v_mfma_f32_32x32x16_bf16 %[a0], v[240:243], v[248:251], %[a0]\n\t"
                "ds_read_b128 v[216:219], %[ub] offset:432\n\t"
                "s_add_u32 %[t0], %[t1], 212992\n\t"
                "buffer_load_dwordx4 a[244:247], %[go], %[so], %[t0] offen\n\t"
                "s_waitcnt lgkmcnt(10)\n\t"
                "v_mfma_f32_32x32x16_bf16 %[a1], v[252:255], v[248:251], %[a1]\n\t"
                "ds_read_b64 v[220:221], %[kb] offset:3712\n\t"
                "ds_read_b64 v[222:223], %[kb] offset:3720\n\t"
                "s_waitcnt lgkmcnt(9)\n\t"
                "v_mfma_f32_32x32x16_bf16 %[a1], v[192:195], v[196:199], %[a1]\n\t"
                "ds_read_b64 v[224:225], %[kb] offset:1760\n\t"
                "ds_read_b64 v[226:227], %[kb] offset:1768\n\t"
                "ds_read_b128 v[228:231], %[ub] offset:2880\n\t"
                "s_waitcnt lgkmcnt(11)\n\t"
                "v_mfma_f32_32x32x16_bf16 %[a0], v[192:195], v[200:203], %[a0]\n\t"
                "ds_read_b128 v[232:235], %[ub] offset:320\n\t"
                "s_waitcnt lgkmcnt(10)\n\t"
                "v_mfma_f32_32x32x16_bf16 %[a1], v[204:207], v[200:203], %[a1]\n\t"
                "ds_read_b64 v[236:237], %[kb] offset:3808\n\t"
                "ds_read_b64 v[238:239], %[kb] offset:3816\n\t"
                "s_waitcnt lgkmcnt(9)\n\t"
                "v_mfma_f32_32x32x16_bf16 %[a1], v[208:211], v[212:215], %[a1]\n\t"
                "ds_read_b64 v[240:241], %[kb] offset:1728\n\t"
                "ds_read_b64 v[242:243], %[kb] offset:1736\n\t"
                "ds_read_b128 v[244:247], %[ub] offset:2912\n\t"
                "s_waitcnt lgkmcnt(11)\n\t"
                "v_mfma_f32_32x32x16_bf16 %[a0], v[208:211], v[216:219], %[a0]\n\t"
                "ds_read_b128 v[248:251], %[ub] offset:352\n\t"
                "s_add_u32 %[t0], %[t1], 229376\n\t"
                "buffer_load_dwordx4 a[248:251], %[go], %[so], %[t0] offen\n\t"
                "s_waitcnt lgkmcnt(10)\n\t"
                "v_mfma_f32_32x32x16_bf16 %[a1], v[220:223], v[216:219], %[a1]\n\t"
                "ds_read_b64 v[252:253], %[kb] offset:3776\n\t"
                "ds_read_b64 v[254:255], %[kb] offset:3784\n\t"
                "s_waitcnt lgkmcnt(9)\n\t"
                "v_mfma_f32_32x32x16_bf16 %[a1], v[224:227], v[228:231], %[a1]\n\t"
                "ds_read_b64 v[192:193], %[kb] offset:1824\n\t"
                "ds_read_b64 v[194:195], %[kb] offset:1832\n\t"
                "ds_read_b128 v[196:199], %[ub] offset:2800\n\t"
                "s_waitcnt lgkmcnt(11)\n\t"
                "v_mfma_f32_32x32x16_bf16 %[a0], v[224:227], v[232:235], %[a0]\n\t"
                "ds_read_b128 v[200:203], %[ub] offset:240\n\t"
                "s_waitcnt lgkmcnt(10)\n\t"
                "v_mfma_f32_32x32x16_bf16 %[a1], v[236:239], v[232:235], %[a1]\n\t"
                "ds_read_b64 v[204:205], %[kb] offset:3872\n\t"
                "ds_read_b64 v[206:207], %[kb] offset:3880\n\t"
                "s_waitcnt lgkmcnt(9)\n\t"
                "v_mfma_f32_32x32x16_bf16 %[a1], v[240:243], v[244:247], %[a1]\n\t"
                "ds_read_b64 v[208:209], %[kb] offset:1792\n\t"
                "ds_read_b64 v[210:211], %[kb] offset:1800\n\t"
                "ds_read_b128 v[212:215], %[ub] offset:2832\n\t"
                "s_waitcnt lgkmcnt(11)\n\t"
                "v_mfma_f32_32x32x16_bf16 %[a0], v[240:243], v[248:251], %[a0]\n\t"
                "ds_read_b128 v[216:219], %[ub] offset:272\n\t"
                "s_add_u32 %[t0], %[t1], 245760\n\t"
                "buffer_load_dwordx4 a[252:255], %[go], %[so], %[t0] offen\n\t"
                "s_waitcnt lgkmcnt(10)\n\t"
                "v_mfma_f32_32x32x16_bf16 %[a1], v[252:255], v[248:251], %[a1]\n\t"
                "ds_read_b64 v[220:221], %[kb] offset:3840\n\t"
                "ds_read_b64 v[222:223], %[kb] offset:3848\n\t"
                "s_waitcnt lgkmcnt(9)\n\t"
                "v_mfma_f32_32x32x16_bf16 %[a1], v[192:195], v[196:199], %[a1]\n\t"
                "ds_read_b64 v[224:225], %[kb] offset:1888\n\t"
                "ds_read_b64 v[226:227], %[kb] offset:1896\n\t"
                "ds_read_b128 v[228:231], %[ub] offset:2720\n\t"
                "s_waitcnt lgkmcnt(11)\n\t"
                "v_mfma_f32_32x32x16_bf16 %[a0], v[192:195], v[200:203], %[a0]\n\t"
                "ds_read_b128 v[232:235], %[ub] offset:160\n\t"
                "s_waitcnt lgkmcnt(10)\n\t"
                "v_mfma_f32_32x32x16_bf16 %[a1], v[204:207], v[200:203], %[a1]\n\t"
                "ds_read_b64 v[236:237], %[kb] offset:3936\n\t"
                "ds_read_b64 v[238:239], %[kb] offset:3944\n\t"
                "s_waitcnt lgkmcnt(9)\n\t"
                "v_mfma_f32_32x32x16_bf16 %[a1], v[208:211], v[212:215], %[a1]\n\t"
                "ds_read_b64 v[240:241], %[kb] offset:1856\n\t"
                "ds_read_b64 v[242:243], %[kb] offset:1864\n\t"
                "ds_read_b128 v[244:247], %[ub] offset:2752\n\t"
                "s_waitcnt lgkmcnt(11)\n\t"
                "v_mfma_f32_32x32x16_bf16 %[a0], v[208:211], v[216:219], %[a0]\n\t"
                "ds_read_b128 v[248:251], %[ub] offset:192\n\t"
                "s_waitcnt lgkmcnt(10)\n\t"
                "v_mfma_f32_32x32x16_bf16 %[a1], v[220:223], v[216:219], %[a1]\n\t"
                "ds_read_b64 v[252:253], %[kb] offset:3904\n\t"
                "ds_read_b64 v[254:255], %[kb] offset:3912\n\t"
                "s_waitcnt lgkmcnt(9)\n\t"
                "v_mfma_f32_32x32x16_bf16 %[a1], v[224:227], v[228:231], %[a1]\n\t"
                "ds_read_b64 v[192:193], %[kb] offset:1952\n\t"
                "ds_read_b64 v[194:195], %[kb] offset:1960\n\t"
                "ds_read_b128 v[196:199], %[ub] offset:2640\n\t"
                "s_waitcnt lgkmcnt(11)\n\t"
                "v_mfma_f32_32x32x16_bf16 %[a0], v[224:227], v[232:235], %[a0]\n\t"
                "ds_read_b128 v[200:203], %[ub] offset:80\n\t"
                "s_waitcnt lgkmcnt(10)\n\t"
                "v_mfma_f32_32x32x16_bf16 %[a1], v[236:239], v[232:235], %[a1]\n\t"
                "ds_read_b64 v[204:205], %[kb] offset:4000\n\t"
                "ds_read_b64 v[206:207], %[kb] offset:4008\n\t"
                "s_waitcnt lgkmcnt(9)\n\t"
                "v_mfma_f32_32x32x16_bf16 %[a1], v[240:243], v[244:247], %[a1]\n\t"
                "ds_read_b64 v[208:209], %[kb] offset:1920\n\t"
                "ds_read_b64 v[210:211], %[kb] offset:1928\n\t"
                "ds_read_b128 v[212:215], %[ub] offset:2672\n\t"
                "s_waitcnt lgkmcnt(11)\n\t"
                "v_mfma_f32_32x32x16_bf16 %[a0], v[240:243], v[248:251], %[a0]\n\t"
                "ds_read_b128 v[216:219], %[ub] offset:112\n\t"
                "s_waitcnt lgkmcnt(10)\n\t"
                "v_mfma_f32_32x32x16_bf16 %[a1], v[252:255], v[248:251], %[a1]\n\t"
                "ds_read_b64 v[220:221], %[kb] offset:3968\n\t"
                "ds_read_b64 v[222:223], %[kb] offset:3976\n\t"
                "s_waitcnt lgkmcnt(9)\n\t"
                "v_mfma_f32_32x32x16_bf16 %[a1], v[192:195], v[196:199], %[a1]\n\t"
                "ds_read_b64 v[224:225], %[kb] offset:2016\n\t"
                "ds_read_b64 v[226:227], %[kb] offset:2024\n\t"
                "ds_read_b128 v[228:231], %[ub] offset:2560\n\t"
                "s_waitcnt lgkmcnt(11)\n\t"
                "v_mfma_f32_32x32x16_bf16 %[a0], v[192:195], v[200:203], %[a0]\n\t"
                "ds_read_b128 v[232:235], %[ub] offset:0\n\t"
                "s_waitcnt lgkmcnt(10)\n\t"
                "v_mfma_f32_32x32x16_bf16 %[a1], v[204:207], v[200:203], %[a1]\n\t"
                "ds_read_b64 v[236:237], %[kb] offset:4064\n\t"
                "ds_read_b64 v[238:239], %[kb] offset:4072\n\t"
                "s_waitcnt lgkmcnt(9)\n\t"
                "v_mfma_f32_32x32x16_bf16 %[a1], v[208:211], v[212:215], %[a1]\n\t"
                "ds_read_b64 v[240:241], %[kb] offset:1984\n\t"
                "ds_read_b64 v[242:243], %[kb] offset:1992\n\t"
                "ds_read_b128 v[244:247], %[ub] offset:2592\n\t"
                "s_waitcnt lgkmcnt(11)\n\t"
                "v_mfma_f32_32x32x16_bf16 %[a0], v[208:211], v[216:219], %[a0]\n\t"
                "ds_read_b128 v[248:251], %[ub] offset:32\n\t"
                "s_waitcnt lgkmcnt(10)\n\t"
                "v_mfma_f32_32x32x16_bf16 %[a1], v[220:223], v[216:219], %[a1]\n\t"
                "ds_read_b64 v[252:253], %[kb] offset:4032\n\t"
                "ds_read_b64 v[254:255], %[kb] offset:4040\n\t"
                "s_waitcnt lgkmcnt(9)\n\t"
                "v_mfma_f32_32x32x16_bf16 %[a1], v[224:227], v[228:231], %[a1]\n\t"
                "s_waitcnt lgkmcnt(8)\n\t"
                "v_mfma_f32_32x32x16_bf16 %[a0], v[224:227], v[232:235], %[a0]\n\t"
                "s_waitcnt lgkmcnt(6)\n\t"
                "v_mfma_f32_32x32x16_bf16 %[a1], v[236:239], v[232:235], %[a1]\n\t"
                "s_waitcnt lgkmcnt(3)\n\t"
                "v_mfma_f32_32x32x16_bf16 %[a1], v[240:243], v[244:247], %[a1]\n\t"
                "s_waitcnt lgkmcnt(2)\n\t"
                "v_mfma_f32_32x32x16_bf16 %[a0], v[240:243], v[248:251], %[a0]\n\t"
                "s_waitcnt lgkmcnt(0)\n\t"
                "v_mfma_f32_32x32x16_bf16 %[a1], v[252:255], v[248:251], %[a1]\n\t"
                "s_waitcnt vmcnt(0)\n\t"
                "s_nop 7\n\t"
                "s_nop 3\n\t"
                : [a0] "=&v"(acc0), [a1] "=&v"(acc1), [t0] "=&s"(t0), [t1] "=&s"(t1)
                : [kb] "v"(kb), [ub] "v"(ub0 + c8 * CV_U), [go] "v"(go), [so] "s"(so), [ro] "s"(ro) : "memory", "scc", "v192", "v193", "v194", "v195", "v196", "v197", "v198", "v199", "v200", "v201", "v202", "v203", "v204", "v205", "v206", "v207", "v208", "v209", "v210", "v211", "v212", "v213", "v214", "v215", "v216", "v217", "v218", "v219", "v220", "v221", "v222", "v223", "v224", "v225", "v226", "v227", "v228", "v229", "v230", "v231", "v232", "v233", "v234", "v235", "v236", "v237", "v238", "v239", "v240", "v241", "v242", "v243", "v244", "v245", "v246", "v247", "v248", "v249", "v250", "v251", "v252", "v253", "v254", "v255", "a0", "a1", "a2", "a3", "a4", "a5", "a6", "a7", "a8", "a9", "a10", "a11", "a12", "a13", "a14", "a15", "a16", "a17", "a18", "a19", "a20", "a21", "a22", "a23", "a24", "a25", "a26", "a27", "a28", "a29", "a30", "a31", "a32", "a33", "a34", "a35", "a36", "a37", "a38", "a39", "a40", "a41", "a42", "a43", "a44", "a45", "a46", "a47", "a48", "a49", "a50", "a51", "a52", "a53", "a54", "a55", "a56", "a57", "a58", "a59", "a60", "a61", "a62", "a63", "a64", "a65", "a66", "a67", "a68", "a69", "a70", "a71", "a72", "a73", "a74", "a75", "a76", "a77", "a78", "a79", "a80", "a81", "a82", "a83", "a84", "a85", "a86", "a87", "a88", "a89", "a90", "a91", "a92", "a93", "a94", "a95", "a96", "a97", "a98", "a99", "a100", "a101", "a102", "a103", "a104", "a105", "a106", "a107", "a108", "a109", "a110", "a111", "a112", "a113", "a114", "a115", "a116", "a117", "a118", "a119", "a120", "a121", "a122", "a123", "a124", "a125", "a126", "a127", "a128", "a129", "a130", "a131", "a132", "a133", "a134", "a135", "a136", "a137", "a138", "a139", "a140", "a141", "a142", "a143", "a144", "a145", "a146", "a147", "a148", "a149", "a150", "a151", "a152", "a153", "a154", "a155", "a156", "a157", "a158", "a159", "a160", "a161", "a162", "a163", "a164", "a165", "a166", "a167", "a168", "a169", "a170", "a171", "a172", "a173", "a174", "a175", "a176", "a177", "a178", "a179", "a180", "a181", "a182", "a183", "a184", "a185", "a186", "a187", "a188", "a189", "a190", "a191", "a192", "a193", "a194", "a195", "a196", "a197", "a198", "a199", "a200", "a201", "a202", "a203", "a204", "a205", "a206", "a207", "a208", "a209", "a210", "a211", "a212", "a213", "a214", "a215", "a216", "a217", "a218", "a219", "a220", "a221", "a222", "a223", "a224", "a225", "a226", "a227", "a228", "a229", "a230", "a231", "a232", "a233", "a234", "a235", "a236", "a237", "a238", "a239", "a240", "a241", "a242", "a243", "a244", "a245", "a246", "a247", "a248", "a249", "a250", "a251", "a252", "a253", "a254", "a255");
            unsigned char* uc = lds + c8 * CV_U + (32 + b) * CV_RP + kg * 8;
#pragma unroll
            for (int q = 0; q < 4; ++q) {
                *(v2u*)(uc + q * 16) = (v2u){pack2(acc0[4 * q], acc0[4 * q + 1]), pack2(acc0[4 * q + 2], acc0[4 * q + 3])};
                *(v2u*)(uc + 32 * CV_RP + q * 16) = (v2u){pack2(acc1[4 * q], acc1[4 * q + 1]), pack2(acc1[4 * q + 2], acc1[4 * q + 3])}; }
            __builtin_amdgcn_wave_barrier();
        }
    __syncthreads();
#pragma unroll
    for (int G = 0; G < 4; ++G) { const v4u lo = *(const v4u*)(urow + G * 16), hh = *(const v4u*)(urow + CV_U + G * 16); v4u o[2];
#pragma unroll
        for (int i = 0; i < 8; ++i) { const unsigned u0 = ur[G][(7 - i) >> 1], u1 = ur[4 + G][(7 - i) >> 1], qq = qr[2 * G + (i >> 2)][i & 3];
            const unsigned ud = pack2(((i & 1) ? blo(u0) : bhi(u0)) * blo(dv), ((i & 1) ? blo(u1) : bhi(u1)) * bhi(dv));
            const unsigned y = pack2(((i & 1) ? bhi(lo[i >> 1]) : blo(lo[i >> 1])) + blo(ud), ((i & 1) ? bhi(hh[i >> 1]) : blo(hh[i >> 1])) + bhi(ud));
            o[i >> 2][i & 3] = pack2(blo(qq) * blo(y), bhi(qq) * bhi(y)); }
        st16(r, W_A + un * 32768 + 2 * G * 4096 + tid * 16, o[0]); st16(r, W_A + un * 32768 + (2 * G + 1) * 4096 + tid * 16, o[1]); }
    DRAIN(); __syncthreads();
    if (tid == 0) __hip_atomic_fetch_add((unsigned*)(p.ws + F_CVY + (un >> 3) * 128), 1u, __ATOMIC_RELAXED, __HIP_MEMORY_SCOPE_AGENT);
    return true;
}

__device__ __forceinline__ void proj_block(const Params& p, unsigned char* lds, const int wv, const int h, const int c) {
    const int w = wv, l = otid() & 63, n = l & 15, g = l >> 4, tm = w >> 1, tn = w & 1;
    const unsigned lb = __builtin_amdgcn_readfirstlane((unsigned)(size_t)lds);
    const v4u sx = mk_srd(p.Aq, S * D * 2), sq = mk_srd(c < 2 ? p.Wq : p.Wk, 1024 * 4096 * 2), sv = mk_srd(p.Wv, 1024 * 4096 * 2), sw = mk_srd(p.ws, W_TOTAL);
    const unsigned lx = lb + (32 * tm + n) * 128 + ((g ^ (n & 7)) << 4), lr = lb + 131072, rr = 8 * g + (n >> 2);
    const int cv = ((c & 1) ^ 1) * 2 + (c >> 1);
    const unsigned aq = (c < 2 ? W_QX : W_KX) + (tm * 32 + h) * 8192 + (2 * (c & 1) + tn) * 1024 + l * 16;
    unsigned t0, xo, so, cn;
    asm volatile(
        "s_nop 4\n\t"
        "s_mov_b32 %[xo], 0\n\t"
        "s_mov_b32 %[so], 0\n\t"
        "s_mov_b32 %[cn], 1\n\t"
        "s_add_u32 m0, %[ld], 0\n\t"
        "s_add_u32 %[t0], %[xo], 0\n\t"
        "buffer_load_dwordx4 v100, %[sx], %[t0] offen lds\n\t"
        "s_add_u32 m0, %[ld], 1024\n\t"
        "s_add_u32 %[t0], %[xo], 16384\n\t"
        "buffer_load_dwordx4 v100, %[sx], %[t0] offen lds\n\t"
        "s_add_u32 m0, %[ld], 8192\n\t"
        "s_add_u32 %[t0], %[xo], 128\n\t"
        "buffer_load_dwordx4 v100, %[sx], %[t0] offen lds\n\t"
        "s_add_u32 m0, %[ld], 9216\n\t"
        "s_add_u32 %[t0], %[xo], 16512\n\t"
        "buffer_load_dwordx4 v100, %[sx], %[t0] offen lds\n\t"
        "s_add_u32 m0, %[ld], 16384\n\t"
        "s_add_u32 %[t0], %[xo], 256\n\t"
        "buffer_load_dwordx4 v100, %[sx], %[t0] offen lds\n\t"
        "s_add_u32 m0, %[ld], 17408\n\t"
        "s_add_u32 %[t0], %[xo], 16640\n\t"
        "buffer_load_dwordx4 v100, %[sx], %[t0] offen lds\n\t"
        "s_add_u32 m0, %[ld], 24576\n\t"
        "s_add_u32 %[t0], %[xo], 384\n\t"
        "buffer_load_dwordx4 v100, %[sx], %[t0] offen lds\n\t"
        "s_add_u32 m0, %[ld], 25600\n\t"
        "s_add_u32 %[t0], %[xo], 16768\n\t"
        "buffer_load_dwordx4 v100, %[sx], %[t0] offen lds\n\t"
        "s_mov_b32 %[t0], 0\n\t"
        "buffer_load_dwordx4 a[192:195], v101, %[sq], %[t0] offen\n\t"
        "buffer_load_dwordx4 a[200:203], v112, %[sv], %[t0] offen\n\t"
        "s_mov_b32 %[t0], 262144\n\t"
        "buffer_load_dwordx4 a[196:199], v101, %[sq], %[t0] offen\n\t"
        "s_mov_b32 %[t0], 524288\n\t"
        "buffer_load_dwordx4 a[204:207], v101, %[sq], %[t0] offen\n\t"
        "buffer_load_dwordx4 a[212:215], v112, %[sv], %[t0] offen\n\t"
        "s_mov_b32 %[t0], 786432\n\t"
        "buffer_load_dwordx4 a[208:211], v101, %[sq], %[t0] offen\n\t"
        "s_mov_b32 %[t0], 1048576\n\t"
        "buffer_load_dwordx4 a[216:219], v101, %[sq], %[t0] offen\n\t"
        "buffer_load_dwordx4 a[224:227], v112, %[sv], %[t0] offen\n\t"
        "s_mov_b32 %[t0], 1310720\n\t"
        "buffer_load_dwordx4 a[220:223], v101, %[sq], %[t0] offen\n\t"
        "s_mov_b32 %[t0], 1572864\n\t"
        "buffer_load_dwordx4 a[228:231], v101, %[sq], %[t0] offen\n\t"
        "buffer_load_dwordx4 a[236:239], v112, %[sv], %[t0] offen\n\t"
        "s_mov_b32 %[t0], 1835008\n\t"
        "buffer_load_dwordx4 a[232:235], v101, %[sq], %[t0] offen\n\t"
        "s_waitcnt vmcnt(9)\n\t"
        "ds_write_b128 v102, a[192:195] offset:0\n\t"
        "ds_write_b128 v102, a[196:199] offset:4096\n\t"
        "ds_write_b128 v116, a[200:203] offset:8192\n\t"
        "s_waitcnt lgkmcnt(0)\n\t"
        "s_barrier\n\t"
        "ds_read_b64_tr_b16 a[0:1], v103 offset:0\n\t"
        "ds_read_b64_tr_b16 a[2:3], v103 offset:512\n\t"
        "ds_read_b64_tr_b16 a[4:5], v103 offset:8\n\t"
        "ds_read_b64_tr_b16 a[6:7], v103 offset:520\n\t"
        "ds_read_b64_tr_b16 a[8:9], v104 offset:0\n\t"
        "ds_read_b64_tr_b16 a[10:11], v104 offset:256\n\t"
        "s_waitcnt lgkmcnt(5)\n\t"
        "ds_read_b64_tr_b16 a[12:13], v103 offset:4096\n\t"
        "ds_read_b64_tr_b16 a[14:15], v103 offset:4608\n\t"
        "ds_read_b64_tr_b16 a[16:17], v103 offset:4104\n\t"
        "ds_read_b64_tr_b16 a[18:19], v103 offset:4616\n\t"
        "ds_read_b64_tr_b16 a[20:21], v104 offset:2048\n\t"
        "ds_read_b64_tr_b16 a[22:23], v104 offset:2304\n\t"
        "ds_read_b128 v[56:59], v105 offset:0\n\t"
        "ds_read_b128 v[60:63], v105 offset:2048\n\t"
        "ds_read_b128 v[64:67], v106 offset:0\n\t"
        "ds_read_b128 v[68:71], v106 offset:2048\n\t"
        "s_add_u32 m0, %[ld], 32768\n\t"
        "s_add_u32 %[t0], %[xo], 512\n\t"
        "buffer_load_dwordx4 v100, %[sx], %[t0] offen lds\n\t"
        "s_add_u32 m0, %[ld], 33792\n\t"
        "s_add_u32 %[t0], %[xo], 16896\n\t"
        "buffer_load_dwordx4 v100, %[sx], %[t0] offen lds\n\t"
        "s_add_u32 m0, %[ld], 40960\n\t"
        "s_add_u32 %[t0], %[xo], 640\n\t"
        "buffer_load_dwordx4 v100, %[sx], %[t0] offen lds\n\t"
        "s_add_u32 m0, %[ld], 41984\n\t"
        "s_add_u32 %[t0], %[xo], 17024\n\t"
        "buffer_load_dwordx4 v100, %[sx], %[t0] offen lds\n\t"
        "s_add_u32 m0, %[ld], 49152\n\t"
        "s_add_u32 %[t0], %[xo], 768\n\t"
        "buffer_load_dwordx4 v100, %[sx], %[t0] offen lds\n\t"
        "s_add_u32 m0, %[ld], 50176\n\t"
        "s_add_u32 %[t0], %[xo], 17152\n\t"
        "buffer_load_dwordx4 v100, %[sx], %[t0] offen lds\n\t"
        "s_add_u32 m0, %[ld], 57344\n\t"
        "s_add_u32 %[t0], %[xo], 896\n\t"
        "buffer_load_dwordx4 v100, %[sx], %[t0] offen lds\n\t"
        "s_add_u32 m0, %[ld], 58368\n\t"
        "s_add_u32 %[t0], %[xo], 17280\n\t"
        "buffer_load_dwordx4 v100, %[sx], %[t0] offen lds\n\t"
        "s_mov_b32 %[t0], 2097152\n\t"
        "buffer_load_dwordx4 a[240:243], v101, %[sq], %[t0] offen\n\t"
        "buffer_load_dwordx4 a[248:251], v112, %[sv], %[t0] offen\n\t"
        "s_mov_b32 %[t0], 2359296\n\t"
        "buffer_load_dwordx4 a[244:247], v101, %[sq], %[t0] offen\n\t"
        "s_waitcnt vmcnt(17)\n\t"
        "ds_write_b128 v102, a[204:207] offset:16384\n\t"
        "ds_write_b128 v102, a[208:211] offset:20480\n\t"
        "ds_write_b128 v116, a[212:215] offset:24576\n\t"
        "s_waitcnt lgkmcnt(0)\n\t"
        "s_barrier\n\t"
        "v_mfma_f32_16x16x32_bf16 v[8:11], a[0:3], v[56:59], 0\n\t"
        "ds_read_b64_tr_b16 a[24:25], v103 offset:16384\n\t"
        "ds_read_b64_tr_b16 a[26:27], v103 offset:16896\n\t"
        "v_mfma_f32_16x16x32_bf16 v[12:15], a[4:7], v[56:59], 0\n\t"
        "ds_read_b64_tr_b16 a[28:29], v103 offset:16392\n\t"
        "ds_read_b64_tr_b16 a[30:31], v103 offset:16904\n\t"
        "v_mfma_f32_16x16x32_bf16 v[16:19], v[56:59], a[8:11], 0\n\t"
        "ds_read_b64_tr_b16 a[32:33], v104 offset:16384\n\t"
        "ds_read_b64_tr_b16 a[34:35], v104 offset:16640\n\t"
        "v_mfma_f32_16x16x32_bf16 v[20:23], a[0:3], v[60:63], 0\n\t"
        "s_waitcnt lgkmcnt(5)\n\t"
        "ds_read_b64_tr_b16 a[36:37], v103 offset:20480\n\t"
        "v_mfma_f32_16x16x32_bf16 v[24:27], a[4:7], v[60:63], 0\n\t"
        "ds_read_b64_tr_b16 a[38:39], v103 offset:20992\n\t"
        "ds_read_b64_tr_b16 a[40:41], v103 offset:20488\n\t"
        "v_mfma_f32_16x16x32_bf16 v[28:31], v[60:63], a[8:11], 0\n\t"
        "ds_read_b64_tr_b16 a[42:43], v103 offset:21000\n\t"
        "ds_read_b64_tr_b16 a[44:45], v104 offset:18432\n\t"
        "v_mfma_f32_16x16x32_bf16 v[8:11], a[12:15], v[64:67], v[8:11]\n\t"
        "ds_read_b64_tr_b16 a[46:47], v104 offset:18688\n\t"
        "ds_read_b128 v[72:75], v105 offset:8192\n\t"
        "v_mfma_f32_16x16x32_bf16 v[12:15], a[16:19], v[64:67], v[12:15]\n\t"
        "ds_read_b128 v[76:79], v105 offset:10240\n\t"
        "ds_read_b128 v[80:83], v106 offset:8192\n\t"
        "v_mfma_f32_16x16x32_bf16 v[16:19], v[64:67], a[20:23], v[16:19]\n\t"
        "ds_read_b128 v[84:87], v106 offset:10240\n\t"
        "v_mfma_f32_16x16x32_bf16 v[20:23], a[12:15], v[68:71], v[20:23]\n\t"
        "v_mfma_f32_16x16x32_bf16 v[24:27], a[16:19], v[68:71], v[24:27]\n\t"
        "v_mfma_f32_16x16x32_bf16 v[28:31], v[68:71], a[20:23], v[28:31]\n\t"
        "s_mov_b32 %[t0], 2621440\n\t"
        "buffer_load_dwordx4 a[252:255], v101, %[sq], %[t0] offen\n\t"
        "buffer_load_dwordx4 v[132:135], v112, %[sv], %[t0] offen\n\t"
        "s_mov_b32 %[t0], 2883584\n\t"
        "buffer_load_dwordx4 v[128:131], v101, %[sq], %[t0] offen\n\t"
        "s_waitcnt vmcnt(17)\n\t"
        "ds_write_b128 v102, a[216:219] offset:0\n\t"
        "ds_write_b128 v102, a[220:223] offset:4096\n\t"
        "ds_write_b128 v116, a[224:227] offset:8192\n\t"
        "s_waitcnt lgkmcnt(0)\n\t"
        "s_barrier\n\t"
        "v_mfma_f32_16x16x32_bf16 v[8:11], a[24:27], v[72:75], v[8:11]\n\t"
        "ds_read_b64_tr_b16 a[48:49], v103 offset:0\n\t"
        "ds_read_b64_tr_b16 a[50:51], v103 offset:512\n\t"
        "v_mfma_f32_16x16x32_bf16 v[12:15], a[28:31], v[72:75], v[12:15]\n\t"
        "ds_read_b64_tr_b16 a[52:53], v103 offset:8\n\t"
        "ds_read_b64_tr_b16 a[54:55], v103 offset:520\n\t"
        "v_mfma_f32_16x16x32_bf16 v[16:19], v[72:75], a[32:35], v[16:19]\n\t"
        "ds_read_b64_tr_b16 a[56:57], v104 offset:0\n\t"
        "ds_read_b64_tr_b16 a[58:59], v104 offset:256\n\t"
        "v_mfma_f32_16x16x32_bf16 v[20:23], a[24:27], v[76:79], v[20:23]\n\t"
        "s_waitcnt lgkmcnt(5)\n\t"
        "ds_read_b64_tr_b16 a[60:61], v103 offset:4096\n\t"
        "v_mfma_f32_16x16x32_bf16 v[24:27], a[28:31], v[76:79], v[24:27]\n\t"
        "ds_read_b64_tr_b16 a[62:63], v103 offset:4608\n\t"
        "ds_read_b64_tr_b16 a[64:65], v103 offset:4104\n\t"
        "v_mfma_f32_16x16x32_bf16 v[28:31], v[76:79], a[32:35], v[28:31]\n\t"
        "ds_read_b64_tr_b16 a[66:67], v103 offset:4616\n\t"
        "ds_read_b64_tr_b16 a[68:69], v104 offset:2048\n\t"
        "v_mfma_f32_16x16x32_bf16 v[8:11], a[36:39], v[80:83], v[8:11]\n\t"
        "ds_read_b64_tr_b16 a[70:71], v104 offset:2304\n\t"
        "ds_read_b128 v[56:59], v105 offset:16384\n\t"
        "v_mfma_f32_16x16x32_bf16 v[12:15], a[40:43], v[80:83], v[12:15]\n\t"
        "ds_read_b128 v[60:63], v105 offset:18432\n\t"
        "ds_read_b128 v[64:67], v106 offset:16384\n\t"
        "v_mfma_f32_16x16x32_bf16 v[16:19], v[80:83], a[44:47], v[16:19]\n\t"
        "ds_read_b128 v[68:71], v106 offset:18432\n\t"
        "v_mfma_f32_16x16x32_bf16 v[20:23], a[36:39], v[84:87], v[20:23]\n\t"
        "v_mfma_f32_16x16x32_bf16 v[24:27], a[40:43], v[84:87], v[24:27]\n\t"
        "v_mfma_f32_16x16x32_bf16 v[28:31], v[84:87], a[44:47], v[28:31]\n\t"
        "s_mov_b32 %[t0], 3145728\n\t"
        "buffer_load_dwordx4 v[136:139], v101, %[sq], %[t0] offen\n\t"
        "buffer_load_dwordx4 v[144:147], v112, %[sv], %[t0] offen\n\t"
        "s_mov_b32 %[t0], 3407872\n\t"
        "buffer_load_dwordx4 v[140:143], v101, %[sq], %[t0] offen\n\t"
        "s_waitcnt vmcnt(17)\n\t"
        "ds_write_b128 v102, a[228:231] offset:16384\n\t"
        "ds_write_b128 v102, a[232:235] offset:20480\n\t"
        "ds_write_b128 v116, a[236:239] offset:24576\n\t"
        "s_waitcnt lgkmcnt(0)\n\t"
        "s_barrier\n\t"
        "v_mfma_f32_16x16x32_bf16 v[8:11], a[48:51], v[56:59], v[8:11]\n\t"
        "ds_read_b64_tr_b16 a[72:73], v103 offset:16384\n\t"
        "ds_read_b64_tr_b16 a[74:75], v103 offset:16896\n\t"
        "v_mfma_f32_16x16x32_bf16 v[12:15], a[52:55], v[56:59], v[12:15]\n\t"
        "ds_read_b64_tr_b16 a[76:77], v103 offset:16392\n\t"
        "ds_read_b64_tr_b16 a[78:79], v103 offset:16904\n\t"
        "v_mfma_f32_16x16x32_bf16 v[16:19], v[56:59], a[56:59], v[16:19]\n\t"
        "ds_read_b64_tr_b16 a[80:81], v104 offset:16384\n\t"
        "ds_read_b64_tr_b16 a[82:83], v104 offset:16640\n\t"
        "v_mfma_f32_16x16x32_bf16 v[20:23], a[48:51], v[60:63], v[20:23]\n\t"
        "s_waitcnt lgkmcnt(5)\n\t"
        "ds_read_b64_tr_b16 a[84:85], v103 offset:20480\n\t"
        "v_mfma_f32_16x16x32_bf16 v[24:27], a[52:55], v[60:63], v[24:27]\n\t"
        "ds_read_b64_tr_b16 a[86:87], v103 offset:20992\n\t"
        "ds_read_b64_tr_b16 a[88:89], v103 offset:20488\n\t"
        "v_mfma_f32_16x16x32_bf16 v[28:31], v[60:63], a[56:59], v[28:31]\n\t"
        "ds_read_b64_tr_b16 a[90:91], v103 offset:21000\n\t"
        "ds_read_b64_tr_b16 a[92:93], v104 offset:18432\n\t"
        "v_mfma_f32_16x16x32_bf16 v[8:11], a[60:63], v[64:67], v[8:11]\n\t"
        "ds_read_b64_tr_b16 a[94:95], v104 offset:18688\n\t"
        "ds_read_b128 v[72:75], v105 offset:24576\n\t"
        "v_mfma_f32_16x16x32_bf16 v[12:15], a[64:67], v[64:67], v[12:15]\n\t"
        "ds_read_b128 v[76:79], v105 offset:26624\n\t"
        "ds_read_b128 v[80:83], v106 offset:24576\n\t"
        "v_mfma_f32_16x16x32_bf16 v[16:19], v[64:67], a[68:71], v[16:19]\n\t"
        "ds_read_b128 v[84:87], v106 offset:26624\n\t"
        "v_mfma_f32_16x16x32_bf16 v[20:23], a[60:63], v[68:71], v[20:23]\n\t"
        "v_mfma_f32_16x16x32_bf16 v[24:27], a[64:67], v[68:71], v[24:27]\n\t"
        "v_mfma_f32_16x16x32_bf16 v[28:31], v[68:71], a[68:71], v[28:31]\n\t"
        "s_mov_b32 %[t0], 3670016\n\t"
        "buffer_load_dwordx4 v[148:151], v101, %[sq], %[t0] offen\n\t"
        "buffer_load_dwordx4 v[156:159], v112, %[sv], %[t0] offen\n\t"
        "s_mov_b32 %[t0], 3932160\n\t"
        "buffer_load_dwordx4 v[152:155], v101, %[sq], %[t0] offen\n\t"
        "s_waitcnt vmcnt(9)\n\t"
        "ds_write_b128 v102, a[240:243] offset:0\n\t"
        "ds_write_b128 v102, a[244:247] offset:4096\n\t"
        "ds_write_b128 v116, a[248:251] offset:8192\n\t"
        "s_waitcnt lgkmcnt(0)\n\t"
        "s_barrier\n\t"
        "v_mfma_f32_16x16x32_bf16 v[8:11], a[72:75], v[72:75], v[8:11]\n\t"
        "ds_read_b64_tr_b16 a[96:97], v103 offset:0\n\t"
        "ds_read_b64_tr_b16 a[98:99], v103 offset:512\n\t"
        "v_mfma_f32_16x16x32_bf16 v[12:15], a[76:79], v[72:75], v[12:15]\n\t"
        "ds_read_b64_tr_b16 a[100:101], v103 offset:8\n\t"
        "ds_read_b64_tr_b16 a[102:103], v103 offset:520\n\t"
        "v_mfma_f32_16x16x32_bf16 v[16:19], v[72:75], a[80:83], v[16:19]\n\t"
        "ds_read_b64_tr_b16 a[104:105], v104 offset:0\n\t"
        "ds_read_b64_tr_b16 a[106:107], v104 offset:256\n\t"
        "v_mfma_f32_16x16x32_bf16 v[20:23], a[72:75], v[76:79], v[20:23]\n\t"
        "s_waitcnt lgkmcnt(5)\n\t"
        "ds_read_b64_tr_b16 a[108:109], v103 offset:4096\n\t"
        "v_mfma_f32_16x16x32_bf16 v[24:27], a[76:79], v[76:79], v[24:27]\n\t"
        "ds_read_b64_tr_b16 a[110:111], v103 offset:4608\n\t"
        "ds_read_b64_tr_b16 a[112:113], v103 offset:4104\n\t"
        "v_mfma_f32_16x16x32_bf16 v[28:31], v[76:79], a[80:83], v[28:31]\n\t"
        "ds_read_b64_tr_b16 a[114:115], v103 offset:4616\n\t"
        "ds_read_b64_tr_b16 a[116:117], v104 offset:2048\n\t"
        "v_mfma_f32_16x16x32_bf16 v[8:11], a[84:87], v[80:83], v[8:11]\n\t"
        "ds_read_b64_tr_b16 a[118:119], v104 offset:2304\n\t"
        "ds_read_b128 v[56:59], v105 offset:32768\n\t"
        "v_mfma_f32_16x16x32_bf16 v[12:15], a[88:91], v[80:83], v[12:15]\n\t"
        "ds_read_b128 v[60:63], v105 offset:34816\n\t"
        "ds_read_b128 v[64:67], v106 offset:32768\n\t"
        "v_mfma_f32_16x16x32_bf16 v[16:19], v[80:83], a[92:95], v[16:19]\n\t"
        "ds_read_b128 v[68:71], v106 offset:34816\n\t"
        "v_mfma_f32_16x16x32_bf16 v[20:23], a[84:87], v[84:87], v[20:23]\n\t"
        "v_mfma_f32_16x16x32_bf16 v[24:27], a[88:91], v[84:87], v[24:27]\n\t"
        "v_mfma_f32_16x16x32_bf16 v[28:31], v[84:87], a[92:95], v[28:31]\n\t"
        "s_add_u32 m0, %[ld], 65536\n\t"
        "s_add_u32 %[t0], %[xo], 1024\n\t"
        "buffer_load_dwordx4 v100, %[sx], %[t0] offen lds\n\t"
        "s_add_u32 m0, %[ld], 66560\n\t"
        "s_add_u32 %[t0], %[xo], 17408\n\t"
        "buffer_load_dwordx4 v100, %[sx], %[t0] offen lds\n\t"
        "s_add_u32 m0, %[ld], 73728\n\t"
        "s_add_u32 %[t0], %[xo], 1152\n\t"
        "buffer_load_dwordx4 v100, %[sx], %[t0] offen lds\n\t"
        "s_add_u32 m0, %[ld], 74752\n\t"
        "s_add_u32 %[t0], %[xo], 17536\n\t"
        "buffer_load_dwordx4 v100, %[sx], %[t0] offen lds\n\t"
        "s_add_u32 m0, %[ld], 81920\n\t"
        "s_add_u32 %[t0], %[xo], 1280\n\t"
        "buffer_load_dwordx4 v100, %[sx], %[t0] offen lds\n\t"
        "s_add_u32 m0, %[ld], 82944\n\t"
        "s_add_u32 %[t0], %[xo], 17664\n\t"
        "buffer_load_dwordx4 v100, %[sx], %[t0] offen lds\n\t"
        "s_add_u32 m0, %[ld], 90112\n\t"
        "s_add_u32 %[t0], %[xo], 1408\n\t"
        "buffer_load_dwordx4 v100, %[sx], %[t0] offen lds\n\t"
        "s_add_u32 m0, %[ld], 91136\n\t"
        "s_add_u32 %[t0], %[xo], 17792\n\t"
        "buffer_load_dwordx4 v100, %[sx], %[t0] offen lds\n\t"
        "s_mov_b32 %[t0], 4194304\n\t"
        "buffer_load_dwordx4 v[160:163], v101, %[sq], %[t0] offen\n\t"
        "buffer_load_dwordx4 v[168:171], v112, %[sv], %[t0] offen\n\t"
        "s_mov_b32 %[t0], 4456448\n\t"
        "buffer_load_dwordx4 v[164:167], v101, %[sq], %[t0] offen\n\t"
        "s_waitcnt vmcnt(17)\n\t"
        "ds_write_b128 v102, a[252:255] offset:16384\n\t"
        "ds_write_b128 v102, v[128:131] offset:20480\n\t"
        "ds_write_b128 v116, v[132:135] offset:24576\n\t"
        "s_waitcnt lgkmcnt(0)\n\t"
        "s_barrier\n\t"
        "v_mfma_f32_16x16x32_bf16 v[8:11], a[96:99], v[56:59], v[8:11]\n\t"
        "ds_read_b64_tr_b16 a[120:121], v103 offset:16384\n\t"
        "ds_read_b64_tr_b16 a[122:123], v103 offset:16896\n\t"
        "v_mfma_f32_16x16x32_bf16 v[12:15], a[100:103], v[56:59], v[12:15]\n\t"
        "ds_read_b64_tr_b16 a[124:125], v103 offset:16392\n\t"
        "ds_read_b64_tr_b16 a[126:127], v103 offset:16904\n\t"
        "v_mfma_f32_16x16x32_bf16 v[16:19], v[56:59], a[104:107], v[16:19]\n\t"
        "ds_read_b64_tr_b16 a[128:129], v104 offset:16384\n\t"
        "ds_read_b64_tr_b16 a[130:131], v104 offset:16640\n\t"
        "v_mfma_f32_16x16x32_bf16 v[20:23], a[96:99], v[60:63], v[20:23]\n\t"
        "s_waitcnt lgkmcnt(5)\n\t"
        "ds_read_b64_tr_b16 a[132:133], v103 offset:20480\n\t"
        "v_mfma_f32_16x16x32_bf16 v[24:27], a[100:103], v[60:63], v[24:27]\n\t"
        "ds_read_b64_tr_b16 a[134:135], v103 offset:20992\n\t"
        "ds_read_b64_tr_b16 a[136:137], v103 offset:20488\n\t"
        "v_mfma_f32_16x16x32_bf16 v[28:31], v[60:63], a[104:107], v[28:31]\n\t"
        "ds_read_b64_tr_b16 a[138:139], v103 offset:21000\n\t"
        "ds_read_b64_tr_b16 a[140:141], v104 offset:18432\n\t"
        "v_mfma_f32_16x16x32_bf16 v[8:11], a[108:111], v[64:67], v[8:11]\n\t"
        "ds_read_b64_tr_b16 a[142:143], v104 offset:18688\n\t"
        "ds_read_b128 v[72:75], v105 offset:40960\n\t"
        "v_mfma_f32_16x16x32_bf16 v[12:15], a[112:115], v[64:67], v[12:15]\n\t"
        "ds_read_b128 v[76:79], v105 offset:43008\n\t"
        "ds_read_b128 v[80:83], v106 offset:40960\n\t"
        "v_mfma_f32_16x16x32_bf16 v[16:19], v[64:67], a[116:119], v[16:19]\n\t"
        "ds_read_b128 v[84:87], v106 offset:43008\n\t"
        "v_mfma_f32_16x16x32_bf16 v[20:23], a[108:111], v[68:71], v[20:23]\n\t"
        "v_mfma_f32_16x16x32_bf16 v[24:27], a[112:115], v[68:71], v[24:27]\n\t"
        "v_mfma_f32_16x16x32_bf16 v[28:31], v[68:71], a[116:119], v[28:31]\n\t"
        "s_mov_b32 %[t0], 4718592\n\t"
        "buffer_load_dwordx4 v[172:175], v101, %[sq], %[t0] offen\n\t"
        "buffer_load_dwordx4 v[180:183], v112, %[sv], %[t0] offen\n\t"
        "s_mov_b32 %[t0], 4980736\n\t"
        "buffer_load_dwordx4 v[176:179], v101, %[sq], %[t0] offen\n\t"
        "s_waitcnt vmcnt(17)\n\t"
        "ds_write_b128 v102, v[136:139] offset:0\n\t"
        "ds_write_b128 v102, v[140:143] offset:4096\n\t"
        "ds_write_b128 v116, v[144:147] offset:8192\n\t"
        "s_waitcnt lgkmcnt(0)\n\t"
        "s_barrier\n\t"
        "v_mfma_f32_16x16x32_bf16 v[8:11], a[120:123], v[72:75], v[8:11]\n\t"
        "ds_read_b64_tr_b16 a[144:145], v103 offset:0\n\t"
        "ds_read_b64_tr_b16 a[146:147], v103 offset:512\n\t"
        "v_mfma_f32_16x16x32_bf16 v[12:15], a[124:127], v[72:75], v[12:15]\n\t"
        "ds_read_b64_tr_b16 a[148:149], v103 offset:8\n\t"
        "ds_read_b64_tr_b16 a[150:151], v103 offset:520\n\t"
        "v_mfma_f32_16x16x32_bf16 v[16:19], v[72:75], a[128:131], v[16:19]\n\t"
        "ds_read_b64_tr_b16 a[152:153], v104 offset:0\n\t"
        "ds_read_b64_tr_b16 a[154:155], v104 offset:256\n\t"
        "v_mfma_f32_16x16x32_bf16 v[20:23], a[120:123], v[76:79], v[20:23]\n\t"
        "s_waitcnt lgkmcnt(5)\n\t"
        "ds_read_b64_tr_b16 a[156:157], v103 offset:4096\n\t"
        "v_mfma_f32_16x16x32_bf16 v[24:27], a[124:127], v[76:79], v[24:27]\n\t"
        "ds_read_b64_tr_b16 a[158:159], v103 offset:4608\n\t"
        "ds_read_b64_tr_b16 a[160:161], v103 offset:4104\n\t"
        "v_mfma_f32_16x16x32_bf16 v[28:31], v[76:79], a[128:131], v[28:31]\n\t"
        "ds_read_b64_tr_b16 a[162:163], v103 offset:4616\n\t"
        "ds_read_b64_tr_b16 a[164:165], v104 offset:2048\n\t"
        "v_mfma_f32_16x16x32_bf16 v[8:11], a[132:135], v[80:83], v[8:11]\n\t"
        "ds_read_b64_tr_b16 a[166:167], v104 offset:2304\n\t"
        "ds_read_b128 v[56:59], v105 offset:49152\n\t"
        "v_mfma_f32_16x16x32_bf16 v[12:15], a[136:139], v[80:83], v[12:15]\n\t"
        "ds_read_b128 v[60:63], v105 offset:51200\n\t"
        "ds_read_b128 v[64:67], v106 offset:49152\n\t"
        "v_mfma_f32_16x16x32_bf16 v[16:19], v[80:83], a[140:143], v[16:19]\n\t"
        "ds_read_b128 v[68:71], v106 offset:51200\n\t"
        "v_mfma_f32_16x16x32_bf16 v[20:23], a[132:135], v[84:87], v[20:23]\n\t"
        "v_mfma_f32_16x16x32_bf16 v[24:27], a[136:139], v[84:87], v[24:27]\n\t"
        "v_mfma_f32_16x16x32_bf16 v[28:31], v[84:87], a[140:143], v[28:31]\n\t"
        "s_mov_b32 %[t0], 5242880\n\t"
        "buffer_load_dwordx4 v[184:187], v101, %[sq], %[t0] offen\n\t"
        "buffer_load_dwordx4 v[192:195], v112, %[sv], %[t0] offen\n\t"
        "s_mov_b32 %[t0], 5505024\n\t"
        "buffer_load_dwordx4 v[188:191], v101, %[sq], %[t0] offen\n\t"
        "s_waitcnt vmcnt(17)\n\t"
        "ds_write_b128 v102, v[148:151] offset:16384\n\t"
        "ds_write_b128 v102, v[152:155] offset:20480\n\t"
        "ds_write_b128 v116, v[156:159] offset:24576\n\t"
        "s_waitcnt lgkmcnt(0)\n\t"
        "s_barrier\n\t"
        "v_mfma_f32_16x16x32_bf16 v[8:11], a[144:147], v[56:59], v[8:11]\n\t"
        "ds_read_b64_tr_b16 a[168:169], v103 offset:16384\n\t"
        "ds_read_b64_tr_b16 a[170:171], v103 offset:16896\n\t"
        "v_mfma_f32_16x16x32_bf16 v[12:15], a[148:151], v[56:59], v[12:15]\n\t"
        "ds_read_b64_tr_b16 a[172:173], v103 offset:16392\n\t"
        "ds_read_b64_tr_b16 a[174:175], v103 offset:16904\n\t"
        "v_mfma_f32_16x16x32_bf16 v[16:19], v[56:59], a[152:155], v[16:19]\n\t"
        "ds_read_b64_tr_b16 a[176:177], v104 offset:16384\n\t"
        "ds_read_b64_tr_b16 a[178:179], v104 offset:16640\n\t"
        "v_mfma_f32_16x16x32_bf16 v[20:23], a[144:147], v[60:63], v[20:23]\n\t"
        "s_waitcnt lgkmcnt(5)\n\t"
        "ds_read_b64_tr_b16 a[180:181], v103 offset:20480\n\t"
        "v_mfma_f32_16x16x32_bf16 v[24:27], a[148:151], v[60:63], v[24:27]\n\t"
        "ds_read_b64_tr_b16 a[182:183], v103 offset:20992\n\t"
        "ds_read_b64_tr_b16 a[184:185], v103 offset:20488\n\t"
        "v_mfma_f32_16x16x32_bf16 v[28:31], v[60:63], a[152:155], v[28:31]\n\t"
        "ds_read_b64_tr_b16 a[186:187], v103 offset:21000\n\t"
        "ds_read_b64_tr_b16 a[188:189], v104 offset:18432\n\t"
        "v_mfma_f32_16x16x32_bf16 v[8:11], a[156:159], v[64:67], v[8:11]\n\t"
        "ds_read_b64_tr_b16 a[190:191], v104 offset:18688\n\t"
        "ds_read_b128 v[72:75], v105 offset:57344\n\t"
        "v_mfma_f32_16x16x32_bf16 v[12:15], a[160:163], v[64:67], v[12:15]\n\t"
        "ds_read_b128 v[76:79], v105 offset:59392\n\t"
        "ds_read_b128 v[80:83], v106 offset:57344\n\t"
        "v_mfma_f32_16x16x32_bf16 v[16:19], v[64:67], a[164:167], v[16:19]\n\t"
        "ds_read_b128 v[84:87], v106 offset:59392\n\t"
        "v_mfma_f32_16x16x32_bf16 v[20:23], a[156:159], v[68:71], v[20:23]\n\t"
        "v_mfma_f32_16x16x32_bf16 v[24:27], a[160:163], v[68:71], v[24:27]\n\t"
        "v_mfma_f32_16x16x32_bf16 v[28:31], v[68:71], a[164:167], v[28:31]\n\t"
        "s_mov_b32 %[t0], 5767168\n\t"
        "buffer_load_dwordx4 v[196:199], v101, %[sq], %[t0] offen\n\t"
        "buffer_load_dwordx4 v[204:207], v112, %[sv], %[t0] offen\n\t"
        "s_mov_b32 %[t0], 6029312\n\t"
        "buffer_load_dwordx4 v[200:203], v101, %[sq], %[t0] offen\n\t"
        "s_waitcnt vmcnt(9)\n\t"
        "ds_write_b128 v102, v[160:163] offset:0\n\t"
        "ds_write_b128 v102, v[164:167] offset:4096\n\t"
        "ds_write_b128 v116, v[168:171] offset:8192\n\t"
        "s_waitcnt lgkmcnt(0)\n\t"
        "s_barrier\n\t"
        "v_mfma_f32_16x16x32_bf16 v[8:11], a[168:171], v[72:75], v[8:11]\n\t"
        "ds_read_b64_tr_b16 a[192:193], v103 offset:0\n\t"
        "ds_read_b64_tr_b16 a[194:195], v103 offset:512\n\t"
        "v_mfma_f32_16x16x32_bf16 v[12:15], a[172:175], v[72:75], v[12:15]\n\t"
        "ds_read_b64_tr_b16 a[196:197], v103 offset:8\n\t"
        "ds_read_b64_tr_b16 a[198:199], v103 offset:520\n\t"
        "v_mfma_f32_16x16x32_bf16 v[16:19], v[72:75], a[176:179], v[16:19]\n\t"
        "ds_read_b64_tr_b16 a[200:201], v104 offset:0\n\t"
        "ds_read_b64_tr_b16 a[202:203], v104 offset:256\n\t"
        "v_mfma_f32_16x16x32_bf16 v[20:23], a[168:171], v[76:79], v[20:23]\n\t"
        "s_waitcnt lgkmcnt(5)\n\t"
        "ds_read_b64_tr_b16 a[204:205], v103 offset:4096\n\t"
        "v_mfma_f32_16x16x32_bf16 v[24:27], a[172:175], v[76:79], v[24:27]\n\t"
        "ds_read_b64_tr_b16 a[206:207], v103 offset:4608\n\t"
        "ds_read_b64_tr_b16 a[208:209], v103 offset:4104\n\t"
        "v_mfma_f32_16x16x32_bf16 v[28:31], v[76:79], a[176:179], v[28:31]\n\t"
        "ds_read_b64_tr_b16 a[210:211], v103 offset:4616\n\t"
        "ds_read_b64_tr_b16 a[212:213], v104 offset:2048\n\t"
        "v_mfma_f32_16x16x32_bf16 v[8:11], a[180:183], v[80:83], v[8:11]\n\t"
        "ds_read_b64_tr_b16 a[214:215], v104 offset:2304\n\t"
        "ds_read_b128 v[56:59], v107 offset:0\n\t"
        "v_mfma_f32_16x16x32_bf16 v[12:15], a[184:187], v[80:83], v[12:15]\n\t"
        "ds_read_b128 v[60:63], v107 offset:2048\n\t"
        "ds_read_b128 v[64:67], v108 offset:0\n\t"
        "v_mfma_f32_16x16x32_bf16 v[16:19], v[80:83], a[188:191], v[16:19]\n\t"
        "ds_read_b128 v[68:71], v108 offset:2048\n\t"
        "v_mfma_f32_16x16x32_bf16 v[20:23], a[180:183], v[84:87], v[20:23]\n\t"
        "v_mfma_f32_16x16x32_bf16 v[24:27], a[184:187], v[84:87], v[24:27]\n\t"
        "v_mfma_f32_16x16x32_bf16 v[28:31], v[84:87], a[188:191], v[28:31]\n\t"
        "s_add_u32 m0, %[ld], 98304\n\t"
        "s_add_u32 %[t0], %[xo], 1536\n\t"
        "buffer_load_dwordx4 v100, %[sx], %[t0] offen lds\n\t"
        "s_add_u32 m0, %[ld], 99328\n\t"
        "s_add_u32 %[t0], %[xo], 17920\n\t"
        "buffer_load_dwordx4 v100, %[sx], %[t0] offen lds\n\t"
        "s_add_u32 m0, %[ld], 106496\n\t"
        "s_add_u32 %[t0], %[xo], 1664\n\t"
        "buffer_load_dwordx4 v100, %[sx], %[t0] offen lds\n\t"
        "s_add_u32 m0, %[ld], 107520\n\t"
        "s_add_u32 %[t0], %[xo], 18048\n\t"
        "buffer_load_dwordx4 v100, %[sx], %[t0] offen lds\n\t"
        "s_add_u32 m0, %[ld], 114688\n\t"
        "s_add_u32 %[t0], %[xo], 1792\n\t"
        "buffer_load_dwordx4 v100, %[sx], %[t0] offen lds\n\t"
        "s_add_u32 m0, %[ld], 115712\n\t"
        "s_add_u32 %[t0], %[xo], 18176\n\t"
        "buffer_load_dwordx4 v100, %[sx], %[t0] offen lds\n\t"
        "s_add_u32 m0, %[ld], 122880\n\t"
        "s_add_u32 %[t0], %[xo], 1920\n\t"
        "buffer_load_dwordx4 v100, %[sx], %[t0] offen lds\n\t"
        "s_add_u32 m0, %[ld], 123904\n\t"
        "s_add_u32 %[t0], %[xo], 18304\n\t"
        "buffer_load_dwordx4 v100, %[sx], %[t0] offen lds\n\t"
        "s_mov_b32 %[t0], 6291456\n\t"
        "buffer_load_dwordx4 v[208:211], v101, %[sq], %[t0] offen\n\t"
        "buffer_load_dwordx4 v[216:219], v112, %[sv], %[t0] offen\n\t"
        "s_mov_b32 %[t0], 6553600\n\t"
        "buffer_load_dwordx4 v[212:215], v101, %[sq], %[t0] offen\n\t"
        "s_waitcnt vmcnt(17)\n\t"
        "ds_write_b128 v102, v[172:175] offset:16384\n\t"
        "ds_write_b128 v102, v[176:179] offset:20480\n\t"
        "ds_write_b128 v116, v[180:183] offset:24576\n\t"
        "s_waitcnt lgkmcnt(0)\n\t"
        "s_barrier\n\t"
        "v_mfma_f32_16x16x32_bf16 v[8:11], a[192:195], v[56:59], v[8:11]\n\t"
        "ds_read_b64_tr_b16 a[216:217], v103 offset:16384\n\t"
        "ds_read_b64_tr_b16 a[218:219], v103 offset:16896\n\t"
        "v_mfma_f32_16x16x32_bf16 v[12:15], a[196:199], v[56:59], v[12:15]\n\t"
        "ds_read_b64_tr_b16 a[220:221], v103 offset:16392\n\t"
        "ds_read_b64_tr_b16 a[222:223], v103 offset:16904\n\t"
        "v_mfma_f32_16x16x32_bf16 v[16:19], v[56:59], a[200:203], v[16:19]\n\t"
        "ds_read_b64_tr_b16 a[224:225], v104 offset:16384\n\t"
        "ds_read_b64_tr_b16 a[226:227], v104 offset:16640\n\t"
        "v_mfma_f32_16x16x32_bf16 v[20:23], a[192:195], v[60:63], v[20:23]\n\t"
        "s_waitcnt lgkmcnt(5)\n\t"
        "ds_read_b64_tr_b16 a[228:229], v103 offset:20480\n\t"
        "v_mfma_f32_16x16x32_bf16 v[24:27], a[196:199], v[60:63], v[24:27]\n\t"
        "ds_read_b64_tr_b16 a[230:231], v103 offset:20992\n\t"
        "ds_read_b64_tr_b16 a[232:233], v103 offset:20488\n\t"
        "v_mfma_f32_16x16x32_bf16 v[28:31], v[60:63], a[200:203], v[28:31]\n\t"
        "ds_read_b64_tr_b16 a[234:235], v103 offset:21000\n\t"
        "ds_read_b64_tr_b16 a[236:237], v104 offset:18432\n\t"
        "v_mfma_f32_16x16x32_bf16 v[8:11], a[204:207], v[64:67], v[8:11]\n\t"
        "ds_read_b64_tr_b16 a[238:239], v104 offset:18688\n\t"
        "ds_read_b128 v[72:75], v107 offset:8192\n\t"
        "v_mfma_f32_16x16x32_bf16 v[12:15], a[208:211], v[64:67], v[12:15]\n\t"
        "ds_read_b128 v[76:79], v107 offset:10240\n\t"
        "ds_read_b128 v[80:83], v108 offset:8192\n\t"
        "v_mfma_f32_16x16x32_bf16 v[16:19], v[64:67], a[212:215], v[16:19]\n\t"
        "ds_read_b128 v[84:87], v108 offset:10240\n\t"
        "v_mfma_f32_16x16x32_bf16 v[20:23], a[204:207], v[68:71], v[20:23]\n\t"
        "v_mfma_f32_16x16x32_bf16 v[24:27], a[208:211], v[68:71], v[24:27]\n\t"
        "v_mfma_f32_16x16x32_bf16 v[28:31], v[68:71], a[212:215], v[28:31]\n\t"
        "s_mov_b32 %[t0], 6815744\n\t"
        "buffer_load_dwordx4 v[220:223], v101, %[sq], %[t0] offen\n\t"
        "buffer_load_dwordx4 v[228:231], v112, %[sv], %[t0] offen\n\t"
        "s_mov_b32 %[t0], 7077888\n\t"
        "buffer_load_dwordx4 v[224:227], v101, %[sq], %[t0] offen\n\t"
        "s_waitcnt vmcnt(17)\n\t"
        "ds_write_b128 v102, v[184:187] offset:0\n\t"
        "ds_write_b128 v102, v[188:191] offset:4096\n\t"
        "ds_write_b128 v116, v[192:195] offset:8192\n\t"
        "s_waitcnt lgkmcnt(0)\n\t"
        "s_barrier\n\t"
        "v_mfma_f32_16x16x32_bf16 v[8:11], a[216:219], v[72:75], v[8:11]\n\t"
        "ds_read_b64_tr_b16 a[240:241], v103 offset:0\n\t"
        "ds_read_b64_tr_b16 a[242:243], v103 offset:512\n\t"
        "v_mfma_f32_16x16x32_bf16 v[12:15], a[220:223], v[72:75], v[12:15]\n\t"
        "ds_read_b64_tr_b16 a[244:245], v103 offset:8\n\t"
        "ds_read_b64_tr_b16 a[246:247], v103 offset:520\n\t"
        "v_mfma_f32_16x16x32_bf16 v[16:19], v[72:75], a[224:227], v[16:19]\n\t"
        "ds_read_b64_tr_b16 a[248:249], v104 offset:0\n\t"
        "ds_read_b64_tr_b16 a[250:251], v104 offset:256\n\t"
        "v_mfma_f32_16x16x32_bf16 v[20:23], a[216:219], v[76:79], v[20:23]\n\t"
        "s_waitcnt lgkmcnt(5)\n\t"
        "ds_read_b64_tr_b16 a[252:253], v103 offset:4096\n\t"
        "v_mfma_f32_16x16x32_bf16 v[24:27], a[220:223], v[76:79], v[24:27]\n\t"
        "ds_read_b64_tr_b16 a[254:255], v103 offset:4608\n\t"
        "ds_read_b64_tr_b16 v[128:129], v103 offset:4104\n\t"
        "v_mfma_f32_16x16x32_bf16 v[28:31], v[76:79], a[224:227], v[28:31]\n\t"
        "ds_read_b64_tr_b16 v[130:131], v103 offset:4616\n\t"
        "ds_read_b64_tr_b16 v[132:133], v104 offset:2048\n\t"
        "v_mfma_f32_16x16x32_bf16 v[8:11], a[228:231], v[80:83], v[8:11]\n\t"
        "ds_read_b64_tr_b16 v[134:135], v104 offset:2304\n\t"
        "ds_read_b128 v[56:59], v107 offset:16384\n\t"
        "v_mfma_f32_16x16x32_bf16 v[12:15], a[232:235], v[80:83], v[12:15]\n\t"
        "ds_read_b128 v[60:63], v107 offset:18432\n\t"
        "ds_read_b128 v[64:67], v108 offset:16384\n\t"
        "v_mfma_f32_16x16x32_bf16 v[16:19], v[80:83], a[236:239], v[16:19]\n\t"
        "ds_read_b128 v[68:71], v108 offset:18432\n\t"
        "v_mfma_f32_16x16x32_bf16 v[20:23], a[228:231], v[84:87], v[20:23]\n\t"
        "v_mfma_f32_16x16x32_bf16 v[24:27], a[232:235], v[84:87], v[24:27]\n\t"
        "v_mfma_f32_16x16x32_bf16 v[28:31], v[84:87], a[236:239], v[28:31]\n\t"
        "s_mov_b32 %[t0], 7340032\n\t"
        "buffer_load_dwordx4 v[232:235], v101, %[sq], %[t0] offen\n\t"
        "buffer_load_dwordx4 v[240:243], v112, %[sv], %[t0] offen\n\t"
        "s_mov_b32 %[t0], 7602176\n\t"
        "buffer_load_dwordx4 v[236:239], v101, %[sq], %[t0] offen\n\t"
        "s_waitcnt vmcnt(17)\n\t"
        "ds_write_b128 v102, v[196:199] offset:16384\n\t"
        "ds_write_b128 v102, v[200:203] offset:20480\n\t"
        "ds_write_b128 v116, v[204:207] offset:24576\n\t"
        "s_waitcnt lgkmcnt(0)\n\t"
        "s_barrier\n\t"
        "v_mfma_f32_16x16x32_bf16 v[8:11], a[240:243], v[56:59], v[8:11]\n\t"
        "ds_read_b64_tr_b16 v[136:137], v103 offset:16384\n\t"
        "ds_read_b64_tr_b16 v[138:139], v103 offset:16896\n\t"
        "v_mfma_f32_16x16x32_bf16 v[12:15], a[244:247], v[56:59], v[12:15]\n\t"
        "ds_read_b64_tr_b16 v[140:141], v103 offset:16392\n\t"
        "ds_read_b64_tr_b16 v[142:143], v103 offset:16904\n\t"
        "v_mfma_f32_16x16x32_bf16 v[16:19], v[56:59], a[248:251], v[16:19]\n\t"
        "ds_read_b64_tr_b16 v[144:145], v104 offset:16384\n\t"
        "ds_read_b64_tr_b16 v[146:147], v104 offset:16640\n\t"
        "v_mfma_f32_16x16x32_bf16 v[20:23], a[240:243], v[60:63], v[20:23]\n\t"
        "s_waitcnt lgkmcnt(5)\n\t"
        "ds_read_b64_tr_b16 v[148:149], v103 offset:20480\n\t"
        "v_mfma_f32_16x16x32_bf16 v[24:27], a[244:247], v[60:63], v[24:27]\n\t"
        "ds_read_b64_tr_b16 v[150:151], v103 offset:20992\n\t"
        "ds_read_b64_tr_b16 v[152:153], v103 offset:20488\n\t"
        "v_mfma_f32_16x16x32_bf16 v[28:31], v[60:63], a[248:251], v[28:31]\n\t"
        "ds_read_b64_tr_b16 v[154:155], v103 offset:21000\n\t"
        "ds_read_b64_tr_b16 v[156:157], v104 offset:18432\n\t"
        "v_mfma_f32_16x16x32_bf16 v[8:11], a[252:255], v[64:67], v[8:11]\n\t"
        "ds_read_b64_tr_b16 v[158:159], v104 offset:18688\n\t"
        "ds_read_b128 v[72:75], v107 offset:24576\n\t"
        "v_mfma_f32_16x16x32_bf16 v[12:15], v[128:131], v[64:67], v[12:15]\n\t"
        "ds_read_b128 v[76:79], v107 offset:26624\n\t"
        "ds_read_b128 v[80:83], v108 offset:24576\n\t"
        "v_mfma_f32_16x16x32_bf16 v[16:19], v[64:67], v[132:135], v[16:19]\n\t"
        "ds_read_b128 v[84:87], v108 offset:26624\n\t"
        "v_mfma_f32_16x16x32_bf16 v[20:23], a[252:255], v[68:71], v[20:23]\n\t"
        "v_mfma_f32_16x16x32_bf16 v[24:27], v[128:131], v[68:71], v[24:27]\n\t"
        "v_mfma_f32_16x16x32_bf16 v[28:31], v[68:71], v[132:135], v[28:31]\n\t"
        "s_mov_b32 %[t0], 7864320\n\t"
        "buffer_load_dwordx4 v[244:247], v101, %[sq], %[t0] offen\n\t"
        "buffer_load_dwordx4 v[252:255], v112, %[sv], %[t0] offen\n\t"
        "s_mov_b32 %[t0], 8126464\n\t"
        "buffer_load_dwordx4 v[248:251], v101, %[sq], %[t0] offen\n\t"
        "s_waitcnt vmcnt(9)\n\t"
        "ds_write_b128 v102, v[208:211] offset:0\n\t"
        "ds_write_b128 v102, v[212:215] offset:4096\n\t"
        "ds_write_b128 v116, v[216:219] offset:8192\n\t"
        "s_waitcnt lgkmcnt(0)\n\t"
        "s_barrier\n\t"
        "v_mfma_f32_16x16x32_bf16 v[8:11], v[136:139], v[72:75], v[8:11]\n\t"
        "ds_read_b64_tr_b16 v[160:161], v103 offset:0\n\t"
        "ds_read_b64_tr_b16 v[162:163], v103 offset:512\n\t"
        "v_mfma_f32_16x16x32_bf16 v[12:15], v[140:143], v[72:75], v[12:15]\n\t"
        "ds_read_b64_tr_b16 v[164:165], v103 offset:8\n\t"
        "ds_read_b64_tr_b16 v[166:167], v103 offset:520\n\t"
        "v_mfma_f32_16x16x32_bf16 v[16:19], v[72:75], v[144:147], v[16:19]\n\t"
        "ds_read_b64_tr_b16 v[168:169], v104 offset:0\n\t"
        "ds_read_b64_tr_b16 v[170:171], v104 offset:256\n\t"
        "v_mfma_f32_16x16x32_bf16 v[20:23], v[136:139], v[76:79], v[20:23]\n\t"
        "s_waitcnt lgkmcnt(5)\n\t"
        "ds_read_b64_tr_b16 v[172:173], v103 offset:4096\n\t"
        "v_mfma_f32_16x16x32_bf16 v[24:27], v[140:143], v[76:79], v[24:27]\n\t"
        "ds_read_b64_tr_b16 v[174:175], v103 offset:4608\n\t"
        "ds_read_b64_tr_b16 v[176:177], v103 offset:4104\n\t"
        "v_mfma_f32_16x16x32_bf16 v[28:31], v[76:79], v[144:147], v[28:31]\n\t"
        "ds_read_b64_tr_b16 v[178:179], v103 offset:4616\n\t"
        "ds_read_b64_tr_b16 v[180:181], v104 offset:2048\n\t"
        "v_mfma_f32_16x16x32_bf16 v[8:11], v[148:151], v[80:83], v[8:11]\n\t"
        "ds_read_b64_tr_b16 v[182:183], v104 offset:2304\n\t"
        "ds_read_b128 v[56:59], v107 offset:32768\n\t"
        "v_mfma_f32_16x16x32_bf16 v[12:15], v[152:155], v[80:83], v[12:15]\n\t"
        "ds_read_b128 v[60:63], v107 offset:34816\n\t"
        "ds_read_b128 v[64:67], v108 offset:32768\n\t"
        "v_mfma_f32_16x16x32_bf16 v[16:19], v[80:83], v[156:159], v[16:19]\n\t"
        "ds_read_b128 v[68:71], v108 offset:34816\n\t"
        "v_mfma_f32_16x16x32_bf16 v[20:23], v[148:151], v[84:87], v[20:23]\n\t"
        "v_mfma_f32_16x16x32_bf16 v[24:27], v[152:155], v[84:87], v[24:27]\n\t"
        "v_mfma_f32_16x16x32_bf16 v[28:31], v[84:87], v[156:159], v[28:31]\n\t"
        "s_mov_b32 %[xo], 0x20000\n\t"
        "s_add_u32 m0, %[ld], 0\n\t"
        "s_add_u32 %[t0], %[xo], 0\n\t"
        "buffer_load_dwordx4 v100, %[sx], %[t0] offen lds\n\t"
        "s_add_u32 m0, %[ld], 1024\n\t"
        "s_add_u32 %[t0], %[xo], 16384\n\t"
        "buffer_load_dwordx4 v100, %[sx], %[t0] offen lds\n\t"
        "s_add_u32 m0, %[ld], 8192\n\t"
        "s_add_u32 %[t0], %[xo], 128\n\t"
        "buffer_load_dwordx4 v100, %[sx], %[t0] offen lds\n\t"
        "s_add_u32 m0, %[ld], 9216\n\t"
        "s_add_u32 %[t0], %[xo], 16512\n\t"
        "buffer_load_dwordx4 v100, %[sx], %[t0] offen lds\n\t"
        "s_add_u32 m0, %[ld], 16384\n\t"
        "s_add_u32 %[t0], %[xo], 256\n\t"
        "buffer_load_dwordx4 v100, %[sx], %[t0] offen lds\n\t"
        "s_add_u32 m0, %[ld], 17408\n\t"
        "s_add_u32 %[t0], %[xo], 16640\n\t"
        "buffer_load_dwordx4 v100, %[sx], %[t0] offen lds\n\t"
        "s_add_u32 m0, %[ld], 24576\n\t"
        "s_add_u32 %[t0], %[xo], 384\n\t"
        "buffer_load_dwordx4 v100, %[sx], %[t0] offen lds\n\t"
        "s_add_u32 m0, %[ld], 25600\n\t"
        "s_add_u32 %[t0], %[xo], 16768\n\t"
        "buffer_load_dwordx4 v100, %[sx], %[t0] offen lds\n\t"
        "s_mov_b32 %[xo], 0\n\t"
        "s_waitcnt vmcnt(14)\n\t"
        "ds_write_b128 v102, v[220:223] offset:16384\n\t"
        "ds_write_b128 v102, v[224:227] offset:20480\n\t"
        "ds_write_b128 v116, v[228:231] offset:24576\n\t"
        "s_waitcnt lgkmcnt(0)\n\t"
        "s_barrier\n\t"
        "v_mfma_f32_16x16x32_bf16 v[8:11], v[160:163], v[56:59], v[8:11]\n\t"
        "ds_read_b64_tr_b16 v[184:185], v103 offset:16384\n\t"
        "ds_read_b64_tr_b16 v[186:187], v103 offset:16896\n\t"
        "v_mfma_f32_16x16x32_bf16 v[12:15], v[164:167], v[56:59], v[12:15]\n\t"
        "ds_read_b64_tr_b16 v[188:189], v103 offset:16392\n\t"
        "ds_read_b64_tr_b16 v[190:191], v103 offset:16904\n\t"
        "v_mfma_f32_16x16x32_bf16 v[16:19], v[56:59], v[168:171], v[16:19]\n\t"
        "ds_read_b64_tr_b16 v[192:193], v104 offset:16384\n\t"
        "ds_read_b64_tr_b16 v[194:195], v104 offset:16640\n\t"
        "v_mfma_f32_16x16x32_bf16 v[20:23], v[160:163], v[60:63], v[20:23]\n\t"
        "s_waitcnt lgkmcnt(5)\n\t"
        "ds_read_b64_tr_b16 v[196:197], v103 offset:20480\n\t"
        "v_mfma_f32_16x16x32_bf16 v[24:27], v[164:167], v[60:63], v[24:27]\n\t"
        "ds_read_b64_tr_b16 v[198:199], v103 offset:20992\n\t"
        "ds_read_b64_tr_b16 v[200:201], v103 offset:20488\n\t"
        "v_mfma_f32_16x16x32_bf16 v[28:31], v[60:63], v[168:171], v[28:31]\n\t"
        "ds_read_b64_tr_b16 v[202:203], v103 offset:21000\n\t"
        "ds_read_b64_tr_b16 v[204:205], v104 offset:18432\n\t"
        "v_mfma_f32_16x16x32_bf16 v[8:11], v[172:175], v[64:67], v[8:11]\n\t"
        "ds_read_b64_tr_b16 v[206:207], v104 offset:18688\n\t"
        "ds_read_b128 v[72:75], v107 offset:40960\n\t"
        "v_mfma_f32_16x16x32_bf16 v[12:15], v[176:179], v[64:67], v[12:15]\n\t"
        "ds_read_b128 v[76:79], v107 offset:43008\n\t"
        "ds_read_b128 v[80:83], v108 offset:40960\n\t"
        "v_mfma_f32_16x16x32_bf16 v[16:19], v[64:67], v[180:183], v[16:19]\n\t"
        "ds_read_b128 v[84:87], v108 offset:43008\n\t"
        "v_mfma_f32_16x16x32_bf16 v[20:23], v[172:175], v[68:71], v[20:23]\n\t"
        "v_mfma_f32_16x16x32_bf16 v[24:27], v[176:179], v[68:71], v[24:27]\n\t"
        "v_mfma_f32_16x16x32_bf16 v[28:31], v[68:71], v[180:183], v[28:31]\n\t"
        "s_waitcnt vmcnt(11)\n\t"
        "ds_write_b128 v102, v[232:235] offset:0\n\t"
        "ds_write_b128 v102, v[236:239] offset:4096\n\t"
        "ds_write_b128 v116, v[240:243] offset:8192\n\t"
        "s_waitcnt lgkmcnt(0)\n\t"
        "s_barrier\n\t"
        "v_mfma_f32_16x16x32_bf16 v[8:11], v[184:187], v[72:75], v[8:11]\n\t"
        "ds_read_b64_tr_b16 v[208:209], v103 offset:0\n\t"
        "ds_read_b64_tr_b16 v[210:211], v103 offset:512\n\t"
        "v_mfma_f32_16x16x32_bf16 v[12:15], v[188:191], v[72:75], v[12:15]\n\t"
        "ds_read_b64_tr_b16 v[212:213], v103 offset:8\n\t"
        "ds_read_b64_tr_b16 v[214:215], v103 offset:520\n\t"
        "v_mfma_f32_16x16x32_bf16 v[16:19], v[72:75], v[192:195], v[16:19]\n\t"
        "ds_read_b64_tr_b16 v[216:217], v104 offset:0\n\t"
        "ds_read_b64_tr_b16 v[218:219], v104 offset:256\n\t"
        "v_mfma_f32_16x16x32_bf16 v[20:23], v[184:187], v[76:79], v[20:23]\n\t"
        "s_waitcnt lgkmcnt(5)\n\t"
        "ds_read_b64_tr_b16 v[220:221], v103 offset:4096\n\t"
        "v_mfma_f32_16x16x32_bf16 v[24:27], v[188:191], v[76:79], v[24:27]\n\t"
        "ds_read_b64_tr_b16 v[222:223], v103 offset:4608\n\t"
        "ds_read_b64_tr_b16 v[224:225], v103 offset:4104\n\t"
        "v_mfma_f32_16x16x32_bf16 v[28:31], v[76:79], v[192:195], v[28:31]\n\t"
        "ds_read_b64_tr_b16 v[226:227], v103 offset:4616\n\t"
        "ds_read_b64_tr_b16 v[228:229], v104 offset:2048\n\t"
        "v_mfma_f32_16x16x32_bf16 v[8:11], v[196:199], v[80:83], v[8:11]\n\t"
        "ds_read_b64_tr_b16 v[230:231], v104 offset:2304\n\t"
        "ds_read_b128 v[56:59], v107 offset:49152\n\t"
        "v_mfma_f32_16x16x32_bf16 v[12:15], v[200:203], v[80:83], v[12:15]\n\t"
        "ds_read_b128 v[60:63], v107 offset:51200\n\t"
        "ds_read_b128 v[64:67], v108 offset:49152\n\t"
        "v_mfma_f32_16x16x32_bf16 v[16:19], v[80:83], v[204:207], v[16:19]\n\t"
        "ds_read_b128 v[68:71], v108 offset:51200\n\t"
        "v_mfma_f32_16x16x32_bf16 v[20:23], v[196:199], v[84:87], v[20:23]\n\t"
        "v_mfma_f32_16x16x32_bf16 v[24:27], v[200:203], v[84:87], v[24:27]\n\t"
        "v_mfma_f32_16x16x32_bf16 v[28:31], v[84:87], v[204:207], v[28:31]\n\t"
        "s_waitcnt vmcnt(8)\n\t"
        "ds_write_b128 v102, v[244:247] offset:16384\n\t"
        "ds_write_b128 v102, v[248:251] offset:20480\n\t"
        "ds_write_b128 v116, v[252:255] offset:24576\n\t"
        "s_waitcnt lgkmcnt(0)\n\t"
        "s_barrier\n\t"
        "v_mfma_f32_16x16x32_bf16 v[8:11], v[208:211], v[56:59], v[8:11]\n\t"
        "ds_read_b64_tr_b16 v[232:233], v103 offset:16384\n\t"
        "ds_read_b64_tr_b16 v[234:235], v103 offset:16896\n\t"
        "v_mfma_f32_16x16x32_bf16 v[12:15], v[212:215], v[56:59], v[12:15]\n\t"
        "ds_read_b64_tr_b16 v[236:237], v103 offset:16392\n\t"
        "ds_read_b64_tr_b16 v[238:239], v103 offset:16904\n\t"
        "v_mfma_f32_16x16x32_bf16 v[16:19], v[56:59], v[216:219], v[16:19]\n\t"
        "ds_read_b64_tr_b16 v[240:241], v104 offset:16384\n\t"
        "ds_read_b64_tr_b16 v[242:243], v104 offset:16640\n\t"
        "v_mfma_f32_16x16x32_bf16 v[20:23], v[208:211], v[60:63], v[20:23]\n\t"
        "s_waitcnt lgkmcnt(5)\n\t"
        "ds_read_b64_tr_b16 v[244:245], v103 offset:20480\n\t"
        "v_mfma_f32_16x16x32_bf16 v[24:27], v[212:215], v[60:63], v[24:27]\n\t"
        "ds_read_b64_tr_b16 v[246:247], v103 offset:20992\n\t"
        "ds_read_b64_tr_b16 v[248:249], v103 offset:20488\n\t"
        "v_mfma_f32_16x16x32_bf16 v[28:31], v[60:63], v[216:219], v[28:31]\n\t"
        "ds_read_b64_tr_b16 v[250:251], v103 offset:21000\n\t"
        "ds_read_b64_tr_b16 v[252:253], v104 offset:18432\n\t"
        "v_mfma_f32_16x16x32_bf16 v[8:11], v[220:223], v[64:67], v[8:11]\n\t"
        "ds_read_b64_tr_b16 v[254:255], v104 offset:18688\n\t"
        "ds_read_b128 v[72:75], v107 offset:57344\n\t"
        "v_mfma_f32_16x16x32_bf16 v[12:15], v[224:227], v[64:67], v[12:15]\n\t"
        "ds_read_b128 v[76:79], v107 offset:59392\n\t"
        "ds_read_b128 v[80:83], v108 offset:57344\n\t"
        "v_mfma_f32_16x16x32_bf16 v[16:19], v[64:67], v[228:231], v[16:19]\n\t"
        "ds_read_b128 v[84:87], v108 offset:59392\n\t"
        "v_mfma_f32_16x16x32_bf16 v[20:23], v[220:223], v[68:71], v[20:23]\n\t"
        "v_mfma_f32_16x16x32_bf16 v[24:27], v[224:227], v[68:71], v[24:27]\n\t"
        "v_mfma_f32_16x16x32_bf16 v[28:31], v[68:71], v[228:231], v[28:31]\n\t"
        "s_waitcnt vmcnt(0) lgkmcnt(0)\n\t"
        "s_barrier\n\t"
        "v_mfma_f32_16x16x32_bf16 v[8:11], v[232:235], v[72:75], v[8:11]\n\t"
        "v_mfma_f32_16x16x32_bf16 v[12:15], v[236:239], v[72:75], v[12:15]\n\t"
        "v_mfma_f32_16x16x32_bf16 v[16:19], v[72:75], v[240:243], v[16:19]\n\t"
        "v_mfma_f32_16x16x32_bf16 v[20:23], v[232:235], v[76:79], v[20:23]\n\t"
        "v_mfma_f32_16x16x32_bf16 v[24:27], v[236:239], v[76:79], v[24:27]\n\t"
        "v_mfma_f32_16x16x32_bf16 v[28:31], v[76:79], v[240:243], v[28:31]\n\t"
        "v_mfma_f32_16x16x32_bf16 v[8:11], v[244:247], v[80:83], v[8:11]\n\t"
        "v_mfma_f32_16x16x32_bf16 v[12:15], v[248:251], v[80:83], v[12:15]\n\t"
        "v_mfma_f32_16x16x32_bf16 v[16:19], v[80:83], v[252:255], v[16:19]\n\t"
        "v_mfma_f32_16x16x32_bf16 v[20:23], v[244:247], v[84:87], v[20:23]\n\t"
        "v_mfma_f32_16x16x32_bf16 v[24:27], v[248:251], v[84:87], v[24:27]\n\t"
        "v_mfma_f32_16x16x32_bf16 v[28:31], v[84:87], v[252:255], v[28:31]\n\t"
        "s_mov_b32 %[xo], 0x20000\n\t"
        "ds_read_b128 v[56:59], v105 offset:0\n\t"
        "ds_read_b128 v[60:63], v105 offset:2048\n\t"
        "ds_read_b128 v[64:67], v106 offset:0\n\t"
        "ds_read_b128 v[68:71], v106 offset:2048\n\t"
        "ds_read_b128 v[72:75], v105 offset:8192\n\t"
        "ds_read_b128 v[76:79], v105 offset:10240\n\t"
        "ds_read_b128 v[80:83], v106 offset:8192\n\t"
        "v_cvt_pk_bf16_f32 v88, v8, v9\n\t"
        "v_cvt_pk_bf16_f32 v89, v10, v11\n\t"
        "v_cvt_pk_bf16_f32 v90, v12, v13\n\t"
        "v_cvt_pk_bf16_f32 v91, v14, v15\n\t"
        "buffer_store_dwordx4 v[88:91], v109, %[sw], %[so] offen sc1\n\t"
        "v_cvt_pk_bf16_f32 v92, v20, v21\n\t"
        "v_cvt_pk_bf16_f32 v93, v22, v23\n\t"
        "v_cvt_pk_bf16_f32 v94, v24, v25\n\t"
        "v_cvt_pk_bf16_f32 v95, v26, v27\n\t"
        "buffer_store_dwordx4 v[92:95], v110, %[sw], %[so] offen sc1\n\t"
        "v_cvt_pk_bf16_f32 v96, v16, v17\n\t"
        "v_cvt_pk_bf16_f32 v97, v18, v19\n\t"
        "v_cvt_pk_bf16_f32 v98, v28, v29\n\t"
        "v_cvt_pk_bf16_f32 v99, v30, v31\n\t"
        "buffer_store_dwordx4 v[96:99], v111, %[sw], %[so] offen sc1\n\t"
        "s_mov_b32 %[so], 0xf0000000\n\t"
        "s_mov_b32 %[xo], 33792\n\t"
        "s_mov_b32 %[cn], 0\n\t"
        "LG_cal%=:\n\t"
        "buffer_load_dword v118, off, %[sw], %[xo] sc1\n\t"
        "s_waitcnt vmcnt(0)\n\t"
        "v_readfirstlane_b32 %[t0], v118\n\t"
        "s_cmp_lg_u32 %[t0], 0\n\t"
        "s_cbranch_scc1 LG_calok%=\n\t"
        "s_sleep 4\n\t"
        "s_add_u32 %[cn], %[cn], 1\n\t"
        "s_cmp_lt_u32 %[cn], 0x100000\n\t"
        "s_cbranch_scc1 LG_cal%=\n\t"
        "s_endpgm\n\t"
        "LG_calok%=:\n\t"
        "s_mov_b32 %[xo], 0x20000\n\t"
        "s_mov_b32 %[cn], 1\n\t"
        "v_mov_b32_e32 v119, %[cn]\n\t"
        "buffer_store_dword v119, v113, %[sw], 0 offen sc1\n\t"
        "s_add_u32 m0, %[ld], 32768\n\t"
        "s_add_u32 %[t0], %[xo], 512\n\t"
        "buffer_load_dwordx4 v100, %[sx], %[t0] offen lds\n\t"
        "s_add_u32 m0, %[ld], 33792\n\t"
        "s_add_u32 %[t0], %[xo], 16896\n\t"
        "buffer_load_dwordx4 v100, %[sx], %[t0] offen lds\n\t"
        "s_add_u32 m0, %[ld], 40960\n\t"
        "s_add_u32 %[t0], %[xo], 640\n\t"
        "buffer_load_dwordx4 v100, %[sx], %[t0] offen lds\n\t"
        "s_add_u32 m0, %[ld], 41984\n\t"
        "s_add_u32 %[t0], %[xo], 17024\n\t"
        "buffer_load_dwordx4 v100, %[sx], %[t0] offen lds\n\t"
        "s_add_u32 m0, %[ld], 49152\n\t"
        "s_add_u32 %[t0], %[xo], 768\n\t"
        "buffer_load_dwordx4 v100, %[sx], %[t0] offen lds\n\t"
        "s_add_u32 m0, %[ld], 50176\n\t"
        "s_add_u32 %[t0], %[xo], 17152\n\t"
        "buffer_load_dwordx4 v100, %[sx], %[t0] offen lds\n\t"
        "s_add_u32 m0, %[ld], 57344\n\t"
        "s_add_u32 %[t0], %[xo], 896\n\t"
        "buffer_load_dwordx4 v100, %[sx], %[t0] offen lds\n\t"
        "s_add_u32 m0, %[ld], 58368\n\t"
        "s_add_u32 %[t0], %[xo], 17280\n\t"
        "buffer_load_dwordx4 v100, %[sx], %[t0] offen lds\n\t"
        "s_add_u32 m0, %[ld], 65536\n\t"
        "s_add_u32 %[t0], %[xo], 1024\n\t"
        "buffer_load_dwordx4 v100, %[sx], %[t0] offen lds\n\t"
        "s_add_u32 m0, %[ld], 66560\n\t"
        "s_add_u32 %[t0], %[xo], 17408\n\t"
        "buffer_load_dwordx4 v100, %[sx], %[t0] offen lds\n\t"
        "s_add_u32 m0, %[ld], 73728\n\t"
        "s_add_u32 %[t0], %[xo], 1152\n\t"
        "buffer_load_dwordx4 v100, %[sx], %[t0] offen lds\n\t"
        "s_add_u32 m0, %[ld], 74752\n\t"
        "s_add_u32 %[t0], %[xo], 17536\n\t"
        "buffer_load_dwordx4 v100, %[sx], %[t0] offen lds\n\t"
        "s_add_u32 m0, %[ld], 81920\n\t"
        "s_add_u32 %[t0], %[xo], 1280\n\t"
        "buffer_load_dwordx4 v100, %[sx], %[t0] offen lds\n\t"
        "s_add_u32 m0, %[ld], 82944\n\t"
        "s_add_u32 %[t0], %[xo], 17664\n\t"
        "buffer_load_dwordx4 v100, %[sx], %[t0] offen lds\n\t"
        "s_add_u32 m0, %[ld], 90112\n\t"
        "s_add_u32 %[t0], %[xo], 1408\n\t"
        "buffer_load_dwordx4 v100, %[sx], %[t0] offen lds\n\t"
        "s_add_u32 m0, %[ld], 91136\n\t"
        "s_add_u32 %[t0], %[xo], 17792\n\t"
        "buffer_load_dwordx4 v100, %[sx], %[t0] offen lds\n\t"
        "s_add_u32 m0, %[ld], 98304\n\t"
        "s_add_u32 %[t0], %[xo], 1536\n\t"
        "buffer_load_dwordx4 v100, %[sx], %[t0] offen lds\n\t"
        "s_add_u32 m0, %[ld], 99328\n\t"
        "s_add_u32 %[t0], %[xo], 17920\n\t"
        "buffer_load_dwordx4 v100, %[sx], %[t0] offen lds\n\t"
        "s_add_u32 m0, %[ld], 106496\n\t"
        "s_add_u32 %[t0], %[xo], 1664\n\t"
        "buffer_load_dwordx4 v100, %[sx], %[t0] offen lds\n\t"
        "s_branch LG_mid%=\n\t"
        "LG_loop%=:\n\t"
        "s_waitcnt lgkmcnt(6)\n\t"
        "v_mfma_f32_16x16x32_bf16 v[8:11], a[0:3], v[56:59], 0\n\t"
        "v_mfma_f32_16x16x32_bf16 v[12:15], a[4:7], v[56:59], 0\n\t"
        "ds_read_b128 v[84:87], v106 offset:10240\n\t"
        "v_mfma_f32_16x16x32_bf16 v[16:19], v[56:59], a[8:11], 0\n\t"
        "s_add_u32 m0, %[ld], 107520\n\t"
        "s_add_u32 %[t0], %[xo], 18048\n\t"
        "s_waitcnt lgkmcnt(6)\n\t"
        "v_mfma_f32_16x16x32_bf16 v[20:23], a[0:3], v[60:63], 0\n\t"
        "buffer_load_dwordx4 v100, %[sx], %[t0] offen lds\n\t"
        "v_mfma_f32_16x16x32_bf16 v[24:27], a[4:7], v[60:63], 0\n\t"
        "ds_read_b128 v[56:59], v105 offset:16384\n\t"
        "v_mfma_f32_16x16x32_bf16 v[28:31], v[60:63], a[8:11], 0\n\t"
        "s_waitcnt lgkmcnt(6)\n\t"
        "v_mfma_f32_16x16x32_bf16 v[8:11], a[12:15], v[64:67], v[8:11]\n\t"
        "v_cvt_pk_bf16_f32 v88, v32, v33\n\t"
        "v_cvt_pk_bf16_f32 v89, v34, v35\n\t"
        "v_mfma_f32_16x16x32_bf16 v[12:15], a[16:19], v[64:67], v[12:15]\n\t"
        "ds_read_b128 v[60:63], v105 offset:18432\n\t"
        "v_cvt_pk_bf16_f32 v90, v36, v37\n\t"
        "v_mfma_f32_16x16x32_bf16 v[16:19], v[64:67], a[20:23], v[16:19]\n\t"
        "s_add_u32 m0, %[ld], 114688\n\t"
        "s_add_u32 %[t0], %[xo], 1792\n\t"
        "s_waitcnt lgkmcnt(6)\n\t"
        "v_mfma_f32_16x16x32_bf16 v[20:23], a[12:15], v[68:71], v[20:23]\n\t"
        "buffer_load_dwordx4 v100, %[sx], %[t0] offen lds\n\t"
        "v_mfma_f32_16x16x32_bf16 v[24:27], a[16:19], v[68:71], v[24:27]\n\t"
        "ds_read_b128 v[64:67], v106 offset:16384\n\t"
        "v_cvt_pk_bf16_f32 v91, v38, v39\n\t"
        "v_mfma_f32_16x16x32_bf16 v[28:31], v[68:71], a[20:23], v[28:31]\n\t"
        "buffer_store_dwordx4 v[88:91], v109, %[sw], %[so] offen sc1\n\t"
        "s_waitcnt lgkmcnt(6)\n\t"
        "v_mfma_f32_16x16x32_bf16 v[8:11], a[24:27], v[72:75], v[8:11]\n\t"
        "v_cvt_pk_bf16_f32 v92, v44, v45\n\t"
        "v_cvt_pk_bf16_f32 v93, v46, v47\n\t"
        "v_mfma_f32_16x16x32_bf16 v[12:15], a[28:31], v[72:75], v[12:15]\n\t"
        "ds_read_b128 v[68:71], v106 offset:18432\n\t"
        "v_cvt_pk_bf16_f32 v94, v48, v49\n\t"
        "v_mfma_f32_16x16x32_bf16 v[16:19], v[72:75], a[32:35], v[16:19]\n\t"
        "s_add_u32 m0, %[ld], 115712\n\t"
        "s_add_u32 %[t0], %[xo], 18176\n\t"
        "s_waitcnt lgkmcnt(6)\n\t"
        "v_mfma_f32_16x16x32_bf16 v[20:23], a[24:27], v[76:79], v[20:23]\n\t"
        "buffer_load_dwordx4 v100, %[sx], %[t0] offen lds\n\t"
        "v_mfma_f32_16x16x32_bf16 v[24:27], a[28:31], v[76:79], v[24:27]\n\t"
        "ds_read_b128 v[72:75], v105 offset:24576\n\t"
        "v_cvt_pk_bf16_f32 v95, v50, v51\n\t"
        "v_mfma_f32_16x16x32_bf16 v[28:31], v[76:79], a[32:35], v[28:31]\n\t"
        "buffer_store_dwordx4 v[92:95], v110, %[sw], %[so] offen sc1\n\t"
        "s_waitcnt lgkmcnt(6)\n\t"
        "v_mfma_f32_16x16x32_bf16 v[8:11], a[36:39], v[80:83], v[8:11]\n\t"
        "v_cvt_pk_bf16_f32 v96, v40, v41\n\t"
        "v_cvt_pk_bf16_f32 v97, v42, v43\n\t"
        "v_mfma_f32_16x16x32_bf16 v[12:15], a[40:43], v[80:83], v[12:15]\n\t"
        "ds_read_b128 v[76:79], v105 offset:26624\n\t"
        "v_cvt_pk_bf16_f32 v98, v52, v53\n\t"
        "v_mfma_f32_16x16x32_bf16 v[16:19], v[80:83], a[44:47], v[16:19]\n\t"
        "s_add_u32 m0, %[ld], 122880\n\t"
        "s_add_u32 %[t0], %[xo], 1920\n\t"
        "s_waitcnt lgkmcnt(6)\n\t"
        "v_mfma_f32_16x16x32_bf16 v[20:23], a[36:39], v[84:87], v[20:23]\n\t"
        "buffer_load_dwordx4 v100, %[sx], %[t0] offen lds\n\t"
        "v_mfma_f32_16x16x32_bf16 v[24:27], a[40:43], v[84:87], v[24:27]\n\t"
        "ds_read_b128 v[80:83], v106 offset:24576\n\t"
        "v_cvt_pk_bf16_f32 v99, v54, v55\n\t"
        "v_mfma_f32_16x16x32_bf16 v[28:31], v[84:87], a[44:47], v[28:31]\n\t"
        "buffer_store_dwordx4 v[96:99], v111, %[sw], %[so] offen sc1\n\t"
        "s_waitcnt lgkmcnt(6)\n\t"
        "v_mfma_f32_16x16x32_bf16 v[8:11], a[48:51], v[56:59], v[8:11]\n\t"
        "s_lshl_b32 %[so], %[cn], 19\n\t"
        "s_cmp_eq_u32 %[cn], 2\n\t"
        "s_cbranch_scc1 LG_first%=\n\t"
        "LG_back%=:\n\t"
        "v_mfma_f32_16x16x32_bf16 v[12:15], a[52:55], v[56:59], v[12:15]\n\t"
        "ds_read_b128 v[84:87], v106 offset:26624\n\t"
        "v_mfma_f32_16x16x32_bf16 v[16:19], v[56:59], a[56:59], v[16:19]\n\t"
        "s_add_u32 m0, %[ld], 123904\n\t"
        "s_add_u32 %[t0], %[xo], 18304\n\t"
        "s_waitcnt lgkmcnt(6)\n\t"
        "v_mfma_f32_16x16x32_bf16 v[20:23], a[48:51], v[60:63], v[20:23]\n\t"
        "buffer_load_dwordx4 v100, %[sx], %[t0] offen lds\n\t"
        "v_mfma_f32_16x16x32_bf16 v[24:27], a[52:55], v[60:63], v[24:27]\n\t"
        "v_mfma_f32_16x16x32_bf16 v[28:31], v[60:63], a[56:59], v[28:31]\n\t"
        "s_waitcnt lgkmcnt(5)\n\t"
        "v_mfma_f32_16x16x32_bf16 v[8:11], a[60:63], v[64:67], v[8:11]\n\t"
        "s_waitcnt vmcnt(20) lgkmcnt(0)\n\t"
        "s_barrier\n\t"
        "s_add_u32 %[xo], %[xo], 0x20000\n\t"
        "v_mfma_f32_16x16x32_bf16 v[12:15], a[64:67], v[64:67], v[12:15]\n\t"
        "ds_read_b128 v[56:59], v105 offset:32768\n\t"
        "v_mfma_f32_16x16x32_bf16 v[16:19], v[64:67], a[68:71], v[16:19]\n\t"
        "ds_read_b128 v[60:63], v105 offset:34816\n\t"
        "s_add_u32 m0, %[ld], 0\n\t"
        "s_add_u32 %[t0], %[xo], 0\n\t"
        "v_mfma_f32_16x16x32_bf16 v[20:23], a[60:63], v[68:71], v[20:23]\n\t"
        "buffer_load_dwordx4 v100, %[sx], %[t0] offen lds\n\t"
        "v_mfma_f32_16x16x32_bf16 v[24:27], a[64:67], v[68:71], v[24:27]\n\t"
        "ds_read_b128 v[64:67], v106 offset:32768\n\t"
        "v_mfma_f32_16x16x32_bf16 v[28:31], v[68:71], a[68:71], v[28:31]\n\t"
        "v_mfma_f32_16x16x32_bf16 v[8:11], a[72:75], v[72:75], v[8:11]\n\t"
        "v_mfma_f32_16x16x32_bf16 v[12:15], a[76:79], v[72:75], v[12:15]\n\t"
        "ds_read_b128 v[68:71], v106 offset:34816\n\t"
        "v_mfma_f32_16x16x32_bf16 v[16:19], v[72:75], a[80:83], v[16:19]\n\t"
        "s_add_u32 m0, %[ld], 1024\n\t"
        "s_add_u32 %[t0], %[xo], 16384\n\t"
        "v_mfma_f32_16x16x32_bf16 v[20:23], a[72:75], v[76:79], v[20:23]\n\t"
        "buffer_load_dwordx4 v100, %[sx], %[t0] offen lds\n\t"
        "v_mfma_f32_16x16x32_bf16 v[24:27], a[76:79], v[76:79], v[24:27]\n\t"
        "ds_read_b128 v[72:75], v105 offset:40960\n\t"
        "v_mfma_f32_16x16x32_bf16 v[28:31], v[76:79], a[80:83], v[28:31]\n\t"
        "v_mfma_f32_16x16x32_bf16 v[8:11], a[84:87], v[80:83], v[8:11]\n\t"
        "v_mfma_f32_16x16x32_bf16 v[12:15], a[88:91], v[80:83], v[12:15]\n\t"
        "ds_read_b128 v[76:79], v105 offset:43008\n\t"
        "v_mfma_f32_16x16x32_bf16 v[16:19], v[80:83], a[92:95], v[16:19]\n\t"
        "s_add_u32 m0, %[ld], 8192\n\t"
        "s_add_u32 %[t0], %[xo], 128\n\t"
        "v_mfma_f32_16x16x32_bf16 v[20:23], a[84:87], v[84:87], v[20:23]\n\t"
        "buffer_load_dwordx4 v100, %[sx], %[t0] offen lds\n\t"
        "v_mfma_f32_16x16x32_bf16 v[24:27], a[88:91], v[84:87], v[24:27]\n\t"
        "ds_read_b128 v[80:83], v106 offset:40960\n\t"
        "v_mfma_f32_16x16x32_bf16 v[28:31], v[84:87], a[92:95], v[28:31]\n\t"
        "s_waitcnt lgkmcnt(6)\n\t"
        "v_mfma_f32_16x16x32_bf16 v[8:11], a[96:99], v[56:59], v[8:11]\n\t"
        "v_mfma_f32_16x16x32_bf16 v[12:15], a[100:103], v[56:59], v[12:15]\n\t"
        "ds_read_b128 v[84:87], v106 offset:43008\n\t"
        "v_mfma_f32_16x16x32_bf16 v[16:19], v[56:59], a[104:107], v[16:19]\n\t"
        "s_add_u32 m0, %[ld], 9216\n\t"
        "s_add_u32 %[t0], %[xo], 16512\n\t"
        "s_waitcnt lgkmcnt(6)\n\t"
        "v_mfma_f32_16x16x32_bf16 v[20:23], a[96:99], v[60:63], v[20:23]\n\t"
        "buffer_load_dwordx4 v100, %[sx], %[t0] offen lds\n\t"
        "v_mfma_f32_16x16x32_bf16 v[24:27], a[100:103], v[60:63], v[24:27]\n\t"
        "ds_read_b128 v[56:59], v105 offset:49152\n\t"
        "v_mfma_f32_16x16x32_bf16 v[28:31], v[60:63], a[104:107], v[28:31]\n\t"
        "s_waitcnt lgkmcnt(6)\n\t"
        "v_mfma_f32_16x16x32_bf16 v[8:11], a[108:111], v[64:67], v[8:11]\n\t"
        "v_mfma_f32_16x16x32_bf16 v[12:15], a[112:115], v[64:67], v[12:15]\n\t"
        "ds_read_b128 v[60:63], v105 offset:51200\n\t"
        "v_mfma_f32_16x16x32_bf16 v[16:19], v[64:67], a[116:119], v[16:19]\n\t"
        "s_add_u32 m0, %[ld], 16384\n\t"
        "s_add_u32 %[t0], %[xo], 256\n\t"
        "s_waitcnt lgkmcnt(6)\n\t"
        "v_mfma_f32_16x16x32_bf16 v[20:23], a[108:111], v[68:71], v[20:23]\n\t"
        "buffer_load_dwordx4 v100, %[sx], %[t0] offen lds\n\t"
        "v_mfma_f32_16x16x32_bf16 v[24:27], a[112:115], v[68:71], v[24:27]\n\t"
        "ds_read_b128 v[64:67], v106 offset:49152\n\t"
        "v_mfma_f32_16x16x32_bf16 v[28:31], v[68:71], a[116:119], v[28:31]\n\t"
        "s_waitcnt lgkmcnt(6)\n\t"
        "v_mfma_f32_16x16x32_bf16 v[8:11], a[120:123], v[72:75], v[8:11]\n\t"
        "v_mfma_f32_16x16x32_bf16 v[12:15], a[124:127], v[72:75], v[12:15]\n\t"
        "ds_read_b128 v[68:71], v106 offset:51200\n\t"
        "v_mfma_f32_16x16x32_bf16 v[16:19], v[72:75], a[128:131], v[16:19]\n\t"
        "s_add_u32 m0, %[ld], 17408\n\t"
        "s_add_u32 %[t0], %[xo], 16640\n\t"
        "s_waitcnt lgkmcnt(6)\n\t"
        "v_mfma_f32_16x16x32_bf16 v[20:23], a[120:123], v[76:79], v[20:23]\n\t"
        "buffer_load_dwordx4 v100, %[sx], %[t0] offen lds\n\t"
        "v_mfma_f32_16x16x32_bf16 v[24:27], a[124:127], v[76:79], v[24:27]\n\t"
        "ds_read_b128 v[72:75], v105 offset:57344\n\t"
        "v_mfma_f32_16x16x32_bf16 v[28:31], v[76:79], a[128:131], v[28:31]\n\t"
        "s_waitcnt lgkmcnt(6)\n\t"
        "v_mfma_f32_16x16x32_bf16 v[8:11], a[132:135], v[80:83], v[8:11]\n\t"
        "v_mfma_f32_16x16x32_bf16 v[12:15], a[136:139], v[80:83], v[12:15]\n\t"
        "ds_read_b128 v[76:79], v105 offset:59392\n\t"
        "v_mfma_f32_16x16x32_bf16 v[16:19], v[80:83], a[140:143], v[16:19]\n\t"
        "s_add_u32 m0, %[ld], 24576\n\t"
        "s_add_u32 %[t0], %[xo], 384\n\t"
        "s_waitcnt lgkmcnt(6)\n\t"
        "v_mfma_f32_16x16x32_bf16 v[20:23], a[132:135], v[84:87], v[20:23]\n\t"
        "buffer_load_dwordx4 v100, %[sx], %[t0] offen lds\n\t"
        "v_mfma_f32_16x16x32_bf16 v[24:27], a[136:139], v[84:87], v[24:27]\n\t"
        "ds_read_b128 v[80:83], v106 offset:57344\n\t"
        "v_mfma_f32_16x16x32_bf16 v[28:31], v[84:87], a[140:143], v[28:31]\n\t"
        "s_waitcnt lgkmcnt(6)\n\t"
        "v_mfma_f32_16x16x32_bf16 v[8:11], a[144:147], v[56:59], v[8:11]\n\t"
        "v_mfma_f32_16x16x32_bf16 v[12:15], a[148:151], v[56:59], v[12:15]\n\t"
        "ds_read_b128 v[84:87], v106 offset:59392\n\t"
        "v_mfma_f32_16x16x32_bf16 v[16:19], v[56:59], a[152:155], v[16:19]\n\t"
        "s_add_u32 m0, %[ld], 25600\n\t"
        "s_add_u32 %[t0], %[xo], 16768\n\t"
        "s_waitcnt lgkmcnt(6)\n\t"
        "v_mfma_f32_16x16x32_bf16 v[20:23], a[144:147], v[60:63], v[20:23]\n\t"
        "buffer_load_dwordx4 v100, %[sx], %[t0] offen lds\n\t"
        "v_mfma_f32_16x16x32_bf16 v[24:27], a[148:151], v[60:63], v[24:27]\n\t"
        "v_mfma_f32_16x16x32_bf16 v[28:31], v[60:63], a[152:155], v[28:31]\n\t"
        "s_waitcnt lgkmcnt(5)\n\t"
        "v_mfma_f32_16x16x32_bf16 v[8:11], a[156:159], v[64:67], v[8:11]\n\t"
        "s_waitcnt vmcnt(9) lgkmcnt(0)\n\t"
        "s_barrier\n\t"
        "v_mfma_f32_16x16x32_bf16 v[12:15], a[160:163], v[64:67], v[12:15]\n\t"
        "ds_read_b128 v[56:59], v107 offset:0\n\t"
        "v_mfma_f32_16x16x32_bf16 v[16:19], v[64:67], a[164:167], v[16:19]\n\t"
        "ds_read_b128 v[60:63], v107 offset:2048\n\t"
        "s_add_u32 m0, %[ld], 32768\n\t"
        "s_add_u32 %[t0], %[xo], 512\n\t"
        "v_mfma_f32_16x16x32_bf16 v[20:23], a[156:159], v[68:71], v[20:23]\n\t"
        "buffer_load_dwordx4 v100, %[sx], %[t0] offen lds\n\t"
        "v_mfma_f32_16x16x32_bf16 v[24:27], a[160:163], v[68:71], v[24:27]\n\t"
        "ds_read_b128 v[64:67], v108 offset:0\n\t"
        "v_mfma_f32_16x16x32_bf16 v[28:31], v[68:71], a[164:167], v[28:31]\n\t"
        "v_mfma_f32_16x16x32_bf16 v[8:11], a[168:171], v[72:75], v[8:11]\n\t"
        "v_mfma_f32_16x16x32_bf16 v[12:15], a[172:175], v[72:75], v[12:15]\n\t"
        "ds_read_b128 v[68:71], v108 offset:2048\n\t"
        "v_mov_b32_e32 v119, %[cn]\n\t"
        "buffer_store_dword v119, v113, %[sw], 0 offen sc1\n\t"
        "s_add_u32 %[cn], %[cn], 1\n\t"
        "v_mfma_f32_16x16x32_bf16 v[16:19], v[72:75], a[176:179], v[16:19]\n\t"
        "s_add_u32 m0, %[ld], 33792\n\t"
        "s_add_u32 %[t0], %[xo], 16896\n\t"
        "v_mfma_f32_16x16x32_bf16 v[20:23], a[168:171], v[76:79], v[20:23]\n\t"
        "buffer_load_dwordx4 v100, %[sx], %[t0] offen lds\n\t"
        "v_mfma_f32_16x16x32_bf16 v[24:27], a[172:175], v[76:79], v[24:27]\n\t"
        "ds_read_b128 v[72:75], v107 offset:8192\n\t"
        "v_mfma_f32_16x16x32_bf16 v[28:31], v[76:79], a[176:179], v[28:31]\n\t"
        "v_mfma_f32_16x16x32_bf16 v[8:11], a[180:183], v[80:83], v[8:11]\n\t"
        "v_mfma_f32_16x16x32_bf16 v[12:15], a[184:187], v[80:83], v[12:15]\n\t"
        "ds_read_b128 v[76:79], v107 offset:10240\n\t"
        "v_mfma_f32_16x16x32_bf16 v[16:19], v[80:83], a[188:191], v[16:19]\n\t"
        "s_add_u32 m0, %[ld], 40960\n\t"
        "s_add_u32 %[t0], %[xo], 640\n\t"
        "v_mfma_f32_16x16x32_bf16 v[20:23], a[180:183], v[84:87], v[20:23]\n\t"
        "buffer_load_dwordx4 v100, %[sx], %[t0] offen lds\n\t"
        "v_mfma_f32_16x16x32_bf16 v[24:27], a[184:187], v[84:87], v[24:27]\n\t"
        "ds_read_b128 v[80:83], v108 offset:8192\n\t"
        "v_mfma_f32_16x16x32_bf16 v[28:31], v[84:87], a[188:191], v[28:31]\n\t"
        "s_waitcnt lgkmcnt(6)\n\t"
        "v_mfma_f32_16x16x32_bf16 v[8:11], a[192:195], v[56:59], v[8:11]\n\t"
        "v_mfma_f32_16x16x32_bf16 v[12:15], a[196:199], v[56:59], v[12:15]\n\t"
        "ds_read_b128 v[84:87], v108 offset:10240\n\t"
        "v_mfma_f32_16x16x32_bf16 v[16:19], v[56:59], a[200:203], v[16:19]\n\t"
        "s_add_u32 m0, %[ld], 41984\n\t"
        "s_add_u32 %[t0], %[xo], 17024\n\t"
        "s_waitcnt lgkmcnt(6)\n\t"
        "v_mfma_f32_16x16x32_bf16 v[20:23], a[192:195], v[60:63], v[20:23]\n\t"
        "buffer_load_dwordx4 v100, %[sx], %[t0] offen lds\n\t"
        "v_mfma_f32_16x16x32_bf16 v[24:27], a[196:199], v[60:63], v[24:27]\n\t"
        "ds_read_b128 v[56:59], v107 offset:16384\n\t"
        "v_mfma_f32_16x16x32_bf16 v[28:31], v[60:63], a[200:203], v[28:31]\n\t"
        "s_waitcnt lgkmcnt(6)\n\t"
        "v_mfma_f32_16x16x32_bf16 v[8:11], a[204:207], v[64:67], v[8:11]\n\t"
        "v_mfma_f32_16x16x32_bf16 v[12:15], a[208:211], v[64:67], v[12:15]\n\t"
        "ds_read_b128 v[60:63], v107 offset:18432\n\t"
        "v_mfma_f32_16x16x32_bf16 v[16:19], v[64:67], a[212:215], v[16:19]\n\t"
        "s_add_u32 m0, %[ld], 49152\n\t"
        "s_add_u32 %[t0], %[xo], 768\n\t"
        "s_waitcnt lgkmcnt(6)\n\t"
        "v_mfma_f32_16x16x32_bf16 v[20:23], a[204:207], v[68:71], v[20:23]\n\t"
        "buffer_load_dwordx4 v100, %[sx], %[t0] offen lds\n\t"
        "v_mfma_f32_16x16x32_bf16 v[24:27], a[208:211], v[68:71], v[24:27]\n\t"
        "ds_read_b128 v[64:67], v108 offset:16384\n\t"
        "v_mfma_f32_16x16x32_bf16 v[28:31], v[68:71], a[212:215], v[28:31]\n\t"
        "s_waitcnt lgkmcnt(6)\n\t"
        "v_mfma_f32_16x16x32_bf16 v[8:11], a[216:219], v[72:75], v[8:11]\n\t"
        "v_mfma_f32_16x16x32_bf16 v[12:15], a[220:223], v[72:75], v[12:15]\n\t"
        "ds_read_b128 v[68:71], v108 offset:18432\n\t"
        "v_mfma_f32_16x16x32_bf16 v[16:19], v[72:75], a[224:227], v[16:19]\n\t"
        "s_add_u32 m0, %[ld], 50176\n\t"
        "s_add_u32 %[t0], %[xo], 17152\n\t"
        "s_waitcnt lgkmcnt(6)\n\t"
        "v_mfma_f32_16x16x32_bf16 v[20:23], a[216:219], v[76:79], v[20:23]\n\t"
        "buffer_load_dwordx4 v100, %[sx], %[t0] offen lds\n\t"
        "v_mfma_f32_16x16x32_bf16 v[24:27], a[220:223], v[76:79], v[24:27]\n\t"
        "ds_read_b128 v[72:75], v107 offset:24576\n\t"
        "v_mfma_f32_16x16x32_bf16 v[28:31], v[76:79], a[224:227], v[28:31]\n\t"
        "s_waitcnt lgkmcnt(6)\n\t"
        "v_mfma_f32_16x16x32_bf16 v[8:11], a[228:231], v[80:83], v[8:11]\n\t"
        "v_mfma_f32_16x16x32_bf16 v[12:15], a[232:235], v[80:83], v[12:15]\n\t"
        "ds_read_b128 v[76:79], v107 offset:26624\n\t"
        "v_mfma_f32_16x16x32_bf16 v[16:19], v[80:83], a[236:239], v[16:19]\n\t"
        "s_add_u32 m0, %[ld], 57344\n\t"
        "s_add_u32 %[t0], %[xo], 896\n\t"
        "s_waitcnt lgkmcnt(6)\n\t"
        "v_mfma_f32_16x16x32_bf16 v[20:23], a[228:231], v[84:87], v[20:23]\n\t"
        "buffer_load_dwordx4 v100, %[sx], %[t0] offen lds\n\t"
        "v_mfma_f32_16x16x32_bf16 v[24:27], a[232:235], v[84:87], v[24:27]\n\t"
        "ds_read_b128 v[80:83], v108 offset:24576\n\t"
        "v_mfma_f32_16x16x32_bf16 v[28:31], v[84:87], a[236:239], v[28:31]\n\t"
        "s_waitcnt lgkmcnt(6)\n\t"
        "v_mfma_f32_16x16x32_bf16 v[8:11], a[240:243], v[56:59], v[8:11]\n\t"
        "v_mfma_f32_16x16x32_bf16 v[12:15], a[244:247], v[56:59], v[12:15]\n\t"
        "ds_read_b128 v[84:87], v108 offset:26624\n\t"
        "v_mfma_f32_16x16x32_bf16 v[16:19], v[56:59], a[248:251], v[16:19]\n\t"
        "s_add_u32 m0, %[ld], 58368\n\t"
        "s_add_u32 %[t0], %[xo], 17280\n\t"
        "s_waitcnt lgkmcnt(6)\n\t"
        "v_mfma_f32_16x16x32_bf16 v[20:23], a[240:243], v[60:63], v[20:23]\n\t"
        "buffer_load_dwordx4 v100, %[sx], %[t0] offen lds\n\t"
        "v_mfma_f32_16x16x32_bf16 v[24:27], a[244:247], v[60:63], v[24:27]\n\t"
        "v_mfma_f32_16x16x32_bf16 v[28:31], v[60:63], a[248:251], v[28:31]\n\t"
        "s_waitcnt lgkmcnt(5)\n\t"
        "v_mfma_f32_16x16x32_bf16 v[8:11], a[252:255], v[64:67], v[8:11]\n\t"
        "s_waitcnt vmcnt(17) lgkmcnt(0)\n\t"
        "s_barrier\n\t"
        "v_mfma_f32_16x16x32_bf16 v[12:15], v[128:131], v[64:67], v[12:15]\n\t"
        "ds_read_b128 v[56:59], v107 offset:32768\n\t"
        "v_mfma_f32_16x16x32_bf16 v[16:19], v[64:67], v[132:135], v[16:19]\n\t"
        "ds_read_b128 v[60:63], v107 offset:34816\n\t"
        "s_add_u32 m0, %[ld], 65536\n\t"
        "s_add_u32 %[t0], %[xo], 1024\n\t"
        "v_mfma_f32_16x16x32_bf16 v[20:23], a[252:255], v[68:71], v[20:23]\n\t"
        "buffer_load_dwordx4 v100, %[sx], %[t0] offen lds\n\t"
        "v_mfma_f32_16x16x32_bf16 v[24:27], v[128:131], v[68:71], v[24:27]\n\t"
        "ds_read_b128 v[64:67], v108 offset:32768\n\t"
        "v_mfma_f32_16x16x32_bf16 v[28:31], v[68:71], v[132:135], v[28:31]\n\t"
        "v_mfma_f32_16x16x32_bf16 v[8:11], v[136:139], v[72:75], v[8:11]\n\t"
        "v_mfma_f32_16x16x32_bf16 v[12:15], v[140:143], v[72:75], v[12:15]\n\t"
        "ds_read_b128 v[68:71], v108 offset:34816\n\t"
        "v_mfma_f32_16x16x32_bf16 v[16:19], v[72:75], v[144:147], v[16:19]\n\t"
        "s_add_u32 m0, %[ld], 66560\n\t"
        "s_add_u32 %[t0], %[xo], 17408\n\t"
        "v_mfma_f32_16x16x32_bf16 v[20:23], v[136:139], v[76:79], v[20:23]\n\t"
        "buffer_load_dwordx4 v100, %[sx], %[t0] offen lds\n\t"
        "v_mfma_f32_16x16x32_bf16 v[24:27], v[140:143], v[76:79], v[24:27]\n\t"
        "ds_read_b128 v[72:75], v107 offset:40960\n\t"
        "v_mfma_f32_16x16x32_bf16 v[28:31], v[76:79], v[144:147], v[28:31]\n\t"
        "v_mfma_f32_16x16x32_bf16 v[8:11], v[148:151], v[80:83], v[8:11]\n\t"
        "v_mfma_f32_16x16x32_bf16 v[12:15], v[152:155], v[80:83], v[12:15]\n\t"
        "ds_read_b128 v[76:79], v107 offset:43008\n\t"
        "v_mfma_f32_16x16x32_bf16 v[16:19], v[80:83], v[156:159], v[16:19]\n\t"
        "s_add_u32 m0, %[ld], 73728\n\t"
        "s_add_u32 %[t0], %[xo], 1152\n\t"
        "v_mfma_f32_16x16x32_bf16 v[20:23], v[148:151], v[84:87], v[20:23]\n\t"
        "buffer_load_dwordx4 v100, %[sx], %[t0] offen lds\n\t"
        "v_mfma_f32_16x16x32_bf16 v[24:27], v[152:155], v[84:87], v[24:27]\n\t"
        "ds_read_b128 v[80:83], v108 offset:40960\n\t"
        "v_mfma_f32_16x16x32_bf16 v[28:31], v[84:87], v[156:159], v[28:31]\n\t"
        "s_waitcnt lgkmcnt(6)\n\t"
        "v_mfma_f32_16x16x32_bf16 v[8:11], v[160:163], v[56:59], v[8:11]\n\t"
        "v_mfma_f32_16x16x32_bf16 v[12:15], v[164:167], v[56:59], v[12:15]\n\t"
        "ds_read_b128 v[84:87], v108 offset:43008\n\t"
        "v_mfma_f32_16x16x32_bf16 v[16:19], v[56:59], v[168:171], v[16:19]\n\t"
        "s_add_u32 m0, %[ld], 74752\n\t"
        "s_add_u32 %[t0], %[xo], 17536\n\t"
        "s_waitcnt lgkmcnt(6)\n\t"
        "v_mfma_f32_16x16x32_bf16 v[20:23], v[160:163], v[60:63], v[20:23]\n\t"
        "buffer_load_dwordx4 v100, %[sx], %[t0] offen lds\n\t"
        "v_mfma_f32_16x16x32_bf16 v[24:27], v[164:167], v[60:63], v[24:27]\n\t"
        "ds_read_b128 v[56:59], v107 offset:49152\n\t"
        "v_mfma_f32_16x16x32_bf16 v[28:31], v[60:63], v[168:171], v[28:31]\n\t"
        "s_waitcnt lgkmcnt(6)\n\t"
        "v_mfma_f32_16x16x32_bf16 v[8:11], v[172:175], v[64:67], v[8:11]\n\t"
        "v_mfma_f32_16x16x32_bf16 v[12:15], v[176:179], v[64:67], v[12:15]\n\t"
        "ds_read_b128 v[60:63], v107 offset:51200\n\t"
        "v_mfma_f32_16x16x32_bf16 v[16:19], v[64:67], v[180:183], v[16:19]\n\t"
        "s_add_u32 m0, %[ld], 81920\n\t"
        "s_add_u32 %[t0], %[xo], 1280\n\t"
        "s_waitcnt lgkmcnt(6)\n\t"
        "v_mfma_f32_16x16x32_bf16 v[20:23], v[172:175], v[68:71], v[20:23]\n\t"
        "buffer_load_dwordx4 v100, %[sx], %[t0] offen lds\n\t"
        "v_mfma_f32_16x16x32_bf16 v[24:27], v[176:179], v[68:71], v[24:27]\n\t"
        "ds_read_b128 v[64:67], v108 offset:49152\n\t"
        "v_mfma_f32_16x16x32_bf16 v[28:31], v[68:71], v[180:183], v[28:31]\n\t"
        "s_waitcnt lgkmcnt(6)\n\t"
        "v_mfma_f32_16x16x32_bf16 v[8:11], v[184:187], v[72:75], v[8:11]\n\t"
        "v_mfma_f32_16x16x32_bf16 v[12:15], v[188:191], v[72:75], v[12:15]\n\t"
        "ds_read_b128 v[68:71], v108 offset:51200\n\t"
        "v_mfma_f32_16x16x32_bf16 v[16:19], v[72:75], v[192:195], v[16:19]\n\t"
        "s_add_u32 m0, %[ld], 82944\n\t"
        "s_add_u32 %[t0], %[xo], 17664\n\t"
        "s_waitcnt lgkmcnt(6)\n\t"
        "v_mfma_f32_16x16x32_bf16 v[20:23], v[184:187], v[76:79], v[20:23]\n\t"
        "buffer_load_dwordx4 v100, %[sx], %[t0] offen lds\n\t"
        "v_mfma_f32_16x16x32_bf16 v[24:27], v[188:191], v[76:79], v[24:27]\n\t"
        "ds_read_b128 v[72:75], v107 offset:57344\n\t"
        "v_mfma_f32_16x16x32_bf16 v[28:31], v[76:79], v[192:195], v[28:31]\n\t"
        "s_waitcnt lgkmcnt(6)\n\t"
        "v_mfma_f32_16x16x32_bf16 v[8:11], v[196:199], v[80:83], v[8:11]\n\t"
        "v_mfma_f32_16x16x32_bf16 v[12:15], v[200:203], v[80:83], v[12:15]\n\t"
        "ds_read_b128 v[76:79], v107 offset:59392\n\t"
        "v_mfma_f32_16x16x32_bf16 v[16:19], v[80:83], v[204:207], v[16:19]\n\t"
        "s_add_u32 m0, %[ld], 90112\n\t"
        "s_add_u32 %[t0], %[xo], 1408\n\t"
        "s_waitcnt lgkmcnt(6)\n\t"
        "v_mfma_f32_16x16x32_bf16 v[20:23], v[196:199], v[84:87], v[20:23]\n\t"
        "buffer_load_dwordx4 v100, %[sx], %[t0] offen lds\n\t"
        "v_mfma_f32_16x16x32_bf16 v[24:27], v[200:203], v[84:87], v[24:27]\n\t"
        "ds_read_b128 v[80:83], v108 offset:57344\n\t"
        "v_mfma_f32_16x16x32_bf16 v[28:31], v[84:87], v[204:207], v[28:31]\n\t"
        "s_waitcnt lgkmcnt(6)\n\t"
        "v_mfma_f32_16x16x32_bf16 v[8:11], v[208:211], v[56:59], v[8:11]\n\t"
        "v_mfma_f32_16x16x32_bf16 v[12:15], v[212:215], v[56:59], v[12:15]\n\t"
        "ds_read_b128 v[84:87], v108 offset:59392\n\t"
        "v_mfma_f32_16x16x32_bf16 v[16:19], v[56:59], v[216:219], v[16:19]\n\t"
        "s_add_u32 m0, %[ld], 91136\n\t"
        "s_add_u32 %[t0], %[xo], 17792\n\t"
        "s_waitcnt lgkmcnt(6)\n\t"
        "v_mfma_f32_16x16x32_bf16 v[20:23], v[208:211], v[60:63], v[20:23]\n\t"
        "buffer_load_dwordx4 v100, %[sx], %[t0] offen lds\n\t"
        "v_mfma_f32_16x16x32_bf16 v[24:27], v[212:215], v[60:63], v[24:27]\n\t"
        "v_mfma_f32_16x16x32_bf16 v[28:31], v[60:63], v[216:219], v[28:31]\n\t"
        "s_waitcnt lgkmcnt(5)\n\t"
        "v_mfma_f32_16x16x32_bf16 v[8:11], v[220:223], v[64:67], v[8:11]\n\t"
        "s_waitcnt vmcnt(17) lgkmcnt(0)\n\t"
        "s_barrier\n\t"
        "v_mfma_f32_16x16x32_bf16 v[12:15], v[224:227], v[64:67], v[12:15]\n\t"
        "ds_read_b128 v[56:59], v105 offset:0\n\t"
        "v_mfma_f32_16x16x32_bf16 v[16:19], v[64:67], v[228:231], v[16:19]\n\t"
        "ds_read_b128 v[60:63], v105 offset:2048\n\t"
        "s_add_u32 m0, %[ld], 98304\n\t"
        "s_add_u32 %[t0], %[xo], 1536\n\t"
        "v_mfma_f32_16x16x32_bf16 v[20:23], v[220:223], v[68:71], v[20:23]\n\t"
        "buffer_load_dwordx4 v100, %[sx], %[t0] offen lds\n\t"
        "v_mfma_f32_16x16x32_bf16 v[24:27], v[224:227], v[68:71], v[24:27]\n\t"
        "ds_read_b128 v[64:67], v106 offset:0\n\t"
        "v_mfma_f32_16x16x32_bf16 v[28:31], v[68:71], v[228:231], v[28:31]\n\t"
        "v_mfma_f32_16x16x32_bf16 v[8:11], v[232:235], v[72:75], v[8:11]\n\t"
        "v_mfma_f32_16x16x32_bf16 v[12:15], v[236:239], v[72:75], v[12:15]\n\t"
        "ds_read_b128 v[68:71], v106 offset:2048\n\t"
        "v_mfma_f32_16x16x32_bf16 v[16:19], v[72:75], v[240:243], v[16:19]\n\t"
        "s_add_u32 m0, %[ld], 99328\n\t"
        "s_add_u32 %[t0], %[xo], 17920\n\t"
        "v_mfma_f32_16x16x32_bf16 v[20:23], v[232:235], v[76:79], v[20:23]\n\t"
        "buffer_load_dwordx4 v100, %[sx], %[t0] offen lds\n\t"
        "v_mfma_f32_16x16x32_bf16 v[24:27], v[236:239], v[76:79], v[24:27]\n\t"
        "ds_read_b128 v[72:75], v105 offset:8192\n\t"
        "v_mfma_f32_16x16x32_bf16 v[28:31], v[76:79], v[240:243], v[28:31]\n\t"
        "v_mfma_f32_16x16x32_bf16 v[8:11], v[244:247], v[80:83], v[8:11]\n\t"
        "v_mfma_f32_16x16x32_bf16 v[12:15], v[248:251], v[80:83], v[12:15]\n\t"
        "ds_read_b128 v[76:79], v105 offset:10240\n\t"
        "v_mfma_f32_16x16x32_bf16 v[16:19], v[80:83], v[252:255], v[16:19]\n\t"
        "s_add_u32 m0, %[ld], 106496\n\t"
        "s_add_u32 %[t0], %[xo], 1664\n\t"
        "v_mfma_f32_16x16x32_bf16 v[20:23], v[244:247], v[84:87], v[20:23]\n\t"
        "buffer_load_dwordx4 v100, %[sx], %[t0] offen lds\n\t"
        "v_mfma_f32_16x16x32_bf16 v[24:27], v[248:251], v[84:87], v[24:27]\n\t"
        "ds_read_b128 v[80:83], v106 offset:8192\n\t"
        "v_mfma_f32_16x16x32_bf16 v[28:31], v[84:87], v[252:255], v[28:31]\n\t"
        "LG_mid%=:\n\t"
        "s_waitcnt lgkmcnt(6)\n\t"
        "v_mfma_f32_16x16x32_bf16 v[32:35], a[0:3], v[56:59], 0\n\t"
        "v_mfma_f32_16x16x32_bf16 v[36:39], a[4:7], v[56:59], 0\n\t"
        "ds_read_b128 v[84:87], v106 offset:10240\n\t"
        "v_mfma_f32_16x16x32_bf16 v[40:43], v[56:59], a[8:11], 0\n\t"
        "s_add_u32 m0, %[ld], 107520\n\t"
        "s_add_u32 %[t0], %[xo], 18048\n\t"
        "s_waitcnt lgkmcnt(6)\n\t"
        "v_mfma_f32_16x16x32_bf16 v[44:47], a[0:3], v[60:63], 0\n\t"
        "buffer_load_dwordx4 v100, %[sx], %[t0] offen lds\n\t"
        "v_mfma_f32_16x16x32_bf16 v[48:51], a[4:7], v[60:63], 0\n\t"
        "ds_read_b128 v[56:59], v105 offset:16384\n\t"
        "v_mfma_f32_16x16x32_bf16 v[52:55], v[60:63], a[8:11], 0\n\t"
        "s_waitcnt lgkmcnt(6)\n\t"
        "v_mfma_f32_16x16x32_bf16 v[32:35], a[12:15], v[64:67], v[32:35]\n\t"
        "v_cvt_pk_bf16_f32 v88, v8, v9\n\t"
        "v_cvt_pk_bf16_f32 v89, v10, v11\n\t"
        "v_mfma_f32_16x16x32_bf16 v[36:39], a[16:19], v[64:67], v[36:39]\n\t"
        "ds_read_b128 v[60:63], v105 offset:18432\n\t"
        "v_cvt_pk_bf16_f32 v90, v12, v13\n\t"
        "v_mfma_f32_16x16x32_bf16 v[40:43], v[64:67], a[20:23], v[40:43]\n\t"
        "s_add_u32 m0, %[ld], 114688\n\t"
        "s_add_u32 %[t0], %[xo], 1792\n\t"
        "s_waitcnt lgkmcnt(6)\n\t"
        "v_mfma_f32_16x16x32_bf16 v[44:47], a[12:15], v[68:71], v[44:47]\n\t"
        "buffer_load_dwordx4 v100, %[sx], %[t0] offen lds\n\t"
        "v_mfma_f32_16x16x32_bf16 v[48:51], a[16:19], v[68:71], v[48:51]\n\t"
        "ds_read_b128 v[64:67], v106 offset:16384\n\t"
        "v_cvt_pk_bf16_f32 v91, v14, v15\n\t"
        "v_mfma_f32_16x16x32_bf16 v[52:55], v[68:71], a[20:23], v[52:55]\n\t"
        "buffer_store_dwordx4 v[88:91], v109, %[sw], %[so] offen sc1\n\t"
        "s_waitcnt lgkmcnt(6)\n\t"
        "v_mfma_f32_16x16x32_bf16 v[32:35], a[24:27], v[72:75], v[32:35]\n\t"
        "v_cvt_pk_bf16_f32 v92, v20, v21\n\t"
        "v_cvt_pk_bf16_f32 v93, v22, v23\n\t"
        "v_mfma_f32_16x16x32_bf16 v[36:39], a[28:31], v[72:75], v[36:39]\n\t"
        "ds_read_b128 v[68:71], v106 offset:18432\n\t"
        "v_cvt_pk_bf16_f32 v94, v24, v25\n\t"
        "v_mfma_f32_16x16x32_bf16 v[40:43], v[72:75], a[32:35], v[40:43]\n\t"
        "s_add_u32 m0, %[ld], 115712\n\t"
        "s_add_u32 %[t0], %[xo], 18176\n\t"
        "s_waitcnt lgkmcnt(6)\n\t"
        "v_mfma_f32_16x16x32_bf16 v[44:47], a[24:27], v[76:79], v[44:47]\n\t"
        "buffer_load_dwordx4 v100, %[sx], %[t0] offen lds\n\t"
        "v_mfma_f32_16x16x32_bf16 v[48:51], a[28:31], v[76:79], v[48:51]\n\t"
        "ds_read_b128 v[72:75], v105 offset:24576\n\t"
        "v_cvt_pk_bf16_f32 v95, v26, v27\n\t"
        "v_mfma_f32_16x16x32_bf16 v[52:55], v[76:79], a[32:35], v[52:55]\n\t"
        "buffer_store_dwordx4 v[92:95], v110, %[sw], %[so] offen sc1\n\t"
        "s_waitcnt lgkmcnt(6)\n\t"
        "v_mfma_f32_16x16x32_bf16 v[32:35], a[36:39], v[80:83], v[32:35]\n\t"
        "v_cvt_pk_bf16_f32 v96, v16, v17\n\t"
        "v_cvt_pk_bf16_f32 v97, v18, v19\n\t"
        "v_mfma_f32_16x16x32_bf16 v[36:39], a[40:43], v[80:83], v[36:39]\n\t"
        "ds_read_b128 v[76:79], v105 offset:26624\n\t"
        "v_cvt_pk_bf16_f32 v98, v28, v29\n\t"
        "v_mfma_f32_16x16x32_bf16 v[40:43], v[80:83], a[44:47], v[40:43]\n\t"
        "s_add_u32 m0, %[ld], 122880\n\t"
        "s_add_u32 %[t0], %[xo], 1920\n\t"
        "s_waitcnt lgkmcnt(6)\n\t"
        "v_mfma_f32_16x16x32_bf16 v[44:47], a[36:39], v[84:87], v[44:47]\n\t"
        "buffer_load_dwordx4 v100, %[sx], %[t0] offen lds\n\t"
        "v_mfma_f32_16x16x32_bf16 v[48:51], a[40:43], v[84:87], v[48:51]\n\t"
        "ds_read_b128 v[80:83], v106 offset:24576\n\t"
        "v_cvt_pk_bf16_f32 v99, v30, v31\n\t"
        "v_mfma_f32_16x16x32_bf16 v[52:55], v[84:87], a[44:47], v[52:55]\n\t"
        "buffer_store_dwordx4 v[96:99], v111, %[sw], %[so] offen sc1\n\t"
        "s_waitcnt lgkmcnt(6)\n\t"
        "v_mfma_f32_16x16x32_bf16 v[32:35], a[48:51], v[56:59], v[32:35]\n\t"
        "s_lshl_b32 %[so], %[cn], 19\n\t"
        "v_mfma_f32_16x16x32_bf16 v[36:39], a[52:55], v[56:59], v[36:39]\n\t"
        "ds_read_b128 v[84:87], v106 offset:26624\n\t"
        "v_mfma_f32_16x16x32_bf16 v[40:43], v[56:59], a[56:59], v[40:43]\n\t"
        "s_add_u32 m0, %[ld], 123904\n\t"
        "s_add_u32 %[t0], %[xo], 18304\n\t"
        "s_waitcnt lgkmcnt(6)\n\t"
        "v_mfma_f32_16x16x32_bf16 v[44:47], a[48:51], v[60:63], v[44:47]\n\t"
        "buffer_load_dwordx4 v100, %[sx], %[t0] offen lds\n\t"
        "v_mfma_f32_16x16x32_bf16 v[48:51], a[52:55], v[60:63], v[48:51]\n\t"
        "v_mfma_f32_16x16x32_bf16 v[52:55], v[60:63], a[56:59], v[52:55]\n\t"
        "s_waitcnt lgkmcnt(5)\n\t"
        "v_mfma_f32_16x16x32_bf16 v[32:35], a[60:63], v[64:67], v[32:35]\n\t"
        "s_waitcnt vmcnt(19) lgkmcnt(0)\n\t"
        "s_barrier\n\t"
        "s_add_u32 %[xo], %[xo], 0x20000\n\t"
        "v_mfma_f32_16x16x32_bf16 v[36:39], a[64:67], v[64:67], v[36:39]\n\t"
        "ds_read_b128 v[56:59], v105 offset:32768\n\t"
        "v_mfma_f32_16x16x32_bf16 v[40:43], v[64:67], a[68:71], v[40:43]\n\t"
        "ds_read_b128 v[60:63], v105 offset:34816\n\t"
        "s_add_u32 m0, %[ld], 0\n\t"
        "s_add_u32 %[t0], %[xo], 0\n\t"
        "v_mfma_f32_16x16x32_bf16 v[44:47], a[60:63], v[68:71], v[44:47]\n\t"
        "buffer_load_dwordx4 v100, %[sx], %[t0] offen lds\n\t"
        "v_mfma_f32_16x16x32_bf16 v[48:51], a[64:67], v[68:71], v[48:51]\n\t"
        "ds_read_b128 v[64:67], v106 offset:32768\n\t"
        "v_mfma_f32_16x16x32_bf16 v[52:55], v[68:71], a[68:71], v[52:55]\n\t"
        "v_mfma_f32_16x16x32_bf16 v[32:35], a[72:75], v[72:75], v[32:35]\n\t"
        "v_mfma_f32_16x16x32_bf16 v[36:39], a[76:79], v[72:75], v[36:39]\n\t"
        "ds_read_b128 v[68:71], v106 offset:34816\n\t"
        "v_mfma_f32_16x16x32_bf16 v[40:43], v[72:75], a[80:83], v[40:43]\n\t"
        "s_add_u32 m0, %[ld], 1024\n\t"
        "s_add_u32 %[t0], %[xo], 16384\n\t"
        "v_mfma_f32_16x16x32_bf16 v[44:47], a[72:75], v[76:79], v[44:47]\n\t"
        "buffer_load_dwordx4 v100, %[sx], %[t0] offen lds\n\t"
        "v_mfma_f32_16x16x32_bf16 v[48:51], a[76:79], v[76:79], v[48:51]\n\t"
        "ds_read_b128 v[72:75], v105 offset:40960\n\t"
        "v_mfma_f32_16x16x32_bf16 v[52:55], v[76:79], a[80:83], v[52:55]\n\t"
        "v_mfma_f32_16x16x32_bf16 v[32:35], a[84:87], v[80:83], v[32:35]\n\t"
        "v_mfma_f32_16x16x32_bf16 v[36:39], a[88:91], v[80:83], v[36:39]\n\t"
        "ds_read_b128 v[76:79], v105 offset:43008\n\t"
        "v_mfma_f32_16x16x32_bf16 v[40:43], v[80:83], a[92:95], v[40:43]\n\t"
        "s_add_u32 m0, %[ld], 8192\n\t"
        "s_add_u32 %[t0], %[xo], 128\n\t"
        "v_mfma_f32_16x16x32_bf16 v[44:47], a[84:87], v[84:87], v[44:47]\n\t"
        "buffer_load_dwordx4 v100, %[sx], %[t0] offen lds\n\t"
        "v_mfma_f32_16x16x32_bf16 v[48:51], a[88:91], v[84:87], v[48:51]\n\t"
        "ds_read_b128 v[80:83], v106 offset:40960\n\t"
        "v_mfma_f32_16x16x32_bf16 v[52:55], v[84:87], a[92:95], v[52:55]\n\t"
        "s_waitcnt lgkmcnt(6)\n\t"
        "v_mfma_f32_16x16x32_bf16 v[32:35], a[96:99], v[56:59], v[32:35]\n\t"
        "v_mfma_f32_16x16x32_bf16 v[36:39], a[100:103], v[56:59], v[36:39]\n\t"
        "ds_read_b128 v[84:87], v106 offset:43008\n\t"
        "v_mfma_f32_16x16x32_bf16 v[40:43], v[56:59], a[104:107], v[40:43]\n\t"
        "s_add_u32 m0, %[ld], 9216\n\t"
        "s_add_u32 %[t0], %[xo], 16512\n\t"
        "s_waitcnt lgkmcnt(6)\n\t"
        "v_mfma_f32_16x16x32_bf16 v[44:47], a[96:99], v[60:63], v[44:47]\n\t"
        "buffer_load_dwordx4 v100, %[sx], %[t0] offen lds\n\t"
        "v_mfma_f32_16x16x32_bf16 v[48:51], a[100:103], v[60:63], v[48:51]\n\t"
        "ds_read_b128 v[56:59], v105 offset:49152\n\t"
        "v_mfma_f32_16x16x32_bf16 v[52:55], v[60:63], a[104:107], v[52:55]\n\t"
        "s_waitcnt lgkmcnt(6)\n\t"
        "v_mfma_f32_16x16x32_bf16 v[32:35], a[108:111], v[64:67], v[32:35]\n\t"
        "v_mfma_f32_16x16x32_bf16 v[36:39], a[112:115], v[64:67], v[36:39]\n\t"
        "ds_read_b128 v[60:63], v105 offset:51200\n\t"
        "v_mfma_f32_16x16x32_bf16 v[40:43], v[64:67], a[116:119], v[40:43]\n\t"
        "s_add_u32 m0, %[ld], 16384\n\t"
        "s_add_u32 %[t0], %[xo], 256\n\t"
        "s_waitcnt lgkmcnt(6)\n\t"
        "v_mfma_f32_16x16x32_bf16 v[44:47], a[108:111], v[68:71], v[44:47]\n\t"
        "buffer_load_dwordx4 v100, %[sx], %[t0] offen lds\n\t"
        "v_mfma_f32_16x16x32_bf16 v[48:51], a[112:115], v[68:71], v[48:51]\n\t"
        "ds_read_b128 v[64:67], v106 offset:49152\n\t"
        "v_mfma_f32_16x16x32_bf16 v[52:55], v[68:71], a[116:119], v[52:55]\n\t"
        "s_waitcnt lgkmcnt(6)\n\t"
        "v_mfma_f32_16x16x32_bf16 v[32:35], a[120:123], v[72:75], v[32:35]\n\t"
        "v_mfma_f32_16x16x32_bf16 v[36:39], a[124:127], v[72:75], v[36:39]\n\t"
        "ds_read_b128 v[68:71], v106 offset:51200\n\t"
        "v_mfma_f32_16x16x32_bf16 v[40:43], v[72:75], a[128:131], v[40:43]\n\t"
        "s_add_u32 m0, %[ld], 17408\n\t"
        "s_add_u32 %[t0], %[xo], 16640\n\t"
        "s_waitcnt lgkmcnt(6)\n\t"
        "v_mfma_f32_16x16x32_bf16 v[44:47], a[120:123], v[76:79], v[44:47]\n\t"
        "buffer_load_dwordx4 v100, %[sx], %[t0] offen lds\n\t"
        "v_mfma_f32_16x16x32_bf16 v[48:51], a[124:127], v[76:79], v[48:51]\n\t"
        "ds_read_b128 v[72:75], v105 offset:57344\n\t"
        "v_mfma_f32_16x16x32_bf16 v[52:55], v[76:79], a[128:131], v[52:55]\n\t"
        "s_waitcnt lgkmcnt(6)\n\t"
        "v_mfma_f32_16x16x32_bf16 v[32:35], a[132:135], v[80:83], v[32:35]\n\t"
        "v_mfma_f32_16x16x32_bf16 v[36:39], a[136:139], v[80:83], v[36:39]\n\t"
        "ds_read_b128 v[76:79], v105 offset:59392\n\t"
        "v_mfma_f32_16x16x32_bf16 v[40:43], v[80:83], a[140:143], v[40:43]\n\t"
        "s_add_u32 m0, %[ld], 24576\n\t"
        "s_add_u32 %[t0], %[xo], 384\n\t"
        "s_waitcnt lgkmcnt(6)\n\t"
        "v_mfma_f32_16x16x32_bf16 v[44:47], a[132:135], v[84:87], v[44:47]\n\t"
        "buffer_load_dwordx4 v100, %[sx], %[t0] offen lds\n\t"
        "v_mfma_f32_16x16x32_bf16 v[48:51], a[136:139], v[84:87], v[48:51]\n\t"
        "ds_read_b128 v[80:83], v106 offset:57344\n\t"
        "v_mfma_f32_16x16x32_bf16 v[52:55], v[84:87], a[140:143], v[52:55]\n\t"
        "s_waitcnt lgkmcnt(6)\n\t"
        "v_mfma_f32_16x16x32_bf16 v[32:35], a[144:147], v[56:59], v[32:35]\n\t"
        "v_mfma_f32_16x16x32_bf16 v[36:39], a[148:151], v[56:59], v[36:39]\n\t"
        "ds_read_b128 v[84:87], v106 offset:59392\n\t"
        "v_mfma_f32_16x16x32_bf16 v[40:43], v[56:59], a[152:155], v[40:43]\n\t"
        "s_add_u32 m0, %[ld], 25600\n\t"
        "s_add_u32 %[t0], %[xo], 16768\n\t"
        "s_waitcnt lgkmcnt(6)\n\t"
        "v_mfma_f32_16x16x32_bf16 v[44:47], a[144:147], v[60:63], v[44:47]\n\t"
        "buffer_load_dwordx4 v100, %[sx], %[t0] offen lds\n\t"
        "v_mfma_f32_16x16x32_bf16 v[48:51], a[148:151], v[60:63], v[48:51]\n\t"
        "v_mfma_f32_16x16x32_bf16 v[52:55], v[60:63], a[152:155], v[52:55]\n\t"
        "s_waitcnt lgkmcnt(5)\n\t"
        "v_mfma_f32_16x16x32_bf16 v[32:35], a[156:159], v[64:67], v[32:35]\n\t"
        "s_waitcnt vmcnt(9) lgkmcnt(0)\n\t"
        "s_barrier\n\t"
        "v_mfma_f32_16x16x32_bf16 v[36:39], a[160:163], v[64:67], v[36:39]\n\t"
        "ds_read_b128 v[56:59], v107 offset:0\n\t"
        "v_mfma_f32_16x16x32_bf16 v[40:43], v[64:67], a[164:167], v[40:43]\n\t"
        "ds_read_b128 v[60:63], v107 offset:2048\n\t"
        "s_add_u32 m0, %[ld], 32768\n\t"
        "s_add_u32 %[t0], %[xo], 512\n\t"
        "v_mfma_f32_16x16x32_bf16 v[44:47], a[156:159], v[68:71], v[44:47]\n\t"
        "buffer_load_dwordx4 v100, %[sx], %[t0] offen lds\n\t"
        "v_mfma_f32_16x16x32_bf16 v[48:51], a[160:163], v[68:71], v[48:51]\n\t"
        "ds_read_b128 v[64:67], v108 offset:0\n\t"
        "v_mfma_f32_16x16x32_bf16 v[52:55], v[68:71], a[164:167], v[52:55]\n\t"
        "v_mfma_f32_16x16x32_bf16 v[32:35], a[168:171], v[72:75], v[32:35]\n\t"
        "v_mfma_f32_16x16x32_bf16 v[36:39], a[172:175], v[72:75], v[36:39]\n\t"
        "ds_read_b128 v[68:71], v108 offset:2048\n\t"
        "v_mov_b32_e32 v119, %[cn]\n\t"
        "buffer_store_dword v119, v113, %[sw], 0 offen sc1\n\t"
        "s_add_u32 %[cn], %[cn], 1\n\t"
        "v_mfma_f32_16x16x32_bf16 v[40:43], v[72:75], a[176:179], v[40:43]\n\t"
        "s_add_u32 m0, %[ld], 33792\n\t"
        "s_add_u32 %[t0], %[xo], 16896\n\t"
        "v_mfma_f32_16x16x32_bf16 v[44:47], a[168:171], v[76:79], v[44:47]\n\t"
        "buffer_load_dwordx4 v100, %[sx], %[t0] offen lds\n\t"
        "v_mfma_f32_16x16x32_bf16 v[48:51], a[172:175], v[76:79], v[48:51]\n\t"
        "ds_read_b128 v[72:75], v107 offset:8192\n\t"
        "v_mfma_f32_16x16x32_bf16 v[52:55], v[76:79], a[176:179], v[52:55]\n\t"
        "v_mfma_f32_16x16x32_bf16 v[32:35], a[180:183], v[80:83], v[32:35]\n\t"
        "v_mfma_f32_16x16x32_bf16 v[36:39], a[184:187], v[80:83], v[36:39]\n\t"
        "ds_read_b128 v[76:79], v107 offset:10240\n\t"
        "v_mfma_f32_16x16x32_bf16 v[40:43], v[80:83], a[188:191], v[40:43]\n\t"
        "s_add_u32 m0, %[ld], 40960\n\t"
        "s_add_u32 %[t0], %[xo], 640\n\t"
        "v_mfma_f32_16x16x32_bf16 v[44:47], a[180:183], v[84:87], v[44:47]\n\t"
        "buffer_load_dwordx4 v100, %[sx], %[t0] offen lds\n\t"
        "v_mfma_f32_16x16x32_bf16 v[48:51], a[184:187], v[84:87], v[48:51]\n\t"
        "ds_read_b128 v[80:83], v108 offset:8192\n\t"
        "v_mfma_f32_16x16x32_bf16 v[52:55], v[84:87], a[188:191], v[52:55]\n\t"
        "s_waitcnt lgkmcnt(6)\n\t"
        "v_mfma_f32_16x16x32_bf16 v[32:35], a[192:195], v[56:59], v[32:35]\n\t"
        "v_mfma_f32_16x16x32_bf16 v[36:39], a[196:199], v[56:59], v[36:39]\n\t"
        "ds_read_b128 v[84:87], v108 offset:10240\n\t"
        "v_mfma_f32_16x16x32_bf16 v[40:43], v[56:59], a[200:203], v[40:43]\n\t"
        "s_add_u32 m0, %[ld], 41984\n\t"
        "s_add_u32 %[t0], %[xo], 17024\n\t"
        "s_waitcnt lgkmcnt(6)\n\t"
        "v_mfma_f32_16x16x32_bf16 v[44:47], a[192:195], v[60:63], v[44:47]\n\t"
        "buffer_load_dwordx4 v100, %[sx], %[t0] offen lds\n\t"
        "v_mfma_f32_16x16x32_bf16 v[48:51], a[196:199], v[60:63], v[48:51]\n\t"
        "ds_read_b128 v[56:59], v107 offset:16384\n\t"
        "v_mfma_f32_16x16x32_bf16 v[52:55], v[60:63], a[200:203], v[52:55]\n\t"
        "s_waitcnt lgkmcnt(6)\n\t"
        "v_mfma_f32_16x16x32_bf16 v[32:35], a[204:207], v[64:67], v[32:35]\n\t"
        "v_mfma_f32_16x16x32_bf16 v[36:39], a[208:211], v[64:67], v[36:39]\n\t"
        "ds_read_b128 v[60:63], v107 offset:18432\n\t"
        "v_mfma_f32_16x16x32_bf16 v[40:43], v[64:67], a[212:215], v[40:43]\n\t"
        "s_add_u32 m0, %[ld], 49152\n\t"
        "s_add_u32 %[t0], %[xo], 768\n\t"
        "s_waitcnt lgkmcnt(6)\n\t"
        "v_mfma_f32_16x16x32_bf16 v[44:47], a[204:207], v[68:71], v[44:47]\n\t"
        "buffer_load_dwordx4 v100, %[sx], %[t0] offen lds\n\t"
        "v_mfma_f32_16x16x32_bf16 v[48:51], a[208:211], v[68:71], v[48:51]\n\t"
        "ds_read_b128 v[64:67], v108 offset:16384\n\t"
        "v_mfma_f32_16x16x32_bf16 v[52:55], v[68:71], a[212:215], v[52:55]\n\t"
        "s_waitcnt lgkmcnt(6)\n\t"
        "v_mfma_f32_16x16x32_bf16 v[32:35], a[216:219], v[72:75], v[32:35]\n\t"
        "v_mfma_f32_16x16x32_bf16 v[36:39], a[220:223], v[72:75], v[36:39]\n\t"
        "ds_read_b128 v[68:71], v108 offset:18432\n\t"
        "v_mfma_f32_16x16x32_bf16 v[40:43], v[72:75], a[224:227], v[40:43]\n\t"
        "s_add_u32 m0, %[ld], 50176\n\t"
        "s_add_u32 %[t0], %[xo], 17152\n\t"
        "s_waitcnt lgkmcnt(6)\n\t"
        "v_mfma_f32_16x16x32_bf16 v[44:47], a[216:219], v[76:79], v[44:47]\n\t"
        "buffer_load_dwordx4 v100, %[sx], %[t0] offen lds\n\t"
        "v_mfma_f32_16x16x32_bf16 v[48:51], a[220:223], v[76:79], v[48:51]\n\t"
        "ds_read_b128 v[72:75], v107 offset:24576\n\t"
        "v_mfma_f32_16x16x32_bf16 v[52:55], v[76:79], a[224:227], v[52:55]\n\t"
        "s_waitcnt lgkmcnt(6)\n\t"
        "v_mfma_f32_16x16x32_bf16 v[32:35], a[228:231], v[80:83], v[32:35]\n\t"
        "v_mfma_f32_16x16x32_bf16 v[36:39], a[232:235], v[80:83], v[36:39]\n\t"
        "ds_read_b128 v[76:79], v107 offset:26624\n\t"
        "v_mfma_f32_16x16x32_bf16 v[40:43], v[80:83], a[236:239], v[40:43]\n\t"
        "s_add_u32 m0, %[ld], 57344\n\t"
        "s_add_u32 %[t0], %[xo], 896\n\t"
        "s_waitcnt lgkmcnt(6)\n\t"
        "v_mfma_f32_16x16x32_bf16 v[44:47], a[228:231], v[84:87], v[44:47]\n\t"
        "buffer_load_dwordx4 v100, %[sx], %[t0] offen lds\n\t"
        "v_mfma_f32_16x16x32_bf16 v[48:51], a[232:235], v[84:87], v[48:51]\n\t"
        "ds_read_b128 v[80:83], v108 offset:24576\n\t"
        "v_mfma_f32_16x16x32_bf16 v[52:55], v[84:87], a[236:239], v[52:55]\n\t"
        "s_waitcnt lgkmcnt(6)\n\t"
        "v_mfma_f32_16x16x32_bf16 v[32:35], a[240:243], v[56:59], v[32:35]\n\t"
        "v_mfma_f32_16x16x32_bf16 v[36:39], a[244:247], v[56:59], v[36:39]\n\t"
        "ds_read_b128 v[84:87], v108 offset:26624\n\t"
        "v_mfma_f32_16x16x32_bf16 v[40:43], v[56:59], a[248:251], v[40:43]\n\t"
        "s_add_u32 m0, %[ld], 58368\n\t"
        "s_add_u32 %[t0], %[xo], 17280\n\t"
        "s_waitcnt lgkmcnt(6)\n\t"
        "v_mfma_f32_16x16x32_bf16 v[44:47], a[240:243], v[60:63], v[44:47]\n\t"
        "buffer_load_dwordx4 v100, %[sx], %[t0] offen lds\n\t"
        "v_mfma_f32_16x16x32_bf16 v[48:51], a[244:247], v[60:63], v[48:51]\n\t"
        "v_mfma_f32_16x16x32_bf16 v[52:55], v[60:63], a[248:251], v[52:55]\n\t"
        "s_waitcnt lgkmcnt(5)\n\t"
        "v_mfma_f32_16x16x32_bf16 v[32:35], a[252:255], v[64:67], v[32:35]\n\t"
        "s_waitcnt vmcnt(17) lgkmcnt(0)\n\t"
        "s_barrier\n\t"
        "v_mfma_f32_16x16x32_bf16 v[36:39], v[128:131], v[64:67], v[36:39]\n\t"
        "ds_read_b128 v[56:59], v107 offset:32768\n\t"
        "s_cmp_eq_u32 %[cn], %[wv]\n\t"
        "s_cselect_b32 %[t0], 0, 0xf0000000\n\t"
        "buffer_load_dword v118, v117, %[sx], %[t0] offen\n\t"
        "v_mfma_f32_16x16x32_bf16 v[40:43], v[64:67], v[132:135], v[40:43]\n\t"
        "ds_read_b128 v[60:63], v107 offset:34816\n\t"
        "s_add_u32 m0, %[ld], 65536\n\t"
        "s_add_u32 %[t0], %[xo], 1024\n\t"
        "v_mfma_f32_16x16x32_bf16 v[44:47], a[252:255], v[68:71], v[44:47]\n\t"
        "buffer_load_dwordx4 v100, %[sx], %[t0] offen lds\n\t"
        "v_mfma_f32_16x16x32_bf16 v[48:51], v[128:131], v[68:71], v[48:51]\n\t"
        "ds_read_b128 v[64:67], v108 offset:32768\n\t"
        "v_mfma_f32_16x16x32_bf16 v[52:55], v[68:71], v[132:135], v[52:55]\n\t"
        "v_mfma_f32_16x16x32_bf16 v[32:35], v[136:139], v[72:75], v[32:35]\n\t"
        "v_mfma_f32_16x16x32_bf16 v[36:39], v[140:143], v[72:75], v[36:39]\n\t"
        "ds_read_b128 v[68:71], v108 offset:34816\n\t"
        "v_mfma_f32_16x16x32_bf16 v[40:43], v[72:75], v[144:147], v[40:43]\n\t"
        "s_add_u32 m0, %[ld], 66560\n\t"
        "s_add_u32 %[t0], %[xo], 17408\n\t"
        "v_mfma_f32_16x16x32_bf16 v[44:47], v[136:139], v[76:79], v[44:47]\n\t"
        "buffer_load_dwordx4 v100, %[sx], %[t0] offen lds\n\t"
        "v_mfma_f32_16x16x32_bf16 v[48:51], v[140:143], v[76:79], v[48:51]\n\t"
        "ds_read_b128 v[72:75], v107 offset:40960\n\t"
        "v_mfma_f32_16x16x32_bf16 v[52:55], v[76:79], v[144:147], v[52:55]\n\t"
        "v_mfma_f32_16x16x32_bf16 v[32:35], v[148:151], v[80:83], v[32:35]\n\t"
        "v_mfma_f32_16x16x32_bf16 v[36:39], v[152:155], v[80:83], v[36:39]\n\t"
        "ds_read_b128 v[76:79], v107 offset:43008\n\t"
        "v_mfma_f32_16x16x32_bf16 v[40:43], v[80:83], v[156:159], v[40:43]\n\t"
        "s_add_u32 m0, %[ld], 73728\n\t"
        "s_add_u32 %[t0], %[xo], 1152\n\t"
        "v_mfma_f32_16x16x32_bf16 v[44:47], v[148:151], v[84:87], v[44:47]\n\t"
        "buffer_load_dwordx4 v100, %[sx], %[t0] offen lds\n\t"
        "v_mfma_f32_16x16x32_bf16 v[48:51], v[152:155], v[84:87], v[48:51]\n\t"
        "ds_read_b128 v[80:83], v108 offset:40960\n\t"
        "v_mfma_f32_16x16x32_bf16 v[52:55], v[84:87], v[156:159], v[52:55]\n\t"
        "s_waitcnt lgkmcnt(6)\n\t"
        "v_mfma_f32_16x16x32_bf16 v[32:35], v[160:163], v[56:59], v[32:35]\n\t"
        "v_mfma_f32_16x16x32_bf16 v[36:39], v[164:167], v[56:59], v[36:39]\n\t"
        "ds_read_b128 v[84:87], v108 offset:43008\n\t"
        "v_mfma_f32_16x16x32_bf16 v[40:43], v[56:59], v[168:171], v[40:43]\n\t"
        "s_add_u32 m0, %[ld], 74752\n\t"
        "s_add_u32 %[t0], %[xo], 17536\n\t"
        "s_waitcnt lgkmcnt(6)\n\t"
        "v_mfma_f32_16x16x32_bf16 v[44:47], v[160:163], v[60:63], v[44:47]\n\t"
        "buffer_load_dwordx4 v100, %[sx], %[t0] offen lds\n\t"
        "v_mfma_f32_16x16x32_bf16 v[48:51], v[164:167], v[60:63], v[48:51]\n\t"
        "ds_read_b128 v[56:59], v107 offset:49152\n\t"
        "v_mfma_f32_16x16x32_bf16 v[52:55], v[60:63], v[168:171], v[52:55]\n\t"
        "s_waitcnt lgkmcnt(6)\n\t"
        "v_mfma_f32_16x16x32_bf16 v[32:35], v[172:175], v[64:67], v[32:35]\n\t"
        "v_mfma_f32_16x16x32_bf16 v[36:39], v[176:179], v[64:67], v[36:39]\n\t"
        "ds_read_b128 v[60:63], v107 offset:51200\n\t"
        "v_mfma_f32_16x16x32_bf16 v[40:43], v[64:67], v[180:183], v[40:43]\n\t"
        "s_add_u32 m0, %[ld], 81920\n\t"
        "s_add_u32 %[t0], %[xo], 1280\n\t"
        "s_waitcnt lgkmcnt(6)\n\t"
        "v_mfma_f32_16x16x32_bf16 v[44:47], v[172:175], v[68:71], v[44:47]\n\t"
        "buffer_load_dwordx4 v100, %[sx], %[t0] offen lds\n\t"
        "v_mfma_f32_16x16x32_bf16 v[48:51], v[176:179], v[68:71], v[48:51]\n\t"
        "ds_read_b128 v[64:67], v108 offset:49152\n\t"
        "v_mfma_f32_16x16x32_bf16 v[52:55], v[68:71], v[180:183], v[52:55]\n\t"
        "s_waitcnt lgkmcnt(6)\n\t"
        "v_mfma_f32_16x16x32_bf16 v[32:35], v[184:187], v[72:75], v[32:35]\n\t"
        "v_mfma_f32_16x16x32_bf16 v[36:39], v[188:191], v[72:75], v[36:39]\n\t"
        "ds_read_b128 v[68:71], v108 offset:51200\n\t"
        "v_mfma_f32_16x16x32_bf16 v[40:43], v[72:75], v[192:195], v[40:43]\n\t"
        "s_add_u32 m0, %[ld], 82944\n\t"
        "s_add_u32 %[t0], %[xo], 17664\n\t"
        "s_waitcnt lgkmcnt(6)\n\t"
        "v_mfma_f32_16x16x32_bf16 v[44:47], v[184:187], v[76:79], v[44:47]\n\t"
        "buffer_load_dwordx4 v100, %[sx], %[t0] offen lds\n\t"
        "v_mfma_f32_16x16x32_bf16 v[48:51], v[188:191], v[76:79], v[48:51]\n\t"
        "ds_read_b128 v[72:75], v107 offset:57344\n\t"
        "v_mfma_f32_16x16x32_bf16 v[52:55], v[76:79], v[192:195], v[52:55]\n\t"
        "s_waitcnt lgkmcnt(6)\n\t"
        "v_mfma_f32_16x16x32_bf16 v[32:35], v[196:199], v[80:83], v[32:35]\n\t"
        "v_mfma_f32_16x16x32_bf16 v[36:39], v[200:203], v[80:83], v[36:39]\n\t"
        "ds_read_b128 v[76:79], v107 offset:59392\n\t"
        "v_mfma_f32_16x16x32_bf16 v[40:43], v[80:83], v[204:207], v[40:43]\n\t"
        "s_add_u32 m0, %[ld], 90112\n\t"
        "s_add_u32 %[t0], %[xo], 1408\n\t"
        "s_waitcnt lgkmcnt(6)\n\t"
        "v_mfma_f32_16x16x32_bf16 v[44:47], v[196:199], v[84:87], v[44:47]\n\t"
        "buffer_load_dwordx4 v100, %[sx], %[t0] offen lds\n\t"
        "v_mfma_f32_16x16x32_bf16 v[48:51], v[200:203], v[84:87], v[48:51]\n\t"
        "ds_read_b128 v[80:83], v108 offset:57344\n\t"
        "v_mfma_f32_16x16x32_bf16 v[52:55], v[84:87], v[204:207], v[52:55]\n\t"
        "s_waitcnt lgkmcnt(6)\n\t"
        "v_mfma_f32_16x16x32_bf16 v[32:35], v[208:211], v[56:59], v[32:35]\n\t"
        "v_mfma_f32_16x16x32_bf16 v[36:39], v[212:215], v[56:59], v[36:39]\n\t"
        "ds_read_b128 v[84:87], v108 offset:59392\n\t"
        "v_mfma_f32_16x16x32_bf16 v[40:43], v[56:59], v[216:219], v[40:43]\n\t"
        "s_add_u32 m0, %[ld], 91136\n\t"
        "s_add_u32 %[t0], %[xo], 17792\n\t"
        "s_waitcnt lgkmcnt(6)\n\t"
        "v_mfma_f32_16x16x32_bf16 v[44:47], v[208:211], v[60:63], v[44:47]\n\t"
        "buffer_load_dwordx4 v100, %[sx], %[t0] offen lds\n\t"
        "v_mfma_f32_16x16x32_bf16 v[48:51], v[212:215], v[60:63], v[48:51]\n\t"
        "v_mfma_f32_16x16x32_bf16 v[52:55], v[60:63], v[216:219], v[52:55]\n\t"
        "s_waitcnt lgkmcnt(5)\n\t"
        "v_mfma_f32_16x16x32_bf16 v[32:35], v[220:223], v[64:67], v[32:35]\n\t"
        "s_waitcnt vmcnt(18) lgkmcnt(0)\n\t"
        "s_barrier\n\t"
        "v_mfma_f32_16x16x32_bf16 v[36:39], v[224:227], v[64:67], v[36:39]\n\t"
        "ds_read_b128 v[56:59], v105 offset:0\n\t"
        "v_mfma_f32_16x16x32_bf16 v[40:43], v[64:67], v[228:231], v[40:43]\n\t"
        "ds_read_b128 v[60:63], v105 offset:2048\n\t"
        "s_add_u32 m0, %[ld], 98304\n\t"
        "s_add_u32 %[t0], %[xo], 1536\n\t"
        "v_mfma_f32_16x16x32_bf16 v[44:47], v[220:223], v[68:71], v[44:47]\n\t"
        "buffer_load_dwordx4 v100, %[sx], %[t0] offen lds\n\t"
        "v_mfma_f32_16x16x32_bf16 v[48:51], v[224:227], v[68:71], v[48:51]\n\t"
        "ds_read_b128 v[64:67], v106 offset:0\n\t"
        "v_mfma_f32_16x16x32_bf16 v[52:55], v[68:71], v[228:231], v[52:55]\n\t"
        "v_mfma_f32_16x16x32_bf16 v[32:35], v[232:235], v[72:75], v[32:35]\n\t"
        "v_mfma_f32_16x16x32_bf16 v[36:39], v[236:239], v[72:75], v[36:39]\n\t"
        "ds_read_b128 v[68:71], v106 offset:2048\n\t"
        "v_mfma_f32_16x16x32_bf16 v[40:43], v[72:75], v[240:243], v[40:43]\n\t"
        "s_add_u32 m0, %[ld], 99328\n\t"
        "s_add_u32 %[t0], %[xo], 17920\n\t"
        "v_mfma_f32_16x16x32_bf16 v[44:47], v[232:235], v[76:79], v[44:47]\n\t"
        "buffer_load_dwordx4 v100, %[sx], %[t0] offen lds\n\t"
        "v_mfma_f32_16x16x32_bf16 v[48:51], v[236:239], v[76:79], v[48:51]\n\t"
        "ds_read_b128 v[72:75], v105 offset:8192\n\t"
        "v_mfma_f32_16x16x32_bf16 v[52:55], v[76:79], v[240:243], v[52:55]\n\t"
        "v_mfma_f32_16x16x32_bf16 v[32:35], v[244:247], v[80:83], v[32:35]\n\t"
        "v_mfma_f32_16x16x32_bf16 v[36:39], v[248:251], v[80:83], v[36:39]\n\t"
        "ds_read_b128 v[76:79], v105 offset:10240\n\t"
        "v_mfma_f32_16x16x32_bf16 v[40:43], v[80:83], v[252:255], v[40:43]\n\t"
        "s_add_u32 m0, %[ld], 106496\n\t"
        "s_add_u32 %[t0], %[xo], 1664\n\t"
        "v_mfma_f32_16x16x32_bf16 v[44:47], v[244:247], v[84:87], v[44:47]\n\t"
        "buffer_load_dwordx4 v100, %[sx], %[t0] offen lds\n\t"
        "v_mfma_f32_16x16x32_bf16 v[48:51], v[248:251], v[84:87], v[48:51]\n\t"
        "ds_read_b128 v[80:83], v106 offset:8192\n\t"
        "v_mfma_f32_16x16x32_bf16 v[52:55], v[84:87], v[252:255], v[52:55]\n\t"
        "s_cmp_lt_u32 %[cn], 32\n\t"
        "s_cbranch_scc1 LG_loop%=\n\t"
        "s_nop 7\n\t"
        "v_cvt_pk_bf16_f32 v88, v32, v33\n\t"
        "v_cvt_pk_bf16_f32 v89, v34, v35\n\t"
        "v_cvt_pk_bf16_f32 v90, v36, v37\n\t"
        "v_cvt_pk_bf16_f32 v91, v38, v39\n\t"
        "buffer_store_dwordx4 v[88:91], v109, %[sw], %[so] offen sc1\n\t"
        "v_cvt_pk_bf16_f32 v92, v44, v45\n\t"
        "v_cvt_pk_bf16_f32 v93, v46, v47\n\t"
        "v_cvt_pk_bf16_f32 v94, v48, v49\n\t"
        "v_cvt_pk_bf16_f32 v95, v50, v51\n\t"
        "buffer_store_dwordx4 v[92:95], v110, %[sw], %[so] offen sc1\n\t"
        "v_cvt_pk_bf16_f32 v96, v40, v41\n\t"
        "v_cvt_pk_bf16_f32 v97, v42, v43\n\t"
        "v_cvt_pk_bf16_f32 v98, v52, v53\n\t"
        "v_cvt_pk_bf16_f32 v99, v54, v55\n\t"
        "buffer_store_dwordx4 v[96:99], v111, %[sw], %[so] offen sc1\n\t"
        "s_waitcnt vmcnt(0) lgkmcnt(0)\n\t"
        "v_mov_b32_e32 v119, %[cn]\n\t"
        "buffer_store_dword v119, v113, %[sw], 0 offen sc1\n\t"
        "s_barrier\n\t"
        "s_branch LG_end%=\n\t"
        "LG_first%=:\n\t"
        "s_waitcnt vmcnt(0)\n\t"
        "v_mov_b32_e32 v119, %[cn]\n\t"
        "buffer_store_dword v119, v113, %[sw], 0 offen sc1\n\t"
        "s_branch LG_back%=\n\t"
        "LG_end%=:\n\t"
        ".Lk_end_G:\n\t"
        : [t0] "=&s"(t0), [xo] "=&s"(xo), [so] "=&s"(so), [cn] "=&s"(cn)
        : [sx] "s"(sx), [sq] "s"(sq), [sv] "s"(sv), [sw] "s"(sw), [ld] "s"(lb + w * 2048), [wv] "s"(2 * w + 2),
          "{v117}"((w * 8192 + (h * 4 + c) * 64 + l) * 128),
          "{v100}"((16 * w + (l >> 3)) * 2048 + (((l & 7) ^ (l >> 3)) << 4)), "{v101}"((8 * w + (l >> 3)) * 8192 + h * 256 + (c & 1) * 128 + (l & 7) * 16),
          "{v112}"((16 * w + (l >> 2)) * 8192 + h * 256 + cv * 64 + (l & 3) * 16), "{v102}"(lb + 131072 + w * 1024 + ((l * 16) ^ ((l & 16) << 2))), "{v116}"(lb + 131072 + w * 1024 + ((l * 16) ^ (l & 32))),
          "{v103}"(lr + rr * 128 + ((tn ^ (n >> 3)) * 64) + (n & 3) * 16), "{v104}"(lr + 8192 + rr * 64 + ((tn ^ (g & 1)) * 32) + (n & 3) * 8), "{v105}"(lx), "{v106}"(lx ^ 64u), "{v107}"(lx + 65536), "{v108}"((lx ^ 64u) + 65536),
          "{v109}"(aq), "{v110}"(aq + 4096), "{v111}"(W_VF + (tm * 32 + h) * 8192 + (2 * cv + tn) * 1024 + l * 16), "{v113}"(l == 0 ? F_KVQ + h * 1024 + (c * 4 + w) * 64 : OOB)
        : "memory", "scc", "m0", "v8", "v9", "v10", "v11", "v12", "v13", "v14", "v15", "v16", "v17", "v18", "v19", "v20", "v21", "v22", "v23", "v24", "v25", "v26", "v27", "v28", "v29", "v30", "v31", "v32", "v33", "v34", "v35", "v36", "v37", "v38", "v39", "v40", "v41", "v42", "v43", "v44", "v45", "v46", "v47", "v48", "v49", "v50", "v51", "v52", "v53", "v54", "v55", "v56", "v57", "v58", "v59", "v60", "v61", "v62", "v63", "v64", "v65", "v66", "v67", "v68", "v69", "v70", "v71", "v72", "v73", "v74", "v75", "v76", "v77", "v78", "v79", "v80", "v81", "v82", "v83", "v84", "v85", "v86", "v87", "v88", "v89", "v90", "v91", "v92", "v93", "v94", "v95", "v96", "v97", "v98", "v99", "v118", "v119", "v120", "v121", "v122", "v123", "v124", "v125", "v126", "v127", "v128", "v129", "v130", "v131", "v132", "v133", "v134", "v135", "v136", "v137", "v138", "v139", "v140", "v141", "v142", "v143", "v144", "v145", "v146", "v147", "v148", "v149", "v150", "v151", "v152", "v153", "v154", "v155", "v156", "v157", "v158", "v159", "v160", "v161", "v162", "v163", "v164", "v165", "v166", "v167", "v168", "v169", "v170", "v171", "v172", "v173", "v174", "v175", "v176", "v177", "v178", "v179", "v180", "v181", "v182", "v183", "v184", "v185", "v186", "v187", "v188", "v189", "v190", "v191", "v192", "v193", "v194", "v195", "v196", "v197", "v198", "v199", "v200", "v201", "v202", "v203", "v204", "v205", "v206", "v207", "v208", "v209", "v210", "v211", "v212", "v213", "v214", "v215", "v216", "v217", "v218", "v219", "v220", "v221", "v222", "v223", "v224", "v225", "v226", "v227", "v228", "v229", "v230", "v231", "v232", "v233", "v234", "v235", "v236", "v237", "v238", "v239", "v240", "v241", "v242", "v243", "v244", "v245", "v246", "v247", "v248", "v249", "v250", "v251", "v252", "v253", "v254", "v255", "a0", "a1", "a2", "a3", "a4", "a5", "a6", "a7", "a8", "a9", "a10", "a11", "a12", "a13", "a14", "a15", "a16", "a17", "a18", "a19", "a20", "a21", "a22", "a23", "a24", "a25", "a26", "a27", "a28", "a29", "a30", "a31", "a32", "a33", "a34", "a35", "a36", "a37", "a38", "a39", "a40", "a41", "a42", "a43", "a44", "a45", "a46", "a47", "a48", "a49", "a50", "a51", "a52", "a53", "a54", "a55", "a56", "a57", "a58", "a59", "a60", "a61", "a62", "a63", "a64", "a65", "a66", "a67", "a68", "a69", "a70", "a71", "a72", "a73", "a74", "a75", "a76", "a77", "a78", "a79", "a80", "a81", "a82", "a83", "a84", "a85", "a86", "a87", "a88", "a89", "a90", "a91", "a92", "a93", "a94", "a95", "a96", "a97", "a98", "a99", "a100", "a101", "a102", "a103", "a104", "a105", "a106", "a107", "a108", "a109", "a110", "a111", "a112", "a113", "a114", "a115", "a116", "a117", "a118", "a119", "a120", "a121", "a122", "a123", "a124", "a125", "a126", "a127", "a128", "a129", "a130", "a131", "a132", "a133", "a134", "a135", "a136", "a137", "a138", "a139", "a140", "a141", "a142", "a143", "a144", "a145", "a146", "a147", "a148", "a149", "a150", "a151", "a152", "a153", "a154", "a155", "a156", "a157", "a158", "a159", "a160", "a161", "a162", "a163", "a164", "a165", "a166", "a167", "a168", "a169", "a170", "a171", "a172", "a173", "a174", "a175", "a176", "a177", "a178", "a179", "a180", "a181", "a182", "a183", "a184", "a185", "a186", "a187", "a188", "a189", "a190", "a191", "a192", "a193", "a194", "a195", "a196", "a197", "a198", "a199", "a200", "a201", "a202", "a203", "a204", "a205", "a206", "a207", "a208", "a209", "a210", "a211", "a212", "a213", "a214", "a215", "a216", "a217", "a218", "a219", "a220", "a221", "a222", "a223", "a224", "a225", "a226", "a227", "a228", "a229", "a230", "a231", "a232", "a233", "a234", "a235", "a236", "a237", "a238", "a239", "a240", "a241", "a242", "a243", "a244", "a245", "a246", "a247", "a248", "a249", "a250", "a251", "a252", "a253", "a254", "a255");
}

__device__ __forceinline__ void out_block(const Params& p, unsigned char* lds, const int wv, const int rp, const int ro, const int nt) {
    const int w = wv, l = otid() & 63, n = l & 15, g = l >> 4;
    const unsigned lb = __builtin_amdgcn_readfirstlane((unsigned)(size_t)lds), stg = lb + 131072 + w * 8192;
    const v4u so = mk_srd(p.Wo, 1024 * 4096 * 2), sw = mk_srd(p.ws, W_TOTAL), su = mk_srd(p.out, S * D * 2);
    const unsigned lr = stg + (4 * g + (n >> 2)) * 128 + ((2 * (g & 1) + (n >> 3)) << 5) + (n & 3) * 8;
    unsigned t0, t1, tl = rp, pg, cu, nx, sp, re;
    asm volatile(
        "s_nop 4\n\t"
        "ds_write_b128 v111, a[0:3] offset:0\n\t"
        "ds_write_b128 v111, a[4:7] offset:1024\n\t"
        "ds_write_b128 v111, a[8:11] offset:2048\n\t"
        "ds_write_b128 v111, a[12:15] offset:3072\n\t"
        "ds_write_b128 v111, a[16:19] offset:4096\n\t"
        "ds_write_b128 v111, a[20:23] offset:5120\n\t"
        "ds_write_b128 v111, a[24:27] offset:6144\n\t"
        "ds_write_b128 v111, a[28:31] offset:7168\n\t"
        "s_add_u32 %[t1], %[ro], 7\n\t"
        "s_and_b32 %[t1], %[t1], 7\n\t"
        "s_lshl_b32 %[t1], %[t1], 18\n\t"
        "s_add_u32 %[t0], %[t1], 131072\n\t"
        "buffer_load_dwordx4 v[232:235], v110, %[so], %[t0] offen\n\t"
        "ds_read_b64_tr_b16 a[0:1], v112 offset:0\n\t"
        "ds_read_b64_tr_b16 a[2:3], v112 offset:2048\n\t"
        "ds_read_b64_tr_b16 a[12:13], v112 offset:4096\n\t"
        "ds_read_b64_tr_b16 a[14:15], v112 offset:6144\n\t"
        "s_add_u32 %[t0], %[t1], 147456\n\t"
        "buffer_load_dwordx4 v[236:239], v110, %[so], %[t0] offen\n\t"
        "s_waitcnt lgkmcnt(8)\n\t"
        "ds_read_b64_tr_b16 a[4:5], v113 offset:0\n\t"
        "ds_read_b64_tr_b16 a[6:7], v113 offset:2048\n\t"
        "ds_read_b64_tr_b16 a[16:17], v113 offset:4096\n\t"
        "ds_read_b64_tr_b16 a[18:19], v113 offset:6144\n\t"
        "s_add_u32 %[t0], %[t1], 163840\n\t"
        "buffer_load_dwordx4 v[240:243], v110, %[so], %[t0] offen\n\t"
        "s_waitcnt lgkmcnt(8)\n\t"
        "ds_read_b64_tr_b16 a[8:9], v114 offset:0\n\t"
        "ds_read_b64_tr_b16 a[10:11], v114 offset:2048\n\t"
        "ds_read_b64_tr_b16 a[20:21], v114 offset:4096\n\t"
        "ds_read_b64_tr_b16 a[22:23], v114 offset:6144\n\t"
        "s_add_u32 %[t0], %[t1], 180224\n\t"
        "buffer_load_dwordx4 v[244:247], v110, %[so], %[t0] offen\n\t"
        "s_waitcnt lgkmcnt(8)\n\t"
        "ds_read_b64_tr_b16 v[80:81], v115 offset:0\n\t"
        "ds_read_b64_tr_b16 v[82:83], v115 offset:2048\n\t"
        "ds_read_b64_tr_b16 v[84:85], v115 offset:4096\n\t"
        "ds_read_b64_tr_b16 v[86:87], v115 offset:6144\n\t"
        "s_add_u32 %[t0], %[t1], 196608\n\t"
        "buffer_load_dwordx4 v[248:251], v110, %[so], %[t0] offen\n\t"
        "s_waitcnt lgkmcnt(0)\n\t"
        "ds_write_b128 v116, v[80:83] offset:0\n\t"
        "ds_write_b128 v116, v[84:87] offset:1024\n\t"
        "ds_write_b128 v111, a[32:35] offset:0\n\t"
        "ds_write_b128 v111, a[36:39] offset:1024\n\t"
        "ds_write_b128 v111, a[40:43] offset:2048\n\t"
        "ds_write_b128 v111, a[44:47] offset:3072\n\t"
        "ds_write_b128 v111, a[48:51] offset:4096\n\t"
        "ds_write_b128 v111, a[52:55] offset:5120\n\t"
        "ds_write_b128 v111, a[56:59] offset:6144\n\t"
        "ds_write_b128 v111, a[60:63] offset:7168\n\t"
        "s_add_u32 %[t0], %[t1], 212992\n\t"
        "buffer_load_dwordx4 v[252:255], v110, %[so], %[t0] offen\n\t"
        "ds_read_b64_tr_b16 a[24:25], v112 offset:0\n\t"
        "ds_read_b64_tr_b16 a[26:27], v112 offset:2048\n\t"
        "ds_read_b64_tr_b16 a[36:37], v112 offset:4096\n\t"
        "ds_read_b64_tr_b16 a[38:39], v112 offset:6144\n\t"
        "s_add_u32 %[t0], %[t1], 229376\n\t"
        "buffer_load_dwordx4 v[24:27], v110, %[so], %[t0] offen\n\t"
        "s_waitcnt lgkmcnt(8)\n\t"
        "ds_read_b64_tr_b16 a[28:29], v113 offset:0\n\t"
        "ds_read_b64_tr_b16 a[30:31], v113 offset:2048\n\t"
        "ds_read_b64_tr_b16 a[40:41], v113 offset:4096\n\t"
        "ds_read_b64_tr_b16 a[42:43], v113 offset:6144\n\t"
        "s_add_u32 %[t0], %[t1], 245760\n\t"
        "buffer_load_dwordx4 v[28:31], v110, %[so], %[t0] offen\n\t"
        "s_waitcnt lgkmcnt(8)\n\t"
        "ds_read_b64_tr_b16 a[32:33], v114 offset:0\n\t"
        "ds_read_b64_tr_b16 a[34:35], v114 offset:2048\n\t"
        "ds_read_b64_tr_b16 a[44:45], v114 offset:4096\n\t"
        "ds_read_b64_tr_b16 a[46:47], v114 offset:6144\n\t"
        "s_add_u32 %[t1], %[ro], 7\n\t"
        "s_and_b32 %[t1], %[t1], 7\n\t"
        "s_lshl_b32 %[t1], %[t1], 18\n\t"
        "s_add_u32 %[t0], %[t1], 0\n\t"
        "buffer_load_dwordx4 v[208:211], v110, %[so], %[t0] offen\n\t"
        "s_waitcnt lgkmcnt(8)\n\t"
        "ds_read_b64_tr_b16 v[80:81], v115 offset:0\n\t"
        "ds_read_b64_tr_b16 v[82:83], v115 offset:2048\n\t"
        "ds_read_b64_tr_b16 v[84:85], v115 offset:4096\n\t"
        "ds_read_b64_tr_b16 v[86:87], v115 offset:6144\n\t"
        "s_add_u32 %[t0], %[t1], 16384\n\t"
        "buffer_load_dwordx4 v[212:215], v110, %[so], %[t0] offen\n\t"
        "s_waitcnt lgkmcnt(0)\n\t"
        "ds_write_b128 v116, v[80:83] offset:2048\n\t"
        "ds_write_b128 v116, v[84:87] offset:3072\n\t"
        "ds_write_b128 v111, a[64:67] offset:0\n\t"
        "ds_write_b128 v111, a[68:71] offset:1024\n\t"
        "ds_write_b128 v111, a[72:75] offset:2048\n\t"
        "ds_write_b128 v111, a[76:79] offset:3072\n\t"
        "ds_write_b128 v111, a[80:83] offset:4096\n\t"
        "ds_write_b128 v111, a[84:87] offset:5120\n\t"
        "ds_write_b128 v111, a[88:91] offset:6144\n\t"
        "ds_write_b128 v111, a[92:95] offset:7168\n\t"
        "s_add_u32 %[t0], %[t1], 32768\n\t"
        "buffer_load_dwordx4 v[216:219], v110, %[so], %[t0] offen\n\t"
        "ds_read_b64_tr_b16 a[48:49], v112 offset:0\n\t"
        "ds_read_b64_tr_b16 a[50:51], v112 offset:2048\n\t"
        "ds_read_b64_tr_b16 a[60:61], v112 offset:4096\n\t"
        "ds_read_b64_tr_b16 a[62:63], v112 offset:6144\n\t"
        "s_add_u32 %[t0], %[t1], 49152\n\t"
        "buffer_load_dwordx4 v[220:223], v110, %[so], %[t0] offen\n\t"
        "s_waitcnt lgkmcnt(8)\n\t"
        "ds_read_b64_tr_b16 a[52:53], v113 offset:0\n\t"
        "ds_read_b64_tr_b16 a[54:55], v113 offset:2048\n\t"
        "ds_read_b64_tr_b16 a[64:65], v113 offset:4096\n\t"
        "ds_read_b64_tr_b16 a[66:67], v113 offset:6144\n\t"
        "s_add_u32 %[t0], %[t1], 65536\n\t"
        "buffer_load_dwordx4 v[224:227], v110, %[so], %[t0] offen\n\t"
        "s_waitcnt lgkmcnt(8)\n\t"
        "ds_read_b64_tr_b16 a[56:57], v114 offset:0\n\t"
        "ds_read_b64_tr_b16 a[58:59], v114 offset:2048\n\t"
        "ds_read_b64_tr_b16 a[68:69], v114 offset:4096\n\t"
        "ds_read_b64_tr_b16 a[70:71], v114 offset:6144\n\t"
        "s_add_u32 %[t0], %[t1], 81920\n\t"
        "buffer_load_dwordx4 v[228:231], v110, %[so], %[t0] offen\n\t"
        "s_waitcnt lgkmcnt(8)\n\t"
        "ds_read_b64_tr_b16 v[80:81], v115 offset:0\n\t"
        "ds_read_b64_tr_b16 v[82:83], v115 offset:2048\n\t"
        "ds_read_b64_tr_b16 v[84:85], v115 offset:4096\n\t"
        "ds_read_b64_tr_b16 v[86:87], v115 offset:6144\n\t"
        "s_add_u32 %[t0], %[t1], 98304\n\t"
        "buffer_load_dwordx4 v[32:35], v110, %[so], %[t0] offen\n\t"
        "s_waitcnt lgkmcnt(0)\n\t"
        "ds_write_b128 v116, v[80:83] offset:4096\n\t"
        "ds_write_b128 v116, v[84:87] offset:5120\n\t"
        "ds_write_b128 v111, a[96:99] offset:0\n\t"
        "ds_write_b128 v111, a[100:103] offset:1024\n\t"
        "ds_write_b128 v111, a[104:107] offset:2048\n\t"
        "ds_write_b128 v111, a[108:111] offset:3072\n\t"
        "ds_write_b128 v111, a[112:115] offset:4096\n\t"
        "ds_write_b128 v111, a[116:119] offset:5120\n\t"
        "ds_write_b128 v111, a[120:123] offset:6144\n\t"
        "ds_write_b128 v111, a[124:127] offset:7168\n\t"
        "s_add_u32 %[t0], %[t1], 114688\n\t"
        "buffer_load_dwordx4 v[36:39], v110, %[so], %[t0] offen\n\t"
        "ds_read_b64_tr_b16 a[72:73], v112 offset:0\n\t"
        "ds_read_b64_tr_b16 a[74:75], v112 offset:2048\n\t"
        "ds_read_b64_tr_b16 a[84:85], v112 offset:4096\n\t"
        "ds_read_b64_tr_b16 a[86:87], v112 offset:6144\n\t"
        "s_add_u32 %[t1], %[ro], 6\n\t"
        "s_and_b32 %[t1], %[t1], 7\n\t"
        "s_lshl_b32 %[t1], %[t1], 18\n\t"
        "s_add_u32 %[t0], %[t1], 131072\n\t"
        "buffer_load_dwordx4 v[184:187], v110, %[so], %[t0] offen\n\t"
        "s_waitcnt lgkmcnt(8)\n\t"
        "ds_read_b64_tr_b16 a[76:77], v113 offset:0\n\t"
        "ds_read_b64_tr_b16 a[78:79], v113 offset:2048\n\t"
        "ds_read_b64_tr_b16 a[88:89], v113 offset:4096\n\t"
        "ds_read_b64_tr_b16 a[90:91], v113 offset:6144\n\t"
        "s_add_u32 %[t0], %[t1], 147456\n\t"
        "buffer_load_dwordx4 v[188:191], v110, %[so], %[t0] offen\n\t"
        "s_waitcnt lgkmcnt(8)\n\t"
        "ds_read_b64_tr_b16 a[80:81], v114 offset:0\n\t"
        "ds_read_b64_tr_b16 a[82:83], v114 offset:2048\n\t"
        "ds_read_b64_tr_b16 a[92:93], v114 offset:4096\n\t"
        "ds_read_b64_tr_b16 a[94:95], v114 offset:6144\n\t"
        "s_add_u32 %[t0], %[t1], 163840\n\t"
        "buffer_load_dwordx4 v[192:195], v110, %[so], %[t0] offen\n\t"
        "s_waitcnt lgkmcnt(8)\n\t"
        "ds_read_b64_tr_b16 v[80:81], v115 offset:0\n\t"
        "ds_read_b64_tr_b16 v[82:83], v115 offset:2048\n\t"
        "ds_read_b64_tr_b16 v[84:85], v115 offset:4096\n\t"
        "ds_read_b64_tr_b16 v[86:87], v115 offset:6144\n\t"
        "s_add_u32 %[t0], %[t1], 180224\n\t"
        "buffer_load_dwordx4 v[196:199], v110, %[so], %[t0] offen\n\t"
        "s_waitcnt lgkmcnt(0)\n\t"
        "ds_write_b128 v116, v[80:83] offset:6144\n\t"
        "ds_write_b128 v116, v[84:87] offset:7168\n\t"
        "ds_write_b128 v111, a[128:131] offset:0\n\t"
        "ds_write_b128 v111, a[132:135] offset:1024\n\t"
        "ds_write_b128 v111, a[136:139] offset:2048\n\t"
        "ds_write_b128 v111, a[140:143] offset:3072\n\t"
        "ds_write_b128 v111, a[144:147] offset:4096\n\t"
        "ds_write_b128 v111, a[148:151] offset:5120\n\t"
        "ds_write_b128 v111, a[152:155] offset:6144\n\t"
        "ds_write_b128 v111, a[156:159] offset:7168\n\t"
        "s_add_u32 %[t0], %[t1], 196608\n\t"
        "buffer_load_dwordx4 v[200:203], v110, %[so], %[t0] offen\n\t"
        "ds_read_b64_tr_b16 a[96:97], v112 offset:0\n\t"
        "ds_read_b64_tr_b16 a[98:99], v112 offset:2048\n\t"
        "ds_read_b64_tr_b16 a[108:109], v112 offset:4096\n\t"
        "ds_read_b64_tr_b16 a[110:111], v112 offset:6144\n\t"
        "s_add_u32 %[t0], %[t1], 212992\n\t"
        "buffer_load_dwordx4 v[204:207], v110, %[so], %[t0] offen\n\t"
        "s_waitcnt lgkmcnt(8)\n\t"
        "ds_read_b64_tr_b16 a[100:101], v113 offset:0\n\t"
        "ds_read_b64_tr_b16 a[102:103], v113 offset:2048\n\t"
        "ds_read_b64_tr_b16 a[112:113], v113 offset:4096\n\t"
        "ds_read_b64_tr_b16 a[114:115], v113 offset:6144\n\t"
        "s_add_u32 %[t0], %[t1], 229376\n\t"
        "buffer_load_dwordx4 v[40:43], v110, %[so], %[t0] offen\n\t"
        "s_waitcnt lgkmcnt(8)\n\t"
        "ds_read_b64_tr_b16 a[104:105], v114 offset:0\n\t"
        "ds_read_b64_tr_b16 a[106:107], v114 offset:2048\n\t"
        "ds_read_b64_tr_b16 a[116:117], v114 offset:4096\n\t"
        "ds_read_b64_tr_b16 a[118:119], v114 offset:6144\n\t"
        "s_add_u32 %[t0], %[t1], 245760\n\t"
        "buffer_load_dwordx4 v[44:47], v110, %[so], %[t0] offen\n\t"
        "s_waitcnt lgkmcnt(8)\n\t"
        "ds_read_b64_tr_b16 v[80:81], v115 offset:0\n\t"
        "ds_read_b64_tr_b16 v[82:83], v115 offset:2048\n\t"
        "ds_read_b64_tr_b16 v[84:85], v115 offset:4096\n\t"
        "ds_read_b64_tr_b16 v[86:87], v115 offset:6144\n\t"
        "s_add_u32 %[t1], %[ro], 6\n\t"
        "s_and_b32 %[t1], %[t1], 7\n\t"
        "s_lshl_b32 %[t1], %[t1], 18\n\t"
        "s_add_u32 %[t0], %[t1], 0\n\t"
        "buffer_load_dwordx4 v[160:163], v110, %[so], %[t0] offen\n\t"
        "s_waitcnt lgkmcnt(0)\n\t"
        "ds_write_b128 v116, v[80:83] offset:8192\n\t"
        "ds_write_b128 v116, v[84:87] offset:9216\n\t"
        "ds_write_b128 v111, a[160:163] offset:0\n\t"
        "ds_write_b128 v111, a[164:167] offset:1024\n\t"
        "ds_write_b128 v111, a[168:171] offset:2048\n\t"
        "ds_write_b128 v111, a[172:175] offset:3072\n\t"
        "ds_write_b128 v111, a[176:179] offset:4096\n\t"
        "ds_write_b128 v111, a[180:183] offset:5120\n\t"
        "ds_write_b128 v111, a[184:187] offset:6144\n\t"
        "ds_write_b128 v111, a[188:191] offset:7168\n\t"
        "s_add_u32 %[t0], %[t1], 16384\n\t"
        "buffer_load_dwordx4 v[164:167], v110, %[so], %[t0] offen\n\t"
        "ds_read_b64_tr_b16 a[120:121], v112 offset:0\n\t"
        "ds_read_b64_tr_b16 a[122:123], v112 offset:2048\n\t"
        "ds_read_b64_tr_b16 a[132:133], v112 offset:4096\n\t"
        "ds_read_b64_tr_b16 a[134:135], v112 offset:6144\n\t"
        "s_add_u32 %[t0], %[t1], 32768\n\t"
        "buffer_load_dwordx4 v[168:171], v110, %[so], %[t0] offen\n\t"
        "s_waitcnt lgkmcnt(8)\n\t"
        "ds_read_b64_tr_b16 a[124:125], v113 offset:0\n\t"
        "ds_read_b64_tr_b16 a[126:127], v113 offset:2048\n\t"
        "ds_read_b64_tr_b16 a[136:137], v113 offset:4096\n\t"
        "ds_read_b64_tr_b16 a[138:139], v113 offset:6144\n\t"
        "s_add_u32 %[t0], %[t1], 49152\n\t"
        "buffer_load_dwordx4 v[172:175], v110, %[so], %[t0] offen\n\t"
        "s_waitcnt lgkmcnt(8)\n\t"
        "ds_read_b64_tr_b16 a[128:129], v114 offset:0\n\t"
        "ds_read_b64_tr_b16 a[130:131], v114 offset:2048\n\t"
        "ds_read_b64_tr_b16 a[140:141], v114 offset:4096\n\t"
        "ds_read_b64_tr_b16 a[142:143], v114 offset:6144\n\t"
        "s_add_u32 %[t0], %[t1], 65536\n\t"
        "buffer_load_dwordx4 v[176:179], v110, %[so], %[t0] offen\n\t"
        "s_waitcnt lgkmcnt(8)\n\t"
        "ds_read_b64_tr_b16 v[80:81], v115 offset:0\n\t"
        "ds_read_b64_tr_b16 v[82:83], v115 offset:2048\n\t"
        "ds_read_b64_tr_b16 v[84:85], v115 offset:4096\n\t"
        "ds_read_b64_tr_b16 v[86:87], v115 offset:6144\n\t"
        "s_add_u32 %[t0], %[t1], 81920\n\t"
        "buffer_load_dwordx4 v[180:183], v110, %[so], %[t0] offen\n\t"
        "s_waitcnt lgkmcnt(0)\n\t"
        "ds_write_b128 v116, v[80:83] offset:10240\n\t"
        "ds_write_b128 v116, v[84:87] offset:11264\n\t"
        "ds_write_b128 v111, a[192:195] offset:0\n\t"
        "ds_write_b128 v111, a[196:199] offset:1024\n\t"
        "ds_write_b128 v111, a[200:203] offset:2048\n\t"
        "ds_write_b128 v111, a[204:207] offset:3072\n\t"
        "ds_write_b128 v111, a[208:211] offset:4096\n\t"
        "ds_write_b128 v111, a[212:215] offset:5120\n\t"
        "ds_write_b128 v111, a[216:219] offset:6144\n\t"
        "ds_write_b128 v111, a[220:223] offset:7168\n\t"
        "s_add_u32 %[t0], %[t1], 98304\n\t"
        "buffer_load_dwordx4 v[48:51], v110, %[so], %[t0] offen\n\t"
        "ds_read_b64_tr_b16 a[144:145], v112 offset:0\n\t"
        "ds_read_b64_tr_b16 a[146:147], v112 offset:2048\n\t"
        "ds_read_b64_tr_b16 a[156:157], v112 offset:4096\n\t"
        "ds_read_b64_tr_b16 a[158:159], v112 offset:6144\n\t"
        "s_add_u32 %[t0], %[t1], 114688\n\t"
        "buffer_load_dwordx4 v[52:55], v110, %[so], %[t0] offen\n\t"
        "s_waitcnt lgkmcnt(8)\n\t"
        "ds_read_b64_tr_b16 a[148:149], v113 offset:0\n\t"
        "ds_read_b64_tr_b16 a[150:151], v113 offset:2048\n\t"
        "ds_read_b64_tr_b16 a[160:161], v113 offset:4096\n\t"
        "ds_read_b64_tr_b16 a[162:163], v113 offset:6144\n\t"
        "s_add_u32 %[t1], %[ro], 5\n\t"
        "s_and_b32 %[t1], %[t1], 7\n\t"
        "s_lshl_b32 %[t1], %[t1], 18\n\t"
        "s_add_u32 %[t0], %[t1], 131072\n\t"
        "buffer_load_dwordx4 v[136:139], v110, %[so], %[t0] offen\n\t"
        "s_waitcnt lgkmcnt(8)\n\t"
        "ds_read_b64_tr_b16 a[152:153], v114 offset:0\n\t"
        "ds_read_b64_tr_b16 a[154:155], v114 offset:2048\n\t"
        "ds_read_b64_tr_b16 a[164:165], v114 offset:4096\n\t"
        "ds_read_b64_tr_b16 a[166:167], v114 offset:6144\n\t"
        "s_add_u32 %[t0], %[t1], 147456\n\t"
        "buffer_load_dwordx4 v[140:143], v110, %[so], %[t0] offen\n\t"
        "s_waitcnt lgkmcnt(8)\n\t"
        "ds_read_b64_tr_b16 v[80:81], v115 offset:0\n\t"
        "ds_read_b64_tr_b16 v[82:83], v115 offset:2048\n\t"
        "ds_read_b64_tr_b16 v[84:85], v115 offset:4096\n\t"
        "ds_read_b64_tr_b16 v[86:87], v115 offset:6144\n\t"
        "s_add_u32 %[t0], %[t1], 163840\n\t"
        "buffer_load_dwordx4 v[144:147], v110, %[so], %[t0] offen\n\t"
        "s_waitcnt lgkmcnt(0)\n\t"
        "ds_write_b128 v116, v[80:83] offset:12288\n\t"
        "ds_write_b128 v116, v[84:87] offset:13312\n\t"
        "ds_write_b128 v111, a[224:227] offset:0\n\t"
        "ds_write_b128 v111, a[228:231] offset:1024\n\t"
        "ds_write_b128 v111, a[232:235] offset:2048\n\t"
        "ds_write_b128 v111, a[236:239] offset:3072\n\t"
        "ds_write_b128 v111, a[240:243] offset:4096\n\t"
        "ds_write_b128 v111, a[244:247] offset:5120\n\t"
        "ds_write_b128 v111, a[248:251] offset:6144\n\t"
        "ds_write_b128 v111, a[252:255] offset:7168\n\t"
        "s_add_u32 %[t0], %[t1], 180224\n\t"
        "buffer_load_dwordx4 v[148:151], v110, %[so], %[t0] offen\n\t"
        "ds_read_b64_tr_b16 a[168:169], v112 offset:0\n\t"
        "ds_read_b64_tr_b16 a[170:171], v112 offset:2048\n\t"
        "ds_read_b64_tr_b16 a[180:181], v112 offset:4096\n\t"
        "ds_read_b64_tr_b16 a[182:183], v112 offset:6144\n\t"
        "s_add_u32 %[t0], %[t1], 196608\n\t"
        "buffer_load_dwordx4 v[152:155], v110, %[so], %[t0] offen\n\t"
        "s_waitcnt lgkmcnt(8)\n\t"
        "ds_read_b64_tr_b16 a[172:173], v113 offset:0\n\t"
        "ds_read_b64_tr_b16 a[174:175], v113 offset:2048\n\t"
        "ds_read_b64_tr_b16 a[184:185], v113 offset:4096\n\t"
        "ds_read_b64_tr_b16 a[186:187], v113 offset:6144\n\t"
        "s_add_u32 %[t0], %[t1], 212992\n\t"
        "buffer_load_dwordx4 v[156:159], v110, %[so], %[t0] offen\n\t"
        "s_waitcnt lgkmcnt(8)\n\t"
        "ds_read_b64_tr_b16 a[176:177], v114 offset:0\n\t"
        "ds_read_b64_tr_b16 a[178:179], v114 offset:2048\n\t"
        "ds_read_b64_tr_b16 a[188:189], v114 offset:4096\n\t"
        "ds_read_b64_tr_b16 a[190:191], v114 offset:6144\n\t"
        "s_add_u32 %[t0], %[t1], 229376\n\t"
        "buffer_load_dwordx4 v[56:59], v110, %[so], %[t0] offen\n\t"
        "s_waitcnt lgkmcnt(8)\n\t"
        "ds_read_b64_tr_b16 v[80:81], v115 offset:0\n\t"
        "ds_read_b64_tr_b16 v[82:83], v115 offset:2048\n\t"
        "ds_read_b64_tr_b16 v[84:85], v115 offset:4096\n\t"
        "ds_read_b64_tr_b16 v[86:87], v115 offset:6144\n\t"
        "s_add_u32 %[t0], %[t1], 245760\n\t"
        "buffer_load_dwordx4 v[60:63], v110, %[so], %[t0] offen\n\t"
        "s_waitcnt lgkmcnt(0)\n\t"
        "ds_write_b128 v116, v[80:83] offset:14336\n\t"
        "ds_write_b128 v116, v[84:87] offset:15360\n\t"
        "s_add_u32 %[t1], %[ro], 5\n\t"
        "s_and_b32 %[t1], %[t1], 7\n\t"
        "s_lshl_b32 %[t1], %[t1], 18\n\t"
        "s_add_u32 %[t0], %[t1], 0\n\t"
        "buffer_load_dwordx4 a[240:243], v110, %[so], %[t0] offen\n\t"
        "s_waitcnt vmcnt(33)\n\t"
        "ds_write_b128 v111, v[232:235] offset:0\n\t"
        "ds_write_b128 v111, v[236:239] offset:1024\n\t"
        "ds_write_b128 v111, v[240:243] offset:2048\n\t"
        "ds_write_b128 v111, v[244:247] offset:3072\n\t"
        "ds_write_b128 v111, v[248:251] offset:4096\n\t"
        "ds_write_b128 v111, v[252:255] offset:5120\n\t"
        "ds_write_b128 v111, v[24:27] offset:6144\n\t"
        "ds_write_b128 v111, v[28:31] offset:7168\n\t"
        "s_add_u32 %[t0], %[t1], 16384\n\t"
        "buffer_load_dwordx4 a[244:247], v110, %[so], %[t0] offen\n\t"
        "ds_read_b64_tr_b16 v[232:233], v112 offset:0\n\t"
        "ds_read_b64_tr_b16 v[234:235], v112 offset:2048\n\t"
        "ds_read_b64_tr_b16 v[244:245], v112 offset:4096\n\t"
        "ds_read_b64_tr_b16 v[246:247], v112 offset:6144\n\t"
        "s_add_u32 %[t0], %[t1], 32768\n\t"
        "buffer_load_dwordx4 a[248:251], v110, %[so], %[t0] offen\n\t"
        "s_waitcnt lgkmcnt(8)\n\t"
        "ds_read_b64_tr_b16 v[236:237], v113 offset:0\n\t"
        "ds_read_b64_tr_b16 v[238:239], v113 offset:2048\n\t"
        "ds_read_b64_tr_b16 v[248:249], v113 offset:4096\n\t"
        "ds_read_b64_tr_b16 v[250:251], v113 offset:6144\n\t"
        "s_add_u32 %[t0], %[t1], 49152\n\t"
        "buffer_load_dwordx4 a[252:255], v110, %[so], %[t0] offen\n\t"
        "s_waitcnt lgkmcnt(8)\n\t"
        "ds_read_b64_tr_b16 v[240:241], v114 offset:0\n\t"
        "ds_read_b64_tr_b16 v[242:243], v114 offset:2048\n\t"
        "ds_read_b64_tr_b16 v[252:253], v114 offset:4096\n\t"
        "ds_read_b64_tr_b16 v[254:255], v114 offset:6144\n\t"
        "s_add_u32 %[t0], %[t1], 65536\n\t"
        "buffer_load_dwordx4 v[128:131], v110, %[so], %[t0] offen\n\t"
        "s_waitcnt lgkmcnt(8)\n\t"
        "ds_read_b64_tr_b16 v[80:81], v115 offset:0\n\t"
        "ds_read_b64_tr_b16 v[82:83], v115 offset:2048\n\t"
        "ds_read_b64_tr_b16 v[84:85], v115 offset:4096\n\t"
        "ds_read_b64_tr_b16 v[86:87], v115 offset:6144\n\t"
        "s_add_u32 %[t0], %[t1], 81920\n\t"
        "buffer_load_dwordx4 v[132:135], v110, %[so], %[t0] offen\n\t"
        "s_waitcnt lgkmcnt(0)\n\t"
        "ds_write_b128 v116, v[80:83] offset:30720\n\t"
        "ds_write_b128 v116, v[84:87] offset:31744\n\t"
        "s_add_u32 %[t0], %[t1], 98304\n\t"
        "buffer_load_dwordx4 v[64:67], v110, %[so], %[t0] offen\n\t"
        "s_waitcnt vmcnt(31)\n\t"
        "ds_write_b128 v111, v[208:211] offset:0\n\t"
        "ds_write_b128 v111, v[212:215] offset:1024\n\t"
        "ds_write_b128 v111, v[216:219] offset:2048\n\t"
        "ds_write_b128 v111, v[220:223] offset:3072\n\t"
        "ds_write_b128 v111, v[224:227] offset:4096\n\t"
        "ds_write_b128 v111, v[228:231] offset:5120\n\t"
        "ds_write_b128 v111, v[32:35] offset:6144\n\t"
        "ds_write_b128 v111, v[36:39] offset:7168\n\t"
        "s_add_u32 %[t0], %[t1], 114688\n\t"
        "buffer_load_dwordx4 v[68:71], v110, %[so], %[t0] offen\n\t"
        "ds_read_b64_tr_b16 v[208:209], v112 offset:0\n\t"
        "ds_read_b64_tr_b16 v[210:211], v112 offset:2048\n\t"
        "ds_read_b64_tr_b16 v[220:221], v112 offset:4096\n\t"
        "ds_read_b64_tr_b16 v[222:223], v112 offset:6144\n\t"
        "s_add_u32 %[t1], %[ro], 4\n\t"
        "s_and_b32 %[t1], %[t1], 7\n\t"
        "s_lshl_b32 %[t1], %[t1], 18\n\t"
        "s_add_u32 %[t0], %[t1], 131072\n\t"
        "buffer_load_dwordx4 a[216:219], v110, %[so], %[t0] offen\n\t"
        "s_waitcnt lgkmcnt(8)\n\t"
        "ds_read_b64_tr_b16 v[212:213], v113 offset:0\n\t"
        "ds_read_b64_tr_b16 v[214:215], v113 offset:2048\n\t"
        "ds_read_b64_tr_b16 v[224:225], v113 offset:4096\n\t"
        "ds_read_b64_tr_b16 v[226:227], v113 offset:6144\n\t"
        "s_add_u32 %[t0], %[t1], 147456\n\t"
        "buffer_load_dwordx4 a[220:223], v110, %[so], %[t0] offen\n\t"
        "s_waitcnt lgkmcnt(8)\n\t"
        "ds_read_b64_tr_b16 v[216:217], v114 offset:0\n\t"
        "ds_read_b64_tr_b16 v[218:219], v114 offset:2048\n\t"
        "ds_read_b64_tr_b16 v[228:229], v114 offset:4096\n\t"
        "ds_read_b64_tr_b16 v[230:231], v114 offset:6144\n\t"
        "s_add_u32 %[t0], %[t1], 163840\n\t"
        "buffer_load_dwordx4 a[224:227], v110, %[so], %[t0] offen\n\t"
        "s_waitcnt lgkmcnt(8)\n\t"
        "ds_read_b64_tr_b16 v[80:81], v115 offset:0\n\t"
        "ds_read_b64_tr_b16 v[82:83], v115 offset:2048\n\t"
        "ds_read_b64_tr_b16 v[84:85], v115 offset:4096\n\t"
        "ds_read_b64_tr_b16 v[86:87], v115 offset:6144\n\t"
        "s_add_u32 %[t0], %[t1], 180224\n\t"
        "buffer_load_dwordx4 a[228:231], v110, %[so], %[t0] offen\n\t"
        "s_waitcnt lgkmcnt(0)\n\t"
        "ds_write_b128 v116, v[80:83] offset:28672\n\t"
        "ds_write_b128 v116, v[84:87] offset:29696\n\t"
        "s_add_u32 %[t0], %[t1], 196608\n\t"
        "buffer_load_dwordx4 a[232:235], v110, %[so], %[t0] offen\n\t"
        "s_waitcnt vmcnt(29)\n\t"
        "ds_write_b128 v111, v[184:187] offset:0\n\t"
        "ds_write_b128 v111, v[188:191] offset:1024\n\t"
        "ds_write_b128 v111, v[192:195] offset:2048\n\t"
        "ds_write_b128 v111, v[196:199] offset:3072\n\t"
        "ds_write_b128 v111, v[200:203] offset:4096\n\t"
        "ds_write_b128 v111, v[204:207] offset:5120\n\t"
        "ds_write_b128 v111, v[40:43] offset:6144\n\t"
        "ds_write_b128 v111, v[44:47] offset:7168\n\t"
        "s_add_u32 %[t0], %[t1], 212992\n\t"
        "buffer_load_dwordx4 a[236:239], v110, %[so], %[t0] offen\n\t"
        "ds_read_b64_tr_b16 v[184:185], v112 offset:0\n\t"
        "ds_read_b64_tr_b16 v[186:187], v112 offset:2048\n\t"
        "ds_read_b64_tr_b16 v[196:197], v112 offset:4096\n\t"
        "ds_read_b64_tr_b16 v[198:199], v112 offset:6144\n\t"
        "s_add_u32 %[t0], %[t1], 229376\n\t"
        "buffer_load_dwordx4 v[72:75], v110, %[so], %[t0] offen\n\t"
        "s_waitcnt lgkmcnt(8)\n\t"
        "ds_read_b64_tr_b16 v[188:189], v113 offset:0\n\t"
        "ds_read_b64_tr_b16 v[190:191], v113 offset:2048\n\t"
        "ds_read_b64_tr_b16 v[200:201], v113 offset:4096\n\t"
        "ds_read_b64_tr_b16 v[202:203], v113 offset:6144\n\t"
        "s_add_u32 %[t0], %[t1], 245760\n\t"
        "buffer_load_dwordx4 v[76:79], v110, %[so], %[t0] offen\n\t"
        "s_waitcnt lgkmcnt(8)\n\t"
        "ds_read_b64_tr_b16 v[192:193], v114 offset:0\n\t"
        "ds_read_b64_tr_b16 v[194:195], v114 offset:2048\n\t"
        "ds_read_b64_tr_b16 v[204:205], v114 offset:4096\n\t"
        "ds_read_b64_tr_b16 v[206:207], v114 offset:6144\n\t"
        "s_add_u32 %[t1], %[ro], 4\n\t"
        "s_and_b32 %[t1], %[t1], 7\n\t"
        "s_lshl_b32 %[t1], %[t1], 18\n\t"
        "s_add_u32 %[t0], %[t1], 0\n\t"
        "buffer_load_dwordx4 a[192:195], v110, %[so], %[t0] offen\n\t"
        "s_waitcnt lgkmcnt(8)\n\t"
        "ds_read_b64_tr_b16 v[80:81], v115 offset:0\n\t"
        "ds_read_b64_tr_b16 v[82:83], v115 offset:2048\n\t"
        "ds_read_b64_tr_b16 v[84:85], v115 offset:4096\n\t"
        "ds_read_b64_tr_b16 v[86:87], v115 offset:6144\n\t"
        "s_add_u32 %[t0], %[t1], 16384\n\t"
        "buffer_load_dwordx4 a[196:199], v110, %[so], %[t0] offen\n\t"
        "s_waitcnt lgkmcnt(0)\n\t"
        "ds_write_b128 v116, v[80:83] offset:26624\n\t"
        "ds_write_b128 v116, v[84:87] offset:27648\n\t"
        "s_add_u32 %[t0], %[t1], 32768\n\t"
        "buffer_load_dwordx4 a[200:203], v110, %[so], %[t0] offen\n\t"
        "s_waitcnt vmcnt(27)\n\t"
        "ds_write_b128 v111, v[160:163] offset:0\n\t"
        "ds_write_b128 v111, v[164:167] offset:1024\n\t"
        "ds_write_b128 v111, v[168:171] offset:2048\n\t"
        "ds_write_b128 v111, v[172:175] offset:3072\n\t"
        "ds_write_b128 v111, v[176:179] offset:4096\n\t"
        "ds_write_b128 v111, v[180:183] offset:5120\n\t"
        "ds_write_b128 v111, v[48:51] offset:6144\n\t"
        "ds_write_b128 v111, v[52:55] offset:7168\n\t"
        "s_add_u32 %[t0], %[t1], 49152\n\t"
        "buffer_load_dwordx4 a[204:207], v110, %[so], %[t0] offen\n\t"
        "ds_read_b64_tr_b16 v[160:161], v112 offset:0\n\t"
        "ds_read_b64_tr_b16 v[162:163], v112 offset:2048\n\t"
        "ds_read_b64_tr_b16 v[172:173], v112 offset:4096\n\t"
        "ds_read_b64_tr_b16 v[174:175], v112 offset:6144\n\t"
        "s_add_u32 %[t0], %[t1], 65536\n\t"
        "buffer_load_dwordx4 a[208:211], v110, %[so], %[t0] offen\n\t"
        "s_waitcnt lgkmcnt(8)\n\t"
        "ds_read_b64_tr_b16 v[164:165], v113 offset:0\n\t"
        "ds_read_b64_tr_b16 v[166:167], v113 offset:2048\n\t"
        "ds_read_b64_tr_b16 v[176:177], v113 offset:4096\n\t"
        "ds_read_b64_tr_b16 v[178:179], v113 offset:6144\n\t"
        "s_add_u32 %[t0], %[t1], 81920\n\t"
        "buffer_load_dwordx4 a[212:215], v110, %[so], %[t0] offen\n\t"
        "s_waitcnt lgkmcnt(8)\n\t"
        "ds_read_b64_tr_b16 v[168:169], v114 offset:0\n\t"
        "ds_read_b64_tr_b16 v[170:171], v114 offset:2048\n\t"
        "ds_read_b64_tr_b16 v[180:181], v114 offset:4096\n\t"
        "ds_read_b64_tr_b16 v[182:183], v114 offset:6144\n\t"
        "s_add_u32 %[t0], %[t1], 98304\n\t"
        "buffer_load_dwordx4 v[24:27], v110, %[so], %[t0] offen\n\t"
        "s_waitcnt lgkmcnt(8)\n\t"
        "ds_read_b64_tr_b16 v[80:81], v115 offset:0\n\t"
        "ds_read_b64_tr_b16 v[82:83], v115 offset:2048\n\t"
        "ds_read_b64_tr_b16 v[84:85], v115 offset:4096\n\t"
        "ds_read_b64_tr_b16 v[86:87], v115 offset:6144\n\t"
        "s_add_u32 %[t0], %[t1], 114688\n\t"
        "buffer_load_dwordx4 v[28:31], v110, %[so], %[t0] offen\n\t"
        "s_waitcnt lgkmcnt(0)\n\t"
        "ds_write_b128 v116, v[80:83] offset:24576\n\t"
        "ds_write_b128 v116, v[84:87] offset:25600\n\t"
        "s_waitcnt vmcnt(24)\n\t"
        "ds_write_b128 v111, v[136:139] offset:0\n\t"
        "ds_write_b128 v111, v[140:143] offset:1024\n\t"
        "ds_write_b128 v111, v[144:147] offset:2048\n\t"
        "ds_write_b128 v111, v[148:151] offset:3072\n\t"
        "ds_write_b128 v111, v[152:155] offset:4096\n\t"
        "ds_write_b128 v111, v[156:159] offset:5120\n\t"
        "ds_write_b128 v111, v[56:59] offset:6144\n\t"
        "ds_write_b128 v111, v[60:63] offset:7168\n\t"
        "ds_read_b64_tr_b16 v[136:137], v112 offset:0\n\t"
        "ds_read_b64_tr_b16 v[138:139], v112 offset:2048\n\t"
        "ds_read_b64_tr_b16 v[148:149], v112 offset:4096\n\t"
        "ds_read_b64_tr_b16 v[150:151], v112 offset:6144\n\t"
        "s_waitcnt lgkmcnt(8)\n\t"
        "ds_read_b64_tr_b16 v[140:141], v113 offset:0\n\t"
        "ds_read_b64_tr_b16 v[142:143], v113 offset:2048\n\t"
        "ds_read_b64_tr_b16 v[152:153], v113 offset:4096\n\t"
        "ds_read_b64_tr_b16 v[154:155], v113 offset:6144\n\t"
        "s_waitcnt lgkmcnt(8)\n\t"
        "ds_read_b64_tr_b16 v[144:145], v114 offset:0\n\t"
        "ds_read_b64_tr_b16 v[146:147], v114 offset:2048\n\t"
        "ds_read_b64_tr_b16 v[156:157], v114 offset:4096\n\t"
        "ds_read_b64_tr_b16 v[158:159], v114 offset:6144\n\t"
        "s_waitcnt lgkmcnt(8)\n\t"
        "ds_read_b64_tr_b16 v[80:81], v115 offset:0\n\t"
        "ds_read_b64_tr_b16 v[82:83], v115 offset:2048\n\t"
        "ds_read_b64_tr_b16 v[84:85], v115 offset:4096\n\t"
        "ds_read_b64_tr_b16 v[86:87], v115 offset:6144\n\t"
        "s_waitcnt lgkmcnt(0)\n\t"
        "ds_write_b128 v116, v[80:83] offset:22528\n\t"
        "ds_write_b128 v116, v[84:87] offset:23552\n\t"
        "s_waitcnt vmcnt(16)\n\t"
        "ds_write_b128 v111, a[240:243] offset:0\n\t"
        "ds_write_b128 v111, a[244:247] offset:1024\n\t"
        "ds_write_b128 v111, a[248:251] offset:2048\n\t"
        "ds_write_b128 v111, a[252:255] offset:3072\n\t"
        "ds_write_b128 v111, v[128:131] offset:4096\n\t"
        "ds_write_b128 v111, v[132:135] offset:5120\n\t"
        "ds_write_b128 v111, v[64:67] offset:6144\n\t"
        "ds_write_b128 v111, v[68:71] offset:7168\n\t"
        "ds_read_b64_tr_b16 a[240:241], v112 offset:0\n\t"
        "ds_read_b64_tr_b16 a[242:243], v112 offset:2048\n\t"
        "ds_read_b64_tr_b16 a[252:253], v112 offset:4096\n\t"
        "ds_read_b64_tr_b16 a[254:255], v112 offset:6144\n\t"
        "s_waitcnt lgkmcnt(8)\n\t"
        "ds_read_b64_tr_b16 a[244:245], v113 offset:0\n\t"
        "ds_read_b64_tr_b16 a[246:247], v113 offset:2048\n\t"
        "ds_read_b64_tr_b16 v[128:129], v113 offset:4096\n\t"
        "ds_read_b64_tr_b16 v[130:131], v113 offset:6144\n\t"
        "s_waitcnt lgkmcnt(8)\n\t"
        "ds_read_b64_tr_b16 a[248:249], v114 offset:0\n\t"
        "ds_read_b64_tr_b16 a[250:251], v114 offset:2048\n\t"
        "ds_read_b64_tr_b16 v[132:133], v114 offset:4096\n\t"
        "ds_read_b64_tr_b16 v[134:135], v114 offset:6144\n\t"
        "s_waitcnt lgkmcnt(8)\n\t"
        "ds_read_b64_tr_b16 v[80:81], v115 offset:0\n\t"
        "ds_read_b64_tr_b16 v[82:83], v115 offset:2048\n\t"
        "ds_read_b64_tr_b16 v[84:85], v115 offset:4096\n\t"
        "ds_read_b64_tr_b16 v[86:87], v115 offset:6144\n\t"
        "s_waitcnt lgkmcnt(0)\n\t"
        "ds_write_b128 v116, v[80:83] offset:20480\n\t"
        "ds_write_b128 v116, v[84:87] offset:21504\n\t"
        "s_waitcnt vmcnt(8)\n\t"
        "ds_write_b128 v111, a[216:219] offset:0\n\t"
        "ds_write_b128 v111, a[220:223] offset:1024\n\t"
        "ds_write_b128 v111, a[224:227] offset:2048\n\t"
        "ds_write_b128 v111, a[228:231] offset:3072\n\t"
        "ds_write_b128 v111, a[232:235] offset:4096\n\t"
        "ds_write_b128 v111, a[236:239] offset:5120\n\t"
        "ds_write_b128 v111, v[72:75] offset:6144\n\t"
        "ds_write_b128 v111, v[76:79] offset:7168\n\t"
        "ds_read_b64_tr_b16 a[216:217], v112 offset:0\n\t"
        "ds_read_b64_tr_b16 a[218:219], v112 offset:2048\n\t"
        "ds_read_b64_tr_b16 a[228:229], v112 offset:4096\n\t"
        "ds_read_b64_tr_b16 a[230:231], v112 offset:6144\n\t"
        "s_waitcnt lgkmcnt(8)\n\t"
        "ds_read_b64_tr_b16 a[220:221], v113 offset:0\n\t"
        "ds_read_b64_tr_b16 a[222:223], v113 offset:2048\n\t"
        "ds_read_b64_tr_b16 a[232:233], v113 offset:4096\n\t"
        "ds_read_b64_tr_b16 a[234:235], v113 offset:6144\n\t"
        "s_waitcnt lgkmcnt(8)\n\t"
        "ds_read_b64_tr_b16 a[224:225], v114 offset:0\n\t"
        "ds_read_b64_tr_b16 a[226:227], v114 offset:2048\n\t"
        "ds_read_b64_tr_b16 a[236:237], v114 offset:4096\n\t"
        "ds_read_b64_tr_b16 a[238:239], v114 offset:6144\n\t"
        "s_waitcnt lgkmcnt(8)\n\t"
        "ds_read_b64_tr_b16 v[80:81], v115 offset:0\n\t"
        "ds_read_b64_tr_b16 v[82:83], v115 offset:2048\n\t"
        "ds_read_b64_tr_b16 v[84:85], v115 offset:4096\n\t"
        "ds_read_b64_tr_b16 v[86:87], v115 offset:6144\n\t"
        "s_waitcnt lgkmcnt(0)\n\t"
        "ds_write_b128 v116, v[80:83] offset:18432\n\t"
        "ds_write_b128 v116, v[84:87] offset:19456\n\t"
        "s_waitcnt vmcnt(0)\n\t"
        "ds_write_b128 v111, a[192:195] offset:0\n\t"
        "ds_write_b128 v111, a[196:199] offset:1024\n\t"
        "ds_write_b128 v111, a[200:203] offset:2048\n\t"
        "ds_write_b128 v111, a[204:207] offset:3072\n\t"
        "ds_write_b128 v111, a[208:211] offset:4096\n\t"
        "ds_write_b128 v111, a[212:215] offset:5120\n\t"
        "ds_write_b128 v111, v[24:27] offset:6144\n\t"
        "ds_write_b128 v111, v[28:31] offset:7168\n\t"
        "ds_read_b64_tr_b16 a[192:193], v112 offset:0\n\t"
        "ds_read_b64_tr_b16 a[194:195], v112 offset:2048\n\t"
        "ds_read_b64_tr_b16 a[204:205], v112 offset:4096\n\t"
        "ds_read_b64_tr_b16 a[206:207], v112 offset:6144\n\t"
        "s_waitcnt lgkmcnt(8)\n\t"
        "ds_read_b64_tr_b16 a[196:197], v113 offset:0\n\t"
        "ds_read_b64_tr_b16 a[198:199], v113 offset:2048\n\t"
        "ds_read_b64_tr_b16 a[208:209], v113 offset:4096\n\t"
        "ds_read_b64_tr_b16 a[210:211], v113 offset:6144\n\t"
        "s_waitcnt lgkmcnt(8)\n\t"
        "ds_read_b64_tr_b16 a[200:201], v114 offset:0\n\t"
        "ds_read_b64_tr_b16 a[202:203], v114 offset:2048\n\t"
        "ds_read_b64_tr_b16 a[212:213], v114 offset:4096\n\t"
        "ds_read_b64_tr_b16 a[214:215], v114 offset:6144\n\t"
        "s_waitcnt lgkmcnt(8)\n\t"
        "ds_read_b64_tr_b16 v[80:81], v115 offset:0\n\t"
        "ds_read_b64_tr_b16 v[82:83], v115 offset:2048\n\t"
        "ds_read_b64_tr_b16 v[84:85], v115 offset:4096\n\t"
        "ds_read_b64_tr_b16 v[86:87], v115 offset:6144\n\t"
        "s_waitcnt lgkmcnt(0)\n\t"
        "ds_write_b128 v116, v[80:83] offset:16384\n\t"
        "ds_write_b128 v116, v[84:87] offset:17408\n\t"
        "ds_read_b128 v[88:91], v116 offset:0\n\t"
        "ds_read_b128 v[92:95], v116 offset:1024\n\t"
        "ds_read_b128 v[96:99], v116 offset:2048\n\t"
        "s_mov_b32 %[sp], 0\n\t"
        "LP_cv%=:\n\t"
        "buffer_load_dword v104, off, %[sw], %[cv] sc1\n\t"
        "s_waitcnt vmcnt(0) lgkmcnt(0)\n\t"
        "v_readfirstlane_b32 %[t0], v104\n\t"
        "s_cmp_ge_u32 %[t0], 8\n\t"
        "s_cbranch_scc1 LP_wait%=\n\t"
        "s_sleep 8\n\t"
        "s_add_u32 %[sp], %[sp], 1\n\t"
        "s_cmp_lt_u32 %[sp], 0x100000\n\t"
        "s_cbranch_scc1 LP_cv%=\n\t"
        "v_mov_b32_e32 v104, 256\n\t"
        "s_mov_b32 %[t0], 33024\n\t"
        "buffer_store_dword v104, off, %[sw], %[t0] sc1\n\t"
        "s_endpgm\n\t"
        "LP_wait%=:\n\t"
        "s_mov_b32 %[re], 0\n\t"
        "s_and_b32 %[sp], %[tl], 1\n\t"
        "s_lshl_b32 %[sp], %[sp], 14\n\t"
        "s_lshr_b32 %[t1], %[tl], 1\n\t"
        "s_lshl_b32 %[t1], %[t1], 6\n\t"
        "s_add_u32 %[t1], %[sp], %[t1]\n\t"
        "buffer_load_dword v100, v119, %[sw], %[t1] offen sc1\n\t"
        "s_add_u32 %[t1], %[t1], 32768\n\t"
        "buffer_load_dword v101, v119, %[sw], %[t1] offen sc1\n\t"
        "s_lshl_b32 %[t1], %[tl], 9\n\t"
        "buffer_load_dword v102, v120, %[sw], %[t1] offen sc1\n\t"
        "buffer_load_dword v103, v118, %[sw], 0 offen sc1\n\t"
        "LP_again%=:\n\t"
        "s_mov_b32 %[sp], 0\n\t"
        "LP_poll%=:\n\t"
        "buffer_load_dword v103, v118, %[sw], 0 offen sc1\n\t"
        "s_lshr_b32 %[t1], %[tl], 1\n\t"
        "s_waitcnt vmcnt(0)\n\t"
        "v_min_u32_dpp v103, v103, v103 quad_perm:[1,0,3,2] row_mask:0xf bank_mask:0xf\n\t"
        "s_nop 1\n\t"
        "v_min_u32_dpp v103, v103, v103 quad_perm:[2,3,0,1] row_mask:0xf bank_mask:0xf\n\t"
        "s_nop 1\n\t"
        "v_min_u32_dpp v103, v103, v103 row_ror:4 row_mask:0xf bank_mask:0xf\n\t"
        "s_nop 0\n\t"
        "v_readfirstlane_b32 %[pg], v103\n\t"
        "s_cmp_gt_u32 %[pg], %[t1]\n\t"
        "s_cbranch_scc1 LP_have%=\n\t"
        "s_add_u32 %[t0], %[pg], %[re]\n\t"
        "s_cmp_eq_u32 %[t0], 63\n\t"
        "s_cbranch_scc0 LP_no%=\n\t"
        "s_sleep 16\n\t"
        "s_branch LP_have%=\n\t"
        "LP_no%=:\n\t"
        "s_min_u32 %[t1], %[t1], 62\n\t"
        "s_cmp_ge_u32 %[pg], %[t1]\n\t"
        "s_cbranch_scc1 LP_near%=\n\t"
        "s_sleep 24\n\t"
        "LP_near%=:\n\t"
        "s_sleep 2\n\t"
        "s_add_u32 %[sp], %[sp], 1\n\t"
        "s_cmp_lt_u32 %[sp], 0x100000\n\t"
        "s_cbranch_scc1 LP_poll%=\n\t"
        "v_mov_b32_e32 v104, 128\n\t"
        "s_mov_b32 %[t0], 33024\n\t"
        "buffer_store_dword v104, off, %[sw], %[t0] sc1\n\t"
        "s_endpgm\n\t"
        "LP_have%=:\n\t"
        "s_lshl_b32 %[nx], %[tl], 17\n\t"
        "s_add_u32 %[t1], %[t1], 4\n\t"
        "s_cmp_gt_u32 %[pg], %[t1]\n\t"
        "s_cbranch_scc1 LP_run%=\n\t"
        "s_add_u32 %[t0], %[ro], 0\n\t"
        "s_and_b32 %[t0], %[t0], 7\n\t"
        "s_lshl_b32 %[t0], %[t0], 12\n\t"
        "s_add_u32 %[t0], %[t0], %[nx]\n\t"
        "buffer_load_dwordx4 v[24:27], v117, %[sw], %[t0] offen offset:0 sc1\n\t"
        "buffer_load_dwordx4 v[28:31], v117, %[sw], %[t0] offen offset:1024 sc1\n\t"
        "buffer_load_dwordx4 v[32:35], v117, %[sw], %[t0] offen offset:2048 sc1\n\t"
        "buffer_load_dwordx4 v[36:39], v117, %[sw], %[t0] offen offset:3072 sc1\n\t"
        "s_add_u32 %[t0], %[ro], 1\n\t"
        "s_and_b32 %[t0], %[t0], 7\n\t"
        "s_lshl_b32 %[t0], %[t0], 12\n\t"
        "s_add_u32 %[t0], %[t0], %[nx]\n\t"
        "buffer_load_dwordx4 v[40:43], v117, %[sw], %[t0] offen offset:0 sc1\n\t"
        "buffer_load_dwordx4 v[44:47], v117, %[sw], %[t0] offen offset:1024 sc1\n\t"
        "s_waitcnt vmcnt(5) lgkmcnt(2)\n\t"
        "v_mfma_f32_16x16x32_bf16 v[20:23], v[88:91], v[24:27], 0\n\t"
        "ds_read_b128 v[88:91], v116 offset:3072\n\t"
        "v_mfma_f32_16x16x32_bf16 v[8:11], a[0:3], v[24:27], 0\n\t"
        "v_max3_u32 v125, v24, v25, v26\n\t"
        "buffer_load_dwordx4 v[48:51], v117, %[sw], %[t0] offen offset:2048 sc1\n\t"
        "v_mfma_f32_16x16x32_bf16 v[12:15], a[4:7], v[24:27], 0\n\t"
        "v_max_u32_e32 v125, v27, v125\n\t"
        "v_mfma_f32_16x16x32_bf16 v[16:19], a[8:11], v[24:27], 0\n\t"
        "buffer_load_dwordx4 v[52:55], v117, %[sw], %[t0] offen offset:3072 sc1\n\t"
        "s_waitcnt vmcnt(6) lgkmcnt(2)\n\t"
        "v_mfma_f32_16x16x32_bf16 v[20:23], v[92:95], v[28:31], v[20:23]\n\t"
        "ds_read_b128 v[92:95], v116 offset:4096\n\t"
        "v_mfma_f32_16x16x32_bf16 v[8:11], a[12:15], v[28:31], v[8:11]\n\t"
        "v_max3_u32 v125, v28, v29, v125\n\t"
        "s_add_u32 %[t0], %[ro], 2\n\t"
        "s_and_b32 %[t0], %[t0], 7\n\t"
        "s_lshl_b32 %[t0], %[t0], 12\n\t"
        "s_add_u32 %[t0], %[t0], %[nx]\n\t"
        "buffer_load_dwordx4 v[56:59], v117, %[sw], %[t0] offen offset:0 sc1\n\t"
        "v_mfma_f32_16x16x32_bf16 v[12:15], a[16:19], v[28:31], v[12:15]\n\t"
        "v_max3_u32 v125, v30, v31, v125\n\t"
        "v_mfma_f32_16x16x32_bf16 v[16:19], a[20:23], v[28:31], v[16:19]\n\t"
        "buffer_load_dwordx4 v[60:63], v117, %[sw], %[t0] offen offset:1024 sc1\n\t"
        "s_waitcnt vmcnt(7) lgkmcnt(2)\n\t"
        "v_mfma_f32_16x16x32_bf16 v[20:23], v[96:99], v[32:35], v[20:23]\n\t"
        "ds_read_b128 v[96:99], v116 offset:5120\n\t"
        "v_mfma_f32_16x16x32_bf16 v[8:11], a[24:27], v[32:35], v[8:11]\n\t"
        "v_max3_u32 v125, v32, v33, v125\n\t"
        "buffer_load_dwordx4 v[64:67], v117, %[sw], %[t0] offen offset:2048 sc1\n\t"
        "v_mfma_f32_16x16x32_bf16 v[12:15], a[28:31], v[32:35], v[12:15]\n\t"
        "v_max3_u32 v125, v34, v35, v125\n\t"
        "v_mfma_f32_16x16x32_bf16 v[16:19], a[32:35], v[32:35], v[16:19]\n\t"
        "buffer_load_dwordx4 v[68:71], v117, %[sw], %[t0] offen offset:3072 sc1\n\t"
        "s_waitcnt vmcnt(8) lgkmcnt(2)\n\t"
        "v_mfma_f32_16x16x32_bf16 v[20:23], v[88:91], v[36:39], v[20:23]\n\t"
        "ds_read_b128 v[88:91], v116 offset:6144\n\t"
        "v_mfma_f32_16x16x32_bf16 v[8:11], a[36:39], v[36:39], v[8:11]\n\t"
        "v_max3_u32 v125, v36, v37, v125\n\t"
        "s_add_u32 %[t0], %[ro], 3\n\t"
        "s_and_b32 %[t0], %[t0], 7\n\t"
        "s_lshl_b32 %[t0], %[t0], 12\n\t"
        "s_add_u32 %[t0], %[t0], %[nx]\n\t"
        "buffer_load_dwordx4 v[72:75], v117, %[sw], %[t0] offen offset:0 sc1\n\t"
        "v_mfma_f32_16x16x32_bf16 v[12:15], a[40:43], v[36:39], v[12:15]\n\t"
        "v_max3_u32 v125, v38, v39, v125\n\t"
        "v_mfma_f32_16x16x32_bf16 v[16:19], a[44:47], v[36:39], v[16:19]\n\t"
        "buffer_load_dwordx4 v[76:79], v117, %[sw], %[t0] offen offset:1024 sc1\n\t"
        "s_waitcnt vmcnt(9) lgkmcnt(2)\n\t"
        "v_mfma_f32_16x16x32_bf16 v[20:23], v[92:95], v[40:43], v[20:23]\n\t"
        "ds_read_b128 v[92:95], v116 offset:7168\n\t"
        "v_mfma_f32_16x16x32_bf16 v[8:11], a[48:51], v[40:43], v[8:11]\n\t"
        "v_max3_u32 v125, v40, v41, v125\n\t"
        "buffer_load_dwordx4 v[80:83], v117, %[sw], %[t0] offen offset:2048 sc1\n\t"
        "v_mfma_f32_16x16x32_bf16 v[12:15], a[52:55], v[40:43], v[12:15]\n\t"
        "v_max3_u32 v125, v42, v43, v125\n\t"
        "v_mfma_f32_16x16x32_bf16 v[16:19], a[56:59], v[40:43], v[16:19]\n\t"
        "buffer_load_dwordx4 v[84:87], v117, %[sw], %[t0] offen offset:3072 sc1\n\t"
        "s_waitcnt vmcnt(10) lgkmcnt(2)\n\t"
        "v_mfma_f32_16x16x32_bf16 v[20:23], v[96:99], v[44:47], v[20:23]\n\t"
        "ds_read_b128 v[96:99], v116 offset:8192\n\t"
        "v_mfma_f32_16x16x32_bf16 v[8:11], a[60:63], v[44:47], v[8:11]\n\t"
        "v_max3_u32 v125, v44, v45, v125\n\t"
        "s_add_u32 %[t0], %[ro], 4\n\t"
        "s_and_b32 %[t0], %[t0], 7\n\t"
        "s_lshl_b32 %[t0], %[t0], 12\n\t"
        "s_add_u32 %[t0], %[t0], %[nx]\n\t"
        "buffer_load_dwordx4 v[24:27], v117, %[sw], %[t0] offen offset:0 sc1\n\t"
        "v_mfma_f32_16x16x32_bf16 v[12:15], a[64:67], v[44:47], v[12:15]\n\t"
        "v_max3_u32 v125, v46, v47, v125\n\t"
        "v_mfma_f32_16x16x32_bf16 v[16:19], a[68:71], v[44:47], v[16:19]\n\t"
        "buffer_load_dwordx4 v[28:31], v117, %[sw], %[t0] offen offset:1024 sc1\n\t"
        "s_waitcnt vmcnt(11) lgkmcnt(2)\n\t"
        "v_mfma_f32_16x16x32_bf16 v[20:23], v[88:91], v[48:51], v[20:23]\n\t"
        "ds_read_b128 v[88:91], v116 offset:9216\n\t"
        "v_mfma_f32_16x16x32_bf16 v[8:11], a[72:75], v[48:51], v[8:11]\n\t"
        "v_max3_u32 v125, v48, v49, v125\n\t"
        "v_mfma_f32_16x16x32_bf16 v[12:15], a[76:79], v[48:51], v[12:15]\n\t"
        "v_max3_u32 v125, v50, v51, v125\n\t"
        "v_mfma_f32_16x16x32_bf16 v[16:19], a[80:83], v[48:51], v[16:19]\n\t"
        "buffer_load_dwordx4 v[32:35], v117, %[sw], %[t0] offen offset:2048 sc1\n\t"
        "s_waitcnt vmcnt(11) lgkmcnt(2)\n\t"
        "v_mfma_f32_16x16x32_bf16 v[20:23], v[92:95], v[52:55], v[20:23]\n\t"
        "ds_read_b128 v[92:95], v116 offset:10240\n\t"
        "v_mfma_f32_16x16x32_bf16 v[8:11], a[84:87], v[52:55], v[8:11]\n\t"
        "v_max3_u32 v125, v52, v53, v125\n\t"
        "v_mfma_f32_16x16x32_bf16 v[12:15], a[88:91], v[52:55], v[12:15]\n\t"
        "v_max3_u32 v125, v54, v55, v125\n\t"
        "v_mfma_f32_16x16x32_bf16 v[16:19], a[92:95], v[52:55], v[16:19]\n\t"
        "buffer_load_dwordx4 v[36:39], v117, %[sw], %[t0] offen offset:3072 sc1\n\t"
        "s_waitcnt vmcnt(11) lgkmcnt(2)\n\t"
        "s_cmp_lg_u32 %[re], 0\n\t"
        "s_cbranch_scc1 LP_nb%=\n\t"
        "s_barrier\n\t"
        "LP_nb%=:\n\t"
        "v_mfma_f32_16x16x32_bf16 v[20:23], v[96:99], v[56:59], v[20:23]\n\t"
        "ds_read_b128 v[96:99], v116 offset:11264\n\t"
        "v_mfma_f32_16x16x32_bf16 v[8:11], a[96:99], v[56:59], v[8:11]\n\t"
        "v_max3_u32 v125, v56, v57, v125\n\t"
        "v_mfma_f32_16x16x32_bf16 v[12:15], a[100:103], v[56:59], v[12:15]\n\t"
        "v_max3_u32 v125, v58, v59, v125\n\t"
        "v_mfma_f32_16x16x32_bf16 v[16:19], a[104:107], v[56:59], v[16:19]\n\t"
        "s_add_u32 %[t0], %[ro], 5\n\t"
        "s_and_b32 %[t0], %[t0], 7\n\t"
        "s_lshl_b32 %[t0], %[t0], 12\n\t"
        "s_add_u32 %[t0], %[t0], %[nx]\n\t"
        "buffer_load_dwordx4 v[40:43], v117, %[sw], %[t0] offen offset:0 sc1\n\t"
        "s_waitcnt vmcnt(11) lgkmcnt(2)\n\t"
        "v_mfma_f32_16x16x32_bf16 v[20:23], v[88:91], v[60:63], v[20:23]\n\t"
        "ds_read_b128 v[88:91], v116 offset:12288\n\t"
        "v_mfma_f32_16x16x32_bf16 v[8:11], a[108:111], v[60:63], v[8:11]\n\t"
        "v_max3_u32 v125, v60, v61, v125\n\t"
        "v_mfma_f32_16x16x32_bf16 v[12:15], a[112:115], v[60:63], v[12:15]\n\t"
        "v_max3_u32 v125, v62, v63, v125\n\t"
        "v_mfma_f32_16x16x32_bf16 v[16:19], a[116:119], v[60:63], v[16:19]\n\t"
        "buffer_load_dwordx4 v[44:47], v117, %[sw], %[t0] offen offset:1024 sc1\n\t"
        "s_waitcnt vmcnt(11) lgkmcnt(2)\n\t"
        "v_mfma_f32_16x16x32_bf16 v[20:23], v[92:95], v[64:67], v[20:23]\n\t"
        "ds_read_b128 v[92:95], v116 offset:13312\n\t"
        "v_mfma_f32_16x16x32_bf16 v[8:11], a[120:123], v[64:67], v[8:11]\n\t"
        "v_max3_u32 v125, v64, v65, v125\n\t"
        "v_mfma_f32_16x16x32_bf16 v[12:15], a[124:127], v[64:67], v[12:15]\n\t"
        "v_max3_u32 v125, v66, v67, v125\n\t"
        "v_mfma_f32_16x16x32_bf16 v[16:19], a[128:131], v[64:67], v[16:19]\n\t"
        "buffer_load_dwordx4 v[48:51], v117, %[sw], %[t0] offen offset:2048 sc1\n\t"
        "s_waitcnt vmcnt(11) lgkmcnt(2)\n\t"
        "v_mfma_f32_16x16x32_bf16 v[20:23], v[96:99], v[68:71], v[20:23]\n\t"
        "ds_read_b128 v[96:99], v116 offset:14336\n\t"
        "v_mfma_f32_16x16x32_bf16 v[8:11], a[132:135], v[68:71], v[8:11]\n\t"
        "v_max3_u32 v125, v68, v69, v125\n\t"
        "v_mfma_f32_16x16x32_bf16 v[12:15], a[136:139], v[68:71], v[12:15]\n\t"
        "v_max3_u32 v125, v70, v71, v125\n\t"
        "v_mfma_f32_16x16x32_bf16 v[16:19], a[140:143], v[68:71], v[16:19]\n\t"
        "buffer_load_dwordx4 v[52:55], v117, %[sw], %[t0] offen offset:3072 sc1\n\t"
        "s_waitcnt vmcnt(11) lgkmcnt(2)\n\t"
        "v_mfma_f32_16x16x32_bf16 v[20:23], v[88:91], v[72:75], v[20:23]\n\t"
        "ds_read_b128 v[88:91], v116 offset:15360\n\t"
        "v_mfma_f32_16x16x32_bf16 v[8:11], a[144:147], v[72:75], v[8:11]\n\t"
        "v_max3_u32 v125, v72, v73, v125\n\t"
        "v_mfma_f32_16x16x32_bf16 v[12:15], a[148:151], v[72:75], v[12:15]\n\t"
        "v_max3_u32 v125, v74, v75, v125\n\t"
        "v_mfma_f32_16x16x32_bf16 v[16:19], a[152:155], v[72:75], v[16:19]\n\t"
        "s_add_u32 %[t0], %[ro], 6\n\t"
        "s_and_b32 %[t0], %[t0], 7\n\t"
        "s_lshl_b32 %[t0], %[t0], 12\n\t"
        "s_add_u32 %[t0], %[t0], %[nx]\n\t"
        "buffer_load_dwordx4 v[56:59], v117, %[sw], %[t0] offen offset:0 sc1\n\t"
        "s_waitcnt vmcnt(11) lgkmcnt(2)\n\t"
        "v_mfma_f32_16x16x32_bf16 v[20:23], v[92:95], v[76:79], v[20:23]\n\t"
        "ds_read_b128 v[92:95], v116 offset:16384\n\t"
        "v_mfma_f32_16x16x32_bf16 v[8:11], a[156:159], v[76:79], v[8:11]\n\t"
        "v_max3_u32 v125, v76, v77, v125\n\t"
        "v_mfma_f32_16x16x32_bf16 v[12:15], a[160:163], v[76:79], v[12:15]\n\t"
        "v_max3_u32 v125, v78, v79, v125\n\t"
        "v_mfma_f32_16x16x32_bf16 v[16:19], a[164:167], v[76:79], v[16:19]\n\t"
        "buffer_load_dwordx4 v[60:63], v117, %[sw], %[t0] offen offset:1024 sc1\n\t"
        "s_waitcnt vmcnt(11) lgkmcnt(2)\n\t"
        "v_mfma_f32_16x16x32_bf16 v[20:23], v[96:99], v[80:83], v[20:23]\n\t"
        "ds_read_b128 v[96:99], v116 offset:17408\n\t"
        "v_mfma_f32_16x16x32_bf16 v[8:11], a[168:171], v[80:83], v[8:11]\n\t"
        "v_max3_u32 v125, v80, v81, v125\n\t"
        "v_mfma_f32_16x16x32_bf16 v[12:15], a[172:175], v[80:83], v[12:15]\n\t"
        "v_max3_u32 v125, v82, v83, v125\n\t"
        "v_mfma_f32_16x16x32_bf16 v[16:19], a[176:179], v[80:83], v[16:19]\n\t"
        "buffer_load_dwordx4 v[64:67], v117, %[sw], %[t0] offen offset:2048 sc1\n\t"
        "s_waitcnt vmcnt(11) lgkmcnt(2)\n\t"
        "v_mfma_f32_16x16x32_bf16 v[20:23], v[88:91], v[84:87], v[20:23]\n\t"
        "ds_read_b128 v[88:91], v116 offset:18432\n\t"
        "v_mfma_f32_16x16x32_bf16 v[8:11], a[180:183], v[84:87], v[8:11]\n\t"
        "v_max3_u32 v125, v84, v85, v125\n\t"
        "v_mfma_f32_16x16x32_bf16 v[12:15], a[184:187], v[84:87], v[12:15]\n\t"
        "v_max3_u32 v125, v86, v87, v125\n\t"
        "v_mfma_f32_16x16x32_bf16 v[16:19], a[188:191], v[84:87], v[16:19]\n\t"
        "buffer_load_dwordx4 v[68:71], v117, %[sw], %[t0] offen offset:3072 sc1\n\t"
        "s_waitcnt vmcnt(11) lgkmcnt(2)\n\t"
        "v_mfma_f32_16x16x32_bf16 v[20:23], v[92:95], v[24:27], v[20:23]\n\t"
        "ds_read_b128 v[92:95], v116 offset:19456\n\t"
        "v_mfma_f32_16x16x32_bf16 v[8:11], a[192:195], v[24:27], v[8:11]\n\t"
        "v_max3_u32 v125, v24, v25, v125\n\t"
        "v_mfma_f32_16x16x32_bf16 v[12:15], a[196:199], v[24:27], v[12:15]\n\t"
        "v_max3_u32 v125, v26, v27, v125\n\t"
        "v_mfma_f32_16x16x32_bf16 v[16:19], a[200:203], v[24:27], v[16:19]\n\t"
        "s_add_u32 %[t0], %[ro], 7\n\t"
        "s_and_b32 %[t0], %[t0], 7\n\t"
        "s_lshl_b32 %[t0], %[t0], 12\n\t"
        "s_add_u32 %[t0], %[t0], %[nx]\n\t"
        "buffer_load_dwordx4 v[72:75], v117, %[sw], %[t0] offen offset:0 sc1\n\t"
        "s_waitcnt vmcnt(11) lgkmcnt(2)\n\t"
        "v_mfma_f32_16x16x32_bf16 v[20:23], v[96:99], v[28:31], v[20:23]\n\t"
        "ds_read_b128 v[96:99], v116 offset:20480\n\t"
        "v_mfma_f32_16x16x32_bf16 v[8:11], a[204:207], v[28:31], v[8:11]\n\t"
        "v_max3_u32 v125, v28, v29, v125\n\t"
        "v_mfma_f32_16x16x32_bf16 v[12:15], a[208:211], v[28:31], v[12:15]\n\t"
        "v_max3_u32 v125, v30, v31, v125\n\t"
        "v_sub_f32_e32 v104, 1.0, v102\n\t"
        "v_mfma_f32_16x16x32_bf16 v[16:19], a[212:215], v[28:31], v[16:19]\n\t"
        "buffer_load_dwordx4 v[76:79], v117, %[sw], %[t0] offen offset:1024 sc1\n\t"
        "s_waitcnt vmcnt(11) lgkmcnt(2)\n\t"
        "v_mfma_f32_16x16x32_bf16 v[20:23], v[88:91], v[32:35], v[20:23]\n\t"
        "ds_read_b128 v[88:91], v116 offset:21504\n\t"
        "v_mfma_f32_16x16x32_bf16 v[8:11], a[216:219], v[32:35], v[8:11]\n\t"
        "v_max3_u32 v125, v32, v33, v125\n\t"
        "v_mfma_f32_16x16x32_bf16 v[12:15], a[220:223], v[32:35], v[12:15]\n\t"
        "v_max3_u32 v125, v34, v35, v125\n\t"
        "v_cvt_pk_bf16_f32 v104, 0, v104\n\t"
        "v_mfma_f32_16x16x32_bf16 v[16:19], a[224:227], v[32:35], v[16:19]\n\t"
        "buffer_load_dwordx4 v[80:83], v117, %[sw], %[t0] offen offset:2048 sc1\n\t"
        "s_waitcnt vmcnt(11) lgkmcnt(2)\n\t"
        "v_mfma_f32_16x16x32_bf16 v[20:23], v[92:95], v[36:39], v[20:23]\n\t"
        "ds_read_b128 v[92:95], v116 offset:22528\n\t"
        "v_mfma_f32_16x16x32_bf16 v[8:11], a[228:231], v[36:39], v[8:11]\n\t"
        "v_max3_u32 v125, v36, v37, v125\n\t"
        "v_mfma_f32_16x16x32_bf16 v[12:15], a[232:235], v[36:39], v[12:15]\n\t"
        "v_max3_u32 v125, v38, v39, v125\n\t"
        "v_lshlrev_b32_e32 v106, 16, v100\n\t"
        "v_mfma_f32_16x16x32_bf16 v[16:19], a[236:239], v[36:39], v[16:19]\n\t"
        "buffer_load_dwordx4 v[84:87], v117, %[sw], %[t0] offen offset:3072 sc1\n\t"
        "s_waitcnt vmcnt(11) lgkmcnt(2)\n\t"
        "v_mfma_f32_16x16x32_bf16 v[20:23], v[96:99], v[40:43], v[20:23]\n\t"
        "ds_read_b128 v[96:99], v116 offset:23552\n\t"
        "v_mfma_f32_16x16x32_bf16 v[8:11], a[240:243], v[40:43], v[8:11]\n\t"
        "v_max3_u32 v125, v40, v41, v125\n\t"
        "v_mfma_f32_16x16x32_bf16 v[12:15], a[244:247], v[40:43], v[12:15]\n\t"
        "v_max3_u32 v125, v42, v43, v125\n\t"
        "v_mul_f32_e32 v106, v102, v106\n\t"
        "v_mfma_f32_16x16x32_bf16 v[16:19], a[248:251], v[40:43], v[16:19]\n\t"
        "s_waitcnt vmcnt(10) lgkmcnt(2)\n\t"
        "v_mfma_f32_16x16x32_bf16 v[20:23], v[88:91], v[44:47], v[20:23]\n\t"
        "ds_read_b128 v[88:91], v116 offset:24576\n\t"
        "v_mfma_f32_16x16x32_bf16 v[8:11], a[252:255], v[44:47], v[8:11]\n\t"
        "v_max3_u32 v125, v44, v45, v125\n\t"
        "v_mfma_f32_16x16x32_bf16 v[12:15], v[128:131], v[44:47], v[12:15]\n\t"
        "v_max3_u32 v125, v46, v47, v125\n\t"
        "v_cvt_pk_bf16_f32 v106, 0, v106\n\t"
        "v_mfma_f32_16x16x32_bf16 v[16:19], v[132:135], v[44:47], v[16:19]\n\t"
        "s_waitcnt vmcnt(9) lgkmcnt(2)\n\t"
        "v_mfma_f32_16x16x32_bf16 v[20:23], v[92:95], v[48:51], v[20:23]\n\t"
        "ds_read_b128 v[92:95], v116 offset:25600\n\t"
        "v_mfma_f32_16x16x32_bf16 v[8:11], v[136:139], v[48:51], v[8:11]\n\t"
        "v_max3_u32 v125, v48, v49, v125\n\t"
        "v_mfma_f32_16x16x32_bf16 v[12:15], v[140:143], v[48:51], v[12:15]\n\t"
        "v_max3_u32 v125, v50, v51, v125\n\t"
        "v_and_b32_e32 v107, 0xffff0000, v100\n\t"
        "v_mfma_f32_16x16x32_bf16 v[16:19], v[144:147], v[48:51], v[16:19]\n\t"
        "s_waitcnt vmcnt(8) lgkmcnt(2)\n\t"
        "v_mfma_f32_16x16x32_bf16 v[20:23], v[96:99], v[52:55], v[20:23]\n\t"
        "ds_read_b128 v[96:99], v116 offset:26624\n\t"
        "v_mfma_f32_16x16x32_bf16 v[8:11], v[148:151], v[52:55], v[8:11]\n\t"
        "v_max3_u32 v125, v52, v53, v125\n\t"
        "v_mfma_f32_16x16x32_bf16 v[12:15], v[152:155], v[52:55], v[12:15]\n\t"
        "v_max3_u32 v125, v54, v55, v125\n\t"
        "v_mul_f32_e32 v107, v102, v107\n\t"
        "v_mfma_f32_16x16x32_bf16 v[16:19], v[156:159], v[52:55], v[16:19]\n\t"
        "s_waitcnt vmcnt(7) lgkmcnt(2)\n\t"
        "v_mfma_f32_16x16x32_bf16 v[20:23], v[88:91], v[56:59], v[20:23]\n\t"
        "ds_read_b128 v[88:91], v116 offset:27648\n\t"
        "v_mfma_f32_16x16x32_bf16 v[8:11], v[160:163], v[56:59], v[8:11]\n\t"
        "v_max3_u32 v125, v56, v57, v125\n\t"
        "v_mfma_f32_16x16x32_bf16 v[12:15], v[164:167], v[56:59], v[12:15]\n\t"
        "v_max3_u32 v125, v58, v59, v125\n\t"
        "v_cvt_pk_bf16_f32 v107, 0, v107\n\t"
        "v_mfma_f32_16x16x32_bf16 v[16:19], v[168:171], v[56:59], v[16:19]\n\t"
        "s_waitcnt vmcnt(6) lgkmcnt(2)\n\t"
        "v_mfma_f32_16x16x32_bf16 v[20:23], v[92:95], v[60:63], v[20:23]\n\t"
        "ds_read_b128 v[92:95], v116 offset:28672\n\t"
        "v_mfma_f32_16x16x32_bf16 v[8:11], v[172:175], v[60:63], v[8:11]\n\t"
        "v_max3_u32 v125, v60, v61, v125\n\t"
        "v_mfma_f32_16x16x32_bf16 v[12:15], v[176:179], v[60:63], v[12:15]\n\t"
        "v_max3_u32 v125, v62, v63, v125\n\t"
        "v_lshlrev_b32_e32 v108, 16, v101\n\t"
        "v_mfma_f32_16x16x32_bf16 v[16:19], v[180:183], v[60:63], v[16:19]\n\t"
        "s_waitcnt vmcnt(5) lgkmcnt(2)\n\t"
        "v_mfma_f32_16x16x32_bf16 v[20:23], v[96:99], v[64:67], v[20:23]\n\t"
        "ds_read_b128 v[96:99], v116 offset:29696\n\t"
        "v_mfma_f32_16x16x32_bf16 v[8:11], v[184:187], v[64:67], v[8:11]\n\t"
        "v_max3_u32 v125, v64, v65, v125\n\t"
        "v_mfma_f32_16x16x32_bf16 v[12:15], v[188:191], v[64:67], v[12:15]\n\t"
        "v_max3_u32 v125, v66, v67, v125\n\t"
        "v_mul_f32_e32 v108, v102, v108\n\t"
        "v_mfma_f32_16x16x32_bf16 v[16:19], v[192:195], v[64:67], v[16:19]\n\t"
        "s_waitcnt vmcnt(4) lgkmcnt(2)\n\t"
        "v_mfma_f32_16x16x32_bf16 v[20:23], v[88:91], v[68:71], v[20:23]\n\t"
        "ds_read_b128 v[88:91], v116 offset:30720\n\t"
        "v_mfma_f32_16x16x32_bf16 v[8:11], v[196:199], v[68:71], v[8:11]\n\t"
        "v_max3_u32 v125, v68, v69, v125\n\t"
        "v_mfma_f32_16x16x32_bf16 v[12:15], v[200:203], v[68:71], v[12:15]\n\t"
        "v_max3_u32 v125, v70, v71, v125\n\t"
        "v_cvt_pk_bf16_f32 v108, 0, v108\n\t"
        "v_mfma_f32_16x16x32_bf16 v[16:19], v[204:207], v[68:71], v[16:19]\n\t"
        "s_waitcnt vmcnt(3) lgkmcnt(2)\n\t"
        "v_mfma_f32_16x16x32_bf16 v[20:23], v[92:95], v[72:75], v[20:23]\n\t"
        "ds_read_b128 v[92:95], v116 offset:31744\n\t"
        "v_mfma_f32_16x16x32_bf16 v[8:11], v[208:211], v[72:75], v[8:11]\n\t"
        "v_max3_u32 v125, v72, v73, v125\n\t"
        "v_mfma_f32_16x16x32_bf16 v[12:15], v[212:215], v[72:75], v[12:15]\n\t"
        "v_max3_u32 v125, v74, v75, v125\n\t"
        "v_and_b32_e32 v109, 0xffff0000, v101\n\t"
        "v_mfma_f32_16x16x32_bf16 v[16:19], v[216:219], v[72:75], v[16:19]\n\t"
        "s_waitcnt vmcnt(2) lgkmcnt(2)\n\t"
        "v_mfma_f32_16x16x32_bf16 v[20:23], v[96:99], v[76:79], v[20:23]\n\t"
        "v_mfma_f32_16x16x32_bf16 v[8:11], v[220:223], v[76:79], v[8:11]\n\t"
        "v_max3_u32 v125, v76, v77, v125\n\t"
        "v_mfma_f32_16x16x32_bf16 v[12:15], v[224:227], v[76:79], v[12:15]\n\t"
        "v_max3_u32 v125, v78, v79, v125\n\t"
        "v_mul_f32_e32 v109, v102, v109\n\t"
        "v_mfma_f32_16x16x32_bf16 v[16:19], v[228:231], v[76:79], v[16:19]\n\t"
        "s_waitcnt vmcnt(1) lgkmcnt(1)\n\t"
        "v_mfma_f32_16x16x32_bf16 v[20:23], v[88:91], v[80:83], v[20:23]\n\t"
        "ds_read_b128 v[88:91], v116 offset:0\n\t"
        "v_mfma_f32_16x16x32_bf16 v[8:11], v[232:235], v[80:83], v[8:11]\n\t"
        "v_max3_u32 v125, v80, v81, v125\n\t"
        "v_mfma_f32_16x16x32_bf16 v[12:15], v[236:239], v[80:83], v[12:15]\n\t"
        "v_max3_u32 v125, v82, v83, v125\n\t"
        "v_cvt_pk_bf16_f32 v109, 0, v109\n\t"
        "v_mfma_f32_16x16x32_bf16 v[16:19], v[240:243], v[80:83], v[16:19]\n\t"
        "s_waitcnt vmcnt(0) lgkmcnt(1)\n\t"
        "v_mfma_f32_16x16x32_bf16 v[20:23], v[92:95], v[84:87], v[20:23]\n\t"
        "ds_read_b128 v[92:95], v116 offset:1024\n\t"
        "v_mfma_f32_16x16x32_bf16 v[8:11], v[244:247], v[84:87], v[8:11]\n\t"
        "ds_read_b128 v[96:99], v116 offset:2048\n\t"
        "v_max3_u32 v125, v84, v85, v125\n\t"
        "v_mfma_f32_16x16x32_bf16 v[12:15], v[248:251], v[84:87], v[12:15]\n\t"
        "v_max3_u32 v125, v86, v87, v125\n\t"
        "v_mfma_f32_16x16x32_bf16 v[16:19], v[252:255], v[84:87], v[16:19]\n\t"
        "v_cmp_eq_u32_e32 vcc, -1, v125\n\t"
        "s_mov_b32 %[nx], 0xf0000000\n\t"
        "s_mov_b32 %[re], 64\n\t"
        "s_cbranch_vccz LP_epi%=\n\t"
        "s_waitcnt lgkmcnt(0)\n\t"
        "s_branch LP_again%=\n\t"
        "LP_run%=:\n\t"
        "s_add_u32 %[t0], %[ro], 0\n\t"
        "s_and_b32 %[t0], %[t0], 7\n\t"
        "s_lshl_b32 %[t0], %[t0], 12\n\t"
        "s_add_u32 %[t0], %[t0], %[nx]\n\t"
        "buffer_load_dwordx4 v[24:27], v117, %[sw], %[t0] offen offset:0 sc1\n\t"
        "buffer_load_dwordx4 v[28:31], v117, %[sw], %[t0] offen offset:1024 sc1\n\t"
        "buffer_load_dwordx4 v[32:35], v117, %[sw], %[t0] offen offset:2048 sc1\n\t"
        "buffer_load_dwordx4 v[36:39], v117, %[sw], %[t0] offen offset:3072 sc1\n\t"
        "s_add_u32 %[t0], %[ro], 1\n\t"
        "s_and_b32 %[t0], %[t0], 7\n\t"
        "s_lshl_b32 %[t0], %[t0], 12\n\t"
        "s_add_u32 %[t0], %[t0], %[nx]\n\t"
        "buffer_load_dwordx4 v[40:43], v117, %[sw], %[t0] offen offset:0 sc1\n\t"
        "buffer_load_dwordx4 v[44:47], v117, %[sw], %[t0] offen offset:1024 sc1\n\t"
        "buffer_load_dwordx4 v[48:51], v117, %[sw], %[t0] offen offset:2048 sc1\n\t"
        "buffer_load_dwordx4 v[52:55], v117, %[sw], %[t0] offen offset:3072 sc1\n\t"
        "s_add_u32 %[t0], %[ro], 2\n\t"
        "s_and_b32 %[t0], %[t0], 7\n\t"
        "s_lshl_b32 %[t0], %[t0], 12\n\t"
        "s_add_u32 %[t0], %[t0], %[nx]\n\t"
        "buffer_load_dwordx4 v[56:59], v117, %[sw], %[t0] offen offset:0 sc1\n\t"
        "buffer_load_dwordx4 v[60:63], v117, %[sw], %[t0] offen offset:1024 sc1\n\t"
        "buffer_load_dwordx4 v[64:67], v117, %[sw], %[t0] offen offset:2048 sc1\n\t"
        "buffer_load_dwordx4 v[68:71], v117, %[sw], %[t0] offen offset:3072 sc1\n\t"
        "s_add_u32 %[t0], %[ro], 3\n\t"
        "s_and_b32 %[t0], %[t0], 7\n\t"
        "s_lshl_b32 %[t0], %[t0], 12\n\t"
        "s_add_u32 %[t0], %[t0], %[nx]\n\t"
        "buffer_load_dwordx4 v[72:75], v117, %[sw], %[t0] offen offset:0 sc1\n\t"
        "buffer_load_dwordx4 v[76:79], v117, %[sw], %[t0] offen offset:1024 sc1\n\t"
        "buffer_load_dwordx4 v[80:83], v117, %[sw], %[t0] offen offset:2048 sc1\n\t"
        "buffer_load_dwordx4 v[84:87], v117, %[sw], %[t0] offen offset:3072 sc1\n\t"
        "buffer_load_dword v103, v118, %[sw], 0 offen sc1\n\t"
        "LP_tile%=:\n\t"
        "s_and_b32 %[sp], %[tl], 1\n\t"
        "s_lshl_b32 %[sp], %[sp], 14\n\t"
        "s_lshr_b32 %[t1], %[tl], 1\n\t"
        "s_lshl_b32 %[t1], %[t1], 6\n\t"
        "s_add_u32 %[t1], %[sp], %[t1]\n\t"
        "buffer_load_dword v100, v119, %[sw], %[t1] offen sc1\n\t"
        "s_add_u32 %[t1], %[t1], 32768\n\t"
        "buffer_load_dword v101, v119, %[sw], %[t1] offen sc1\n\t"
        "s_lshl_b32 %[t1], %[tl], 9\n\t"
        "buffer_load_dword v102, v120, %[sw], %[t1] offen sc1\n\t"
        "buffer_load_dword v103, v118, %[sw], 0 offen sc1\n\t"
        "s_mov_b32 %[cu], %[nx]\n\t"
        "s_waitcnt vmcnt(20) lgkmcnt(2)\n\t"
        "v_mfma_f32_16x16x32_bf16 v[20:23], v[88:91], v[24:27], 0\n\t"
        "ds_read_b128 v[88:91], v116 offset:3072\n\t"
        "v_mfma_f32_16x16x32_bf16 v[8:11], a[0:3], v[24:27], 0\n\t"
        "v_mfma_f32_16x16x32_bf16 v[12:15], a[4:7], v[24:27], 0\n\t"
        "v_mfma_f32_16x16x32_bf16 v[16:19], a[8:11], v[24:27], 0\n\t"
        "s_add_u32 %[t0], %[ro], 4\n\t"
        "s_and_b32 %[t0], %[t0], 7\n\t"
        "s_lshl_b32 %[t0], %[t0], 12\n\t"
        "s_add_u32 %[t0], %[t0], %[cu]\n\t"
        "buffer_load_dwordx4 v[24:27], v117, %[sw], %[t0] offen offset:0 sc1\n\t"
        "s_waitcnt vmcnt(20) lgkmcnt(2)\n\t"
        "v_mfma_f32_16x16x32_bf16 v[20:23], v[92:95], v[28:31], v[20:23]\n\t"
        "ds_read_b128 v[92:95], v116 offset:4096\n\t"
        "v_mfma_f32_16x16x32_bf16 v[8:11], a[12:15], v[28:31], v[8:11]\n\t"
        "v_mfma_f32_16x16x32_bf16 v[12:15], a[16:19], v[28:31], v[12:15]\n\t"
        "v_mfma_f32_16x16x32_bf16 v[16:19], a[20:23], v[28:31], v[16:19]\n\t"
        "buffer_load_dwordx4 v[28:31], v117, %[sw], %[t0] offen offset:1024 sc1\n\t"
        "s_waitcnt vmcnt(20) lgkmcnt(2)\n\t"
        "v_mfma_f32_16x16x32_bf16 v[20:23], v[96:99], v[32:35], v[20:23]\n\t"
        "ds_read_b128 v[96:99], v116 offset:5120\n\t"
        "v_mfma_f32_16x16x32_bf16 v[8:11], a[24:27], v[32:35], v[8:11]\n\t"
        "v_mfma_f32_16x16x32_bf16 v[12:15], a[28:31], v[32:35], v[12:15]\n\t"
        "v_mfma_f32_16x16x32_bf16 v[16:19], a[32:35], v[32:35], v[16:19]\n\t"
        "buffer_load_dwordx4 v[32:35], v117, %[sw], %[t0] offen offset:2048 sc1\n\t"
        "s_waitcnt vmcnt(20) lgkmcnt(2)\n\t"
        "v_mfma_f32_16x16x32_bf16 v[20:23], v[88:91], v[36:39], v[20:23]\n\t"
        "ds_read_b128 v[88:91], v116 offset:6144\n\t"
        "v_mfma_f32_16x16x32_bf16 v[8:11], a[36:39], v[36:39], v[8:11]\n\t"
        "v_mfma_f32_16x16x32_bf16 v[12:15], a[40:43], v[36:39], v[12:15]\n\t"
        "v_mfma_f32_16x16x32_bf16 v[16:19], a[44:47], v[36:39], v[16:19]\n\t"
        "buffer_load_dwordx4 v[36:39], v117, %[sw], %[t0] offen offset:3072 sc1\n\t"
        "s_waitcnt vmcnt(20) lgkmcnt(2)\n\t"
        "v_mfma_f32_16x16x32_bf16 v[20:23], v[92:95], v[40:43], v[20:23]\n\t"
        "ds_read_b128 v[92:95], v116 offset:7168\n\t"
        "v_mfma_f32_16x16x32_bf16 v[8:11], a[48:51], v[40:43], v[8:11]\n\t"
        "v_mfma_f32_16x16x32_bf16 v[12:15], a[52:55], v[40:43], v[12:15]\n\t"
        "v_mfma_f32_16x16x32_bf16 v[16:19], a[56:59], v[40:43], v[16:19]\n\t"
        "s_add_u32 %[t0], %[ro], 5\n\t"
        "s_and_b32 %[t0], %[t0], 7\n\t"
        "s_lshl_b32 %[t0], %[t0], 12\n\t"
        "s_add_u32 %[t0], %[t0], %[cu]\n\t"
        "buffer_load_dwordx4 v[40:43], v117, %[sw], %[t0] offen offset:0 sc1\n\t"
        "s_waitcnt vmcnt(20) lgkmcnt(2)\n\t"
        "v_mfma_f32_16x16x32_bf16 v[20:23], v[96:99], v[44:47], v[20:23]\n\t"
        "ds_read_b128 v[96:99], v116 offset:8192\n\t"
        "v_mfma_f32_16x16x32_bf16 v[8:11], a[60:63], v[44:47], v[8:11]\n\t"
        "v_mfma_f32_16x16x32_bf16 v[12:15], a[64:67], v[44:47], v[12:15]\n\t"
        "v_mfma_f32_16x16x32_bf16 v[16:19], a[68:71], v[44:47], v[16:19]\n\t"
        "buffer_load_dwordx4 v[44:47], v117, %[sw], %[t0] offen offset:1024 sc1\n\t"
        "s_waitcnt vmcnt(20) lgkmcnt(2)\n\t"
        "v_mfma_f32_16x16x32_bf16 v[20:23], v[88:91], v[48:51], v[20:23]\n\t"
        "ds_read_b128 v[88:91], v116 offset:9216\n\t"
        "v_mfma_f32_16x16x32_bf16 v[8:11], a[72:75], v[48:51], v[8:11]\n\t"
        "v_mfma_f32_16x16x32_bf16 v[12:15], a[76:79], v[48:51], v[12:15]\n\t"
        "v_mfma_f32_16x16x32_bf16 v[16:19], a[80:83], v[48:51], v[16:19]\n\t"
        "buffer_load_dwordx4 v[48:51], v117, %[sw], %[t0] offen offset:2048 sc1\n\t"
        "s_waitcnt vmcnt(20) lgkmcnt(2)\n\t"
        "v_mfma_f32_16x16x32_bf16 v[20:23], v[92:95], v[52:55], v[20:23]\n\t"
        "ds_read_b128 v[92:95], v116 offset:10240\n\t"
        "v_mfma_f32_16x16x32_bf16 v[8:11], a[84:87], v[52:55], v[8:11]\n\t"
        "v_mfma_f32_16x16x32_bf16 v[12:15], a[88:91], v[52:55], v[12:15]\n\t"
        "v_mfma_f32_16x16x32_bf16 v[16:19], a[92:95], v[52:55], v[16:19]\n\t"
        "buffer_load_dwordx4 v[52:55], v117, %[sw], %[t0] offen offset:3072 sc1\n\t"
        "s_waitcnt vmcnt(20) lgkmcnt(2)\n\t"
        "s_barrier\n\t"
        "v_mfma_f32_16x16x32_bf16 v[20:23], v[96:99], v[56:59], v[20:23]\n\t"
        "ds_read_b128 v[96:99], v116 offset:11264\n\t"
        "v_mfma_f32_16x16x32_bf16 v[8:11], a[96:99], v[56:59], v[8:11]\n\t"
        "v_mfma_f32_16x16x32_bf16 v[12:15], a[100:103], v[56:59], v[12:15]\n\t"
        "v_mfma_f32_16x16x32_bf16 v[16:19], a[104:107], v[56:59], v[16:19]\n\t"
        "s_add_u32 %[t0], %[ro], 6\n\t"
        "s_and_b32 %[t0], %[t0], 7\n\t"
        "s_lshl_b32 %[t0], %[t0], 12\n\t"
        "s_add_u32 %[t0], %[t0], %[cu]\n\t"
        "buffer_load_dwordx4 v[56:59], v117, %[sw], %[t0] offen offset:0 sc1\n\t"
        "s_waitcnt vmcnt(20) lgkmcnt(2)\n\t"
        "v_mfma_f32_16x16x32_bf16 v[20:23], v[88:91], v[60:63], v[20:23]\n\t"
        "ds_read_b128 v[88:91], v116 offset:12288\n\t"
        "v_mfma_f32_16x16x32_bf16 v[8:11], a[108:111], v[60:63], v[8:11]\n\t"
        "v_mfma_f32_16x16x32_bf16 v[12:15], a[112:115], v[60:63], v[12:15]\n\t"
        "v_mfma_f32_16x16x32_bf16 v[16:19], a[116:119], v[60:63], v[16:19]\n\t"
        "buffer_load_dwordx4 v[60:63], v117, %[sw], %[t0] offen offset:1024 sc1\n\t"
        "s_waitcnt vmcnt(20) lgkmcnt(2)\n\t"
        "v_mfma_f32_16x16x32_bf16 v[20:23], v[92:95], v[64:67], v[20:23]\n\t"
        "ds_read_b128 v[92:95], v116 offset:13312\n\t"
        "v_mfma_f32_16x16x32_bf16 v[8:11], a[120:123], v[64:67], v[8:11]\n\t"
        "v_mfma_f32_16x16x32_bf16 v[12:15], a[124:127], v[64:67], v[12:15]\n\t"
        "v_mfma_f32_16x16x32_bf16 v[16:19], a[128:131], v[64:67], v[16:19]\n\t"
        "buffer_load_dwordx4 v[64:67], v117, %[sw], %[t0] offen offset:2048 sc1\n\t"
        "s_waitcnt vmcnt(20) lgkmcnt(2)\n\t"
        "v_mfma_f32_16x16x32_bf16 v[20:23], v[96:99], v[68:71], v[20:23]\n\t"
        "ds_read_b128 v[96:99], v116 offset:14336\n\t"
        "v_mfma_f32_16x16x32_bf16 v[8:11], a[132:135], v[68:71], v[8:11]\n\t"
        "v_mfma_f32_16x16x32_bf16 v[12:15], a[136:139], v[68:71], v[12:15]\n\t"
        "v_mfma_f32_16x16x32_bf16 v[16:19], a[140:143], v[68:71], v[16:19]\n\t"
        "buffer_load_dwordx4 v[68:71], v117, %[sw], %[t0] offen offset:3072 sc1\n\t"
        "s_waitcnt vmcnt(20) lgkmcnt(2)\n\t"
        "v_mfma_f32_16x16x32_bf16 v[20:23], v[88:91], v[72:75], v[20:23]\n\t"
        "ds_read_b128 v[88:91], v116 offset:15360\n\t"
        "v_mfma_f32_16x16x32_bf16 v[8:11], a[144:147], v[72:75], v[8:11]\n\t"
        "v_mfma_f32_16x16x32_bf16 v[12:15], a[148:151], v[72:75], v[12:15]\n\t"
        "v_mfma_f32_16x16x32_bf16 v[16:19], a[152:155], v[72:75], v[16:19]\n\t"
        "s_add_u32 %[t0], %[ro], 7\n\t"
        "s_and_b32 %[t0], %[t0], 7\n\t"
        "s_lshl_b32 %[t0], %[t0], 12\n\t"
        "s_add_u32 %[t0], %[t0], %[cu]\n\t"
        "buffer_load_dwordx4 v[72:75], v117, %[sw], %[t0] offen offset:0 sc1\n\t"
        "s_waitcnt vmcnt(13) lgkmcnt(2)\n\t"
        "v_mfma_f32_16x16x32_bf16 v[20:23], v[92:95], v[76:79], v[20:23]\n\t"
        "ds_read_b128 v[92:95], v116 offset:16384\n\t"
        "v_min_u32_dpp v103, v103, v103 quad_perm:[1,0,3,2] row_mask:0xf bank_mask:0xf\n\t"
        "s_nop 0\n\t"
        "v_mfma_f32_16x16x32_bf16 v[8:11], a[156:159], v[76:79], v[8:11]\n\t"
        "v_min_u32_dpp v103, v103, v103 quad_perm:[2,3,0,1] row_mask:0xf bank_mask:0xf\n\t"
        "s_nop 0\n\t"
        "v_mfma_f32_16x16x32_bf16 v[12:15], a[160:163], v[76:79], v[12:15]\n\t"
        "v_min_u32_dpp v103, v103, v103 row_ror:4 row_mask:0xf bank_mask:0xf\n\t"
        "s_nop 0\n\t"
        "v_mfma_f32_16x16x32_bf16 v[16:19], a[164:167], v[76:79], v[16:19]\n\t"
        "buffer_load_dwordx4 v[76:79], v117, %[sw], %[t0] offen offset:1024 sc1\n\t"
        "s_waitcnt vmcnt(20) lgkmcnt(2)\n\t"
        "v_mfma_f32_16x16x32_bf16 v[20:23], v[96:99], v[80:83], v[20:23]\n\t"
        "ds_read_b128 v[96:99], v116 offset:17408\n\t"
        "v_readfirstlane_b32 %[pg], v103\n\t"
        "s_add_u32 %[t1], %[tl], 8\n\t"
        "s_lshl_b32 %[nx], %[t1], 17\n\t"
        "s_lshr_b32 %[t1], %[t1], 1\n\t"
        "v_mfma_f32_16x16x32_bf16 v[8:11], a[168:171], v[80:83], v[8:11]\n\t"
        "s_cmp_gt_u32 %[pg], %[t1]\n\t"
        "s_cselect_b32 %[nx], %[nx], 0xf0000000\n\t"
        "v_mfma_f32_16x16x32_bf16 v[12:15], a[172:175], v[80:83], v[12:15]\n\t"
        "v_mfma_f32_16x16x32_bf16 v[16:19], a[176:179], v[80:83], v[16:19]\n\t"
        "buffer_load_dwordx4 v[80:83], v117, %[sw], %[t0] offen offset:2048 sc1\n\t"
        "s_waitcnt vmcnt(20) lgkmcnt(2)\n\t"
        "v_mfma_f32_16x16x32_bf16 v[20:23], v[88:91], v[84:87], v[20:23]\n\t"
        "ds_read_b128 v[88:91], v116 offset:18432\n\t"
        "v_mfma_f32_16x16x32_bf16 v[8:11], a[180:183], v[84:87], v[8:11]\n\t"
        "v_mfma_f32_16x16x32_bf16 v[12:15], a[184:187], v[84:87], v[12:15]\n\t"
        "v_mfma_f32_16x16x32_bf16 v[16:19], a[188:191], v[84:87], v[16:19]\n\t"
        "buffer_load_dwordx4 v[84:87], v117, %[sw], %[t0] offen offset:3072 sc1\n\t"
        "s_waitcnt vmcnt(15) lgkmcnt(2)\n\t"
        "v_mfma_f32_16x16x32_bf16 v[20:23], v[92:95], v[24:27], v[20:23]\n\t"
        "ds_read_b128 v[92:95], v116 offset:19456\n\t"
        "v_mfma_f32_16x16x32_bf16 v[8:11], a[192:195], v[24:27], v[8:11]\n\t"
        "v_mfma_f32_16x16x32_bf16 v[12:15], a[196:199], v[24:27], v[12:15]\n\t"
        "v_mfma_f32_16x16x32_bf16 v[16:19], a[200:203], v[24:27], v[16:19]\n\t"
        "s_add_u32 %[t0], %[ro], 0\n\t"
        "s_and_b32 %[t0], %[t0], 7\n\t"
        "s_lshl_b32 %[t0], %[t0], 12\n\t"
        "s_add_u32 %[t0], %[t0], %[nx]\n\t"
        "buffer_load_dwordx4 v[24:27], v117, %[sw], %[t0] offen offset:0 sc1\n\t"
        "s_waitcnt vmcnt(15) lgkmcnt(2)\n\t"
        "v_mfma_f32_16x16x32_bf16 v[20:23], v[96:99], v[28:31], v[20:23]\n\t"
        "ds_read_b128 v[96:99], v116 offset:20480\n\t"
        "v_mfma_f32_16x16x32_bf16 v[8:11], a[204:207], v[28:31], v[8:11]\n\t"
        "v_mfma_f32_16x16x32_bf16 v[12:15], a[208:211], v[28:31], v[12:15]\n\t"
        "v_sub_f32_e32 v104, 1.0, v102\n\t"
        "v_mfma_f32_16x16x32_bf16 v[16:19], a[212:215], v[28:31], v[16:19]\n\t"
        "buffer_load_dwordx4 v[28:31], v117, %[sw], %[t0] offen offset:1024 sc1\n\t"
        "s_waitcnt vmcnt(15) lgkmcnt(2)\n\t"
        "v_mfma_f32_16x16x32_bf16 v[20:23], v[88:91], v[32:35], v[20:23]\n\t"
        "ds_read_b128 v[88:91], v116 offset:21504\n\t"
        "v_mfma_f32_16x16x32_bf16 v[8:11], a[216:219], v[32:35], v[8:11]\n\t"
        "v_mfma_f32_16x16x32_bf16 v[12:15], a[220:223], v[32:35], v[12:15]\n\t"
        "v_cvt_pk_bf16_f32 v104, 0, v104\n\t"
        "v_mfma_f32_16x16x32_bf16 v[16:19], a[224:227], v[32:35], v[16:19]\n\t"
        "buffer_load_dwordx4 v[32:35], v117, %[sw], %[t0] offen offset:2048 sc1\n\t"
        "s_waitcnt vmcnt(15) lgkmcnt(2)\n\t"
        "v_mfma_f32_16x16x32_bf16 v[20:23], v[92:95], v[36:39], v[20:23]\n\t"
        "ds_read_b128 v[92:95], v116 offset:22528\n\t"
        "v_mfma_f32_16x16x32_bf16 v[8:11], a[228:231], v[36:39], v[8:11]\n\t"
        "v_mfma_f32_16x16x32_bf16 v[12:15], a[232:235], v[36:39], v[12:15]\n\t"
        "v_lshlrev_b32_e32 v106, 16, v100\n\t"
        "v_mfma_f32_16x16x32_bf16 v[16:19], a[236:239], v[36:39], v[16:19]\n\t"
        "buffer_load_dwordx4 v[36:39], v117, %[sw], %[t0] offen offset:3072 sc1\n\t"
        "s_waitcnt vmcnt(15) lgkmcnt(2)\n\t"
        "v_mfma_f32_16x16x32_bf16 v[20:23], v[96:99], v[40:43], v[20:23]\n\t"
        "ds_read_b128 v[96:99], v116 offset:23552\n\t"
        "v_mfma_f32_16x16x32_bf16 v[8:11], a[240:243], v[40:43], v[8:11]\n\t"
        "v_mfma_f32_16x16x32_bf16 v[12:15], a[244:247], v[40:43], v[12:15]\n\t"
        "v_mul_f32_e32 v106, v102, v106\n\t"
        "v_mfma_f32_16x16x32_bf16 v[16:19], a[248:251], v[40:43], v[16:19]\n\t"
        "s_add_u32 %[t0], %[ro], 1\n\t"
        "s_and_b32 %[t0], %[t0], 7\n\t"
        "s_lshl_b32 %[t0], %[t0], 12\n\t"
        "s_add_u32 %[t0], %[t0], %[nx]\n\t"
        "buffer_load_dwordx4 v[40:43], v117, %[sw], %[t0] offen offset:0 sc1\n\t"
        "s_waitcnt vmcnt(15) lgkmcnt(2)\n\t"
        "v_mfma_f32_16x16x32_bf16 v[20:23], v[88:91], v[44:47], v[20:23]\n\t"
        "ds_read_b128 v[88:91], v116 offset:24576\n\t"
        "v_mfma_f32_16x16x32_bf16 v[8:11], a[252:255], v[44:47], v[8:11]\n\t"
        "v_mfma_f32_16x16x32_bf16 v[12:15], v[128:131], v[44:47], v[12:15]\n\t"
        "v_cvt_pk_bf16_f32 v106, 0, v106\n\t"
        "v_mfma_f32_16x16x32_bf16 v[16:19], v[132:135], v[44:47], v[16:19]\n\t"
        "buffer_load_dwordx4 v[44:47], v117, %[sw], %[t0] offen offset:1024 sc1\n\t"
        "s_waitcnt vmcnt(15) lgkmcnt(2)\n\t"
        "v_mfma_f32_16x16x32_bf16 v[20:23], v[92:95], v[48:51], v[20:23]\n\t"
        "ds_read_b128 v[92:95], v116 offset:25600\n\t"
        "v_mfma_f32_16x16x32_bf16 v[8:11], v[136:139], v[48:51], v[8:11]\n\t"
        "v_mfma_f32_16x16x32_bf16 v[12:15], v[140:143], v[48:51], v[12:15]\n\t"
        "v_and_b32_e32 v107, 0xffff0000, v100\n\t"
        "v_mfma_f32_16x16x32_bf16 v[16:19], v[144:147], v[48:51], v[16:19]\n\t"
        "buffer_load_dwordx4 v[48:51], v117, %[sw], %[t0] offen offset:2048 sc1\n\t"
        "s_waitcnt vmcnt(15) lgkmcnt(2)\n\t"
        "v_mfma_f32_16x16x32_bf16 v[20:23], v[96:99], v[52:55], v[20:23]\n\t"
        "ds_read_b128 v[96:99], v116 offset:26624\n\t"
        "v_mfma_f32_16x16x32_bf16 v[8:11], v[148:151], v[52:55], v[8:11]\n\t"
        "v_mfma_f32_16x16x32_bf16 v[12:15], v[152:155], v[52:55], v[12:15]\n\t"
        "v_mul_f32_e32 v107, v102, v107\n\t"
        "v_mfma_f32_16x16x32_bf16 v[16:19], v[156:159], v[52:55], v[16:19]\n\t"
        "buffer_load_dwordx4 v[52:55], v117, %[sw], %[t0] offen offset:3072 sc1\n\t"
        "s_waitcnt vmcnt(15) lgkmcnt(2)\n\t"
        "v_mfma_f32_16x16x32_bf16 v[20:23], v[88:91], v[56:59], v[20:23]\n\t"
        "ds_read_b128 v[88:91], v116 offset:27648\n\t"
        "v_mfma_f32_16x16x32_bf16 v[8:11], v[160:163], v[56:59], v[8:11]\n\t"
        "v_mfma_f32_16x16x32_bf16 v[12:15], v[164:167], v[56:59], v[12:15]\n\t"
        "v_cvt_pk_bf16_f32 v107, 0, v107\n\t"
        "v_mfma_f32_16x16x32_bf16 v[16:19], v[168:171], v[56:59], v[16:19]\n\t"
        "s_add_u32 %[t0], %[ro], 2\n\t"
        "s_and_b32 %[t0], %[t0], 7\n\t"
        "s_lshl_b32 %[t0], %[t0], 12\n\t"
        "s_add_u32 %[t0], %[t0], %[nx]\n\t"
        "buffer_load_dwordx4 v[56:59], v117, %[sw], %[t0] offen offset:0 sc1\n\t"
        "s_waitcnt vmcnt(15) lgkmcnt(2)\n\t"
        "v_mfma_f32_16x16x32_bf16 v[20:23], v[92:95], v[60:63], v[20:23]\n\t"
        "ds_read_b128 v[92:95], v116 offset:28672\n\t"
        "v_mfma_f32_16x16x32_bf16 v[8:11], v[172:175], v[60:63], v[8:11]\n\t"
        "v_mfma_f32_16x16x32_bf16 v[12:15], v[176:179], v[60:63], v[12:15]\n\t"
        "v_lshlrev_b32_e32 v108, 16, v101\n\t"
        "v_mfma_f32_16x16x32_bf16 v[16:19], v[180:183], v[60:63], v[16:19]\n\t"
        "buffer_load_dwordx4 v[60:63], v117, %[sw], %[t0] offen offset:1024 sc1\n\t"
        "s_waitcnt vmcnt(15) lgkmcnt(2)\n\t"
        "v_mfma_f32_16x16x32_bf16 v[20:23], v[96:99], v[64:67], v[20:23]\n\t"
        "ds_read_b128 v[96:99], v116 offset:29696\n\t"
        "v_mfma_f32_16x16x32_bf16 v[8:11], v[184:187], v[64:67], v[8:11]\n\t"
        "v_mfma_f32_16x16x32_bf16 v[12:15], v[188:191], v[64:67], v[12:15]\n\t"
        "v_mul_f32_e32 v108, v102, v108\n\t"
        "v_mfma_f32_16x16x32_bf16 v[16:19], v[192:195], v[64:67], v[16:19]\n\t"
        "buffer_load_dwordx4 v[64:67], v117, %[sw], %[t0] offen offset:2048 sc1\n\t"
        "s_waitcnt vmcnt(15) lgkmcnt(2)\n\t"
        "v_mfma_f32_16x16x32_bf16 v[20:23], v[88:91], v[68:71], v[20:23]\n\t"
        "ds_read_b128 v[88:91], v116 offset:30720\n\t"
        "v_mfma_f32_16x16x32_bf16 v[8:11], v[196:199], v[68:71], v[8:11]\n\t"
        "v_mfma_f32_16x16x32_bf16 v[12:15], v[200:203], v[68:71], v[12:15]\n\t"
        "v_cvt_pk_bf16_f32 v108, 0, v108\n\t"
        "v_mfma_f32_16x16x32_bf16 v[16:19], v[204:207], v[68:71], v[16:19]\n\t"
        "buffer_load_dwordx4 v[68:71], v117, %[sw], %[t0] offen offset:3072 sc1\n\t"
        "s_waitcnt vmcnt(15) lgkmcnt(2)\n\t"
        "v_mfma_f32_16x16x32_bf16 v[20:23], v[92:95], v[72:75], v[20:23]\n\t"
        "ds_read_b128 v[92:95], v116 offset:31744\n\t"
        "v_mfma_f32_16x16x32_bf16 v[8:11], v[208:211], v[72:75], v[8:11]\n\t"
        "v_mfma_f32_16x16x32_bf16 v[12:15], v[212:215], v[72:75], v[12:15]\n\t"
        "v_and_b32_e32 v109, 0xffff0000, v101\n\t"
        "v_mfma_f32_16x16x32_bf16 v[16:19], v[216:219], v[72:75], v[16:19]\n\t"
        "s_add_u32 %[t0], %[ro], 3\n\t"
        "s_and_b32 %[t0], %[t0], 7\n\t"
        "s_lshl_b32 %[t0], %[t0], 12\n\t"
        "s_add_u32 %[t0], %[t0], %[nx]\n\t"
        "buffer_load_dwordx4 v[72:75], v117, %[sw], %[t0] offen offset:0 sc1\n\t"
        "s_waitcnt vmcnt(15) lgkmcnt(2)\n\t"
        "v_mfma_f32_16x16x32_bf16 v[20:23], v[96:99], v[76:79], v[20:23]\n\t"
        "v_mfma_f32_16x16x32_bf16 v[8:11], v[220:223], v[76:79], v[8:11]\n\t"
        "v_mfma_f32_16x16x32_bf16 v[12:15], v[224:227], v[76:79], v[12:15]\n\t"
        "v_mul_f32_e32 v109, v102, v109\n\t"
        "v_mfma_f32_16x16x32_bf16 v[16:19], v[228:231], v[76:79], v[16:19]\n\t"
        "buffer_load_dwordx4 v[76:79], v117, %[sw], %[t0] offen offset:1024 sc1\n\t"
        "s_waitcnt vmcnt(15) lgkmcnt(1)\n\t"
        "v_mfma_f32_16x16x32_bf16 v[20:23], v[88:91], v[80:83], v[20:23]\n\t"
        "ds_read_b128 v[88:91], v116 offset:0\n\t"
        "v_mfma_f32_16x16x32_bf16 v[8:11], v[232:235], v[80:83], v[8:11]\n\t"
        "v_mfma_f32_16x16x32_bf16 v[12:15], v[236:239], v[80:83], v[12:15]\n\t"
        "v_cvt_pk_bf16_f32 v109, 0, v109\n\t"
        "v_mfma_f32_16x16x32_bf16 v[16:19], v[240:243], v[80:83], v[16:19]\n\t"
        "buffer_load_dwordx4 v[80:83], v117, %[sw], %[t0] offen offset:2048 sc1\n\t"
        "s_waitcnt vmcnt(15) lgkmcnt(1)\n\t"
        "v_mfma_f32_16x16x32_bf16 v[20:23], v[92:95], v[84:87], v[20:23]\n\t"
        "ds_read_b128 v[92:95], v116 offset:1024\n\t"
        "v_mfma_f32_16x16x32_bf16 v[8:11], v[244:247], v[84:87], v[8:11]\n\t"
        "ds_read_b128 v[96:99], v116 offset:2048\n\t"
        "v_mfma_f32_16x16x32_bf16 v[12:15], v[248:251], v[84:87], v[12:15]\n\t"
        "v_mfma_f32_16x16x32_bf16 v[16:19], v[252:255], v[84:87], v[16:19]\n\t"
        "buffer_load_dwordx4 v[84:87], v117, %[sw], %[t0] offen offset:3072 sc1\n\t"
        "LP_epi%=:\n\t"
        "s_lshl_b32 %[t0], %[tl], 15\n\t"
        "s_nop 0\n\t"
        "ds_write_b128 v122, v[20:23] offset:192\n\t"
        "s_nop 0\n\t"
        "ds_write_b128 v122, v[8:11] offset:0\n\t"
        "s_nop 0\n\t"
        "ds_write_b128 v122, v[12:15] offset:64\n\t"
        "ds_write_b128 v122, v[16:19] offset:128\n\t"
        "s_waitcnt lgkmcnt(0)\n\t"
        "s_barrier\n\t"
        "ds_read_b128 v[8:11], v123 offset:0\n\t"
        "ds_read_b128 v[12:15], v123 offset:4352\n\t"
        "ds_read_b128 v[16:19], v123 offset:8704\n\t"
        "ds_read_b128 v[20:23], v123 offset:13056\n\t"
        "s_waitcnt lgkmcnt(2)\n\t"
        "v_pk_add_f32 v[8:9], v[8:9], v[12:13]\n\t"
        "v_pk_add_f32 v[10:11], v[10:11], v[14:15]\n\t"
        "s_waitcnt lgkmcnt(0)\n\t"
        "v_pk_add_f32 v[16:17], v[16:17], v[20:21]\n\t"
        "v_pk_add_f32 v[18:19], v[18:19], v[22:23]\n\t"
        "v_pk_add_f32 v[8:9], v[8:9], v[16:17]\n\t"
        "v_pk_add_f32 v[10:11], v[10:11], v[18:19]\n\t"
        "v_cvt_pk_bf16_f32 v8, 0, v8\n\t"
        "v_cvt_pk_bf16_f32 v9, 0, v9\n\t"
        "v_cvt_pk_bf16_f32 v10, 0, v10\n\t"
        "v_cvt_pk_bf16_f32 v11, 0, v11\n\t"
        "v_mul_f32_e32 v8, v104, v8\n\t"
        "v_mul_f32_e32 v9, v104, v9\n\t"
        "v_mul_f32_e32 v10, v104, v10\n\t"
        "v_mul_f32_e32 v11, v104, v11\n\t"
        "v_cvt_pk_bf16_f32 v8, 0, v8\n\t"
        "v_cvt_pk_bf16_f32 v9, 0, v9\n\t"
        "v_cvt_pk_bf16_f32 v10, 0, v10\n\t"
        "v_cvt_pk_bf16_f32 v11, 0, v11\n\t"
        "v_add_f32_e32 v8, v106, v8\n\t"
        "v_add_f32_e32 v9, v107, v9\n\t"
        "v_add_f32_e32 v10, v108, v10\n\t"
        "v_add_f32_e32 v11, v109, v11\n\t"
        "v_cvt_pk_bf16_f32 v104, v8, v9\n\t"
        "v_cvt_pk_bf16_f32 v105, v10, v11\n\t"
        "buffer_store_dwordx2 v[104:105], v121, %[su], %[t0] offen sc1\n\t"
        "s_add_u32 %[tl], %[tl], 8\n\t"
        "s_cmp_ge_u32 %[tl], 128\n\t"
        "s_cbranch_scc1 LP_end%=\n\t"
        "s_cmp_lg_u32 %[nx], 0xf0000000\n\t"
        "s_cbranch_scc1 LP_tile%=\n\t"
        "s_branch LP_wait%=\n\t"
        "LP_end%=:\n\t"
        "s_waitcnt vmcnt(0) lgkmcnt(0)\n\t"
        ".Lk_end_P:\n\t"
        : [t0] "=&s"(t0), [t1] "=&s"(t1), [tl] "+s"(tl), [pg] "=&s"(pg), [cu] "=&s"(cu), [nx] "=&s"(nx), [sp] "=&s"(sp), [re] "=&s"(re)
        : [so] "s"(so), [sw] "s"(sw), [su] "s"(su), [cv] "s"(F_CVY + nt * 128), [ro] "s"(ro),
          "{v110}"((w * 1024 + (l >> 3)) * 2048 + nt * 128 + (l & 7) * 16), "{v111}"(stg + ((l * 16) ^ ((l & 48) << 1))), "{v112}"(lr), "{v113}"(lr ^ 32u), "{v114}"(lr ^ 64u), "{v115}"(lr ^ 96u),
          "{v116}"(lb + w * 32768 + l * 16), "{v117}"(W_OX + w * 32768 + l * 16), "{v118}"(F_PROG + (w * 8 + (l & 7)) * 128),
          "{v119}"(W_A + ((nt * 2 + (n >> 3)) * 4 + 2 * (n & 1)) * 32768 + w * 4096 + ((n & 7) >> 1) * 16 + g * 4), "{v120}"(W_GATE + w * 128 + g * 4), "{v121}"((4 * w + g) * 2048 + nt * 128 + n * 8),
          "{v122}"(lb + 131072 + w * 4352 + n * 272 + g * 16), "{v123}"(lb + 131072 + (4 * w + g) * 272 + n * 16)
        : "memory", "scc", "vcc", "v8", "v9", "v10", "v11", "v12", "v13", "v14", "v15", "v16", "v17", "v18", "v19", "v20", "v21", "v22", "v23", "v24", "v25", "v26", "v27", "v28", "v29", "v30", "v31", "v32", "v33", "v34", "v35", "v36", "v37", "v38", "v39", "v40", "v41", "v42", "v43", "v44", "v45", "v46", "v47", "v48", "v49", "v50", "v51", "v52", "v53", "v54", "v55", "v56", "v57", "v58", "v59", "v60", "v61", "v62", "v63", "v64", "v65", "v66", "v67", "v68", "v69", "v70", "v71", "v72", "v73", "v74", "v75", "v76", "v77", "v78", "v79", "v80", "v81", "v82", "v83", "v84", "v85", "v86", "v87", "v88", "v89", "v90", "v91", "v92", "v93", "v94", "v95", "v96", "v97", "v98", "v99", "v100", "v101", "v102", "v103", "v104", "v105", "v106", "v107", "v108", "v109", "v124", "v125", "v126", "v127", "v128", "v129", "v130", "v131", "v132", "v133", "v134", "v135", "v136", "v137", "v138", "v139", "v140", "v141", "v142", "v143", "v144", "v145", "v146", "v147", "v148", "v149", "v150", "v151", "v152", "v153", "v154", "v155", "v156", "v157", "v158", "v159", "v160", "v161", "v162", "v163", "v164", "v165", "v166", "v167", "v168", "v169", "v170", "v171", "v172", "v173", "v174", "v175", "v176", "v177", "v178", "v179", "v180", "v181", "v182", "v183", "v184", "v185", "v186", "v187", "v188", "v189", "v190", "v191", "v192", "v193", "v194", "v195", "v196", "v197", "v198", "v199", "v200", "v201", "v202", "v203", "v204", "v205", "v206", "v207", "v208", "v209", "v210", "v211", "v212", "v213", "v214", "v215", "v216", "v217", "v218", "v219", "v220", "v221", "v222", "v223", "v224", "v225", "v226", "v227", "v228", "v229", "v230", "v231", "v232", "v233", "v234", "v235", "v236", "v237", "v238", "v239", "v240", "v241", "v242", "v243", "v244", "v245", "v246", "v247", "v248", "v249", "v250", "v251", "v252", "v253", "v254", "v255", "a0", "a1", "a2", "a3", "a4", "a5", "a6", "a7", "a8", "a9", "a10", "a11", "a12", "a13", "a14", "a15", "a16", "a17", "a18", "a19", "a20", "a21", "a22", "a23", "a24", "a25", "a26", "a27", "a28", "a29", "a30", "a31", "a32", "a33", "a34", "a35", "a36", "a37", "a38", "a39", "a40", "a41", "a42", "a43", "a44", "a45", "a46", "a47", "a48", "a49", "a50", "a51", "a52", "a53", "a54", "a55", "a56", "a57", "a58", "a59", "a60", "a61", "a62", "a63", "a64", "a65", "a66", "a67", "a68", "a69", "a70", "a71", "a72", "a73", "a74", "a75", "a76", "a77", "a78", "a79", "a80", "a81", "a82", "a83", "a84", "a85", "a86", "a87", "a88", "a89", "a90", "a91", "a92", "a93", "a94", "a95", "a96", "a97", "a98", "a99", "a100", "a101", "a102", "a103", "a104", "a105", "a106", "a107", "a108", "a109", "a110", "a111", "a112", "a113", "a114", "a115", "a116", "a117", "a118", "a119", "a120", "a121", "a122", "a123", "a124", "a125", "a126", "a127", "a128", "a129", "a130", "a131", "a132", "a133", "a134", "a135", "a136", "a137", "a138", "a139", "a140", "a141", "a142", "a143", "a144", "a145", "a146", "a147", "a148", "a149", "a150", "a151", "a152", "a153", "a154", "a155", "a156", "a157", "a158", "a159", "a160", "a161", "a162", "a163", "a164", "a165", "a166", "a167", "a168", "a169", "a170", "a171", "a172", "a173", "a174", "a175", "a176", "a177", "a178", "a179", "a180", "a181", "a182", "a183", "a184", "a185", "a186", "a187", "a188", "a189", "a190", "a191", "a192", "a193", "a194", "a195", "a196", "a197", "a198", "a199", "a200", "a201", "a202", "a203", "a204", "a205", "a206", "a207", "a208", "a209", "a210", "a211", "a212", "a213", "a214", "a215", "a216", "a217", "a218", "a219", "a220", "a221", "a222", "a223", "a224", "a225", "a226", "a227", "a228", "a229", "a230", "a231", "a232", "a233", "a234", "a235", "a236", "a237", "a238", "a239", "a240", "a241", "a242", "a243", "a244", "a245", "a246", "a247", "a248", "a249", "a250", "a251", "a252", "a253", "a254", "a255");
}
__device__ __forceinline__ void bulk_block(const Params& p, rsrc_t r, unsigned char* lds, const int wv, const int j) {
    proj_block(p, lds, wv, (j & 6) * 4 + (j >> 4), ((j >> 2) & 2) | (j & 1));
    const int un = ((j & 7) >> 1) * 32 + (j >> 3) * 2 + (j & 1);
    conv_prep(p, r, wv, un >> 2, un & 3, j);
    const int rp = ((j & 7) >> 1) + (j & 1) * 4, nt = (j >> 3) ^ 2, ro = (nt + 2 * rp) & 7;
    if (!conv_core(p, r, lds, wv, un, nt, ro)) return;
    if (!wait_word(r, F_CONV, 128u, 4096u)) return;
    out_block(p, lds, wv, rp, ro, nt);
}

__device__ __forceinline__ void rec_block(const Params& p, rsrc_t r, unsigned char* lds, const int wv, const int b) {
    const int xq = (b & 7) >> 1, t = (b >> 3) * 2 + (b & 1);
    const unsigned lb = __builtin_amdgcn_readfirstlane((unsigned)(size_t)lds);
    if (t < 8) rec_a(p, lb, wv, xq * 8 + t, xq);
    else if (t < 24) rec_b(p, lb, wv, (xq & 1) * 16 + t - 8, xq >> 1, xq);
    else {
        const int hi = xq * 8 + t - 24;
        rec_helper(p, r, lb, wv, hi);
    }
}

__global__ void __launch_bounds__(256, 1) k_main(Params p) {
    extern __shared__ __attribute__((aligned(16))) unsigned char lds[];
    rsrc_t r = __builtin_amdgcn_make_buffer_rsrc(p.ws, 0, W_TOTAL, 0x00020000);
    const int wv = __builtin_amdgcn_readfirstlane(threadIdx.x >> 6);
    {
        unsigned long long pc; int ea, eb, eg;
        asm volatile("s_getpc_b64 %0\n.Lk_begin:\n\ts_mov_b32 %1, .Lk_end_A-.Lk_begin\n\ts_mov_b32 %2, .Lk_end_B-.Lk_begin\n\ts_mov_b32 %3, .Lk_end_P-.Lk_begin" : "=s"(pc), "=s"(ea), "=s"(eb), "=s"(eg));
        const int o = ((blockIdx.x >> 3) * 64 + (threadIdx.x >> 2)) * 128;
        if ((threadIdx.x & 3) == 0 && o < max(ea, max(eb, eg)) - 4) touch_l2((const unsigned char*)pc + o, __builtin_amdgcn_readfirstlane((unsigned)(size_t)lds) + 1024);
    }
    if (wv == 3) {
        unsigned xc; asm volatile("s_getreg_b32 %0, hwreg(HW_REG_XCC_ID)" : "=s"(xc));
        if (xc & 4u) touch_l2(p.Aq + ((blockIdx.x >> 3) * 64 + (threadIdx.x & 63)) * 64, __builtin_amdgcn_readfirstlane((unsigned)(size_t)lds) + 2048);
    }
    if (blockIdx.x == 0 && wv == 1) {
        const int l = threadIdx.x & 63; const v4u z = {0u, 0u, 0u, 0u};
#pragma unroll
        for (int i = 0; i < 6; ++i) st16(r, F_CONV + i * 1024 + l * 16, z);
        DRAIN();
        for (unsigned spin = 0; spin < (1u << 20); ++spin) {
            if (l < 8) __hip_atomic_fetch_add((unsigned*)(p.ws + F_BEAT + l * 128), 1u, __ATOMIC_RELAXED, __HIP_MEMORY_SCOPE_AGENT);
            unsigned c = ld4(r, l < 8 ? F_TKT + l * 64 : OOB);
            c += __shfl_xor(c, 1); c += __shfl_xor(c, 2); c += __shfl_xor(c, 4);
            if (__builtin_amdgcn_readfirstlane(c) >= 256u) break; }
    }
    if (threadIdx.x == 0) {
        const unsigned bo = F_BEAT + (blockIdx.x & 7) * 128, b0 = ld4(r, bo);
        for (unsigned spin = 0; spin < (1u << 22); ++spin) { MEMBAR(); if (ld4(r, bo) != b0) break; }
        MEMBAR();
        unsigned xcc; asm volatile("s_getreg_b32 %0, hwreg(HW_REG_XCC_ID)" : "=s"(xcc)); xcc &= 7u;
        unsigned* tk = (unsigned*)(p.ws + F_TKT);
        unsigned t = __hip_atomic_fetch_add(tk + xcc * 16, 1u, __ATOMIC_RELAXED, __HIP_MEMORY_SCOPE_AGENT);
        if (t >= 32u) {
            unsigned o = __hip_atomic_fetch_add(tk + 128, 1u, __ATOMIC_RELAXED, __HIP_MEMORY_SCOPE_AGENT);
            for (unsigned spin = 0; spin < (1u << 22); ++spin) { unsigned tot = 0u;
                for (int x = 0; x < 8; ++x) tot += __hip_atomic_load(tk + x * 16, __ATOMIC_RELAXED, __HIP_MEMORY_SCOPE_AGENT);
                if (tot >= 256u) break; }
            for (int x = 0; x < 8; ++x) { const unsigned cnt = __hip_atomic_load(tk + x * 16, __ATOMIC_RELAXED, __HIP_MEMORY_SCOPE_AGENT), fr = cnt < 32u ? 32u - cnt : 0u;
                if (o < fr) { xcc = (unsigned)x; t = cnt + o; break; }
                o -= fr; }
        }
        *(volatile int*)lds = (int)((xcc >> 2) * 128u + (t >> 1) * 8u + (xcc & 3u) * 2u + (t & 1u));
    }
    DRAIN();
    __syncthreads();
    const int role = __builtin_amdgcn_readfirstlane(*(volatile int*)lds) & 255;
    __syncthreads();
    if (role >= 128) bulk_block(p, r, lds, wv, role - 128); else rec_block(p, r, lds, wv, role);
}

extern "C" void kernel_launch(void* const* d_in, const int* in_sizes, int n_in, void* d_out, int out_size, void* d_ws, size_t ws_size, hipStream_t stream) {
    Params p;
    p.Aq = (const bf16_t*)d_in[0]; p.Ak = (const bf16_t*)d_in[1]; p.Av = (const bf16_t*)d_in[2];
    p.shift_k = (const bf16_t*)d_in[3]; p.ssm_k = (const bf16_t*)d_in[4]; p.ssm_D = (const bf16_t*)d_in[5];
    p.mem0 = (const bf16_t*)d_in[7]; p.Wq = (const bf16_t*)d_in[8]; p.Wk = (const bf16_t*)d_in[9];
    p.Wv = (const bf16_t*)d_in[10]; p.Wo = (const bf16_t*)d_in[11]; p.gate_w = (const bf16_t*)d_in[13];
    p.out = (bf16_t*)d_out; p.ws = (unsigned char*)d_ws;
    (void)hipFuncSetAttribute((const void*)k_main, hipFuncAttributeMaxDynamicSharedMemorySize, L_TOTAL);
    k_main<<<256, 256, L_TOTAL, stream>>>(p);
}
```
